```python
import torch

def _softmax(x, axis=-1):
    return torch.softmax(x, dim=axis)
def _gelu(x):
    return torch.nn.functional.gelu(x, approximate="tanh")
def _silu(x):
    return torch.nn.functional.silu(x)
def _sigmoid(x):
    return torch.sigmoid(x)
def _layernorm(x, axis=-1, eps=1e-5):
    m = x.mean(dim=axis, keepdim=True)
    v = x.var(dim=axis, keepdim=True, unbiased=False)
    return (x - m) / torch.sqrt(v + eps)
def _scatter_add(tgt, idx, src, axis=0):
    return tgt.clone().index_add_(axis, idx, src)
def _segment_sum(src, seg, n):
    out = torch.zeros((n,) + src.shape[1:], dtype=src.dtype)
    return out.index_add_(0, seg, src)
def _pad(x, pw):
    flat = tuple(v for pair in reversed(pw) for v in pair)
    return torch.nn.functional.pad(x, flat)

kernel_name = 'standalone__modern_conv_block_slak__vision'

def setup_inputs():

    _w = (lambda a: (lambda t: t.to(torch.bfloat16) if t.is_floating_point() else t)(torch.as_tensor(a)))
    H = W = 384
    C = 384
    K = 1
    return {
        "x": _w((lambda *s: torch.randn(*s, dtype=torch.bfloat16) * 0.05)(1, H, W, C)),
        "dw": _w((lambda *s: torch.randn(*s, dtype=torch.bfloat16) * 0.05)(K, K, C)),
        "w1": _w((lambda *s: torch.randn(*s, dtype=torch.bfloat16) * 0.05)(C, 4 * C)),
        "w2": _w((lambda *s: torch.randn(*s, dtype=torch.bfloat16) * 0.05)(4 * C, C)),
        "grn_g": _w((lambda *s: torch.randn(*s, dtype=torch.bfloat16) * 0.05)(4 * C)),
        "grn_b": _w((lambda *s: torch.randn(*s, dtype=torch.bfloat16) * 0.05)(4 * C)),
    }

def reference(x, dw, w1, w2, grn_g, grn_b):
    B, H, W, C = x.shape
    K = dw.shape[0]
    pad = K // 2
    xp = _pad(x, ((0,0),(pad,pad),(pad,pad),(0,0)))
    out = torch.zeros_like(x)
    for i in range(K):
        for j in range(K):
            out = out + xp[:, i:i+H, j:j+W, :] * dw[i, j][None, None, None, :]
    h = _layernorm(out, axis=-1)
    h = _gelu(h @ w1)
    gx = torch.sqrt((h * h).sum(axis=(1, 2), keepdims=True) + 1e-6)
    nx = gx / (gx.mean(axis=-1, keepdims=True) + 1e-6)
    h = grn_g[None, None, None, :] * (h * nx) + grn_b[None, None, None, :] + h
    return x + h @ w2
```

```cpp
#include <hip/hip_runtime.h>
#include <hip/hip_bf16.h>
#include <stdint.h>
#include <stdio.h>

typedef short bf16x8 __attribute__((ext_vector_type(8)));
typedef short s16x4 __attribute__((ext_vector_type(4)));
typedef short v4i16_t __attribute__((ext_vector_type(4)));
typedef float f32x16 __attribute__((ext_vector_type(16)));
typedef float f32x4 __attribute__((ext_vector_type(4)));
typedef float f32x2 __attribute__((ext_vector_type(2)));
typedef unsigned u32x4 __attribute__((ext_vector_type(4)));
typedef __bf16 bf16x2_t __attribute__((ext_vector_type(2)));
typedef unsigned short u16;

#define SW1 0x1p-12f
#define SZS 0x1p-5f
#define SG 0x1p-16f
#define SH 0x1p-15f
#define SW2 0x1p-12f
#define DI __device__ __forceinline__
#define LAS __attribute__((address_space(3)))

constexpr int NPIX = 147456;
constexpr int CH = 384;
constexpr int HID = 1536;
constexpr int NTILE = NPIX / 32;
constexpr int NWG = 256;
constexpr int TPW = NTILE / NWG;

constexpr size_t H_BYTES = (size_t)NPIX * HID * 2;
constexpr unsigned K1_LDS = 163840, K1_PF_OFF = 130048, K1_SMAX = 10, K1_NREP = 4, K1_G_TAB = 1228800, K1_G_CNT = 1239040, K1_G_LOC = 1236992, K1_G_DUM = 1255424,
  K1_G_ACC = 1320960, K1_G_LUT = 1382400, K1_G_BYTES = 1398784;
constexpr bool K1_LUT = true;
constexpr float K1_ZS = 16.0f;
constexpr unsigned K1_GST = 25600, K1_WCH = 6400, K1_ZC = 1365;
constexpr unsigned K2_SHT = 37443;
constexpr int K2_LDS = 163840, K2_HB = 98304, K2_NU = 14, K2_BIAS_OFF = 155648, K2_PF_OFF = 161792;
#define K2_ROLES 0
constexpr int K2_TS = 1, K2_S16_OFF = 109568;

__device__ __attribute__((aligned(256))) char g_buf[K1_G_BYTES];
__device__ __attribute__((aligned(256))) float g_bias[2048];
__device__ __attribute__((aligned(256))) char g_w2r[96 * 12 * 1024];
__device__ __attribute__((aligned(256))) char g_w2f[96 * 12 * 1024];
__device__ __attribute__((aligned(256))) char g_w2q[16 * 12 * 1024];
__device__ __attribute__((aligned(256))) float g_sc[64];
constexpr unsigned G_FLAG = K1_G_CNT + 14336;
constexpr unsigned G_PUB = K1_G_DUM;
__device__ __attribute__((aligned(256))) unsigned g_prm[1024 * 64];
constexpr unsigned G_STMP = K1_G_CNT + 12288;
constexpr unsigned G_BAR = K1_G_CNT + 13312;
constexpr unsigned H_TS = 49152, H_RS = 48 * H_TS, NPAGE = 217, NPAGX = 55;
#define TS(i) do { } while (0)
#define KST(i) do { } while (0)
#define MK_START(i) do { } while (0)
#define MK_END(i) do { } while (0)

DI unsigned pack_bf16(float a, float b) {
  f32x2 v = {a, b};
  bf16x2_t r = __builtin_convertvector(v, bf16x2_t);
  return __builtin_bit_cast(unsigned, r);
}
typedef _Float16 f16x2_t __attribute__((ext_vector_type(2)));
DI unsigned pack_f16(float a, float b) {
  f32x2 v = {a, b};
  f16x2_t r = __builtin_convertvector(v, f16x2_t);
  return __builtin_bit_cast(unsigned, r);
}
DI float bf_lo(unsigned u) { return __uint_as_float(u << 16); }
DI float bf_hi(unsigned u) { return __uint_as_float(u & 0xffff0000u); }
DI float round_bf16(float a) { return bf_lo(pack_bf16(a, 0.f)); }
DI float dot2acc(unsigned a, unsigned b, float c) {
  return __builtin_amdgcn_fdot2_f32_bf16(__builtin_bit_cast(bf16x2_t, a), __builtin_bit_cast(bf16x2_t, b), c, false);
}
DI float ld_bf16(const u16* p) { return __uint_as_float((unsigned)(*p) << 16); }

DI s16x4 tr_read(const char* p) {
  return __builtin_bit_cast(s16x4, __builtin_amdgcn_ds_read_tr16_b64_v4i16((LAS v4i16_t*)p));
}

#define MFMA32(a, b, c) __builtin_amdgcn_mfma_f32_32x32x16_bf16((a), (b), (c), 0, 0, 0)
#define BAR() __builtin_amdgcn_s_barrier()

DI void dma16(unsigned voff, const char* sbase, unsigned lds_dst) {
  unsigned keep;
  asm volatile("s_mov_b32 %0, m0\n\ts_mov_b32 m0, %3\n\ts_nop 0\n\tglobal_load_lds_dwordx4 %1, %2\n\ts_mov_b32 m0, %0"
               : "=&s"(keep) : "v"(voff), "s"(sbase), "s"(lds_dst) : "memory");
}
DI unsigned lds_addr(const char* p) { return (unsigned)(size_t)(const LAS char*)p; }

DI float gelu_tanh(float x) {
  const float c0 = -2.0f * 0.7978845608028654f * 1.4426950408889634f;
  const float c1 = c0 * 0.044715f;
  float x2 = x * x;
  float t = __builtin_fmaf(x2, c1, c0);
  float e = __builtin_amdgcn_exp2f(x * t);
  float r = __builtin_amdgcn_rcpf(1.0f + e);
  return x * r;
}

DI void gload_b128_asm(u32x4& dst, const char* sbase, unsigned voff) {
  asm volatile("global_load_dwordx4 %0, %1, %2" : "=v"(dst) : "v"(voff), "s"(sbase) : "memory");
}
DI void lds_rd128(u32x4& dst, unsigned addr, int off) {
  asm volatile("ds_read_b128 %0, %1 offset:%2" : "=v"(dst) : "v"(addr), "i"(off));
}
DI void lds_rd128_far(u32x4& dst, unsigned voff, unsigned sbase, int imm) {
  unsigned t;
  asm volatile("v_add_u32 %1, %3, %2\n\tv_add_u32 %1, %4, %1\n\tds_read_b128 %0, %1" : "=v"(dst), "=&v"(t) : "v"(voff), "s"(sbase), "i"(imm));
}
DI void gload16i(u32x4& dst, const char* sbase, unsigned voff, int imm) {
  unsigned t;
  asm volatile("v_add_u32 %1, %4, %2\n\tglobal_load_dwordx4 %0, %1, %3" : "=v"(dst), "=&v"(t) : "v"(voff), "s"(sbase), "i"(imm) : "memory");
}
DI void dma16i(const char* sbase, unsigned voff, int immv, unsigned lbase, int imml) {
  unsigned t, keep;
  asm volatile("s_mov_b32 %1, m0\n\ts_add_u32 m0, %4, %6\n\tv_add_u32 %0, %5, %2\n\tglobal_load_lds_dwordx4 %0, %3\n\ts_mov_b32 m0, %1"
               : "=&v"(t), "=&s"(keep) : "v"(voff), "s"(sbase), "s"(lbase), "i"(immv), "i"(imml) : "memory", "scc");
}

DI void st16_wt(void* p, u32x4 v) { asm volatile("global_store_dwordx4 %0, %1, off\n\ts_nop 1" :: "v"(p), "v"(v) : "memory"); }
template <int I> struct IC { static constexpr int value = I; };
template <int N, int I = 0, typename F> DI void static_for(F&& f) {
  if constexpr (I < N) { f(IC<I>{}); static_for<N, I + 1>(f); }
}
template <int C, int M> DI float dpp_add(float v) { return v + __int_as_float(__builtin_amdgcn_update_dpp(0, __float_as_int(v), C, M, 0xf, false)); }
DI float wave_sum63(float v) {
  v = dpp_add<0x111, 0xf>(v); v = dpp_add<0x112, 0xf>(v); v = dpp_add<0x114, 0xf>(v); v = dpp_add<0x118, 0xf>(v);
  v = dpp_add<0x142, 0xa>(v); return dpp_add<0x143, 0xc>(v);
}
DI uint64_t uni64(uint64_t v) { return (uint64_t)__builtin_amdgcn_readfirstlane(v >> 32) << 32 | (unsigned)__builtin_amdgcn_readfirstlane(v); }
DI unsigned wg_side(unsigned b) { return (((const unsigned*)(g_buf + K1_G_LOC))[b & 3] ^ (b >> 2)) & 1; }
DI unsigned h_flip(const char* ws, unsigned region, unsigned t) { return __popc(((const unsigned*)(g_buf + K1_G_LOC + 64))[(((unsigned)(size_t)ws & 0x1fffffu) + region * H_RS + t * H_TS) >> 21]) > 2; }
DI unsigned x_flip(const u16* x, unsigned region) { return __popc(((const unsigned*)(g_buf + K1_G_LOC + 1024))[(((unsigned)(size_t)x & 0x1fffffu) + (12 * region + 6) * 49152u) >> 21]) > 2; }
DI unsigned sub_pixel(const u16* x, unsigned sub, unsigned m) {
  const unsigned blk = sub / 24, u = sub % 24;
  const unsigned region = blk < 256 ? (blk >> 3) * 4 + (blk & 3) : 128 + ((blk - 256) >> 1), side = blk < 256 ? wg_side(blk) : blk & 1;
  return (12 * region + (u >> 1)) * 64 + ((0xda43cb52fe987610ull >> (32 * (u & 1) + 16 * (side ^ x_flip(x, region)) + (m & 12))) & 15) * 4 + (m & 3);
}
DI unsigned h_piece(const char* ws, unsigned sub, unsigned t) {
  const unsigned blk = sub / 24, u = sub % 24;
  const unsigned region = blk < 256 ? (blk >> 3) * 4 + (blk & 3) : 128 + ((blk - 256) >> 1), side = blk < 256 ? wg_side(blk) : blk & 1;
  return region * H_RS + t * H_TS + (u >> 3) * 16384 + (u & 7) * 1024 + ((side ^ h_flip(ws, region, t)) << 13);
}
constexpr unsigned K0_NB = 145 + 24 + 96 + 16 + 24;
constexpr float U24 = 0x1p-24f;
struct Sc { float w1, zs, g, h, w2; };
DI Sc scales() { return Sc{SW1, SZS, SG, SH, SW2}; }
__global__ __launch_bounds__(256) void k0_prep(const u16* __restrict__ w1, const u16* __restrict__ dw, const char* __restrict__ ws, const char* __restrict__ x,
                                                const u16* __restrict__ grn_b, const u16* __restrict__ w2) {
  MK_START(0);
  uint64_t pc; int d1;
  asm volatile("s_getpc_b64 %0\n1:\n\ts_mov_b32 %1, k1_code-1b" : "=s"(pc), "=s"(d1));
  if (threadIdx.x >= 64) pc += (int64_t)d1 + (blockIdx.x >> 3) * 8192;
  unsigned pfd = 0;
  if (threadIdx.x < 64 || (1 && blockIdx.x < 48 && (threadIdx.x >> 6) == 1)) pfd = *(const __attribute__((address_space(1))) unsigned*)((pc & ~127ull) + (threadIdx.x & 63) * 128);
#define K0_END() do { asm volatile("" :: "v"(pfd)); MK_END(0); } while (0)
  unsigned xcc;
  asm volatile("s_getreg_b32 %0, hwreg(HW_REG_XCC_ID)" : "=s"(xcc));
  xcc = (xcc >> 2) & 1;
  if (blockIdx.x < 4 && threadIdx.x == 0) ((unsigned*)(g_buf + K1_G_LOC))[blockIdx.x] = 7 * xcc;
  constexpr unsigned NP = NPAGE + NPAGX;
  if (blockIdx.x < 8) {
    __shared__ unsigned cv[4];
    const unsigned w = threadIdx.x >> 6;
    if ((threadIdx.x & 63) == 0) {
      const char* a = x + (1536 + 24 * (4 * blockIdx.x + w)) * 49152u + 20480, *b = a;
      unsigned long long t0, t1;
      int n = 2;
      asm volatile("" : "+s"(n));
#pragma unroll 1
      do {
        unsigned v;
        asm volatile("s_memrealtime %0\n\ts_waitcnt lgkmcnt(0)\n\tglobal_load_dword %2, %3, off sc1\n\ts_waitcnt vmcnt(0)\n\tglobal_load_dword %2, %4, off sc1\n\ts_waitcnt vmcnt(0)\n\ts_memrealtime %1\n\ts_waitcnt lgkmcnt(0)"
                     : "=&s"(t0), "=&s"(t1), "=&v"(v) : "v"(a), "v"(b) : "memory");
        a += 5120; b = a + 8192;
      } while (--n);
      cv[w] = (unsigned)(t1 - t0) > 72;
    }
    __syncthreads();
    if (threadIdx.x == 0) g_buf[K1_G_CNT + 16320 + blockIdx.x] = cv[0] | cv[1] << 1 | cv[2] << 2 | cv[3] << 3;
    K0_END();
    return;
  }
  const unsigned bi = blockIdx.x - 8;
  const unsigned bx = bi < NP ? bi + K0_NB : bi - NP;
  if (bx >= K0_NB) {
    __shared__ unsigned lat[8];
    const unsigned pg = bx - K0_NB, w = threadIdx.x >> 6, lane = threadIdx.x & 63;
    auto gran = [&](unsigned p) {
      const bool h = p < NPAGE;
      const char* buf = h ? ws : x;
      const int o0 = (int)((h ? p : p - NPAGE) << 21) - (int)((unsigned)(size_t)buf & 0x1fffffu);
      const unsigned al = h ? 49152 : 12 * 49152;
      const unsigned q = ((((unsigned)max(o0, 0) + al - 1) >> (h ? 14 : 16)) * (h ? 87382u : 29128u)) >> 18;
      return buf + min(q * al, (unsigned)(h ? H_BYTES : H_BYTES / 4) - 16384u);
    };
    if (lane < 5) {
      const char* a = gran(lane < 4 ? min(pg + NP - 1 - (lane >> 1), pg - 1 - (lane >> 1)) : pg) + (lane & 1) * 8192 + w * 2048 + (lane < 4 ? 1024 : 0);
      const char* b = a;
      const char* own = (const char*)uni64(__builtin_amdgcn_readlane((uint64_t)a >> 32, 4) * 4294967296ull + (unsigned)__builtin_amdgcn_readlane((unsigned)(uint64_t)a, 4));
      unsigned long long t0, t1, t2;
      int n = 2;
      asm volatile("" : "+s"(n));
#pragma unroll 1
      do {
        unsigned v;
        asm volatile("s_cmp_eq_u32 %6, 2\n\ts_cbranch_scc1 1f\n\ts_sleep 4\n1:\n\ts_memtime %0\n\ts_waitcnt lgkmcnt(0)\n\tglobal_load_dword %3, %4, off sc1\n\ts_waitcnt vmcnt(0)\n\ts_memtime %1\n\ts_waitcnt lgkmcnt(0)\n\t"
                     "global_load_dword %3, %5, off sc1\n\ts_waitcnt vmcnt(0)\n\ts_memtime %2\n\ts_waitcnt lgkmcnt(0)" : "=&s"(t0), "=&s"(t1), "=&s"(t2), "=&v"(v) : "v"(a), "v"(b), "s"(n) : "memory", "scc");
        a = own + 1024 + (w & 1) * 8192; b = a + 8192 - (w & 1) * 16384;
      } while (--n);
      lat[w + 4 * (w & 1)] = (unsigned)(t1 - t0); lat[w + 4 - 4 * (w & 1)] = (unsigned)(t2 - t1);
    }
    __syncthreads();
    if (threadIdx.x == 0) {
      auto fastest = [&](const unsigned* l) { unsigned m = 9999; for (int i = 0; i < 4; ++i) m = l[i] < 300 ? m : min(m, l[i]); return (int)m; };
      const int df = fastest(lat) - fastest(lat + 4);
      const unsigned v = xcc ^ (df > 0), sure = abs(df) > 80;
      char* e = g_buf + K1_G_LOC + 64 + 4 * (pg < NPAGE ? pg : pg + 240 - NPAGE);
      *(u16*)(e + 2) = v ? 3 + 4 * sure : 1 - sure;
      if (pg == 0 || pg == NPAGE) e[0] = v; else e[-3] = v ^ 1;
      if (pg == NPAGE - 1 || pg == NP - 1) e[1] = v; else e[4] = v ^ 1;
    }
    K0_END();
    return;
  }
  if (bx >= 145 + 24 + 96 + 16) {
    __shared__ __attribute__((aligned(16))) float red[2][16][64];
    const Sc sc = scales();
    const int c0 = 64 * (bx - 281), c8 = (threadIdx.x & 7) * 8;
    float a[8], q[8];
#pragma unroll
    for (int i = 0; i < 8; ++i) a[i] = q[i] = 0.f;
#pragma unroll
    for (int k = 0; k < 12; ++k) {
      const u32x4 v = *(const u32x4*)(w1 + (size_t)(32 * k + (threadIdx.x >> 3)) * HID + c0 + c8);
#pragma unroll
      for (int i = 0; i < 8; ++i) {
        const float f = (float)(_Float16)(sc.w1 * (i & 1 ? bf_hi(v[i >> 1]) : bf_lo(v[i >> 1]))) * 0x1p24f;
        a[i] += f; q[i] = __builtin_fmaf(f, f, q[i]);
      }
    }
#pragma unroll
    for (int i = 0; i < 8; ++i) { a[i] = dpp_add<0x118, 0xf>(a[i]); q[i] = dpp_add<0x118, 0xf>(q[i]); }
    if (threadIdx.x & 8) {
#pragma unroll
      for (int i = 0; i < 2; ++i) {
        *(f32x4*)&red[0][threadIdx.x >> 4][c8 + 4 * i] = (f32x4){a[4 * i], a[4 * i + 1], a[4 * i + 2], a[4 * i + 3]};
        *(f32x4*)&red[1][threadIdx.x >> 4][c8 + 4 * i] = (f32x4){q[4 * i], q[4 * i + 1], q[4 * i + 2], q[4 * i + 3]};
      }
    }
    __syncthreads();
    if (threadIdx.x < 64) {
      const int n = c0 + threadIdx.x;
      float s = 0.f, s2 = 0.f;
#pragma unroll
      for (int i = 0; i < 16; ++i) { s += red[0][i][threadIdx.x]; s2 += red[1][i][threadIdx.x]; }
      const float c = (0.f - (float)K1_ZC * s) * 0x1p-48f;
      char* p = g_buf + (n >> 5) * K1_GST + 5120;
      *(f32x4*)(p + 272 * ((n >> 4) & 1) + (n & 15) * 16) = (f32x4){c, c, c, c};
      if ((n & 31) == 0) *(u32x4*)(p + 256) = (u32x4){0u, 0u, 0u, 0u};
      const bool bad = !(s2 >= 384.f * 64 * 64 && s2 <= 384.f * 1024 * 1024);
      const bool any = __ballot(bad) != 0;
      if (threadIdx.x == 0) ((unsigned*)(g_buf + G_FLAG))[98 + bx - 281] = any;
    }
    K0_END();
    return;
  }
  if (bx >= 145 + 24 + 96) {
    const Sc sc = scales();
    const unsigned e = (bx - 265) * 256 + threadIdx.x;
    const float u = 1.0f / (sc.g * sc.w1);
    float r[2];
#pragma unroll
    for (int i = 0; i < 2; ++i) {
      const unsigned c = 2 * e + i;
      r[i] = sc.h * gelu_tanh(u * __uint_as_float((c >> 12) << 31 | ((c & 4095) + 79 * 128) << 16 | 0x8000u));
    }
    *(unsigned*)(g_buf + K1_G_LUT + 4 * e) = pack_f16(r[0], r[1]);
    K0_END();
    return;
  }
  if (bx >= 145 + 24) {
    __shared__ __attribute__((aligned(16))) u16 tw[32][200];
    const int kappa = (bx - 169) >> 1, nh = (bx - 169) & 1;
#pragma unroll
    for (int i = 0; i < 3; ++i) {
      const int e = threadIdx.x + 256 * i, r = e / 24, c = (e % 24) * 8;
      *(u32x4*)&tw[r][c] = *(const u32x4*)(w2 + (size_t)(32 * kappa + r) * CH + 192 * nh + c);
    }
    __syncthreads();
    const int lane = threadIdx.x & 63, g = lane >> 4;
    const float s2 = 256.f * scales().w2;
    bool bad = false;
#pragma unroll
    for (int i = 0; i < 3; ++i) {
      const int fi = (threadIdx.x >> 6) + 4 * i, n = 64 * (fi >> 2) + 4 * (lane & 15) + (fi & 3);
      float wv[8];
      unsigned bits = 0;
#pragma unroll
      for (int j = 0; j < 8; ++j) {
        wv[j] = ld_bf16(&tw[16 * ((j >> 2) ^ (g & K2_TS)) + 4 * g + (j & 3)][n]);
        bits |= wv[j] != 0.f ? 256u << j : 0u;
        wv[j] *= s2;
        bits |= fabsf(wv[j]) >= 48 * 256 * U24 ? 1u << j : 0u;
        bad |= !(fabsf(wv[j]) <= 500.f);
      }
      const unsigned o = (((kappa & ~1) + fi / 6) * 24 + nh * 12 + (fi % 6) * 2 + (kappa & 1)) * 1024 + lane * 16;
      st16_wt(g_w2r + o, (u32x4){pack_f16(wv[0], wv[1]), pack_f16(wv[2], wv[3]), pack_f16(wv[4], wv[5]), pack_f16(wv[6], wv[7])});
      constexpr float q = 1.0f / 256;
      if (kappa < 8) st16_wt(g_w2q + o, (u32x4){pack_f16(q * wv[0], q * wv[1]), pack_f16(q * wv[2], q * wv[3]), pack_f16(q * wv[4], q * wv[5]), pack_f16(q * wv[6], q * wv[7])});
      static_for<4>([&](auto ic) { bits |= __builtin_amdgcn_update_dpp(0u, bits, 0x120 + (1 << decltype(ic)::value), 0xf, 0xf, false); });
      bad |= ((bits >> 8) & ~bits & 255u) != 0;
    }
    const bool any = __ballot(bad) != 0;
    if (lane == 0) ((char*)(g_buf + G_FLAG))[4 * (bx - 169) + (threadIdx.x >> 6)] = any;
    K0_END();
    return;
  }
  if (bx >= 145) {
    __shared__ __attribute__((aligned(16))) float red[16][64];
    const int c0 = 64 * ((bx - 145) >> 2), rg = (bx - 145) & 3, c8 = (threadIdx.x & 7) * 8;
    float a[8];
#pragma unroll
    for (int i = 0; i < 8; ++i) a[i] = 0.f;
#pragma unroll
    for (int k = 0; k < 12; ++k) {
      const int ch = 384 * rg + 32 * k + (threadIdx.x >> 3);
      const float b = ld_bf16(grn_b + ch);
      const u32x4 v = *(const u32x4*)(w2 + (size_t)ch * CH + c0 + c8);
#pragma unroll
      for (int i = 0; i < 4; ++i) { a[2 * i] = __builtin_fmaf(b, bf_lo(v[i]), a[2 * i]); a[2 * i + 1] = __builtin_fmaf(b, bf_hi(v[i]), a[2 * i + 1]); }
    }
#pragma unroll
    for (int i = 0; i < 8; ++i) a[i] = dpp_add<0x118, 0xf>(a[i]);
    if (threadIdx.x & 8) {
      *(f32x4*)&red[threadIdx.x >> 4][c8] = (f32x4){a[0], a[1], a[2], a[3]};
      *(f32x4*)&red[threadIdx.x >> 4][c8 + 4] = (f32x4){a[4], a[5], a[6], a[7]};
    }
    __syncthreads();
    if (threadIdx.x < 64) {
      const int n = c0 + threadIdx.x, m = n % 192;
      float s = 0.f;
#pragma unroll
      for (int i = 0; i < 16; ++i) s += red[i][threadIdx.x];
      g_bias[(((n / 192) * 12 + 4 * (m >> 6) + (m & 3)) * 16 + ((m & 63) >> 2)) * 4 + rg] = s;
    }
    K0_END();
    return;
  }
  const unsigned bid = bx;
  if (bid == 0) {
    const Sc sc = scales();
#pragma unroll
    for (int i = 0; i < 2; ++i) {
      const int e = threadIdx.x + 256 * i;
      u32x4 v = {0u, 0u, 0u, 0u};
      if (e < 192) {
        const int ks = e >> 4, m = e & 15, q = m & 3;
        const unsigned lo = pack_bf16(sc.zs * ld_bf16(dw + 32 * ks + 8 * (m >> 2) + q), 0.f) << (16 * (q & 1));
        const unsigned hi = pack_bf16(sc.zs * ld_bf16(dw + 32 * ks + 8 * (m >> 2) + 4 + q), 0.f) << (16 * (q & 1));
        v = (u32x4){q < 2 ? hi : 0u, q < 2 ? 0u : hi, q < 2 ? lo : 0u, q < 2 ? 0u : lo};
      } else if (e >= 384 && e < 432) {
        const u16* p = dw + 8 * (e - 384);
#pragma unroll
        for (int k = 0; k < 4; ++k) v[k] = pack_bf16(sc.zs * ld_bf16(p + 2 * k), sc.zs * ld_bf16(p + 2 * k + 1));
      } else if (e >= 432 && e < 436) {
        v = (u32x4){__float_as_uint(1e-5f * sc.zs * sc.zs), __float_as_uint(sc.g * 0x1p14f), __float_as_uint(sc.g), 0u};
      }
      *(u32x4*)(g_buf + K1_G_TAB + e * 16) = v;
    }
#pragma unroll
    for (int i = 0; i < 4; ++i) if (threadIdx.x + 256 * i < 896) *(u32x4*)(g_buf + K1_G_CNT + (threadIdx.x + 256 * i) * 16) = (u32x4){0u, 0u, 0u, 0u};
    if (threadIdx.x < 2) ((unsigned*)(g_buf + G_FLAG))[96 + threadIdx.x] = 0u;
    if (threadIdx.x == 0) *(f32x4*)g_sc = (f32x4){sc.h * sc.w2, 1.0f / (sc.h * sc.w2), 0x1p-28f / (sc.h * sc.h), 1.0f / sc.h};
    *(unsigned*)(g_buf + G_PUB + threadIdx.x * 256) = 0u;
    K0_END();
    return;
  }
  __shared__ __attribute__((aligned(16))) u16 tl[32][136];
  const int e = (bid - 1) * 256 + threadIdx.x, ks = (bid - 1) % 12, cb = (bid - 1) / 12;
#pragma unroll
  for (int i = 0; i < 2; ++i) {
    const int r = (threadIdx.x >> 4) + 16 * i, c = (threadIdx.x & 15) * 8;
    *(u32x4*)&tl[r][c] = *(const u32x4*)(w1 + (size_t)(32 * ks + r) * HID + 128 * cb + c);
  }
  const float s1 = scales().w1;
  if (e < (int)(K1_NREP + 1) * 768) *(u32x4*)(g_buf + K1_G_ACC + e * 16) = (u32x4){0u, 0u, 0u, 0u};
  __syncthreads();
#pragma unroll
  for (int i = 0; i < 2; ++i) {
    const int lane = threadIdx.x & 63, f = (threadIdx.x >> 6) + 4 * i, n = 16 * f + (lane & 15), k0 = 8 * (lane >> 4);
    u32x4 P;
#pragma unroll
    for (int jj = 0; jj < 4; ++jj) P[jj] = pack_f16(s1 * ld_bf16(&tl[k0 + 2 * jj][n]), s1 * ld_bf16(&tl[k0 + 2 * jj + 1][n]));
    const int fs = (f & 1) * 12 + ks;
    st16_wt(g_buf + (4 * cb + (f >> 1)) * K1_GST + (fs + (fs >= 5)) * 1024 + lane * 16, P);
  }
  K0_END();
}

constexpr unsigned G_ACC2 = K1_G_ACC + K1_NREP * 12288;

#define VA_CLOB \
  "v0", "v1", "v2", "v3", "v4", "v5", "v6", "v7", "v8", "v9", "v10", "v11", "v12", "v13", "v14", "v15", "v16", "v17", "v18", "v19", "v20", "v21", "v22", "v23", "v24", "v25", "v26", "v27", "v28", "v29", "v30", "v31", \
  "v32", "v33", "v34", "v35", "v36", "v37", "v38", "v39", "v40", "v41", "v42", "v43", "v44", "v45", "v46", "v47", "v48", "v49", "v50", "v51", "v52", "v53", "v54", "v55", "v56", "v57", "v58", "v59", "v60", "v61", "v62", "v63", \
  "v64", "v65", "v66", "v67", "v68", "v69", "v70", "v71", "v72", "v73", "v74", "v75", "v76", "v77", "v78", "v79", "v80", "v81", "v82", "v83", "v84", "v85", "v86", "v87", "v88", "v89", "v90", "v91", "v92", "v93", "v94", "v95", \
  "v96", "v97", "v98", "v99", "v100", "v101", "v102", "v103", "v104", "v105", "v106", "v107", "v108", "v109", "v110", "v111", "v112", "v113", "v114", "v115", "v116", "v117", "v118", "v119", "v120", "v121", "v122", "v123", "v124", "v125", "v126", "v127", \
  "v128", "v129", "v130", "v131", "v132", "v133", "v134", "v135", "v136", "v137", "v138", "v139", "v140", "v141", "v142", "v143", "v144", "v145", "v146", "v147", "v148", "v149", "v150", "v151", "v152", "v153", "v154", "v155", "v156", "v157", "v158", "v159", \
  "v160", "v161", "v162", "v163", "v164", "v165", "v166", "v167", "v168", "v169", "v170", "v171", "v172", "v173", "v174", "v175", "v176", "v177", "v178", "v179", "v180", "v181", "v182", "v183", "v184", "v185", "v186", "v187", "v188", "v189", "v190", "v191", \
  "v192", "v193", "v194", "v195", "v196", "v197", "v198", "v199", "v200", "v201", "v202", "v203", "v204", "v205", "v206", "v207", "v208", "v209", "v210", "v211", "v212", "v213", "v214", "v215", "v216", "v217", "v218", "v219", "v220", "v221", "v222", "v223", \
  "v224", "v225", "v226", "v227", "v228", "v229", "v230", "v231", "v232", "v233", "v234", "v235", "v236", "v237", "v238", "v239", "v240", "v241", "v242", "v243", "v244", "v245", "v246", "v247", "v248", "v249", "v250", "v251", "v252", "v253", "v254", "v255", \
  "a0", "a1", "a2", "a3", "a4", "a5", "a6", "a7", "a8", "a9", "a10", "a11", "a12", "a13", "a14", "a15", "a16", "a17", "a18", "a19", "a20", "a21", "a22", "a23", "a24", "a25", "a26", "a27", "a28", "a29", "a30", "a31", \
  "a32", "a33", "a34", "a35", "a36", "a37", "a38", "a39", "a40", "a41", "a42", "a43", "a44", "a45", "a46", "a47", "a48", "a49", "a50", "a51", "a52", "a53", "a54", "a55", "a56", "a57", "a58", "a59", "a60", "a61", "a62", "a63", \
  "a64", "a65", "a66", "a67", "a68", "a69", "a70", "a71", "a72", "a73", "a74", "a75", "a76", "a77", "a78", "a79", "a80", "a81", "a82", "a83", "a84", "a85", "a86", "a87", "a88", "a89", "a90", "a91", "a92", "a93", "a94", "a95", \
  "a96", "a97", "a98", "a99", "a100", "a101", "a102", "a103", "a104", "a105", "a106", "a107", "a108", "a109", "a110", "a111", "a112", "a113", "a114", "a115", "a116", "a117", "a118", "a119", "a120", "a121", "a122", "a123", "a124", "a125", "a126", "a127", \
  "a128", "a129", "a130", "a131", "a132", "a133", "a134", "a135", "a136", "a137", "a138", "a139", "a140", "a141", "a142", "a143", "a144", "a145", "a146", "a147", "a148", "a149", "a150", "a151", "a152", "a153", "a154", "a155", "a156", "a157", "a158", "a159", \
  "a160", "a161", "a162", "a163", "a164", "a165", "a166", "a167", "a168", "a169", "a170", "a171", "a172", "a173", "a174", "a175", "a176", "a177", "a178", "a179", "a180", "a181", "a182", "a183", "a184", "a185", "a186", "a187", "a188", "a189", "a190", "a191", \
  "a192", "a193", "a194", "a195", "a196", "a197", "a198", "a199", "a200", "a201", "a202", "a203", "a204", "a205", "a206", "a207", "a208", "a209", "a210", "a211", "a212", "a213", "a214", "a215", "a216", "a217", "a218", "a219", "a220", "a221", "a222", "a223", \
  "a224", "a225", "a226", "a227", "a228", "a229", "a230", "a231", "a232", "a233", "a234", "a235", "a236", "a237", "a238", "a239", "a240", "a241", "a242", "a243", "a244", "a245", "a246", "a247", "a248", "a249", "a250", "a251", "a252", "a253", "a254", "a255"
#define K1_CLOB "memory", "vcc", "scc", "s36", "s37", "s38", "s39", "s40", "s41", "s42", "s43", "s44", "s45", "s46", "s47", "s48", "s49", "s50", "s51", "s52", "s53", "s54", "s55", "s56", "s57", "s58", "s59", "s60", "s61", "s62", "s63", "s64", "s65", "s66", "s67", "s68", "s69", "s70", "s71", "s72", "s73", "s74", "s75", "s76", "s77", "s78", "s79", "s80", "s81", "s82", "s83", "s84", "s85", "s86", "s87", "s88", "s89", "s90", "s91", "s92", "s93", "s94", "s95", "s96", "s97", VA_CLOB
DI void k2_params(const u16* x, const char* hws, const u16* out, const u16* grn_g, unsigned tid) {
  const unsigned e = (tid >> 2) * 32 + blockIdx.x * 4 + (tid & 3);
  const unsigned b = e >> 2, w = e & 3, pg = w & 1, nh = w >> 1, q = (b >> 3) * 4 + (b & 3), rs = 128 + (q >> 1), hf = q & 1, side = wg_side(b);
  auto fmask = [&](unsigned region) {
    const unsigned o = ((unsigned)(size_t)hws & 0x1fffffu) + region * H_RS + nh * H_TS;
    const unsigned* p = (const unsigned*)(g_buf + K1_G_LOC + 64) + (o >> 21);
    const unsigned r = 0x200000u + 2 * H_TS - 1 - (o & 0x1fffffu), d1 = ((r >> 15) * 171) >> 9, d2 = (((r + 0x200000u) >> 15) * 171) >> 9;
    const unsigned a1 = ~0u << d1, a2 = d2 < 32 ? ~0u << d2 : 0u;
    return ((__popc(p[0]) > 2 ? ~a1 : 0u) | (__popc(p[1]) > 2 ? a1 & ~a2 : 0u) | (__popc(p[2]) > 2 ? a2 : 0u)) ^ (0u - side);
  };
  const unsigned f0 = fmask(q), f1 = fmask(rs);
  const unsigned fx = ((side ^ x_flip(x, q)) ? ~K2_SHT : 0u) | ((side ^ x_flip(x, rs)) ? K2_SHT : 0u);
  const uint64_t h0 = (uint64_t)hws + q * H_RS + nh * H_TS + pg * 4096, h1 = (uint64_t)hws + rs * H_RS + nh * H_TS + hf * 4096 + pg * 2048;
  const unsigned p0 = (12 * q + 2 * pg) * 49152 + nh * 384, p1 = (12 * rs + 2 * hf + pg) * 49152 + nh * 384, e0 = 288 * b + 72 * w;
  typedef uint64_t u64x2 __attribute__((ext_vector_type(2)));
  u64x2* P = (u64x2*)(g_prm + 64 * e);
  P[0] = (u64x2){f0 | (uint64_t)f1 << 32, fx | (uint64_t)e0 << 32};
  const u32x4 sc = *(const u32x4*)g_sc;
  P[1] = (u64x2){(((b >> 3) * 4 + w) & 7) * 1024 | (uint64_t)sc[0] << 32, sc[1] | (uint64_t)sc[2] << 32};
  P[2] = (u64x2){h0, h0 + 8192};
  P[3] = (u64x2){h1, h1 + 8192};
  P[4] = (u64x2){0, 0};
  P[5] = (u64x2){(uint64_t)x + p0, (uint64_t)x + p1};
  P[6] = (u64x2){(uint64_t)out + p0, (uint64_t)out + p1};
  P[7] = (u64x2){0x1fffffffffffffffull, (uint64_t)g_buf + G_FLAG};
  P[8] = (u64x2){(uint64_t)g_w2f + w * 6144, (uint64_t)g_w2q + w * 6144};
  P[9] = (u64x2){(uint64_t)g_w2r + e0 * 16, (uint64_t)g_w2f + e0 * 16};
  P[10] = (u64x2){(uint64_t)grn_g + w * 256, (uint64_t)g_bias + w * 1536};
  P[11] = (u64x2){(uint64_t)g_buf + G_PUB + b * 256, (uint64_t)g_buf + G_PUB + w * 16384};
  P[12] = (u64x2){(uint64_t)g_w2f + w * 6144, 0};
  P[13] = (u64x2){h0, h0 + 8192};
  P[14] = (u64x2){(uint64_t)x + p0, (uint64_t)out + p0};
  P[15] = (u64x2){f0, fx};
}
__global__ __launch_bounds__(256, 1) void k1_fc1(const u16* __restrict__ x, char* __restrict__ hws, const u16* __restrict__ out, const u16* __restrict__ grn_g) {
  extern __shared__ __attribute__((aligned(16))) char smem[];
  const unsigned w = __builtin_amdgcn_readfirstlane(threadIdx.x >> 6);
  if (blockIdx.x < 8) {
    if (w < 2) k2_params(x, hws, out, grn_g, w * 64 + __builtin_amdgcn_mbcnt_hi(~0u, __builtin_amdgcn_mbcnt_lo(~0u, 0u)));
    return;
  }
  const unsigned lds0 = lds_addr(smem);
  const unsigned b = blockIdx.x - 8;
  const unsigned dir = (b >> 3) & 1;
  TS(0); MK_START(1);
  const uint64_t xp = (uint64_t)x, hp = (uint64_t)hws, gp = (uint64_t)g_buf, wp = gp + w * K1_WCH;
  const unsigned d = dir ? ~0u : 1u, wrap = dir ? ~0u : 48u, rst = dir ? 47u : 0u, pos0 = dir ? K1_SMAX - 1 : 0u;
  const unsigned xo0 = (12 * ((b >> 3) * 4 + (b & 3)) + w) * 49152, xs1 = (1536 + w + (dir ? 8 : 0)) * 49152, sb1 = 2048 * w + (dir ? 33792 : 0);
  const unsigned r0o = ((b >> 3) * 4 + (b & 3)) * H_RS, tsel = 4 * (b & 3), s2 = (b >> 2) & 1;
  const uint64_t ap = gp + K1_G_ACC + (b % K1_NREP) * 12288;
  const unsigned ca0 = w == 0 ? dir * 64 : K1_G_DUM - K1_G_CNT + (4 * b + w) * 64;
  const unsigned pm = lds0 + K1_PF_OFF, pf = ((b >> 3) * 4 + w) * 1024;
  unsigned nb;
  asm volatile(
R"(.macro M t,f,w
.if \f < 31
v_mfma_f32_16x16x32_f16 v[56+4*\t:56+4*\t+3], v[128+4*\f:128+4*\f+3], a[216+4*\w:216+4*\w+3], v[56+4*\t:56+4*\t+3]
.else
v_mfma_f32_16x16x32_f16 v[56+4*\t:56+4*\t+3], a[4*\f-4*31:4*\f-4*31+3], a[216+4*\w:216+4*\w+3], v[56+4*\t:56+4*\t+3]
.endif
.endm
.macro Z t,f,w
.if \f < 31
v_mfma_f32_16x16x32_f16 v[56+4*\t:56+4*\t+3], v[128+4*\f:128+4*\f+3], a[216+4*\w:216+4*\w+3], v[252:255]
.else
v_mfma_f32_16x16x32_f16 v[56+4*\t:56+4*\t+3], a[4*\f-4*31:4*\f-4*31+3], a[216+4*\w:216+4*\w+3], v[252:255]
.endif
.endm
.macro LZ a,f
.if \f < 31
v_mfma_f32_16x16x32_bf16 v[36:36+3], v[\a:\a+3], v[128+4*\f:128+4*\f+3], 0
.else
v_mfma_f32_16x16x32_bf16 v[36:36+3], v[\a:\a+3], a[4*\f-4*31:4*\f-4*31+3], 0
.endif
.endm
.macro LS a,f
.if \f < 31
v_mfma_f32_16x16x32_bf16 v[36:36+3], v[\a:\a+3], v[128+4*\f:128+4*\f+3], v[36:36+3]
.else
v_mfma_f32_16x16x32_bf16 v[36:36+3], v[\a:\a+3], a[4*\f-4*31:4*\f-4*31+3], v[36:36+3]
.endif
.endm
.macro LD d,a,f
.if \f < 31
v_mfma_f32_16x16x32_bf16 v[\d:\d+3], a[\a:\a+3], v[128+4*\f:128+4*\f+3], v[36:36+3]
.else
v_mfma_f32_16x16x32_bf16 v[\d:\d+3], a[\a:\a+3], a[4*\f-4*31:4*\f-4*31+3], v[36:36+3]
.endif
.endm
.macro GZ f
.if \f < 31
v_mfma_f32_16x16x32_f16 v[52:55], v[128+4*\f:128+4*\f+3], v[128+4*\f:128+4*\f+3], 0
.else
v_mfma_f32_16x16x32_f16 v[52:55], a[4*\f-4*31:4*\f-4*31+3], a[4*\f-4*31:4*\f-4*31+3], 0
.endif
.endm
.macro GS f
.if \f < 31
v_mfma_f32_16x16x32_f16 v[52:55], v[128+4*\f:128+4*\f+3], v[128+4*\f:128+4*\f+3], v[52:55]
.else
v_mfma_f32_16x16x32_f16 v[52:55], a[4*\f-4*31:4*\f-4*31+3], a[4*\f-4*31:4*\f-4*31+3], v[52:55]
.endif
.endm
.macro CL p,z,s
v_fma_mixlo_f16 v[\p], v[\z], v[\s], 0 op_sel_hi:[0,1,0]
.endm
.macro CH p,z,s
v_fma_mixhi_f16 v[\p], v[\z], v[\s], 0 op_sel:[0,1,0] op_sel_hi:[0,1,0]
.endm
.macro XL f,o
.if \f < 31
global_load_dwordx4 v[128+4*\f:128+4*\f+3], v3, s[46:47] offset:\o
.else
global_load_dwordx4 a[4*\f-4*31:4*\f-4*31+3], v3, s[46:47] offset:\o
.endif
.endm
.macro XN f,o
.if \f < 31
global_load_dwordx4 v[128+4*\f:128+4*\f+3], v3, s[46:47] offset:\o nt
.else
global_load_dwordx4 a[4*\f-4*31:4*\f-4*31+3], v3, s[46:47] offset:\o nt
.endif
.endm
.macro F t,z
v_fma_f32 v[\t], -v[\z], v[\z], s36
.endm
.macro U t,z
v_mul_f32 v[\t], v[\z], v[\t]
.endm
.macro E t
v_exp_f32 v[\t], v[\t]
.endm
.macro A t
v_add_f32 v[\t], 1.0, v[\t]
.endm
.macro R t
v_rcp_f32 v[\t], v[\t]
.endm
.macro C d,a,b
v_cvt_pk_bf16_f32 v[\d], v[\a], v[\b]
.endm
.macro S0 q,t
v_mul_f32 v[\q], v[\t], v[\t]
.endm
.macro S q,t
v_fmac_f32 v[\q], v[\t], v[\t]
.endm
.macro HC d,z
v_cvt_pk_f16_f32 v[\d], v[\z], v[\z+1]
.endm
.macro HF t,p
v_pk_fma_f16 v[\t], v[\p], v[\p], s36 neg_lo:[1,0,0] neg_hi:[1,0,0]
.endm
.macro HM t,p
v_pk_mul_f16 v[\t], v[\p], v[\t]
.endm
.macro HE d,s
v_exp_f16 v[\d], v[\s]
.endm
.macro HG d,s
v_exp_f16_sdwa v[\d], v[\s] dst_sel:WORD_1 dst_unused:UNUSED_PRESERVE src0_sel:WORD_1
.endm
.macro HA t
v_pk_add_f16 v[\t], v[\t], 1.0 op_sel_hi:[1,0]
.endm
.macro HR d,s
v_rcp_f16 v[\d], v[\s]
.endm
.macro HQ d,s
v_rcp_f16_sdwa v[\d], v[\s] dst_sel:WORD_1 dst_unused:UNUSED_PRESERVE src0_sel:WORD_1
.endm
.macro HP d,s
v_rcp_f16_sdwa v[\d], v[\s] dst_sel:DWORD dst_unused:UNUSED_PAD src0_sel:WORD_1
.endm
.macro HY t,z
v_fma_mix_f32 v[\t], v[\z], v[\t], 0 op_sel_hi:[0,1,0]
.endm
.macro HK p
v_and_b32 v[\p], s56, v[\p]
.endm
.macro LQ p
v_lshrrev_b32 v[\p], 2, v[\p]
.endm
.macro LL t,z
v_lshlrev_b32_sdwa v[\t], 1, v[\z] dst_sel:DWORD dst_unused:UNUSED_PAD src0_sel:DWORD src1_sel:WORD_1
.endm
.macro LH t,p
v_and_b32_sdwa v[\t], s56, v[\p] dst_sel:DWORD dst_unused:UNUSED_PAD src0_sel:DWORD src1_sel:WORD_1
.endm
.macro LA t
ds_read_u16 v[\t], v[\t] offset:5376
.endm
.macro LC_ t
ds_read_u16_d16_hi v[\t], v[\t] offset:5376
.endm
.macro LO p,a,b
v_bfi_b32 v[\p], s36, v[\a], v[\b]
.endm
.macro HZ q,p
v_pk_mul_f16 v[\q], v[\p], v[\p]
.endm
.macro HS q,p
v_pk_fma_f16 v[\q], v[\p], v[\p], v[\q]
.endm
.macro HT q,p
v_fma_mix_f32 v[\q], v[\p], v[\p], v[\q] op_sel:[1,1,0] op_sel_hi:[1,1,0]
.endm
.macro HX q
v_fma_mix_f32 v[\q], v[\q], 1.0, v[\q] op_sel:[0,0,1] op_sel_hi:[1,0,1]
.endm
.macro UX t,z
v_mul_f32_e64 v[\t], v[\z], v[\t]
.endm
.macro EX t
v_exp_f32_e64 v[\t], v[\t]
.endm
.macro AX t
v_add_f32_e64 v[\t], 1.0, v[\t]
.endm
.macro RX t
v_rcp_f32_e64 v[\t], v[\t]
.endm
.macro TX q,t
v_mul_f32_e64 v[\q], v[\t], v[\t]
.endm
.macro SX q,t
v_fmac_f32_e64 v[\q], v[\t], v[\t]
.endm
.macro AW a,s
v_accvgpr_write_b32 a[\a], v[\s]
.endm
.macro RA d,v,o
ds_read_b128 a[\d:\d+3], v[\v] offset:\o
.endm
.macro RV d,v,o
ds_read_b128 v[\d:\d+3], v[\v] offset:\o
.endm
.macro W2 a,b,o
ds_write2st64_b32 v4, v[\a], v[\b] offset0:\o offset1:\o+4
.endm
.macro W1 a,o
ds_write_b32 v4, v[\a] offset:\o
.endm
.macro DW o
global_load_lds_dwordx4 v0, s[40:41] offset:\o
.endm
.macro DF
global_load_lds_dwordx4 v0, s[70:71]
.endm
.macro DX
global_load_lds_dword v3, s[46:47]
.endm
.macro ST d
global_store_dwordx4 v2, v[\d:\d+3], s[42:43] nt sc1
.endm
.macro SU d
global_store_dwordx4 v2, v[\d:\d+3], s[42:43] sc1
.endm
.macro SP
global_atomic_add_f64 v6, v[14:15], s[48:49]
.endm
.macro AT
global_atomic_add v11, v9, v10, s[68:69] sc0
.endm
.macro PFA
s_getpc_b64 s[70:71]
s_add_u32 s70, s70, 7f-.
s_addc_u32 s71, s71, 0
s_andn2_b32 s70, s70, 0x7f
s_min_u32 s53, %[pf], 8f-7f-1152
s_andn2_b32 s53, s53, 0x7f
s_add_u32 s70, s70, s53
s_addc_u32 s71, s71, 0
.endm
.macro PFN
s_getpc_b64 s[70:71]
s_add_u32 s70, s70, k2_code@rel32@lo-1020
s_addc_u32 s71, s71, k2_code@rel32@hi-1012
s_add_u32 s70, s70, %[pf]
s_addc_u32 s71, s71, 0
s_andn2_b32 s70, s70, 0x7f
.endm
.macro PFW
s_getpc_b64 s[70:71]
s_add_u32 s70, s70, g_w2q@rel32@lo+4
s_addc_u32 s71, s71, g_w2q@rel32@hi+12
s_add_u32 s70, s70, %[pf]
s_addc_u32 s71, s71, 0
.endm
.macro PF0
s_getpc_b64 s[70:71]
s_andn2_b32 s70, s70, 0x7f
s_min_u32 s53, %[pf], 7f-.-1152
s_andn2_b32 s53, s53, 0x7f
s_add_u32 s70, s70, s53
s_addc_u32 s71, s71, 0
.endm
.macro WL n
s_waitcnt lgkmcnt(\n)
.endm
.macro WV n
s_waitcnt vmcnt(\n)
.endm
.macro LU t,p
v_pk_mul_f16 v[\t], v[\p], v34
.endm
.macro CF d,a,b
v_cvt_pk_f16_f32 v[\d], v[\a], v[\b]
.endm
.macro XS d
v_pk_fma_f16 v[\d], v[\d], v13, s56
.endm
.macro AR d,a
v_accvgpr_read_b32 v[\d], a[\a]
.endm
.macro DQ o
global_load_lds_dword v23, s[40:41] offset:\o
.endm
.macro GE
v_cmp_eq_u32 vcc, 0, v35
s_nop 4
s_cbranch_vccz 9f
v_mov_b32 v35, 14720
s_mov_b64 exec, 1
global_store_dword v35, v10, s[68:69] sc1
s_mov_b64 exec, -1
9:
.endm
k1_code:
v_mbcnt_lo_u32_b32 v16, -1, 0
v_mbcnt_hi_u32_b32 v16, -1, v16
v_lshlrev_b32 v0, 4, v16
v_and_b32 v17, 15, v16
v_lshrrev_b32 v18, 4, v16
s_add_u32 s68, %[glo], 1239040
s_addc_u32 s69, %[ghi], 0
s_load_dwordx2 s[66:67], s[68:69], 16320
s_mov_b32 s53, %[r0o]
s_add_u32 s82, %[hlo], s53
s_addc_u32 s83, %[hhi], 0
s_add_u32 s84, s82, 8192
s_addc_u32 s85, s83, 0
s_and_b32 s90, %[hlo], 0x1fffff
s_add_u32 s90, s90, s53
s_mov_b32 s91, 3072
v_mov_b32 v11, s90
v_mad_u32_u24 v11, v0, s91, v11
v_lshrrev_b32 v11, 21, v11
v_lshl_add_u32 v11, v11, 2, 64
v_writelane_b32 v11, %[tsel], 63
s_sub_u32 s53, s82, %[hlo]
s_lshr_b32 s53, s53, 2
s_and_b32 s90, %[xlo], 0x1fffff
s_add_u32 s53, s53, s90
s_add_u32 s53, s53, 0x20048000
s_lshr_b32 s53, s53, 21
s_lshl_b32 s90, s53, 2
v_writelane_b32 v11, s90, 48
global_load_dword v11, v11, s[68:69] offset:-2048
v_and_b32 v21, 3, v16
v_mul_u32_u24 v21, 0x300, v21
v_lshl_add_u32 v21, v18, 4, v21
s_mov_b32 s97, 3072
s_mov_b32 s50, %[pos0]
s_mov_b32 s51, %[pos0]
s_mov_b32 s52, %[pos0]
s_add_u32 s74, %[lds], 0
s_add_u32 s75, s74, 163840
s_mov_b32 s37, s74
s_mul_i32 s53, %[w], 6400
s_add_u32 s76, s74, s53
s_add_u32 s77, s76, 163840
s_mov_b32 s38, s76
s_lshl_b32 s60, %[w], 11
s_add_u32 s61, s60, 1024
s_add_u32 s62, s60, 16384
s_add_u32 s63, s60, 17408
s_add_u32 s64, s60, 32768
s_add_u32 s65, s60, 33792
v_lshlrev_b32 v23, 2, v16
s_lshl_b32 s53, %[w], 11
s_min_u32 s53, s53, 5120
s_add_u32 s72, s53, 1228800
s_add_u32 s40, %[glo], s72
s_addc_u32 s41, %[ghi], 0
s_add_u32 s53, s53, %[lds]
s_add_u32 m0, s53, 58368
s_nop 0
DW 0
DW 1024
s_nop 0 ;PS
s_mul_i32 s53, s50, 25600
s_add_u32 s40, %[wlo], s53
s_addc_u32 s41, %[whi], 0
s_mov_b32 m0, s38
s_nop 0
DW 0
DW 1024
DW 2048
s_add_u32 s40, s40, 3072
s_addc_u32 s41, s41, 0
s_add_u32 m0, s38, 3072
s_nop 0
DW 0
DW 1024
DW 2048
DQ 3072
s_add_u32 s50, s50, %[d]
s_cmp_eq_u32 s50, %[wrap]
s_cselect_b32 s50, %[rst], s50
s_add_u32 s38, s38, 32768
s_cmp_eq_u32 s38, s77
s_cselect_b32 s38, s76, s38
PF0
s_mov_b32 m0, %[pm]
s_nop 0
DF
PFA
v_add_u32 v1, s37, v0
v_lshl_add_u32 v19, v18, 2, v17
v_and_b32 v19, 15, v19
v_lshlrev_b32 v19, 4, v19
v_lshl_add_u32 v2, v18, 8, v19
s_lshl_b32 s53, %[w], 2
s_add_u32 s53, s53, %[lds]
s_add_u32 s53, s53, 123904
s_add_u32 s53, %[lds], 123904
v_bfe_u32 v4, v17, 1, 2
v_and_b32 v5, 8, v17
v_add3_u32 v4, v4, v5, %[w]
v_lshl_add_u32 v4, v18, 2, v4
v_and_b32 v4, 15, v4
v_lshl_add_u32 v4, v17, 4, v4
v_lshl_add_u32 v4, v4, 2, s53
s_lshl_b32 s53, %[w], 9
s_add_u32 s53, s53, %[lds]
s_add_u32 s53, s53, 123904
v_lshlrev_b32 v5, 3, v16
v_add_u32 v5, s53, v5
s_lshl_b32 s53, %[w], 6
v_and_b32 v6, 0x38, v16
v_add_u32 v6, s53, v6
s_add_u32 s53, %[lds], 58368
v_lshrrev_b32 v19, 2, v17
v_cmp_eq_u32 vcc, v19, v18
v_mov_b32 v19, 0xc00
s_nop 1
v_cndmask_b32 v7, v19, 0, vcc
v_lshl_add_u32 v7, v17, 4, v7
v_add_u32 v7, s53, v7
v_lshlrev_b32 v8, 4, v18
v_add_u32 v8, s53, v8
v_mov_b32 v10, 1
s_mov_b32 s36, 0xffff
)"
R"(s_mov_b32 s56, 0x5550555
s_mov_b32 s39, 32768
s_mov_b32 s54, 0x01010101
s_mov_b32 s55, 0x01010101
s_mov_b32 %[nb], 0
v_and_b32 v28, 12, v17
v_lshl_add_u32 v28, v28, 2, v17
v_lshlrev_b32 v28, 2, v28
v_mov_b32 v34, 0x74007400
v_mov_b32 v35, -1
v_and_b32 v22, 0xfffffcff, v1
s_mov_b32 s78, 0xaaaaaaaa
s_mov_b32 s79, 0xaaaaaaaa
s_mov_b32 s80, 0xcccccccc
s_mov_b32 s81, 0xcccccccc
v_accvgpr_write_b32 a240, 0
v_accvgpr_write_b32 a241, 0
v_accvgpr_write_b32 a246, 0
v_accvgpr_write_b32 a247, 0
v_accvgpr_write_b32 a248, 0
v_accvgpr_write_b32 a249, 0
v_accvgpr_write_b32 a254, 0
v_accvgpr_write_b32 a255, 0
WV 10
v_bcnt_u32_b32 v11, v11, 0
v_cmp_lt_u32 s[90:91], 2, v11
s_nop 0
s_lshr_b32 s96, s91, 31
s_xor_b32 s96, s96, %[s2]
s_sub_u32 s53, 0, s96
s_xor_b32 s90, s90, s53
s_xor_b32 s91, s91, s53
s_add_u32 s53, %[xo0], 0
s_add_u32 s46, %[xlo], s53
s_addc_u32 s47, %[xhi], 0
s_bitcmp1_b64 s[90:91], 48
s_cselect_b32 s58, 16, 0
s_lshr_b32 s58, 0xfe987610, s58
v_lshrrev_b32 v3, 4, v0
v_and_b32 v3, 12, v3
v_bfe_u32 v3, s58, v3, 4
v_mad_u32_u24 v3, v3, s97, v21
XL 0,0
XL 1,64
XL 2,128
XL 3,192
XL 4,256
XL 5,320
XL 6,384
XL 7,448
XL 8,512
XL 9,576
XL 10,640
XL 11,704
s_mul_i32 s53, s96, 8320
s_cmp_lt_u32 %[ca0], 128
s_cselect_b32 s53, s53, 0
s_add_u32 s73, s53, %[ca0]
v_mov_b32 v9, s73
WV 20
s_barrier
RV 40,8,6144
RV 44,8,6208
RV 48,8,6272
RA 242,7,0
RA 250,7,256
s_mul_i32 s53, s50, 25600
s_add_u32 s40, %[wlo], s53
s_addc_u32 s41, %[whi], 0
s_mov_b32 m0, s38
s_nop 0
DW 0
DW 1024
DW 2048
s_add_u32 s40, s40, 3072
s_addc_u32 s41, s41, 0
s_add_u32 m0, s38, 3072
s_nop 0
DW 0
DW 1024
DW 2048
DQ 3072
s_add_u32 s50, s50, %[d]
s_cmp_eq_u32 s50, %[wrap]
s_cselect_b32 s50, %[rst], s50
s_add_u32 s38, s38, 32768
s_cmp_eq_u32 s38, s77
s_cselect_b32 s38, s76, s38
s_and_b32 s53, %[w], 1
s_mul_i32 s53, s53, 3072
s_lshr_b32 s72, %[w], 1
s_lshl_b32 s40, s72, 16
s_add_u32 s40, s40, s53
s_add_u32 m0, s40, 25600
s_lshl_b32 s72, s72, 13
s_add_u32 s53, s53, s72
s_add_u32 s53, s53, 1382400
s_add_u32 s40, %[glo], s53
s_addc_u32 s41, %[ghi], 0
DW 0
DW 1024
DW 2048
DW 3072
WL 4
WV 22
LZ 40,0
WL 3
WV 21
LS 44,1
WL 2
RV 40,8,6336
WV 20
LS 48,2
RV 44,8,6400
s_mul_i32 s53, s50, 25600
s_add_u32 s40, %[wlo], s53
s_addc_u32 s41, %[whi], 0
s_mov_b32 m0, s38
WL 1
WV 19
LS 40,3
RV 48,8,6464
WL 1
WV 18
LS 44,4
RV 40,8,6528
DW 0
WL 1
WV 18
LS 48,5
RV 44,8,6592
WL 1
WV 17
LS 40,6
RV 48,8,6656
WL 1
WV 16
LS 44,7
RV 40,8,6720
DW 1024
WL 1
WV 16
LS 48,8
RV 44,8,6784
WL 1
WV 15
LS 40,9
RV 48,8,6848
DW 2048
WL 1
WV 15
LS 44,10
WL 0
WV 14
LS 48,11
s_nop 7
v_mul_f32 v36, 0xbb2aaaab, v36
v_mul_f32 v37, 0xbb2aaaab, v37
v_mul_f32 v38, 0xbb2aaaab, v38
v_mul_f32 v39, 0xbb2aaaab, v39
s_add_u32 s40, s40, 3072
s_addc_u32 s41, s41, 0
s_add_u32 m0, s38, 3072
LD 40,244,0
LD 44,240,0
RA 242,7,512
DW 0
LD 48,252,1
s_nop 3
CF 128,40,41
CF 129,42,43
LD 40,248,1
RA 250,7,768
CF 130,44,45
CF 131,46,47
DW 1024
WL 1
LD 44,244,2
CF 132,48,49
GZ 0
CF 133,50,51
LD 48,240,2
RA 242,7,1024
CF 134,40,41
CF 135,42,43
DW 2048
WL 1
LD 40,252,3
CF 136,44,45
GS 1
DQ 3072
CF 137,46,47
LD 44,248,3
RA 250,7,1280
CF 138,48,49
CF 139,50,51
WL 1
LD 48,244,4
CF 140,40,41
GS 2
s_add_u32 s50, s50, %[d]
s_cmp_eq_u32 s50, %[wrap]
s_cselect_b32 s50, %[rst], s50
CF 141,42,43
LD 40,240,4
WL 0
RA 242,7,1536
CF 142,44,45
CF 143,46,47
LD 44,252,5
s_add_u32 s38, s38, 32768
s_cmp_eq_u32 s38, s77
s_cselect_b32 s38, s76, s38
CF 144,48,49
GS 3
CF 145,50,51
LD 48,248,5
RA 250,7,1792
CF 146,40,41
CF 147,42,43
s_add_u32 s53, %[xo0], 0
s_add_u32 s46, %[xlo], s53
s_addc_u32 s47, %[xhi], 0
s_bitcmp1_b64 s[90:91], 48
s_cselect_b32 s58, 16, 0
s_lshr_b32 s58, 0xda43cb52, s58
v_lshrrev_b32 v3, 4, v0
v_and_b32 v3, 12, v3
v_bfe_u32 v3, s58, v3, 4
v_mad_u32_u24 v3, v3, s97, v21
WL 1
LD 40,244,6
CF 148,44,45
GS 4
CF 149,46,47
LD 44,240,6
WL 0
RA 242,7,2048
CF 150,48,49
CF 151,50,51
XL 12,0
LD 48,252,7
CF 152,40,41
GS 5
XL 13,64
CF 153,42,43
LD 40,248,7
RA 250,7,2304
CF 154,44,45
CF 155,46,47
WL 1
LD 44,244,8
CF 156,48,49
GS 6
XL 14,128
CF 157,50,51
LD 48,240,8
RA 242,7,2560
CF 158,40,41
CF 159,42,43
WL 1
LD 40,252,9
XL 15,192
CF 160,44,45
GS 7
CF 161,46,47
LD 44,248,9
RA 250,7,2816
CF 162,48,49
CF 163,50,51
WL 1
LD 48,244,10
XL 16,256
CF 164,40,41
GS 8
CF 165,42,43
LD 40,240,10
XL 17,320
CF 166,44,45
CF 167,46,47
WL 0
LD 44,252,11
CF 168,48,49
GS 9
XL 18,384
CF 169,50,51
LD 48,248,11
CF 170,40,41
CF 171,42,43
s_nop 1
GS 10
CF 172,44,45
CF 173,46,47
s_nop 0
CF 174,48,49
CF 175,50,51
XL 19,448
s_nop 0
GS 11
RV 36,8,6912
XL 20,512
WV 16
s_barrier
RA 216,1,0
RA 220,1,1024
RA 224,1,2048
RA 228,1,3072
RV 252,22,5120
v_cndmask_b32 v52, v52, v53, s[78:79]
v_cndmask_b32 v54, v54, v55, s[78:79]
v_cndmask_b32 v52, v52, v54, s[80:81]
v_mul_f32 v52, 0x3b2aaaab, v52
C 52,52,52
v_lshlrev_b32 v52, 16, v52
WL 5
v_add_f32 v52, v36, v52
C 52,52,52
v_lshlrev_b32 v52, 16, v52
v_sqrt_f32 v52, v52
XL 21,576
C 52,52,52
v_lshlrev_b32 v52, 16, v52
v_cmp_ge_f32 vcc, v37, v52
v_rcp_f32 v52, v52
s_nop 0
v_mul_f32 v52, v38, v52
v_cndmask_b32 v52, 0, v52, vcc
CF 52,52,52
XL 22,640
ds_bpermute_b32 v13, v28, v52
WL 0
XS 128
XS 129
XS 130
XS 131
XS 132
XS 133
XS 134
XS 135
XS 136
XS 137
XS 138
XS 139
XS 140
XS 141
XS 142
XS 143
XS 144
XS 145
XS 146
XS 147
XS 148
XS 149
XS 150
XS 151
XS 152
XS 153
XS 154
XS 155
XS 156
XS 157
XS 158
XS 159
XS 160
XS 161
XS 162
XS 163
XS 164
XS 165
XS 166
XS 167
XS 168
XS 169
XS 170
XS 171
XS 172
XS 173
XS 174
XS 175
v_min_u32 v35, v35, v13
XL 23,704
Z 0,0,0
M 0,1,1
s_mul_i32 s53, s50, 25600
RA 232,1,4096
M 0,2,2
s_add_u32 s40, %[wlo], s53
RA 236,1,6144
s_addc_u32 s41, %[whi], 0
s_mov_b32 m0, s38
M 0,3,3
RA 216,1,7168
DW 0
WL 2
M 0,4,4
RA 220,1,8192
WL 2
M 0,5,5
RA 224,1,9216
DW 1024
)"
R"(WL 2
M 0,6,0
RA 228,1,10240
WL 2
M 0,7,1
RA 232,1,11264
DW 2048
WL 2
M 0,8,2
RV 252,22,5392
RA 236,1,12288
WL 3
M 0,9,3
s_add_u32 s40, s40, 3072
RA 216,1,13312
s_addc_u32 s41, s41, 0
s_add_u32 m0, s38, 3072
WL 3
M 0,10,4
RA 220,1,14336
WL 2
M 0,11,5
RA 224,1,15360
DW 0
WL 2
Z 6,0,0
RA 228,1,16384
DW 1024
WL 2
M 6,1,1
RA 232,1,17408
WL 2
M 6,2,2
LL 16,56
LL 104,57
RA 236,1,18432
DW 2048
WL 2
M 6,3,3
s_add_u32 s37, s37, 32768
LA 16
LC_ 104
s_cmp_eq_u32 s37, s75
s_cselect_b32 s37, s74, s37
WL 3
M 6,4,4
WV 18
RA 216,1,19456
RA 220,1,20480
DQ 3072
WL 4
M 6,5,5
s_mul_i32 s53, s51, 49152
LL 17,58
LL 105,59
WL 1
M 6,6,0
s_bitcmp1_b64 s[90:91], s51
LA 17
LC_ 105
RA 224,1,21504
RA 228,1,22528
s_cselect_b64 s[44:45], s[84:85], s[82:83]
s_add_u32 s44, s44, s53
s_addc_u32 s45, s45, 0
s_add_u32 s51, s51, %[d]
s_cmp_eq_u32 s51, %[wrap]
s_cselect_b32 s51, %[rst], s51
s_add_u32 s50, s50, %[d]
s_cmp_eq_u32 s50, %[wrap]
s_cselect_b32 s50, %[rst], s50
WL 4
M 6,7,1
WL 0
LO 104,16,104
LU 16,104
HZ 24,16
M 6,8,2
s_add_u32 s38, s38, 32768
RA 232,1,23552
RA 236,1,24576
s_nop 1
v_add_u32 v1, s37, v0
s_cmp_eq_u32 s38, s77
s_cselect_b32 s38, s76, s38
M 6,9,3
WL 0
s_barrier
RA 216,1,0
v_and_b32 v22, 0xfffffcff, v1
M 6,10,4
LO 105,17,105
LU 17,105
HS 24,17
RA 220,1,1024
M 6,11,5
RV 252,22,5120
HX 24
WL 0
Z 0,0,0
WV 17
RA 224,1,2048
RA 228,1,3072
M 0,1,1
s_mul_i32 s53, s50, 25600
RA 232,1,4096
W1 24,0
RV 40,8,6144
RV 44,8,6208
RV 48,8,6272
RA 242,7,0
RA 250,7,256
RA 236,1,6144
s_add_u32 s40, %[wlo], s53
s_addc_u32 s41, %[whi], 0
s_mov_b32 m0, s38
WL 5
LZ 40,12
WL 4
LS 44,13
WV 16
RV 40,8,6336
WL 4
LS 48,14
WV 15
RV 44,8,6400
WL 1
LS 40,15
WV 14
RV 48,8,6464
WL 1
LS 44,16
WV 13
RV 40,8,6528
WL 1
LS 48,17
WV 12
RV 44,8,6592
WL 1
LS 40,18
WV 11
RV 48,8,6656
WL 1
LS 44,19
RV 40,8,6720
M 0,2,2
LL 18,80
LL 106,81
M 0,3,3
WV 10
LA 18
LC_ 106
DW 0
WL 3
LS 48,20
WV 10
RV 44,8,6784
RA 216,1,7168
RA 220,1,8192
WL 5
LS 40,21
WV 9
RV 48,8,6848
DW 1024
WL 3
LS 44,22
WL 0
WV 9
LS 48,23
s_nop 7
v_mul_f32 v36, 0xbb2aaaab, v36
v_mul_f32 v37, 0xbb2aaaab, v37
v_mul_f32 v38, 0xbb2aaaab, v38
v_mul_f32 v39, 0xbb2aaaab, v39
s_nop 1
LD 40,244,12
LD 44,240,12
RA 242,7,512
LD 48,252,13
s_nop 4
CF 176,40,41
CF 177,42,43
M 0,4,4
M 0,5,5
LL 19,82
LL 107,83
DW 2048
LD 40,248,13
RA 250,7,768
CF 178,44,45
CF 179,46,47
RA 224,1,9216
RA 228,1,10240
WL 3
LD 44,244,14
CF 180,48,49
GZ 12
s_add_u32 s40, s40, 3072
CF 181,50,51
s_addc_u32 s41, s41, 0
s_add_u32 m0, s38, 3072
LD 48,240,14
WL 2
RA 242,7,1024
CF 182,40,41
CF 183,42,43
LD 40,252,15
CF 184,44,45
GS 13
CF 185,46,47
LD 44,248,15
RA 250,7,1280
CF 186,48,49
CF 187,50,51
WL 1
LD 48,244,16
CF 188,40,41
M 0,6,0
LA 19
LC_ 107
M 0,7,1
LO 106,18,106
LU 18,106
HZ 25,18
DW 0
GS 14
RA 232,1,11264
CF 189,42,43
LD 40,240,16
WL 3
RA 242,7,1536
CF 190,44,45
CF 191,46,47
LD 44,252,17
CF 192,48,49
GS 15
DW 1024
CF 193,50,51
LD 48,248,17
RA 250,7,1792
CF 194,40,41
CF 195,42,43
WL 1
LD 40,244,18
CF 196,44,45
GS 16
CF 197,46,47
LD 44,240,18
RA 242,7,2048
CF 198,48,49
CF 199,50,51
WL 1
LD 48,252,19
CF 200,40,41
M 0,8,2
RV 252,22,5392
RA 236,1,12288
M 0,9,3
RA 216,1,13312
DW 2048
GS 17
RA 220,1,14336
CF 201,42,43
s_add_u32 s42, s44, s60
s_addc_u32 s43, s45, 0
LD 40,248,19
WL 4
RA 250,7,2304
CF 202,44,45
CF 203,46,47
LD 44,244,20
CF 204,48,49
GS 18
CF 205,50,51
LD 48,240,20
RA 242,7,2560
CF 206,40,41
CF 207,42,43
DQ 3072
WL 1
LD 40,252,21
CF 208,44,45
GS 19
CF 209,46,47
LD 44,248,21
RA 250,7,2816
CF 210,48,49
CF 211,50,51
M 0,10,4
WL 1
LO 107,19,107
LU 19,107
HS 25,19
M 0,11,5
s_add_u32 s50, s50, %[d]
RA 224,1,15360
HX 25
s_cmp_eq_u32 s50, %[wrap]
s_cselect_b32 s50, %[rst], s50
LD 48,244,22
RA 228,1,16384
CF 212,40,41
ST 104
GS 20
CF 213,42,43
LD 40,240,22
WL 2
CF 214,44,45
CF 215,46,47
LD 44,252,23
CF 216,48,49
GS 21
CF 217,50,51
LD 48,248,23
s_add_u32 s38, s38, 32768
CF 218,40,41
CF 219,42,43
s_cmp_eq_u32 s38, s77
s_cselect_b32 s38, s76, s38
GS 22
CF 220,44,45
CF 221,46,47
CF 222,48,49
CF 223,50,51
Z 6,0,0
W1 25,1024
M 6,1,1
s_add_u32 s53, %[xo0], 196608
RA 232,1,17408
v_add_u32 v4, s39, v4
s_add_u32 s46, %[xlo], s53
s_addc_u32 s47, %[xhi], 0
s_bitcmp1_b64 s[90:91], 48
s_cselect_b32 s58, 16, 0
s_lshr_b32 s58, 0xfe987610, s58
v_lshrrev_b32 v3, 4, v0
v_and_b32 v3, 12, v3
v_bfe_u32 v3, s58, v3, 4
v_mad_u32_u24 v3, v3, s97, v21
GS 23
RA 236,1,18432
RV 36,8,6912
s_nop 5
v_cndmask_b32 v52, v52, v53, s[78:79]
v_cndmask_b32 v54, v54, v55, s[78:79]
v_cndmask_b32 v52, v52, v54, s[80:81]
v_mul_f32 v52, 0x3b2aaaab, v52
C 52,52,52
v_lshlrev_b32 v52, 16, v52
WL 0
v_add_f32 v52, v36, v52
C 52,52,52
v_lshlrev_b32 v52, 16, v52
v_sqrt_f32 v52, v52
s_nop 0
C 52,52,52
v_lshlrev_b32 v52, 16, v52
v_cmp_ge_f32 vcc, v37, v52
v_rcp_f32 v52, v52
s_nop 0
v_mul_f32 v52, v38, v52
v_cndmask_b32 v52, 0, v52, vcc
CF 52,52,52
XL 24,0
ds_bpermute_b32 v13, v28, v52
M 6,2,2
s_add_u32 s37, s37, 32768
LL 16,56
LL 104,57
M 6,3,3
s_cmp_eq_u32 s37, s75
LA 16
LC_ 104
s_cselect_b32 s37, s74, s37
XL 25,64
M 6,4,4
WV 10
RA 216,1,19456
RA 220,1,20480
XL 26,128
M 6,5,5
WL 4
LL 17,58
LL 105,59
XL 27,192
XS 176
XS 177
XS 178
XS 179
XS 180
XS 181
XS 182
XS 183
XS 184
XS 185
XS 186
XS 187
XS 188
XS 189
XS 190
)"
R"(XS 191
XS 192
XS 193
XS 194
XS 195
XS 196
XS 197
XS 198
XS 199
XS 200
XS 201
XS 202
XS 203
XS 204
XS 205
XS 206
XS 207
WL 1
M 6,6,0
s_mul_i32 s53, s51, 49152
LA 17
LC_ 105
RA 224,1,21504
RA 228,1,22528
s_bitcmp1_b64 s[90:91], s51
s_cselect_b64 s[44:45], s[84:85], s[82:83]
s_add_u32 s44, s44, s53
s_addc_u32 s45, s45, 0
s_add_u32 s51, s51, %[d]
s_cmp_eq_u32 s51, %[wrap]
s_cselect_b32 s51, %[rst], s51
XL 28,256
XS 208
WL 4
M 6,7,1
WL 0
LO 104,16,104
LU 16,104
HZ 24,16
M 6,8,2
s_lshl_b32 s53, s52, 8
RA 232,1,23552
RA 236,1,24576
s_nop 1
v_add_u32 v1, s37, v0
s_add_u32 s48, %[alo], s53
s_addc_u32 s49, %[ahi], 0
s_add_u32 s52, s52, %[d]
s_cmp_eq_u32 s52, %[wrap]
s_cselect_b32 s52, %[rst], s52
XL 29,320
XS 209
M 6,9,3
WL 0
s_barrier
RA 216,1,0
v_and_b32 v22, 0xfffffcff, v1
XS 210
M 6,10,4
LO 105,17,105
LU 17,105
HS 24,17
RA 220,1,1024
s_sub_u32 s39, 0, s39
XS 211
M 6,11,5
RV 252,22,5120
ds_read_b64 v[14:15], v5 offset:0
HX 24
WL 1
Z 0,0,0
RA 224,1,2048
v_subrev_u32 v5, s39, v5
M 0,1,1
s_mul_i32 s53, s50, 25600
RA 228,1,3072
XS 212
Z 1,12,0
s_add_u32 s40, %[wlo], s53
LL 18,80
LL 106,81
M 1,13,1
s_addc_u32 s41, %[whi], 0
LA 18
LC_ 106
W1 24,0
XS 213
WL 4
M 0,2,2
s_mov_b32 m0, s38
RA 232,1,4096
v_add_f32 v14, v14, v15
XS 214
WL 4
M 0,3,3
LL 19,82
LL 107,83
M 1,14,2
LA 19
LC_ 107
RA 236,1,6144
DW 0
XS 215
M 1,15,3
WL 3
LO 106,18,106
LU 18,106
HZ 25,18
v_add_f32_dpp v14, v14, v14 quad_perm:[1,0,3,2] row_mask:0xf bank_mask:0xf
XS 216
M 0,4,4
RA 216,1,7168
v_add_f32_dpp v14, v14, v14 quad_perm:[2,3,0,1] row_mask:0xf bank_mask:0xf
WL 1
M 0,5,5
RA 220,1,8192
v_add_f32_dpp v14, v14, v14 row_half_mirror row_mask:0xf bank_mask:0xf
DW 1024
XS 217
M 1,16,4
LO 107,19,107
LU 19,107
HS 25,19
v_cvt_f64_f32 v[14:15], v14
XS 218
M 1,17,5
WL 1
RA 224,1,9216
s_mov_b64 exec, s[54:55]
SP
s_mov_b64 exec, -1
HX 25
DW 2048
XS 219
M 0,6,0
s_add_u32 s42, s44, s60
RA 228,1,10240
s_addc_u32 s43, s45, 0
XS 220
WL 2
M 0,7,1
RA 232,1,11264
XS 221
M 1,18,0
RV 252,22,5392
ST 104
s_add_u32 s40, s40, 3072
s_addc_u32 s41, s41, 0
s_add_u32 m0, s38, 3072
XS 222
M 1,19,1
WL 3
RA 236,1,12288
W1 25,1024
XS 223
M 0,8,2
WL 4
DW 0
v_add_u32 v4, s39, v4
M 0,9,3
RA 216,1,13312
v_min_u32 v35, v35, v13
M 1,20,2
DW 1024
M 1,21,3
WL 4
RA 220,1,14336
M 0,10,4
WL 3
DW 2048
M 0,11,5
RA 224,1,15360
M 1,22,4
DQ 3072
M 1,23,5
WL 2
RA 228,1,16384
Z 6,0,0
s_nop 0
LL 16,56
LL 104,57
WL 2
M 6,1,1
s_add_u32 s50, s50, %[d]
LA 16
RA 232,1,17408
Z 7,12,0
s_cmp_eq_u32 s50, %[wrap]
LC_ 104
M 7,13,1
s_cselect_b32 s50, %[rst], s50
RA 236,1,18432
WL 5
M 6,2,2
s_add_u32 s38, s38, 32768
WL 4
M 6,3,3
s_cmp_eq_u32 s38, s77
RA 216,1,19456
M 7,14,2
s_cselect_b32 s38, s76, s38
LL 17,58
LL 105,59
M 7,15,3
s_add_u32 s37, s37, 32768
LA 17
RA 220,1,20480
s_cmp_eq_u32 s37, s75
s_cselect_b32 s37, s74, s37
WL 5
M 6,4,4
WL 3
LC_ 105
XL 30,384
M 6,5,5
RA 224,1,21504
M 7,16,4
LO 104,16,104
LU 16,104
M 7,17,5
WL 2
RA 228,1,22528
HZ 24,16
XL 31,448
M 6,6,0
s_mul_i32 s53, s51, 49152
LL 18,60
LL 108,61
s_bitcmp1_b64 s[90:91], s51
s_cselect_b64 s[44:45], s[84:85], s[82:83]
s_add_u32 s44, s44, s53
s_addc_u32 s45, s45, 0
s_add_u32 s51, s51, %[d]
s_cmp_eq_u32 s51, %[wrap]
s_cselect_b32 s51, %[rst], s51
M 6,7,1
LA 18
RA 232,1,23552
M 7,18,0
LC_ 108
XL 32,512
M 7,19,1
WL 4
RA 236,1,24576
s_nop 1
v_add_u32 v1, s37, v0
M 6,8,2
WL 4
LO 105,17,105
LU 17,105
XL 33,576
M 6,9,3
RA 216,1,0
HS 24,17
M 7,20,2
LL 19,62
LL 109,63
M 7,21,3
WL 1
LA 19
XL 34,640
M 6,10,4
s_lshl_b32 s53, s52, 8
LC_ 109
v_and_b32 v22, 0xfffffcff, v1
M 6,11,5
s_add_u32 s48, %[alo], s53
RA 220,1,1024
M 7,22,4
WV 19
LO 108,18,108
LU 18,108
s_addc_u32 s49, %[ahi], 0
s_add_u32 s52, s52, %[d]
s_cmp_eq_u32 s52, %[wrap]
s_cselect_b32 s52, %[rst], s52
XL 35,704
M 7,23,5
s_barrier
RV 252,22,5120
HS 24,18
WL 0
Z 0,0,0
s_sub_u32 s39, 0, s39
LL 16,80
LL 106,81
RA 224,1,2048
M 0,1,1
s_mul_i32 s53, s50, 25600
LA 16
RA 228,1,3072
RV 40,8,6144
RV 44,8,6208
RV 48,8,6272
RA 242,7,0
RA 250,7,256
WL 4
LZ 40,24
WV 19
RA 232,1,4096
WL 4
LS 44,25
WV 18
RV 40,8,6336
WL 4
LS 48,26
WV 17
RV 44,8,6400
Z 1,12,0
s_add_u32 s40, %[wlo], s53
LC_ 106
ds_read_b64 v[14:15], v5 offset:0
M 1,13,1
s_addc_u32 s41, %[whi], 0
RA 236,1,6144
v_subrev_u32 v5, s39, v5
WL 4
LS 40,27
WV 16
RV 48,8,6464
s_mov_b32 m0, s38
WL 4
LS 44,28
WV 15
RV 40,8,6528
WL 1
LS 48,29
RV 44,8,6592
M 0,2,2
LO 109,19,109
LU 19,109
M 0,3,3
RA 216,1,7168
HS 24,19
WL 2
WV 5
LS 40,30
RV 48,8,6656
WL 2
WV 4
LS 44,31
RV 40,8,6720
DW 0
WL 1
WV 4
LS 48,32
RV 44,8,6784
M 1,14,2
LL 17,82
LL 107,83
M 1,15,3
LA 17
RA 220,1,8192
WL 3
WV 3
LS 40,33
RV 48,8,6848
v_add_f32 v14, v14, v15
s_nop 1
v_add_f32_dpp v14, v14, v14 quad_perm:[1,0,3,2] row_mask:0xf bank_mask:0xf
s_nop 1
v_add_f32_dpp v14, v14, v14 quad_perm:[2,3,0,1] row_mask:0xf bank_mask:0xf
WL 3
WV 2
LS 44,34
DW 1024
v_add_f32_dpp v14, v14, v14 row_half_mirror row_mask:0xf bank_mask:0xf
WL 0
WV 2
LS 48,35
v_cvt_f64_f32 v[14:15], v14
M 0,4,4
LC_ 107
s_mov_b64 exec, s[54:55]
SP
s_mov_b64 exec, -1
M 0,5,5
RA 224,1,9216
HX 24
v_mul_f32 v36, 0xbb2aaaab, v36
v_mul_f32 v37, 0xbb2aaaab, v37
v_mul_f32 v38, 0xbb2aaaab, v38
v_mul_f32 v39, 0xbb2aaaab, v39
W1 24,0
s_nop 0
LD 40,244,24
DW 2048
LD 44,240,24
RA 242,7,512
LD 48,252,25
s_nop 3
CF 224,40,41
CF 225,42,43
M 1,16,4
LO 106,16,106
LU 16,106
M 1,17,5
s_add_u32 s40, s40, 3072
RA 228,1,10240
HZ 25,16
)"
R"(LD 40,248,25
WL 1
RA 250,7,768
CF 226,44,45
CF 227,46,47
LD 44,244,26
s_addc_u32 s41, s41, 0
CF 228,48,49
GZ 24
s_add_u32 m0, s38, 3072
CF 229,50,51
M 0,6,0
LL 18,84
LL 110,85
M 0,7,1
LA 18
RA 232,1,11264
LD 48,240,26
WL 2
RA 242,7,1024
CF 230,40,41
CF 231,42,43
LD 40,252,27
DW 0
CF 232,44,45
GS 25
CF 233,46,47
M 1,18,0
LC_ 110
RV 252,22,5392
M 1,19,1
s_add_u32 s42, s44, s60
RA 236,1,12288
LD 44,248,27
WL 3
RA 250,7,1280
CF 234,48,49
CF 235,50,51
LD 48,244,28
s_addc_u32 s43, s45, 0
CF 236,40,41
GS 26
CF 237,42,43
M 0,8,2
LO 107,17,107
LU 17,107
M 0,9,3
RA 216,1,13312
HS 25,17
LD 40,240,28
WL 1
RA 242,7,1536
CF 238,44,45
CF 239,46,47
LD 44,252,29
DW 1024
CF 240,48,49
GS 27
ST 104
CF 241,50,51
M 1,20,2
LL 19,86
LL 111,87
M 1,21,3
LA 19
RA 220,1,14336
LD 48,248,29
WL 2
RA 250,7,1792
CF 242,40,41
CF 243,42,43
LD 40,244,30
DW 2048
CF 244,44,45
GS 28
CF 245,46,47
LD 44,240,30
RA 242,7,2048
CF 246,48,49
CF 247,50,51
M 0,10,4
LC_ 111
M 0,11,5
WL 2
RA 224,1,15360
LD 48,252,31
DQ 3072
CF 248,40,41
GS 29
CF 249,42,43
LD 40,248,31
RA 250,7,2304
CF 250,44,45
CF 251,46,47
M 1,22,4
LO 110,18,110
LU 18,110
M 1,23,5
WL 3
RA 228,1,16384
HS 25,18
LD 44,244,32
s_add_u32 s50, s50, %[d]
CF 48,48,49
CF 50,50,51
GS 30
s_cmp_eq_u32 s50, %[wrap]
AW 0,48
AW 1,50
LD 48,240,32
RA 242,7,2560
CF 40,40,41
CF 42,42,43
AW 2,40
AW 3,42
Z 6,0,0
s_cselect_b32 s50, %[rst], s50
LL 16,56
LL 104,57
M 6,1,1
WL 2
LA 16
RA 232,1,17408
LD 40,252,33
CF 44,44,45
CF 46,46,47
GS 31
AW 4,44
AW 5,46
LD 44,248,33
WL 2
RA 250,7,2816
CF 48,48,49
CF 50,50,51
AW 6,48
AW 7,50
Z 7,12,0
s_add_u32 s38, s38, 32768
LC_ 104
M 7,13,1
s_cmp_eq_u32 s38, s77
RA 236,1,18432
LD 48,244,34
s_cselect_b32 s38, s76, s38
CF 40,40,41
CF 42,42,43
GS 32
s_add_u32 s42, s44, s61
AW 8,40
AW 9,42
LD 40,240,34
s_addc_u32 s43, s45, 0
CF 44,44,45
CF 46,46,47
AW 10,44
AW 11,46
WL 2
LD 44,252,35
CF 48,48,49
CF 50,50,51
M 6,2,2
LO 111,19,111
LU 19,111
M 6,3,3
s_add_u32 s37, s37, 32768
RA 216,1,19456
HS 25,19
GS 33
s_cmp_eq_u32 s37, s75
AW 12,48
AW 13,50
LD 48,248,35
s_cselect_b32 s37, s74, s37
CF 40,40,41
CF 42,42,43
AW 14,40
AW 15,42
s_nop 1
GS 34
s_add_u32 s53, %[xo0], 196608
CF 44,44,45
CF 46,46,47
AW 16,44
AW 17,46
M 7,14,2
s_add_u32 s46, %[xlo], s53
LL 17,58
LL 105,59
M 7,15,3
s_addc_u32 s47, %[xhi], 0
LA 17
RA 220,1,20480
s_bitcmp1_b64 s[90:91], 48
s_cselect_b32 s58, 16, 0
s_lshr_b32 s58, 0xda43cb52, s58
v_lshrrev_b32 v3, 4, v0
v_and_b32 v3, 12, v3
v_bfe_u32 v3, s58, v3, 4
v_mad_u32_u24 v3, v3, s97, v21
CF 48,48,49
CF 50,50,51
AW 18,48
AW 19,50
HX 25
ST 108
GS 35
XL 36,0
RV 36,8,6912
M 6,4,4
WL 4
LC_ 105
W1 25,1024
M 6,5,5
s_mul_i32 s53, s51, 49152
RA 224,1,21504
v_add_u32 v4, s39, v4
v_cndmask_b32 v52, v52, v53, s[78:79]
v_cndmask_b32 v54, v54, v55, s[78:79]
v_cndmask_b32 v52, v52, v54, s[80:81]
v_mul_f32 v52, 0x3b2aaaab, v52
C 52,52,52
v_lshlrev_b32 v52, 16, v52
WL 3
v_add_f32 v52, v36, v52
C 52,52,52
v_lshlrev_b32 v52, 16, v52
v_sqrt_f32 v52, v52
M 7,16,4
LO 104,16,104
LU 16,104
M 7,17,5
s_bitcmp1_b64 s[90:91], s51
RA 228,1,22528
HZ 24,16
C 52,52,52
v_lshlrev_b32 v52, 16, v52
v_cmp_ge_f32 vcc, v37, v52
v_rcp_f32 v52, v52
s_cselect_b64 s[44:45], s[84:85], s[82:83]
s_add_u32 s44, s44, s53
s_addc_u32 s45, s45, 0
s_add_u32 s51, s51, %[d]
s_cmp_eq_u32 s51, %[wrap]
s_cselect_b32 s51, %[rst], s51
XL 37,64
v_mul_f32 v52, v38, v52
v_cndmask_b32 v52, 0, v52, vcc
CF 52,52,52
ds_bpermute_b32 v13, v28, v52
M 6,6,0
LL 18,60
LL 108,61
M 6,7,1
LA 18
RA 232,1,23552
M 7,18,0
LC_ 108
XL 38,128
M 7,19,1
RA 236,1,24576
s_nop 1
v_add_u32 v1, s37, v0
WL 4
XS 224
XS 225
XS 226
XS 227
XS 228
XS 229
XS 230
XS 231
XS 232
XS 233
XS 234
XS 235
XS 236
XS 237
XS 238
XS 239
XS 240
XS 241
XS 242
XS 243
XS 244
XS 245
XS 246
XS 247
XS 248
XS 249
XS 250
XS 251
AR 52,0
AR 53,1
AR 54,2
AR 55,3
XS 52
XS 53
XS 54
XS 55
AW 0,52
AW 1,53
AW 2,54
AW 3,55
AR 52,4
AR 53,5
AR 54,6
AR 55,7
XS 52
XS 53
XS 54
M 6,8,2
LO 105,17,105
LU 17,105
XS 55
M 6,9,3
RA 216,1,0
HS 24,17
XL 39,192
AW 4,52
M 7,20,2
LL 19,62
LL 109,63
AW 5,53
M 7,21,3
WL 1
LA 19
XL 40,256
AW 6,54
M 6,10,4
s_lshl_b32 s53, s52, 8
LC_ 109
v_and_b32 v22, 0xfffffcff, v1
AW 7,55
M 6,11,5
s_add_u32 s48, %[alo], s53
RA 220,1,1024
AR 52,8
M 7,22,4
LO 108,18,108
LU 18,108
s_addc_u32 s49, %[ahi], 0
s_add_u32 s52, s52, %[d]
s_cmp_eq_u32 s52, %[wrap]
s_cselect_b32 s52, %[rst], s52
XL 41,320
AR 53,9
AR 54,10
M 7,23,5
s_barrier
RV 252,22,5120
HS 24,18
AR 55,11
WL 0
Z 0,0,0
s_sub_u32 s39, 0, s39
LL 16,80
LL 106,81
M 0,1,1
s_mul_i32 s53, s50, 25600
LA 16
RA 224,1,2048
Z 1,12,0
s_add_u32 s40, %[wlo], s53
LC_ 106
RA 228,1,3072
XS 52
M 1,13,1
s_addc_u32 s41, %[whi], 0
RA 232,1,4096
XS 53
Z 2,24,0
WL 1
LO 109,19,109
LU 19,109
XS 54
M 2,25,1
s_mov_b32 m0, s38
RA 236,1,6144
HS 24,19
ds_read_b64 v[14:15], v5 offset:0
XS 55
M 0,2,2
LL 17,82
LL 107,83
AW 8,52
M 0,3,3
LA 17
DW 0
AW 9,53
M 1,14,2
LC_ 107
v_subrev_u32 v5, s39, v5
AW 10,54
AW 11,55
M 1,15,3
RA 216,1,7168
HX 24
AR 52,12
M 2,26,2
WL 4
)"
R"(LO 106,16,106
LU 16,106
AR 53,13
M 2,27,3
RA 220,1,8192
HZ 25,16
AR 54,14
M 0,4,4
LL 18,84
LL 110,85
W1 24,0
AR 55,15
M 0,5,5
LA 18
DW 1024
XS 52
M 1,16,4
WL 6
LC_ 110
DW 2048
XS 53
M 1,17,5
s_add_u32 s42, s44, s60
RA 224,1,9216
v_add_f32 v14, v14, v15
s_nop 1
v_add_f32_dpp v14, v14, v14 quad_perm:[1,0,3,2] row_mask:0xf bank_mask:0xf
XS 54
M 2,28,4
WL 4
LO 107,17,107
LU 17,107
XS 55
AW 12,52
M 2,29,5
s_addc_u32 s43, s45, 0
RA 228,1,10240
HS 25,17
v_add_f32_dpp v14, v14, v14 quad_perm:[2,3,0,1] row_mask:0xf bank_mask:0xf
AW 13,53
M 0,6,0
LL 19,86
LL 111,87
AW 14,54
M 0,7,1
LA 19
ST 104
v_add_f32_dpp v14, v14, v14 row_half_mirror row_mask:0xf bank_mask:0xf
AW 15,55
M 1,18,0
s_add_u32 s40, s40, 3072
LC_ 111
v_cvt_f64_f32 v[14:15], v14
AR 52,16
M 1,19,1
s_addc_u32 s41, s41, 0
RA 232,1,11264
AR 53,17
M 2,30,0
WL 3
LO 110,18,110
LU 18,110
s_add_u32 m0, s38, 3072
AR 54,18
M 2,31,1
RV 252,22,5392
HS 25,18
s_mov_b64 exec, s[54:55]
SP
s_mov_b64 exec, -1
AR 55,19
M 0,8,2
RA 236,1,12288
XS 52
XS 53
M 0,9,3
DW 0
XS 54
M 1,20,2
DW 1024
XS 55
M 1,21,3
s_add_u32 s42, s44, s61
RA 216,1,13312
AW 16,52
M 2,32,2
WL 1
LO 111,19,111
LU 19,111
AW 17,53
M 2,33,3
s_addc_u32 s43, s45, 0
RA 220,1,14336
HS 25,19
AW 18,54
M 0,10,4
ST 108
HX 25
AW 19,55
M 0,11,5
DW 2048
v_min_u32 v35, v35, v13
M 1,22,4
W1 25,1024
M 1,23,5
RA 224,1,15360
v_add_u32 v4, s39, v4
M 2,34,4
WL 2
LL 16,56
LL 104,57
M 2,35,5
LA 16
RA 228,1,16384
Z 6,0,0
LC_ 104
DQ 3072
M 6,1,1
Z 7,12,0
M 7,13,1
s_add_u32 s50, s50, %[d]
RA 232,1,17408
Z 8,24,0
WL 2
LL 17,58
LL 105,59
M 8,25,1
s_cmp_eq_u32 s50, %[wrap]
LA 17
RA 236,1,18432
M 6,2,2
s_cselect_b32 s50, %[rst], s50
LC_ 105
M 6,3,3
s_add_u32 s38, s38, 32768
M 7,14,2
WL 4
LO 104,16,104
LU 16,104
M 7,15,3
s_cmp_eq_u32 s38, s77
RA 216,1,19456
HZ 24,16
M 8,26,2
WL 2
LL 18,60
LL 108,61
M 8,27,3
s_cselect_b32 s38, s76, s38
LA 18
RA 220,1,20480
M 6,4,4
s_add_u32 s37, s37, 32768
LC_ 108
s_cmp_eq_u32 s37, s75
s_cselect_b32 s37, s74, s37
M 6,5,5
XL 42,384
M 7,16,4
WL 4
LO 105,17,105
LU 17,105
M 7,17,5
s_lshl_b32 s53, s52, 8
RA 224,1,21504
HS 24,17
M 8,28,4
WL 2
LL 19,62
LL 109,63
M 8,29,5
s_add_u32 s48, %[alo], s53
LA 19
RA 228,1,22528
M 6,6,0
s_addc_u32 s49, %[ahi], 0
LC_ 109
s_add_u32 s52, s52, %[d]
s_cmp_eq_u32 s52, %[wrap]
s_cselect_b32 s52, %[rst], s52
s_mul_i32 s53, s51, 49152
s_bitcmp1_b64 s[90:91], s51
s_cselect_b64 s[44:45], s[84:85], s[82:83]
s_add_u32 s44, s44, s53
s_addc_u32 s45, s45, 0
s_add_u32 s51, s51, %[d]
s_cmp_eq_u32 s51, %[wrap]
s_cselect_b32 s51, %[rst], s51
M 6,7,1
XL 43,448
M 7,18,0
WL 4
LO 108,18,108
LU 18,108
M 7,19,1
RA 232,1,23552
HS 24,18
M 8,30,0
WL 2
LL 16,64
LL 112,65
M 8,31,1
LA 16
RA 236,1,24576
XL 44,512
M 6,8,2
LC_ 112
XL 45,576
M 6,9,3
v_add_u32 v1, s37, v0
M 7,20,2
WL 4
LO 109,19,109
LU 19,109
M 7,21,3
RA 216,1,0
HS 24,19
M 8,32,2
WL 2
LL 17,66
LL 113,67
M 8,33,3
WV 21
LA 17
XL 46,640
M 6,10,4
s_barrier
LC_ 113
v_and_b32 v22, 0xfffffcff, v1
M 6,11,5
s_sub_u32 s39, 0, s39
RA 220,1,1024
M 7,22,4
WL 4
LO 112,16,112
LU 16,112
M 7,23,5
RV 252,22,5120
HS 24,16
M 8,34,4
WL 1
LL 18,80
LL 106,81
M 8,35,5
LA 18
RA 224,1,2048
WL 2
Z 0,0,0
WV 20
LC_ 106
RA 228,1,3072
ds_read_b64 v[14:15], v5 offset:0
XL 47,704
M 0,1,1
s_mul_i32 s53, s50, 25600
v_subrev_u32 v5, s39, v5
RV 40,8,6144
RV 44,8,6208
RV 48,8,6272
RA 242,7,0
RA 250,7,256
WL 4
LZ 40,36
s_add_u32 s40, %[wlo], s53
Z 1,12,0
WV 19
LO 113,17,113
LU 17,113
M 1,13,1
s_addc_u32 s41, %[whi], 0
RA 232,1,4096
HS 24,17
WL 4
LS 44,37
WL 3
RV 40,8,6336
LS 48,38
RV 44,8,6400
Z 2,24,0
WV 18
LL 19,82
LL 107,83
M 2,25,1
s_mov_b32 m0, s38
LA 19
RA 236,1,6144
WL 3
LS 40,39
WV 17
RV 48,8,6464
HX 24
WL 3
LS 44,40
RV 40,8,6528
M 0,2,2
LC_ 107
DW 0
M 0,3,3
WV 17
W1 24,0
WL 3
LS 48,41
RV 44,8,6592
WL 3
WV 6
LS 40,42
RV 48,8,6656
M 1,14,2
LO 106,18,106
LU 18,106
M 1,15,3
RA 216,1,7168
HZ 25,18
WL 2
WV 5
LS 44,43
RV 40,8,6720
WL 2
WV 4
LS 48,44
RV 44,8,6784
M 2,26,2
LL 16,84
LL 110,85
M 2,27,3
LA 16
RA 220,1,8192
WL 3
WV 3
LS 40,45
RV 48,8,6848
v_add_f32 v14, v14, v15
s_nop 1
v_add_f32_dpp v14, v14, v14 quad_perm:[1,0,3,2] row_mask:0xf bank_mask:0xf
s_nop 1
v_add_f32_dpp v14, v14, v14 quad_perm:[2,3,0,1] row_mask:0xf bank_mask:0xf
WL 3
WV 2
LS 44,46
DW 1024
v_add_f32_dpp v14, v14, v14 row_half_mirror row_mask:0xf bank_mask:0xf
M 0,4,4
LC_ 110
v_cvt_f64_f32 v[14:15], v14
M 0,5,5
WL 1
DW 2048
WV 3
LS 48,47
s_mov_b64 exec, s[54:55]
SP
s_mov_b64 exec, -1
s_nop 4
v_mul_f32 v36, 0xbb2aaaab, v36
v_mul_f32 v37, 0xbb2aaaab, v37
v_mul_f32 v38, 0xbb2aaaab, v38
v_mul_f32 v39, 0xbb2aaaab, v39
M 1,16,4
LO 107,19,107
LU 19,107
M 1,17,5
s_add_u32 s42, s44, s60
RA 224,1,9216
HS 25,19
LD 40,244,36
s_addc_u32 s43, s45, 0
LD 44,240,36
RA 242,7,512
M 2,28,4
LL 17,86
LL 111,87
M 2,29,5
LA 17
RA 228,1,10240
LD 48,252,37
ST 104
CF 40,40,41
CF 42,42,43
AW 20,40
AW 21,42
LD 40,248,37
WL 2
RA 250,7,768
CF 44,44,45
CF 46,46,47
AW 22,44
AW 23,46
M 0,6,0
s_add_u32 s40, s40, 3072
LC_ 111
M 0,7,1
s_addc_u32 s41, s41, 0
LD 44,244,38
s_add_u32 m0, s38, 3072
CF 48,48,49
CF 50,50,51
GZ 36
AW 24,48
AW 25,50
M 1,18,0
)"
R"(LO 110,16,110
LU 16,110
M 1,19,1
RA 232,1,11264
HS 25,16
LD 48,240,38
WL 2
RA 242,7,1024
CF 40,40,41
CF 42,42,43
AW 26,40
AW 27,42
LD 40,252,39
DW 0
CF 44,44,45
CF 46,46,47
M 2,30,0
LL 18,88
LL 114,89
M 2,31,1
LA 18
RV 252,22,5392
GS 37
RA 236,1,12288
AW 28,44
AW 29,46
LD 44,248,39
RA 250,7,1280
CF 48,48,49
CF 50,50,51
AW 30,48
AW 31,50
M 0,8,2
LC_ 114
M 0,9,3
WL 5
DW 1024
LD 48,244,40
s_add_u32 s42, s44, s61
CF 40,40,41
CF 42,42,43
GS 38
s_addc_u32 s43, s45, 0
AW 32,40
AW 33,42
M 1,20,2
LO 111,17,111
LU 17,111
M 1,21,3
RA 216,1,13312
HS 25,17
LD 40,240,40
WL 2
RA 242,7,1536
CF 44,44,45
CF 46,46,47
AW 34,44
AW 35,46
LD 44,252,41
ST 108
CF 48,48,49
CF 50,50,51
M 2,32,2
LL 19,90
LL 115,91
M 2,33,3
LA 19
RA 220,1,14336
GS 39
DW 2048
AW 36,48
AW 37,50
LD 48,248,41
RA 250,7,1792
CF 40,40,41
CF 42,42,43
AW 38,40
AW 39,42
M 0,10,4
LC_ 115
M 0,11,5
WL 4
LD 40,244,42
CF 44,44,45
CF 46,46,47
GS 40
AW 40,44
AW 41,46
M 1,22,4
LO 114,18,114
LU 18,114
M 1,23,5
RA 224,1,15360
HS 25,18
LD 44,240,42
WL 2
RA 242,7,2048
CF 48,48,49
CF 50,50,51
AW 42,48
AW 43,50
LD 48,252,43
DQ 3072
CF 40,40,41
CF 42,42,43
M 2,34,4
LL 16,56
LL 104,57
M 2,35,5
LA 16
RA 228,1,16384
GS 41
AW 44,40
AW 45,42
LD 40,248,43
WL 2
RA 250,7,2304
CF 44,44,45
CF 46,46,47
AW 46,44
AW 47,46
Z 6,0,0
s_add_u32 s50, s50, %[d]
LC_ 104
M 6,1,1
s_cmp_eq_u32 s50, %[wrap]
LD 44,244,44
s_cselect_b32 s50, %[rst], s50
CF 48,48,49
CF 50,50,51
GS 42
s_add_u32 s42, s44, s62
AW 48,48
AW 49,50
Z 7,12,0
LO 115,19,115
LU 19,115
M 7,13,1
s_addc_u32 s43, s45, 0
RA 232,1,17408
HS 25,19
LD 48,240,44
WL 2
RA 242,7,2560
CF 40,40,41
CF 42,42,43
AW 50,40
AW 51,42
LD 40,252,45
ST 112
CF 44,44,45
CF 46,46,47
Z 8,24,0
LL 17,58
LL 105,59
M 8,25,1
s_add_u32 s38, s38, 32768
LA 17
RA 236,1,18432
GS 43
s_cmp_eq_u32 s38, s77
AW 52,44
AW 53,46
HX 25
LD 44,248,45
WL 2
RA 250,7,2816
CF 48,48,49
CF 50,50,51
AW 54,48
AW 55,50
M 6,2,2
s_cselect_b32 s38, s76, s38
LC_ 105
W1 25,1024
M 6,3,3
s_add_u32 s37, s37, 32768
v_add_u32 v4, s39, v4
LD 48,244,46
s_cmp_eq_u32 s37, s75
CF 40,40,41
CF 42,42,43
GS 44
s_cselect_b32 s37, s74, s37
AW 56,40
AW 57,42
M 7,14,2
WL 2
LO 104,16,104
LU 16,104
M 7,15,3
s_add_u32 s53, %[xo0], 393216
RA 216,1,19456
HZ 24,16
LD 40,240,46
s_add_u32 s46, %[xlo], s53
CF 44,44,45
CF 46,46,47
AW 58,44
AW 59,46
LD 44,252,47
s_addc_u32 s47, %[xhi], 0
CF 48,48,49
CF 50,50,51
M 8,26,2
LL 18,60
LL 108,61
M 8,27,3
s_bitcmp1_b64 s[90:91], 48
LA 18
RA 220,1,20480
GS 45
s_cselect_b32 s58, 16, 0
AW 60,48
AW 61,50
LD 48,248,47
s_lshr_b32 s58, 0xfe987610, s58
CF 40,40,41
CF 42,42,43
AW 62,40
AW 63,42
v_lshrrev_b32 v3, 4, v0
M 6,4,4
LC_ 108
v_and_b32 v3, 12, v3
v_bfe_u32 v3, s58, v3, 4
v_mad_u32_u24 v3, v3, s97, v21
M 6,5,5
XL 48,0
GS 46
s_lshl_b32 s53, s52, 8
CF 44,44,45
CF 46,46,47
AW 64,44
AW 65,46
CF 48,48,49
CF 50,50,51
AW 66,48
AW 67,50
M 7,16,4
WL 4
LO 105,17,105
LU 17,105
M 7,17,5
s_add_u32 s48, %[alo], s53
RA 224,1,21504
HS 24,17
GS 47
s_addc_u32 s49, %[ahi], 0
RV 36,8,6912
M 8,28,4
WL 3
LL 19,62
LL 109,63
M 8,29,5
s_add_u32 s52, s52, %[d]
LA 19
RA 228,1,22528
v_cndmask_b32 v52, v52, v53, s[78:79]
v_cndmask_b32 v54, v54, v55, s[78:79]
v_cndmask_b32 v52, v52, v54, s[80:81]
v_mul_f32 v52, 0x3b2aaaab, v52
s_cmp_eq_u32 s52, %[wrap]
s_cselect_b32 s52, %[rst], s52
s_mul_i32 s53, s51, 49152
s_bitcmp1_b64 s[90:91], s51
s_cselect_b64 s[44:45], s[84:85], s[82:83]
s_add_u32 s44, s44, s53
s_addc_u32 s45, s45, 0
s_add_u32 s51, s51, %[d]
s_cmp_eq_u32 s51, %[wrap]
s_cselect_b32 s51, %[rst], s51
C 52,52,52
v_lshlrev_b32 v52, 16, v52
WL 2
v_add_f32 v52, v36, v52
C 52,52,52
M 6,6,0
LC_ 109
XL 49,64
M 6,7,1
v_lshlrev_b32 v52, 16, v52
v_sqrt_f32 v52, v52
s_nop 0
C 52,52,52
v_lshlrev_b32 v52, 16, v52
v_cmp_ge_f32 vcc, v37, v52
v_rcp_f32 v52, v52
M 7,18,0
LO 108,18,108
LU 18,108
M 7,19,1
RA 232,1,23552
HS 24,18
v_mul_f32 v52, v38, v52
v_cndmask_b32 v52, 0, v52, vcc
CF 52,52,52
ds_bpermute_b32 v13, v28, v52
M 8,30,0
WL 3
LL 16,64
LL 112,65
M 8,31,1
LA 16
RA 236,1,24576
M 6,8,2
LC_ 112
XL 50,128
M 6,9,3
WL 3
XL 51,192
v_add_u32 v1, s37, v0
AR 52,20
AR 53,21
AR 54,22
AR 55,23
XS 52
XS 53
XS 54
XS 55
AW 20,52
AW 21,53
AW 22,54
AW 23,55
AR 52,24
AR 53,25
AR 54,26
AR 55,27
XS 52
XS 53
XS 54
XS 55
AW 24,52
AW 25,53
AW 26,54
AW 27,55
AR 52,28
AR 53,29
AR 54,30
AR 55,31
XS 52
XS 53
XS 54
XS 55
AW 28,52
AW 29,53
AW 30,54
AW 31,55
AR 52,32
AR 53,33
AR 54,34
AR 55,35
XS 52
XS 53
XS 54
XS 55
AW 32,52
AW 33,53
AW 34,54
AW 35,55
AR 52,36
AR 53,37
AR 54,38
AR 55,39
XS 52
XS 53
XS 54
XS 55
AW 36,52
AW 37,53
AW 38,54
AW 39,55
AR 52,40
AR 53,41
AR 54,42
M 7,20,2
LO 109,19,109
LU 19,109
AR 55,43
M 7,21,3
RA 216,1,0
HS 24,19
XS 52
XS 53
M 8,32,2
WL 1
LL 17,66
LL 113,67
XS 54
M 8,33,3
LA 17
XL 52,256
XS 55
AW 40,52
M 6,10,4
s_barrier
LC_ 113
v_and_b32 v22, 0xfffffcff, v1
AW 41,53
AW 42,54
)"
R"(M 6,11,5
s_sub_u32 s39, 0, s39
RA 220,1,1024
AW 43,55
M 7,22,4
LO 112,16,112
LU 16,112
AR 52,44
AR 53,45
M 7,23,5
RV 252,22,5120
HS 24,16
AR 54,46
M 8,34,4
WL 1
LL 18,80
LL 106,81
AR 55,47
XS 52
M 8,35,5
LA 18
RA 224,1,2048
XS 53
XS 54
WL 2
Z 0,0,0
LC_ 106
RA 228,1,3072
M 0,1,1
ds_read_b64 v[14:15], v5 offset:0
XS 55
Z 1,12,0
LO 113,17,113
LU 17,113
XL 53,320
M 1,13,1
s_mul_i32 s53, s50, 25600
RA 232,1,4096
HS 24,17
AW 44,52
AW 45,53
Z 2,24,0
s_add_u32 s40, %[wlo], s53
LL 19,82
LL 107,83
AW 46,54
M 2,25,1
s_addc_u32 s41, %[whi], 0
LA 19
v_subrev_u32 v5, s39, v5
AW 47,55
AR 52,48
Z 3,36,0
WL 3
LC_ 107
HX 24
AR 53,49
AR 54,50
M 3,37,1
s_mov_b32 m0, s38
RA 236,1,6144
AR 55,51
M 0,2,2
LO 106,18,106
LU 18,106
XS 52
XS 53
M 0,3,3
W1 24,0
HZ 25,18
XS 54
M 1,14,2
LL 16,84
LL 110,85
XS 55
AW 48,52
M 1,15,3
LA 16
RA 216,1,7168
AW 49,53
AW 50,54
M 2,26,2
LC_ 110
DW 0
AW 51,55
M 2,27,3
WL 8
DW 1024
v_add_f32 v14, v14, v15
s_nop 1
v_add_f32_dpp v14, v14, v14 quad_perm:[1,0,3,2] row_mask:0xf bank_mask:0xf
AR 52,52
AR 53,53
M 3,38,2
WL 4
LO 107,19,107
LU 19,107
v_add_f32_dpp v14, v14, v14 quad_perm:[2,3,0,1] row_mask:0xf bank_mask:0xf
AR 54,54
M 3,39,3
s_add_u32 s42, s44, s60
RA 220,1,8192
HS 25,19
AR 55,55
XS 52
M 0,4,4
s_addc_u32 s43, s45, 0
LL 17,86
LL 111,87
v_add_f32_dpp v14, v14, v14 row_half_mirror row_mask:0xf bank_mask:0xf
XS 53
M 0,5,5
LA 17
ST 104
XS 54
XS 55
M 1,16,4
LC_ 111
DW 2048
v_cvt_f64_f32 v[14:15], v14
AW 52,52
AW 53,53
M 1,17,5
RA 224,1,9216
AW 54,54
M 2,28,4
WL 4
LO 110,16,110
LU 16,110
s_mov_b64 exec, s[54:55]
SP
s_mov_b64 exec, -1
AW 55,55
AR 52,56
M 2,29,5
HS 25,16
AR 53,57
M 3,40,4
WL 3
LL 18,88
LL 114,89
AR 54,58
AR 55,59
M 3,41,5
LA 18
RA 228,1,10240
XS 52
XS 53
M 0,6,0
s_add_u32 s40, s40, 3072
LC_ 114
XS 54
M 0,7,1
s_addc_u32 s41, s41, 0
XS 55
AW 56,52
M 1,18,0
WL 4
LO 111,17,111
LU 17,111
AW 57,53
M 1,19,1
s_add_u32 m0, s38, 3072
RA 232,1,11264
HS 25,17
AW 58,54
AW 59,55
M 2,30,0
s_add_u32 s42, s44, s61
LL 19,90
LL 115,91
AR 52,60
AR 53,61
M 2,31,1
s_addc_u32 s43, s45, 0
LA 19
RV 252,22,5392
AR 54,62
M 3,42,0
WL 4
LC_ 115
ST 108
AR 55,63
XS 52
M 3,43,1
RA 236,1,12288
XS 53
M 0,8,2
WL 5
LO 114,18,114
LU 18,114
XS 54
XS 55
M 0,9,3
DW 0
HS 25,18
AW 60,52
AW 61,53
M 1,20,2
AW 62,54
AW 63,55
M 1,21,3
RA 216,1,13312
AR 52,64
M 2,32,2
DW 1024
AR 53,65
M 2,33,3
s_add_u32 s42, s44, s62
AR 54,66
AR 55,67
M 3,44,2
WL 1
LO 115,19,115
LU 19,115
XS 52
XS 53
M 3,45,3
s_addc_u32 s43, s45, 0
RA 220,1,14336
HS 25,19
XS 54
M 0,10,4
ST 112
HX 25
XS 55
AW 64,52
M 0,11,5
DW 2048
AW 65,53
M 1,22,4
W1 25,1024
AW 66,54
AW 67,55
M 1,23,5
RA 224,1,15360
v_add_u32 v4, s39, v4
v_min_u32 v35, v35, v13
M 2,34,4
LL 16,56
LL 104,57
M 2,35,5
LA 16
M 3,46,4
WL 3
LC_ 104
DQ 3072
M 3,47,5
RA 228,1,16384
Z 6,0,0
M 6,1,1
Z 7,12,0
LL 17,58
LL 105,59
M 7,13,1
LA 17
RA 232,1,17408
Z 8,24,0
s_add_u32 s50, s50, %[d]
LC_ 105
M 8,25,1
s_cmp_eq_u32 s50, %[wrap]
Z 9,36,0
WL 3
LO 104,16,104
LU 16,104
M 9,37,1
s_cselect_b32 s50, %[rst], s50
RA 236,1,18432
HZ 24,16
M 6,2,2
LL 18,60
LL 108,61
M 6,3,3
s_add_u32 s37, s37, 32768
LA 18
M 7,14,2
s_cmp_eq_u32 s37, s75
LC_ 108
M 7,15,3
s_cselect_b32 s37, s74, s37
RA 216,1,19456
M 8,26,2
WL 4
LO 105,17,105
LU 17,105
M 8,27,3
s_add_u32 s38, s38, 32768
HS 24,17
M 9,38,2
WL 3
LL 19,62
LL 109,63
M 9,39,3
s_cmp_eq_u32 s38, s77
LA 19
RA 220,1,20480
M 6,4,4
s_cselect_b32 s38, s76, s38
LC_ 109
XL 54,384
M 6,5,5
M 7,16,4
WL 4
LO 108,18,108
LU 18,108
M 7,17,5
s_mul_i32 s53, s51, 49152
RA 224,1,21504
HS 24,18
M 8,28,4
s_bitcmp1_b64 s[90:91], s51
LL 16,64
LL 112,65
M 8,29,5
s_cselect_b64 s[44:45], s[84:85], s[82:83]
LA 16
M 9,40,4
WL 3
LC_ 112
M 9,41,5
s_add_u32 s44, s44, s53
RA 228,1,22528
M 6,6,0
WL 4
LO 109,19,109
LU 19,109
M 6,7,1
s_addc_u32 s45, s45, 0
HS 24,19
s_add_u32 s51, s51, %[d]
s_cmp_eq_u32 s51, %[wrap]
s_cselect_b32 s51, %[rst], s51
M 7,18,0
LL 17,66
LL 113,67
M 7,19,1
LA 17
RA 232,1,23552
XL 55,448
M 8,30,0
s_lshl_b32 s53, s52, 8
LC_ 113
M 8,31,1
s_add_u32 s48, %[alo], s53
M 9,42,0
WL 3
LO 112,16,112
LU 16,112
M 9,43,1
s_addc_u32 s49, %[ahi], 0
RA 236,1,24576
HS 24,16
M 6,8,2
s_add_u32 s52, s52, %[d]
LL 18,68
LL 116,69
s_cmp_eq_u32 s52, %[wrap]
s_cselect_b32 s52, %[rst], s52
M 6,9,3
LA 18
XL 56,512
M 7,20,2
LC_ 116
XL 57,576
M 7,21,3
v_add_u32 v1, s37, v0
M 8,32,2
WL 3
LO 113,17,113
LU 17,113
M 8,33,3
RA 216,1,0
HS 24,17
M 9,44,2
WL 3
LL 19,70
LL 117,71
M 9,45,3
WV 22
LA 19
XL 58,640
M 6,10,4
s_barrier
LC_ 117
v_and_b32 v22, 0xfffffcff, v1
M 6,11,5
s_sub_u32 s39, 0, s39
RA 220,1,1024
M 7,22,4
WL 4
LO 116,18,116
LU 18,116
M 7,23,5
RV 252,22,5120
HS 24,18
M 8,34,4
LL 16,80
LL 106,81
M 8,35,5
)"
R"(LA 16
RA 224,1,2048
M 9,46,4
WL 3
LC_ 106
ds_read_b64 v[14:15], v5 offset:0
M 9,47,5
WL 4
RA 228,1,3072
v_subrev_u32 v5, s39, v5
Z 0,0,0
LO 117,19,117
LU 19,117
M 0,1,1
WV 20
XL 59,704
HS 24,19
RV 40,8,6144
RV 44,8,6208
RV 48,8,6272
RA 242,7,0
RA 250,7,256
WL 4
LZ 40,48
s_mul_i32 s53, s50, 25600
Z 1,12,0
s_add_u32 s40, %[wlo], s53
LL 17,82
LL 107,83
M 1,13,1
s_addc_u32 s41, %[whi], 0
LA 17
RA 232,1,4096
WL 5
LS 44,49
WV 20
RV 40,8,6336
HX 24
Z 2,24,0
s_mov_b32 m0, s38
LC_ 107
W1 24,0
M 2,25,1
WL 7
LS 48,50
WV 19
RV 44,8,6400
WL 3
LS 40,51
RV 48,8,6464
Z 3,36,0
LO 106,16,106
LU 16,106
M 3,37,1
WV 18
RA 236,1,6144
HZ 25,16
WL 2
LS 44,52
RV 40,8,6528
M 0,2,2
WV 17
LL 18,84
LL 110,85
M 0,3,3
WL 2
LA 18
DW 0
LS 48,53
RV 44,8,6592
v_add_f32 v14, v14, v15
WL 2
WV 6
LS 40,54
RV 48,8,6656
v_add_f32_dpp v14, v14, v14 quad_perm:[1,0,3,2] row_mask:0xf bank_mask:0xf
M 1,14,2
LC_ 110
v_add_f32_dpp v14, v14, v14 quad_perm:[2,3,0,1] row_mask:0xf bank_mask:0xf
M 1,15,3
s_add_u32 s42, s44, s60
RA 216,1,7168
v_add_f32_dpp v14, v14, v14 row_half_mirror row_mask:0xf bank_mask:0xf
WL 3
WV 5
LS 44,55
RV 40,8,6720
v_cvt_f64_f32 v[14:15], v14
M 2,26,2
WL 3
LO 107,17,107
LU 17,107
M 2,27,3
s_addc_u32 s43, s45, 0
DW 1024
HS 25,17
WV 5
LS 48,56
RV 44,8,6784
WL 1
WV 4
LS 40,57
RV 48,8,6848
M 3,38,2
LL 19,86
LL 111,87
M 3,39,3
LA 19
RA 220,1,8192
WL 3
WV 3
LS 44,58
s_mov_b64 exec, s[54:55]
SP
s_mov_b64 exec, -1
M 0,4,4
LC_ 111
ST 104
M 0,5,5
WL 3
DW 2048
WV 5
LS 48,59
s_nop 7
v_mul_f32 v36, 0xbb2aaaab, v36
v_mul_f32 v37, 0xbb2aaaab, v37
v_mul_f32 v38, 0xbb2aaaab, v38
v_mul_f32 v39, 0xbb2aaaab, v39
M 1,16,4
LO 110,18,110
LU 18,110
M 1,17,5
RA 224,1,9216
HS 25,18
LD 40,244,48
M 2,28,4
LL 16,88
LL 114,89
M 2,29,5
LA 16
LD 44,240,48
RA 242,7,512
M 3,40,4
WL 4
LC_ 114
M 3,41,5
s_add_u32 s40, s40, 3072
RA 228,1,10240
LD 48,252,49
s_addc_u32 s41, s41, 0
CF 40,40,41
CF 42,42,43
AW 68,40
AW 69,42
LD 40,248,49
RA 250,7,768
CF 44,44,45
CF 46,46,47
AW 70,44
AW 71,46
M 0,6,0
WL 3
LO 111,19,111
LU 19,111
M 0,7,1
s_add_u32 m0, s38, 3072
HS 25,19
LD 44,244,50
s_add_u32 s42, s44, s61
CF 48,48,49
CF 50,50,51
M 1,18,0
s_addc_u32 s43, s45, 0
LL 17,90
LL 115,91
M 1,19,1
LA 17
RA 232,1,11264
GZ 48
ST 108
AW 72,48
AW 73,50
LD 48,240,50
RA 242,7,1024
CF 40,40,41
CF 42,42,43
AW 74,40
AW 75,42
M 2,30,0
LC_ 115
RV 252,22,5392
M 2,31,1
WL 5
DW 0
LD 40,252,51
CF 44,44,45
CF 46,46,47
M 3,42,0
LO 114,16,114
LU 16,114
M 3,43,1
RA 236,1,12288
HS 25,16
GS 49
AW 76,44
AW 77,46
LD 44,248,51
RA 250,7,1280
CF 48,48,49
CF 50,50,51
AW 78,48
AW 79,50
M 0,8,2
LL 18,92
LL 118,93
M 0,9,3
WL 4
LA 18
DW 1024
LD 48,244,52
CF 40,40,41
CF 42,42,43
M 1,20,2
LC_ 118
M 1,21,3
s_add_u32 s42, s44, s62
RA 216,1,13312
GS 50
s_addc_u32 s43, s45, 0
AW 80,40
AW 81,42
LD 40,240,52
RA 242,7,1536
CF 44,44,45
CF 46,46,47
AW 82,44
AW 83,46
M 2,32,2
WL 7
LO 115,17,115
LU 17,115
M 2,33,3
WL 4
ST 112
HS 25,17
LD 44,252,53
CF 48,48,49
CF 50,50,51
M 3,44,2
LL 19,94
LL 119,95
M 3,45,3
LA 19
RA 220,1,14336
GS 51
DW 2048
AW 84,48
AW 85,50
M 0,10,4
LC_ 119
M 0,11,5
LD 48,248,53
WL 3
RA 250,7,1792
CF 40,40,41
CF 42,42,43
AW 86,40
AW 87,42
LD 40,244,54
CF 44,44,45
CF 46,46,47
M 1,22,4
LO 118,18,118
LU 18,118
M 1,23,5
RA 224,1,15360
HS 25,18
GS 52
DQ 3072
AW 88,44
AW 89,46
M 2,34,4
LL 16,56
LL 104,57
M 2,35,5
LA 16
LD 44,240,54
WL 2
RA 242,7,2048
CF 48,48,49
CF 50,50,51
AW 90,48
AW 91,50
LD 48,252,55
CF 40,40,41
CF 42,42,43
M 3,46,4
LC_ 104
M 3,47,5
s_add_u32 s42, s44, s63
RA 228,1,16384
GS 53
s_addc_u32 s43, s45, 0
AW 92,40
AW 93,42
Z 6,0,0
LO 119,19,119
LU 19,119
M 6,1,1
s_add_u32 s50, s50, %[d]
HS 25,19
LD 40,248,55
WL 2
RA 250,7,2304
CF 44,44,45
CF 46,46,47
AW 94,44
AW 95,46
LD 44,244,56
s_cmp_eq_u32 s50, %[wrap]
CF 48,48,49
CF 50,50,51
Z 7,12,0
s_cselect_b32 s50, %[rst], s50
LL 17,58
LL 105,59
M 7,13,1
LA 17
RA 232,1,17408
GS 54
ST 116
AW 96,48
AW 97,50
HX 25
Z 8,24,0
LC_ 105
W1 25,1024
M 8,25,1
s_nop 0
v_add_u32 v4, s39, v4
LD 48,240,56
WL 4
RA 242,7,2560
CF 40,40,41
CF 42,42,43
AW 98,40
AW 99,42
LD 40,252,57
CF 44,44,45
CF 46,46,47
Z 9,36,0
LO 104,16,104
LU 16,104
M 9,37,1
s_add_u32 s38, s38, 32768
RA 236,1,18432
HZ 24,16
GS 55
s_cmp_eq_u32 s38, s77
AW 100,44
AW 101,46
M 6,2,2
s_cselect_b32 s38, s76, s38
LL 18,60
LL 108,61
M 6,3,3
s_add_u32 s37, s37, 32768
LA 18
LD 44,248,57
WL 2
RA 250,7,2816
CF 48,48,49
CF 50,50,51
AW 102,48
AW 103,50
LD 48,244,58
s_cmp_eq_u32 s37, s75
CF 40,40,41
CF 42,42,43
M 7,14,2
s_cselect_b32 s37, s74, s37
LC_ 108
M 7,15,3
RA 216,1,19456
GS 56
s_add_u32 s53, %[xo0], 393216
AW 104,40
AW 105,42
M 8,26,2
LO 105,17,105
LU 17,105
M 8,27,3
s_add_u32 s46, %[xlo], s53
HS 24,17
LD 40,240,58
s_addc_u32 s47, %[xhi], 0
CF 44,44,45
CF 46,46,47
AW 106,44
AW 107,46
)"
R"(M 9,38,2
WL 2
LL 19,62
LL 109,63
M 9,39,3
s_bitcmp1_b64 s[90:91], 48
LA 19
RA 220,1,20480
LD 44,252,59
s_cselect_b32 s58, 16, 0
CF 48,48,49
CF 50,50,51
GS 57
s_lshr_b32 s58, 0xda43cb52, s58
AW 108,48
AW 109,50
v_lshrrev_b32 v3, 4, v0
M 6,4,4
LC_ 109
v_and_b32 v3, 12, v3
M 6,5,5
v_bfe_u32 v3, s58, v3, 4
LD 48,248,59
CF 40,40,41
CF 42,42,43
AW 110,40
AW 111,42
v_mad_u32_u24 v3, v3, s97, v21
M 7,16,4
WL 4
LO 108,18,108
LU 18,108
M 7,17,5
RA 224,1,21504
HS 24,18
GS 58
XL 60,0
CF 44,44,45
CF 46,46,47
AW 112,44
AW 113,46
CF 48,48,49
CF 50,50,51
AW 114,48
AW 115,50
M 8,28,4
s_mul_i32 s53, s51, 49152
LL 16,64
LL 112,65
M 8,29,5
s_bitcmp1_b64 s[90:91], s51
LA 16
GS 59
s_cselect_b64 s[44:45], s[84:85], s[82:83]
M 9,40,4
WL 3
LC_ 112
M 9,41,5
s_add_u32 s44, s44, s53
RA 228,1,22528
RV 36,8,6912
v_cndmask_b32 v52, v52, v53, s[78:79]
v_cndmask_b32 v54, v54, v55, s[78:79]
v_cndmask_b32 v52, v52, v54, s[80:81]
v_mul_f32 v52, 0x3b2aaaab, v52
M 6,6,0
WL 5
LO 109,19,109
LU 19,109
M 6,7,1
s_addc_u32 s45, s45, 0
HS 24,19
s_add_u32 s51, s51, %[d]
s_cmp_eq_u32 s51, %[wrap]
s_cselect_b32 s51, %[rst], s51
C 52,52,52
v_lshlrev_b32 v52, 16, v52
WL 0
v_add_f32 v52, v36, v52
C 52,52,52
M 7,18,0
LL 17,66
LL 113,67
M 7,19,1
LA 17
RA 232,1,23552
v_lshlrev_b32 v52, 16, v52
v_sqrt_f32 v52, v52
s_nop 0
C 52,52,52
v_lshlrev_b32 v52, 16, v52
v_cmp_ge_f32 vcc, v37, v52
v_rcp_f32 v52, v52
M 8,30,0
LC_ 113
XL 61,64
M 8,31,1
s_lshl_b32 s53, s52, 8
v_mul_f32 v52, v38, v52
v_cndmask_b32 v52, 0, v52, vcc
CF 52,52,52
M 9,42,0
LO 112,16,112
LU 16,112
M 9,43,1
s_add_u32 s48, %[alo], s53
RA 236,1,24576
HS 24,16
ds_bpermute_b32 v13, v28, v52
s_addc_u32 s49, %[ahi], 0
s_add_u32 s52, s52, %[d]
s_cmp_eq_u32 s52, %[wrap]
s_cselect_b32 s52, %[rst], s52
M 6,8,2
LL 18,68
LL 116,69
M 6,9,3
LA 18
XL 62,128
M 7,20,2
LC_ 116
XL 63,192
M 7,21,3
WL 2
v_add_u32 v1, s37, v0
AR 52,68
AR 53,69
AR 54,70
AR 55,71
XS 52
XS 53
XS 54
XS 55
AW 68,52
AW 69,53
AW 70,54
AW 71,55
AR 52,72
AR 53,73
AR 54,74
AR 55,75
XS 52
XS 53
XS 54
XS 55
AW 72,52
AW 73,53
AW 74,54
AW 75,55
AR 52,76
AR 53,77
AR 54,78
AR 55,79
XS 52
XS 53
XS 54
XS 55
AW 76,52
AW 77,53
AW 78,54
AW 79,55
AR 52,80
AR 53,81
AR 54,82
AR 55,83
XS 52
XS 53
XS 54
XS 55
AW 80,52
AW 81,53
AW 82,54
AW 83,55
AR 52,84
AR 53,85
AR 54,86
AR 55,87
XS 52
M 8,32,2
LO 113,17,113
LU 17,113
XS 53
M 8,33,3
RA 216,1,0
HS 24,17
XS 54
M 9,44,2
LL 19,70
LL 117,71
XS 55
AW 84,52
M 9,45,3
LA 19
XL 64,256
AW 85,53
M 6,10,4
s_barrier
LC_ 117
v_and_b32 v22, 0xfffffcff, v1
AW 86,54
AW 87,55
M 6,11,5
s_sub_u32 s39, 0, s39
RA 220,1,1024
AR 52,88
M 7,22,4
WL 4
LO 116,18,116
LU 18,116
AR 53,89
M 7,23,5
RV 252,22,5120
HS 24,18
AR 54,90
AR 55,91
M 8,34,4
LL 16,80
LL 106,81
XS 52
M 8,35,5
LA 16
RA 224,1,2048
XS 53
XS 54
M 9,46,4
WL 3
LC_ 106
ds_read_b64 v[14:15], v5 offset:0
XS 55
M 9,47,5
WL 4
RA 228,1,3072
v_subrev_u32 v5, s39, v5
AW 88,52
Z 0,0,0
LO 117,19,117
LU 19,117
M 0,1,1
XL 65,320
HS 24,19
Z 1,12,0
s_mul_i32 s53, s50, 25600
LL 17,82
LL 107,83
AW 89,53
AW 90,54
M 1,13,1
s_add_u32 s40, %[wlo], s53
LA 17
RA 232,1,4096
AW 91,55
Z 2,24,0
s_addc_u32 s41, %[whi], 0
LC_ 107
HX 24
AR 52,92
AR 53,93
M 2,25,1
s_mov_b32 m0, s38
W1 24,0
AR 54,94
Z 3,36,0
WL 6
LO 106,16,106
LU 16,106
AR 55,95
XS 52
M 3,37,1
HZ 25,16
XS 53
Z 4,48,0
WL 4
LL 18,84
LL 110,85
XS 54
M 4,49,1
LA 18
RA 236,1,6144
XS 55
AW 92,52
M 0,2,2
LC_ 110
DW 0
AW 93,53
M 0,3,3
s_add_u32 s42, s44, s60
v_add_f32 v14, v14, v15
AW 94,54
AW 95,55
M 1,14,2
WL 4
LO 107,17,107
LU 17,107
AR 52,96
M 1,15,3
s_addc_u32 s43, s45, 0
RA 216,1,7168
HS 25,17
AR 53,97
M 2,26,2
LL 19,86
LL 111,87
v_add_f32_dpp v14, v14, v14 quad_perm:[1,0,3,2] row_mask:0xf bank_mask:0xf
AR 54,98
AR 55,99
M 2,27,3
LA 19
ST 104
XS 52
M 3,38,2
LC_ 111
DW 1024
v_add_f32_dpp v14, v14, v14 quad_perm:[2,3,0,1] row_mask:0xf bank_mask:0xf
XS 53
XS 54
M 3,39,3
v_add_f32_dpp v14, v14, v14 row_half_mirror row_mask:0xf bank_mask:0xf
XS 55
M 4,50,2
WL 3
LO 110,18,110
LU 18,110
AW 96,52
M 4,51,3
RA 220,1,8192
HS 25,18
AW 97,53
AW 98,54
M 0,4,4
LL 16,88
LL 114,89
v_cvt_f64_f32 v[14:15], v14
AW 99,55
M 0,5,5
LA 16
s_mov_b64 exec, s[54:55]
SP
s_mov_b64 exec, -1
AR 52,100
AR 53,101
M 1,16,4
LC_ 114
AR 54,102
M 1,17,5
RA 224,1,9216
AR 55,103
M 2,28,4
WL 4
LO 111,19,111
LU 19,111
XS 52
XS 53
M 2,29,5
DW 2048
HS 25,19
XS 54
M 3,40,4
s_add_u32 s42, s44, s61
LL 17,90
LL 115,91
XS 55
AW 100,52
M 3,41,5
s_addc_u32 s43, s45, 0
LA 17
ST 108
AW 101,53
M 4,52,4
WL 4
LC_ 115
AW 102,54
M 4,53,5
RA 228,1,10240
AW 103,55
AR 52,104
M 0,6,0
WL 4
LO 114,16,114
LU 16,114
AR 53,105
M 0,7,1
s_add_u32 s40, s40, 3072
HS 25,16
AR 54,106
AR 55,107
M 1,18,0
s_addc_u32 s41, s41, 0
LL 18,92
LL 118,93
XS 52
M 1,19,1
s_add_u32 m0, s38, 3072
LA 18
RA 232,1,11264
XS 53
XS 54
M 2,30,0
LC_ 118
RV 252,22,5392
XS 55
)"
R"(M 2,31,1
s_add_u32 s42, s44, s62
AW 104,52
AW 105,53
M 3,42,0
WL 5
LO 115,17,115
LU 17,115
AW 106,54
M 3,43,1
s_addc_u32 s43, s45, 0
DW 0
HS 25,17
AW 107,55
M 4,54,0
WL 4
LL 19,94
LL 119,95
AR 52,108
AR 53,109
M 4,55,1
LA 19
RA 236,1,12288
AR 54,110
M 0,8,2
LC_ 119
ST 112
AR 55,111
M 0,9,3
XS 52
XS 53
M 1,20,2
WL 4
LO 118,18,118
LU 18,118
XS 54
M 1,21,3
RA 216,1,13312
HS 25,18
XS 55
AW 108,52
M 2,32,2
DW 1024
AW 109,53
M 2,33,3
AW 110,54
AW 111,55
M 3,44,2
AR 52,112
AR 53,113
M 3,45,3
s_add_u32 s42, s44, s63
AR 54,114
AR 55,115
M 4,56,2
WL 1
LO 119,19,119
LU 19,119
M 4,57,3
s_addc_u32 s43, s45, 0
RA 220,1,14336
HS 25,19
XS 52
M 0,10,4
ST 116
HX 25
XS 53
M 0,11,5
DW 2048
XS 54
XS 55
M 1,22,4
W1 25,1024
AW 112,52
M 1,23,5
RA 224,1,15360
v_add_u32 v4, s39, v4
AW 113,53
AW 114,54
M 2,34,4
LL 16,56
LL 104,57
AW 115,55
M 2,35,5
LA 16
v_min_u32 v35, v35, v13
M 3,46,4
LC_ 104
M 3,47,5
M 4,58,4
WL 4
DQ 3072
M 4,59,5
RA 228,1,16384
Z 6,0,0
LL 17,58
LL 105,59
M 6,1,1
LA 17
Z 7,12,0
LC_ 105
M 7,13,1
RA 232,1,17408
Z 8,24,0
WL 4
LO 104,16,104
LU 16,104
M 8,25,1
HZ 24,16
Z 9,36,0
s_add_u32 s50, s50, %[d]
LL 18,60
LL 108,61
M 9,37,1
s_cmp_eq_u32 s50, %[wrap]
LA 18
Z 10,48,0
WL 4
LC_ 108
M 10,49,1
s_cselect_b32 s50, %[rst], s50
RA 236,1,18432
M 6,2,2
WL 4
LO 105,17,105
LU 17,105
M 6,3,3
s_add_u32 s37, s37, 32768
HS 24,17
M 7,14,2
s_cmp_eq_u32 s37, s75
LL 19,62
LL 109,63
M 7,15,3
s_cselect_b32 s37, s74, s37
LA 19
RA 216,1,19456
M 8,26,2
s_add_u32 s38, s38, 32768
LC_ 109
M 8,27,3
s_cmp_eq_u32 s38, s77
M 9,38,2
WL 4
LO 108,18,108
LU 18,108
M 9,39,3
s_cselect_b32 s38, s76, s38
HS 24,18
M 10,50,2
WL 3
LL 16,64
LL 112,65
M 10,51,3
LA 16
RA 220,1,20480
M 6,4,4
LC_ 112
XL 66,384
M 6,5,5
M 7,16,4
WL 3
LO 109,19,109
LU 19,109
M 7,17,5
s_mul_i32 s53, s51, 49152
RA 224,1,21504
HS 24,19
M 8,28,4
s_bitcmp1_b64 s[90:91], s51
LL 17,66
LL 113,67
M 8,29,5
s_cselect_b64 s[44:45], s[84:85], s[82:83]
LA 17
M 9,40,4
s_add_u32 s44, s44, s53
LC_ 113
M 9,41,5
s_addc_u32 s45, s45, 0
M 10,52,4
WL 3
LO 112,16,112
LU 16,112
M 10,53,5
s_add_u32 s51, s51, %[d]
RA 228,1,22528
HS 24,16
M 6,6,0
s_cmp_eq_u32 s51, %[wrap]
LL 18,68
LL 116,69
M 6,7,1
s_cselect_b32 s51, %[rst], s51
LA 18
XL 67,448
M 7,18,0
LC_ 116
M 7,19,1
RA 232,1,23552
M 8,30,0
WL 4
LO 113,17,113
LU 17,113
M 8,31,1
XL 68,512
HS 24,17
M 9,42,0
LL 19,70
LL 117,71
M 9,43,1
s_lshl_b32 s53, s52, 8
LA 19
M 10,54,0
WL 4
LC_ 117
M 10,55,1
s_add_u32 s48, %[alo], s53
RA 236,1,24576
M 6,8,2
WL 4
LO 116,18,116
LU 18,116
M 6,9,3
s_addc_u32 s49, %[ahi], 0
HS 24,18
v_add_u32 v1, s37, v0
M 7,20,2
s_add_u32 s52, s52, %[d]
LL 16,72
LL 120,73
M 7,21,3
s_cmp_eq_u32 s52, %[wrap]
LA 16
RA 216,1,0
M 8,32,2
s_cselect_b32 s52, %[rst], s52
LC_ 120
XL 69,576
M 8,33,3
v_and_b32 v22, 0xfffffcff, v1
M 9,44,2
WL 4
LO 117,19,117
LU 19,117
M 9,45,3
HS 24,19
M 10,56,2
WL 3
LL 17,74
LL 121,75
M 10,57,3
LA 17
RA 220,1,1024
M 6,10,4
LC_ 121
RV 252,22,5120
M 6,11,5
WV 23
XL 70,640
M 7,22,4
WL 4
LO 120,16,120
LU 16,120
M 7,23,5
s_barrier
RA 224,1,2048
HS 24,16
M 8,34,4
s_sub_u32 s39, 0, s39
LL 18,80
LL 106,81
M 8,35,5
LA 18
ds_read_b64 v[14:15], v5 offset:0
M 9,46,4
LC_ 106
XL 71,704
M 9,47,5
v_subrev_u32 v5, s39, v5
M 10,58,4
WL 5
LO 121,17,121
LU 17,121
M 10,59,5
WL 4
RA 228,1,3072
HS 24,17
Z 0,0,0
WV 23
LL 19,82
LL 107,83
M 0,1,1
s_mul_i32 s53, s50, 25600
LA 19
HX 24
RV 40,8,6144
RV 44,8,6208
RV 48,8,6272
RA 242,7,0
RA 250,7,256
WL 4
LZ 40,60
WV 22
W1 24,0
Z 1,12,0
s_add_u32 s40, %[wlo], s53
LC_ 107
M 1,13,1
s_addc_u32 s41, %[whi], 0
RA 232,1,4096
WL 6
LS 44,61
WV 21
RV 40,8,6336
Z 2,24,0
WL 6
LO 106,18,106
LU 18,106
M 2,25,1
s_mov_b32 m0, s38
HZ 25,18
v_add_f32 v14, v14, v15
LS 48,62
RV 44,8,6400
v_add_f32_dpp v14, v14, v14 quad_perm:[1,0,3,2] row_mask:0xf bank_mask:0xf
Z 3,36,0
WV 20
LL 16,84
LL 110,85
M 3,37,1
WL 1
LA 16
DW 0
LS 40,63
WV 20
RV 48,8,6464
v_add_f32_dpp v14, v14, v14 quad_perm:[2,3,0,1] row_mask:0xf bank_mask:0xf
Z 4,48,0
WL 2
LC_ 110
M 4,49,1
s_add_u32 s42, s44, s60
RA 236,1,6144
v_add_f32_dpp v14, v14, v14 row_half_mirror row_mask:0xf bank_mask:0xf
LS 44,64
WV 19
RV 40,8,6528
M 0,2,2
WL 3
LO 107,19,107
LU 19,107
M 0,3,3
s_addc_u32 s43, s45, 0
ST 104
HS 25,19
LS 48,65
RV 44,8,6592
v_cvt_f64_f32 v[14:15], v14
M 1,14,2
LL 17,86
LL 111,87
M 1,15,3
WL 1
LA 17
RA 216,1,7168
WV 7
LS 40,66
RV 48,8,6656
M 2,26,2
WL 3
LC_ 111
DW 1024
M 2,27,3
s_mov_b64 exec, s[54:55]
SP
s_mov_b64 exec, -1
WV 8
LS 44,67
RV 40,8,6720
WL 2
WV 7
LS 48,68
RV 44,8,6784
M 3,38,2
LO 110,16,110
LU 16,110
M 3,39,3
HS 25,16
WL 1
WV 6
LS 40,69
RV 48,8,6848
M 4,50,2
LL 18,88
LL 114,89
M 4,51,3
WL 1
LA 18
RA 220,1,8192
)"
R"(WV 5
LS 44,70
DW 2048
M 0,4,4
WL 2
LC_ 114
M 0,5,5
s_add_u32 s42, s44, s61
WV 5
LS 48,71
s_addc_u32 s43, s45, 0
M 1,16,4
LO 111,17,111
LU 17,111
M 1,17,5
s_add_u32 s40, s40, 3072
RA 224,1,9216
HS 25,17
v_mul_f32 v36, 0xbb2aaaab, v36
v_mul_f32 v37, 0xbb2aaaab, v37
v_mul_f32 v38, 0xbb2aaaab, v38
v_mul_f32 v39, 0xbb2aaaab, v39
M 2,28,4
s_addc_u32 s41, s41, 0
LL 19,90
LL 115,91
M 2,29,5
s_add_u32 m0, s38, 3072
LA 19
ST 108
LD 40,244,60
M 3,40,4
LC_ 115
M 3,41,5
LD 44,240,60
RA 242,7,512
M 4,52,4
WL 4
LO 114,18,114
LU 18,114
M 4,53,5
RA 228,1,10240
HS 25,18
LD 48,252,61
DW 0
CF 40,40,41
CF 42,42,43
AW 116,40
AW 117,42
LD 40,248,61
RA 250,7,768
CF 44,44,45
CF 46,46,47
AW 118,44
AW 119,46
M 0,6,0
LL 16,92
LL 118,93
M 0,7,1
WL 2
LA 16
LD 44,244,62
CF 48,48,49
CF 50,50,51
M 1,18,0
LC_ 118
M 1,19,1
s_add_u32 s42, s44, s62
RA 232,1,11264
GZ 60
s_addc_u32 s43, s45, 0
AW 120,48
AW 121,50
M 2,30,0
LO 115,19,115
LU 19,115
M 2,31,1
RV 252,22,5392
HS 25,19
LD 48,240,62
RA 242,7,1024
CF 40,40,41
CF 42,42,43
AW 122,40
AW 123,42
M 3,42,0
LL 17,94
LL 119,95
M 3,43,1
WL 5
LA 17
DW 1024
LD 40,252,63
ST 112
CF 44,44,45
CF 46,46,47
M 4,54,0
LC_ 119
M 4,55,1
RA 236,1,12288
GS 61
DW 2048
AW 124,44
AW 125,46
M 0,8,2
WL 6
LO 118,16,118
LU 16,118
M 0,9,3
HS 25,16
LD 44,248,63
RA 250,7,1280
CF 48,48,49
CF 50,50,51
AW 126,48
AW 127,50
M 1,20,2
LL 18,96
LL 122,97
M 1,21,3
WL 4
LA 18
RA 216,1,13312
LD 48,244,64
CF 40,40,41
CF 42,42,43
GS 62
AW 128,40
AW 129,42
M 2,32,2
LC_ 122
M 2,33,3
DQ 3072
LD 40,240,64
RA 242,7,1536
CF 44,44,45
CF 46,46,47
AW 130,44
AW 131,46
M 3,44,2
WL 4
LO 119,17,119
LU 17,119
M 3,45,3
s_add_u32 s42, s44, s63
HS 25,17
LD 44,252,65
s_addc_u32 s43, s45, 0
CF 48,48,49
CF 50,50,51
M 4,56,2
LL 19,98
LL 123,99
M 4,57,3
LA 19
RA 220,1,14336
GS 63
ST 116
AW 132,48
AW 133,50
M 0,10,4
s_add_u32 s50, s50, %[d]
LC_ 123
M 0,11,5
s_cmp_eq_u32 s50, %[wrap]
LD 48,248,65
RA 250,7,1792
CF 40,40,41
CF 42,42,43
AW 134,40
AW 135,42
M 1,22,4
WL 4
LO 122,18,122
LU 18,122
M 1,23,5
s_cselect_b32 s50, %[rst], s50
RA 224,1,15360
HS 25,18
LD 40,244,66
CF 44,44,45
CF 46,46,47
M 2,34,4
LL 16,56
LL 104,57
M 2,35,5
LA 16
GS 64
s_add_u32 s38, s38, 32768
AW 136,44
AW 137,46
M 3,46,4
s_cmp_eq_u32 s38, s77
LC_ 104
M 3,47,5
s_cselect_b32 s38, s76, s38
LD 44,240,66
RA 242,7,2048
CF 48,48,49
CF 50,50,51
AW 138,48
AW 139,50
M 4,58,4
WL 4
LO 123,19,123
LU 19,123
M 4,59,5
s_add_u32 s42, s44, s64
RA 228,1,16384
HS 25,19
LD 48,252,67
s_addc_u32 s43, s45, 0
CF 40,40,41
CF 42,42,43
GS 65
ST 120
AW 140,40
AW 141,42
Z 6,0,0
LL 17,58
LL 105,59
M 6,1,1
s_mul_i32 s53, s50, 25600
LA 17
HX 25
LD 40,248,67
WL 2
RA 250,7,2304
CF 44,44,45
CF 46,46,47
AW 142,44
AW 143,46
Z 7,12,0
s_add_u32 s40, %[wlo], s53
LC_ 105
W1 25,1024
M 7,13,1
s_addc_u32 s41, %[whi], 0
RA 232,1,17408
v_add_u32 v4, s39, v4
LD 44,244,68
s_mov_b32 m0, s38
CF 48,48,49
CF 50,50,51
Z 8,24,0
LO 104,16,104
LU 16,104
M 8,25,1
HZ 24,16
GS 66
AW 144,48
AW 145,50
Z 9,36,0
LL 18,60
LL 108,61
M 9,37,1
LA 18
DW 0
LD 48,240,68
RA 242,7,2560
CF 40,40,41
CF 42,42,43
AW 146,40
AW 147,42
Z 10,48,0
WL 7
LC_ 108
M 10,49,1
WL 6
RA 236,1,18432
LD 40,252,69
CF 44,44,45
CF 46,46,47
M 6,2,2
WL 6
LO 105,17,105
LU 17,105
M 6,3,3
s_add_u32 s37, s37, 32768
HS 24,17
GS 67
s_cmp_eq_u32 s37, s75
AW 148,44
AW 149,46
M 7,14,2
s_cselect_b32 s37, s74, s37
LL 19,62
LL 109,63
M 7,15,3
LA 19
RA 216,1,19456
LD 44,248,69
WL 4
RA 250,7,2816
CF 48,48,49
CF 50,50,51
AW 150,48
AW 151,50
LD 48,244,70
DW 1024
CF 40,40,41
CF 42,42,43
M 8,26,2
LC_ 109
M 8,27,3
GS 68
AW 152,40
AW 153,42
M 9,38,2
WL 5
LO 108,18,108
LU 18,108
M 9,39,3
DW 2048
HS 24,18
LD 40,240,70
CF 44,44,45
CF 46,46,47
AW 154,44
AW 155,46
M 10,50,2
WL 4
LL 16,64
LL 112,65
M 10,51,3
WL 1
LA 16
RA 220,1,20480
LD 44,252,71
CF 48,48,49
CF 50,50,51
M 6,4,4
LC_ 112
M 6,5,5
GS 69
AW 156,48
AW 157,50
M 7,16,4
WL 3
LO 109,19,109
LU 19,109
M 7,17,5
s_mul_i32 s53, s51, 49152
RA 224,1,21504
HS 24,19
LD 48,248,71
s_bitcmp1_b64 s[90:91], s51
CF 40,40,41
CF 42,42,43
AW 158,40
AW 159,42
M 8,28,4
s_cselect_b64 s[44:45], s[84:85], s[82:83]
LL 17,66
LL 113,67
M 8,29,5
s_add_u32 s44, s44, s53
LA 17
GS 70
s_addc_u32 s45, s45, 0
CF 44,44,45
CF 46,46,47
AW 160,44
AW 161,46
M 9,40,4
s_add_u32 s51, s51, %[d]
LC_ 113
M 9,41,5
s_cmp_eq_u32 s51, %[wrap]
CF 48,48,49
CF 50,50,51
AW 162,48
AW 163,50
s_nop 1
GS 71
s_cselect_b32 s51, %[rst], s51
M 10,52,4
WL 3
LO 112,16,112
LU 16,112
M 10,53,5
s_add_u32 s40, s40, 3072
RA 228,1,22528
HS 24,16
RV 36,8,6912
M 6,6,0
s_addc_u32 s41, s41, 0
LL 18,68
LL 116,69
M 6,7,1
s_add_u32 m0, s38, 3072
LA 18
DW 0
v_cndmask_b32 v52, v52, v53, s[78:79]
v_cndmask_b32 v54, v54, v55, s[78:79]
v_cndmask_b32 v52, v52, v54, s[80:81]
v_mul_f32 v52, 0x3b2aaaab, v52
M 7,18,0
LC_ 116
M 7,19,1
)"
R"(WL 2
RA 232,1,23552
C 52,52,52
v_lshlrev_b32 v52, 16, v52
v_add_f32 v52, v36, v52
C 52,52,52
M 8,30,0
LO 113,17,113
LU 17,113
M 8,31,1
DW 1024
HS 24,17
v_lshlrev_b32 v52, 16, v52
v_sqrt_f32 v52, v52
M 9,42,0
LL 19,70
LL 117,71
M 9,43,1
s_lshl_b32 s53, s52, 8
LA 19
C 52,52,52
v_lshlrev_b32 v52, 16, v52
v_cmp_ge_f32 vcc, v37, v52
v_rcp_f32 v52, v52
M 10,54,0
LC_ 117
M 10,55,1
s_add_u32 s48, %[alo], s53
RA 236,1,24576
v_mul_f32 v52, v38, v52
v_cndmask_b32 v52, 0, v52, vcc
CF 52,52,52
M 6,8,2
WL 4
LO 116,18,116
LU 18,116
M 6,9,3
s_addc_u32 s49, %[ahi], 0
HS 24,18
v_add_u32 v1, s37, v0
ds_bpermute_b32 v13, v28, v52
M 7,20,2
s_add_u32 s52, s52, %[d]
LL 16,72
LL 120,73
M 7,21,3
s_cmp_eq_u32 s52, %[wrap]
LA 16
RA 216,1,0
M 8,32,2
s_cselect_b32 s52, %[rst], s52
LC_ 120
DW 2048
M 8,33,3
DQ 3072
v_and_b32 v22, 0xfffffcff, v1
M 9,44,2
WL 5
LO 117,19,117
LU 19,117
M 9,45,3
HS 24,19
M 10,56,2
WL 4
LL 17,74
LL 121,75
M 10,57,3
WL 3
LA 17
RA 220,1,1024
M 6,10,4
s_add_u32 s50, s50, %[d]
LC_ 121
RV 252,22,5120
AR 52,116
AR 53,117
AR 54,118
AR 55,119
XS 52
XS 53
XS 54
XS 55
AW 116,52
AW 117,53
AW 118,54
AW 119,55
AR 52,120
AR 53,121
AR 54,122
AR 55,123
XS 52
XS 53
XS 54
XS 55
AW 120,52
AW 121,53
AW 122,54
AW 123,55
AR 52,124
AR 53,125
AR 54,126
AR 55,127
XS 52
XS 53
XS 54
XS 55
AW 124,52
AW 125,53
AW 126,54
AW 127,55
AR 52,128
AR 53,129
AR 54,130
AR 55,131
XS 52
XS 53
XS 54
XS 55
AW 128,52
AW 129,53
AW 130,54
AW 131,55
AR 52,132
AR 53,133
AR 54,134
AR 55,135
XS 52
M 6,11,5
s_cmp_eq_u32 s50, %[wrap]
XS 53
XS 54
M 7,22,4
WL 1
LO 120,16,120
LU 16,120
M 7,23,5
s_cselect_b32 s50, %[rst], s50
RA 224,1,2048
HS 24,16
XS 55
AW 132,52
M 8,34,4
s_barrier
LL 18,80
LL 106,81
AW 133,53
M 8,35,5
s_sub_u32 s39, 0, s39
LA 18
ds_read_b64 v[14:15], v5 offset:0
AW 134,54
M 9,46,4
s_add_u32 s38, s38, 32768
LC_ 106
v_subrev_u32 v5, s39, v5
AW 135,55
AR 52,136
M 9,47,5
s_cmp_eq_u32 s38, s77
AR 53,137
AR 54,138
M 10,58,4
WL 4
LO 121,17,121
LU 17,121
M 10,59,5
s_cselect_b32 s38, s76, s38
RA 228,1,3072
HS 24,17
AR 55,139
XS 52
Z 0,0,0
LL 19,82
LL 107,83
M 0,1,1
LA 19
HX 24
Z 1,12,0
LC_ 107
W1 24,0
M 1,13,1
RA 232,1,4096
XS 53
Z 2,24,0
WL 5
LO 106,18,106
LU 18,106
M 2,25,1
HZ 25,18
v_add_f32 v14, v14, v15
Z 3,36,0
LL 16,84
LL 110,85
M 3,37,1
LA 16
v_add_f32_dpp v14, v14, v14 quad_perm:[1,0,3,2] row_mask:0xf bank_mask:0xf
XS 54
Z 4,48,0
LC_ 110
v_add_f32_dpp v14, v14, v14 quad_perm:[2,3,0,1] row_mask:0xf bank_mask:0xf
XS 55
M 4,49,1
s_add_u32 s42, s44, s60
RA 236,1,6144
v_add_f32_dpp v14, v14, v14 row_half_mirror row_mask:0xf bank_mask:0xf
AW 136,52
AW 137,53
Z 5,60,0
WL 5
LO 107,19,107
LU 19,107
AW 138,54
M 5,61,1
s_addc_u32 s43, s45, 0
ST 104
HS 25,19
AW 139,55
M 0,2,2
LL 17,86
LL 111,87
AR 52,140
AR 53,141
M 0,3,3
LA 17
v_cvt_f64_f32 v[14:15], v14
AR 54,142
M 1,14,2
LC_ 111
s_mov_b64 exec, s[54:55]
SP
s_mov_b64 exec, -1
AR 55,143
M 1,15,3
RA 216,1,7168
XS 52
XS 53
M 2,26,2
WL 4
LO 110,16,110
LU 16,110
XS 54
M 2,27,3
HS 25,16
XS 55
M 3,38,2
LL 18,88
LL 114,89
AW 140,52
M 3,39,3
LA 18
AW 141,53
AW 142,54
M 4,50,2
LC_ 114
AW 143,55
M 4,51,3
s_add_u32 s42, s44, s61
RA 220,1,8192
AR 52,144
M 5,62,2
WL 4
LO 111,17,111
LU 17,111
AR 53,145
AR 54,146
M 5,63,3
s_addc_u32 s43, s45, 0
HS 25,17
AR 55,147
M 0,4,4
s_mov_b32 m0, %[pm]
LL 19,90
LL 115,91
XS 52
M 0,5,5
LA 19
DF
XS 53
XS 54
M 1,16,4
LC_ 115
ST 108
XS 55
M 1,17,5
RA 224,1,9216
AW 144,52
M 2,28,4
WL 4
LO 114,18,114
LU 18,114
AW 145,53
AW 146,54
M 2,29,5
HS 25,18
AW 147,55
M 3,40,4
LL 16,92
LL 118,93
AR 52,148
M 3,41,5
LA 16
AR 53,149
M 4,52,4
LC_ 118
AR 54,150
AR 55,151
M 4,53,5
s_add_u32 s42, s44, s62
RA 228,1,10240
XS 52
M 5,64,4
WL 4
LO 115,19,115
LU 19,115
XS 53
M 5,65,5
s_addc_u32 s43, s45, 0
ST 112
HS 25,19
XS 54
XS 55
M 0,6,0
LL 17,94
LL 119,95
AW 148,52
M 0,7,1
LA 17
AW 149,53
M 1,18,0
LC_ 119
AW 150,54
AW 151,55
M 1,19,1
RA 232,1,11264
AR 52,152
M 2,30,0
WL 4
LO 118,16,118
LU 16,118
AR 53,153
M 2,31,1
RV 252,22,5392
HS 25,16
AR 54,154
AR 55,155
M 3,42,0
LL 18,96
LL 122,97
XS 52
M 3,43,1
LA 18
XS 53
M 4,54,0
LC_ 122
XS 54
M 4,55,1
s_add_u32 s42, s44, s63
RA 236,1,12288
XS 55
AW 152,52
M 5,66,0
WL 5
LO 119,17,119
LU 17,119
AW 153,53
M 5,67,1
s_addc_u32 s43, s45, 0
ST 116
HS 25,17
AW 154,54
M 0,8,2
LL 19,98
LL 123,99
AW 155,55
AR 52,156
M 0,9,3
LA 19
AR 53,157
M 1,20,2
LC_ 123
AR 54,158
M 1,21,3
RA 216,1,13312
AR 55,159
XS 52
M 2,32,2
WL 4
LO 122,18,122
LU 18,122
XS 53
M 2,33,3
HS 25,18
XS 54
M 3,44,2
XS 55
AW 156,52
M 3,45,3
AW 157,53
AW 158,54
M 4,56,2
)"
R"(AW 159,55
AR 52,160
M 4,57,3
s_add_u32 s42, s44, s64
RA 220,1,14336
AR 53,161
M 5,68,2
WL 2
LO 123,19,123
LU 19,123
M 5,69,3
s_addc_u32 s43, s45, 0
ST 120
HS 25,19
AR 54,162
M 0,10,4
HX 25
AR 55,163
M 0,11,5
W1 25,1024
XS 52
XS 53
M 1,22,4
v_add_u32 v4, s39, v4
XS 54
M 1,23,5
RA 224,1,15360
XS 55
M 2,34,4
LL 16,56
LL 104,57
AW 160,52
AW 161,53
M 2,35,5
LA 16
AW 162,54
M 3,46,4
LC_ 104
AW 163,55
M 3,47,5
v_min_u32 v35, v35, v13
M 4,58,4
M 4,59,5
RA 228,1,16384
M 5,70,4
WL 5
LL 17,58
LL 105,59
M 5,71,5
LA 17
Z 6,0,0
LC_ 105
M 6,1,1
Z 7,12,0
WL 3
LO 104,16,104
LU 16,104
M 7,13,1
RA 232,1,17408
HZ 24,16
Z 8,24,0
LL 18,60
LL 108,61
M 8,25,1
s_mul_i32 s53, s50, 25600
LA 18
Z 9,36,0
s_add_u32 s40, %[wlo], s53
LC_ 108
M 9,37,1
s_addc_u32 s41, %[whi], 0
Z 10,48,0
WL 3
LO 105,17,105
LU 17,105
M 10,49,1
s_mov_b32 m0, s38
RA 236,1,18432
HS 24,17
Z 11,60,0
LL 19,62
LL 109,63
M 11,61,1
LA 19
DW 0
M 6,2,2
LC_ 109
M 6,3,3
M 7,14,2
WL 3
LO 108,18,108
LU 18,108
M 7,15,3
s_add_u32 s37, s37, 32768
RA 216,1,19456
HS 24,18
M 8,26,2
s_cmp_eq_u32 s37, s75
LL 16,64
LL 112,65
M 8,27,3
s_cselect_b32 s37, s74, s37
LA 16
DW 1024
M 9,38,2
LC_ 112
M 9,39,3
M 10,50,2
WL 3
LO 109,19,109
LU 19,109
M 10,51,3
RA 220,1,20480
HS 24,19
M 11,62,2
LL 17,66
LL 113,67
M 11,63,3
LA 17
DW 2048
M 6,4,4
LC_ 113
M 6,5,5
M 7,16,4
WL 3
LO 112,16,112
LU 16,112
M 7,17,5
s_mul_i32 s53, s51, 49152
RA 224,1,21504
HS 24,16
M 8,28,4
s_bitcmp1_b64 s[90:91], s51
LL 18,68
LL 116,69
M 8,29,5
s_cselect_b64 s[44:45], s[84:85], s[82:83]
LA 18
M 9,40,4
s_add_u32 s44, s44, s53
LC_ 116
M 9,41,5
s_addc_u32 s45, s45, 0
M 10,52,4
WL 3
LO 113,17,113
LU 17,113
M 10,53,5
s_add_u32 s51, s51, %[d]
RA 228,1,22528
HS 24,17
M 11,64,4
LL 19,70
LL 117,71
M 11,65,5
s_cmp_eq_u32 s51, %[wrap]
LA 19
M 6,6,0
s_cselect_b32 s51, %[rst], s51
LC_ 117
M 6,7,1
s_add_u32 s40, s40, 3072
M 7,18,0
WL 3
LO 116,18,116
LU 18,116
M 7,19,1
s_addc_u32 s41, s41, 0
RA 232,1,23552
HS 24,18
M 8,30,0
s_add_u32 m0, s38, 3072
LL 16,72
LL 120,73
M 8,31,1
LA 16
DW 0
M 9,42,0
LC_ 120
DW 1024
M 9,43,1
M 10,54,0
WL 3
LO 117,19,117
LU 19,117
M 10,55,1
RA 236,1,24576
HS 24,19
M 11,66,0
LL 17,74
LL 121,75
M 11,67,1
LA 17
DW 2048
M 6,8,2
LC_ 121
M 6,9,3
s_lshl_b32 s53, s52, 8
v_add_u32 v1, s37, v0
M 7,20,2
WL 3
LO 120,16,120
LU 16,120
M 7,21,3
s_add_u32 s48, %[alo], s53
RA 216,1,0
HS 24,16
M 8,32,2
s_addc_u32 s49, %[ahi], 0
LL 18,76
LL 124,77
M 8,33,3
s_add_u32 s52, s52, %[d]
LA 18
v_and_b32 v22, 0xfffffcff, v1
M 9,44,2
s_cmp_eq_u32 s52, %[wrap]
LC_ 124
M 9,45,3
s_cselect_b32 s52, %[rst], s52
DQ 3072
M 10,56,2
WL 3
LO 121,17,121
LU 17,121
M 10,57,3
s_add_u32 s50, s50, %[d]
RA 220,1,1024
HS 24,17
M 11,68,2
LL 19,78
LL 125,79
M 11,69,3
s_cmp_eq_u32 s50, %[wrap]
LA 19
RV 252,22,5120
M 6,10,4
s_cselect_b32 s50, %[rst], s50
LC_ 125
M 6,11,5
WV 23
M 7,22,4
WL 4
LO 124,18,124
LU 18,124
M 7,23,5
s_add_u32 s38, s38, 32768
RA 224,1,2048
HS 24,18
M 8,34,4
s_cmp_eq_u32 s38, s77
LL 16,80
LL 106,81
M 8,35,5
s_cselect_b32 s38, s76, s38
LA 16
M 9,46,4
s_barrier
LC_ 106
M 9,47,5
s_sub_u32 s39, 0, s39
M 10,58,4
WL 3
LO 125,19,125
LU 19,125
M 10,59,5
RA 228,1,3072
HS 24,19
M 11,70,4
LL 17,82
LL 107,83
M 11,71,5
LA 17
HX 24
Z 0,0,0
LC_ 107
ds_read_b64 v[14:15], v5 offset:0
M 0,1,1
W1 24,0
v_subrev_u32 v5, s39, v5
Z 1,12,0
WL 5
LO 106,16,106
LU 16,106
M 1,13,1
RA 232,1,4096
HZ 25,16
Z 2,24,0
LL 18,84
LL 110,85
M 2,25,1
LA 18
Z 3,36,0
LC_ 110
M 3,37,1
s_add_u32 s42, s44, s60
Z 4,48,0
WL 5
LO 107,17,107
LU 17,107
M 4,49,1
s_addc_u32 s43, s45, 0
RA 236,1,6144
HS 25,17
Z 5,60,0
LL 19,86
LL 111,87
M 5,61,1
LA 19
ST 104
M 0,2,2
WL 6
LC_ 111
v_add_f32 v14, v14, v15
M 0,3,3
s_nop 0
v_add_f32_dpp v14, v14, v14 quad_perm:[1,0,3,2] row_mask:0xf bank_mask:0xf
M 1,14,2
WL 3
LO 110,18,110
LU 18,110
M 1,15,3
RA 216,1,7168
HS 25,18
M 2,26,2
LL 16,88
LL 114,89
M 2,27,3
LA 16
v_add_f32_dpp v14, v14, v14 quad_perm:[2,3,0,1] row_mask:0xf bank_mask:0xf
M 3,38,2
LC_ 114
v_add_f32_dpp v14, v14, v14 row_half_mirror row_mask:0xf bank_mask:0xf
M 3,39,3
s_add_u32 s42, s44, s61
v_cvt_f64_f32 v[14:15], v14
M 4,50,2
WL 3
LO 111,19,111
LU 19,111
M 4,51,3
s_addc_u32 s43, s45, 0
RA 220,1,8192
HS 25,19
M 5,62,2
LL 17,90
LL 115,91
M 5,63,3
LA 17
s_mov_b64 exec, s[54:55]
SP
s_mov_b64 exec, -1
M 0,4,4
LC_ 115
ST 108
M 0,5,5
s_mov_b32 m0, %[pm]
s_nop 0
DF
M 1,16,4
WL 3
LO 114,16,114
LU 16,114
M 1,17,5
RA 224,1,9216
HS 25,16
M 2,28,4
LL 18,92
LL 118,93
M 2,29,5
LA 18
M 3,40,4
LC_ 118
M 3,41,5
s_add_u32 s42, s44, s62
M 4,52,4
WL 3
LO 115,17,115
LU 17,115
M 4,53,5
s_addc_u32 s43, s45, 0
RA 228,1,10240
HS 25,17
M 5,64,4
LL 19,94
LL 119,95
M 5,65,5
LA 19
ST 112
M 0,6,0
LC_ 119
M 0,7,1
M 1,18,0
WL 3
LO 118,18,118
LU 18,118
M 1,19,1
RA 232,1,11264
HS 25,18
M 2,30,0
)"
R"(LL 16,96
LL 122,97
M 2,31,1
LA 16
RV 252,22,5392
M 3,42,0
LC_ 122
M 3,43,1
s_add_u32 s42, s44, s63
M 4,54,0
WL 4
LO 119,19,119
LU 19,119
M 4,55,1
s_addc_u32 s43, s45, 0
RA 236,1,12288
HS 25,19
M 5,66,0
LL 17,98
LL 123,99
M 5,67,1
LA 17
ST 116
M 0,8,2
LC_ 123
M 0,9,3
M 1,20,2
WL 3
LO 122,16,122
LU 16,122
M 1,21,3
RA 216,1,13312
HS 25,16
M 2,32,2
LL 18,100
LL 126,101
M 2,33,3
LA 18
M 3,44,2
LC_ 126
M 3,45,3
s_add_u32 s42, s44, s64
M 4,56,2
WL 3
LO 123,17,123
LU 17,123
M 4,57,3
s_addc_u32 s43, s45, 0
RA 220,1,14336
HS 25,17
M 5,68,2
LL 19,102
LL 127,103
M 5,69,3
LA 19
ST 120
M 0,10,4
LC_ 127
M 0,11,5
M 1,22,4
WL 3
LO 126,18,126
LU 18,126
M 1,23,5
RA 224,1,15360
HS 25,18
M 2,34,4
LL 16,56
LL 104,57
M 2,35,5
LA 16
M 3,46,4
LC_ 104
M 3,47,5
s_add_u32 s42, s44, s65
M 4,58,4
WL 3
LO 127,19,127
LU 19,127
M 4,59,5
s_addc_u32 s43, s45, 0
RA 228,1,16384
HS 25,19
M 5,70,4
LL 17,58
LL 105,59
M 5,71,5
LA 17
ST 124
Z 6,0,0
LC_ 105
HX 25
M 6,1,1
W1 25,1024
Z 7,12,0
WL 4
LO 104,16,104
LU 16,104
M 7,13,1
RA 232,1,17408
HZ 24,16
Z 8,24,0
LL 18,60
LL 108,61
M 8,25,1
s_mul_i32 s53, s50, 25600
LA 18
v_add_u32 v4, s39, v4
Z 9,36,0
s_add_u32 s40, %[wlo], s53
LC_ 108
M 9,37,1
s_addc_u32 s41, %[whi], 0
Z 10,48,0
WL 4
LO 105,17,105
LU 17,105
M 10,49,1
s_mov_b32 m0, s38
RA 236,1,18432
HS 24,17
Z 11,60,0
LL 19,62
LL 109,63
M 11,61,1
LA 19
DW 0
M 6,2,2
LC_ 109
M 6,3,3
M 7,14,2
WL 3
LO 108,18,108
LU 18,108
M 7,15,3
s_add_u32 s37, s37, 32768
RA 216,1,19456
HS 24,18
M 8,26,2
s_cmp_eq_u32 s37, s75
LL 16,64
LL 112,65
M 8,27,3
s_cselect_b32 s37, s74, s37
LA 16
DW 1024
M 9,38,2
LC_ 112
M 9,39,3
M 10,50,2
WL 3
LO 109,19,109
LU 19,109
M 10,51,3
RA 220,1,20480
HS 24,19
M 11,62,2
LL 17,66
LL 113,67
M 11,63,3
LA 17
DW 2048
M 6,4,4
LC_ 113
M 6,5,5
M 7,16,4
WL 3
LO 112,16,112
LU 16,112
M 7,17,5
s_mul_i32 s53, s51, 49152
RA 224,1,21504
HS 24,16
M 8,28,4
s_bitcmp1_b64 s[90:91], s51
LL 18,68
LL 116,69
M 8,29,5
s_cselect_b64 s[44:45], s[84:85], s[82:83]
LA 18
M 9,40,4
s_add_u32 s44, s44, s53
LC_ 116
M 9,41,5
s_addc_u32 s45, s45, 0
M 10,52,4
WL 3
LO 113,17,113
LU 17,113
M 10,53,5
s_add_u32 s51, s51, %[d]
RA 228,1,22528
HS 24,17
M 11,64,4
LL 19,70
LL 117,71
M 11,65,5
s_cmp_eq_u32 s51, %[wrap]
LA 19
M 6,6,0
s_cselect_b32 s51, %[rst], s51
LC_ 117
M 6,7,1
s_add_u32 s40, s40, 3072
M 7,18,0
WL 3
LO 116,18,116
LU 18,116
M 7,19,1
s_addc_u32 s41, s41, 0
RA 232,1,23552
HS 24,18
M 8,30,0
s_add_u32 m0, s38, 3072
LL 16,72
LL 120,73
M 8,31,1
LA 16
DW 0
M 9,42,0
LC_ 120
DW 1024
M 9,43,1
M 10,54,0
WL 3
LO 117,19,117
LU 19,117
M 10,55,1
RA 236,1,24576
HS 24,19
M 11,66,0
LL 17,74
LL 121,75
M 11,67,1
LA 17
DW 2048
M 6,8,2
LC_ 121
M 6,9,3
s_lshl_b32 s53, s52, 8
v_add_u32 v1, s37, v0
M 7,20,2
WL 3
LO 120,16,120
LU 16,120
M 7,21,3
s_add_u32 s48, %[alo], s53
RA 216,1,0
HS 24,16
M 8,32,2
s_addc_u32 s49, %[ahi], 0
LL 18,76
LL 124,77
M 8,33,3
s_add_u32 s52, s52, %[d]
LA 18
v_and_b32 v22, 0xfffffcff, v1
M 9,44,2
s_cmp_eq_u32 s52, %[wrap]
LC_ 124
M 9,45,3
s_cselect_b32 s52, %[rst], s52
DQ 3072
M 10,56,2
WL 3
LO 121,17,121
LU 17,121
M 10,57,3
s_add_u32 s50, s50, %[d]
RA 220,1,1024
HS 24,17
M 11,68,2
LL 19,78
LL 125,79
M 11,69,3
s_cmp_eq_u32 s50, %[wrap]
LA 19
RV 252,22,5120
M 6,10,4
s_cselect_b32 s50, %[rst], s50
LC_ 125
M 6,11,5
WV 29
M 7,22,4
WL 4
LO 124,18,124
LU 18,124
M 7,23,5
s_add_u32 s38, s38, 32768
RA 224,1,2048
HS 24,18
M 8,34,4
s_cmp_eq_u32 s38, s77
LL 16,80
LL 106,81
M 8,35,5
s_cselect_b32 s38, s76, s38
LA 16
M 9,46,4
s_barrier
LC_ 106
M 9,47,5
s_sub_u32 s39, 0, s39
M 10,58,4
WL 3
LO 125,19,125
LU 19,125
M 10,59,5
RA 228,1,3072
HS 24,19
M 11,70,4
LL 17,82
LL 107,83
M 11,71,5
s_waitcnt lgkmcnt(0)
s_bcnt1_i32_b64 vcc_lo, s[66:67]
s_lshr_b32 s66, %[pf], 12
s_mul_i32 s66, s66, 5
s_lshr_b32 s66, s66, 5
s_add_u32 s66, s66, 1
s_lshl_b32 s67, s66, 25
s_sub_u32 s67, 306319492, s67
s_sub_u32 s66, s66, 1
s_mul_i32 s46, %[pf], 288
s_mul_i32 s47, %[s2], 147456
s_add_u32 s46, s46, s47
s_mul_i32 s47, %[tsel], 9216
s_add_u32 s46, s46, s47
s_add_u32 s46, s46, 75497472
s_add_u32 s46, s46, %[xlo]
s_addc_u32 s47, %[xhi], 0
v_min_u32 v3, 0x2f0, v0
v_lshlrev_b32 v3, 3, v3
s_cmp_gt_u32 vcc_lo, 8
s_cselect_b32 s66, s66, 28
s_cselect_b32 s67, s67, 0
1:
LA 17
HX 24
Z 0,0,0
LC_ 107
ds_read_b64 v[14:15], v5 offset:0
M 0,1,1
W1 24,0
v_subrev_u32 v5, s39, v5
Z 1,12,0
WL 5
LO 106,16,106
LU 16,106
M 1,13,1
RA 232,1,4096
HZ 25,16
Z 2,24,0
LL 18,84
LL 110,85
M 2,25,1
LA 18
Z 3,36,0
LC_ 110
M 3,37,1
s_add_u32 s42, s44, s60
Z 4,48,0
WL 5
LO 107,17,107
LU 17,107
M 4,49,1
s_addc_u32 s43, s45, 0
RA 236,1,6144
HS 25,17
Z 5,60,0
LL 19,86
LL 111,87
M 5,61,1
LA 19
ST 104
M 0,2,2
WL 6
LC_ 111
v_add_f32 v14, v14, v15
M 0,3,3
s_nop 0
v_add_f32_dpp v14, v14, v14 quad_perm:[1,0,3,2] row_mask:0xf bank_mask:0xf
M 1,14,2
WL 3
LO 110,18,110
LU 18,110
M 1,15,3
RA 216,1,7168
HS 25,18
M 2,26,2
LL 16,88
LL 114,89
M 2,27,3
LA 16
v_add_f32_dpp v14, v14, v14 quad_perm:[2,3,0,1] row_mask:0xf bank_mask:0xf
M 3,38,2
LC_ 114
v_add_f32_dpp v14, v14, v14 row_half_mirror row_mask:0xf bank_mask:0xf
M 3,39,3
s_add_u32 s42, s44, s61
)"
R"(v_cvt_f64_f32 v[14:15], v14
M 4,50,2
WL 3
LO 111,19,111
LU 19,111
M 4,51,3
s_addc_u32 s43, s45, 0
RA 220,1,8192
HS 25,19
M 5,62,2
LL 17,90
LL 115,91
M 5,63,3
LA 17
s_mov_b64 exec, s[54:55]
SP
s_mov_b64 exec, -1
M 0,4,4
LC_ 115
ST 108
M 0,5,5
s_mov_b32 m0, %[pm]
s_nop 0
DF
M 1,16,4
WL 3
LO 114,16,114
LU 16,114
M 1,17,5
RA 224,1,9216
HS 25,16
M 2,28,4
LL 18,92
LL 118,93
M 2,29,5
LA 18
M 3,40,4
LC_ 118
M 3,41,5
s_add_u32 s42, s44, s62
M 4,52,4
WL 3
LO 115,17,115
LU 17,115
M 4,53,5
s_addc_u32 s43, s45, 0
RA 228,1,10240
HS 25,17
M 5,64,4
LL 19,94
LL 119,95
M 5,65,5
LA 19
ST 112
M 0,6,0
LC_ 119
M 0,7,1
M 1,18,0
WL 3
LO 118,18,118
LU 18,118
M 1,19,1
RA 232,1,11264
HS 25,18
M 2,30,0
LL 16,96
LL 122,97
M 2,31,1
LA 16
RV 252,22,5392
M 3,42,0
LC_ 122
M 3,43,1
s_add_u32 s42, s44, s63
M 4,54,0
WL 4
LO 119,19,119
LU 19,119
M 4,55,1
s_addc_u32 s43, s45, 0
RA 236,1,12288
HS 25,19
M 5,66,0
LL 17,98
LL 123,99
M 5,67,1
LA 17
ST 116
M 0,8,2
LC_ 123
M 0,9,3
M 1,20,2
WL 3
LO 122,16,122
LU 16,122
M 1,21,3
RA 216,1,13312
HS 25,16
M 2,32,2
LL 18,100
LL 126,101
M 2,33,3
LA 18
M 3,44,2
LC_ 126
M 3,45,3
s_add_u32 s42, s44, s64
M 4,56,2
WL 3
LO 123,17,123
LU 17,123
M 4,57,3
s_addc_u32 s43, s45, 0
RA 220,1,14336
HS 25,17
M 5,68,2
LL 19,102
LL 127,103
M 5,69,3
LA 19
ST 120
M 0,10,4
LC_ 127
M 0,11,5
M 1,22,4
WL 3
LO 126,18,126
LU 18,126
M 1,23,5
RA 224,1,15360
HS 25,18
M 2,34,4
LL 16,56
LL 104,57
M 2,35,5
LA 16
M 3,46,4
LC_ 104
M 3,47,5
s_add_u32 s42, s44, s65
M 4,58,4
WL 3
LO 127,19,127
LU 19,127
M 4,59,5
s_addc_u32 s43, s45, 0
RA 228,1,16384
HS 25,19
M 5,70,4
LL 17,58
LL 105,59
M 5,71,5
LA 17
ST 124
Z 6,0,0
LC_ 105
HX 25
M 6,1,1
W1 25,1024
Z 7,12,0
WL 4
LO 104,16,104
LU 16,104
M 7,13,1
RA 232,1,17408
HZ 24,16
Z 8,24,0
LL 18,60
LL 108,61
M 8,25,1
s_mul_i32 s53, s50, 25600
LA 18
v_add_u32 v4, s39, v4
Z 9,36,0
s_add_u32 s40, %[wlo], s53
LC_ 108
M 9,37,1
s_addc_u32 s41, %[whi], 0
Z 10,48,0
WL 4
LO 105,17,105
LU 17,105
M 10,49,1
s_mov_b32 m0, s38
RA 236,1,18432
HS 24,17
Z 11,60,0
LL 19,62
LL 109,63
M 11,61,1
LA 19
DW 0
M 6,2,2
LC_ 109
M 6,3,3
M 7,14,2
WL 3
LO 108,18,108
LU 18,108
M 7,15,3
s_add_u32 s37, s37, 32768
RA 216,1,19456
HS 24,18
M 8,26,2
s_cmp_eq_u32 s37, s75
LL 16,64
LL 112,65
M 8,27,3
s_cselect_b32 s37, s74, s37
LA 16
DW 1024
M 9,38,2
LC_ 112
M 9,39,3
M 10,50,2
WL 3
LO 109,19,109
LU 19,109
M 10,51,3
RA 220,1,20480
HS 24,19
M 11,62,2
LL 17,66
LL 113,67
M 11,63,3
LA 17
DW 2048
M 6,4,4
LC_ 113
M 6,5,5
M 7,16,4
WL 3
LO 112,16,112
LU 16,112
M 7,17,5
s_mul_i32 s53, s51, 49152
RA 224,1,21504
HS 24,16
M 8,28,4
s_bitcmp1_b64 s[90:91], s51
LL 18,68
LL 116,69
M 8,29,5
s_cselect_b64 s[44:45], s[84:85], s[82:83]
LA 18
M 9,40,4
s_add_u32 s44, s44, s53
LC_ 116
M 9,41,5
s_addc_u32 s45, s45, 0
M 10,52,4
WL 3
LO 113,17,113
LU 17,113
M 10,53,5
s_add_u32 s51, s51, %[d]
RA 228,1,22528
HS 24,17
M 11,64,4
LL 19,70
LL 117,71
M 11,65,5
s_cmp_eq_u32 s51, %[wrap]
LA 19
M 6,6,0
s_cselect_b32 s51, %[rst], s51
LC_ 117
M 6,7,1
s_add_u32 s40, s40, 3072
M 7,18,0
WL 3
LO 116,18,116
LU 18,116
M 7,19,1
s_addc_u32 s41, s41, 0
RA 232,1,23552
HS 24,18
M 8,30,0
s_add_u32 m0, s38, 3072
LL 16,72
LL 120,73
M 8,31,1
LA 16
DW 0
M 9,42,0
LC_ 120
DW 1024
M 9,43,1
M 10,54,0
WL 3
LO 117,19,117
LU 19,117
M 10,55,1
RA 236,1,24576
HS 24,19
M 11,66,0
LL 17,74
LL 121,75
M 11,67,1
LA 17
DW 2048
M 6,8,2
LC_ 121
M 6,9,3
s_lshl_b32 s53, s52, 8
v_add_u32 v1, s37, v0
M 7,20,2
WL 3
LO 120,16,120
LU 16,120
M 7,21,3
s_add_u32 s48, %[alo], s53
RA 216,1,0
HS 24,16
M 8,32,2
s_addc_u32 s49, %[ahi], 0
LL 18,76
LL 124,77
M 8,33,3
s_add_u32 s52, s52, %[d]
LA 18
v_and_b32 v22, 0xfffffcff, v1
M 9,44,2
s_cmp_eq_u32 s52, %[wrap]
LC_ 124
M 9,45,3
s_cselect_b32 s52, %[rst], s52
DQ 3072
M 10,56,2
WL 3
LO 121,17,121
LU 17,121
M 10,57,3
s_add_u32 s50, s50, %[d]
RA 220,1,1024
HS 24,17
M 11,68,2
LL 19,78
LL 125,79
M 11,69,3
s_cmp_eq_u32 s50, %[wrap]
LA 19
RV 252,22,5120
M 6,10,4
s_cselect_b32 s50, %[rst], s50
LC_ 125
M 6,11,5
WV 30
M 7,22,4
WL 4
LO 124,18,124
LU 18,124
M 7,23,5
s_add_u32 s38, s38, 32768
RA 224,1,2048
HS 24,18
M 8,34,4
s_cmp_eq_u32 s38, s77
LL 16,80
LL 106,81
M 8,35,5
s_cselect_b32 s38, s76, s38
LA 16
M 9,46,4
s_barrier
LC_ 106
M 9,47,5
s_sub_u32 s39, 0, s39
M 10,58,4
WL 3
LO 125,19,125
LU 19,125
M 10,59,5
RA 228,1,3072
HS 24,19
M 11,70,4
s_sub_u32 s66, s66, 1
LL 17,82
LL 107,83
M 11,71,5
s_cbranch_scc0 1b
s_cmp_eq_u32 s67, 0
s_cbranch_scc1 7f
s_mov_b32 m0, %[pm]
s_and_b32 s66, s67, 31
s_sub_u32 s66, s66, 1
s_lshr_b32 s67, s67, 5
DX
s_add_u32 s46, s46, 6144
s_addc_u32 s47, s47, 0
s_branch 1b
7:
LA 17
HX 24
Z 0,0,0
LC_ 107
ds_read_b64 v[14:15], v5 offset:0
M 0,1,1
W1 24,0
v_subrev_u32 v5, s39, v5
)"
R"(Z 1,12,0
WL 5
LO 106,16,106
LU 16,106
M 1,13,1
RA 232,1,4096
HZ 25,16
Z 2,24,0
LL 18,84
LL 110,85
M 2,25,1
LA 18
Z 3,36,0
LC_ 110
M 3,37,1
s_add_u32 s42, s44, s60
Z 4,48,0
WL 5
LO 107,17,107
LU 17,107
M 4,49,1
s_addc_u32 s43, s45, 0
RA 236,1,6144
HS 25,17
Z 5,60,0
LL 19,86
LL 111,87
M 5,61,1
LA 19
ST 104
M 0,2,2
WL 6
LC_ 111
v_add_f32 v14, v14, v15
M 0,3,3
s_nop 0
v_add_f32_dpp v14, v14, v14 quad_perm:[1,0,3,2] row_mask:0xf bank_mask:0xf
M 1,14,2
WL 3
LO 110,18,110
LU 18,110
M 1,15,3
RA 216,1,7168
HS 25,18
M 2,26,2
LL 16,88
LL 114,89
M 2,27,3
LA 16
v_add_f32_dpp v14, v14, v14 quad_perm:[2,3,0,1] row_mask:0xf bank_mask:0xf
M 3,38,2
LC_ 114
v_add_f32_dpp v14, v14, v14 row_half_mirror row_mask:0xf bank_mask:0xf
M 3,39,3
s_add_u32 s42, s44, s61
v_cvt_f64_f32 v[14:15], v14
M 4,50,2
WL 3
LO 111,19,111
LU 19,111
M 4,51,3
s_addc_u32 s43, s45, 0
RA 220,1,8192
HS 25,19
M 5,62,2
LL 17,90
LL 115,91
M 5,63,3
LA 17
s_mov_b64 exec, s[54:55]
SP
s_mov_b64 exec, -1
M 0,4,4
LC_ 115
ST 108
M 0,5,5
s_mov_b32 m0, %[pm]
s_nop 0
DF
M 1,16,4
WL 3
LO 114,16,114
LU 16,114
M 1,17,5
RA 224,1,9216
HS 25,16
M 2,28,4
LL 18,92
LL 118,93
M 2,29,5
LA 18
M 3,40,4
LC_ 118
M 3,41,5
s_add_u32 s42, s44, s62
M 4,52,4
WL 3
LO 115,17,115
LU 17,115
M 4,53,5
s_addc_u32 s43, s45, 0
RA 228,1,10240
HS 25,17
M 5,64,4
LL 19,94
LL 119,95
M 5,65,5
LA 19
ST 112
M 0,6,0
LC_ 119
M 0,7,1
M 1,18,0
WL 3
LO 118,18,118
LU 18,118
M 1,19,1
RA 232,1,11264
HS 25,18
M 2,30,0
LL 16,96
LL 122,97
M 2,31,1
LA 16
RV 252,22,5392
M 3,42,0
LC_ 122
M 3,43,1
s_add_u32 s42, s44, s63
M 4,54,0
WL 4
LO 119,19,119
LU 19,119
M 4,55,1
s_addc_u32 s43, s45, 0
RA 236,1,12288
HS 25,19
M 5,66,0
LL 17,98
LL 123,99
M 5,67,1
LA 17
ST 116
M 0,8,2
LC_ 123
M 0,9,3
M 1,20,2
WL 3
LO 122,16,122
LU 16,122
M 1,21,3
RA 216,1,13312
HS 25,16
M 2,32,2
LL 18,100
LL 126,101
M 2,33,3
LA 18
M 3,44,2
LC_ 126
M 3,45,3
s_add_u32 s42, s44, s64
M 4,56,2
WL 3
LO 123,17,123
LU 17,123
M 4,57,3
s_addc_u32 s43, s45, 0
RA 220,1,14336
HS 25,17
M 5,68,2
LL 19,102
LL 127,103
M 5,69,3
LA 19
ST 120
M 0,10,4
LC_ 127
M 0,11,5
M 1,22,4
WL 3
LO 126,18,126
LU 18,126
M 1,23,5
RA 224,1,15360
HS 25,18
M 2,34,4
LL 16,56
LL 104,57
M 2,35,5
LA 16
M 3,46,4
LC_ 104
M 3,47,5
s_add_u32 s42, s44, s65
M 4,58,4
WL 3
LO 127,19,127
LU 19,127
M 4,59,5
s_addc_u32 s43, s45, 0
RA 228,1,16384
HS 25,19
M 5,70,4
LL 17,58
LL 105,59
M 5,71,5
LA 17
ST 124
Z 6,0,0
LC_ 105
HX 25
M 6,1,1
W1 25,1024
Z 7,12,0
WL 4
LO 104,16,104
LU 16,104
M 7,13,1
RA 232,1,17408
HZ 24,16
Z 8,24,0
LL 18,60
LL 108,61
M 8,25,1
s_mul_i32 s53, s50, 25600
LA 18
v_add_u32 v4, s39, v4
Z 9,36,0
s_add_u32 s40, %[wlo], s53
LC_ 108
s_mov_b64 exec, 1
AT
s_mov_b64 exec, -1
M 9,37,1
s_addc_u32 s41, %[whi], 0
Z 10,48,0
WL 4
LO 105,17,105
LU 17,105
M 10,49,1
s_mov_b32 m0, s38
RA 236,1,18432
HS 24,17
Z 11,60,0
LL 19,62
LL 109,63
M 11,61,1
LA 19
DW 0
M 6,2,2
LC_ 109
M 6,3,3
M 7,14,2
WL 3
LO 108,18,108
LU 18,108
M 7,15,3
s_add_u32 s37, s37, 32768
RA 216,1,19456
HS 24,18
M 8,26,2
s_cmp_eq_u32 s37, s75
LL 16,64
LL 112,65
M 8,27,3
s_cselect_b32 s37, s74, s37
LA 16
DW 1024
M 9,38,2
LC_ 112
M 9,39,3
M 10,50,2
WL 3
LO 109,19,109
LU 19,109
M 10,51,3
RA 220,1,20480
HS 24,19
M 11,62,2
LL 17,66
LL 113,67
M 11,63,3
LA 17
DW 2048
M 6,4,4
LC_ 113
M 6,5,5
M 7,16,4
WL 3
LO 112,16,112
LU 16,112
M 7,17,5
s_mul_i32 s53, s51, 49152
RA 224,1,21504
HS 24,16
M 8,28,4
s_bitcmp1_b64 s[90:91], s51
LL 18,68
LL 116,69
M 8,29,5
s_cselect_b64 s[44:45], s[84:85], s[82:83]
LA 18
M 9,40,4
s_add_u32 s44, s44, s53
LC_ 116
M 9,41,5
s_addc_u32 s45, s45, 0
M 10,52,4
WL 3
LO 113,17,113
LU 17,113
M 10,53,5
s_add_u32 s51, s51, %[d]
RA 228,1,22528
HS 24,17
M 11,64,4
LL 19,70
LL 117,71
M 11,65,5
s_cmp_eq_u32 s51, %[wrap]
LA 19
M 6,6,0
s_cselect_b32 s51, %[rst], s51
LC_ 117
M 6,7,1
s_add_u32 s40, s40, 3072
M 7,18,0
WL 3
LO 116,18,116
LU 18,116
M 7,19,1
s_addc_u32 s41, s41, 0
RA 232,1,23552
HS 24,18
M 8,30,0
s_add_u32 m0, s38, 3072
LL 16,72
LL 120,73
M 8,31,1
LA 16
DW 0
M 9,42,0
LC_ 120
DW 1024
M 9,43,1
M 10,54,0
WL 3
LO 117,19,117
LU 19,117
M 10,55,1
RA 236,1,24576
HS 24,19
M 11,66,0
LL 17,74
LL 121,75
M 11,67,1
LA 17
DW 2048
M 6,8,2
LC_ 121
M 6,9,3
s_lshl_b32 s53, s52, 8
v_add_u32 v1, s37, v0
M 7,20,2
WL 3
LO 120,16,120
LU 16,120
M 7,21,3
s_add_u32 s48, %[alo], s53
RA 216,1,0
HS 24,16
M 8,32,2
s_addc_u32 s49, %[ahi], 0
LL 18,76
LL 124,77
M 8,33,3
s_add_u32 s52, s52, %[d]
LA 18
v_and_b32 v22, 0xfffffcff, v1
M 9,44,2
s_cmp_eq_u32 s52, %[wrap]
LC_ 124
M 9,45,3
s_cselect_b32 s52, %[rst], s52
DQ 3072
M 10,56,2
WL 3
LO 121,17,121
LU 17,121
M 10,57,3
s_add_u32 s50, s50, %[d]
RA 220,1,1024
HS 24,17
M 11,68,2
LL 19,78
LL 125,79
M 11,69,3
s_cmp_eq_u32 s50, %[wrap]
LA 19
)"
R"(RV 252,22,5120
M 6,10,4
s_cselect_b32 s50, %[rst], s50
LC_ 125
M 6,11,5
WV 31
M 7,22,4
WL 4
LO 124,18,124
LU 18,124
M 7,23,5
s_add_u32 s38, s38, 32768
RA 224,1,2048
HS 24,18
M 8,34,4
s_cmp_eq_u32 s38, s77
LL 16,80
LL 106,81
M 8,35,5
s_cselect_b32 s38, s76, s38
LA 16
M 9,46,4
s_barrier
LC_ 106
M 9,47,5
s_sub_u32 s39, 0, s39
M 10,58,4
WL 3
LO 125,19,125
LU 19,125
M 10,59,5
RA 228,1,3072
HS 24,19
M 11,70,4
LL 17,82
LL 107,83
M 11,71,5
LA 17
HX 24
Z 0,0,0
LC_ 107
ds_read_b64 v[14:15], v5 offset:0
M 0,1,1
W1 24,0
v_subrev_u32 v5, s39, v5
Z 1,12,0
WL 5
LO 106,16,106
LU 16,106
M 1,13,1
RA 232,1,4096
HZ 25,16
Z 2,24,0
LL 18,84
LL 110,85
M 2,25,1
LA 18
Z 3,36,0
LC_ 110
M 3,37,1
s_add_u32 s42, s44, s60
Z 4,48,0
WL 5
LO 107,17,107
LU 17,107
M 4,49,1
s_addc_u32 s43, s45, 0
RA 236,1,6144
HS 25,17
Z 5,60,0
LL 19,86
LL 111,87
M 5,61,1
LA 19
ST 104
M 0,2,2
WL 6
LC_ 111
v_add_f32 v14, v14, v15
M 0,3,3
s_nop 0
v_add_f32_dpp v14, v14, v14 quad_perm:[1,0,3,2] row_mask:0xf bank_mask:0xf
M 1,14,2
WL 3
LO 110,18,110
LU 18,110
M 1,15,3
RA 216,1,7168
HS 25,18
M 2,26,2
LL 16,88
LL 114,89
M 2,27,3
LA 16
v_add_f32_dpp v14, v14, v14 quad_perm:[2,3,0,1] row_mask:0xf bank_mask:0xf
M 3,38,2
LC_ 114
v_add_f32_dpp v14, v14, v14 row_half_mirror row_mask:0xf bank_mask:0xf
M 3,39,3
s_add_u32 s42, s44, s61
v_cvt_f64_f32 v[14:15], v14
M 4,50,2
WL 3
LO 111,19,111
LU 19,111
M 4,51,3
s_addc_u32 s43, s45, 0
RA 220,1,8192
HS 25,19
M 5,62,2
LL 17,90
LL 115,91
M 5,63,3
LA 17
s_mov_b64 exec, s[54:55]
SP
s_mov_b64 exec, -1
M 0,4,4
LC_ 115
ST 108
M 0,5,5
s_mov_b32 m0, %[pm]
s_nop 0
DF
M 1,16,4
WL 3
LO 114,16,114
LU 16,114
M 1,17,5
RA 224,1,9216
HS 25,16
M 2,28,4
LL 18,92
LL 118,93
M 2,29,5
LA 18
M 3,40,4
LC_ 118
M 3,41,5
s_add_u32 s42, s44, s62
M 4,52,4
WL 3
LO 115,17,115
LU 17,115
M 4,53,5
s_addc_u32 s43, s45, 0
RA 228,1,10240
HS 25,17
M 5,64,4
LL 19,94
LL 119,95
M 5,65,5
LA 19
ST 112
M 0,6,0
LC_ 119
M 0,7,1
M 1,18,0
WL 3
LO 118,18,118
LU 18,118
M 1,19,1
RA 232,1,11264
HS 25,18
M 2,30,0
LL 16,96
LL 122,97
M 2,31,1
LA 16
RV 252,22,5392
M 3,42,0
LC_ 122
M 3,43,1
s_add_u32 s42, s44, s63
M 4,54,0
WL 4
LO 119,19,119
LU 19,119
M 4,55,1
s_addc_u32 s43, s45, 0
RA 236,1,12288
HS 25,19
M 5,66,0
LL 17,98
LL 123,99
M 5,67,1
LA 17
ST 116
M 0,8,2
LC_ 123
M 0,9,3
M 1,20,2
WL 3
LO 122,16,122
LU 16,122
M 1,21,3
RA 216,1,13312
HS 25,16
M 2,32,2
LL 18,100
LL 126,101
M 2,33,3
LA 18
M 3,44,2
LC_ 126
M 3,45,3
s_add_u32 s42, s44, s64
M 4,56,2
WL 3
LO 123,17,123
LU 17,123
M 4,57,3
s_addc_u32 s43, s45, 0
RA 220,1,14336
HS 25,17
M 5,68,2
LL 19,102
LL 127,103
M 5,69,3
LA 19
ST 120
M 0,10,4
LC_ 127
M 0,11,5
M 1,22,4
WL 3
LO 126,18,126
LU 18,126
M 1,23,5
RA 224,1,15360
HS 25,18
M 2,34,4
LL 16,56
LL 104,57
M 2,35,5
LA 16
M 3,46,4
LC_ 104
M 3,47,5
s_add_u32 s42, s44, s65
M 4,58,4
WL 3
LO 127,19,127
LU 19,127
M 4,59,5
s_addc_u32 s43, s45, 0
RA 228,1,16384
HS 25,19
M 5,70,4
LL 17,58
LL 105,59
M 5,71,5
LA 17
ST 124
Z 6,0,0
LC_ 105
HX 25
M 6,1,1
WV 15
W1 25,1024
v_readfirstlane_b32 s72, v11
Z 7,12,0
WL 4
LO 104,16,104
LU 16,104
M 7,13,1
RA 232,1,17408
HZ 24,16
Z 8,24,0
LL 18,60
LL 108,61
M 8,25,1
s_mul_i32 s53, s50, 25600
LA 18
v_mov_b32 v12, s72
Z 9,36,0
s_add_u32 s40, %[wlo], s53
LC_ 108
W2 12,12,16
M 9,37,1
s_addc_u32 s41, %[whi], 0
v_add_u32 v4, s39, v4
Z 10,48,0
WL 5
LO 105,17,105
LU 17,105
M 10,49,1
s_mov_b32 m0, s38
RA 236,1,18432
HS 24,17
Z 11,60,0
LL 19,62
LL 109,63
M 11,61,1
LA 19
DW 0
M 6,2,2
LC_ 109
M 6,3,3
M 7,14,2
WL 4
LO 108,18,108
LU 18,108
M 7,15,3
s_add_u32 s37, s37, 32768
RA 216,1,19456
HS 24,18
M 8,26,2
s_cmp_eq_u32 s37, s75
LL 16,64
LL 112,65
M 8,27,3
s_cselect_b32 s37, s74, s37
LA 16
DW 1024
M 9,38,2
LC_ 112
M 9,39,3
M 10,50,2
WL 3
LO 109,19,109
LU 19,109
M 10,51,3
RA 220,1,20480
HS 24,19
M 11,62,2
LL 17,66
LL 113,67
M 11,63,3
LA 17
DW 2048
M 6,4,4
LC_ 113
M 6,5,5
M 7,16,4
WL 3
LO 112,16,112
LU 16,112
M 7,17,5
s_mul_i32 s53, s51, 49152
RA 224,1,21504
HS 24,16
M 8,28,4
s_bitcmp1_b64 s[90:91], s51
LL 18,68
LL 116,69
M 8,29,5
s_cselect_b64 s[44:45], s[84:85], s[82:83]
LA 18
M 9,40,4
s_add_u32 s44, s44, s53
LC_ 116
M 9,41,5
s_addc_u32 s45, s45, 0
M 10,52,4
WL 3
LO 113,17,113
LU 17,113
M 10,53,5
s_add_u32 s51, s51, %[d]
RA 228,1,22528
HS 24,17
M 11,64,4
LL 19,70
LL 117,71
M 11,65,5
s_cmp_eq_u32 s51, %[wrap]
LA 19
M 6,6,0
s_cselect_b32 s51, %[rst], s51
LC_ 117
M 6,7,1
s_add_u32 s40, s40, 3072
M 7,18,0
WL 3
LO 116,18,116
LU 18,116
M 7,19,1
s_addc_u32 s41, s41, 0
RA 232,1,23552
HS 24,18
M 8,30,0
s_add_u32 m0, s38, 3072
LL 16,72
LL 120,73
M 8,31,1
LA 16
DW 0
M 9,42,0
LC_ 120
DW 1024
M 9,43,1
M 10,54,0
WL 3
LO 117,19,117
LU 19,117
M 10,55,1
RA 236,1,24576
HS 24,19
M 11,66,0
)"
R"(LL 17,74
LL 121,75
M 11,67,1
LA 17
DW 2048
M 6,8,2
LC_ 121
M 6,9,3
s_lshl_b32 s53, s52, 8
v_add_u32 v1, s37, v0
M 7,20,2
WL 3
LO 120,16,120
LU 16,120
M 7,21,3
s_add_u32 s48, %[alo], s53
RA 216,1,0
HS 24,16
M 8,32,2
s_addc_u32 s49, %[ahi], 0
LL 18,76
LL 124,77
M 8,33,3
s_add_u32 s52, s52, %[d]
LA 18
v_and_b32 v22, 0xfffffcff, v1
M 9,44,2
s_cmp_eq_u32 s52, %[wrap]
LC_ 124
M 9,45,3
s_cselect_b32 s52, %[rst], s52
DQ 3072
M 10,56,2
WL 3
LO 121,17,121
LU 17,121
M 10,57,3
s_add_u32 s50, s50, %[d]
RA 220,1,1024
HS 24,17
M 11,68,2
LL 19,78
LL 125,79
M 11,69,3
s_cmp_eq_u32 s50, %[wrap]
LA 19
RV 252,22,5120
M 6,10,4
s_cselect_b32 s50, %[rst], s50
LC_ 125
M 6,11,5
M 7,22,4
WL 4
LO 124,18,124
LU 18,124
M 7,23,5
s_add_u32 s38, s38, 32768
RA 224,1,2048
HS 24,18
M 8,34,4
s_cmp_eq_u32 s38, s77
LL 16,80
LL 106,81
M 8,35,5
s_cselect_b32 s38, s76, s38
LA 16
M 9,46,4
s_barrier
LC_ 106
M 9,47,5
s_sub_u32 s39, 0, s39
M 10,58,4
WL 3
LO 125,19,125
LU 19,125
M 10,59,5
RA 228,1,3072
HS 24,19
M 11,70,4
LL 17,82
LL 107,83
M 11,71,5
LA 17
HX 24
Z 0,0,0
LC_ 107
ds_read_b32 v20, v5 offset:4096
M 0,1,1
ds_read_b64 v[14:15], v5 offset:0
Z 1,12,0
WL 5
LO 106,16,106
LU 16,106
M 1,13,1
RA 232,1,4096
HZ 25,16
Z 2,24,0
LL 18,84
LL 110,85
M 2,25,1
LA 18
W1 24,0
Z 3,36,0
LC_ 110
v_subrev_u32 v5, s39, v5
M 3,37,1
s_add_u32 s42, s44, s60
Z 4,48,0
WL 6
LO 107,17,107
LU 17,107
M 4,49,1
s_addc_u32 s43, s45, 0
RA 236,1,6144
HS 25,17
Z 5,60,0
LL 19,86
LL 111,87
M 5,61,1
LA 19
ST 104
M 0,2,2
WL 6
LC_ 111
v_add_f32 v14, v14, v15
M 0,3,3
s_nop 0
v_add_f32_dpp v14, v14, v14 quad_perm:[1,0,3,2] row_mask:0xf bank_mask:0xf
M 1,14,2
WL 3
LO 110,18,110
LU 18,110
M 1,15,3
RA 216,1,7168
HS 25,18
M 2,26,2
LL 16,88
LL 114,89
M 2,27,3
LA 16
v_add_f32_dpp v14, v14, v14 quad_perm:[2,3,0,1] row_mask:0xf bank_mask:0xf
M 3,38,2
LC_ 114
v_add_f32_dpp v14, v14, v14 row_half_mirror row_mask:0xf bank_mask:0xf
M 3,39,3
s_add_u32 s42, s44, s61
v_cvt_f64_f32 v[14:15], v14
M 4,50,2
WL 3
LO 111,19,111
LU 19,111
M 4,51,3
s_addc_u32 s43, s45, 0
RA 220,1,8192
HS 25,19
M 5,62,2
LL 17,90
LL 115,91
M 5,63,3
LA 17
s_mov_b64 exec, s[54:55]
SP
s_mov_b64 exec, -1
M 0,4,4
LC_ 115
ST 108
M 0,5,5
s_mov_b32 m0, %[pm]
s_nop 0
DF
v_readfirstlane_b32 s72, v20
M 1,16,4
WL 3
LO 114,16,114
LU 16,114
M 1,17,5
s_sub_u32 s53, 63, s72
RA 224,1,9216
HS 25,16
M 2,28,4
s_cmp_eq_u32 %[d], 1
LL 18,92
LL 118,93
M 2,29,5
s_cselect_b32 s72, s72, s53
LA 18
M 3,40,4
s_and_b32 s72, s72, 63
LC_ 118
M 3,41,5
s_add_u32 s53, s72, 128
M 4,52,4
WL 3
LO 115,17,115
LU 17,115
M 4,53,5
s_mul_i32 s53, s53, 2359296
RA 228,1,10240
HS 25,17
M 5,64,4
LL 19,94
LL 119,95
M 5,65,5
s_add_u32 s86, %[hlo], s53
LA 19
M 0,6,0
s_addc_u32 s87, %[hhi], 0
LC_ 119
M 0,7,1
s_add_u32 s88, s86, 8192
M 1,18,0
WL 3
LO 118,18,118
LU 18,118
M 1,19,1
s_addc_u32 s89, s87, 0
RA 232,1,11264
HS 25,18
M 2,30,0
s_and_b32 s92, %[hlo], 0x1fffff
LL 16,96
LL 122,97
M 2,31,1
s_add_u32 s92, s92, s53
LA 16
RV 252,22,5392
M 3,42,0
s_mov_b32 s93, 3072
LC_ 122
M 3,43,1
s_add_u32 s42, s44, s62
M 4,54,0
WL 4
LO 119,19,119
LU 19,119
M 4,55,1
s_addc_u32 s43, s45, 0
RA 236,1,12288
HS 25,19
M 5,66,0
LL 17,98
LL 123,99
M 5,67,1
s_mul_i32 s57, s72, 589824
LA 17
s_add_u32 s57, s57, %[xs1]
s_lshl_b32 s72, s72, 1
s_add_u32 s72, s72, s96
M 0,8,2
LC_ 123
ST 112
M 0,9,3
s_lshl_b32 s53, s72, 6
M 1,20,2
WL 3
LO 122,16,122
LU 16,122
s_add_u32 s53, s53, 128
s_cmp_lt_u32 %[ca0], 128
s_cselect_b32 s73, s53, %[ca0]
M 1,21,3
RA 216,1,13312
HS 25,16
M 2,32,2
LL 18,100
LL 126,101
M 2,33,3
LA 18
v_mov_b32 v9, s73
M 3,44,2
LC_ 126
v_mov_b32 v11, s92
M 3,45,3
s_add_u32 s42, s44, s63
M 4,56,2
WL 3
LO 123,17,123
LU 17,123
M 4,57,3
s_addc_u32 s43, s45, 0
RA 220,1,14336
HS 25,17
M 5,68,2
LL 19,102
LL 127,103
M 5,69,3
LA 19
v_mad_u32_u24 v11, v0, s93, v11
M 0,10,4
LC_ 127
ST 116
M 0,11,5
v_lshrrev_b32 v11, 21, v11
M 1,22,4
WL 3
LO 126,18,126
LU 18,126
M 1,23,5
RA 224,1,15360
HS 25,18
M 2,34,4
LL 16,56
LL 104,57
M 2,35,5
LA 16
v_lshl_add_u32 v11, v11, 2, 64
M 3,46,4
LC_ 104
v_writelane_b32 v11, %[tsel], 63
M 3,47,5
s_add_u32 s42, s44, s64
M 4,58,4
WL 3
LO 127,19,127
LU 19,127
M 4,59,5
s_addc_u32 s43, s45, 0
RA 228,1,16384
HS 25,19
M 5,70,4
LL 17,58
LL 105,59
M 5,71,5
s_sub_u32 s53, s86, %[hlo]
LA 17
HX 25
s_lshr_b32 s53, s53, 2
s_and_b32 s92, %[xlo], 0x1fffff
s_add_u32 s53, s53, s92
s_add_u32 s53, s53, 0x20048000
s_lshr_b32 s53, s53, 21
s_lshl_b32 s92, s53, 2
Z 6,0,0
LC_ 105
ST 120
M 6,1,1
W1 25,1024
v_writelane_b32 v11, s92, 48
Z 7,12,0
WL 4
LO 104,16,104
LU 16,104
M 7,13,1
RA 232,1,17408
HZ 24,16
Z 8,24,0
LL 18,60
LL 108,61
M 8,25,1
LA 18
global_load_dword v11, v11, s[68:69] offset:-2048
Z 9,36,0
s_add_u32 s42, s44, s65
LC_ 108
v_add_u32 v4, s39, v4
M 9,37,1
s_addc_u32 s43, s45, 0
ST 124
Z 10,48,0
WL 4
LO 105,17,105
LU 17,105
M 10,49,1
s_mul_i32 s53, s50, 25600
RA 236,1,18432
HS 24,17
Z 11,60,0
LL 19,62
LL 109,63
M 11,61,1
s_add_u32 s40, %[wlo], s53
LA 19
M 6,2,2
s_addc_u32 s41, %[whi], 0
LC_ 109
M 6,3,3
s_mov_b32 m0, s38
s_nop 0
DW 0
M 7,14,2
WL 3
LO 108,18,108
LU 18,108
M 7,15,3
s_add_u32 s37, s37, 32768
RA 216,1,19456
HS 24,18
M 8,26,2
s_cmp_eq_u32 s37, s75
LL 16,64
LL 112,65
M 8,27,3
s_cselect_b32 s37, s74, s37
LA 16
DW 1024
M 9,38,2
LC_ 112
M 9,39,3
M 10,50,2
WL 3
)"
R"(LO 109,19,109
LU 19,109
M 10,51,3
RA 220,1,20480
HS 24,19
M 11,62,2
LL 17,66
LL 113,67
M 11,63,3
LA 17
DW 2048
M 6,4,4
LC_ 113
M 6,5,5
M 7,16,4
WL 3
LO 112,16,112
LU 16,112
M 7,17,5
s_mul_i32 s53, s51, 49152
RA 224,1,21504
HS 24,16
M 8,28,4
s_bitcmp1_b64 s[90:91], s51
LL 18,68
LL 116,69
M 8,29,5
s_cselect_b64 s[44:45], s[84:85], s[82:83]
LA 18
M 9,40,4
s_add_u32 s44, s44, s53
LC_ 116
M 9,41,5
s_addc_u32 s45, s45, 0
M 10,52,4
WL 3
LO 113,17,113
LU 17,113
M 10,53,5
s_add_u32 s51, s51, %[d]
RA 228,1,22528
HS 24,17
M 11,64,4
LL 19,70
LL 117,71
M 11,65,5
s_cmp_eq_u32 s51, %[wrap]
LA 19
M 6,6,0
s_cselect_b32 s51, %[rst], s51
LC_ 117
M 6,7,1
s_add_u32 s40, s40, 3072
M 7,18,0
WL 3
LO 116,18,116
LU 18,116
M 7,19,1
s_addc_u32 s41, s41, 0
RA 232,1,23552
HS 24,18
M 8,30,0
s_add_u32 m0, s38, 3072
LL 16,72
LL 120,73
M 8,31,1
LA 16
DW 0
M 9,42,0
LC_ 120
DW 1024
M 9,43,1
M 10,54,0
WL 3
LO 117,19,117
LU 19,117
M 10,55,1
RA 236,1,24576
HS 24,19
M 11,66,0
LL 17,74
LL 121,75
M 11,67,1
LA 17
DW 2048
M 6,8,2
LC_ 121
M 6,9,3
s_lshl_b32 s53, s52, 8
v_add_u32 v1, s37, v0
M 7,20,2
WL 3
LO 120,16,120
LU 16,120
M 7,21,3
s_add_u32 s48, %[alo], s53
RA 216,1,0
HS 24,16
M 8,32,2
s_addc_u32 s49, %[ahi], 0
LL 18,76
LL 124,77
M 8,33,3
s_add_u32 s52, s52, %[d]
LA 18
v_and_b32 v22, 0xfffffcff, v1
M 9,44,2
s_cmp_eq_u32 s52, %[wrap]
LC_ 124
M 9,45,3
s_cselect_b32 s52, %[rst], s52
DQ 3072
M 10,56,2
WL 3
LO 121,17,121
LU 17,121
M 10,57,3
s_add_u32 s50, s50, %[d]
RA 220,1,1024
HS 24,17
M 11,68,2
LL 19,78
LL 125,79
M 11,69,3
s_cmp_eq_u32 s50, %[wrap]
LA 19
RV 252,22,5120
M 6,10,4
s_cselect_b32 s50, %[rst], s50
LC_ 125
M 6,11,5
WV 31
M 7,22,4
WL 4
LO 124,18,124
LU 18,124
M 7,23,5
s_add_u32 s38, s38, 32768
RA 224,1,2048
HS 24,18
M 8,34,4
s_cmp_eq_u32 s38, s77
LL 16,80
LL 106,81
M 8,35,5
s_cselect_b32 s38, s76, s38
LA 16
M 9,46,4
s_barrier
LC_ 106
M 9,47,5
s_sub_u32 s39, 0, s39
M 10,58,4
WL 3
LO 125,19,125
LU 19,125
M 10,59,5
RA 228,1,3072
HS 24,19
M 11,70,4
LL 17,82
LL 107,83
M 11,71,5
LA 17
HX 24
Z 0,0,0
LC_ 107
ds_read_b64 v[14:15], v5 offset:0
M 0,1,1
W1 24,0
v_subrev_u32 v5, s39, v5
Z 1,12,0
WL 5
LO 106,16,106
LU 16,106
M 1,13,1
RA 232,1,4096
HZ 25,16
Z 2,24,0
LL 18,84
LL 110,85
M 2,25,1
LA 18
Z 3,36,0
LC_ 110
M 3,37,1
s_add_u32 s42, s44, s60
Z 4,48,0
WL 5
LO 107,17,107
LU 17,107
M 4,49,1
s_addc_u32 s43, s45, 0
RA 236,1,6144
HS 25,17
Z 5,60,0
LL 19,86
LL 111,87
M 5,61,1
LA 19
ST 104
M 0,2,2
WL 6
LC_ 111
v_add_f32 v14, v14, v15
M 0,3,3
s_nop 0
v_add_f32_dpp v14, v14, v14 quad_perm:[1,0,3,2] row_mask:0xf bank_mask:0xf
M 1,14,2
WL 3
LO 110,18,110
LU 18,110
M 1,15,3
RA 216,1,7168
HS 25,18
M 2,26,2
LL 16,88
LL 114,89
M 2,27,3
LA 16
v_add_f32_dpp v14, v14, v14 quad_perm:[2,3,0,1] row_mask:0xf bank_mask:0xf
M 3,38,2
LC_ 114
v_add_f32_dpp v14, v14, v14 row_half_mirror row_mask:0xf bank_mask:0xf
M 3,39,3
s_add_u32 s42, s44, s61
v_cvt_f64_f32 v[14:15], v14
M 4,50,2
WL 3
LO 111,19,111
LU 19,111
M 4,51,3
s_addc_u32 s43, s45, 0
RA 220,1,8192
HS 25,19
M 5,62,2
LL 17,90
LL 115,91
M 5,63,3
LA 17
s_mov_b64 exec, s[54:55]
SP
s_mov_b64 exec, -1
M 0,4,4
LC_ 115
ST 108
M 0,5,5
s_mov_b32 m0, %[pm]
s_nop 0
DF
M 1,16,4
WL 3
LO 114,16,114
LU 16,114
M 1,17,5
RA 224,1,9216
HS 25,16
M 2,28,4
LL 18,92
LL 118,93
M 2,29,5
LA 18
M 3,40,4
LC_ 118
M 3,41,5
s_add_u32 s42, s44, s62
M 4,52,4
WL 3
LO 115,17,115
LU 17,115
M 4,53,5
s_addc_u32 s43, s45, 0
RA 228,1,10240
HS 25,17
M 5,64,4
LL 19,94
LL 119,95
M 5,65,5
LA 19
ST 112
M 0,6,0
LC_ 119
M 0,7,1
M 1,18,0
WL 3
LO 118,18,118
LU 18,118
M 1,19,1
RA 232,1,11264
HS 25,18
M 2,30,0
LL 16,96
LL 122,97
M 2,31,1
LA 16
RV 252,22,5392
M 3,42,0
LC_ 122
M 3,43,1
s_add_u32 s42, s44, s63
M 4,54,0
WL 4
LO 119,19,119
LU 19,119
M 4,55,1
s_addc_u32 s43, s45, 0
RA 236,1,12288
HS 25,19
M 5,66,0
LL 17,98
LL 123,99
M 5,67,1
LA 17
ST 116
M 0,8,2
LC_ 123
M 0,9,3
M 1,20,2
WL 3
LO 122,16,122
LU 16,122
M 1,21,3
RA 216,1,13312
HS 25,16
M 2,32,2
LL 18,100
LL 126,101
M 2,33,3
LA 18
M 3,44,2
LC_ 126
M 3,45,3
s_add_u32 s42, s44, s64
M 4,56,2
WL 3
LO 123,17,123
LU 17,123
M 4,57,3
s_addc_u32 s43, s45, 0
RA 220,1,14336
HS 25,17
M 5,68,2
LL 19,102
LL 127,103
M 5,69,3
LA 19
ST 120
M 0,10,4
LC_ 127
M 0,11,5
M 1,22,4
WL 3
LO 126,18,126
LU 18,126
M 1,23,5
RA 224,1,15360
HS 25,18
M 2,34,4
LL 16,56
LL 104,57
M 2,35,5
LA 16
M 3,46,4
LC_ 104
M 3,47,5
s_add_u32 s42, s44, s65
M 4,58,4
WL 3
LO 127,19,127
LU 19,127
M 4,59,5
s_addc_u32 s43, s45, 0
RA 228,1,16384
HS 25,19
M 5,70,4
LL 17,58
LL 105,59
M 5,71,5
LA 17
ST 124
Z 6,0,0
LC_ 105
HX 25
M 6,1,1
W1 25,1024
Z 7,12,0
WL 4
LO 104,16,104
LU 16,104
M 7,13,1
RA 232,1,17408
HZ 24,16
Z 8,24,0
LL 18,60
LL 108,61
M 8,25,1
s_mul_i32 s53, s50, 25600
LA 18
v_add_u32 v4, s39, v4
Z 9,36,0
)"
R"(s_add_u32 s40, %[wlo], s53
LC_ 108
M 9,37,1
s_addc_u32 s41, %[whi], 0
Z 10,48,0
WL 4
LO 105,17,105
LU 17,105
M 10,49,1
s_mov_b32 m0, s38
RA 236,1,18432
HS 24,17
Z 11,60,0
LL 19,62
LL 109,63
M 11,61,1
LA 19
DW 0
M 6,2,2
LC_ 109
M 6,3,3
M 7,14,2
WL 3
LO 108,18,108
LU 18,108
M 7,15,3
s_add_u32 s37, s37, 32768
RA 216,1,19456
HS 24,18
M 8,26,2
s_cmp_eq_u32 s37, s75
LL 16,64
LL 112,65
M 8,27,3
s_cselect_b32 s37, s74, s37
LA 16
DW 1024
M 9,38,2
LC_ 112
M 9,39,3
M 10,50,2
WL 3
LO 109,19,109
LU 19,109
M 10,51,3
RA 220,1,20480
HS 24,19
M 11,62,2
LL 17,66
LL 113,67
M 11,63,3
LA 17
DW 2048
M 6,4,4
LC_ 113
M 6,5,5
M 7,16,4
WL 3
LO 112,16,112
LU 16,112
M 7,17,5
s_mul_i32 s53, s51, 49152
RA 224,1,21504
HS 24,16
M 8,28,4
s_bitcmp1_b64 s[90:91], s51
LL 18,68
LL 116,69
M 8,29,5
s_cselect_b64 s[44:45], s[84:85], s[82:83]
LA 18
M 9,40,4
s_add_u32 s44, s44, s53
LC_ 116
M 9,41,5
s_addc_u32 s45, s45, 0
M 10,52,4
WL 3
LO 113,17,113
LU 17,113
M 10,53,5
s_add_u32 s51, s51, %[d]
RA 228,1,22528
HS 24,17
M 11,64,4
LL 19,70
LL 117,71
M 11,65,5
s_cmp_eq_u32 s51, %[wrap]
LA 19
M 6,6,0
s_cselect_b32 s51, %[rst], s51
LC_ 117
M 6,7,1
s_add_u32 s40, s40, 3072
M 7,18,0
WL 3
LO 116,18,116
LU 18,116
M 7,19,1
s_addc_u32 s41, s41, 0
RA 232,1,23552
HS 24,18
M 8,30,0
s_add_u32 m0, s38, 3072
LL 16,72
LL 120,73
M 8,31,1
LA 16
DW 0
M 9,42,0
LC_ 120
DW 1024
M 9,43,1
M 10,54,0
WL 3
LO 117,19,117
LU 19,117
M 10,55,1
RA 236,1,24576
HS 24,19
M 11,66,0
LL 17,74
LL 121,75
M 11,67,1
LA 17
DW 2048
M 6,8,2
LC_ 121
M 6,9,3
WV 22
v_add_u32 v1, s37, v0
v_bcnt_u32_b32 v11, v11, 0
M 7,20,2
WL 3
LO 120,16,120
LU 16,120
M 7,21,3
s_lshl_b32 s53, s52, 8
RA 216,1,0
HS 24,16
M 8,32,2
s_add_u32 s48, %[alo], s53
LL 18,76
LL 124,77
M 8,33,3
s_addc_u32 s49, %[ahi], 0
LA 18
v_and_b32 v22, 0xfffffcff, v1
M 9,44,2
s_add_u32 s52, s52, %[d]
LC_ 124
M 9,45,3
s_cmp_eq_u32 s52, %[wrap]
M 10,56,2
WL 3
LO 121,17,121
LU 17,121
M 10,57,3
s_cselect_b32 s52, %[rst], s52
RA 220,1,1024
HS 24,17
M 11,68,2
LL 19,78
LL 125,79
M 11,69,3
LA 19
RV 252,22,5120
M 6,10,4
LC_ 125
DQ 3072
M 6,11,5
v_cmp_lt_u32 s[92:93], 2, v11
M 7,22,4
WL 4
LO 124,18,124
LU 18,124
M 7,23,5
s_nop 0
RA 224,1,2048
HS 24,18
M 8,34,4
s_sub_u32 s53, 0, s96
LL 16,80
LL 106,81
M 8,35,5
s_xor_b32 s92, s92, s53
LA 16
M 9,46,4
s_xor_b32 s93, s93, s53
LC_ 106
M 9,47,5
s_add_u32 s50, s50, %[d]
M 10,58,4
WL 3
LO 125,19,125
LU 19,125
M 10,59,5
RA 228,1,3072
HS 24,19
M 11,70,4
LL 17,82
LL 107,83
M 11,71,5
LA 17
HX 24
s_cmp_eq_u32 s50, %[wrap]
s_cselect_b32 s50, %[rst], s50
Z 0,0,0
s_add_u32 s38, s38, 32768
LC_ 107
M 0,1,1
s_cmp_eq_u32 s38, s77
Z 1,12,0
WL 3
LO 106,16,106
LU 16,106
M 1,13,1
s_cselect_b32 s38, s76, s38
RA 232,1,4096
HZ 25,16
Z 2,24,0
s_barrier
LL 18,84
LL 110,85
M 2,25,1
s_sub_u32 s39, 0, s39
LA 18
ds_read_b64 v[14:15], v5 offset:0
Z 3,36,0
LC_ 110
W1 24,0
M 3,37,1
s_add_u32 s42, s44, s60
v_subrev_u32 v5, s39, v5
Z 4,48,0
WL 5
LO 107,17,107
LU 17,107
M 4,49,1
s_addc_u32 s43, s45, 0
RA 236,1,6144
HS 25,17
Z 5,60,0
LL 19,86
LL 111,87
M 5,61,1
LA 19
ST 104
M 0,2,2
LC_ 111
M 0,3,3
M 1,14,2
WL 4
LO 110,18,110
LU 18,110
M 1,15,3
RA 216,1,7168
HS 25,18
M 2,26,2
LL 16,88
LL 114,89
M 2,27,3
LA 16
v_add_f32 v14, v14, v15
M 3,38,2
LC_ 114
v_add_f32_dpp v14, v14, v14 quad_perm:[1,0,3,2] row_mask:0xf bank_mask:0xf
M 3,39,3
s_add_u32 s42, s44, s61
v_add_f32_dpp v14, v14, v14 quad_perm:[2,3,0,1] row_mask:0xf bank_mask:0xf
M 4,50,2
WL 3
LO 111,19,111
LU 19,111
M 4,51,3
s_addc_u32 s43, s45, 0
RA 220,1,8192
HS 25,19
M 5,62,2
LL 17,90
LL 115,91
M 5,63,3
LA 17
ST 108
M 0,4,4
s_mov_b32 m0, %[pm]
LC_ 115
DF
v_add_f32_dpp v14, v14, v14 row_half_mirror row_mask:0xf bank_mask:0xf
M 0,5,5
v_cvt_f64_f32 v[14:15], v14
M 1,16,4
WL 3
LO 114,16,114
LU 16,114
M 1,17,5
RA 224,1,9216
HS 25,16
M 2,28,4
LL 18,92
LL 118,93
s_mov_b64 exec, s[54:55]
SP
s_mov_b64 exec, -1
M 2,29,5
LA 18
M 3,40,4
LC_ 118
M 3,41,5
s_add_u32 s42, s44, s62
M 4,52,4
WL 3
LO 115,17,115
LU 17,115
M 4,53,5
s_addc_u32 s43, s45, 0
RA 228,1,10240
HS 25,17
M 5,64,4
LL 19,94
LL 119,95
M 5,65,5
LA 19
ST 112
M 0,6,0
LC_ 119
M 0,7,1
M 1,18,0
WL 3
LO 118,18,118
LU 18,118
M 1,19,1
RA 232,1,11264
HS 25,18
M 2,30,0
LL 16,96
LL 122,97
M 2,31,1
LA 16
RV 252,22,5392
M 3,42,0
LC_ 122
M 3,43,1
s_add_u32 s42, s44, s63
M 4,54,0
WL 4
LO 119,19,119
LU 19,119
M 4,55,1
s_addc_u32 s43, s45, 0
RA 236,1,12288
HS 25,19
M 5,66,0
LL 17,98
LL 123,99
M 5,67,1
LA 17
ST 116
M 0,8,2
LC_ 123
M 0,9,3
M 1,20,2
WL 3
LO 122,16,122
LU 16,122
M 1,21,3
RA 216,1,13312
HS 25,16
M 2,32,2
LL 18,100
LL 126,101
M 2,33,3
LA 18
M 3,44,2
LC_ 126
M 3,45,3
s_add_u32 s42, s44, s64
M 4,56,2
WL 3
LO 123,17,123
LU 17,123
M 4,57,3
s_addc_u32 s43, s45, 0
RA 220,1,14336
HS 25,17
M 5,68,2
LL 19,102
LL 127,103
M 5,69,3
LA 19
ST 120
M 0,10,4
LC_ 127
M 0,11,5
M 1,22,4
WL 3
LO 126,18,126
LU 18,126
M 1,23,5
)"
R"(RA 224,1,15360
HS 25,18
M 2,34,4
LL 16,56
LL 104,57
M 2,35,5
LA 16
M 3,46,4
LC_ 104
M 3,47,5
s_add_u32 s42, s44, s65
M 4,58,4
WL 3
LO 127,19,127
LU 19,127
M 4,59,5
s_addc_u32 s43, s45, 0
RA 228,1,16384
HS 25,19
M 5,70,4
LL 17,58
LL 105,59
M 5,71,5
LA 17
ST 124
Z 6,0,0
LC_ 105
HX 25
M 6,1,1
W1 25,1024
Z 7,12,0
WL 4
LO 104,16,104
LU 16,104
M 7,13,1
RA 232,1,17408
HZ 24,16
Z 8,24,0
LL 18,60
LL 108,61
M 8,25,1
s_mul_i32 s53, s50, 25600
LA 18
v_add_u32 v4, s39, v4
Z 9,36,0
s_add_u32 s40, %[wlo], s53
LC_ 108
M 9,37,1
s_addc_u32 s41, %[whi], 0
Z 10,48,0
WL 4
LO 105,17,105
LU 17,105
M 10,49,1
s_mov_b32 m0, s38
RA 236,1,18432
HS 24,17
Z 11,60,0
LL 19,62
LL 109,63
M 11,61,1
s_mul_i32 s53, %[d], 0
LA 19
M 6,2,2
s_add_u32 s53, s53, s57
LC_ 109
M 6,3,3
s_add_u32 s46, %[xlo], s53
M 7,14,2
WL 3
LO 108,18,108
LU 18,108
M 7,15,3
s_addc_u32 s47, %[xhi], 0
RA 216,1,19456
HS 24,18
M 8,26,2
s_mov_b32 s53, 0xfe987610
LL 16,64
LL 112,65
M 8,27,3
s_cmp_eq_u32 %[d], 1
LA 16
M 9,38,2
s_cselect_b32 s53, s53, 0xda43cb52
LC_ 112
M 9,39,3
s_bitcmp1_b64 s[92:93], 48
M 10,50,2
WL 3
LO 109,19,109
LU 19,109
M 10,51,3
s_cselect_b32 s58, 16, 0
RA 220,1,20480
HS 24,19
M 11,62,2
LL 17,66
LL 113,67
M 11,63,3
s_lshr_b32 s58, s53, s58
LA 17
v_lshrrev_b32 v3, 4, v0
M 6,4,4
LC_ 113
v_and_b32 v3, 12, v3
M 6,5,5
v_bfe_u32 v3, s58, v3, 4
v_mad_u32_u24 v3, v3, s97, v21
DW 0
M 7,16,4
WL 3
LO 112,16,112
LU 16,112
M 7,17,5
s_add_u32 s37, s37, 32768
RA 224,1,21504
HS 24,16
M 8,28,4
s_cmp_eq_u32 s37, s75
LL 18,68
LL 116,69
M 8,29,5
s_cselect_b32 s37, s74, s37
LA 18
DW 1024
M 9,40,4
LC_ 116
XL 72,0
M 9,41,5
DW 2048
M 10,52,4
WL 3
LO 113,17,113
LU 17,113
M 10,53,5
s_mul_i32 s53, s51, 49152
RA 228,1,22528
HS 24,17
M 11,64,4
LL 19,70
LL 117,71
M 11,65,5
s_bitcmp1_b64 s[90:91], s51
LA 19
M 6,6,0
s_cselect_b64 s[44:45], s[84:85], s[82:83]
LC_ 117
M 6,7,1
s_add_u32 s44, s44, s53
M 7,18,0
WL 3
LO 116,18,116
LU 18,116
M 7,19,1
s_addc_u32 s45, s45, 0
RA 232,1,23552
HS 24,18
M 8,30,0
s_add_u32 s51, s51, %[d]
LL 16,72
LL 120,73
M 8,31,1
s_cmp_eq_u32 s51, %[wrap]
LA 16
s_cselect_b32 s51, %[rst], s51
M 9,42,0
s_add_u32 s40, s40, 3072
LC_ 120
s_addc_u32 s41, s41, 0
s_add_u32 m0, s38, 3072
M 9,43,1
XL 73,64
M 10,54,0
WL 3
LO 117,19,117
LU 19,117
M 10,55,1
RA 236,1,24576
HS 24,19
M 11,66,0
LL 17,74
LL 121,75
M 11,67,1
LA 17
DW 0
M 6,8,2
LC_ 121
DW 1024
M 6,9,3
XL 74,128
v_add_u32 v1, s37, v0
M 7,20,2
WL 3
LO 120,16,120
LU 16,120
M 7,21,3
RA 216,1,0
HS 24,16
M 8,32,2
LL 18,76
LL 124,77
M 8,33,3
LA 18
DW 2048
M 9,44,2
s_lshl_b32 s53, s52, 8
LC_ 124
v_and_b32 v22, 0xfffffcff, v1
M 9,45,3
s_add_u32 s48, %[alo], s53
M 10,56,2
WL 3
LO 121,17,121
LU 17,121
M 10,57,3
s_addc_u32 s49, %[ahi], 0
RA 220,1,1024
HS 24,17
M 11,68,2
LL 19,78
LL 125,79
M 11,69,3
s_add_u32 s52, s52, %[d]
LA 19
RV 252,22,5120
M 6,10,4
s_cmp_eq_u32 s52, %[wrap]
LC_ 125
M 6,11,5
s_cselect_b32 s52, %[rst], s52
DQ 3072
M 7,22,4
WL 4
LO 124,18,124
LU 18,124
M 7,23,5
RA 224,1,2048
HS 24,18
M 8,34,4
LL 16,80
LL 106,81
M 8,35,5
LA 16
XL 75,192
M 9,46,4
s_add_u32 s50, s50, %[d]
LC_ 106
M 9,47,5
M 10,58,4
WL 3
LO 125,19,125
LU 19,125
M 10,59,5
RA 228,1,3072
HS 24,19
M 11,70,4
LL 17,82
LL 107,83
M 11,71,5
LA 17
HX 24
s_cmp_eq_u32 s50, %[wrap]
s_cselect_b32 s50, %[rst], s50
Z 0,0,0
s_add_u32 s38, s38, 32768
LC_ 107
M 0,1,1
s_cmp_eq_u32 s38, s77
Z 1,12,0
WL 3
LO 106,16,106
LU 16,106
M 1,13,1
s_cselect_b32 s38, s76, s38
RA 232,1,4096
HZ 25,16
Z 2,24,0
LL 18,84
LL 110,85
M 2,25,1
WV 34
LA 18
XL 76,256
Z 3,36,0
s_barrier
LC_ 110
M 3,37,1
s_sub_u32 s39, 0, s39
ds_read_b64 v[14:15], v5 offset:0
Z 4,48,0
WL 4
LO 107,17,107
LU 17,107
M 4,49,1
RA 236,1,6144
HS 25,17
Z 5,60,0
LL 19,86
LL 111,87
M 5,61,1
LA 19
W1 24,0
M 0,2,2
LC_ 111
XL 77,320
M 0,3,3
s_add_u32 s42, s44, s60
v_subrev_u32 v5, s39, v5
M 1,14,2
WL 5
LO 110,18,110
LU 18,110
M 1,15,3
s_addc_u32 s43, s45, 0
RA 216,1,7168
HS 25,18
M 2,26,2
LL 16,88
LL 114,89
M 2,27,3
LA 16
ST 104
M 3,38,2
WL 6
LC_ 114
XL 78,384
M 3,39,3
s_add_u32 s42, s44, s61
v_add_f32 v14, v14, v15
M 4,50,2
WL 3
LO 111,19,111
LU 19,111
v_add_f32_dpp v14, v14, v14 quad_perm:[1,0,3,2] row_mask:0xf bank_mask:0xf
M 4,51,3
s_addc_u32 s43, s45, 0
RA 220,1,8192
HS 25,19
M 5,62,2
LL 17,90
LL 115,91
v_add_f32_dpp v14, v14, v14 quad_perm:[2,3,0,1] row_mask:0xf bank_mask:0xf
M 5,63,3
LA 17
ST 108
M 0,4,4
s_mov_b32 m0, %[pm]
LC_ 115
DF
v_add_f32_dpp v14, v14, v14 row_half_mirror row_mask:0xf bank_mask:0xf
M 0,5,5
XL 79,448
v_cvt_f64_f32 v[14:15], v14
M 1,16,4
WL 3
LO 114,16,114
LU 16,114
M 1,17,5
RA 224,1,9216
HS 25,16
M 2,28,4
LL 18,92
LL 118,93
s_mov_b64 exec, s[54:55]
SP
s_mov_b64 exec, -1
M 2,29,5
LA 18
RV 40,8,6144
RV 44,8,6208
RV 48,8,6272
RA 242,7,0
RA 250,7,256
M 3,40,4
LC_ 118
M 3,41,5
s_add_u32 s42, s44, s62
M 4,52,4
WL 8
LO 115,17,115
LU 17,115
M 4,53,5
s_addc_u32 s43, s45, 0
RA 228,1,10240
HS 25,17
M 5,64,4
WV 16
LL 19,94
LL 119,95
M 5,65,5
WL 6
LA 19
ST 112
LZ 40,72
XL 80,512
M 0,6,0
LC_ 119
M 0,7,1
M 1,18,0
)"
R"(WL 3
LO 118,18,118
LU 18,118
M 1,19,1
RA 232,1,11264
HS 25,18
M 2,30,0
LL 16,96
LL 122,97
M 2,31,1
WV 16
LA 16
RV 252,22,5392
LS 44,73
RV 40,8,6336
M 3,42,0
LC_ 122
M 3,43,1
s_add_u32 s42, s44, s63
M 4,54,0
WL 5
LO 119,19,119
LU 19,119
M 4,55,1
s_addc_u32 s43, s45, 0
RA 236,1,12288
HS 25,19
M 5,66,0
LL 17,98
LL 123,99
M 5,67,1
LA 17
ST 116
M 0,8,2
LC_ 123
XL 81,576
M 0,9,3
WV 15
LS 48,74
RV 44,8,6400
M 1,20,2
WL 4
LO 122,16,122
LU 16,122
M 1,21,3
RA 216,1,13312
HS 25,16
M 2,32,2
LL 18,100
LL 126,101
M 2,33,3
LA 18
M 3,44,2
LC_ 126
M 3,45,3
s_add_u32 s42, s44, s64
WV 12
LS 40,75
RV 48,8,6464
M 4,56,2
WL 5
LO 123,17,123
LU 17,123
M 4,57,3
s_addc_u32 s43, s45, 0
RA 220,1,14336
HS 25,17
M 5,68,2
LL 19,102
LL 127,103
M 5,69,3
LA 19
ST 120
M 0,10,4
LC_ 127
XL 82,640
M 0,11,5
M 1,22,4
WL 4
LO 126,18,126
LU 18,126
M 1,23,5
RA 224,1,15360
HS 25,18
WV 13
LS 44,76
RV 40,8,6528
M 2,34,4
LL 16,56
LL 104,57
M 2,35,5
LA 16
M 3,46,4
LC_ 104
M 3,47,5
s_add_u32 s42, s44, s65
M 4,58,4
WL 4
LO 127,19,127
LU 19,127
M 4,59,5
s_addc_u32 s43, s45, 0
RA 228,1,16384
HS 25,19
WV 12
LS 48,77
RV 44,8,6592
M 5,70,4
LL 17,58
LL 105,59
M 5,71,5
LA 17
XL 83,704
Z 6,0,0
LC_ 105
ST 124
M 6,1,1
HX 25
Z 7,12,0
WL 4
LO 104,16,104
LU 16,104
M 7,13,1
RA 232,1,17408
HZ 24,16
Z 8,24,0
LL 18,60
LL 108,61
M 8,25,1
s_mul_i32 s53, s50, 25600
LA 18
W1 25,1024
WV 12
LS 40,78
RV 48,8,6656
Z 9,36,0
s_add_u32 s40, %[wlo], s53
LC_ 108
v_add_u32 v4, s39, v4
M 9,37,1
s_addc_u32 s41, %[whi], 0
Z 10,48,0
WL 5
LO 105,17,105
LU 17,105
M 10,49,1
s_mov_b32 m0, s38
RA 236,1,18432
HS 24,17
Z 11,60,0
LL 19,62
LL 109,63
M 11,61,1
LA 19
DW 0
WV 10
LS 44,79
RV 40,8,6720
M 6,2,2
LC_ 109
M 6,3,3
M 7,14,2
WL 4
LO 108,18,108
LU 18,108
M 7,15,3
s_add_u32 s37, s37, 32768
RA 216,1,19456
HS 24,18
M 8,26,2
s_cmp_eq_u32 s37, s75
LL 16,64
LL 112,65
M 8,27,3
s_cselect_b32 s37, s74, s37
LA 16
DW 1024
M 9,38,2
LC_ 112
M 9,39,3
WV 8
LS 48,80
RV 44,8,6784
M 10,50,2
WL 4
LO 109,19,109
LU 19,109
M 10,51,3
RA 220,1,20480
HS 24,19
M 11,62,2
LL 17,66
LL 113,67
M 11,63,3
LA 17
DW 2048
M 6,4,4
LC_ 113
M 6,5,5
WV 7
LS 40,81
RV 48,8,6848
M 7,16,4
WL 5
LO 112,16,112
LU 16,112
M 7,17,5
s_mul_i32 s53, s51, 49152
RA 224,1,21504
HS 24,16
M 8,28,4
s_bitcmp1_b64 s[90:91], s51
LL 18,68
LL 116,69
M 8,29,5
s_cselect_b64 s[44:45], s[84:85], s[82:83]
LA 18
M 9,40,4
s_add_u32 s44, s44, s53
LC_ 116
M 9,41,5
s_addc_u32 s45, s45, 0
M 10,52,4
WL 4
LO 113,17,113
LU 17,113
M 10,53,5
s_add_u32 s51, s51, %[d]
RA 228,1,22528
HS 24,17
WV 5
LS 44,82
s_cmp_eq_u32 s51, %[wrap]
M 11,64,4
LL 19,70
LL 117,71
M 11,65,5
s_cselect_b32 s51, %[rst], s51
LA 19
M 6,6,0
s_add_u32 s40, s40, 3072
LC_ 117
M 6,7,1
s_addc_u32 s41, s41, 0
M 7,18,0
WL 3
LO 116,18,116
LU 18,116
M 7,19,1
s_add_u32 m0, s38, 3072
RA 232,1,23552
HS 24,18
WV 4
LS 48,83
DW 0
M 8,30,0
LL 16,72
LL 120,73
M 8,31,1
LA 16
DW 1024
M 9,42,0
LC_ 120
M 9,43,1
M 10,54,0
WL 3
LO 117,19,117
LU 19,117
M 10,55,1
RA 236,1,24576
HS 24,19
M 11,66,0
LL 17,74
LL 121,75
M 11,67,1
LA 17
DW 2048
v_mul_f32 v36, 0xbb2aaaab, v36
v_mul_f32 v37, 0xbb2aaaab, v37
v_mul_f32 v38, 0xbb2aaaab, v38
v_mul_f32 v39, 0xbb2aaaab, v39
M 6,8,2
LC_ 121
M 6,9,3
s_lshl_b32 s53, s52, 8
v_add_u32 v1, s37, v0
LD 40,244,72
s_add_u32 s48, %[alo], s53
M 7,20,2
WL 3
LO 120,16,120
LU 16,120
M 7,21,3
s_addc_u32 s49, %[ahi], 0
RA 216,1,0
HS 24,16
LD 44,240,72
RA 242,7,512
v_and_b32 v22, 0xfffffcff, v1
M 8,32,2
s_add_u32 s52, s52, %[d]
LL 18,76
LL 124,77
M 8,33,3
s_cmp_eq_u32 s52, %[wrap]
LA 18
M 9,44,2
s_cselect_b32 s52, %[rst], s52
LC_ 124
DQ 3072
M 9,45,3
LD 48,252,73
CF 40,40,41
CF 42,42,43
AW 164,40
AW 165,42
M 10,56,2
WL 4
LO 121,17,121
LU 17,121
M 10,57,3
s_add_u32 s50, s50, %[d]
RA 220,1,1024
HS 24,17
LD 40,248,73
RA 250,7,768
CF 44,44,45
CF 46,46,47
AW 166,44
AW 167,46
M 11,68,2
LL 19,78
LL 125,79
M 11,69,3
s_cmp_eq_u32 s50, %[wrap]
LA 19
RV 252,22,5120
M 6,10,4
s_cselect_b32 s50, %[rst], s50
LC_ 125
M 6,11,5
WL 7
LD 44,244,74
CF 48,48,49
CF 50,50,51
M 7,22,4
WL 5
LO 124,18,124
LU 18,124
M 7,23,5
s_add_u32 s38, s38, 32768
RA 224,1,2048
HS 24,18
GZ 72
s_cmp_eq_u32 s38, s77
AW 168,48
AW 169,50
M 8,34,4
s_cselect_b32 s38, s76, s38
LL 16,80
LL 106,81
M 8,35,5
s_barrier
LA 16
M 9,46,4
s_sub_u32 s39, 0, s39
LC_ 106
ds_read_b64 v[14:15], v5 offset:0
M 9,47,5
s_nop 0
v_subrev_u32 v5, s39, v5
LD 48,240,74
RA 242,7,1024
CF 40,40,41
CF 42,42,43
AW 170,40
AW 171,42
M 10,58,4
WL 5
LO 125,19,125
LU 19,125
M 10,59,5
RA 228,1,3072
HS 24,19
LD 40,252,75
CF 44,44,45
CF 46,46,47
M 11,70,4
LL 17,82
LL 107,83
M 11,71,5
LA 17
HX 24
Z 0,0,0
LC_ 107
W1 24,0
M 0,1,1
GS 73
AW 172,44
AW 173,46
)"
R"(Z 1,12,0
WL 6
LO 106,16,106
LU 16,106
M 1,13,1
RA 232,1,4096
HZ 25,16
LD 44,248,75
RA 250,7,1280
CF 48,48,49
CF 50,50,51
AW 174,48
AW 175,50
Z 2,24,0
LL 18,84
LL 110,85
M 2,25,1
WL 6
LA 18
v_add_f32 v14, v14, v15
LD 48,244,76
CF 40,40,41
CF 42,42,43
v_add_f32_dpp v14, v14, v14 quad_perm:[1,0,3,2] row_mask:0xf bank_mask:0xf
Z 3,36,0
LC_ 110
M 3,37,1
s_add_u32 s42, s44, s60
v_add_f32_dpp v14, v14, v14 quad_perm:[2,3,0,1] row_mask:0xf bank_mask:0xf
Z 4,48,0
WL 5
LO 107,17,107
LU 17,107
M 4,49,1
s_addc_u32 s43, s45, 0
RA 236,1,6144
HS 25,17
GS 74
ST 104
AW 176,40
AW 177,42
v_add_f32_dpp v14, v14, v14 row_half_mirror row_mask:0xf bank_mask:0xf
Z 5,60,0
LL 19,86
LL 111,87
M 5,61,1
LA 19
v_cvt_f64_f32 v[14:15], v14
LD 40,240,76
RA 242,7,1536
CF 44,44,45
CF 46,46,47
AW 178,44
AW 179,46
M 0,2,2
LC_ 111
M 0,3,3
s_mov_b64 exec, s[54:55]
SP
s_mov_b64 exec, -1
M 1,14,2
WL 4
LO 110,18,110
LU 18,110
M 1,15,3
RA 216,1,7168
HS 25,18
LD 44,252,77
CF 48,48,49
CF 50,50,51
M 2,26,2
LL 16,88
LL 114,89
M 2,27,3
LA 16
GS 75
AW 180,48
AW 181,50
M 3,38,2
LC_ 114
M 3,39,3
s_add_u32 s42, s44, s61
M 4,50,2
WL 3
LO 111,19,111
LU 19,111
M 4,51,3
s_addc_u32 s43, s45, 0
RA 220,1,8192
HS 25,19
LD 48,248,77
RA 250,7,1792
CF 40,40,41
CF 42,42,43
AW 182,40
AW 183,42
M 5,62,2
LL 17,90
LL 115,91
M 5,63,3
LA 17
ST 108
LD 40,244,78
s_mov_b32 m0, %[pm]
CF 44,44,45
CF 46,46,47
M 0,4,4
LC_ 115
DF
M 0,5,5
M 1,16,4
WL 4
LO 114,16,114
LU 16,114
M 1,17,5
RA 224,1,9216
HS 25,16
GS 76
AW 184,44
AW 185,46
M 2,28,4
LL 18,92
LL 118,93
M 2,29,5
LA 18
LD 44,240,78
RA 242,7,2048
CF 48,48,49
CF 50,50,51
AW 186,48
AW 187,50
M 3,40,4
WL 5
LC_ 118
M 3,41,5
s_add_u32 s42, s44, s62
LD 48,252,79
s_addc_u32 s43, s45, 0
CF 40,40,41
CF 42,42,43
M 4,52,4
WL 4
LO 115,17,115
LU 17,115
M 4,53,5
RA 228,1,10240
HS 25,17
M 5,64,4
LL 19,94
LL 119,95
M 5,65,5
LA 19
ST 112
GS 77
AW 188,40
AW 189,42
M 0,6,0
LC_ 119
M 0,7,1
LD 40,248,79
RA 250,7,2304
CF 44,44,45
CF 46,46,47
AW 190,44
AW 191,46
M 1,18,0
WL 4
LO 118,18,118
LU 18,118
M 1,19,1
RA 232,1,11264
HS 25,18
M 2,30,0
LL 16,96
LL 122,97
M 2,31,1
LA 16
RV 252,22,5392
LD 44,244,80
CF 48,48,49
CF 50,50,51
M 3,42,0
LC_ 122
M 3,43,1
s_add_u32 s42, s44, s63
GS 78
s_addc_u32 s43, s45, 0
AW 192,48
AW 193,50
M 4,54,0
WL 5
LO 119,19,119
LU 19,119
M 4,55,1
RA 236,1,12288
HS 25,19
M 5,66,0
LL 17,98
LL 123,99
M 5,67,1
LA 17
ST 116
LD 48,240,80
RA 242,7,2560
CF 40,40,41
CF 42,42,43
AW 194,40
AW 195,42
M 0,8,2
LC_ 123
M 0,9,3
WL 8
LD 40,252,81
CF 44,44,45
CF 46,46,47
M 1,20,2
WL 4
LO 122,16,122
LU 16,122
M 1,21,3
RA 216,1,13312
HS 25,16
M 2,32,2
LL 18,100
LL 126,101
M 2,33,3
LA 18
GS 79
AW 196,44
AW 197,46
M 3,44,2
LC_ 126
M 3,45,3
s_add_u32 s42, s44, s64
LD 44,248,81
RA 250,7,2816
CF 48,48,49
CF 50,50,51
AW 198,48
AW 199,50
M 4,56,2
WL 4
LO 123,17,123
LU 17,123
M 4,57,3
s_addc_u32 s43, s45, 0
RA 220,1,14336
HS 25,17
LD 48,244,82
ST 120
CF 40,40,41
CF 42,42,43
M 5,68,2
LL 19,102
LL 127,103
M 5,69,3
LA 19
M 0,10,4
LC_ 127
M 0,11,5
GS 80
AW 200,40
AW 201,42
M 1,22,4
WL 4
LO 126,18,126
LU 18,126
M 1,23,5
RA 224,1,15360
HS 25,18
LD 40,240,82
CF 44,44,45
CF 46,46,47
AW 202,44
AW 203,46
M 2,34,4
LL 16,56
LL 104,57
M 2,35,5
LA 16
M 3,46,4
WL 5
LC_ 104
M 3,47,5
s_add_u32 s42, s44, s65
LD 44,252,83
s_addc_u32 s43, s45, 0
CF 48,48,49
CF 50,50,51
M 4,58,4
WL 3
LO 127,19,127
LU 19,127
M 4,59,5
RA 228,1,16384
HS 25,19
GS 81
ST 124
AW 204,48
AW 205,50
M 5,70,4
LL 17,58
LL 105,59
M 5,71,5
LA 17
HX 25
Z 6,0,0
LC_ 105
W1 25,1024
M 6,1,1
s_nop 0
v_add_u32 v4, s39, v4
LD 48,248,83
CF 40,40,41
CF 42,42,43
AW 206,40
AW 207,42
Z 7,12,0
WL 4
LO 104,16,104
LU 16,104
M 7,13,1
RA 232,1,17408
HZ 24,16
GS 82
CF 44,44,45
CF 46,46,47
AW 208,44
AW 209,46
Z 8,24,0
LL 18,60
LL 108,61
M 8,25,1
s_mul_i32 s53, s50, 25600
LA 18
Z 9,36,0
s_add_u32 s40, %[wlo], s53
LC_ 108
M 9,37,1
s_addc_u32 s41, %[whi], 0
CF 48,48,49
CF 50,50,51
AW 210,48
AW 211,50
Z 10,48,0
WL 4
LO 105,17,105
LU 17,105
M 10,49,1
s_mov_b32 m0, s38
RA 236,1,18432
HS 24,17
GS 83
Z 11,60,0
LL 19,62
LL 109,63
M 11,61,1
s_mul_i32 s53, %[d], 0
LA 19
RV 36,8,6912
M 6,2,2
s_add_u32 s53, s53, s57
LC_ 109
M 6,3,3
s_add_u32 s46, %[xlo], s53
M 7,14,2
WL 4
LO 108,18,108
LU 18,108
M 7,15,3
s_addc_u32 s47, %[xhi], 0
RA 216,1,19456
HS 24,18
v_cndmask_b32 v52, v52, v53, s[78:79]
v_cndmask_b32 v54, v54, v55, s[78:79]
v_cndmask_b32 v52, v52, v54, s[80:81]
v_mul_f32 v52, 0x3b2aaaab, v52
M 8,26,2
s_mov_b32 s53, 0xda43cb52
LL 16,64
LL 112,65
M 8,27,3
s_cmp_eq_u32 %[d], 1
LA 16
C 52,52,52
v_lshlrev_b32 v52, 16, v52
WL 3
v_add_f32 v52, v36, v52
C 52,52,52
M 9,38,2
s_cselect_b32 s53, s53, 0xfe987610
LC_ 112
M 9,39,3
s_bitcmp1_b64 s[92:93], 48
M 10,50,2
WL 3
LO 109,19,109
LU 19,109
M 10,51,3
s_cselect_b32 s58, 16, 0
)"
R"(RA 220,1,20480
HS 24,19
v_lshlrev_b32 v52, 16, v52
v_sqrt_f32 v52, v52
M 11,62,2
LL 17,66
LL 113,67
M 11,63,3
s_lshr_b32 s58, s53, s58
LA 17
v_lshrrev_b32 v3, 4, v0
C 52,52,52
v_lshlrev_b32 v52, 16, v52
v_cmp_ge_f32 vcc, v37, v52
v_rcp_f32 v52, v52
v_and_b32 v3, 12, v3
M 6,4,4
LC_ 113
v_bfe_u32 v3, s58, v3, 4
M 6,5,5
v_mad_u32_u24 v3, v3, s97, v21
DW 0
M 7,16,4
WL 3
LO 112,16,112
LU 16,112
M 7,17,5
s_add_u32 s37, s37, 32768
RA 224,1,21504
HS 24,16
v_mul_f32 v52, v38, v52
v_cndmask_b32 v52, 0, v52, vcc
CF 52,52,52
M 8,28,4
s_cmp_eq_u32 s37, s75
LL 18,68
LL 116,69
M 8,29,5
s_cselect_b32 s37, s74, s37
LA 18
DW 1024
ds_bpermute_b32 v13, v28, v52
M 9,40,4
LC_ 116
XL 0,0
M 9,41,5
DW 2048
M 10,52,4
WL 4
LO 113,17,113
LU 17,113
M 10,53,5
s_mul_i32 s53, s51, 49152
RA 228,1,22528
HS 24,17
M 11,64,4
LL 19,70
LL 117,71
M 11,65,5
s_bitcmp1_b64 s[90:91], s51
LA 19
M 6,6,0
s_cselect_b64 s[44:45], s[84:85], s[82:83]
LC_ 117
M 6,7,1
s_add_u32 s44, s44, s53
M 7,18,0
WL 3
LO 116,18,116
LU 18,116
M 7,19,1
s_addc_u32 s45, s45, 0
RA 232,1,23552
HS 24,18
M 8,30,0
s_add_u32 s51, s51, %[d]
LL 16,72
LL 120,73
M 8,31,1
s_cmp_eq_u32 s51, %[wrap]
LA 16
s_cselect_b32 s51, %[rst], s51
AR 52,164
M 9,42,0
s_add_u32 s40, s40, 3072
LC_ 120
s_addc_u32 s41, s41, 0
s_add_u32 m0, s38, 3072
AR 53,165
M 9,43,1
XL 1,64
AR 54,166
M 10,54,0
WL 3
LO 117,19,117
LU 19,117
M 10,55,1
RA 236,1,24576
HS 24,19
M 11,66,0
LL 17,74
LL 121,75
M 11,67,1
LA 17
DW 0
M 6,8,2
LC_ 121
DW 1024
AR 55,167
M 6,9,3
XL 2,128
v_add_u32 v1, s37, v0
XS 52
XS 53
M 7,20,2
WL 3
LO 120,16,120
LU 16,120
XS 54
M 7,21,3
RA 216,1,0
HS 24,16
XS 55
AW 164,52
M 8,32,2
LL 18,76
LL 124,77
AW 165,53
AW 166,54
M 8,33,3
LA 18
DW 2048
AW 167,55
AR 52,168
M 9,44,2
s_lshl_b32 s53, s52, 8
LC_ 124
v_and_b32 v22, 0xfffffcff, v1
AR 53,169
M 9,45,3
s_add_u32 s48, %[alo], s53
AR 54,170
AR 55,171
M 10,56,2
WL 3
LO 121,17,121
LU 17,121
XS 52
XS 53
M 10,57,3
s_addc_u32 s49, %[ahi], 0
RA 220,1,1024
HS 24,17
XS 54
XS 55
M 11,68,2
LL 19,78
LL 125,79
AW 168,52
M 11,69,3
s_add_u32 s52, s52, %[d]
LA 19
RV 252,22,5120
AW 169,53
AW 170,54
M 6,10,4
s_cmp_eq_u32 s52, %[wrap]
LC_ 125
AW 171,55
AR 52,172
M 6,11,5
s_cselect_b32 s52, %[rst], s52
XL 3,192
AR 53,173
AR 54,174
M 7,22,4
WL 4
LO 124,18,124
LU 18,124
AR 55,175
M 7,23,5
RA 224,1,2048
HS 24,18
XS 52
XS 53
M 8,34,4
LL 16,80
LL 106,81
XS 54
XS 55
M 8,35,5
LA 16
DQ 3072
AW 172,52
AW 173,53
M 9,46,4
s_add_u32 s50, s50, %[d]
LC_ 106
AW 174,54
M 9,47,5
AW 175,55
AR 52,176
M 10,58,4
WL 3
LO 125,19,125
LU 19,125
AR 53,177
AR 54,178
M 10,59,5
RA 228,1,3072
HS 24,19
M 11,70,4
LL 17,82
LL 107,83
M 11,71,5
LA 17
HX 24
s_cmp_eq_u32 s50, %[wrap]
s_cselect_b32 s50, %[rst], s50
AR 55,179
XS 52
XS 53
XS 54
XS 55
Z 0,72,0
LC_ 107
XL 4,256
AW 176,52
AW 177,53
M 0,73,1
s_add_u32 s38, s38, 32768
AW 178,54
AW 179,55
Z 1,12,0
WL 3
LO 106,16,106
LU 16,106
AR 52,180
M 1,13,1
s_cmp_eq_u32 s38, s77
RA 232,1,4096
HZ 25,16
AR 53,181
AR 54,182
Z 2,24,0
s_cselect_b32 s38, s76, s38
LL 18,84
LL 110,85
AR 55,183
XS 52
M 2,25,1
s_barrier
LA 18
XS 53
XS 54
Z 3,36,0
s_sub_u32 s39, 0, s39
LC_ 110
ds_read_b64 v[14:15], v5 offset:0
XS 55
M 3,37,1
XL 5,320
v_subrev_u32 v5, s39, v5
AW 180,52
AW 181,53
Z 4,48,0
WL 4
LO 107,17,107
LU 17,107
AW 182,54
AW 183,55
M 4,49,1
s_add_u32 s42, s44, s60
RA 236,1,6144
HS 25,17
AR 52,184
AR 53,185
Z 5,60,0
LL 19,86
LL 111,87
AR 54,186
M 5,61,1
s_addc_u32 s43, s45, 0
LA 19
W1 24,0
AR 55,187
XS 52
M 0,74,2
LC_ 111
XL 6,384
XS 53
XS 54
M 0,75,3
ST 104
XS 55
AW 184,52
M 1,14,2
WL 5
LO 110,18,110
LU 18,110
AW 185,53
M 1,15,3
RA 216,1,7168
HS 25,18
AW 186,54
AW 187,55
M 2,26,2
LL 16,88
LL 114,89
AR 52,188
AR 53,189
M 2,27,3
WL 5
LA 16
v_add_f32 v14, v14, v15
AR 54,190
AR 55,191
M 3,38,2
LC_ 114
XL 7,448
XS 52
M 3,39,3
s_add_u32 s42, s44, s61
v_add_f32_dpp v14, v14, v14 quad_perm:[1,0,3,2] row_mask:0xf bank_mask:0xf
XS 53
XS 54
M 4,50,2
WL 3
LO 111,19,111
LU 19,111
XS 55
AW 188,52
M 4,51,3
s_addc_u32 s43, s45, 0
RA 220,1,8192
HS 25,19
AW 189,53
AW 190,54
M 5,62,2
LL 17,90
LL 115,91
v_add_f32_dpp v14, v14, v14 quad_perm:[2,3,0,1] row_mask:0xf bank_mask:0xf
AW 191,55
M 5,63,3
LA 17
ST 108
AR 52,192
AR 53,193
M 0,76,4
s_mov_b32 m0, %[pm]
LC_ 115
DF
v_add_f32_dpp v14, v14, v14 row_half_mirror row_mask:0xf bank_mask:0xf
AR 54,194
AR 55,195
M 0,77,5
v_cvt_f64_f32 v[14:15], v14
XS 52
XS 53
M 1,16,4
WL 3
LO 114,16,114
LU 16,114
XS 54
M 1,17,5
RA 224,1,9216
HS 25,16
XS 55
AW 192,52
M 2,28,4
LL 18,92
LL 118,93
s_mov_b64 exec, s[54:55]
SP
s_mov_b64 exec, -1
AW 193,53
AW 194,54
M 2,29,5
LA 18
XL 8,512
AW 195,55
AR 52,196
RV 40,8,6144
RV 44,8,6208
RV 48,8,6272
RA 242,7,0
RA 250,7,256
AR 53,197
M 3,40,4
LC_ 118
AR 54,198
M 3,41,5
s_add_u32 s42, s44, s62
AR 55,199
XS 52
M 4,52,4
WL 8
LO 115,17,115
LU 17,115
XS 53
)"
R"(M 4,53,5
s_addc_u32 s43, s45, 0
RA 228,1,10240
HS 25,17
XS 54
XS 55
M 5,64,4
WV 17
LL 19,94
LL 119,95
AW 196,52
M 5,65,5
WL 6
LA 19
ST 112
AW 197,53
AW 198,54
LZ 40,0
AW 199,55
M 0,78,0
LC_ 119
AR 52,200
M 0,79,1
AR 53,201
AR 54,202
M 1,18,0
WL 3
LO 118,18,118
LU 18,118
AR 55,203
M 1,19,1
RA 232,1,11264
HS 25,18
XS 52
XS 53
M 2,30,0
LL 16,96
LL 122,97
XS 54
XS 55
M 2,31,1
WV 16
LA 16
RV 252,22,5392
AW 200,52
AW 201,53
LS 44,1
RV 40,8,6336
AW 202,54
M 3,42,0
LC_ 122
XL 9,576
M 3,43,1
s_add_u32 s42, s44, s63
AW 203,55
AR 52,204
M 4,54,0
WL 5
LO 119,19,119
LU 19,119
AR 53,205
AR 54,206
M 4,55,1
s_addc_u32 s43, s45, 0
RA 236,1,12288
HS 25,19
AR 55,207
XS 52
M 5,66,0
LL 17,98
LL 123,99
XS 53
M 5,67,1
LA 17
ST 116
XS 54
XS 55
M 0,80,2
LC_ 123
XL 10,640
AW 204,52
AW 205,53
M 0,81,3
WV 16
AW 206,54
AW 207,55
LS 48,2
RV 44,8,6400
AR 52,208
M 1,20,2
WL 4
LO 122,16,122
LU 16,122
M 1,21,3
RA 216,1,13312
HS 25,16
AR 53,209
AR 54,210
M 2,32,2
LL 18,100
LL 126,101
AR 55,211
XS 52
M 2,33,3
LA 18
XS 53
XS 54
M 3,44,2
WV 14
LC_ 126
XS 55
M 3,45,3
s_add_u32 s42, s44, s64
AW 208,52
AW 209,53
LS 40,3
RV 48,8,6464
AW 210,54
M 4,56,2
WL 5
LO 123,17,123
LU 17,123
AW 211,55
M 4,57,3
s_addc_u32 s43, s45, 0
RA 220,1,14336
HS 25,17
v_min_u32 v35, v35, v13
M 5,68,2
LL 19,102
LL 127,103
M 5,69,3
LA 19
ST 120
M 0,82,4
LC_ 127
XL 11,704
M 0,83,5
M 1,22,4
WL 4
LO 126,18,126
LU 18,126
M 1,23,5
WV 14
RA 224,1,15360
HS 25,18
LS 44,4
RV 40,8,6528
M 2,34,4
LL 16,56
LL 104,57
M 2,35,5
LA 16
M 3,46,4
WV 13
LC_ 104
M 3,47,5
s_add_u32 s42, s44, s65
M 4,58,4
WL 4
LO 127,19,127
LU 19,127
M 4,59,5
s_addc_u32 s43, s45, 0
RA 228,1,16384
HS 25,19
LS 48,5
RV 44,8,6592
M 5,70,4
LL 17,58
LL 105,59
M 5,71,5
LA 17
ST 124
Z 6,72,0
LC_ 105
HX 25
M 6,73,1
W1 25,1024
Z 7,12,0
WL 5
LO 104,16,104
LU 16,104
M 7,13,1
RA 232,1,17408
HZ 26,16
Z 8,24,0
WV 13
LL 18,60
LL 108,61
M 8,25,1
s_mul_i32 s53, s50, 25600
LA 18
v_add_u32 v4, s39, v4
LS 40,6
RV 48,8,6656
Z 9,36,0
s_add_u32 s40, %[wlo], s53
LC_ 108
M 9,37,1
s_addc_u32 s41, %[whi], 0
Z 10,48,0
WL 5
LO 105,17,105
LU 17,105
M 10,49,1
s_mov_b32 m0, s38
RA 236,1,18432
HS 26,17
Z 11,60,0
LL 19,62
LL 109,63
M 11,61,1
WV 11
LA 19
DW 0
LS 44,7
RV 40,8,6720
HX 26
M 6,74,2
LC_ 109
M 6,75,3
M 7,14,2
WL 4
LO 108,18,108
LU 18,108
M 7,15,3
s_add_u32 s37, s37, 32768
RA 216,1,19456
HZ 24,18
M 8,26,2
s_cmp_eq_u32 s37, s75
LL 16,64
LL 112,65
M 8,27,3
s_cselect_b32 s37, s74, s37
LA 16
DW 1024
M 9,38,2
LC_ 112
M 9,39,3
WV 9
LS 48,8
RV 44,8,6784
M 10,50,2
WL 4
LO 109,19,109
LU 19,109
M 10,51,3
RA 220,1,20480
HS 24,19
M 11,62,2
LL 17,66
LL 113,67
M 11,63,3
LA 17
DW 2048
M 6,76,4
LC_ 113
M 6,77,5
WV 8
LS 40,9
RV 48,8,6848
M 7,16,4
WL 5
LO 112,16,112
LU 16,112
M 7,17,5
s_mul_i32 s53, s51, 49152
RA 224,1,21504
HS 24,16
M 8,28,4
s_bitcmp1_b64 s[90:91], s51
LL 18,68
LL 116,69
M 8,29,5
s_cselect_b64 s[44:45], s[84:85], s[82:83]
LA 18
M 9,40,4
s_add_u32 s44, s44, s53
LC_ 116
M 9,41,5
s_addc_u32 s45, s45, 0
M 10,52,4
WL 4
LO 113,17,113
LU 17,113
M 10,53,5
s_bitcmp1_b64 s[92:93], s51
RA 228,1,22528
HS 24,17
WV 6
LS 44,10
s_cselect_b64 s[94:95], s[88:89], s[86:87]
M 11,64,4
LL 19,70
LL 117,71
M 11,65,5
s_add_u32 s94, s94, s53
LA 19
M 6,78,0
s_addc_u32 s95, s95, 0
LC_ 117
M 6,79,1
s_add_u32 s51, s51, %[d]
M 7,18,0
WL 3
LO 116,18,116
LU 18,116
M 7,19,1
s_cmp_eq_u32 s51, %[wrap]
RA 232,1,23552
HS 24,18
WV 4
LS 48,11
s_cselect_b32 s51, %[rst], s51
M 8,30,0
s_mul_i32 s53, %[d], 0
LL 16,72
LL 120,73
M 8,31,1
s_add_u32 s60, s53, %[sb1]
LA 16
M 9,42,0
s_add_u32 s40, s40, 3072
LC_ 120
s_addc_u32 s41, s41, 0
s_add_u32 m0, s38, 3072
M 9,43,1
DW 0
M 10,54,0
WL 3
LO 117,19,117
LU 19,117
M 10,55,1
RA 236,1,24576
HS 24,19
M 11,66,0
LL 17,74
LL 121,75
M 11,67,1
LA 17
DW 1024
v_mul_f32 v36, 0xbb2aaaab, v36
v_mul_f32 v37, 0xbb2aaaab, v37
v_mul_f32 v38, 0xbb2aaaab, v38
v_mul_f32 v39, 0xbb2aaaab, v39
M 6,80,2
LC_ 121
DW 2048
M 6,81,3
s_lshl_b32 s53, s52, 8
v_add_u32 v1, s37, v0
LD 40,244,0
s_add_u32 s48, %[alo], s53
M 7,20,2
WL 3
LO 120,16,120
LU 16,120
M 7,21,3
s_addc_u32 s49, %[ahi], 0
RA 216,1,0
HS 24,16
LD 44,240,0
RA 242,7,512
v_and_b32 v22, 0xfffffcff, v1
M 8,32,2
s_add_u32 s52, s52, %[d]
LL 18,76
LL 124,77
M 8,33,3
s_cmp_eq_u32 s52, %[wrap]
LA 18
M 9,44,2
s_cselect_b32 s52, %[rst], s52
LC_ 124
DQ 3072
M 9,45,3
LD 48,252,1
CF 128,40,41
CF 129,42,43
M 10,56,2
WL 4
LO 121,17,121
LU 17,121
M 10,57,3
s_add_u32 s50, s50, %[d]
RA 220,1,1024
HS 24,17
LD 40,248,1
RA 250,7,768
CF 130,44,45
CF 131,46,47
M 11,68,2
LL 19,78
LL 125,79
M 11,69,3
s_cmp_eq_u32 s50, %[wrap]
LA 19
RV 252,22,5120
)"
R"(M 6,82,4
s_cselect_b32 s50, %[rst], s50
LC_ 125
M 6,83,5
WL 7
LD 44,244,2
CF 132,48,49
M 7,22,4
WL 5
LO 124,18,124
LU 18,124
M 7,23,5
s_add_u32 s38, s38, 32768
RA 224,1,2048
HS 24,18
GZ 0
s_cmp_eq_u32 s38, s77
CF 133,50,51
M 8,34,4
s_cselect_b32 s38, s76, s38
LL 16,80
LL 106,81
M 8,35,5
s_barrier
LA 16
M 9,46,4
s_sub_u32 s39, 0, s39
LC_ 106
ds_read_b64 v[14:15], v5 offset:0
M 9,47,5
s_nop 0
v_subrev_u32 v5, s39, v5
LD 48,240,2
RA 242,7,1024
CF 134,40,41
CF 135,42,43
M 10,58,4
WL 5
LO 125,19,125
LU 19,125
M 10,59,5
RA 228,1,3072
HS 24,19
LD 40,252,3
CF 136,44,45
M 11,70,4
LL 17,82
LL 107,83
M 11,71,5
LA 17
HX 24
Z 0,72,0
LC_ 107
W1 24,0
M 0,73,1
W1 26,2048
GS 1
CF 137,46,47
Z 1,12,0
WL 7
LO 106,16,106
LU 16,106
M 1,13,1
RA 232,1,4096
HZ 27,16
LD 44,248,3
RA 250,7,1280
CF 138,48,49
CF 139,50,51
Z 2,24,0
LL 18,84
LL 110,85
M 2,25,1
WL 7
LA 18
v_add_f32 v14, v14, v15
LD 48,244,4
CF 140,40,41
v_add_f32_dpp v14, v14, v14 quad_perm:[1,0,3,2] row_mask:0xf bank_mask:0xf
Z 3,36,0
LC_ 110
M 3,37,1
s_add_u32 s42, s94, s60
v_add_f32_dpp v14, v14, v14 quad_perm:[2,3,0,1] row_mask:0xf bank_mask:0xf
Z 4,48,0
WL 6
LO 107,17,107
LU 17,107
M 4,49,1
s_addc_u32 s43, s95, 0
RA 236,1,6144
HS 27,17
GS 2
SU 104
CF 141,42,43
v_add_f32_dpp v14, v14, v14 row_half_mirror row_mask:0xf bank_mask:0xf
Z 5,60,0
LL 19,86
LL 111,87
M 5,61,1
LA 19
v_cvt_f64_f32 v[14:15], v14
LD 40,240,4
RA 242,7,1536
CF 142,44,45
CF 143,46,47
HX 27
M 0,74,2
LC_ 111
M 0,75,3
s_mov_b64 exec, s[54:55]
SP
s_mov_b64 exec, -1
M 1,14,2
WL 4
LO 110,18,110
LU 18,110
M 1,15,3
RA 216,1,7168
HZ 25,18
LD 44,252,5
CF 144,48,49
M 2,26,2
LL 16,88
LL 114,89
M 2,27,3
LA 16
GS 3
CF 145,50,51
M 3,38,2
LC_ 114
M 3,39,3
s_add_u32 s42, s44, s61
M 4,50,2
WL 3
LO 111,19,111
LU 19,111
M 4,51,3
s_addc_u32 s43, s45, 0
RA 220,1,8192
HS 25,19
LD 48,248,5
RA 250,7,1792
CF 146,40,41
CF 147,42,43
M 5,62,2
LL 17,90
LL 115,91
M 5,63,3
LA 17
ST 108
LD 40,244,6
s_mov_b32 m0, %[pm]
CF 148,44,45
M 0,76,4
LC_ 115
DF
M 0,77,5
M 1,16,4
WL 4
LO 114,16,114
LU 16,114
M 1,17,5
RA 224,1,9216
HS 25,16
GS 4
CF 149,46,47
M 2,28,4
LL 18,92
LL 118,93
M 2,29,5
LA 18
LD 44,240,6
RA 242,7,2048
CF 150,48,49
CF 151,50,51
M 3,40,4
WL 5
LC_ 118
M 3,41,5
s_add_u32 s42, s44, s62
LD 48,252,7
s_addc_u32 s43, s45, 0
CF 152,40,41
M 4,52,4
WL 4
LO 115,17,115
LU 17,115
M 4,53,5
RA 228,1,10240
HS 25,17
M 5,64,4
LL 19,94
LL 119,95
M 5,65,5
LA 19
ST 112
GS 5
CF 153,42,43
M 0,78,0
LC_ 119
M 0,79,1
LD 40,248,7
RA 250,7,2304
CF 154,44,45
CF 155,46,47
M 1,18,0
WL 4
LO 118,18,118
LU 18,118
M 1,19,1
RA 232,1,11264
HS 25,18
M 2,30,0
LL 16,96
LL 122,97
M 2,31,1
LA 16
RV 252,22,5392
LD 44,244,8
CF 156,48,49
M 3,42,0
LC_ 122
M 3,43,1
s_add_u32 s42, s44, s63
GS 6
s_addc_u32 s43, s45, 0
CF 157,50,51
M 4,54,0
WL 5
LO 119,19,119
LU 19,119
M 4,55,1
RA 236,1,12288
HS 25,19
M 5,66,0
LL 17,98
LL 123,99
M 5,67,1
LA 17
ST 116
LD 48,240,8
RA 242,7,2560
CF 158,40,41
CF 159,42,43
M 0,80,2
LC_ 123
M 0,81,3
WL 8
LD 40,252,9
CF 160,44,45
M 1,20,2
WL 4
LO 122,16,122
LU 16,122
M 1,21,3
RA 216,1,13312
HS 25,16
M 2,32,2
LL 18,100
LL 126,101
M 2,33,3
LA 18
GS 7
CF 161,46,47
M 3,44,2
LC_ 126
M 3,45,3
s_add_u32 s42, s44, s64
LD 44,248,9
RA 250,7,2816
CF 162,48,49
CF 163,50,51
M 4,56,2
WL 4
LO 123,17,123
LU 17,123
M 4,57,3
s_addc_u32 s43, s45, 0
RA 220,1,14336
HS 25,17
LD 48,244,10
ST 120
CF 164,40,41
M 5,68,2
LL 19,102
LL 127,103
M 5,69,3
LA 19
M 0,82,4
LC_ 127
M 0,83,5
GS 8
CF 165,42,43
M 1,22,4
WL 4
LO 126,18,126
LU 18,126
M 1,23,5
RA 224,1,15360
HS 25,18
LD 40,240,10
CF 166,44,45
CF 167,46,47
M 2,34,4
LL 16,56
LL 104,57
M 2,35,5
LA 16
M 3,46,4
WL 5
LC_ 104
M 3,47,5
s_add_u32 s42, s44, s65
LD 44,252,11
s_addc_u32 s43, s45, 0
CF 168,48,49
M 4,58,4
WL 3
LO 127,19,127
LU 19,127
M 4,59,5
RA 228,1,16384
HS 25,19
GS 9
ST 124
CF 169,50,51
M 5,70,4
LL 17,58
LL 105,59
M 5,71,5
LA 17
HX 25
Z 6,72,0
LC_ 105
W1 25,1024
M 6,73,1
W1 27,3072
LD 48,248,11
CF 170,40,41
CF 171,42,43
Z 7,12,0
WL 5
LO 104,16,104
LU 16,104
M 7,13,1
RA 232,1,17408
HZ 26,16
GS 10
CF 172,44,45
CF 173,46,47
v_add_u32 v4, s39, v4
Z 8,24,0
LL 18,60
LL 108,61
M 8,25,1
s_mul_i32 s53, s50, 25600
LA 18
Z 9,36,0
s_add_u32 s40, %[wlo], s53
LC_ 108
M 9,37,1
s_addc_u32 s41, %[whi], 0
CF 174,48,49
CF 175,50,51
Z 10,48,0
WL 5
LO 105,17,105
LU 17,105
M 10,49,1
s_mov_b32 m0, s38
RA 236,1,18432
HS 26,17
GS 11
Z 11,60,0
LL 19,62
LL 109,63
M 11,61,1
s_mul_i32 s53, %[d], 196608
LA 19
HX 26
RV 36,8,6912
M 6,74,2
s_add_u32 s53, s53, s57
LC_ 109
M 6,75,3
s_add_u32 s46, %[xlo], s53
M 7,14,2
WL 4
LO 108,18,108
LU 18,108
M 7,15,3
s_addc_u32 s47, %[xhi], 0
RA 216,1,19456
HZ 24,18
v_cndmask_b32 v52, v52, v53, s[78:79]
v_cndmask_b32 v54, v54, v55, s[78:79]
v_cndmask_b32 v52, v52, v54, s[80:81]
v_mul_f32 v52, 0x3b2aaaab, v52
M 8,26,2
)"
R"(s_mov_b32 s53, 0xfe987610
LL 16,64
LL 112,65
M 8,27,3
s_cmp_eq_u32 %[d], 1
LA 16
C 52,52,52
v_lshlrev_b32 v52, 16, v52
WL 3
v_add_f32 v52, v36, v52
C 52,52,52
M 9,38,2
s_cselect_b32 s53, s53, 0xda43cb52
LC_ 112
M 9,39,3
s_bitcmp1_b64 s[92:93], 48
M 10,50,2
WL 3
LO 109,19,109
LU 19,109
M 10,51,3
s_cselect_b32 s58, 16, 0
RA 220,1,20480
HS 24,19
v_lshlrev_b32 v52, 16, v52
v_sqrt_f32 v52, v52
M 11,62,2
LL 17,66
LL 113,67
M 11,63,3
s_lshr_b32 s58, s53, s58
LA 17
v_lshrrev_b32 v3, 4, v0
C 52,52,52
v_lshlrev_b32 v52, 16, v52
v_cmp_ge_f32 vcc, v37, v52
v_rcp_f32 v52, v52
v_and_b32 v3, 12, v3
M 6,76,4
LC_ 113
v_bfe_u32 v3, s58, v3, 4
M 6,77,5
v_mad_u32_u24 v3, v3, s97, v21
DW 0
M 7,16,4
WL 3
LO 112,16,112
LU 16,112
M 7,17,5
s_add_u32 s37, s37, 32768
RA 224,1,21504
HS 24,16
v_mul_f32 v52, v38, v52
v_cndmask_b32 v52, 0, v52, vcc
CF 52,52,52
M 8,28,4
s_cmp_eq_u32 s37, s75
LL 18,68
LL 116,69
M 8,29,5
s_cselect_b32 s37, s74, s37
LA 18
DW 1024
ds_bpermute_b32 v13, v28, v52
M 9,40,4
LC_ 116
XL 12,0
M 9,41,5
DW 2048
M 10,52,4
WL 4
LO 113,17,113
LU 17,113
M 10,53,5
s_mul_i32 s53, s51, 49152
RA 228,1,22528
HS 24,17
M 11,64,4
LL 19,70
LL 117,71
M 11,65,5
s_bitcmp1_b64 s[90:91], s51
LA 19
M 6,78,0
s_cselect_b64 s[44:45], s[84:85], s[82:83]
LC_ 117
M 6,79,1
s_add_u32 s44, s44, s53
M 7,18,0
WL 3
LO 116,18,116
LU 18,116
M 7,19,1
s_addc_u32 s45, s45, 0
RA 232,1,23552
HS 24,18
M 8,30,0
s_bitcmp1_b64 s[92:93], s51
LL 16,72
LL 120,73
M 8,31,1
s_cselect_b64 s[94:95], s[88:89], s[86:87]
LA 16
s_add_u32 s94, s94, s53
s_addc_u32 s95, s95, 0
s_add_u32 s51, s51, %[d]
s_cmp_eq_u32 s51, %[wrap]
s_cselect_b32 s51, %[rst], s51
XS 128
M 9,42,0
s_add_u32 s40, s40, 3072
LC_ 120
s_addc_u32 s41, s41, 0
s_add_u32 m0, s38, 3072
XS 129
M 9,43,1
XL 13,64
XS 130
M 10,54,0
WL 3
LO 117,19,117
LU 19,117
M 10,55,1
RA 236,1,24576
HS 24,19
M 11,66,0
LL 17,74
LL 121,75
M 11,67,1
LA 17
DW 0
M 6,80,2
LC_ 121
DW 1024
M 6,81,3
XL 14,128
v_add_u32 v1, s37, v0
M 7,20,2
WL 3
LO 120,16,120
LU 16,120
M 7,21,3
RA 216,1,0
HS 24,16
M 8,32,2
LL 18,76
LL 124,77
XS 131
M 8,33,3
LA 18
DW 2048
XS 132
M 9,44,2
s_lshl_b32 s53, s52, 8
LC_ 124
v_and_b32 v22, 0xfffffcff, v1
M 9,45,3
s_add_u32 s48, %[alo], s53
XS 133
XS 134
M 10,56,2
WL 3
LO 121,17,121
LU 17,121
M 10,57,3
s_addc_u32 s49, %[ahi], 0
RA 220,1,1024
HS 24,17
M 11,68,2
LL 19,78
LL 125,79
M 11,69,3
s_add_u32 s52, s52, %[d]
LA 19
RV 252,22,5120
XS 135
M 6,82,4
s_cmp_eq_u32 s52, %[wrap]
LC_ 125
XS 136
M 6,83,5
s_cselect_b32 s52, %[rst], s52
XL 15,192
XS 137
M 7,22,4
WL 4
LO 124,18,124
LU 18,124
M 7,23,5
RA 224,1,2048
HS 24,18
M 8,34,4
LL 16,80
LL 106,81
XS 138
M 8,35,5
LA 16
DQ 3072
M 9,46,4
s_add_u32 s50, s50, %[d]
LC_ 106
XS 139
M 9,47,5
XS 140
XS 141
M 10,58,4
WL 3
LO 125,19,125
LU 19,125
M 10,59,5
RA 228,1,3072
HS 24,19
M 11,70,4
LL 17,82
LL 107,83
M 11,71,5
LA 17
HX 24
s_cmp_eq_u32 s50, %[wrap]
s_cselect_b32 s50, %[rst], s50
XS 142
Z 0,72,0
LC_ 107
XL 16,256
M 0,73,1
s_add_u32 s38, s38, 32768
XS 143
XS 144
Z 1,0,0
WL 3
LO 106,16,106
LU 16,106
M 1,1,1
s_cmp_eq_u32 s38, s77
RA 232,1,4096
HZ 27,16
Z 2,24,0
s_cselect_b32 s38, s76, s38
LL 18,84
LL 110,85
XS 145
M 2,25,1
s_barrier
LA 18
XS 146
Z 3,36,0
s_sub_u32 s39, 0, s39
LC_ 110
ds_read_b64 v[14:15], v5 offset:0
M 3,37,1
XL 17,320
XS 147
Z 4,48,0
WL 4
LO 107,17,107
LU 17,107
M 4,49,1
s_add_u32 s42, s94, s60
RA 236,1,6144
HS 27,17
XS 148
Z 5,60,0
LL 19,86
LL 111,87
M 5,61,1
s_addc_u32 s43, s95, 0
LA 19
W1 24,0
XS 149
M 0,74,2
LC_ 111
W1 26,2048
M 0,75,3
XL 18,384
HX 27
XS 150
M 1,2,2
WL 6
LO 110,18,110
LU 18,110
XS 151
M 1,3,3
RA 216,1,7168
HZ 25,18
M 2,26,2
LL 16,88
LL 114,89
XS 152
M 2,27,3
LA 16
SU 104
M 3,38,2
WL 7
LC_ 114
XL 19,448
XS 153
M 3,39,3
s_add_u32 s42, s44, s61
v_add_f32 v14, v14, v15
XS 154
M 4,50,2
WL 4
LO 111,19,111
LU 19,111
v_add_f32_dpp v14, v14, v14 quad_perm:[1,0,3,2] row_mask:0xf bank_mask:0xf
M 4,51,3
s_addc_u32 s43, s45, 0
RA 220,1,8192
HS 25,19
XS 155
M 5,62,2
LL 17,90
LL 115,91
v_add_f32_dpp v14, v14, v14 quad_perm:[2,3,0,1] row_mask:0xf bank_mask:0xf
M 5,63,3
LA 17
ST 108
XS 156
M 0,76,4
s_mov_b32 m0, %[pm]
LC_ 115
DF
v_add_f32_dpp v14, v14, v14 row_half_mirror row_mask:0xf bank_mask:0xf
M 0,77,5
v_cvt_f64_f32 v[14:15], v14
XS 157
M 1,4,4
WL 3
LO 114,16,114
LU 16,114
M 1,5,5
RA 224,1,9216
HS 25,16
XS 158
M 2,28,4
LL 18,92
LL 118,93
s_mov_b64 exec, s[54:55]
SP
s_mov_b64 exec, -1
XS 159
M 2,29,5
LA 18
ds_read_b64 v[14:15], v5 offset:2048
RV 40,8,6144
RV 44,8,6208
RV 48,8,6272
RA 242,7,0
RA 250,7,256
XS 160
M 3,40,4
LC_ 118
XL 20,512
M 3,41,5
s_add_u32 s42, s44, s62
v_subrev_u32 v5, s39, v5
XS 161
M 4,52,4
WL 9
LO 115,17,115
LU 17,115
M 4,53,5
s_addc_u32 s43, s45, 0
RA 228,1,10240
HS 25,17
XS 162
M 5,64,4
WV 17
LL 19,94
LL 119,95
M 5,65,5
WL 6
LA 19
ST 112
XS 163
LZ 40,12
XS 164
M 0,78,0
LC_ 119
XS 165
M 0,79,1
XS 166
XS 167
M 1,6,0
WL 3
LO 118,18,118
LU 18,118
M 1,7,1
RA 232,1,11264
HS 25,18
M 2,30,0
LL 16,96
LL 122,97
M 2,31,1
WV 16
LA 16
RV 252,22,5392
LS 44,13
RV 40,8,6336
v_add_f32 v14, v14, v15
)"
R"(s_nop 1
v_add_f32_dpp v14, v14, v14 quad_perm:[1,0,3,2] row_mask:0xf bank_mask:0xf
M 3,42,0
LC_ 122
XL 21,576
M 3,43,1
s_add_u32 s42, s44, s63
v_add_f32_dpp v14, v14, v14 quad_perm:[2,3,0,1] row_mask:0xf bank_mask:0xf
XS 168
M 4,54,0
WL 5
LO 119,19,119
LU 19,119
M 4,55,1
s_addc_u32 s43, s45, 0
RA 236,1,12288
HS 25,19
v_add_f32_dpp v14, v14, v14 row_half_mirror row_mask:0xf bank_mask:0xf
M 5,66,0
LL 17,98
LL 123,99
XS 169
M 5,67,1
LA 17
ST 116
v_cvt_f64_f32 v[14:15], v14
XS 170
M 0,80,2
LC_ 123
s_mov_b64 exec, s[54:55]
SP
s_mov_b64 exec, -1
M 0,81,3
WV 16
XL 22,640
XS 171
LS 48,14
RV 44,8,6400
XS 172
M 1,8,2
WL 4
LO 122,16,122
LU 16,122
M 1,9,3
RA 216,1,13312
HS 25,16
M 2,32,2
LL 18,100
LL 126,101
M 2,33,3
LA 18
XS 173
M 3,44,2
WV 15
LC_ 126
XS 174
M 3,45,3
s_add_u32 s42, s44, s64
XS 175
v_min_u32 v35, v35, v13
LS 40,15
RV 48,8,6464
M 4,56,2
WL 5
LO 123,17,123
LU 17,123
M 4,57,3
s_addc_u32 s43, s45, 0
RA 220,1,14336
HS 25,17
M 5,68,2
LL 19,102
LL 127,103
M 5,69,3
LA 19
ST 120
M 0,82,4
LC_ 127
XL 23,704
M 0,83,5
M 1,10,4
WL 4
LO 126,18,126
LU 18,126
M 1,11,5
WV 15
RA 224,1,15360
HS 25,18
LS 44,16
RV 40,8,6528
M 2,34,4
LL 16,56
LL 104,57
M 2,35,5
LA 16
M 3,46,4
WV 14
LC_ 104
M 3,47,5
s_add_u32 s42, s44, s65
M 4,58,4
WL 4
LO 127,19,127
LU 19,127
M 4,59,5
s_addc_u32 s43, s45, 0
RA 228,1,16384
HS 25,19
LS 48,17
RV 44,8,6592
M 5,70,4
LL 17,58
LL 105,59
M 5,71,5
LA 17
ST 124
Z 6,72,0
LC_ 105
HX 25
M 6,73,1
W1 25,1024
Z 7,0,0
WL 5
LO 104,16,104
LU 16,104
M 7,1,1
RA 232,1,17408
HZ 26,16
Z 8,24,0
WV 14
LL 18,60
LL 108,61
M 8,25,1
s_mul_i32 s53, s50, 25600
LA 18
W1 27,3072
LS 40,18
RV 48,8,6656
Z 9,36,0
s_add_u32 s40, %[wlo], s53
LC_ 108
v_add_u32 v4, s39, v4
M 9,37,1
s_addc_u32 s41, %[whi], 0
Z 10,48,0
WL 6
LO 105,17,105
LU 17,105
M 10,49,1
s_mov_b32 m0, s38
RA 236,1,18432
HS 26,17
Z 11,60,0
LL 19,62
LL 109,63
M 11,61,1
WV 12
LA 19
DW 0
LS 44,19
RV 40,8,6720
M 6,74,2
LC_ 109
M 6,75,3
M 7,2,2
WL 4
LO 108,18,108
LU 18,108
M 7,3,3
s_add_u32 s37, s37, 32768
RA 216,1,19456
HZ 29,18
M 8,26,2
s_cmp_eq_u32 s37, s75
LL 16,64
LL 112,65
M 8,27,3
s_cselect_b32 s37, s74, s37
LA 16
DW 1024
M 9,38,2
LC_ 112
M 9,39,3
WV 10
LS 48,20
RV 44,8,6784
M 10,50,2
WL 4
LO 109,19,109
LU 19,109
M 10,51,3
RA 220,1,20480
HS 29,19
M 11,62,2
LL 17,66
LL 113,67
M 11,63,3
LA 17
DW 2048
M 6,76,4
LC_ 113
v_pk_add_f16 v26, v29, v26
M 6,77,5
HX 26
WV 9
LS 40,21
RV 48,8,6848
M 7,4,4
WL 5
LO 112,16,112
LU 16,112
M 7,5,5
s_mul_i32 s53, s51, 49152
RA 224,1,21504
HZ 24,16
M 8,28,4
s_bitcmp1_b64 s[90:91], s51
LL 18,68
LL 116,69
M 8,29,5
s_cselect_b64 s[44:45], s[84:85], s[82:83]
LA 18
M 9,40,4
s_add_u32 s44, s44, s53
LC_ 116
M 9,41,5
s_addc_u32 s45, s45, 0
M 10,52,4
WL 4
LO 113,17,113
LU 17,113
M 10,53,5
s_bitcmp1_b64 s[92:93], s51
RA 228,1,22528
HS 24,17
WV 6
LS 44,22
s_cselect_b64 s[94:95], s[88:89], s[86:87]
M 11,64,4
LL 19,70
LL 117,71
M 11,65,5
s_add_u32 s94, s94, s53
LA 19
M 6,78,0
s_addc_u32 s95, s95, 0
LC_ 117
M 6,79,1
s_add_u32 s51, s51, %[d]
M 7,6,0
WL 3
LO 116,18,116
LU 18,116
M 7,7,1
s_cmp_eq_u32 s51, %[wrap]
RA 232,1,23552
HS 24,18
WV 4
LS 48,23
s_cselect_b32 s51, %[rst], s51
M 8,30,0
s_mul_i32 s53, %[d], 1024
LL 16,72
LL 120,73
M 8,31,1
s_add_u32 s61, s53, %[sb1]
LA 16
M 9,42,0
s_add_u32 s40, s40, 3072
LC_ 120
s_addc_u32 s41, s41, 0
s_add_u32 m0, s38, 3072
M 9,43,1
DW 0
M 10,54,0
WL 3
LO 117,19,117
LU 19,117
M 10,55,1
RA 236,1,24576
HS 24,19
M 11,66,0
LL 17,74
LL 121,75
M 11,67,1
LA 17
DW 1024
v_mul_f32 v36, 0xbb2aaaab, v36
v_mul_f32 v37, 0xbb2aaaab, v37
v_mul_f32 v38, 0xbb2aaaab, v38
v_mul_f32 v39, 0xbb2aaaab, v39
M 6,80,2
LC_ 121
DW 2048
M 6,81,3
s_lshl_b32 s53, s52, 8
v_add_u32 v1, s37, v0
LD 40,244,12
s_add_u32 s48, %[alo], s53
M 7,8,2
WL 3
LO 120,16,120
LU 16,120
M 7,9,3
s_addc_u32 s49, %[ahi], 0
RA 216,1,0
HS 24,16
LD 44,240,12
RA 242,7,512
v_and_b32 v22, 0xfffffcff, v1
M 8,32,2
s_add_u32 s52, s52, %[d]
LL 18,76
LL 124,77
M 8,33,3
s_cmp_eq_u32 s52, %[wrap]
LA 18
M 9,44,2
s_cselect_b32 s52, %[rst], s52
LC_ 124
DQ 3072
M 9,45,3
LD 48,252,13
CF 176,40,41
CF 177,42,43
M 10,56,2
WL 4
LO 121,17,121
LU 17,121
M 10,57,3
s_add_u32 s50, s50, %[d]
RA 220,1,1024
HS 24,17
LD 40,248,13
RA 250,7,768
CF 178,44,45
CF 179,46,47
M 11,68,2
LL 19,78
LL 125,79
M 11,69,3
s_cmp_eq_u32 s50, %[wrap]
LA 19
RV 252,22,5120
M 6,82,4
s_cselect_b32 s50, %[rst], s50
LC_ 125
M 6,83,5
WL 7
LD 44,244,14
CF 180,48,49
M 7,10,4
WL 5
LO 124,18,124
LU 18,124
M 7,11,5
s_add_u32 s38, s38, 32768
RA 224,1,2048
HS 24,18
GZ 12
s_cmp_eq_u32 s38, s77
CF 181,50,51
M 8,34,4
s_cselect_b32 s38, s76, s38
LL 16,80
LL 106,81
M 8,35,5
s_barrier
LA 16
M 9,46,4
s_sub_u32 s39, 0, s39
LC_ 106
ds_read_b64 v[14:15], v5 offset:0
M 9,47,5
LD 48,240,14
RA 242,7,1024
CF 182,40,41
CF 183,42,43
M 10,58,4
WL 5
LO 125,19,125
LU 19,125
M 10,59,5
RA 228,1,3072
HS 24,19
LD 40,252,15
CF 184,44,45
M 11,70,4
LL 17,82
LL 107,83
M 11,71,5
LA 17
HX 24
Z 0,72,0
LC_ 107
W1 24,0
M 0,73,1
W1 26,2048
GS 13
CF 185,46,47
)"
R"(Z 1,0,0
WL 7
LO 106,16,106
LU 16,106
M 1,1,1
RA 232,1,4096
HZ 27,16
LD 44,248,15
RA 250,7,1280
CF 186,48,49
CF 187,50,51
Z 2,24,0
LL 18,84
LL 110,85
M 2,25,1
WL 7
LA 18
v_add_f32 v14, v14, v15
LD 48,244,16
CF 188,40,41
v_add_f32_dpp v14, v14, v14 quad_perm:[1,0,3,2] row_mask:0xf bank_mask:0xf
Z 3,36,0
LC_ 110
M 3,37,1
s_add_u32 s42, s94, s60
v_add_f32_dpp v14, v14, v14 quad_perm:[2,3,0,1] row_mask:0xf bank_mask:0xf
Z 4,48,0
WL 6
LO 107,17,107
LU 17,107
M 4,49,1
s_addc_u32 s43, s95, 0
RA 236,1,6144
HS 27,17
GS 14
SU 104
CF 189,42,43
v_add_f32_dpp v14, v14, v14 row_half_mirror row_mask:0xf bank_mask:0xf
Z 5,60,0
LL 19,86
LL 111,87
M 5,61,1
LA 19
v_cvt_f64_f32 v[14:15], v14
LD 40,240,16
RA 242,7,1536
CF 190,44,45
CF 191,46,47
M 0,74,2
LC_ 111
M 0,75,3
s_mov_b64 exec, s[54:55]
SP
s_mov_b64 exec, -1
M 1,2,2
WL 4
LO 110,18,110
LU 18,110
M 1,3,3
RA 216,1,7168
HZ 29,18
LD 44,252,17
ds_read_b64 v[14:15], v5 offset:2048
CF 192,48,49
M 2,26,2
LL 16,88
LL 114,89
M 2,27,3
LA 16
v_subrev_u32 v5, s39, v5
GS 15
CF 193,50,51
M 3,38,2
LC_ 114
M 3,39,3
s_add_u32 s42, s94, s61
M 4,50,2
WL 4
LO 111,19,111
LU 19,111
M 4,51,3
s_addc_u32 s43, s95, 0
RA 220,1,8192
HS 29,19
LD 48,248,17
RA 250,7,1792
CF 194,40,41
CF 195,42,43
M 5,62,2
LL 17,90
LL 115,91
M 5,63,3
LA 17
SU 108
LD 40,244,18
s_mov_b32 m0, %[pm]
CF 196,44,45
v_pk_add_f16 v27, v29, v27
M 0,76,4
LC_ 115
DF
M 0,77,5
HX 27
M 1,4,4
WL 4
LO 114,16,114
LU 16,114
M 1,5,5
RA 224,1,9216
HZ 25,16
GS 16
CF 197,46,47
v_add_f32 v14, v14, v15
M 2,28,4
LL 18,92
LL 118,93
M 2,29,5
LA 18
v_add_f32_dpp v14, v14, v14 quad_perm:[1,0,3,2] row_mask:0xf bank_mask:0xf
LD 44,240,18
RA 242,7,2048
CF 198,48,49
CF 199,50,51
v_add_f32_dpp v14, v14, v14 quad_perm:[2,3,0,1] row_mask:0xf bank_mask:0xf
M 3,40,4
WL 5
LC_ 118
v_add_f32_dpp v14, v14, v14 row_half_mirror row_mask:0xf bank_mask:0xf
M 3,41,5
s_add_u32 s42, s44, s62
LD 48,252,19
s_addc_u32 s43, s45, 0
CF 200,40,41
M 4,52,4
WL 4
LO 115,17,115
LU 17,115
M 4,53,5
RA 228,1,10240
HS 25,17
M 5,64,4
LL 19,94
LL 119,95
M 5,65,5
LA 19
ST 112
GS 17
CF 201,42,43
v_cvt_f64_f32 v[14:15], v14
M 0,78,0
LC_ 119
s_mov_b64 exec, s[54:55]
SP
s_mov_b64 exec, -1
M 0,79,1
LD 40,248,19
RA 250,7,2304
CF 202,44,45
CF 203,46,47
M 1,6,0
WL 4
LO 118,18,118
LU 18,118
M 1,7,1
RA 232,1,11264
HS 25,18
M 2,30,0
LL 16,96
LL 122,97
M 2,31,1
LA 16
RV 252,22,5392
LD 44,244,20
CF 204,48,49
M 3,42,0
LC_ 122
M 3,43,1
s_add_u32 s42, s44, s63
GS 18
s_addc_u32 s43, s45, 0
CF 205,50,51
M 4,54,0
WL 5
LO 119,19,119
LU 19,119
M 4,55,1
RA 236,1,12288
HS 25,19
M 5,66,0
LL 17,98
LL 123,99
M 5,67,1
LA 17
ST 116
LD 48,240,20
RA 242,7,2560
CF 206,40,41
CF 207,42,43
M 0,80,2
LC_ 123
M 0,81,3
WL 8
LD 40,252,21
CF 208,44,45
M 1,8,2
WL 4
LO 122,16,122
LU 16,122
M 1,9,3
RA 216,1,13312
HS 25,16
M 2,32,2
LL 18,100
LL 126,101
M 2,33,3
LA 18
GS 19
CF 209,46,47
M 3,44,2
LC_ 126
M 3,45,3
s_add_u32 s42, s44, s64
LD 44,248,21
RA 250,7,2816
CF 210,48,49
CF 211,50,51
M 4,56,2
WL 4
LO 123,17,123
LU 17,123
M 4,57,3
s_addc_u32 s43, s45, 0
RA 220,1,14336
HS 25,17
LD 48,244,22
ST 120
CF 212,40,41
M 5,68,2
LL 19,102
LL 127,103
M 5,69,3
LA 19
M 0,82,4
LC_ 127
M 0,83,5
GS 20
CF 213,42,43
M 1,10,4
WL 4
LO 126,18,126
LU 18,126
M 1,11,5
RA 224,1,15360
HS 25,18
LD 40,240,22
CF 214,44,45
CF 215,46,47
M 2,34,4
LL 16,56
LL 104,57
M 2,35,5
LA 16
M 3,46,4
WL 5
LC_ 104
M 3,47,5
s_add_u32 s42, s44, s65
LD 44,252,23
s_addc_u32 s43, s45, 0
CF 216,48,49
M 4,58,4
WL 3
LO 127,19,127
LU 19,127
M 4,59,5
RA 228,1,16384
HS 25,19
GS 21
ST 124
CF 217,50,51
M 5,70,4
LL 17,58
LL 105,59
M 5,71,5
LA 17
HX 25
Z 6,72,0
LC_ 105
W1 25,1024
M 6,73,1
W1 27,3072
LD 48,248,23
CF 218,40,41
CF 219,42,43
Z 7,0,0
WL 5
LO 104,16,104
LU 16,104
M 7,1,1
RA 232,1,17408
HZ 26,16
GS 22
CF 220,44,45
CF 221,46,47
v_add_u32 v4, s39, v4
Z 8,24,0
LL 18,60
LL 108,61
M 8,25,1
s_mul_i32 s53, s50, 25600
LA 18
Z 9,36,0
s_add_u32 s40, %[wlo], s53
LC_ 108
M 9,37,1
s_addc_u32 s41, %[whi], 0
CF 222,48,49
CF 223,50,51
Z 10,48,0
WL 5
LO 105,17,105
LU 17,105
M 10,49,1
s_mov_b32 m0, s38
RA 236,1,18432
HS 26,17
GS 23
Z 11,60,0
LL 19,62
LL 109,63
M 11,61,1
s_mul_i32 s53, %[d], 196608
LA 19
RV 36,8,6912
M 6,74,2
s_add_u32 s53, s53, s57
LC_ 109
M 6,75,3
s_add_u32 s46, %[xlo], s53
M 7,2,2
WL 4
LO 108,18,108
LU 18,108
M 7,3,3
s_addc_u32 s47, %[xhi], 0
RA 216,1,19456
HZ 29,18
v_cndmask_b32 v52, v52, v53, s[78:79]
v_cndmask_b32 v54, v54, v55, s[78:79]
v_cndmask_b32 v52, v52, v54, s[80:81]
v_mul_f32 v52, 0x3b2aaaab, v52
M 8,26,2
s_mov_b32 s53, 0xda43cb52
LL 16,64
LL 112,65
M 8,27,3
s_cmp_eq_u32 %[d], 1
LA 16
C 52,52,52
v_lshlrev_b32 v52, 16, v52
WL 3
v_add_f32 v52, v36, v52
C 52,52,52
M 9,38,2
s_cselect_b32 s53, s53, 0xfe987610
LC_ 112
M 9,39,3
s_bitcmp1_b64 s[92:93], 48
M 10,50,2
WL 3
LO 109,19,109
LU 19,109
M 10,51,3
s_cselect_b32 s58, 16, 0
RA 220,1,20480
HS 29,19
v_lshlrev_b32 v52, 16, v52
v_sqrt_f32 v52, v52
M 11,62,2
LL 17,66
LL 113,67
M 11,63,3
s_lshr_b32 s58, s53, s58
LA 17
v_pk_add_f16 v26, v29, v26
C 52,52,52
v_lshlrev_b32 v52, 16, v52
v_cmp_ge_f32 vcc, v37, v52
v_rcp_f32 v52, v52
HX 26
M 6,76,4
LC_ 113
v_lshrrev_b32 v3, 4, v0
M 6,77,5
v_and_b32 v3, 12, v3
v_bfe_u32 v3, s58, v3, 4
v_mad_u32_u24 v3, v3, s97, v21
DW 0
M 7,4,4
WL 3
)"
R"(LO 112,16,112
LU 16,112
M 7,5,5
s_add_u32 s37, s37, 32768
RA 224,1,21504
HZ 24,16
v_mul_f32 v52, v38, v52
v_cndmask_b32 v52, 0, v52, vcc
CF 52,52,52
M 8,28,4
s_cmp_eq_u32 s37, s75
LL 18,68
LL 116,69
M 8,29,5
s_cselect_b32 s37, s74, s37
LA 18
DW 1024
ds_bpermute_b32 v13, v28, v52
M 9,40,4
LC_ 116
XL 24,0
M 9,41,5
DW 2048
M 10,52,4
WL 4
LO 113,17,113
LU 17,113
M 10,53,5
s_mul_i32 s53, s51, 49152
RA 228,1,22528
HS 24,17
M 11,64,4
LL 19,70
LL 117,71
M 11,65,5
s_bitcmp1_b64 s[90:91], s51
LA 19
M 6,78,0
s_cselect_b64 s[44:45], s[84:85], s[82:83]
LC_ 117
M 6,79,1
s_add_u32 s44, s44, s53
M 7,6,0
WL 3
LO 116,18,116
LU 18,116
M 7,7,1
s_addc_u32 s45, s45, 0
RA 232,1,23552
HS 24,18
M 8,30,0
s_bitcmp1_b64 s[92:93], s51
LL 16,72
LL 120,73
M 8,31,1
s_cselect_b64 s[94:95], s[88:89], s[86:87]
LA 16
s_add_u32 s94, s94, s53
s_addc_u32 s95, s95, 0
s_add_u32 s51, s51, %[d]
s_cmp_eq_u32 s51, %[wrap]
s_cselect_b32 s51, %[rst], s51
XS 176
M 9,42,0
s_add_u32 s40, s40, 3072
LC_ 120
s_addc_u32 s41, s41, 0
s_add_u32 m0, s38, 3072
XS 177
M 9,43,1
XL 25,64
XS 178
M 10,54,0
WL 3
LO 117,19,117
LU 19,117
M 10,55,1
RA 236,1,24576
HS 24,19
M 11,66,0
LL 17,74
LL 121,75
M 11,67,1
LA 17
DW 0
M 6,80,2
LC_ 121
DW 1024
M 6,81,3
XL 26,128
v_add_u32 v1, s37, v0
M 7,8,2
WL 3
LO 120,16,120
LU 16,120
M 7,9,3
RA 216,1,0
HS 24,16
M 8,32,2
LL 18,76
LL 124,77
XS 179
M 8,33,3
LA 18
DW 2048
M 9,44,2
s_lshl_b32 s53, s52, 8
LC_ 124
v_and_b32 v22, 0xfffffcff, v1
XS 180
M 9,45,3
s_add_u32 s48, %[alo], s53
XS 181
XS 182
M 10,56,2
WL 3
LO 121,17,121
LU 17,121
M 10,57,3
s_addc_u32 s49, %[ahi], 0
RA 220,1,1024
HS 24,17
M 11,68,2
LL 19,78
LL 125,79
M 11,69,3
s_add_u32 s52, s52, %[d]
LA 19
RV 252,22,5120
XS 183
M 6,82,4
s_cmp_eq_u32 s52, %[wrap]
LC_ 125
XS 184
M 6,83,5
s_cselect_b32 s52, %[rst], s52
XL 27,192
XS 185
M 7,10,4
WL 4
LO 124,18,124
LU 18,124
M 7,11,5
RA 224,1,2048
HS 24,18
M 8,34,4
LL 16,80
LL 106,81
XS 186
M 8,35,5
LA 16
DQ 3072
M 9,46,4
s_add_u32 s50, s50, %[d]
LC_ 106
XS 187
M 9,47,5
XS 188
XS 189
M 10,58,4
WL 3
LO 125,19,125
LU 19,125
M 10,59,5
RA 228,1,3072
HS 24,19
M 11,70,4
LL 17,82
LL 107,83
M 11,71,5
LA 17
HX 24
s_cmp_eq_u32 s50, %[wrap]
s_cselect_b32 s50, %[rst], s50
XS 190
Z 0,72,0
LC_ 107
XL 28,256
M 0,73,1
s_add_u32 s38, s38, 32768
XS 191
XS 192
Z 1,0,0
WL 3
LO 106,16,106
LU 16,106
M 1,1,1
s_cmp_eq_u32 s38, s77
RA 232,1,4096
HZ 27,16
Z 2,12,0
s_cselect_b32 s38, s76, s38
LL 18,84
LL 110,85
M 2,13,1
s_barrier
LA 18
XS 193
Z 3,36,0
s_sub_u32 s39, 0, s39
LC_ 110
ds_read_b64 v[14:15], v5 offset:0
XS 194
M 3,37,1
XL 29,320
XS 195
Z 4,48,0
WL 4
LO 107,17,107
LU 17,107
M 4,49,1
s_add_u32 s42, s94, s60
RA 236,1,6144
HS 27,17
Z 5,60,0
LL 19,86
LL 111,87
XS 196
M 5,61,1
s_addc_u32 s43, s95, 0
LA 19
W1 24,0
M 0,74,2
LC_ 111
W1 26,2048
XS 197
M 0,75,3
XL 30,384
XS 198
M 1,2,2
WL 6
LO 110,18,110
LU 18,110
M 1,3,3
RA 216,1,7168
HZ 29,18
XS 199
M 2,14,2
LL 16,88
LL 114,89
M 2,15,3
LA 16
SU 104
XS 200
M 3,38,2
WL 7
LC_ 114
XL 31,448
M 3,39,3
s_add_u32 s42, s94, s61
v_add_f32 v14, v14, v15
XS 201
M 4,50,2
WL 4
LO 111,19,111
LU 19,111
v_add_f32_dpp v14, v14, v14 quad_perm:[1,0,3,2] row_mask:0xf bank_mask:0xf
M 4,51,3
s_addc_u32 s43, s95, 0
RA 220,1,8192
HS 29,19
XS 202
M 5,62,2
LL 17,90
LL 115,91
v_add_f32_dpp v14, v14, v14 quad_perm:[2,3,0,1] row_mask:0xf bank_mask:0xf
XS 203
M 5,63,3
LA 17
SU 108
M 0,76,4
s_mov_b32 m0, %[pm]
LC_ 115
DF
v_add_f32_dpp v14, v14, v14 row_half_mirror row_mask:0xf bank_mask:0xf
XS 204
M 0,77,5
v_cvt_f64_f32 v[14:15], v14
v_pk_add_f16 v27, v29, v27
M 1,4,4
WL 3
LO 114,16,114
LU 16,114
XS 205
M 1,5,5
RA 224,1,9216
HZ 25,16
M 2,16,4
LL 18,92
LL 118,93
s_mov_b64 exec, s[54:55]
SP
s_mov_b64 exec, -1
XS 206
M 2,17,5
LA 18
ds_read_b64 v[14:15], v5 offset:2048
RV 40,8,6144
RV 44,8,6208
RV 48,8,6272
RA 242,7,0
RA 250,7,256
HX 27
XS 207
M 3,40,4
LC_ 118
XL 32,512
M 3,41,5
s_add_u32 s42, s44, s62
v_subrev_u32 v5, s39, v5
XS 208
M 4,52,4
WL 9
LO 115,17,115
LU 17,115
M 4,53,5
s_addc_u32 s43, s45, 0
RA 228,1,10240
HS 25,17
XS 209
M 5,64,4
WV 17
LL 19,94
LL 119,95
M 5,65,5
WL 6
LA 19
ST 112
XS 210
LZ 40,24
XS 211
M 0,78,0
LC_ 119
XS 212
M 0,79,1
XS 213
XS 214
M 1,6,0
WL 3
LO 118,18,118
LU 18,118
M 1,7,1
RA 232,1,11264
HS 25,18
M 2,18,0
LL 16,96
LL 122,97
M 2,19,1
WV 16
LA 16
RV 252,22,5392
LS 44,25
RV 40,8,6336
v_add_f32 v14, v14, v15
s_nop 1
v_add_f32_dpp v14, v14, v14 quad_perm:[1,0,3,2] row_mask:0xf bank_mask:0xf
M 3,42,0
LC_ 122
XL 33,576
M 3,43,1
s_add_u32 s42, s44, s63
v_add_f32_dpp v14, v14, v14 quad_perm:[2,3,0,1] row_mask:0xf bank_mask:0xf
XS 215
M 4,54,0
WL 5
LO 119,19,119
LU 19,119
M 4,55,1
s_addc_u32 s43, s45, 0
RA 236,1,12288
HS 25,19
v_add_f32_dpp v14, v14, v14 row_half_mirror row_mask:0xf bank_mask:0xf
M 5,66,0
LL 17,98
LL 123,99
XS 216
M 5,67,1
LA 17
ST 116
v_cvt_f64_f32 v[14:15], v14
XS 217
M 0,80,2
LC_ 123
s_mov_b64 exec, s[54:55]
SP
s_mov_b64 exec, -1
M 0,81,3
WV 16
XL 34,640
XS 218
LS 48,26
RV 44,8,6400
XS 219
M 1,8,2
WL 4
LO 122,16,122
LU 16,122
M 1,9,3
RA 216,1,13312
HS 25,16
)"
R"(M 2,20,2
LL 18,100
LL 126,101
M 2,21,3
LA 18
XS 220
M 3,44,2
WV 15
LC_ 126
XS 221
M 3,45,3
s_add_u32 s42, s44, s64
XS 222
XS 223
LS 40,27
RV 48,8,6464
v_min_u32 v35, v35, v13
M 4,56,2
WL 5
LO 123,17,123
LU 17,123
M 4,57,3
s_addc_u32 s43, s45, 0
RA 220,1,14336
HS 25,17
M 5,68,2
LL 19,102
LL 127,103
M 5,69,3
LA 19
ST 120
M 0,82,4
LC_ 127
XL 35,704
M 0,83,5
M 1,10,4
WL 4
LO 126,18,126
LU 18,126
M 1,11,5
WV 15
RA 224,1,15360
HS 25,18
LS 44,28
RV 40,8,6528
M 2,22,4
LL 16,56
LL 104,57
M 2,23,5
LA 16
M 3,46,4
WV 14
LC_ 104
M 3,47,5
s_add_u32 s42, s44, s65
M 4,58,4
WL 4
LO 127,19,127
LU 19,127
M 4,59,5
s_addc_u32 s43, s45, 0
RA 228,1,16384
HS 25,19
LS 48,29
RV 44,8,6592
M 5,70,4
LL 17,58
LL 105,59
M 5,71,5
LA 17
ST 124
Z 6,72,0
LC_ 105
HX 25
M 6,73,1
W1 25,1024
Z 7,0,0
WL 5
LO 104,16,104
LU 16,104
M 7,1,1
RA 232,1,17408
HZ 26,16
Z 8,12,0
WV 14
LL 18,60
LL 108,61
M 8,13,1
s_mul_i32 s53, s50, 25600
LA 18
W1 27,3072
LS 40,30
RV 48,8,6656
Z 9,36,0
s_add_u32 s40, %[wlo], s53
LC_ 108
v_add_u32 v4, s39, v4
M 9,37,1
s_addc_u32 s41, %[whi], 0
Z 10,48,0
WL 6
LO 105,17,105
LU 17,105
M 10,49,1
s_mov_b32 m0, s38
RA 236,1,18432
HS 26,17
Z 11,60,0
LL 19,62
LL 109,63
M 11,61,1
WV 12
LA 19
DW 0
LS 44,31
RV 40,8,6720
M 6,74,2
LC_ 109
M 6,75,3
M 7,2,2
WL 4
LO 108,18,108
LU 18,108
M 7,3,3
s_add_u32 s37, s37, 32768
RA 216,1,19456
HZ 29,18
M 8,14,2
s_cmp_eq_u32 s37, s75
LL 16,64
LL 112,65
M 8,15,3
s_cselect_b32 s37, s74, s37
LA 16
DW 1024
M 9,38,2
LC_ 112
M 9,39,3
WV 10
LS 48,32
RV 44,8,6784
M 10,50,2
WL 4
LO 109,19,109
LU 19,109
M 10,51,3
RA 220,1,20480
HS 29,19
M 11,62,2
LL 17,66
LL 113,67
M 11,63,3
LA 17
DW 2048
M 6,76,4
LC_ 113
M 6,77,5
WV 9
LS 40,33
RV 48,8,6848
M 7,4,4
WL 5
LO 112,16,112
LU 16,112
M 7,5,5
s_mul_i32 s53, s51, 49152
RA 224,1,21504
HZ 30,16
M 8,16,4
s_bitcmp1_b64 s[90:91], s51
LL 18,68
LL 116,69
M 8,17,5
s_cselect_b64 s[44:45], s[84:85], s[82:83]
LA 18
M 9,40,4
s_add_u32 s44, s44, s53
LC_ 116
M 9,41,5
s_addc_u32 s45, s45, 0
M 10,52,4
WL 4
LO 113,17,113
LU 17,113
M 10,53,5
s_bitcmp1_b64 s[92:93], s51
RA 228,1,22528
HS 30,17
WV 6
LS 44,34
s_cselect_b64 s[94:95], s[88:89], s[86:87]
M 11,64,4
LL 19,70
LL 117,71
M 11,65,5
s_add_u32 s94, s94, s53
LA 19
v_pk_add_f16 v29, v30, v29
M 6,78,0
s_addc_u32 s95, s95, 0
LC_ 117
v_pk_add_f16 v26, v29, v26
M 6,79,1
s_add_u32 s51, s51, %[d]
HX 26
M 7,6,0
WL 3
LO 116,18,116
LU 18,116
M 7,7,1
s_cmp_eq_u32 s51, %[wrap]
RA 232,1,23552
HZ 24,18
WV 4
LS 48,35
s_cselect_b32 s51, %[rst], s51
M 8,18,0
s_mul_i32 s53, %[d], 16384
LL 16,72
LL 120,73
M 8,19,1
s_add_u32 s62, s53, %[sb1]
LA 16
M 9,42,0
s_add_u32 s40, s40, 3072
LC_ 120
s_addc_u32 s41, s41, 0
s_add_u32 m0, s38, 3072
M 9,43,1
DW 0
M 10,54,0
WL 3
LO 117,19,117
LU 19,117
M 10,55,1
RA 236,1,24576
HS 24,19
M 11,66,0
LL 17,74
LL 121,75
M 11,67,1
LA 17
DW 1024
v_mul_f32 v36, 0xbb2aaaab, v36
v_mul_f32 v37, 0xbb2aaaab, v37
v_mul_f32 v38, 0xbb2aaaab, v38
v_mul_f32 v39, 0xbb2aaaab, v39
M 6,80,2
LC_ 121
DW 2048
M 6,81,3
s_lshl_b32 s53, s52, 8
v_add_u32 v1, s37, v0
LD 40,244,24
s_add_u32 s48, %[alo], s53
M 7,8,2
WL 3
LO 120,16,120
LU 16,120
M 7,9,3
s_addc_u32 s49, %[ahi], 0
RA 216,1,0
HS 24,16
LD 44,240,24
RA 242,7,512
v_and_b32 v22, 0xfffffcff, v1
M 8,20,2
s_add_u32 s52, s52, %[d]
LL 18,76
LL 124,77
M 8,21,3
s_cmp_eq_u32 s52, %[wrap]
LA 18
M 9,44,2
s_cselect_b32 s52, %[rst], s52
LC_ 124
DQ 3072
M 9,45,3
LD 48,252,25
CF 224,40,41
CF 225,42,43
M 10,56,2
WL 4
LO 121,17,121
LU 17,121
M 10,57,3
s_add_u32 s50, s50, %[d]
RA 220,1,1024
HS 24,17
LD 40,248,25
RA 250,7,768
CF 226,44,45
CF 227,46,47
M 11,68,2
LL 19,78
LL 125,79
M 11,69,3
s_cmp_eq_u32 s50, %[wrap]
LA 19
RV 252,22,5120
M 6,82,4
s_cselect_b32 s50, %[rst], s50
LC_ 125
M 6,83,5
WL 7
LD 44,244,26
CF 228,48,49
M 7,10,4
WL 5
LO 124,18,124
LU 18,124
M 7,11,5
s_add_u32 s38, s38, 32768
RA 224,1,2048
HS 24,18
GZ 24
s_cmp_eq_u32 s38, s77
CF 229,50,51
M 8,22,4
s_cselect_b32 s38, s76, s38
LL 16,80
LL 106,81
M 8,23,5
s_barrier
LA 16
M 9,46,4
s_sub_u32 s39, 0, s39
LC_ 106
ds_read_b64 v[14:15], v5 offset:0
M 9,47,5
LD 48,240,26
RA 242,7,1024
CF 230,40,41
CF 231,42,43
M 10,58,4
WL 5
LO 125,19,125
LU 19,125
M 10,59,5
RA 228,1,3072
HS 24,19
LD 40,252,27
CF 232,44,45
M 11,70,4
LL 17,82
LL 107,83
M 11,71,5
LA 17
HX 24
Z 0,72,0
LC_ 107
W1 24,0
M 0,73,1
W1 26,2048
GS 25
CF 233,46,47
Z 1,0,0
WL 7
LO 106,16,106
LU 16,106
M 1,1,1
RA 232,1,4096
HZ 27,16
LD 44,248,27
RA 250,7,1280
CF 234,48,49
CF 235,50,51
Z 2,12,0
LL 18,84
LL 110,85
M 2,13,1
WL 7
LA 18
v_add_f32 v14, v14, v15
LD 48,244,28
CF 236,40,41
v_add_f32_dpp v14, v14, v14 quad_perm:[1,0,3,2] row_mask:0xf bank_mask:0xf
Z 3,36,0
LC_ 110
M 3,37,1
s_add_u32 s42, s94, s60
v_add_f32_dpp v14, v14, v14 quad_perm:[2,3,0,1] row_mask:0xf bank_mask:0xf
Z 4,48,0
WL 6
LO 107,17,107
LU 17,107
M 4,49,1
s_addc_u32 s43, s95, 0
RA 236,1,6144
HS 27,17
GS 26
SU 104
CF 237,42,43
v_add_f32_dpp v14, v14, v14 row_half_mirror row_mask:0xf bank_mask:0xf
Z 5,60,0
LL 19,86
LL 111,87
M 5,61,1
LA 19
v_cvt_f64_f32 v[14:15], v14
)"
R"(LD 40,240,28
RA 242,7,1536
CF 238,44,45
CF 239,46,47
M 0,74,2
LC_ 111
M 0,75,3
s_mov_b64 exec, s[54:55]
SP
s_mov_b64 exec, -1
M 1,2,2
WL 4
LO 110,18,110
LU 18,110
M 1,3,3
RA 216,1,7168
HZ 29,18
LD 44,252,29
ds_read_b64 v[14:15], v5 offset:2048
CF 240,48,49
M 2,14,2
LL 16,88
LL 114,89
M 2,15,3
LA 16
v_subrev_u32 v5, s39, v5
GS 27
CF 241,50,51
M 3,38,2
LC_ 114
M 3,39,3
s_add_u32 s42, s94, s61
M 4,50,2
WL 4
LO 111,19,111
LU 19,111
M 4,51,3
s_addc_u32 s43, s95, 0
RA 220,1,8192
HS 29,19
LD 48,248,29
RA 250,7,1792
CF 242,40,41
CF 243,42,43
M 5,62,2
LL 17,90
LL 115,91
M 5,63,3
LA 17
SU 108
LD 40,244,30
s_mov_b32 m0, %[pm]
CF 244,44,45
M 0,76,4
LC_ 115
DF
M 0,77,5
M 1,4,4
WL 4
LO 114,16,114
LU 16,114
M 1,5,5
RA 224,1,9216
HZ 30,16
GS 28
CF 245,46,47
v_add_f32 v14, v14, v15
M 2,16,4
LL 18,92
LL 118,93
M 2,17,5
LA 18
v_add_f32_dpp v14, v14, v14 quad_perm:[1,0,3,2] row_mask:0xf bank_mask:0xf
LD 44,240,30
RA 242,7,2048
CF 246,48,49
CF 247,50,51
v_add_f32_dpp v14, v14, v14 quad_perm:[2,3,0,1] row_mask:0xf bank_mask:0xf
M 3,40,4
WL 5
LC_ 118
v_add_f32_dpp v14, v14, v14 row_half_mirror row_mask:0xf bank_mask:0xf
M 3,41,5
s_add_u32 s42, s94, s62
LD 48,252,31
s_addc_u32 s43, s95, 0
CF 248,40,41
M 4,52,4
WL 4
LO 115,17,115
LU 17,115
M 4,53,5
RA 228,1,10240
HS 30,17
M 5,64,4
LL 19,94
LL 119,95
M 5,65,5
LA 19
SU 112
GS 29
CF 249,42,43
v_cvt_f64_f32 v[14:15], v14
M 0,78,0
LC_ 119
s_mov_b64 exec, s[54:55]
SP
s_mov_b64 exec, -1
M 0,79,1
v_pk_add_f16 v29, v30, v29
LD 40,248,31
RA 250,7,2304
CF 250,44,45
CF 251,46,47
v_pk_add_f16 v27, v29, v27
M 1,6,0
WL 4
LO 118,18,118
LU 18,118
M 1,7,1
RA 232,1,11264
HZ 25,18
M 2,18,0
LL 16,96
LL 122,97
M 2,19,1
LA 16
RV 252,22,5392
LD 44,244,32
CF 48,48,49
CF 50,50,51
HX 27
M 3,42,0
LC_ 122
M 3,43,1
s_add_u32 s42, s44, s63
GS 30
s_addc_u32 s43, s45, 0
AW 0,48
AW 1,50
M 4,54,0
WL 5
LO 119,19,119
LU 19,119
M 4,55,1
RA 236,1,12288
HS 25,19
M 5,66,0
LL 17,98
LL 123,99
M 5,67,1
LA 17
ST 116
LD 48,240,32
RA 242,7,2560
CF 40,40,41
CF 42,42,43
AW 2,40
AW 3,42
M 0,80,2
LC_ 123
M 0,81,3
WL 8
LD 40,252,33
CF 44,44,45
CF 46,46,47
M 1,8,2
WL 4
LO 122,16,122
LU 16,122
M 1,9,3
RA 216,1,13312
HS 25,16
M 2,20,2
LL 18,100
LL 126,101
M 2,21,3
LA 18
GS 31
AW 4,44
AW 5,46
M 3,44,2
LC_ 126
M 3,45,3
s_add_u32 s42, s44, s64
LD 44,248,33
RA 250,7,2816
CF 48,48,49
CF 50,50,51
AW 6,48
AW 7,50
M 4,56,2
WL 4
LO 123,17,123
LU 17,123
M 4,57,3
s_addc_u32 s43, s45, 0
RA 220,1,14336
HS 25,17
LD 48,244,34
ST 120
CF 40,40,41
CF 42,42,43
M 5,68,2
LL 19,102
LL 127,103
M 5,69,3
LA 19
M 0,82,4
LC_ 127
M 0,83,5
GS 32
AW 8,40
AW 9,42
M 1,10,4
WL 4
LO 126,18,126
LU 18,126
M 1,11,5
RA 224,1,15360
HS 25,18
LD 40,240,34
CF 44,44,45
CF 46,46,47
AW 10,44
AW 11,46
M 2,22,4
LL 16,56
LL 104,57
M 2,23,5
LA 16
M 3,46,4
WL 5
LC_ 104
M 3,47,5
s_add_u32 s42, s44, s65
LD 44,252,35
s_addc_u32 s43, s45, 0
CF 48,48,49
CF 50,50,51
M 4,58,4
WL 3
LO 127,19,127
LU 19,127
M 4,59,5
RA 228,1,16384
HS 25,19
GS 33
ST 124
AW 12,48
AW 13,50
M 5,70,4
LL 17,58
LL 105,59
M 5,71,5
LA 17
HX 25
Z 6,72,0
LC_ 105
W1 25,1024
M 6,73,1
W1 27,3072
LD 48,248,35
CF 40,40,41
CF 42,42,43
AW 14,40
AW 15,42
Z 7,0,0
WL 5
LO 104,16,104
LU 16,104
M 7,1,1
RA 232,1,17408
HZ 26,16
GS 34
CF 44,44,45
CF 46,46,47
AW 16,44
AW 17,46
v_add_u32 v4, s39, v4
Z 8,12,0
LL 18,60
LL 108,61
M 8,13,1
s_mul_i32 s53, s50, 25600
LA 18
Z 9,36,0
s_add_u32 s40, %[wlo], s53
LC_ 108
M 9,37,1
s_addc_u32 s41, %[whi], 0
CF 48,48,49
CF 50,50,51
AW 18,48
AW 19,50
Z 10,48,0
WL 5
LO 105,17,105
LU 17,105
M 10,49,1
s_mov_b32 m0, s38
RA 236,1,18432
HS 26,17
GS 35
Z 11,60,0
LL 19,62
LL 109,63
M 11,61,1
s_mul_i32 s53, %[d], 393216
LA 19
RV 36,8,6912
M 6,74,2
s_add_u32 s53, s53, s57
LC_ 109
M 6,75,3
s_add_u32 s46, %[xlo], s53
M 7,2,2
WL 4
LO 108,18,108
LU 18,108
M 7,3,3
s_addc_u32 s47, %[xhi], 0
RA 216,1,19456
HZ 29,18
v_cndmask_b32 v52, v52, v53, s[78:79]
v_cndmask_b32 v54, v54, v55, s[78:79]
v_cndmask_b32 v52, v52, v54, s[80:81]
v_mul_f32 v52, 0x3b2aaaab, v52
M 8,14,2
s_mov_b32 s53, 0xfe987610
LL 16,64
LL 112,65
M 8,15,3
s_cmp_eq_u32 %[d], 1
LA 16
C 52,52,52
v_lshlrev_b32 v52, 16, v52
WL 3
v_add_f32 v52, v36, v52
C 52,52,52
M 9,38,2
s_cselect_b32 s53, s53, 0xda43cb52
LC_ 112
M 9,39,3
s_bitcmp1_b64 s[92:93], 48
M 10,50,2
WL 3
LO 109,19,109
LU 19,109
M 10,51,3
s_cselect_b32 s58, 16, 0
RA 220,1,20480
HS 29,19
v_lshlrev_b32 v52, 16, v52
v_sqrt_f32 v52, v52
M 11,62,2
LL 17,66
LL 113,67
M 11,63,3
s_lshr_b32 s58, s53, s58
LA 17
v_lshrrev_b32 v3, 4, v0
C 52,52,52
v_lshlrev_b32 v52, 16, v52
v_cmp_ge_f32 vcc, v37, v52
v_rcp_f32 v52, v52
v_and_b32 v3, 12, v3
M 6,76,4
LC_ 113
v_bfe_u32 v3, s58, v3, 4
M 6,77,5
v_mad_u32_u24 v3, v3, s97, v21
DW 0
M 7,4,4
WL 3
LO 112,16,112
LU 16,112
M 7,5,5
s_add_u32 s37, s37, 32768
RA 224,1,21504
HZ 30,16
v_mul_f32 v52, v38, v52
v_cndmask_b32 v52, 0, v52, vcc
CF 52,52,52
M 8,16,4
s_cmp_eq_u32 s37, s75
LL 18,68
LL 116,69
M 8,17,5
s_cselect_b32 s37, s74, s37
LA 18
DW 1024
ds_bpermute_b32 v13, v28, v52
M 9,40,4
LC_ 116
XL 36,0
M 9,41,5
DW 2048
M 10,52,4
WL 4
)"
R"(LO 113,17,113
LU 17,113
M 10,53,5
s_mul_i32 s53, s51, 49152
RA 228,1,22528
HS 30,17
M 11,64,4
LL 19,70
LL 117,71
M 11,65,5
s_bitcmp1_b64 s[90:91], s51
LA 19
v_pk_add_f16 v29, v30, v29
M 6,78,0
s_cselect_b64 s[44:45], s[84:85], s[82:83]
LC_ 117
v_pk_add_f16 v26, v29, v26
M 6,79,1
s_add_u32 s44, s44, s53
HX 26
M 7,6,0
WL 3
LO 116,18,116
LU 18,116
M 7,7,1
s_addc_u32 s45, s45, 0
RA 232,1,23552
HZ 24,18
M 8,18,0
s_bitcmp1_b64 s[92:93], s51
LL 16,72
LL 120,73
M 8,19,1
s_cselect_b64 s[94:95], s[88:89], s[86:87]
LA 16
s_add_u32 s94, s94, s53
s_addc_u32 s95, s95, 0
s_add_u32 s51, s51, %[d]
s_cmp_eq_u32 s51, %[wrap]
s_cselect_b32 s51, %[rst], s51
XS 224
M 9,42,0
s_add_u32 s40, s40, 3072
LC_ 120
s_addc_u32 s41, s41, 0
s_add_u32 m0, s38, 3072
XS 225
M 9,43,1
XL 37,64
XS 226
M 10,54,0
WL 3
LO 117,19,117
LU 19,117
M 10,55,1
RA 236,1,24576
HS 24,19
M 11,66,0
LL 17,74
LL 121,75
M 11,67,1
LA 17
DW 0
M 6,80,2
LC_ 121
DW 1024
M 6,81,3
XL 38,128
v_add_u32 v1, s37, v0
M 7,8,2
WL 3
LO 120,16,120
LU 16,120
XS 227
M 7,9,3
RA 216,1,0
HS 24,16
XS 228
M 8,20,2
LL 18,76
LL 124,77
XS 229
M 8,21,3
LA 18
DW 2048
XS 230
M 9,44,2
s_lshl_b32 s53, s52, 8
LC_ 124
v_and_b32 v22, 0xfffffcff, v1
XS 231
M 9,45,3
s_add_u32 s48, %[alo], s53
XS 232
XS 233
M 10,56,2
WL 3
LO 121,17,121
LU 17,121
M 10,57,3
s_addc_u32 s49, %[ahi], 0
RA 220,1,1024
HS 24,17
XS 234
M 11,68,2
LL 19,78
LL 125,79
XS 235
M 11,69,3
s_add_u32 s52, s52, %[d]
LA 19
RV 252,22,5120
XS 236
M 6,82,4
s_cmp_eq_u32 s52, %[wrap]
LC_ 125
XS 237
M 6,83,5
s_cselect_b32 s52, %[rst], s52
XL 39,192
XS 238
M 7,10,4
WL 4
LO 124,18,124
LU 18,124
XS 239
M 7,11,5
RA 224,1,2048
HS 24,18
XS 240
M 8,22,4
LL 16,80
LL 106,81
XS 241
M 8,23,5
LA 16
DQ 3072
XS 242
M 9,46,4
s_add_u32 s50, s50, %[d]
LC_ 106
XS 243
M 9,47,5
XS 244
XS 245
M 10,58,4
WL 3
LO 125,19,125
LU 19,125
M 10,59,5
RA 228,1,3072
HS 24,19
M 11,70,4
LL 17,82
LL 107,83
M 11,71,5
LA 17
HX 24
s_cmp_eq_u32 s50, %[wrap]
s_cselect_b32 s50, %[rst], s50
XS 246
XS 247
XS 248
Z 0,72,0
LC_ 107
XL 40,256
XS 249
M 0,73,1
s_add_u32 s38, s38, 32768
XS 250
XS 251
Z 1,0,0
WL 3
LO 106,16,106
LU 16,106
M 1,1,1
s_cmp_eq_u32 s38, s77
RA 232,1,4096
HZ 27,16
AR 52,0
Z 2,12,0
s_cselect_b32 s38, s76, s38
LL 18,84
LL 110,85
AR 53,1
M 2,13,1
s_barrier
LA 18
AR 54,2
Z 3,24,0
s_sub_u32 s39, 0, s39
LC_ 110
ds_read_b64 v[14:15], v5 offset:0
AR 55,3
M 3,25,1
XL 41,320
XS 52
Z 4,48,0
WL 4
LO 107,17,107
LU 17,107
XS 53
M 4,49,1
s_add_u32 s42, s94, s60
RA 236,1,6144
HS 27,17
XS 54
Z 5,60,0
LL 19,86
LL 111,87
XS 55
M 5,61,1
s_addc_u32 s43, s95, 0
LA 19
W1 24,0
AW 0,52
M 0,74,2
LC_ 111
W1 26,2048
AW 1,53
M 0,75,3
XL 42,384
AW 2,54
M 1,2,2
WL 6
LO 110,18,110
LU 18,110
AW 3,55
M 1,3,3
RA 216,1,7168
HZ 29,18
AR 52,4
M 2,14,2
LL 16,88
LL 114,89
AR 53,5
M 2,15,3
LA 16
SU 104
AR 54,6
M 3,26,2
WL 7
LC_ 114
XL 43,448
AR 55,7
M 3,27,3
s_add_u32 s42, s94, s61
v_add_f32 v14, v14, v15
XS 52
M 4,50,2
WL 4
LO 111,19,111
LU 19,111
v_add_f32_dpp v14, v14, v14 quad_perm:[1,0,3,2] row_mask:0xf bank_mask:0xf
XS 53
M 4,51,3
s_addc_u32 s43, s95, 0
RA 220,1,8192
HS 29,19
XS 54
M 5,62,2
LL 17,90
LL 115,91
v_add_f32_dpp v14, v14, v14 quad_perm:[2,3,0,1] row_mask:0xf bank_mask:0xf
XS 55
M 5,63,3
LA 17
SU 108
AW 4,52
M 0,76,4
s_mov_b32 m0, %[pm]
LC_ 115
DF
v_add_f32_dpp v14, v14, v14 row_half_mirror row_mask:0xf bank_mask:0xf
AW 5,53
M 0,77,5
v_cvt_f64_f32 v[14:15], v14
AW 6,54
M 1,4,4
WL 3
LO 114,16,114
LU 16,114
AW 7,55
M 1,5,5
RA 224,1,9216
HZ 30,16
AR 52,8
M 2,16,4
LL 18,92
LL 118,93
s_mov_b64 exec, s[54:55]
SP
s_mov_b64 exec, -1
AR 53,9
M 2,17,5
LA 18
ds_read_b64 v[14:15], v5 offset:2048
AR 54,10
RV 40,8,6144
RV 44,8,6208
RV 48,8,6272
RA 242,7,0
RA 250,7,256
AR 55,11
M 3,28,4
LC_ 118
XL 44,512
M 3,29,5
s_add_u32 s42, s94, s62
v_subrev_u32 v5, s39, v5
XS 52
M 4,52,4
WL 9
LO 115,17,115
LU 17,115
XS 53
M 4,53,5
s_addc_u32 s43, s95, 0
RA 228,1,10240
HS 30,17
XS 54
M 5,64,4
WV 17
LL 19,94
LL 119,95
XS 55
M 5,65,5
WL 6
LA 19
SU 112
AW 8,52
LZ 40,36
v_pk_add_f16 v29, v30, v29
AW 9,53
M 0,78,0
LC_ 119
v_pk_add_f16 v27, v29, v27
M 0,79,1
HX 27
AW 10,54
M 1,6,0
WL 3
LO 118,18,118
LU 18,118
AW 11,55
M 1,7,1
RA 232,1,11264
HZ 25,18
AR 52,12
M 2,18,0
LL 16,96
LL 122,97
AR 53,13
M 2,19,1
WV 16
LA 16
RV 252,22,5392
AR 54,14
LS 44,37
RV 40,8,6336
v_add_f32 v14, v14, v15
s_nop 1
v_add_f32_dpp v14, v14, v14 quad_perm:[1,0,3,2] row_mask:0xf bank_mask:0xf
AR 55,15
M 3,30,0
LC_ 122
XL 45,576
M 3,31,1
s_add_u32 s42, s44, s63
v_add_f32_dpp v14, v14, v14 quad_perm:[2,3,0,1] row_mask:0xf bank_mask:0xf
XS 52
M 4,54,0
WL 5
LO 119,19,119
LU 19,119
XS 53
M 4,55,1
s_addc_u32 s43, s45, 0
RA 236,1,12288
HS 25,19
v_add_f32_dpp v14, v14, v14 row_half_mirror row_mask:0xf bank_mask:0xf
XS 54
M 5,66,0
LL 17,98
LL 123,99
XS 55
M 5,67,1
LA 17
ST 116
v_cvt_f64_f32 v[14:15], v14
AW 12,52
M 0,80,2
LC_ 123
s_mov_b64 exec, s[54:55]
SP
s_mov_b64 exec, -1
AW 13,53
M 0,81,3
WV 16
XL 46,640
)"
R"(AW 14,54
LS 48,38
RV 44,8,6400
AW 15,55
M 1,8,2
WL 4
LO 122,16,122
LU 16,122
M 1,9,3
RA 216,1,13312
HS 25,16
AR 52,16
M 2,20,2
LL 18,100
LL 126,101
AR 53,17
M 2,21,3
LA 18
AR 54,18
M 3,32,2
WV 15
LC_ 126
AR 55,19
M 3,33,3
s_add_u32 s42, s44, s64
XS 52
XS 53
LS 40,39
RV 48,8,6464
XS 54
M 4,56,2
WL 5
LO 123,17,123
LU 17,123
M 4,57,3
s_addc_u32 s43, s45, 0
RA 220,1,14336
HS 25,17
M 5,68,2
LL 19,102
LL 127,103
XS 55
M 5,69,3
LA 19
ST 120
AW 16,52
M 0,82,4
LC_ 127
XL 47,704
AW 17,53
M 0,83,5
AW 18,54
AW 19,55
M 1,10,4
WL 4
LO 126,18,126
LU 18,126
M 1,11,5
WV 15
RA 224,1,15360
HS 25,18
v_min_u32 v35, v35, v13
LS 44,40
RV 40,8,6528
M 2,22,4
LL 16,56
LL 104,57
M 2,23,5
LA 16
M 3,34,4
WV 14
LC_ 104
M 3,35,5
s_add_u32 s42, s44, s65
M 4,58,4
WL 4
LO 127,19,127
LU 19,127
M 4,59,5
s_addc_u32 s43, s45, 0
RA 228,1,16384
HS 25,19
LS 48,41
RV 44,8,6592
M 5,70,4
LL 17,58
LL 105,59
M 5,71,5
LA 17
ST 124
Z 6,72,0
LC_ 105
HX 25
M 6,73,1
W1 25,1024
Z 7,0,0
WL 5
LO 104,16,104
LU 16,104
M 7,1,1
RA 232,1,17408
HZ 26,16
Z 8,12,0
WV 14
LL 18,60
LL 108,61
M 8,13,1
s_mul_i32 s53, s50, 25600
LA 18
W1 27,3072
LS 40,42
RV 48,8,6656
Z 9,24,0
s_add_u32 s40, %[wlo], s53
LC_ 108
v_add_u32 v4, s39, v4
M 9,25,1
s_addc_u32 s41, %[whi], 0
Z 10,48,0
WL 6
LO 105,17,105
LU 17,105
M 10,49,1
s_mov_b32 m0, s38
RA 236,1,18432
HS 26,17
Z 11,60,0
LL 19,62
LL 109,63
M 11,61,1
WV 12
LA 19
DW 0
LS 44,43
RV 40,8,6720
M 6,74,2
LC_ 109
M 6,75,3
M 7,2,2
WL 4
LO 108,18,108
LU 18,108
M 7,3,3
s_add_u32 s37, s37, 32768
RA 216,1,19456
HZ 29,18
M 8,14,2
s_cmp_eq_u32 s37, s75
LL 16,64
LL 112,65
M 8,15,3
s_cselect_b32 s37, s74, s37
LA 16
DW 1024
M 9,26,2
LC_ 112
M 9,27,3
WV 10
LS 48,44
RV 44,8,6784
M 10,50,2
WL 4
LO 109,19,109
LU 19,109
M 10,51,3
RA 220,1,20480
HS 29,19
M 11,62,2
LL 17,66
LL 113,67
M 11,63,3
LA 17
DW 2048
M 6,76,4
LC_ 113
M 6,77,5
WV 9
LS 40,45
RV 48,8,6848
M 7,4,4
WL 5
LO 112,16,112
LU 16,112
M 7,5,5
s_mul_i32 s53, s51, 49152
RA 224,1,21504
HZ 30,16
M 8,16,4
s_bitcmp1_b64 s[90:91], s51
LL 18,68
LL 116,69
M 8,17,5
s_cselect_b64 s[44:45], s[84:85], s[82:83]
LA 18
M 9,28,4
s_add_u32 s44, s44, s53
LC_ 116
M 9,29,5
s_addc_u32 s45, s45, 0
M 10,52,4
WL 4
LO 113,17,113
LU 17,113
M 10,53,5
s_bitcmp1_b64 s[92:93], s51
RA 228,1,22528
HS 30,17
WV 6
LS 44,46
s_cselect_b64 s[94:95], s[88:89], s[86:87]
M 11,64,4
LL 19,70
LL 117,71
M 11,65,5
s_add_u32 s94, s94, s53
LA 19
M 6,78,0
s_addc_u32 s95, s95, 0
LC_ 117
M 6,79,1
s_add_u32 s51, s51, %[d]
M 7,6,0
WL 3
LO 116,18,116
LU 18,116
M 7,7,1
s_cmp_eq_u32 s51, %[wrap]
RA 232,1,23552
HZ 31,18
WV 4
LS 48,47
s_cselect_b32 s51, %[rst], s51
M 8,18,0
s_mul_i32 s53, %[d], 17408
LL 16,72
LL 120,73
M 8,19,1
s_add_u32 s63, s53, %[sb1]
LA 16
M 9,30,0
s_add_u32 s40, s40, 3072
LC_ 120
s_addc_u32 s41, s41, 0
s_add_u32 m0, s38, 3072
M 9,31,1
DW 0
M 10,54,0
WL 3
LO 117,19,117
LU 19,117
M 10,55,1
RA 236,1,24576
HS 31,19
M 11,66,0
LL 17,74
LL 121,75
M 11,67,1
LA 17
DW 1024
v_mul_f32 v36, 0xbb2aaaab, v36
v_mul_f32 v37, 0xbb2aaaab, v37
v_mul_f32 v38, 0xbb2aaaab, v38
v_mul_f32 v39, 0xbb2aaaab, v39
v_pk_add_f16 v31, v30, v31
M 6,80,2
LC_ 121
DW 2048
M 6,81,3
s_lshl_b32 s53, s52, 8
v_add_u32 v1, s37, v0
v_pk_add_f16 v29, v31, v29
LD 40,244,36
s_add_u32 s48, %[alo], s53
v_pk_add_f16 v26, v29, v26
M 7,8,2
WL 3
LO 120,16,120
LU 16,120
M 7,9,3
s_addc_u32 s49, %[ahi], 0
RA 216,1,0
HZ 24,16
LD 44,240,36
RA 242,7,512
v_and_b32 v22, 0xfffffcff, v1
M 8,20,2
s_add_u32 s52, s52, %[d]
LL 18,76
LL 124,77
M 8,21,3
s_cmp_eq_u32 s52, %[wrap]
LA 18
HX 26
M 9,32,2
s_cselect_b32 s52, %[rst], s52
LC_ 124
DQ 3072
M 9,33,3
LD 48,252,37
CF 40,40,41
CF 42,42,43
AW 20,40
AW 21,42
M 10,56,2
WL 4
LO 121,17,121
LU 17,121
M 10,57,3
s_add_u32 s50, s50, %[d]
RA 220,1,1024
HS 24,17
LD 40,248,37
RA 250,7,768
CF 44,44,45
CF 46,46,47
AW 22,44
AW 23,46
M 11,68,2
LL 19,78
LL 125,79
M 11,69,3
s_cmp_eq_u32 s50, %[wrap]
LA 19
RV 252,22,5120
M 6,82,4
s_cselect_b32 s50, %[rst], s50
LC_ 125
M 6,83,5
WL 7
LD 44,244,38
CF 48,48,49
CF 50,50,51
M 7,10,4
WL 5
LO 124,18,124
LU 18,124
M 7,11,5
s_add_u32 s38, s38, 32768
RA 224,1,2048
HS 24,18
GZ 36
s_cmp_eq_u32 s38, s77
AW 24,48
AW 25,50
M 8,22,4
s_cselect_b32 s38, s76, s38
LL 16,80
LL 106,81
M 8,23,5
s_barrier
LA 16
M 9,34,4
s_sub_u32 s39, 0, s39
LC_ 106
ds_read_b64 v[14:15], v5 offset:0
M 9,35,5
LD 48,240,38
RA 242,7,1024
CF 40,40,41
CF 42,42,43
AW 26,40
AW 27,42
M 10,58,4
WL 5
LO 125,19,125
LU 19,125
M 10,59,5
RA 228,1,3072
HS 24,19
LD 40,252,39
CF 44,44,45
CF 46,46,47
M 11,70,4
LL 17,82
LL 107,83
M 11,71,5
LA 17
HX 24
Z 0,72,0
LC_ 107
W1 24,0
M 0,73,1
W1 26,2048
GS 37
AW 28,44
AW 29,46
Z 1,0,0
WL 7
LO 106,16,106
LU 16,106
M 1,1,1
RA 232,1,4096
HZ 27,16
LD 44,248,39
RA 250,7,1280
CF 48,48,49
CF 50,50,51
AW 30,48
AW 31,50
Z 2,12,0
)"
R"(LL 18,84
LL 110,85
M 2,13,1
WL 7
LA 18
v_add_f32 v14, v14, v15
LD 48,244,40
CF 40,40,41
CF 42,42,43
v_add_f32_dpp v14, v14, v14 quad_perm:[1,0,3,2] row_mask:0xf bank_mask:0xf
Z 3,24,0
LC_ 110
M 3,25,1
s_add_u32 s42, s94, s60
v_add_f32_dpp v14, v14, v14 quad_perm:[2,3,0,1] row_mask:0xf bank_mask:0xf
Z 4,48,0
WL 6
LO 107,17,107
LU 17,107
M 4,49,1
s_addc_u32 s43, s95, 0
RA 236,1,6144
HS 27,17
GS 38
SU 104
AW 32,40
AW 33,42
v_add_f32_dpp v14, v14, v14 row_half_mirror row_mask:0xf bank_mask:0xf
Z 5,60,0
LL 19,86
LL 111,87
M 5,61,1
LA 19
v_cvt_f64_f32 v[14:15], v14
LD 40,240,40
RA 242,7,1536
CF 44,44,45
CF 46,46,47
AW 34,44
AW 35,46
M 0,74,2
LC_ 111
M 0,75,3
s_mov_b64 exec, s[54:55]
SP
s_mov_b64 exec, -1
M 1,2,2
WL 4
LO 110,18,110
LU 18,110
M 1,3,3
RA 216,1,7168
HZ 29,18
LD 44,252,41
ds_read_b64 v[14:15], v5 offset:2048
CF 48,48,49
CF 50,50,51
M 2,14,2
LL 16,88
LL 114,89
M 2,15,3
LA 16
v_subrev_u32 v5, s39, v5
GS 39
AW 36,48
AW 37,50
M 3,26,2
LC_ 114
M 3,27,3
s_add_u32 s42, s94, s61
M 4,50,2
WL 4
LO 111,19,111
LU 19,111
M 4,51,3
s_addc_u32 s43, s95, 0
RA 220,1,8192
HS 29,19
LD 48,248,41
RA 250,7,1792
CF 40,40,41
CF 42,42,43
AW 38,40
AW 39,42
M 5,62,2
LL 17,90
LL 115,91
M 5,63,3
LA 17
SU 108
LD 40,244,42
s_mov_b32 m0, %[pm]
CF 44,44,45
CF 46,46,47
M 0,76,4
LC_ 115
DF
M 0,77,5
M 1,4,4
WL 4
LO 114,16,114
LU 16,114
M 1,5,5
RA 224,1,9216
HZ 30,16
GS 40
AW 40,44
AW 41,46
v_add_f32 v14, v14, v15
M 2,16,4
LL 18,92
LL 118,93
M 2,17,5
LA 18
v_add_f32_dpp v14, v14, v14 quad_perm:[1,0,3,2] row_mask:0xf bank_mask:0xf
LD 44,240,42
RA 242,7,2048
CF 48,48,49
CF 50,50,51
AW 42,48
AW 43,50
v_add_f32_dpp v14, v14, v14 quad_perm:[2,3,0,1] row_mask:0xf bank_mask:0xf
M 3,28,4
WL 5
LC_ 118
v_add_f32_dpp v14, v14, v14 row_half_mirror row_mask:0xf bank_mask:0xf
M 3,29,5
s_add_u32 s42, s94, s62
LD 48,252,43
s_addc_u32 s43, s95, 0
CF 40,40,41
CF 42,42,43
M 4,52,4
WL 4
LO 115,17,115
LU 17,115
M 4,53,5
RA 228,1,10240
HS 30,17
M 5,64,4
LL 19,94
LL 119,95
M 5,65,5
LA 19
SU 112
GS 41
AW 44,40
AW 45,42
v_cvt_f64_f32 v[14:15], v14
M 0,78,0
LC_ 119
s_mov_b64 exec, s[54:55]
SP
s_mov_b64 exec, -1
M 0,79,1
LD 40,248,43
RA 250,7,2304
CF 44,44,45
CF 46,46,47
AW 46,44
AW 47,46
M 1,6,0
WL 4
LO 118,18,118
LU 18,118
M 1,7,1
RA 232,1,11264
HZ 31,18
M 2,18,0
LL 16,96
LL 122,97
M 2,19,1
LA 16
RV 252,22,5392
LD 44,244,44
CF 48,48,49
CF 50,50,51
M 3,30,0
LC_ 122
M 3,31,1
s_add_u32 s42, s94, s63
GS 42
s_addc_u32 s43, s95, 0
AW 48,48
AW 49,50
M 4,54,0
WL 5
LO 119,19,119
LU 19,119
M 4,55,1
RA 236,1,12288
HS 31,19
M 5,66,0
LL 17,98
LL 123,99
M 5,67,1
LA 17
SU 116
LD 48,240,44
RA 242,7,2560
CF 40,40,41
CF 42,42,43
AW 50,40
AW 51,42
v_pk_add_f16 v31, v30, v31
M 0,80,2
LC_ 123
v_pk_add_f16 v29, v31, v29
M 0,81,3
WL 8
v_pk_add_f16 v27, v29, v27
LD 40,252,45
CF 44,44,45
CF 46,46,47
HX 27
M 1,8,2
WL 4
LO 122,16,122
LU 16,122
M 1,9,3
RA 216,1,13312
HZ 25,16
M 2,20,2
LL 18,100
LL 126,101
M 2,21,3
LA 18
GS 43
AW 52,44
AW 53,46
M 3,32,2
LC_ 126
M 3,33,3
s_add_u32 s42, s44, s64
LD 44,248,45
RA 250,7,2816
CF 48,48,49
CF 50,50,51
AW 54,48
AW 55,50
M 4,56,2
WL 4
LO 123,17,123
LU 17,123
M 4,57,3
s_addc_u32 s43, s45, 0
RA 220,1,14336
HS 25,17
LD 48,244,46
ST 120
CF 40,40,41
CF 42,42,43
M 5,68,2
LL 19,102
LL 127,103
M 5,69,3
LA 19
M 0,82,4
LC_ 127
M 0,83,5
GS 44
AW 56,40
AW 57,42
M 1,10,4
WL 4
LO 126,18,126
LU 18,126
M 1,11,5
RA 224,1,15360
HS 25,18
LD 40,240,46
CF 44,44,45
CF 46,46,47
AW 58,44
AW 59,46
M 2,22,4
LL 16,56
LL 104,57
M 2,23,5
LA 16
M 3,34,4
WL 5
LC_ 104
M 3,35,5
s_add_u32 s42, s44, s65
LD 44,252,47
s_addc_u32 s43, s45, 0
CF 48,48,49
CF 50,50,51
M 4,58,4
WL 3
LO 127,19,127
LU 19,127
M 4,59,5
RA 228,1,16384
HS 25,19
GS 45
ST 124
AW 60,48
AW 61,50
M 5,70,4
LL 17,58
LL 105,59
M 5,71,5
LA 17
HX 25
Z 6,72,0
LC_ 105
W1 25,1024
M 6,73,1
W1 27,3072
LD 48,248,47
CF 40,40,41
CF 42,42,43
AW 62,40
AW 63,42
Z 7,0,0
WL 5
LO 104,16,104
LU 16,104
M 7,1,1
RA 232,1,17408
HZ 26,16
GS 46
CF 44,44,45
CF 46,46,47
AW 64,44
AW 65,46
v_add_u32 v4, s39, v4
Z 8,12,0
LL 18,60
LL 108,61
M 8,13,1
s_mul_i32 s53, s50, 25600
LA 18
Z 9,24,0
s_add_u32 s40, %[wlo], s53
LC_ 108
M 9,25,1
s_addc_u32 s41, %[whi], 0
CF 48,48,49
CF 50,50,51
AW 66,48
AW 67,50
Z 10,48,0
WL 5
LO 105,17,105
LU 17,105
M 10,49,1
s_mov_b32 m0, s38
RA 236,1,18432
HS 26,17
GS 47
Z 11,60,0
LL 19,62
LL 109,63
M 11,61,1
s_mul_i32 s53, %[d], 393216
LA 19
RV 36,8,6912
M 6,74,2
s_add_u32 s53, s53, s57
LC_ 109
M 6,75,3
s_add_u32 s46, %[xlo], s53
M 7,2,2
WL 4
LO 108,18,108
LU 18,108
M 7,3,3
s_addc_u32 s47, %[xhi], 0
RA 216,1,19456
HZ 29,18
v_cndmask_b32 v52, v52, v53, s[78:79]
v_cndmask_b32 v54, v54, v55, s[78:79]
v_cndmask_b32 v52, v52, v54, s[80:81]
v_mul_f32 v52, 0x3b2aaaab, v52
M 8,14,2
s_mov_b32 s53, 0xda43cb52
LL 16,64
LL 112,65
M 8,15,3
s_cmp_eq_u32 %[d], 1
LA 16
C 52,52,52
v_lshlrev_b32 v52, 16, v52
WL 3
v_add_f32 v52, v36, v52
C 52,52,52
M 9,26,2
s_cselect_b32 s53, s53, 0xfe987610
LC_ 112
M 9,27,3
s_bitcmp1_b64 s[92:93], 48
M 10,50,2
WL 3
LO 109,19,109
LU 19,109
M 10,51,3
s_cselect_b32 s58, 16, 0
)"
R"(RA 220,1,20480
HS 29,19
v_lshlrev_b32 v52, 16, v52
v_sqrt_f32 v52, v52
M 11,62,2
LL 17,66
LL 113,67
M 11,63,3
s_lshr_b32 s58, s53, s58
LA 17
v_lshrrev_b32 v3, 4, v0
C 52,52,52
v_lshlrev_b32 v52, 16, v52
v_cmp_ge_f32 vcc, v37, v52
v_rcp_f32 v52, v52
v_and_b32 v3, 12, v3
M 6,76,4
LC_ 113
v_bfe_u32 v3, s58, v3, 4
M 6,77,5
v_mad_u32_u24 v3, v3, s97, v21
DW 0
M 7,4,4
WL 3
LO 112,16,112
LU 16,112
M 7,5,5
s_add_u32 s37, s37, 32768
RA 224,1,21504
HZ 30,16
v_mul_f32 v52, v38, v52
v_cndmask_b32 v52, 0, v52, vcc
CF 52,52,52
M 8,16,4
s_cmp_eq_u32 s37, s75
LL 18,68
LL 116,69
M 8,17,5
s_cselect_b32 s37, s74, s37
LA 18
DW 1024
ds_bpermute_b32 v13, v28, v52
M 9,28,4
LC_ 116
XL 48,0
M 9,29,5
DW 2048
M 10,52,4
WL 4
LO 113,17,113
LU 17,113
M 10,53,5
s_mul_i32 s53, s51, 49152
RA 228,1,22528
HS 30,17
M 11,64,4
LL 19,70
LL 117,71
M 11,65,5
s_bitcmp1_b64 s[90:91], s51
LA 19
M 6,78,0
s_cselect_b64 s[44:45], s[84:85], s[82:83]
LC_ 117
M 6,79,1
s_add_u32 s44, s44, s53
M 7,6,0
WL 3
LO 116,18,116
LU 18,116
M 7,7,1
s_addc_u32 s45, s45, 0
RA 232,1,23552
HZ 31,18
M 8,18,0
s_bitcmp1_b64 s[92:93], s51
LL 16,72
LL 120,73
M 8,19,1
s_cselect_b64 s[94:95], s[88:89], s[86:87]
LA 16
s_add_u32 s94, s94, s53
s_addc_u32 s95, s95, 0
s_add_u32 s51, s51, %[d]
s_cmp_eq_u32 s51, %[wrap]
s_cselect_b32 s51, %[rst], s51
AR 52,20
M 9,30,0
s_add_u32 s40, s40, 3072
LC_ 120
s_addc_u32 s41, s41, 0
s_add_u32 m0, s38, 3072
AR 53,21
M 9,31,1
XL 49,64
AR 54,22
M 10,54,0
WL 3
LO 117,19,117
LU 19,117
M 10,55,1
RA 236,1,24576
HS 31,19
M 11,66,0
LL 17,74
LL 121,75
M 11,67,1
LA 17
DW 0
M 6,80,2
LC_ 121
DW 1024
AR 55,23
M 6,81,3
XL 50,128
v_add_u32 v1, s37, v0
XS 52
M 7,8,2
WL 3
LO 120,16,120
LU 16,120
XS 53
XS 54
M 7,9,3
RA 216,1,0
HZ 24,16
XS 55
M 8,20,2
LL 18,76
LL 124,77
AW 20,52
AW 21,53
M 8,21,3
LA 18
DW 2048
AW 22,54
AW 23,55
M 9,32,2
s_lshl_b32 s53, s52, 8
LC_ 124
v_and_b32 v22, 0xfffffcff, v1
AR 52,24
M 9,33,3
s_add_u32 s48, %[alo], s53
v_pk_add_f16 v31, v30, v31
AR 53,25
AR 54,26
M 10,56,2
WL 3
LO 121,17,121
LU 17,121
AR 55,27
M 10,57,3
s_addc_u32 s49, %[ahi], 0
RA 220,1,1024
HS 24,17
XS 52
XS 53
M 11,68,2
LL 19,78
LL 125,79
XS 54
XS 55
M 11,69,3
s_add_u32 s52, s52, %[d]
LA 19
RV 252,22,5120
AW 24,52
M 6,82,4
s_cmp_eq_u32 s52, %[wrap]
LC_ 125
v_pk_add_f16 v29, v31, v29
AW 25,53
AW 26,54
M 6,83,5
s_cselect_b32 s52, %[rst], s52
XL 51,192
v_pk_add_f16 v26, v29, v26
AW 27,55
M 7,10,4
WL 4
LO 124,18,124
LU 18,124
AR 52,28
AR 53,29
M 7,11,5
RA 224,1,2048
HS 24,18
AR 54,30
AR 55,31
M 8,22,4
LL 16,80
LL 106,81
HX 26
XS 52
M 8,23,5
LA 16
DQ 3072
XS 53
XS 54
M 9,34,4
s_add_u32 s50, s50, %[d]
LC_ 106
XS 55
M 9,35,5
AW 28,52
AW 29,53
M 10,58,4
WL 3
LO 125,19,125
LU 19,125
AW 30,54
AW 31,55
M 10,59,5
RA 228,1,3072
HS 24,19
M 11,70,4
LL 17,82
LL 107,83
M 11,71,5
LA 17
HX 24
s_cmp_eq_u32 s50, %[wrap]
s_cselect_b32 s50, %[rst], s50
AR 52,32
AR 53,33
AR 54,34
AR 55,35
Z 0,72,0
LC_ 107
XL 52,256
XS 52
XS 53
M 0,73,1
s_add_u32 s38, s38, 32768
XS 54
XS 55
Z 1,0,0
WL 3
LO 106,16,106
LU 16,106
AW 32,52
M 1,1,1
s_cmp_eq_u32 s38, s77
RA 232,1,4096
HZ 27,16
AW 33,53
AW 34,54
Z 2,12,0
s_cselect_b32 s38, s76, s38
LL 18,84
LL 110,85
AW 35,55
M 2,13,1
s_barrier
LA 18
AR 52,36
AR 53,37
Z 3,24,0
s_sub_u32 s39, 0, s39
LC_ 110
ds_read_b64 v[14:15], v5 offset:0
AR 54,38
M 3,25,1
XL 53,320
AR 55,39
XS 52
Z 4,36,0
WL 4
LO 107,17,107
LU 17,107
XS 53
XS 54
M 4,37,1
s_add_u32 s42, s94, s60
RA 236,1,6144
HS 27,17
XS 55
Z 5,60,0
LL 19,86
LL 111,87
AW 36,52
AW 37,53
M 5,61,1
s_addc_u32 s43, s95, 0
LA 19
W1 24,0
AW 38,54
M 0,74,2
LC_ 111
W1 26,2048
AW 39,55
AR 52,40
M 0,75,3
XL 54,384
AR 53,41
AR 54,42
M 1,2,2
WL 6
LO 110,18,110
LU 18,110
AR 55,43
M 1,3,3
RA 216,1,7168
HZ 29,18
XS 52
XS 53
M 2,14,2
LL 16,88
LL 114,89
XS 54
M 2,15,3
LA 16
SU 104
XS 55
AW 40,52
M 3,26,2
WL 7
LC_ 114
XL 55,448
AW 41,53
AW 42,54
M 3,27,3
s_add_u32 s42, s94, s61
v_add_f32 v14, v14, v15
AW 43,55
M 4,38,2
WL 4
LO 111,19,111
LU 19,111
v_add_f32_dpp v14, v14, v14 quad_perm:[1,0,3,2] row_mask:0xf bank_mask:0xf
AR 52,44
AR 53,45
M 4,39,3
s_addc_u32 s43, s95, 0
RA 220,1,8192
HS 29,19
AR 54,46
M 5,62,2
LL 17,90
LL 115,91
v_add_f32_dpp v14, v14, v14 quad_perm:[2,3,0,1] row_mask:0xf bank_mask:0xf
AR 55,47
XS 52
M 5,63,3
LA 17
SU 108
XS 53
XS 54
M 0,76,4
LC_ 115
v_add_f32_dpp v14, v14, v14 row_half_mirror row_mask:0xf bank_mask:0xf
XS 55
M 0,77,5
v_cvt_f64_f32 v[14:15], v14
AW 44,52
AW 45,53
M 1,4,4
WL 3
LO 114,16,114
LU 16,114
AW 46,54
M 1,5,5
RA 224,1,9216
HZ 30,16
AW 47,55
AR 52,48
M 2,16,4
LL 18,92
LL 118,93
s_mov_b64 exec, s[54:55]
SP
s_mov_b64 exec, -1
AR 53,49
AR 54,50
M 2,17,5
LA 18
ds_read_b64 v[14:15], v5 offset:2048
AR 55,51
RV 40,8,6144
RV 44,8,6208
RV 48,8,6272
RA 242,7,0
RA 250,7,256
XS 52
M 3,28,4
LC_ 118
XL 56,512
XS 53
M 3,29,5
s_add_u32 s42, s94, s62
v_subrev_u32 v5, s39, v5
XS 54
M 4,40,4
WL 9
LO 115,17,115
LU 17,115
XS 55
AW 48,52
M 4,41,5
)"
R"(s_addc_u32 s43, s95, 0
RA 228,1,10240
HS 30,17
AW 49,53
AW 50,54
M 5,64,4
WV 16
LL 19,94
LL 119,95
AW 51,55
M 5,65,5
WL 6
LA 19
SU 112
AR 52,52
AR 53,53
LZ 40,48
AR 54,54
M 0,78,0
LC_ 119
AR 55,55
M 0,79,1
XS 52
XS 53
M 1,6,0
WL 3
LO 118,18,118
LU 18,118
M 1,7,1
RA 232,1,11264
HZ 31,18
XS 54
XS 55
M 2,18,0
LL 16,96
LL 122,97
AW 52,52
AW 53,53
M 2,19,1
WV 15
LA 16
RV 252,22,5392
AW 54,54
LS 44,49
RV 40,8,6336
v_add_f32 v14, v14, v15
s_nop 1
v_add_f32_dpp v14, v14, v14 quad_perm:[1,0,3,2] row_mask:0xf bank_mask:0xf
AW 55,55
M 3,30,0
LC_ 122
XL 57,576
AR 52,56
M 3,31,1
s_add_u32 s42, s94, s63
v_add_f32_dpp v14, v14, v14 quad_perm:[2,3,0,1] row_mask:0xf bank_mask:0xf
AR 53,57
M 4,42,0
WL 5
LO 119,19,119
LU 19,119
AR 54,58
AR 55,59
M 4,43,1
s_addc_u32 s43, s95, 0
RA 236,1,12288
HS 31,19
v_add_f32_dpp v14, v14, v14 row_half_mirror row_mask:0xf bank_mask:0xf
XS 52
XS 53
M 5,66,0
LL 17,98
LL 123,99
XS 54
M 5,67,1
LA 17
SU 116
v_cvt_f64_f32 v[14:15], v14
XS 55
AW 56,52
M 0,80,2
LC_ 123
s_mov_b64 exec, s[54:55]
SP
s_mov_b64 exec, -1
AW 57,53
M 0,81,3
WV 15
XL 58,640
v_pk_add_f16 v31, v30, v31
AW 58,54
AW 59,55
LS 48,50
RV 44,8,6400
v_pk_add_f16 v29, v31, v29
AR 52,60
M 1,8,2
WL 4
LO 122,16,122
LU 16,122
AR 53,61
M 1,9,3
RA 216,1,13312
HZ 25,16
AR 54,62
M 2,20,2
LL 18,100
LL 126,101
AR 55,63
XS 52
M 2,21,3
LA 18
v_pk_add_f16 v27, v29, v27
XS 53
M 3,32,2
WV 14
LC_ 126
HX 27
XS 54
XS 55
M 3,33,3
s_add_u32 s42, s44, s64
AW 60,52
AW 61,53
LS 40,51
RV 48,8,6464
AW 62,54
M 4,44,2
WL 5
LO 123,17,123
LU 17,123
M 4,45,3
s_addc_u32 s43, s45, 0
RA 220,1,14336
HS 25,17
AW 63,55
AR 52,64
M 5,68,2
LL 19,102
LL 127,103
AR 53,65
M 5,69,3
LA 19
ST 120
AR 54,66
AR 55,67
M 0,82,4
LC_ 127
XL 59,704
XS 52
XS 53
M 0,83,5
XS 54
XS 55
M 1,10,4
WL 4
LO 126,18,126
LU 18,126
AW 64,52
M 1,11,5
WV 14
RA 224,1,15360
HS 25,18
AW 65,53
LS 44,52
RV 40,8,6528
AW 66,54
M 2,22,4
LL 16,56
LL 104,57
AW 67,55
M 2,23,5
LA 16
v_min_u32 v35, v35, v13
M 3,34,4
WV 13
LC_ 104
M 3,35,5
s_add_u32 s42, s44, s65
M 4,46,4
WL 4
LO 127,19,127
LU 19,127
M 4,47,5
s_addc_u32 s43, s45, 0
RA 228,1,16384
HS 25,19
LS 48,53
RV 44,8,6592
M 5,70,4
LL 17,58
LL 105,59
M 5,71,5
LA 17
ST 124
Z 6,72,0
LC_ 105
HX 25
M 6,73,1
W1 25,1024
Z 7,0,0
WL 5
LO 104,16,104
LU 16,104
M 7,1,1
RA 232,1,17408
HZ 26,16
Z 8,12,0
WV 13
LL 18,60
LL 108,61
M 8,13,1
s_mul_i32 s53, s50, 25600
LA 18
W1 27,3072
LS 40,54
RV 48,8,6656
Z 9,24,0
s_add_u32 s40, %[wlo], s53
LC_ 108
v_add_u32 v4, s39, v4
M 9,25,1
s_addc_u32 s41, %[whi], 0
Z 10,36,0
WL 6
LO 105,17,105
LU 17,105
M 10,37,1
s_mov_b32 m0, s38
RA 236,1,18432
HS 26,17
Z 11,60,0
LL 19,62
LL 109,63
M 11,61,1
WV 11
LA 19
DW 0
LS 44,55
RV 40,8,6720
M 6,74,2
LC_ 109
M 6,75,3
M 7,2,2
WL 4
LO 108,18,108
LU 18,108
M 7,3,3
s_add_u32 s37, s37, 32768
RA 216,1,19456
HZ 29,18
M 8,14,2
s_cmp_eq_u32 s37, s75
LL 16,64
LL 112,65
M 8,15,3
s_cselect_b32 s37, s74, s37
LA 16
DW 1024
M 9,26,2
LC_ 112
M 9,27,3
WV 10
LS 48,56
RV 44,8,6784
M 10,38,2
WL 4
LO 109,19,109
LU 19,109
M 10,39,3
RA 220,1,20480
HS 29,19
M 11,62,2
LL 17,66
LL 113,67
M 11,63,3
LA 17
DW 2048
M 6,76,4
LC_ 113
M 6,77,5
WV 9
LS 40,57
RV 48,8,6848
M 7,4,4
WL 5
LO 112,16,112
LU 16,112
M 7,5,5
s_mul_i32 s53, s51, 49152
RA 224,1,21504
HZ 30,16
M 8,16,4
s_bitcmp1_b64 s[90:91], s51
LL 18,68
LL 116,69
M 8,17,5
s_cselect_b64 s[44:45], s[84:85], s[82:83]
LA 18
M 9,28,4
s_add_u32 s44, s44, s53
LC_ 116
M 9,29,5
s_addc_u32 s45, s45, 0
M 10,40,4
WL 4
LO 113,17,113
LU 17,113
M 10,41,5
s_bitcmp1_b64 s[92:93], s51
RA 228,1,22528
HS 30,17
WV 6
LS 44,58
s_cselect_b64 s[94:95], s[88:89], s[86:87]
M 11,64,4
LL 19,70
LL 117,71
M 11,65,5
s_add_u32 s94, s94, s53
LA 19
M 6,78,0
s_addc_u32 s95, s95, 0
LC_ 117
M 6,79,1
s_add_u32 s51, s51, %[d]
M 7,6,0
WL 3
LO 116,18,116
LU 18,116
M 7,7,1
s_cmp_eq_u32 s51, %[wrap]
RA 232,1,23552
HZ 31,18
WV 4
LS 48,59
s_cselect_b32 s51, %[rst], s51
M 8,18,0
s_mul_i32 s53, %[d], 32768
LL 16,72
LL 120,73
M 8,19,1
s_add_u32 s64, s53, %[sb1]
LA 16
M 9,30,0
s_add_u32 s40, s40, 3072
LC_ 120
s_addc_u32 s41, s41, 0
s_add_u32 m0, s38, 3072
M 9,31,1
DW 0
M 10,42,0
WL 3
LO 117,19,117
LU 19,117
M 10,43,1
RA 236,1,24576
HS 31,19
M 11,66,0
LL 17,74
LL 121,75
M 11,67,1
LA 17
DW 1024
v_mul_f32 v36, 0xbb2aaaab, v36
v_mul_f32 v37, 0xbb2aaaab, v37
v_mul_f32 v38, 0xbb2aaaab, v38
v_mul_f32 v39, 0xbb2aaaab, v39
v_pk_add_f16 v31, v30, v31
M 6,80,2
LC_ 121
DW 2048
M 6,81,3
s_lshl_b32 s53, s52, 8
v_add_u32 v1, s37, v0
LD 40,244,48
s_add_u32 s48, %[alo], s53
M 7,8,2
WL 3
LO 120,16,120
LU 16,120
M 7,9,3
s_addc_u32 s49, %[ahi], 0
RA 216,1,0
HZ 32,16
LD 44,240,48
RA 242,7,512
v_and_b32 v22, 0xfffffcff, v1
M 8,20,2
s_add_u32 s52, s52, %[d]
LL 18,76
LL 124,77
M 8,21,3
s_cmp_eq_u32 s52, %[wrap]
LA 18
M 9,32,2
s_cselect_b32 s52, %[rst], s52
LC_ 124
DQ 3072
M 9,33,3
)"
R"(LD 48,252,49
CF 40,40,41
CF 42,42,43
AW 68,40
AW 69,42
M 10,44,2
WL 4
LO 121,17,121
LU 17,121
M 10,45,3
s_add_u32 s50, s50, %[d]
RA 220,1,1024
HS 32,17
LD 40,248,49
RA 250,7,768
CF 44,44,45
CF 46,46,47
AW 70,44
AW 71,46
M 11,68,2
LL 19,78
LL 125,79
M 11,69,3
s_cmp_eq_u32 s50, %[wrap]
LA 19
RV 252,22,5120
M 6,82,4
s_cselect_b32 s50, %[rst], s50
LC_ 125
v_pk_add_f16 v32, v29, v32
M 6,83,5
WL 7
v_pk_add_f16 v32, v31, v32
LD 44,244,50
CF 48,48,49
CF 50,50,51
v_pk_add_f16 v26, v32, v26
M 7,10,4
WL 5
LO 124,18,124
LU 18,124
M 7,11,5
s_add_u32 s38, s38, 32768
RA 224,1,2048
HZ 24,18
GZ 48
s_cmp_eq_u32 s38, s77
AW 72,48
AW 73,50
HX 26
M 8,22,4
s_cselect_b32 s38, s76, s38
LL 16,80
LL 106,81
M 8,23,5
s_barrier
LA 16
M 9,34,4
s_sub_u32 s39, 0, s39
LC_ 106
ds_read_b64 v[14:15], v5 offset:0
M 9,35,5
LD 48,240,50
RA 242,7,1024
CF 40,40,41
CF 42,42,43
AW 74,40
AW 75,42
M 10,46,4
WL 5
LO 125,19,125
LU 19,125
M 10,47,5
RA 228,1,3072
HS 24,19
LD 40,252,51
CF 44,44,45
CF 46,46,47
M 11,70,4
LL 17,82
LL 107,83
M 11,71,5
LA 17
HX 24
Z 0,72,0
LC_ 107
W1 24,0
M 0,73,1
W1 26,2048
GS 49
AW 76,44
AW 77,46
Z 1,0,0
WL 7
LO 106,16,106
LU 16,106
M 1,1,1
RA 232,1,4096
HZ 27,16
LD 44,248,51
RA 250,7,1280
CF 48,48,49
CF 50,50,51
AW 78,48
AW 79,50
Z 2,12,0
LL 18,84
LL 110,85
M 2,13,1
WL 7
LA 18
v_add_f32 v14, v14, v15
LD 48,244,52
CF 40,40,41
CF 42,42,43
v_add_f32_dpp v14, v14, v14 quad_perm:[1,0,3,2] row_mask:0xf bank_mask:0xf
Z 3,24,0
LC_ 110
M 3,25,1
s_add_u32 s42, s94, s60
v_add_f32_dpp v14, v14, v14 quad_perm:[2,3,0,1] row_mask:0xf bank_mask:0xf
Z 4,36,0
WL 6
LO 107,17,107
LU 17,107
M 4,37,1
s_addc_u32 s43, s95, 0
RA 236,1,6144
HS 27,17
GS 50
SU 104
AW 80,40
AW 81,42
v_add_f32_dpp v14, v14, v14 row_half_mirror row_mask:0xf bank_mask:0xf
Z 5,60,0
LL 19,86
LL 111,87
M 5,61,1
LA 19
v_cvt_f64_f32 v[14:15], v14
LD 40,240,52
RA 242,7,1536
CF 44,44,45
CF 46,46,47
AW 82,44
AW 83,46
M 0,74,2
LC_ 111
M 0,75,3
s_mov_b64 exec, s[54:55]
SP
s_mov_b64 exec, -1
M 1,2,2
WL 4
LO 110,18,110
LU 18,110
M 1,3,3
RA 216,1,7168
HZ 29,18
LD 44,252,53
ds_read_b64 v[14:15], v5 offset:2048
CF 48,48,49
CF 50,50,51
M 2,14,2
LL 16,88
LL 114,89
M 2,15,3
LA 16
v_subrev_u32 v5, s39, v5
GS 51
AW 84,48
AW 85,50
M 3,26,2
LC_ 114
M 3,27,3
s_add_u32 s42, s94, s61
M 4,38,2
WL 4
LO 111,19,111
LU 19,111
M 4,39,3
s_addc_u32 s43, s95, 0
RA 220,1,8192
HS 29,19
LD 48,248,53
RA 250,7,1792
CF 40,40,41
CF 42,42,43
AW 86,40
AW 87,42
M 5,62,2
LL 17,90
LL 115,91
M 5,63,3
LA 17
SU 108
LD 40,244,54
CF 44,44,45
CF 46,46,47
M 0,76,4
LC_ 115
M 0,77,5
M 1,4,4
WL 4
LO 114,16,114
LU 16,114
M 1,5,5
RA 224,1,9216
HZ 30,16
GS 52
AW 88,44
AW 89,46
v_add_f32 v14, v14, v15
M 2,16,4
LL 18,92
LL 118,93
M 2,17,5
LA 18
v_add_f32_dpp v14, v14, v14 quad_perm:[1,0,3,2] row_mask:0xf bank_mask:0xf
LD 44,240,54
RA 242,7,2048
CF 48,48,49
CF 50,50,51
AW 90,48
AW 91,50
v_add_f32_dpp v14, v14, v14 quad_perm:[2,3,0,1] row_mask:0xf bank_mask:0xf
M 3,28,4
WL 5
LC_ 118
v_add_f32_dpp v14, v14, v14 row_half_mirror row_mask:0xf bank_mask:0xf
M 3,29,5
s_add_u32 s42, s94, s62
LD 48,252,55
s_addc_u32 s43, s95, 0
CF 40,40,41
CF 42,42,43
M 4,40,4
WL 4
LO 115,17,115
LU 17,115
M 4,41,5
RA 228,1,10240
HS 30,17
M 5,64,4
LL 19,94
LL 119,95
M 5,65,5
LA 19
SU 112
GS 53
AW 92,40
AW 93,42
v_cvt_f64_f32 v[14:15], v14
M 0,78,0
LC_ 119
s_mov_b64 exec, s[54:55]
SP
s_mov_b64 exec, -1
M 0,79,1
LD 40,248,55
RA 250,7,2304
CF 44,44,45
CF 46,46,47
AW 94,44
AW 95,46
M 1,6,0
WL 4
LO 118,18,118
LU 18,118
M 1,7,1
RA 232,1,11264
HZ 31,18
M 2,18,0
LL 16,96
LL 122,97
M 2,19,1
LA 16
RV 252,22,5392
LD 44,244,56
CF 48,48,49
CF 50,50,51
M 3,30,0
LC_ 122
M 3,31,1
s_add_u32 s42, s94, s63
GS 54
s_addc_u32 s43, s95, 0
AW 96,48
AW 97,50
M 4,42,0
WL 5
LO 119,19,119
LU 19,119
M 4,43,1
RA 236,1,12288
HS 31,19
M 5,66,0
LL 17,98
LL 123,99
M 5,67,1
LA 17
SU 116
LD 48,240,56
RA 242,7,2560
CF 40,40,41
CF 42,42,43
AW 98,40
AW 99,42
v_pk_add_f16 v31, v30, v31
M 0,80,2
LC_ 123
M 0,81,3
WL 8
LD 40,252,57
CF 44,44,45
CF 46,46,47
M 1,8,2
WL 4
LO 122,16,122
LU 16,122
M 1,9,3
RA 216,1,13312
HZ 32,16
M 2,20,2
LL 18,100
LL 126,101
M 2,21,3
LA 18
GS 55
AW 100,44
AW 101,46
M 3,32,2
LC_ 126
M 3,33,3
s_add_u32 s42, s94, s64
LD 44,248,57
RA 250,7,2816
CF 48,48,49
CF 50,50,51
AW 102,48
AW 103,50
M 4,44,2
WL 4
LO 123,17,123
LU 17,123
M 4,45,3
s_addc_u32 s43, s95, 0
RA 220,1,14336
HS 32,17
LD 48,244,58
SU 120
CF 40,40,41
CF 42,42,43
M 5,68,2
LL 19,102
LL 127,103
M 5,69,3
LA 19
v_pk_add_f16 v32, v29, v32
M 0,82,4
LC_ 127
v_pk_add_f16 v32, v31, v32
M 0,83,5
v_pk_add_f16 v27, v32, v27
GS 56
AW 104,40
AW 105,42
HX 27
M 1,10,4
WL 4
LO 126,18,126
LU 18,126
M 1,11,5
RA 224,1,15360
HZ 25,18
LD 40,240,58
CF 44,44,45
CF 46,46,47
AW 106,44
AW 107,46
M 2,22,4
LL 16,56
LL 104,57
M 2,23,5
LA 16
M 3,34,4
WL 5
LC_ 104
M 3,35,5
s_add_u32 s42, s44, s65
LD 44,252,59
s_addc_u32 s43, s45, 0
CF 48,48,49
CF 50,50,51
M 4,46,4
WL 3
LO 127,19,127
LU 19,127
M 4,47,5
RA 228,1,16384
)"
R"(HS 25,19
GS 57
ST 124
AW 108,48
AW 109,50
M 5,70,4
LL 17,58
LL 105,59
M 5,71,5
LA 17
HX 25
Z 6,72,0
LC_ 105
W1 25,1024
M 6,73,1
W1 27,3072
LD 48,248,59
CF 40,40,41
CF 42,42,43
AW 110,40
AW 111,42
Z 7,0,0
WL 5
LO 104,16,104
LU 16,104
M 7,1,1
RA 232,1,17408
HZ 26,16
GS 58
CF 44,44,45
CF 46,46,47
AW 112,44
AW 113,46
v_add_u32 v4, s39, v4
Z 8,12,0
LL 18,60
LL 108,61
M 8,13,1
s_mul_i32 s53, s50, 25600
LA 18
Z 9,24,0
s_add_u32 s40, %[wlo], s53
LC_ 108
M 9,25,1
s_addc_u32 s41, %[whi], 0
CF 48,48,49
CF 50,50,51
AW 114,48
AW 115,50
Z 10,36,0
WL 5
LO 105,17,105
LU 17,105
M 10,37,1
s_mov_b32 m0, s38
RA 236,1,18432
HS 26,17
GS 59
Z 11,60,0
LL 19,62
LL 109,63
M 11,61,1
LA 19
DW 0
RV 36,8,6912
M 6,74,2
LC_ 109
M 6,75,3
M 7,2,2
WL 4
LO 108,18,108
LU 18,108
M 7,3,3
s_add_u32 s37, s37, 32768
RA 216,1,19456
HZ 29,18
v_cndmask_b32 v52, v52, v53, s[78:79]
v_cndmask_b32 v54, v54, v55, s[78:79]
v_cndmask_b32 v52, v52, v54, s[80:81]
v_mul_f32 v52, 0x3b2aaaab, v52
M 8,14,2
s_cmp_eq_u32 s37, s75
LL 16,64
LL 112,65
M 8,15,3
s_cselect_b32 s37, s74, s37
LA 16
DW 1024
C 52,52,52
v_lshlrev_b32 v52, 16, v52
WL 3
v_add_f32 v52, v36, v52
C 52,52,52
M 9,26,2
LC_ 112
M 9,27,3
M 10,38,2
WL 3
LO 109,19,109
LU 19,109
M 10,39,3
RA 220,1,20480
HS 29,19
v_lshlrev_b32 v52, 16, v52
v_sqrt_f32 v52, v52
M 11,62,2
LL 17,66
LL 113,67
M 11,63,3
LA 17
DW 2048
C 52,52,52
v_lshlrev_b32 v52, 16, v52
v_cmp_ge_f32 vcc, v37, v52
v_rcp_f32 v52, v52
M 6,76,4
LC_ 113
M 6,77,5
M 7,4,4
WL 3
LO 112,16,112
LU 16,112
M 7,5,5
s_mul_i32 s53, s51, 49152
RA 224,1,21504
HZ 30,16
v_mul_f32 v52, v38, v52
v_cndmask_b32 v52, 0, v52, vcc
CF 52,52,52
M 8,16,4
s_bitcmp1_b64 s[90:91], s51
LL 18,68
LL 116,69
M 8,17,5
s_cselect_b64 s[44:45], s[84:85], s[82:83]
LA 18
ds_bpermute_b32 v13, v28, v52
M 9,28,4
s_add_u32 s44, s44, s53
LC_ 116
M 9,29,5
s_addc_u32 s45, s45, 0
M 10,40,4
WL 4
LO 113,17,113
LU 17,113
M 10,41,5
s_bitcmp1_b64 s[92:93], s51
RA 228,1,22528
HS 30,17
M 11,64,4
LL 19,70
LL 117,71
M 11,65,5
s_cselect_b64 s[94:95], s[88:89], s[86:87]
LA 19
M 6,78,0
s_add_u32 s94, s94, s53
LC_ 117
M 6,79,1
s_addc_u32 s95, s95, 0
M 7,6,0
WL 3
LO 116,18,116
LU 18,116
M 7,7,1
s_add_u32 s51, s51, %[d]
RA 232,1,23552
HZ 31,18
M 8,18,0
s_cmp_eq_u32 s51, %[wrap]
LL 16,72
LL 120,73
M 8,19,1
s_cselect_b32 s51, %[rst], s51
LA 16
AR 52,68
M 9,30,0
s_add_u32 s40, s40, 3072
LC_ 120
s_addc_u32 s41, s41, 0
s_add_u32 m0, s38, 3072
AR 53,69
M 9,31,1
DW 0
AR 54,70
M 10,42,0
WL 3
LO 117,19,117
LU 19,117
M 10,43,1
RA 236,1,24576
HS 31,19
M 11,66,0
LL 17,74
LL 121,75
M 11,67,1
LA 17
DW 1024
M 6,80,2
LC_ 121
DW 2048
AR 55,71
M 6,81,3
s_lshl_b32 s53, s52, 8
v_add_u32 v1, s37, v0
v_pk_add_f16 v31, v30, v31
XS 52
M 7,8,2
WL 3
LO 120,16,120
LU 16,120
XS 53
XS 54
M 7,9,3
s_add_u32 s48, %[alo], s53
RA 216,1,0
HZ 32,16
XS 55
M 8,20,2
s_addc_u32 s49, %[ahi], 0
LL 18,76
LL 124,77
AW 68,52
AW 69,53
M 8,21,3
s_add_u32 s52, s52, %[d]
LA 18
v_and_b32 v22, 0xfffffcff, v1
AW 70,54
M 9,32,2
s_cmp_eq_u32 s52, %[wrap]
LC_ 124
AW 71,55
AR 52,72
M 9,33,3
s_cselect_b32 s52, %[rst], s52
DQ 3072
AR 53,73
AR 54,74
M 10,44,2
WL 3
LO 121,17,121
LU 17,121
AR 55,75
M 10,45,3
s_add_u32 s50, s50, %[d]
RA 220,1,1024
HS 32,17
XS 52
XS 53
M 11,68,2
LL 19,78
LL 125,79
XS 54
M 11,69,3
s_cmp_eq_u32 s50, %[wrap]
LA 19
RV 252,22,5120
XS 55
AW 72,52
M 6,82,4
s_cselect_b32 s50, %[rst], s50
LC_ 125
v_pk_add_f16 v32, v29, v32
AW 73,53
M 6,83,5
v_pk_add_f16 v32, v31, v32
AW 74,54
AW 75,55
M 7,10,4
WL 4
LO 124,18,124
LU 18,124
AR 52,76
M 7,11,5
s_add_u32 s38, s38, 32768
RA 224,1,2048
HZ 24,18
AR 53,77
AR 54,78
M 8,22,4
s_cmp_eq_u32 s38, s77
LL 16,80
LL 106,81
AR 55,79
XS 52
M 8,23,5
s_cselect_b32 s38, s76, s38
LA 16
v_pk_add_f16 v26, v32, v26
XS 53
M 9,34,4
s_barrier
LC_ 106
HX 26
XS 54
XS 55
M 9,35,5
s_sub_u32 s39, 0, s39
AW 76,52
AW 77,53
M 10,46,4
WL 3
LO 125,19,125
LU 19,125
AW 78,54
M 10,47,5
RA 228,1,3072
HS 24,19
M 11,70,4
LL 17,82
LL 107,83
M 11,71,5
LA 17
HX 24
AW 79,55
AR 52,80
AR 53,81
AR 54,82
Z 0,72,0
LC_ 107
ds_read_b64 v[14:15], v5 offset:0
AR 55,83
XS 52
M 0,73,1
W1 24,0
XS 53
XS 54
Z 1,0,0
WL 5
LO 106,16,106
LU 16,106
XS 55
M 1,1,1
RA 232,1,4096
HZ 27,16
AW 80,52
AW 81,53
Z 2,12,0
LL 18,84
LL 110,85
AW 82,54
M 2,13,1
LA 18
W1 26,2048
AW 83,55
AR 52,84
Z 3,24,0
LC_ 110
AR 53,85
M 3,25,1
s_add_u32 s42, s94, s60
AR 54,86
AR 55,87
Z 4,36,0
WL 6
LO 107,17,107
LU 17,107
XS 52
XS 53
M 4,37,1
s_addc_u32 s43, s95, 0
RA 236,1,6144
HS 27,17
XS 54
Z 5,48,0
LL 19,86
LL 111,87
XS 55
AW 84,52
M 5,49,1
LA 19
SU 104
AW 85,53
M 0,74,2
WL 7
LC_ 111
v_add_f32 v14, v14, v15
AW 86,54
AW 87,55
M 0,75,3
v_add_f32_dpp v14, v14, v14 quad_perm:[1,0,3,2] row_mask:0xf bank_mask:0xf
AR 52,88
M 1,2,2
WL 3
LO 110,18,110
LU 18,110
AR 53,89
AR 54,90
M 1,3,3
RA 216,1,7168
HZ 29,18
AR 55,91
M 2,14,2
LL 16,88
LL 114,89
XS 52
XS 53
M 2,15,3
)"
R"(LA 16
v_add_f32_dpp v14, v14, v14 quad_perm:[2,3,0,1] row_mask:0xf bank_mask:0xf
XS 54
XS 55
M 3,26,2
LC_ 114
v_add_f32_dpp v14, v14, v14 row_half_mirror row_mask:0xf bank_mask:0xf
AW 88,52
M 3,27,3
s_add_u32 s42, s94, s61
v_cvt_f64_f32 v[14:15], v14
AW 89,53
AW 90,54
M 4,38,2
WL 3
LO 111,19,111
LU 19,111
AW 91,55
M 4,39,3
s_addc_u32 s43, s95, 0
RA 220,1,8192
HS 29,19
AR 52,92
AR 53,93
M 5,50,2
LL 17,90
LL 115,91
AR 54,94
M 5,51,3
LA 17
s_mov_b64 exec, s[54:55]
SP
s_mov_b64 exec, -1
AR 55,95
XS 52
M 0,76,4
LC_ 115
ds_read_b64 v[14:15], v5 offset:2048
XS 53
M 0,77,5
SU 108
v_subrev_u32 v5, s39, v5
XS 54
XS 55
M 1,4,4
WL 4
LO 114,16,114
LU 16,114
AW 92,52
AW 93,53
M 1,5,5
RA 224,1,9216
HZ 30,16
AW 94,54
M 2,16,4
LL 18,92
LL 118,93
AW 95,55
AR 52,96
M 2,17,5
LA 18
AR 53,97
M 3,28,4
LC_ 118
AR 54,98
AR 55,99
M 3,29,5
s_add_u32 s42, s94, s62
XS 52
XS 53
M 4,40,4
WL 4
LO 115,17,115
LU 17,115
XS 54
M 4,41,5
s_addc_u32 s43, s95, 0
RA 228,1,10240
HS 30,17
XS 55
M 5,52,4
LL 19,94
LL 119,95
AW 96,52
AW 97,53
M 5,53,5
LA 19
SU 112
AW 98,54
AW 99,55
M 0,78,0
WL 5
LC_ 119
v_add_f32 v14, v14, v15
AR 52,100
M 0,79,1
v_add_f32_dpp v14, v14, v14 quad_perm:[1,0,3,2] row_mask:0xf bank_mask:0xf
AR 53,101
AR 54,102
M 1,6,0
WL 3
LO 118,18,118
LU 18,118
AR 55,103
M 1,7,1
RA 232,1,11264
HZ 31,18
XS 52
XS 53
M 2,18,0
LL 16,96
LL 122,97
XS 54
M 2,19,1
LA 16
RV 252,22,5392
XS 55
AW 100,52
M 3,30,0
LC_ 122
v_add_f32_dpp v14, v14, v14 quad_perm:[2,3,0,1] row_mask:0xf bank_mask:0xf
AW 101,53
AW 102,54
M 3,31,1
s_add_u32 s42, s94, s63
v_add_f32_dpp v14, v14, v14 row_half_mirror row_mask:0xf bank_mask:0xf
AW 103,55
M 4,42,0
WL 4
LO 119,19,119
LU 19,119
AR 52,104
AR 53,105
M 4,43,1
s_addc_u32 s43, s95, 0
RA 236,1,12288
HS 31,19
AR 54,106
M 5,54,0
LL 17,98
LL 123,99
AR 55,107
XS 52
M 5,55,1
LA 17
SU 116
v_cvt_f64_f32 v[14:15], v14
XS 53
M 0,80,2
LC_ 123
s_mov_b64 exec, s[54:55]
SP
s_mov_b64 exec, -1
XS 54
XS 55
M 0,81,3
v_pk_add_f16 v31, v30, v31
AW 104,52
M 1,8,2
WL 3
LO 122,16,122
LU 16,122
AW 105,53
AW 106,54
M 1,9,3
RA 216,1,13312
HZ 32,16
AW 107,55
AR 52,108
M 2,20,2
LL 18,100
LL 126,101
AR 53,109
M 2,21,3
LA 18
AR 54,110
AR 55,111
M 3,32,2
LC_ 126
XS 52
M 3,33,3
s_add_u32 s42, s94, s64
XS 53
XS 54
M 4,44,2
WL 3
LO 123,17,123
LU 17,123
XS 55
M 4,45,3
s_addc_u32 s43, s95, 0
RA 220,1,14336
HS 32,17
AW 108,52
AW 109,53
M 5,56,2
LL 19,102
LL 127,103
AW 110,54
M 5,57,3
LA 19
SU 120
AW 111,55
AR 52,112
M 0,82,4
LC_ 127
v_pk_add_f16 v32, v29, v32
AR 53,113
AR 54,114
M 0,83,5
v_pk_add_f16 v32, v31, v32
AR 55,115
M 1,10,4
WL 3
LO 126,18,126
LU 18,126
XS 52
XS 53
M 1,11,5
RA 224,1,15360
HZ 25,18
XS 54
M 2,22,4
LL 16,56
LL 104,57
XS 55
AW 112,52
M 2,23,5
LA 16
v_pk_add_f16 v27, v32, v27
AW 113,53
M 3,34,4
LC_ 104
HX 27
AW 114,54
AW 115,55
M 3,35,5
s_add_u32 s42, s44, s65
v_min_u32 v35, v35, v13
M 4,46,4
WL 3
LO 127,19,127
LU 19,127
M 4,47,5
s_addc_u32 s43, s45, 0
RA 228,1,16384
HS 25,19
M 5,58,4
LL 17,58
LL 105,59
M 5,59,5
LA 17
ST 124
Z 6,72,0
LC_ 105
HX 25
M 6,73,1
W1 25,1024
Z 7,0,0
WL 4
LO 104,16,104
LU 16,104
M 7,1,1
RA 232,1,17408
HZ 26,16
Z 8,12,0
LL 18,60
LL 108,61
M 8,13,1
s_mul_i32 s53, s50, 25600
LA 18
W1 27,3072
Z 9,24,0
s_add_u32 s40, %[wlo], s53
LC_ 108
v_add_u32 v4, s39, v4
M 9,25,1
s_addc_u32 s41, %[whi], 0
Z 10,36,0
WL 5
LO 105,17,105
LU 17,105
M 10,37,1
s_mov_b32 m0, s38
RA 236,1,18432
HS 26,17
Z 11,48,0
LL 19,62
LL 109,63
M 11,49,1
LA 19
DW 0
M 6,74,2
LC_ 109
M 6,75,3
M 7,2,2
WL 3
LO 108,18,108
LU 18,108
M 7,3,3
s_add_u32 s37, s37, 32768
RA 216,1,19456
HZ 29,18
M 8,14,2
s_cmp_eq_u32 s37, s75
LL 16,64
LL 112,65
M 8,15,3
s_cselect_b32 s37, s74, s37
LA 16
DW 1024
M 9,26,2
LC_ 112
M 9,27,3
M 10,38,2
WL 3
LO 109,19,109
LU 19,109
M 10,39,3
RA 220,1,20480
HS 29,19
M 11,50,2
LL 17,66
LL 113,67
M 11,51,3
LA 17
DW 2048
M 6,76,4
LC_ 113
M 6,77,5
M 7,4,4
WL 3
LO 112,16,112
LU 16,112
M 7,5,5
s_mul_i32 s53, s51, 49152
RA 224,1,21504
HZ 30,16
M 8,16,4
s_bitcmp1_b64 s[92:93], s51
LL 18,68
LL 116,69
M 8,17,5
s_cselect_b64 s[94:95], s[88:89], s[86:87]
LA 18
M 9,28,4
s_add_u32 s94, s94, s53
LC_ 116
M 9,29,5
s_addc_u32 s95, s95, 0
M 10,40,4
WL 3
LO 113,17,113
LU 17,113
M 10,41,5
s_add_u32 s51, s51, %[d]
RA 228,1,22528
HS 30,17
M 11,52,4
LL 19,70
LL 117,71
M 11,53,5
s_cmp_eq_u32 s51, %[wrap]
LA 19
M 6,78,0
s_cselect_b32 s51, %[rst], s51
LC_ 117
M 6,79,1
s_mul_i32 s53, %[d], 33792
M 7,6,0
WL 3
LO 116,18,116
LU 18,116
M 7,7,1
s_add_u32 s65, s53, %[sb1]
RA 232,1,23552
HZ 31,18
M 8,18,0
s_add_u32 s40, s40, 3072
LL 16,72
LL 120,73
M 8,19,1
s_addc_u32 s41, s41, 0
LA 16
M 9,30,0
s_add_u32 m0, s38, 3072
LC_ 120
DW 0
M 9,31,1
DW 1024
M 10,42,0
WL 3
LO 117,19,117
LU 19,117
M 10,43,1
RA 236,1,24576
HS 31,19
M 11,54,0
LL 17,74
LL 121,75
)"
R"(M 11,55,1
LA 17
DW 2048
M 6,80,2
LC_ 121
v_pk_add_f16 v31, v30, v31
M 6,81,3
s_lshl_b32 s53, s52, 8
v_add_u32 v1, s37, v0
M 7,8,2
WL 3
LO 120,16,120
LU 16,120
M 7,9,3
s_add_u32 s48, %[alo], s53
RA 216,1,0
HZ 32,16
M 8,20,2
s_addc_u32 s49, %[ahi], 0
LL 18,76
LL 124,77
M 8,21,3
s_add_u32 s52, s52, %[d]
LA 18
v_and_b32 v22, 0xfffffcff, v1
M 9,32,2
s_cmp_eq_u32 s52, %[wrap]
LC_ 124
M 9,33,3
s_cselect_b32 s52, %[rst], s52
DQ 3072
M 10,44,2
WL 3
LO 121,17,121
LU 17,121
M 10,45,3
s_add_u32 s50, s50, %[d]
RA 220,1,1024
HS 32,17
M 11,56,2
LL 19,78
LL 125,79
M 11,57,3
s_cmp_eq_u32 s50, %[wrap]
LA 19
RV 252,22,5120
M 6,82,4
s_cselect_b32 s50, %[rst], s50
LC_ 125
v_pk_add_f16 v32, v29, v32
M 6,83,5
WV 30
v_pk_add_f16 v32, v31, v32
M 7,10,4
WL 4
LO 124,18,124
LU 18,124
M 7,11,5
s_add_u32 s38, s38, 32768
RA 224,1,2048
HZ 33,18
M 8,22,4
s_cmp_eq_u32 s38, s77
LL 16,80
LL 106,81
M 8,23,5
s_cselect_b32 s38, s76, s38
LA 16
M 9,34,4
s_barrier
LC_ 106
M 9,35,5
s_sub_u32 s39, 0, s39
M 10,46,4
WL 3
LO 125,19,125
LU 19,125
M 10,47,5
RA 228,1,3072
HS 33,19
M 11,58,4
LL 17,82
LL 107,83
M 11,59,5
LA 17
v_pk_add_f16 v33, v26, v33
Z 0,72,0
LC_ 107
ds_read_b64 v[14:15], v5 offset:0
M 0,73,1
v_pk_add_f16 v26, v32, v33
Z 1,0,0
WL 4
LO 106,16,106
LU 16,106
M 1,1,1
RA 232,1,4096
HZ 27,16
Z 2,12,0
LL 18,84
LL 110,85
M 2,13,1
LA 18
HX 26
Z 3,24,0
LC_ 110
W1 26,2048
M 3,25,1
s_add_u32 s42, s94, s60
Z 4,36,0
WL 5
LO 107,17,107
LU 17,107
M 4,37,1
s_addc_u32 s43, s95, 0
RA 236,1,6144
HS 27,17
Z 5,48,0
LL 19,86
LL 111,87
M 5,49,1
LA 19
SU 104
M 0,74,2
WL 6
LC_ 111
v_add_f32 v14, v14, v15
M 0,75,3
s_nop 0
v_add_f32_dpp v14, v14, v14 quad_perm:[1,0,3,2] row_mask:0xf bank_mask:0xf
M 1,2,2
WL 4
LO 110,18,110
LU 18,110
M 1,3,3
RA 216,1,7168
HZ 29,18
M 2,14,2
LL 16,88
LL 114,89
M 2,15,3
LA 16
v_add_f32_dpp v14, v14, v14 quad_perm:[2,3,0,1] row_mask:0xf bank_mask:0xf
M 3,26,2
LC_ 114
v_add_f32_dpp v14, v14, v14 row_half_mirror row_mask:0xf bank_mask:0xf
M 3,27,3
s_add_u32 s42, s94, s61
v_cvt_f64_f32 v[14:15], v14
M 4,38,2
WL 3
LO 111,19,111
LU 19,111
M 4,39,3
s_addc_u32 s43, s95, 0
RA 220,1,8192
HS 29,19
M 5,50,2
LL 17,90
LL 115,91
M 5,51,3
LA 17
s_mov_b64 exec, s[54:55]
SP
s_mov_b64 exec, -1
M 0,76,4
LC_ 115
ds_read_b64 v[14:15], v5 offset:2048
M 0,77,5
SU 108
v_subrev_u32 v5, s39, v5
M 1,4,4
WL 4
LO 114,16,114
LU 16,114
M 1,5,5
PFN
RA 224,1,9216
HZ 30,16
M 2,16,4
s_mov_b32 m0, %[pm]
LL 18,92
LL 118,93
M 2,17,5
LA 18
DF
M 3,28,4
LC_ 118
M 3,29,5
s_add_u32 s42, s94, s62
M 4,40,4
WL 4
LO 115,17,115
LU 17,115
M 4,41,5
s_addc_u32 s43, s95, 0
RA 228,1,10240
HS 30,17
M 5,52,4
LL 19,94
LL 119,95
M 5,53,5
LA 19
SU 112
M 0,78,0
WL 5
LC_ 119
v_add_f32 v14, v14, v15
M 0,79,1
s_nop 0
v_add_f32_dpp v14, v14, v14 quad_perm:[1,0,3,2] row_mask:0xf bank_mask:0xf
M 1,6,0
WL 3
LO 118,18,118
LU 18,118
M 1,7,1
RA 232,1,11264
HZ 31,18
M 2,18,0
LL 16,96
LL 122,97
M 2,19,1
LA 16
RV 252,22,5392
M 3,30,0
LC_ 122
v_add_f32_dpp v14, v14, v14 quad_perm:[2,3,0,1] row_mask:0xf bank_mask:0xf
M 3,31,1
s_add_u32 s42, s94, s63
v_add_f32_dpp v14, v14, v14 row_half_mirror row_mask:0xf bank_mask:0xf
M 4,42,0
WL 4
LO 119,19,119
LU 19,119
M 4,43,1
s_addc_u32 s43, s95, 0
RA 236,1,12288
HS 31,19
M 5,54,0
LL 17,98
LL 123,99
M 5,55,1
LA 17
SU 116
v_cvt_f64_f32 v[14:15], v14
M 0,80,2
LC_ 123
s_mov_b64 exec, s[54:55]
SP
s_mov_b64 exec, -1
M 0,81,3
v_pk_add_f16 v31, v30, v31
M 1,8,2
WL 3
LO 122,16,122
LU 16,122
M 1,9,3
RA 216,1,13312
HZ 32,16
M 2,20,2
LL 18,100
LL 126,101
M 2,21,3
LA 18
M 3,32,2
LC_ 126
M 3,33,3
s_add_u32 s42, s94, s64
M 4,44,2
WL 3
LO 123,17,123
LU 17,123
M 4,45,3
s_addc_u32 s43, s95, 0
RA 220,1,14336
HS 32,17
M 5,56,2
LL 19,102
LL 127,103
M 5,57,3
LA 19
SU 120
M 0,82,4
LC_ 127
v_pk_add_f16 v32, v29, v32
M 0,83,5
v_pk_add_f16 v32, v31, v32
M 1,10,4
WL 3
LO 126,18,126
LU 18,126
M 1,11,5
RA 224,1,15360
HZ 33,18
M 2,22,4
LL 16,56
LL 104,57
M 2,23,5
LA 16
M 3,34,4
LC_ 104
M 3,35,5
s_add_u32 s42, s94, s65
M 4,46,4
WL 3
LO 127,19,127
LU 19,127
M 4,47,5
s_addc_u32 s43, s95, 0
RA 228,1,16384
HS 33,19
M 5,58,4
LL 17,58
LL 105,59
M 5,59,5
LA 17
SU 124
Z 6,72,0
LC_ 105
v_pk_add_f16 v33, v27, v33
M 6,73,1
v_pk_add_f16 v27, v32, v33
Z 7,0,0
WL 3
LO 104,16,104
LU 16,104
M 7,1,1
RA 232,1,17408
HZ 26,16
Z 8,12,0
LL 18,60
LL 108,61
M 8,13,1
s_mul_i32 s53, s50, 25600
LA 18
HX 27
Z 9,24,0
s_add_u32 s40, %[wlo], s53
LC_ 108
W1 27,3072
M 9,25,1
s_addc_u32 s41, %[whi], 0
v_add_u32 v4, s39, v4
Z 10,36,0
WL 4
LO 105,17,105
LU 17,105
M 10,37,1
s_mov_b32 m0, s38
RA 236,1,18432
HS 26,17
Z 11,48,0
LL 19,62
LL 109,63
M 11,49,1
LA 19
s_mov_b64 exec, 1
AT
s_mov_b64 exec, -1
M 6,74,2
LC_ 109
DW 0
M 6,75,3
M 7,2,2
WL 4
LO 108,18,108
LU 18,108
M 7,3,3
s_add_u32 s37, s37, 32768
RA 216,1,19456
HZ 29,18
M 8,14,2
s_cmp_eq_u32 s37, s75
LL 16,64
LL 112,65
M 8,15,3
s_cselect_b32 s37, s74, s37
LA 16
DW 1024
M 9,26,2
LC_ 112
M 9,27,3
M 10,38,2
WL 3
LO 109,19,109
LU 19,109
M 10,39,3
RA 220,1,20480
HS 29,19
M 11,50,2
LL 17,66
LL 113,67
M 11,51,3
LA 17
DW 2048
M 6,76,4
LC_ 113
M 6,77,5
M 7,4,4
WL 3
)"
R"(LO 112,16,112
LU 16,112
M 7,5,5
s_mul_i32 s53, s51, 49152
RA 224,1,21504
HZ 30,16
M 8,16,4
s_bitcmp1_b64 s[92:93], s51
LL 18,68
LL 116,69
M 8,17,5
s_cselect_b64 s[94:95], s[88:89], s[86:87]
LA 18
M 9,28,4
s_add_u32 s94, s94, s53
LC_ 116
M 9,29,5
s_addc_u32 s95, s95, 0
M 10,40,4
WL 3
LO 113,17,113
LU 17,113
M 10,41,5
s_add_u32 s51, s51, %[d]
RA 228,1,22528
HS 30,17
M 11,52,4
LL 19,70
LL 117,71
M 11,53,5
s_cmp_eq_u32 s51, %[wrap]
LA 19
M 6,78,0
s_cselect_b32 s51, %[rst], s51
LC_ 117
M 6,79,1
s_add_u32 s40, s40, 3072
M 7,6,0
WL 3
LO 116,18,116
LU 18,116
M 7,7,1
s_addc_u32 s41, s41, 0
RA 232,1,23552
HZ 31,18
M 8,18,0
s_add_u32 m0, s38, 3072
LL 16,72
LL 120,73
M 8,19,1
LA 16
DW 0
M 9,30,0
LC_ 120
DW 1024
M 9,31,1
M 10,42,0
WL 3
LO 117,19,117
LU 19,117
M 10,43,1
RA 236,1,24576
HS 31,19
M 11,54,0
LL 17,74
LL 121,75
M 11,55,1
LA 17
DW 2048
M 6,80,2
LC_ 121
v_pk_add_f16 v31, v30, v31
M 6,81,3
s_lshl_b32 s53, s52, 8
v_add_u32 v1, s37, v0
M 7,8,2
WL 3
LO 120,16,120
LU 16,120
M 7,9,3
s_add_u32 s48, %[alo], s53
RA 216,1,0
HZ 32,16
M 8,20,2
s_addc_u32 s49, %[ahi], 0
LL 18,76
LL 124,77
M 8,21,3
s_add_u32 s52, s52, %[d]
LA 18
v_and_b32 v22, 0xfffffcff, v1
M 9,32,2
s_cmp_eq_u32 s52, %[wrap]
LC_ 124
M 9,33,3
s_cselect_b32 s52, %[rst], s52
DQ 3072
M 10,44,2
WL 3
LO 121,17,121
LU 17,121
M 10,45,3
s_add_u32 s50, s50, %[d]
RA 220,1,1024
HS 32,17
M 11,56,2
LL 19,78
LL 125,79
M 11,57,3
s_cmp_eq_u32 s50, %[wrap]
LA 19
RV 252,22,5120
M 6,82,4
s_cselect_b32 s50, %[rst], s50
LC_ 125
v_pk_add_f16 v32, v29, v32
M 6,83,5
WV 32
v_pk_add_f16 v32, v31, v32
M 7,10,4
WL 4
LO 124,18,124
LU 18,124
M 7,11,5
s_add_u32 s38, s38, 32768
RA 224,1,2048
HZ 33,18
M 8,22,4
s_cmp_eq_u32 s38, s77
LL 16,80
LL 106,81
M 8,23,5
s_cselect_b32 s38, s76, s38
LA 16
M 9,34,4
s_barrier
LC_ 106
M 9,35,5
s_sub_u32 s39, 0, s39
M 10,46,4
WL 3
LO 125,19,125
LU 19,125
M 10,47,5
RA 228,1,3072
HS 33,19
M 11,58,4
LL 17,82
LL 107,83
M 11,59,5
LA 17
v_pk_add_f16 v33, v26, v33
Z 0,72,0
LC_ 107
ds_read_b64 v[14:15], v5 offset:2048
M 0,73,1
s_nop 0
v_subrev_u32 v5, s39, v5
v_pk_add_f16 v26, v32, v33
Z 1,0,0
WL 4
LO 106,16,106
LU 16,106
M 1,1,1
RA 232,1,4096
HZ 27,16
Z 2,12,0
LL 18,84
LL 110,85
M 2,13,1
LA 18
HX 26
Z 3,24,0
LC_ 110
W1 26,2048
M 3,25,1
s_add_u32 s42, s94, s60
Z 4,36,0
WL 5
LO 107,17,107
LU 17,107
M 4,37,1
s_addc_u32 s43, s95, 0
RA 236,1,6144
HS 27,17
Z 5,48,0
LL 19,86
LL 111,87
M 5,49,1
LA 19
SU 104
M 0,74,2
WL 6
LC_ 111
v_add_f32 v14, v14, v15
M 0,75,3
s_nop 0
v_add_f32_dpp v14, v14, v14 quad_perm:[1,0,3,2] row_mask:0xf bank_mask:0xf
M 1,2,2
WL 4
LO 110,18,110
LU 18,110
M 1,3,3
RA 216,1,7168
HZ 29,18
M 2,14,2
LL 16,88
LL 114,89
M 2,15,3
LA 16
v_add_f32_dpp v14, v14, v14 quad_perm:[2,3,0,1] row_mask:0xf bank_mask:0xf
M 3,26,2
LC_ 114
v_add_f32_dpp v14, v14, v14 row_half_mirror row_mask:0xf bank_mask:0xf
M 3,27,3
s_add_u32 s42, s94, s61
v_cvt_f64_f32 v[14:15], v14
M 4,38,2
WL 3
LO 111,19,111
LU 19,111
M 4,39,3
s_addc_u32 s43, s95, 0
RA 220,1,8192
HS 29,19
M 5,50,2
LL 17,90
LL 115,91
M 5,51,3
LA 17
s_mov_b64 exec, s[54:55]
SP
s_mov_b64 exec, -1
M 0,76,4
LC_ 115
SU 108
M 0,77,5
PFW
M 1,4,4
WL 3
LO 114,16,114
LU 16,114
M 1,5,5
s_mov_b32 m0, %[pm]
RA 224,1,9216
HZ 30,16
M 2,16,4
LL 18,92
LL 118,93
M 2,17,5
LA 18
DF
M 3,28,4
LC_ 118
M 3,29,5
s_add_u32 s42, s94, s62
M 4,40,4
WL 3
LO 115,17,115
LU 17,115
M 4,41,5
s_addc_u32 s43, s95, 0
RA 228,1,10240
HS 30,17
M 5,52,4
LL 19,94
LL 119,95
M 5,53,5
LA 19
SU 112
M 0,78,0
LC_ 119
M 0,79,1
M 1,6,0
WL 3
LO 118,18,118
LU 18,118
M 1,7,1
RA 232,1,11264
HZ 31,18
M 2,18,0
LL 16,96
LL 122,97
M 2,19,1
LA 16
RV 252,22,5392
M 3,30,0
LC_ 122
M 3,31,1
s_add_u32 s42, s94, s63
M 4,42,0
WL 4
LO 119,19,119
LU 19,119
M 4,43,1
s_addc_u32 s43, s95, 0
RA 236,1,12288
HS 31,19
M 5,54,0
LL 17,98
LL 123,99
M 5,55,1
LA 17
SU 116
M 0,80,2
LC_ 123
v_pk_add_f16 v31, v30, v31
M 0,81,3
M 1,8,2
WL 3
LO 122,16,122
LU 16,122
M 1,9,3
RA 216,1,13312
HZ 32,16
M 2,20,2
LL 18,100
LL 126,101
M 2,21,3
LA 18
M 3,32,2
LC_ 126
M 3,33,3
s_add_u32 s42, s94, s64
M 4,44,2
WL 3
LO 123,17,123
LU 17,123
M 4,45,3
s_addc_u32 s43, s95, 0
RA 220,1,14336
HS 32,17
M 5,56,2
LL 19,102
LL 127,103
M 5,57,3
LA 19
SU 120
M 0,82,4
LC_ 127
v_pk_add_f16 v32, v29, v32
M 0,83,5
v_pk_add_f16 v32, v31, v32
M 1,10,4
WL 3
LO 126,18,126
LU 18,126
M 1,11,5
RA 224,1,15360
HZ 33,18
M 2,22,4
LL 16,56
LL 104,57
M 2,23,5
LA 16
M 3,34,4
LC_ 104
M 3,35,5
s_add_u32 s42, s94, s65
M 4,46,4
WL 3
LO 127,19,127
LU 19,127
M 4,47,5
s_addc_u32 s43, s95, 0
RA 228,1,16384
HS 33,19
M 5,58,4
LL 17,58
LL 105,59
M 5,59,5
LA 17
SU 124
Z 6,72,0
LC_ 105
v_pk_add_f16 v33, v27, v33
M 6,73,1
v_pk_add_f16 v27, v32, v33
Z 7,0,0
WL 3
LO 104,16,104
LU 16,104
M 7,1,1
RA 232,1,17408
HZ 26,16
Z 8,12,0
WV 15
LL 18,60
LL 108,61
M 8,13,1
s_mul_i32 s53, s50, 25600
LA 18
HX 27
Z 9,24,0
s_add_u32 s40, %[wlo], s53
LC_ 108
W1 27,3072
M 9,25,1
s_addc_u32 s41, %[whi], 0
v_readfirstlane_b32 s72, v11
)"
R"(Z 10,36,0
WL 4
LO 105,17,105
LU 17,105
M 10,37,1
s_mov_b32 m0, s38
RA 236,1,18432
HS 26,17
Z 11,48,0
LL 19,62
LL 109,63
M 11,49,1
LA 19
DW 0
M 6,74,2
LC_ 109
v_mov_b32 v12, s72
M 6,75,3
W2 12,12,16
M 7,2,2
WL 5
LO 108,18,108
LU 18,108
M 7,3,3
s_add_u32 s37, s37, 32768
RA 216,1,19456
HZ 29,18
M 8,14,2
s_cmp_eq_u32 s37, s75
LL 16,64
LL 112,65
M 8,15,3
s_cselect_b32 s37, s74, s37
LA 16
DW 1024
M 9,26,2
LC_ 112
v_add_u32 v4, s39, v4
M 9,27,3
s_mov_b64 exec, 1
AT
s_mov_b64 exec, -1
M 10,38,2
WL 4
LO 109,19,109
LU 19,109
M 10,39,3
RA 220,1,20480
HS 29,19
M 11,50,2
LL 17,66
LL 113,67
M 11,51,3
LA 17
DW 2048
M 6,76,4
LC_ 113
M 6,77,5
M 7,4,4
WL 3
LO 112,16,112
LU 16,112
M 7,5,5
s_mul_i32 s53, s51, 49152
RA 224,1,21504
HZ 30,16
M 8,16,4
s_bitcmp1_b64 s[92:93], s51
LL 18,68
LL 116,69
M 8,17,5
s_cselect_b64 s[94:95], s[88:89], s[86:87]
LA 18
M 9,28,4
s_add_u32 s94, s94, s53
LC_ 116
M 9,29,5
s_addc_u32 s95, s95, 0
M 10,40,4
WL 3
LO 113,17,113
LU 17,113
M 10,41,5
s_add_u32 s51, s51, %[d]
RA 228,1,22528
HS 30,17
M 11,52,4
LL 19,70
LL 117,71
M 11,53,5
s_cmp_eq_u32 s51, %[wrap]
LA 19
M 6,78,0
s_cselect_b32 s51, %[rst], s51
LC_ 117
M 6,79,1
s_add_u32 s40, s40, 3072
M 7,6,0
WL 3
LO 116,18,116
LU 18,116
M 7,7,1
s_addc_u32 s41, s41, 0
RA 232,1,23552
HZ 31,18
M 8,18,0
s_add_u32 m0, s38, 3072
LL 16,72
LL 120,73
M 8,19,1
LA 16
DW 0
M 9,30,0
LC_ 120
DW 1024
M 9,31,1
M 10,42,0
WL 3
LO 117,19,117
LU 19,117
M 10,43,1
RA 236,1,24576
HS 31,19
M 11,54,0
LL 17,74
LL 121,75
M 11,55,1
LA 17
DW 2048
M 6,80,2
LC_ 121
v_pk_add_f16 v31, v30, v31
M 6,81,3
s_lshl_b32 s53, s52, 8
v_add_u32 v1, s37, v0
M 7,8,2
WL 3
LO 120,16,120
LU 16,120
M 7,9,3
s_add_u32 s48, %[alo], s53
RA 216,1,0
HZ 32,16
M 8,20,2
s_addc_u32 s49, %[ahi], 0
LL 18,76
LL 124,77
M 8,21,3
s_add_u32 s52, s52, %[d]
LA 18
v_and_b32 v22, 0xfffffcff, v1
M 9,32,2
s_cmp_eq_u32 s52, %[wrap]
LC_ 124
M 9,33,3
s_cselect_b32 s52, %[rst], s52
DQ 3072
M 10,44,2
WL 3
LO 121,17,121
LU 17,121
M 10,45,3
s_add_u32 s50, s50, %[d]
RA 220,1,1024
HS 32,17
M 11,56,2
LL 19,78
LL 125,79
M 11,57,3
s_cmp_eq_u32 s50, %[wrap]
LA 19
RV 252,22,5120
M 6,82,4
s_cselect_b32 s50, %[rst], s50
LC_ 125
v_pk_add_f16 v32, v29, v32
M 6,83,5
v_pk_add_f16 v32, v31, v32
M 7,10,4
WL 4
LO 124,18,124
LU 18,124
M 7,11,5
s_add_u32 s38, s38, 32768
RA 224,1,2048
HZ 33,18
M 8,22,4
s_cmp_eq_u32 s38, s77
LL 16,80
LL 106,81
M 8,23,5
s_cselect_b32 s38, s76, s38
LA 16
M 9,34,4
s_barrier
LC_ 106
M 9,35,5
s_sub_u32 s39, 0, s39
M 10,46,4
WL 3
LO 125,19,125
LU 19,125
M 10,47,5
RA 228,1,3072
HS 33,19
M 11,58,4
LL 17,82
LL 107,83
M 11,59,5
2:
LA 17
v_pk_add_f16 v33, v26, v33
Z 0,72,0
LC_ 107
ds_read_b32 v20, v5 offset:4096
M 0,73,1
ds_read_b64 v[14:15], v5 offset:2048
v_pk_add_f16 v26, v32, v33
Z 1,0,0
WL 5
LO 106,16,106
LU 16,106
M 1,1,1
RA 232,1,4096
HZ 27,16
Z 2,12,0
LL 18,84
LL 110,85
M 2,13,1
LA 18
v_subrev_u32 v5, s39, v5
Z 3,24,0
LC_ 110
HX 26
M 3,25,1
s_add_u32 s42, s94, s60
W1 26,2048
Z 4,36,0
WL 6
LO 107,17,107
LU 17,107
M 4,37,1
s_addc_u32 s43, s95, 0
RA 236,1,6144
HS 27,17
Z 5,48,0
LL 19,86
LL 111,87
M 5,49,1
LA 19
SU 104
M 0,74,2
WL 6
LC_ 111
v_add_f32 v14, v14, v15
M 0,75,3
s_nop 0
v_add_f32_dpp v14, v14, v14 quad_perm:[1,0,3,2] row_mask:0xf bank_mask:0xf
M 1,2,2
WL 4
LO 110,18,110
LU 18,110
M 1,3,3
RA 216,1,7168
HZ 29,18
M 2,14,2
LL 16,88
LL 114,89
M 2,15,3
LA 16
v_add_f32_dpp v14, v14, v14 quad_perm:[2,3,0,1] row_mask:0xf bank_mask:0xf
M 3,26,2
LC_ 114
v_add_f32_dpp v14, v14, v14 row_half_mirror row_mask:0xf bank_mask:0xf
M 3,27,3
s_add_u32 s42, s94, s61
v_cvt_f64_f32 v[14:15], v14
M 4,38,2
WL 3
LO 111,19,111
LU 19,111
M 4,39,3
s_addc_u32 s43, s95, 0
RA 220,1,8192
HS 29,19
M 5,50,2
LL 17,90
LL 115,91
M 5,51,3
LA 17
s_mov_b64 exec, s[54:55]
SP
s_mov_b64 exec, -1
M 0,76,4
LC_ 115
SU 108
M 0,77,5
v_readfirstlane_b32 s59, v20
M 1,4,4
WL 3
LO 114,16,114
LU 16,114
M 1,5,5
RA 224,1,9216
HZ 30,16
M 2,16,4
LL 18,92
LL 118,93
M 2,17,5
LA 18
M 3,28,4
LC_ 118
M 3,29,5
s_add_u32 s42, s94, s62
M 4,40,4
WL 3
LO 115,17,115
LU 17,115
M 4,41,5
s_addc_u32 s43, s95, 0
RA 228,1,10240
HS 30,17
M 5,52,4
LL 19,94
LL 119,95
M 5,53,5
LA 19
SU 112
M 0,78,0
LC_ 119
M 0,79,1
M 1,6,0
WL 3
LO 118,18,118
LU 18,118
M 1,7,1
RA 232,1,11264
HZ 31,18
M 2,18,0
LL 16,96
LL 122,97
M 2,19,1
LA 16
RV 252,22,5392
M 3,30,0
LC_ 122
M 3,31,1
s_add_u32 s42, s94, s63
M 4,42,0
WL 4
LO 119,19,119
LU 19,119
M 4,43,1
s_addc_u32 s43, s95, 0
RA 236,1,12288
HS 31,19
M 5,54,0
LL 17,98
LL 123,99
M 5,55,1
LA 17
SU 116
M 0,80,2
LC_ 123
v_pk_add_f16 v31, v30, v31
M 0,81,3
M 1,8,2
WL 3
LO 122,16,122
LU 16,122
M 1,9,3
RA 216,1,13312
HZ 32,16
M 2,20,2
LL 18,100
LL 126,101
M 2,21,3
LA 18
M 3,32,2
LC_ 126
M 3,33,3
s_add_u32 s42, s94, s64
M 4,44,2
WL 3
LO 123,17,123
LU 17,123
M 4,45,3
s_addc_u32 s43, s95, 0
RA 220,1,14336
HS 32,17
M 5,56,2
LL 19,102
LL 127,103
M 5,57,3
LA 19
SU 120
M 0,82,4
LC_ 127
v_pk_add_f16 v32, v29, v32
M 0,83,5
)"
R"(v_pk_add_f16 v32, v31, v32
M 1,10,4
WL 3
LO 126,18,126
LU 18,126
M 1,11,5
RA 224,1,15360
HZ 33,18
M 2,22,4
LL 16,56
LL 104,57
M 2,23,5
LA 16
M 3,34,4
LC_ 104
M 3,35,5
s_add_u32 s42, s94, s65
M 4,46,4
WL 3
LO 127,19,127
LU 19,127
M 4,47,5
s_addc_u32 s43, s95, 0
RA 228,1,16384
HS 33,19
M 5,58,4
LL 17,58
LL 105,59
M 5,59,5
LA 17
SU 124
Z 6,72,0
LC_ 105
v_pk_add_f16 v33, v27, v33
M 6,73,1
v_pk_add_f16 v27, v32, v33
Z 7,0,0
WL 3
LO 104,16,104
LU 16,104
M 7,1,1
RA 232,1,17408
HZ 26,16
Z 8,12,0
WV 12
LL 18,60
LL 108,61
M 8,13,1
s_mul_i32 s53, s50, 25600
LA 18
HX 27
Z 9,24,0
s_add_u32 s40, %[wlo], s53
LC_ 108
W1 27,3072
M 9,25,1
s_addc_u32 s41, %[whi], 0
v_readfirstlane_b32 s72, v11
Z 10,36,0
WL 4
LO 105,17,105
LU 17,105
M 10,37,1
s_mov_b32 m0, s38
RA 236,1,18432
HS 26,17
Z 11,48,0
LL 19,62
LL 109,63
M 11,49,1
LA 19
DW 0
M 6,74,2
LC_ 109
v_mov_b32 v12, s72
M 6,75,3
W2 12,12,16
M 7,2,2
WL 5
LO 108,18,108
LU 18,108
M 7,3,3
s_add_u32 s37, s37, 32768
RA 216,1,19456
HZ 29,18
M 8,14,2
s_cmp_eq_u32 s37, s75
LL 16,64
LL 112,65
M 8,15,3
s_cselect_b32 s37, s74, s37
LA 16
DW 1024
M 9,26,2
LC_ 112
v_add_u32 v4, s39, v4
M 9,27,3
s_max_u32 s59, s59, %[nb]
s_mov_b64 exec, 1
AT
s_mov_b64 exec, -1
M 10,38,2
WL 4
LO 109,19,109
LU 19,109
M 10,39,3
s_add_u32 %[nb], %[nb], 1
RA 220,1,20480
HS 29,19
M 11,50,2
LL 17,66
LL 113,67
M 11,51,3
LA 17
DW 2048
M 6,76,4
LC_ 113
M 6,77,5
M 7,4,4
WL 3
LO 112,16,112
LU 16,112
M 7,5,5
s_mul_i32 s53, s51, 49152
RA 224,1,21504
HZ 30,16
M 8,16,4
s_bitcmp1_b64 s[92:93], s51
LL 18,68
LL 116,69
M 8,17,5
s_cselect_b64 s[94:95], s[88:89], s[86:87]
LA 18
M 9,28,4
s_add_u32 s94, s94, s53
LC_ 116
M 9,29,5
s_addc_u32 s95, s95, 0
M 10,40,4
WL 3
LO 113,17,113
LU 17,113
M 10,41,5
s_add_u32 s51, s51, %[d]
RA 228,1,22528
HS 30,17
M 11,52,4
LL 19,70
LL 117,71
M 11,53,5
s_cmp_eq_u32 s51, %[wrap]
LA 19
M 6,78,0
s_cselect_b32 s51, %[rst], s51
LC_ 117
M 6,79,1
s_add_u32 s40, s40, 3072
M 7,6,0
WL 3
LO 116,18,116
LU 18,116
M 7,7,1
s_addc_u32 s41, s41, 0
RA 232,1,23552
HZ 31,18
M 8,18,0
s_add_u32 m0, s38, 3072
LL 16,72
LL 120,73
M 8,19,1
LA 16
DW 0
M 9,30,0
LC_ 120
DW 1024
M 9,31,1
M 10,42,0
WL 3
LO 117,19,117
LU 19,117
M 10,43,1
RA 236,1,24576
HS 31,19
M 11,54,0
LL 17,74
LL 121,75
M 11,55,1
LA 17
DW 2048
M 6,80,2
LC_ 121
v_pk_add_f16 v31, v30, v31
M 6,81,3
s_lshl_b32 s53, s52, 8
v_add_u32 v1, s37, v0
M 7,8,2
WL 3
LO 120,16,120
LU 16,120
M 7,9,3
s_add_u32 s48, %[alo], s53
RA 216,1,0
HZ 32,16
M 8,20,2
s_addc_u32 s49, %[ahi], 0
LL 18,76
LL 124,77
M 8,21,3
s_add_u32 s52, s52, %[d]
LA 18
v_and_b32 v22, 0xfffffcff, v1
M 9,32,2
s_cmp_eq_u32 s52, %[wrap]
LC_ 124
M 9,33,3
s_cselect_b32 s52, %[rst], s52
DQ 3072
M 10,44,2
WL 3
LO 121,17,121
LU 17,121
M 10,45,3
s_add_u32 s50, s50, %[d]
RA 220,1,1024
HS 32,17
M 11,56,2
LL 19,78
LL 125,79
M 11,57,3
s_cmp_eq_u32 s50, %[wrap]
LA 19
RV 252,22,5120
M 6,82,4
s_cselect_b32 s50, %[rst], s50
LC_ 125
v_pk_add_f16 v32, v29, v32
M 6,83,5
v_pk_add_f16 v32, v31, v32
M 7,10,4
WL 4
LO 124,18,124
LU 18,124
M 7,11,5
s_add_u32 s38, s38, 32768
RA 224,1,2048
HZ 33,18
M 8,22,4
s_cmp_eq_u32 s38, s77
LL 16,80
LL 106,81
M 8,23,5
s_cselect_b32 s38, s76, s38
LA 16
M 9,34,4
s_barrier
LC_ 106
M 9,35,5
s_sub_u32 s39, 0, s39
M 10,46,4
WL 3
LO 125,19,125
LU 19,125
M 10,47,5
RA 228,1,3072
HS 33,19
M 11,58,4
s_cmp_lt_u32 s59, 30
LL 17,82
LL 107,83
M 11,59,5
s_cbranch_scc1 2b
LA 17
v_pk_add_f16 v33, v26, v33
LC_ 107
ds_read_b64 v[14:15], v5 offset:2048
v_pk_add_f16 v26, v32, v33
HX 26
v_subrev_u32 v5, s39, v5
W1 26,2048
WL 0
LO 106,16,106
LU 16,106
HZ 27,16
LL 18,84
LL 110,85
LA 18
v_add_f32 v14, v14, v15
s_nop 1
v_add_f32_dpp v14, v14, v14 quad_perm:[1,0,3,2] row_mask:0xf bank_mask:0xf
s_nop 1
v_add_f32_dpp v14, v14, v14 quad_perm:[2,3,0,1] row_mask:0xf bank_mask:0xf
s_nop 1
v_add_f32_dpp v14, v14, v14 row_half_mirror row_mask:0xf bank_mask:0xf
v_cvt_f64_f32 v[14:15], v14
s_mov_b64 exec, s[54:55]
SP
s_mov_b64 exec, -1
LC_ 110
s_add_u32 s42, s94, s60
s_addc_u32 s43, s95, 0
LO 107,17,107
LU 17,107
HS 27,17
SU 104
LL 19,86
LL 111,87
LA 19
LC_ 111
WL 0
LO 110,18,110
LU 18,110
HZ 29,18
LL 16,88
LL 114,89
LA 16
LC_ 114
s_add_u32 s42, s94, s61
s_addc_u32 s43, s95, 0
LO 111,19,111
LU 19,111
HS 29,19
SU 108
LL 17,90
LL 115,91
LA 17
LC_ 115
WL 0
LO 114,16,114
LU 16,114
HZ 30,16
LL 18,92
LL 118,93
LA 18
LC_ 118
s_add_u32 s42, s94, s62
s_addc_u32 s43, s95, 0
LO 115,17,115
LU 17,115
HS 30,17
SU 112
LL 19,94
LL 119,95
LA 19
LC_ 119
WL 0
LO 118,18,118
LU 18,118
HZ 31,18
LL 16,96
LL 122,97
LA 16
LC_ 122
s_add_u32 s42, s94, s63
s_addc_u32 s43, s95, 0
LO 119,19,119
LU 19,119
HS 31,19
SU 116
LL 17,98
LL 123,99
v_pk_add_f16 v31, v30, v31
LA 17
LC_ 123
WL 0
LO 122,16,122
LU 16,122
HZ 32,16
LL 18,100
LL 126,101
LA 18
LC_ 126
s_add_u32 s42, s94, s64
s_addc_u32 s43, s95, 0
LO 123,17,123
LU 17,123
HS 32,17
SU 120
LL 19,102
LL 127,103
v_pk_add_f16 v32, v29, v32
LA 19
v_pk_add_f16 v32, v31, v32
LC_ 127
WL 0
LO 126,18,126
LU 18,126
HZ 33,18
s_add_u32 s42, s94, s65
s_addc_u32 s43, s95, 0
LO 127,19,127
LU 19,127
HS 33,19
SU 124
v_pk_add_f16 v33, v27, v33
v_pk_add_f16 v27, v32, v33
HX 27
W1 27,3072
s_nop 1
v_add_u32 v4, s39, v4
s_lshl_b32 s53, s52, 8
)"
R"(s_add_u32 s48, %[alo], s53
s_addc_u32 s49, %[ahi], 0
s_add_u32 s52, s52, %[d]
s_cmp_eq_u32 s52, %[wrap]
s_cselect_b32 s52, %[rst], s52
WL 0
WV 0
s_barrier
s_sub_u32 s39, 0, s39
ds_read_b64 v[14:15], v5 offset:2048
s_nop 1
v_subrev_u32 v5, s39, v5
WL 0
v_add_f32 v14, v14, v15
s_nop 1
v_add_f32_dpp v14, v14, v14 quad_perm:[1,0,3,2] row_mask:0xf bank_mask:0xf
s_nop 1
v_add_f32_dpp v14, v14, v14 quad_perm:[2,3,0,1] row_mask:0xf bank_mask:0xf
s_nop 1
v_add_f32_dpp v14, v14, v14 row_half_mirror row_mask:0xf bank_mask:0xf
v_cvt_f64_f32 v[14:15], v14
s_mov_b64 exec, s[54:55]
SP
s_mov_b64 exec, -1
GE
s_waitcnt vmcnt(0) lgkmcnt(0)
8:
.purgem M
.purgem Z
.purgem LZ
.purgem LS
.purgem LD
.purgem GZ
.purgem GS
.purgem CL
.purgem CH
.purgem XL
.purgem XN
.purgem F
.purgem U
.purgem E
.purgem A
.purgem R
.purgem C
.purgem S0
.purgem S
.purgem HC
.purgem HF
.purgem HM
.purgem HE
.purgem HG
.purgem HA
.purgem HR
.purgem HQ
.purgem HP
.purgem HY
.purgem HK
.purgem LQ
.purgem LL
.purgem LH
.purgem LA
.purgem LC_
.purgem LO
.purgem HZ
.purgem HS
.purgem HT
.purgem HX
.purgem UX
.purgem EX
.purgem AX
.purgem RX
.purgem TX
.purgem SX
.purgem AW
.purgem RA
.purgem RV
.purgem W2
.purgem W1
.purgem DW
.purgem DF
.purgem DX
.purgem ST
.purgem SU
.purgem SP
.purgem AT
.purgem PFA
.purgem PFN
.purgem PFW
.purgem PF0
.purgem WL
.purgem WV
.purgem LU
.purgem CF
.purgem XS
.purgem AR
.purgem DQ
.purgem GE
)"
      : [nb] "=&s"(nb)
      : [xlo] "s"((unsigned)xp), [xhi] "s"((unsigned)(xp >> 32)), [hlo] "s"((unsigned)hp), [hhi] "s"((unsigned)(hp >> 32)),
        [glo] "s"((unsigned)gp), [ghi] "s"((unsigned)(gp >> 32)), [wlo] "s"((unsigned)wp), [whi] "s"((unsigned)(wp >> 32)),
        [w] "s"(w), [lds] "s"(lds0), [d] "s"(d), [wrap] "s"(wrap), [rst] "s"(rst), [pos0] "s"(pos0), [xo0] "s"(xo0), [xs1] "s"(xs1), [sb1] "s"(sb1), [r0o] "s"(r0o), [tsel] "s"(tsel), [s2] "s"(s2),
        [alo] "s"((unsigned)ap), [ahi] "s"((unsigned)(ap >> 32)), [ca0] "s"(ca0), [pm] "s"(pm), [pf] "s"(pf)
      : K1_CLOB);
  TS(6);
  MK_END(1);
}

typedef __bf16 bf16x8_t __attribute__((ext_vector_type(8)));
DI f32x4 mfma16(u32x4 a, u32x4 b, f32x4 c) {
  return __builtin_amdgcn_mfma_f32_16x16x32_bf16(__builtin_bit_cast(bf16x8_t, a), __builtin_bit_cast(bf16x8_t, b), c, 0, 0, 0);
}
DI float grn_gx(double s, float scale) { return round_bf16(__builtin_amdgcn_sqrtf(round_bf16(round_bf16((float)s * scale) + 1e-6f))); }
DI void slow_fc1(const u16* __restrict__ x, const u16* __restrict__ dw, char* __restrict__ hws, float* sacc, int w) {
  const int lane = __builtin_amdgcn_mbcnt_hi(~0u, __builtin_amdgcn_mbcnt_lo(~0u, 0u)), m = lane & 15, g = lane >> 4;
  for (int i = lane; i < HID; i += 64) sacc[i] = 0.f;
#pragma unroll 1
  for (int it = 0; it < 9; ++it) {
    const int sub = blockIdx.x * 36 + it * 4 + w;
    const unsigned px = sub_pixel(x, sub, m);
    u32x4 A[12];
    float sum = 0.f;
    const u16* dq = dw;
    asm volatile("" : "+s"(dq));
#pragma unroll
    for (int ks = 0; ks < 12; ++ks) {
      const u32x4 v = *(const u32x4*)(x + (size_t)px * CH + 32 * ks + 8 * g);
      const u32x4 d = *(const u32x4*)(dq + 32 * ks + 8 * g);
#pragma unroll
      for (int j = 0; j < 4; ++j) {
        A[ks][j] = pack_bf16(bf_lo(v[j]) * bf_lo(d[j]), bf_hi(v[j]) * bf_hi(d[j]));
        sum += bf_lo(A[ks][j]) + bf_hi(A[ks][j]);
      }
      asm volatile("" : "+v"(A[ks]));
    }
    sum += __shfl_xor(sum, 16); sum += __shfl_xor(sum, 32);
    const float mean = sum * (1.0f / CH);
    float var = 0.f;
#pragma unroll
    for (int ks = 0; ks < 12; ++ks) {
#pragma unroll
      for (int j = 0; j < 4; ++j) {
        const float a = bf_lo(A[ks][j]) - mean, b = bf_hi(A[ks][j]) - mean;
        var += a * a + b * b;
      }
      asm volatile("" : "+v"(A[ks]));
    }
    var += __shfl_xor(var, 16); var += __shfl_xor(var, 32);
    const float mb = round_bf16(mean);
    const float rd = 1.0f / round_bf16(sqrtf(round_bf16(round_bf16(var * (1.0f / CH)) + 1e-5f)));
#pragma unroll
    for (int ks = 0; ks < 12; ++ks)
#pragma unroll
      for (int j = 0; j < 4; ++j) A[ks][j] = pack_bf16((bf_lo(A[ks][j]) - mb) * rd, (bf_hi(A[ks][j]) - mb) * rd);
#pragma unroll 1
    for (int tc = 0; tc < 96; ++tc) {
      f32x4 acc = {0.f, 0.f, 0.f, 0.f};
#pragma unroll
      for (int ks = 0; ks < 12; ++ks) acc = mfma16(A[ks], *(const u32x4*)(g_buf + (size_t)(tc * 12 + ks) * 1024 + lane * 16), acc);
      float q = 0.f;
#pragma unroll
      for (int r = 0; r < 4; ++r) { acc[r] = gelu_tanh(acc[r]); q = __builtin_fmaf(acc[r], acc[r], q); }
      typedef unsigned u32x2_ __attribute__((ext_vector_type(2)));
      *(u32x2_*)(hws + h_piece(hws, sub, tc >> 1) + g * 256 + ((m + 4 * g) & 15) * 16 + (tc & 1) * 8) = (u32x2_){pack_bf16(acc[0], acc[1]), pack_bf16(acc[2], acc[3])};
      q += __shfl_xor(q, 16); q += __shfl_xor(q, 32);
      if (g == 0) sacc[tc * 16 + m] += q;
    }
  }
  __builtin_amdgcn_wave_barrier();
  __builtin_amdgcn_fence(__ATOMIC_ACQ_REL, "workgroup");
  for (int i = lane; i < HID; i += 64) atomicAdd((double*)(g_buf + G_ACC2) + i, (double)sacc[i]);
}
DI void slow_bar(unsigned* c, int w) {
  const int lane = __builtin_amdgcn_mbcnt_hi(~0u, __builtin_amdgcn_mbcnt_lo(~0u, 0u));
  asm volatile("s_waitcnt vmcnt(0)" ::: "memory");
  __syncthreads();
  if (w == 0 && lane == 0) {
    __threadfence();
    asm volatile("s_waitcnt vmcnt(0)" ::: "memory");
    __hip_atomic_fetch_add(c, 1u, __ATOMIC_RELAXED, __HIP_MEMORY_SCOPE_AGENT);
    for (int i = 0; i < (1 << 22) && __hip_atomic_load(c, __ATOMIC_RELAXED, __HIP_MEMORY_SCOPE_AGENT) < (unsigned)NWG; ++i) __builtin_amdgcn_s_sleep(32);
    __threadfence();
    asm volatile("s_waitcnt vmcnt(0)" ::: "memory");
  }
  __syncthreads();
}

#define K2_CLOB "memory", "vcc", "scc", "s48", "s49", "s50", "s51", "s52", "s53", "s54", "s55", "s56", "s57", "s58", "s59", "s60", "s61", "s62", "s63", "s64", "s65", "s66", "s67", \
  "s68", "s69", "s70", "s71", "s72", "s73", "s74", "s75", "s76", "s77", "s78", "s79", "s80", "s81", "s84", "s85", "s86", "s87", "s88", "s89", "s90", "s91", "s92", "s93", "s94", "s95", "s96", "s97", "s98", "s99", "s38", "s39", "s40", "s41", "s42", "s43", "s44", "s45", "s46", "s47", VA_CLOB
DI void slow_fc2(const u16* __restrict__ x, const char* __restrict__ hws, u16* __restrict__ out, const u16* __restrict__ grn_g, const u16* __restrict__ w2,
                 char* smem, bool hbf, int w) {
  float* sc = (float*)smem;
  float* red = sc + HID;
  u16* wf = (u16*)(smem + 8192);
  const int lane = __builtin_amdgcn_mbcnt_hi(~0u, __builtin_amdgcn_mbcnt_lo(~0u, 0u)), tid = w * 64 + lane, m = lane & 15, g = lane >> 4;
  __syncthreads();
  float part = 0.f;
  for (int c = tid; c < HID; c += 256) {
    double s = 0.0;
    if (hbf) s = ((const double*)(g_buf + G_ACC2))[c];
    else for (int r = 0; r < (int)K1_NREP; ++r) s += ((const double*)(g_buf + K1_G_ACC + r * 12288))[c];
    const float gx = grn_gx(s, hbf ? 1.0f : g_sc[2]);
    sc[c] = gx; part += gx;
  }
#pragma unroll
  for (int o = 32; o > 0; o >>= 1) part += __shfl_xor(part, o);
  if (lane == 0) red[tid >> 6] = part;
  __syncthreads();
  const float gmean = round_bf16(round_bf16(((red[0] + red[1]) + (red[2] + red[3])) * (1.0f / HID)) + 1e-6f);
  for (int c = tid; c < HID; c += 256) sc[c] = (1.0f + ld_bf16(grn_g + c) * round_bf16(sc[c] / gmean)) ;
  const float hs = g_sc[3];
#pragma unroll 1
  for (int it = 0; it < 9; ++it) {
    const int sub = blockIdx.x * 36 + it * 4 + (tid >> 6);
    f32x4 acc[24];
#pragma unroll
    for (int i = 0; i < 24; ++i) acc[i] = (f32x4){0.f, 0.f, 0.f, 0.f};
#pragma unroll 1
    for (int t = 0; t < 48; ++t) {
      __syncthreads();
      for (int e = tid; e < 32 * CH; e += 256) {
        const int k = e / CH, n = e % CH;
        wf[((n >> 4) * 64 + (k >> 3) * 16 + (n & 15)) * 8 + (k & 7)] = (u16)pack_bf16(sc[32 * t + k] * ld_bf16(w2 + (size_t)(32 * t + k) * CH + n), 0.f);
      }
      __syncthreads();
      const char* hp = hws + h_piece(hws, sub, t) + (m >> 2) * 256 + (g >> 1) * 8 + 2 * (m & 3);
      u32x4 A;
#pragma unroll
      for (int j = 0; j < 4; ++j) {
        unsigned v[2];
#pragma unroll
        for (int k = 0; k < 2; ++k) {
          v[k] = *(const u16*)(hp + ((8 * (g & 1) + 2 * j + k + (m & 12)) & 15) * 16);
          if (!hbf) v[k] = pack_bf16(hs * (float)__builtin_bit_cast(_Float16, (u16)v[k]), 0.f) & 0xffffu;
        }
        A[j] = v[0] | (v[1] << 16);
      }
#pragma unroll
      for (int i = 0; i < 24; ++i) acc[i] = mfma16(A, *(const u32x4*)(wf + (i * 64 + lane) * 8), acc[i]);
    }
#pragma unroll
    for (int i = 0; i < 24; ++i) {
      const int n = 16 * i + m, mm = n % 192;
      const f32x4 b4 = *(const f32x4*)(g_bias + (((n / 192) * 12 + 4 * (mm >> 6) + (mm & 3)) * 16 + ((mm & 63) >> 2)) * 4);
      const float bias = (b4[0] + b4[1]) + (b4[2] + b4[3]);
#pragma unroll
      for (int r = 0; r < 4; ++r) {
        const size_t o = (size_t)(sub_pixel(x, sub, 4 * g) + r) * CH + n;
        out[o] = (u16)pack_bf16(acc[i][r] + bias + ld_bf16(x + o), 0.f);
      }
    }
  }
}
__global__ __launch_bounds__(256, 1) void k2_fc2(const u16* __restrict__ x, char* __restrict__ hws, u16* __restrict__ out,
                                                 const u16* __restrict__ grn_g, const u16* __restrict__ w2, const u16* __restrict__ dw,
                                                 const u16* __restrict__ w1) {
  extern __shared__ __attribute__((aligned(16))) char smem[];
  const int w = __builtin_amdgcn_readfirstlane(threadIdx.x >> 6);
  const unsigned pg = w & 1, nh = w >> 1;
  const unsigned lds0 = lds_addr(smem);
  unsigned cl = blockIdx.x & 7;
  asm volatile("" : "+s"(cl));
  MK_START(4); TS(9);
  const uint64_t pp = uni64((uint64_t)g_prm + (blockIdx.x * 4 + w) * 256), ap = (uint64_t)g_buf + K1_G_ACC + w * 1024;
  const unsigned wm = lds0 + w * 6144, ab = lds0 + nh * 12288;
  const unsigned hb = lds0 + K2_HB + pg * 2 * K2_NU * 1024, hm = hb + nh * K2_NU * 1024;
  const unsigned bm = lds0 + K2_BIAS_OFF + w * 1536, bb = lds0 + K2_BIAS_OFF + nh * 3072;
  const unsigned pm = lds0 + K2_PF_OFF, sb = lds0 + K2_S16_OFF, sw = sb + w * 256;
  const uint64_t tb = uni64((uint64_t)g_buf + G_STMP), ta = uni64(tb + cl * 128);
  const unsigned em = __builtin_amdgcn_readfirstlane(w == 0);
  uint64_t flg;
#define K2_OPS [pp] "s"(pp), [plo] "s"((unsigned)pp), [phi] "s"((unsigned)(pp >> 32)), [cl] "s"(cl), [tb] "s"(tb), [ta] "s"(ta), [em] "s"(em), [alo] "s"((unsigned)ap), [ahi] "s"((unsigned)(ap >> 32)), [wm] "s"(wm), [hm] "s"(hm), [bm] "s"(bm), [ab] "s"(ab), [hb] "s"(hb), [bb] "s"(bb), \
         [sb] "s"(sb), [sw] "s"(sw), [pm] "s"(pm)
  asm volatile(
R"(.macro MA t,a,b
v_mfma_f32_16x16x32_f16 a[\t:\t+3], a[\b:\b+3], a[\a:\a+3], a[\t:\t+3]
.endm
.macro MV t,a,b
v_mfma_f32_16x16x32_f16 v[\t:\t+3], a[\b:\b+3], a[\a:\a+3], v[\t:\t+3]
.endm
.macro AN d
v_and_b32 v[\d], s39, v[\d]
.endm
.macro AW a,s
v_accvgpr_write_b32 a[\a], v[\s]
.endm
.macro AR d,a
v_accvgpr_read_b32 v[\d], a[\a]
.endm
.macro UL d,x
v_lshlrev_b32 v[\d], 16, v[\x]
.endm
.macro UH d,x
v_and_b32 v[\d], s38, v[\x]
.endm
.macro AD d,b
v_fmac_f32 v[\d], s53, v[\b]
.endm
.macro TR d,o
ds_read_b64_tr_b16 a[\d:\d+1], v2 offset:\o
.endm
.macro TQ d,o
ds_read_b64_tr_b16 a[\d:\d+1], v54 offset:\o
.endm
.macro RA d,o
ds_read_b128 a[\d:\d+3], v1 offset:\o
.endm
.macro RC d,o
ds_read_b128 a[\d:\d+3], v5 offset:\o
.endm
.macro BV d,o
ds_read_b128 v[\d:\d+3], v4 offset:\o
.endm
.macro BA d,o
ds_read_b128 a[\d:\d+3], v4 offset:\o
.endm
.macro PA a
s_mov_b32 m0, %[pm]
s_getpc_b64 s[46:47]
s_andn2_b32 s46, s46, 0x7f
s_add_u32 s46, s46, s81
s_addc_u32 s47, s47, 0
s_add_u32 s46, s46, \a
s_addc_u32 s47, s47, 0
.endm
.macro DF
global_load_lds_dwordx4 v0, s[46:47]
.endm
.macro DW o
global_load_lds_dwordx4 v0, s[40:41] offset:\o sc1
.endm
.macro DH o
global_load_lds_dwordx4 v0, s[40:41] offset:\o nt
.endm
.macro DS o
global_load_lds_dwordx4 v0, s[40:41] offset:\o
.endm
.macro XL d,o
global_load_dwordx2 v[\d:\d+1], v52, s[44:45] offset:\o
.endm
.macro ST d,o
global_store_dwordx2 v53, v[\d:\d+1], s[42:43] offset:\o nt sc1
.endm
.macro SU d,o
global_store_dwordx2 v53, v[\d:\d+1], s[42:43] offset:\o
.endm
.macro SA d,o
global_store_dwordx2 v53, a[\d:\d+1], s[42:43] offset:\o nt sc1
.endm
.macro CV d,a,b
v_cvt_pk_bf16_f32 v[\d], v[\a], v[\b]
.endm
.macro DR o
global_load_lds_dwordx4 v0, s[40:41] offset:\o
.endm
.macro TY d,o
ds_read_b64_tr_b16 v[\d:\d+1], v2 offset:\o
.endm
.macro TZ d,o
ds_read_b64_tr_b16 v[\d:\d+1], v54 offset:\o
.endm
.macro RS d,o
ds_read_b128 v[\d:\d+3], v46 offset:\o
.endm
.macro PM d,s
v_pk_mul_f16 v[\d], v[\d], v[\s]
.endm
.macro FS
global_store_dword v0, v10, s[96:97] sc1
.endm
.macro FL
global_load_dword v6, v7, s[98:99] sc1
.endm
.macro FB
s_cbranch_vccnz 21b
22:
.endm
.macro FG
s_or_b64 s[54:55], s[44:45], vcc
s_cbranch_scc1 23b
24:
.endm
.macro FX
s_branch 25f
21:
s_sleep 4
global_load_dword v6, v7, s[98:99] sc1
s_waitcnt vmcnt(0)
v_cmp_ne_u32 vcc, 1, v6
s_sub_u32 s80, s80, 1
s_cbranch_scc1 22f
s_nop 2
s_cbranch_vccnz 21b
s_branch 22f
23:
s_cmp_eq_u64 s[44:45], 0
s_cselect_b32 s54, 4, 0
v_mov_b32 v11, s54
v_mov_b32 v10, 1
s_mov_b64 exec, 1
global_store_dword v11, v10, s[78:79] offset:384 sc1
s_mov_b64 exec, -1
s_nop 4
s_branch 24f
25:
.endm
.macro PB
s_cbranch_vccnz 31f
32:
.endm
.macro PQ a
s_mov_b32 m0, %[pm]
s_add_u32 s46, s100, s81
s_addc_u32 s47, s101, 0
s_add_u32 s46, s46, \a
s_addc_u32 s47, s47, 0
.endm
.macro PJ
s_getpc_b64 s[100:101]
38:
s_add_u32 s92, s92, 39f-38b
s_add_u32 s100, s100, s92
s_addc_u32 s101, s101, 0
.endm
.macro WL n
s_waitcnt lgkmcnt(\n)
.endm
.macro WV n
s_waitcnt vmcnt(\n)
.endm
.macro EF d
v_fma_f32 v[\d], v[\d], s39, v55
.endm
.macro M n
.if \n < 44
v_mfma_f32_16x16x32_f16 a[72+4*(\n/2):72+4*(\n/2)+3], a[24+8*((\n/2)%%6)+4*(\n&1):24+8*((\n/2)%%6)+4*(\n&1)+3], a[8*((\n/12)%%3)+4*(\n&1):8*((\n/12)%%3)+4*(\n&1)+3], a[72+4*(\n/2):72+4*(\n/2)+3]
.else
v_mfma_f32_16x16x32_f16 v[-32+4*(\n/2):-32+4*(\n/2)+3], a[24+8*((\n/2)%%6)+4*(\n&1):24+8*((\n/2)%%6)+4*(\n&1)+3], a[8*((\n/12)%%3)+4*(\n&1):8*((\n/12)%%3)+4*(\n&1)+3], v[-32+4*(\n/2):-32+4*(\n/2)+3]
.endif
.endm
.macro R1 n
.if \n < 64
RA 8*(((\n%%24)/2)%%3)+4*(\n&1),1024*\n
.else
RC 8*(((\n%%24)/2)%%3)+4*(\n&1),1024*\n-49152
.endif
.endm
.macro R n
R1 \n
R1 (\n+1)
.endm
.macro T r,u
TR \r,1024*\u
TQ \r+2,1024*\u
.endm
.macro mw k
s_add_u32 m0, %[wm], 1024*\k
.endm
.macro mh k
s_add_u32 m0, %[hm], 1024*\k
.endm
.macro ad r,k
s_add_u32 s40, s[\r], 1024*\k
s_addc_u32 s41, s[\r+1], 0
.endm
.macro ae d,r,k
s_add_u32 s[\d], s[\r], 1024*\k
s_addc_u32 s[\d+1], s[\r+1], 0
.endm
.macro bc m,b,d,r
s_bitcmp1_b32 s[\m], \b
s_cselect_b64 s[\d:\d+1], s[\r+2:\r+3], s[\r:\r+1]
.endm
.macro W k
DW 1024*\k
.endm
.macro H k
DH 1024*\k
.endm
k2_code:
s_load_dwordx16 s[48:63], %[pp], 0x0
s_load_dwordx16 s[64:79], %[pp], 0x40
s_load_dwordx16 s[84:99], %[pp], 0x80
v_mbcnt_lo_u32_b32 v7, -1, 0
v_mbcnt_hi_u32_b32 v7, -1, v7
v_lshlrev_b32 v0, 4, v7
v_add_u32 v204, 0x1000, v0
v_add_u32 v205, 0x2000, v0
s_mov_b32 s44, %[alo]
s_mov_b32 s45, %[ahi]
global_load_dwordx4 v[56:59], v0, s[44:45]
global_load_dwordx4 v[60:63], v204, s[44:45]
global_load_dwordx4 v[72:75], v205, s[44:45]
ae 44,44,12
global_load_dwordx4 v[76:79], v0, s[44:45]
global_load_dwordx4 v[96:99], v204, s[44:45]
global_load_dwordx4 v[100:103], v205, s[44:45]
s_waitcnt lgkmcnt(0)
bc 49,0,66,60
mh 0
ad 66,0
H 0
H 1
ae 44,44,12
global_load_dwordx4 v[120:123], v0, s[44:45]
global_load_dwordx4 v[124:127], v204, s[44:45]
global_load_dwordx4 v[144:147], v205, s[44:45]
ae 44,44,12
global_load_dwordx4 v[148:151], v0, s[44:45]
global_load_dwordx4 v[152:155], v204, s[44:45]
global_load_dwordx4 v[156:159], v205, s[44:45]
v_and_b32 v8, 15, v7
v_lshrrev_b32 v9, 4, v7
global_load_dwordx4 v[224:227], v0, s[94:95]
global_load_dwordx4 v[228:231], v0, s[94:95] offset:512
v_lshlrev_b32 v10, 2, v7
global_load_dword v168, v10, s[92:93] offset:0
global_load_dword v169, v10, s[92:93] offset:1024
global_load_dword v170, v10, s[92:93] offset:2048
global_load_dwordx4 v[80:83], v0, s[88:89]
s_mov_b64 exec, 0xff
global_load_dwordx4 v[84:87], v0, s[88:89] offset:1024
s_mov_b64 exec, -1
FX
v_add_u32 v1, %[ab], v0
v_add_u32 v5, 0xc000, v1
v_and_b32 v10, 3, v7
v_lshrrev_b32 v11, 2, v7
v_lshl_add_u32 v11, v10, 2, v11
v_and_b32 v11, 15, v11
v_lshlrev_b32 v11, 4, v11
mw 0
ad 86,0
DR 0
v_lshl_add_u32 v11, v10, 8, v11
v_add_u32 v2, %[hb], v11
v_lshrrev_b32 v10, 1, v7
v_and_b32 v10, 8, v10
v_add_u32 v2, v10, v2
v_xor_b32 v54, 8, v2
v_lshlrev_b32 v4, 4, v8
DR 1024
v_add_u32 v4, %[bb], v4
s_mov_b32 s38, 0xffff0000
s_mov_b32 s80, 0x40000
s_mov_b32 s81, s52
s_mov_b32 s39, s54
v_mov_b32 v55, s54
v_mul_f32 v55, 0xb5400000, v55
v_lshl_add_u32 v46, v9, 4, %[sb]
v_add_u32 v46, 53248, v46
v_bfe_u32 v10, v7, 1, 2
v_bfe_u32 v11, v7, 3, 1
v_bfe_u32 v12, v7, 1, 1
DR 2048
v_xor_b32 v11, v11, v12
v_and_b32 v12, 1, v7
v_lshl_add_u32 v11, v11, 1, v12
v_lshl_add_u32 v10, v10, 2, v11
v_lshl_add_u32 v10, v9, 4, v10
v_lshl_add_u32 v206, v10, 2, %[sw]
v_lshl_add_u32 v207, v7, 2, %[sw]
mw 3
ad 86,3
DR 0
v_lshrrev_b32 v13, 4, v0
s_movk_i32 s42, 1366
v_add_u32 v128, s51, v13
v_lshrrev_b32 v11, 6, v128
v_mul_u32_u24 v12, s42, v11
v_lshrrev_b32 v12, 16, v12
v_and_b32 v11, 1, v11
v_lshl_add_u32 v11, v12, 1, v11
v_bfe_u32 v128, v128, 4, 2
DR 1024
v_lshl_add_u32 v128, v11, 2, v128
v_lshl_add_u32 v128, v128, 4, %[sb]
v_cmp_gt_u32 vcc, 16, v11
v_add_u32 v12, 53248, v128
s_nop 0
v_cndmask_b32 v128, v128, v12, vcc
v_add_u32 v129, s51, v13
v_add_u32 v129, 64, v129
v_lshrrev_b32 v11, 6, v129
v_mul_u32_u24 v12, s42, v11
DR 2048
v_lshrrev_b32 v12, 16, v12
v_and_b32 v11, 1, v11
v_lshl_add_u32 v11, v12, 1, v11
v_bfe_u32 v129, v129, 4, 2
v_lshl_add_u32 v129, v11, 2, v129
v_lshl_add_u32 v129, v129, 4, %[sb]
v_cmp_gt_u32 vcc, 16, v11
v_add_u32 v12, 53248, v129
s_nop 0
v_cndmask_b32 v129, v129, v12, vcc
bc 49,1,66,60
mh 2
ad 66,96
H 0
WV 13
v_add_f32 v224, v224, v225
v_add_f32 v226, v226, v227
WV 12
v_add_f32 v228, v228, v229
v_add_f32 v230, v230, v231
v_add_u32 v240, %[bm], v0
v_add_f32 v224, v224, v226
v_add_f32 v228, v228, v230
H 1
v_sub_f32 v224, v224, v55
v_sub_f32 v228, v228, v55
v_mul_f32 v224, s53, v224
v_mul_f32 v228, s53, v228
v_mov_b32 v225, v224
v_mov_b32 v229, v228
v_mov_b32 v226, v224
bc 48,1,64,56
mh 4
ad 64,96
H 0
v_mov_b32 v230, v228
v_mov_b32 v227, v224
v_mov_b32 v231, v228
ds_write_b128 v240, v[224:227] offset:0
ds_write_b128 v240, v[228:231] offset:512
v_mov_b32 v219, 0x7f800000
s_mov_b32 s44, 0x44100000
v_add_f64 v[56:57], v[56:57], v[76:77]
v_add_f64 v[60:61], v[60:61], v[96:97]
v_add_f64 v[72:73], v[72:73], v[100:101]
v_add_f64 v[58:59], v[58:59], v[78:79]
v_add_f64 v[62:63], v[62:63], v[98:99]
H 1
v_add_f64 v[74:75], v[74:75], v[102:103]
v_add_f64 v[56:57], v[56:57], v[120:121]
v_add_f64 v[60:61], v[60:61], v[124:125]
v_add_f64 v[72:73], v[72:73], v[144:145]
v_add_f64 v[58:59], v[58:59], v[122:123]
v_add_f64 v[62:63], v[62:63], v[126:127]
v_add_f64 v[74:75], v[74:75], v[146:147]
mw 24
ad 86,24
DR 0
v_add_f64 v[56:57], v[56:57], v[148:149]
v_add_f64 v[60:61], v[60:61], v[152:153]
v_add_f64 v[72:73], v[72:73], v[156:157]
v_add_f64 v[58:59], v[58:59], v[150:151]
v_add_f64 v[62:63], v[62:63], v[154:155]
v_add_f64 v[74:75], v[74:75], v[158:159]
v_cvt_f32_f64 v172, v[56:57]
v_cvt_f32_f64 v176, v[60:61]
DR 1024
v_cvt_f32_f64 v180, v[72:73]
v_cvt_f32_f64 v173, v[58:59]
v_cvt_f32_f64 v177, v[62:63]
v_cvt_f32_f64 v181, v[74:75]
v_mul_f32 v174, s55, v172
v_mul_f32 v178, s55, v176
v_mul_f32 v182, s55, v180
DR 2048
v_mul_f32 v175, s55, v173
v_mul_f32 v179, s55, v177
v_mul_f32 v183, s55, v181
CV 175,174,175
CV 179,178,179
CV 183,182,183
UL 174,175
mw 27
ad 86,27
DR 0
UL 178,179
UL 182,183
UH 175,175
)"
R"(UH 179,179
UH 183,183
v_add_f32 v174, 0x358637bd, v174
v_add_f32 v178, 0x358637bd, v178
DR 1024
v_add_f32 v182, 0x358637bd, v182
v_add_f32 v175, 0x358637bd, v175
v_add_f32 v179, 0x358637bd, v179
v_add_f32 v183, 0x358637bd, v183
CV 175,174,175
CV 179,178,179
CV 183,182,183
DR 2048
UL 174,175
UL 178,179
UL 182,183
UH 175,175
UH 179,179
UH 183,183
v_sqrt_f32 v174, v174
bc 49,2,66,60
mh 6
ad 66,192
H 0
v_sqrt_f32 v178, v178
v_sqrt_f32 v182, v182
v_sqrt_f32 v175, v175
v_sqrt_f32 v179, v179
v_sqrt_f32 v183, v183
CV 175,174,175
CV 179,178,179
H 1
CV 183,182,183
UL 174,175
UL 178,179
UL 182,183
UH 175,175
UH 179,179
UH 183,183
bc 48,2,64,56
mh 8
ad 64,192
H 0
v_cmp_le_f32 vcc, s44, v172
v_cmp_le_f32 s[54:55], s44, v173
s_nop 0
v_cndmask_b32 v172, v219, v174, vcc
v_cndmask_b32 v173, v219, v175, s[54:55]
v_add_f32 v172, v172, v173
v_cmp_le_f32 vcc, s44, v176
v_cmp_le_f32 s[54:55], s44, v177
s_nop 0
v_cndmask_b32 v176, v219, v178, vcc
v_cndmask_b32 v177, v219, v179, s[54:55]
v_add_f32 v176, v176, v177
v_cmp_le_f32 vcc, s44, v180
v_cmp_le_f32 s[54:55], s44, v181
s_nop 0
v_cndmask_b32 v180, v219, v182, vcc
v_cndmask_b32 v181, v219, v183, s[54:55]
v_add_f32 v180, v180, v181
v_add_f32 v216, v172, v176
H 1
v_add_f32 v216, v216, v180
s_nop 1
v_add_f32_dpp v216, v216, v216 row_shr:1 row_mask:0xf bank_mask:0xf
s_nop 1
v_add_f32_dpp v216, v216, v216 row_shr:2 row_mask:0xf bank_mask:0xf
s_nop 1
v_add_f32_dpp v216, v216, v216 row_shr:4 row_mask:0xf bank_mask:0xf
s_nop 1
v_add_f32_dpp v216, v216, v216 row_shr:8 row_mask:0xf bank_mask:0xf
s_nop 1
v_add_f32_dpp v216, v216, v216 row_bcast:15 row_mask:0xa bank_mask:0xf
s_nop 1
v_add_f32_dpp v216, v216, v216 row_bcast:31 row_mask:0xc bank_mask:0xf
ds_write_b32 v207, v216 offset:52224
v_mov_b32 v10, %[sb]
WL 0
s_barrier
ds_read_b32 v220, v10 offset:52476
ds_read_b32 v221, v10 offset:52732
ds_read_b32 v222, v10 offset:52988
ds_read_b32 v223, v10 offset:53244
BA 72,0
BA 76,0
BA 96,256
BA 100,256
BA 120,512
BA 124,512
BA 144,768
BA 148,768
BV 64,1024
BV 68,1024
BV 88,1280
WL 9
BV 92,1280
BV 112,1536
BV 116,1536
BV 136,1792
BV 140,1792
BV 160,2048
WL 9
BV 164,2048
BV 184,2304
BV 188,2304
BV 208,2560
BV 212,2560
BV 232,2816
WL 9
BV 236,2816
WV 24
UL 192,168
WV 23
UL 196,169
WV 22
UL 200,170
UH 193,168
UH 197,169
UH 201,170
v_mov_b32 v217, 0
H 2
v_add_f32 v220, v220, v221
v_add_f32 v222, v222, v223
v_add_f32 v220, v220, v222
v_mul_f32 v218, 0x3a2aaaab, v220
v_cmp_nlt_f32 s[44:45], v220, v219
CV 218,218,218
UL 218,218
v_add_f32 v218, 0x358637bd, v218
H 3
CV 218,218,218
UL 218,218
v_rcp_f32 v218, v218
s_nop 0
v_mul_f32 v174, v218, v174
v_mul_f32 v178, v218, v178
v_mul_f32 v182, v218, v182
v_mul_f32 v175, v218, v175
v_mul_f32 v179, v218, v179
mw 48
ad 86,48
DR 0
v_mul_f32 v183, v218, v183
CV 175,174,175
CV 179,178,179
CV 183,182,183
UL 174,175
UL 178,179
UL 182,183
DR 1024
UH 175,175
UH 179,179
UH 183,183
v_fma_f32 v174, v192, v174, 1.0
v_fma_f32 v178, v196, v178, 1.0
v_fma_f32 v182, v200, v182, 1.0
v_fma_f32 v175, v193, v175, 1.0
DR 2048
v_fma_f32 v179, v197, v179, 1.0
v_fma_f32 v183, v201, v183, 1.0
v_cvt_pk_f16_f32 v192, v174, v175
v_cvt_pk_f16_f32 v196, v178, v179
v_cvt_pk_f16_f32 v200, v182, v183
ds_write_b32 v206, v192 offset:53248
ds_write_b32 v206, v196 offset:1024
ds_write_b32 v206, v200 offset:2048
v_max3_f32 v217, |v174|, |v175|, v217
v_max3_f32 v217, |v178|, |v179|, v217
v_max3_f32 v217, |v182|, |v183|, v217
mw 51
ad 86,51
DR 0
v_cmp_lt_f32 vcc, 0x43000000, v217
WL 0
WV 18
s_barrier
RS 14,0
RS 18,64
TY 22,0
TZ 24,0
TY 26,14336
TZ 28,14336
TY 30,1024
TZ 32,1024
TY 34,15360
TZ 36,15360
R 0
R 2
R1 4
WL 9
R1 5
ds_read_b128 v[104:107], v128
ds_read_b128 v[108:111], v129
FG
PM 22,14
PM 23,15
PM 24,16
PM 25,17
PM 26,18
PM 27,19
PM 28,20
DR 1024
PM 29,21
AW 24,22
AW 25,23
AW 26,24
AW 27,25
AW 28,26
AW 29,27
DR 2048
AW 30,28
AW 31,29
WL 8
PM 30,14
PM 31,15
PM 32,16
PM 33,17
PM 34,18
PM 35,19
PM 36,20
PM 37,21
AW 32,30
AW 33,31
AW 34,32
AW 35,33
AW 36,34
AW 37,35
AW 38,36
AW 39,37
WL 1
PM 80,104
PM 81,105
PM 82,106
PM 83,107
WL 0
PM 84,108
PM 85,109
PM 86,110
PM 87,111
s_mov_b32 s42, 0x1c001c00
v_pk_mul_f16 v80, v80, s42
v_pk_mul_f16 v81, v81, s42
v_pk_mul_f16 v82, v82, s42
v_pk_mul_f16 v83, v83, s42
v_pk_mul_f16 v84, v84, s42
v_pk_mul_f16 v85, v85, s42
v_pk_mul_f16 v86, v86, s42
v_pk_mul_f16 v87, v87, s42
global_store_dwordx4 v0, v[80:83], s[90:91] sc1
s_mov_b64 exec, 0xff
global_store_dwordx4 v0, v[84:87], s[90:91] offset:1024 sc1
s_mov_b64 exec, -1
M 0
mw 72
RS 14,128
RS 18,192
M 1
TY 38,2048
TZ 40,2048
M 2
ad 86,72
DR 0
M 3
R 6
M 12
DR 1024
TY 42,16384
M 13
DR 2048
TZ 44,16384
M 14
mw 75
TY 22,3072
TZ 24,3072
M 15
R 8
M 24
ad 86,75
DR 0
M 25
DR 1024
TY 26,17408
M 26
WL 5
DR 2048
PM 38,14
M 27
R 10
M 36
PM 39,15
PM 40,16
M 37
WV 20
PM 41,17
PM 42,18
M 38
WL 0
s_barrier
PM 43,19
PM 44,20
M 39
R 24
M 48
mw 0
PM 45,21
AW 160,38
M 49
ad 86,96
DR 0
M 50
DR 1024
AW 161,39
M 51
R 26
M 60
DR 2048
AW 162,40
M 61
mw 3
AW 163,41
AW 164,42
M 62
ad 86,99
DR 0
M 63
WL 2
R 28
M 72
DR 1024
AW 165,43
M 73
DR 2048
AW 166,44
M 74
bc 49,3,66,60
AW 167,45
M 75
WL 2
R 30
M 84
mh 12
TZ 28,17408
BA 80,0
M 85
ad 66,288
H 0
M 86
H 1
BA 104,256
M 87
WL 5
R 32
M 96
BA 128,512
BA 152,768
M 97
WL 6
PM 22,14
PM 23,15
BA 84,0
M 98
PM 24,16
PM 25,17
BA 108,256
M 99
R 34
BA 132,512
M 108
PM 26,18
PM 27,19
BA 156,768
M 109
WV 16
PM 28,20
PM 29,21
M 110
WL 2
s_barrier
AW 168,22
AW 169,23
TY 30,4096
TZ 32,4096
M 111
R 48
TY 34,18432
TZ 36,18432
M 120
mw 24
AW 170,24
AW 171,25
TY 38,5120
M 121
ad 86,120
DR 0
TZ 40,5120
M 132
DR 1024
AW 172,26
TY 42,19456
)"
R"(M 133
WL 3
DR 2048
AW 173,27
TZ 44,19456
M 122
mw 27
AW 174,28
AW 175,29
PM 30,14
PM 31,15
M 123
R 50
PM 32,16
PM 33,17
M 134
ad 86,123
DR 0
PM 34,18
PM 35,19
M 135
R 52
PM 36,20
PM 37,21
MA 72,0,160
DR 1024
AW 176,30
AW 177,31
AW 178,32
MA 72,4,164
DR 2048
AW 179,33
AW 180,34
AW 181,35
MA 76,0,168
WL 4
AW 182,36
AW 183,37
PM 38,14
MA 76,4,172
PM 39,15
PM 40,16
PM 41,17
MA 80,0,176
PM 42,18
PM 43,19
PM 44,20
PM 45,21
AW 184,38
AW 185,39
AW 186,40
AW 187,41
AW 188,42
AW 189,43
AW 190,44
AW 191,45
MA 80,4,180
RS 14,256
RS 18,320
TY 22,6144
MA 84,0,184
TZ 24,6144
TY 26,20480
TZ 28,20480
MA 84,4,188
WL 6
R 54
MA 96,8,160
TY 30,7168
TZ 32,7168
MA 96,12,164
TY 34,21504
TZ 36,21504
MA 100,8,168
BV 72,1024
BV 96,1280
MA 100,12,172
WL 8
PM 22,14
PM 23,15
PM 24,16
MA 104,8,176
PM 25,17
PM 26,18
PM 27,19
MA 104,12,180
PM 28,20
PM 29,21
AW 24,22
MA 108,8,184
AW 25,23
AW 26,24
AW 27,25
MA 108,12,188
R 56
AW 28,26
MA 120,16,160
AW 29,27
AW 30,28
AW 31,29
MA 120,20,164
WL 4
PM 30,14
PM 31,15
MA 124,16,168
PM 32,16
PM 33,17
MA 124,20,172
PM 34,18
PM 35,19
MA 128,16,176
PM 36,20
PM 37,21
MA 128,20,180
AW 32,30
AW 33,31
MA 132,16,184
AW 34,32
AW 35,33
MA 132,20,188
R 58
MA 144,0,160
AW 36,34
AW 37,35
MA 144,4,164
AW 38,36
AW 39,37
MA 148,0,168
BV 120,1536
BV 144,1792
MA 148,4,172
BV 76,1024
BV 100,1280
MA 152,0,176
WV 14
BV 124,1536
BV 148,1792
MA 152,4,180
WL 6
BV 168,2048
BV 192,2304
MA 156,0,184
s_barrier
v_mov_b32 v10, 1
s_mov_b64 exec, 1
FS
s_mov_b64 exec, -1
MA 156,4,188
R 72
MV 64,8,160
mw 48
TY 38,8192
TZ 40,8192
MV 64,12,164
ad 86,144
DR 0
MV 68,8,168
TY 42,22528
TZ 44,22528
MV 68,12,172
DR 1024
TY 22,9216
MV 72,8,176
WL 9
TZ 24,9216
TY 26,23552
MV 72,12,180
DR 2048
TZ 28,23552
MV 76,8,184
BV 216,2560
BV 240,2816
MV 76,12,188
WL 6
R 74
BV 172,2048
MV 88,16,160
mw 51
PM 38,14
PM 39,15
BV 196,2304
MV 88,20,164
ad 86,147
DR 0
BV 220,2560
MV 92,16,168
PM 40,16
PM 41,17
BV 244,2816
MV 92,20,172
DR 1024
PM 42,18
MV 96,16,176
PM 43,19
PM 44,20
MV 96,20,180
DR 2048
PM 45,21
MV 100,16,184
AW 40,38
AW 41,39
MV 100,20,188
R 76
MV 112,0,160
bc 48,3,64,56
AW 42,40
MV 112,4,164
mh 0
AW 43,41
AW 44,42
MV 116,0,168
ad 64,288
H 0
MV 116,4,172
AW 45,43
AW 46,44
MV 120,0,176
H 1
AW 47,45
MV 120,4,180
WL 3
PM 22,14
PM 23,15
MV 124,0,184
H 2
PM 24,16
MV 124,4,188
R 78
MV 136,8,160
H 3
PM 25,17
MV 136,12,164
PM 26,18
PM 27,19
MV 140,8,168
bc 49,4,66,60
PM 28,20
MV 140,12,172
mh 4
PM 29,21
AW 48,22
MV 144,8,176
ad 66,384
H 0
MV 144,12,180
AW 49,23
AW 50,24
MV 148,8,184
H 1
AW 51,25
MV 148,12,188
WL 2
R 80
MV 160,16,160
AW 52,26
AW 53,27
MV 160,20,164
AW 54,28
AW 55,29
MV 164,16,168
TY 30,10240
TZ 32,10240
MV 164,20,172
TY 34,24576
TZ 36,24576
MV 168,16,176
TY 38,11264
TZ 40,11264
MV 168,20,180
TY 42,25600
TZ 44,25600
MV 172,16,184
BA 88,0
BA 112,256
MV 172,20,188
WL 8
R 82
MV 184,0,160
WL 8
PM 30,14
PM 31,15
MV 184,4,164
PM 32,16
PM 33,17
MV 188,0,168
PM 34,18
PM 35,19
MV 188,4,172
PM 36,20
PM 37,21
MV 192,0,176
WV 21
AW 56,30
AW 57,31
MV 192,4,180
WL 0
AW 58,32
AW 59,33
MV 196,0,184
s_barrier
AW 60,34
AW 61,35
MV 196,4,188
R 0
MV 208,8,160
mw 72
AW 62,36
AW 63,37
MV 208,12,164
ad 86,168
DR 0
MV 232,16,160
PM 38,14
PM 39,15
MV 232,20,164
DR 1024
PM 40,16
MV 212,8,168
PM 41,17
PM 42,18
MV 212,12,172
DR 2048
PM 43,19
MV 236,16,168
PM 44,20
PM 45,21
MV 236,20,172
mw 75
AW 64,38
AW 65,39
MV 216,8,176
ad 86,171
DR 0
MV 216,12,180
AW 66,40
AW 67,41
MV 240,16,176
DR 1024
AW 68,42
MV 240,20,180
AW 69,43
AW 70,44
MV 220,8,184
DR 2048
AW 71,45
MV 220,12,188
R 2
MV 244,16,184
bc 48,4,64,56
RS 14,384
MV 244,20,188
WL 3
R 4
M 0
mh 6
RS 18,448
BA 136,512
M 1
BV 56,768
M 2
ad 64,384
H 0
M 3
BA 92,0
BA 116,256
M 4
H 1
BA 140,512
M 5
BV 60,768
M 6
H 2
M 7
M 8
H 3
M 9
WL 3
M 10
M 11
R 6
M 12
M 13
M 14
M 15
M 16
BV 80,1024
BV 104,1280
M 17
BV 128,1536
BV 152,1792
M 18
BV 84,1024
BV 108,1280
M 19
BV 132,1536
BV 156,1792
M 20
v_lshrrev_b32 v9, 8, v0
v_bfe_u32 v8, v0, 4, 4
M 21
)"
R"(WL 12
v_lshlrev_b32 v10, 2, v9
M 22
s_mov_b32 s42, 0xcb527610
M 23
R 8
M 24
s_movk_i32 s43, 0xc00
v_lshlrev_b32 v11, 3, v8
v_bfe_u32 v3, s42, v10, 4
M 25
v_add_u32 v10, 16, v10
v_mad_u32_u24 v3, v3, s43, v11
M 26
v_bfe_u32 v50, s42, v10, 4
v_add_u32 v10, 1, v9
M 27
v_mad_u32_u24 v50, v50, s43, v11
v_and_b32 v10, 2, v10
M 28
v_sub_u32 v10, 1, v10
v_mul_i32_i24 v51, s43, v10
M 29
M 30
M 31
M 32
M 33
WL 7
M 34
M 35
R 10
M 36
M 37
M 38
M 39
M 40
M 41
M 42
M 43
M 44
WV 23
M 45
WL 0
s_barrier
TY 22,12288
TZ 24,12288
M 46
TY 26,26624
TZ 28,26624
M 47
R 24
M 48
M 49
M 50
M 51
WL 2
PM 22,14
PM 23,15
M 52
PM 24,16
PM 25,17
M 53
PM 26,18
PM 27,19
M 54
PM 28,20
PM 29,21
M 55
AW 160,22
AW 161,23
M 56
AW 162,24
AW 163,25
M 57
AW 164,26
AW 165,27
M 58
AW 166,28
AW 167,29
M 59
R 26
M 60
M 61
M 62
M 63
M 64
BV 176,2048
BV 200,2304
M 65
BV 224,2560
BV 248,2816
M 66
v_lshlrev_b32 v7, 4, v0
BV 180,2048
M 67
FL
TY 30,13312
M 68
TZ 32,13312
TY 34,27648
M 69
TZ 36,27648
BV 204,2304
M 70
WL 8
BV 228,2560
BV 252,2816
M 71
R 28
M 72
M 73
M 74
WL 5
PM 30,14
PM 31,15
M 75
PM 32,16
PM 33,17
M 76
PM 34,18
PM 35,19
M 77
PM 36,20
PM 37,21
M 78
AW 168,30
AW 169,31
M 79
AW 170,32
AW 171,33
M 80
AW 172,34
AW 173,35
M 81
AW 174,36
AW 175,37
M 82
M 83
R 30
M 84
M 85
bc 49,5,66,60
M 86
mh 10
M 87
ad 66,480
H 0
M 88
M 89
M 90
M 91
H 1
M 92
M 93
M 94
M 95
WL 2
R 32
M 96
M 97
M 98
M 99
M 100
M 101
M 102
M 103
M 104
M 105
M 106
M 107
WL 2
R 34
M 108
M 109
M 110
WV 2
v_cmp_ne_u32 vcc, 1, v6
M 111
M 112
M 113
FB
M 114
M 115
M 116
WL 0
M 117
s_barrier
TY 38,0
TZ 40,0
TY 42,14336
M 118
mw 0
TZ 44,14336
TY 22,1024
TZ 24,1024
M 119
R 48
TY 26,15360
M 120
ad 84,192
W 0
TZ 28,15360
M 121
W 1
TY 30,2048
TZ 32,2048
M 132
WL 8
W 2
TY 34,16384
TZ 36,16384
M 133
mw 3
PM 38,14
PM 39,15
PM 40,16
M 122
ad 84,195
W 0
PM 41,17
M 123
W 1
PM 42,18
PM 43,19
M 134
W 2
PM 44,20
PM 45,21
M 135
mw 24
AW 176,38
AW 177,39
AW 178,40
M 124
ad 84,216
W 0
AW 179,41
M 125
W 1
AW 180,42
AW 181,43
M 136
WL 1
W 2
AW 182,44
AW 183,45
M 137
mw 27
PM 22,14
PM 23,15
PM 24,16
M 126
ad 84,219
W 0
PM 25,17
M 127
W 1
PM 26,18
PM 27,19
M 138
W 2
PM 28,20
PM 29,21
M 139
AW 184,22
AW 185,23
AW 186,24
M 128
AW 187,25
AW 188,26
AW 189,27
M 129
WL 0
AW 190,28
AW 191,29
PM 30,14
M 140
PM 31,15
PM 32,16
PM 33,17
M 141
PM 34,18
PM 35,19
PM 36,20
M 130
PM 37,21
AW 192,30
AW 193,31
M 131
R 50
AW 194,32
M 142
AW 195,33
AW 196,34
AW 197,35
M 143
R 52
AW 198,36
MA 72,0,160
AW 199,37
TY 38,3072
TZ 40,3072
MA 72,4,164
TY 42,17408
TZ 44,17408
MA 76,0,168
MA 76,4,172
MA 80,0,176
MA 80,4,180
MA 84,0,184
WL 0
PM 38,14
PM 39,15
PM 40,16
MA 84,4,188
PM 41,17
PM 42,18
PM 43,19
MA 88,0,192
PM 44,20
PM 45,21
AW 200,38
AW 201,39
AW 202,40
AW 203,41
AW 204,42
AW 205,43
AW 206,44
AW 207,45
MA 88,4,196
MA 92,0,200
MA 92,4,204
R 54
MA 96,8,160
MA 96,12,164
MA 100,8,168
MA 100,12,172
MA 104,8,176
MA 104,12,180
MA 108,8,184
MA 108,12,188
MA 112,8,192
MA 112,12,196
MA 116,8,200
MA 116,12,204
R 56
MA 120,16,160
MA 120,20,164
MA 124,16,168
MA 124,20,172
MA 128,16,176
MA 128,20,180
MA 132,16,184
MA 132,20,188
MA 136,16,192
MA 136,20,196
MA 140,16,200
MA 140,20,204
WL 2
R 58
MA 144,0,160
MA 144,4,164
MA 148,0,168
MA 148,4,172
MA 152,0,176
MA 152,4,180
MA 156,0,184
MA 156,4,188
MV 56,0,192
MV 56,4,196
WL 0
s_barrier
MV 60,0,200
mw 48
MV 60,4,204
R 72
MV 64,8,160
ad 84,240
W 0
MV 64,12,164
v_lshrrev_b32 v8, 1, v0
s_mov_b64 exec, s[76:77]
global_load_dwordx2 v[6:7], v8, s[78:79] sc1
s_mov_b64 exec, -1
MV 68,8,168
MV 68,12,172
MV 72,8,176
W 1
MV 72,12,180
MV 76,8,184
MV 76,12,188
MV 80,8,192
W 2
MV 80,12,196
MV 84,8,200
MV 84,12,204
R 74
MV 88,16,160
mw 51
MV 88,20,164
)"
R"(ad 84,243
W 0
MV 92,16,168
MV 92,20,172
MV 96,16,176
MV 96,20,180
W 1
MV 100,16,184
MV 100,20,188
MV 104,16,192
MV 104,20,196
W 2
MV 108,16,200
MV 108,20,204
WL 2
R 76
MV 112,0,160
MV 112,4,164
bc 48,5,64,56
MV 116,0,168
mh 12
MV 116,4,172
ad 64,480
H 0
MV 120,0,176
MV 120,4,180
MV 124,0,184
MV 124,4,188
H 1
MV 128,0,192
MV 128,4,196
MV 132,0,200
MV 132,4,204
WL 2
R 78
MV 136,8,160
mh 0
MV 136,12,164
ad 64,482
H 0
MV 140,8,168
MV 140,12,172
MV 144,8,176
MV 144,12,180
H 1
MV 148,8,184
MV 148,12,188
MV 152,8,192
MV 152,12,196
bc 49,6,66,60
MV 156,8,200
mh 2
MV 156,12,204
WL 2
R 80
MV 160,16,160
ad 66,576
H 0
MV 160,20,164
MV 164,16,168
MV 164,20,172
MV 168,16,176
H 1
MV 168,20,180
MV 172,16,184
MV 172,20,188
MV 176,16,192
MV 176,20,196
MV 180,16,200
MV 180,20,204
WL 2
R 82
MV 184,0,160
MV 184,4,164
MV 188,0,168
MV 188,4,172
MV 192,0,176
MV 192,4,180
MV 196,0,184
MV 196,4,188
MV 200,0,192
WV 19
MV 200,4,196
WL 0
s_barrier
T 24,4
MV 204,0,200
mw 72
T 28,18
MV 204,4,204
R 0
MV 208,8,160
ad 84,264
W 0
MV 208,12,164
T 32,5
MV 232,16,160
T 36,19
MV 232,20,164
MV 212,8,168
W 1
MV 212,12,172
T 40,6
MV 236,16,168
T 44,20
MV 236,20,172
MV 216,8,176
W 2
MV 216,12,180
WL 8
T 48,7
MV 240,16,176
T 52,21
MV 240,20,180
ae 44,70,0
MV 220,8,184
mw 75
MV 220,12,188
T 56,8
MV 244,16,184
WL 8
T 60,22
MV 244,20,188
ad 84,267
W 0
MV 224,8,192
s_bitcmp1_b32 s50, 0
s_cselect_b32 s51, 24576, 0
v_add_u32 v52, s51, v3
MV 224,12,196
T 64,9
MV 248,16,192
T 68,23
MV 248,20,196
W 1
XL 14,0
MV 228,8,200
XL 16,768
MV 228,12,204
WL 8
R 2
MV 252,16,200
XL 18,1536
MV 252,20,204
R 4
M 0
W 2
M 1
M 2
XL 20,2304
M 3
M 4
M 5
WV 21
v_or_b32 v6, v6, v7
s_mov_b64 exec, s[76:77]
v_cmp_ne_u32 %[flg], 0, v6
s_mov_b64 exec, -1
M 6
M 7
WL 8
M 8
M 9
WL 4
M 10
M 11
WL 2
R 6
M 12
M 13
M 14
M 15
M 16
M 17
M 18
M 19
M 20
M 21
M 22
M 23
WL 2
R 8
M 24
M 25
M 26
M 27
M 28
M 29
M 30
M 31
M 32
M 33
M 34
M 35
WL 2
R 10
M 36
M 37
M 38
M 39
M 40
M 41
M 42
M 43
M 44
M 45
WL 0
s_barrier
M 46
mw 0
M 47
R 24
M 48
ad 84,288
W 0
M 49
M 50
M 51
M 52
W 1
M 53
M 54
M 55
M 56
W 2
M 57
M 58
M 59
R 26
M 60
mw 3
M 61
ad 84,291
W 0
M 62
M 63
M 64
M 65
W 1
M 66
M 67
M 68
M 69
W 2
M 70
M 71
WL 2
R 28
M 72
M 73
bc 48,6,64,56
M 74
mh 4
M 75
ad 64,576
H 0
M 76
M 77
M 78
M 79
H 1
M 80
M 81
M 82
M 83
WL 2
R 30
M 84
H 2
M 85
M 86
M 87
M 88
H 3
M 89
M 90
M 91
M 92
bc 49,7,66,60
M 93
mh 8
M 94
ad 66,672
H 0
M 95
WL 2
R 32
M 96
M 97
M 98
H 1
M 99
M 100
M 101
M 102
M 103
M 104
M 105
M 106
M 107
WL 2
R 34
M 108
M 109
M 110
M 111
M 112
M 113
M 114
M 115
M 116
WV 24
M 117
WL 0
s_barrier
M 118
mw 24
M 119
R 48
M 120
ad 84,312
W 0
M 121
M 132
M 133
T 24,10
M 122
T 28,24
M 123
W 1
M 134
M 135
T 32,11
M 124
T 36,25
M 125
W 2
M 136
ae 44,70,0
M 137
s_bfe_u32 s51, s50, 0x10001
T 40,12
M 126
T 44,26
M 127
mw 27
v_mad_i32_i24 v52, v51, s51, v50
XL 22,0
M 138
ad 84,315
W 0
M 139
WL 8
T 48,13
M 128
T 52,27
M 129
XL 24,768
M 140
W 1
M 141
T 56,0
M 130
WL 8
T 60,14
M 131
R 50
M 142
W 2
XL 26,1536
M 143
T 64,1
M 0
WL 8
T 68,15
M 1
R 52
M 2
M 3
M 4
XL 28,2304
M 5
M 6
M 7
WL 8
M 8
M 9
WL 2
M 10
M 11
R 54
M 12
M 13
M 14
M 15
M 16
M 17
M 18
M 19
M 20
)"
R"(M 21
M 22
M 23
WL 2
R 56
M 24
M 25
M 26
M 27
M 28
M 29
M 30
M 31
M 32
M 33
M 34
M 35
WL 2
R 58
M 36
M 37
M 38
M 39
M 40
WV 24
UL 6,14
UH 7,14
M 41
UL 8,15
UH 9,15
M 42
AR 10,72
AR 11,96
M 43
WV 23
AR 12,120
AR 13,144
M 44
WL 0
AD 10,6
AD 11,7
M 45
s_barrier
AD 12,8
AD 13,9
M 46
mw 48
AW 72,10
AW 96,11
M 47
R 72
M 48
ad 84,336
W 0
M 49
AW 120,12
AW 144,13
M 50
UL 6,16
UH 7,16
M 51
UL 8,17
UH 9,17
M 52
W 1
AR 10,73
M 53
AR 11,97
AR 12,121
M 54
AR 13,145
AD 10,6
M 55
AD 11,7
AD 12,8
M 56
W 2
AD 13,9
M 57
AW 73,10
AW 97,11
M 58
AW 121,12
AW 145,13
M 59
R 74
M 60
mw 51
UL 6,18
UH 7,18
M 61
ad 84,339
W 0
M 62
UL 8,19
UH 9,19
M 63
AR 10,74
AR 11,98
M 64
AR 12,122
AR 13,146
M 65
W 1
AD 10,6
M 66
AD 11,7
AD 12,8
M 67
AD 13,9
AW 74,10
M 68
AW 98,11
AW 122,12
M 69
W 2
AW 146,13
M 70
WV 28
UL 6,20
UH 7,20
M 71
WL 0
R 76
M 72
UL 8,21
UH 9,21
M 73
bc 48,7,64,56
AR 10,75
M 74
mh 10
AR 11,99
AR 12,123
M 75
ad 64,672
H 0
M 76
AR 13,147
AD 10,6
M 77
AD 11,7
AD 12,8
M 78
AD 13,9
AW 75,10
M 79
H 1
AW 99,11
M 80
AW 123,12
AW 147,13
M 81
M 82
M 83
R 78
M 84
H 2
M 85
M 86
M 87
M 88
H 3
M 89
M 90
M 91
M 92
bc 49,8,66,60
M 93
mh 0
M 94
ad 66,768
H 0
M 95
WL 2
R 80
M 96
M 97
M 98
H 1
M 99
M 100
M 101
M 102
M 103
M 104
M 105
M 106
M 107
WL 2
R 82
M 108
M 109
M 110
M 111
M 112
M 113
M 114
M 115
M 116
WV 24
M 117
WL 0
s_barrier
M 118
mw 72
M 119
R 0
M 120
ad 84,360
W 0
M 121
M 132
M 133
T 24,2
M 122
T 28,16
M 123
W 1
M 134
M 135
T 32,3
M 124
T 36,17
M 125
W 2
M 136
M 137
T 40,4
M 126
T 44,18
M 127
mw 75
M 138
ad 84,363
W 0
M 139
WL 8
T 48,5
M 128
T 52,19
M 129
ae 44,68,0
M 140
W 1
M 141
T 56,6
M 130
WL 8
T 60,20
M 131
R 2
M 142
W 2
M 143
T 64,7
M 0
WL 8
T 68,21
M 1
R 4
M 2
s_bitcmp1_b32 s50, 2
s_cselect_b32 s51, 24576, 0
M 3
v_add_u32 v52, s51, v3
XL 30,0
M 4
XL 32,768
M 5
XL 34,1536
M 6
XL 36,2304
M 7
WL 8
M 8
M 9
WL 2
M 10
M 11
R 6
M 12
M 13
M 14
M 15
M 16
M 17
M 18
M 19
M 20
M 21
M 22
M 23
WL 2
R 8
M 24
M 25
M 26
M 27
M 28
M 29
M 30
M 31
M 32
M 33
M 34
M 35
WL 2
R 10
M 36
M 37
M 38
M 39
M 40
M 41
M 42
WV 28
UL 6,22
UH 7,22
M 43
WV 24
UL 8,23
UH 9,23
M 44
WL 0
AR 10,76
AR 11,100
M 45
s_barrier
AR 12,124
AR 13,148
M 46
mw 0
AD 10,6
AD 11,7
M 47
R 24
M 48
ad 84,384
W 0
M 49
AD 12,8
AD 13,9
M 50
AW 76,10
AW 100,11
M 51
AW 124,12
AW 148,13
M 52
W 1
UL 6,24
M 53
UH 7,24
UL 8,25
M 54
UH 9,25
AR 10,77
M 55
AR 11,101
AR 12,125
M 56
W 2
AR 13,149
M 57
AD 10,6
AD 11,7
M 58
AD 12,8
AD 13,9
M 59
R 26
M 60
mw 3
AW 77,10
AW 101,11
M 61
ad 84,387
W 0
M 62
AW 125,12
AW 149,13
M 63
WV 26
UL 6,26
UH 7,26
M 64
UL 8,27
UH 9,27
M 65
W 1
AR 10,78
M 66
AR 11,102
AR 12,126
M 67
AR 13,150
AD 10,6
M 68
AD 11,7
)"
R"(AD 12,8
M 69
W 2
AD 13,9
M 70
AW 78,10
AW 102,11
M 71
WL 0
R 28
M 72
AW 126,12
AW 150,13
M 73
bc 48,8,64,56
UL 6,28
M 74
mh 2
UH 7,28
UL 8,29
M 75
ad 64,768
H 0
M 76
UH 9,29
AR 10,79
M 77
AR 11,103
AR 12,127
M 78
AR 13,151
AD 10,6
M 79
H 1
AD 11,7
M 80
AD 12,8
AD 13,9
M 81
AW 79,10
AW 103,11
M 82
AW 127,12
AW 151,13
M 83
R 30
M 84
H 2
M 85
M 86
M 87
M 88
H 3
M 89
M 90
M 91
M 92
bc 49,9,66,60
M 93
mh 6
M 94
ad 66,864
H 0
M 95
WL 2
R 32
M 96
M 97
M 98
H 1
M 99
M 100
M 101
M 102
M 103
M 104
M 105
M 106
M 107
WL 2
R 34
M 108
M 109
M 110
M 111
M 112
M 113
M 114
M 115
M 116
WV 24
M 117
WL 0
s_barrier
M 118
mw 24
M 119
R 48
M 120
ad 84,408
W 0
M 121
M 132
M 133
T 24,8
M 122
T 28,22
M 123
W 1
M 134
M 135
T 32,9
M 124
T 36,23
M 125
W 2
M 136
M 137
T 40,10
M 126
T 44,24
M 127
mw 27
M 138
ad 84,411
W 0
M 139
WL 8
T 48,11
M 128
T 52,25
M 129
ae 44,68,0
M 140
s_bfe_u32 s51, s50, 0x10003
W 1
v_mad_i32_i24 v52, v51, s51, v50
M 141
T 56,12
M 130
WL 8
T 60,26
M 131
R 50
M 142
W 2
XL 38,0
M 143
T 64,13
M 0
WL 8
T 68,27
M 1
R 52
M 2
XL 40,768
M 3
XL 42,1536
M 4
M 5
M 6
M 7
WL 8
M 8
XL 44,2304
M 9
WL 2
M 10
M 11
R 54
M 12
M 13
M 14
M 15
M 16
M 17
M 18
M 19
M 20
M 21
M 22
M 23
WL 2
R 56
M 24
M 25
M 26
M 27
M 28
M 29
M 30
M 31
M 32
M 33
M 34
M 35
WL 2
R 58
M 36
M 37
M 38
M 39
M 40
M 41
M 42
M 43
M 44
WV 22
UL 6,30
UH 7,30
M 45
WL 0
s_barrier
UL 8,31
UH 9,31
M 46
mw 48
AR 10,80
AR 11,104
M 47
R 72
M 48
ad 84,432
W 0
M 49
AR 12,128
AR 13,152
M 50
AD 10,6
AD 11,7
M 51
AD 12,8
AD 13,9
M 52
W 1
AW 80,10
M 53
AW 104,11
AW 128,12
M 54
AW 152,13
UL 6,32
M 55
UH 7,32
UL 8,33
M 56
W 2
UH 9,33
M 57
AR 10,81
AR 11,105
M 58
AR 12,129
AR 13,153
M 59
R 74
M 60
mw 51
AD 10,6
AD 11,7
M 61
ad 84,435
W 0
M 62
AD 12,8
AD 13,9
M 63
AW 81,10
AW 105,11
M 64
AW 129,12
AW 153,13
M 65
W 1
UL 6,34
M 66
UH 7,34
UL 8,35
M 67
UH 9,35
AR 10,82
M 68
AR 11,106
AR 12,130
M 69
W 2
AR 13,154
M 70
AD 10,6
AD 11,7
M 71
WL 0
R 76
M 72
AD 12,8
AD 13,9
M 73
bc 48,9,64,56
AW 82,10
M 74
mh 8
AW 106,11
AW 130,12
M 75
ad 64,864
H 0
M 76
AW 154,13
UL 6,36
M 77
UH 7,36
UL 8,37
M 78
UH 9,37
AR 10,83
M 79
H 1
AR 11,107
M 80
AR 12,131
AR 13,155
M 81
AD 10,6
AD 11,7
M 82
AD 12,8
AD 13,9
M 83
R 78
M 84
H 2
AW 83,10
M 85
AW 107,11
AW 131,12
M 86
AW 155,13
M 87
M 88
H 3
M 89
M 90
M 91
M 92
bc 49,10,66,60
M 93
mh 12
M 94
ad 66,960
H 0
M 95
WL 2
R 80
M 96
M 97
M 98
H 1
M 99
M 100
M 101
M 102
M 103
M 104
M 105
M 106
M 107
WL 2
R 82
M 108
M 109
M 110
M 111
M 112
M 113
M 114
M 115
M 116
WV 24
M 117
WL 0
s_barrier
M 118
mw 72
M 119
R 0
M 120
ad 84,456
W 0
M 121
M 132
M 133
T 24,0
M 122
T 28,14
M 123
W 1
M 134
M 135
T 32,1
M 124
)"
R"(T 36,15
M 125
W 2
M 136
M 137
T 40,2
M 126
T 44,16
M 127
mw 75
M 138
ad 84,459
W 0
M 139
WL 8
T 48,3
M 128
T 52,17
M 129
ae 44,68,48
M 140
W 1
M 141
T 56,4
M 130
WL 8
T 60,18
M 131
R 2
M 142
W 2
M 143
T 64,5
M 0
WL 8
T 68,19
M 1
R 4
M 2
s_bitcmp1_b32 s50, 4
s_cselect_b32 s51, 24576, 0
M 3
v_add_u32 v52, s51, v3
XL 46,0
M 4
XL 48,768
M 5
XL 14,1536
M 6
M 7
WL 8
M 8
M 9
WL 2
M 10
XL 16,2304
M 11
R 6
M 12
M 13
M 14
M 15
M 16
M 17
M 18
M 19
M 20
M 21
M 22
M 23
WL 2
R 8
M 24
M 25
M 26
M 27
M 28
M 29
M 30
M 31
M 32
M 33
M 34
M 35
WL 2
R 10
M 36
M 37
M 38
M 39
M 40
M 41
M 42
M 43
M 44
WV 22
M 45
WL 0
s_barrier
M 46
mw 0
UL 6,38
UH 7,38
M 47
R 24
M 48
ad 84,480
W 0
M 49
UL 8,39
UH 9,39
M 50
AR 10,84
AR 11,108
M 51
AR 12,132
AR 13,156
M 52
W 1
AD 10,6
M 53
AD 11,7
AD 12,8
M 54
AD 13,9
AW 84,10
M 55
AW 108,11
AW 132,12
M 56
W 2
AW 156,13
M 57
UL 6,40
UH 7,40
M 58
UL 8,41
UH 9,41
M 59
R 26
M 60
mw 3
AR 10,85
AR 11,109
M 61
ad 84,483
W 0
M 62
AR 12,133
AR 13,157
M 63
AD 10,6
AD 11,7
M 64
AD 12,8
AD 13,9
M 65
W 1
AW 85,10
M 66
AW 109,11
AW 133,12
M 67
AW 157,13
UL 6,42
M 68
UH 7,42
UL 8,43
M 69
W 2
UH 9,43
M 70
AR 10,86
AR 11,110
M 71
WL 0
R 28
M 72
AR 12,134
AR 13,158
M 73
bc 48,10,64,56
AD 10,6
M 74
mh 0
AD 11,7
AD 12,8
M 75
ad 64,960
H 0
M 76
AD 13,9
AW 86,10
M 77
AW 110,11
AW 134,12
M 78
AW 158,13
UL 6,44
M 79
H 1
UH 7,44
M 80
UL 8,45
UH 9,45
M 81
AR 10,87
AR 11,111
M 82
AR 12,135
AR 13,159
M 83
R 30
M 84
H 2
AD 10,6
M 85
AD 11,7
AD 12,8
M 86
AD 13,9
AW 87,10
M 87
AW 111,11
AW 135,12
M 88
H 3
AW 159,13
M 89
M 90
M 91
M 92
bc 49,11,66,60
M 93
mh 4
M 94
ad 66,1056
H 0
M 95
WL 2
R 32
M 96
M 97
M 98
H 1
M 99
M 100
M 101
M 102
M 103
M 104
M 105
M 106
M 107
WL 2
R 34
M 108
M 109
M 110
M 111
M 112
M 113
M 114
M 115
M 116
WV 24
M 117
WL 0
s_barrier
M 118
mw 24
M 119
R 48
M 120
ad 84,504
W 0
M 121
M 132
M 133
T 24,6
M 122
T 28,20
M 123
W 1
M 134
M 135
T 32,7
M 124
T 36,21
M 125
W 2
M 136
M 137
T 40,8
M 126
T 44,22
M 127
mw 27
M 138
ad 84,507
W 0
M 139
WL 8
T 48,9
M 128
T 52,23
M 129
M 140
W 1
M 141
T 56,10
M 130
WL 8
T 60,24
M 131
R 50
M 142
W 2
M 143
T 64,11
M 0
WL 8
T 68,25
M 1
R 52
M 2
ae 44,68,48
M 3
M 4
s_bfe_u32 s51, s50, 0x10005
v_mad_i32_i24 v52, v51, s51, v50
XL 18,0
M 5
XL 20,768
M 6
XL 22,1536
M 7
WL 8
M 8
M 9
WL 2
M 10
M 11
R 54
M 12
XL 24,2304
M 13
M 14
M 15
M 16
M 17
M 18
M 19
M 20
M 21
M 22
M 23
WL 2
R 56
M 24
M 25
M 26
M 27
M 28
M 29
M 30
M 31
M 32
M 33
M 34
M 35
WL 2
R 58
M 36
M 37
M 38
M 39
M 40
M 41
M 42
M 43
M 44
WV 26
M 45
WL 0
s_barrier
M 46
mw 48
M 47
R 72
M 48
ad 84,528
W 0
M 49
WV 23
UL 6,46
UH 7,46
M 50
UL 8,47
UH 9,47
)"
R"(M 51
AR 10,88
AR 11,112
M 52
W 1
AR 12,136
M 53
AD 10,6
AD 11,7
M 54
AD 12,8
AD 56,9
M 55
AW 88,10
AW 112,11
M 56
W 2
AW 136,12
M 57
UL 6,48
UH 7,48
M 58
UL 8,49
UH 9,49
M 59
R 74
M 60
mw 51
AR 10,89
AR 11,113
M 61
ad 84,531
W 0
M 62
AR 12,137
AD 10,6
M 63
AD 11,7
AD 12,8
M 64
AD 57,9
AW 89,10
M 65
W 1
AW 113,11
M 66
AW 137,12
UL 6,14
M 67
UH 7,14
UL 8,15
M 68
UH 9,15
AR 10,90
M 69
W 2
AR 11,114
M 70
AR 12,138
AD 10,6
M 71
WL 0
R 76
M 72
AD 11,7
AD 12,8
M 73
bc 48,11,64,56
AD 58,9
M 74
mh 6
AW 90,10
AW 114,11
M 75
ad 64,1056
H 0
M 76
AW 138,12
UL 6,16
M 77
UH 7,16
UL 8,17
M 78
UH 9,17
AR 10,91
M 79
H 1
AR 11,115
M 80
AR 12,139
AD 10,6
M 81
AD 11,7
AD 12,8
M 82
AD 59,9
AW 91,10
M 83
R 78
M 84
H 2
AW 115,11
M 85
AW 139,12
M 86
M 87
M 88
H 3
M 89
M 90
M 91
M 92
bc 49,12,66,60
M 93
mh 10
M 94
ad 66,1152
H 0
M 95
WL 2
R 80
M 96
M 97
M 98
H 1
M 99
M 100
M 101
M 102
M 103
M 104
M 105
M 106
M 107
WL 2
R 82
M 108
M 109
M 110
M 111
M 112
M 113
M 114
M 115
M 116
WV 24
M 117
WL 0
s_barrier
M 118
mw 72
M 119
R 0
M 120
ad 84,552
W 0
M 121
M 132
M 133
T 24,12
M 122
T 28,26
M 123
W 1
M 134
M 135
T 32,13
M 124
T 36,27
M 125
W 2
M 136
M 137
T 40,0
M 126
T 44,14
M 127
mw 75
M 138
ad 84,555
W 0
M 139
WL 8
T 48,1
M 128
T 52,15
M 129
M 140
W 1
M 141
T 56,2
M 130
WL 8
T 60,16
M 131
R 2
M 142
W 2
M 143
T 64,3
M 0
WL 8
T 68,17
M 1
R 4
M 2
M 3
M 4
M 5
M 6
M 7
WL 8
M 8
M 9
WL 2
M 10
M 11
R 6
M 12
M 13
M 14
M 15
M 16
M 17
M 18
M 19
M 20
M 21
M 22
M 23
WL 2
R 8
M 24
M 25
M 26
M 27
M 28
M 29
M 30
M 31
M 32
ae 44,70,0
M 33
s_bitcmp1_b32 s50, 0
s_cselect_b32 s51, 24576, 0
v_add_u32 v52, s51, v3
M 34
XL 26,128
M 35
WL 2
R 10
M 36
M 37
M 38
XL 28,896
M 39
M 40
M 41
M 42
M 43
WV 24
M 44
WL 0
XL 30,1664
M 45
s_barrier
M 46
mw 0
M 47
R 24
M 48
ad 84,576
W 0
M 49
M 50
WV 22
XL 32,2432
UL 6,18
M 51
UH 7,18
UL 8,19
M 52
W 1
UH 9,19
M 53
AR 10,92
AR 11,116
M 54
AR 12,140
AD 10,6
M 55
AD 11,7
AD 12,8
M 56
W 2
AD 60,9
M 57
AW 92,10
AW 116,11
M 58
AW 140,12
UL 6,20
M 59
R 26
M 60
mw 3
UH 7,20
UL 8,21
M 61
ad 84,579
W 0
M 62
UH 9,21
AR 10,93
M 63
AR 11,117
AR 12,141
M 64
AD 10,6
AD 11,7
M 65
W 1
AD 12,8
M 66
AD 61,9
AW 93,10
M 67
AW 117,11
AW 141,12
M 68
UL 6,22
UH 7,22
M 69
W 2
UL 8,23
M 70
UH 9,23
AR 10,94
M 71
WL 0
R 28
M 72
AR 11,118
AR 12,142
M 73
bc 48,12,64,56
AD 10,6
M 74
mh 12
AD 11,7
AD 12,8
M 75
ad 64,1152
H 0
M 76
AD 62,9
AW 94,10
M 77
AW 118,11
AW 142,12
M 78
UL 6,24
UH 7,24
M 79
H 1
UL 8,25
M 80
UH 9,25
AR 10,95
M 81
AR 11,119
AR 12,143
M 82
AD 10,6
AD 11,7
M 83
R 30
M 84
mh 0
AD 12,8
AD 63,9
M 85
ad 64,1154
H 0
M 86
AW 95,10
AW 119,11
M 87
AW 143,12
M 88
M 89
H 1
M 90
M 91
M 92
M 93
bc 49,13,66,60
M 94
mh 2
M 95
)"
R"(WL 2
R 32
M 96
ad 66,1248
H 0
M 97
M 98
M 99
M 100
H 1
M 101
M 102
M 103
M 104
M 105
M 106
M 107
WL 2
R 34
M 108
M 109
M 110
M 111
M 112
M 113
M 114
M 115
M 116
WV 24
M 117
WL 0
s_barrier
M 118
mw 24
M 119
R 48
M 120
ad 84,600
W 0
M 121
M 132
M 133
T 24,4
M 122
T 28,18
M 123
W 1
M 134
M 135
T 32,5
M 124
T 36,19
M 125
W 2
M 136
M 137
T 40,6
M 126
T 44,20
M 127
mw 27
M 138
ad 84,603
W 0
M 139
WL 8
T 48,7
M 128
T 52,21
M 129
M 140
W 1
M 141
T 56,8
M 130
WL 8
T 60,22
M 131
R 50
M 142
W 2
M 143
T 64,9
M 0
WL 8
T 68,23
M 1
R 52
M 2
M 3
M 4
M 5
M 6
M 7
WL 8
M 8
M 9
WL 2
M 10
M 11
R 54
M 12
M 13
M 14
M 15
M 16
M 17
M 18
M 19
M 20
M 21
M 22
M 23
WL 2
R 56
M 24
M 25
M 26
M 27
M 28
M 29
M 30
M 31
M 32
M 33
M 34
ae 44,70,0
M 35
WL 2
R 58
M 36
s_bfe_u32 s51, s50, 0x10001
v_mad_i32_i24 v52, v51, s51, v50
XL 34,128
M 37
M 38
M 39
M 40
XL 36,896
M 41
M 42
M 43
WV 24
M 44
WL 0
M 45
s_barrier
M 46
mw 48
XL 38,1664
M 47
R 72
M 48
ad 84,624
W 0
M 49
M 50
M 51
M 52
W 1
XL 40,2432
M 53
M 54
M 55
M 56
W 2
M 57
M 58
M 59
R 74
M 60
mw 51
M 61
ad 84,627
W 0
M 62
M 63
M 64
M 65
W 1
M 66
M 67
M 68
M 69
W 2
M 70
M 71
WL 2
R 76
M 72
M 73
bc 48,13,64,56
M 74
mh 4
M 75
ad 64,1248
H 0
M 76
M 77
M 78
M 79
H 1
M 80
M 81
M 82
M 83
WL 2
R 78
M 84
H 2
M 85
M 86
M 87
M 88
WV 32
H 3
UL 6,26
M 89
UH 7,26
UL 8,27
M 90
UH 9,27
AD 64,6
M 91
AD 88,7
AD 112,8
M 92
bc 49,14,66,60
AD 136,9
M 93
mh 8
UL 6,28
UH 7,28
M 94
ad 66,1344
H 0
M 95
WL 2
R 80
M 96
UL 8,29
UH 9,29
M 97
AD 65,6
AD 89,7
M 98
H 1
AD 113,8
M 99
AD 137,9
UL 6,30
M 100
UH 7,30
UL 8,31
M 101
UH 9,31
AD 66,6
M 102
AD 90,7
AD 114,8
M 103
WV 33
AD 138,9
UL 6,32
M 104
UH 7,32
UL 8,33
M 105
UH 9,33
AD 67,6
M 106
AD 91,7
AD 115,8
M 107
WL 2
R 82
M 108
AD 139,9
M 109
M 110
M 111
M 112
M 113
M 114
M 115
WV 24
M 116
WL 0
M 117
s_barrier
M 118
mw 72
M 119
R 0
M 120
ad 84,648
W 0
M 121
M 132
M 133
T 24,10
M 122
T 28,24
M 123
W 1
M 134
M 135
T 32,11
M 124
T 36,25
M 125
W 2
M 136
M 137
T 40,12
M 126
T 44,26
M 127
mw 75
M 138
ad 84,651
W 0
M 139
WL 8
T 48,13
M 128
T 52,27
M 129
M 140
W 1
M 141
T 56,0
M 130
WL 8
T 60,14
M 131
R 2
M 142
W 2
M 143
T 64,1
M 0
WL 8
T 68,15
M 1
R 4
M 2
M 3
M 4
M 5
M 6
M 7
WL 8
M 8
M 9
WL 2
M 10
M 11
R 6
M 12
M 13
M 14
M 15
M 16
M 17
M 18
M 19
M 20
M 21
M 22
M 23
WL 2
R 8
M 24
M 25
M 26
M 27
M 28
M 29
M 30
M 31
M 32
M 33
M 34
M 35
WL 2
R 10
M 36
ae 44,68,0
M 37
s_bitcmp1_b32 s50, 2
s_cselect_b32 s51, 24576, 0
v_add_u32 v52, s51, v3
M 38
XL 42,128
M 39
M 40
M 41
M 42
XL 44,896
)"
R"(M 43
WV 24
M 44
WL 0
M 45
s_barrier
M 46
mw 0
M 47
R 24
M 48
ad 84,672
W 0
M 49
XL 46,1664
M 50
M 51
M 52
W 1
M 53
M 54
XL 48,2432
M 55
M 56
W 2
M 57
M 58
M 59
R 26
M 60
mw 3
M 61
ad 84,675
W 0
M 62
M 63
M 64
M 65
W 1
M 66
M 67
M 68
M 69
W 2
M 70
M 71
WL 2
R 28
M 72
M 73
bc 48,14,64,56
M 74
mh 10
M 75
ad 64,1344
H 0
M 76
M 77
M 78
M 79
H 1
M 80
M 81
M 82
M 83
WL 2
R 30
M 84
H 2
M 85
M 86
M 87
M 88
H 3
M 89
M 90
WV 33
UL 6,34
UH 7,34
M 91
UL 8,35
UH 9,35
M 92
bc 49,15,66,60
AD 68,6
M 93
mh 0
AD 92,7
AD 116,8
M 94
ad 66,1440
H 0
M 95
WL 2
R 32
M 96
AD 140,9
UL 6,36
M 97
UH 7,36
UL 8,37
M 98
H 1
UH 9,37
M 99
AD 69,6
AD 93,7
M 100
AD 117,8
AD 141,9
M 101
UL 6,38
UH 7,38
M 102
UL 8,39
UH 9,39
M 103
AD 70,6
AD 94,7
M 104
AD 118,8
AD 142,9
M 105
WV 32
UL 6,40
UH 7,40
M 106
UL 8,41
UH 9,41
M 107
WL 2
R 34
M 108
AD 71,6
AD 95,7
M 109
AD 119,8
AD 143,9
M 110
M 111
M 112
M 113
M 114
M 115
WV 24
M 116
WL 0
M 117
s_barrier
M 118
mw 24
M 119
R 48
M 120
ad 84,696
W 0
M 121
M 132
M 133
T 24,2
M 122
T 28,16
M 123
W 1
M 134
M 135
T 32,3
M 124
T 36,17
M 125
W 2
M 136
M 137
T 40,4
M 126
T 44,18
M 127
mw 27
M 138
ad 84,699
W 0
M 139
WL 8
T 48,5
M 128
T 52,19
M 129
M 140
W 1
M 141
T 56,6
M 130
WL 8
T 60,20
M 131
R 50
M 142
W 2
M 143
T 64,7
M 0
WL 8
T 68,21
M 1
R 52
M 2
M 3
M 4
M 5
M 6
M 7
WL 8
M 8
M 9
WL 2
M 10
M 11
R 54
M 12
M 13
M 14
M 15
M 16
M 17
M 18
M 19
M 20
M 21
M 22
M 23
WL 2
R 56
M 24
M 25
M 26
M 27
M 28
M 29
M 30
M 31
M 32
M 33
M 34
M 35
WL 2
R 58
M 36
M 37
M 38
ae 44,68,0
M 39
s_bfe_u32 s51, s50, 0x10003
v_mad_i32_i24 v52, v51, s51, v50
XL 14,128
M 40
M 41
M 42
M 43
WV 23
M 44
WL 0
XL 16,896
M 45
s_barrier
M 46
mw 48
M 47
R 72
M 48
ad 84,720
W 0
M 49
M 50
XL 18,1664
M 51
M 52
W 1
M 53
M 54
M 55
M 56
W 2
XL 20,2432
M 57
M 58
M 59
R 74
M 60
mw 51
M 61
ad 84,723
W 0
M 62
M 63
M 64
M 65
W 1
M 66
M 67
M 68
M 69
W 2
M 70
M 71
WL 2
R 76
M 72
M 73
bc 48,15,64,56
M 74
mh 2
M 75
ad 64,1440
H 0
M 76
M 77
M 78
M 79
H 1
M 80
M 81
M 82
M 83
WL 2
R 78
M 84
H 2
M 85
M 86
M 87
M 88
H 3
M 89
M 90
M 91
WV 34
M 92
bc 49,16,66,60
UL 6,42
M 93
mh 6
UH 7,42
UL 8,43
M 94
ad 66,1536
H 0
M 95
WL 2
R 80
M 96
UH 9,43
AD 72,6
M 97
AD 96,7
AD 120,8
M 98
H 1
AD 144,9
M 99
UL 6,44
UH 7,44
M 100
UL 8,45
UH 9,45
M 101
AD 73,6
AD 97,7
M 102
AD 121,8
AD 145,9
M 103
WV 34
UL 6,46
UH 7,46
M 104
UL 8,47
UH 9,47
M 105
AD 74,6
AD 98,7
M 106
AD 122,8
AD 146,9
M 107
WL 2
R 82
M 108
WV 32
UL 6,48
UH 7,48
M 109
UL 8,49
UH 9,49
M 110
AD 75,6
AD 99,7
M 111
AD 123,8
AD 147,9
M 112
)"
R"(M 113
M 114
M 115
WV 24
M 116
WL 0
M 117
s_barrier
M 118
mw 72
M 119
R 0
M 120
ad 84,744
W 0
M 121
M 132
M 133
T 24,8
M 122
T 28,22
M 123
W 1
M 134
M 135
T 32,9
M 124
T 36,23
M 125
W 2
M 136
M 137
T 40,10
M 126
T 44,24
M 127
mw 75
M 138
ad 84,747
W 0
M 139
WL 8
T 48,11
M 128
T 52,25
M 129
M 140
W 1
M 141
T 56,12
M 130
WL 8
T 60,26
M 131
R 2
M 142
W 2
M 143
T 64,13
M 0
WL 8
T 68,27
M 1
R 4
M 2
M 3
M 4
M 5
M 6
M 7
WL 8
M 8
M 9
WL 2
M 10
M 11
R 6
M 12
M 13
M 14
M 15
M 16
M 17
M 18
M 19
M 20
M 21
M 22
M 23
WL 2
R 8
M 24
M 25
M 26
M 27
M 28
M 29
M 30
M 31
M 32
M 33
M 34
M 35
WL 2
R 10
M 36
M 37
M 38
M 39
M 40
ae 44,68,48
M 41
s_bitcmp1_b32 s50, 4
s_cselect_b32 s51, 24576, 0
v_add_u32 v52, s51, v3
M 42
XL 22,128
M 43
WV 23
M 44
WL 0
M 45
s_barrier
M 46
mw 0
XL 24,896
M 47
R 24
M 48
ad 84,768
W 0
M 49
M 50
M 51
M 52
W 1
XL 26,1664
M 53
M 54
M 55
M 56
W 2
M 57
M 58
XL 28,2432
M 59
R 26
M 60
mw 3
M 61
ad 84,771
W 0
M 62
M 63
M 64
M 65
W 1
M 66
M 67
M 68
M 69
W 2
M 70
M 71
WL 2
R 28
M 72
M 73
bc 48,16,64,56
M 74
mh 8
M 75
ad 64,1536
H 0
M 76
M 77
M 78
M 79
H 1
M 80
M 81
M 82
M 83
WL 2
R 30
M 84
H 2
M 85
M 86
M 87
M 88
H 3
M 89
M 90
M 91
M 92
bc 49,17,66,60
M 93
mh 12
M 94
ad 66,1632
H 0
M 95
WL 2
R 32
M 96
WV 35
UL 6,14
UH 7,14
M 97
UL 8,15
UH 9,15
M 98
H 1
AD 76,6
M 99
AD 100,7
AD 124,8
M 100
AD 148,9
UL 6,16
M 101
UH 7,16
UL 8,17
M 102
UH 9,17
AD 77,6
M 103
AD 101,7
AD 125,8
M 104
WV 34
AD 149,9
UL 6,18
M 105
UH 7,18
UL 8,19
M 106
UH 9,19
AD 78,6
M 107
WL 2
R 34
M 108
AD 102,7
AD 126,8
M 109
WV 31
AD 150,9
UL 6,20
M 110
UH 7,20
UL 8,21
M 111
UH 9,21
AD 79,6
M 112
AD 103,7
AD 127,8
M 113
AD 151,9
M 114
M 115
WV 24
M 116
WL 0
M 117
s_barrier
M 118
mw 24
M 119
R 48
M 120
ad 84,792
W 0
M 121
M 132
M 133
T 24,0
M 122
T 28,14
M 123
W 1
M 134
M 135
T 32,1
M 124
T 36,15
M 125
W 2
M 136
M 137
T 40,2
M 126
T 44,16
M 127
mw 27
M 138
ad 84,795
W 0
M 139
WL 8
T 48,3
M 128
T 52,17
M 129
M 140
W 1
M 141
T 56,4
M 130
WL 8
T 60,18
M 131
R 50
M 142
W 2
M 143
T 64,5
M 0
WL 8
T 68,19
M 1
R 52
M 2
M 3
M 4
M 5
M 6
M 7
WL 8
M 8
M 9
WL 2
M 10
M 11
R 54
M 12
M 13
M 14
M 15
M 16
M 17
M 18
M 19
M 20
M 21
M 22
M 23
WL 2
R 56
M 24
M 25
M 26
M 27
M 28
M 29
M 30
M 31
M 32
M 33
M 34
M 35
WL 2
R 58
M 36
M 37
M 38
M 39
M 40
M 41
M 42
ae 44,68,48
s_bfe_u32 s51, s50, 0x10005
M 43
WV 22
v_mad_i32_i24 v52, v51, s51, v50
XL 30,128
M 44
WL 0
M 45
s_barrier
M 46
mw 48
M 47
R 72
M 48
ad 84,816
W 0
M 49
XL 32,896
M 50
M 51
M 52
W 1
M 53
M 54
XL 34,1664
M 55
M 56
)"
R"(W 2
M 57
M 58
M 59
R 74
M 60
mw 51
XL 36,2432
M 61
ad 84,819
W 0
M 62
M 63
M 64
M 65
W 1
M 66
M 67
M 68
M 69
W 2
M 70
M 71
WL 2
R 76
M 72
M 73
bc 48,17,64,56
M 74
mh 0
M 75
ad 64,1632
H 0
M 76
M 77
M 78
M 79
H 1
M 80
M 81
M 82
M 83
WL 2
R 78
M 84
H 2
M 85
M 86
M 87
M 88
H 3
M 89
M 90
M 91
M 92
bc 49,18,66,60
M 93
mh 4
M 94
ad 66,1728
H 0
M 95
WL 2
R 80
M 96
WV 35
UL 6,22
UH 7,22
M 97
UL 8,23
UH 9,23
M 98
H 1
AD 80,6
M 99
AD 104,7
AD 128,8
M 100
AD 152,9
UL 6,24
M 101
UH 7,24
UL 8,25
M 102
UH 9,25
AD 81,6
M 103
AD 105,7
AD 129,8
M 104
WV 33
AD 153,9
UL 6,26
M 105
UH 7,26
UL 8,27
M 106
UH 9,27
AD 82,6
M 107
WL 2
R 82
M 108
AD 106,7
AD 130,8
M 109
WV 31
AD 154,9
UL 6,28
M 110
UH 7,28
UL 8,29
M 111
UH 9,29
AD 83,6
M 112
AD 107,7
AD 131,8
M 113
AD 155,9
M 114
M 115
WV 24
M 116
WL 0
M 117
s_barrier
M 118
mw 72
M 119
R 0
M 120
ad 84,840
W 0
M 121
M 132
M 133
T 24,6
M 122
T 28,20
M 123
W 1
M 134
M 135
T 32,7
M 124
T 36,21
M 125
W 2
M 136
M 137
T 40,8
M 126
T 44,22
M 127
mw 75
M 138
ad 84,843
W 0
M 139
WL 8
T 48,9
M 128
T 52,23
M 129
M 140
W 1
M 141
T 56,10
M 130
WL 8
T 60,24
M 131
R 2
M 142
W 2
M 143
T 64,11
M 0
WL 8
T 68,25
M 1
R 4
M 2
M 3
M 4
M 5
M 6
M 7
WL 8
M 8
M 9
WL 2
M 10
M 11
R 6
M 12
M 13
M 14
M 15
M 16
M 17
M 18
M 19
M 20
M 21
M 22
M 23
WL 2
R 8
M 24
M 25
M 26
M 27
M 28
M 29
M 30
M 31
M 32
M 33
M 34
M 35
WL 2
R 10
M 36
M 37
M 38
M 39
M 40
M 41
M 42
M 43
M 44
WV 22
M 45
WL 0
s_barrier
M 46
mw 0
M 47
R 24
M 48
ad 84,864
W 0
M 49
M 50
M 51
M 52
W 1
M 53
M 54
M 55
M 56
W 2
M 57
M 58
M 59
R 26
M 60
mw 3
M 61
ad 84,867
W 0
M 62
M 63
M 64
M 65
W 1
M 66
M 67
M 68
M 69
W 2
M 70
ae 44,70,0
M 71
WL 2
R 28
M 72
s_bitcmp1_b32 s50, 0
s_cselect_b32 s51, 24576, 0
v_add_u32 v52, s51, v3
M 73
bc 48,18,64,56
XL 38,256
M 74
mh 6
M 75
ad 64,1728
H 0
M 76
XL 40,1024
M 77
M 78
M 79
H 1
M 80
M 81
M 82
XL 42,1792
M 83
WL 2
R 30
M 84
H 2
M 85
M 86
M 87
M 88
H 3
XL 44,2560
M 89
M 90
M 91
M 92
bc 49,19,66,60
M 93
mh 10
M 94
ad 66,1824
H 0
M 95
WL 2
R 32
M 96
M 97
M 98
WV 36
H 1
UL 6,30
M 99
UH 7,30
UL 8,31
M 100
UH 9,31
AD 84,6
M 101
AD 108,7
AD 132,8
M 102
WV 35
AD 156,9
UL 6,32
M 103
UH 7,32
UL 8,33
M 104
UH 9,33
AD 85,6
M 105
AD 109,7
AD 133,8
M 106
WV 33
AD 157,9
UL 6,34
M 107
WL 2
R 34
M 108
UH 7,34
UL 8,35
M 109
UH 9,35
AD 86,6
M 110
AD 110,7
AD 134,8
M 111
WV 31
AD 158,9
UL 6,36
M 112
UH 7,36
UL 8,37
M 113
UH 9,37
AD 87,6
M 114
AD 111,7
AD 135,8
M 115
WV 24
AD 159,9
M 116
WL 0
M 117
s_barrier
M 118
mw 24
M 119
R 48
M 120
ad 84,888
W 0
M 121
M 132
M 133
T 24,12
M 122
)"
R"(T 28,26
M 123
W 1
M 134
M 135
T 32,13
M 124
T 36,27
M 125
W 2
M 136
M 137
T 40,0
M 126
T 44,14
M 127
mw 27
M 138
ad 84,891
W 0
M 139
WL 8
T 48,1
M 128
T 52,15
M 129
M 140
W 1
M 141
T 56,2
M 130
WL 8
T 60,16
M 131
R 50
M 142
W 2
M 143
T 64,3
M 0
WL 8
T 68,17
M 1
R 52
M 2
M 3
M 4
M 5
M 6
M 7
WL 8
M 8
M 9
WL 2
M 10
M 11
R 54
M 12
M 13
M 14
M 15
M 16
M 17
M 18
M 19
M 20
M 21
M 22
M 23
WL 2
R 56
M 24
M 25
M 26
M 27
M 28
M 29
M 30
M 31
M 32
M 33
M 34
M 35
WL 2
R 58
M 36
M 37
M 38
M 39
M 40
M 41
M 42
M 43
M 44
WV 22
M 45
WL 0
s_barrier
M 46
mw 48
M 47
R 72
M 48
ad 84,912
W 0
M 49
M 50
M 51
M 52
W 1
M 53
M 54
M 55
M 56
W 2
M 57
M 58
M 59
R 74
M 60
mw 51
M 61
ad 84,915
W 0
M 62
M 63
M 64
M 65
W 1
M 66
M 67
M 68
M 69
W 2
M 70
M 71
WL 2
R 76
M 72
M 73
bc 48,19,64,56
M 74
mh 12
M 75
ad 64,1824
H 0
M 76
ae 44,70,0
M 77
s_bfe_u32 s51, s50, 0x10001
v_mad_i32_i24 v52, v51, s51, v50
XL 46,256
M 78
M 79
H 1
M 80
XL 48,1024
M 81
M 82
M 83
WL 2
R 78
M 84
mh 0
M 85
ad 64,1826
H 0
M 86
XL 14,1792
M 87
M 88
M 89
H 1
M 90
M 91
M 92
XL 16,2560
M 93
bc 49,20,66,60
M 94
mh 2
M 95
WL 2
R 80
M 96
ad 66,1920
H 0
M 97
M 98
M 99
M 100
H 1
M 101
M 102
M 103
M 104
M 105
M 106
M 107
WL 2
R 82
M 108
M 109
M 110
M 111
M 112
M 113
M 114
M 115
M 116
WV 25
M 117
WL 0
s_barrier
M 118
mw 72
M 119
R 0
M 120
ad 84,936
W 0
M 121
M 132
M 133
T 24,4
M 122
T 28,18
M 123
W 1
M 134
UL 6,38
UH 7,38
M 135
T 32,5
M 124
T 36,19
M 125
W 2
UL 8,39
M 136
UH 9,39
AD 160,6
M 137
T 40,6
M 126
T 44,20
M 127
mw 75
AD 184,7
AD 208,8
M 138
ad 84,939
W 0
M 139
WL 8
T 48,7
M 128
T 52,21
M 129
AD 232,9
UL 6,40
M 140
W 1
UH 7,40
M 141
T 56,8
M 130
WL 8
T 60,22
M 131
R 2
M 142
W 2
UL 8,41
M 143
T 64,9
M 0
WL 8
T 68,23
M 1
R 4
M 2
UH 9,41
AD 161,6
M 3
AD 185,7
AD 209,8
M 4
AD 233,9
UL 6,42
M 5
UH 7,42
UL 8,43
M 6
UH 9,43
AD 162,6
M 7
WL 8
AD 186,7
AD 210,8
M 8
WV 30
AD 234,9
UL 6,44
M 9
WL 2
UH 7,44
UL 8,45
M 10
UH 9,45
AD 163,6
M 11
R 6
M 12
AD 187,7
AD 211,8
M 13
AD 235,9
M 14
M 15
M 16
M 17
M 18
M 19
M 20
M 21
M 22
M 23
WL 2
R 8
M 24
M 25
M 26
M 27
M 28
M 29
M 30
M 31
M 32
M 33
M 34
M 35
WL 2
R 10
M 36
M 37
M 38
M 39
M 40
M 41
M 42
M 43
M 44
WV 22
M 45
WL 0
s_barrier
M 46
mw 0
M 47
R 24
M 48
ad 84,960
W 0
M 49
M 50
M 51
M 52
W 1
M 53
M 54
M 55
M 56
W 2
M 57
M 58
M 59
R 26
M 60
mw 3
M 61
ad 84,963
W 0
M 62
M 63
M 64
M 65
W 1
M 66
M 67
M 68
M 69
W 2
M 70
M 71
WL 2
R 28
M 72
M 73
bc 48,20,64,56
)"
R"(M 74
mh 4
M 75
ad 64,1920
H 0
M 76
M 77
M 78
ae 44,68,0
M 79
H 1
M 80
s_bitcmp1_b32 s50, 2
s_cselect_b32 s51, 24576, 0
v_add_u32 v52, s51, v3
M 81
XL 18,256
M 82
M 83
WL 2
R 30
M 84
H 2
XL 20,1024
M 85
M 86
M 87
M 88
H 3
M 89
M 90
XL 22,1792
M 91
M 92
bc 49,21,66,60
M 93
mh 8
M 94
ad 66,2016
H 0
M 95
WL 2
R 32
M 96
XL 24,2560
M 97
M 98
H 1
M 99
M 100
M 101
M 102
M 103
M 104
M 105
M 106
M 107
WL 2
R 34
M 108
M 109
M 110
M 111
M 112
M 113
M 114
M 115
M 116
WV 25
M 117
WL 0
s_barrier
M 118
mw 24
M 119
R 48
M 120
ad 84,984
W 0
M 121
M 132
M 133
T 24,10
M 122
T 28,24
M 123
W 1
M 134
M 135
T 32,11
M 124
T 36,25
M 125
W 2
M 136
UL 6,46
UH 7,46
M 137
T 40,12
M 126
T 44,26
M 127
mw 27
UL 8,47
UH 9,47
M 138
ad 84,987
W 0
M 139
WL 8
T 48,13
M 128
T 52,27
M 129
AD 164,6
AD 188,7
M 140
W 1
AD 212,8
M 141
T 56,0
M 130
WL 8
T 60,14
M 131
R 50
M 142
W 2
AD 236,9
M 143
T 64,1
M 0
WL 8
T 68,15
M 1
R 52
M 2
UL 6,48
UH 7,48
M 3
UL 8,49
UH 9,49
M 4
AD 165,6
AD 189,7
M 5
AD 213,8
AD 237,9
M 6
UL 6,14
UH 7,14
M 7
WL 8
UL 8,15
UH 9,15
M 8
AD 166,6
AD 190,7
M 9
WL 2
AD 214,8
AD 238,9
M 10
WV 30
UL 6,16
UH 7,16
M 11
R 54
M 12
UL 8,17
UH 9,17
M 13
AD 167,6
AD 191,7
M 14
AD 215,8
AD 239,9
M 15
M 16
M 17
M 18
M 19
M 20
M 21
M 22
M 23
WL 2
R 56
M 24
M 25
M 26
M 27
M 28
M 29
M 30
M 31
M 32
M 33
M 34
M 35
WL 2
R 58
M 36
M 37
M 38
M 39
M 40
M 41
M 42
M 43
M 44
WV 22
M 45
WL 0
s_barrier
M 46
mw 48
M 47
R 72
M 48
ad 84,1008
W 0
M 49
M 50
M 51
M 52
W 1
M 53
M 54
M 55
M 56
W 2
M 57
M 58
M 59
R 74
M 60
mw 51
M 61
ad 84,1011
W 0
M 62
M 63
M 64
M 65
W 1
M 66
M 67
M 68
M 69
W 2
M 70
M 71
WL 2
R 76
M 72
M 73
bc 48,21,64,56
M 74
mh 10
M 75
ad 64,2016
H 0
M 76
M 77
M 78
M 79
H 1
M 80
M 81
M 82
ae 44,68,0
M 83
WL 2
R 78
M 84
s_bfe_u32 s51, s50, 0x10003
H 2
v_mad_i32_i24 v52, v51, s51, v50
M 85
XL 26,256
M 86
M 87
M 88
H 3
XL 28,1024
M 89
M 90
M 91
M 92
bc 49,22,66,60
M 93
mh 0
M 94
ad 66,2112
H 0
M 95
WL 2
R 80
M 96
XL 30,1792
M 97
M 98
H 1
M 99
M 100
XL 32,2560
M 101
M 102
M 103
M 104
M 105
M 106
M 107
WL 2
R 82
M 108
M 109
M 110
M 111
M 112
M 113
M 114
M 115
M 116
WV 26
M 117
WL 0
s_barrier
M 118
mw 72
M 119
R 0
M 120
ad 84,1032
W 0
M 121
M 132
M 133
T 24,2
M 122
T 28,16
M 123
W 1
M 134
M 135
T 32,3
M 124
T 36,17
M 125
W 2
M 136
M 137
T 40,4
M 126
T 44,18
M 127
mw 75
M 138
ad 84,1035
W 0
M 139
WL 8
T 48,5
M 128
T 52,19
M 129
UL 6,18
UH 7,18
M 140
W 1
UL 8,19
M 141
T 56,6
M 130
WL 8
T 60,20
M 131
R 2
M 142
W 2
UH 9,19
M 143
T 64,7
M 0
WL 8
T 68,21
M 1
R 4
M 2
AD 168,6
AD 192,7
M 3
AD 216,8
AD 240,9
M 4
UL 6,20
UH 7,20
M 5
UL 8,21
)"
R"(UH 9,21
M 6
AD 169,6
AD 193,7
M 7
WL 8
AD 217,8
AD 241,9
M 8
WV 31
UL 6,22
UH 7,22
M 9
WL 2
UL 8,23
UH 9,23
M 10
AD 170,6
AD 194,7
M 11
R 6
M 12
AD 218,8
AD 242,9
M 13
WV 29
UL 6,24
UH 7,24
M 14
UL 8,25
UH 9,25
M 15
AD 171,6
AD 195,7
M 16
AD 219,8
AD 243,9
M 17
M 18
M 19
M 20
M 21
M 22
M 23
WL 2
R 8
M 24
M 25
M 26
M 27
M 28
M 29
M 30
M 31
M 32
M 33
M 34
M 35
WL 2
R 10
M 36
M 37
M 38
M 39
M 40
M 41
M 42
M 43
M 44
WV 22
M 45
WL 0
s_barrier
M 46
mw 0
M 47
R 24
M 48
ad 84,1056
W 0
M 49
M 50
M 51
M 52
W 1
M 53
M 54
M 55
M 56
W 2
M 57
M 58
M 59
R 26
M 60
mw 3
M 61
ad 84,1059
W 0
M 62
M 63
M 64
M 65
W 1
M 66
M 67
M 68
M 69
W 2
M 70
M 71
WL 2
R 28
M 72
M 73
bc 48,22,64,56
M 74
mh 2
M 75
ad 64,2112
H 0
M 76
M 77
M 78
M 79
H 1
M 80
M 81
M 82
M 83
WL 2
R 30
M 84
H 2
M 85
M 86
ae 44,68,48
M 87
s_bitcmp1_b32 s50, 4
s_cselect_b32 s51, 24576, 0
v_add_u32 v52, s51, v3
M 88
H 3
XL 34,256
M 89
M 90
M 91
M 92
bc 49,23,66,60
XL 36,1024
M 93
mh 6
M 94
ad 66,2208
H 0
M 95
WL 2
R 32
M 96
M 97
M 98
H 1
XL 38,1792
M 99
M 100
M 101
M 102
M 103
M 104
XL 40,2560
M 105
M 106
M 107
WL 2
R 34
M 108
M 109
M 110
M 111
M 112
M 113
M 114
M 115
M 116
WV 27
M 117
WL 0
s_barrier
M 118
mw 24
M 119
R 48
M 120
ad 84,1080
W 0
M 121
M 132
M 133
T 24,8
M 122
T 28,22
M 123
W 1
M 134
M 135
T 32,9
M 124
T 36,23
M 125
W 2
M 136
M 137
T 40,10
M 126
T 44,24
M 127
mw 27
M 138
ad 84,1083
W 0
M 139
WL 8
T 48,11
M 128
T 52,25
M 129
M 140
W 1
UL 6,26
M 141
T 56,12
M 130
WL 8
T 60,26
M 131
R 50
M 142
W 2
UH 7,26
M 143
T 64,13
M 0
WL 8
T 68,27
M 1
R 52
M 2
UL 8,27
UH 9,27
M 3
AD 172,6
AD 196,7
M 4
AD 220,8
AD 244,9
M 5
WV 32
UL 6,28
UH 7,28
M 6
UL 8,29
UH 9,29
M 7
WL 8
AD 173,6
AD 197,7
M 8
WL 2
AD 221,8
AD 245,9
M 9
WV 30
UL 6,30
UH 7,30
M 10
UL 8,31
UH 9,31
M 11
R 54
M 12
AD 174,6
AD 198,7
M 13
AD 222,8
AD 246,9
M 14
WV 28
UL 6,32
UH 7,32
M 15
UL 8,33
UH 9,33
M 16
AD 175,6
AD 199,7
M 17
AD 223,8
AD 247,9
M 18
M 19
M 20
M 21
M 22
M 23
WL 2
R 56
M 24
M 25
M 26
M 27
M 28
M 29
M 30
M 31
M 32
M 33
M 34
M 35
WL 2
R 58
M 36
M 37
M 38
M 39
M 40
M 41
M 42
M 43
M 44
WV 22
M 45
WL 0
s_barrier
M 46
mw 48
M 47
R 72
M 48
ad 84,1104
W 0
M 49
M 50
M 51
M 52
W 1
M 53
M 54
M 55
M 56
W 2
M 57
M 58
M 59
R 74
M 60
mw 51
M 61
ad 84,1107
W 0
M 62
M 63
M 64
M 65
W 1
M 66
M 67
M 68
M 69
W 2
M 70
M 71
WL 2
R 76
M 72
M 73
bc 48,23,64,56
M 74
mh 8
M 75
ad 64,2208
H 0
M 76
M 77
M 78
M 79
H 1
M 80
M 81
M 82
M 83
WL 2
R 78
M 84
H 2
M 85
M 86
M 87
M 88
H 3
M 89
)"
R"(M 90
ae 44,68,48
M 91
s_bfe_u32 s51, s50, 0x10005
v_mad_i32_i24 v52, v51, s51, v50
XL 42,256
M 92
bc 48,0,64,56
M 93
mh 12
M 94
ad 64,0
H 0
M 95
WL 2
R 80
M 96
XL 44,1024
M 97
M 98
H 1
M 99
M 100
M 101
M 102
XL 46,1792
M 103
M 104
M 105
M 106
M 107
WL 2
R 82
M 108
XL 48,2560
M 109
M 110
M 111
M 112
M 113
M 114
M 115
WV 28
M 116
WL 0
M 117
s_barrier
M 118
mw 72
M 119
R 0
M 120
ad 84,1128
W 0
M 121
M 132
M 133
T 24,0
M 122
T 28,14
M 123
W 1
M 134
M 135
T 32,1
M 124
T 36,15
M 125
W 2
M 136
M 137
T 40,2
M 126
T 44,16
M 127
mw 75
M 138
ad 84,1131
W 0
M 139
WL 8
T 48,3
M 128
T 52,17
M 129
M 140
W 1
M 141
T 56,4
M 130
WL 8
T 60,18
M 131
R 2
M 142
WV 31
W 2
UL 6,34
M 143
T 64,5
M 0
WL 8
T 68,19
M 1
R 4
M 2
PA 12288
DF
UH 7,34
M 3
UL 8,35
UH 9,35
M 4
AD 176,6
AD 200,7
M 5
AD 224,8
AD 248,9
M 6
UL 6,36
UH 7,36
M 7
WL 8
UL 8,37
UH 9,37
M 8
AD 177,6
AD 201,7
M 9
WL 2
AD 225,8
AD 249,9
M 10
WV 29
UL 6,38
UH 7,38
M 11
R 6
M 12
UL 8,39
UH 9,39
M 13
AD 178,6
AD 202,7
M 14
AD 226,8
AD 250,9
M 15
UL 6,40
UH 7,40
M 16
UL 8,41
UH 9,41
M 17
AD 179,6
AD 203,7
M 18
AD 227,8
AD 251,9
M 19
M 20
M 21
M 22
M 23
WL 2
R 8
M 24
M 25
M 26
M 27
M 28
M 29
M 30
M 31
M 32
M 33
M 34
M 35
WL 2
R 10
M 36
M 37
M 38
M 39
M 40
M 41
M 42
M 43
M 44
WV 23
M 45
WL 0
s_barrier
M 46
mw 0
M 47
R 24
M 48
ad 84,0
W 0
M 49
M 50
M 51
M 52
W 1
M 53
M 54
M 55
M 56
W 2
M 57
M 58
M 59
R 26
M 60
mw 3
M 61
ad 84,3
W 0
M 62
M 63
M 64
M 65
W 1
M 66
M 67
M 68
M 69
W 2
M 70
M 71
WL 2
R 28
M 72
M 73
mh 0
M 74
ad 64,2
H 0
M 75
M 76
M 77
M 78
H 1
M 79
M 80
M 81
M 82
bc 49,0,66,60
M 83
WL 2
R 30
M 84
mh 2
M 85
ad 66,16
H 0
M 86
M 87
M 88
M 89
mh 3
M 90
ad 64,16
H 0
M 91
M 92
M 93
M 94
bc 48,1,64,56
M 95
WL 2
R 32
M 96
mh 4
M 97
ad 64,98
H 0
M 98
M 99
M 100
M 101
H 1
M 102
M 103
M 104
M 105
M 106
M 107
WL 2
R 34
M 108
M 109
M 110
M 111
M 112
M 113
M 114
M 115
M 116
WV 25
M 117
WL 0
s_barrier
M 118
mw 24
M 119
R 48
M 120
ad 84,24
W 0
M 121
M 132
M 133
T 24,6
M 122
T 28,20
M 123
W 1
M 134
M 135
T 32,7
M 124
T 36,21
M 125
W 2
M 136
M 137
T 40,8
M 126
T 44,22
M 127
mw 27
M 138
ad 84,27
W 0
M 139
WL 8
T 48,9
M 128
T 52,23
M 129
M 140
W 1
M 141
T 56,10
M 130
WL 8
T 60,24
M 131
R 50
M 142
W 2
M 143
T 64,11
M 0
WL 8
T 68,25
M 1
R 52
M 2
WV 30
UL 6,42
UH 7,42
M 3
UL 8,43
UH 9,43
M 4
AD 180,6
AD 204,7
M 5
AD 228,8
AD 252,9
M 6
WV 28
UL 6,44
UH 7,44
M 7
WL 8
UL 8,45
UH 9,45
M 8
AD 181,6
AD 205,7
M 9
WL 2
AD 229,8
AD 253,9
M 10
WV 25
UL 6,46
UH 7,46
M 11
R 54
M 12
UL 8,47
UH 9,47
M 13
AD 182,6
AD 206,7
)"
R"(M 14
AD 230,8
AD 254,9
M 15
UL 6,48
UH 7,48
M 16
UL 8,49
UH 9,49
M 17
AD 183,6
AD 207,7
M 18
AD 231,8
AD 255,9
M 19
M 20
M 21
M 22
M 23
WL 2
R 56
M 24
M 25
M 26
M 27
M 28
M 29
M 30
M 31
M 32
M 33
M 34
M 35
WL 2
R 58
M 36
M 37
M 38
M 39
M 40
AR 6,72
AR 7,96
M 41
AR 8,120
s_nop 0
AR 9,144
M 42
EF 6
EF 7
M 43
WV 19
EF 8
EF 9
M 44
WL 0
CV 14,6,7
CV 15,8,9
M 45
s_barrier
AR 6,73
AR 7,97
M 46
mw 48
AR 8,121
AR 9,145
M 47
R 72
M 48
ad 84,48
W 0
M 49
EF 6
EF 7
M 50
EF 8
EF 9
M 51
CV 16,6,7
CV 17,8,9
M 52
W 1
AR 6,74
M 53
AR 7,98
AR 8,122
M 54
AR 9,146
EF 6
M 55
EF 7
EF 8
M 56
W 2
EF 9
M 57
CV 18,6,7
CV 19,8,9
M 58
AR 6,75
AR 7,99
M 59
R 74
M 60
mw 51
AR 8,123
AR 9,147
M 61
ad 84,51
W 0
M 62
EF 6
EF 7
M 63
EF 8
EF 9
M 64
CV 20,6,7
CV 21,8,9
M 65
W 1
BA 72,0
M 66
BA 96,256
BA 120,512
M 67
BA 144,768
M 68
ae 42,74,0
AR 6,76
M 69
W 2
AR 7,100
M 70
s_bitcmp1_b32 s50, 0
s_cselect_b32 s52, 24576, 0
v_add_u32 v53, s52, v3
M 71
WL 4
R 76
M 72
ST 14,0
AR 8,124
M 73
bc 49,1,66,60
AR 9,148
M 74
mh 6
EF 6
EF 7
M 75
ad 66,112
H 0
M 76
EF 8
EF 9
M 77
CV 22,6,7
CV 23,8,9
M 78
AR 6,77
AR 7,101
M 79
mh 7
AR 8,125
AR 9,149
M 80
ad 64,112
H 0
M 81
EF 6
EF 7
M 82
EF 8
EF 9
M 83
R 78
M 84
H 1
CV 24,6,7
M 85
CV 25,8,9
AR 6,78
M 86
AR 7,102
AR 8,126
M 87
AR 9,150
EF 6
M 88
mh 9
EF 7
EF 8
M 89
ad 66,113
H 0
M 90
EF 9
CV 26,6,7
M 91
CV 27,8,9
AR 6,79
M 92
AR 7,103
AR 8,127
M 93
bc 49,2,66,60
AR 9,151
M 94
mh 10
EF 6
EF 7
M 95
WL 0
R 80
M 96
ad 66,208
H 0
M 97
EF 8
EF 9
M 98
CV 28,6,7
CV 29,8,9
M 99
BA 76,0
BA 100,256
M 100
BA 124,512
BA 148,768
M 101
EF 64
EF 88
M 102
EF 112
EF 136
M 103
CV 30,64,88
CV 31,112,136
M 104
EF 65
EF 89
M 105
EF 113
EF 137
M 106
CV 32,65,89
CV 33,113,137
M 107
R 82
M 108
EF 66
EF 90
M 109
EF 114
EF 138
M 110
ST 16,768
CV 34,66,90
M 111
CV 35,114,138
EF 67
M 112
EF 91
EF 115
M 113
EF 139
CV 36,67,91
M 114
CV 37,115,139
BV 64,1024
M 115
WV 21
BV 88,1280
BV 112,1536
M 116
WL 3
BV 136,1792
M 117
s_barrier
EF 68
EF 92
M 118
mw 72
EF 116
EF 140
M 119
R 0
M 120
ad 84,72
W 0
M 121
CV 38,68,92
CV 39,116,140
M 132
EF 69
EF 93
M 133
T 24,2
M 122
T 28,16
M 123
W 1
EF 117
M 134
EF 141
CV 40,69,93
M 135
T 32,3
M 124
T 36,17
M 125
W 2
CV 41,117,141
M 136
EF 70
EF 94
M 137
WL 8
T 40,12
M 126
T 44,26
M 127
mw 75
EF 118
EF 142
M 138
ad 84,75
W 0
M 139
T 48,13
M 128
WL 8
T 52,27
M 129
CV 42,70,94
CV 43,118,142
M 140
W 1
EF 71
M 141
T 56,0
M 130
T 60,14
M 131
WL 8
R 2
M 142
W 2
EF 95
M 143
T 64,1
M 4
T 68,15
M 5
WL 8
R 4
M 6
EF 119
EF 143
M 7
WL 8
CV 44,71,95
CV 45,119,143
M 8
ST 18,1536
BV 68,1024
M 9
WL 3
BV 92,1280
BV 116,1536
M 10
BV 140,1792
M 11
EF 160
EF 184
M 0
EF 208
EF 232
M 1
CV 10,160,184
CV 11,208,232
M 2
AW 160,10
AW 161,11
M 3
R 6
M 16
EF 161
EF 185
M 17
EF 209
EF 233
M 18
CV 10,161,185
CV 11,209,233
M 19
AW 162,10
AW 163,11
M 20
)"
R"(EF 162
EF 186
M 21
EF 210
EF 234
M 22
CV 10,162,186
CV 11,210,234
M 23
AW 164,10
AW 165,11
M 12
EF 163
EF 187
M 13
EF 211
EF 235
M 14
CV 10,163,187
CV 11,211,235
M 15
WL 2
R 8
M 28
AW 166,10
AW 167,11
M 29
BV 160,2048
BV 184,2304
M 30
BV 208,2560
BV 232,2816
M 31
EF 164
EF 188
M 32
EF 212
EF 236
M 33
CV 10,164,188
CV 11,212,236
M 34
AW 168,10
AW 169,11
M 35
EF 165
EF 189
M 24
EF 213
EF 237
M 25
CV 10,165,189
CV 11,213,237
M 26
AW 170,10
AW 171,11
M 27
WL 6
R 10
M 40
EF 166
EF 190
M 41
EF 214
EF 238
M 42
CV 10,166,190
CV 11,214,238
M 43
AW 172,10
AW 173,11
M 44
EF 167
EF 191
M 45
EF 215
EF 239
M 46
ST 20,2304
CV 10,167,191
M 47
WV 21
CV 11,215,239
AW 174,10
M 36
WL 0
AW 175,11
BV 164,2048
M 37
s_barrier
BV 188,2304
BV 212,2560
M 38
mw 0
BV 236,2816
M 39
R 24
M 52
ad 84,96
W 0
M 53
AR 6,80
AR 7,104
M 54
AR 8,128
AR 9,152
M 55
EF 6
EF 7
M 56
W 1
EF 8
M 57
EF 9
CV 10,6,7
M 58
CV 11,8,9
AW 176,10
M 59
AW 177,11
AR 6,81
M 48
W 2
AR 7,105
M 49
AR 8,129
AR 9,153
M 50
EF 6
EF 7
M 51
R 26
M 64
mw 3
EF 8
EF 9
M 65
ad 84,99
W 0
M 66
CV 10,6,7
CV 11,8,9
M 67
AW 178,10
AW 179,11
M 68
AR 6,82
AR 7,106
M 69
W 1
AR 8,130
M 70
AR 9,154
EF 6
M 71
EF 7
EF 8
M 60
EF 9
CV 10,6,7
M 61
W 2
CV 11,8,9
M 62
AW 180,10
AW 181,11
M 63
WL 0
R 28
M 76
AR 6,83
AR 7,107
M 77
bc 48,2,64,56
AR 8,131
M 78
mh 11
AR 9,155
EF 6
M 79
ad 64,208
H 0
M 80
EF 7
EF 8
M 81
EF 9
CV 10,6,7
M 82
CV 11,8,9
AW 182,10
M 83
H 1
AW 183,11
M 72
ae 42,74,0
BA 80,0
M 73
s_bfe_u32 s52, s50, 0x10001
v_mad_i32_i24 v53, v51, s52, v50
ST 22,0
M 74
BA 104,256
BA 128,512
M 75
R 30
M 88
mh 13
BA 152,768
M 89
ad 66,209
H 0
M 90
AR 6,84
AR 7,108
M 91
AR 8,132
AR 9,156
M 92
EF 6
EF 7
M 93
mh 0
EF 8
EF 9
M 94
ad 64,210
H 0
M 95
CV 10,6,7
CV 11,8,9
M 84
AW 184,10
AW 185,11
M 85
AR 6,85
AR 7,109
M 86
H 1
AR 8,133
M 87
WL 1
R 32
M 100
AR 9,157
EF 6
M 101
EF 7
EF 8
M 102
bc 49,3,66,60
EF 9
M 103
mh 2
CV 10,6,7
CV 11,8,9
M 104
ad 66,304
H 0
M 105
AW 186,10
AW 187,11
M 106
AR 6,86
AR 7,110
M 107
AR 8,134
AR 9,158
M 96
EF 6
EF 7
M 97
EF 8
EF 9
M 98
CV 10,6,7
CV 11,8,9
M 99
R 34
M 112
AW 188,10
AW 189,11
M 113
AR 6,87
AR 7,111
M 114
AR 8,135
AR 9,159
M 115
EF 6
EF 7
M 116
EF 8
EF 9
M 117
CV 10,6,7
CV 11,8,9
M 118
AW 190,10
AW 191,11
M 119
WV 23
BA 84,0
BA 108,256
M 108
WL 2
BA 132,512
BA 156,768
M 109
s_barrier
EF 72
EF 96
M 110
mw 24
EF 120
EF 144
M 111
R 48
M 124
ad 84,120
W 0
M 125
ST 24,768
CV 10,72,96
M 136
CV 11,120,144
AW 192,10
M 137
T 40,8
M 126
T 44,22
M 127
W 1
AW 193,11
M 138
EF 73
EF 97
M 139
T 48,9
M 128
T 52,23
M 129
W 2
EF 121
M 140
EF 145
CV 10,73,97
M 141
WL 8
T 56,4
M 130
T 60,18
M 131
mw 27
CV 11,121,145
AW 194,10
M 142
ad 84,123
W 0
M 143
T 64,5
M 120
WL 8
T 68,19
M 121
AW 195,11
EF 74
M 132
W 1
EF 98
M 133
T 24,6
M 122
T 28,20
M 123
WL 8
R 50
M 134
W 2
EF 122
M 135
T 32,7
M 8
T 36,21
M 9
WL 8
R 52
M 10
EF 146
CV 10,74,98
M 11
WL 8
CV 11,122,146
AW 196,10
M 0
AW 197,11
EF 75
M 1
WL 2
EF 99
)"
R"(EF 123
M 2
EF 147
CV 10,75,99
M 3
CV 11,123,147
AW 198,10
M 4
AW 199,11
BV 72,1024
M 5
BV 96,1280
BV 120,1536
M 6
BV 144,1792
M 7
R 54
M 20
EF 76
EF 100
M 21
EF 124
EF 148
M 22
CV 10,76,100
CV 11,124,148
M 23
ST 26,1536
AW 200,10
M 12
AW 201,11
EF 77
M 13
EF 101
EF 125
M 14
EF 149
CV 10,77,101
M 15
CV 11,125,149
AW 202,10
M 16
AW 203,11
EF 78
M 17
EF 102
EF 126
M 18
EF 150
CV 10,78,102
M 19
WL 2
R 56
M 32
CV 11,126,150
AW 204,10
M 33
AW 205,11
EF 79
M 34
EF 103
EF 127
M 35
EF 151
CV 10,79,103
M 24
CV 11,127,151
AW 206,10
M 25
AW 207,11
BV 76,1024
M 26
BV 100,1280
BV 124,1536
M 27
BV 148,1792
M 28
EF 168
EF 192
M 29
EF 216
EF 240
M 30
CV 10,168,192
CV 11,216,240
M 31
WL 6
R 58
M 44
AW 208,10
AW 209,11
M 45
EF 169
EF 193
M 46
EF 217
EF 241
M 47
CV 10,169,193
CV 11,217,241
M 36
AW 210,10
AW 211,11
M 37
EF 170
EF 194
M 38
EF 218
EF 242
M 39
WV 23
CV 10,170,194
CV 11,218,242
M 40
WL 0
AW 212,10
AW 213,11
M 41
s_barrier
EF 171
EF 195
M 42
mw 48
EF 219
EF 243
M 43
R 72
M 56
ad 84,144
W 0
M 57
CV 10,171,195
CV 11,219,243
M 58
AW 214,10
AW 215,11
M 59
BV 168,2048
BV 192,2304
M 48
W 1
BV 216,2560
M 49
ST 28,2304
BV 240,2816
M 50
ae 44,70,192
M 51
s_bitcmp1_b32 s50, 6
s_cselect_b32 s51, 24576, 0
v_add_u32 v52, s51, v3
M 52
W 2
XL 46,0
M 53
XL 48,768
EF 172
M 54
XL 14,1536
EF 196
M 55
R 74
M 68
mw 51
XL 16,2304
EF 220
M 69
ad 84,147
W 0
M 70
EF 244
CV 10,172,196
M 71
CV 11,220,244
AW 216,10
M 60
AW 217,11
EF 173
M 61
W 1
EF 197
M 62
EF 221
EF 245
M 63
CV 10,173,197
CV 11,221,245
M 64
AW 218,10
AW 219,11
M 65
W 2
EF 174
M 66
EF 198
EF 222
M 67
WL 0
R 76
M 80
EF 246
CV 10,174,198
M 81
bc 48,3,64,56
CV 11,222,246
M 82
mh 3
AW 220,10
AW 221,11
M 83
ad 64,304
H 0
M 72
EF 175
EF 199
M 73
EF 223
EF 247
M 74
CV 10,175,199
CV 11,223,247
M 75
H 1
AW 222,10
M 76
AW 223,11
BV 172,2048
M 77
BV 196,2304
BV 220,2560
M 78
BV 244,2816
M 79
R 78
M 92
mh 5
AR 6,88
AR 7,112
M 93
ad 66,305
H 0
M 94
AR 8,136
EF 6
M 95
EF 7
EF 8
M 84
EF 56
CV 10,6,7
M 85
mh 6
CV 11,8,56
AW 224,10
M 86
ad 64,306
H 0
M 87
ae 42,74,0
AW 225,11
M 88
s_bitcmp1_b32 s50, 0
s_cselect_b32 s52, 24576, 0
v_add_u32 v53, s52, v3
M 89
ST 30,128
AR 6,89
M 90
H 1
AR 7,113
M 91
WL 0
R 80
M 104
AR 8,137
EF 6
M 105
EF 7
EF 8
M 106
bc 49,4,66,60
EF 57
M 107
mh 8
CV 10,6,7
CV 11,8,57
M 96
ad 66,400
H 0
M 97
AW 226,10
AW 227,11
M 98
AR 6,90
AR 7,114
M 99
AR 8,138
EF 6
M 100
PA 12288
DF
EF 7
M 101
EF 8
EF 58
M 102
CV 10,6,7
CV 11,8,58
M 103
R 82
M 116
AW 228,10
AW 229,11
M 117
AR 6,91
AR 7,115
M 118
AR 8,139
EF 6
M 119
EF 7
EF 8
M 108
EF 59
CV 10,6,7
M 109
CV 11,8,59
AW 230,10
M 110
AW 231,11
BA 88,0
M 111
WV 28
BA 112,256
BA 136,512
M 112
WL 3
BV 56,768
M 113
s_barrier
AR 6,92
AR 7,116
AR 8,140
M 114
mw 72
EF 6
EF 7
EF 8
M 115
R 0
EF 60
M 128
ad 84,168
W 0
CV 10,6,7
M 129
CV 11,8,60
AW 232,10
AW 233,11
M 140
AR 6,93
AR 7,117
AR 8,141
M 141
T 56,0
EF 6
M 130
T 60,14
EF 7
M 131
W 1
EF 8
EF 61
M 142
CV 10,6,7
CV 11,8,61
AW 234,10
M 143
T 64,1
AW 235,11
M 120
T 68,15
AR 6,94
M 121
W 2
AR 7,118
AR 8,142
M 132
EF 6
EF 7
EF 8
M 133
WL 8
T 24,10
EF 62
M 122
T 28,24
CV 10,6,7
M 123
mw 75
)"
R"(ST 32,896
CV 11,8,62
AW 236,10
M 134
ad 84,171
W 0
AW 237,11
M 135
T 32,11
AR 6,95
M 124
WL 8
T 36,25
AR 7,119
M 125
AR 8,143
EF 6
EF 7
M 136
W 1
EF 8
EF 63
M 137
T 40,12
CV 10,6,7
M 126
T 44,26
CV 11,8,63
M 127
WL 8
R 2
AW 238,10
M 138
W 2
AW 239,11
BA 92,0
M 139
T 48,13
BA 116,256
M 0
WL 8
T 52,27
BA 140,512
M 1
R 4
BV 60,768
M 2
EF 80
EF 104
M 3
WL 12
EF 128
EF 152
M 4
CV 10,80,104
CV 11,128,152
M 5
WL 4
AW 240,10
AW 241,11
M 6
EF 81
EF 105
M 7
EF 129
EF 153
M 8
CV 10,81,105
CV 11,129,153
M 9
AW 242,10
AW 243,11
M 10
EF 82
EF 106
M 11
R 6
M 12
EF 130
EF 154
M 13
CV 10,82,106
CV 11,130,154
M 14
AW 244,10
AW 245,11
M 15
EF 83
EF 107
M 16
EF 131
EF 155
M 17
CV 10,83,107
CV 11,131,155
M 18
AW 246,10
AW 247,11
M 19
BV 80,1024
BV 104,1280
M 20
BV 128,1536
BV 152,1792
M 21
EF 84
EF 108
M 22
EF 132
EF 156
M 23
WL 6
R 8
M 24
CV 10,84,108
CV 11,132,156
M 25
AW 248,10
AW 249,11
M 26
EF 85
EF 109
M 27
ST 34,1664
EF 133
M 28
EF 157
CV 10,85,109
M 29
CV 11,133,157
AW 250,10
M 30
AW 251,11
EF 86
M 31
EF 110
EF 134
M 32
EF 158
CV 10,86,110
M 33
CV 11,134,158
AW 252,10
M 34
AW 253,11
EF 87
M 35
WL 2
R 10
M 36
EF 111
EF 135
M 37
EF 159
CV 10,87,111
M 38
CV 11,135,159
AW 254,10
M 39
AW 255,11
BV 84,1024
M 40
BV 108,1280
BV 132,1536
M 41
BV 156,1792
M 42
EF 176
EF 200
M 43
WV 28
EF 224
EF 248
M 44
WL 4
CV 18,176,200
CV 19,224,248
M 45
s_barrier
EF 177
EF 201
M 46
mw 0
EF 225
EF 249
M 47
R 24
M 48
ad 84,192
W 0
M 49
CV 20,177,201
CV 21,225,249
M 50
EF 178
EF 202
M 51
EF 226
EF 250
M 52
W 1
CV 22,178,202
M 53
CV 23,226,250
EF 179
M 54
EF 203
EF 227
M 55
EF 251
CV 24,179,203
M 56
W 2
CV 25,227,251
M 57
WL 2
BV 176,2048
BV 200,2304
M 58
BV 224,2560
BV 248,2816
M 59
R 26
M 60
mw 3
EF 180
EF 204
M 61
ad 84,195
W 0
M 62
EF 228
EF 252
M 63
CV 26,180,204
CV 27,228,252
M 64
EF 181
EF 205
M 65
W 1
ST 36,2432
M 66
EF 229
EF 253
M 67
CV 28,181,205
CV 29,229,253
M 68
EF 182
EF 206
M 69
W 2
EF 230
M 70
EF 254
CV 30,182,206
M 71
WL 0
R 28
M 72
CV 31,230,254
EF 183
M 73
bc 48,4,64,56
EF 207
M 74
mh 9
EF 231
EF 255
M 75
ad 64,400
H 0
M 76
CV 32,183,207
CV 33,231,255
M 77
BV 180,2048
BV 204,2304
M 78
BV 228,2560
BV 252,2816
M 79
WV 27
H 1
UL 6,46
M 80
ae 44,68,192
M 81
s_bitcmp1_b32 s50, 7
s_cselect_b32 s51, 24576, 0
v_add_u32 v52, s51, v3
M 82
XL 34,0
UH 7,46
M 83
R 30
M 84
mh 11
XL 36,768
UL 8,47
M 85
ad 66,401
H 0
M 86
UH 9,47
AR 10,72
M 87
AR 11,96
AR 12,120
M 88
AR 13,144
AD 10,6
M 89
mh 12
AD 11,7
AD 12,8
M 90
ad 64,402
H 0
M 91
AD 13,9
AW 72,10
M 92
AW 96,11
AW 120,12
M 93
AW 144,13
UL 6,48
M 94
H 1
UH 7,48
M 95
WL 0
R 32
M 96
UL 8,49
UH 9,49
M 97
AR 10,73
AR 11,97
M 98
bc 49,5,66,60
AR 12,121
M 99
mh 0
AR 13,145
AD 10,6
M 100
ad 66,496
H 0
M 101
AD 11,7
AD 12,8
M 102
AD 13,9
AW 73,10
M 103
ae 42,74,0
AW 97,11
M 104
s_bfe_u32 s52, s50, 0x10001
v_mad_i32_i24 v53, v51, s52, v50
ST 38,128
M 105
s_nop 0
XL 38,1536
AW 121,12
M 106
AW 145,13
UL 6,14
M 107
R 34
M 108
UH 7,14
UL 8,15
M 109
UH 9,15
AR 10,74
M 110
AR 11,98
AR 12,122
M 111
AR 13,146
AD 10,6
M 112
AD 11,7
AD 12,8
M 113
AD 13,9
AW 74,10
M 114
AW 98,11
AW 122,12
M 115
WV 27
AW 146,13
UL 6,16
M 116
WL 0
)"
R"(UH 7,16
UL 8,17
M 117
s_barrier
UH 9,17
AR 10,75
M 118
mw 24
AR 11,99
AR 12,123
M 119
R 48
M 120
ad 84,216
W 0
M 121
AR 13,147
AD 10,6
M 132
AD 11,7
AD 12,8
M 133
T 24,2
M 122
T 28,16
M 123
W 1
AD 13,9
M 134
AW 75,10
AW 99,11
M 135
T 32,3
M 124
T 36,17
M 125
W 2
AW 123,12
M 136
AW 147,13
M 137
T 40,4
M 126
T 44,18
M 127
mw 27
M 138
ad 84,219
W 0
M 139
WL 8
T 48,5
M 128
T 52,19
M 129
M 140
W 1
M 141
T 56,6
M 130
WL 8
T 60,20
M 131
R 50
M 142
W 2
ST 40,896
M 143
T 64,7
M 0
WL 8
T 68,21
M 1
R 52
M 2
XL 40,2304
M 3
M 4
ae 44,68,192
M 5
M 6
s_bfe_u32 s51, s50, 0x10008
v_mad_i32_i24 v52, v51, s51, v50
XL 46,0
M 7
WL 8
XL 48,768
M 8
XL 14,1536
M 9
WL 2
XL 16,2304
M 10
M 11
R 54
M 12
M 13
M 14
M 15
M 16
M 17
M 18
M 19
M 20
M 21
M 22
M 23
WL 2
R 56
M 24
M 25
M 26
M 27
M 28
M 29
M 30
M 31
M 32
M 33
M 34
M 35
WL 2
R 58
M 36
ST 42,1664
M 37
M 38
M 39
M 40
M 41
M 42
WV 20
UL 6,34
UH 7,34
M 43
UL 8,35
UH 9,35
M 44
WL 0
AR 10,76
AR 11,100
M 45
s_barrier
AR 12,124
AR 13,148
M 46
mw 48
AD 10,6
AD 11,7
M 47
R 72
M 48
ad 84,240
W 0
M 49
AD 12,8
AD 13,9
M 50
AW 76,10
AW 100,11
M 51
AW 124,12
AW 148,13
M 52
WV 20
W 1
UL 6,36
M 53
UH 7,36
UL 8,37
M 54
UH 9,37
AR 10,77
M 55
AR 11,101
AR 12,125
M 56
W 2
AR 13,149
M 57
AD 10,6
AD 11,7
M 58
AD 12,8
AD 13,9
M 59
R 74
M 60
mw 51
AW 77,10
AW 101,11
M 61
ad 84,243
W 0
M 62
AW 125,12
AW 149,13
M 63
WV 17
UL 6,38
UH 7,38
M 64
UL 8,39
UH 9,39
M 65
W 1
AR 10,78
M 66
AR 11,102
AR 12,126
M 67
AR 13,150
AD 10,6
M 68
AD 11,7
AD 12,8
M 69
W 2
AD 13,9
M 70
AW 78,10
AW 102,11
M 71
WL 0
R 76
M 72
WV 11
AW 126,12
AW 150,13
M 73
bc 48,5,64,56
UL 6,40
M 74
mh 1
ST 44,2432
UH 7,40
M 75
ad 64,496
H 0
M 76
UL 8,41
UH 9,41
M 77
AR 10,79
AR 11,103
M 78
AR 12,127
AR 13,151
M 79
H 1
AD 10,6
M 80
AD 11,7
AD 12,8
M 81
AD 13,9
AW 79,10
M 82
AW 103,11
AW 127,12
M 83
R 78
M 84
mh 3
AW 151,13
M 85
ad 66,497
H 0
M 86
M 87
M 88
M 89
mh 4
M 90
ad 64,498
H 0
M 91
M 92
M 93
M 94
H 1
M 95
WL 2
R 80
M 96
M 97
M 98
bc 49,6,66,60
M 99
mh 6
M 100
ad 66,592
H 0
M 101
M 102
M 103
M 104
M 105
M 106
M 107
WL 2
R 82
M 108
M 109
M 110
M 111
M 112
ae 42,74,0
M 113
s_bitcmp1_b32 s50, 0
s_cselect_b32 s52, 24576, 0
v_add_u32 v53, s52, v3
M 114
SA 160,256
M 115
M 116
WL 0
M 117
s_barrier
M 118
mw 72
M 119
R 0
M 120
ad 84,264
W 0
M 121
M 132
M 133
T 24,8
M 122
T 28,22
M 123
W 1
M 134
M 135
T 32,9
M 124
T 36,23
M 125
W 2
M 136
M 137
T 40,10
M 126
T 44,24
M 127
mw 75
M 138
ad 84,267
W 0
M 139
WL 8
T 48,11
M 128
T 52,25
M 129
ae 44,70,192
M 140
s_bfe_u32 s51, s50, 0x10009
W 1
v_mad_i32_i24 v52, v51, s51, v50
M 141
T 56,12
M 130
WL 8
T 60,26
M 131
R 2
M 142
W 2
XL 42,0
M 143
T 64,13
M 0
WL 8
T 68,27
M 1
R 4
M 2
XL 44,768
M 3
XL 34,1536
M 4
M 5
M 6
M 7
WL 8
M 8
SA 162,1024
XL 36,2304
M 9
WL 2
M 10
M 11
R 6
M 12
M 13
M 14
M 15
)"
R"(M 16
M 17
M 18
M 19
M 20
M 21
M 22
M 23
WL 2
R 8
M 24
M 25
M 26
M 27
M 28
M 29
M 30
M 31
M 32
M 33
M 34
M 35
WL 2
R 10
M 36
M 37
M 38
M 39
M 40
M 41
M 42
M 43
M 44
WV 26
UL 6,46
UH 7,46
M 45
WL 0
s_barrier
UL 8,47
UH 9,47
M 46
mw 0
SA 164,1792
AR 10,80
M 47
R 24
M 48
ad 84,288
W 0
M 49
AR 11,104
AR 12,128
M 50
AR 13,152
AD 10,6
M 51
AD 11,7
AD 12,8
M 52
W 1
AD 13,9
M 53
AW 80,10
AW 104,11
M 54
AW 128,12
AW 152,13
M 55
UL 6,48
UH 7,48
M 56
W 2
UL 8,49
M 57
UH 9,49
AR 10,81
M 58
AR 11,105
AR 12,129
M 59
R 26
M 60
mw 3
AR 13,153
AD 10,6
M 61
ad 84,291
W 0
M 62
AD 11,7
AD 12,8
M 63
AD 13,9
AW 81,10
M 64
AW 105,11
AW 129,12
M 65
W 1
AW 153,13
M 66
UL 6,14
UH 7,14
M 67
UL 8,15
UH 9,15
M 68
AR 10,82
AR 11,106
M 69
W 2
AR 12,130
M 70
AR 13,154
AD 10,6
M 71
WL 0
R 28
M 72
AD 11,7
AD 12,8
M 73
bc 48,6,64,56
AD 13,9
M 74
mh 7
AW 82,10
AW 106,11
M 75
ad 64,592
H 0
M 76
AW 130,12
AW 154,13
M 77
UL 6,16
UH 7,16
M 78
UL 8,17
UH 9,17
M 79
H 1
AR 10,83
M 80
AR 11,107
AR 12,131
M 81
AR 13,155
AD 10,6
M 82
AD 11,7
AD 12,8
M 83
R 30
M 84
mh 9
SA 166,2560
AD 13,9
M 85
ad 66,593
H 0
M 86
AW 83,10
AW 107,11
M 87
AW 131,12
AW 155,13
M 88
M 89
mh 10
M 90
ad 64,594
H 0
M 91
M 92
M 93
M 94
H 1
M 95
WL 2
R 32
M 96
M 97
M 98
bc 49,7,66,60
M 99
mh 12
M 100
ad 66,688
H 0
M 101
M 102
M 103
M 104
bc 48,7,64,56
M 105
mh 13
M 106
ad 64,688
H 0
M 107
WL 2
R 34
M 108
M 109
M 110
M 111
M 112
M 113
M 114
M 115
M 116
WV 28
M 117
WL 0
s_barrier
M 118
mw 24
M 119
R 48
M 120
ad 84,312
W 0
M 121
M 132
ae 42,74,0
M 133
s_bfe_u32 s52, s50, 0x10001
T 24,0
M 122
T 28,14
M 123
W 1
v_mad_i32_i24 v53, v51, s52, v50
M 134
SA 168,256
M 135
T 32,1
M 124
T 36,15
M 125
W 2
M 136
M 137
T 40,2
M 126
T 44,16
M 127
mw 27
M 138
ad 84,315
W 0
M 139
WL 8
T 48,3
M 128
T 52,17
M 129
ae 44,68,240
M 140
W 1
M 141
T 56,4
M 130
WL 8
T 60,18
M 131
R 50
M 142
W 2
M 143
T 64,5
M 0
WL 8
T 68,19
M 1
R 52
M 2
s_bitcmp1_b32 s50, 10
s_cselect_b32 s51, 24576, 0
M 3
v_add_u32 v52, s51, v3
XL 38,0
M 4
XL 40,768
M 5
XL 46,1536
M 6
M 7
WL 8
M 8
M 9
WL 2
M 10
XL 48,2304
M 11
R 54
M 12
M 13
M 14
M 15
M 16
M 17
M 18
M 19
M 20
SA 170,1024
M 21
M 22
M 23
WL 2
R 56
M 24
M 25
M 26
M 27
M 28
M 29
M 30
M 31
M 32
M 33
M 34
M 35
WL 2
R 58
M 36
M 37
M 38
M 39
M 40
M 41
M 42
M 43
M 44
WV 29
M 45
WL 0
s_barrier
M 46
mw 48
UL 6,42
UH 7,42
M 47
R 72
M 48
ad 84,336
W 0
M 49
UL 8,43
UH 9,43
M 50
AR 10,84
AR 11,108
M 51
AR 12,132
AR 13,156
M 52
W 1
AD 10,6
M 53
AD 11,7
AD 12,8
M 54
AD 13,9
AW 84,10
M 55
AW 108,11
AW 132,12
M 56
W 2
AW 156,13
M 57
UL 6,44
UH 7,44
M 58
SA 172,1792
UL 8,45
M 59
R 74
M 60
mw 51
UH 9,45
AR 10,85
M 61
ad 84,339
W 0
M 62
AR 11,109
AR 12,133
M 63
AR 13,157
AD 10,6
M 64
AD 11,7
AD 12,8
M 65
W 1
AD 13,9
)"
R"(M 66
AW 85,10
AW 109,11
M 67
AW 133,12
AW 157,13
M 68
UL 6,34
UH 7,34
M 69
W 2
UL 8,35
M 70
UH 9,35
AR 10,86
M 71
WL 0
R 76
M 72
AR 11,110
AR 12,134
M 73
mh 0
AR 13,158
AD 10,6
M 74
ad 64,689
H 0
M 75
AD 11,7
AD 12,8
M 76
AD 13,9
AW 86,10
M 77
WV 35
AW 110,11
AW 134,12
M 78
mh 1
AW 158,13
UL 6,36
M 79
ad 66,689
H 0
M 80
UH 7,36
UL 8,37
M 81
UH 9,37
AR 10,87
M 82
AR 11,111
AR 12,135
M 83
R 78
M 84
mh 2
AR 13,159
AD 10,6
M 85
ad 64,690
H 0
M 86
AD 11,7
AD 12,8
M 87
AD 13,9
AW 87,10
M 88
AW 111,11
AW 135,12
M 89
H 1
AW 159,13
M 90
M 91
M 92
M 93
bc 49,8,66,60
M 94
mh 4
M 95
WL 2
R 80
M 96
ad 66,784
H 0
M 97
SA 174,2560
M 98
M 99
M 100
PA 12288
DF
M 101
M 102
M 103
M 104
M 105
M 106
M 107
WL 2
R 82
M 108
M 109
M 110
M 111
M 112
M 113
M 114
M 115
M 116
WV 28
M 117
WL 0
s_barrier
M 118
mw 72
M 119
R 0
M 120
ad 84,360
W 0
M 121
M 132
M 133
T 24,6
M 122
T 28,20
M 123
W 1
M 134
M 135
T 32,7
M 124
T 36,21
M 125
W 2
M 136
M 137
T 40,8
M 126
T 44,22
M 127
mw 75
M 138
ad 84,363
W 0
M 139
WL 8
T 48,9
M 128
T 52,23
M 129
ae 42,72,0
M 140
W 1
M 141
T 56,10
M 130
WL 8
T 60,24
M 131
R 2
M 142
W 2
M 143
T 64,11
M 0
WL 8
T 68,25
M 1
R 4
M 2
s_bitcmp1_b32 s50, 2
s_cselect_b32 s52, 24576, 0
M 3
v_add_u32 v53, s52, v3
SA 176,0
M 4
ae 44,68,240
M 5
M 6
s_bfe_u32 s51, s50, 0x1000b
v_mad_i32_i24 v52, v51, s51, v50
XL 14,0
M 7
WL 8
XL 16,768
M 8
XL 42,1536
M 9
WL 2
M 10
M 11
R 6
M 12
XL 44,2304
M 13
M 14
M 15
M 16
M 17
M 18
M 19
M 20
M 21
M 22
M 23
WL 2
R 8
M 24
M 25
M 26
M 27
M 28
M 29
M 30
M 31
M 32
M 33
M 34
M 35
WL 2
R 10
M 36
M 37
M 38
M 39
M 40
M 41
SA 178,768
M 42
M 43
WV 31
M 44
WL 0
M 45
s_barrier
M 46
mw 0
M 47
R 24
M 48
ad 84,384
W 0
M 49
WV 28
UL 6,38
UH 7,38
M 50
UL 8,39
UH 9,39
M 51
AR 10,88
AR 11,112
M 52
W 1
AR 12,136
M 53
AD 10,6
AD 11,7
M 54
AD 12,8
AD 56,9
M 55
AW 88,10
AW 112,11
M 56
W 2
AW 136,12
M 57
UL 6,40
UH 7,40
M 58
UL 8,41
UH 9,41
M 59
R 26
M 60
mw 3
AR 10,89
AR 11,113
M 61
ad 84,387
W 0
M 62
AR 12,137
AD 10,6
M 63
AD 11,7
AD 12,8
M 64
AD 57,9
AW 89,10
M 65
W 1
AW 113,11
M 66
AW 137,12
UL 6,46
M 67
UH 7,46
UL 8,47
M 68
UH 9,47
AR 10,90
M 69
W 2
AR 11,114
M 70
AR 12,138
AD 10,6
M 71
WL 0
R 28
M 72
AD 11,7
AD 12,8
M 73
bc 48,8,64,56
AD 58,9
M 74
mh 5
AW 90,10
AW 114,11
M 75
ad 64,784
H 0
M 76
AW 138,12
UL 6,48
M 77
UH 7,48
UL 8,49
M 78
UH 9,49
AR 10,91
M 79
H 1
SA 180,1536
M 80
AR 11,115
AR 12,139
M 81
AD 10,6
AD 11,7
M 82
AD 12,8
AD 59,9
M 83
R 30
M 84
mh 7
AW 91,10
AW 115,11
M 85
ad 66,785
H 0
M 86
AW 139,12
M 87
M 88
M 89
mh 8
M 90
ad 64,786
H 0
M 91
M 92
M 93
M 94
H 1
M 95
WL 2
R 32
M 96
M 97
M 98
bc 49,9,66,60
M 99
mh 10
M 100
ad 66,880
H 0
M 101
M 102
M 103
M 104
M 105
M 106
M 107
WL 2
R 34
M 108
M 109
M 110
M 111
M 112
)"
R"(M 113
M 114
M 115
M 116
WV 28
M 117
WL 0
s_barrier
SA 182,2304
M 118
mw 24
M 119
R 48
M 120
ad 84,408
W 0
M 121
M 132
M 133
T 24,12
M 122
T 28,26
M 123
W 1
M 134
M 135
T 32,13
M 124
T 36,27
M 125
W 2
M 136
M 137
T 40,0
M 126
T 44,14
M 127
mw 27
M 138
ad 84,411
W 0
M 139
WL 8
T 48,1
M 128
T 52,15
M 129
M 140
W 1
M 141
T 56,2
M 130
WL 8
T 60,16
M 131
R 50
M 142
W 2
M 143
T 64,3
M 0
WL 8
T 68,17
M 1
R 52
M 2
M 3
M 4
M 5
M 6
M 7
WL 8
M 8
M 9
WL 2
M 10
M 11
R 54
M 12
ae 42,72,0
M 13
s_bfe_u32 s52, s50, 0x10003
v_mad_i32_i24 v53, v51, s52, v50
SA 184,0
M 14
M 15
M 16
M 17
M 18
M 19
M 20
M 21
M 22
M 23
WL 2
R 56
M 24
M 25
M 26
M 27
M 28
M 29
M 30
M 31
M 32
M 33
M 34
M 35
WL 2
R 58
M 36
M 37
M 38
M 39
M 40
M 41
M 42
M 43
M 44
WV 27
M 45
WL 0
s_barrier
M 46
mw 48
M 47
R 72
M 48
ad 84,432
W 0
M 49
M 50
WV 23
UL 6,14
UH 7,14
M 51
SA 186,768
UL 8,15
M 52
W 1
UH 9,15
M 53
AR 10,92
AR 11,116
M 54
AR 12,140
AD 10,6
M 55
AD 11,7
AD 12,8
M 56
W 2
AD 60,9
M 57
AW 92,10
AW 116,11
M 58
AW 140,12
UL 6,16
M 59
R 74
M 60
mw 51
UH 7,16
UL 8,17
M 61
ad 84,435
W 0
M 62
UH 9,17
AR 10,93
M 63
AR 11,117
AR 12,141
M 64
AD 10,6
AD 11,7
M 65
W 1
AD 12,8
M 66
AD 61,9
AW 93,10
M 67
AW 117,11
AW 141,12
M 68
UL 6,42
UH 7,42
M 69
W 2
UL 8,43
M 70
UH 9,43
AR 10,94
M 71
WL 0
R 76
M 72
AR 11,118
AR 12,142
M 73
bc 48,9,64,56
AD 10,6
M 74
mh 11
AD 11,7
AD 12,8
M 75
ad 64,880
H 0
M 76
AD 62,9
AW 94,10
M 77
AW 118,11
AW 142,12
M 78
UL 6,44
UH 7,44
M 79
H 1
UL 8,45
M 80
UH 9,45
AR 10,95
M 81
AR 11,119
AR 12,143
M 82
AD 10,6
AD 11,7
M 83
R 78
M 84
mh 13
AD 12,8
AD 63,9
M 85
ad 66,881
H 0
M 86
AW 95,10
AW 119,11
M 87
AW 143,12
M 88
M 89
mh 0
SA 188,1536
M 90
ad 64,882
H 0
M 91
M 92
M 93
M 94
H 1
M 95
WL 2
R 80
M 96
M 97
M 98
bc 49,10,66,60
M 99
mh 2
M 100
ad 66,976
H 0
M 101
M 102
M 103
M 104
M 105
M 106
M 107
WL 2
R 82
M 108
M 109
M 110
M 111
M 112
M 113
M 114
M 115
M 116
WV 23
M 117
WL 0
s_barrier
M 118
mw 72
M 119
R 0
M 120
ad 84,456
W 0
M 121
M 132
M 133
T 24,4
M 122
T 28,18
M 123
W 1
M 134
M 135
T 32,5
M 124
T 36,19
M 125
W 2
SA 190,2304
M 136
M 137
T 40,6
M 126
T 44,20
M 127
mw 75
M 138
ad 84,459
W 0
M 139
WL 8
T 48,7
M 128
T 52,21
M 129
M 140
W 1
M 141
T 56,8
M 130
WL 8
T 60,22
M 131
R 2
M 142
W 2
M 143
T 64,9
M 0
WL 8
T 68,23
M 1
R 4
M 2
M 3
M 4
M 5
M 6
M 7
WL 8
M 8
M 9
WL 2
M 10
M 11
R 6
M 12
M 13
M 14
M 15
M 16
M 17
M 18
M 19
M 20
M 21
M 22
M 23
WL 2
R 8
M 24
ae 42,72,0
M 25
s_bitcmp1_b32 s50, 2
s_cselect_b32 s52, 24576, 0
v_add_u32 v53, s52, v3
M 26
SA 192,128
M 27
M 28
M 29
M 30
M 31
M 32
ae 44,70,192
M 33
s_bitcmp1_b32 s50, 6
s_cselect_b32 s51, 24576, 0
v_add_u32 v52, s51, v3
M 34
XL 34,128
M 35
WL 2
R 10
)"
R"(M 36
M 37
M 38
XL 36,896
M 39
M 40
M 41
M 42
M 43
WV 25
M 44
WL 0
XL 38,1664
M 45
s_barrier
M 46
mw 0
M 47
R 24
M 48
ad 84,480
W 0
M 49
M 50
XL 40,2432
M 51
M 52
W 1
M 53
M 54
M 55
M 56
W 2
M 57
M 58
M 59
R 26
M 60
mw 3
M 61
ad 84,483
W 0
M 62
M 63
M 64
SA 194,896
M 65
W 1
M 66
M 67
M 68
M 69
W 2
M 70
M 71
WL 2
R 28
M 72
M 73
bc 48,10,64,56
M 74
mh 3
M 75
ad 64,976
H 0
M 76
M 77
M 78
M 79
H 1
M 80
M 81
M 82
M 83
WL 2
R 30
M 84
mh 5
M 85
ad 66,977
H 0
M 86
M 87
M 88
M 89
mh 6
M 90
ad 64,978
H 0
M 91
M 92
M 93
M 94
H 1
M 95
WL 2
R 32
M 96
M 97
M 98
bc 49,11,66,60
M 99
mh 8
M 100
ad 66,1072
H 0
M 101
M 102
SA 196,1664
M 103
M 104
M 105
M 106
M 107
WL 2
R 34
M 108
M 109
M 110
M 111
M 112
M 113
M 114
M 115
M 116
WV 27
M 117
WL 0
s_barrier
M 118
mw 24
M 119
R 48
M 120
ad 84,504
W 0
M 121
M 132
M 133
T 24,10
M 122
T 28,24
M 123
W 1
M 134
M 135
T 32,11
M 124
T 36,25
M 125
W 2
M 136
M 137
T 40,12
M 126
T 44,26
M 127
mw 27
M 138
ad 84,507
W 0
M 139
WL 8
T 48,13
M 128
T 52,27
M 129
M 140
W 1
M 141
T 56,0
M 130
WL 8
T 60,14
M 131
R 50
M 142
W 2
SA 198,2432
M 143
T 64,1
M 0
WL 8
T 68,15
M 1
R 52
M 2
M 3
M 4
M 5
M 6
M 7
WL 8
M 8
M 9
WL 2
M 10
M 11
R 54
M 12
M 13
M 14
M 15
M 16
M 17
M 18
M 19
M 20
M 21
M 22
M 23
WL 2
R 56
M 24
M 25
M 26
M 27
M 28
M 29
M 30
M 31
M 32
M 33
M 34
ae 44,68,192
M 35
WL 2
R 58
M 36
ae 42,72,0
M 37
s_bfe_u32 s52, s50, 0x10003
v_mad_i32_i24 v53, v51, s52, v50
SA 200,128
M 38
s_bitcmp1_b32 s50, 7
s_cselect_b32 s51, 24576, 0
v_add_u32 v52, s51, v3
M 39
XL 46,128
M 40
XL 48,896
M 41
M 42
M 43
WV 29
M 44
WL 0
M 45
s_barrier
M 46
mw 48
XL 14,1664
M 47
R 72
M 48
ad 84,528
W 0
M 49
M 50
M 51
M 52
W 1
XL 16,2432
M 53
M 54
M 55
M 56
W 2
M 57
M 58
M 59
R 74
M 60
mw 51
M 61
ad 84,531
W 0
M 62
M 63
M 64
M 65
W 1
M 66
M 67
M 68
M 69
W 2
M 70
M 71
WL 2
R 76
M 72
M 73
bc 48,11,64,56
M 74
mh 9
M 75
ad 64,1072
H 0
M 76
SA 202,896
M 77
M 78
M 79
H 1
M 80
M 81
M 82
M 83
WL 2
R 78
M 84
mh 11
M 85
ad 66,1073
H 0
M 86
M 87
M 88
WV 37
UL 6,34
UH 7,34
M 89
mh 12
UL 8,35
UH 9,35
M 90
ad 64,1074
H 0
M 91
AD 64,6
AD 88,7
M 92
AD 112,8
AD 136,9
M 93
UL 6,36
UH 7,36
M 94
H 1
UL 8,37
M 95
WL 2
R 80
M 96
UH 9,37
AD 65,6
M 97
AD 89,7
AD 113,8
M 98
bc 49,12,66,60
AD 137,9
M 99
mh 0
UL 6,38
UH 7,38
M 100
ad 66,1168
H 0
M 101
UL 8,39
UH 9,39
M 102
AD 66,6
AD 90,7
M 103
WV 38
AD 114,8
AD 138,9
M 104
PA 12288
DF
UL 6,40
M 105
UH 7,40
UL 8,41
M 106
UH 9,41
AD 67,6
M 107
WL 0
R 82
M 108
AD 91,7
AD 115,8
M 109
AD 139,9
M 110
M 111
M 112
M 113
M 114
SA 204,1664
M 115
WV 29
M 116
WL 0
M 117
)"
R"(s_barrier
M 118
mw 72
M 119
R 0
M 120
ad 84,552
W 0
M 121
M 132
M 133
T 24,2
M 122
T 28,16
M 123
W 1
M 134
M 135
T 32,3
M 124
T 36,17
M 125
W 2
M 136
M 137
T 40,4
M 126
T 44,18
M 127
mw 75
M 138
ad 84,555
W 0
M 139
WL 8
T 48,5
M 128
T 52,19
M 129
M 140
W 1
M 141
T 56,6
M 130
WL 8
T 60,20
M 131
R 2
M 142
W 2
M 143
T 64,7
M 0
WL 8
T 68,21
M 1
R 4
M 2
M 3
M 4
M 5
M 6
M 7
WL 8
M 8
SA 206,2432
M 9
WL 2
M 10
M 11
R 6
M 12
M 13
M 14
M 15
M 16
M 17
M 18
M 19
M 20
M 21
M 22
M 23
WL 2
R 8
M 24
M 25
M 26
M 27
M 28
M 29
M 30
M 31
M 32
M 33
M 34
M 35
WL 2
R 10
M 36
ae 44,68,192
M 37
s_bfe_u32 s51, s50, 0x10008
v_mad_i32_i24 v52, v51, s51, v50
XL 42,128
M 38
M 39
M 40
M 41
M 42
XL 44,896
M 43
WV 30
M 44
WL 0
M 45
s_barrier
M 46
mw 0
M 47
R 24
M 48
ad 84,576
W 0
M 49
ae 42,72,0
XL 34,1664
M 50
s_bitcmp1_b32 s50, 2
s_cselect_b32 s52, 24576, 0
v_add_u32 v53, s52, v3
M 51
SA 208,256
M 52
W 1
M 53
M 54
XL 36,2432
M 55
M 56
W 2
M 57
M 58
M 59
R 26
M 60
mw 3
M 61
ad 84,579
W 0
M 62
M 63
M 64
M 65
W 1
M 66
M 67
M 68
M 69
W 2
M 70
M 71
WL 2
R 28
M 72
M 73
bc 48,12,64,56
M 74
mh 1
M 75
ad 64,1168
H 0
M 76
M 77
M 78
M 79
H 1
M 80
M 81
M 82
M 83
WL 2
R 30
M 84
mh 3
M 85
ad 66,1169
H 0
M 86
M 87
M 88
M 89
mh 4
SA 210,1024
M 90
ad 64,1170
H 0
M 91
WV 39
UL 6,46
UH 7,46
M 92
UL 8,47
UH 9,47
M 93
AD 68,6
AD 92,7
M 94
H 1
AD 116,8
M 95
WL 2
R 32
M 96
AD 140,9
UL 6,48
M 97
UH 7,48
UL 8,49
M 98
bc 49,13,66,60
UH 9,49
M 99
mh 6
AD 69,6
AD 93,7
M 100
ad 66,1264
H 0
M 101
AD 117,8
AD 141,9
M 102
UL 6,14
UH 7,14
M 103
UL 8,15
UH 9,15
M 104
AD 70,6
AD 94,7
M 105
AD 118,8
AD 142,9
M 106
WV 38
UL 6,16
UH 7,16
M 107
WL 0
R 34
M 108
UL 8,17
UH 9,17
M 109
AD 71,6
AD 95,7
M 110
AD 119,8
AD 143,9
M 111
M 112
M 113
M 114
M 115
WV 28
M 116
WL 0
M 117
s_barrier
M 118
mw 24
M 119
R 48
M 120
ad 84,600
W 0
M 121
M 132
M 133
T 24,8
M 122
T 28,22
M 123
W 1
M 134
M 135
T 32,9
M 124
T 36,23
M 125
W 2
SA 212,1792
M 136
M 137
T 40,10
M 126
T 44,24
M 127
mw 27
M 138
ad 84,603
W 0
M 139
WL 8
T 48,11
M 128
T 52,25
M 129
M 140
W 1
M 141
T 56,12
M 130
WL 8
T 60,26
M 131
R 50
M 142
W 2
M 143
T 64,13
M 0
WL 8
T 68,27
M 1
R 52
M 2
M 3
M 4
M 5
M 6
M 7
WL 8
M 8
M 9
WL 2
M 10
M 11
R 54
M 12
M 13
M 14
M 15
M 16
M 17
M 18
M 19
M 20
M 21
M 22
M 23
WL 2
R 56
M 24
SA 214,2560
M 25
M 26
M 27
M 28
M 29
M 30
M 31
M 32
M 33
M 34
M 35
WL 2
R 58
M 36
M 37
M 38
ae 44,70,192
M 39
s_bfe_u32 s51, s50, 0x10009
v_mad_i32_i24 v52, v51, s51, v50
XL 38,128
M 40
M 41
M 42
M 43
WV 28
M 44
WL 0
XL 40,896
M 45
s_barrier
M 46
mw 48
M 47
R 72
M 48
ad 84,624
W 0
M 49
M 50
XL 46,1664
M 51
M 52
W 1
)"
R"(M 53
M 54
M 55
M 56
W 2
XL 48,2432
M 57
M 58
M 59
R 74
M 60
mw 51
M 61
ad 84,627
W 0
M 62
ae 42,72,0
M 63
s_bfe_u32 s52, s50, 0x10003
v_mad_i32_i24 v53, v51, s52, v50
SA 216,256
M 64
M 65
W 1
M 66
M 67
M 68
M 69
W 2
M 70
M 71
WL 2
R 76
M 72
M 73
bc 48,13,64,56
M 74
mh 7
M 75
ad 64,1264
H 0
M 76
M 77
M 78
M 79
H 1
M 80
M 81
M 82
M 83
WL 2
R 78
M 84
mh 9
M 85
ad 66,1265
H 0
M 86
M 87
M 88
M 89
mh 10
M 90
ad 64,1266
H 0
M 91
M 92
WV 39
UL 6,42
UH 7,42
M 93
UL 8,43
UH 9,43
M 94
H 1
AD 72,6
M 95
WL 2
R 80
M 96
AD 96,7
AD 120,8
M 97
AD 144,9
UL 6,44
M 98
bc 49,14,66,60
UH 7,44
M 99
mh 12
UL 8,45
UH 9,45
M 100
ad 66,1360
H 0
M 101
SA 218,1024
AD 73,6
M 102
AD 97,7
AD 121,8
M 103
WV 40
AD 145,9
UL 6,34
M 104
bc 48,14,64,56
UH 7,34
M 105
mh 13
UL 8,35
UH 9,35
M 106
ad 64,1360
H 0
M 107
WL 0
R 82
M 108
AD 74,6
AD 98,7
M 109
AD 122,8
AD 146,9
M 110
WV 38
UL 6,36
UH 7,36
M 111
UL 8,37
UH 9,37
M 112
AD 75,6
AD 99,7
M 113
AD 123,8
AD 147,9
M 114
M 115
WV 28
M 116
WL 0
M 117
s_barrier
M 118
mw 72
M 119
R 0
M 120
ad 84,648
W 0
M 121
M 132
M 133
T 24,0
M 122
T 28,14
M 123
W 1
M 134
M 135
T 32,1
M 124
T 36,15
M 125
W 2
M 136
M 137
T 40,2
M 126
T 44,16
M 127
mw 75
M 138
ad 84,651
W 0
M 139
WL 8
T 48,3
M 128
T 52,17
M 129
M 140
W 1
M 141
T 56,4
M 130
WL 8
T 60,18
M 131
R 2
M 142
W 2
SA 220,1792
M 143
T 64,5
M 0
WL 8
T 68,19
M 1
R 4
M 2
M 3
M 4
M 5
M 6
M 7
WL 8
M 8
M 9
WL 2
M 10
M 11
R 6
M 12
M 13
M 14
M 15
M 16
M 17
M 18
M 19
M 20
M 21
M 22
M 23
WL 2
R 8
M 24
M 25
M 26
M 27
M 28
M 29
M 30
M 31
M 32
M 33
M 34
M 35
WL 2
R 10
M 36
SA 222,2560
M 37
M 38
M 39
M 40
ae 44,68,240
M 41
s_bitcmp1_b32 s50, 10
s_cselect_b32 s51, 24576, 0
v_add_u32 v52, s51, v3
M 42
XL 14,128
M 43
WV 29
M 44
WL 0
M 45
s_barrier
M 46
mw 0
XL 16,896
M 47
R 24
M 48
ad 84,672
W 0
M 49
M 50
M 51
M 52
W 1
XL 42,1664
M 53
M 54
M 55
M 56
W 2
M 57
M 58
XL 44,2432
M 59
R 26
M 60
mw 3
M 61
ad 84,675
W 0
M 62
M 63
M 64
M 65
W 1
M 66
M 67
M 68
M 69
W 2
M 70
M 71
WL 2
R 28
M 72
M 73
mh 0
M 74
ad 64,1361
H 0
M 75
ae 42,72,48
M 76
s_bitcmp1_b32 s50, 4
s_cselect_b32 s52, 24576, 0
v_add_u32 v53, s52, v3
M 77
SA 224,0
M 78
mh 1
M 79
ad 66,1361
H 0
M 80
M 81
M 82
M 83
WL 2
R 30
M 84
mh 2
M 85
ad 64,1362
H 0
M 86
M 87
M 88
M 89
H 1
M 90
M 91
M 92
WV 40
M 93
bc 49,15,66,60
M 94
mh 4
UL 6,38
UH 7,38
M 95
WL 2
R 32
M 96
ad 66,1456
H 0
M 97
UL 8,39
UH 9,39
M 98
AD 76,6
AD 100,7
M 99
AD 124,8
AD 148,9
M 100
UL 6,40
UH 7,40
M 101
UL 8,41
UH 9,41
M 102
AD 77,6
AD 101,7
M 103
AD 125,8
AD 149,9
M 104
WV 39
UL 6,46
UH 7,46
M 105
UL 8,47
UH 9,47
M 106
AD 78,6
AD 102,7
M 107
WL 1
R 34
M 108
AD 126,8
AD 150,9
M 109
WV 36
UL 6,48
UH 7,48
M 110
UL 8,49
UH 9,49
M 111
)"
R"(AD 79,6
AD 103,7
M 112
AD 127,8
AD 151,9
M 113
M 114
M 115
WV 27
SA 226,768
M 116
WL 0
M 117
s_barrier
M 118
mw 24
M 119
R 48
M 120
ad 84,696
W 0
M 121
M 132
M 133
T 24,6
M 122
T 28,20
M 123
W 1
M 134
M 135
T 32,7
M 124
T 36,21
M 125
W 2
M 136
M 137
T 40,8
M 126
T 44,22
M 127
mw 27
M 138
ad 84,699
W 0
M 139
WL 8
T 48,9
M 128
T 52,23
M 129
M 140
W 1
M 141
T 56,10
M 130
WL 8
T 60,24
M 131
R 50
M 142
W 2
M 143
T 64,11
M 0
WL 8
T 68,25
M 1
R 52
M 2
M 3
M 4
M 5
M 6
M 7
WL 8
M 8
M 9
WL 2
SA 228,1536
M 10
M 11
R 54
M 12
M 13
M 14
M 15
M 16
M 17
M 18
M 19
M 20
M 21
M 22
M 23
WL 2
R 56
M 24
M 25
M 26
M 27
M 28
M 29
M 30
M 31
M 32
M 33
M 34
M 35
WL 2
R 58
M 36
M 37
M 38
M 39
M 40
M 41
M 42
ae 44,68,240
s_bfe_u32 s51, s50, 0x1000b
M 43
WV 26
v_mad_i32_i24 v52, v51, s51, v50
XL 34,128
M 44
WL 0
M 45
s_barrier
M 46
mw 48
M 47
R 72
M 48
ad 84,720
W 0
M 49
SA 230,2304
XL 36,896
M 50
M 51
M 52
W 1
M 53
M 54
XL 38,1664
M 55
M 56
W 2
M 57
M 58
M 59
R 74
M 60
mw 51
XL 40,2432
M 61
ad 84,723
W 0
M 62
M 63
M 64
M 65
W 1
M 66
M 67
M 68
M 69
W 2
M 70
M 71
WL 2
R 76
M 72
M 73
bc 48,15,64,56
M 74
mh 5
M 75
ad 64,1456
H 0
M 76
M 77
M 78
M 79
H 1
M 80
M 81
M 82
M 83
WL 2
R 78
M 84
mh 7
M 85
ad 66,1457
H 0
M 86
M 87
ae 42,72,48
M 88
s_bfe_u32 s52, s50, 0x10005
v_mad_i32_i24 v53, v51, s52, v50
SA 232,0
M 89
mh 8
M 90
ad 64,1458
H 0
M 91
M 92
M 93
M 94
H 1
M 95
WL 2
R 80
M 96
WV 39
UL 6,14
UH 7,14
M 97
UL 8,15
UH 9,15
M 98
bc 49,16,66,60
AD 80,6
M 99
mh 10
AD 104,7
AD 128,8
M 100
ad 66,1552
H 0
M 101
AD 152,9
UL 6,16
M 102
UH 7,16
UL 8,17
M 103
UH 9,17
AD 81,6
M 104
PA 12288
DF
AD 105,7
M 105
AD 129,8
AD 153,9
M 106
WV 38
UL 6,42
UH 7,42
M 107
WL 0
R 82
M 108
UL 8,43
UH 9,43
M 109
AD 82,6
AD 106,7
M 110
AD 130,8
AD 154,9
M 111
WV 36
UL 6,44
UH 7,44
M 112
UL 8,45
UH 9,45
M 113
AD 83,6
AD 107,7
M 114
AD 131,8
AD 155,9
M 115
WV 28
M 116
WL 0
M 117
s_barrier
M 118
mw 72
M 119
R 0
M 120
ad 84,744
W 0
M 121
M 132
M 133
T 24,12
M 122
T 28,26
M 123
W 1
M 134
SA 234,768
M 135
T 32,13
M 124
T 36,27
M 125
W 2
M 136
M 137
T 40,0
M 126
T 44,14
M 127
mw 75
M 138
ad 84,747
W 0
M 139
WL 8
T 48,1
M 128
T 52,15
M 129
M 140
W 1
M 141
T 56,2
M 130
WL 8
T 60,16
M 131
R 2
M 142
W 2
M 143
T 64,3
M 0
WL 8
T 68,17
M 1
R 4
M 2
M 3
M 4
M 5
M 6
M 7
WL 8
M 8
M 9
WL 2
M 10
M 11
R 6
M 12
M 13
M 14
M 15
M 16
M 17
M 18
M 19
M 20
SA 236,1536
M 21
M 22
M 23
WL 2
R 8
M 24
M 25
M 26
M 27
M 28
M 29
M 30
M 31
M 32
M 33
M 34
M 35
WL 2
R 10
M 36
M 37
M 38
M 39
M 40
M 41
M 42
M 43
M 44
WV 28
M 45
WL 0
s_barrier
M 46
mw 0
M 47
)"
R"(R 24
M 48
ad 84,768
W 0
M 49
M 50
M 51
M 52
W 1
M 53
M 54
M 55
M 56
W 2
M 57
M 58
SA 238,2304
M 59
R 26
M 60
mw 3
M 61
ad 84,771
W 0
M 62
M 63
M 64
M 65
W 1
M 66
M 67
M 68
M 69
W 2
M 70
M 71
WL 2
R 28
M 72
M 73
bc 48,16,64,56
M 74
mh 11
M 75
ad 64,1552
H 0
M 76
M 77
M 78
M 79
H 1
M 80
M 81
M 82
M 83
WL 2
R 30
M 84
mh 13
M 85
ad 66,1553
H 0
M 86
M 87
M 88
M 89
mh 0
M 90
ad 64,1554
H 0
M 91
M 92
M 93
M 94
H 1
M 95
WL 2
R 32
M 96
ae 42,72,48
M 97
s_bitcmp1_b32 s50, 4
s_cselect_b32 s52, 24576, 0
v_add_u32 v53, s52, v3
M 98
bc 49,17,66,60
SA 240,128
M 99
mh 2
WV 39
UL 6,34
UH 7,34
M 100
ad 66,1648
H 0
M 101
UL 8,35
UH 9,35
M 102
AD 84,6
AD 108,7
M 103
AD 132,8
AD 156,9
M 104
WV 37
UL 6,36
UH 7,36
M 105
UL 8,37
UH 9,37
M 106
AD 85,6
AD 109,7
M 107
WL 0
R 34
M 108
AD 133,8
AD 157,9
M 109
WV 35
UL 6,38
UH 7,38
M 110
UL 8,39
UH 9,39
M 111
AD 86,6
AD 110,7
M 112
AD 134,8
AD 158,9
M 113
WV 33
UL 6,40
UH 7,40
M 114
UL 8,41
UH 9,41
M 115
WV 24
AD 87,6
AD 111,7
M 116
WL 0
AD 135,8
AD 159,9
M 117
s_barrier
M 118
mw 24
M 119
R 48
M 120
ad 84,792
W 0
M 121
M 132
M 133
T 24,4
M 122
T 28,18
M 123
W 1
M 134
M 135
T 32,5
M 124
T 36,19
M 125
W 2
M 136
M 137
T 40,6
M 126
T 44,20
M 127
mw 27
M 138
ad 84,795
W 0
M 139
WL 8
T 48,7
M 128
T 52,21
M 129
SA 242,896
M 140
W 1
M 141
T 56,8
M 130
WL 8
T 60,22
M 131
R 50
M 142
W 2
M 143
T 64,9
M 0
WL 8
T 68,23
M 1
R 52
M 2
M 3
M 4
M 5
M 6
M 7
WL 8
M 8
M 9
WL 2
M 10
M 11
R 54
M 12
M 13
M 14
M 15
M 16
M 17
M 18
M 19
M 20
M 21
M 22
M 23
WL 2
R 56
M 24
M 25
M 26
M 27
M 28
M 29
M 30
M 31
SA 244,1664
M 32
M 33
M 34
M 35
WL 2
R 58
M 36
M 37
M 38
M 39
M 40
M 41
M 42
M 43
M 44
WV 23
M 45
WL 0
s_barrier
M 46
mw 48
M 47
R 72
M 48
ad 84,816
W 0
M 49
M 50
M 51
M 52
W 1
M 53
M 54
M 55
M 56
W 2
M 57
M 58
M 59
R 74
M 60
mw 51
M 61
ad 84,819
W 0
M 62
M 63
M 64
M 65
W 1
M 66
M 67
M 68
M 69
W 2
SA 246,2432
M 70
ae 44,70,192
M 71
WL 2
R 76
M 72
s_bitcmp1_b32 s50, 6
s_cselect_b32 s51, 24576, 0
v_add_u32 v52, s51, v3
M 73
bc 48,17,64,56
XL 46,256
M 74
mh 3
M 75
ad 64,1648
H 0
M 76
XL 48,1024
M 77
M 78
M 79
H 1
M 80
M 81
M 82
XL 14,1792
M 83
WL 2
R 78
M 84
mh 5
M 85
ad 66,1649
H 0
M 86
M 87
M 88
XL 16,2560
M 89
mh 6
M 90
ad 64,1650
H 0
M 91
M 92
M 93
M 94
H 1
M 95
WL 2
R 80
M 96
M 97
M 98
bc 49,18,66,60
M 99
mh 8
M 100
ad 66,1744
H 0
M 101
M 102
M 103
M 104
M 105
M 106
M 107
WL 2
R 82
M 108
ae 42,72,48
M 109
s_bfe_u32 s52, s50, 0x10005
v_mad_i32_i24 v53, v51, s52, v50
SA 248,128
M 110
M 111
M 112
M 113
M 114
M 115
WV 28
M 116
WL 0
M 117
s_barrier
M 118
mw 72
M 119
R 0
M 120
ad 84,840
W 0
M 121
M 132
M 133
T 24,10
M 122
T 28,24
M 123
)"
R"(W 1
M 134
M 135
T 32,11
M 124
T 36,25
M 125
W 2
M 136
M 137
T 40,12
M 126
T 44,26
M 127
mw 75
M 138
ad 84,843
W 0
M 139
WL 8
T 48,13
M 128
T 52,27
M 129
M 140
W 1
M 141
T 56,0
M 130
WL 8
T 60,14
M 131
R 2
M 142
W 2
M 143
T 64,1
M 0
WL 8
T 68,15
M 1
R 4
M 2
M 3
SA 250,896
M 4
M 5
M 6
M 7
WL 8
M 8
M 9
WL 2
M 10
M 11
R 6
M 12
M 13
M 14
M 15
M 16
M 17
M 18
M 19
M 20
M 21
M 22
M 23
WL 2
R 8
M 24
M 25
M 26
M 27
M 28
M 29
M 30
M 31
M 32
M 33
M 34
M 35
WL 2
R 10
M 36
M 37
M 38
M 39
M 40
M 41
SA 252,1664
M 42
M 43
WV 27
M 44
WL 0
M 45
s_barrier
M 46
mw 0
M 47
R 24
M 48
ad 84,864
W 0
M 49
M 50
M 51
M 52
W 1
M 53
M 54
M 55
M 56
W 2
M 57
M 58
M 59
R 26
M 60
mw 3
M 61
ad 84,867
W 0
M 62
M 63
M 64
M 65
W 1
M 66
M 67
M 68
M 69
W 2
M 70
M 71
WL 2
R 28
M 72
M 73
bc 48,18,64,56
M 74
mh 9
M 75
ad 64,1744
H 0
M 76
ae 44,68,192
M 77
s_bitcmp1_b32 s50, 7
s_cselect_b32 s51, 24576, 0
v_add_u32 v52, s51, v3
M 78
XL 42,256
M 79
H 1
SA 254,2432
M 80
XL 44,1024
M 81
M 82
M 83
WL 2
R 30
M 84
mh 11
M 85
ad 66,1745
H 0
M 86
XL 34,1792
M 87
M 88
M 89
mh 12
M 90
ad 64,1746
H 0
M 91
M 92
XL 36,2560
M 93
M 94
H 1
M 95
WL 2
R 32
M 96
M 97
M 98
bc 49,19,66,60
M 99
mh 0
M 100
ad 66,1840
H 0
M 101
M 102
M 103
M 104
M 105
M 106
M 107
WL 2
R 34
M 108
M 109
M 110
M 111
M 112
M 113
M 114
M 115
M 116
WV 27
M 117
WL 0
s_barrier
M 118
mw 24
M 119
R 48
M 120
ad 84,888
W 0
M 121
ae 42,72,48
M 132
s_bitcmp1_b32 s50, 4
s_cselect_b32 s52, 24576, 0
v_add_u32 v53, s52, v3
M 133
T 24,2
M 122
T 28,16
M 123
W 1
ST 18,256
M 134
UL 6,46
UH 7,46
M 135
T 32,3
M 124
T 36,17
M 125
W 2
UL 8,47
M 136
UH 9,47
AD 160,6
M 137
T 40,4
M 126
T 44,18
M 127
mw 27
AD 184,7
AD 208,8
M 138
ad 84,891
W 0
M 139
WL 8
T 48,5
M 128
T 52,19
M 129
AD 232,9
UL 6,48
M 140
W 1
UH 7,48
M 141
T 56,6
M 130
WL 8
T 60,20
M 131
R 50
M 142
W 2
UL 8,49
M 143
T 64,7
M 0
WL 8
T 68,21
M 1
R 52
M 2
UH 9,49
AD 161,6
M 3
AD 185,7
AD 209,8
M 4
AD 233,9
UL 6,14
M 5
UH 7,14
UL 8,15
M 6
UH 9,15
AD 162,6
M 7
WL 8
AD 186,7
AD 210,8
M 8
AD 234,9
UL 6,16
M 9
WL 2
UH 7,16
UL 8,17
M 10
UH 9,17
AD 163,6
M 11
R 54
M 12
AD 187,7
AD 211,8
M 13
AD 235,9
M 14
M 15
M 16
M 17
M 18
M 19
ST 20,1024
M 20
M 21
M 22
M 23
WL 2
R 56
M 24
M 25
M 26
M 27
M 28
M 29
M 30
M 31
M 32
M 33
M 34
M 35
WL 2
R 58
M 36
M 37
M 38
M 39
M 40
M 41
M 42
M 43
M 44
WV 27
M 45
WL 0
s_barrier
M 46
mw 48
M 47
R 72
M 48
ad 84,912
W 0
M 49
M 50
M 51
M 52
W 1
M 53
M 54
M 55
M 56
W 2
M 57
ST 22,1792
M 58
M 59
R 74
M 60
mw 51
M 61
ad 84,915
W 0
M 62
M 63
M 64
M 65
W 1
M 66
M 67
)"
R"(M 68
M 69
W 2
M 70
M 71
WL 2
R 76
M 72
M 73
bc 48,19,64,56
M 74
mh 1
M 75
ad 64,1840
H 0
M 76
M 77
M 78
ae 44,68,192
M 79
s_bfe_u32 s51, s50, 0x10008
H 1
v_mad_i32_i24 v52, v51, s51, v50
M 80
XL 38,256
M 81
M 82
M 83
WL 2
R 78
M 84
mh 3
XL 40,1024
M 85
ad 66,1841
H 0
M 86
M 87
M 88
M 89
mh 4
M 90
ad 64,1842
H 0
M 91
XL 18,1792
M 92
M 93
M 94
H 1
M 95
WL 2
R 80
M 96
ST 24,2560
XL 46,2560
M 97
M 98
bc 49,20,66,60
M 99
mh 6
M 100
ad 66,1936
H 0
M 101
M 102
M 103
M 104
PA 12288
DF
M 105
M 106
M 107
WL 2
R 82
M 108
M 109
M 110
M 111
M 112
M 113
M 114
M 115
M 116
WV 28
M 117
WL 0
s_barrier
M 118
mw 72
M 119
R 0
M 120
ad 84,936
W 0
M 121
M 132
M 133
T 24,8
M 122
T 28,22
M 123
W 1
M 134
M 135
T 32,9
M 124
T 36,23
M 125
W 2
M 136
UL 6,42
UH 7,42
M 137
T 40,10
M 126
T 44,24
M 127
mw 75
UL 8,43
UH 9,43
M 138
ad 84,939
W 0
M 139
WL 8
T 48,11
M 128
T 52,25
M 129
ae 42,72,48
AD 164,6
M 140
s_bfe_u32 s52, s50, 0x10005
W 1
v_mad_i32_i24 v53, v51, s52, v50
M 141
T 56,12
M 130
WL 8
T 60,26
M 131
R 2
M 142
W 2
ST 26,256
M 143
T 64,13
M 0
WL 8
T 68,27
M 1
R 4
M 2
AD 188,7
AD 212,8
M 3
AD 236,9
UL 6,44
M 4
UH 7,44
UL 8,45
M 5
UH 9,45
AD 165,6
M 6
AD 189,7
AD 213,8
M 7
WL 8
AD 237,9
UL 6,34
M 8
UH 7,34
UL 8,35
M 9
WL 2
UH 9,35
AD 166,6
M 10
AD 190,7
AD 214,8
M 11
R 6
M 12
AD 238,9
UL 6,36
M 13
UH 7,36
UL 8,37
M 14
UH 9,37
AD 167,6
M 15
AD 191,7
AD 215,8
M 16
AD 239,9
M 17
M 18
M 19
M 20
M 21
M 22
M 23
WL 2
R 8
M 24
M 25
M 26
M 27
M 28
M 29
M 30
M 31
M 32
M 33
M 34
M 35
WL 2
R 10
M 36
ST 28,1024
M 37
M 38
M 39
M 40
M 41
M 42
M 43
WV 28
M 44
WL 0
M 45
s_barrier
M 46
mw 0
M 47
R 24
M 48
ad 84,960
W 0
M 49
M 50
M 51
M 52
W 1
M 53
M 54
M 55
M 56
W 2
M 57
M 58
M 59
R 26
M 60
mw 3
M 61
ad 84,963
W 0
M 62
M 63
M 64
M 65
W 1
M 66
M 67
M 68
M 69
W 2
M 70
M 71
WL 2
R 28
M 72
M 73
bc 48,20,64,56
M 74
mh 7
ST 30,1792
M 75
ad 64,1936
H 0
M 76
M 77
M 78
M 79
H 1
M 80
M 81
M 82
ae 44,70,192
M 83
WL 2
R 30
M 84
mh 9
M 85
ad 66,1937
H 0
M 86
s_bfe_u32 s51, s50, 0x10009
v_mad_i32_i24 v52, v51, s51, v50
XL 48,256
M 87
M 88
XL 14,1024
M 89
mh 10
M 90
ad 64,1938
H 0
M 91
M 92
M 93
M 94
H 1
XL 16,1792
M 95
WL 2
R 32
M 96
M 97
M 98
bc 49,21,66,60
M 99
mh 12
M 100
ad 66,2032
H 0
M 101
XL 20,2560
M 102
M 103
M 104
bc 48,21,64,56
M 105
mh 13
M 106
ad 64,2032
H 0
M 107
WL 2
R 34
M 108
M 109
M 110
M 111
M 112
ST 32,2560
M 113
M 114
M 115
WV 31
M 116
WL 0
M 117
s_barrier
M 118
mw 24
M 119
R 48
M 120
ad 84,984
W 0
M 121
M 132
M 133
T 24,0
M 122
T 28,14
M 123
W 1
M 134
M 135
T 32,1
M 124
T 36,15
M 125
W 2
M 136
M 137
T 40,2
M 126
T 44,16
M 127
mw 27
M 138
ad 84,987
W 0
M 139
WL 8
T 48,3
M 128
T 52,17
M 129
UL 6,38
UH 7,38
M 140
)"
R"(W 1
UL 8,39
M 141
T 56,4
M 130
WL 8
T 60,18
M 131
R 50
M 142
W 2
UH 9,39
M 143
T 64,5
M 0
WL 8
T 68,19
M 1
R 52
M 2
AD 168,6
AD 192,7
M 3
AD 216,8
AD 240,9
M 4
UL 6,40
UH 7,40
M 5
UL 8,41
UH 9,41
M 6
AD 169,6
AD 193,7
M 7
WL 8
AD 217,8
AD 241,9
M 8
UL 6,18
UH 7,18
M 9
WL 2
UL 8,19
UH 9,19
M 10
AD 170,6
AD 194,7
M 11
R 54
M 12
AD 218,8
AD 242,9
M 13
WV 35
UL 6,46
UH 7,46
M 14
UL 8,47
UH 9,47
M 15
AD 171,6
AD 195,7
M 16
AD 219,8
AD 243,9
M 17
M 18
M 19
M 20
M 21
M 22
M 23
WL 2
R 56
M 24
M 25
M 26
M 27
M 28
M 29
M 30
M 31
M 32
M 33
M 34
M 35
WL 2
R 58
M 36
M 37
M 38
M 39
M 40
M 41
M 42
M 43
M 44
WV 27
M 45
WL 0
s_barrier
M 46
mw 48
M 47
R 72
M 48
ad 84,1008
W 0
M 49
M 50
M 51
M 52
W 1
M 53
M 54
M 55
M 56
W 2
M 57
M 58
M 59
R 74
M 60
mw 51
M 61
ad 84,1011
W 0
M 62
M 63
M 64
M 65
W 1
M 66
M 67
M 68
M 69
W 2
M 70
M 71
WL 2
R 76
M 72
M 73
mh 0
M 74
ad 64,2033
H 0
M 75
M 76
M 77
M 78
mh 1
M 79
ad 66,2033
H 0
M 80
M 81
M 82
M 83
WL 2
R 78
M 84
mh 2
M 85
ad 64,2034
H 0
M 86
ae 44,68,240
M 87
s_bitcmp1_b32 s50, 10
s_cselect_b32 s51, 24576, 0
v_add_u32 v52, s51, v3
M 88
XL 22,256
M 89
H 1
M 90
M 91
M 92
XL 24,1024
M 93
bc 49,22,66,60
M 94
mh 4
M 95
WL 2
R 80
M 96
ad 66,2128
H 0
M 97
M 98
XL 26,1792
M 99
M 100
M 101
M 102
M 103
M 104
XL 42,2560
M 105
M 106
M 107
WL 2
R 82
M 108
M 109
M 110
M 111
M 112
M 113
M 114
M 115
M 116
WV 26
M 117
WL 0
s_barrier
M 118
mw 72
M 119
R 0
M 120
ad 84,1032
W 0
M 121
M 132
M 133
T 24,6
M 122
T 28,20
M 123
W 1
M 134
M 135
T 32,7
M 124
T 36,21
M 125
W 2
M 136
M 137
T 40,8
M 126
T 44,22
M 127
mw 75
M 138
ad 84,1035
W 0
M 139
WL 8
T 48,9
M 128
T 52,23
M 129
M 140
W 1
UL 6,48
M 141
T 56,10
M 130
WL 8
T 60,24
M 131
R 2
M 142
W 2
UH 7,48
M 143
T 64,11
M 0
WL 8
T 68,25
M 1
R 4
M 2
UL 8,49
UH 9,49
M 3
AD 172,6
AD 196,7
M 4
AD 220,8
AD 244,9
M 5
UL 6,14
UH 7,14
M 6
UL 8,15
UH 9,15
M 7
WL 8
AD 173,6
AD 197,7
M 8
WL 2
AD 221,8
AD 245,9
M 9
WV 31
UL 6,16
UH 7,16
M 10
UL 8,17
UH 9,17
M 11
R 6
M 12
AD 174,6
AD 198,7
M 13
AD 222,8
AD 246,9
M 14
WV 29
UL 6,20
UH 7,20
M 15
UL 8,21
UH 9,21
M 16
AD 175,6
AD 199,7
M 17
AD 223,8
AD 247,9
M 18
M 19
M 20
M 21
M 22
M 23
WL 2
R 8
M 24
M 25
M 26
M 27
M 28
M 29
M 30
M 31
M 32
M 33
M 34
M 35
WL 2
R 10
M 36
M 37
M 38
M 39
M 40
M 41
M 42
M 43
M 44
WV 21
M 45
WL 0
s_barrier
M 46
mw 0
M 47
R 24
M 48
ad 84,1056
W 0
M 49
M 50
M 51
M 52
W 1
M 53
M 54
M 55
M 56
W 2
M 57
M 58
M 59
R 26
M 60
mw 3
M 61
ad 84,1059
W 0
M 62
M 63
M 64
M 65
W 1
M 66
M 67
M 68
M 69
W 2
M 70
)"
R"(M 71
WL 2
R 28
M 72
M 73
bc 48,22,64,56
M 74
mh 5
M 75
ad 64,2128
H 0
M 76
M 77
M 78
M 79
H 1
M 80
M 81
M 82
M 83
WL 2
R 30
M 84
mh 7
M 85
ad 66,2129
H 0
M 86
M 87
M 88
M 89
mh 8
M 90
ad 64,2130
H 0
M 91
ae 44,68,240
M 92
s_bfe_u32 s51, s50, 0x1000b
v_mad_i32_i24 v52, v51, s51, v50
XL 44,256
M 93
M 94
H 1
M 95
WL 2
R 32
M 96
XL 34,1024
M 97
M 98
bc 49,23,66,60
M 99
mh 10
M 100
ad 66,2224
H 0
M 101
M 102
XL 36,1792
M 103
M 104
M 105
M 106
M 107
WL 2
R 34
M 108
XL 28,2560
M 109
M 110
M 111
M 112
M 113
M 114
M 115
WV 26
M 116
WL 0
M 117
s_barrier
M 118
mw 24
M 119
R 48
M 120
ad 84,1080
W 0
M 121
M 132
M 133
T 24,12
M 122
T 28,26
M 123
W 1
M 134
M 135
T 32,13
M 124
T 36,27
M 125
W 2
M 136
M 137
T 40,0
M 126
T 44,14
M 127
mw 27
M 138
ad 84,1083
W 0
M 139
WL 8
T 48,1
M 128
T 52,15
M 129
M 140
W 1
M 141
T 56,2
M 130
WL 8
T 60,16
M 131
R 50
M 142
W 2
UL 6,22
M 143
T 64,3
M 0
WL 8
T 68,17
M 1
R 52
M 2
UH 7,22
UL 8,23
M 3
UH 9,23
AD 176,6
M 4
AD 200,7
AD 224,8
M 5
WV 31
AD 248,9
UL 6,24
M 6
UH 7,24
UL 8,25
M 7
WL 8
UH 9,25
AD 177,6
M 8
WL 2
AD 201,7
AD 225,8
M 9
WV 28
AD 249,9
UL 6,26
M 10
UH 7,26
UL 8,27
M 11
R 54
M 12
UH 9,27
AD 178,6
M 13
AD 202,7
AD 226,8
M 14
AD 250,9
UL 6,42
M 15
UH 7,42
UL 8,43
M 16
UH 9,43
AD 179,6
M 17
AD 203,7
AD 227,8
M 18
AD 251,9
M 19
M 20
M 21
M 22
M 23
WL 2
R 56
M 24
M 25
M 26
M 27
M 28
M 29
M 30
M 31
M 32
M 33
M 34
M 35
WL 2
R 58
M 36
M 37
M 38
M 39
M 40
M 41
M 42
M 43
M 44
WV 22
M 45
WL 0
s_barrier
M 46
mw 48
M 47
R 72
M 48
ad 84,1104
W 0
M 49
M 50
M 51
M 52
W 1
M 53
M 54
M 55
M 56
W 2
M 57
M 58
M 59
R 74
M 60
mw 51
M 61
ad 84,1107
W 0
M 62
M 63
M 64
M 65
W 1
M 66
M 67
M 68
M 69
W 2
M 70
M 71
WL 2
R 76
M 72
M 73
bc 48,23,64,56
M 74
mh 11
M 75
ad 64,2224
H 0
M 76
M 77
M 78
M 79
H 1
M 80
M 81
M 82
M 83
WL 2
R 78
M 84
mh 13
M 85
ad 66,2225
H 0
M 86
M 87
M 88
M 89
mh 0
M 90
ad 64,2226
H 0
M 91
M 92
M 93
M 94
H 1
M 95
WL 2
R 80
M 96
M 97
M 98
bc 48,0,64,56
M 99
mh 2
M 100
ad 64,17
H 0
M 101
M 102
M 103
M 104
bc 49,0,66,60
M 105
mh 3
M 106
ad 66,17
H 0
M 107
WL 2
R 82
M 108
M 109
M 110
PA 12288
DF
M 111
M 112
M 113
M 114
M 115
WV 24
M 116
WL 0
M 117
s_barrier
M 118
mw 72
M 119
R 0
M 120
ad 84,1128
W 0
M 121
M 132
M 133
T 24,4
M 122
T 28,18
M 123
W 1
M 134
M 135
T 32,5
M 124
T 36,19
M 125
W 2
M 136
M 137
T 40,6
M 126
T 44,20
M 127
mw 75
M 138
ad 84,1131
W 0
M 139
WL 8
T 48,7
M 128
T 52,21
M 129
M 140
W 1
M 141
T 56,8
M 130
WL 8
T 60,22
M 131
R 2
M 142
W 2
M 143
T 64,9
M 0
WL 8
T 68,23
M 1
R 4
M 2
UL 6,44
)"
R"(UH 7,44
M 3
UL 8,45
UH 9,45
M 4
AD 180,6
AD 204,7
M 5
AD 228,8
AD 252,9
M 6
WV 29
UL 6,34
UH 7,34
M 7
WL 8
UL 8,35
UH 9,35
M 8
AD 181,6
AD 205,7
M 9
WL 2
AD 229,8
AD 253,9
M 10
WV 26
UL 6,36
UH 7,36
M 11
R 6
M 12
UL 8,37
UH 9,37
M 13
AD 182,6
AD 206,7
M 14
AD 230,8
AD 254,9
M 15
UL 6,28
UH 7,28
M 16
UL 8,29
UH 9,29
M 17
AD 183,6
AD 207,7
M 18
AD 231,8
AD 255,9
M 19
M 20
M 21
M 22
M 23
WL 2
R 8
M 24
M 25
M 26
M 27
M 28
M 29
M 30
M 31
M 32
M 33
M 34
M 35
WL 2
R 10
M 36
M 37
M 38
M 39
M 40
M 41
M 42
M 43
M 44
WV 20
M 45
WL 0
s_barrier
M 46
mw 0
M 47
R 24
M 48
ad 84,0
W 0
M 49
M 50
M 51
M 52
W 1
M 53
M 54
M 55
M 56
W 2
M 57
M 58
M 59
R 26
M 60
mw 3
M 61
ad 84,3
W 0
M 62
M 63
M 64
M 65
W 1
M 66
M 67
M 68
M 69
W 2
M 70
M 71
WL 2
R 28
M 72
M 73
mh 4
M 74
ad 64,18
H 0
M 75
M 76
M 77
M 78
H 1
M 79
M 80
M 81
M 82
mh 6
M 83
WL 2
R 30
M 84
ad 66,32
H 0
M 85
M 86
M 87
M 88
mh 7
M 89
ad 64,32
H 0
M 90
M 91
M 92
M 93
bc 48,1,64,56
M 94
mh 8
M 95
WL 2
R 32
M 96
ad 64,114
H 0
M 97
M 98
M 99
M 100
H 1
M 101
M 102
M 103
M 104
M 105
M 106
M 107
WL 2
R 34
M 108
M 109
M 110
M 111
M 112
M 113
M 114
M 115
M 116
WV 21
M 117
WL 0
s_barrier
M 118
mw 24
M 119
R 48
M 120
ad 84,24
W 0
M 121
M 132
M 133
T 24,10
M 122
T 28,24
M 123
W 1
M 134
M 135
T 32,11
M 124
T 36,25
M 125
W 2
M 136
M 137
T 40,12
M 126
T 44,26
M 127
mw 27
M 138
ad 84,27
W 0
M 139
WL 8
T 48,13
M 128
T 52,27
M 129
M 140
W 1
M 141
T 56,0
M 130
WL 8
T 60,14
M 131
R 50
M 142
W 2
M 143
T 64,1
M 0
WL 8
T 68,15
M 1
R 52
M 2
M 3
M 4
M 5
M 6
M 7
WL 8
M 8
M 9
WL 2
M 10
M 11
R 54
M 12
M 13
M 14
M 15
M 16
M 17
M 18
M 19
M 20
M 21
M 22
M 23
WL 2
R 56
M 24
M 25
M 26
M 27
M 28
M 29
M 30
M 31
M 32
M 33
M 34
M 35
WL 2
R 58
M 36
M 37
M 38
M 39
M 40
AR 6,72
AR 7,96
M 41
AR 8,120
s_nop 0
AR 9,144
M 42
EF 6
EF 7
M 43
WV 18
EF 8
EF 9
M 44
WL 0
CV 30,6,7
CV 31,8,9
M 45
s_barrier
AR 6,73
AR 7,97
M 46
mw 48
AR 8,121
AR 9,145
M 47
R 72
M 48
ad 84,48
W 0
M 49
EF 6
EF 7
M 50
EF 8
EF 9
M 51
CV 32,6,7
CV 33,8,9
M 52
W 1
AR 6,74
M 53
AR 7,98
AR 8,122
M 54
AR 9,146
EF 6
M 55
EF 7
EF 8
M 56
W 2
EF 9
M 57
CV 38,6,7
CV 39,8,9
M 58
AR 6,75
AR 7,99
M 59
R 74
M 60
mw 51
AR 8,123
AR 9,147
M 61
ad 84,51
W 0
M 62
EF 6
EF 7
M 63
EF 8
EF 9
M 64
CV 40,6,7
CV 41,8,9
M 65
W 1
BA 72,0
M 66
BA 96,256
BA 120,512
M 67
BA 144,768
M 68
ae 42,74,192
AR 6,76
M 69
W 2
AR 7,100
M 70
s_bitcmp1_b32 s50, 6
s_cselect_b32 s52, 24576, 0
v_add_u32 v53, s52, v3
M 71
WL 4
R 76
M 72
ST 30,0
AR 8,124
)"
R"(M 73
bc 49,1,66,60
AR 9,148
M 74
mh 10
EF 6
EF 7
M 75
ad 66,128
H 0
M 76
EF 8
EF 9
M 77
CV 18,6,7
CV 19,8,9
M 78
AR 6,77
AR 7,101
M 79
mh 11
AR 8,125
AR 9,149
M 80
ad 64,128
H 0
M 81
EF 6
EF 7
M 82
EF 8
EF 9
M 83
R 78
M 84
H 1
CV 46,6,7
M 85
CV 47,8,9
AR 6,78
M 86
AR 7,102
AR 8,126
M 87
AR 9,150
EF 6
M 88
mh 13
EF 7
EF 8
M 89
ad 66,129
H 0
M 90
EF 9
CV 48,6,7
M 91
CV 49,8,9
AR 6,79
M 92
AR 7,103
AR 8,127
M 93
bc 49,2,66,60
AR 9,151
M 94
mh 0
EF 6
EF 7
M 95
WL 0
R 80
M 96
ad 66,224
H 0
M 97
EF 8
EF 9
M 98
CV 14,6,7
CV 15,8,9
M 99
BA 76,0
BA 100,256
M 100
BA 124,512
BA 148,768
M 101
EF 64
EF 88
M 102
EF 112
EF 136
M 103
CV 16,64,88
CV 17,112,136
M 104
EF 65
EF 89
M 105
EF 113
EF 137
M 106
CV 20,65,89
CV 21,113,137
M 107
R 82
M 108
EF 66
EF 90
M 109
EF 114
EF 138
M 110
ST 32,768
CV 22,66,90
M 111
CV 23,114,138
EF 67
M 112
EF 91
EF 115
M 113
EF 139
CV 24,67,91
M 114
CV 25,115,139
BV 64,1024
M 115
WV 21
BV 88,1280
BV 112,1536
M 116
WL 3
BV 136,1792
M 117
s_barrier
EF 68
EF 92
M 118
mw 72
EF 116
EF 140
M 119
R 0
M 120
ad 84,72
W 0
M 121
CV 26,68,92
CV 27,116,140
M 132
EF 69
EF 93
M 133
T 24,6
M 122
T 28,20
M 123
W 1
EF 117
M 134
EF 141
CV 42,69,93
M 135
T 32,7
M 124
T 36,21
M 125
W 2
CV 43,117,141
M 136
EF 70
EF 94
M 137
WL 8
T 40,2
M 126
T 44,16
M 127
mw 75
EF 118
EF 142
M 138
ad 84,75
W 0
M 139
T 48,3
M 128
WL 8
T 52,17
M 129
CV 44,70,94
CV 45,118,142
M 140
W 1
EF 71
M 141
T 56,4
M 130
T 60,18
M 131
WL 8
R 2
M 142
W 2
EF 95
M 143
T 64,5
M 4
T 68,19
M 5
WL 8
R 4
M 6
EF 119
EF 143
M 7
WL 8
CV 34,71,95
CV 35,119,143
M 8
ST 38,1536
BV 68,1024
M 9
WL 3
BV 92,1280
BV 116,1536
M 10
BV 140,1792
M 11
EF 160
EF 184
M 0
EF 208
EF 232
M 1
CV 10,160,184
CV 11,208,232
M 2
AW 160,10
AW 161,11
M 3
R 6
M 16
EF 161
EF 185
M 17
EF 209
EF 233
M 18
CV 10,161,185
CV 11,209,233
M 19
AW 162,10
AW 163,11
M 20
EF 162
EF 186
M 21
EF 210
EF 234
M 22
CV 10,162,186
CV 11,210,234
M 23
AW 164,10
AW 165,11
M 12
EF 163
EF 187
M 13
EF 211
EF 235
M 14
CV 10,163,187
CV 11,211,235
M 15
WL 2
R 8
M 28
AW 166,10
AW 167,11
M 29
BV 160,2048
BV 184,2304
M 30
BV 208,2560
BV 232,2816
M 31
EF 164
EF 188
M 32
EF 212
EF 236
M 33
CV 10,164,188
CV 11,212,236
M 34
AW 168,10
AW 169,11
M 35
EF 165
EF 189
M 24
EF 213
EF 237
M 25
CV 10,165,189
CV 11,213,237
M 26
AW 170,10
AW 171,11
M 27
WL 6
R 10
M 40
EF 166
EF 190
M 41
EF 214
EF 238
M 42
CV 10,166,190
CV 11,214,238
M 43
AW 172,10
AW 173,11
M 44
EF 167
EF 191
M 45
EF 215
EF 239
M 46
ST 40,2304
CV 10,167,191
M 47
WV 21
CV 11,215,239
AW 174,10
M 36
WL 0
AW 175,11
BV 164,2048
M 37
s_barrier
BV 188,2304
BV 212,2560
M 38
mw 0
BV 236,2816
M 39
R 24
M 52
ad 84,96
W 0
M 53
AR 6,80
AR 7,104
M 54
AR 8,128
AR 9,152
M 55
EF 6
EF 7
M 56
W 1
EF 8
M 57
EF 9
CV 10,6,7
M 58
CV 11,8,9
AW 176,10
M 59
AW 177,11
AR 6,81
M 48
W 2
AR 7,105
M 49
AR 8,129
AR 9,153
M 50
EF 6
EF 7
M 51
R 26
M 64
mw 3
EF 8
EF 9
M 65
ad 84,99
W 0
M 66
CV 10,6,7
CV 11,8,9
M 67
)"
R"(AW 178,10
AW 179,11
M 68
AR 6,82
AR 7,106
M 69
W 1
AR 8,130
M 70
AR 9,154
EF 6
M 71
EF 7
EF 8
M 60
EF 9
CV 10,6,7
M 61
W 2
CV 11,8,9
M 62
AW 180,10
AW 181,11
M 63
WL 0
R 28
M 76
AR 6,83
AR 7,107
M 77
bc 48,2,64,56
AR 8,131
M 78
mh 1
AR 9,155
EF 6
M 79
ad 64,224
H 0
M 80
EF 7
EF 8
M 81
EF 9
CV 10,6,7
M 82
CV 11,8,9
AW 182,10
M 83
H 1
AW 183,11
M 72
ae 42,72,192
BA 80,0
M 73
s_bitcmp1_b32 s50, 7
s_cselect_b32 s52, 24576, 0
v_add_u32 v53, s52, v3
M 74
ST 18,0
BA 104,256
M 75
R 30
M 88
mh 3
BA 128,512
BA 152,768
M 89
ad 66,225
H 0
M 90
AR 6,84
AR 7,108
M 91
AR 8,132
AR 9,156
M 92
EF 6
EF 7
M 93
mh 4
EF 8
EF 9
M 94
ad 64,226
H 0
M 95
CV 10,6,7
CV 11,8,9
M 84
AW 184,10
AW 185,11
M 85
AR 6,85
AR 7,109
M 86
H 1
AR 8,133
M 87
WL 2
R 32
M 100
AR 9,157
EF 6
M 101
EF 7
EF 8
M 102
bc 49,3,66,60
EF 9
M 103
mh 6
CV 10,6,7
CV 11,8,9
M 104
ad 66,320
H 0
M 105
AW 186,10
AW 187,11
M 106
AR 6,86
AR 7,110
M 107
AR 8,134
AR 9,158
M 96
EF 6
EF 7
M 97
EF 8
EF 9
M 98
CV 10,6,7
CV 11,8,9
M 99
R 34
M 112
AW 188,10
AW 189,11
M 113
AR 6,87
AR 7,111
M 114
AR 8,135
AR 9,159
M 115
EF 6
EF 7
M 116
EF 8
EF 9
M 117
CV 10,6,7
CV 11,8,9
M 118
AW 190,10
AW 191,11
M 119
WV 23
BA 84,0
BA 108,256
M 108
WL 2
BA 132,512
BA 156,768
M 109
s_barrier
EF 72
EF 96
M 110
mw 24
EF 120
EF 144
M 111
R 48
M 124
ad 84,120
W 0
M 125
ST 46,768
CV 10,72,96
M 136
CV 11,120,144
AW 192,10
M 137
T 40,12
M 126
T 44,26
M 127
W 1
AW 193,11
M 138
EF 73
EF 97
M 139
T 48,13
M 128
T 52,27
M 129
W 2
EF 121
M 140
EF 145
CV 10,73,97
M 141
WL 8
T 56,8
M 130
T 60,22
M 131
mw 27
CV 11,121,145
AW 194,10
M 142
ad 84,123
W 0
M 143
T 64,9
M 120
WL 8
T 68,23
M 121
AW 195,11
EF 74
M 132
W 1
EF 98
M 133
T 24,10
M 122
T 28,24
M 123
WL 8
R 50
M 134
W 2
EF 122
M 135
T 32,11
M 8
T 36,25
M 9
WL 8
R 52
M 10
EF 146
CV 10,74,98
M 11
WL 8
CV 11,122,146
AW 196,10
M 0
AW 197,11
EF 75
M 1
WL 2
EF 99
EF 123
M 2
EF 147
CV 10,75,99
M 3
CV 11,123,147
AW 198,10
M 4
AW 199,11
BV 72,1024
M 5
BV 96,1280
BV 120,1536
M 6
BV 144,1792
M 7
R 54
M 20
EF 76
EF 100
M 21
EF 124
EF 148
M 22
CV 10,76,100
CV 11,124,148
M 23
ST 48,1536
AW 200,10
M 12
AW 201,11
EF 77
M 13
EF 101
EF 125
M 14
EF 149
CV 10,77,101
M 15
CV 11,125,149
AW 202,10
M 16
AW 203,11
EF 78
M 17
EF 102
EF 126
M 18
EF 150
CV 10,78,102
M 19
WL 2
R 56
M 32
CV 11,126,150
AW 204,10
M 33
AW 205,11
EF 79
M 34
EF 103
EF 127
M 35
EF 151
CV 10,79,103
M 24
CV 11,127,151
AW 206,10
M 25
AW 207,11
BV 76,1024
M 26
BV 100,1280
BV 124,1536
M 27
BV 148,1792
M 28
EF 168
EF 192
M 29
EF 216
EF 240
M 30
CV 10,168,192
CV 11,216,240
M 31
WL 6
R 58
M 44
AW 208,10
AW 209,11
M 45
EF 169
EF 193
M 46
EF 217
EF 241
M 47
CV 10,169,193
CV 11,217,241
M 36
AW 210,10
AW 211,11
M 37
EF 170
EF 194
M 38
EF 218
EF 242
M 39
WV 23
CV 10,170,194
CV 11,218,242
M 40
WL 0
AW 212,10
AW 213,11
M 41
s_barrier
EF 171
EF 195
M 42
mw 48
EF 219
EF 243
M 43
R 72
M 56
ad 84,144
W 0
M 57
CV 10,171,195
CV 11,219,243
M 58
AW 214,10
AW 215,11
M 59
BV 168,2048
BV 192,2304
M 48
W 1
BV 216,2560
)"
R"(M 49
ST 14,2304
BV 240,2816
M 50
ae 44,70,384
M 51
s_bitcmp1_b32 s50, 12
s_cselect_b32 s51, 24576, 0
v_add_u32 v52, s51, v3
M 52
W 2
XL 36,0
M 53
XL 28,768
EF 172
M 54
XL 30,1536
EF 196
M 55
R 74
M 68
mw 51
XL 32,2304
EF 220
M 69
ad 84,147
W 0
M 70
EF 244
CV 10,172,196
M 71
CV 11,220,244
AW 216,10
M 60
AW 217,11
EF 173
M 61
W 1
EF 197
M 62
EF 221
EF 245
M 63
CV 10,173,197
CV 11,221,245
M 64
AW 218,10
AW 219,11
M 65
W 2
EF 174
M 66
EF 198
EF 222
M 67
WL 0
R 76
M 80
EF 246
CV 10,174,198
M 81
bc 48,3,64,56
CV 11,222,246
M 82
mh 7
AW 220,10
AW 221,11
M 83
ad 64,320
H 0
M 72
EF 175
EF 199
M 73
EF 223
EF 247
M 74
CV 10,175,199
CV 11,223,247
M 75
H 1
AW 222,10
M 76
AW 223,11
BV 172,2048
M 77
BV 196,2304
BV 220,2560
M 78
BV 244,2816
M 79
R 78
M 92
mh 9
AR 6,88
AR 7,112
M 93
ad 66,321
H 0
M 94
AR 8,136
EF 6
M 95
EF 7
EF 8
M 84
EF 56
CV 10,6,7
M 85
mh 10
CV 11,8,56
AW 224,10
M 86
ad 64,322
H 0
M 87
ae 42,74,192
AW 225,11
M 88
s_bitcmp1_b32 s50, 6
s_cselect_b32 s52, 24576, 0
v_add_u32 v53, s52, v3
M 89
ST 16,128
AR 6,89
M 90
H 1
AR 7,113
M 91
WL 0
R 80
M 104
AR 8,137
EF 6
M 105
EF 7
EF 8
M 106
bc 49,4,66,60
EF 57
M 107
mh 12
CV 10,6,7
CV 11,8,57
M 96
ad 66,416
H 0
M 97
AW 226,10
AW 227,11
M 98
AR 6,90
AR 7,114
M 99
AR 8,138
EF 6
M 100
bc 48,4,64,56
EF 7
M 101
mh 13
EF 8
EF 58
M 102
ad 64,416
H 0
M 103
R 82
M 116
CV 10,6,7
CV 11,8,58
M 117
AW 228,10
AW 229,11
M 118
PA 12288
DF
AR 6,91
M 119
AR 7,115
AR 8,139
M 108
EF 6
EF 7
M 109
EF 8
EF 59
M 110
CV 10,6,7
CV 11,8,59
M 111
WV 29
AW 230,10
AW 231,11
M 112
WL 0
BA 88,0
BA 112,256
M 113
s_barrier
BA 136,512
BV 56,768
M 114
mw 72
AR 6,92
AR 7,116
AR 8,140
M 115
R 0
EF 6
M 128
ad 84,168
W 0
EF 7
M 129
EF 8
EF 60
CV 10,6,7
M 140
CV 11,8,60
AW 232,10
AW 233,11
M 141
T 56,4
AR 6,93
M 130
T 60,18
AR 7,117
M 131
W 1
AR 8,141
EF 6
M 142
EF 7
EF 8
EF 61
M 143
T 64,5
CV 10,6,7
M 120
T 68,19
CV 11,8,61
M 121
W 2
AW 234,10
AW 235,11
M 132
AR 6,94
AR 7,118
AR 8,142
M 133
WL 8
T 24,0
EF 6
M 122
T 28,14
EF 7
M 123
mw 75
ST 20,896
EF 8
EF 62
M 134
ad 84,171
W 0
CV 10,6,7
M 135
T 32,1
CV 11,8,62
M 124
WL 8
T 36,15
AW 236,10
M 125
AW 237,11
AR 6,95
AR 7,119
M 136
W 1
AR 8,143
EF 6
M 137
T 40,2
EF 7
M 126
T 44,16
EF 8
M 127
WL 8
R 2
EF 63
M 138
W 2
CV 10,6,7
CV 11,8,63
M 139
T 48,3
AW 238,10
M 0
T 52,17
AW 239,11
M 1
WL 8
R 4
BA 92,0
M 2
BA 116,256
BA 140,512
BV 60,768
M 3
WL 12
EF 80
EF 104
M 4
EF 128
EF 152
M 5
WL 6
CV 10,80,104
CV 11,128,152
M 6
AW 240,10
AW 241,11
M 7
EF 81
EF 105
M 8
EF 129
EF 153
M 9
WL 3
CV 10,81,105
CV 11,129,153
M 10
AW 242,10
AW 243,11
M 11
R 6
M 12
EF 82
EF 106
M 13
EF 130
EF 154
M 14
CV 10,82,106
CV 11,130,154
M 15
AW 244,10
AW 245,11
M 16
EF 83
EF 107
M 17
EF 131
EF 155
M 18
CV 10,83,107
CV 11,131,155
M 19
AW 246,10
AW 247,11
M 20
BV 80,1024
BV 104,1280
M 21
WL 4
BV 128,1536
BV 152,1792
M 22
EF 84
EF 108
M 23
R 8
M 24
EF 132
EF 156
M 25
CV 10,84,108
CV 11,132,156
M 26
AW 248,10
AW 249,11
M 27
ST 22,1664
EF 85
M 28
EF 109
EF 133
M 29
EF 157
CV 10,85,109
M 30
CV 11,133,157
AW 250,10
M 31
AW 251,11
EF 86
M 32
EF 110
)"
R"(EF 134
M 33
EF 158
CV 10,86,110
M 34
CV 11,134,158
AW 252,10
M 35
WL 2
R 10
M 36
AW 253,11
EF 87
M 37
EF 111
EF 135
M 38
EF 159
CV 10,87,111
M 39
CV 11,135,159
AW 254,10
M 40
AW 255,11
BV 84,1024
M 41
BV 108,1280
BV 132,1536
M 42
BV 156,1792
M 43
WV 29
EF 176
EF 200
M 44
WL 4
EF 224
EF 248
M 45
s_barrier
CV 38,176,200
CV 39,224,248
M 46
mw 0
EF 177
EF 201
M 47
R 24
M 48
ad 84,192
W 0
M 49
EF 225
EF 249
M 50
CV 40,177,201
CV 41,225,249
M 51
EF 178
EF 202
M 52
W 1
EF 226
M 53
EF 250
CV 18,178,202
M 54
CV 19,226,250
EF 179
M 55
EF 203
EF 227
M 56
W 2
EF 251
M 57
WL 2
CV 46,179,203
CV 47,227,251
M 58
BV 176,2048
BV 200,2304
M 59
R 26
M 60
mw 3
BV 224,2560
BV 248,2816
M 61
ad 84,195
W 0
M 62
EF 180
EF 204
M 63
EF 228
EF 252
M 64
CV 48,180,204
CV 49,228,252
M 65
W 1
ST 24,2432
M 66
EF 181
EF 205
M 67
EF 229
EF 253
M 68
CV 14,181,205
CV 15,229,253
M 69
W 2
EF 182
M 70
EF 206
EF 230
M 71
WL 2
R 28
M 72
EF 254
CV 16,182,206
M 73
mh 0
CV 17,230,254
EF 183
M 74
ad 64,417
H 0
M 75
EF 207
EF 231
M 76
EF 255
CV 20,183,207
M 77
CV 21,231,255
BV 180,2048
M 78
mh 1
BV 204,2304
BV 228,2560
M 79
ad 66,417
H 0
M 80
BV 252,2816
M 81
ae 44,68,384
M 82
s_bitcmp1_b32 s50, 13
s_cselect_b32 s51, 24576, 0
v_add_u32 v52, s51, v3
M 83
WV 29
R 30
M 84
mh 2
XL 22,0
UL 6,36
M 85
ad 64,418
H 0
M 86
XL 24,768
UH 7,36
M 87
UL 8,37
UH 9,37
M 88
AR 10,72
AR 11,96
M 89
H 1
AR 12,120
M 90
AR 13,144
AD 10,6
M 91
AD 11,7
AD 12,8
M 92
AD 13,9
AW 72,10
M 93
bc 49,5,66,60
AW 96,11
M 94
mh 4
AW 120,12
AW 144,13
M 95
WL 0
R 32
M 96
ad 66,512
H 0
M 97
UL 6,28
UH 7,28
M 98
UL 8,29
UH 9,29
M 99
AR 10,73
AR 11,97
M 100
AR 12,121
AR 13,145
M 101
AD 10,6
AD 11,7
M 102
AD 12,8
AD 13,9
M 103
ae 42,72,192
AW 73,10
M 104
s_bitcmp1_b32 s50, 7
s_cselect_b32 s52, 24576, 0
v_add_u32 v53, s52, v3
M 105
ST 26,128
AW 97,11
M 106
XL 26,1536
AW 121,12
M 107
R 34
M 108
AW 145,13
UL 6,30
M 109
UH 7,30
UL 8,31
M 110
UH 9,31
AR 10,74
M 111
AR 11,98
AR 12,122
M 112
AR 13,146
AD 10,6
M 113
AD 11,7
AD 12,8
M 114
AD 13,9
AW 74,10
M 115
WV 27
AW 98,11
AW 122,12
M 116
WL 0
AW 146,13
UL 6,32
M 117
s_barrier
UH 7,32
UL 8,33
M 118
mw 24
UH 9,33
AR 10,75
M 119
R 48
M 120
ad 84,216
W 0
M 121
AR 11,99
AR 12,123
M 132
AR 13,147
AD 10,6
M 133
T 24,6
M 122
T 28,20
M 123
W 1
AD 11,7
M 134
AD 12,8
AD 13,9
M 135
T 32,7
M 124
T 36,21
M 125
W 2
AW 75,10
M 136
AW 99,11
AW 123,12
M 137
T 40,8
M 126
T 44,22
M 127
mw 27
AW 147,13
M 138
ad 84,219
W 0
M 139
WL 8
T 48,9
M 128
T 52,23
M 129
M 140
W 1
M 141
T 56,10
M 130
WL 8
T 60,24
M 131
R 50
M 142
W 2
M 143
T 64,11
M 0
WL 8
T 68,25
M 1
R 52
M 2
ST 42,896
M 3
s_nop 0
XL 42,2304
M 4
ae 44,68,384
M 5
M 6
s_bfe_u32 s51, s50, 0x1000e
v_mad_i32_i24 v52, v51, s51, v50
XL 36,0
M 7
WL 8
XL 28,768
M 8
XL 30,1536
M 9
WL 2
XL 32,2304
M 10
M 11
R 54
M 12
M 13
M 14
M 15
M 16
M 17
M 18
M 19
M 20
M 21
M 22
M 23
WL 2
R 56
M 24
M 25
M 26
M 27
M 28
M 29
M 30
M 31
M 32
M 33
M 34
M 35
WL 2
R 58
M 36
M 37
M 38
M 39
M 40
ST 44,1664
M 41
M 42
WV 19
UL 6,22
UH 7,22
M 43
UL 8,23
UH 9,23
M 44
)"
R"(WL 0
AR 10,76
AR 11,100
M 45
s_barrier
AR 12,124
AR 13,148
M 46
mw 48
AD 10,6
AD 11,7
M 47
R 72
M 48
ad 84,240
W 0
M 49
AD 12,8
AD 13,9
M 50
AW 76,10
AW 100,11
M 51
AW 124,12
AW 148,13
M 52
WV 18
W 1
UL 6,24
M 53
UH 7,24
UL 8,25
M 54
UH 9,25
AR 10,77
M 55
AR 11,101
AR 12,125
M 56
W 2
AR 13,149
M 57
AD 10,6
AD 11,7
M 58
AD 12,8
AD 13,9
M 59
R 74
M 60
mw 51
AW 77,10
AW 101,11
M 61
ad 84,243
W 0
M 62
AW 125,12
AW 149,13
M 63
WV 17
UL 6,26
UH 7,26
M 64
UL 8,27
UH 9,27
M 65
W 1
AR 10,78
M 66
AR 11,102
AR 12,126
M 67
AR 13,150
AD 10,6
M 68
AD 11,7
AD 12,8
M 69
W 2
AD 13,9
M 70
AW 78,10
AW 102,11
M 71
WL 0
R 76
M 72
WV 11
AW 126,12
AW 150,13
M 73
bc 48,5,64,56
UL 6,42
M 74
mh 5
UH 7,42
UL 8,43
M 75
ad 64,512
H 0
M 76
UH 9,43
AR 10,79
M 77
AR 11,103
AR 12,127
M 78
ST 34,2432
AR 13,151
M 79
H 1
AD 10,6
M 80
AD 11,7
AD 12,8
M 81
AD 13,9
AW 79,10
M 82
AW 103,11
AW 127,12
M 83
R 78
M 84
mh 7
AW 151,13
M 85
ad 66,513
H 0
M 86
M 87
M 88
M 89
mh 8
M 90
ad 64,514
H 0
M 91
M 92
M 93
M 94
H 1
M 95
WL 2
R 80
M 96
M 97
M 98
bc 49,6,66,60
M 99
mh 10
M 100
ad 66,608
H 0
M 101
M 102
M 103
M 104
M 105
M 106
M 107
WL 2
R 82
M 108
M 109
M 110
M 111
M 112
M 113
M 114
M 115
M 116
M 117
WL 0
s_barrier
M 118
mw 72
M 119
R 0
M 120
ad 84,264
W 0
M 121
ae 42,74,192
M 132
s_bitcmp1_b32 s50, 6
s_cselect_b32 s52, 24576, 0
v_add_u32 v53, s52, v3
M 133
T 24,12
M 122
T 28,26
M 123
W 1
SA 160,256
M 134
M 135
T 32,13
M 124
T 36,27
M 125
W 2
M 136
M 137
T 40,0
M 126
T 44,14
M 127
mw 75
M 138
ad 84,267
W 0
M 139
WL 8
T 48,1
M 128
T 52,15
M 129
ae 44,70,384
M 140
s_bfe_u32 s51, s50, 0x1000f
W 1
v_mad_i32_i24 v52, v51, s51, v50
M 141
T 56,2
M 130
WL 8
T 60,16
M 131
R 2
M 142
W 2
XL 44,0
M 143
T 64,3
M 0
WL 8
T 68,17
M 1
R 4
M 2
XL 34,768
M 3
XL 22,1536
M 4
M 5
M 6
M 7
WL 8
M 8
XL 24,2304
M 9
WL 2
M 10
M 11
R 6
M 12
M 13
M 14
M 15
M 16
M 17
M 18
M 19
SA 162,1024
M 20
M 21
M 22
M 23
WL 2
R 8
M 24
M 25
M 26
M 27
M 28
M 29
M 30
M 31
M 32
M 33
M 34
M 35
WL 2
R 10
M 36
M 37
M 38
M 39
M 40
M 41
M 42
M 43
M 44
WV 26
UL 6,36
UH 7,36
M 45
WL 0
s_barrier
UL 8,37
UH 9,37
M 46
mw 0
AR 10,80
AR 11,104
M 47
R 24
M 48
ad 84,288
W 0
M 49
AR 12,128
AR 13,152
M 50
AD 10,6
AD 11,7
M 51
AD 12,8
AD 13,9
M 52
W 1
AW 80,10
M 53
AW 104,11
AW 128,12
M 54
AW 152,13
UL 6,28
M 55
UH 7,28
UL 8,29
M 56
W 2
UH 9,29
M 57
SA 164,1792
AR 10,81
M 58
AR 11,105
AR 12,129
M 59
R 26
M 60
mw 3
AR 13,153
AD 10,6
M 61
ad 84,291
W 0
M 62
AD 11,7
AD 12,8
M 63
AD 13,9
AW 81,10
M 64
AW 105,11
AW 129,12
M 65
W 1
AW 153,13
M 66
UL 6,30
UH 7,30
M 67
UL 8,31
UH 9,31
M 68
AR 10,82
AR 11,106
M 69
W 2
AR 12,130
M 70
AR 13,154
AD 10,6
M 71
WL 0
R 28
M 72
AD 11,7
AD 12,8
M 73
bc 48,6,64,56
AD 13,9
M 74
mh 11
AW 82,10
AW 106,11
M 75
ad 64,608
H 0
M 76
AW 130,12
AW 154,13
M 77
UL 6,32
UH 7,32
)"
R"(M 78
UL 8,33
UH 9,33
M 79
H 1
AR 10,83
M 80
AR 11,107
AR 12,131
M 81
AR 13,155
AD 10,6
M 82
AD 11,7
AD 12,8
M 83
R 30
M 84
mh 13
AD 13,9
AW 83,10
M 85
ad 66,609
H 0
M 86
AW 107,11
AW 131,12
M 87
AW 155,13
M 88
M 89
mh 0
M 90
ad 64,610
H 0
M 91
M 92
M 93
M 94
H 1
M 95
WL 2
R 32
M 96
SA 166,2560
M 97
M 98
bc 49,7,66,60
M 99
mh 2
M 100
ad 66,704
H 0
M 101
M 102
M 103
M 104
M 105
M 106
M 107
WL 2
R 34
M 108
M 109
M 110
M 111
M 112
M 113
M 114
M 115
M 116
WV 27
M 117
WL 0
s_barrier
M 118
mw 24
M 119
R 48
M 120
ad 84,312
W 0
M 121
M 132
M 133
T 24,4
M 122
T 28,18
M 123
W 1
M 134
M 135
T 32,5
M 124
T 36,19
M 125
W 2
M 136
M 137
T 40,6
M 126
T 44,20
M 127
mw 27
M 138
ad 84,315
W 0
M 139
WL 8
T 48,7
M 128
T 52,21
M 129
ae 42,72,192
M 140
W 1
M 141
T 56,8
M 130
WL 8
T 60,22
M 131
R 50
M 142
W 2
M 143
T 64,9
M 0
WL 8
T 68,23
M 1
R 52
M 2
s_bitcmp1_b32 s50, 7
s_cselect_b32 s52, 24576, 0
M 3
v_add_u32 v53, s52, v3
SA 168,256
M 4
ae 44,68,432
M 5
M 6
s_bitcmp1_b32 s50, 16
s_cselect_b32 s51, 24576, 0
v_add_u32 v52, s51, v3
M 7
WL 8
XL 26,0
M 8
XL 42,768
M 9
WL 2
XL 36,1536
M 10
XL 28,2304
M 11
R 54
M 12
M 13
M 14
M 15
M 16
M 17
M 18
M 19
M 20
M 21
M 22
M 23
WL 2
R 56
M 24
M 25
M 26
M 27
M 28
M 29
M 30
M 31
M 32
M 33
M 34
M 35
WL 2
R 58
M 36
M 37
M 38
M 39
M 40
M 41
SA 170,1024
M 42
M 43
WV 27
M 44
WL 0
M 45
s_barrier
M 46
mw 48
UL 6,44
UH 7,44
M 47
R 72
M 48
ad 84,336
W 0
M 49
UL 8,45
UH 9,45
M 50
AR 10,84
AR 11,108
M 51
AR 12,132
AR 13,156
M 52
W 1
AD 10,6
M 53
AD 11,7
AD 12,8
M 54
AD 13,9
AW 84,10
M 55
AW 108,11
AW 132,12
M 56
W 2
AW 156,13
M 57
UL 6,34
UH 7,34
M 58
UL 8,35
UH 9,35
M 59
R 74
M 60
mw 51
AR 10,85
AR 11,109
M 61
ad 84,339
W 0
M 62
AR 12,133
AR 13,157
M 63
AD 10,6
AD 11,7
M 64
AD 12,8
AD 13,9
M 65
W 1
AW 85,10
M 66
AW 109,11
AW 133,12
M 67
AW 157,13
UL 6,22
M 68
UH 7,22
UL 8,23
M 69
W 2
UH 9,23
M 70
AR 10,86
AR 11,110
M 71
WL 0
R 76
M 72
AR 12,134
AR 13,158
M 73
bc 48,7,64,56
AD 10,6
M 74
mh 3
AD 11,7
AD 12,8
M 75
ad 64,704
H 0
M 76
AD 13,9
AW 86,10
M 77
AW 110,11
AW 134,12
M 78
AW 158,13
UL 6,24
M 79
H 1
SA 172,1792
M 80
UH 7,24
UL 8,25
M 81
UH 9,25
AR 10,87
M 82
AR 11,111
AR 12,135
M 83
R 78
M 84
mh 5
AR 13,159
AD 10,6
M 85
ad 66,705
H 0
M 86
AD 11,7
AD 12,8
M 87
AD 13,9
AW 87,10
M 88
AW 111,11
AW 135,12
M 89
mh 6
AW 159,13
M 90
ad 64,706
H 0
M 91
M 92
M 93
M 94
H 1
M 95
WL 2
R 80
M 96
M 97
M 98
bc 49,8,66,60
M 99
mh 8
M 100
ad 66,800
H 0
M 101
M 102
M 103
M 104
PA 12288
DF
M 105
M 106
M 107
WL 2
R 82
M 108
M 109
M 110
M 111
M 112
M 113
M 114
M 115
M 116
WV 28
M 117
WL 0
s_barrier
SA 174,2560
M 118
mw 72
M 119
R 0
M 120
ad 84,360
W 0
M 121
M 132
M 133
T 24,10
M 122
T 28,24
M 123
W 1
M 134
M 135
T 32,11
M 124
T 36,25
M 125
W 2
M 136
)"
R"(M 137
T 40,12
M 126
T 44,26
M 127
mw 75
M 138
ad 84,363
W 0
M 139
WL 8
T 48,13
M 128
T 52,27
M 129
M 140
W 1
M 141
T 56,0
M 130
WL 8
T 60,14
M 131
R 2
M 142
W 2
M 143
T 64,1
M 0
WL 8
T 68,15
M 1
R 4
M 2
ae 44,68,432
M 3
M 4
s_bfe_u32 s51, s50, 0x10011
v_mad_i32_i24 v52, v51, s51, v50
XL 30,0
M 5
XL 32,768
M 6
XL 44,1536
M 7
WL 8
M 8
M 9
WL 2
M 10
M 11
R 6
M 12
ae 42,72,192
XL 34,2304
M 13
s_bfe_u32 s52, s50, 0x10008
v_mad_i32_i24 v53, v51, s52, v50
SA 176,0
M 14
M 15
M 16
M 17
M 18
M 19
M 20
M 21
M 22
M 23
WL 2
R 8
M 24
M 25
M 26
M 27
M 28
M 29
M 30
M 31
M 32
M 33
M 34
M 35
WL 2
R 10
M 36
M 37
M 38
M 39
M 40
M 41
M 42
M 43
M 44
WV 32
M 45
WL 0
s_barrier
M 46
mw 0
M 47
R 24
M 48
ad 84,384
W 0
M 49
WV 28
UL 6,26
UH 7,26
M 50
UL 8,27
UH 9,27
M 51
SA 178,768
AR 10,88
M 52
W 1
AR 11,112
M 53
AR 12,136
AD 10,6
M 54
AD 11,7
AD 12,8
M 55
AD 56,9
AW 88,10
M 56
W 2
AW 112,11
M 57
AW 136,12
UL 6,42
M 58
UH 7,42
UL 8,43
M 59
R 26
M 60
mw 3
UH 9,43
AR 10,89
M 61
ad 84,387
W 0
M 62
AR 11,113
AR 12,137
M 63
AD 10,6
AD 11,7
M 64
AD 12,8
AD 57,9
M 65
W 1
AW 89,10
M 66
AW 113,11
AW 137,12
M 67
UL 6,36
UH 7,36
M 68
UL 8,37
UH 9,37
M 69
W 2
AR 10,90
M 70
AR 11,114
AR 12,138
M 71
WL 0
R 28
M 72
AD 10,6
AD 11,7
M 73
bc 48,8,64,56
AD 12,8
M 74
mh 9
AD 58,9
AW 90,10
M 75
ad 64,800
H 0
M 76
AW 114,11
AW 138,12
M 77
UL 6,28
UH 7,28
M 78
UL 8,29
UH 9,29
M 79
H 1
AR 10,91
M 80
AR 11,115
AR 12,139
M 81
AD 10,6
AD 11,7
M 82
AD 12,8
AD 59,9
M 83
R 30
M 84
mh 11
AW 91,10
AW 115,11
M 85
ad 66,801
H 0
M 86
AW 139,12
M 87
M 88
M 89
mh 12
SA 180,1536
M 90
ad 64,802
H 0
M 91
M 92
M 93
M 94
H 1
M 95
WL 2
R 32
M 96
M 97
M 98
bc 49,9,66,60
M 99
mh 0
M 100
ad 66,896
H 0
M 101
M 102
M 103
M 104
M 105
M 106
M 107
WL 2
R 34
M 108
M 109
M 110
M 111
M 112
M 113
M 114
M 115
M 116
WV 28
M 117
WL 0
s_barrier
M 118
mw 24
M 119
R 48
M 120
ad 84,408
W 0
M 121
M 132
M 133
T 24,2
M 122
T 28,16
M 123
W 1
M 134
M 135
T 32,3
M 124
T 36,17
M 125
W 2
SA 182,2304
M 136
M 137
T 40,4
M 126
T 44,18
M 127
mw 27
M 138
ad 84,411
W 0
M 139
WL 8
T 48,5
M 128
T 52,19
M 129
M 140
W 1
M 141
T 56,6
M 130
WL 8
T 60,20
M 131
R 50
M 142
W 2
M 143
T 64,7
M 0
WL 8
T 68,21
M 1
R 52
M 2
M 3
M 4
M 5
M 6
M 7
WL 8
M 8
M 9
WL 2
M 10
M 11
R 54
M 12
M 13
M 14
M 15
M 16
M 17
M 18
M 19
M 20
M 21
M 22
M 23
WL 2
R 56
M 24
ae 42,74,192
M 25
s_bfe_u32 s52, s50, 0x10009
v_mad_i32_i24 v53, v51, s52, v50
SA 184,0
M 26
M 27
M 28
M 29
M 30
M 31
M 32
M 33
M 34
M 35
WL 2
R 58
M 36
M 37
M 38
M 39
M 40
M 41
M 42
M 43
M 44
WV 27
M 45
WL 0
s_barrier
M 46
mw 48
M 47
R 72
M 48
ad 84,432
W 0
M 49
M 50
WV 24
UL 6,30
UH 7,30
M 51
UL 8,31
UH 9,31
M 52
W 1
AR 10,92
)"
R"(M 53
AR 11,116
AR 12,140
M 54
AD 10,6
AD 11,7
M 55
AD 12,8
AD 60,9
M 56
W 2
AW 92,10
M 57
AW 116,11
AW 140,12
M 58
UL 6,32
UH 7,32
M 59
R 74
M 60
mw 51
UL 8,33
UH 9,33
M 61
ad 84,435
W 0
M 62
AR 10,93
AR 11,117
M 63
SA 186,768
AR 12,141
M 64
AD 10,6
AD 11,7
M 65
W 1
AD 12,8
M 66
AD 61,9
AW 93,10
M 67
AW 117,11
AW 141,12
M 68
UL 6,44
UH 7,44
M 69
W 2
UL 8,45
M 70
UH 9,45
AR 10,94
M 71
WL 0
R 76
M 72
AR 11,118
AR 12,142
M 73
bc 48,9,64,56
AD 10,6
M 74
mh 1
AD 11,7
AD 12,8
M 75
ad 64,896
H 0
M 76
AD 62,9
AW 94,10
M 77
AW 118,11
AW 142,12
M 78
UL 6,34
UH 7,34
M 79
H 1
UL 8,35
M 80
UH 9,35
AR 10,95
M 81
AR 11,119
AR 12,143
M 82
AD 10,6
AD 11,7
M 83
R 78
M 84
mh 3
AD 12,8
AD 63,9
M 85
ad 66,897
H 0
M 86
AW 95,10
AW 119,11
M 87
AW 143,12
M 88
M 89
mh 4
M 90
ad 64,898
H 0
M 91
M 92
M 93
M 94
H 1
M 95
WL 2
R 80
M 96
M 97
M 98
bc 49,10,66,60
M 99
mh 6
M 100
ad 66,992
H 0
M 101
SA 188,1536
M 102
M 103
M 104
M 105
M 106
M 107
WL 2
R 82
M 108
M 109
M 110
M 111
M 112
M 113
M 114
M 115
M 116
WV 23
M 117
WL 0
s_barrier
M 118
mw 72
M 119
R 0
M 120
ad 84,456
W 0
M 121
M 132
M 133
T 24,8
M 122
T 28,22
M 123
W 1
M 134
M 135
T 32,9
M 124
T 36,23
M 125
W 2
M 136
M 137
T 40,10
M 126
T 44,24
M 127
mw 75
M 138
ad 84,459
W 0
M 139
WL 8
T 48,11
M 128
T 52,25
M 129
M 140
W 1
M 141
T 56,12
M 130
WL 8
T 60,26
M 131
R 2
M 142
W 2
SA 190,2304
M 143
T 64,13
M 0
WL 8
T 68,27
M 1
R 4
M 2
M 3
M 4
M 5
M 6
M 7
WL 8
M 8
M 9
WL 2
M 10
M 11
R 6
M 12
M 13
M 14
M 15
M 16
M 17
M 18
M 19
M 20
M 21
M 22
M 23
WL 2
R 8
M 24
M 25
M 26
M 27
M 28
M 29
M 30
M 31
M 32
ae 44,70,384
M 33
s_bitcmp1_b32 s50, 12
s_cselect_b32 s51, 24576, 0
v_add_u32 v52, s51, v3
M 34
XL 22,128
M 35
WL 2
R 10
M 36
ae 42,72,192
M 37
s_bfe_u32 s52, s50, 0x10008
v_mad_i32_i24 v53, v51, s52, v50
SA 192,128
M 38
XL 24,896
M 39
M 40
M 41
M 42
M 43
WV 25
M 44
WL 0
XL 26,1664
M 45
s_barrier
M 46
mw 0
M 47
R 24
M 48
ad 84,480
W 0
M 49
M 50
XL 42,2432
M 51
M 52
W 1
M 53
M 54
M 55
M 56
W 2
M 57
M 58
M 59
R 26
M 60
mw 3
M 61
ad 84,483
W 0
M 62
M 63
M 64
M 65
W 1
M 66
M 67
M 68
M 69
W 2
M 70
M 71
WL 2
R 28
M 72
M 73
bc 48,10,64,56
M 74
mh 7
M 75
ad 64,992
H 0
M 76
SA 194,896
M 77
M 78
M 79
H 1
M 80
M 81
M 82
M 83
WL 2
R 30
M 84
mh 9
M 85
ad 66,993
H 0
M 86
M 87
M 88
M 89
mh 10
M 90
ad 64,994
H 0
M 91
M 92
M 93
M 94
H 1
M 95
WL 2
R 32
M 96
M 97
M 98
bc 49,11,66,60
M 99
mh 12
M 100
ad 66,1088
H 0
M 101
M 102
M 103
M 104
bc 48,11,64,56
M 105
mh 13
M 106
ad 64,1088
H 0
M 107
WL 2
R 34
M 108
M 109
M 110
M 111
M 112
M 113
M 114
SA 196,1664
M 115
WV 29
M 116
WL 0
M 117
s_barrier
M 118
mw 24
M 119
R 48
M 120
ad 84,504
W 0
M 121
M 132
)"
R"(M 133
T 24,0
M 122
T 28,14
M 123
W 1
M 134
M 135
T 32,1
M 124
T 36,15
M 125
W 2
M 136
M 137
T 40,2
M 126
T 44,16
M 127
mw 27
M 138
ad 84,507
W 0
M 139
WL 8
T 48,3
M 128
T 52,17
M 129
M 140
W 1
M 141
T 56,4
M 130
WL 8
T 60,18
M 131
R 50
M 142
W 2
M 143
T 64,5
M 0
WL 8
T 68,19
M 1
R 52
M 2
M 3
M 4
M 5
M 6
M 7
WL 8
M 8
SA 198,2432
M 9
WL 2
M 10
M 11
R 54
M 12
M 13
M 14
M 15
M 16
M 17
M 18
M 19
M 20
M 21
M 22
M 23
WL 2
R 56
M 24
M 25
M 26
M 27
M 28
M 29
M 30
M 31
M 32
M 33
M 34
ae 44,68,384
M 35
WL 2
R 58
M 36
s_bitcmp1_b32 s50, 13
s_cselect_b32 s51, 24576, 0
v_add_u32 v52, s51, v3
M 37
XL 36,128
M 38
M 39
M 40
XL 28,896
M 41
M 42
M 43
WV 30
M 44
WL 0
M 45
s_barrier
M 46
mw 48
XL 30,1664
M 47
R 72
M 48
ad 84,528
W 0
M 49
ae 42,74,192
M 50
s_bfe_u32 s52, s50, 0x10009
v_mad_i32_i24 v53, v51, s52, v50
SA 200,128
M 51
M 52
W 1
XL 32,2432
M 53
M 54
M 55
M 56
W 2
M 57
M 58
M 59
R 74
M 60
mw 51
M 61
ad 84,531
W 0
M 62
M 63
M 64
M 65
W 1
M 66
M 67
M 68
M 69
W 2
M 70
M 71
WL 2
R 76
M 72
M 73
mh 0
M 74
ad 64,1089
H 0
M 75
M 76
M 77
M 78
mh 1
M 79
ad 66,1089
H 0
M 80
M 81
M 82
M 83
WL 2
R 78
M 84
mh 2
M 85
ad 64,1090
H 0
M 86
M 87
M 88
WV 40
SA 202,896
UL 6,22
M 89
H 1
UH 7,22
M 90
UL 8,23
UH 9,23
M 91
AD 64,6
AD 88,7
M 92
WV 39
AD 112,8
AD 136,9
M 93
bc 49,12,66,60
UL 6,24
M 94
mh 4
UH 7,24
UL 8,25
M 95
WL 2
R 80
M 96
ad 66,1184
H 0
M 97
UH 9,25
AD 65,6
M 98
AD 89,7
AD 113,8
M 99
AD 137,9
UL 6,26
M 100
UH 7,26
UL 8,27
M 101
UH 9,27
AD 66,6
M 102
AD 90,7
AD 114,8
M 103
WV 38
AD 138,9
UL 6,42
M 104
UH 7,42
UL 8,43
M 105
UH 9,43
AD 67,6
M 106
AD 91,7
AD 115,8
M 107
WL 1
R 82
M 108
AD 139,9
M 109
M 110
M 111
M 112
M 113
M 114
M 115
WV 27
M 116
WL 0
M 117
s_barrier
M 118
mw 72
M 119
R 0
M 120
ad 84,552
W 0
M 121
M 132
M 133
T 24,6
M 122
T 28,20
M 123
W 1
M 134
SA 204,1664
M 135
T 32,7
M 124
T 36,21
M 125
W 2
M 136
M 137
T 40,8
M 126
T 44,22
M 127
mw 75
M 138
ad 84,555
W 0
M 139
WL 8
T 48,9
M 128
T 52,23
M 129
M 140
W 1
M 141
T 56,10
M 130
WL 8
T 60,24
M 131
R 2
M 142
W 2
M 143
T 64,11
M 0
WL 8
T 68,25
M 1
R 4
M 2
M 3
M 4
M 5
M 6
M 7
WL 8
M 8
M 9
WL 2
M 10
M 11
R 6
M 12
M 13
M 14
M 15
M 16
M 17
M 18
M 19
M 20
SA 206,2432
M 21
M 22
M 23
WL 2
R 8
M 24
M 25
M 26
M 27
M 28
M 29
M 30
M 31
M 32
M 33
M 34
M 35
WL 2
R 10
M 36
ae 44,68,384
M 37
s_bfe_u32 s51, s50, 0x1000e
v_mad_i32_i24 v52, v51, s51, v50
XL 44,128
M 38
M 39
M 40
M 41
M 42
XL 34,896
M 43
WV 28
M 44
WL 0
M 45
s_barrier
M 46
mw 0
M 47
R 24
M 48
ad 84,576
W 0
M 49
XL 22,1664
M 50
M 51
M 52
W 1
M 53
M 54
XL 24,2432
M 55
M 56
W 2
M 57
M 58
ae 42,72,192
M 59
R 26
)"
R"(M 60
mw 3
M 61
ad 84,579
W 0
M 62
s_bfe_u32 s52, s50, 0x10008
v_mad_i32_i24 v53, v51, s52, v50
SA 208,256
M 63
M 64
M 65
W 1
M 66
M 67
M 68
M 69
W 2
M 70
M 71
WL 2
R 28
M 72
M 73
bc 48,12,64,56
M 74
mh 5
M 75
ad 64,1184
H 0
M 76
M 77
M 78
M 79
H 1
M 80
M 81
M 82
M 83
WL 2
R 30
M 84
mh 7
M 85
ad 66,1185
H 0
M 86
M 87
M 88
M 89
mh 8
M 90
ad 64,1186
H 0
M 91
WV 37
UL 6,36
UH 7,36
M 92
UL 8,37
UH 9,37
M 93
AD 68,6
AD 92,7
M 94
H 1
AD 116,8
M 95
WL 2
R 32
M 96
AD 140,9
UL 6,28
M 97
UH 7,28
UL 8,29
M 98
bc 49,13,66,60
UH 9,29
M 99
mh 10
AD 69,6
AD 93,7
M 100
ad 66,1280
H 0
M 101
SA 210,1024
AD 117,8
M 102
AD 141,9
UL 6,30
M 103
UH 7,30
UL 8,31
M 104
UH 9,31
AD 70,6
M 105
AD 94,7
AD 118,8
M 106
WV 36
AD 142,9
UL 6,32
M 107
WL 0
R 34
M 108
UH 7,32
UL 8,33
M 109
UH 9,33
AD 71,6
M 110
AD 95,7
AD 119,8
M 111
AD 143,9
M 112
M 113
M 114
M 115
WV 27
M 116
WL 0
M 117
s_barrier
M 118
mw 24
M 119
R 48
M 120
ad 84,600
W 0
M 121
M 132
M 133
T 24,12
M 122
T 28,26
M 123
W 1
M 134
M 135
T 32,13
M 124
T 36,27
M 125
W 2
M 136
M 137
T 40,0
M 126
T 44,14
M 127
mw 27
M 138
ad 84,603
W 0
M 139
WL 8
T 48,1
M 128
T 52,15
M 129
M 140
W 1
M 141
T 56,2
M 130
WL 8
T 60,16
M 131
R 50
M 142
W 2
SA 212,1792
M 143
T 64,3
M 0
WL 8
T 68,17
M 1
R 52
M 2
M 3
M 4
M 5
M 6
M 7
WL 8
M 8
M 9
WL 2
M 10
M 11
R 54
M 12
M 13
M 14
M 15
M 16
M 17
M 18
M 19
M 20
M 21
M 22
M 23
WL 2
R 56
M 24
M 25
M 26
M 27
M 28
M 29
M 30
M 31
M 32
M 33
M 34
M 35
WL 2
R 58
M 36
SA 214,2560
M 37
M 38
ae 44,70,384
M 39
s_bfe_u32 s51, s50, 0x1000f
v_mad_i32_i24 v52, v51, s51, v50
XL 26,128
M 40
M 41
M 42
M 43
WV 28
M 44
WL 0
XL 42,896
M 45
s_barrier
M 46
mw 48
M 47
R 72
M 48
ad 84,624
W 0
M 49
M 50
XL 36,1664
M 51
M 52
W 1
M 53
M 54
M 55
M 56
W 2
XL 28,2432
M 57
M 58
M 59
R 74
M 60
mw 51
M 61
ad 84,627
W 0
M 62
M 63
M 64
M 65
W 1
M 66
M 67
M 68
M 69
W 2
M 70
M 71
WL 2
R 76
M 72
M 73
bc 48,13,64,56
M 74
mh 11
M 75
ad 64,1280
H 0
M 76
ae 42,74,192
M 77
s_bfe_u32 s52, s50, 0x10009
v_mad_i32_i24 v53, v51, s52, v50
SA 216,256
M 78
M 79
H 1
M 80
M 81
M 82
M 83
WL 2
R 78
M 84
mh 13
M 85
ad 66,1281
H 0
M 86
M 87
M 88
M 89
mh 0
M 90
ad 64,1282
H 0
M 91
M 92
WV 39
UL 6,44
UH 7,44
M 93
UL 8,45
UH 9,45
M 94
H 1
AD 72,6
M 95
WL 2
R 80
M 96
AD 96,7
AD 120,8
M 97
AD 144,9
UL 6,34
M 98
bc 49,14,66,60
UH 7,34
M 99
mh 2
UL 8,35
UH 9,35
M 100
ad 66,1376
H 0
M 101
AD 73,6
AD 97,7
M 102
AD 121,8
AD 145,9
M 103
WV 39
UL 6,22
UH 7,22
M 104
UL 8,23
UH 9,23
M 105
AD 74,6
AD 98,7
M 106
AD 122,8
AD 146,9
M 107
WL 0
R 82
M 108
WV 37
UL 6,24
UH 7,24
M 109
UL 8,25
UH 9,25
M 110
AD 75,6
AD 99,7
M 111
AD 123,8
AD 147,9
M 112
M 113
M 114
M 115
WV 27
SA 218,1024
M 116
WL 0
M 117
s_barrier
M 118
mw 72
)"
R"(M 119
R 0
M 120
ad 84,648
W 0
M 121
M 132
M 133
T 24,4
M 122
T 28,18
M 123
W 1
M 134
M 135
T 32,5
M 124
T 36,19
M 125
W 2
M 136
M 137
T 40,6
M 126
T 44,20
M 127
mw 75
M 138
ad 84,651
W 0
M 139
WL 8
T 48,7
M 128
T 52,21
M 129
M 140
W 1
M 141
T 56,8
M 130
WL 8
T 60,22
M 131
R 2
M 142
W 2
M 143
T 64,9
M 0
WL 8
T 68,23
M 1
R 4
M 2
M 3
M 4
M 5
M 6
M 7
WL 8
M 8
M 9
WL 2
SA 220,1792
M 10
M 11
R 6
M 12
M 13
M 14
M 15
M 16
M 17
M 18
M 19
M 20
M 21
M 22
M 23
WL 2
R 8
M 24
M 25
M 26
M 27
M 28
M 29
M 30
M 31
M 32
M 33
M 34
M 35
WL 2
R 10
M 36
M 37
M 38
M 39
M 40
ae 44,68,432
M 41
s_bitcmp1_b32 s50, 16
s_cselect_b32 s51, 24576, 0
v_add_u32 v52, s51, v3
M 42
XL 30,128
M 43
WV 28
M 44
WL 0
M 45
s_barrier
M 46
mw 0
XL 32,896
M 47
R 24
M 48
ad 84,672
W 0
M 49
SA 222,2560
M 50
M 51
M 52
W 1
XL 44,1664
M 53
M 54
M 55
M 56
W 2
M 57
M 58
XL 34,2432
M 59
R 26
M 60
mw 3
M 61
ad 84,675
W 0
M 62
M 63
M 64
M 65
W 1
M 66
M 67
M 68
M 69
W 2
M 70
M 71
WL 2
R 28
M 72
M 73
bc 48,14,64,56
M 74
mh 3
M 75
ad 64,1376
H 0
M 76
M 77
M 78
M 79
H 1
M 80
M 81
M 82
M 83
WL 2
R 30
M 84
mh 5
M 85
ad 66,1377
H 0
M 86
M 87
ae 42,72,240
M 88
s_bitcmp1_b32 s50, 10
s_cselect_b32 s52, 24576, 0
v_add_u32 v53, s52, v3
M 89
mh 6
SA 224,0
M 90
ad 64,1378
H 0
M 91
M 92
M 93
M 94
WV 39
H 1
UL 6,26
M 95
WL 2
R 32
M 96
UH 7,26
UL 8,27
M 97
UH 9,27
AD 76,6
M 98
bc 49,15,66,60
AD 100,7
M 99
mh 8
AD 124,8
AD 148,9
M 100
ad 66,1472
H 0
M 101
UL 6,42
UH 7,42
M 102
UL 8,43
UH 9,43
M 103
AD 77,6
AD 101,7
M 104
AD 125,8
AD 149,9
M 105
WV 39
UL 6,36
UH 7,36
M 106
UL 8,37
UH 9,37
M 107
WL 0
R 34
M 108
AD 78,6
AD 102,7
M 109
AD 126,8
AD 150,9
M 110
WV 36
UL 6,28
UH 7,28
M 111
UL 8,29
UH 9,29
M 112
AD 79,6
AD 103,7
M 113
AD 127,8
AD 151,9
M 114
M 115
WV 27
M 116
WL 0
M 117
s_barrier
M 118
mw 24
M 119
R 48
M 120
ad 84,696
W 0
M 121
M 132
M 133
T 24,10
M 122
T 28,24
M 123
W 1
M 134
M 135
T 32,11
M 124
T 36,25
M 125
W 2
SA 226,768
M 136
M 137
T 40,12
M 126
T 44,26
M 127
mw 27
M 138
ad 84,699
W 0
M 139
WL 8
T 48,13
M 128
T 52,27
M 129
M 140
W 1
M 141
T 56,0
M 130
WL 8
T 60,14
M 131
R 50
M 142
W 2
M 143
T 64,1
M 0
WL 8
T 68,15
M 1
R 52
M 2
M 3
M 4
M 5
M 6
M 7
WL 8
M 8
M 9
WL 2
M 10
M 11
R 54
M 12
M 13
M 14
M 15
M 16
M 17
M 18
M 19
M 20
M 21
M 22
M 23
WL 2
R 56
M 24
SA 228,1536
M 25
M 26
M 27
M 28
M 29
M 30
M 31
M 32
M 33
M 34
M 35
WL 2
R 58
M 36
M 37
M 38
M 39
M 40
M 41
M 42
ae 44,68,432
s_bfe_u32 s51, s50, 0x10011
M 43
WV 27
v_mad_i32_i24 v52, v51, s51, v50
XL 22,128
M 44
WL 0
M 45
s_barrier
M 46
mw 48
M 47
R 72
M 48
ad 84,720
W 0
M 49
XL 24,896
M 50
M 51
M 52
W 1
M 53
M 54
)"
R"(XL 26,1664
M 55
M 56
W 2
M 57
M 58
M 59
R 74
M 60
mw 51
XL 42,2432
M 61
ad 84,723
W 0
M 62
SA 230,2304
M 63
M 64
M 65
W 1
M 66
M 67
M 68
M 69
W 2
M 70
M 71
WL 2
R 76
M 72
M 73
bc 48,15,64,56
M 74
mh 9
M 75
ad 64,1472
H 0
M 76
M 77
M 78
M 79
H 1
M 80
M 81
M 82
M 83
WL 2
R 78
M 84
mh 11
M 85
ad 66,1473
H 0
M 86
M 87
M 88
M 89
mh 12
M 90
ad 64,1474
H 0
M 91
M 92
M 93
M 94
H 1
M 95
WL 2
R 80
M 96
WV 40
UL 6,30
UH 7,30
M 97
UL 8,31
UH 9,31
M 98
bc 49,16,66,60
AD 80,6
M 99
mh 0
AD 104,7
AD 128,8
M 100
ad 66,1568
H 0
M 101
ae 42,72,240
AD 152,9
M 102
s_bfe_u32 s52, s50, 0x1000b
v_mad_i32_i24 v53, v51, s52, v50
SA 232,0
M 103
UL 6,32
UH 7,32
M 104
UL 8,33
UH 9,33
M 105
AD 81,6
AD 105,7
M 106
AD 129,8
AD 153,9
M 107
WL 0
R 82
M 108
WV 38
UL 6,44
UH 7,44
M 109
UL 8,45
UH 9,45
M 110
AD 82,6
AD 106,7
M 111
AD 130,8
AD 154,9
M 112
WV 36
UL 6,34
UH 7,34
M 113
UL 8,35
UH 9,35
M 114
AD 83,6
AD 107,7
M 115
WV 27
AD 131,8
AD 155,9
M 116
WL 0
M 117
s_barrier
M 118
mw 72
M 119
R 0
M 120
ad 84,744
W 0
M 121
M 132
M 133
T 24,2
M 122
T 28,16
M 123
W 1
M 134
M 135
T 32,3
M 124
T 36,17
M 125
W 2
M 136
M 137
T 40,4
M 126
T 44,18
M 127
mw 75
M 138
ad 84,747
W 0
M 139
WL 8
T 48,5
M 128
T 52,19
M 129
M 140
W 1
M 141
T 56,6
M 130
WL 8
T 60,20
M 131
R 2
M 142
W 2
SA 234,768
M 143
T 64,7
M 0
WL 8
T 68,21
M 1
R 4
M 2
M 3
M 4
M 5
M 6
M 7
WL 8
M 8
M 9
WL 2
M 10
M 11
R 6
M 12
M 13
M 14
M 15
M 16
M 17
M 18
M 19
M 20
M 21
M 22
M 23
WL 2
R 8
M 24
M 25
M 26
M 27
M 28
M 29
M 30
M 31
M 32
M 33
M 34
M 35
WL 2
R 10
M 36
SA 236,1536
M 37
M 38
M 39
M 40
M 41
M 42
M 43
WV 27
M 44
WL 0
M 45
s_barrier
M 46
mw 0
M 47
R 24
M 48
ad 84,768
W 0
M 49
M 50
M 51
M 52
W 1
M 53
M 54
M 55
M 56
W 2
M 57
M 58
M 59
R 26
M 60
mw 3
M 61
ad 84,771
W 0
M 62
M 63
M 64
M 65
W 1
M 66
M 67
M 68
M 69
W 2
M 70
M 71
WL 2
R 28
M 72
M 73
bc 48,16,64,56
M 74
mh 1
SA 238,2304
M 75
ad 64,1568
H 0
M 76
M 77
M 78
M 79
H 1
M 80
M 81
M 82
M 83
WL 2
R 30
M 84
mh 3
M 85
ad 66,1569
H 0
M 86
M 87
M 88
M 89
mh 4
M 90
ad 64,1570
H 0
M 91
M 92
M 93
M 94
H 1
M 95
WL 2
R 32
M 96
M 97
WV 37
M 98
bc 49,17,66,60
UL 6,22
M 99
mh 6
UH 7,22
UL 8,23
M 100
ad 66,1664
H 0
M 101
UH 9,23
AD 84,6
M 102
AD 108,7
AD 132,8
M 103
WV 36
AD 156,9
UL 6,24
M 104
UH 7,24
UL 8,25
M 105
UH 9,25
AD 85,6
M 106
AD 109,7
AD 133,8
M 107
WL 2
R 34
M 108
WV 34
AD 157,9
UL 6,26
M 109
UH 7,26
UL 8,27
M 110
UH 9,27
AD 86,6
M 111
AD 110,7
AD 134,8
M 112
ae 42,72,240
AD 158,9
M 113
s_bitcmp1_b32 s50, 10
s_cselect_b32 s52, 24576, 0
v_add_u32 v53, s52, v3
M 114
WV 32
SA 240,128
UL 6,42
M 115
WV 24
UH 7,42
UL 8,43
M 116
WL 0
UH 9,43
AD 87,6
M 117
)"
R"(s_barrier
AD 111,7
AD 135,8
M 118
mw 24
AD 159,9
M 119
R 48
M 120
ad 84,792
W 0
M 121
M 132
M 133
T 24,8
M 122
T 28,22
M 123
W 1
M 134
M 135
T 32,9
M 124
T 36,23
M 125
W 2
M 136
M 137
T 40,10
M 126
T 44,24
M 127
mw 27
M 138
ad 84,795
W 0
M 139
WL 8
T 48,11
M 128
T 52,25
M 129
M 140
W 1
M 141
T 56,12
M 130
WL 8
T 60,26
M 131
R 50
M 142
W 2
M 143
T 64,13
M 0
WL 8
T 68,27
M 1
R 52
M 2
M 3
M 4
M 5
M 6
M 7
WL 8
M 8
SA 242,896
M 9
WL 2
M 10
M 11
R 54
M 12
M 13
M 14
M 15
M 16
M 17
M 18
M 19
M 20
M 21
M 22
M 23
WL 2
R 56
M 24
M 25
M 26
M 27
M 28
M 29
M 30
M 31
M 32
M 33
M 34
M 35
WL 2
R 58
M 36
M 37
M 38
M 39
M 40
M 41
M 42
M 43
M 44
WV 23
M 45
WL 0
s_barrier
M 46
mw 48
SA 244,1664
M 47
R 72
M 48
ad 84,816
W 0
M 49
M 50
M 51
M 52
W 1
M 53
M 54
M 55
M 56
W 2
M 57
M 58
M 59
R 74
M 60
mw 51
M 61
ad 84,819
W 0
M 62
M 63
M 64
M 65
W 1
M 66
M 67
M 68
M 69
W 2
M 70
ae 44,70,384
M 71
WL 2
R 76
M 72
s_bitcmp1_b32 s50, 12
s_cselect_b32 s51, 24576, 0
v_add_u32 v52, s51, v3
M 73
bc 48,17,64,56
XL 36,256
M 74
mh 7
M 75
ad 64,1664
H 0
M 76
XL 28,1024
M 77
M 78
M 79
H 1
M 80
M 81
M 82
XL 30,1792
M 83
WL 2
R 78
M 84
mh 9
SA 246,2432
M 85
ad 66,1665
H 0
M 86
M 87
M 88
XL 32,2560
M 89
mh 10
M 90
ad 64,1666
H 0
M 91
M 92
M 93
M 94
H 1
M 95
WL 2
R 80
M 96
M 97
M 98
bc 49,18,66,60
M 99
mh 12
M 100
ad 66,1760
H 0
M 101
M 102
M 103
M 104
bc 48,18,64,56
M 105
mh 13
M 106
ad 64,1760
H 0
M 107
WL 2
R 82
M 108
M 109
M 110
M 111
M 112
M 113
M 114
M 115
M 116
WV 28
M 117
WL 0
s_barrier
M 118
mw 72
M 119
R 0
M 120
ad 84,840
W 0
M 121
M 132
ae 42,72,240
M 133
s_bfe_u32 s52, s50, 0x1000b
T 24,0
M 122
T 28,14
M 123
W 1
v_mad_i32_i24 v53, v51, s52, v50
M 134
SA 248,128
M 135
T 32,1
M 124
T 36,15
M 125
W 2
M 136
M 137
T 40,2
M 126
T 44,16
M 127
mw 75
M 138
ad 84,843
W 0
M 139
WL 8
T 48,3
M 128
T 52,17
M 129
M 140
W 1
M 141
T 56,4
M 130
WL 8
T 60,18
M 131
R 2
M 142
W 2
M 143
T 64,5
M 0
WL 8
T 68,19
M 1
R 4
M 2
M 3
M 4
M 5
M 6
M 7
WL 8
M 8
M 9
WL 2
M 10
M 11
R 6
M 12
M 13
M 14
M 15
M 16
M 17
M 18
M 19
M 20
SA 250,896
M 21
M 22
M 23
WL 2
R 8
M 24
M 25
M 26
M 27
M 28
M 29
M 30
M 31
M 32
M 33
M 34
M 35
WL 2
R 10
M 36
M 37
M 38
M 39
M 40
M 41
M 42
M 43
M 44
WV 28
M 45
WL 0
s_barrier
M 46
mw 0
M 47
R 24
M 48
ad 84,864
W 0
M 49
M 50
M 51
M 52
W 1
M 53
M 54
M 55
M 56
W 2
M 57
M 58
SA 252,1664
M 59
R 26
M 60
mw 3
M 61
ad 84,867
W 0
M 62
M 63
M 64
M 65
W 1
M 66
M 67
M 68
M 69
W 2
M 70
M 71
WL 2
R 28
M 72
M 73
mh 0
M 74
)"
R"(ad 64,1761
H 0
M 75
ae 44,68,384
M 76
s_bitcmp1_b32 s50, 13
s_cselect_b32 s51, 24576, 0
v_add_u32 v52, s51, v3
M 77
XL 44,256
M 78
mh 1
M 79
ad 66,1761
H 0
M 80
XL 34,1024
M 81
M 82
M 83
WL 2
R 30
M 84
mh 2
M 85
ad 64,1762
H 0
M 86
XL 22,1792
M 87
M 88
M 89
H 1
M 90
M 91
M 92
XL 24,2560
M 93
bc 49,19,66,60
M 94
mh 4
M 95
WL 2
R 32
M 96
ad 66,1856
H 0
M 97
SA 254,2432
M 98
M 99
M 100
M 101
M 102
M 103
M 104
M 105
M 106
M 107
WL 2
R 34
M 108
M 109
M 110
M 111
M 112
M 113
M 114
M 115
M 116
WV 27
M 117
WL 0
s_barrier
M 118
mw 24
M 119
R 48
M 120
ad 84,888
W 0
M 121
M 132
M 133
T 24,6
M 122
T 28,20
M 123
W 1
M 134
UL 6,36
UH 7,36
M 135
T 32,7
M 124
T 36,21
M 125
W 2
UL 8,37
M 136
UH 9,37
AD 160,6
M 137
T 40,8
M 126
T 44,22
M 127
mw 27
AD 184,7
AD 208,8
M 138
ad 84,891
W 0
M 139
WL 8
T 48,9
M 128
T 52,23
M 129
ae 42,72,240
AD 232,9
M 140
W 1
UL 6,28
M 141
T 56,10
M 130
WL 8
T 60,24
M 131
R 50
M 142
W 2
UH 7,28
M 143
T 64,11
M 0
WL 8
T 68,25
M 1
R 52
M 2
s_bitcmp1_b32 s50, 10
s_cselect_b32 s52, 24576, 0
M 3
v_add_u32 v53, s52, v3
ST 38,256
M 4
UL 8,29
UH 9,29
M 5
AD 161,6
AD 185,7
M 6
AD 209,8
AD 233,9
M 7
WL 8
UL 6,30
UH 7,30
M 8
UL 8,31
UH 9,31
M 9
WL 2
AD 162,6
AD 186,7
M 10
AD 210,8
AD 234,9
M 11
R 54
M 12
UL 6,32
UH 7,32
M 13
UL 8,33
UH 9,33
M 14
AD 163,6
AD 187,7
M 15
AD 211,8
AD 235,9
M 16
M 17
M 18
M 19
M 20
M 21
M 22
M 23
WL 2
R 56
M 24
M 25
M 26
M 27
M 28
M 29
M 30
M 31
M 32
M 33
M 34
M 35
WL 2
R 58
M 36
M 37
M 38
M 39
M 40
M 41
ST 40,1024
M 42
M 43
WV 26
M 44
WL 0
M 45
s_barrier
M 46
mw 48
M 47
R 72
M 48
ad 84,912
W 0
M 49
M 50
M 51
M 52
W 1
M 53
M 54
M 55
M 56
W 2
M 57
M 58
M 59
R 74
M 60
mw 51
M 61
ad 84,915
W 0
M 62
M 63
M 64
M 65
W 1
M 66
M 67
M 68
M 69
W 2
M 70
M 71
WL 2
R 76
M 72
M 73
bc 48,19,64,56
M 74
mh 5
M 75
ad 64,1856
H 0
M 76
M 77
M 78
ae 44,68,384
M 79
s_bfe_u32 s51, s50, 0x1000e
H 1
ST 18,1792
M 80
v_mad_i32_i24 v52, v51, s51, v50
XL 26,256
M 81
M 82
M 83
WL 2
R 78
M 84
mh 7
XL 42,1024
M 85
ad 66,1857
H 0
M 86
M 87
M 88
M 89
mh 8
M 90
ad 64,1858
H 0
M 91
XL 38,1792
M 92
M 93
M 94
H 1
M 95
WL 2
R 80
M 96
XL 36,2560
M 97
M 98
bc 49,20,66,60
M 99
mh 10
M 100
ad 66,1952
H 0
M 101
M 102
M 103
M 104
M 105
M 106
M 107
WL 2
R 82
M 108
M 109
M 110
M 111
M 112
M 113
M 114
M 115
M 116
WV 28
M 117
WL 0
s_barrier
ST 46,2560
M 118
mw 72
M 119
R 0
M 120
ad 84,936
W 0
M 121
M 132
M 133
T 24,12
M 122
T 28,26
M 123
W 1
M 134
M 135
T 32,13
M 124
T 36,27
M 125
W 2
M 136
UL 6,44
UH 7,44
M 137
T 40,0
M 126
T 44,14
M 127
mw 75
UL 8,45
UH 9,45
M 138
ad 84,939
W 0
M 139
WL 8
T 48,1
M 128
T 52,15
M 129
AD 164,6
AD 188,7
M 140
W 1
AD 212,8
M 141
T 56,2
M 130
WL 8
T 60,16
M 131
R 2
M 142
W 2
AD 236,9
M 143
)"
R"(T 64,3
M 0
WL 8
T 68,17
M 1
R 4
M 2
UL 6,34
UH 7,34
M 3
UL 8,35
UH 9,35
M 4
AD 165,6
AD 189,7
M 5
AD 213,8
AD 237,9
M 6
UL 6,22
UH 7,22
M 7
WL 8
UL 8,23
UH 9,23
M 8
AD 166,6
AD 190,7
M 9
WL 2
AD 214,8
AD 238,9
M 10
WV 34
UL 6,24
UH 7,24
M 11
R 6
M 12
ae 42,72,240
UL 8,25
M 13
s_bfe_u32 s52, s50, 0x1000b
v_mad_i32_i24 v53, v51, s52, v50
ST 48,256
M 14
UH 9,25
AD 167,6
M 15
AD 191,7
AD 215,8
M 16
AD 239,9
M 17
M 18
M 19
M 20
M 21
M 22
M 23
WL 2
R 8
M 24
M 25
M 26
M 27
M 28
M 29
M 30
M 31
M 32
M 33
M 34
M 35
WL 2
R 10
M 36
M 37
M 38
M 39
M 40
M 41
M 42
M 43
M 44
WV 27
M 45
WL 0
s_barrier
M 46
mw 0
M 47
R 24
M 48
ad 84,960
W 0
M 49
M 50
M 51
ST 14,1024
M 52
W 1
M 53
M 54
M 55
M 56
W 2
M 57
M 58
M 59
R 26
M 60
mw 3
M 61
ad 84,963
W 0
M 62
M 63
M 64
M 65
W 1
M 66
M 67
M 68
M 69
W 2
M 70
M 71
WL 2
R 28
M 72
M 73
bc 48,20,64,56
M 74
mh 11
M 75
ad 64,1952
H 0
M 76
M 77
M 78
M 79
H 1
M 80
M 81
M 82
ae 44,70,384
M 83
WL 2
R 30
M 84
mh 13
M 85
ad 66,1953
H 0
M 86
s_bfe_u32 s51, s50, 0x1000f
v_mad_i32_i24 v52, v51, s51, v50
XL 28,256
M 87
M 88
XL 30,1024
M 89
mh 0
ST 16,1792
M 90
ad 64,1954
H 0
M 91
M 92
M 93
M 94
H 1
XL 32,1792
M 95
WL 2
R 32
M 96
M 97
M 98
bc 49,21,66,60
M 99
mh 2
M 100
ad 66,2048
H 0
M 101
XL 40,2560
M 102
M 103
M 104
M 105
M 106
M 107
WL 2
R 34
M 108
M 109
M 110
M 111
M 112
M 113
M 114
M 115
M 116
WV 28
M 117
WL 0
s_barrier
M 118
mw 24
M 119
R 48
M 120
ad 84,984
W 0
M 121
M 132
M 133
T 24,4
M 122
T 28,18
M 123
W 1
M 134
M 135
T 32,5
M 124
T 36,19
M 125
W 2
ST 20,2560
M 136
M 137
T 40,6
M 126
T 44,20
M 127
mw 27
M 138
ad 84,987
W 0
M 139
WL 8
T 48,7
M 128
T 52,21
M 129
UL 6,26
UH 7,26
M 140
W 1
UL 8,27
M 141
T 56,8
M 130
WL 8
T 60,22
M 131
R 50
M 142
W 2
UH 9,27
M 143
T 64,9
M 0
WL 8
T 68,23
M 1
R 52
M 2
AD 168,6
AD 192,7
M 3
AD 216,8
AD 240,9
M 4
UL 6,42
UH 7,42
M 5
UL 8,43
UH 9,43
M 6
AD 169,6
AD 193,7
M 7
WL 8
AD 217,8
AD 241,9
M 8
UL 6,38
UH 7,38
M 9
WL 2
UL 8,39
UH 9,39
M 10
AD 170,6
AD 194,7
M 11
R 54
M 12
AD 218,8
AD 242,9
M 13
WV 34
UL 6,36
UH 7,36
M 14
UL 8,37
UH 9,37
M 15
AD 171,6
AD 195,7
M 16
AD 219,8
AD 243,9
M 17
M 18
M 19
M 20
M 21
M 22
M 23
WL 2
R 56
M 24
M 25
M 26
M 27
M 28
M 29
M 30
M 31
M 32
M 33
M 34
M 35
WL 2
R 58
M 36
M 37
M 38
M 39
M 40
M 41
M 42
M 43
M 44
WV 26
M 45
WL 0
s_barrier
M 46
mw 48
M 47
R 72
M 48
ad 84,1008
W 0
M 49
M 50
M 51
M 52
W 1
M 53
M 54
M 55
M 56
W 2
M 57
M 58
M 59
R 74
M 60
mw 51
M 61
ad 84,1011
W 0
M 62
M 63
M 64
M 65
W 1
M 66
M 67
M 68
M 69
W 2
M 70
M 71
WL 2
R 76
M 72
M 73
bc 48,21,64,56
M 74
mh 3
M 75
ad 64,2048
)"
R"(H 0
M 76
M 77
M 78
M 79
H 1
M 80
M 81
M 82
M 83
WL 2
R 78
M 84
mh 5
M 85
ad 66,2049
H 0
M 86
ae 44,68,432
M 87
s_bitcmp1_b32 s50, 16
s_cselect_b32 s51, 24576, 0
v_add_u32 v52, s51, v3
M 88
XL 18,256
M 89
mh 6
M 90
ad 64,2050
H 0
M 91
M 92
XL 46,1024
M 93
M 94
H 1
M 95
WL 2
R 80
M 96
M 97
M 98
bc 49,22,66,60
XL 48,1792
M 99
mh 8
M 100
ad 66,2144
H 0
M 101
M 102
M 103
M 104
XL 44,2560
M 105
M 106
M 107
WL 2
R 82
M 108
M 109
M 110
M 111
M 112
M 113
M 114
M 115
M 116
WV 26
M 117
WL 0
s_barrier
M 118
mw 72
M 119
R 0
M 120
ad 84,1032
W 0
M 121
M 132
M 133
T 24,10
M 122
T 28,24
M 123
W 1
M 134
M 135
T 32,11
M 124
T 36,25
M 125
W 2
M 136
M 137
T 40,12
M 126
T 44,26
M 127
mw 75
M 138
ad 84,1035
W 0
M 139
WL 8
T 48,13
M 128
T 52,27
M 129
M 140
W 1
UL 6,28
M 141
T 56,0
M 130
WL 8
T 60,14
M 131
R 2
M 142
W 2
UH 7,28
M 143
T 64,1
M 0
WL 8
T 68,15
M 1
R 4
M 2
UL 8,29
UH 9,29
M 3
AD 172,6
AD 196,7
M 4
AD 220,8
AD 244,9
M 5
UL 6,30
UH 7,30
M 6
UL 8,31
UH 9,31
M 7
WL 8
AD 173,6
AD 197,7
M 8
WL 2
AD 221,8
AD 245,9
M 9
WV 31
UL 6,32
UH 7,32
M 10
UL 8,33
UH 9,33
M 11
R 6
M 12
AD 174,6
AD 198,7
M 13
AD 222,8
AD 246,9
M 14
WV 29
UL 6,40
UH 7,40
M 15
UL 8,41
UH 9,41
M 16
AD 175,6
AD 199,7
M 17
AD 223,8
AD 247,9
M 18
M 19
M 20
M 21
M 22
M 23
WL 2
R 8
M 24
M 25
M 26
M 27
M 28
M 29
M 30
M 31
M 32
M 33
M 34
M 35
WL 2
R 10
M 36
M 37
M 38
M 39
M 40
M 41
M 42
M 43
M 44
WV 22
M 45
WL 0
s_barrier
M 46
mw 0
M 47
R 24
M 48
ad 84,1056
W 0
M 49
M 50
M 51
M 52
W 1
M 53
M 54
M 55
M 56
W 2
M 57
M 58
M 59
R 26
M 60
mw 3
M 61
ad 84,1059
W 0
M 62
M 63
M 64
M 65
W 1
M 66
M 67
M 68
M 69
W 2
M 70
M 71
WL 2
R 28
M 72
M 73
bc 48,22,64,56
M 74
mh 9
M 75
ad 64,2144
H 0
M 76
M 77
M 78
M 79
H 1
M 80
M 81
M 82
M 83
WL 2
R 30
M 84
mh 11
M 85
ad 66,2145
H 0
M 86
M 87
M 88
M 89
mh 12
M 90
ad 64,2146
H 0
M 91
ae 44,68,432
M 92
s_bfe_u32 s51, s50, 0x10011
v_mad_i32_i24 v52, v51, s51, v50
XL 34,256
M 93
M 94
H 1
M 95
WL 2
R 32
M 96
XL 22,1024
M 97
M 98
bc 49,23,66,60
M 99
mh 0
M 100
ad 66,2240
H 0
M 101
M 102
XL 24,1792
M 103
M 104
M 105
M 106
M 107
WL 2
R 34
M 108
XL 14,2560
M 109
M 110
M 111
M 112
M 113
M 114
M 115
WV 25
M 116
WL 0
M 117
s_barrier
M 118
mw 24
M 119
R 48
M 120
ad 84,1080
W 0
M 121
M 132
M 133
T 24,2
M 122
T 28,16
M 123
W 1
M 134
M 135
T 32,3
M 124
T 36,17
M 125
W 2
M 136
M 137
T 40,4
M 126
T 44,18
M 127
mw 27
M 138
ad 84,1083
W 0
M 139
WL 8
T 48,5
M 128
T 52,19
M 129
M 140
W 1
M 141
T 56,6
M 130
WL 8
T 60,20
M 131
R 50
M 142
W 2
UL 6,18
M 143
T 64,7
M 0
WL 8
T 68,21
M 1
R 52
M 2
UH 7,18
UL 8,19
M 3
UH 9,19
AD 176,6
M 4
AD 200,7
AD 224,8
)"
R"(M 5
AD 248,9
UL 6,46
M 6
UH 7,46
UL 8,47
M 7
WL 8
UH 9,47
AD 177,6
M 8
WL 2
AD 201,7
AD 225,8
M 9
WV 30
AD 249,9
UL 6,48
M 10
UH 7,48
UL 8,49
M 11
R 54
M 12
UH 9,49
AD 178,6
M 13
AD 202,7
AD 226,8
M 14
WV 28
AD 250,9
UL 6,44
M 15
UH 7,44
UL 8,45
M 16
UH 9,45
AD 179,6
M 17
AD 203,7
AD 227,8
M 18
AD 251,9
M 19
M 20
M 21
M 22
M 23
WL 2
R 56
M 24
M 25
M 26
M 27
M 28
M 29
M 30
M 31
M 32
M 33
M 34
M 35
WL 2
R 58
M 36
M 37
M 38
M 39
M 40
M 41
M 42
M 43
M 44
WV 22
M 45
WL 0
s_barrier
M 46
mw 48
M 47
R 72
M 48
ad 84,1104
W 0
M 49
M 50
M 51
M 52
W 1
M 53
M 54
M 55
M 56
W 2
M 57
M 58
M 59
R 74
M 60
mw 51
M 61
ad 84,1107
W 0
M 62
M 63
M 64
M 65
W 1
M 66
M 67
M 68
M 69
W 2
M 70
M 71
WL 2
R 76
M 72
M 73
bc 48,23,64,56
M 74
mh 1
M 75
ad 64,2240
H 0
M 76
M 77
M 78
M 79
H 1
M 80
M 81
M 82
M 83
WL 2
R 78
M 84
mh 3
M 85
ad 66,2241
H 0
M 86
M 87
M 88
M 89
mh 4
M 90
ad 64,2242
H 0
M 91
M 92
M 93
M 94
H 1
M 95
WL 2
R 80
M 96
M 97
M 98
bc 48,0,64,56
M 99
mh 6
M 100
ad 64,33
H 0
M 101
M 102
M 103
M 104
M 105
M 106
M 107
WL 2
R 82
M 108
M 109
M 110
M 111
M 112
M 113
M 114
M 115
M 116
WV 22
M 117
WL 0
s_barrier
M 118
mw 72
M 119
R 0
M 120
ad 84,1128
W 0
M 121
M 132
M 133
T 24,8
M 122
T 28,22
M 123
W 1
M 134
M 135
T 32,9
M 124
T 36,23
M 125
W 2
M 136
M 137
T 40,10
M 126
T 44,24
M 127
mw 75
M 138
ad 84,1131
W 0
M 139
WL 8
T 48,11
M 128
T 52,25
M 129
M 140
W 1
M 141
T 56,12
M 130
WL 8
T 60,26
M 131
R 2
M 142
W 2
M 143
T 64,13
M 0
WL 8
T 68,27
M 1
R 4
M 2
bc 49,0,66,60
UL 6,34
M 3
UH 7,34
UL 8,35
M 4
mh 7
UH 9,35
AD 180,6
M 5
WL 8
AD 204,7
AD 228,8
M 6
ad 66,33
H 0
M 7
WV 28
AD 252,9
UL 6,22
M 8
UH 7,22
UL 8,23
M 9
WL 2
UH 9,23
AD 181,6
M 10
AD 205,7
AD 229,8
M 11
R 6
M 12
WV 25
AD 253,9
UL 6,24
M 13
UH 7,24
UL 8,25
M 14
UH 9,25
AD 182,6
M 15
AD 206,7
AD 230,8
M 16
AD 254,9
UL 6,14
M 17
UH 7,14
UL 8,15
M 18
UH 9,15
AD 183,6
M 19
AD 207,7
AD 231,8
M 20
AD 255,9
M 21
M 22
M 23
WL 2
R 8
M 24
M 25
M 26
M 27
M 28
M 29
M 30
M 31
M 32
M 33
M 34
M 35
WL 2
R 10
M 36
M 37
M 38
M 39
M 40
M 41
M 42
M 43
M 44
WV 19
M 45
WL 0
s_barrier
M 46
mw 0
M 47
R 24
M 48
ad 84,0
W 0
M 49
M 50
M 51
M 52
W 1
M 53
M 54
M 55
M 56
W 2
M 57
M 58
M 59
R 26
M 60
mw 3
M 61
ad 84,3
W 0
M 62
M 63
M 64
M 65
W 1
M 66
M 67
M 68
M 69
W 2
M 70
M 71
WL 2
R 28
M 72
M 73
mh 8
M 74
ad 64,34
H 0
M 75
M 76
M 77
M 78
H 1
M 79
M 80
M 81
M 82
bc 48,1,64,56
M 83
WL 2
R 30
M 84
mh 10
M 85
ad 64,130
)"
R"(H 0
M 86
M 87
M 88
M 89
H 1
M 90
M 91
M 92
M 93
M 94
M 95
WL 2
R 32
M 96
M 97
M 98
M 99
M 100
M 101
M 102
M 103
M 104
M 105
M 106
M 107
WL 2
R 34
M 108
M 109
M 110
M 111
M 112
M 113
M 114
M 115
M 116
WV 18
M 117
WL 0
s_barrier
M 118
mw 24
M 119
R 48
M 120
ad 84,24
W 0
M 121
M 132
M 133
T 24,0
M 122
T 28,14
M 123
W 1
M 134
M 135
T 32,1
M 124
T 36,15
M 125
W 2
M 136
M 137
T 40,2
M 126
T 44,16
M 127
mw 27
M 138
ad 84,27
W 0
M 139
WL 8
T 48,3
M 128
T 52,17
M 129
M 140
W 1
M 141
T 56,4
M 130
WL 8
T 60,18
M 131
R 50
M 142
W 2
M 143
T 64,5
M 0
WL 8
T 68,19
M 1
R 52
M 2
M 3
M 4
M 5
M 6
M 7
WL 8
M 8
M 9
WL 2
M 10
M 11
R 54
M 12
M 13
M 14
M 15
M 16
M 17
M 18
M 19
M 20
M 21
M 22
M 23
WL 2
R 56
M 24
M 25
M 26
M 27
M 28
M 29
M 30
M 31
M 32
M 33
M 34
M 35
WL 2
R 58
M 36
M 37
M 38
M 39
M 40
AR 6,72
AR 7,96
M 41
AR 8,120
s_nop 0
AR 9,144
M 42
EF 6
EF 7
M 43
WV 17
EF 8
EF 9
M 44
WL 0
CV 16,6,7
CV 17,8,9
M 45
s_barrier
AR 6,73
AR 7,97
M 46
mw 48
AR 8,121
AR 9,145
M 47
R 72
M 48
ad 84,48
W 0
M 49
EF 6
EF 7
M 50
EF 8
EF 9
M 51
CV 20,6,7
CV 21,8,9
M 52
W 1
AR 6,74
M 53
AR 7,98
AR 8,122
M 54
AR 9,146
EF 6
M 55
EF 7
EF 8
M 56
W 2
EF 9
M 57
CV 26,6,7
CV 27,8,9
M 58
AR 6,75
AR 7,99
M 59
R 74
M 60
mw 51
AR 8,123
AR 9,147
M 61
ad 84,51
W 0
M 62
EF 6
EF 7
M 63
EF 8
EF 9
M 64
CV 42,6,7
CV 43,8,9
M 65
W 1
AR 6,76
M 66
ae 42,74,384
AR 7,100
M 67
s_bitcmp1_b32 s50, 12
s_cselect_b32 s52, 24576, 0
v_add_u32 v53, s52, v3
M 68
ST 16,0
AR 8,124
M 69
W 2
AR 9,148
M 70
EF 6
EF 7
M 71
WL 0
R 76
M 72
ST 20,768
EF 8
M 73
EF 9
CV 38,6,7
M 74
CV 39,8,9
AR 6,77
M 75
AR 7,101
AR 8,125
M 76
ST 26,1536
AR 9,149
M 77
EF 6
EF 7
M 78
EF 8
EF 9
M 79
CV 36,6,7
CV 37,8,9
M 80
ST 42,2304
AR 6,78
M 81
AR 7,102
AR 8,126
M 82
AR 9,150
EF 6
M 83
R 78
M 84
EF 7
EF 8
M 85
EF 9
CV 28,6,7
M 86
CV 29,8,9
AR 6,79
M 87
AR 7,103
AR 8,127
M 88
AR 9,151
EF 6
M 89
EF 7
EF 8
M 90
EF 9
CV 30,6,7
M 91
CV 31,8,9
M 92
ae 42,72,384
EF 64
M 93
s_bitcmp1_b32 s50, 13
s_cselect_b32 s52, 24576, 0
v_add_u32 v53, s52, v3
M 94
ST 38,0
EF 88
M 95
WL 1
R 80
M 96
EF 112
EF 136
M 97
CV 32,64,88
CV 33,112,136
M 98
ST 36,768
EF 65
M 99
EF 89
EF 113
M 100
EF 137
CV 40,65,89
M 101
CV 41,113,137
EF 66
M 102
ST 28,1536
EF 90
M 103
EF 114
EF 138
M 104
CV 18,66,90
CV 19,114,138
M 105
EF 67
EF 91
M 106
ST 30,2304
EF 115
M 107
WL 2
R 82
M 108
EF 139
CV 46,67,91
M 109
CV 47,115,139
M 110
ae 42,74,384
EF 68
M 111
s_bitcmp1_b32 s50, 12
s_cselect_b32 s52, 24576, 0
v_add_u32 v53, s52, v3
M 112
ST 32,128
EF 92
M 113
EF 116
EF 140
M 114
CV 48,68,92
CV 49,116,140
M 115
WV 23
EF 69
EF 93
M 116
WL 0
ST 40,896
EF 117
M 117
s_barrier
EF 141
CV 44,69,93
M 118
mw 72
CV 45,117,141
EF 70
M 119
R 0
M 120
ad 84,72
W 0
M 121
ST 18,1664
EF 94
M 132
EF 118
EF 142
M 133
CV 34,70,94
)"
R"(CV 35,118,142
M 122
W 1
EF 71
M 123
ST 46,2432
EF 95
M 134
EF 119
EF 143
M 135
CV 22,71,95
CV 23,119,143
M 124
W 2
EF 160
M 125
ae 42,72,384
EF 184
M 136
s_bitcmp1_b32 s50, 13
s_cselect_b32 s52, 24576, 0
v_add_u32 v53, s52, v3
M 137
T 40,6
M 126
mw 75
T 44,20
M 127
ad 84,75
W 0
M 138
ST 48,128
EF 208
M 139
T 48,7
M 128
T 52,21
M 129
W 1
EF 232
M 140
ST 44,896
CV 24,160,184
M 141
T 56,8
M 130
T 60,22
M 131
WL 8
R 2
M 142
W 2
ST 34,1664
M 143
T 64,9
M 4
T 68,23
M 5
WL 8
R 4
M 6
ST 22,2432
CV 25,208,232
M 7
WL 8
EF 161
EF 185
M 8
EF 209
EF 233
M 9
WL 2
CV 14,161,185
CV 15,209,233
M 10
EF 162
EF 186
M 11
R 6
M 16
EF 210
EF 234
M 17
CV 16,162,186
CV 17,210,234
M 18
EF 163
EF 187
M 19
EF 211
EF 235
M 20
CV 20,163,187
CV 21,211,235
M 21
ae 42,74,384
EF 164
M 22
s_bitcmp1_b32 s50, 12
s_cselect_b32 s52, 24576, 0
v_add_u32 v53, s52, v3
M 23
WL 2
R 8
M 28
ST 24,256
EF 188
M 29
EF 212
EF 236
M 30
CV 26,164,188
CV 27,212,236
M 31
EF 165
EF 189
M 32
ST 14,1024
EF 213
M 33
EF 237
CV 42,165,189
M 34
CV 43,213,237
EF 166
M 35
WL 2
R 10
M 40
ST 16,1792
EF 190
M 41
EF 214
EF 238
M 42
CV 38,166,190
CV 39,214,238
M 43
EF 167
EF 191
M 44
WV 31
ST 20,2560
EF 215
M 45
WL 0
EF 239
CV 36,167,191
M 46
s_barrier
CV 37,215,239
M 47
R 24
M 52
ae 42,72,384
AR 6,80
M 53
s_bitcmp1_b32 s50, 13
s_cselect_b32 s52, 24576, 0
v_add_u32 v53, s52, v3
M 54
ST 26,256
AR 7,104
M 55
AR 8,128
AR 9,152
M 56
EF 6
EF 7
M 57
EF 8
EF 9
M 58
ST 42,1024
CV 28,6,7
M 59
R 26
M 64
CV 29,8,9
AR 6,81
M 65
AR 7,105
AR 8,129
M 66
ST 38,1792
AR 9,153
M 67
EF 6
EF 7
M 68
EF 8
EF 9
M 69
CV 30,6,7
CV 31,8,9
M 70
ST 36,2560
AR 6,82
M 71
WL 2
R 28
M 76
AR 7,106
AR 8,130
M 77
AR 9,154
EF 6
M 78
EF 7
EF 8
M 79
EF 9
CV 32,6,7
M 80
CV 33,8,9
AR 6,83
M 81
AR 7,107
AR 8,131
M 82
AR 9,155
EF 6
M 83
WL 2
R 30
M 88
EF 7
EF 8
M 89
EF 9
CV 40,6,7
M 90
CV 41,8,9
M 91
ae 42,72,384
AR 6,84
M 92
s_bfe_u32 s52, s50, 0x1000e
v_mad_i32_i24 v53, v51, s52, v50
ST 28,0
M 93
AR 7,108
AR 8,132
M 94
AR 9,156
EF 6
M 95
WL 2
R 32
M 100
ST 30,768
EF 7
M 101
EF 8
EF 9
M 102
CV 18,6,7
CV 19,8,9
M 103
AR 6,85
AR 7,109
M 104
ST 32,1536
AR 8,133
M 105
AR 9,157
EF 6
M 106
EF 7
EF 8
M 107
WL 2
R 34
M 112
ST 40,2304
EF 9
M 113
CV 46,6,7
CV 47,8,9
M 114
AR 6,86
AR 7,110
M 115
AR 8,134
AR 9,158
M 116
WV 33
EF 6
EF 7
M 117
WL 0
EF 8
EF 9
M 118
s_barrier
CV 48,6,7
CV 49,8,9
M 119
R 48
M 124
AR 6,87
AR 7,111
M 125
AR 8,135
AR 9,159
M 136
EF 6
EF 7
M 137
EF 8
EF 9
M 126
CV 44,6,7
CV 45,8,9
M 127
ae 42,74,384
EF 72
M 138
s_bfe_u32 s52, s50, 0x1000f
v_mad_i32_i24 v53, v51, s52, v50
ST 18,0
M 139
EF 96
EF 120
M 128
EF 144
CV 34,72,96
M 129
CV 35,120,144
EF 73
M 140
ST 46,768
EF 97
M 141
T 56,10
M 130
T 60,24
M 131
R 50
M 142
ST 48,1536
EF 121
M 143
T 64,11
WL 4
M 8
T 68,25
M 9
R 52
WL 2
M 10
ST 44,2304
EF 145
M 11
R 54
M 20
CV 22,73,97
CV 23,121,145
M 21
EF 74
EF 98
M 22
EF 122
EF 146
M 23
WL 2
R 56
M 32
CV 24,74,98
CV 25,122,146
M 33
EF 75
EF 99
M 34
EF 123
EF 147
M 35
WL 2
R 58
M 44
CV 14,75,99
CV 15,123,147
M 45
WV 18
EF 76
EF 100
M 46
WL 0
s_barrier
EF 124
EF 148
M 47
R 72
M 56
ae 42,72,384
CV 16,76,100
M 57
s_bfe_u32 s52, s50, 0x1000e
v_mad_i32_i24 v53, v51, s52, v50
)"
R"(ST 34,128
M 58
CV 17,124,148
EF 77
M 59
R 74
M 68
EF 101
EF 125
M 69
ST 22,896
EF 149
M 70
CV 20,77,101
CV 21,125,149
M 71
WL 2
R 76
M 80
EF 78
EF 102
M 81
ST 24,1664
EF 126
M 82
EF 150
CV 26,78,102
M 83
WL 2
R 78
M 92
CV 27,126,150
EF 79
M 93
ST 14,2432
EF 103
M 94
EF 127
EF 151
M 95
WL 2
R 80
M 104
CV 42,79,103
CV 43,127,151
M 105
ae 42,74,384
EF 168
EF 192
M 106
s_bfe_u32 s52, s50, 0x1000f
v_mad_i32_i24 v53, v51, s52, v50
ST 16,128
EF 216
M 107
WL 2
R 82
EF 240
M 116
CV 38,168,192
CV 39,216,240
EF 169
M 117
EF 193
EF 217
EF 241
M 118
ST 20,896
CV 36,169,193
CV 37,217,241
M 119
WL 0
EF 170
EF 194
EF 218
M 128
EF 242
CV 28,170,194
CV 29,218,242
M 129
EF 171
EF 195
EF 219
M 140
ST 26,1664
EF 243
CV 30,171,195
M 141
CV 31,219,243
M 130
EF 172
EF 196
EF 220
EF 244
CV 32,172,196
CV 33,220,244
M 131
EF 173
EF 197
EF 221
EF 245
CV 40,173,197
CV 41,221,245
M 142
ST 42,2432
EF 174
EF 198
EF 222
EF 246
CV 18,174,198
M 143
ae 42,72,384
s_bfe_u32 s52, s50, 0x1000e
v_mad_i32_i24 v53, v51, s52, v50
ST 38,256
ST 36,1024
ST 28,1792
ST 30,2560
CV 19,222,246
EF 175
EF 199
EF 223
EF 247
CV 46,175,199
CV 47,223,247
ae 42,74,384
s_bfe_u32 s52, s50, 0x1000f
v_mad_i32_i24 v53, v51, s52, v50
ST 32,256
ST 40,1024
ST 18,1792
ST 46,2560
AR 6,88
AR 7,112
AR 8,136
EF 6
EF 7
EF 8
EF 56
CV 48,6,7
CV 49,8,56
AR 6,89
AR 7,113
AR 8,137
EF 6
EF 7
EF 8
EF 57
CV 44,6,7
CV 45,8,57
AR 6,90
AR 7,114
AR 8,138
EF 6
EF 7
EF 8
EF 58
CV 34,6,7
CV 35,8,58
AR 6,91
AR 7,115
AR 8,139
EF 6
EF 7
EF 8
EF 59
CV 22,6,7
CV 23,8,59
ae 42,72,432
s_bitcmp1_b32 s50, 16
s_cselect_b32 s52, 24576, 0
v_add_u32 v53, s52, v3
ST 48,0
ST 44,768
ST 34,1536
ST 22,2304
AR 6,92
AR 7,116
AR 8,140
EF 6
EF 7
EF 8
EF 60
CV 24,6,7
CV 25,8,60
AR 6,93
AR 7,117
AR 8,141
EF 6
EF 7
EF 8
EF 61
CV 14,6,7
CV 15,8,61
AR 6,94
AR 7,118
AR 8,142
EF 6
EF 7
EF 8
EF 62
CV 16,6,7
CV 17,8,62
AR 6,95
AR 7,119
AR 8,143
EF 6
EF 7
EF 8
EF 63
CV 20,6,7
CV 21,8,63
ae 42,72,432
s_bfe_u32 s52, s50, 0x10011
v_mad_i32_i24 v53, v51, s52, v50
ST 24,0
ST 14,768
ST 16,1536
ST 20,2304
EF 80
EF 104
EF 128
EF 152
CV 26,80,104
CV 27,128,152
EF 81
EF 105
EF 129
EF 153
CV 42,81,105
CV 43,129,153
EF 82
EF 106
EF 130
EF 154
CV 38,82,106
CV 39,130,154
EF 83
EF 107
EF 131
EF 155
CV 36,83,107
CV 37,131,155
ae 42,72,432
s_bitcmp1_b32 s50, 16
s_cselect_b32 s52, 24576, 0
v_add_u32 v53, s52, v3
ST 26,128
ST 42,896
ST 38,1664
ST 36,2432
EF 84
EF 108
EF 132
EF 156
CV 28,84,108
CV 29,132,156
EF 85
EF 109
EF 133
EF 157
CV 30,85,109
CV 31,133,157
EF 86
EF 110
EF 134
EF 158
CV 32,86,110
CV 33,134,158
EF 87
EF 111
EF 135
EF 159
CV 40,87,111
CV 41,135,159
ae 42,72,432
s_bfe_u32 s52, s50, 0x10011
v_mad_i32_i24 v53, v51, s52, v50
ST 28,128
ST 30,896
ST 32,1664
ST 40,2432
EF 176
EF 200
EF 224
EF 248
CV 18,176,200
CV 19,224,248
EF 177
EF 201
EF 225
EF 249
CV 46,177,201
CV 47,225,249
EF 178
EF 202
EF 226
EF 250
CV 48,178,202
CV 49,226,250
EF 179
EF 203
EF 227
EF 251
CV 44,179,203
CV 45,227,251
ae 42,72,432
s_bitcmp1_b32 s50, 16
s_cselect_b32 s52, 24576, 0
v_add_u32 v53, s52, v3
ST 18,256
ST 46,1024
ST 48,1792
ST 44,2560
EF 180
EF 204
EF 228
EF 252
CV 34,180,204
CV 35,228,252
EF 181
EF 205
EF 229
EF 253
CV 22,181,205
CV 23,229,253
EF 182
EF 206
EF 230
EF 254
CV 24,182,206
CV 25,230,254
EF 183
EF 207
EF 231
EF 255
CV 14,183,207
CV 15,231,255
ae 42,72,432
s_bfe_u32 s52, s50, 0x10011
v_mad_i32_i24 v53, v51, s52, v50
ST 34,256
ST 22,1024
ST 24,1792
ST 14,2560
s_waitcnt vmcnt(0) lgkmcnt(0)
.purgem MA
.purgem MV
.purgem AN
.purgem AW
.purgem AR
.purgem UL
.purgem UH
.purgem AD
.purgem TR
.purgem TQ
.purgem RA
.purgem RC
.purgem BV
.purgem BA
.purgem PA
.purgem DF
.purgem DW
.purgem DH
.purgem DS
.purgem XL
.purgem ST
.purgem SU
.purgem SA
.purgem CV
.purgem DR
.purgem TY
.purgem TZ
.purgem RS
.purgem PM
.purgem FS
.purgem FL
.purgem FB
.purgem FG
.purgem FX
.purgem PB
.purgem PQ
.purgem PJ
.purgem WL
.purgem WV
.purgem EF
.purgem M
.purgem R1
.purgem R
.purgem T
.purgem mw
.purgem mh
.purgem ad
.purgem ae
.purgem bc
.purgem W
.purgem H
)"
      : [flg] "=&s"(flg) : K2_OPS : K2_CLOB);
  if (flg) {
    const unsigned lane = __builtin_amdgcn_mbcnt_hi(~0u, __builtin_amdgcn_mbcnt_lo(~0u, 0u));
    const bool hbf = __ballot(lane < 26 && lane != 1 && __hip_atomic_load((const unsigned*)(g_buf + G_FLAG) + 96 + min(lane, 25u), __ATOMIC_RELAXED, __HIP_MEMORY_SCOPE_AGENT) != 0) != 0;
    const u16* const volatile __attribute__((address_space(4)))* ka = (const u16* const volatile __attribute__((address_space(4)))*)__builtin_amdgcn_kernarg_segment_ptr();
    if (hbf) {
      const u16* w1p = ka[6];
#pragma unroll 1
      for (unsigned f = blockIdx.x * 4 + w; f < 1152; f += 1024) {
        const u16* p = w1p + (32 * (f % 12) + 8 * (lane >> 4)) * HID + 16 * (f / 12) + (lane & 15);
        u32x4 v;
#pragma unroll
        for (int j = 0; j < 4; ++j) v[j] = p[2 * j * HID] | (unsigned)p[(2 * j + 1) * HID] << 16;
        *(u32x4*)(g_buf + f * 1024 + lane * 16) = v;
      }
    }
    slow_bar((unsigned*)(g_buf + G_BAR), w);
    if (hbf) {
      slow_fc1(ka[0], ka[5], (char*)ka[1], (float*)smem + w * HID, w);
      slow_bar((unsigned*)(g_buf + G_BAR + 64), w);
    }
    slow_fc2(ka[0], (const char*)ka[1], (u16*)ka[2], ka[3], ka[4], smem, hbf, w);
  }
  TS(15);
  MK_END(4);
}

extern "C" void kernel_launch(void* const* d_in, const int* in_sizes, int n_in,
                              void* d_out, int out_size, void* d_ws, size_t ws_size,
                              hipStream_t stream) {
  const u16* x = (const u16*)d_in[0];
  const u16* dw = (const u16*)d_in[1];
  const u16* w1 = (const u16*)d_in[2];
  const u16* w2 = (const u16*)d_in[3];
  const u16* grn_g = (const u16*)d_in[4];
  const u16* grn_b = (const u16*)d_in[5];
  char* ws = (char*)d_ws;
  if (ws_size < H_BYTES) return;
  (void)hipFuncSetAttribute((const void*)k1_fc1, hipFuncAttributeMaxDynamicSharedMemorySize, K1_LDS);
  (void)hipFuncSetAttribute((const void*)k2_fc2, hipFuncAttributeMaxDynamicSharedMemorySize, K2_LDS);

  k0_prep<<<8 + K0_NB + NPAGE + NPAGX, 256, 0, stream>>>(w1, dw, ws, (const char*)x, grn_b, w2);
  k1_fc1<<<NWG + 8, 256, K1_LDS, stream>>>(x, ws, (const u16*)d_out, grn_g);
  k2_fc2<<<NWG, 256, K2_LDS, stream>>>(x, ws, (u16*)d_out, grn_g, w2, dw, w1);
}
```

```cpp
#include <hip/hip_runtime.h>
#include <hip/hip_bf16.h>
#include <stdint.h>
#include <stdio.h>

typedef short bf16x8 __attribute__((ext_vector_type(8)));
typedef short s16x4 __attribute__((ext_vector_type(4)));
typedef short v4i16_t __attribute__((ext_vector_type(4)));
typedef float f32x16 __attribute__((ext_vector_type(16)));
typedef float f32x4 __attribute__((ext_vector_type(4)));
typedef float f32x2 __attribute__((ext_vector_type(2)));
typedef unsigned u32x4 __attribute__((ext_vector_type(4)));
typedef __bf16 bf16x2_t __attribute__((ext_vector_type(2)));
typedef unsigned short u16;

#define SW1 0x1p-12f
#define SZS 0x1p-5f
#define SG 0x1p-16f
#define SH 0x1p-15f
#define SW2 0x1p-12f
#define DI __device__ __forceinline__
#define LAS __attribute__((address_space(3)))

constexpr int NPIX = 147456;
constexpr int CH = 384;
constexpr int HID = 1536;
constexpr int NTILE = NPIX / 32;
constexpr int NWG = 256;
constexpr int TPW = NTILE / NWG;

constexpr size_t H_BYTES = (size_t)NPIX * HID * 2;
constexpr unsigned K1_LDS = 163840, K1_PF_OFF = 130048, K1_SMAX = 10, K1_NREP = 4, K1_G_TAB = 1228800, K1_G_CNT = 1239040, K1_G_LOC = 1236992, K1_G_DUM = 1255424,
  K1_G_ACC = 1320960, K1_G_LUT = 1382400, K1_G_BYTES = 1398784;
constexpr bool K1_LUT = true;
constexpr float K1_ZS = 16.0f;
constexpr unsigned K1_GST = 25600, K1_WCH = 6400, K1_ZC = 1365;
constexpr bool K1_F2 = true;
constexpr unsigned K2_SHT = 37443;
constexpr int K2_LDS = 163840, K2_HB = 98304, K2_NU = 14, K2_BIAS_OFF = 155648, K2_PF_OFF = 161792;
#define K2_ROLES 0
constexpr int K2_TS = 1, K2_S16_OFF = 109568;

__device__ __attribute__((aligned(256))) char g_buf[K1_G_BYTES];
__device__ __attribute__((aligned(256))) float g_bias[2048];
__device__ __attribute__((aligned(256))) char g_w2r[96 * 12 * 1024];
__device__ __attribute__((aligned(256))) char g_w2f[96 * 12 * 1024];
__device__ __attribute__((aligned(256))) char g_w2q[16 * 12 * 1024];
__device__ __attribute__((aligned(256))) float g_sc[64];
constexpr unsigned G_FLAG = K1_G_CNT + 14336;
constexpr unsigned G_PUB = K1_G_DUM;
__device__ __attribute__((aligned(256))) unsigned g_prm[1024 * 64];
constexpr unsigned G_STMP = K1_G_CNT + 12288;
constexpr unsigned G_BAR = K1_G_CNT + 13312;
constexpr unsigned H_TS = 49152, H_RS = 48 * H_TS, NPAGE = 217, NPAGX = 55;
#define TS(i) do { } while (0)
#define KST(i) do { } while (0)
#define MK_START(i) do { } while (0)
#define MK_END(i) do { } while (0)

DI unsigned pack_bf16(float a, float b) {
  f32x2 v = {a, b};
  bf16x2_t r = __builtin_convertvector(v, bf16x2_t);
  return __builtin_bit_cast(unsigned, r);
}
typedef _Float16 f16x2_t __attribute__((ext_vector_type(2)));
DI unsigned pack_f16(float a, float b) {
  f32x2 v = {a, b};
  f16x2_t r = __builtin_convertvector(v, f16x2_t);
  return __builtin_bit_cast(unsigned, r);
}
DI float bf_lo(unsigned u) { return __uint_as_float(u << 16); }
DI float bf_hi(unsigned u) { return __uint_as_float(u & 0xffff0000u); }
DI float round_bf16(float a) { return bf_lo(pack_bf16(a, 0.f)); }
DI float dot2acc(unsigned a, unsigned b, float c) {
  return __builtin_amdgcn_fdot2_f32_bf16(__builtin_bit_cast(bf16x2_t, a), __builtin_bit_cast(bf16x2_t, b), c, false);
}
DI float ld_bf16(const u16* p) { return __uint_as_float((unsigned)(*p) << 16); }

DI s16x4 tr_read(const char* p) {
  return __builtin_bit_cast(s16x4, __builtin_amdgcn_ds_read_tr16_b64_v4i16((LAS v4i16_t*)p));
}

#define MFMA32(a, b, c) __builtin_amdgcn_mfma_f32_32x32x16_bf16((a), (b), (c), 0, 0, 0)
#define BAR() __builtin_amdgcn_s_barrier()

DI void dma16(unsigned voff, const char* sbase, unsigned lds_dst) {
  unsigned keep;
  asm volatile("s_mov_b32 %0, m0\n\ts_mov_b32 m0, %3\n\ts_nop 0\n\tglobal_load_lds_dwordx4 %1, %2\n\ts_mov_b32 m0, %0"
               : "=&s"(keep) : "v"(voff), "s"(sbase), "s"(lds_dst) : "memory");
}
DI unsigned lds_addr(const char* p) { return (unsigned)(size_t)(const LAS char*)p; }

DI float gelu_tanh(float x) {
  const float c0 = -2.0f * 0.7978845608028654f * 1.4426950408889634f;
  const float c1 = c0 * 0.044715f;
  float x2 = x * x;
  float t = __builtin_fmaf(x2, c1, c0);
  float e = __builtin_amdgcn_exp2f(x * t);
  float r = __builtin_amdgcn_rcpf(1.0f + e);
  return x * r;
}

DI void gload_b128_asm(u32x4& dst, const char* sbase, unsigned voff) {
  asm volatile("global_load_dwordx4 %0, %1, %2" : "=v"(dst) : "v"(voff), "s"(sbase) : "memory");
}
DI void lds_rd128(u32x4& dst, unsigned addr, int off) {
  asm volatile("ds_read_b128 %0, %1 offset:%2" : "=v"(dst) : "v"(addr), "i"(off));
}
DI void lds_rd128_far(u32x4& dst, unsigned voff, unsigned sbase, int imm) {
  unsigned t;
  asm volatile("v_add_u32 %1, %3, %2\n\tv_add_u32 %1, %4, %1\n\tds_read_b128 %0, %1" : "=v"(dst), "=&v"(t) : "v"(voff), "s"(sbase), "i"(imm));
}
DI void gload16i(u32x4& dst, const char* sbase, unsigned voff, int imm) {
  unsigned t;
  asm volatile("v_add_u32 %1, %4, %2\n\tglobal_load_dwordx4 %0, %1, %3" : "=v"(dst), "=&v"(t) : "v"(voff), "s"(sbase), "i"(imm) : "memory");
}
DI void dma16i(const char* sbase, unsigned voff, int immv, unsigned lbase, int imml) {
  unsigned t, keep;
  asm volatile("s_mov_b32 %1, m0\n\ts_add_u32 m0, %4, %6\n\tv_add_u32 %0, %5, %2\n\tglobal_load_lds_dwordx4 %0, %3\n\ts_mov_b32 m0, %1"
               : "=&v"(t), "=&s"(keep) : "v"(voff), "s"(sbase), "s"(lbase), "i"(immv), "i"(imml) : "memory", "scc");
}

DI void st16_wt(void* p, u32x4 v) { asm volatile("global_store_dwordx4 %0, %1, off\n\ts_nop 1" :: "v"(p), "v"(v) : "memory"); }
template <int I> struct IC { static constexpr int value = I; };
template <int N, int I = 0, typename F> DI void static_for(F&& f) {
  if constexpr (I < N) { f(IC<I>{}); static_for<N, I + 1>(f); }
}
template <int C, int M> DI float dpp_add(float v) { return v + __int_as_float(__builtin_amdgcn_update_dpp(0, __float_as_int(v), C, M, 0xf, false)); }
DI float wave_sum63(float v) {
  v = dpp_add<0x111, 0xf>(v); v = dpp_add<0x112, 0xf>(v); v = dpp_add<0x114, 0xf>(v); v = dpp_add<0x118, 0xf>(v);
  v = dpp_add<0x142, 0xa>(v); return dpp_add<0x143, 0xc>(v);
}
DI uint64_t uni64(uint64_t v) { return (uint64_t)__builtin_amdgcn_readfirstlane(v >> 32) << 32 | (unsigned)__builtin_amdgcn_readfirstlane(v); }
DI unsigned wg_side(unsigned b) { return (((const unsigned*)(g_buf + K1_G_LOC))[b & 3] ^ (b >> 2)) & 1; }
DI unsigned h_flip(const char* ws, unsigned region, unsigned t) { return __popc(((const unsigned*)(g_buf + K1_G_LOC + 64))[(((unsigned)(size_t)ws & 0x1fffffu) + region * H_RS + t * H_TS) >> 21]) > 2; }
DI unsigned x_flip(const u16* x, unsigned region) { return __popc(((const unsigned*)(g_buf + K1_G_LOC + 1024))[(((unsigned)(size_t)x & 0x1fffffu) + (12 * region + 6) * 49152u) >> 21]) > 2; }
DI unsigned sub_pixel(const u16* x, unsigned sub, unsigned m) {
  const unsigned blk = sub / 24, u = sub % 24;
  const unsigned region = blk < 256 ? (blk >> 3) * 4 + (blk & 3) : 128 + ((blk - 256) >> 1), side = blk < 256 ? wg_side(blk) : blk & 1;
  return (12 * region + (u >> 1)) * 64 + ((0xda43cb52fe987610ull >> (32 * (u & 1) + 16 * (side ^ x_flip(x, region)) + (m & 12))) & 15) * 4 + (m & 3);
}
DI unsigned h_piece(const char* ws, unsigned sub, unsigned t) {
  const unsigned blk = sub / 24, u = sub % 24;
  const unsigned region = blk < 256 ? (blk >> 3) * 4 + (blk & 3) : 128 + ((blk - 256) >> 1), side = blk < 256 ? wg_side(blk) : blk & 1;
  return region * H_RS + t * H_TS + (u >> 3) * 16384 + (u & 7) * 1024 + ((side ^ h_flip(ws, region, t)) << 13);
}
constexpr unsigned K0_NB = 145 + 24 + 96 + 16 + 24;
constexpr float U24 = 0x1p-24f;
struct Sc { float w1, zs, g, h, w2; };
DI Sc scales() { return Sc{SW1, SZS, SG, SH, SW2}; }
__global__ __launch_bounds__(256) void k0_prep(const u16* __restrict__ w1, const u16* __restrict__ dw, const char* __restrict__ ws, const char* __restrict__ x,
                                                const u16* __restrict__ grn_b, const u16* __restrict__ w2) {
  MK_START(0);
  uint64_t pc; int d1;
  asm volatile("s_getpc_b64 %0\n1:\n\ts_mov_b32 %1, k1_code-1b" : "=s"(pc), "=s"(d1));
  if (threadIdx.x >= 64) pc += (int64_t)d1 + (blockIdx.x >> 3) * 8192;
  unsigned pfd = 0;
  if (threadIdx.x < 64 || (1 && blockIdx.x < 48 && (threadIdx.x >> 6) == 1)) pfd = *(const __attribute__((address_space(1))) unsigned*)((pc & ~127ull) + (threadIdx.x & 63) * 128);
#define K0_END() do { asm volatile("" :: "v"(pfd)); MK_END(0); } while (0)
  unsigned xcc;
  asm volatile("s_getreg_b32 %0, hwreg(HW_REG_XCC_ID)" : "=s"(xcc));
  xcc = (xcc >> 2) & 1;
  if (blockIdx.x < 4 && threadIdx.x == 0) ((unsigned*)(g_buf + K1_G_LOC))[blockIdx.x] = 7 * xcc;
  constexpr unsigned NP = NPAGE + NPAGX;
  if (blockIdx.x < 8) {
    __shared__ unsigned cv[4];
    const unsigned w = threadIdx.x >> 6;
    if ((threadIdx.x & 63) == 0) {
      const char* a = x + (1536 + 24 * (4 * blockIdx.x + w)) * 49152u + 20480, *b = a;
      unsigned long long t0, t1;
      int n = 2;
      asm volatile("" : "+s"(n));
#pragma unroll 1
      do {
        unsigned v;
        asm volatile("s_memrealtime %0\n\ts_waitcnt lgkmcnt(0)\n\tglobal_load_dword %2, %3, off sc1\n\ts_waitcnt vmcnt(0)\n\tglobal_load_dword %2, %4, off sc1\n\ts_waitcnt vmcnt(0)\n\ts_memrealtime %1\n\ts_waitcnt lgkmcnt(0)"
                     : "=&s"(t0), "=&s"(t1), "=&v"(v) : "v"(a), "v"(b) : "memory");
        a += 5120; b = a + 8192;
      } while (--n);
      cv[w] = (unsigned)(t1 - t0) > 72;
    }
    __syncthreads();
    if (threadIdx.x == 0) g_buf[K1_G_CNT + 16320 + blockIdx.x] = cv[0] | cv[1] << 1 | cv[2] << 2 | cv[3] << 3;
    K0_END();
    return;
  }
  const unsigned bi = blockIdx.x - 8;
  const unsigned bx = bi < NP ? bi + K0_NB : bi - NP;
  if (bx >= K0_NB) {
    __shared__ unsigned lat[8];
    const unsigned pg = bx - K0_NB, w = threadIdx.x >> 6, lane = threadIdx.x & 63;
    auto gran = [&](unsigned p) {
      const bool h = p < NPAGE;
      const char* buf = h ? ws : x;
      const int o0 = (int)((h ? p : p - NPAGE) << 21) - (int)((unsigned)(size_t)buf & 0x1fffffu);
      const unsigned al = h ? 49152 : 12 * 49152;
      const unsigned q = ((((unsigned)max(o0, 0) + al - 1) >> (h ? 14 : 16)) * (h ? 87382u : 29128u)) >> 18;
      return buf + min(q * al, (unsigned)(h ? H_BYTES : H_BYTES / 4) - 16384u);
    };
    if (lane < 5) {
      const char* a = gran(lane < 4 ? min(pg + NP - 1 - (lane >> 1), pg - 1 - (lane >> 1)) : pg) + (lane & 1) * 8192 + w * 2048 + (lane < 4 ? 1024 : 0);
      const char* b = a;
      const char* own = (const char*)uni64(__builtin_amdgcn_readlane((uint64_t)a >> 32, 4) * 4294967296ull + (unsigned)__builtin_amdgcn_readlane((unsigned)(uint64_t)a, 4));
      unsigned long long t0, t1, t2;
      int n = 2;
      asm volatile("" : "+s"(n));
#pragma unroll 1
      do {
        unsigned v;
        asm volatile("s_cmp_eq_u32 %6, 2\n\ts_cbranch_scc1 1f\n\ts_sleep 4\n1:\n\ts_memtime %0\n\ts_waitcnt lgkmcnt(0)\n\tglobal_load_dword %3, %4, off sc1\n\ts_waitcnt vmcnt(0)\n\ts_memtime %1\n\ts_waitcnt lgkmcnt(0)\n\t"
                     "global_load_dword %3, %5, off sc1\n\ts_waitcnt vmcnt(0)\n\ts_memtime %2\n\ts_waitcnt lgkmcnt(0)" : "=&s"(t0), "=&s"(t1), "=&s"(t2), "=&v"(v) : "v"(a), "v"(b), "s"(n) : "memory", "scc");
        a = own + 1024 + (w & 1) * 8192; b = a + 8192 - (w & 1) * 16384;
      } while (--n);
      lat[w + 4 * (w & 1)] = (unsigned)(t1 - t0); lat[w + 4 - 4 * (w & 1)] = (unsigned)(t2 - t1);
    }
    __syncthreads();
    if (threadIdx.x == 0) {
      auto fastest = [&](const unsigned* l) { unsigned m = 9999; for (int i = 0; i < 4; ++i) m = l[i] < 300 ? m : min(m, l[i]); return (int)m; };
      const int df = fastest(lat) - fastest(lat + 4);
      const unsigned v = xcc ^ (df > 0), sure = abs(df) > 80;
      char* e = g_buf + K1_G_LOC + 64 + 4 * (pg < NPAGE ? pg : pg + 240 - NPAGE);
      *(u16*)(e + 2) = v ? 3 + 4 * sure : 1 - sure;
      if (pg == 0 || pg == NPAGE) e[0] = v; else e[-3] = v ^ 1;
      if (pg == NPAGE - 1 || pg == NP - 1) e[1] = v; else e[4] = v ^ 1;
    }
    K0_END();
    return;
  }
  if (bx >= 145 + 24 + 96 + 16) {
    __shared__ __attribute__((aligned(16))) float red[2][16][64];
    const Sc sc = scales();
    const int c0 = 64 * (bx - 281), c8 = (threadIdx.x & 7) * 8;
    float a[8], q[8];
#pragma unroll
    for (int i = 0; i < 8; ++i) a[i] = q[i] = 0.f;
#pragma unroll
    for (int k = 0; k < 12; ++k) {
      const u32x4 v = *(const u32x4*)(w1 + (size_t)(32 * k + (threadIdx.x >> 3)) * HID + c0 + c8);
#pragma unroll
      for (int i = 0; i < 8; ++i) {
        const float f = (float)(_Float16)(sc.w1 * (i & 1 ? bf_hi(v[i >> 1]) : bf_lo(v[i >> 1]))) * 0x1p24f;
        a[i] += f; q[i] = __builtin_fmaf(f, f, q[i]);
      }
    }
#pragma unroll
    for (int i = 0; i < 8; ++i) { a[i] = dpp_add<0x118, 0xf>(a[i]); q[i] = dpp_add<0x118, 0xf>(q[i]); }
    if (threadIdx.x & 8) {
#pragma unroll
      for (int i = 0; i < 2; ++i) {
        *(f32x4*)&red[0][threadIdx.x >> 4][c8 + 4 * i] = (f32x4){a[4 * i], a[4 * i + 1], a[4 * i + 2], a[4 * i + 3]};
        *(f32x4*)&red[1][threadIdx.x >> 4][c8 + 4 * i] = (f32x4){q[4 * i], q[4 * i + 1], q[4 * i + 2], q[4 * i + 3]};
      }
    }
    __syncthreads();
    if (threadIdx.x < 64) {
      const int n = c0 + threadIdx.x;
      float s = 0.f, s2 = 0.f;
#pragma unroll
      for (int i = 0; i < 16; ++i) { s += red[0][i][threadIdx.x]; s2 += red[1][i][threadIdx.x]; }
      const float c = (0.f - (float)K1_ZC * s) * 0x1p-48f;
      char* p = g_buf + (n >> 5) * K1_GST + 5120;
      *(f32x4*)(p + 272 * ((n >> 4) & 1) + (n & 15) * 16) = (f32x4){c, c, c, c};
      if ((n & 31) == 0) *(u32x4*)(p + 256) = (u32x4){0u, 0u, 0u, 0u};
      const bool bad = !(s2 >= 384.f * 64 * 64 && s2 <= 384.f * 1024 * 1024);
      const bool any = __ballot(bad) != 0;
      if (threadIdx.x == 0) ((unsigned*)(g_buf + G_FLAG))[98 + bx - 281] = any;
    }
    K0_END();
    return;
  }
  if (bx >= 145 + 24 + 96) {
    const Sc sc = scales();
    const unsigned e = (bx - 265) * 256 + threadIdx.x;
    const float u = 1.0f / (sc.g * sc.w1);
    float r[2];
#pragma unroll
    for (int i = 0; i < 2; ++i) {
      const unsigned c = 2 * e + i;
      r[i] = sc.h * gelu_tanh(u * __uint_as_float((c >> 12) << 31 | ((c & 4095) + 79 * 128) << 16 | 0x8000u));
    }
    *(unsigned*)(g_buf + K1_G_LUT + 4 * e) = pack_f16(r[0], r[1]);
    K0_END();
    return;
  }
  if (bx >= 145 + 24) {
    __shared__ __attribute__((aligned(16))) u16 tw[32][200];
    const int kappa = (bx - 169) >> 1, nh = (bx - 169) & 1;
#pragma unroll
    for (int i = 0; i < 3; ++i) {
      const int e = threadIdx.x + 256 * i, r = e / 24, c = (e % 24) * 8;
      *(u32x4*)&tw[r][c] = *(const u32x4*)(w2 + (size_t)(32 * kappa + r) * CH + 192 * nh + c);
    }
    __syncthreads();
    const int lane = threadIdx.x & 63, g = lane >> 4;
    const float s2 = 256.f * scales().w2;
    bool bad = false;
#pragma unroll
    for (int i = 0; i < 3; ++i) {
      const int fi = (threadIdx.x >> 6) + 4 * i, n = 64 * (fi >> 2) + 4 * (lane & 15) + (fi & 3);
      float wv[8];
      unsigned bits = 0;
#pragma unroll
      for (int j = 0; j < 8; ++j) {
        wv[j] = ld_bf16(&tw[16 * ((j >> 2) ^ (g & K2_TS)) + 4 * g + (j & 3)][n]);
        bits |= wv[j] != 0.f ? 256u << j : 0u;
        wv[j] *= s2;
        bits |= fabsf(wv[j]) >= 48 * 256 * U24 ? 1u << j : 0u;
        bad |= !(fabsf(wv[j]) <= 500.f);
      }
      const unsigned o = (((kappa & ~1) + fi / 6) * 24 + nh * 12 + (fi % 6) * 2 + (kappa & 1)) * 1024 + lane * 16;
      st16_wt(g_w2r + o, (u32x4){pack_f16(wv[0], wv[1]), pack_f16(wv[2], wv[3]), pack_f16(wv[4], wv[5]), pack_f16(wv[6], wv[7])});
      constexpr float q = 1.0f / 256;
      if (kappa < 8) st16_wt(g_w2q + o, (u32x4){pack_f16(q * wv[0], q * wv[1]), pack_f16(q * wv[2], q * wv[3]), pack_f16(q * wv[4], q * wv[5]), pack_f16(q * wv[6], q * wv[7])});
      static_for<4>([&](auto ic) { bits |= __builtin_amdgcn_update_dpp(0u, bits, 0x120 + (1 << decltype(ic)::value), 0xf, 0xf, false); });
      bad |= ((bits >> 8) & ~bits & 255u) != 0;
    }
    const bool any = __ballot(bad) != 0;
    if (lane == 0) ((char*)(g_buf + G_FLAG))[4 * (bx - 169) + (threadIdx.x >> 6)] = any;
    K0_END();
    return;
  }
  if (bx >= 145) {
    __shared__ __attribute__((aligned(16))) float red[16][64];
    const int c0 = 64 * ((bx - 145) >> 2), rg = (bx - 145) & 3, c8 = (threadIdx.x & 7) * 8;
    float a[8];
#pragma unroll
    for (int i = 0; i < 8; ++i) a[i] = 0.f;
#pragma unroll
    for (int k = 0; k < 12; ++k) {
      const int ch = 384 * rg + 32 * k + (threadIdx.x >> 3);
      const float b = ld_bf16(grn_b + ch);
      const u32x4 v = *(const u32x4*)(w2 + (size_t)ch * CH + c0 + c8);
#pragma unroll
      for (int i = 0; i < 4; ++i) { a[2 * i] = __builtin_fmaf(b, bf_lo(v[i]), a[2 * i]); a[2 * i + 1] = __builtin_fmaf(b, bf_hi(v[i]), a[2 * i + 1]); }
    }
#pragma unroll
    for (int i = 0; i < 8; ++i) a[i] = dpp_add<0x118, 0xf>(a[i]);
    if (threadIdx.x & 8) {
      *(f32x4*)&red[threadIdx.x >> 4][c8] = (f32x4){a[0], a[1], a[2], a[3]};
      *(f32x4*)&red[threadIdx.x >> 4][c8 + 4] = (f32x4){a[4], a[5], a[6], a[7]};
    }
    __syncthreads();
    if (threadIdx.x < 64) {
      const int n = c0 + threadIdx.x, m = n % 192;
      float s = 0.f;
#pragma unroll
      for (int i = 0; i < 16; ++i) s += red[i][threadIdx.x];
      g_bias[(((n / 192) * 12 + 4 * (m >> 6) + (m & 3)) * 16 + ((m & 63) >> 2)) * 4 + rg] = s;
    }
    K0_END();
    return;
  }
  const unsigned bid = bx;
  if (bid == 0) {
    const Sc sc = scales();
#pragma unroll
    for (int i = 0; i < 2; ++i) {
      const int e = threadIdx.x + 256 * i;
      u32x4 v = {0u, 0u, 0u, 0u};
      if (e < 192) {
        const int ks = e >> 4, m = e & 15, q = m & 3;
        const unsigned lo = pack_bf16(sc.zs * ld_bf16(dw + 32 * ks + 8 * (m >> 2) + q), 0.f) << (16 * (q & 1));
        const unsigned hi = pack_bf16(sc.zs * ld_bf16(dw + 32 * ks + 8 * (m >> 2) + 4 + q), 0.f) << (16 * (q & 1));
        v = K1_F2 ? (u32x4){q < 2 ? lo : 0u, q < 2 ? 0u : lo, q < 2 ? hi : 0u, q < 2 ? 0u : hi} : (u32x4){q < 2 ? hi : 0u, q < 2 ? 0u : hi, q < 2 ? lo : 0u, q < 2 ? 0u : lo};
      } else if (e >= 384 && e < 432) {
        const u16* p = dw + 8 * (e - 384);
#pragma unroll
        for (int k = 0; k < 4; ++k) v[k] = pack_bf16(sc.zs * ld_bf16(p + 2 * k), sc.zs * ld_bf16(p + 2 * k + 1));
      } else if (e >= 432 && e < 436) {
        v = (u32x4){__float_as_uint(1e-5f * sc.zs * sc.zs), __float_as_uint(sc.g * 0x1p14f), __float_as_uint(sc.g), 0u};
      }
      *(u32x4*)(g_buf + K1_G_TAB + e * 16) = v;
    }
#pragma unroll
    for (int i = 0; i < 4; ++i) if (threadIdx.x + 256 * i < 896) *(u32x4*)(g_buf + K1_G_CNT + (threadIdx.x + 256 * i) * 16) = (u32x4){0u, 0u, 0u, 0u};
    if (threadIdx.x < 2) ((unsigned*)(g_buf + G_FLAG))[96 + threadIdx.x] = 0u;
    if (threadIdx.x == 0) *(f32x4*)g_sc = (f32x4){sc.h * sc.w2, 1.0f / (sc.h * sc.w2), 0x1p-28f / (sc.h * sc.h), 1.0f / sc.h};
    *(unsigned*)(g_buf + G_PUB + threadIdx.x * 256) = 0u;
    K0_END();
    return;
  }
  __shared__ __attribute__((aligned(16))) u16 tl[32][136];
  const int e = (bid - 1) * 256 + threadIdx.x, ks = (bid - 1) % 12, cb = (bid - 1) / 12;
#pragma unroll
  for (int i = 0; i < 2; ++i) {
    const int r = (threadIdx.x >> 4) + 16 * i, c = (threadIdx.x & 15) * 8;
    *(u32x4*)&tl[r][c] = *(const u32x4*)(w1 + (size_t)(32 * ks + r) * HID + 128 * cb + c);
  }
  const float s1 = scales().w1;
  if (e < (int)(K1_NREP + 1) * 768) *(u32x4*)(g_buf + K1_G_ACC + e * 16) = (u32x4){0u, 0u, 0u, 0u};
  __syncthreads();
#pragma unroll
  for (int i = 0; i < 2; ++i) {
    const int lane = threadIdx.x & 63, f = (threadIdx.x >> 6) + 4 * i, n = 16 * f + (lane & 15), k0 = 8 * (lane >> 4);
    u32x4 P;
#pragma unroll
    for (int jj = 0; jj < 4; ++jj) P[jj] = pack_f16(s1 * ld_bf16(&tl[k0 + 2 * jj][n]), s1 * ld_bf16(&tl[k0 + 2 * jj + 1][n]));
    const int fs = (f & 1) * 12 + ks;
    st16_wt(g_buf + (4 * cb + (f >> 1)) * K1_GST + (fs + (fs >= 5)) * 1024 + lane * 16, P);
  }
  K0_END();
}

constexpr unsigned G_ACC2 = K1_G_ACC + K1_NREP * 12288;

#define VA_CLOB \
  "v0", "v1", "v2", "v3", "v4", "v5", "v6", "v7", "v8", "v9", "v10", "v11", "v12", "v13", "v14", "v15", "v16", "v17", "v18", "v19", "v20", "v21", "v22", "v23", "v24", "v25", "v26", "v27", "v28", "v29", "v30", "v31", \
  "v32", "v33", "v34", "v35", "v36", "v37", "v38", "v39", "v40", "v41", "v42", "v43", "v44", "v45", "v46", "v47", "v48", "v49", "v50", "v51", "v52", "v53", "v54", "v55", "v56", "v57", "v58", "v59", "v60", "v61", "v62", "v63", \
  "v64", "v65", "v66", "v67", "v68", "v69", "v70", "v71", "v72", "v73", "v74", "v75", "v76", "v77", "v78", "v79", "v80", "v81", "v82", "v83", "v84", "v85", "v86", "v87", "v88", "v89", "v90", "v91", "v92", "v93", "v94", "v95", \
  "v96", "v97", "v98", "v99", "v100", "v101", "v102", "v103", "v104", "v105", "v106", "v107", "v108", "v109", "v110", "v111", "v112", "v113", "v114", "v115", "v116", "v117", "v118", "v119", "v120", "v121", "v122", "v123", "v124", "v125", "v126", "v127", \
  "v128", "v129", "v130", "v131", "v132", "v133", "v134", "v135", "v136", "v137", "v138", "v139", "v140", "v141", "v142", "v143", "v144", "v145", "v146", "v147", "v148", "v149", "v150", "v151", "v152", "v153", "v154", "v155", "v156", "v157", "v158", "v159", \
  "v160", "v161", "v162", "v163", "v164", "v165", "v166", "v167", "v168", "v169", "v170", "v171", "v172", "v173", "v174", "v175", "v176", "v177", "v178", "v179", "v180", "v181", "v182", "v183", "v184", "v185", "v186", "v187", "v188", "v189", "v190", "v191", \
  "v192", "v193", "v194", "v195", "v196", "v197", "v198", "v199", "v200", "v201", "v202", "v203", "v204", "v205", "v206", "v207", "v208", "v209", "v210", "v211", "v212", "v213", "v214", "v215", "v216", "v217", "v218", "v219", "v220", "v221", "v222", "v223", \
  "v224", "v225", "v226", "v227", "v228", "v229", "v230", "v231", "v232", "v233", "v234", "v235", "v236", "v237", "v238", "v239", "v240", "v241", "v242", "v243", "v244", "v245", "v246", "v247", "v248", "v249", "v250", "v251", "v252", "v253", "v254", "v255", \
  "a0", "a1", "a2", "a3", "a4", "a5", "a6", "a7", "a8", "a9", "a10", "a11", "a12", "a13", "a14", "a15", "a16", "a17", "a18", "a19", "a20", "a21", "a22", "a23", "a24", "a25", "a26", "a27", "a28", "a29", "a30", "a31", \
  "a32", "a33", "a34", "a35", "a36", "a37", "a38", "a39", "a40", "a41", "a42", "a43", "a44", "a45", "a46", "a47", "a48", "a49", "a50", "a51", "a52", "a53", "a54", "a55", "a56", "a57", "a58", "a59", "a60", "a61", "a62", "a63", \
  "a64", "a65", "a66", "a67", "a68", "a69", "a70", "a71", "a72", "a73", "a74", "a75", "a76", "a77", "a78", "a79", "a80", "a81", "a82", "a83", "a84", "a85", "a86", "a87", "a88", "a89", "a90", "a91", "a92", "a93", "a94", "a95", \
  "a96", "a97", "a98", "a99", "a100", "a101", "a102", "a103", "a104", "a105", "a106", "a107", "a108", "a109", "a110", "a111", "a112", "a113", "a114", "a115", "a116", "a117", "a118", "a119", "a120", "a121", "a122", "a123", "a124", "a125", "a126", "a127", \
  "a128", "a129", "a130", "a131", "a132", "a133", "a134", "a135", "a136", "a137", "a138", "a139", "a140", "a141", "a142", "a143", "a144", "a145", "a146", "a147", "a148", "a149", "a150", "a151", "a152", "a153", "a154", "a155", "a156", "a157", "a158", "a159", \
  "a160", "a161", "a162", "a163", "a164", "a165", "a166", "a167", "a168", "a169", "a170", "a171", "a172", "a173", "a174", "a175", "a176", "a177", "a178", "a179", "a180", "a181", "a182", "a183", "a184", "a185", "a186", "a187", "a188", "a189", "a190", "a191", \
  "a192", "a193", "a194", "a195", "a196", "a197", "a198", "a199", "a200", "a201", "a202", "a203", "a204", "a205", "a206", "a207", "a208", "a209", "a210", "a211", "a212", "a213", "a214", "a215", "a216", "a217", "a218", "a219", "a220", "a221", "a222", "a223", \
  "a224", "a225", "a226", "a227", "a228", "a229", "a230", "a231", "a232", "a233", "a234", "a235", "a236", "a237", "a238", "a239", "a240", "a241", "a242", "a243", "a244", "a245", "a246", "a247", "a248", "a249", "a250", "a251", "a252", "a253", "a254", "a255"
#define K1_CLOB "memory", "vcc", "scc", "s36", "s37", "s38", "s39", "s40", "s41", "s42", "s43", "s44", "s45", "s46", "s47", "s48", "s49", "s50", "s51", "s52", "s53", "s54", "s55", "s56", "s57", "s58", "s59", "s60", "s61", "s62", "s63", "s64", "s65", "s66", "s67", "s68", "s69", "s70", "s71", "s72", "s73", "s74", "s75", "s76", "s77", "s78", "s79", "s80", "s81", "s82", "s83", "s84", "s85", "s86", "s87", "s88", "s89", "s90", "s91", "s92", "s93", "s94", "s95", "s96", "s97", VA_CLOB
DI void k2_params(const u16* x, const char* hws, const u16* out, const u16* grn_g, unsigned tid) {
  const unsigned e = (tid >> 2) * 32 + blockIdx.x * 4 + (tid & 3);
  const unsigned b = e >> 2, w = e & 3, pg = w & 1, nh = w >> 1, q = (b >> 3) * 4 + (b & 3), rs = 128 + (q >> 1), hf = q & 1, side = wg_side(b);
  auto fmask = [&](unsigned region) {
    const unsigned o = ((unsigned)(size_t)hws & 0x1fffffu) + region * H_RS + nh * H_TS;
    const unsigned* p = (const unsigned*)(g_buf + K1_G_LOC + 64) + (o >> 21);
    const unsigned r = 0x200000u + 2 * H_TS - 1 - (o & 0x1fffffu), d1 = ((r >> 15) * 171) >> 9, d2 = (((r + 0x200000u) >> 15) * 171) >> 9;
    const unsigned a1 = ~0u << d1, a2 = d2 < 32 ? ~0u << d2 : 0u;
    return ((__popc(p[0]) > 2 ? ~a1 : 0u) | (__popc(p[1]) > 2 ? a1 & ~a2 : 0u) | (__popc(p[2]) > 2 ? a2 : 0u)) ^ (0u - side);
  };
  const unsigned f0 = fmask(q), f1 = fmask(rs);
  const unsigned fx = ((side ^ x_flip(x, q)) ? ~K2_SHT : 0u) | ((side ^ x_flip(x, rs)) ? K2_SHT : 0u);
  const uint64_t h0 = (uint64_t)hws + q * H_RS + nh * H_TS + pg * 4096, h1 = (uint64_t)hws + rs * H_RS + nh * H_TS + hf * 4096 + pg * 2048;
  const unsigned p0 = (12 * q + 2 * pg) * 49152 + nh * 384, p1 = (12 * rs + 2 * hf + pg) * 49152 + nh * 384, e0 = 288 * b + 72 * w;
  typedef uint64_t u64x2 __attribute__((ext_vector_type(2)));
  u64x2* P = (u64x2*)(g_prm + 64 * e);
  P[0] = (u64x2){f0 | (uint64_t)f1 << 32, fx | (uint64_t)e0 << 32};
  const u32x4 sc = *(const u32x4*)g_sc;
  P[1] = (u64x2){(((b >> 3) * 4 + w) & 7) * 1024 | (uint64_t)sc[0] << 32, sc[1] | (uint64_t)sc[2] << 32};
  P[2] = (u64x2){h0, h0 + 8192};
  P[3] = (u64x2){h1, h1 + 8192};
  P[4] = (u64x2){0, 0};
  P[5] = (u64x2){(uint64_t)x + p0, (uint64_t)x + p1};
  P[6] = (u64x2){(uint64_t)out + p0, (uint64_t)out + p1};
  P[7] = (u64x2){0x1fffffffffffffffull, (uint64_t)g_buf + G_FLAG};
  P[8] = (u64x2){(uint64_t)g_w2f + w * 6144, (uint64_t)g_w2q + w * 6144};
  P[9] = (u64x2){(uint64_t)g_w2r + e0 * 16, (uint64_t)g_w2f + e0 * 16};
  P[10] = (u64x2){(uint64_t)grn_g + w * 256, (uint64_t)g_bias + w * 1536};
  P[11] = (u64x2){(uint64_t)g_buf + G_PUB + b * 256, (uint64_t)g_buf + G_PUB + w * 16384};
  P[12] = (u64x2){(uint64_t)g_w2f + w * 6144, 0};
  P[13] = (u64x2){h0, h0 + 8192};
  P[14] = (u64x2){(uint64_t)x + p0, (uint64_t)out + p0};
  P[15] = (u64x2){f0, fx};
}
__global__ __launch_bounds__(256, 1) void k1_fc1(const u16* __restrict__ x, char* __restrict__ hws, const u16* __restrict__ out, const u16* __restrict__ grn_g) {
  extern __shared__ __attribute__((aligned(16))) char smem[];
  const unsigned w = __builtin_amdgcn_readfirstlane(threadIdx.x >> 6);
  if (blockIdx.x < 8) {
    if (w < 2) k2_params(x, hws, out, grn_g, w * 64 + __builtin_amdgcn_mbcnt_hi(~0u, __builtin_amdgcn_mbcnt_lo(~0u, 0u)));
    return;
  }
  const unsigned lds0 = lds_addr(smem);
  const unsigned b = blockIdx.x - 8;
  const unsigned dir = (b >> 3) & 1;
  TS(0); MK_START(1);
  const uint64_t xp = (uint64_t)x, hp = (uint64_t)hws, gp = (uint64_t)g_buf, wp = gp + w * K1_WCH;
  const unsigned d = dir ? ~0u : 1u, wrap = dir ? ~0u : 48u, rst = dir ? 47u : 0u, pos0 = dir ? K1_SMAX - 1 : 0u;
  const unsigned xo0 = (12 * ((b >> 3) * 4 + (b & 3)) + w) * 49152, xs1 = (1536 + w + (dir ? 8 : 0)) * 49152, sb1 = 2048 * w + (dir ? 33792 : 0);
  const unsigned r0o = ((b >> 3) * 4 + (b & 3)) * H_RS, tsel = 4 * (b & 3), s2 = (b >> 2) & 1;
  const uint64_t ap = gp + K1_G_ACC + (b % K1_NREP) * 12288;
  const unsigned ca0 = w == 0 ? dir * 64 : K1_G_DUM - K1_G_CNT + (4 * b + w) * 64;
  const unsigned pm = lds0 + K1_PF_OFF, pf = ((b >> 3) * 4 + w) * 1024;
  unsigned nb;
  asm volatile(
R"(.macro M t,f,w
.if \f < 30
v_mfma_f32_16x16x32_f16 v[56+4*\t:56+4*\t+3], v[128+4*\f:128+4*\f+3], a[216+4*\w:216+4*\w+3], v[56+4*\t:56+4*\t+3]
.else
v_mfma_f32_16x16x32_f16 v[56+4*\t:56+4*\t+3], a[4*\f-4*30:4*\f-4*30+3], a[216+4*\w:216+4*\w+3], v[56+4*\t:56+4*\t+3]
.endif
.endm
.macro Z t,f,w
.if \f < 30
v_mfma_f32_16x16x32_f16 v[56+4*\t:56+4*\t+3], v[128+4*\f:128+4*\f+3], a[216+4*\w:216+4*\w+3], v[252:255]
.else
v_mfma_f32_16x16x32_f16 v[56+4*\t:56+4*\t+3], a[4*\f-4*30:4*\f-4*30+3], a[216+4*\w:216+4*\w+3], v[252:255]
.endif
.endm
.macro LZ a,f
.if \f < 30
v_mfma_f32_16x16x32_bf16 v[36:36+3], v[\a:\a+3], v[128+4*\f:128+4*\f+3], 0
.else
v_mfma_f32_16x16x32_bf16 v[36:36+3], v[\a:\a+3], a[4*\f-4*30:4*\f-4*30+3], 0
.endif
.endm
.macro LS a,f
.if \f < 30
v_mfma_f32_16x16x32_bf16 v[36:36+3], v[\a:\a+3], v[128+4*\f:128+4*\f+3], v[36:36+3]
.else
v_mfma_f32_16x16x32_bf16 v[36:36+3], v[\a:\a+3], a[4*\f-4*30:4*\f-4*30+3], v[36:36+3]
.endif
.endm
.macro LD d,a,f
.if \f < 30
v_mfma_f32_16x16x32_bf16 v[\d:\d+3], a[\a:\a+3], v[128+4*\f:128+4*\f+3], v[36:36+3]
.else
v_mfma_f32_16x16x32_bf16 v[\d:\d+3], a[\a:\a+3], a[4*\f-4*30:4*\f-4*30+3], v[36:36+3]
.endif
.endm
.macro GZ f
.if \f < 30
v_mfma_f32_16x16x32_f16 v[52:55], v[128+4*\f:128+4*\f+3], v[128+4*\f:128+4*\f+3], 0
.else
v_mfma_f32_16x16x32_f16 v[52:55], a[4*\f-4*30:4*\f-4*30+3], a[4*\f-4*30:4*\f-4*30+3], 0
.endif
.endm
.macro GS f
.if \f < 30
v_mfma_f32_16x16x32_f16 v[52:55], v[128+4*\f:128+4*\f+3], v[128+4*\f:128+4*\f+3], v[52:55]
.else
v_mfma_f32_16x16x32_f16 v[52:55], a[4*\f-4*30:4*\f-4*30+3], a[4*\f-4*30:4*\f-4*30+3], v[52:55]
.endif
.endm
.macro CL p,z,s
v_fma_mixlo_f16 v[\p], v[\z], v[\s], 0 op_sel_hi:[0,1,0]
.endm
.macro CH p,z,s
v_fma_mixhi_f16 v[\p], v[\z], v[\s], 0 op_sel:[0,1,0] op_sel_hi:[0,1,0]
.endm
.macro XL f,o
.if \f < 30
global_load_dwordx4 v[128+4*\f:128+4*\f+3], v3, s[46:47] offset:\o
.else
global_load_dwordx4 a[4*\f-4*30:4*\f-4*30+3], v3, s[46:47] offset:\o
.endif
.endm
.macro XN f,o
.if \f < 30
global_load_dwordx4 v[128+4*\f:128+4*\f+3], v3, s[46:47] offset:\o nt
.else
global_load_dwordx4 a[4*\f-4*30:4*\f-4*30+3], v3, s[46:47] offset:\o nt
.endif
.endm
.macro F t,z
v_fma_f32 v[\t], -v[\z], v[\z], s36
.endm
.macro U t,z
v_mul_f32 v[\t], v[\z], v[\t]
.endm
.macro E t
v_exp_f32 v[\t], v[\t]
.endm
.macro A t
v_add_f32 v[\t], 1.0, v[\t]
.endm
.macro R t
v_rcp_f32 v[\t], v[\t]
.endm
.macro C d,a,b
v_cvt_pk_bf16_f32 v[\d], v[\a], v[\b]
.endm
.macro S0 q,t
v_mul_f32 v[\q], v[\t], v[\t]
.endm
.macro S q,t
v_fmac_f32 v[\q], v[\t], v[\t]
.endm
.macro HC d,z
v_cvt_pk_f16_f32 v[\d], v[\z], v[\z+1]
.endm
.macro HF t,p
v_pk_fma_f16 v[\t], v[\p], v[\p], s36 neg_lo:[1,0,0] neg_hi:[1,0,0]
.endm
.macro HM t,p
v_pk_mul_f16 v[\t], v[\p], v[\t]
.endm
.macro HE d,s
v_exp_f16 v[\d], v[\s]
.endm
.macro HG d,s
v_exp_f16_sdwa v[\d], v[\s] dst_sel:WORD_1 dst_unused:UNUSED_PRESERVE src0_sel:WORD_1
.endm
.macro HA t
v_pk_add_f16 v[\t], v[\t], 1.0 op_sel_hi:[1,0]
.endm
.macro HR d,s
v_rcp_f16 v[\d], v[\s]
.endm
.macro HQ d,s
v_rcp_f16_sdwa v[\d], v[\s] dst_sel:WORD_1 dst_unused:UNUSED_PRESERVE src0_sel:WORD_1
.endm
.macro HP d,s
v_rcp_f16_sdwa v[\d], v[\s] dst_sel:DWORD dst_unused:UNUSED_PAD src0_sel:WORD_1
.endm
.macro HY t,z
v_fma_mix_f32 v[\t], v[\z], v[\t], 0 op_sel_hi:[0,1,0]
.endm
.macro HK p
v_and_b32 v[\p], s56, v[\p]
.endm
.macro LQ p
v_lshrrev_b32 v[\p], 2, v[\p]
.endm
.macro LL t,z
v_lshlrev_b32_sdwa v[\t], 1, v[\z] dst_sel:DWORD dst_unused:UNUSED_PAD src0_sel:DWORD src1_sel:WORD_1
.endm
.macro LH t,p
v_and_b32_sdwa v[\t], s56, v[\p] dst_sel:DWORD dst_unused:UNUSED_PAD src0_sel:DWORD src1_sel:WORD_1
.endm
.macro LA t
ds_read_u16 v[\t], v[\t] offset:5376
.endm
.macro LC_ t
ds_read_u16_d16_hi v[\t], v[\t] offset:5376
.endm
.macro LO p,a,b
v_bfi_b32 v[\p], s36, v[\a], v[\b]
.endm
.macro HZ q,p
v_pk_mul_f16 v[\q], v[\p], v[\p]
.endm
.macro HS q,p
v_pk_fma_f16 v[\q], v[\p], v[\p], v[\q]
.endm
.macro HT q,p
v_fma_mix_f32 v[\q], v[\p], v[\p], v[\q] op_sel:[1,1,0] op_sel_hi:[1,1,0]
.endm
.macro HX q
v_fma_mix_f32 v[\q], v[\q], 1.0, v[\q] op_sel:[0,0,1] op_sel_hi:[1,0,1]
.endm
.macro UX t,z
v_mul_f32_e64 v[\t], v[\z], v[\t]
.endm
.macro EX t
v_exp_f32_e64 v[\t], v[\t]
.endm
.macro AX t
v_add_f32_e64 v[\t], 1.0, v[\t]
.endm
.macro RX t
v_rcp_f32_e64 v[\t], v[\t]
.endm
.macro TX q,t
v_mul_f32_e64 v[\q], v[\t], v[\t]
.endm
.macro SX q,t
v_fmac_f32_e64 v[\q], v[\t], v[\t]
.endm
.macro AW a,s
v_accvgpr_write_b32 a[\a], v[\s]
.endm
.macro RA d,v,o
ds_read_b128 a[\d:\d+3], v[\v] offset:\o
.endm
.macro RV d,v,o
ds_read_b128 v[\d:\d+3], v[\v] offset:\o
.endm
.macro W2 a,b,o
ds_write2st64_b32 v4, v[\a], v[\b] offset0:\o offset1:\o+4
.endm
.macro W1 a,o
ds_write_b32 v4, v[\a] offset:\o
.endm
.macro DW o
global_load_lds_dwordx4 v0, s[40:41] offset:\o
.endm
.macro DF
global_load_lds_dwordx4 v0, s[70:71]
.endm
.macro DX
global_load_lds_dword v3, s[46:47]
.endm
.macro ST d
global_store_dwordx4 v2, v[\d:\d+3], s[42:43] nt sc1
.endm
.macro SU d
global_store_dwordx4 v2, v[\d:\d+3], s[42:43] sc1
.endm
.macro SP
global_atomic_add_f64 v6, v[14:15], s[48:49]
.endm
.macro AT
global_atomic_add v11, v9, v10, s[68:69] sc0
.endm
.macro PFA
s_getpc_b64 s[70:71]
s_add_u32 s70, s70, 7f-.
s_addc_u32 s71, s71, 0
s_andn2_b32 s70, s70, 0x7f
s_min_u32 s53, %[pf], 8f-7f-1152
s_andn2_b32 s53, s53, 0x7f
s_add_u32 s70, s70, s53
s_addc_u32 s71, s71, 0
.endm
.macro PFN
s_getpc_b64 s[70:71]
s_add_u32 s70, s70, k2_code@rel32@lo-1020
s_addc_u32 s71, s71, k2_code@rel32@hi-1012
s_add_u32 s70, s70, %[pf]
s_addc_u32 s71, s71, 0
s_andn2_b32 s70, s70, 0x7f
.endm
.macro PFW
s_getpc_b64 s[70:71]
s_add_u32 s70, s70, g_w2q@rel32@lo+4
s_addc_u32 s71, s71, g_w2q@rel32@hi+12
s_add_u32 s70, s70, %[pf]
s_addc_u32 s71, s71, 0
.endm
.macro PF0
s_getpc_b64 s[70:71]
s_andn2_b32 s70, s70, 0x7f
s_min_u32 s53, %[pf], 7f-.-1152
s_andn2_b32 s53, s53, 0x7f
s_add_u32 s70, s70, s53
s_addc_u32 s71, s71, 0
.endm
.macro WL n
s_waitcnt lgkmcnt(\n)
.endm
.macro WV n
s_waitcnt vmcnt(\n)
.endm
.macro LU t,p
v_pk_mul_f16 v[\t], v[\p], v34
.endm
.macro CF d,a,b
v_cvt_pk_f16_f32 v[\d], v[\a], v[\b]
.endm
.macro XS d
v_pk_fma_f16 v[\d], v[\d], v13, s56
.endm
.macro AR d,a
v_accvgpr_read_b32 v[\d], a[\a]
.endm
.macro DQ o
global_load_lds_dword v23, s[40:41] offset:\o
.endm
.macro GE
v_cmp_eq_u32 vcc, 0, v35
s_nop 4
s_cbranch_vccz 9f
v_mov_b32 v35, 14720
s_mov_b64 exec, 1
global_store_dword v35, v10, s[68:69] sc1
s_mov_b64 exec, -1
9:
.endm
.macro BW f
.if \f < 30
ds_write_b128 v12, v[128+4*\f:128+4*\f+3]
.else
ds_write_b128 v12, a[4*\f-120:4*\f-120+3]
.endif
.endm
.macro BR f
.if \f < 30
ds_read_b128 v[128+4*\f:128+4*\f+3], v20
.else
ds_read_b128 a[4*\f-120:4*\f-120+3], v20
.endif
.endm
.macro LK d,a,f,h
.if \f < 30
v_mfma_f32_4x4x4_16b_bf16 v[\d:\d+3], a[\a:\a+1], v[128+4*\f+\h:128+4*\f+\h+1], v[36:39]
.else
v_mfma_f32_4x4x4_16b_bf16 v[\d:\d+3], a[\a:\a+1], a[4*\f-120+\h:4*\f-120+\h+1], v[36:39]
.endif
.endm
k1_code:
v_mbcnt_lo_u32_b32 v16, -1, 0
v_mbcnt_hi_u32_b32 v16, -1, v16
v_lshlrev_b32 v0, 4, v16
v_and_b32 v17, 15, v16
v_lshrrev_b32 v18, 4, v16
s_add_u32 s68, %[glo], 1239040
s_addc_u32 s69, %[ghi], 0
s_load_dwordx2 s[66:67], s[68:69], 16320
s_mov_b32 s53, %[r0o]
s_add_u32 s82, %[hlo], s53
s_addc_u32 s83, %[hhi], 0
s_add_u32 s84, s82, 8192
s_addc_u32 s85, s83, 0
s_and_b32 s90, %[hlo], 0x1fffff
s_add_u32 s90, s90, s53
s_mov_b32 s91, 3072
v_mov_b32 v11, s90
v_mad_u32_u24 v11, v0, s91, v11
v_lshrrev_b32 v11, 21, v11
v_lshl_add_u32 v11, v11, 2, 64
v_writelane_b32 v11, %[tsel], 63
s_sub_u32 s53, s82, %[hlo]
s_lshr_b32 s53, s53, 2
s_and_b32 s90, %[xlo], 0x1fffff
s_add_u32 s53, s53, s90
s_add_u32 s53, s53, 0x20048000
s_lshr_b32 s53, s53, 21
s_lshl_b32 s90, s53, 2
v_writelane_b32 v11, s90, 48
global_load_dword v11, v11, s[68:69] offset:-2048
v_lshlrev_b32 v19, 1, v18
s_movk_i32 s53, 0x78
v_bfe_u32 v21, v16, 2, 2
v_lshrrev_b32 v19, v19, s53
v_mul_u32_u24 v21, 0x300, v21
v_xor_b32 v19, v19, v16
v_and_b32 v19, 3, v19
v_lshl_add_u32 v21, v19, 4, v21
s_mov_b32 s97, 3072
s_mov_b32 s50, %[pos0]
s_mov_b32 s51, %[pos0]
s_mov_b32 s52, %[pos0]
s_add_u32 s74, %[lds], 0
s_add_u32 s75, s74, 163840
s_mov_b32 s37, s74
s_mul_i32 s53, %[w], 6400
s_add_u32 s76, s74, s53
s_add_u32 s77, s76, 163840
s_mov_b32 s38, s76
s_lshl_b32 s60, %[w], 11
s_add_u32 s61, s60, 1024
s_add_u32 s62, s60, 16384
s_add_u32 s63, s60, 17408
s_add_u32 s64, s60, 32768
s_add_u32 s65, s60, 33792
v_lshlrev_b32 v23, 2, v16
s_lshl_b32 s53, %[w], 11
s_min_u32 s53, s53, 5120
s_add_u32 s72, s53, 1228800
s_add_u32 s40, %[glo], s72
s_addc_u32 s41, %[ghi], 0
s_add_u32 s53, s53, %[lds]
s_add_u32 m0, s53, 58368
s_nop 0
DW 0
DW 1024
s_nop 0 ;PS
s_mul_i32 s53, s50, 25600
s_add_u32 s40, %[wlo], s53
s_addc_u32 s41, %[whi], 0
s_mov_b32 m0, s38
s_nop 0
DW 0
DW 1024
DW 2048
s_add_u32 s40, s40, 3072
s_addc_u32 s41, s41, 0
s_add_u32 m0, s38, 3072
s_nop 0
DW 0
DW 1024
DW 2048
DQ 3072
s_add_u32 s50, s50, %[d]
s_cmp_eq_u32 s50, %[wrap]
s_cselect_b32 s50, %[rst], s50
s_add_u32 s38, s38, 32768
s_cmp_eq_u32 s38, s77
s_cselect_b32 s38, s76, s38
s_mul_i32 s53, s50, 25600
s_add_u32 s40, %[wlo], s53
s_addc_u32 s41, %[whi], 0
s_mov_b32 m0, s38
s_nop 0
DW 0
DW 1024
DW 2048
s_add_u32 s40, s40, 3072
s_addc_u32 s41, s41, 0
s_add_u32 m0, s38, 3072
s_nop 0
DW 0
DW 1024
DW 2048
)"
R"(DQ 3072
s_add_u32 s50, s50, %[d]
s_cmp_eq_u32 s50, %[wrap]
s_cselect_b32 s50, %[rst], s50
s_add_u32 s38, s38, 32768
s_cmp_eq_u32 s38, s77
s_cselect_b32 s38, s76, s38
s_and_b32 s53, %[w], 1
s_mul_i32 s53, s53, 3072
s_lshr_b32 s72, %[w], 1
s_lshl_b32 s40, s72, 16
s_add_u32 s40, s40, s53
s_add_u32 m0, s40, 25600
s_lshl_b32 s72, s72, 13
s_add_u32 s53, s53, s72
s_add_u32 s53, s53, 1382400
s_add_u32 s40, %[glo], s53
s_addc_u32 s41, %[ghi], 0
DW 0
DW 1024
DW 2048
DW 3072
v_add_u32 v1, s37, v0
v_lshl_add_u32 v19, v18, 2, v17
v_and_b32 v19, 15, v19
v_lshlrev_b32 v19, 4, v19
v_lshl_add_u32 v2, v18, 8, v19
s_lshl_b32 s53, %[w], 2
s_add_u32 s53, s53, %[lds]
s_add_u32 s53, s53, 123904
s_add_u32 s53, %[lds], 123904
v_bfe_u32 v4, v17, 1, 2
v_and_b32 v5, 8, v17
v_add3_u32 v4, v4, v5, %[w]
v_lshl_add_u32 v4, v18, 2, v4
v_and_b32 v4, 15, v4
v_lshl_add_u32 v4, v17, 4, v4
v_lshl_add_u32 v4, v4, 2, s53
s_lshl_b32 s53, %[w], 9
s_add_u32 s53, s53, %[lds]
s_add_u32 s53, s53, 123904
v_lshlrev_b32 v5, 3, v16
v_add_u32 v5, s53, v5
s_lshl_b32 s53, %[w], 6
v_and_b32 v6, 0x38, v16
v_add_u32 v6, s53, v6
s_add_u32 s53, %[lds], 58368
v_and_b32 v19, 3, v16
v_lshl_add_u32 v19, v18, 2, v19
v_lshl_add_u32 v7, v19, 4, s53
v_lshlrev_b32 v8, 4, v18
v_add_u32 v8, s53, v8
v_mov_b32 v10, 1
s_mov_b32 s36, 0xffff
s_mov_b32 s56, 0x5550555
s_mov_b32 s39, 32768
s_mov_b32 s54, 0x01010101
s_mov_b32 s55, 0x01010101
s_mov_b32 %[nb], 0
v_and_b32 v28, 12, v17
v_lshl_add_u32 v28, v28, 2, v17
v_lshlrev_b32 v28, 2, v28
v_mov_b32 v34, 0x74007400
v_mov_b32 v35, -1
v_and_b32 v22, 0xfffffcff, v1
s_mov_b32 s78, 0xaaaaaaaa
s_mov_b32 s79, 0xaaaaaaaa
s_mov_b32 s80, 0xcccccccc
s_mov_b32 s81, 0xcccccccc
s_and_b32 s53, %[w], 1
s_lshl_b32 s53, s53, 10
s_lshr_b32 s72, %[w], 1
s_mul_i32 s72, s72, 32768
s_add_u32 s72, s72, s53
s_add_u32 s72, s72, 128000
s_movk_i32 s53, 0x78
v_lshrrev_b32 v12, 5, v0
v_and_b32 v12, 6, v12
v_bfe_u32 v12, s53, v12, 2
v_lshrrev_b32 v20, 8, v0
v_xor_b32 v20, v12, v20
v_lshl_add_u32 v20, v20, 4, s72
v_and_b32 v12, 0xf0, v0
v_lshl_add_u32 v20, v12, 2, v20
v_add_u32 v12, s72, v0
WV 20
v_bcnt_u32_b32 v11, v11, 0
v_cmp_lt_u32 s[90:91], 2, v11
s_nop 0
s_lshr_b32 s96, s91, 31
s_xor_b32 s96, s96, %[s2]
s_sub_u32 s53, 0, s96
s_xor_b32 s90, s90, s53
s_xor_b32 s91, s91, s53
s_add_u32 s53, %[xo0], 0
s_add_u32 s46, %[xlo], s53
s_addc_u32 s47, %[xhi], 0
s_bitcmp1_b64 s[90:91], 48
s_cselect_b32 s58, 16, 0
s_lshr_b32 s58, 0xfe987610, s58
v_lshrrev_b32 v3, 6, v0
v_and_b32 v3, 12, v3
v_bfe_u32 v3, s58, v3, 4
v_mad_u32_u24 v3, v3, s97, v21
XL 12,0
XL 13,64
XL 14,128
XL 15,192
XL 16,256
XL 17,320
XL 18,384
XL 19,448
XL 20,512
XL 21,576
XL 22,640
XL 23,704
s_mul_i32 s53, s96, 8320
s_cmp_lt_u32 %[ca0], 128
s_cselect_b32 s53, s53, 0
s_add_u32 s73, s53, %[ca0]
v_mov_b32 v9, s73
WV 30
s_barrier
WV 11
BW 12
RV 40,8,6144
RV 44,8,6208
RV 48,8,6272
RV 248,8,6336
BR 12
WV 10
BW 13
s_nop 1
BR 13
WL 2
LZ 40,12
WV 9
BW 14
RV 40,8,6400
s_nop 0
BR 14
PF0
s_mov_b32 m0, %[pm]
s_nop 0
DF
PFA
WL 3
LS 44,13
WV 9
BW 15
RV 44,8,6464
s_nop 0
BR 15
WL 3
LS 48,14
WV 8
BW 16
RV 48,8,6528
s_nop 0
BR 16
s_add_u32 s53, %[xo0], 0
s_add_u32 s46, %[xlo], s53
s_addc_u32 s47, %[xhi], 0
s_bitcmp1_b64 s[90:91], 48
s_cselect_b32 s58, 16, 0
s_lshr_b32 s58, 0xda43cb52, s58
v_lshrrev_b32 v3, 6, v0
v_and_b32 v3, 12, v3
v_bfe_u32 v3, s58, v3, 4
v_mad_u32_u24 v3, v3, s97, v21
WL 3
LS 248,15
WV 7
BW 17
RV 248,8,6592
s_nop 0
BR 17
WL 3
LS 40,16
WV 6
BW 18
RV 40,8,6656
s_nop 0
BR 18
XL 24,0
WL 3
LS 44,17
WV 6
BW 19
RV 44,8,6720
s_nop 0
BR 19
WL 3
LS 48,18
WV 5
BW 20
RV 48,8,6784
s_nop 0
BR 20
XL 25,64
WL 3
LS 248,19
WV 5
BW 21
RV 248,8,6848
s_nop 0
BR 21
XL 26,128
WL 3
LS 40,20
WV 5
BW 22
RA 240,7,0
s_nop 0
BR 22
WL 3
LS 44,21
WV 4
BW 23
RA 244,7,256
s_nop 0
BR 23
XL 27,192
WL 3
LS 48,22
RA 248,7,512
WL 1
LS 248,23
RA 252,7,768
s_nop 6
v_mul_f32 v36, 0xbb2aaaab, v36
v_mul_f32 v37, 0xbb2aaaab, v37
v_mul_f32 v38, 0xbb2aaaab, v38
v_mul_f32 v39, 0xbb2aaaab, v39
XL 28,256
s_nop 0
LK 40,240,12,0
LK 44,242,12,2
RA 240,7,1024
LK 48,244,13,0
s_nop 1
CF 176,40,41
LK 248,246,13,2
WL 2
RA 244,7,1280
CF 177,42,43
CF 178,44,45
CF 179,46,47
XL 29,320
LK 40,248,14,0
CF 180,48,49
LK 44,250,14,2
RA 248,7,1536
CF 181,50,51
XL 30,384
GZ 12
WL 3
CF 182,248,249
CF 183,250,251
LK 48,252,15,0
CF 184,40,41
LK 248,254,15,2
RA 252,7,1792
CF 185,42,43
XL 31,448
GS 13
WL 3
CF 186,44,45
CF 187,46,47
LK 40,240,16,0
CF 188,48,49
LK 44,242,16,2
RA 240,7,2048
CF 189,50,51
XL 32,512
GS 14
WL 3
CF 190,248,249
CF 191,250,251
LK 48,244,17,0
CF 192,40,41
LK 248,246,17,2
RA 244,7,2304
CF 193,42,43
XL 33,576
GS 15
WL 3
XL 34,640
CF 194,44,45
CF 195,46,47
LK 40,248,18,0
CF 196,48,49
LK 44,250,18,2
RA 248,7,2560
CF 197,50,51
GS 16
WL 3
XL 35,704
CF 198,248,249
CF 199,250,251
LK 48,252,19,0
CF 200,40,41
LK 248,254,19,2
WL 2
RA 252,7,2816
CF 201,42,43
GS 17
s_mul_i32 s53, s50, 25600
s_add_u32 s40, %[wlo], s53
s_addc_u32 s41, %[whi], 0
s_mov_b32 m0, s38
CF 202,44,45
CF 203,46,47
LK 40,240,20,0
CF 204,48,49
LK 44,242,20,2
CF 205,50,51
GS 18
WL 2
DW 0
CF 206,248,249
CF 207,250,251
LK 48,244,21,0
CF 208,40,41
LK 248,246,21,2
CF 209,42,43
DW 1024
GS 19
WL 1
CF 210,44,45
CF 211,46,47
LK 40,248,22,0
CF 212,48,49
LK 44,250,22,2
CF 213,50,51
DW 2048
GS 20
WL 0
CF 214,248,249
CF 215,250,251
LK 48,252,23,0
CF 216,40,41
LK 248,254,23,2
CF 217,42,43
s_add_u32 s40, s40, 3072
s_addc_u32 s41, s41, 0
s_add_u32 m0, s38, 3072
GS 21
CF 218,44,45
CF 219,46,47
RV 36,8,6912
s_nop 0
GS 22
DW 0
CF 220,48,49
CF 221,50,51
CF 222,248,249
CF 223,250,251
s_nop 1
GS 23
DW 1024
s_barrier
RA 216,1,0
RA 220,1,1024
RA 224,1,2048
RA 228,1,3072
RV 252,22,5120
s_nop 0
v_cndmask_b32 v52, v52, v53, s[78:79]
v_cndmask_b32 v54, v54, v55, s[78:79]
v_cndmask_b32 v52, v52, v54, s[80:81]
v_mul_f32 v52, 0x3b2aaaab, v52
C 52,52,52
v_lshlrev_b32 v52, 16, v52
WL 5
v_add_f32 v52, v36, v52
C 52,52,52
v_lshlrev_b32 v52, 16, v52
v_sqrt_f32 v52, v52
DW 2048
C 52,52,52
v_lshlrev_b32 v52, 16, v52
v_cmp_ge_f32 vcc, v37, v52
v_rcp_f32 v52, v52
s_nop 0
v_mul_f32 v52, v38, v52
v_cndmask_b32 v52, 0, v52, vcc
CF 52,52,52
ds_bpermute_b32 v13, v28, v52
WL 0
XS 176
XS 177
DQ 3072
XS 178
XS 179
XS 180
XS 181
XS 182
XS 183
v_min_u32 v35, v35, v13
)"
R"(XS 184
XS 185
XS 186
s_add_u32 s50, s50, %[d]
s_cmp_eq_u32 s50, %[wrap]
s_cselect_b32 s50, %[rst], s50
XS 187
XS 188
XS 189
XS 190
XS 191
s_add_u32 s38, s38, 32768
s_cmp_eq_u32 s38, s77
s_cselect_b32 s38, s76, s38
Z 0,12,0
XS 192
XS 193
XS 194
XS 195
XS 196
XS 197
XS 198
XS 199
XS 200
XS 201
XS 202
XS 203
XS 204
M 0,13,1
s_mul_i32 s53, s50, 25600
RA 232,1,4096
XS 205
XS 206
XS 207
M 0,14,2
s_add_u32 s40, %[wlo], s53
RA 236,1,6144
s_addc_u32 s41, %[whi], 0
s_mov_b32 m0, s38
XS 208
XS 209
M 0,15,3
WL 1
RA 216,1,7168
DW 0
XS 210
XS 211
M 0,16,4
RA 220,1,8192
DW 1024
XS 212
XS 213
WL 2
M 0,17,5
RA 224,1,9216
DW 2048
XS 214
XS 215
XS 216
WL 2
M 0,18,0
s_add_u32 s40, s40, 3072
RA 228,1,10240
s_addc_u32 s41, s41, 0
s_add_u32 m0, s38, 3072
XS 217
XS 218
WL 2
M 0,19,1
WL 1
RA 232,1,11264
DW 0
XS 219
XS 220
M 0,20,2
WV 22
RV 252,22,5392
RA 236,1,12288
DW 1024
XS 221
BW 24
RV 40,8,6144
XS 222
RV 44,8,6208
XS 223
WL 6
M 0,21,3
WV 22
RA 216,1,13312
RV 48,8,6272
DW 2048
BR 24
BW 25
WL 9
M 0,22,4
WL 7
RA 220,1,14336
BR 25
DQ 3072
M 0,23,5
s_add_u32 s50, s50, %[d]
RA 224,1,15360
s_cmp_eq_u32 s50, %[wrap]
s_cselect_b32 s50, %[rst], s50
WL 4
LZ 40,24
WV 23
BW 26
RV 40,8,6336
s_nop 0
BR 26
WL 4
LS 44,25
WV 22
BW 27
Z 6,12,0
s_add_u32 s38, s38, 32768
RA 228,1,16384
RV 44,8,6400
s_cmp_eq_u32 s38, s77
s_cselect_b32 s38, s76, s38
BR 27
M 6,13,1
s_add_u32 s53, %[xo0], 196608
RA 232,1,17408
s_add_u32 s46, %[xlo], s53
s_addc_u32 s47, %[xhi], 0
s_bitcmp1_b64 s[90:91], 48
s_cselect_b32 s58, 16, 0
s_lshr_b32 s58, 0xfe987610, s58
v_lshrrev_b32 v3, 6, v0
v_and_b32 v3, 12, v3
v_bfe_u32 v3, s58, v3, 4
v_mad_u32_u24 v3, v3, s97, v21
WL 5
LS 48,26
WV 21
BW 28
RV 48,8,6464
M 6,14,2
WL 3
LL 16,56
LL 104,57
RA 236,1,18432
XL 36,0
BR 28
M 6,15,3
s_add_u32 s37, s37, 32768
LA 16
LC_ 104
s_cmp_eq_u32 s37, s75
s_cselect_b32 s37, s74, s37
XL 37,64
LS 40,27
WV 22
BW 29
RV 40,8,6528
s_nop 0
BR 29
WL 5
LS 44,28
WV 21
BW 30
M 6,16,4
RA 216,1,19456
RV 44,8,6592
RA 220,1,20480
XL 38,128
BR 30
M 6,17,5
WV 21
LL 17,58
LL 105,59
XL 39,192
WL 5
LS 48,29
s_mul_i32 s53, s51, 49152
BW 31
RV 48,8,6656
s_nop 0
BR 31
WL 6
M 6,18,0
s_bitcmp1_b64 s[90:91], s51
LA 17
LC_ 105
RA 224,1,21504
RA 228,1,22528
s_cselect_b64 s[44:45], s[84:85], s[82:83]
s_add_u32 s44, s44, s53
s_addc_u32 s45, s45, 0
s_add_u32 s51, s51, %[d]
s_cmp_eq_u32 s51, %[wrap]
s_cselect_b32 s51, %[rst], s51
XL 40,256
WL 8
M 6,19,1
WL 4
LO 104,16,104
LU 16,104
HZ 24,16
LS 40,30
WV 22
BW 32
RV 40,8,6720
s_nop 0
BR 32
LS 44,31
WV 21
BW 33
WL 5
M 6,20,2
WL 4
RA 232,1,23552
RV 44,8,6784
RA 236,1,24576
s_nop 1
v_add_u32 v1, s37, v0
XL 41,320
BR 33
M 6,21,3
WL 1
WV 13
s_barrier
RA 216,1,0
v_and_b32 v22, 0xfffffcff, v1
LS 48,32
BW 34
RV 48,8,6848
RA 240,7,0
M 6,22,4
LO 105,17,105
LU 17,105
HS 24,17
RA 220,1,1024
BR 34
M 6,23,5
RV 252,22,5120
HX 24
WL 0
Z 0,12,0
RA 224,1,2048
RA 228,1,3072
M 0,13,1
s_mul_i32 s53, s50, 25600
RA 232,1,4096
W1 24,0
LS 40,33
s_add_u32 s40, %[wlo], s53
BW 35
RA 244,7,256
s_nop 0
BR 35
LS 44,34
s_addc_u32 s41, %[whi], 0
RA 248,7,512
WL 1
LS 48,35
s_mov_b32 m0, s38
RA 252,7,768
s_nop 5
v_mul_f32 v36, 0xbb2aaaab, v36
v_mul_f32 v37, 0xbb2aaaab, v37
v_mul_f32 v38, 0xbb2aaaab, v38
v_mul_f32 v39, 0xbb2aaaab, v39
s_nop 1
LK 40,240,24,0
LK 44,242,24,2
RA 240,7,1024
LK 48,244,25,0
s_nop 1
CF 224,40,41
LK 248,246,25,2
RA 244,7,1280
CF 225,42,43
M 0,14,2
LL 18,80
LL 106,81
RA 236,1,6144
CF 226,44,45
CF 227,46,47
M 0,15,3
WL 4
LA 18
LC_ 106
DW 0
LK 40,248,26,0
CF 228,48,49
LK 44,250,26,2
RA 248,7,1536
CF 229,50,51
GZ 24
WL 6
CF 230,248,249
CF 231,250,251
LK 48,252,27,0
CF 232,40,41
LK 248,254,27,2
RA 252,7,1792
CF 233,42,43
GS 25
CF 234,44,45
CF 235,46,47
M 0,16,4
WL 4
RA 216,1,7168
RA 220,1,8192
DW 1024
M 0,17,5
LL 19,82
LL 107,83
DW 2048
LK 40,240,28,0
CF 236,48,49
LK 44,242,28,2
RA 240,7,2048
CF 237,50,51
GS 26
CF 238,248,249
CF 239,250,251
LK 48,244,29,0
CF 240,40,41
LK 248,246,29,2
RA 244,7,2304
CF 241,42,43
GS 27
WL 5
CF 242,44,45
CF 243,46,47
LK 40,248,30,0
CF 244,48,49
LK 44,250,30,2
RA 248,7,2560
CF 245,50,51
GS 28
WL 5
CF 246,248,249
CF 247,250,251
LK 48,252,31,0
CF 152,40,41
LK 248,254,31,2
RA 252,7,2816
CF 153,42,43
GS 29
WL 4
CF 154,44,45
CF 155,46,47
M 0,18,0
s_add_u32 s40, s40, 3072
LA 19
LC_ 107
RA 224,1,9216
RA 228,1,10240
s_addc_u32 s41, s41, 0
s_add_u32 m0, s38, 3072
M 0,19,1
WL 7
LO 106,18,106
LU 18,106
HZ 25,18
DW 0
LK 40,240,32,0
CF 156,48,49
LK 44,242,32,2
CF 157,50,51
GS 6
WL 6
CF 158,248,249
CF 159,250,251
LK 48,244,33,0
CF 160,40,41
LK 248,246,33,2
CF 161,42,43
GS 7
WL 1
CF 162,44,45
CF 163,46,47
M 0,20,2
WL 0
RA 232,1,11264
DW 1024
M 0,21,3
RV 252,22,5392
RA 236,1,12288
DW 2048
LK 40,248,34,0
CF 164,48,49
LK 44,250,34,2
s_add_u32 s42, s44, s60
CF 165,50,51
GS 8
s_addc_u32 s43, s45, 0
CF 166,248,249
CF 167,250,251
LK 48,252,35,0
CF 168,40,41
LK 248,254,35,2
CF 169,42,43
GS 9
WL 2
)"
R"(CF 170,44,45
CF 171,46,47
M 0,22,4
WL 0
LO 107,19,107
LU 19,107
HS 25,19
RA 216,1,13312
RA 220,1,14336
M 0,23,5
RA 224,1,15360
RV 36,8,6912
DQ 3072
GS 10
CF 172,48,49
CF 173,50,51
WL 3
Z 6,12,0
RA 228,1,16384
HX 25
ST 104
s_add_u32 s50, s50, %[d]
s_cmp_eq_u32 s50, %[wrap]
s_cselect_b32 s50, %[rst], s50
CF 174,248,249
CF 175,250,251
WL 3
M 6,13,1
s_add_u32 s38, s38, 32768
RA 232,1,17408
W1 25,1024
s_cmp_eq_u32 s38, s77
s_cselect_b32 s38, s76, s38
GS 11
WL 4
RA 236,1,18432
s_nop 5
v_cndmask_b32 v52, v52, v53, s[78:79]
v_cndmask_b32 v54, v54, v55, s[78:79]
v_cndmask_b32 v52, v52, v54, s[80:81]
v_mul_f32 v52, 0x3b2aaaab, v52
M 6,14,2
WL 3
LL 16,56
LL 104,57
XL 42,384
C 52,52,52
v_lshlrev_b32 v52, 16, v52
v_add_f32 v52, v36, v52
C 52,52,52
M 6,15,3
s_add_u32 s37, s37, 32768
LA 16
LC_ 104
v_add_u32 v4, s39, v4
s_cmp_eq_u32 s37, s75
s_cselect_b32 s37, s74, s37
XL 43,448
v_lshlrev_b32 v52, 16, v52
v_sqrt_f32 v52, v52
s_nop 0
C 52,52,52
v_lshlrev_b32 v52, 16, v52
WL 4
M 6,16,4
WL 2
RA 216,1,19456
RA 220,1,20480
XL 44,512
v_cmp_ge_f32 vcc, v37, v52
v_rcp_f32 v52, v52
s_nop 0
v_mul_f32 v52, v38, v52
v_cndmask_b32 v52, 0, v52, vcc
M 6,17,5
WV 17
LL 17,58
LL 105,59
XL 45,576
CF 52,52,52
ds_bpermute_b32 v13, v28, v52
WL 2
M 6,18,0
s_mul_i32 s53, s51, 49152
LA 17
LC_ 105
RA 224,1,21504
RA 228,1,22528
s_bitcmp1_b64 s[90:91], s51
s_cselect_b64 s[44:45], s[84:85], s[82:83]
s_add_u32 s44, s44, s53
s_addc_u32 s45, s45, 0
s_add_u32 s51, s51, %[d]
s_cmp_eq_u32 s51, %[wrap]
s_cselect_b32 s51, %[rst], s51
XL 46,640
WL 5
M 6,19,1
WL 1
LO 104,16,104
LU 16,104
HZ 24,16
M 6,20,2
s_lshl_b32 s53, s52, 8
RA 232,1,23552
RA 236,1,24576
s_nop 1
v_add_u32 v1, s37, v0
s_add_u32 s48, %[alo], s53
s_addc_u32 s49, %[ahi], 0
s_add_u32 s52, s52, %[d]
s_cmp_eq_u32 s52, %[wrap]
s_cselect_b32 s52, %[rst], s52
XL 47,704
XS 224
XS 225
WL 2
M 6,21,3
WL 0
s_barrier
RA 216,1,0
v_and_b32 v22, 0xfffffcff, v1
XS 226
XS 227
XS 228
M 6,22,4
LO 105,17,105
LU 17,105
HS 24,17
RA 220,1,1024
s_sub_u32 s39, 0, s39
XS 229
XS 230
XS 231
M 6,23,5
RV 252,22,5120
ds_read_b64 v[14:15], v5 offset:0
HX 24
v_min_u32 v35, v35, v13
XS 232
XS 233
WL 1
Z 0,12,0
RA 224,1,2048
v_subrev_u32 v5, s39, v5
XS 234
XS 235
XS 236
M 0,13,1
s_mul_i32 s53, s50, 25600
RA 228,1,3072
XS 237
XS 238
XS 239
Z 1,24,0
s_add_u32 s40, %[wlo], s53
LL 18,80
LL 106,81
XS 240
XS 241
XS 242
M 1,25,1
s_addc_u32 s41, %[whi], 0
LA 18
LC_ 106
W1 24,0
XS 243
XS 244
XS 245
WL 4
M 0,14,2
s_mov_b32 m0, s38
RA 232,1,4096
v_add_f32 v14, v14, v15
XS 246
XS 247
WL 4
M 0,15,3
LL 19,82
LL 107,83
XS 152
XS 153
XS 154
M 1,26,2
LA 19
LC_ 107
RA 236,1,6144
DW 0
XS 155
AW 0,152
AW 1,153
M 1,27,3
WL 3
LO 106,18,106
LU 18,106
HZ 25,18
v_add_f32_dpp v14, v14, v14 quad_perm:[1,0,3,2] row_mask:0xf bank_mask:0xf
AW 2,154
AW 3,155
XS 156
M 0,16,4
WL 0
RA 216,1,7168
v_add_f32_dpp v14, v14, v14 quad_perm:[2,3,0,1] row_mask:0xf bank_mask:0xf
DW 1024
XS 157
XS 158
XS 159
M 0,17,5
RA 220,1,8192
v_add_f32_dpp v14, v14, v14 row_half_mirror row_mask:0xf bank_mask:0xf
AW 4,156
AW 5,157
AW 6,158
M 1,28,4
LO 107,19,107
LU 19,107
HS 25,19
v_cvt_f64_f32 v[14:15], v14
AW 7,159
XS 160
XS 161
M 1,29,5
WL 0
RA 224,1,9216
s_mov_b64 exec, s[54:55]
SP
s_mov_b64 exec, -1
HX 25
DW 2048
XS 162
XS 163
AW 8,160
M 0,18,0
s_add_u32 s42, s44, s60
RA 228,1,10240
s_addc_u32 s43, s45, 0
AW 9,161
AW 10,162
AW 11,163
M 0,19,1
s_add_u32 s40, s40, 3072
RA 232,1,11264
s_addc_u32 s41, s41, 0
s_add_u32 m0, s38, 3072
XS 164
XS 165
XS 166
M 1,30,0
WL 2
RV 252,22,5392
ST 104
XS 167
AW 12,164
AW 13,165
M 1,31,1
WV 24
RA 236,1,12288
W1 25,1024
AW 14,166
BW 36
AW 15,167
XS 168
M 0,20,2
WL 5
DW 0
RV 40,8,6144
XS 169
XS 170
RV 44,8,6208
XS 171
M 0,21,3
RA 216,1,13312
RV 48,8,6272
AW 16,168
AW 17,169
AW 18,170
BR 36
M 1,32,2
WV 24
DW 1024
v_add_u32 v4, s39, v4
AW 19,171
XS 172
BW 37
XS 173
M 1,33,3
RA 220,1,14336
BR 37
XS 174
XS 175
AW 20,172
WL 3
LZ 40,36
WV 24
BW 38
M 0,22,4
DW 2048
RV 40,8,6336
AW 21,173
AW 22,174
AW 23,175
M 0,23,5
RA 224,1,15360
BR 38
M 1,34,4
DQ 3072
M 1,35,5
WL 4
RA 228,1,16384
LS 44,37
WV 23
BW 39
RV 44,8,6400
Z 6,12,0
s_add_u32 s50, s50, %[d]
LL 16,56
LL 104,57
BR 39
M 6,13,1
s_cmp_eq_u32 s50, %[wrap]
LA 16
RA 232,1,17408
WL 6
LS 48,38
s_cselect_b32 s50, %[rst], s50
BW 40
Z 7,24,0
s_add_u32 s38, s38, 32768
LC_ 104
RV 48,8,6464
M 7,25,1
s_cmp_eq_u32 s38, s77
RA 236,1,18432
BR 40
WL 7
LS 40,39
s_cselect_b32 s38, s76, s38
BW 41
M 6,14,2
s_add_u32 s37, s37, 32768
RV 40,8,6528
BR 41
M 6,15,3
s_cmp_eq_u32 s37, s75
RA 216,1,19456
M 7,26,2
s_cselect_b32 s37, s74, s37
LL 17,58
LL 105,59
M 7,27,3
s_add_u32 s53, %[xo0], 196608
LA 17
RA 220,1,20480
s_add_u32 s46, %[xlo], s53
s_addc_u32 s47, %[xhi], 0
s_bitcmp1_b64 s[90:91], 48
s_cselect_b32 s58, 16, 0
s_lshr_b32 s58, 0xda43cb52, s58
v_lshrrev_b32 v3, 6, v0
v_and_b32 v3, 12, v3
v_bfe_u32 v3, s58, v3, 4
v_mad_u32_u24 v3, v3, s97, v21
WL 6
LS 44,40
WV 13
BW 42
RV 44,8,6592
M 6,16,4
WL 5
LC_ 105
XL 48,0
BR 42
M 6,17,5
s_mul_i32 s53, s51, 49152
RA 224,1,21504
LS 48,41
s_bitcmp1_b64 s[90:91], s51
BW 43
M 7,28,4
WV 13
LO 104,16,104
LU 16,104
RV 48,8,6656
BR 43
M 7,29,5
s_cselect_b64 s[44:45], s[84:85], s[82:83]
RA 228,1,22528
HZ 24,16
WL 5
LS 40,42
s_add_u32 s44, s44, s53
BW 44
M 6,18,0
s_addc_u32 s45, s45, 0
LL 18,60
LL 108,61
s_add_u32 s51, s51, %[d]
s_cmp_eq_u32 s51, %[wrap]
s_cselect_b32 s51, %[rst], s51
)"
R"(XL 49,64
RV 40,8,6720
M 6,19,1
LA 18
RA 232,1,23552
BR 44
M 7,30,0
LC_ 108
XL 50,128
M 7,31,1
WL 7
RA 236,1,24576
s_nop 1
v_add_u32 v1, s37, v0
LS 44,43
WV 14
BW 45
RV 44,8,6784
s_nop 0
BR 45
M 6,20,2
WL 10
LO 105,17,105
LU 17,105
M 6,21,3
WL 5
RA 216,1,0
HS 24,17
XL 51,192
LS 48,44
WV 14
BW 46
RV 48,8,6848
M 7,32,2
WV 13
LL 19,62
LL 109,63
RA 240,7,0
M 7,33,3
WL 4
LA 19
XL 52,256
BR 46
LS 40,45
s_lshl_b32 s53, s52, 8
BW 47
M 6,22,4
s_add_u32 s48, %[alo], s53
LC_ 109
RA 244,7,256
v_and_b32 v22, 0xfffffcff, v1
M 6,23,5
s_addc_u32 s49, %[ahi], 0
RA 220,1,1024
BR 47
M 7,34,4
LO 108,18,108
LU 18,108
s_add_u32 s52, s52, %[d]
s_cmp_eq_u32 s52, %[wrap]
s_cselect_b32 s52, %[rst], s52
XL 53,320
M 7,35,5
s_barrier
RV 252,22,5120
HS 24,18
WL 0
Z 0,12,0
s_sub_u32 s39, 0, s39
LL 16,80
LL 106,81
RA 224,1,2048
M 0,13,1
s_mul_i32 s53, s50, 25600
LA 16
RA 228,1,3072
LS 44,46
s_add_u32 s40, %[wlo], s53
RA 248,7,512
LS 48,47
s_addc_u32 s41, %[whi], 0
RA 252,7,768
s_nop 5
v_mul_f32 v36, 0xbb2aaaab, v36
v_mul_f32 v37, 0xbb2aaaab, v37
v_mul_f32 v38, 0xbb2aaaab, v38
v_mul_f32 v39, 0xbb2aaaab, v39
s_nop 1
LK 40,240,36,0
LK 44,242,36,2
s_mov_b32 m0, s38
RA 240,7,1024
LK 48,244,37,0
s_nop 0
CF 128,40,41
LK 248,246,37,2
RA 244,7,1280
CF 129,42,43
Z 1,24,0
LC_ 106
RA 232,1,4096
M 1,25,1
WL 8
RA 236,1,6144
ds_read_b64 v[14:15], v5 offset:0
CF 130,44,45
M 0,14,2
WL 8
LO 109,19,109
LU 19,109
CF 131,46,47
M 0,15,3
WL 7
RA 216,1,7168
HS 24,19
LK 40,248,38,0
CF 132,48,49
LK 44,250,38,2
RA 248,7,1536
CF 133,50,51
GZ 0
WL 8
CF 134,248,249
CF 135,250,251
LK 48,252,39,0
CF 136,40,41
LK 248,254,39,2
RA 252,7,1792
CF 137,42,43
GS 1
WL 8
CF 138,44,45
CF 139,46,47
LK 40,240,40,0
CF 140,48,49
LK 44,242,40,2
RA 240,7,2048
CF 141,50,51
GS 2
WL 8
CF 142,248,249
CF 143,250,251
LK 48,244,41,0
CF 144,40,41
LK 248,246,41,2
RA 244,7,2304
CF 145,42,43
GS 3
CF 146,44,45
CF 147,46,47
M 1,26,2
LL 17,82
LL 107,83
DW 0
M 1,27,3
WL 3
LA 17
RA 220,1,8192
v_subrev_u32 v5, s39, v5
LK 40,248,42,0
CF 148,48,49
LK 44,250,42,2
RA 248,7,2560
CF 149,50,51
GS 4
CF 150,248,249
CF 151,250,251
M 0,16,4
LC_ 107
DW 1024
M 0,17,5
WL 6
RA 224,1,9216
v_add_f32 v14, v14, v15
s_nop 1
v_add_f32_dpp v14, v14, v14 quad_perm:[1,0,3,2] row_mask:0xf bank_mask:0xf
s_nop 1
v_add_f32_dpp v14, v14, v14 quad_perm:[2,3,0,1] row_mask:0xf bank_mask:0xf
s_nop 1
v_add_f32_dpp v14, v14, v14 row_half_mirror row_mask:0xf bank_mask:0xf
HX 24
LK 48,252,43,0
CF 152,40,41
LK 248,254,43,2
RA 252,7,2816
CF 153,42,43
GS 5
CF 154,44,45
CF 155,46,47
M 1,28,4
LO 106,16,106
LU 16,106
v_cvt_f64_f32 v[14:15], v14
W1 24,0
M 1,29,5
WL 8
RA 228,1,10240
HZ 25,16
s_mov_b64 exec, s[54:55]
SP
s_mov_b64 exec, -1
DW 2048
LK 40,240,44,0
CF 156,48,49
LK 44,242,44,2
CF 157,50,51
GS 6
WL 6
CF 158,248,249
CF 159,250,251
M 0,18,0
s_add_u32 s40, s40, 3072
LL 18,84
LL 110,85
M 0,19,1
s_addc_u32 s41, s41, 0
LA 18
RA 232,1,11264
LK 48,244,45,0
CF 160,40,41
LK 248,246,45,2
s_add_u32 m0, s38, 3072
CF 161,42,43
GS 7
CF 162,44,45
CF 163,46,47
M 1,30,0
LC_ 110
RV 252,22,5392
M 1,31,1
WL 9
RA 236,1,12288
LK 40,248,46,0
CF 164,48,49
LK 44,250,46,2
CF 165,50,51
GS 8
WL 8
CF 166,248,249
CF 167,250,251
M 0,20,2
WL 5
LO 107,17,107
LU 17,107
M 0,21,3
RA 216,1,13312
HS 25,17
DW 0
LK 48,252,47,0
CF 168,40,41
LK 248,254,47,2
s_add_u32 s42, s44, s60
CF 169,42,43
GS 9
s_addc_u32 s43, s45, 0
CF 170,44,45
CF 171,46,47
M 1,32,2
LL 19,86
LL 111,87
M 1,33,3
WL 4
LA 19
RA 220,1,14336
DW 1024
M 0,22,4
WL 3
LC_ 111
ST 104
M 0,23,5
RA 224,1,15360
RV 36,8,6912
GS 10
CF 172,48,49
CF 173,50,51
M 1,34,4
LO 110,18,110
LU 18,110
DW 2048
M 1,35,5
WL 5
RA 228,1,16384
HS 25,18
Z 6,12,0
WL 4
LL 16,56
LL 104,57
CF 174,248,249
CF 175,250,251
M 6,13,1
WL 2
LA 16
RA 232,1,17408
DQ 3072
GS 11
s_add_u32 s50, s50, %[d]
s_nop 6
v_cndmask_b32 v52, v52, v53, s[78:79]
v_cndmask_b32 v54, v54, v55, s[78:79]
Z 7,24,0
s_cmp_eq_u32 s50, %[wrap]
LC_ 104
v_cndmask_b32 v52, v52, v54, s[80:81]
v_mul_f32 v52, 0x3b2aaaab, v52
M 7,25,1
s_cselect_b32 s50, %[rst], s50
RA 236,1,18432
C 52,52,52
v_lshlrev_b32 v52, 16, v52
M 6,14,2
WL 2
LO 111,19,111
LU 19,111
v_add_f32 v52, v36, v52
C 52,52,52
M 6,15,3
s_add_u32 s42, s44, s61
RA 216,1,19456
HS 25,19
v_lshlrev_b32 v52, 16, v52
v_sqrt_f32 v52, v52
M 7,26,2
s_addc_u32 s43, s45, 0
LL 17,58
LL 105,59
s_add_u32 s38, s38, 32768
s_cmp_eq_u32 s38, s77
s_cselect_b32 s38, s76, s38
C 52,52,52
v_lshlrev_b32 v52, 16, v52
M 7,27,3
s_add_u32 s37, s37, 32768
LA 17
RA 220,1,20480
s_cmp_eq_u32 s37, s75
s_cselect_b32 s37, s74, s37
v_cmp_ge_f32 vcc, v37, v52
v_rcp_f32 v52, v52
M 6,16,4
WL 3
LC_ 105
ST 108
XL 54,384
v_mul_f32 v52, v38, v52
v_cndmask_b32 v52, 0, v52, vcc
M 6,17,5
RA 224,1,21504
HX 25
CF 52,52,52
M 7,28,4
LO 104,16,104
LU 16,104
W1 25,1024
ds_bpermute_b32 v13, v28, v52
M 7,29,5
WL 4
RA 228,1,22528
HZ 24,16
XL 55,448
M 6,18,0
s_mul_i32 s53, s51, 49152
LL 18,60
LL 108,61
s_bitcmp1_b64 s[90:91], s51
s_cselect_b64 s[44:45], s[84:85], s[82:83]
s_add_u32 s44, s44, s53
s_addc_u32 s45, s45, 0
s_add_u32 s51, s51, %[d]
s_cmp_eq_u32 s51, %[wrap]
s_cselect_b32 s51, %[rst], s51
M 6,19,1
LA 18
RA 232,1,23552
M 7,30,0
LC_ 108
XL 56,512
v_add_u32 v4, s39, v4
M 7,31,1
WL 6
RA 236,1,24576
s_nop 1
v_add_u32 v1, s37, v0
M 6,20,2
WL 4
LO 105,17,105
LU 17,105
XL 57,576
M 6,21,3
RA 216,1,0
HS 24,17
M 7,32,2
LL 19,62
LL 109,63
M 7,33,3
WL 1
LA 19
XL 58,640
XS 128
XS 129
M 6,22,4
)"
R"(s_lshl_b32 s53, s52, 8
LC_ 109
v_and_b32 v22, 0xfffffcff, v1
XS 130
XS 131
M 6,23,5
s_add_u32 s48, %[alo], s53
RA 220,1,1024
AW 24,128
AW 25,129
AW 26,130
M 7,34,4
WV 21
LO 108,18,108
LU 18,108
s_addc_u32 s49, %[ahi], 0
s_add_u32 s52, s52, %[d]
s_cmp_eq_u32 s52, %[wrap]
s_cselect_b32 s52, %[rst], s52
XL 59,704
AW 27,131
XS 132
M 7,35,5
s_barrier
RV 252,22,5120
HS 24,18
XS 133
XS 134
XS 135
WL 0
Z 0,12,0
s_sub_u32 s39, 0, s39
LL 16,80
LL 106,81
AW 28,132
AW 29,133
M 0,13,1
s_mul_i32 s53, s50, 25600
LA 16
RA 224,1,2048
AW 30,134
AW 31,135
v_min_u32 v35, v35, v13
Z 1,24,0
s_add_u32 s40, %[wlo], s53
LC_ 106
RA 228,1,3072
XS 136
XS 137
M 1,25,1
s_addc_u32 s41, %[whi], 0
RA 232,1,4096
XS 138
XS 139
AW 32,136
Z 2,36,0
WL 1
LO 109,19,109
LU 19,109
AW 33,137
AW 34,138
AW 35,139
M 2,37,1
s_mov_b32 m0, s38
RA 236,1,6144
HS 24,19
ds_read_b64 v[14:15], v5 offset:0
XS 140
XS 141
M 0,14,2
LL 17,82
LL 107,83
XS 142
XS 143
AW 36,140
M 0,15,3
LA 17
DW 0
AW 37,141
AW 38,142
M 1,26,2
LC_ 107
v_subrev_u32 v5, s39, v5
AW 39,143
XS 144
XS 145
M 1,27,3
RA 216,1,7168
HX 24
XS 146
XS 147
M 2,38,2
WL 4
LO 106,16,106
LU 16,106
AW 40,144
AW 41,145
AW 42,146
M 2,39,3
RA 220,1,8192
HZ 25,16
AW 43,147
XS 148
XS 149
M 0,16,4
LL 18,84
LL 110,85
W1 24,0
XS 150
XS 151
M 0,17,5
LA 18
DW 1024
AW 44,148
AW 45,149
AW 46,150
M 1,28,4
WL 6
LC_ 110
DW 2048
AW 47,151
XS 152
M 1,29,5
s_add_u32 s42, s44, s60
RA 224,1,9216
v_add_f32 v14, v14, v15
s_nop 1
v_add_f32_dpp v14, v14, v14 quad_perm:[1,0,3,2] row_mask:0xf bank_mask:0xf
XS 153
XS 154
XS 155
M 2,40,4
WL 4
LO 107,17,107
LU 17,107
AW 48,152
AW 49,153
M 2,41,5
s_addc_u32 s43, s45, 0
RA 228,1,10240
HS 25,17
v_add_f32_dpp v14, v14, v14 quad_perm:[2,3,0,1] row_mask:0xf bank_mask:0xf
AW 50,154
AW 51,155
XS 156
M 0,18,0
s_add_u32 s40, s40, 3072
LL 19,86
LL 111,87
XS 157
XS 158
XS 159
M 0,19,1
s_addc_u32 s41, s41, 0
LA 19
v_add_f32_dpp v14, v14, v14 row_half_mirror row_mask:0xf bank_mask:0xf
AW 52,156
AW 53,157
M 1,30,0
s_add_u32 m0, s38, 3072
LC_ 111
ST 104
AW 54,158
AW 55,159
XS 160
M 1,31,1
WV 25
RA 232,1,11264
v_cvt_f64_f32 v[14:15], v14
XS 161
XS 162
M 2,42,0
WL 3
LO 110,18,110
LU 18,110
XS 163
AW 56,160
BW 48
AW 57,161
M 2,43,1
RV 252,22,5392
HS 25,18
s_mov_b64 exec, s[54:55]
SP
s_mov_b64 exec, -1
AW 58,162
RV 40,8,6144
AW 59,163
M 0,20,2
RA 236,1,12288
RV 44,8,6208
XS 164
XS 165
XS 166
M 0,21,3
DW 0
RV 48,8,6272
XS 167
AW 60,164
AW 61,165
M 1,32,2
WV 26
DW 1024
BR 48
AW 62,166
AW 63,167
M 1,33,3
s_add_u32 s42, s44, s61
RA 216,1,13312
XS 168
BW 49
XS 169
XS 170
M 2,44,2
WL 5
LO 111,19,111
LU 19,111
XS 171
BR 49
AW 64,168
M 2,45,3
s_addc_u32 s43, s45, 0
RA 220,1,14336
HS 25,19
AW 65,169
AW 66,170
AW 67,171
WL 4
LZ 40,48
WV 26
BW 50
M 0,22,4
DW 2048
RV 40,8,6336
XS 172
XS 173
M 0,23,5
ST 108
BR 50
XS 174
XS 175
AW 68,172
M 1,34,4
HX 25
AW 69,173
AW 70,174
AW 71,175
M 1,35,5
WL 4
RA 224,1,15360
LS 44,49
WV 27
BW 51
M 2,46,4
WL 5
LL 16,56
LL 104,57
M 2,47,5
LA 16
RA 228,1,16384
RV 44,8,6400
Z 6,12,0
WV 26
LC_ 104
W1 25,1024
BR 51
M 6,13,1
WL 8
DQ 3072
v_add_u32 v4, s39, v4
LS 48,50
s_add_u32 s50, s50, %[d]
BW 52
Z 7,24,0
s_cmp_eq_u32 s50, %[wrap]
RV 48,8,6464
BR 52
M 7,25,1
s_cselect_b32 s50, %[rst], s50
RA 232,1,17408
Z 8,36,0
WL 4
LL 17,58
LL 105,59
M 8,37,1
s_add_u32 s38, s38, 32768
LA 17
RA 236,1,18432
LS 40,51
s_cmp_eq_u32 s38, s77
WV 26
BW 53
M 6,14,2
s_cselect_b32 s38, s76, s38
LC_ 105
RV 40,8,6528
M 6,15,3
s_add_u32 s37, s37, 32768
BR 53
M 7,26,2
WL 7
LO 104,16,104
LU 16,104
M 7,27,3
s_cmp_eq_u32 s37, s75
RA 216,1,19456
HZ 24,16
LS 44,52
s_cselect_b32 s37, s74, s37
WV 15
BW 54
M 8,38,2
WL 6
LL 18,60
LL 108,61
RV 44,8,6592
M 8,39,3
s_add_u32 s53, %[xo0], 393216
LA 18
RA 220,1,20480
BR 54
M 6,16,4
s_add_u32 s46, %[xlo], s53
LC_ 108
s_addc_u32 s47, %[xhi], 0
s_bitcmp1_b64 s[90:91], 48
s_cselect_b32 s58, 16, 0
s_lshr_b32 s58, 0xfe987610, s58
v_lshrrev_b32 v3, 6, v0
v_and_b32 v3, 12, v3
v_bfe_u32 v3, s58, v3, 4
v_mad_u32_u24 v3, v3, s97, v21
M 6,17,5
WL 7
XL 60,0
LS 48,53
s_lshl_b32 s53, s52, 8
WV 15
BW 55
M 7,28,4
WV 14
LO 105,17,105
LU 17,105
RV 48,8,6656
M 7,29,5
s_add_u32 s48, %[alo], s53
RA 224,1,21504
HS 24,17
BR 55
M 8,40,4
WL 5
LL 19,62
LL 109,63
M 8,41,5
s_addc_u32 s49, %[ahi], 0
LA 19
RA 228,1,22528
LS 40,54
s_add_u32 s52, s52, %[d]
BW 56
M 6,18,0
s_cmp_eq_u32 s52, %[wrap]
LC_ 109
RV 40,8,6720
s_cselect_b32 s52, %[rst], s52
s_mul_i32 s53, s51, 49152
s_bitcmp1_b64 s[90:91], s51
s_cselect_b64 s[44:45], s[84:85], s[82:83]
s_add_u32 s44, s44, s53
s_addc_u32 s45, s45, 0
s_add_u32 s51, s51, %[d]
s_cmp_eq_u32 s51, %[wrap]
s_cselect_b32 s51, %[rst], s51
M 6,19,1
XL 61,64
BR 56
M 7,30,0
WL 10
LO 108,18,108
LU 18,108
M 7,31,1
WL 6
RA 232,1,23552
HS 24,18
LS 44,55
WV 14
BW 57
M 8,42,0
WL 6
LL 16,64
LL 112,65
RV 44,8,6784
M 8,43,1
LA 16
RA 236,1,24576
BR 57
M 6,20,2
LC_ 112
XL 62,128
M 6,21,3
)"
R"(WL 7
XL 63,192
v_add_u32 v1, s37, v0
LS 48,56
WV 15
BW 58
M 7,32,2
LO 109,19,109
LU 19,109
RV 48,8,6848
M 7,33,3
WV 14
RA 216,1,0
HS 24,19
RA 240,7,0
M 8,44,2
WL 6
LL 17,66
LL 113,67
BR 58
M 8,45,3
WL 6
LA 17
XL 64,256
LS 40,57
s_barrier
BW 59
M 6,22,4
s_sub_u32 s39, 0, s39
LC_ 113
ds_read_b64 v[14:15], v5 offset:0
M 6,23,5
XL 65,320
RA 244,7,256
M 7,34,4
WL 10
LO 112,16,112
LU 16,112
v_and_b32 v22, 0xfffffcff, v1
BR 59
M 7,35,5
WL 6
RA 220,1,1024
HS 24,16
LS 44,58
RA 248,7,512
v_subrev_u32 v5, s39, v5
M 8,46,4
WL 1
LL 18,80
LL 106,81
M 8,47,5
LA 18
RV 252,22,5120
WL 0
Z 0,12,0
LC_ 106
RA 224,1,2048
M 0,13,1
s_mul_i32 s53, s50, 25600
RA 228,1,3072
LS 48,59
s_add_u32 s40, %[wlo], s53
RA 252,7,768
s_nop 5
v_mul_f32 v36, 0xbb2aaaab, v36
v_mul_f32 v37, 0xbb2aaaab, v37
v_mul_f32 v38, 0xbb2aaaab, v38
Z 1,24,0
LO 113,17,113
LU 17,113
v_mul_f32 v39, 0xbb2aaaab, v39
M 1,25,1
s_addc_u32 s41, %[whi], 0
RA 232,1,4096
HS 24,17
LK 40,240,48,0
LK 44,242,48,2
s_mov_b32 m0, s38
RA 240,7,1024
LK 48,244,49,0
s_nop 0
CF 128,40,41
LK 248,246,49,2
RA 244,7,1280
CF 129,42,43
Z 2,36,0
WL 4
LL 19,82
LL 107,83
M 2,37,1
LA 19
RA 236,1,6144
CF 130,44,45
CF 131,46,47
LK 40,248,50,0
CF 132,48,49
LK 44,250,50,2
RA 248,7,1536
CF 133,50,51
GZ 0
WL 6
CF 134,248,249
CF 135,250,251
LK 48,252,51,0
CF 136,40,41
LK 248,254,51,2
RA 252,7,1792
CF 137,42,43
GS 1
CF 138,44,45
CF 139,46,47
M 0,14,2
LC_ 107
DW 0
M 0,15,3
WL 6
v_add_f32 v14, v14, v15
HX 24
LK 40,240,52,0
CF 140,48,49
LK 44,242,52,2
RA 240,7,2048
CF 141,50,51
GS 2
WL 6
CF 142,248,249
CF 143,250,251
LK 48,244,53,0
CF 144,40,41
LK 248,246,53,2
RA 244,7,2304
CF 145,42,43
GS 3
CF 146,44,45
CF 147,46,47
M 1,26,2
LO 106,18,106
LU 18,106
M 1,27,3
RA 216,1,7168
HZ 25,18
M 2,38,2
WL 6
LL 16,84
LL 110,85
v_add_f32_dpp v14, v14, v14 quad_perm:[1,0,3,2] row_mask:0xf bank_mask:0xf
W1 24,0
M 2,39,3
WL 6
LA 16
RA 220,1,8192
LK 40,248,54,0
CF 148,48,49
LK 44,250,54,2
RA 248,7,2560
CF 149,50,51
GS 4
CF 150,248,249
CF 151,250,251
M 0,16,4
WL 8
LC_ 110
DW 1024
v_add_f32_dpp v14, v14, v14 quad_perm:[2,3,0,1] row_mask:0xf bank_mask:0xf
M 0,17,5
s_add_u32 s42, s44, s60
v_add_f32_dpp v14, v14, v14 row_half_mirror row_mask:0xf bank_mask:0xf
LK 48,252,55,0
CF 152,40,41
LK 248,254,55,2
s_addc_u32 s43, s45, 0
RA 252,7,2816
CF 153,42,43
GS 5
CF 154,44,45
CF 155,46,47
M 1,28,4
WL 9
LO 107,19,107
LU 19,107
M 1,29,5
RA 224,1,9216
HS 25,19
M 2,40,4
WL 4
LL 17,86
LL 111,87
v_cvt_f64_f32 v[14:15], v14
M 2,41,5
LA 17
RA 228,1,10240
LK 40,240,56,0
CF 156,48,49
LK 44,242,56,2
CF 157,50,51
GS 6
CF 158,248,249
CF 159,250,251
M 0,18,0
LC_ 111
s_mov_b64 exec, s[54:55]
SP
s_mov_b64 exec, -1
DW 2048
M 0,19,1
ST 104
LK 48,244,57,0
CF 160,40,41
LK 248,246,57,2
s_add_u32 s40, s40, 3072
CF 161,42,43
GS 7
s_addc_u32 s41, s41, 0
CF 162,44,45
CF 163,46,47
M 1,30,0
WL 5
LO 110,16,110
LU 16,110
M 1,31,1
s_add_u32 m0, s38, 3072
RA 232,1,11264
HS 25,16
M 2,42,0
WL 2
LL 18,88
LL 114,89
M 2,43,1
LA 18
RV 252,22,5392
LK 40,248,58,0
CF 164,48,49
LK 44,250,58,2
CF 165,50,51
GS 8
CF 166,248,249
CF 167,250,251
M 0,20,2
LC_ 114
RA 236,1,12288
M 0,21,3
DW 0
LK 48,252,59,0
CF 168,40,41
LK 248,254,59,2
s_add_u32 s42, s44, s61
CF 169,42,43
GS 9
s_addc_u32 s43, s45, 0
CF 170,44,45
CF 171,46,47
M 1,32,2
WL 5
LO 111,17,111
LU 17,111
M 1,33,3
RA 216,1,13312
HS 25,17
M 2,44,2
WL 1
LL 19,90
LL 115,91
M 2,45,3
LA 19
RA 220,1,14336
M 0,22,4
LC_ 115
DW 1024
M 0,23,5
ST 108
RV 36,8,6912
GS 10
CF 172,48,49
CF 173,50,51
M 1,34,4
LO 114,18,114
LU 18,114
M 1,35,5
RA 224,1,15360
HS 25,18
M 2,46,4
WL 3
LL 16,56
LL 104,57
DW 2048
M 2,47,5
LA 16
RA 228,1,16384
CF 174,248,249
Z 6,12,0
LC_ 104
DQ 3072
CF 175,250,251
M 6,13,1
s_add_u32 s42, s44, s62
GS 11
s_addc_u32 s43, s45, 0
s_nop 6
v_cndmask_b32 v52, v52, v53, s[78:79]
v_cndmask_b32 v54, v54, v55, s[78:79]
Z 7,24,0
WL 5
LO 115,19,115
LU 19,115
v_cndmask_b32 v52, v52, v54, s[80:81]
M 7,25,1
s_add_u32 s50, s50, %[d]
RA 232,1,17408
HS 25,19
v_mul_f32 v52, 0x3b2aaaab, v52
Z 8,36,0
WL 2
LL 17,58
LL 105,59
C 52,52,52
v_lshlrev_b32 v52, 16, v52
M 8,37,1
s_cmp_eq_u32 s50, %[wrap]
LA 17
RA 236,1,18432
v_add_f32 v52, v36, v52
M 6,14,2
s_cselect_b32 s50, %[rst], s50
LC_ 105
ST 112
C 52,52,52
M 6,15,3
s_add_u32 s38, s38, 32768
HX 25
v_lshlrev_b32 v52, 16, v52
M 7,26,2
WL 4
LO 104,16,104
LU 16,104
v_sqrt_f32 v52, v52
s_nop 0
C 52,52,52
M 7,27,3
s_cmp_eq_u32 s38, s77
RA 216,1,19456
HZ 24,16
v_lshlrev_b32 v52, 16, v52
M 8,38,2
WL 2
LL 18,60
LL 108,61
W1 25,1024
v_cmp_ge_f32 vcc, v37, v52
M 8,39,3
s_cselect_b32 s38, s76, s38
LA 18
RA 220,1,20480
v_rcp_f32 v52, v52
M 6,16,4
s_add_u32 s37, s37, 32768
LC_ 108
v_add_u32 v4, s39, v4
s_cmp_eq_u32 s37, s75
s_cselect_b32 s37, s74, s37
v_mul_f32 v52, v38, v52
v_cndmask_b32 v52, 0, v52, vcc
M 6,17,5
XL 66,384
CF 52,52,52
M 7,28,4
WL 5
LO 105,17,105
LU 17,105
ds_bpermute_b32 v13, v28, v52
M 7,29,5
s_lshl_b32 s53, s52, 8
RA 224,1,21504
HS 24,17
M 8,40,4
WL 3
LL 19,62
LL 109,63
M 8,41,5
s_add_u32 s48, %[alo], s53
LA 19
RA 228,1,22528
M 6,18,0
s_addc_u32 s49, %[ahi], 0
LC_ 109
s_add_u32 s52, s52, %[d]
s_cmp_eq_u32 s52, %[wrap]
s_cselect_b32 s52, %[rst], s52
s_mul_i32 s53, s51, 49152
s_bitcmp1_b64 s[90:91], s51
s_cselect_b64 s[44:45], s[84:85], s[82:83]
s_add_u32 s44, s44, s53
s_addc_u32 s45, s45, 0
s_add_u32 s51, s51, %[d]
s_cmp_eq_u32 s51, %[wrap]
s_cselect_b32 s51, %[rst], s51
M 6,19,1
XL 67,448
M 7,30,0
WL 5
)"
R"(LO 108,18,108
LU 18,108
M 7,31,1
RA 232,1,23552
HS 24,18
M 8,42,0
WL 2
LL 16,64
LL 112,65
M 8,43,1
LA 16
RA 236,1,24576
XL 68,512
M 6,20,2
LC_ 112
XL 69,576
XS 128
XS 129
M 6,21,3
v_add_u32 v1, s37, v0
XS 130
M 7,32,2
WL 4
LO 109,19,109
LU 19,109
XS 131
AW 72,128
M 7,33,3
RA 216,1,0
HS 24,19
AW 73,129
AW 74,130
M 8,44,2
WL 2
LL 17,66
LL 113,67
AW 75,131
M 8,45,3
WV 21
LA 17
XL 70,640
XS 132
XS 133
M 6,22,4
s_barrier
LC_ 113
v_and_b32 v22, 0xfffffcff, v1
XS 134
XS 135
M 6,23,5
s_sub_u32 s39, 0, s39
RA 220,1,1024
AW 76,132
M 7,34,4
WL 4
LO 112,16,112
LU 16,112
AW 77,133
AW 78,134
M 7,35,5
RV 252,22,5120
HS 24,16
AW 79,135
v_min_u32 v35, v35, v13
M 8,46,4
WL 1
LL 18,80
LL 106,81
XS 136
M 8,47,5
LA 18
RA 224,1,2048
XS 137
XS 138
WL 2
Z 0,12,0
LC_ 106
RA 228,1,3072
XS 139
AW 80,136
M 0,13,1
ds_read_b64 v[14:15], v5 offset:0
AW 81,137
AW 82,138
Z 1,24,0
LO 113,17,113
LU 17,113
XL 71,704
AW 83,139
M 1,25,1
s_mul_i32 s53, s50, 25600
RA 232,1,4096
HS 24,17
XS 140
XS 141
Z 2,36,0
s_add_u32 s40, %[wlo], s53
LL 19,82
LL 107,83
XS 142
XS 143
M 2,37,1
s_addc_u32 s41, %[whi], 0
LA 19
v_subrev_u32 v5, s39, v5
AW 84,140
Z 3,48,0
WL 3
LC_ 107
HX 24
AW 85,141
AW 86,142
M 3,49,1
s_mov_b32 m0, s38
RA 236,1,6144
AW 87,143
XS 144
M 0,14,2
LO 106,18,106
LU 18,106
XS 145
M 0,15,3
W1 24,0
HZ 25,18
XS 146
XS 147
M 1,26,2
LL 16,84
LL 110,85
AW 88,144
AW 89,145
M 1,27,3
LA 16
RA 216,1,7168
AW 90,146
AW 91,147
M 2,38,2
LC_ 110
DW 0
XS 148
M 2,39,3
WL 8
DW 1024
v_add_f32 v14, v14, v15
s_nop 1
v_add_f32_dpp v14, v14, v14 quad_perm:[1,0,3,2] row_mask:0xf bank_mask:0xf
XS 149
XS 150
M 3,50,2
WL 4
LO 107,19,107
LU 19,107
v_add_f32_dpp v14, v14, v14 quad_perm:[2,3,0,1] row_mask:0xf bank_mask:0xf
XS 151
AW 92,148
M 3,51,3
s_add_u32 s42, s44, s60
RA 220,1,8192
HS 25,19
AW 93,149
M 0,16,4
s_addc_u32 s43, s45, 0
LL 17,86
LL 111,87
v_add_f32_dpp v14, v14, v14 row_half_mirror row_mask:0xf bank_mask:0xf
AW 94,150
AW 95,151
M 0,17,5
LA 17
ST 104
XS 152
XS 153
M 1,28,4
LC_ 111
DW 2048
v_cvt_f64_f32 v[14:15], v14
XS 154
M 1,29,5
RA 224,1,9216
XS 155
AW 96,152
M 2,40,4
WL 4
LO 110,16,110
LU 16,110
s_mov_b64 exec, s[54:55]
SP
s_mov_b64 exec, -1
AW 97,153
AW 98,154
M 2,41,5
WV 27
HS 25,16
AW 99,155
M 3,52,4
WL 3
LL 18,88
LL 114,89
XS 156
XS 157
M 3,53,5
s_add_u32 s40, s40, 3072
LA 18
RA 228,1,10240
XS 158
XS 159
M 0,18,0
s_addc_u32 s41, s41, 0
LC_ 114
AW 100,156
AW 101,157
M 0,19,1
s_add_u32 m0, s38, 3072
BW 60
AW 102,158
M 1,30,0
WL 5
LO 111,17,111
LU 17,111
AW 103,159
XS 160
M 1,31,1
s_add_u32 s42, s44, s61
RA 232,1,11264
HS 25,17
XS 161
XS 162
M 2,42,0
s_addc_u32 s43, s45, 0
LL 19,90
LL 115,91
XS 163
M 2,43,1
LA 19
RV 252,22,5392
AW 104,160
AW 105,161
M 3,54,0
WL 5
LC_ 115
ST 108
AW 106,162
AW 107,163
M 3,55,1
RA 236,1,12288
RV 40,8,6144
XS 164
M 0,20,2
WL 7
LO 114,18,114
LU 18,114
XS 165
RV 44,8,6208
XS 166
M 0,21,3
DW 0
HS 25,18
XS 167
RV 48,8,6272
AW 108,164
M 1,32,2
DW 1024
BR 60
AW 109,165
AW 110,166
M 1,33,3
RA 216,1,13312
AW 111,167
M 2,44,2
WV 29
BW 61
XS 168
XS 169
M 2,45,3
s_add_u32 s42, s44, s62
BR 61
XS 170
XS 171
M 3,56,2
WL 3
LO 115,19,115
LU 19,115
AW 112,168
M 3,57,3
s_addc_u32 s43, s45, 0
RA 220,1,14336
HS 25,19
AW 113,169
AW 114,170
LZ 40,60
WV 28
BW 62
M 0,22,4
DW 2048
RV 40,8,6336
AW 115,171
XS 172
M 0,23,5
ST 112
BR 62
XS 173
M 1,34,4
HX 25
XS 174
XS 175
M 1,35,5
WL 4
RA 224,1,15360
AW 116,172
AW 117,173
LS 44,61
WV 29
BW 63
M 2,46,4
LL 16,56
LL 104,57
AW 118,174
AW 119,175
M 2,47,5
LA 16
W1 25,1024
M 3,58,4
WL 7
LC_ 104
DQ 3072
RV 44,8,6400
M 3,59,5
WV 29
RA 228,1,16384
BR 63
Z 6,12,0
WL 8
v_add_u32 v4, s39, v4
M 6,13,1
s_add_u32 s50, s50, %[d]
LS 48,62
s_cmp_eq_u32 s50, %[wrap]
BW 64
Z 7,24,0
s_cselect_b32 s50, %[rst], s50
LL 17,58
LL 105,59
M 7,25,1
LA 17
RA 232,1,17408
RV 48,8,6464
Z 8,36,0
LC_ 105
BR 64
M 8,37,1
WV 28
Z 9,48,0
WL 6
LO 104,16,104
LU 16,104
M 9,49,1
s_add_u32 s38, s38, 32768
RA 236,1,18432
HZ 24,16
LS 40,63
s_cmp_eq_u32 s38, s77
BW 65
M 6,14,2
s_cselect_b32 s38, s76, s38
LL 18,60
LL 108,61
RV 40,8,6528
M 6,15,3
s_add_u32 s37, s37, 32768
LA 18
BR 65
M 7,26,2
s_cmp_eq_u32 s37, s75
LC_ 108
M 7,27,3
s_cselect_b32 s37, s74, s37
RA 216,1,19456
WL 7
LS 44,64
WV 16
BW 66
M 8,38,2
WV 15
LO 105,17,105
LU 17,105
M 8,39,3
s_add_u32 s53, %[xo0], 393216
RV 44,8,6592
HS 24,17
M 9,50,2
WL 4
LL 19,62
LL 109,63
M 9,51,3
s_add_u32 s46, %[xlo], s53
LA 19
RA 220,1,20480
BR 66
M 6,16,4
s_addc_u32 s47, %[xhi], 0
)"
R"(LC_ 109
M 6,17,5
s_bitcmp1_b64 s[90:91], 48
LS 48,65
s_cselect_b32 s58, 16, 0
BW 67
M 7,28,4
WL 8
LO 108,18,108
LU 18,108
s_lshr_b32 s58, 0xda43cb52, s58
v_lshrrev_b32 v3, 6, v0
v_and_b32 v3, 12, v3
v_bfe_u32 v3, s58, v3, 4
v_mad_u32_u24 v3, v3, s97, v21
M 7,29,5
RA 224,1,21504
HS 24,18
RV 48,8,6656
M 8,40,4
WV 14
LL 16,64
LL 112,65
BR 67
M 8,41,5
WL 5
LA 16
XL 72,0
LS 40,66
s_mul_i32 s53, s51, 49152
BW 68
M 9,52,4
LC_ 112
RV 40,8,6720
M 9,53,5
s_bitcmp1_b64 s[90:91], s51
RA 228,1,22528
BR 68
M 6,18,0
WL 10
LO 109,19,109
LU 19,109
M 6,19,1
s_cselect_b64 s[44:45], s[84:85], s[82:83]
HS 24,19
s_add_u32 s44, s44, s53
s_addc_u32 s45, s45, 0
s_add_u32 s51, s51, %[d]
s_cmp_eq_u32 s51, %[wrap]
s_cselect_b32 s51, %[rst], s51
M 7,30,0
LL 17,66
LL 113,67
M 7,31,1
WL 6
LA 17
RA 232,1,23552
LS 44,67
WV 14
BW 69
M 8,42,0
WV 13
LC_ 113
XL 73,64
M 8,43,1
s_lshl_b32 s53, s52, 8
RV 44,8,6784
BR 69
M 9,54,0
WL 6
LO 112,16,112
LU 16,112
M 9,55,1
s_add_u32 s48, %[alo], s53
RA 236,1,24576
HS 24,16
LS 48,68
s_addc_u32 s49, %[ahi], 0
BW 70
M 6,20,2
s_add_u32 s52, s52, %[d]
LL 18,68
LL 116,69
s_cmp_eq_u32 s52, %[wrap]
s_cselect_b32 s52, %[rst], s52
M 6,21,3
LA 18
XL 74,128
M 7,32,2
LC_ 116
XL 75,192
RV 48,8,6848
M 7,33,3
RA 240,7,0
BR 70
M 8,44,2
WL 9
LO 113,17,113
LU 17,113
M 8,45,3
WL 7
HS 24,17
v_add_u32 v1, s37, v0
LS 40,69
WV 15
BW 71
M 9,56,2
WL 7
LL 19,70
LL 117,71
M 9,57,3
LA 19
RA 216,1,0
M 6,22,4
LC_ 117
XL 76,256
M 6,23,5
s_barrier
RA 244,7,256
BR 71
M 7,34,4
WL 6
LO 116,18,116
LU 18,116
v_and_b32 v22, 0xfffffcff, v1
M 7,35,5
s_sub_u32 s39, 0, s39
RA 220,1,1024
HS 24,18
LS 44,70
RA 248,7,512
M 8,46,4
LL 16,80
LL 106,81
M 8,47,5
LA 16
RV 252,22,5120
M 9,58,4
WL 3
LC_ 106
RA 224,1,2048
M 9,59,5
RA 228,1,3072
WL 3
Z 0,12,0
LO 117,19,117
LU 19,117
M 0,13,1
ds_read_b64 v[14:15], v5 offset:0
HS 24,19
LS 48,71
RA 252,7,768
s_nop 6
v_mul_f32 v36, 0xbb2aaaab, v36
v_mul_f32 v37, 0xbb2aaaab, v37
v_mul_f32 v38, 0xbb2aaaab, v38
Z 1,24,0
LL 17,82
LL 107,83
XL 77,320
v_mul_f32 v39, 0xbb2aaaab, v39
M 1,25,1
s_mul_i32 s53, s50, 25600
LA 17
RA 232,1,4096
LK 40,240,60,0
LK 44,242,60,2
s_add_u32 s40, %[wlo], s53
RA 240,7,1024
v_subrev_u32 v5, s39, v5
LK 48,244,61,0
CF 128,40,41
LK 248,246,61,2
s_addc_u32 s41, %[whi], 0
RA 244,7,1280
CF 129,42,43
Z 2,36,0
s_mov_b32 m0, s38
LC_ 107
HX 24
M 2,37,1
W1 24,0
CF 130,44,45
Z 3,48,0
WL 8
LO 106,16,106
LU 16,106
M 3,49,1
RA 236,1,6144
HZ 25,16
CF 131,46,47
LK 40,248,62,0
CF 132,48,49
LK 44,250,62,2
RA 248,7,1536
CF 133,50,51
GZ 0
WL 8
CF 134,248,249
CF 135,250,251
LK 48,252,63,0
CF 136,40,41
LK 248,254,63,2
RA 252,7,1792
CF 137,42,43
GS 1
WL 6
CF 138,44,45
CF 139,46,47
LK 40,240,64,0
CF 140,48,49
LK 44,242,64,2
RA 240,7,2048
CF 141,50,51
GS 2
CF 142,248,249
CF 143,250,251
M 0,14,2
LL 18,84
LL 110,85
M 0,15,3
LA 18
DW 0
M 1,26,2
WL 7
LC_ 110
M 1,27,3
s_add_u32 s42, s44, s60
RA 216,1,7168
v_add_f32 v14, v14, v15
LK 48,244,65,0
CF 144,40,41
LK 248,246,65,2
s_addc_u32 s43, s45, 0
RA 244,7,2304
CF 145,42,43
GS 3
CF 146,44,45
CF 147,46,47
M 2,38,2
WL 9
LO 107,17,107
LU 17,107
v_add_f32_dpp v14, v14, v14 quad_perm:[1,0,3,2] row_mask:0xf bank_mask:0xf
M 2,39,3
DW 1024
HS 25,17
M 3,50,2
WL 7
LL 19,86
LL 111,87
v_add_f32_dpp v14, v14, v14 quad_perm:[2,3,0,1] row_mask:0xf bank_mask:0xf
M 3,51,3
WL 6
LA 19
RA 220,1,8192
LK 40,248,66,0
CF 148,48,49
LK 44,250,66,2
RA 248,7,2560
CF 149,50,51
GS 4
CF 150,248,249
CF 151,250,251
M 0,16,4
LC_ 111
ST 104
v_add_f32_dpp v14, v14, v14 row_half_mirror row_mask:0xf bank_mask:0xf
M 0,17,5
v_cvt_f64_f32 v[14:15], v14
M 1,28,4
WL 6
LO 110,18,110
LU 18,110
M 1,29,5
RA 224,1,9216
HS 25,18
LK 48,252,67,0
CF 152,40,41
LK 248,254,67,2
RA 252,7,2816
CF 153,42,43
GS 5
CF 154,44,45
CF 155,46,47
M 2,40,4
LL 16,88
LL 114,89
s_mov_b64 exec, s[54:55]
SP
s_mov_b64 exec, -1
M 2,41,5
LA 16
DW 2048
M 3,52,4
WL 5
LC_ 114
M 3,53,5
s_add_u32 s42, s44, s61
RA 228,1,10240
LK 40,240,68,0
CF 156,48,49
LK 44,242,68,2
s_addc_u32 s43, s45, 0
CF 157,50,51
GS 6
CF 158,248,249
CF 159,250,251
M 0,18,0
WL 5
LO 111,19,111
LU 19,111
M 0,19,1
s_add_u32 s40, s40, 3072
HS 25,19
LK 48,244,69,0
CF 160,40,41
LK 248,246,69,2
s_addc_u32 s41, s41, 0
CF 161,42,43
GS 7
s_add_u32 m0, s38, 3072
CF 162,44,45
CF 163,46,47
M 1,30,0
LL 17,90
LL 115,91
M 1,31,1
LA 17
RA 232,1,11264
M 2,42,0
LC_ 115
RV 252,22,5392
M 2,43,1
ST 108
LK 40,248,70,0
CF 164,48,49
LK 44,250,70,2
CF 165,50,51
GS 8
CF 166,248,249
CF 167,250,251
M 3,54,0
WL 4
LO 114,16,114
LU 16,114
M 3,55,1
RA 236,1,12288
HS 25,16
M 0,20,2
LL 18,92
LL 118,93
M 0,21,3
LA 18
DW 0
LK 48,252,71,0
CF 168,40,41
LK 248,254,71,2
CF 169,42,43
GS 9
CF 170,44,45
CF 171,46,47
M 1,32,2
LC_ 118
RV 36,8,6912
M 1,33,3
RA 216,1,13312
M 2,44,2
WL 6
LO 115,17,115
LU 17,115
M 2,45,3
DW 1024
HS 25,17
GS 10
s_add_u32 s42, s44, s62
CF 172,48,49
CF 173,50,51
M 3,56,2
WL 4
LL 19,94
LL 119,95
M 3,57,3
s_addc_u32 s43, s45, 0
LA 19
RA 220,1,14336
M 0,22,4
LC_ 119
ST 112
M 0,23,5
DW 2048
CF 174,248,249
M 1,34,4
WL 5
LO 118,18,118
LU 18,118
M 1,35,5
RA 224,1,15360
HS 25,18
M 2,46,4
)"
R"(LL 16,56
LL 104,57
M 2,47,5
LA 16
CF 175,250,251
M 3,58,4
WL 3
LC_ 104
DQ 3072
M 3,59,5
s_add_u32 s42, s44, s63
RA 228,1,16384
GS 11
s_addc_u32 s43, s45, 0
Z 6,12,0
WL 4
LO 119,19,119
LU 19,119
s_nop 2
v_cndmask_b32 v52, v52, v53, s[78:79]
M 6,13,1
ST 116
HS 25,19
Z 7,24,0
LL 17,58
LL 105,59
v_cndmask_b32 v54, v54, v55, s[78:79]
M 7,25,1
LA 17
RA 232,1,17408
v_cndmask_b32 v52, v52, v54, s[80:81]
Z 8,36,0
s_add_u32 s50, s50, %[d]
LC_ 105
HX 25
v_mul_f32 v52, 0x3b2aaaab, v52
M 8,37,1
s_cmp_eq_u32 s50, %[wrap]
W1 25,1024
C 52,52,52
Z 9,48,0
WL 4
LO 104,16,104
LU 16,104
v_lshlrev_b32 v52, 16, v52
M 9,49,1
s_cselect_b32 s50, %[rst], s50
RA 236,1,18432
HZ 24,16
v_add_f32 v52, v36, v52
M 6,14,2
LL 18,60
LL 108,61
C 52,52,52
M 6,15,3
s_add_u32 s37, s37, 32768
LA 18
v_add_u32 v4, s39, v4
v_lshlrev_b32 v52, 16, v52
M 7,26,2
s_cmp_eq_u32 s37, s75
LC_ 108
v_sqrt_f32 v52, v52
M 7,27,3
s_cselect_b32 s37, s74, s37
RA 216,1,19456
C 52,52,52
M 8,38,2
WL 5
LO 105,17,105
LU 17,105
v_lshlrev_b32 v52, 16, v52
M 8,39,3
s_add_u32 s38, s38, 32768
HS 24,17
v_cmp_ge_f32 vcc, v37, v52
M 9,50,2
WL 3
LL 19,62
LL 109,63
v_rcp_f32 v52, v52
M 9,51,3
s_cmp_eq_u32 s38, s77
LA 19
RA 220,1,20480
v_mul_f32 v52, v38, v52
M 6,16,4
s_cselect_b32 s38, s76, s38
LC_ 109
XL 78,384
M 6,17,5
v_cndmask_b32 v52, 0, v52, vcc
CF 52,52,52
M 7,28,4
WL 4
LO 108,18,108
LU 18,108
M 7,29,5
s_mul_i32 s53, s51, 49152
RA 224,1,21504
HS 24,18
ds_bpermute_b32 v13, v28, v52
M 8,40,4
s_bitcmp1_b64 s[90:91], s51
LL 16,64
LL 112,65
M 8,41,5
s_cselect_b64 s[44:45], s[84:85], s[82:83]
LA 16
M 9,52,4
WL 4
LC_ 112
M 9,53,5
s_add_u32 s44, s44, s53
RA 228,1,22528
M 6,18,0
WL 5
LO 109,19,109
LU 19,109
M 6,19,1
s_addc_u32 s45, s45, 0
HS 24,19
s_add_u32 s51, s51, %[d]
s_cmp_eq_u32 s51, %[wrap]
s_cselect_b32 s51, %[rst], s51
M 7,30,0
LL 17,66
LL 113,67
M 7,31,1
WL 3
LA 17
RA 232,1,23552
XL 79,448
M 8,42,0
s_lshl_b32 s53, s52, 8
LC_ 113
XS 128
M 8,43,1
s_add_u32 s48, %[alo], s53
XS 129
XS 130
M 9,54,0
WL 3
LO 112,16,112
LU 16,112
M 9,55,1
s_addc_u32 s49, %[ahi], 0
RA 236,1,24576
HS 24,16
XS 131
M 6,20,2
s_add_u32 s52, s52, %[d]
LL 18,68
LL 116,69
s_cmp_eq_u32 s52, %[wrap]
s_cselect_b32 s52, %[rst], s52
AW 120,128
M 6,21,3
LA 18
XL 80,512
AW 121,129
AW 122,130
M 7,32,2
LC_ 116
XL 81,576
AW 123,131
M 7,33,3
v_add_u32 v1, s37, v0
XS 132
M 8,44,2
WL 3
LO 113,17,113
LU 17,113
XS 133
M 8,45,3
RA 216,1,0
HS 24,17
XS 134
XS 135
M 9,56,2
WL 3
LL 19,70
LL 117,71
AW 124,132
M 9,57,3
WV 22
LA 19
XL 82,640
AW 125,133
M 6,22,4
s_barrier
LC_ 117
v_and_b32 v22, 0xfffffcff, v1
AW 126,134
M 6,23,5
s_sub_u32 s39, 0, s39
RA 220,1,1024
AW 127,135
v_min_u32 v35, v35, v13
M 7,34,4
WL 4
LO 116,18,116
LU 18,116
XS 136
M 7,35,5
RV 252,22,5120
HS 24,18
XS 137
M 8,46,4
LL 16,80
LL 106,81
XS 138
XS 139
M 8,47,5
LA 16
RA 224,1,2048
AW 128,136
M 9,58,4
WL 3
LC_ 106
ds_read_b64 v[14:15], v5 offset:0
AW 129,137
M 9,59,5
WL 4
RA 228,1,3072
v_subrev_u32 v5, s39, v5
AW 130,138
Z 0,12,0
LO 117,19,117
LU 19,117
AW 131,139
XS 140
M 0,13,1
XL 83,704
HS 24,19
XS 141
Z 1,24,0
s_mul_i32 s53, s50, 25600
LL 17,82
LL 107,83
XS 142
M 1,25,1
s_add_u32 s40, %[wlo], s53
LA 17
RA 232,1,4096
XS 143
Z 2,36,0
s_addc_u32 s41, %[whi], 0
LC_ 107
HX 24
AW 132,140
AW 133,141
M 2,37,1
s_mov_b32 m0, s38
W1 24,0
AW 134,142
Z 3,48,0
WL 6
LO 106,16,106
LU 16,106
AW 135,143
M 3,49,1
HZ 25,16
XS 144
Z 4,60,0
WL 4
LL 18,84
LL 110,85
XS 145
XS 146
M 4,61,1
LA 18
RA 236,1,6144
XS 147
M 0,14,2
LC_ 110
v_add_f32 v14, v14, v15
AW 136,144
M 0,15,3
s_add_u32 s42, s44, s60
v_add_f32_dpp v14, v14, v14 quad_perm:[1,0,3,2] row_mask:0xf bank_mask:0xf
AW 137,145
AW 138,146
M 1,26,2
WL 4
LO 107,17,107
LU 17,107
AW 139,147
M 1,27,3
s_addc_u32 s43, s45, 0
RA 216,1,7168
HS 25,17
XS 148
M 2,38,2
LL 19,86
LL 111,87
XS 149
M 2,39,3
LA 19
DW 0
XS 150
XS 151
M 3,50,2
LC_ 111
ST 104
v_add_f32_dpp v14, v14, v14 quad_perm:[2,3,0,1] row_mask:0xf bank_mask:0xf
AW 140,148
M 3,51,3
v_add_f32_dpp v14, v14, v14 row_half_mirror row_mask:0xf bank_mask:0xf
AW 141,149
M 4,62,2
WL 3
LO 110,18,110
LU 18,110
AW 142,150
M 4,63,3
RA 220,1,8192
HS 25,18
AW 143,151
XS 152
M 0,16,4
LL 16,88
LL 114,89
v_cvt_f64_f32 v[14:15], v14
XS 153
M 0,17,5
LA 16
s_mov_b64 exec, s[54:55]
SP
s_mov_b64 exec, -1
XS 154
M 1,28,4
LC_ 114
XS 155
M 1,29,5
s_add_u32 s42, s44, s61
RA 224,1,9216
AW 144,152
AW 145,153
M 2,40,4
WL 4
LO 111,19,111
LU 19,111
AW 146,154
M 2,41,5
s_addc_u32 s43, s45, 0
DW 1024
HS 25,19
AW 147,155
M 3,52,4
LL 17,90
LL 115,91
XS 156
XS 157
M 3,53,5
LA 17
ST 108
XS 158
M 4,64,4
WL 4
LC_ 115
XS 159
M 4,65,5
RA 228,1,10240
AW 148,156
M 0,18,0
WL 4
LO 114,16,114
LU 16,114
AW 149,157
AW 150,158
M 0,19,1
HS 25,16
AW 151,159
M 1,30,0
LL 18,92
LL 118,93
XS 160
M 1,31,1
LA 18
RA 232,1,11264
XS 161
M 2,42,0
WV 28
LC_ 118
RV 252,22,5392
XS 162
XS 163
M 2,43,1
s_add_u32 s42, s44, s62
BW 72
AW 152,160
M 3,54,0
WL 6
LO 115,17,115
LU 17,115
AW 153,161
M 3,55,1
s_addc_u32 s43, s45, 0
DW 2048
)"
R"(HS 25,17
AW 154,162
M 4,66,0
WL 5
LL 19,94
LL 119,95
AW 155,163
RV 40,8,6144
XS 164
M 4,67,1
LA 19
RA 236,1,12288
XS 165
M 0,20,2
LC_ 119
ST 112
RV 44,8,6208
XS 166
M 0,21,3
RV 48,8,6272
BR 72
XS 167
AW 156,164
M 1,32,2
WL 9
LO 118,18,118
LU 18,118
AW 157,165
M 1,33,3
RA 216,1,13312
HS 25,18
AW 158,166
M 2,44,2
WV 29
BW 73
AW 159,167
M 2,45,3
BR 73
XS 168
XS 169
M 3,56,2
WV 28
XS 170
XS 171
M 3,57,3
s_add_u32 s40, s40, 3072
AW 160,168
AW 161,169
M 4,68,2
WL 3
LO 119,19,119
LU 19,119
M 4,69,3
s_addc_u32 s41, s41, 0
RA 220,1,14336
HS 25,19
AW 162,170
LZ 40,72
s_add_u32 m0, s38, 3072
BW 74
M 0,22,4
s_add_u32 s42, s44, s63
RV 40,8,6336
BR 74
AW 163,171
M 0,23,5
s_addc_u32 s43, s45, 0
ST 116
HX 25
XS 172
M 1,34,4
W1 25,1024
XS 173
M 1,35,5
WL 5
RA 224,1,15360
v_add_u32 v4, s39, v4
XS 174
XS 175
LS 44,73
WV 28
BW 75
M 2,46,4
LL 16,56
LL 104,57
AW 164,172
M 2,47,5
LA 16
RV 44,8,6400
AW 165,173
M 3,58,4
LC_ 104
BR 75
AW 166,174
AW 167,175
M 3,59,5
WL 7
LS 48,74
WV 27
BW 76
M 4,70,4
DW 0
RV 48,8,6464
M 4,71,5
RA 228,1,16384
BR 76
Z 6,12,0
LL 17,58
LL 105,59
M 6,13,1
LA 17
Z 7,24,0
LC_ 105
M 7,25,1
RA 232,1,17408
Z 8,36,0
WL 8
LO 104,16,104
LU 16,104
M 8,37,1
WL 7
HZ 24,16
LS 40,75
WV 27
BW 77
Z 9,48,0
LL 18,60
LL 108,61
M 9,49,1
LA 18
RV 40,8,6528
Z 10,60,0
WL 7
LC_ 108
BR 77
M 10,61,1
WL 8
RA 236,1,18432
LS 44,76
WV 14
BW 78
M 6,14,2
WL 8
LO 105,17,105
LU 17,105
M 6,15,3
DW 1024
HS 24,17
M 7,26,2
s_add_u32 s37, s37, 32768
LL 19,62
LL 109,63
M 7,27,3
s_cmp_eq_u32 s37, s75
LA 19
RA 216,1,19456
M 8,38,2
s_cselect_b32 s37, s74, s37
LC_ 109
RV 44,8,6592
M 8,39,3
BR 78
M 9,50,2
WL 8
LO 108,18,108
LU 18,108
M 9,51,3
WL 7
HS 24,18
LS 48,77
WV 14
BW 79
M 10,62,2
WL 7
LL 16,64
LL 112,65
M 10,63,3
LA 16
RA 220,1,20480
M 6,16,4
LC_ 112
RV 48,8,6656
M 6,17,5
WV 13
DW 2048
BR 79
M 7,28,4
WL 6
LO 109,19,109
LU 19,109
M 7,29,5
s_mul_i32 s53, s51, 49152
RA 224,1,21504
HS 24,19
LS 40,78
s_bitcmp1_b64 s[90:91], s51
BW 80
M 8,40,4
s_cselect_b64 s[44:45], s[84:85], s[82:83]
LL 17,66
LL 113,67
M 8,41,5
s_add_u32 s44, s44, s53
LA 17
RV 40,8,6720
M 9,52,4
s_addc_u32 s45, s45, 0
LC_ 113
BR 80
M 9,53,5
s_add_u32 s51, s51, %[d]
M 10,64,4
WL 8
LO 112,16,112
LU 16,112
M 10,65,5
s_cmp_eq_u32 s51, %[wrap]
RA 228,1,22528
HS 24,16
M 6,18,0
s_cselect_b32 s51, %[rst], s51
LL 18,68
LL 116,69
M 6,19,1
WL 7
LA 18
LS 44,79
WV 13
BW 81
M 7,30,0
LC_ 116
RV 44,8,6784
M 7,31,1
RA 232,1,23552
BR 81
M 8,42,0
WL 8
LO 113,17,113
LU 17,113
M 8,43,1
WL 7
DQ 3072
HS 24,17
LS 48,80
WV 13
BW 82
M 9,54,0
LL 19,70
LL 117,71
M 9,55,1
s_lshl_b32 s53, s52, 8
LA 19
RV 48,8,6848
M 10,66,0
WL 9
LC_ 117
RA 240,7,0
M 10,67,1
s_add_u32 s48, %[alo], s53
RA 236,1,24576
BR 82
M 6,20,2
WL 7
LO 116,18,116
LU 18,116
M 6,21,3
s_addc_u32 s49, %[ahi], 0
HS 24,18
v_add_u32 v1, s37, v0
M 7,32,2
s_add_u32 s52, s52, %[d]
LL 16,72
LL 120,73
M 7,33,3
s_cmp_eq_u32 s52, %[wrap]
LA 16
RA 216,1,0
LS 40,81
s_cselect_b32 s52, %[rst], s52
WV 12
BW 83
M 8,44,2
s_add_u32 s50, s50, %[d]
LC_ 120
RA 244,7,256
M 8,45,3
s_cmp_eq_u32 s50, %[wrap]
BR 83
v_and_b32 v22, 0xfffffcff, v1
M 9,56,2
WL 9
LO 117,19,117
LU 19,117
M 9,57,3
s_cselect_b32 s50, %[rst], s50
HS 24,19
M 10,68,2
WL 7
LL 17,74
LL 121,75
M 10,69,3
WL 6
LA 17
RA 220,1,1024
LS 44,82
RA 248,7,512
M 6,22,4
LC_ 121
RV 252,22,5120
M 6,23,5
M 7,34,4
WL 5
LO 120,16,120
LU 16,120
M 7,35,5
s_barrier
RA 224,1,2048
HS 24,16
LS 48,83
s_sub_u32 s39, 0, s39
RA 252,7,768
M 8,46,4
LL 18,80
LL 106,81
M 8,47,5
s_add_u32 s38, s38, 32768
LA 18
ds_read_b64 v[14:15], v5 offset:0
M 9,58,4
s_cmp_eq_u32 s38, s77
LC_ 106
v_subrev_u32 v5, s39, v5
M 9,59,5
s_cselect_b32 s38, s76, s38
v_mul_f32 v36, 0xbb2aaaab, v36
v_mul_f32 v37, 0xbb2aaaab, v37
M 10,70,4
WL 6
LO 121,17,121
LU 17,121
M 10,71,5
WL 5
RA 228,1,3072
HS 24,17
Z 0,12,0
LL 19,82
LL 107,83
M 0,13,1
s_mul_i32 s53, s50, 25600
LA 19
HX 24
Z 1,24,0
s_add_u32 s40, %[wlo], s53
LC_ 107
W1 24,0
v_mul_f32 v38, 0xbb2aaaab, v38
v_mul_f32 v39, 0xbb2aaaab, v39
M 1,25,1
s_addc_u32 s41, %[whi], 0
RA 232,1,4096
LK 40,240,72,0
LK 44,242,72,2
s_mov_b32 m0, s38
RA 240,7,1024
LK 48,244,73,0
s_nop 0
CF 128,40,41
LK 248,246,73,2
RA 244,7,1280
CF 129,42,43
Z 2,36,0
WL 7
LO 106,18,106
LU 18,106
M 2,37,1
HZ 25,18
v_add_f32 v14, v14, v15
Z 3,48,0
LL 16,84
LL 110,85
M 3,49,1
LA 16
DW 0
Z 4,60,0
WL 7
LC_ 110
v_add_f32_dpp v14, v14, v14 quad_perm:[1,0,3,2] row_mask:0xf bank_mask:0xf
CF 130,44,45
M 4,61,1
s_add_u32 s42, s44, s60
RA 236,1,6144
v_add_f32_dpp v14, v14, v14 quad_perm:[2,3,0,1] row_mask:0xf bank_mask:0xf
CF 131,46,47
LK 40,248,74,0
CF 132,48,49
LK 44,250,74,2
s_addc_u32 s43, s45, 0
RA 248,7,1536
CF 133,50,51
GZ 0
)"
R"(CF 134,248,249
CF 135,250,251
LK 48,252,75,0
CF 136,40,41
LK 248,254,75,2
RA 252,7,1792
CF 137,42,43
GS 1
WL 6
CF 138,44,45
CF 139,46,47
LK 40,240,76,0
CF 140,48,49
LK 44,242,76,2
RA 240,7,2048
CF 141,50,51
GS 2
CF 142,248,249
CF 143,250,251
M 0,14,2
LO 107,19,107
LU 19,107
M 0,15,3
ST 104
HS 25,19
M 1,26,2
LL 17,86
LL 111,87
M 1,27,3
WL 6
LA 17
RA 216,1,7168
LK 48,244,77,0
CF 144,40,41
LK 248,246,77,2
RA 244,7,2304
CF 145,42,43
GS 3
CF 146,44,45
CF 147,46,47
M 2,38,2
LC_ 111
DW 1024
M 2,39,3
v_add_f32_dpp v14, v14, v14 row_half_mirror row_mask:0xf bank_mask:0xf
M 3,50,2
WL 8
LO 110,16,110
LU 16,110
M 3,51,3
WL 6
HS 25,16
v_cvt_f64_f32 v[14:15], v14
LK 40,248,78,0
CF 148,48,49
LK 44,250,78,2
RA 248,7,2560
CF 149,50,51
GS 4
CF 150,248,249
CF 151,250,251
M 4,62,2
LL 18,88
LL 114,89
M 4,63,3
LA 18
RA 220,1,8192
M 0,16,4
LC_ 114
s_mov_b64 exec, s[54:55]
SP
s_mov_b64 exec, -1
M 0,17,5
DW 2048
M 1,28,4
WL 4
LO 111,17,111
LU 17,111
M 1,29,5
s_add_u32 s42, s44, s61
RA 224,1,9216
HS 25,17
LK 48,252,79,0
CF 152,40,41
LK 248,254,79,2
s_addc_u32 s43, s45, 0
RA 252,7,2816
CF 153,42,43
GS 5
s_add_u32 s40, s40, 3072
CF 154,44,45
CF 155,46,47
M 2,40,4
s_addc_u32 s41, s41, 0
LL 19,90
LL 115,91
M 2,41,5
s_add_u32 m0, s38, 3072
LA 19
ST 108
M 3,52,4
LC_ 115
M 3,53,5
LK 40,240,80,0
CF 156,48,49
LK 44,242,80,2
CF 157,50,51
GS 6
CF 158,248,249
CF 159,250,251
M 4,64,4
WL 4
LO 114,18,114
LU 18,114
M 4,65,5
RA 228,1,10240
HS 25,18
M 0,18,0
LL 16,92
LL 118,93
M 0,19,1
LA 16
DW 0
LK 48,244,81,0
CF 160,40,41
LK 248,246,81,2
CF 161,42,43
GS 7
CF 162,44,45
CF 163,46,47
M 1,30,0
LC_ 118
M 1,31,1
s_add_u32 s42, s44, s62
RA 232,1,11264
M 2,42,0
WL 4
LO 115,19,115
LU 19,115
M 2,43,1
s_addc_u32 s43, s45, 0
RV 252,22,5392
HS 25,19
M 3,54,0
LL 17,94
LL 119,95
M 3,55,1
LA 17
DW 1024
LK 40,248,82,0
CF 164,48,49
LK 44,250,82,2
CF 165,50,51
GS 8
CF 166,248,249
CF 167,250,251
M 4,66,0
WL 5
LC_ 119
ST 112
M 4,67,1
RA 236,1,12288
M 0,20,2
WL 5
LO 118,16,118
LU 16,118
M 0,21,3
DW 2048
HS 25,16
LK 48,252,83,0
CF 168,40,41
LK 248,254,83,2
CF 169,42,43
GS 9
CF 170,44,45
CF 171,46,47
M 1,32,2
LL 18,96
LL 122,97
M 1,33,3
LA 18
RA 216,1,13312
M 2,44,2
LC_ 122
RV 36,8,6912
M 2,45,3
DQ 3072
GS 10
s_add_u32 s42, s44, s63
CF 172,48,49
M 3,56,2
WL 5
LO 119,17,119
LU 17,119
M 3,57,3
s_addc_u32 s43, s45, 0
ST 116
HS 25,17
M 4,68,2
WL 4
LL 19,98
LL 123,99
M 4,69,3
LA 19
RA 220,1,14336
M 0,22,4
s_add_u32 s50, s50, %[d]
LC_ 123
CF 173,50,51
M 0,23,5
s_cmp_eq_u32 s50, %[wrap]
CF 174,248,249
CF 175,250,251
M 1,34,4
WL 4
LO 122,18,122
LU 18,122
M 1,35,5
s_cselect_b32 s50, %[rst], s50
RA 224,1,15360
HS 25,18
GS 11
M 2,46,4
LL 16,56
LL 104,57
M 2,47,5
LA 16
s_nop 2
v_cndmask_b32 v52, v52, v53, s[78:79]
M 3,58,4
s_add_u32 s38, s38, 32768
LC_ 104
v_cndmask_b32 v54, v54, v55, s[78:79]
M 3,59,5
s_cmp_eq_u32 s38, s77
v_cndmask_b32 v52, v52, v54, s[80:81]
v_mul_f32 v52, 0x3b2aaaab, v52
M 4,70,4
WL 3
LO 123,19,123
LU 19,123
M 4,71,5
s_cselect_b32 s38, s76, s38
RA 228,1,16384
HS 25,19
Z 6,12,0
s_add_u32 s42, s44, s64
LL 17,58
LL 105,59
M 6,13,1
s_addc_u32 s43, s45, 0
LA 17
ST 120
Z 7,24,0
s_mul_i32 s53, s50, 25600
LC_ 105
HX 25
M 7,25,1
s_add_u32 s40, %[wlo], s53
RA 232,1,17408
C 52,52,52
Z 8,36,0
WL 4
LO 104,16,104
LU 16,104
M 8,37,1
s_addc_u32 s41, %[whi], 0
W1 25,1024
HZ 24,16
Z 9,48,0
s_mov_b32 m0, s38
LL 18,60
LL 108,61
M 9,49,1
LA 18
DW 0
v_lshlrev_b32 v52, 16, v52
Z 10,60,0
WL 5
LC_ 108
v_add_u32 v4, s39, v4
v_add_f32 v52, v36, v52
M 10,61,1
RA 236,1,18432
C 52,52,52
M 6,14,2
WL 5
LO 105,17,105
LU 17,105
M 6,15,3
s_add_u32 s37, s37, 32768
HS 24,17
v_lshlrev_b32 v52, 16, v52
M 7,26,2
s_cmp_eq_u32 s37, s75
LL 19,62
LL 109,63
v_sqrt_f32 v52, v52
M 7,27,3
s_cselect_b32 s37, s74, s37
LA 19
RA 216,1,19456
M 8,38,2
LC_ 109
DW 1024
C 52,52,52
M 8,39,3
v_lshlrev_b32 v52, 16, v52
v_cmp_ge_f32 vcc, v37, v52
M 9,50,2
WL 4
LO 108,18,108
LU 18,108
M 9,51,3
DW 2048
HS 24,18
M 10,62,2
WL 3
LL 16,64
LL 112,65
v_rcp_f32 v52, v52
M 10,63,3
LA 16
RA 220,1,20480
v_mul_f32 v52, v38, v52
M 6,16,4
LC_ 112
v_cndmask_b32 v52, 0, v52, vcc
M 6,17,5
CF 52,52,52
M 7,28,4
WL 3
LO 109,19,109
LU 19,109
M 7,29,5
s_mul_i32 s53, s51, 49152
RA 224,1,21504
HS 24,19
ds_bpermute_b32 v13, v28, v52
M 8,40,4
s_bitcmp1_b64 s[90:91], s51
LL 17,66
LL 113,67
M 8,41,5
s_cselect_b64 s[44:45], s[84:85], s[82:83]
LA 17
M 9,52,4
s_add_u32 s44, s44, s53
LC_ 113
M 9,53,5
s_addc_u32 s45, s45, 0
M 10,64,4
WL 4
LO 112,16,112
LU 16,112
M 10,65,5
s_add_u32 s51, s51, %[d]
RA 228,1,22528
HS 24,16
M 6,18,0
s_cmp_eq_u32 s51, %[wrap]
LL 18,68
LL 116,69
M 6,19,1
s_cselect_b32 s51, %[rst], s51
LA 18
s_add_u32 s40, s40, 3072
s_addc_u32 s41, s41, 0
s_add_u32 m0, s38, 3072
M 7,30,0
LC_ 116
DW 0
M 7,31,1
WL 5
RA 232,1,23552
XS 128
M 8,42,0
WL 4
LO 113,17,113
LU 17,113
XS 129
M 8,43,1
DW 1024
HS 24,17
XS 130
M 9,54,0
LL 19,70
LL 117,71
XS 131
M 9,55,1
s_lshl_b32 s53, s52, 8
LA 19
AW 168,128
M 10,66,0
WL 4
LC_ 117
AW 169,129
M 10,67,1
s_add_u32 s48, %[alo], s53
RA 236,1,24576
)"
R"(AW 170,130
M 6,20,2
WL 4
LO 116,18,116
LU 18,116
AW 171,131
M 6,21,3
s_addc_u32 s49, %[ahi], 0
HS 24,18
v_add_u32 v1, s37, v0
XS 132
M 7,32,2
s_add_u32 s52, s52, %[d]
LL 16,72
LL 120,73
XS 133
M 7,33,3
s_cmp_eq_u32 s52, %[wrap]
LA 16
RA 216,1,0
XS 134
M 8,44,2
s_cselect_b32 s52, %[rst], s52
LC_ 120
DW 2048
XS 135
M 8,45,3
DQ 3072
v_and_b32 v22, 0xfffffcff, v1
AW 172,132
M 9,56,2
WL 4
LO 117,19,117
LU 19,117
AW 173,133
M 9,57,3
HS 24,19
AW 174,134
M 10,68,2
WL 3
LL 17,74
LL 121,75
AW 175,135
M 10,69,3
WV 20
LA 17
RA 220,1,1024
v_min_u32 v35, v35, v13
M 6,22,4
s_add_u32 s50, s50, %[d]
LC_ 121
RV 252,22,5120
XS 136
M 6,23,5
s_cmp_eq_u32 s50, %[wrap]
XS 137
XS 138
M 7,34,4
WL 4
LO 120,16,120
LU 16,120
M 7,35,5
s_cselect_b32 s50, %[rst], s50
RA 224,1,2048
HS 24,16
XS 139
M 8,46,4
s_barrier
LL 18,80
LL 106,81
AW 176,136
M 8,47,5
s_sub_u32 s39, 0, s39
LA 18
ds_read_b64 v[14:15], v5 offset:0
AW 177,137
M 9,58,4
s_add_u32 s38, s38, 32768
LC_ 106
v_subrev_u32 v5, s39, v5
AW 178,138
M 9,59,5
s_cmp_eq_u32 s38, s77
AW 179,139
XS 140
M 10,70,4
WL 4
LO 121,17,121
LU 17,121
M 10,71,5
s_cselect_b32 s38, s76, s38
RA 228,1,3072
HS 24,17
XS 141
Z 0,12,0
LL 19,82
LL 107,83
XS 142
M 0,13,1
LA 19
HX 24
XS 143
Z 1,24,0
LC_ 107
W1 24,0
AW 180,140
M 1,25,1
RA 232,1,4096
AW 181,141
Z 2,36,0
WL 5
LO 106,18,106
LU 18,106
AW 182,142
M 2,37,1
HZ 25,18
v_add_f32 v14, v14, v15
AW 183,143
Z 3,48,0
LL 16,84
LL 110,85
XS 144
M 3,49,1
LA 16
v_add_f32_dpp v14, v14, v14 quad_perm:[1,0,3,2] row_mask:0xf bank_mask:0xf
XS 145
Z 4,60,0
LC_ 110
v_add_f32_dpp v14, v14, v14 quad_perm:[2,3,0,1] row_mask:0xf bank_mask:0xf
XS 146
M 4,61,1
s_add_u32 s42, s44, s60
RA 236,1,6144
v_add_f32_dpp v14, v14, v14 row_half_mirror row_mask:0xf bank_mask:0xf
XS 147
Z 5,72,0
WL 5
LO 107,19,107
LU 19,107
AW 184,144
M 5,73,1
s_addc_u32 s43, s45, 0
ST 104
HS 25,19
AW 185,145
M 0,14,2
LL 17,86
LL 111,87
AW 186,146
M 0,15,3
LA 17
v_cvt_f64_f32 v[14:15], v14
AW 187,147
M 1,26,2
LC_ 111
s_mov_b64 exec, s[54:55]
SP
s_mov_b64 exec, -1
XS 148
M 1,27,3
RA 216,1,7168
XS 149
M 2,38,2
WL 4
LO 110,16,110
LU 16,110
XS 150
M 2,39,3
HS 25,16
XS 151
M 3,50,2
LL 18,88
LL 114,89
AW 188,148
M 3,51,3
LA 18
AW 189,149
M 4,62,2
LC_ 114
AW 190,150
M 4,63,3
s_add_u32 s42, s44, s61
RA 220,1,8192
AW 191,151
M 5,74,2
WL 4
LO 111,17,111
LU 17,111
XS 152
M 5,75,3
s_addc_u32 s43, s45, 0
HS 25,17
XS 153
M 0,16,4
s_mov_b32 m0, %[pm]
LL 19,90
LL 115,91
XS 154
M 0,17,5
LA 19
DF
XS 155
M 1,28,4
LC_ 115
ST 108
AW 192,152
M 1,29,5
RA 224,1,9216
AW 193,153
M 2,40,4
WL 4
LO 114,18,114
LU 18,114
AW 194,154
M 2,41,5
HS 25,18
AW 195,155
M 3,52,4
LL 16,92
LL 118,93
XS 156
M 3,53,5
LA 16
XS 157
M 4,64,4
LC_ 118
XS 158
M 4,65,5
s_add_u32 s42, s44, s62
RA 228,1,10240
XS 159
M 5,76,4
WL 4
LO 115,19,115
LU 19,115
AW 196,156
M 5,77,5
s_addc_u32 s43, s45, 0
ST 112
HS 25,19
AW 197,157
M 0,18,0
LL 17,94
LL 119,95
AW 198,158
M 0,19,1
LA 17
AW 199,159
M 1,30,0
LC_ 119
XS 160
M 1,31,1
RA 232,1,11264
XS 161
M 2,42,0
WL 4
LO 118,16,118
LU 16,118
XS 162
M 2,43,1
RV 252,22,5392
HS 25,16
XS 163
M 3,54,0
LL 18,96
LL 122,97
AW 200,160
M 3,55,1
LA 18
AW 201,161
M 4,66,0
LC_ 122
AW 202,162
M 4,67,1
s_add_u32 s42, s44, s63
RA 236,1,12288
AW 203,163
M 5,78,0
WL 5
LO 119,17,119
LU 17,119
XS 164
M 5,79,1
s_addc_u32 s43, s45, 0
ST 116
HS 25,17
XS 165
M 0,20,2
LL 19,98
LL 123,99
XS 166
M 0,21,3
LA 19
XS 167
M 1,32,2
LC_ 123
AW 204,164
M 1,33,3
RA 216,1,13312
AW 205,165
M 2,44,2
WL 4
LO 122,18,122
LU 18,122
AW 206,166
M 2,45,3
HS 25,18
AW 207,167
M 3,56,2
XS 168
XS 169
M 3,57,3
XS 170
XS 171
M 4,68,2
AW 208,168
AW 209,169
M 4,69,3
s_add_u32 s42, s44, s64
RA 220,1,14336
AW 210,170
M 5,80,2
WL 2
LO 123,19,123
LU 19,123
M 5,81,3
s_addc_u32 s43, s45, 0
ST 120
HS 25,19
M 0,22,4
HX 25
AW 211,171
M 0,23,5
W1 25,1024
XS 172
M 1,34,4
v_add_u32 v4, s39, v4
XS 173
M 1,35,5
RA 224,1,15360
XS 174
M 2,46,4
LL 16,56
LL 104,57
M 2,47,5
LA 16
XS 175
M 3,58,4
LC_ 104
AW 212,172
M 3,59,5
AW 213,173
AW 214,174
M 4,70,4
AW 215,175
M 4,71,5
RA 228,1,16384
M 5,82,4
WL 5
LL 17,58
LL 105,59
M 5,83,5
LA 17
Z 6,12,0
LC_ 105
M 6,13,1
Z 7,24,0
WL 3
LO 104,16,104
LU 16,104
M 7,25,1
RA 232,1,17408
HZ 24,16
Z 8,36,0
LL 18,60
LL 108,61
M 8,37,1
s_mul_i32 s53, s50, 25600
LA 18
Z 9,48,0
s_add_u32 s40, %[wlo], s53
LC_ 108
M 9,49,1
s_addc_u32 s41, %[whi], 0
Z 10,60,0
WL 3
LO 105,17,105
LU 17,105
M 10,61,1
s_mov_b32 m0, s38
RA 236,1,18432
HS 24,17
Z 11,72,0
LL 19,62
LL 109,63
M 11,73,1
LA 19
DW 0
M 6,14,2
LC_ 109
)"
R"(M 6,15,3
M 7,26,2
WL 3
LO 108,18,108
LU 18,108
M 7,27,3
s_add_u32 s37, s37, 32768
RA 216,1,19456
HS 24,18
M 8,38,2
s_cmp_eq_u32 s37, s75
LL 16,64
LL 112,65
M 8,39,3
s_cselect_b32 s37, s74, s37
LA 16
DW 1024
M 9,50,2
LC_ 112
M 9,51,3
M 10,62,2
WL 3
LO 109,19,109
LU 19,109
M 10,63,3
RA 220,1,20480
HS 24,19
M 11,74,2
LL 17,66
LL 113,67
M 11,75,3
LA 17
DW 2048
M 6,16,4
LC_ 113
M 6,17,5
M 7,28,4
WL 3
LO 112,16,112
LU 16,112
M 7,29,5
s_mul_i32 s53, s51, 49152
RA 224,1,21504
HS 24,16
M 8,40,4
s_bitcmp1_b64 s[90:91], s51
LL 18,68
LL 116,69
M 8,41,5
s_cselect_b64 s[44:45], s[84:85], s[82:83]
LA 18
M 9,52,4
s_add_u32 s44, s44, s53
LC_ 116
M 9,53,5
s_addc_u32 s45, s45, 0
M 10,64,4
WL 3
LO 113,17,113
LU 17,113
M 10,65,5
s_add_u32 s51, s51, %[d]
RA 228,1,22528
HS 24,17
M 11,76,4
LL 19,70
LL 117,71
M 11,77,5
s_cmp_eq_u32 s51, %[wrap]
LA 19
M 6,18,0
s_cselect_b32 s51, %[rst], s51
LC_ 117
M 6,19,1
s_add_u32 s40, s40, 3072
M 7,30,0
WL 3
LO 116,18,116
LU 18,116
M 7,31,1
s_addc_u32 s41, s41, 0
RA 232,1,23552
HS 24,18
M 8,42,0
s_add_u32 m0, s38, 3072
LL 16,72
LL 120,73
M 8,43,1
LA 16
DW 0
M 9,54,0
LC_ 120
DW 1024
M 9,55,1
M 10,66,0
WL 3
LO 117,19,117
LU 19,117
M 10,67,1
RA 236,1,24576
HS 24,19
M 11,78,0
LL 17,74
LL 121,75
M 11,79,1
LA 17
DW 2048
M 6,20,2
LC_ 121
M 6,21,3
s_lshl_b32 s53, s52, 8
v_add_u32 v1, s37, v0
M 7,32,2
WL 3
LO 120,16,120
LU 16,120
M 7,33,3
s_add_u32 s48, %[alo], s53
RA 216,1,0
HS 24,16
M 8,44,2
s_addc_u32 s49, %[ahi], 0
LL 18,76
LL 124,77
M 8,45,3
s_add_u32 s52, s52, %[d]
LA 18
v_and_b32 v22, 0xfffffcff, v1
M 9,56,2
s_cmp_eq_u32 s52, %[wrap]
LC_ 124
M 9,57,3
s_cselect_b32 s52, %[rst], s52
DQ 3072
M 10,68,2
WL 3
LO 121,17,121
LU 17,121
M 10,69,3
s_add_u32 s50, s50, %[d]
RA 220,1,1024
HS 24,17
M 11,80,2
LL 19,78
LL 125,79
M 11,81,3
s_cmp_eq_u32 s50, %[wrap]
LA 19
RV 252,22,5120
M 6,22,4
s_cselect_b32 s50, %[rst], s50
LC_ 125
M 6,23,5
WV 23
M 7,34,4
WL 4
LO 124,18,124
LU 18,124
M 7,35,5
s_add_u32 s38, s38, 32768
RA 224,1,2048
HS 24,18
M 8,46,4
s_cmp_eq_u32 s38, s77
LL 16,80
LL 106,81
M 8,47,5
s_cselect_b32 s38, s76, s38
LA 16
M 9,58,4
s_barrier
LC_ 106
M 9,59,5
s_sub_u32 s39, 0, s39
M 10,70,4
WL 3
LO 125,19,125
LU 19,125
M 10,71,5
RA 228,1,3072
HS 24,19
M 11,82,4
LL 17,82
LL 107,83
M 11,83,5
LA 17
HX 24
Z 0,12,0
LC_ 107
ds_read_b64 v[14:15], v5 offset:0
M 0,13,1
W1 24,0
v_subrev_u32 v5, s39, v5
Z 1,24,0
WL 5
LO 106,16,106
LU 16,106
M 1,25,1
RA 232,1,4096
HZ 25,16
Z 2,36,0
LL 18,84
LL 110,85
M 2,37,1
LA 18
Z 3,48,0
LC_ 110
M 3,49,1
s_add_u32 s42, s44, s60
Z 4,60,0
WL 5
LO 107,17,107
LU 17,107
M 4,61,1
s_addc_u32 s43, s45, 0
RA 236,1,6144
HS 25,17
Z 5,72,0
LL 19,86
LL 111,87
M 5,73,1
LA 19
ST 104
M 0,14,2
WL 6
LC_ 111
v_add_f32 v14, v14, v15
M 0,15,3
s_nop 0
v_add_f32_dpp v14, v14, v14 quad_perm:[1,0,3,2] row_mask:0xf bank_mask:0xf
M 1,26,2
WL 3
LO 110,18,110
LU 18,110
M 1,27,3
RA 216,1,7168
HS 25,18
M 2,38,2
LL 16,88
LL 114,89
M 2,39,3
LA 16
v_add_f32_dpp v14, v14, v14 quad_perm:[2,3,0,1] row_mask:0xf bank_mask:0xf
M 3,50,2
LC_ 114
v_add_f32_dpp v14, v14, v14 row_half_mirror row_mask:0xf bank_mask:0xf
M 3,51,3
s_add_u32 s42, s44, s61
v_cvt_f64_f32 v[14:15], v14
M 4,62,2
WL 3
LO 111,19,111
LU 19,111
M 4,63,3
s_addc_u32 s43, s45, 0
RA 220,1,8192
HS 25,19
M 5,74,2
LL 17,90
LL 115,91
M 5,75,3
LA 17
s_mov_b64 exec, s[54:55]
SP
s_mov_b64 exec, -1
M 0,16,4
LC_ 115
ST 108
M 0,17,5
s_mov_b32 m0, %[pm]
s_nop 0
DF
M 1,28,4
WL 3
LO 114,16,114
LU 16,114
M 1,29,5
RA 224,1,9216
HS 25,16
M 2,40,4
LL 18,92
LL 118,93
M 2,41,5
LA 18
M 3,52,4
LC_ 118
M 3,53,5
s_add_u32 s42, s44, s62
M 4,64,4
WL 3
LO 115,17,115
LU 17,115
M 4,65,5
s_addc_u32 s43, s45, 0
RA 228,1,10240
HS 25,17
M 5,76,4
LL 19,94
LL 119,95
M 5,77,5
LA 19
ST 112
M 0,18,0
LC_ 119
M 0,19,1
M 1,30,0
WL 3
LO 118,18,118
LU 18,118
M 1,31,1
RA 232,1,11264
HS 25,18
M 2,42,0
LL 16,96
LL 122,97
M 2,43,1
LA 16
RV 252,22,5392
M 3,54,0
LC_ 122
M 3,55,1
s_add_u32 s42, s44, s63
M 4,66,0
WL 4
LO 119,19,119
LU 19,119
M 4,67,1
s_addc_u32 s43, s45, 0
RA 236,1,12288
HS 25,19
M 5,78,0
LL 17,98
LL 123,99
M 5,79,1
LA 17
ST 116
M 0,20,2
LC_ 123
M 0,21,3
M 1,32,2
WL 3
LO 122,16,122
LU 16,122
M 1,33,3
RA 216,1,13312
HS 25,16
M 2,44,2
LL 18,100
LL 126,101
M 2,45,3
LA 18
M 3,56,2
LC_ 126
M 3,57,3
s_add_u32 s42, s44, s64
M 4,68,2
WL 3
LO 123,17,123
LU 17,123
M 4,69,3
s_addc_u32 s43, s45, 0
RA 220,1,14336
HS 25,17
M 5,80,2
LL 19,102
LL 127,103
M 5,81,3
LA 19
ST 120
M 0,22,4
LC_ 127
M 0,23,5
M 1,34,4
WL 3
LO 126,18,126
LU 18,126
M 1,35,5
RA 224,1,15360
HS 25,18
M 2,46,4
LL 16,56
LL 104,57
M 2,47,5
LA 16
M 3,58,4
LC_ 104
M 3,59,5
s_add_u32 s42, s44, s65
M 4,70,4
WL 3
LO 127,19,127
LU 19,127
M 4,71,5
s_addc_u32 s43, s45, 0
RA 228,1,16384
HS 25,19
M 5,82,4
LL 17,58
LL 105,59
M 5,83,5
)"
R"(LA 17
ST 124
Z 6,12,0
LC_ 105
HX 25
M 6,13,1
W1 25,1024
Z 7,24,0
WL 4
LO 104,16,104
LU 16,104
M 7,25,1
RA 232,1,17408
HZ 24,16
Z 8,36,0
LL 18,60
LL 108,61
M 8,37,1
s_mul_i32 s53, s50, 25600
LA 18
v_add_u32 v4, s39, v4
Z 9,48,0
s_add_u32 s40, %[wlo], s53
LC_ 108
M 9,49,1
s_addc_u32 s41, %[whi], 0
Z 10,60,0
WL 4
LO 105,17,105
LU 17,105
M 10,61,1
s_mov_b32 m0, s38
RA 236,1,18432
HS 24,17
Z 11,72,0
LL 19,62
LL 109,63
M 11,73,1
LA 19
DW 0
M 6,14,2
LC_ 109
M 6,15,3
M 7,26,2
WL 3
LO 108,18,108
LU 18,108
M 7,27,3
s_add_u32 s37, s37, 32768
RA 216,1,19456
HS 24,18
M 8,38,2
s_cmp_eq_u32 s37, s75
LL 16,64
LL 112,65
M 8,39,3
s_cselect_b32 s37, s74, s37
LA 16
DW 1024
M 9,50,2
LC_ 112
M 9,51,3
M 10,62,2
WL 3
LO 109,19,109
LU 19,109
M 10,63,3
RA 220,1,20480
HS 24,19
M 11,74,2
LL 17,66
LL 113,67
M 11,75,3
LA 17
DW 2048
M 6,16,4
LC_ 113
M 6,17,5
M 7,28,4
WL 3
LO 112,16,112
LU 16,112
M 7,29,5
s_mul_i32 s53, s51, 49152
RA 224,1,21504
HS 24,16
M 8,40,4
s_bitcmp1_b64 s[90:91], s51
LL 18,68
LL 116,69
M 8,41,5
s_cselect_b64 s[44:45], s[84:85], s[82:83]
LA 18
M 9,52,4
s_add_u32 s44, s44, s53
LC_ 116
M 9,53,5
s_addc_u32 s45, s45, 0
M 10,64,4
WL 3
LO 113,17,113
LU 17,113
M 10,65,5
s_add_u32 s51, s51, %[d]
RA 228,1,22528
HS 24,17
M 11,76,4
LL 19,70
LL 117,71
M 11,77,5
s_cmp_eq_u32 s51, %[wrap]
LA 19
M 6,18,0
s_cselect_b32 s51, %[rst], s51
LC_ 117
M 6,19,1
s_add_u32 s40, s40, 3072
M 7,30,0
WL 3
LO 116,18,116
LU 18,116
M 7,31,1
s_addc_u32 s41, s41, 0
RA 232,1,23552
HS 24,18
M 8,42,0
s_add_u32 m0, s38, 3072
LL 16,72
LL 120,73
M 8,43,1
LA 16
DW 0
M 9,54,0
LC_ 120
DW 1024
M 9,55,1
M 10,66,0
WL 3
LO 117,19,117
LU 19,117
M 10,67,1
RA 236,1,24576
HS 24,19
M 11,78,0
LL 17,74
LL 121,75
M 11,79,1
LA 17
DW 2048
M 6,20,2
LC_ 121
M 6,21,3
s_lshl_b32 s53, s52, 8
v_add_u32 v1, s37, v0
M 7,32,2
WL 3
LO 120,16,120
LU 16,120
M 7,33,3
s_add_u32 s48, %[alo], s53
RA 216,1,0
HS 24,16
M 8,44,2
s_addc_u32 s49, %[ahi], 0
LL 18,76
LL 124,77
M 8,45,3
s_add_u32 s52, s52, %[d]
LA 18
v_and_b32 v22, 0xfffffcff, v1
M 9,56,2
s_cmp_eq_u32 s52, %[wrap]
LC_ 124
M 9,57,3
s_cselect_b32 s52, %[rst], s52
DQ 3072
M 10,68,2
WL 3
LO 121,17,121
LU 17,121
M 10,69,3
s_add_u32 s50, s50, %[d]
RA 220,1,1024
HS 24,17
M 11,80,2
LL 19,78
LL 125,79
M 11,81,3
s_cmp_eq_u32 s50, %[wrap]
LA 19
RV 252,22,5120
M 6,22,4
s_cselect_b32 s50, %[rst], s50
LC_ 125
M 6,23,5
WV 29
M 7,34,4
WL 4
LO 124,18,124
LU 18,124
M 7,35,5
s_add_u32 s38, s38, 32768
RA 224,1,2048
HS 24,18
M 8,46,4
s_cmp_eq_u32 s38, s77
LL 16,80
LL 106,81
M 8,47,5
s_cselect_b32 s38, s76, s38
LA 16
M 9,58,4
s_barrier
LC_ 106
M 9,59,5
s_sub_u32 s39, 0, s39
M 10,70,4
WL 3
LO 125,19,125
LU 19,125
M 10,71,5
RA 228,1,3072
HS 24,19
M 11,82,4
LL 17,82
LL 107,83
M 11,83,5
s_waitcnt lgkmcnt(0)
s_bcnt1_i32_b64 vcc_lo, s[66:67]
s_lshr_b32 s66, %[pf], 12
s_mul_i32 s66, s66, 5
s_lshr_b32 s66, s66, 5
s_add_u32 s66, s66, 1
s_lshl_b32 s67, s66, 25
s_sub_u32 s67, 306319492, s67
s_sub_u32 s66, s66, 1
s_mul_i32 s46, %[pf], 288
s_mul_i32 s47, %[s2], 147456
s_add_u32 s46, s46, s47
s_mul_i32 s47, %[tsel], 9216
s_add_u32 s46, s46, s47
s_add_u32 s46, s46, 75497472
s_add_u32 s46, s46, %[xlo]
s_addc_u32 s47, %[xhi], 0
v_min_u32 v3, 0x2f0, v0
v_lshlrev_b32 v3, 3, v3
s_cmp_gt_u32 vcc_lo, 8
s_cselect_b32 s66, s66, 28
s_cselect_b32 s67, s67, 0
1:
LA 17
HX 24
Z 0,12,0
LC_ 107
ds_read_b64 v[14:15], v5 offset:0
M 0,13,1
W1 24,0
v_subrev_u32 v5, s39, v5
Z 1,24,0
WL 5
LO 106,16,106
LU 16,106
M 1,25,1
RA 232,1,4096
HZ 25,16
Z 2,36,0
LL 18,84
LL 110,85
M 2,37,1
LA 18
Z 3,48,0
LC_ 110
M 3,49,1
s_add_u32 s42, s44, s60
Z 4,60,0
WL 5
LO 107,17,107
LU 17,107
M 4,61,1
s_addc_u32 s43, s45, 0
RA 236,1,6144
HS 25,17
Z 5,72,0
LL 19,86
LL 111,87
M 5,73,1
LA 19
ST 104
M 0,14,2
WL 6
LC_ 111
v_add_f32 v14, v14, v15
M 0,15,3
s_nop 0
v_add_f32_dpp v14, v14, v14 quad_perm:[1,0,3,2] row_mask:0xf bank_mask:0xf
M 1,26,2
WL 3
LO 110,18,110
LU 18,110
M 1,27,3
RA 216,1,7168
HS 25,18
M 2,38,2
LL 16,88
LL 114,89
M 2,39,3
LA 16
v_add_f32_dpp v14, v14, v14 quad_perm:[2,3,0,1] row_mask:0xf bank_mask:0xf
M 3,50,2
LC_ 114
v_add_f32_dpp v14, v14, v14 row_half_mirror row_mask:0xf bank_mask:0xf
M 3,51,3
s_add_u32 s42, s44, s61
v_cvt_f64_f32 v[14:15], v14
M 4,62,2
WL 3
LO 111,19,111
LU 19,111
M 4,63,3
s_addc_u32 s43, s45, 0
RA 220,1,8192
HS 25,19
M 5,74,2
LL 17,90
LL 115,91
M 5,75,3
LA 17
s_mov_b64 exec, s[54:55]
SP
s_mov_b64 exec, -1
M 0,16,4
LC_ 115
ST 108
M 0,17,5
s_mov_b32 m0, %[pm]
s_nop 0
DF
M 1,28,4
WL 3
LO 114,16,114
LU 16,114
M 1,29,5
RA 224,1,9216
HS 25,16
M 2,40,4
LL 18,92
LL 118,93
M 2,41,5
LA 18
M 3,52,4
LC_ 118
M 3,53,5
s_add_u32 s42, s44, s62
M 4,64,4
WL 3
LO 115,17,115
LU 17,115
M 4,65,5
s_addc_u32 s43, s45, 0
RA 228,1,10240
HS 25,17
M 5,76,4
LL 19,94
LL 119,95
M 5,77,5
LA 19
ST 112
M 0,18,0
LC_ 119
M 0,19,1
M 1,30,0
WL 3
LO 118,18,118
LU 18,118
M 1,31,1
RA 232,1,11264
HS 25,18
M 2,42,0
LL 16,96
LL 122,97
M 2,43,1
LA 16
RV 252,22,5392
M 3,54,0
LC_ 122
M 3,55,1
s_add_u32 s42, s44, s63
M 4,66,0
WL 4
LO 119,19,119
LU 19,119
M 4,67,1
s_addc_u32 s43, s45, 0
RA 236,1,12288
HS 25,19
M 5,78,0
LL 17,98
LL 123,99
M 5,79,1
LA 17
)"
R"(ST 116
M 0,20,2
LC_ 123
M 0,21,3
M 1,32,2
WL 3
LO 122,16,122
LU 16,122
M 1,33,3
RA 216,1,13312
HS 25,16
M 2,44,2
LL 18,100
LL 126,101
M 2,45,3
LA 18
M 3,56,2
LC_ 126
M 3,57,3
s_add_u32 s42, s44, s64
M 4,68,2
WL 3
LO 123,17,123
LU 17,123
M 4,69,3
s_addc_u32 s43, s45, 0
RA 220,1,14336
HS 25,17
M 5,80,2
LL 19,102
LL 127,103
M 5,81,3
LA 19
ST 120
M 0,22,4
LC_ 127
M 0,23,5
M 1,34,4
WL 3
LO 126,18,126
LU 18,126
M 1,35,5
RA 224,1,15360
HS 25,18
M 2,46,4
LL 16,56
LL 104,57
M 2,47,5
LA 16
M 3,58,4
LC_ 104
M 3,59,5
s_add_u32 s42, s44, s65
M 4,70,4
WL 3
LO 127,19,127
LU 19,127
M 4,71,5
s_addc_u32 s43, s45, 0
RA 228,1,16384
HS 25,19
M 5,82,4
LL 17,58
LL 105,59
M 5,83,5
LA 17
ST 124
Z 6,12,0
LC_ 105
HX 25
M 6,13,1
W1 25,1024
Z 7,24,0
WL 4
LO 104,16,104
LU 16,104
M 7,25,1
RA 232,1,17408
HZ 24,16
Z 8,36,0
LL 18,60
LL 108,61
M 8,37,1
s_mul_i32 s53, s50, 25600
LA 18
v_add_u32 v4, s39, v4
Z 9,48,0
s_add_u32 s40, %[wlo], s53
LC_ 108
M 9,49,1
s_addc_u32 s41, %[whi], 0
Z 10,60,0
WL 4
LO 105,17,105
LU 17,105
M 10,61,1
s_mov_b32 m0, s38
RA 236,1,18432
HS 24,17
Z 11,72,0
LL 19,62
LL 109,63
M 11,73,1
LA 19
DW 0
M 6,14,2
LC_ 109
M 6,15,3
M 7,26,2
WL 3
LO 108,18,108
LU 18,108
M 7,27,3
s_add_u32 s37, s37, 32768
RA 216,1,19456
HS 24,18
M 8,38,2
s_cmp_eq_u32 s37, s75
LL 16,64
LL 112,65
M 8,39,3
s_cselect_b32 s37, s74, s37
LA 16
DW 1024
M 9,50,2
LC_ 112
M 9,51,3
M 10,62,2
WL 3
LO 109,19,109
LU 19,109
M 10,63,3
RA 220,1,20480
HS 24,19
M 11,74,2
LL 17,66
LL 113,67
M 11,75,3
LA 17
DW 2048
M 6,16,4
LC_ 113
M 6,17,5
M 7,28,4
WL 3
LO 112,16,112
LU 16,112
M 7,29,5
s_mul_i32 s53, s51, 49152
RA 224,1,21504
HS 24,16
M 8,40,4
s_bitcmp1_b64 s[90:91], s51
LL 18,68
LL 116,69
M 8,41,5
s_cselect_b64 s[44:45], s[84:85], s[82:83]
LA 18
M 9,52,4
s_add_u32 s44, s44, s53
LC_ 116
M 9,53,5
s_addc_u32 s45, s45, 0
M 10,64,4
WL 3
LO 113,17,113
LU 17,113
M 10,65,5
s_add_u32 s51, s51, %[d]
RA 228,1,22528
HS 24,17
M 11,76,4
LL 19,70
LL 117,71
M 11,77,5
s_cmp_eq_u32 s51, %[wrap]
LA 19
M 6,18,0
s_cselect_b32 s51, %[rst], s51
LC_ 117
M 6,19,1
s_add_u32 s40, s40, 3072
M 7,30,0
WL 3
LO 116,18,116
LU 18,116
M 7,31,1
s_addc_u32 s41, s41, 0
RA 232,1,23552
HS 24,18
M 8,42,0
s_add_u32 m0, s38, 3072
LL 16,72
LL 120,73
M 8,43,1
LA 16
DW 0
M 9,54,0
LC_ 120
DW 1024
M 9,55,1
M 10,66,0
WL 3
LO 117,19,117
LU 19,117
M 10,67,1
RA 236,1,24576
HS 24,19
M 11,78,0
LL 17,74
LL 121,75
M 11,79,1
LA 17
DW 2048
M 6,20,2
LC_ 121
M 6,21,3
s_lshl_b32 s53, s52, 8
v_add_u32 v1, s37, v0
M 7,32,2
WL 3
LO 120,16,120
LU 16,120
M 7,33,3
s_add_u32 s48, %[alo], s53
RA 216,1,0
HS 24,16
M 8,44,2
s_addc_u32 s49, %[ahi], 0
LL 18,76
LL 124,77
M 8,45,3
s_add_u32 s52, s52, %[d]
LA 18
v_and_b32 v22, 0xfffffcff, v1
M 9,56,2
s_cmp_eq_u32 s52, %[wrap]
LC_ 124
M 9,57,3
s_cselect_b32 s52, %[rst], s52
DQ 3072
M 10,68,2
WL 3
LO 121,17,121
LU 17,121
M 10,69,3
s_add_u32 s50, s50, %[d]
RA 220,1,1024
HS 24,17
M 11,80,2
LL 19,78
LL 125,79
M 11,81,3
s_cmp_eq_u32 s50, %[wrap]
LA 19
RV 252,22,5120
M 6,22,4
s_cselect_b32 s50, %[rst], s50
LC_ 125
M 6,23,5
WV 30
M 7,34,4
WL 4
LO 124,18,124
LU 18,124
M 7,35,5
s_add_u32 s38, s38, 32768
RA 224,1,2048
HS 24,18
M 8,46,4
s_cmp_eq_u32 s38, s77
LL 16,80
LL 106,81
M 8,47,5
s_cselect_b32 s38, s76, s38
LA 16
M 9,58,4
s_barrier
LC_ 106
M 9,59,5
s_sub_u32 s39, 0, s39
M 10,70,4
WL 3
LO 125,19,125
LU 19,125
M 10,71,5
RA 228,1,3072
HS 24,19
M 11,82,4
s_sub_u32 s66, s66, 1
LL 17,82
LL 107,83
M 11,83,5
s_cbranch_scc0 1b
s_cmp_eq_u32 s67, 0
s_cbranch_scc1 7f
s_mov_b32 m0, %[pm]
s_and_b32 s66, s67, 31
s_sub_u32 s66, s66, 1
s_lshr_b32 s67, s67, 5
DX
s_add_u32 s46, s46, 6144
s_addc_u32 s47, s47, 0
s_branch 1b
7:
LA 17
HX 24
Z 0,12,0
LC_ 107
ds_read_b64 v[14:15], v5 offset:0
M 0,13,1
W1 24,0
v_subrev_u32 v5, s39, v5
Z 1,24,0
WL 5
LO 106,16,106
LU 16,106
M 1,25,1
RA 232,1,4096
HZ 25,16
Z 2,36,0
LL 18,84
LL 110,85
M 2,37,1
LA 18
Z 3,48,0
LC_ 110
M 3,49,1
s_add_u32 s42, s44, s60
Z 4,60,0
WL 5
LO 107,17,107
LU 17,107
M 4,61,1
s_addc_u32 s43, s45, 0
RA 236,1,6144
HS 25,17
Z 5,72,0
LL 19,86
LL 111,87
M 5,73,1
LA 19
ST 104
M 0,14,2
WL 6
LC_ 111
v_add_f32 v14, v14, v15
M 0,15,3
s_nop 0
v_add_f32_dpp v14, v14, v14 quad_perm:[1,0,3,2] row_mask:0xf bank_mask:0xf
M 1,26,2
WL 3
LO 110,18,110
LU 18,110
M 1,27,3
RA 216,1,7168
HS 25,18
M 2,38,2
LL 16,88
LL 114,89
M 2,39,3
LA 16
v_add_f32_dpp v14, v14, v14 quad_perm:[2,3,0,1] row_mask:0xf bank_mask:0xf
M 3,50,2
LC_ 114
v_add_f32_dpp v14, v14, v14 row_half_mirror row_mask:0xf bank_mask:0xf
M 3,51,3
s_add_u32 s42, s44, s61
v_cvt_f64_f32 v[14:15], v14
M 4,62,2
WL 3
LO 111,19,111
LU 19,111
M 4,63,3
s_addc_u32 s43, s45, 0
RA 220,1,8192
HS 25,19
M 5,74,2
LL 17,90
LL 115,91
M 5,75,3
LA 17
s_mov_b64 exec, s[54:55]
SP
s_mov_b64 exec, -1
M 0,16,4
LC_ 115
ST 108
M 0,17,5
s_mov_b32 m0, %[pm]
s_nop 0
DF
M 1,28,4
WL 3
LO 114,16,114
LU 16,114
M 1,29,5
RA 224,1,9216
HS 25,16
M 2,40,4
)"
R"(LL 18,92
LL 118,93
M 2,41,5
LA 18
M 3,52,4
LC_ 118
M 3,53,5
s_add_u32 s42, s44, s62
M 4,64,4
WL 3
LO 115,17,115
LU 17,115
M 4,65,5
s_addc_u32 s43, s45, 0
RA 228,1,10240
HS 25,17
M 5,76,4
LL 19,94
LL 119,95
M 5,77,5
LA 19
ST 112
M 0,18,0
LC_ 119
M 0,19,1
M 1,30,0
WL 3
LO 118,18,118
LU 18,118
M 1,31,1
RA 232,1,11264
HS 25,18
M 2,42,0
LL 16,96
LL 122,97
M 2,43,1
LA 16
RV 252,22,5392
M 3,54,0
LC_ 122
M 3,55,1
s_add_u32 s42, s44, s63
M 4,66,0
WL 4
LO 119,19,119
LU 19,119
M 4,67,1
s_addc_u32 s43, s45, 0
RA 236,1,12288
HS 25,19
M 5,78,0
LL 17,98
LL 123,99
M 5,79,1
LA 17
ST 116
M 0,20,2
LC_ 123
M 0,21,3
M 1,32,2
WL 3
LO 122,16,122
LU 16,122
M 1,33,3
RA 216,1,13312
HS 25,16
M 2,44,2
LL 18,100
LL 126,101
M 2,45,3
LA 18
M 3,56,2
LC_ 126
M 3,57,3
s_add_u32 s42, s44, s64
M 4,68,2
WL 3
LO 123,17,123
LU 17,123
M 4,69,3
s_addc_u32 s43, s45, 0
RA 220,1,14336
HS 25,17
M 5,80,2
LL 19,102
LL 127,103
M 5,81,3
LA 19
ST 120
M 0,22,4
LC_ 127
M 0,23,5
M 1,34,4
WL 3
LO 126,18,126
LU 18,126
M 1,35,5
RA 224,1,15360
HS 25,18
M 2,46,4
LL 16,56
LL 104,57
M 2,47,5
LA 16
M 3,58,4
LC_ 104
M 3,59,5
s_add_u32 s42, s44, s65
M 4,70,4
WL 3
LO 127,19,127
LU 19,127
M 4,71,5
s_addc_u32 s43, s45, 0
RA 228,1,16384
HS 25,19
M 5,82,4
LL 17,58
LL 105,59
M 5,83,5
LA 17
ST 124
Z 6,12,0
LC_ 105
HX 25
M 6,13,1
W1 25,1024
Z 7,24,0
WL 4
LO 104,16,104
LU 16,104
M 7,25,1
RA 232,1,17408
HZ 24,16
Z 8,36,0
LL 18,60
LL 108,61
M 8,37,1
s_mul_i32 s53, s50, 25600
LA 18
v_add_u32 v4, s39, v4
Z 9,48,0
s_add_u32 s40, %[wlo], s53
LC_ 108
s_mov_b64 exec, 1
AT
s_mov_b64 exec, -1
M 9,49,1
s_addc_u32 s41, %[whi], 0
Z 10,60,0
WL 4
LO 105,17,105
LU 17,105
M 10,61,1
s_mov_b32 m0, s38
RA 236,1,18432
HS 24,17
Z 11,72,0
LL 19,62
LL 109,63
M 11,73,1
LA 19
DW 0
M 6,14,2
LC_ 109
M 6,15,3
M 7,26,2
WL 3
LO 108,18,108
LU 18,108
M 7,27,3
s_add_u32 s37, s37, 32768
RA 216,1,19456
HS 24,18
M 8,38,2
s_cmp_eq_u32 s37, s75
LL 16,64
LL 112,65
M 8,39,3
s_cselect_b32 s37, s74, s37
LA 16
DW 1024
M 9,50,2
LC_ 112
M 9,51,3
M 10,62,2
WL 3
LO 109,19,109
LU 19,109
M 10,63,3
RA 220,1,20480
HS 24,19
M 11,74,2
LL 17,66
LL 113,67
M 11,75,3
LA 17
DW 2048
M 6,16,4
LC_ 113
M 6,17,5
M 7,28,4
WL 3
LO 112,16,112
LU 16,112
M 7,29,5
s_mul_i32 s53, s51, 49152
RA 224,1,21504
HS 24,16
M 8,40,4
s_bitcmp1_b64 s[90:91], s51
LL 18,68
LL 116,69
M 8,41,5
s_cselect_b64 s[44:45], s[84:85], s[82:83]
LA 18
M 9,52,4
s_add_u32 s44, s44, s53
LC_ 116
M 9,53,5
s_addc_u32 s45, s45, 0
M 10,64,4
WL 3
LO 113,17,113
LU 17,113
M 10,65,5
s_add_u32 s51, s51, %[d]
RA 228,1,22528
HS 24,17
M 11,76,4
LL 19,70
LL 117,71
M 11,77,5
s_cmp_eq_u32 s51, %[wrap]
LA 19
M 6,18,0
s_cselect_b32 s51, %[rst], s51
LC_ 117
M 6,19,1
s_add_u32 s40, s40, 3072
M 7,30,0
WL 3
LO 116,18,116
LU 18,116
M 7,31,1
s_addc_u32 s41, s41, 0
RA 232,1,23552
HS 24,18
M 8,42,0
s_add_u32 m0, s38, 3072
LL 16,72
LL 120,73
M 8,43,1
LA 16
DW 0
M 9,54,0
LC_ 120
DW 1024
M 9,55,1
M 10,66,0
WL 3
LO 117,19,117
LU 19,117
M 10,67,1
RA 236,1,24576
HS 24,19
M 11,78,0
LL 17,74
LL 121,75
M 11,79,1
LA 17
DW 2048
M 6,20,2
LC_ 121
M 6,21,3
s_lshl_b32 s53, s52, 8
v_add_u32 v1, s37, v0
M 7,32,2
WL 3
LO 120,16,120
LU 16,120
M 7,33,3
s_add_u32 s48, %[alo], s53
RA 216,1,0
HS 24,16
M 8,44,2
s_addc_u32 s49, %[ahi], 0
LL 18,76
LL 124,77
M 8,45,3
s_add_u32 s52, s52, %[d]
LA 18
v_and_b32 v22, 0xfffffcff, v1
M 9,56,2
s_cmp_eq_u32 s52, %[wrap]
LC_ 124
M 9,57,3
s_cselect_b32 s52, %[rst], s52
DQ 3072
M 10,68,2
WL 3
LO 121,17,121
LU 17,121
M 10,69,3
s_add_u32 s50, s50, %[d]
RA 220,1,1024
HS 24,17
M 11,80,2
LL 19,78
LL 125,79
M 11,81,3
s_cmp_eq_u32 s50, %[wrap]
LA 19
RV 252,22,5120
M 6,22,4
s_cselect_b32 s50, %[rst], s50
LC_ 125
M 6,23,5
WV 31
M 7,34,4
WL 4
LO 124,18,124
LU 18,124
M 7,35,5
s_add_u32 s38, s38, 32768
RA 224,1,2048
HS 24,18
M 8,46,4
s_cmp_eq_u32 s38, s77
LL 16,80
LL 106,81
M 8,47,5
s_cselect_b32 s38, s76, s38
LA 16
M 9,58,4
s_barrier
LC_ 106
M 9,59,5
s_sub_u32 s39, 0, s39
M 10,70,4
WL 3
LO 125,19,125
LU 19,125
M 10,71,5
RA 228,1,3072
HS 24,19
M 11,82,4
LL 17,82
LL 107,83
M 11,83,5
LA 17
HX 24
Z 0,12,0
LC_ 107
ds_read_b64 v[14:15], v5 offset:0
M 0,13,1
W1 24,0
v_subrev_u32 v5, s39, v5
Z 1,24,0
WL 5
LO 106,16,106
LU 16,106
M 1,25,1
RA 232,1,4096
HZ 25,16
Z 2,36,0
LL 18,84
LL 110,85
M 2,37,1
LA 18
Z 3,48,0
LC_ 110
M 3,49,1
s_add_u32 s42, s44, s60
Z 4,60,0
WL 5
LO 107,17,107
LU 17,107
M 4,61,1
s_addc_u32 s43, s45, 0
RA 236,1,6144
HS 25,17
Z 5,72,0
LL 19,86
LL 111,87
M 5,73,1
LA 19
ST 104
M 0,14,2
WL 6
LC_ 111
v_add_f32 v14, v14, v15
M 0,15,3
s_nop 0
v_add_f32_dpp v14, v14, v14 quad_perm:[1,0,3,2] row_mask:0xf bank_mask:0xf
M 1,26,2
WL 3
LO 110,18,110
LU 18,110
M 1,27,3
)"
R"(RA 216,1,7168
HS 25,18
M 2,38,2
LL 16,88
LL 114,89
M 2,39,3
LA 16
v_add_f32_dpp v14, v14, v14 quad_perm:[2,3,0,1] row_mask:0xf bank_mask:0xf
M 3,50,2
LC_ 114
v_add_f32_dpp v14, v14, v14 row_half_mirror row_mask:0xf bank_mask:0xf
M 3,51,3
s_add_u32 s42, s44, s61
v_cvt_f64_f32 v[14:15], v14
M 4,62,2
WL 3
LO 111,19,111
LU 19,111
M 4,63,3
s_addc_u32 s43, s45, 0
RA 220,1,8192
HS 25,19
M 5,74,2
LL 17,90
LL 115,91
M 5,75,3
LA 17
s_mov_b64 exec, s[54:55]
SP
s_mov_b64 exec, -1
M 0,16,4
LC_ 115
ST 108
M 0,17,5
s_mov_b32 m0, %[pm]
s_nop 0
DF
M 1,28,4
WL 3
LO 114,16,114
LU 16,114
M 1,29,5
RA 224,1,9216
HS 25,16
M 2,40,4
LL 18,92
LL 118,93
M 2,41,5
LA 18
M 3,52,4
LC_ 118
M 3,53,5
s_add_u32 s42, s44, s62
M 4,64,4
WL 3
LO 115,17,115
LU 17,115
M 4,65,5
s_addc_u32 s43, s45, 0
RA 228,1,10240
HS 25,17
M 5,76,4
LL 19,94
LL 119,95
M 5,77,5
LA 19
ST 112
M 0,18,0
LC_ 119
M 0,19,1
M 1,30,0
WL 3
LO 118,18,118
LU 18,118
M 1,31,1
RA 232,1,11264
HS 25,18
M 2,42,0
LL 16,96
LL 122,97
M 2,43,1
LA 16
RV 252,22,5392
M 3,54,0
LC_ 122
M 3,55,1
s_add_u32 s42, s44, s63
M 4,66,0
WL 4
LO 119,19,119
LU 19,119
M 4,67,1
s_addc_u32 s43, s45, 0
RA 236,1,12288
HS 25,19
M 5,78,0
LL 17,98
LL 123,99
M 5,79,1
LA 17
ST 116
M 0,20,2
LC_ 123
M 0,21,3
M 1,32,2
WL 3
LO 122,16,122
LU 16,122
M 1,33,3
RA 216,1,13312
HS 25,16
M 2,44,2
LL 18,100
LL 126,101
M 2,45,3
LA 18
M 3,56,2
LC_ 126
M 3,57,3
s_add_u32 s42, s44, s64
M 4,68,2
WL 3
LO 123,17,123
LU 17,123
M 4,69,3
s_addc_u32 s43, s45, 0
RA 220,1,14336
HS 25,17
M 5,80,2
LL 19,102
LL 127,103
M 5,81,3
LA 19
ST 120
M 0,22,4
LC_ 127
M 0,23,5
M 1,34,4
WL 3
LO 126,18,126
LU 18,126
M 1,35,5
RA 224,1,15360
HS 25,18
M 2,46,4
LL 16,56
LL 104,57
M 2,47,5
LA 16
M 3,58,4
LC_ 104
M 3,59,5
s_add_u32 s42, s44, s65
M 4,70,4
WL 3
LO 127,19,127
LU 19,127
M 4,71,5
s_addc_u32 s43, s45, 0
RA 228,1,16384
HS 25,19
M 5,82,4
LL 17,58
LL 105,59
M 5,83,5
LA 17
ST 124
Z 6,12,0
LC_ 105
HX 25
M 6,13,1
WV 15
W1 25,1024
v_readfirstlane_b32 s72, v11
Z 7,24,0
WL 4
LO 104,16,104
LU 16,104
M 7,25,1
RA 232,1,17408
HZ 24,16
Z 8,36,0
LL 18,60
LL 108,61
M 8,37,1
s_mul_i32 s53, s50, 25600
LA 18
v_mov_b32 v12, s72
Z 9,48,0
s_add_u32 s40, %[wlo], s53
LC_ 108
W2 12,12,16
M 9,49,1
s_addc_u32 s41, %[whi], 0
v_add_u32 v4, s39, v4
Z 10,60,0
WL 5
LO 105,17,105
LU 17,105
M 10,61,1
s_mov_b32 m0, s38
RA 236,1,18432
HS 24,17
Z 11,72,0
LL 19,62
LL 109,63
M 11,73,1
LA 19
DW 0
M 6,14,2
LC_ 109
M 6,15,3
M 7,26,2
WL 4
LO 108,18,108
LU 18,108
M 7,27,3
s_add_u32 s37, s37, 32768
RA 216,1,19456
HS 24,18
M 8,38,2
s_cmp_eq_u32 s37, s75
LL 16,64
LL 112,65
M 8,39,3
s_cselect_b32 s37, s74, s37
LA 16
DW 1024
M 9,50,2
LC_ 112
M 9,51,3
M 10,62,2
WL 3
LO 109,19,109
LU 19,109
M 10,63,3
RA 220,1,20480
HS 24,19
M 11,74,2
LL 17,66
LL 113,67
M 11,75,3
LA 17
DW 2048
M 6,16,4
LC_ 113
M 6,17,5
M 7,28,4
WL 3
LO 112,16,112
LU 16,112
M 7,29,5
s_mul_i32 s53, s51, 49152
RA 224,1,21504
HS 24,16
M 8,40,4
s_bitcmp1_b64 s[90:91], s51
LL 18,68
LL 116,69
M 8,41,5
s_cselect_b64 s[44:45], s[84:85], s[82:83]
LA 18
M 9,52,4
s_add_u32 s44, s44, s53
LC_ 116
M 9,53,5
s_addc_u32 s45, s45, 0
M 10,64,4
WL 3
LO 113,17,113
LU 17,113
M 10,65,5
s_add_u32 s51, s51, %[d]
RA 228,1,22528
HS 24,17
M 11,76,4
LL 19,70
LL 117,71
M 11,77,5
s_cmp_eq_u32 s51, %[wrap]
LA 19
M 6,18,0
s_cselect_b32 s51, %[rst], s51
LC_ 117
M 6,19,1
s_add_u32 s40, s40, 3072
M 7,30,0
WL 3
LO 116,18,116
LU 18,116
M 7,31,1
s_addc_u32 s41, s41, 0
RA 232,1,23552
HS 24,18
M 8,42,0
s_add_u32 m0, s38, 3072
LL 16,72
LL 120,73
M 8,43,1
LA 16
DW 0
M 9,54,0
LC_ 120
DW 1024
M 9,55,1
M 10,66,0
WL 3
LO 117,19,117
LU 19,117
M 10,67,1
RA 236,1,24576
HS 24,19
M 11,78,0
LL 17,74
LL 121,75
M 11,79,1
LA 17
DW 2048
M 6,20,2
LC_ 121
M 6,21,3
s_lshl_b32 s53, s52, 8
v_add_u32 v1, s37, v0
M 7,32,2
WL 3
LO 120,16,120
LU 16,120
M 7,33,3
s_add_u32 s48, %[alo], s53
RA 216,1,0
HS 24,16
M 8,44,2
s_addc_u32 s49, %[ahi], 0
LL 18,76
LL 124,77
M 8,45,3
s_add_u32 s52, s52, %[d]
LA 18
v_and_b32 v22, 0xfffffcff, v1
M 9,56,2
s_cmp_eq_u32 s52, %[wrap]
LC_ 124
M 9,57,3
s_cselect_b32 s52, %[rst], s52
DQ 3072
M 10,68,2
WL 3
LO 121,17,121
LU 17,121
M 10,69,3
s_add_u32 s50, s50, %[d]
RA 220,1,1024
HS 24,17
M 11,80,2
LL 19,78
LL 125,79
M 11,81,3
s_cmp_eq_u32 s50, %[wrap]
LA 19
RV 252,22,5120
M 6,22,4
s_cselect_b32 s50, %[rst], s50
LC_ 125
M 6,23,5
M 7,34,4
WL 4
LO 124,18,124
LU 18,124
M 7,35,5
s_add_u32 s38, s38, 32768
RA 224,1,2048
HS 24,18
M 8,46,4
s_cmp_eq_u32 s38, s77
LL 16,80
LL 106,81
M 8,47,5
s_cselect_b32 s38, s76, s38
LA 16
M 9,58,4
s_barrier
LC_ 106
M 9,59,5
s_sub_u32 s39, 0, s39
M 10,70,4
WL 3
LO 125,19,125
LU 19,125
M 10,71,5
RA 228,1,3072
HS 24,19
M 11,82,4
LL 17,82
LL 107,83
M 11,83,5
LA 17
HX 24
Z 0,12,0
LC_ 107
ds_read_b32 v20, v5 offset:4096
)"
R"(M 0,13,1
ds_read_b64 v[14:15], v5 offset:0
Z 1,24,0
WL 5
LO 106,16,106
LU 16,106
M 1,25,1
RA 232,1,4096
HZ 25,16
Z 2,36,0
LL 18,84
LL 110,85
M 2,37,1
LA 18
W1 24,0
Z 3,48,0
LC_ 110
v_subrev_u32 v5, s39, v5
M 3,49,1
s_add_u32 s42, s44, s60
Z 4,60,0
WL 6
LO 107,17,107
LU 17,107
M 4,61,1
s_addc_u32 s43, s45, 0
RA 236,1,6144
HS 25,17
Z 5,72,0
LL 19,86
LL 111,87
M 5,73,1
LA 19
ST 104
M 0,14,2
WL 6
LC_ 111
v_add_f32 v14, v14, v15
M 0,15,3
s_nop 0
v_add_f32_dpp v14, v14, v14 quad_perm:[1,0,3,2] row_mask:0xf bank_mask:0xf
M 1,26,2
WL 3
LO 110,18,110
LU 18,110
M 1,27,3
RA 216,1,7168
HS 25,18
M 2,38,2
LL 16,88
LL 114,89
M 2,39,3
LA 16
v_add_f32_dpp v14, v14, v14 quad_perm:[2,3,0,1] row_mask:0xf bank_mask:0xf
M 3,50,2
LC_ 114
v_add_f32_dpp v14, v14, v14 row_half_mirror row_mask:0xf bank_mask:0xf
M 3,51,3
s_add_u32 s42, s44, s61
v_cvt_f64_f32 v[14:15], v14
M 4,62,2
WL 3
LO 111,19,111
LU 19,111
M 4,63,3
s_addc_u32 s43, s45, 0
RA 220,1,8192
HS 25,19
M 5,74,2
LL 17,90
LL 115,91
M 5,75,3
LA 17
s_mov_b64 exec, s[54:55]
SP
s_mov_b64 exec, -1
M 0,16,4
LC_ 115
ST 108
M 0,17,5
s_mov_b32 m0, %[pm]
s_nop 0
DF
v_readfirstlane_b32 s72, v20
M 1,28,4
WL 3
LO 114,16,114
LU 16,114
M 1,29,5
s_sub_u32 s53, 63, s72
RA 224,1,9216
HS 25,16
M 2,40,4
s_cmp_eq_u32 %[d], 1
LL 18,92
LL 118,93
M 2,41,5
s_cselect_b32 s72, s72, s53
LA 18
M 3,52,4
s_and_b32 s72, s72, 63
LC_ 118
M 3,53,5
s_add_u32 s53, s72, 128
M 4,64,4
WL 3
LO 115,17,115
LU 17,115
M 4,65,5
s_mul_i32 s53, s53, 2359296
RA 228,1,10240
HS 25,17
M 5,76,4
LL 19,94
LL 119,95
M 5,77,5
s_add_u32 s86, %[hlo], s53
LA 19
M 0,18,0
s_addc_u32 s87, %[hhi], 0
LC_ 119
M 0,19,1
s_add_u32 s88, s86, 8192
M 1,30,0
WL 3
LO 118,18,118
LU 18,118
M 1,31,1
s_addc_u32 s89, s87, 0
RA 232,1,11264
HS 25,18
M 2,42,0
s_and_b32 s92, %[hlo], 0x1fffff
LL 16,96
LL 122,97
M 2,43,1
s_add_u32 s92, s92, s53
LA 16
RV 252,22,5392
M 3,54,0
s_mov_b32 s93, 3072
LC_ 122
M 3,55,1
s_add_u32 s42, s44, s62
M 4,66,0
WL 4
LO 119,19,119
LU 19,119
M 4,67,1
s_addc_u32 s43, s45, 0
RA 236,1,12288
HS 25,19
M 5,78,0
LL 17,98
LL 123,99
M 5,79,1
s_mul_i32 s57, s72, 589824
LA 17
s_add_u32 s57, s57, %[xs1]
s_lshl_b32 s72, s72, 1
s_add_u32 s72, s72, s96
M 0,20,2
LC_ 123
ST 112
M 0,21,3
s_lshl_b32 s53, s72, 6
M 1,32,2
WL 3
LO 122,16,122
LU 16,122
s_add_u32 s53, s53, 128
s_cmp_lt_u32 %[ca0], 128
s_cselect_b32 s73, s53, %[ca0]
M 1,33,3
RA 216,1,13312
HS 25,16
M 2,44,2
LL 18,100
LL 126,101
M 2,45,3
LA 18
v_mov_b32 v9, s73
M 3,56,2
LC_ 126
v_mov_b32 v11, s92
M 3,57,3
s_add_u32 s42, s44, s63
M 4,68,2
WL 3
LO 123,17,123
LU 17,123
M 4,69,3
s_addc_u32 s43, s45, 0
RA 220,1,14336
HS 25,17
M 5,80,2
LL 19,102
LL 127,103
M 5,81,3
LA 19
v_mad_u32_u24 v11, v0, s93, v11
M 0,22,4
LC_ 127
ST 116
M 0,23,5
v_lshrrev_b32 v11, 21, v11
M 1,34,4
WL 3
LO 126,18,126
LU 18,126
M 1,35,5
RA 224,1,15360
HS 25,18
M 2,46,4
LL 16,56
LL 104,57
M 2,47,5
LA 16
v_lshl_add_u32 v11, v11, 2, 64
M 3,58,4
LC_ 104
v_writelane_b32 v11, %[tsel], 63
M 3,59,5
s_add_u32 s42, s44, s64
M 4,70,4
WL 3
LO 127,19,127
LU 19,127
M 4,71,5
s_addc_u32 s43, s45, 0
RA 228,1,16384
HS 25,19
M 5,82,4
LL 17,58
LL 105,59
M 5,83,5
s_sub_u32 s53, s86, %[hlo]
LA 17
HX 25
s_lshr_b32 s53, s53, 2
s_and_b32 s92, %[xlo], 0x1fffff
s_add_u32 s53, s53, s92
s_add_u32 s53, s53, 0x20048000
s_lshr_b32 s53, s53, 21
s_lshl_b32 s92, s53, 2
Z 6,12,0
LC_ 105
ST 120
M 6,13,1
W1 25,1024
v_writelane_b32 v11, s92, 48
Z 7,24,0
WL 4
LO 104,16,104
LU 16,104
M 7,25,1
RA 232,1,17408
HZ 24,16
Z 8,36,0
LL 18,60
LL 108,61
M 8,37,1
LA 18
global_load_dword v11, v11, s[68:69] offset:-2048
Z 9,48,0
s_add_u32 s42, s44, s65
LC_ 108
v_add_u32 v4, s39, v4
M 9,49,1
s_addc_u32 s43, s45, 0
ST 124
Z 10,60,0
WL 4
LO 105,17,105
LU 17,105
M 10,61,1
s_mul_i32 s53, s50, 25600
RA 236,1,18432
HS 24,17
Z 11,72,0
LL 19,62
LL 109,63
M 11,73,1
s_add_u32 s40, %[wlo], s53
LA 19
M 6,14,2
s_addc_u32 s41, %[whi], 0
LC_ 109
M 6,15,3
s_mov_b32 m0, s38
s_nop 0
DW 0
M 7,26,2
WL 3
LO 108,18,108
LU 18,108
M 7,27,3
s_add_u32 s37, s37, 32768
RA 216,1,19456
HS 24,18
M 8,38,2
s_cmp_eq_u32 s37, s75
LL 16,64
LL 112,65
M 8,39,3
s_cselect_b32 s37, s74, s37
LA 16
DW 1024
M 9,50,2
LC_ 112
M 9,51,3
M 10,62,2
WL 3
LO 109,19,109
LU 19,109
M 10,63,3
RA 220,1,20480
HS 24,19
M 11,74,2
LL 17,66
LL 113,67
M 11,75,3
LA 17
DW 2048
M 6,16,4
LC_ 113
M 6,17,5
M 7,28,4
WL 3
LO 112,16,112
LU 16,112
M 7,29,5
s_mul_i32 s53, s51, 49152
RA 224,1,21504
HS 24,16
M 8,40,4
s_bitcmp1_b64 s[90:91], s51
LL 18,68
LL 116,69
M 8,41,5
s_cselect_b64 s[44:45], s[84:85], s[82:83]
LA 18
M 9,52,4
s_add_u32 s44, s44, s53
LC_ 116
M 9,53,5
s_addc_u32 s45, s45, 0
M 10,64,4
WL 3
LO 113,17,113
LU 17,113
M 10,65,5
s_add_u32 s51, s51, %[d]
RA 228,1,22528
HS 24,17
M 11,76,4
LL 19,70
LL 117,71
M 11,77,5
s_cmp_eq_u32 s51, %[wrap]
LA 19
M 6,18,0
s_cselect_b32 s51, %[rst], s51
LC_ 117
M 6,19,1
s_add_u32 s40, s40, 3072
M 7,30,0
WL 3
LO 116,18,116
LU 18,116
M 7,31,1
s_addc_u32 s41, s41, 0
RA 232,1,23552
HS 24,18
M 8,42,0
s_add_u32 m0, s38, 3072
LL 16,72
LL 120,73
M 8,43,1
LA 16
DW 0
M 9,54,0
LC_ 120
DW 1024
M 9,55,1
M 10,66,0
WL 3
LO 117,19,117
LU 19,117
M 10,67,1
RA 236,1,24576
HS 24,19
M 11,78,0
LL 17,74
LL 121,75
M 11,79,1
LA 17
DW 2048
M 6,20,2
LC_ 121
)"
R"(M 6,21,3
s_lshl_b32 s53, s52, 8
v_add_u32 v1, s37, v0
M 7,32,2
WL 3
LO 120,16,120
LU 16,120
M 7,33,3
s_add_u32 s48, %[alo], s53
RA 216,1,0
HS 24,16
M 8,44,2
s_addc_u32 s49, %[ahi], 0
LL 18,76
LL 124,77
M 8,45,3
s_add_u32 s52, s52, %[d]
LA 18
v_and_b32 v22, 0xfffffcff, v1
M 9,56,2
s_cmp_eq_u32 s52, %[wrap]
LC_ 124
M 9,57,3
s_cselect_b32 s52, %[rst], s52
DQ 3072
M 10,68,2
WL 3
LO 121,17,121
LU 17,121
M 10,69,3
s_add_u32 s50, s50, %[d]
RA 220,1,1024
HS 24,17
M 11,80,2
LL 19,78
LL 125,79
M 11,81,3
s_cmp_eq_u32 s50, %[wrap]
LA 19
RV 252,22,5120
M 6,22,4
s_cselect_b32 s50, %[rst], s50
LC_ 125
M 6,23,5
WV 31
M 7,34,4
WL 4
LO 124,18,124
LU 18,124
M 7,35,5
s_add_u32 s38, s38, 32768
RA 224,1,2048
HS 24,18
M 8,46,4
s_cmp_eq_u32 s38, s77
LL 16,80
LL 106,81
M 8,47,5
s_cselect_b32 s38, s76, s38
LA 16
M 9,58,4
s_barrier
LC_ 106
M 9,59,5
s_sub_u32 s39, 0, s39
M 10,70,4
WL 3
LO 125,19,125
LU 19,125
M 10,71,5
RA 228,1,3072
HS 24,19
M 11,82,4
LL 17,82
LL 107,83
M 11,83,5
LA 17
HX 24
Z 0,12,0
LC_ 107
ds_read_b64 v[14:15], v5 offset:0
M 0,13,1
W1 24,0
v_subrev_u32 v5, s39, v5
Z 1,24,0
WL 5
LO 106,16,106
LU 16,106
M 1,25,1
RA 232,1,4096
HZ 25,16
Z 2,36,0
LL 18,84
LL 110,85
M 2,37,1
LA 18
Z 3,48,0
LC_ 110
M 3,49,1
s_add_u32 s42, s44, s60
Z 4,60,0
WL 5
LO 107,17,107
LU 17,107
M 4,61,1
s_addc_u32 s43, s45, 0
RA 236,1,6144
HS 25,17
Z 5,72,0
LL 19,86
LL 111,87
M 5,73,1
LA 19
ST 104
M 0,14,2
WL 6
LC_ 111
v_add_f32 v14, v14, v15
M 0,15,3
s_nop 0
v_add_f32_dpp v14, v14, v14 quad_perm:[1,0,3,2] row_mask:0xf bank_mask:0xf
M 1,26,2
WL 3
LO 110,18,110
LU 18,110
M 1,27,3
RA 216,1,7168
HS 25,18
M 2,38,2
LL 16,88
LL 114,89
M 2,39,3
LA 16
v_add_f32_dpp v14, v14, v14 quad_perm:[2,3,0,1] row_mask:0xf bank_mask:0xf
M 3,50,2
LC_ 114
v_add_f32_dpp v14, v14, v14 row_half_mirror row_mask:0xf bank_mask:0xf
M 3,51,3
s_add_u32 s42, s44, s61
v_cvt_f64_f32 v[14:15], v14
M 4,62,2
WL 3
LO 111,19,111
LU 19,111
M 4,63,3
s_addc_u32 s43, s45, 0
RA 220,1,8192
HS 25,19
M 5,74,2
LL 17,90
LL 115,91
M 5,75,3
LA 17
s_mov_b64 exec, s[54:55]
SP
s_mov_b64 exec, -1
M 0,16,4
LC_ 115
ST 108
M 0,17,5
s_mov_b32 m0, %[pm]
s_nop 0
DF
M 1,28,4
WL 3
LO 114,16,114
LU 16,114
M 1,29,5
RA 224,1,9216
HS 25,16
M 2,40,4
LL 18,92
LL 118,93
M 2,41,5
LA 18
M 3,52,4
LC_ 118
M 3,53,5
s_add_u32 s42, s44, s62
M 4,64,4
WL 3
LO 115,17,115
LU 17,115
M 4,65,5
s_addc_u32 s43, s45, 0
RA 228,1,10240
HS 25,17
M 5,76,4
LL 19,94
LL 119,95
M 5,77,5
LA 19
ST 112
M 0,18,0
LC_ 119
M 0,19,1
M 1,30,0
WL 3
LO 118,18,118
LU 18,118
M 1,31,1
RA 232,1,11264
HS 25,18
M 2,42,0
LL 16,96
LL 122,97
M 2,43,1
LA 16
RV 252,22,5392
M 3,54,0
LC_ 122
M 3,55,1
s_add_u32 s42, s44, s63
M 4,66,0
WL 4
LO 119,19,119
LU 19,119
M 4,67,1
s_addc_u32 s43, s45, 0
RA 236,1,12288
HS 25,19
M 5,78,0
LL 17,98
LL 123,99
M 5,79,1
LA 17
ST 116
M 0,20,2
LC_ 123
M 0,21,3
M 1,32,2
WL 3
LO 122,16,122
LU 16,122
M 1,33,3
RA 216,1,13312
HS 25,16
M 2,44,2
LL 18,100
LL 126,101
M 2,45,3
LA 18
M 3,56,2
LC_ 126
M 3,57,3
s_add_u32 s42, s44, s64
M 4,68,2
WL 3
LO 123,17,123
LU 17,123
M 4,69,3
s_addc_u32 s43, s45, 0
RA 220,1,14336
HS 25,17
M 5,80,2
LL 19,102
LL 127,103
M 5,81,3
LA 19
ST 120
M 0,22,4
LC_ 127
M 0,23,5
M 1,34,4
WL 3
LO 126,18,126
LU 18,126
M 1,35,5
RA 224,1,15360
HS 25,18
M 2,46,4
LL 16,56
LL 104,57
M 2,47,5
LA 16
M 3,58,4
LC_ 104
M 3,59,5
s_add_u32 s42, s44, s65
M 4,70,4
WL 3
LO 127,19,127
LU 19,127
M 4,71,5
s_addc_u32 s43, s45, 0
RA 228,1,16384
HS 25,19
M 5,82,4
LL 17,58
LL 105,59
M 5,83,5
LA 17
ST 124
Z 6,12,0
s_and_b32 s53, %[w], 1
LC_ 105
HX 25
M 6,13,1
s_lshl_b32 s53, s53, 10
W1 25,1024
Z 7,24,0
WL 4
LO 104,16,104
LU 16,104
M 7,25,1
s_lshr_b32 s72, %[w], 1
RA 232,1,17408
HZ 24,16
Z 8,36,0
s_mul_i32 s72, s72, 32768
LL 18,60
LL 108,61
M 8,37,1
s_add_u32 s72, s72, s53
LA 18
v_add_u32 v4, s39, v4
Z 9,48,0
s_add_u32 s72, s72, 128000
LC_ 108
M 9,49,1
s_movk_i32 s53, 0x78
v_lshrrev_b32 v12, 5, v0
Z 10,60,0
WL 4
LO 105,17,105
LU 17,105
M 10,61,1
RA 236,1,18432
HS 24,17
Z 11,72,0
LL 19,62
LL 109,63
M 11,73,1
LA 19
v_and_b32 v12, 6, v12
M 6,14,2
LC_ 109
v_bfe_u32 v12, s53, v12, 2
M 6,15,3
v_lshrrev_b32 v20, 8, v0
M 7,26,2
WL 3
LO 108,18,108
LU 18,108
v_xor_b32 v20, v12, v20
v_lshl_add_u32 v20, v20, 4, s72
v_and_b32 v12, 0xf0, v0
v_lshl_add_u32 v20, v12, 2, v20
v_add_u32 v12, s72, v0
M 7,27,3
s_mul_i32 s53, s50, 25600
RA 216,1,19456
HS 24,18
M 8,38,2
s_add_u32 s40, %[wlo], s53
LL 16,64
LL 112,65
M 8,39,3
s_addc_u32 s41, %[whi], 0
LA 16
M 9,50,2
s_mov_b32 m0, s38
LC_ 112
DW 0
M 9,51,3
s_add_u32 s37, s37, 32768
M 10,62,2
WL 3
LO 109,19,109
LU 19,109
M 10,63,3
s_cmp_eq_u32 s37, s75
RA 220,1,20480
HS 24,19
M 11,74,2
LL 17,66
LL 113,67
M 11,75,3
s_cselect_b32 s37, s74, s37
LA 17
DW 1024
M 6,16,4
LC_ 113
DW 2048
M 6,17,5
M 7,28,4
WL 3
LO 112,16,112
LU 16,112
M 7,29,5
s_mul_i32 s53, s51, 49152
RA 224,1,21504
HS 24,16
M 8,40,4
s_bitcmp1_b64 s[90:91], s51
LL 18,68
LL 116,69
M 8,41,5
s_cselect_b64 s[44:45], s[84:85], s[82:83]
LA 18
)"
R"(M 9,52,4
s_add_u32 s44, s44, s53
LC_ 116
M 9,53,5
s_addc_u32 s45, s45, 0
M 10,64,4
WL 3
LO 113,17,113
LU 17,113
M 10,65,5
s_add_u32 s51, s51, %[d]
RA 228,1,22528
HS 24,17
M 11,76,4
LL 19,70
LL 117,71
M 11,77,5
s_cmp_eq_u32 s51, %[wrap]
LA 19
M 6,18,0
s_cselect_b32 s51, %[rst], s51
LC_ 117
M 6,19,1
s_add_u32 s40, s40, 3072
M 7,30,0
WL 3
LO 116,18,116
LU 18,116
M 7,31,1
s_addc_u32 s41, s41, 0
RA 232,1,23552
HS 24,18
M 8,42,0
s_add_u32 m0, s38, 3072
LL 16,72
LL 120,73
M 8,43,1
LA 16
DW 0
M 9,54,0
LC_ 120
DW 1024
M 9,55,1
M 10,66,0
WL 3
LO 117,19,117
LU 19,117
M 10,67,1
RA 236,1,24576
HS 24,19
M 11,78,0
LL 17,74
LL 121,75
M 11,79,1
LA 17
DW 2048
M 6,20,2
LC_ 121
M 6,21,3
WV 22
v_add_u32 v1, s37, v0
v_bcnt_u32_b32 v11, v11, 0
M 7,32,2
WL 3
LO 120,16,120
LU 16,120
M 7,33,3
s_lshl_b32 s53, s52, 8
RA 216,1,0
HS 24,16
M 8,44,2
s_add_u32 s48, %[alo], s53
LL 18,76
LL 124,77
M 8,45,3
s_addc_u32 s49, %[ahi], 0
LA 18
v_and_b32 v22, 0xfffffcff, v1
M 9,56,2
s_add_u32 s52, s52, %[d]
LC_ 124
M 9,57,3
s_cmp_eq_u32 s52, %[wrap]
M 10,68,2
WL 3
LO 121,17,121
LU 17,121
M 10,69,3
s_cselect_b32 s52, %[rst], s52
RA 220,1,1024
HS 24,17
M 11,80,2
LL 19,78
LL 125,79
M 11,81,3
LA 19
RV 252,22,5120
M 6,22,4
LC_ 125
DQ 3072
M 6,23,5
v_cmp_lt_u32 s[92:93], 2, v11
M 7,34,4
WL 4
LO 124,18,124
LU 18,124
M 7,35,5
s_nop 0
RA 224,1,2048
HS 24,18
M 8,46,4
s_sub_u32 s53, 0, s96
LL 16,80
LL 106,81
M 8,47,5
s_xor_b32 s92, s92, s53
LA 16
M 9,58,4
s_xor_b32 s93, s93, s53
LC_ 106
M 9,59,5
s_add_u32 s50, s50, %[d]
M 10,70,4
WL 3
LO 125,19,125
LU 19,125
M 10,71,5
RA 228,1,3072
HS 24,19
M 11,82,4
LL 17,82
LL 107,83
M 11,83,5
LA 17
HX 24
s_cmp_eq_u32 s50, %[wrap]
s_cselect_b32 s50, %[rst], s50
Z 0,12,0
s_add_u32 s38, s38, 32768
LC_ 107
M 0,13,1
s_cmp_eq_u32 s38, s77
Z 1,24,0
WL 3
LO 106,16,106
LU 16,106
M 1,25,1
s_cselect_b32 s38, s76, s38
RA 232,1,4096
HZ 25,16
Z 2,36,0
s_barrier
LL 18,84
LL 110,85
M 2,37,1
s_sub_u32 s39, 0, s39
LA 18
ds_read_b64 v[14:15], v5 offset:0
Z 3,48,0
LC_ 110
W1 24,0
M 3,49,1
s_add_u32 s42, s44, s60
v_subrev_u32 v5, s39, v5
Z 4,60,0
WL 5
LO 107,17,107
LU 17,107
M 4,61,1
s_addc_u32 s43, s45, 0
RA 236,1,6144
HS 25,17
Z 5,72,0
LL 19,86
LL 111,87
M 5,73,1
LA 19
ST 104
M 0,14,2
LC_ 111
M 0,15,3
M 1,26,2
WL 4
LO 110,18,110
LU 18,110
M 1,27,3
RA 216,1,7168
HS 25,18
M 2,38,2
LL 16,88
LL 114,89
M 2,39,3
LA 16
v_add_f32 v14, v14, v15
M 3,50,2
LC_ 114
v_add_f32_dpp v14, v14, v14 quad_perm:[1,0,3,2] row_mask:0xf bank_mask:0xf
M 3,51,3
s_add_u32 s42, s44, s61
v_add_f32_dpp v14, v14, v14 quad_perm:[2,3,0,1] row_mask:0xf bank_mask:0xf
M 4,62,2
WL 3
LO 111,19,111
LU 19,111
M 4,63,3
s_addc_u32 s43, s45, 0
RA 220,1,8192
HS 25,19
M 5,74,2
LL 17,90
LL 115,91
M 5,75,3
LA 17
ST 108
M 0,16,4
s_mov_b32 m0, %[pm]
LC_ 115
DF
v_add_f32_dpp v14, v14, v14 row_half_mirror row_mask:0xf bank_mask:0xf
M 0,17,5
v_cvt_f64_f32 v[14:15], v14
M 1,28,4
WL 3
LO 114,16,114
LU 16,114
M 1,29,5
RA 224,1,9216
HS 25,16
M 2,40,4
LL 18,92
LL 118,93
s_mov_b64 exec, s[54:55]
SP
s_mov_b64 exec, -1
M 2,41,5
LA 18
M 3,52,4
LC_ 118
M 3,53,5
s_add_u32 s42, s44, s62
M 4,64,4
WL 3
LO 115,17,115
LU 17,115
M 4,65,5
s_addc_u32 s43, s45, 0
RA 228,1,10240
HS 25,17
M 5,76,4
LL 19,94
LL 119,95
M 5,77,5
LA 19
ST 112
M 0,18,0
LC_ 119
M 0,19,1
M 1,30,0
WL 3
LO 118,18,118
LU 18,118
M 1,31,1
RA 232,1,11264
HS 25,18
M 2,42,0
LL 16,96
LL 122,97
M 2,43,1
LA 16
RV 252,22,5392
M 3,54,0
LC_ 122
M 3,55,1
s_add_u32 s42, s44, s63
M 4,66,0
WL 4
LO 119,19,119
LU 19,119
M 4,67,1
s_addc_u32 s43, s45, 0
RA 236,1,12288
HS 25,19
M 5,78,0
LL 17,98
LL 123,99
M 5,79,1
LA 17
ST 116
M 0,20,2
LC_ 123
M 0,21,3
M 1,32,2
WL 3
LO 122,16,122
LU 16,122
M 1,33,3
RA 216,1,13312
HS 25,16
M 2,44,2
LL 18,100
LL 126,101
M 2,45,3
LA 18
M 3,56,2
LC_ 126
M 3,57,3
s_add_u32 s42, s44, s64
M 4,68,2
WL 3
LO 123,17,123
LU 17,123
M 4,69,3
s_addc_u32 s43, s45, 0
RA 220,1,14336
HS 25,17
M 5,80,2
LL 19,102
LL 127,103
M 5,81,3
LA 19
ST 120
M 0,22,4
LC_ 127
M 0,23,5
M 1,34,4
WL 3
LO 126,18,126
LU 18,126
M 1,35,5
RA 224,1,15360
HS 25,18
M 2,46,4
LL 16,56
LL 104,57
M 2,47,5
LA 16
M 3,58,4
LC_ 104
M 3,59,5
s_add_u32 s42, s44, s65
M 4,70,4
WL 3
LO 127,19,127
LU 19,127
M 4,71,5
s_addc_u32 s43, s45, 0
RA 228,1,16384
HS 25,19
M 5,82,4
LL 17,58
LL 105,59
M 5,83,5
LA 17
ST 124
Z 6,12,0
LC_ 105
HX 25
M 6,13,1
W1 25,1024
Z 7,24,0
WL 4
LO 104,16,104
LU 16,104
M 7,25,1
RA 232,1,17408
HZ 24,16
Z 8,36,0
LL 18,60
LL 108,61
M 8,37,1
s_mul_i32 s53, s50, 25600
LA 18
v_add_u32 v4, s39, v4
Z 9,48,0
s_add_u32 s40, %[wlo], s53
LC_ 108
M 9,49,1
s_addc_u32 s41, %[whi], 0
Z 10,60,0
WL 4
LO 105,17,105
LU 17,105
M 10,61,1
s_mov_b32 m0, s38
RA 236,1,18432
HS 24,17
Z 11,72,0
LL 19,62
LL 109,63
M 11,73,1
LA 19
DW 0
M 6,14,2
LC_ 109
M 6,15,3
M 7,26,2
WL 3
LO 108,18,108
LU 18,108
M 7,27,3
)"
R"(s_add_u32 s37, s37, 32768
RA 216,1,19456
HS 24,18
M 8,38,2
s_cmp_eq_u32 s37, s75
LL 16,64
LL 112,65
M 8,39,3
s_cselect_b32 s37, s74, s37
LA 16
M 9,50,2
s_mul_i32 s53, %[d], 0
LC_ 112
M 9,51,3
s_add_u32 s53, s53, s57
M 10,62,2
WL 3
LO 109,19,109
LU 19,109
M 10,63,3
s_add_u32 s46, %[xlo], s53
RA 220,1,20480
HS 24,19
M 11,74,2
LL 17,66
LL 113,67
M 11,75,3
s_addc_u32 s47, %[xhi], 0
LA 17
M 6,16,4
s_mov_b32 s53, 0xfe987610
LC_ 113
M 6,17,5
s_cmp_eq_u32 %[d], 1
M 7,28,4
WL 3
LO 112,16,112
LU 16,112
M 7,29,5
s_cselect_b32 s53, s53, 0xda43cb52
RA 224,1,21504
HS 24,16
M 8,40,4
s_bitcmp1_b64 s[92:93], 48
LL 18,68
LL 116,69
M 8,41,5
s_cselect_b32 s58, 16, 0
LA 18
M 9,52,4
s_lshr_b32 s58, s53, s58
LC_ 116
v_lshrrev_b32 v3, 6, v0
v_and_b32 v3, 12, v3
v_bfe_u32 v3, s58, v3, 4
v_mad_u32_u24 v3, v3, s97, v21
M 9,53,5
DW 1024
M 10,64,4
WL 3
LO 113,17,113
LU 17,113
M 10,65,5
RA 228,1,22528
HS 24,17
M 11,76,4
LL 19,70
LL 117,71
M 11,77,5
LA 19
XL 0,0
M 6,18,0
LC_ 117
DW 2048
M 6,19,1
s_mul_i32 s53, s51, 49152
M 7,30,0
WL 3
LO 116,18,116
LU 18,116
M 7,31,1
s_bitcmp1_b64 s[90:91], s51
RA 232,1,23552
HS 24,18
M 8,42,0
s_cselect_b64 s[44:45], s[84:85], s[82:83]
LL 16,72
LL 120,73
M 8,43,1
s_add_u32 s44, s44, s53
LA 16
s_addc_u32 s45, s45, 0
s_add_u32 s51, s51, %[d]
s_cmp_eq_u32 s51, %[wrap]
s_cselect_b32 s51, %[rst], s51
XL 1,64
M 9,54,0
s_add_u32 s40, s40, 3072
LC_ 120
s_addc_u32 s41, s41, 0
s_add_u32 m0, s38, 3072
M 9,55,1
XL 2,128
M 10,66,0
WL 3
LO 117,19,117
LU 19,117
M 10,67,1
RA 236,1,24576
HS 24,19
M 11,78,0
LL 17,74
LL 121,75
M 11,79,1
LA 17
DW 0
M 6,20,2
LC_ 121
XL 3,192
M 6,21,3
DW 1024
v_add_u32 v1, s37, v0
M 7,32,2
WL 3
LO 120,16,120
LU 16,120
M 7,33,3
RA 216,1,0
HS 24,16
M 8,44,2
LL 18,76
LL 124,77
M 8,45,3
LA 18
XL 4,256
M 9,56,2
LC_ 124
DW 2048
M 9,57,3
s_lshl_b32 s53, s52, 8
v_and_b32 v22, 0xfffffcff, v1
M 10,68,2
WL 3
LO 121,17,121
LU 17,121
M 10,69,3
s_add_u32 s48, %[alo], s53
RA 220,1,1024
HS 24,17
M 11,80,2
LL 19,78
LL 125,79
M 11,81,3
s_addc_u32 s49, %[ahi], 0
LA 19
RV 252,22,5120
M 6,22,4
s_add_u32 s52, s52, %[d]
LC_ 125
M 6,23,5
s_cmp_eq_u32 s52, %[wrap]
M 7,34,4
WL 4
LO 124,18,124
LU 18,124
M 7,35,5
s_cselect_b32 s52, %[rst], s52
RA 224,1,2048
HS 24,18
M 8,46,4
LL 16,80
LL 106,81
M 8,47,5
LA 16
XL 5,320
DQ 3072
M 9,58,4
LC_ 106
XL 6,384
M 9,59,5
s_add_u32 s50, s50, %[d]
M 10,70,4
WL 3
LO 125,19,125
LU 19,125
M 10,71,5
RA 228,1,3072
HS 24,19
M 11,82,4
LL 17,82
LL 107,83
M 11,83,5
LA 17
HX 24
s_cmp_eq_u32 s50, %[wrap]
s_cselect_b32 s50, %[rst], s50
Z 0,12,0
LC_ 107
XL 7,448
M 0,13,1
s_add_u32 s38, s38, 32768
WV 12
BW 0
Z 1,24,0
WL 4
LO 106,16,106
LU 16,106
M 1,25,1
s_cmp_eq_u32 s38, s77
RA 232,1,4096
HZ 25,16
Z 2,36,0
s_cselect_b32 s38, s76, s38
LL 18,84
LL 110,85
M 2,37,1
s_barrier
LA 18
RV 40,8,6144
Z 3,48,0
s_sub_u32 s39, 0, s39
LC_ 110
ds_read_b64 v[14:15], v5 offset:0
M 3,49,1
XL 8,512
RV 44,8,6208
Z 4,60,0
WL 7
LO 107,17,107
LU 17,107
M 4,61,1
RA 236,1,6144
HS 25,17
Z 5,72,0
LL 19,86
LL 111,87
M 5,73,1
LA 19
W1 24,0
M 0,14,2
LC_ 111
XL 9,576
M 0,15,3
s_add_u32 s42, s44, s60
RV 48,8,6272
BR 0
M 1,26,2
WL 8
LO 110,18,110
LU 18,110
M 1,27,3
s_addc_u32 s43, s45, 0
RA 216,1,7168
HS 25,18
M 2,38,2
LL 16,88
LL 114,89
M 2,39,3
LA 16
XL 10,640
M 3,50,2
LC_ 114
ST 104
M 3,51,3
XL 11,704
v_subrev_u32 v5, s39, v5
M 4,62,2
WL 5
LO 111,19,111
LU 19,111
M 4,63,3
s_add_u32 s42, s44, s61
RA 220,1,8192
HS 25,19
M 5,74,2
LL 17,90
LL 115,91
M 5,75,3
s_addc_u32 s43, s45, 0
LA 17
ST 108
M 0,16,4
s_mov_b32 m0, %[pm]
LC_ 115
DF
M 0,17,5
WV 17
BW 1
M 1,28,4
WL 4
LO 114,16,114
LU 16,114
M 1,29,5
RA 224,1,9216
HS 25,16
M 2,40,4
LL 18,92
LL 118,93
M 2,41,5
LA 18
BR 1
LZ 40,0
WV 16
BW 2
M 3,52,4
LC_ 118
RV 40,8,6336
M 3,53,5
s_add_u32 s42, s44, s62
BR 2
v_add_f32 v14, v14, v15
s_nop 1
v_add_f32_dpp v14, v14, v14 quad_perm:[1,0,3,2] row_mask:0xf bank_mask:0xf
s_nop 1
v_add_f32_dpp v14, v14, v14 quad_perm:[2,3,0,1] row_mask:0xf bank_mask:0xf
s_nop 1
v_add_f32_dpp v14, v14, v14 row_half_mirror row_mask:0xf bank_mask:0xf
v_cvt_f64_f32 v[14:15], v14
s_mov_b64 exec, s[54:55]
SP
s_mov_b64 exec, -1
M 4,64,4
WL 8
LO 115,17,115
LU 17,115
M 4,65,5
s_addc_u32 s43, s45, 0
RA 228,1,10240
HS 25,17
M 5,76,4
LL 19,94
LL 119,95
M 5,77,5
LA 19
ST 112
M 0,18,0
LC_ 119
M 0,19,1
WL 7
LS 44,1
WV 16
BW 3
M 1,30,0
WL 6
LO 118,18,118
LU 18,118
M 1,31,1
RA 232,1,11264
HS 25,18
M 2,42,0
LL 16,96
LL 122,97
M 2,43,1
WV 14
LA 16
RV 252,22,5392
M 3,54,0
WL 7
LC_ 122
RV 44,8,6400
M 3,55,1
s_add_u32 s42, s44, s63
BR 3
LS 48,2
s_addc_u32 s43, s45, 0
BW 4
M 4,66,0
WL 8
LO 119,19,119
LU 19,119
M 4,67,1
RA 236,1,12288
HS 25,19
M 5,78,0
LL 17,98
LL 123,99
M 5,79,1
LA 17
ST 116
M 0,20,2
LC_ 123
RV 48,8,6464
M 0,21,3
BR 4
M 1,32,2
WL 8
LO 122,16,122
LU 16,122
M 1,33,3
WL 6
RA 216,1,13312
HS 25,16
LS 40,3
WV 13
BW 5
M 2,44,2
LL 18,100
LL 126,101
M 2,45,3
LA 18
)"
R"(RV 40,8,6528
M 3,56,2
LC_ 126
BR 5
M 3,57,3
s_add_u32 s42, s44, s64
M 4,68,2
WL 6
LO 123,17,123
LU 17,123
M 4,69,3
s_addc_u32 s43, s45, 0
RA 220,1,14336
HS 25,17
LS 44,4
WV 11
BW 6
M 5,80,2
LL 19,102
LL 127,103
M 5,81,3
LA 19
ST 120
M 0,22,4
LC_ 127
RV 44,8,6592
M 0,23,5
BR 6
M 1,34,4
WL 7
LO 126,18,126
LU 18,126
M 1,35,5
WL 6
RA 224,1,15360
HS 25,18
LS 48,5
WV 11
BW 7
M 2,46,4
LL 16,56
LL 104,57
M 2,47,5
LA 16
RV 48,8,6656
M 3,58,4
LC_ 104
BR 7
M 3,59,5
s_add_u32 s42, s44, s65
M 4,70,4
WL 6
LO 127,19,127
LU 19,127
M 4,71,5
s_addc_u32 s43, s45, 0
RA 228,1,16384
HS 25,19
LS 40,6
WV 10
BW 8
M 5,82,4
LL 17,58
LL 105,59
M 5,83,5
LA 17
ST 124
Z 6,12,0
LC_ 105
RV 40,8,6720
M 6,13,1
BR 8
HX 25
Z 7,24,0
WL 7
LO 104,16,104
LU 16,104
M 7,25,1
WL 6
RA 232,1,17408
HZ 24,16
LS 44,7
WV 10
BW 9
Z 8,36,0
LL 18,60
LL 108,61
M 8,37,1
s_mul_i32 s53, s50, 25600
LA 18
W1 25,1024
Z 9,48,0
s_add_u32 s40, %[wlo], s53
LC_ 108
RV 44,8,6784
M 9,49,1
s_addc_u32 s41, %[whi], 0
BR 9
v_add_u32 v4, s39, v4
Z 10,60,0
WL 7
LO 105,17,105
LU 17,105
M 10,61,1
s_mov_b32 m0, s38
RA 236,1,18432
HS 24,17
LS 48,8
WV 9
BW 10
Z 11,72,0
LL 19,62
LL 109,63
M 11,73,1
LA 19
DW 0
M 6,14,2
LC_ 109
RV 48,8,6848
M 6,15,3
WV 8
RA 240,7,0
BR 10
M 7,26,2
WL 7
LO 108,18,108
LU 18,108
M 7,27,3
s_add_u32 s37, s37, 32768
RA 216,1,19456
HS 24,18
LS 40,9
s_cmp_eq_u32 s37, s75
BW 11
M 8,38,2
s_cselect_b32 s37, s74, s37
LL 16,64
LL 112,65
M 8,39,3
LA 16
DW 1024
M 9,50,2
LC_ 112
RA 244,7,256
M 9,51,3
BR 11
M 10,62,2
WL 9
LO 109,19,109
LU 19,109
M 10,63,3
RA 220,1,20480
HS 24,19
M 11,74,2
LL 17,66
LL 113,67
M 11,75,3
WL 7
LA 17
DW 2048
LS 44,10
RA 248,7,512
M 6,16,4
LC_ 113
M 6,17,5
M 7,28,4
WL 4
LO 112,16,112
LU 16,112
M 7,29,5
s_mul_i32 s53, s51, 49152
RA 224,1,21504
HS 24,16
LS 48,11
s_bitcmp1_b64 s[90:91], s51
RA 252,7,768
M 8,40,4
s_cselect_b64 s[44:45], s[84:85], s[82:83]
LL 18,68
LL 116,69
M 8,41,5
s_add_u32 s44, s44, s53
LA 18
v_mul_f32 v36, 0xbb2aaaab, v36
M 9,52,4
s_addc_u32 s45, s45, 0
LC_ 116
v_mul_f32 v37, 0xbb2aaaab, v37
M 9,53,5
s_add_u32 s51, s51, %[d]
v_mul_f32 v38, 0xbb2aaaab, v38
v_mul_f32 v39, 0xbb2aaaab, v39
M 10,64,4
WL 4
LO 113,17,113
LU 17,113
M 10,65,5
s_cmp_eq_u32 s51, %[wrap]
RA 228,1,22528
HS 24,17
LK 40,240,0,0
LK 44,242,0,2
s_cselect_b32 s51, %[rst], s51
RA 240,7,1024
LK 48,244,1,0
s_nop 0
CF 128,40,41
LK 248,246,1,2
s_add_u32 s40, s40, 3072
RA 244,7,1280
CF 129,42,43
M 11,76,4
LL 19,70
LL 117,71
M 11,77,5
s_addc_u32 s41, s41, 0
LA 19
CF 130,44,45
M 6,18,0
s_add_u32 m0, s38, 3072
LC_ 117
DW 0
M 6,19,1
CF 131,46,47
LK 40,248,2,0
CF 132,48,49
LK 44,250,2,2
RA 248,7,1536
CF 133,50,51
GZ 0
WL 8
CF 134,248,249
CF 135,250,251
LK 48,252,3,0
CF 136,40,41
LK 248,254,3,2
RA 252,7,1792
CF 137,42,43
GS 1
WL 5
CF 138,44,45
CF 139,46,47
LK 40,240,4,0
CF 140,48,49
LK 44,242,4,2
RA 240,7,2048
CF 141,50,51
GS 2
WL 5
CF 142,248,249
CF 143,250,251
LK 48,244,5,0
CF 144,40,41
LK 248,246,5,2
RA 244,7,2304
CF 145,42,43
GS 3
CF 146,44,45
CF 147,46,47
M 7,30,0
LO 116,18,116
LU 18,116
M 7,31,1
WL 3
RA 232,1,23552
HS 24,18
LK 40,248,6,0
CF 148,48,49
LK 44,250,6,2
RA 248,7,2560
CF 149,50,51
GS 4
WL 4
CF 150,248,249
CF 151,250,251
LK 48,252,7,0
CF 152,40,41
LK 248,254,7,2
RA 252,7,2816
CF 153,42,43
GS 5
WL 4
CF 154,44,45
CF 155,46,47
LK 40,240,8,0
CF 156,48,49
LK 44,242,8,2
CF 157,50,51
GS 6
CF 158,248,249
CF 159,250,251
M 8,42,0
LL 16,72
LL 120,73
M 8,43,1
LA 16
DW 1024
M 9,54,0
LC_ 120
M 9,55,1
WL 5
LK 48,244,9,0
CF 160,40,41
LK 248,246,9,2
CF 161,42,43
GS 7
CF 162,44,45
CF 163,46,47
M 10,66,0
LO 117,19,117
LU 19,117
M 10,67,1
RA 236,1,24576
HS 24,19
M 11,78,0
LL 17,74
LL 121,75
M 11,79,1
LA 17
DW 2048
M 6,20,2
WL 5
LC_ 121
M 6,21,3
s_lshl_b32 s53, s52, 8
v_add_u32 v1, s37, v0
LK 40,248,10,0
CF 164,48,49
LK 44,250,10,2
s_add_u32 s48, %[alo], s53
CF 165,50,51
GS 8
s_addc_u32 s49, %[ahi], 0
CF 166,248,249
CF 167,250,251
M 7,32,2
WL 3
LO 120,16,120
LU 16,120
M 7,33,3
s_add_u32 s52, s52, %[d]
RA 216,1,0
HS 24,16
M 8,44,2
s_cmp_eq_u32 s52, %[wrap]
LL 18,76
LL 124,77
M 8,45,3
s_cselect_b32 s52, %[rst], s52
LA 18
DQ 3072
M 9,56,2
LC_ 124
v_and_b32 v22, 0xfffffcff, v1
M 9,57,3
LK 48,252,11,0
CF 168,40,41
LK 248,254,11,2
CF 169,42,43
GS 9
CF 170,44,45
CF 171,46,47
M 10,68,2
WL 3
LO 121,17,121
LU 17,121
M 10,69,3
s_add_u32 s50, s50, %[d]
RA 220,1,1024
HS 24,17
M 11,80,2
LL 19,78
LL 125,79
M 11,81,3
s_cmp_eq_u32 s50, %[wrap]
LA 19
RV 252,22,5120
M 6,22,4
s_cselect_b32 s50, %[rst], s50
LC_ 125
RV 36,8,6912
M 6,23,5
M 7,34,4
WL 5
LO 124,18,124
LU 18,124
M 7,35,5
s_add_u32 s38, s38, 32768
RA 224,1,2048
HS 24,18
GS 10
s_cmp_eq_u32 s38, s77
CF 172,48,49
CF 173,50,51
M 8,46,4
s_cselect_b32 s38, s76, s38
LL 16,80
LL 106,81
M 8,47,5
s_barrier
)"
R"(LA 16
CF 174,248,249
M 9,58,4
s_sub_u32 s39, 0, s39
LC_ 106
ds_read_b64 v[14:15], v5 offset:0
M 9,59,5
s_nop 0
v_subrev_u32 v5, s39, v5
CF 175,250,251
M 10,70,4
WL 5
LO 125,19,125
LU 19,125
M 10,71,5
RA 228,1,3072
HS 24,19
GS 11
M 11,82,4
LL 17,82
LL 107,83
M 11,83,5
LA 17
HX 24
Z 0,12,0
LC_ 107
W1 24,0
M 0,13,1
v_cndmask_b32 v52, v52, v53, s[78:79]
v_cndmask_b32 v54, v54, v55, s[78:79]
Z 1,24,0
WL 5
LO 106,16,106
LU 16,106
M 1,25,1
RA 232,1,4096
HZ 25,16
Z 2,36,0
LL 18,84
LL 110,85
M 2,37,1
WL 5
LA 18
v_add_f32 v14, v14, v15
Z 3,48,0
LC_ 110
v_add_f32_dpp v14, v14, v14 quad_perm:[1,0,3,2] row_mask:0xf bank_mask:0xf
M 3,49,1
s_add_u32 s42, s44, s60
v_add_f32_dpp v14, v14, v14 quad_perm:[2,3,0,1] row_mask:0xf bank_mask:0xf
v_cndmask_b32 v52, v52, v54, s[80:81]
Z 4,60,0
WL 4
LO 107,17,107
LU 17,107
M 4,61,1
s_addc_u32 s43, s45, 0
RA 236,1,6144
HS 25,17
Z 5,72,0
LL 19,86
LL 111,87
M 5,73,1
LA 19
ST 104
M 0,14,2
LC_ 111
v_add_f32_dpp v14, v14, v14 row_half_mirror row_mask:0xf bank_mask:0xf
M 0,15,3
v_cvt_f64_f32 v[14:15], v14
v_mul_f32 v52, 0x3b2aaaab, v52
M 1,26,2
WL 3
LO 110,18,110
LU 18,110
M 1,27,3
RA 216,1,7168
HS 25,18
M 2,38,2
LL 16,88
LL 114,89
M 2,39,3
LA 16
s_mov_b64 exec, s[54:55]
SP
s_mov_b64 exec, -1
M 3,50,2
LC_ 114
C 52,52,52
M 3,51,3
s_add_u32 s42, s44, s61
v_lshlrev_b32 v52, 16, v52
v_add_f32 v52, v36, v52
M 4,62,2
WL 3
LO 111,19,111
LU 19,111
M 4,63,3
s_addc_u32 s43, s45, 0
RA 220,1,8192
HS 25,19
M 5,74,2
LL 17,90
LL 115,91
M 5,75,3
LA 17
ST 108
M 0,16,4
s_mov_b32 m0, %[pm]
LC_ 115
DF
M 0,17,5
C 52,52,52
v_lshlrev_b32 v52, 16, v52
M 1,28,4
WL 3
LO 114,16,114
LU 16,114
M 1,29,5
RA 224,1,9216
HS 25,16
M 2,40,4
LL 18,92
LL 118,93
M 2,41,5
LA 18
M 3,52,4
LC_ 118
M 3,53,5
s_add_u32 s42, s44, s62
v_sqrt_f32 v52, v52
M 4,64,4
WL 3
LO 115,17,115
LU 17,115
M 4,65,5
s_addc_u32 s43, s45, 0
RA 228,1,10240
HS 25,17
M 5,76,4
LL 19,94
LL 119,95
M 5,77,5
LA 19
ST 112
M 0,18,0
LC_ 119
C 52,52,52
M 0,19,1
v_lshlrev_b32 v52, 16, v52
v_cmp_ge_f32 vcc, v37, v52
M 1,30,0
WL 3
LO 118,18,118
LU 18,118
M 1,31,1
RA 232,1,11264
HS 25,18
M 2,42,0
LL 16,96
LL 122,97
M 2,43,1
LA 16
RV 252,22,5392
M 3,54,0
LC_ 122
M 3,55,1
s_add_u32 s42, s44, s63
v_rcp_f32 v52, v52
M 4,66,0
WL 4
LO 119,19,119
LU 19,119
M 4,67,1
s_addc_u32 s43, s45, 0
RA 236,1,12288
HS 25,19
M 5,78,0
LL 17,98
LL 123,99
M 5,79,1
LA 17
ST 116
M 0,20,2
LC_ 123
v_mul_f32 v52, v38, v52
M 0,21,3
v_cndmask_b32 v52, 0, v52, vcc
CF 52,52,52
M 1,32,2
WL 3
LO 122,16,122
LU 16,122
M 1,33,3
RA 216,1,13312
HS 25,16
M 2,44,2
LL 18,100
LL 126,101
M 2,45,3
LA 18
ds_bpermute_b32 v13, v28, v52
M 3,56,2
LC_ 126
M 3,57,3
s_add_u32 s42, s44, s64
M 4,68,2
WL 4
LO 123,17,123
LU 17,123
M 4,69,3
s_addc_u32 s43, s45, 0
RA 220,1,14336
HS 25,17
M 5,80,2
LL 19,102
LL 127,103
M 5,81,3
LA 19
ST 120
M 0,22,4
LC_ 127
M 0,23,5
M 1,34,4
WL 3
LO 126,18,126
LU 18,126
M 1,35,5
RA 224,1,15360
HS 25,18
M 2,46,4
LL 16,56
LL 104,57
M 2,47,5
LA 16
XS 128
M 3,58,4
LC_ 104
XS 129
M 3,59,5
s_add_u32 s42, s44, s65
XS 130
XS 131
M 4,70,4
WL 3
LO 127,19,127
LU 19,127
M 4,71,5
s_addc_u32 s43, s45, 0
RA 228,1,16384
HS 25,19
M 5,82,4
LL 17,58
LL 105,59
M 5,83,5
LA 17
ST 124
Z 6,12,0
LC_ 105
HX 25
M 6,13,1
W1 25,1024
XS 132
Z 7,24,0
WL 4
LO 104,16,104
LU 16,104
M 7,25,1
RA 232,1,17408
HZ 24,16
Z 8,36,0
LL 18,60
LL 108,61
M 8,37,1
s_mul_i32 s53, s50, 25600
LA 18
v_add_u32 v4, s39, v4
Z 9,48,0
s_add_u32 s40, %[wlo], s53
LC_ 108
XS 133
M 9,49,1
s_addc_u32 s41, %[whi], 0
XS 134
XS 135
Z 10,60,0
WL 4
LO 105,17,105
LU 17,105
M 10,61,1
s_mov_b32 m0, s38
RA 236,1,18432
HS 24,17
Z 11,72,0
LL 19,62
LL 109,63
M 11,73,1
LA 19
DW 0
M 6,14,2
LC_ 109
v_min_u32 v35, v35, v13
M 6,15,3
XS 136
XS 137
M 7,26,2
WL 3
LO 108,18,108
LU 18,108
M 7,27,3
s_add_u32 s37, s37, 32768
RA 216,1,19456
HS 24,18
M 8,38,2
s_cmp_eq_u32 s37, s75
LL 16,64
LL 112,65
M 8,39,3
s_cselect_b32 s37, s74, s37
LA 16
XS 138
M 9,50,2
s_mul_i32 s53, %[d], 0
LC_ 112
XS 139
M 9,51,3
s_add_u32 s53, s53, s57
XS 140
XS 141
M 10,62,2
WL 3
LO 109,19,109
LU 19,109
M 10,63,3
s_add_u32 s46, %[xlo], s53
RA 220,1,20480
HS 24,19
M 11,74,2
LL 17,66
LL 113,67
M 11,75,3
s_addc_u32 s47, %[xhi], 0
LA 17
XS 142
M 6,16,4
s_mov_b32 s53, 0xda43cb52
LC_ 113
XS 143
M 6,17,5
s_cmp_eq_u32 %[d], 1
XS 144
XS 145
M 7,28,4
WL 3
LO 112,16,112
LU 16,112
M 7,29,5
s_cselect_b32 s53, s53, 0xfe987610
RA 224,1,21504
HS 24,16
M 8,40,4
s_bitcmp1_b64 s[92:93], 48
LL 18,68
LL 116,69
M 8,41,5
s_cselect_b32 s58, 16, 0
LA 18
XS 146
M 9,52,4
s_lshr_b32 s58, s53, s58
LC_ 116
v_lshrrev_b32 v3, 6, v0
v_and_b32 v3, 12, v3
v_bfe_u32 v3, s58, v3, 4
v_mad_u32_u24 v3, v3, s97, v21
M 9,53,5
DW 1024
XS 147
M 10,64,4
WL 3
LO 113,17,113
LU 17,113
M 10,65,5
RA 228,1,22528
HS 24,17
M 11,76,4
LL 19,70
LL 117,71
M 11,77,5
LA 19
XL 12,0
M 6,18,0
LC_ 117
DW 2048
M 6,19,1
s_mul_i32 s53, s51, 49152
XS 148
XS 149
M 7,30,0
WL 3
)"
R"(LO 116,18,116
LU 18,116
M 7,31,1
s_bitcmp1_b64 s[90:91], s51
RA 232,1,23552
HS 24,18
M 8,42,0
s_cselect_b64 s[44:45], s[84:85], s[82:83]
LL 16,72
LL 120,73
M 8,43,1
s_add_u32 s44, s44, s53
LA 16
s_addc_u32 s45, s45, 0
s_add_u32 s51, s51, %[d]
s_cmp_eq_u32 s51, %[wrap]
s_cselect_b32 s51, %[rst], s51
XL 13,64
M 9,54,0
s_add_u32 s40, s40, 3072
LC_ 120
s_addc_u32 s41, s41, 0
s_add_u32 m0, s38, 3072
M 9,55,1
XL 14,128
XS 150
M 10,66,0
WL 3
LO 117,19,117
LU 19,117
M 10,67,1
RA 236,1,24576
HS 24,19
M 11,78,0
LL 17,74
LL 121,75
M 11,79,1
LA 17
DW 0
M 6,20,2
LC_ 121
XL 15,192
M 6,21,3
DW 1024
v_add_u32 v1, s37, v0
M 7,32,2
WL 3
LO 120,16,120
LU 16,120
M 7,33,3
RA 216,1,0
HS 24,16
M 8,44,2
LL 18,76
LL 124,77
M 8,45,3
LA 18
XL 16,256
M 9,56,2
LC_ 124
DW 2048
M 9,57,3
s_lshl_b32 s53, s52, 8
v_and_b32 v22, 0xfffffcff, v1
XS 151
M 10,68,2
WL 3
LO 121,17,121
LU 17,121
M 10,69,3
s_add_u32 s48, %[alo], s53
RA 220,1,1024
HS 24,17
M 11,80,2
LL 19,78
LL 125,79
M 11,81,3
s_addc_u32 s49, %[ahi], 0
LA 19
RV 252,22,5120
M 6,22,4
s_add_u32 s52, s52, %[d]
LC_ 125
XS 152
M 6,23,5
s_cmp_eq_u32 s52, %[wrap]
XS 153
XS 154
M 7,34,4
WL 4
LO 124,18,124
LU 18,124
M 7,35,5
s_cselect_b32 s52, %[rst], s52
RA 224,1,2048
HS 24,18
M 8,46,4
LL 16,80
LL 106,81
M 8,47,5
LA 16
XL 17,320
DQ 3072
M 9,58,4
LC_ 106
XL 18,384
M 9,59,5
s_add_u32 s50, s50, %[d]
XS 155
XS 156
M 10,70,4
WL 3
LO 125,19,125
LU 19,125
M 10,71,5
RA 228,1,3072
HS 24,19
M 11,82,4
LL 17,82
LL 107,83
M 11,83,5
LA 17
HX 24
s_cmp_eq_u32 s50, %[wrap]
s_cselect_b32 s50, %[rst], s50
Z 0,0,0
LC_ 107
XL 19,448
M 0,1,1
s_add_u32 s38, s38, 32768
WV 12
BW 12
Z 1,24,0
WL 4
LO 106,16,106
LU 16,106
M 1,25,1
s_cmp_eq_u32 s38, s77
RA 232,1,4096
HZ 25,16
Z 2,36,0
s_cselect_b32 s38, s76, s38
LL 18,84
LL 110,85
M 2,37,1
s_barrier
LA 18
RV 40,8,6144
Z 3,48,0
s_sub_u32 s39, 0, s39
LC_ 110
ds_read_b64 v[14:15], v5 offset:0
M 3,49,1
XL 20,512
RV 44,8,6208
Z 4,60,0
WL 7
LO 107,17,107
LU 17,107
M 4,61,1
RA 236,1,6144
HS 25,17
Z 5,72,0
LL 19,86
LL 111,87
XS 157
M 5,73,1
LA 19
W1 24,0
M 0,2,2
LC_ 111
XL 21,576
M 0,3,3
s_add_u32 s42, s44, s60
RV 48,8,6272
BR 12
XS 158
M 1,26,2
WL 8
LO 110,18,110
LU 18,110
M 1,27,3
s_addc_u32 s43, s45, 0
RA 216,1,7168
HS 25,18
XS 159
M 2,38,2
LL 16,88
LL 114,89
M 2,39,3
LA 16
XL 22,640
M 3,50,2
LC_ 114
ST 104
XS 160
M 3,51,3
XL 23,704
v_subrev_u32 v5, s39, v5
M 4,62,2
WL 5
LO 111,19,111
LU 19,111
M 4,63,3
s_add_u32 s42, s44, s61
RA 220,1,8192
HS 25,19
XS 161
M 5,74,2
LL 17,90
LL 115,91
M 5,75,3
s_addc_u32 s43, s45, 0
LA 17
ST 108
M 0,4,4
s_mov_b32 m0, %[pm]
LC_ 115
DF
XS 162
M 0,5,5
WV 17
BW 13
M 1,28,4
WL 4
LO 114,16,114
LU 16,114
XS 163
M 1,29,5
RA 224,1,9216
HS 25,16
M 2,40,4
LL 18,92
LL 118,93
M 2,41,5
LA 18
BR 13
XS 164
LZ 40,12
WV 16
BW 14
M 3,52,4
LC_ 118
RV 40,8,6336
M 3,53,5
s_add_u32 s42, s44, s62
BR 14
v_add_f32 v14, v14, v15
s_nop 1
v_add_f32_dpp v14, v14, v14 quad_perm:[1,0,3,2] row_mask:0xf bank_mask:0xf
s_nop 1
v_add_f32_dpp v14, v14, v14 quad_perm:[2,3,0,1] row_mask:0xf bank_mask:0xf
s_nop 1
v_add_f32_dpp v14, v14, v14 row_half_mirror row_mask:0xf bank_mask:0xf
v_cvt_f64_f32 v[14:15], v14
s_mov_b64 exec, s[54:55]
SP
s_mov_b64 exec, -1
M 4,64,4
WL 8
LO 115,17,115
LU 17,115
XS 165
M 4,65,5
s_addc_u32 s43, s45, 0
RA 228,1,10240
HS 25,17
M 5,76,4
LL 19,94
LL 119,95
XS 166
M 5,77,5
WL 5
LA 19
ST 112
LS 44,13
WV 16
BW 15
M 0,6,0
LC_ 119
RV 44,8,6400
M 0,7,1
WL 5
BR 15
XS 167
LS 48,14
WV 14
BW 16
M 1,30,0
LO 118,18,118
LU 18,118
M 1,31,1
RA 232,1,11264
HS 25,18
M 2,42,0
LL 16,96
LL 122,97
XS 168
M 2,43,1
LA 16
RV 252,22,5392
M 3,54,0
LC_ 122
RV 48,8,6464
XS 169
M 3,55,1
s_add_u32 s42, s44, s63
BR 16
XS 170
M 4,66,0
WL 7
LO 119,19,119
LU 19,119
M 4,67,1
s_addc_u32 s43, s45, 0
RA 236,1,12288
HS 25,19
LS 40,15
WV 12
BW 17
M 5,78,0
LL 17,98
LL 123,99
M 5,79,1
LA 17
ST 116
M 0,8,2
LC_ 123
RV 40,8,6528
XS 171
M 0,9,3
BR 17
XS 172
M 1,32,2
WL 8
LO 122,16,122
LU 16,122
M 1,33,3
WL 6
RA 216,1,13312
HS 25,16
LS 44,16
WV 11
BW 18
M 2,44,2
LL 18,100
LL 126,101
M 2,45,3
LA 18
RV 44,8,6592
XS 173
M 3,56,2
LC_ 126
BR 18
M 3,57,3
s_add_u32 s42, s44, s64
XS 174
XS 175
M 4,68,2
WL 6
LO 123,17,123
LU 17,123
M 4,69,3
s_addc_u32 s43, s45, 0
RA 220,1,14336
HS 25,17
LS 48,17
WV 10
BW 19
M 5,80,2
LL 19,102
LL 127,103
M 5,81,3
LA 19
ST 120
M 0,10,4
LC_ 127
RV 48,8,6656
M 0,11,5
BR 19
M 1,34,4
WL 7
LO 126,18,126
LU 18,126
M 1,35,5
WL 6
RA 224,1,15360
HS 25,18
LS 40,18
WV 10
BW 20
M 2,46,4
LL 16,56
LL 104,57
M 2,47,5
LA 16
RV 40,8,6720
M 3,58,4
LC_ 104
BR 20
)"
R"(M 3,59,5
s_add_u32 s42, s44, s65
M 4,70,4
WL 6
LO 127,19,127
LU 19,127
M 4,71,5
s_addc_u32 s43, s45, 0
RA 228,1,16384
HS 25,19
LS 44,19
WV 9
BW 21
M 5,82,4
LL 17,58
LL 105,59
M 5,83,5
LA 17
ST 124
Z 6,0,0
LC_ 105
RV 44,8,6784
M 6,1,1
BR 21
HX 25
Z 7,24,0
WL 7
LO 104,16,104
LU 16,104
M 7,25,1
WL 6
RA 232,1,17408
HZ 26,16
LS 48,20
WV 9
BW 22
Z 8,36,0
LL 18,60
LL 108,61
M 8,37,1
s_mul_i32 s53, s50, 25600
LA 18
W1 25,1024
Z 9,48,0
s_add_u32 s40, %[wlo], s53
LC_ 108
RV 48,8,6848
M 9,49,1
s_addc_u32 s41, %[whi], 0
RA 240,7,0
BR 22
Z 10,60,0
WL 8
LO 105,17,105
LU 17,105
M 10,61,1
s_mov_b32 m0, s38
RA 236,1,18432
HS 26,17
LS 40,21
WV 7
BW 23
Z 11,72,0
LL 19,62
LL 109,63
M 11,73,1
LA 19
DW 0
M 6,2,2
LC_ 109
RA 244,7,256
M 6,3,3
BR 23
v_add_u32 v4, s39, v4
M 7,26,2
WL 6
LO 108,18,108
LU 18,108
M 7,27,3
s_add_u32 s37, s37, 32768
RA 216,1,19456
HZ 24,18
M 8,38,2
s_cmp_eq_u32 s37, s75
LL 16,64
LL 112,65
M 8,39,3
s_cselect_b32 s37, s74, s37
LA 16
DW 1024
LS 44,22
RA 248,7,512
HX 26
M 9,50,2
LC_ 112
M 9,51,3
M 10,62,2
WL 6
LO 109,19,109
LU 19,109
M 10,63,3
WL 4
RA 220,1,20480
HS 24,19
LS 48,23
RA 252,7,768
M 11,74,2
LL 17,66
LL 113,67
M 11,75,3
LA 17
DW 2048
M 6,4,4
LC_ 113
v_mul_f32 v36, 0xbb2aaaab, v36
M 6,5,5
v_mul_f32 v37, 0xbb2aaaab, v37
v_mul_f32 v38, 0xbb2aaaab, v38
M 7,28,4
WL 4
LO 112,16,112
LU 16,112
M 7,29,5
s_mul_i32 s53, s51, 49152
RA 224,1,21504
HS 24,16
M 8,40,4
s_bitcmp1_b64 s[90:91], s51
LL 18,68
LL 116,69
M 8,41,5
s_cselect_b64 s[44:45], s[84:85], s[82:83]
LA 18
v_mul_f32 v39, 0xbb2aaaab, v39
M 9,52,4
s_add_u32 s44, s44, s53
LC_ 116
M 9,53,5
s_addc_u32 s45, s45, 0
LK 40,240,12,0
LK 44,242,12,2
s_bitcmp1_b64 s[92:93], s51
RA 240,7,1024
LK 48,244,13,0
s_nop 0
CF 176,40,41
LK 248,246,13,2
s_cselect_b64 s[94:95], s[88:89], s[86:87]
RA 244,7,1280
CF 177,42,43
M 10,64,4
WL 5
LO 113,17,113
LU 17,113
M 10,65,5
s_add_u32 s94, s94, s53
RA 228,1,22528
HS 24,17
M 11,76,4
LL 19,70
LL 117,71
M 11,77,5
s_addc_u32 s95, s95, 0
LA 19
CF 178,44,45
M 6,6,0
s_add_u32 s51, s51, %[d]
LC_ 117
CF 179,46,47
M 6,7,1
s_cmp_eq_u32 s51, %[wrap]
LK 40,248,14,0
CF 180,48,49
LK 44,250,14,2
s_cselect_b32 s51, %[rst], s51
RA 248,7,1536
CF 181,50,51
GZ 12
s_mul_i32 s53, %[d], 0
CF 182,248,249
CF 183,250,251
LK 48,252,15,0
WL 5
CF 184,40,41
LK 248,254,15,2
s_add_u32 s60, s53, %[sb1]
RA 252,7,1792
CF 185,42,43
GS 13
s_add_u32 s40, s40, 3072
CF 186,44,45
CF 187,46,47
LK 40,240,16,0
WL 5
CF 188,48,49
LK 44,242,16,2
s_addc_u32 s41, s41, 0
RA 240,7,2048
CF 189,50,51
GS 14
s_add_u32 m0, s38, 3072
CF 190,248,249
CF 191,250,251
LK 48,244,17,0
CF 192,40,41
LK 248,246,17,2
RA 244,7,2304
CF 193,42,43
GS 15
CF 194,44,45
CF 195,46,47
M 7,30,0
LO 116,18,116
LU 18,116
M 7,31,1
WL 3
RA 232,1,23552
HS 24,18
LK 40,248,18,0
CF 196,48,49
LK 44,250,18,2
RA 248,7,2560
CF 197,50,51
GS 16
WL 4
CF 198,248,249
CF 199,250,251
LK 48,252,19,0
CF 200,40,41
LK 248,254,19,2
RA 252,7,2816
CF 201,42,43
GS 17
WL 4
CF 202,44,45
CF 203,46,47
LK 40,240,20,0
CF 204,48,49
LK 44,242,20,2
CF 205,50,51
GS 18
CF 206,248,249
CF 207,250,251
M 8,42,0
LL 16,72
LL 120,73
M 8,43,1
LA 16
DW 0
M 9,54,0
LC_ 120
DW 1024
M 9,55,1
WL 5
LK 48,244,21,0
CF 208,40,41
LK 248,246,21,2
CF 209,42,43
GS 19
CF 210,44,45
CF 211,46,47
M 10,66,0
LO 117,19,117
LU 19,117
M 10,67,1
RA 236,1,24576
HS 24,19
M 11,78,0
LL 17,74
LL 121,75
M 11,79,1
LA 17
DW 2048
M 6,8,2
WL 5
LC_ 121
M 6,9,3
s_lshl_b32 s53, s52, 8
v_add_u32 v1, s37, v0
LK 40,248,22,0
CF 212,48,49
LK 44,250,22,2
s_add_u32 s48, %[alo], s53
CF 213,50,51
GS 20
s_addc_u32 s49, %[ahi], 0
CF 214,248,249
CF 215,250,251
M 7,32,2
WL 3
LO 120,16,120
LU 16,120
M 7,33,3
s_add_u32 s52, s52, %[d]
RA 216,1,0
HS 24,16
M 8,44,2
s_cmp_eq_u32 s52, %[wrap]
LL 18,76
LL 124,77
M 8,45,3
s_cselect_b32 s52, %[rst], s52
LA 18
DQ 3072
M 9,56,2
LC_ 124
v_and_b32 v22, 0xfffffcff, v1
M 9,57,3
LK 48,252,23,0
CF 216,40,41
LK 248,254,23,2
CF 217,42,43
GS 21
CF 218,44,45
CF 219,46,47
M 10,68,2
WL 3
LO 121,17,121
LU 17,121
M 10,69,3
s_add_u32 s50, s50, %[d]
RA 220,1,1024
HS 24,17
M 11,80,2
LL 19,78
LL 125,79
M 11,81,3
s_cmp_eq_u32 s50, %[wrap]
LA 19
RV 252,22,5120
M 6,10,4
s_cselect_b32 s50, %[rst], s50
LC_ 125
RV 36,8,6912
M 6,11,5
M 7,34,4
WL 5
LO 124,18,124
LU 18,124
M 7,35,5
s_add_u32 s38, s38, 32768
RA 224,1,2048
HS 24,18
GS 22
s_cmp_eq_u32 s38, s77
CF 220,48,49
CF 221,50,51
M 8,46,4
s_cselect_b32 s38, s76, s38
LL 16,80
LL 106,81
M 8,47,5
s_barrier
LA 16
CF 222,248,249
M 9,58,4
s_sub_u32 s39, 0, s39
LC_ 106
ds_read_b64 v[14:15], v5 offset:0
M 9,59,5
s_nop 0
v_subrev_u32 v5, s39, v5
CF 223,250,251
M 10,70,4
WL 5
LO 125,19,125
LU 19,125
M 10,71,5
RA 228,1,3072
HS 24,19
GS 23
M 11,82,4
LL 17,82
LL 107,83
M 11,83,5
LA 17
HX 24
Z 0,0,0
LC_ 107
W1 24,0
M 0,1,1
W1 26,2048
v_cndmask_b32 v52, v52, v53, s[78:79]
Z 1,24,0
WL 6
LO 106,16,106
LU 16,106
M 1,25,1
RA 232,1,4096
HZ 27,16
Z 2,36,0
LL 18,84
LL 110,85
M 2,37,1
WL 6
LA 18
v_add_f32 v14, v14, v15
Z 3,48,0
LC_ 110
v_add_f32_dpp v14, v14, v14 quad_perm:[1,0,3,2] row_mask:0xf bank_mask:0xf
)"
R"(M 3,49,1
s_add_u32 s42, s94, s60
v_add_f32_dpp v14, v14, v14 quad_perm:[2,3,0,1] row_mask:0xf bank_mask:0xf
v_cndmask_b32 v54, v54, v55, s[78:79]
Z 4,60,0
WL 5
LO 107,17,107
LU 17,107
M 4,61,1
s_addc_u32 s43, s95, 0
RA 236,1,6144
HS 27,17
Z 5,72,0
LL 19,86
LL 111,87
M 5,73,1
LA 19
SU 104
M 0,2,2
LC_ 111
v_add_f32_dpp v14, v14, v14 row_half_mirror row_mask:0xf bank_mask:0xf
M 0,3,3
v_cvt_f64_f32 v[14:15], v14
HX 27
M 1,26,2
WL 3
LO 110,18,110
LU 18,110
M 1,27,3
RA 216,1,7168
HZ 25,18
M 2,38,2
LL 16,88
LL 114,89
M 2,39,3
LA 16
s_mov_b64 exec, s[54:55]
SP
s_mov_b64 exec, -1
M 3,50,2
LC_ 114
v_cndmask_b32 v52, v52, v54, s[80:81]
M 3,51,3
s_add_u32 s42, s44, s61
v_mul_f32 v52, 0x3b2aaaab, v52
C 52,52,52
M 4,62,2
WL 3
LO 111,19,111
LU 19,111
M 4,63,3
s_addc_u32 s43, s45, 0
RA 220,1,8192
HS 25,19
M 5,74,2
LL 17,90
LL 115,91
M 5,75,3
LA 17
ST 108
M 0,4,4
s_mov_b32 m0, %[pm]
LC_ 115
DF
M 0,5,5
v_lshlrev_b32 v52, 16, v52
v_add_f32 v52, v36, v52
M 1,28,4
WL 3
LO 114,16,114
LU 16,114
M 1,29,5
RA 224,1,9216
HS 25,16
M 2,40,4
LL 18,92
LL 118,93
M 2,41,5
LA 18
C 52,52,52
M 3,52,4
LC_ 118
v_lshlrev_b32 v52, 16, v52
M 3,53,5
s_add_u32 s42, s44, s62
v_sqrt_f32 v52, v52
M 4,64,4
WL 3
LO 115,17,115
LU 17,115
M 4,65,5
s_addc_u32 s43, s45, 0
RA 228,1,10240
HS 25,17
M 5,76,4
LL 19,94
LL 119,95
M 5,77,5
LA 19
ST 112
M 0,6,0
LC_ 119
C 52,52,52
M 0,7,1
v_lshlrev_b32 v52, 16, v52
v_cmp_ge_f32 vcc, v37, v52
M 1,30,0
WL 3
LO 118,18,118
LU 18,118
M 1,31,1
RA 232,1,11264
HS 25,18
M 2,42,0
LL 16,96
LL 122,97
M 2,43,1
LA 16
RV 252,22,5392
M 3,54,0
LC_ 122
M 3,55,1
s_add_u32 s42, s44, s63
v_rcp_f32 v52, v52
M 4,66,0
WL 4
LO 119,19,119
LU 19,119
M 4,67,1
s_addc_u32 s43, s45, 0
RA 236,1,12288
HS 25,19
M 5,78,0
LL 17,98
LL 123,99
M 5,79,1
LA 17
ST 116
M 0,8,2
LC_ 123
v_mul_f32 v52, v38, v52
M 0,9,3
v_cndmask_b32 v52, 0, v52, vcc
CF 52,52,52
M 1,32,2
WL 3
LO 122,16,122
LU 16,122
M 1,33,3
RA 216,1,13312
HS 25,16
M 2,44,2
LL 18,100
LL 126,101
M 2,45,3
LA 18
ds_bpermute_b32 v13, v28, v52
M 3,56,2
LC_ 126
M 3,57,3
s_add_u32 s42, s44, s64
M 4,68,2
WL 4
LO 123,17,123
LU 17,123
M 4,69,3
s_addc_u32 s43, s45, 0
RA 220,1,14336
HS 25,17
M 5,80,2
LL 19,102
LL 127,103
M 5,81,3
LA 19
ST 120
M 0,10,4
LC_ 127
M 0,11,5
M 1,34,4
WL 3
LO 126,18,126
LU 18,126
M 1,35,5
RA 224,1,15360
HS 25,18
M 2,46,4
LL 16,56
LL 104,57
M 2,47,5
LA 16
XS 176
M 3,58,4
LC_ 104
XS 177
M 3,59,5
s_add_u32 s42, s44, s65
XS 178
XS 179
M 4,70,4
WL 3
LO 127,19,127
LU 19,127
M 4,71,5
s_addc_u32 s43, s45, 0
RA 228,1,16384
HS 25,19
M 5,82,4
LL 17,58
LL 105,59
M 5,83,5
LA 17
ST 124
Z 6,0,0
LC_ 105
HX 25
M 6,1,1
W1 25,1024
XS 180
Z 7,24,0
WL 4
LO 104,16,104
LU 16,104
M 7,25,1
RA 232,1,17408
HZ 26,16
Z 8,36,0
LL 18,60
LL 108,61
M 8,37,1
s_mul_i32 s53, s50, 25600
LA 18
W1 27,3072
Z 9,48,0
s_add_u32 s40, %[wlo], s53
LC_ 108
v_add_u32 v4, s39, v4
M 9,49,1
s_addc_u32 s41, %[whi], 0
XS 181
XS 182
Z 10,60,0
WL 5
LO 105,17,105
LU 17,105
M 10,61,1
s_mov_b32 m0, s38
RA 236,1,18432
HS 26,17
Z 11,72,0
LL 19,62
LL 109,63
M 11,73,1
LA 19
DW 0
M 6,2,2
LC_ 109
HX 26
M 6,3,3
XS 183
v_min_u32 v35, v35, v13
M 7,26,2
WL 3
LO 108,18,108
LU 18,108
M 7,27,3
s_add_u32 s37, s37, 32768
RA 216,1,19456
HZ 24,18
M 8,38,2
s_cmp_eq_u32 s37, s75
LL 16,64
LL 112,65
M 8,39,3
s_cselect_b32 s37, s74, s37
LA 16
XS 184
M 9,50,2
s_mul_i32 s53, %[d], 196608
LC_ 112
XS 185
M 9,51,3
s_add_u32 s53, s53, s57
XS 186
XS 187
M 10,62,2
WL 3
LO 109,19,109
LU 19,109
M 10,63,3
s_add_u32 s46, %[xlo], s53
RA 220,1,20480
HS 24,19
M 11,74,2
LL 17,66
LL 113,67
M 11,75,3
s_addc_u32 s47, %[xhi], 0
LA 17
XS 188
M 6,4,4
s_mov_b32 s53, 0xfe987610
LC_ 113
XS 189
M 6,5,5
s_cmp_eq_u32 %[d], 1
XS 190
XS 191
M 7,28,4
WL 3
LO 112,16,112
LU 16,112
M 7,29,5
s_cselect_b32 s53, s53, 0xda43cb52
RA 224,1,21504
HS 24,16
M 8,40,4
s_bitcmp1_b64 s[92:93], 48
LL 18,68
LL 116,69
M 8,41,5
s_cselect_b32 s58, 16, 0
LA 18
XS 192
M 9,52,4
s_lshr_b32 s58, s53, s58
LC_ 116
v_lshrrev_b32 v3, 6, v0
v_and_b32 v3, 12, v3
v_bfe_u32 v3, s58, v3, 4
v_mad_u32_u24 v3, v3, s97, v21
M 9,53,5
DW 1024
XS 193
M 10,64,4
WL 3
LO 113,17,113
LU 17,113
M 10,65,5
RA 228,1,22528
HS 24,17
M 11,76,4
LL 19,70
LL 117,71
M 11,77,5
LA 19
XL 24,0
M 6,6,0
LC_ 117
DW 2048
M 6,7,1
s_mul_i32 s53, s51, 49152
XS 194
XS 195
M 7,30,0
WL 3
LO 116,18,116
LU 18,116
M 7,31,1
s_bitcmp1_b64 s[90:91], s51
RA 232,1,23552
HS 24,18
M 8,42,0
s_cselect_b64 s[44:45], s[84:85], s[82:83]
LL 16,72
LL 120,73
M 8,43,1
s_add_u32 s44, s44, s53
LA 16
s_addc_u32 s45, s45, 0
s_bitcmp1_b64 s[92:93], s51
s_cselect_b64 s[94:95], s[88:89], s[86:87]
s_add_u32 s94, s94, s53
s_addc_u32 s95, s95, 0
s_add_u32 s51, s51, %[d]
s_cmp_eq_u32 s51, %[wrap]
s_cselect_b32 s51, %[rst], s51
XL 25,64
M 9,54,0
s_add_u32 s40, s40, 3072
LC_ 120
s_addc_u32 s41, s41, 0
s_add_u32 m0, s38, 3072
M 9,55,1
XL 26,128
XS 196
M 10,66,0
WL 3
LO 117,19,117
LU 19,117
M 10,67,1
RA 236,1,24576
HS 24,19
M 11,78,0
LL 17,74
LL 121,75
M 11,79,1
LA 17
DW 0
M 6,8,2
LC_ 121
)"
R"(XL 27,192
M 6,9,3
DW 1024
v_add_u32 v1, s37, v0
M 7,32,2
WL 3
LO 120,16,120
LU 16,120
M 7,33,3
RA 216,1,0
HS 24,16
M 8,44,2
LL 18,76
LL 124,77
M 8,45,3
LA 18
XL 28,256
M 9,56,2
LC_ 124
DW 2048
M 9,57,3
s_lshl_b32 s53, s52, 8
v_and_b32 v22, 0xfffffcff, v1
XS 197
M 10,68,2
WL 3
LO 121,17,121
LU 17,121
M 10,69,3
s_add_u32 s48, %[alo], s53
RA 220,1,1024
HS 24,17
M 11,80,2
LL 19,78
LL 125,79
M 11,81,3
s_addc_u32 s49, %[ahi], 0
LA 19
RV 252,22,5120
M 6,10,4
s_add_u32 s52, s52, %[d]
LC_ 125
XS 198
M 6,11,5
s_cmp_eq_u32 s52, %[wrap]
XS 199
XS 200
M 7,34,4
WL 4
LO 124,18,124
LU 18,124
M 7,35,5
s_cselect_b32 s52, %[rst], s52
RA 224,1,2048
HS 24,18
M 8,46,4
LL 16,80
LL 106,81
M 8,47,5
LA 16
XL 29,320
DQ 3072
M 9,58,4
LC_ 106
XL 30,384
M 9,59,5
s_add_u32 s50, s50, %[d]
XS 201
XS 202
M 10,70,4
WL 3
LO 125,19,125
LU 19,125
M 10,71,5
RA 228,1,3072
HS 24,19
M 11,82,4
LL 17,82
LL 107,83
M 11,83,5
LA 17
HX 24
s_cmp_eq_u32 s50, %[wrap]
s_cselect_b32 s50, %[rst], s50
Z 0,0,0
LC_ 107
XL 31,448
M 0,1,1
s_add_u32 s38, s38, 32768
WV 12
BW 24
Z 1,12,0
WL 4
LO 106,16,106
LU 16,106
M 1,13,1
s_cmp_eq_u32 s38, s77
RA 232,1,4096
HZ 27,16
Z 2,36,0
s_cselect_b32 s38, s76, s38
LL 18,84
LL 110,85
M 2,37,1
s_barrier
LA 18
RV 40,8,6144
Z 3,48,0
s_sub_u32 s39, 0, s39
LC_ 110
ds_read_b64 v[14:15], v5 offset:0
XS 203
M 3,49,1
XL 32,512
RV 44,8,6208
Z 4,60,0
WL 7
LO 107,17,107
LU 17,107
M 4,61,1
RA 236,1,6144
HS 27,17
XS 204
Z 5,72,0
LL 19,86
LL 111,87
M 5,73,1
LA 19
W1 24,0
XS 205
M 0,2,2
LC_ 111
W1 26,2048
M 0,3,3
XL 33,576
RV 48,8,6272
M 1,14,2
WL 8
LO 110,18,110
LU 18,110
XS 206
M 1,15,3
s_add_u32 s42, s94, s60
RA 216,1,7168
HZ 25,18
M 2,38,2
s_addc_u32 s43, s95, 0
LL 16,88
LL 114,89
M 2,39,3
LA 16
XL 34,640
XS 207
M 3,50,2
LC_ 114
SU 104
M 3,51,3
XL 35,704
BR 24
M 4,62,2
WL 6
LO 111,19,111
LU 19,111
XS 208
M 4,63,3
s_add_u32 s42, s44, s61
RA 220,1,8192
HS 25,19
M 5,74,2
LL 17,90
LL 115,91
XS 209
M 5,75,3
s_addc_u32 s43, s45, 0
LA 17
ST 108
M 0,4,4
s_mov_b32 m0, %[pm]
LC_ 115
DF
M 0,5,5
WV 17
BW 25
HX 27
XS 210
M 1,16,4
WL 5
LO 114,16,114
LU 16,114
M 1,17,5
RA 224,1,9216
HS 25,16
M 2,40,4
LL 18,92
LL 118,93
XS 211
M 2,41,5
WL 5
LA 18
BR 25
LZ 40,24
WV 16
BW 26
M 3,52,4
LC_ 118
RV 40,8,6336
XS 212
M 3,53,5
s_add_u32 s42, s44, s62
BR 26
v_add_f32 v14, v14, v15
s_nop 1
v_add_f32_dpp v14, v14, v14 quad_perm:[1,0,3,2] row_mask:0xf bank_mask:0xf
s_nop 1
v_add_f32_dpp v14, v14, v14 quad_perm:[2,3,0,1] row_mask:0xf bank_mask:0xf
s_nop 1
v_add_f32_dpp v14, v14, v14 row_half_mirror row_mask:0xf bank_mask:0xf
v_cvt_f64_f32 v[14:15], v14
s_mov_b64 exec, s[54:55]
SP
s_mov_b64 exec, -1
s_nop 0
ds_read_b64 v[14:15], v5 offset:2048
M 4,64,4
WL 9
LO 115,17,115
LU 17,115
M 4,65,5
s_addc_u32 s43, s45, 0
RA 228,1,10240
HS 25,17
XS 213
M 5,76,4
LL 19,94
LL 119,95
M 5,77,5
WL 6
LA 19
ST 112
LS 44,25
WV 16
BW 27
M 0,6,0
LC_ 119
RV 44,8,6400
XS 214
M 0,7,1
WL 6
BR 27
v_subrev_u32 v5, s39, v5
LS 48,26
WV 14
BW 28
M 1,18,0
LO 118,18,118
LU 18,118
M 1,19,1
RA 232,1,11264
HS 25,18
XS 215
M 2,42,0
LL 16,96
LL 122,97
M 2,43,1
LA 16
RV 252,22,5392
XS 216
M 3,54,0
WL 10
LC_ 122
RV 48,8,6464
M 3,55,1
s_add_u32 s42, s44, s63
BR 28
v_add_f32 v14, v14, v15
s_nop 1
v_add_f32_dpp v14, v14, v14 quad_perm:[1,0,3,2] row_mask:0xf bank_mask:0xf
s_nop 1
v_add_f32_dpp v14, v14, v14 quad_perm:[2,3,0,1] row_mask:0xf bank_mask:0xf
M 4,66,0
WL 7
LO 119,19,119
LU 19,119
XS 217
M 4,67,1
s_addc_u32 s43, s45, 0
RA 236,1,12288
HS 25,19
v_add_f32_dpp v14, v14, v14 row_half_mirror row_mask:0xf bank_mask:0xf
LS 40,27
WV 12
BW 29
M 5,78,0
LL 17,98
LL 123,99
M 5,79,1
LA 17
ST 116
v_cvt_f64_f32 v[14:15], v14
XS 218
M 0,8,2
LC_ 123
s_mov_b64 exec, s[54:55]
SP
s_mov_b64 exec, -1
M 0,9,3
WL 4
RV 40,8,6528
BR 29
LS 44,28
WV 12
BW 30
M 1,20,2
LO 122,16,122
LU 16,122
XS 219
M 1,21,3
RA 216,1,13312
HS 25,16
M 2,44,2
LL 18,100
LL 126,101
XS 220
M 2,45,3
LA 18
RV 44,8,6592
M 3,56,2
LC_ 126
BR 30
M 3,57,3
s_add_u32 s42, s44, s64
XS 221
XS 222
M 4,68,2
WL 6
LO 123,17,123
LU 17,123
M 4,69,3
s_addc_u32 s43, s45, 0
RA 220,1,14336
HS 25,17
LS 48,29
WV 11
BW 31
M 5,80,2
LL 19,102
LL 127,103
M 5,81,3
LA 19
ST 120
M 0,10,4
LC_ 127
RV 48,8,6656
XS 223
M 0,11,5
BR 31
M 1,22,4
WL 7
LO 126,18,126
LU 18,126
M 1,23,5
WL 6
RA 224,1,15360
HS 25,18
LS 40,30
WV 11
BW 32
M 2,46,4
LL 16,56
LL 104,57
M 2,47,5
LA 16
RV 40,8,6720
M 3,58,4
LC_ 104
BR 32
M 3,59,5
s_add_u32 s42, s44, s65
M 4,70,4
WL 6
LO 127,19,127
LU 19,127
M 4,71,5
s_addc_u32 s43, s45, 0
RA 228,1,16384
HS 25,19
LS 44,31
WV 10
BW 33
M 5,82,4
LL 17,58
LL 105,59
M 5,83,5
LA 17
ST 124
Z 6,0,0
LC_ 105
RV 44,8,6784
M 6,1,1
BR 33
)"
R"(HX 25
Z 7,12,0
WL 7
LO 104,16,104
LU 16,104
M 7,13,1
WL 6
RA 232,1,17408
HZ 26,16
LS 48,32
WV 10
BW 34
Z 8,36,0
LL 18,60
LL 108,61
M 8,37,1
s_mul_i32 s53, s50, 25600
LA 18
W1 25,1024
Z 9,48,0
s_add_u32 s40, %[wlo], s53
LC_ 108
W1 27,3072
M 9,49,1
s_addc_u32 s41, %[whi], 0
RV 48,8,6848
RA 240,7,0
Z 10,60,0
WL 10
LO 105,17,105
LU 17,105
M 10,61,1
s_mov_b32 m0, s38
RA 236,1,18432
HS 26,17
Z 11,72,0
LL 19,62
LL 109,63
M 11,73,1
LA 19
DW 0
M 6,2,2
LC_ 109
BR 34
M 6,3,3
WL 12
v_add_u32 v4, s39, v4
LS 40,33
WV 9
BW 35
M 7,14,2
WL 8
LO 108,18,108
LU 18,108
M 7,15,3
s_add_u32 s37, s37, 32768
RA 216,1,19456
HZ 29,18
M 8,38,2
s_cmp_eq_u32 s37, s75
LL 16,64
LL 112,65
M 8,39,3
s_cselect_b32 s37, s74, s37
LA 16
DW 1024
M 9,50,2
LC_ 112
RA 244,7,256
M 9,51,3
BR 35
M 10,62,2
WL 7
LO 109,19,109
LU 19,109
M 10,63,3
WL 6
RA 220,1,20480
HS 29,19
LS 44,34
RA 248,7,512
M 11,74,2
LL 17,66
LL 113,67
M 11,75,3
LA 17
DW 2048
M 6,4,4
LC_ 113
v_pk_add_f16 v26, v29, v26
M 6,5,5
HX 26
M 7,16,4
WL 4
LO 112,16,112
LU 16,112
M 7,17,5
s_mul_i32 s53, s51, 49152
RA 224,1,21504
HZ 24,16
LS 48,35
s_bitcmp1_b64 s[90:91], s51
RA 252,7,768
M 8,40,4
s_cselect_b64 s[44:45], s[84:85], s[82:83]
LL 18,68
LL 116,69
M 8,41,5
s_add_u32 s44, s44, s53
LA 18
v_mul_f32 v36, 0xbb2aaaab, v36
M 9,52,4
s_addc_u32 s45, s45, 0
LC_ 116
v_mul_f32 v37, 0xbb2aaaab, v37
M 9,53,5
s_bitcmp1_b64 s[92:93], s51
v_mul_f32 v38, 0xbb2aaaab, v38
v_mul_f32 v39, 0xbb2aaaab, v39
M 10,64,4
WL 4
LO 113,17,113
LU 17,113
M 10,65,5
s_cselect_b64 s[94:95], s[88:89], s[86:87]
RA 228,1,22528
HS 24,17
LK 40,240,24,0
LK 44,242,24,2
s_add_u32 s94, s94, s53
RA 240,7,1024
LK 48,244,25,0
s_nop 0
CF 224,40,41
LK 248,246,25,2
s_addc_u32 s95, s95, 0
RA 244,7,1280
CF 225,42,43
M 11,76,4
LL 19,70
LL 117,71
M 11,77,5
s_add_u32 s51, s51, %[d]
LA 19
CF 226,44,45
M 6,6,0
s_cmp_eq_u32 s51, %[wrap]
LC_ 117
CF 227,46,47
M 6,7,1
s_cselect_b32 s51, %[rst], s51
LK 40,248,26,0
WL 7
CF 228,48,49
LK 44,250,26,2
s_mul_i32 s53, %[d], 1024
RA 248,7,1536
CF 229,50,51
GZ 24
s_add_u32 s61, s53, %[sb1]
CF 230,248,249
CF 231,250,251
LK 48,252,27,0
WL 4
CF 232,40,41
LK 248,254,27,2
s_add_u32 s40, s40, 3072
RA 252,7,1792
CF 233,42,43
GS 25
s_addc_u32 s41, s41, 0
CF 234,44,45
CF 235,46,47
LK 40,240,28,0
CF 236,48,49
LK 44,242,28,2
s_add_u32 m0, s38, 3072
RA 240,7,2048
CF 237,50,51
GS 26
WL 5
CF 238,248,249
CF 239,250,251
LK 48,244,29,0
CF 240,40,41
LK 248,246,29,2
RA 244,7,2304
CF 241,42,43
GS 27
CF 242,44,45
CF 243,46,47
M 7,18,0
LO 116,18,116
LU 18,116
M 7,19,1
WL 3
RA 232,1,23552
HS 24,18
LK 40,248,30,0
CF 244,48,49
LK 44,250,30,2
RA 248,7,2560
CF 245,50,51
GS 28
WL 4
CF 246,248,249
CF 247,250,251
LK 48,252,31,0
CF 40,40,41
LK 248,254,31,2
RA 252,7,2816
CF 42,42,43
GS 29
CF 44,44,45
CF 46,46,47
M 8,42,0
LL 16,72
LL 120,73
M 8,43,1
LA 16
DW 0
M 9,54,0
LC_ 120
DW 1024
M 9,55,1
AW 0,40
AW 1,42
M 10,66,0
LO 117,19,117
LU 19,117
M 10,67,1
RA 236,1,24576
HS 24,19
M 11,78,0
LL 17,74
LL 121,75
M 11,79,1
LA 17
DW 2048
M 6,8,2
WL 8
LC_ 121
AW 2,44
M 6,9,3
s_lshl_b32 s53, s52, 8
v_add_u32 v1, s37, v0
AW 3,46
LK 40,240,32,0
CF 48,48,49
LK 44,242,32,2
s_add_u32 s48, %[alo], s53
CF 50,50,51
GS 30
s_addc_u32 s49, %[ahi], 0
CF 248,248,249
CF 250,250,251
M 7,20,2
WL 3
LO 120,16,120
LU 16,120
M 7,21,3
s_add_u32 s52, s52, %[d]
RA 216,1,0
HS 24,16
M 8,44,2
s_cmp_eq_u32 s52, %[wrap]
LL 18,76
LL 124,77
M 8,45,3
s_cselect_b32 s52, %[rst], s52
LA 18
DQ 3072
M 9,56,2
LC_ 124
v_and_b32 v22, 0xfffffcff, v1
M 9,57,3
AW 4,48
AW 5,50
M 10,68,2
WL 3
LO 121,17,121
LU 17,121
M 10,69,3
s_add_u32 s50, s50, %[d]
RA 220,1,1024
HS 24,17
M 11,80,2
LL 19,78
LL 125,79
M 11,81,3
s_cmp_eq_u32 s50, %[wrap]
LA 19
RV 252,22,5120
M 6,10,4
s_cselect_b32 s50, %[rst], s50
LC_ 125
AW 6,248
M 6,11,5
AW 7,250
LK 48,244,33,0
CF 40,40,41
LK 248,246,33,2
CF 42,42,43
GS 31
CF 44,44,45
CF 46,46,47
M 7,22,4
WL 4
LO 124,18,124
LU 18,124
M 7,23,5
s_add_u32 s38, s38, 32768
RA 224,1,2048
HS 24,18
M 8,46,4
s_cmp_eq_u32 s38, s77
LL 16,80
LL 106,81
M 8,47,5
s_cselect_b32 s38, s76, s38
LA 16
AW 8,40
M 9,58,4
s_barrier
LC_ 106
AW 9,42
M 9,59,5
s_sub_u32 s39, 0, s39
AW 10,44
AW 11,46
LK 40,248,34,0
CF 48,48,49
LK 44,250,34,2
CF 50,50,51
GS 32
CF 248,248,249
CF 250,250,251
M 10,70,4
WL 3
LO 125,19,125
LU 19,125
M 10,71,5
RA 228,1,3072
HS 24,19
M 11,82,4
LL 17,82
LL 107,83
M 11,83,5
LA 17
HX 24
Z 0,0,0
LC_ 107
ds_read_b64 v[14:15], v5 offset:0
M 0,1,1
W1 24,0
AW 12,48
Z 1,12,0
WL 5
LO 106,16,106
LU 16,106
M 1,13,1
RA 232,1,4096
HZ 27,16
Z 2,36,0
LL 18,84
LL 110,85
M 2,37,1
LA 18
W1 26,2048
Z 3,48,0
LC_ 110
AW 13,50
M 3,49,1
s_add_u32 s42, s94, s60
AW 14,248
AW 15,250
LK 48,252,35,0
CF 40,40,41
LK 248,254,35,2
s_addc_u32 s43, s95, 0
CF 42,42,43
GS 33
CF 44,44,45
CF 46,46,47
Z 4,60,0
WL 6
LO 107,17,107
LU 17,107
M 4,61,1
RA 236,1,6144
HS 27,17
Z 5,72,0
LL 19,86
LL 111,87
M 5,73,1
LA 19
SU 104
M 0,2,2
WL 7
LC_ 111
)"
R"(v_add_f32 v14, v14, v15
M 0,3,3
s_nop 0
v_add_f32_dpp v14, v14, v14 quad_perm:[1,0,3,2] row_mask:0xf bank_mask:0xf
AW 16,40
M 1,14,2
WL 3
LO 110,18,110
LU 18,110
M 1,15,3
RA 216,1,7168
HZ 29,18
M 2,38,2
LL 16,88
LL 114,89
M 2,39,3
LA 16
v_add_f32_dpp v14, v14, v14 quad_perm:[2,3,0,1] row_mask:0xf bank_mask:0xf
M 3,50,2
LC_ 114
v_add_f32_dpp v14, v14, v14 row_half_mirror row_mask:0xf bank_mask:0xf
M 3,51,3
s_add_u32 s42, s94, s61
v_cvt_f64_f32 v[14:15], v14
AW 17,42
M 4,62,2
WL 3
LO 111,19,111
LU 19,111
M 4,63,3
s_addc_u32 s43, s95, 0
RA 220,1,8192
HS 29,19
M 5,74,2
LL 17,90
LL 115,91
M 5,75,3
LA 17
s_mov_b64 exec, s[54:55]
SP
s_mov_b64 exec, -1
M 0,4,4
LC_ 115
ds_read_b64 v[14:15], v5 offset:2048
M 0,5,5
SU 108
v_subrev_u32 v5, s39, v5
M 1,16,4
WL 4
LO 114,16,114
LU 16,114
M 1,17,5
s_mov_b32 m0, %[pm]
RA 224,1,9216
HZ 25,16
M 2,40,4
LL 18,92
LL 118,93
M 2,41,5
LA 18
DF
M 3,52,4
LC_ 118
v_pk_add_f16 v27, v29, v27
M 3,53,5
s_add_u32 s42, s44, s62
HX 27
AW 18,44
M 4,64,4
WL 4
LO 115,17,115
LU 17,115
M 4,65,5
s_addc_u32 s43, s45, 0
RA 228,1,10240
HS 25,17
M 5,76,4
LL 19,94
LL 119,95
M 5,77,5
LA 19
ST 112
M 0,6,0
WL 5
LC_ 119
v_add_f32 v14, v14, v15
M 0,7,1
s_nop 0
v_add_f32_dpp v14, v14, v14 quad_perm:[1,0,3,2] row_mask:0xf bank_mask:0xf
AW 19,46
RV 36,8,6912
M 1,18,0
WL 4
LO 118,18,118
LU 18,118
M 1,19,1
RA 232,1,11264
HS 25,18
GS 34
CF 48,48,49
CF 50,50,51
M 2,42,0
LL 16,96
LL 122,97
CF 248,248,249
M 2,43,1
LA 16
RV 252,22,5392
CF 250,250,251
AW 20,48
M 3,54,0
LC_ 122
v_add_f32_dpp v14, v14, v14 quad_perm:[2,3,0,1] row_mask:0xf bank_mask:0xf
AW 21,50
M 3,55,1
s_add_u32 s42, s44, s63
v_add_f32_dpp v14, v14, v14 row_half_mirror row_mask:0xf bank_mask:0xf
AW 22,248
AW 23,250
M 4,66,0
WL 5
LO 119,19,119
LU 19,119
M 4,67,1
s_addc_u32 s43, s45, 0
RA 236,1,12288
HS 25,19
GS 35
ST 116
v_cvt_f64_f32 v[14:15], v14
s_nop 5
v_cndmask_b32 v52, v52, v53, s[78:79]
M 5,78,0
LL 17,98
LL 123,99
v_cndmask_b32 v54, v54, v55, s[78:79]
M 5,79,1
LA 17
s_mov_b64 exec, s[54:55]
SP
s_mov_b64 exec, -1
v_cndmask_b32 v52, v52, v54, s[80:81]
M 0,8,2
LC_ 123
v_mul_f32 v52, 0x3b2aaaab, v52
M 0,9,3
C 52,52,52
v_lshlrev_b32 v52, 16, v52
M 1,20,2
WL 3
LO 122,16,122
LU 16,122
M 1,21,3
RA 216,1,13312
HS 25,16
v_add_f32 v52, v36, v52
M 2,44,2
LL 18,100
LL 126,101
C 52,52,52
M 2,45,3
LA 18
v_lshlrev_b32 v52, 16, v52
M 3,56,2
LC_ 126
v_sqrt_f32 v52, v52
M 3,57,3
s_add_u32 s42, s44, s64
C 52,52,52
v_lshlrev_b32 v52, 16, v52
M 4,68,2
WL 3
LO 123,17,123
LU 17,123
M 4,69,3
s_addc_u32 s43, s45, 0
RA 220,1,14336
HS 25,17
M 5,80,2
LL 19,102
LL 127,103
v_cmp_ge_f32 vcc, v37, v52
M 5,81,3
LA 19
ST 120
v_rcp_f32 v52, v52
M 0,10,4
LC_ 127
v_mul_f32 v52, v38, v52
M 0,11,5
v_cndmask_b32 v52, 0, v52, vcc
CF 52,52,52
M 1,22,4
WL 3
LO 126,18,126
LU 18,126
M 1,23,5
RA 224,1,15360
HS 25,18
ds_bpermute_b32 v13, v28, v52
M 2,46,4
LL 16,56
LL 104,57
M 2,47,5
LA 16
AR 248,0
M 3,58,4
LC_ 104
AR 249,1
M 3,59,5
s_add_u32 s42, s44, s65
AR 250,2
AR 251,3
M 4,70,4
WL 4
LO 127,19,127
LU 19,127
M 4,71,5
s_addc_u32 s43, s45, 0
RA 228,1,16384
HS 25,19
M 5,82,4
LL 17,58
LL 105,59
M 5,83,5
LA 17
ST 124
Z 6,0,0
LC_ 105
HX 25
M 6,1,1
W1 25,1024
AR 52,4
Z 7,12,0
WL 4
LO 104,16,104
LU 16,104
M 7,13,1
RA 232,1,17408
HZ 26,16
Z 8,36,0
LL 18,60
LL 108,61
M 8,37,1
s_mul_i32 s53, s50, 25600
LA 18
W1 27,3072
Z 9,48,0
s_add_u32 s40, %[wlo], s53
LC_ 108
v_add_u32 v4, s39, v4
M 9,49,1
s_addc_u32 s41, %[whi], 0
AR 53,5
AR 54,6
Z 10,60,0
WL 5
LO 105,17,105
LU 17,105
M 10,61,1
s_mov_b32 m0, s38
RA 236,1,18432
HS 26,17
Z 11,72,0
LL 19,62
LL 109,63
M 11,73,1
LA 19
DW 0
AR 55,7
M 6,2,2
LC_ 109
XS 224
M 6,3,3
XS 225
XS 226
M 7,14,2
WL 3
LO 108,18,108
LU 18,108
M 7,15,3
s_add_u32 s37, s37, 32768
RA 216,1,19456
HZ 29,18
M 8,38,2
s_cmp_eq_u32 s37, s75
LL 16,64
LL 112,65
M 8,39,3
s_cselect_b32 s37, s74, s37
LA 16
XS 227
M 9,50,2
s_mul_i32 s53, %[d], 196608
LC_ 112
XS 228
M 9,51,3
s_add_u32 s53, s53, s57
XS 229
XS 230
M 10,62,2
WL 3
LO 109,19,109
LU 19,109
M 10,63,3
s_add_u32 s46, %[xlo], s53
RA 220,1,20480
HS 29,19
M 11,74,2
LL 17,66
LL 113,67
XS 231
M 11,75,3
s_addc_u32 s47, %[xhi], 0
LA 17
v_pk_add_f16 v26, v29, v26
M 6,4,4
s_mov_b32 s53, 0xda43cb52
LC_ 113
HX 26
v_min_u32 v35, v35, v13
M 6,5,5
s_cmp_eq_u32 %[d], 1
XS 232
XS 233
M 7,16,4
WL 3
LO 112,16,112
LU 16,112
M 7,17,5
s_cselect_b32 s53, s53, 0xfe987610
RA 224,1,21504
HZ 24,16
M 8,40,4
s_bitcmp1_b64 s[92:93], 48
LL 18,68
LL 116,69
XS 234
M 8,41,5
s_cselect_b32 s58, 16, 0
LA 18
XS 235
M 9,52,4
s_lshr_b32 s58, s53, s58
LC_ 116
v_lshrrev_b32 v3, 6, v0
v_and_b32 v3, 12, v3
v_bfe_u32 v3, s58, v3, 4
v_mad_u32_u24 v3, v3, s97, v21
M 9,53,5
DW 1024
XS 236
M 10,64,4
WL 3
LO 113,17,113
LU 17,113
XS 237
M 10,65,5
RA 228,1,22528
HS 24,17
XS 238
M 11,76,4
LL 19,70
LL 117,71
M 11,77,5
LA 19
XL 36,0
XS 239
M 6,6,0
LC_ 117
DW 2048
XS 240
M 6,7,1
s_mul_i32 s53, s51, 49152
XS 241
XS 242
M 7,18,0
WL 3
LO 116,18,116
LU 18,116
M 7,19,1
s_bitcmp1_b64 s[90:91], s51
RA 232,1,23552
HS 24,18
M 8,42,0
s_cselect_b64 s[44:45], s[84:85], s[82:83]
LL 16,72
LL 120,73
XS 243
M 8,43,1
s_add_u32 s44, s44, s53
LA 16
s_addc_u32 s45, s45, 0
s_bitcmp1_b64 s[92:93], s51
s_cselect_b64 s[94:95], s[88:89], s[86:87]
s_add_u32 s94, s94, s53
s_addc_u32 s95, s95, 0
s_add_u32 s51, s51, %[d]
s_cmp_eq_u32 s51, %[wrap]
s_cselect_b32 s51, %[rst], s51
)"
R"(XL 37,64
M 9,54,0
s_add_u32 s40, s40, 3072
LC_ 120
s_addc_u32 s41, s41, 0
s_add_u32 m0, s38, 3072
XS 244
M 9,55,1
XL 38,128
XS 245
M 10,66,0
WL 3
LO 117,19,117
LU 19,117
XS 246
M 10,67,1
RA 236,1,24576
HS 24,19
M 11,78,0
LL 17,74
LL 121,75
XS 247
M 11,79,1
LA 17
DW 0
XS 248
M 6,8,2
LC_ 121
XL 39,192
M 6,9,3
DW 1024
v_add_u32 v1, s37, v0
XS 249
M 7,20,2
WL 3
LO 120,16,120
LU 16,120
XS 250
M 7,21,3
RA 216,1,0
HS 24,16
XS 251
M 8,44,2
LL 18,76
LL 124,77
M 8,45,3
LA 18
XL 40,256
AW 0,248
M 9,56,2
LC_ 124
DW 2048
AW 1,249
M 9,57,3
s_lshl_b32 s53, s52, 8
v_and_b32 v22, 0xfffffcff, v1
AW 2,250
M 10,68,2
WL 3
LO 121,17,121
LU 17,121
M 10,69,3
s_add_u32 s48, %[alo], s53
RA 220,1,1024
HS 24,17
AW 3,251
M 11,80,2
LL 19,78
LL 125,79
XS 52
M 11,81,3
s_addc_u32 s49, %[ahi], 0
LA 19
RV 252,22,5120
M 6,10,4
s_add_u32 s52, s52, %[d]
LC_ 125
XS 53
M 6,11,5
s_cmp_eq_u32 s52, %[wrap]
XS 54
XS 55
M 7,22,4
WL 4
LO 124,18,124
LU 18,124
M 7,23,5
s_cselect_b32 s52, %[rst], s52
RA 224,1,2048
HS 24,18
M 8,46,4
LL 16,80
LL 106,81
AW 4,52
M 8,47,5
LA 16
XL 41,320
DQ 3072
AW 5,53
M 9,58,4
LC_ 106
XL 42,384
M 9,59,5
s_add_u32 s50, s50, %[d]
AW 6,54
AW 7,55
M 10,70,4
WL 3
LO 125,19,125
LU 19,125
M 10,71,5
RA 228,1,3072
HS 24,19
AR 248,8
M 11,82,4
LL 17,82
LL 107,83
M 11,83,5
LA 17
HX 24
s_cmp_eq_u32 s50, %[wrap]
s_cselect_b32 s50, %[rst], s50
AR 249,9
Z 0,0,0
LC_ 107
XL 43,448
AR 250,10
M 0,1,1
s_add_u32 s38, s38, 32768
WV 12
BW 36
Z 1,12,0
WL 4
LO 106,16,106
LU 16,106
AR 251,11
M 1,13,1
s_cmp_eq_u32 s38, s77
RA 232,1,4096
HZ 27,16
XS 248
Z 2,24,0
s_cselect_b32 s38, s76, s38
LL 18,84
LL 110,85
XS 249
M 2,25,1
s_barrier
LA 18
RV 40,8,6144
Z 3,48,0
s_sub_u32 s39, 0, s39
LC_ 110
ds_read_b64 v[14:15], v5 offset:0
XS 250
M 3,49,1
XL 44,512
RV 44,8,6208
XS 251
Z 4,60,0
WL 7
LO 107,17,107
LU 17,107
AW 8,248
M 4,61,1
RA 236,1,6144
HS 27,17
Z 5,72,0
LL 19,86
LL 111,87
AW 9,249
M 5,73,1
LA 19
W1 24,0
AW 10,250
M 0,2,2
LC_ 111
W1 26,2048
M 0,3,3
XL 45,576
RV 48,8,6272
AW 11,251
M 1,14,2
WL 8
LO 110,18,110
LU 18,110
AR 52,12
M 1,15,3
s_add_u32 s42, s94, s60
RA 216,1,7168
HZ 29,18
AR 53,13
M 2,26,2
s_addc_u32 s43, s95, 0
LL 16,88
LL 114,89
M 2,27,3
LA 16
XL 46,640
AR 54,14
M 3,50,2
LC_ 114
SU 104
AR 55,15
M 3,51,3
XL 47,704
BR 36
M 4,62,2
WL 6
LO 111,19,111
LU 19,111
XS 52
M 4,63,3
s_add_u32 s42, s94, s61
RA 220,1,8192
HS 29,19
XS 53
M 5,74,2
LL 17,90
LL 115,91
XS 54
M 5,75,3
s_addc_u32 s43, s95, 0
LA 17
SU 108
M 0,4,4
s_mov_b32 m0, %[pm]
LC_ 115
DF
XS 55
M 0,5,5
WV 17
BW 37
AW 12,52
M 1,16,4
WL 5
LO 114,16,114
LU 16,114
AW 13,53
M 1,17,5
RA 224,1,9216
HZ 25,16
M 2,28,4
LL 18,92
LL 118,93
AW 14,54
M 2,29,5
WL 5
LA 18
BR 37
AW 15,55
LZ 40,36
WV 16
BW 38
M 3,52,4
LC_ 118
RV 40,8,6336
M 3,53,5
s_add_u32 s42, s44, s62
BR 38
v_add_f32 v14, v14, v15
s_nop 1
v_add_f32_dpp v14, v14, v14 quad_perm:[1,0,3,2] row_mask:0xf bank_mask:0xf
s_nop 1
v_add_f32_dpp v14, v14, v14 quad_perm:[2,3,0,1] row_mask:0xf bank_mask:0xf
s_nop 1
v_add_f32_dpp v14, v14, v14 row_half_mirror row_mask:0xf bank_mask:0xf
v_cvt_f64_f32 v[14:15], v14
s_mov_b64 exec, s[54:55]
SP
s_mov_b64 exec, -1
s_nop 0
ds_read_b64 v[14:15], v5 offset:2048
AR 248,16
M 4,64,4
WL 9
LO 115,17,115
LU 17,115
AR 249,17
M 4,65,5
s_addc_u32 s43, s45, 0
RA 228,1,10240
HS 25,17
AR 250,18
M 5,76,4
LL 19,94
LL 119,95
M 5,77,5
WL 6
LA 19
ST 112
AR 251,19
LS 44,37
WV 16
BW 39
M 0,6,0
LC_ 119
RV 44,8,6400
XS 248
M 0,7,1
WL 6
BR 39
v_subrev_u32 v5, s39, v5
v_pk_add_f16 v27, v29, v27
LS 48,38
WV 14
BW 40
M 1,18,0
LO 118,18,118
LU 18,118
HX 27
XS 249
M 1,19,1
RA 232,1,11264
HS 25,18
XS 250
M 2,30,0
LL 16,96
LL 122,97
XS 251
M 2,31,1
WV 12
LA 16
RV 252,22,5392
M 3,54,0
WL 4
LC_ 122
RV 48,8,6464
AW 16,248
M 3,55,1
s_add_u32 s42, s44, s63
BR 40
v_add_f32 v14, v14, v15
s_nop 1
v_add_f32_dpp v14, v14, v14 quad_perm:[1,0,3,2] row_mask:0xf bank_mask:0xf
s_nop 1
v_add_f32_dpp v14, v14, v14 quad_perm:[2,3,0,1] row_mask:0xf bank_mask:0xf
AW 17,249
LS 40,39
s_addc_u32 s43, s45, 0
BW 41
M 4,66,0
LO 119,19,119
LU 19,119
AW 18,250
M 4,67,1
RA 236,1,12288
HS 25,19
v_add_f32_dpp v14, v14, v14 row_half_mirror row_mask:0xf bank_mask:0xf
M 5,78,0
LL 17,98
LL 123,99
AW 19,251
M 5,79,1
LA 17
ST 116
v_cvt_f64_f32 v[14:15], v14
AR 52,20
M 0,8,2
LC_ 123
s_mov_b64 exec, s[54:55]
SP
s_mov_b64 exec, -1
M 0,9,3
WL 4
RV 40,8,6528
BR 41
AR 53,21
LS 44,40
WV 12
BW 42
M 1,20,2
LO 122,16,122
LU 16,122
AR 54,22
M 1,21,3
RA 216,1,13312
HS 25,16
AR 55,23
M 2,32,2
LL 18,100
LL 126,101
M 2,33,3
LA 18
RV 44,8,6592
XS 52
M 3,56,2
LC_ 126
BR 42
XS 53
M 3,57,3
s_add_u32 s42, s44, s64
XS 54
XS 55
M 4,68,2
WL 6
LO 123,17,123
LU 17,123
M 4,69,3
s_addc_u32 s43, s45, 0
)"
R"(RA 220,1,14336
HS 25,17
LS 48,41
WV 11
BW 43
M 5,80,2
LL 19,102
LL 127,103
AW 20,52
M 5,81,3
LA 19
ST 120
M 0,10,4
LC_ 127
RV 48,8,6656
AW 21,53
M 0,11,5
BR 43
AW 22,54
M 1,22,4
WL 7
LO 126,18,126
LU 18,126
AW 23,55
M 1,23,5
WL 6
RA 224,1,15360
HS 25,18
LS 40,42
WV 11
BW 44
M 2,34,4
LL 16,56
LL 104,57
M 2,35,5
LA 16
RV 40,8,6720
M 3,58,4
LC_ 104
BR 44
M 3,59,5
s_add_u32 s42, s44, s65
M 4,70,4
WL 6
LO 127,19,127
LU 19,127
M 4,71,5
s_addc_u32 s43, s45, 0
RA 228,1,16384
HS 25,19
LS 44,43
WV 10
BW 45
M 5,82,4
LL 17,58
LL 105,59
M 5,83,5
LA 17
ST 124
Z 6,0,0
LC_ 105
RV 44,8,6784
M 6,1,1
BR 45
HX 25
Z 7,12,0
WL 7
LO 104,16,104
LU 16,104
M 7,13,1
WL 6
RA 232,1,17408
HZ 26,16
LS 48,44
WV 10
BW 46
Z 8,24,0
LL 18,60
LL 108,61
M 8,25,1
s_mul_i32 s53, s50, 25600
LA 18
W1 25,1024
Z 9,48,0
s_add_u32 s40, %[wlo], s53
LC_ 108
W1 27,3072
M 9,49,1
s_addc_u32 s41, %[whi], 0
RV 48,8,6848
RA 240,7,0
Z 10,60,0
WL 10
LO 105,17,105
LU 17,105
M 10,61,1
s_mov_b32 m0, s38
RA 236,1,18432
HS 26,17
Z 11,72,0
LL 19,62
LL 109,63
M 11,73,1
LA 19
DW 0
M 6,2,2
LC_ 109
BR 46
M 6,3,3
WL 12
v_add_u32 v4, s39, v4
LS 40,45
WV 9
BW 47
M 7,14,2
WL 8
LO 108,18,108
LU 18,108
M 7,15,3
s_add_u32 s37, s37, 32768
RA 216,1,19456
HZ 29,18
M 8,26,2
s_cmp_eq_u32 s37, s75
LL 16,64
LL 112,65
M 8,27,3
s_cselect_b32 s37, s74, s37
LA 16
DW 1024
M 9,50,2
LC_ 112
RA 244,7,256
M 9,51,3
BR 47
M 10,62,2
WL 7
LO 109,19,109
LU 19,109
M 10,63,3
WL 6
RA 220,1,20480
HS 29,19
LS 44,46
RA 248,7,512
M 11,74,2
LL 17,66
LL 113,67
M 11,75,3
LA 17
DW 2048
M 6,4,4
LC_ 113
M 6,5,5
M 7,16,4
WL 4
LO 112,16,112
LU 16,112
M 7,17,5
s_mul_i32 s53, s51, 49152
RA 224,1,21504
HZ 30,16
LS 48,47
s_bitcmp1_b64 s[90:91], s51
RA 252,7,768
M 8,28,4
s_cselect_b64 s[44:45], s[84:85], s[82:83]
LL 18,68
LL 116,69
M 8,29,5
s_add_u32 s44, s44, s53
LA 18
v_mul_f32 v36, 0xbb2aaaab, v36
M 9,52,4
s_addc_u32 s45, s45, 0
LC_ 116
v_mul_f32 v37, 0xbb2aaaab, v37
M 9,53,5
s_bitcmp1_b64 s[92:93], s51
v_mul_f32 v38, 0xbb2aaaab, v38
v_mul_f32 v39, 0xbb2aaaab, v39
M 10,64,4
WL 4
LO 113,17,113
LU 17,113
M 10,65,5
s_cselect_b64 s[94:95], s[88:89], s[86:87]
RA 228,1,22528
HS 30,17
LK 40,240,36,0
LK 44,242,36,2
s_add_u32 s94, s94, s53
RA 240,7,1024
LK 48,244,37,0
s_nop 0
CF 40,40,41
LK 248,246,37,2
s_addc_u32 s95, s95, 0
RA 244,7,1280
CF 42,42,43
M 11,76,4
LL 19,70
LL 117,71
M 11,77,5
s_add_u32 s51, s51, %[d]
LA 19
v_pk_add_f16 v29, v30, v29
M 6,6,0
s_cmp_eq_u32 s51, %[wrap]
LC_ 117
v_pk_add_f16 v26, v29, v26
M 6,7,1
s_cselect_b32 s51, %[rst], s51
HX 26
CF 44,44,45
M 7,18,0
WL 5
LO 116,18,116
LU 18,116
M 7,19,1
s_mul_i32 s53, %[d], 16384
RA 232,1,23552
HZ 24,18
M 8,30,0
s_add_u32 s62, s53, %[sb1]
LL 16,72
LL 120,73
M 8,31,1
s_add_u32 s40, s40, 3072
LA 16
CF 46,46,47
M 9,54,0
s_addc_u32 s41, s41, 0
LC_ 120
s_add_u32 m0, s38, 3072
M 9,55,1
DW 0
AW 24,40
M 10,66,0
WL 3
LO 117,19,117
LU 19,117
M 10,67,1
RA 236,1,24576
HS 24,19
AW 25,42
AW 26,44
M 11,78,0
LL 17,74
LL 121,75
AW 27,46
M 11,79,1
LA 17
DW 1024
LK 40,248,38,0
CF 48,48,49
LK 44,250,38,2
RA 248,7,1536
CF 50,50,51
GZ 36
CF 248,248,249
CF 250,250,251
M 6,8,2
LC_ 121
DW 2048
M 6,9,3
s_lshl_b32 s53, s52, 8
v_add_u32 v1, s37, v0
AW 28,48
M 7,20,2
WL 4
LO 120,16,120
LU 16,120
M 7,21,3
s_add_u32 s48, %[alo], s53
RA 216,1,0
HS 24,16
AW 29,50
M 8,32,2
s_addc_u32 s49, %[ahi], 0
LL 18,76
LL 124,77
AW 30,248
AW 31,250
M 8,33,3
s_add_u32 s52, s52, %[d]
LA 18
v_and_b32 v22, 0xfffffcff, v1
LK 48,252,39,0
CF 40,40,41
LK 248,254,39,2
s_cmp_eq_u32 s52, %[wrap]
RA 252,7,1792
CF 42,42,43
GS 37
s_cselect_b32 s52, %[rst], s52
CF 44,44,45
CF 46,46,47
M 9,56,2
LC_ 124
DQ 3072
M 9,57,3
AW 32,40
AW 33,42
M 10,68,2
WL 4
LO 121,17,121
LU 17,121
M 10,69,3
s_add_u32 s50, s50, %[d]
RA 220,1,1024
HS 24,17
M 11,80,2
LL 19,78
LL 125,79
M 11,81,3
s_cmp_eq_u32 s50, %[wrap]
LA 19
RV 252,22,5120
AW 34,44
AW 35,46
LK 40,240,40,0
CF 48,48,49
LK 44,242,40,2
s_cselect_b32 s50, %[rst], s50
RA 240,7,2048
CF 50,50,51
GS 38
CF 248,248,249
CF 250,250,251
M 6,10,4
LC_ 125
AW 36,48
M 6,11,5
AW 37,50
AW 38,248
M 7,22,4
WL 5
LO 124,18,124
LU 18,124
M 7,23,5
s_add_u32 s38, s38, 32768
RA 224,1,2048
HS 24,18
M 8,34,4
s_cmp_eq_u32 s38, s77
LL 16,80
LL 106,81
M 8,35,5
s_cselect_b32 s38, s76, s38
LA 16
AW 39,250
LK 48,244,41,0
CF 40,40,41
LK 248,246,41,2
s_barrier
RA 244,7,2304
CF 42,42,43
GS 39
s_sub_u32 s39, 0, s39
CF 44,44,45
CF 46,46,47
M 9,58,4
LC_ 106
ds_read_b64 v[14:15], v5 offset:0
M 9,59,5
AW 40,40
AW 41,42
M 10,70,4
WL 5
LO 125,19,125
LU 19,125
M 10,71,5
RA 228,1,3072
HS 24,19
M 11,82,4
LL 17,82
LL 107,83
M 11,83,5
LA 17
HX 24
Z 0,0,0
LC_ 107
W1 24,0
AW 42,44
AW 43,46
M 0,1,1
W1 26,2048
LK 40,248,42,0
CF 48,48,49
LK 44,250,42,2
RA 248,7,2560
CF 50,50,51
GS 40
CF 248,248,249
CF 250,250,251
Z 1,12,0
WL 7
LO 106,16,106
LU 16,106
)"
R"(M 1,13,1
RA 232,1,4096
HZ 27,16
Z 2,24,0
LL 18,84
LL 110,85
M 2,25,1
WL 7
LA 18
v_add_f32 v14, v14, v15
AW 44,48
Z 3,48,0
LC_ 110
v_add_f32_dpp v14, v14, v14 quad_perm:[1,0,3,2] row_mask:0xf bank_mask:0xf
AW 45,50
AW 46,248
M 3,49,1
s_add_u32 s42, s94, s60
v_add_f32_dpp v14, v14, v14 quad_perm:[2,3,0,1] row_mask:0xf bank_mask:0xf
AW 47,250
LK 48,252,43,0
CF 40,40,41
LK 248,254,43,2
s_addc_u32 s43, s95, 0
RA 252,7,2816
CF 42,42,43
GS 41
CF 44,44,45
CF 46,46,47
Z 4,60,0
WL 7
LO 107,17,107
LU 17,107
M 4,61,1
RA 236,1,6144
HS 27,17
Z 5,72,0
LL 19,86
LL 111,87
M 5,73,1
LA 19
SU 104
M 0,2,2
LC_ 111
v_add_f32_dpp v14, v14, v14 row_half_mirror row_mask:0xf bank_mask:0xf
AW 48,40
AW 49,42
M 0,3,3
v_cvt_f64_f32 v[14:15], v14
AW 50,44
M 1,14,2
WL 4
LO 110,18,110
LU 18,110
AW 51,46
M 1,15,3
RA 216,1,7168
HZ 29,18
LK 40,240,44,0
CF 48,48,49
LK 44,242,44,2
CF 50,50,51
GS 42
CF 248,248,249
CF 250,250,251
M 2,26,2
LL 16,88
LL 114,89
M 2,27,3
LA 16
s_mov_b64 exec, s[54:55]
SP
s_mov_b64 exec, -1
M 3,50,2
LC_ 114
ds_read_b64 v[14:15], v5 offset:2048
AW 52,48
M 3,51,3
s_add_u32 s42, s94, s61
v_subrev_u32 v5, s39, v5
AW 53,50
AW 54,248
M 4,62,2
WL 4
LO 111,19,111
LU 19,111
AW 55,250
M 4,63,3
s_addc_u32 s43, s95, 0
RA 220,1,8192
HS 29,19
LK 48,244,45,0
CF 40,40,41
LK 248,246,45,2
CF 42,42,43
GS 43
CF 44,44,45
CF 46,46,47
M 5,74,2
LL 17,90
LL 115,91
M 5,75,3
LA 17
SU 108
M 0,4,4
s_mov_b32 m0, %[pm]
LC_ 115
DF
AW 56,40
M 0,5,5
AW 57,42
AW 58,44
M 1,16,4
WL 4
LO 114,16,114
LU 16,114
AW 59,46
M 1,17,5
RA 224,1,9216
HZ 30,16
LK 40,248,46,0
CF 48,48,49
LK 44,250,46,2
CF 50,50,51
GS 44
CF 248,248,249
CF 250,250,251
M 2,28,4
LL 18,92
LL 118,93
M 2,29,5
WL 4
LA 18
v_add_f32 v14, v14, v15
M 3,52,4
LC_ 118
v_add_f32_dpp v14, v14, v14 quad_perm:[1,0,3,2] row_mask:0xf bank_mask:0xf
AW 60,48
M 3,53,5
s_add_u32 s42, s94, s62
v_add_f32_dpp v14, v14, v14 quad_perm:[2,3,0,1] row_mask:0xf bank_mask:0xf
AW 61,50
AW 62,248
M 4,64,4
WL 3
LO 115,17,115
LU 17,115
AW 63,250
M 4,65,5
s_addc_u32 s43, s95, 0
RA 228,1,10240
HS 30,17
LK 48,252,47,0
CF 40,40,41
LK 248,254,47,2
CF 42,42,43
v_add_f32_dpp v14, v14, v14 row_half_mirror row_mask:0xf bank_mask:0xf
GS 45
CF 44,44,45
CF 46,46,47
M 5,76,4
LL 19,94
LL 119,95
M 5,77,5
LA 19
SU 112
M 0,6,0
LC_ 119
v_cvt_f64_f32 v[14:15], v14
AW 64,40
M 0,7,1
s_mov_b64 exec, s[54:55]
SP
s_mov_b64 exec, -1
v_pk_add_f16 v29, v30, v29
AW 65,42
AW 66,44
M 1,18,0
WL 3
LO 118,18,118
LU 18,118
AW 67,46
RV 36,8,6912
M 1,19,1
RA 232,1,11264
HZ 25,18
GS 46
CF 48,48,49
CF 50,50,51
M 2,30,0
LL 16,96
LL 122,97
M 2,31,1
LA 16
RV 252,22,5392
CF 248,248,249
CF 250,250,251
M 3,54,0
LC_ 122
v_pk_add_f16 v27, v29, v27
AW 68,48
AW 69,50
M 3,55,1
s_add_u32 s42, s44, s63
HX 27
AW 70,248
AW 71,250
M 4,66,0
WL 5
LO 119,19,119
LU 19,119
M 4,67,1
s_addc_u32 s43, s45, 0
RA 236,1,12288
HS 25,19
GS 47
ST 116
s_nop 6
v_cndmask_b32 v52, v52, v53, s[78:79]
M 5,78,0
LL 17,98
LL 123,99
v_cndmask_b32 v54, v54, v55, s[78:79]
M 5,79,1
LA 17
v_cndmask_b32 v52, v52, v54, s[80:81]
M 0,8,2
LC_ 123
v_mul_f32 v52, 0x3b2aaaab, v52
M 0,9,3
C 52,52,52
v_lshlrev_b32 v52, 16, v52
M 1,20,2
WL 3
LO 122,16,122
LU 16,122
M 1,21,3
RA 216,1,13312
HS 25,16
v_add_f32 v52, v36, v52
M 2,32,2
LL 18,100
LL 126,101
C 52,52,52
M 2,33,3
LA 18
v_lshlrev_b32 v52, 16, v52
M 3,56,2
LC_ 126
v_sqrt_f32 v52, v52
M 3,57,3
s_add_u32 s42, s44, s64
C 52,52,52
v_lshlrev_b32 v52, 16, v52
M 4,68,2
WL 3
LO 123,17,123
LU 17,123
M 4,69,3
s_addc_u32 s43, s45, 0
RA 220,1,14336
HS 25,17
M 5,80,2
LL 19,102
LL 127,103
v_cmp_ge_f32 vcc, v37, v52
M 5,81,3
LA 19
ST 120
v_rcp_f32 v52, v52
M 0,10,4
LC_ 127
v_mul_f32 v52, v38, v52
M 0,11,5
v_cndmask_b32 v52, 0, v52, vcc
CF 52,52,52
M 1,22,4
WL 3
LO 126,18,126
LU 18,126
M 1,23,5
RA 224,1,15360
HS 25,18
ds_bpermute_b32 v13, v28, v52
M 2,34,4
LL 16,56
LL 104,57
M 2,35,5
LA 16
AR 248,24
M 3,58,4
LC_ 104
AR 249,25
M 3,59,5
s_add_u32 s42, s44, s65
AR 250,26
AR 251,27
M 4,70,4
WL 4
LO 127,19,127
LU 19,127
M 4,71,5
s_addc_u32 s43, s45, 0
RA 228,1,16384
HS 25,19
M 5,82,4
LL 17,58
LL 105,59
M 5,83,5
LA 17
ST 124
Z 6,0,0
LC_ 105
HX 25
M 6,1,1
W1 25,1024
AR 52,28
Z 7,12,0
WL 4
LO 104,16,104
LU 16,104
M 7,13,1
RA 232,1,17408
HZ 26,16
Z 8,24,0
LL 18,60
LL 108,61
M 8,25,1
s_mul_i32 s53, s50, 25600
LA 18
W1 27,3072
Z 9,48,0
s_add_u32 s40, %[wlo], s53
LC_ 108
v_add_u32 v4, s39, v4
AR 53,29
M 9,49,1
s_addc_u32 s41, %[whi], 0
AR 54,30
AR 55,31
Z 10,60,0
WL 5
LO 105,17,105
LU 17,105
M 10,61,1
s_mov_b32 m0, s38
RA 236,1,18432
HS 26,17
XS 248
Z 11,72,0
LL 19,62
LL 109,63
XS 249
M 11,73,1
LA 19
DW 0
XS 250
M 6,2,2
LC_ 109
XS 251
M 6,3,3
AW 24,248
AW 25,249
M 7,14,2
WL 3
LO 108,18,108
LU 18,108
M 7,15,3
s_add_u32 s37, s37, 32768
RA 216,1,19456
HZ 29,18
AW 26,250
M 8,26,2
s_cmp_eq_u32 s37, s75
LL 16,64
LL 112,65
AW 27,251
XS 52
M 8,27,3
s_cselect_b32 s37, s74, s37
LA 16
XS 53
M 9,50,2
s_mul_i32 s53, %[d], 393216
LC_ 112
XS 54
M 9,51,3
s_add_u32 s53, s53, s57
XS 55
AW 28,52
M 10,62,2
WL 3
LO 109,19,109
LU 19,109
M 10,63,3
s_add_u32 s46, %[xlo], s53
RA 220,1,20480
HS 29,19
AW 29,53
M 11,74,2
)"
R"(LL 17,66
LL 113,67
AW 30,54
M 11,75,3
s_addc_u32 s47, %[xhi], 0
LA 17
AW 31,55
M 6,4,4
s_mov_b32 s53, 0xfe987610
LC_ 113
v_min_u32 v35, v35, v13
M 6,5,5
s_cmp_eq_u32 %[d], 1
AR 248,32
AR 249,33
M 7,16,4
WL 3
LO 112,16,112
LU 16,112
M 7,17,5
s_cselect_b32 s53, s53, 0xda43cb52
RA 224,1,21504
HZ 30,16
AR 250,34
M 8,28,4
s_bitcmp1_b64 s[92:93], 48
LL 18,68
LL 116,69
AR 251,35
M 8,29,5
s_cselect_b32 s58, 16, 0
LA 18
XS 248
M 9,52,4
s_lshr_b32 s58, s53, s58
LC_ 116
v_lshrrev_b32 v3, 6, v0
v_and_b32 v3, 12, v3
v_bfe_u32 v3, s58, v3, 4
v_mad_u32_u24 v3, v3, s97, v21
XS 249
M 9,53,5
DW 1024
XS 250
XS 251
M 10,64,4
WL 3
LO 113,17,113
LU 17,113
AW 32,248
M 10,65,5
RA 228,1,22528
HS 30,17
AW 33,249
M 11,76,4
LL 19,70
LL 117,71
AW 34,250
M 11,77,5
LA 19
XL 48,0
AW 35,251
M 6,6,0
LC_ 117
DW 2048
AR 52,36
M 6,7,1
s_mul_i32 s53, s51, 49152
v_pk_add_f16 v29, v30, v29
AR 53,37
M 7,18,0
WL 3
LO 116,18,116
LU 18,116
AR 54,38
M 7,19,1
s_bitcmp1_b64 s[90:91], s51
RA 232,1,23552
HZ 24,18
AR 55,39
M 8,30,0
s_cselect_b64 s[44:45], s[84:85], s[82:83]
LL 16,72
LL 120,73
XS 52
M 8,31,1
s_add_u32 s44, s44, s53
LA 16
v_pk_add_f16 v26, v29, v26
s_addc_u32 s45, s45, 0
s_bitcmp1_b64 s[92:93], s51
s_cselect_b64 s[94:95], s[88:89], s[86:87]
s_add_u32 s94, s94, s53
s_addc_u32 s95, s95, 0
s_add_u32 s51, s51, %[d]
s_cmp_eq_u32 s51, %[wrap]
s_cselect_b32 s51, %[rst], s51
XL 49,64
XS 53
M 9,54,0
s_add_u32 s40, s40, 3072
LC_ 120
HX 26
s_addc_u32 s41, s41, 0
s_add_u32 m0, s38, 3072
XS 54
M 9,55,1
XL 50,128
XS 55
M 10,66,0
WL 3
LO 117,19,117
LU 19,117
AW 36,52
M 10,67,1
RA 236,1,24576
HS 24,19
AW 37,53
M 11,78,0
LL 17,74
LL 121,75
AW 38,54
AW 39,55
M 11,79,1
LA 17
DW 0
AR 248,40
M 6,8,2
LC_ 121
XL 51,192
AR 249,41
M 6,9,3
DW 1024
v_add_u32 v1, s37, v0
AR 250,42
M 7,20,2
WL 3
LO 120,16,120
LU 16,120
AR 251,43
M 7,21,3
RA 216,1,0
HS 24,16
XS 248
M 8,32,2
LL 18,76
LL 124,77
XS 249
M 8,33,3
LA 18
XL 52,256
XS 250
M 9,56,2
LC_ 124
DW 2048
XS 251
M 9,57,3
s_lshl_b32 s53, s52, 8
v_and_b32 v22, 0xfffffcff, v1
AW 40,248
M 10,68,2
WL 3
LO 121,17,121
LU 17,121
AW 41,249
M 10,69,3
s_add_u32 s48, %[alo], s53
RA 220,1,1024
HS 24,17
AW 42,250
M 11,80,2
LL 19,78
LL 125,79
AW 43,251
M 11,81,3
s_addc_u32 s49, %[ahi], 0
LA 19
RV 252,22,5120
AR 52,44
M 6,10,4
s_add_u32 s52, s52, %[d]
LC_ 125
AR 53,45
M 6,11,5
s_cmp_eq_u32 s52, %[wrap]
AR 54,46
AR 55,47
M 7,22,4
WL 4
LO 124,18,124
LU 18,124
XS 52
M 7,23,5
s_cselect_b32 s52, %[rst], s52
RA 224,1,2048
HS 24,18
XS 53
M 8,34,4
LL 16,80
LL 106,81
XS 54
M 8,35,5
LA 16
XL 53,320
DQ 3072
XS 55
M 9,58,4
LC_ 106
XL 54,384
AW 44,52
M 9,59,5
s_add_u32 s50, s50, %[d]
AW 45,53
AW 46,54
M 10,70,4
WL 3
LO 125,19,125
LU 19,125
M 10,71,5
RA 228,1,3072
HS 24,19
AW 47,55
M 11,82,4
LL 17,82
LL 107,83
AR 248,48
M 11,83,5
LA 17
HX 24
s_cmp_eq_u32 s50, %[wrap]
s_cselect_b32 s50, %[rst], s50
AR 249,49
Z 0,0,0
LC_ 107
XL 55,448
AR 250,50
M 0,1,1
s_add_u32 s38, s38, 32768
WV 12
BW 48
AR 251,51
Z 1,12,0
WL 4
LO 106,16,106
LU 16,106
XS 248
M 1,13,1
s_cmp_eq_u32 s38, s77
RA 232,1,4096
HZ 27,16
XS 249
Z 2,24,0
s_cselect_b32 s38, s76, s38
LL 18,84
LL 110,85
XS 250
XS 251
M 2,25,1
s_barrier
LA 18
RV 40,8,6144
AW 48,248
Z 3,36,0
s_sub_u32 s39, 0, s39
LC_ 110
ds_read_b64 v[14:15], v5 offset:0
AW 49,249
M 3,37,1
XL 56,512
RV 44,8,6208
AW 50,250
Z 4,60,0
WL 7
LO 107,17,107
LU 17,107
AW 51,251
M 4,61,1
RA 236,1,6144
HS 27,17
AR 52,52
Z 5,72,0
LL 19,86
LL 111,87
AR 53,53
M 5,73,1
LA 19
W1 24,0
AR 54,54
M 0,2,2
LC_ 111
W1 26,2048
AR 55,55
M 0,3,3
XL 57,576
RV 48,8,6272
XS 52
M 1,14,2
WL 8
LO 110,18,110
LU 18,110
XS 53
M 1,15,3
s_add_u32 s42, s94, s60
RA 216,1,7168
HZ 29,18
XS 54
M 2,26,2
s_addc_u32 s43, s95, 0
LL 16,88
LL 114,89
XS 55
M 2,27,3
LA 16
XL 58,640
AW 52,52
M 3,38,2
LC_ 114
SU 104
AW 53,53
M 3,39,3
XL 59,704
BR 48
AW 54,54
AW 55,55
M 4,62,2
WL 6
LO 111,19,111
LU 19,111
AR 248,56
M 4,63,3
s_add_u32 s42, s94, s61
RA 220,1,8192
HS 29,19
AR 249,57
M 5,74,2
LL 17,90
LL 115,91
AR 250,58
M 5,75,3
s_addc_u32 s43, s95, 0
LA 17
SU 108
AR 251,59
M 0,4,4
s_mov_b32 m0, %[pm]
LC_ 115
DF
XS 248
M 0,5,5
WV 17
BW 49
XS 249
M 1,16,4
WL 5
LO 114,16,114
LU 16,114
XS 250
M 1,17,5
RA 224,1,9216
HZ 30,16
XS 251
M 2,28,4
LL 18,92
LL 118,93
AW 56,248
M 2,29,5
WL 5
LA 18
BR 49
AW 57,249
LZ 40,48
WV 16
BW 50
M 3,40,4
LC_ 118
RV 40,8,6336
AW 58,250
M 3,41,5
s_add_u32 s42, s94, s62
BR 50
v_add_f32 v14, v14, v15
s_nop 1
v_add_f32_dpp v14, v14, v14 quad_perm:[1,0,3,2] row_mask:0xf bank_mask:0xf
s_nop 1
v_add_f32_dpp v14, v14, v14 quad_perm:[2,3,0,1] row_mask:0xf bank_mask:0xf
s_nop 1
v_add_f32_dpp v14, v14, v14 row_half_mirror row_mask:0xf bank_mask:0xf
v_cvt_f64_f32 v[14:15], v14
s_mov_b64 exec, s[54:55]
SP
s_mov_b64 exec, -1
s_nop 0
ds_read_b64 v[14:15], v5 offset:2048
AW 59,251
M 4,64,4
WL 9
LO 115,17,115
LU 17,115
AR 52,60
M 4,65,5
)"
R"(s_addc_u32 s43, s95, 0
RA 228,1,10240
HS 30,17
AR 53,61
M 5,76,4
LL 19,94
LL 119,95
AR 54,62
AR 55,63
M 5,77,5
WL 6
LA 19
SU 112
XS 52
LS 44,49
WV 16
BW 51
M 0,6,0
LC_ 119
RV 44,8,6400
XS 53
M 0,7,1
WL 6
BR 51
v_subrev_u32 v5, s39, v5
XS 54
LS 48,50
WV 14
BW 52
M 1,18,0
LO 118,18,118
LU 18,118
XS 55
M 1,19,1
RA 232,1,11264
HZ 25,18
AW 60,52
M 2,30,0
LL 16,96
LL 122,97
AW 61,53
M 2,31,1
WV 12
LA 16
RV 252,22,5392
AW 62,54
M 3,42,0
WL 4
LC_ 122
RV 48,8,6464
AW 63,55
M 3,43,1
s_add_u32 s42, s44, s63
BR 52
v_add_f32 v14, v14, v15
s_nop 1
v_add_f32_dpp v14, v14, v14 quad_perm:[1,0,3,2] row_mask:0xf bank_mask:0xf
s_nop 1
v_add_f32_dpp v14, v14, v14 quad_perm:[2,3,0,1] row_mask:0xf bank_mask:0xf
AR 248,64
LS 40,51
s_addc_u32 s43, s45, 0
BW 53
M 4,66,0
LO 119,19,119
LU 19,119
AR 249,65
M 4,67,1
RA 236,1,12288
HS 25,19
v_add_f32_dpp v14, v14, v14 row_half_mirror row_mask:0xf bank_mask:0xf
AR 250,66
M 5,78,0
LL 17,98
LL 123,99
AR 251,67
M 5,79,1
LA 17
ST 116
v_cvt_f64_f32 v[14:15], v14
XS 248
M 0,8,2
LC_ 123
s_mov_b64 exec, s[54:55]
SP
s_mov_b64 exec, -1
XS 249
M 0,9,3
WL 4
RV 40,8,6528
BR 53
v_pk_add_f16 v29, v30, v29
XS 250
XS 251
LS 44,52
WV 12
BW 54
M 1,20,2
LO 122,16,122
LU 16,122
v_pk_add_f16 v27, v29, v27
AW 64,248
M 1,21,3
RA 216,1,13312
HS 25,16
HX 27
AW 65,249
M 2,32,2
LL 18,100
LL 126,101
AW 66,250
M 2,33,3
WV 11
LA 18
RV 44,8,6592
AW 67,251
M 3,44,2
WL 4
LC_ 126
BR 54
AR 52,68
M 3,45,3
s_add_u32 s42, s44, s64
AR 53,69
AR 54,70
LS 48,53
s_addc_u32 s43, s45, 0
BW 55
M 4,68,2
LO 123,17,123
LU 17,123
M 4,69,3
RA 220,1,14336
HS 25,17
AR 55,71
M 5,80,2
LL 19,102
LL 127,103
XS 52
M 5,81,3
LA 19
ST 120
XS 53
M 0,10,4
LC_ 127
RV 48,8,6656
XS 54
M 0,11,5
WL 5
BR 55
XS 55
LS 40,54
WV 11
BW 56
M 1,22,4
LO 126,18,126
LU 18,126
AW 68,52
M 1,23,5
RA 224,1,15360
HS 25,18
AW 69,53
M 2,34,4
LL 16,56
LL 104,57
AW 70,54
AW 71,55
M 2,35,5
LA 16
RV 40,8,6720
M 3,46,4
LC_ 104
BR 56
M 3,47,5
s_add_u32 s42, s44, s65
M 4,70,4
WL 6
LO 127,19,127
LU 19,127
M 4,71,5
s_addc_u32 s43, s45, 0
RA 228,1,16384
HS 25,19
LS 44,55
WV 10
BW 57
M 5,82,4
LL 17,58
LL 105,59
M 5,83,5
LA 17
ST 124
Z 6,0,0
LC_ 105
RV 44,8,6784
M 6,1,1
BR 57
HX 25
Z 7,12,0
WL 7
LO 104,16,104
LU 16,104
M 7,13,1
WL 6
RA 232,1,17408
HZ 26,16
LS 48,56
WV 10
BW 58
Z 8,24,0
LL 18,60
LL 108,61
M 8,25,1
s_mul_i32 s53, s50, 25600
LA 18
W1 25,1024
Z 9,36,0
s_add_u32 s40, %[wlo], s53
LC_ 108
W1 27,3072
M 9,37,1
s_addc_u32 s41, %[whi], 0
RV 48,8,6848
RA 240,7,0
Z 10,60,0
WL 10
LO 105,17,105
LU 17,105
M 10,61,1
s_mov_b32 m0, s38
RA 236,1,18432
HS 26,17
Z 11,72,0
LL 19,62
LL 109,63
M 11,73,1
LA 19
DW 0
M 6,2,2
LC_ 109
BR 58
M 6,3,3
WL 12
v_add_u32 v4, s39, v4
LS 40,57
WV 9
BW 59
M 7,14,2
WL 8
LO 108,18,108
LU 18,108
M 7,15,3
s_add_u32 s37, s37, 32768
RA 216,1,19456
HZ 29,18
M 8,26,2
s_cmp_eq_u32 s37, s75
LL 16,64
LL 112,65
M 8,27,3
s_cselect_b32 s37, s74, s37
LA 16
DW 1024
M 9,38,2
LC_ 112
RA 244,7,256
M 9,39,3
BR 59
M 10,62,2
WL 7
LO 109,19,109
LU 19,109
M 10,63,3
WL 6
RA 220,1,20480
HS 29,19
LS 44,58
RA 248,7,512
M 11,74,2
LL 17,66
LL 113,67
M 11,75,3
LA 17
DW 2048
M 6,4,4
LC_ 113
M 6,5,5
M 7,16,4
WL 4
LO 112,16,112
LU 16,112
M 7,17,5
s_mul_i32 s53, s51, 49152
RA 224,1,21504
HZ 30,16
LS 48,59
s_bitcmp1_b64 s[90:91], s51
RA 252,7,768
M 8,28,4
s_cselect_b64 s[44:45], s[84:85], s[82:83]
LL 18,68
LL 116,69
M 8,29,5
s_add_u32 s44, s44, s53
LA 18
v_mul_f32 v36, 0xbb2aaaab, v36
M 9,40,4
s_addc_u32 s45, s45, 0
LC_ 116
v_mul_f32 v37, 0xbb2aaaab, v37
M 9,41,5
s_bitcmp1_b64 s[92:93], s51
v_mul_f32 v38, 0xbb2aaaab, v38
v_mul_f32 v39, 0xbb2aaaab, v39
M 10,64,4
WL 4
LO 113,17,113
LU 17,113
M 10,65,5
s_cselect_b64 s[94:95], s[88:89], s[86:87]
RA 228,1,22528
HS 30,17
LK 40,240,48,0
LK 44,242,48,2
s_add_u32 s94, s94, s53
RA 240,7,1024
LK 48,244,49,0
s_nop 0
CF 40,40,41
LK 248,246,49,2
s_addc_u32 s95, s95, 0
RA 244,7,1280
CF 42,42,43
M 11,76,4
LL 19,70
LL 117,71
M 11,77,5
s_add_u32 s51, s51, %[d]
LA 19
CF 44,44,45
M 6,6,0
s_cmp_eq_u32 s51, %[wrap]
LC_ 117
CF 46,46,47
M 6,7,1
s_cselect_b32 s51, %[rst], s51
AW 72,40
AW 73,42
M 7,18,0
WL 5
LO 116,18,116
LU 18,116
M 7,19,1
s_mul_i32 s53, %[d], 17408
RA 232,1,23552
HZ 31,18
M 8,30,0
s_add_u32 s63, s53, %[sb1]
LL 16,72
LL 120,73
M 8,31,1
s_add_u32 s40, s40, 3072
LA 16
AW 74,44
M 9,42,0
s_addc_u32 s41, s41, 0
LC_ 120
s_add_u32 m0, s38, 3072
M 9,43,1
DW 0
AW 75,46
LK 40,248,50,0
CF 48,48,49
LK 44,250,50,2
RA 248,7,1536
CF 50,50,51
GZ 48
CF 248,248,249
CF 250,250,251
M 10,66,0
WL 4
LO 117,19,117
LU 19,117
M 10,67,1
RA 236,1,24576
HS 31,19
M 11,78,0
LL 17,74
LL 121,75
M 11,79,1
LA 17
DW 1024
M 6,8,2
LC_ 121
DW 2048
M 6,9,3
s_lshl_b32 s53, s52, 8
v_add_u32 v1, s37, v0
v_pk_add_f16 v31, v30, v31
M 7,20,2
)"
R"(WL 4
LO 120,16,120
LU 16,120
M 7,21,3
s_add_u32 s48, %[alo], s53
RA 216,1,0
HZ 24,16
AW 76,48
AW 77,50
M 8,32,2
s_addc_u32 s49, %[ahi], 0
LL 18,76
LL 124,77
AW 78,248
AW 79,250
M 8,33,3
s_add_u32 s52, s52, %[d]
LA 18
v_and_b32 v22, 0xfffffcff, v1
LK 48,252,51,0
CF 40,40,41
LK 248,254,51,2
s_cmp_eq_u32 s52, %[wrap]
RA 252,7,1792
CF 42,42,43
GS 49
s_cselect_b32 s52, %[rst], s52
CF 44,44,45
CF 46,46,47
M 9,44,2
LC_ 124
DQ 3072
M 9,45,3
v_pk_add_f16 v29, v31, v29
AW 80,40
M 10,68,2
WL 4
LO 121,17,121
LU 17,121
M 10,69,3
s_add_u32 s50, s50, %[d]
RA 220,1,1024
HS 24,17
M 11,80,2
LL 19,78
LL 125,79
AW 81,42
M 11,81,3
s_cmp_eq_u32 s50, %[wrap]
LA 19
RV 252,22,5120
AW 82,44
AW 83,46
LK 40,240,52,0
CF 48,48,49
LK 44,242,52,2
s_cselect_b32 s50, %[rst], s50
RA 240,7,2048
CF 50,50,51
GS 50
CF 248,248,249
CF 250,250,251
M 6,10,4
LC_ 125
v_pk_add_f16 v26, v29, v26
M 6,11,5
HX 26
AW 84,48
M 7,22,4
WL 5
LO 124,18,124
LU 18,124
M 7,23,5
s_add_u32 s38, s38, 32768
RA 224,1,2048
HS 24,18
M 8,34,4
s_cmp_eq_u32 s38, s77
LL 16,80
LL 106,81
M 8,35,5
s_cselect_b32 s38, s76, s38
LA 16
AW 85,50
AW 86,248
M 9,46,4
s_barrier
LC_ 106
AW 87,250
M 9,47,5
s_sub_u32 s39, 0, s39
LK 48,244,53,0
CF 40,40,41
LK 248,246,53,2
RA 244,7,2304
CF 42,42,43
GS 51
CF 44,44,45
CF 46,46,47
M 10,70,4
WL 4
LO 125,19,125
LU 19,125
M 10,71,5
RA 228,1,3072
HS 24,19
M 11,82,4
LL 17,82
LL 107,83
M 11,83,5
LA 17
HX 24
AW 88,40
AW 89,42
Z 0,0,0
LC_ 107
ds_read_b64 v[14:15], v5 offset:0
AW 90,44
AW 91,46
M 0,1,1
W1 24,0
LK 40,248,54,0
CF 48,48,49
LK 44,250,54,2
RA 248,7,2560
CF 50,50,51
GS 52
CF 248,248,249
CF 250,250,251
Z 1,12,0
WL 7
LO 106,16,106
LU 16,106
M 1,13,1
RA 232,1,4096
HZ 27,16
Z 2,24,0
LL 18,84
LL 110,85
M 2,25,1
LA 18
W1 26,2048
AW 92,48
Z 3,36,0
LC_ 110
AW 93,50
AW 94,248
M 3,37,1
s_add_u32 s42, s94, s60
AW 95,250
LK 48,252,55,0
CF 40,40,41
LK 248,254,55,2
s_addc_u32 s43, s95, 0
RA 252,7,2816
CF 42,42,43
GS 53
CF 44,44,45
CF 46,46,47
Z 4,60,0
WL 8
LO 107,17,107
LU 17,107
M 4,61,1
RA 236,1,6144
HS 27,17
Z 5,72,0
LL 19,86
LL 111,87
M 5,73,1
LA 19
SU 104
M 0,2,2
WL 9
LC_ 111
v_add_f32 v14, v14, v15
AW 96,40
AW 97,42
M 0,3,3
v_add_f32_dpp v14, v14, v14 quad_perm:[1,0,3,2] row_mask:0xf bank_mask:0xf
AW 98,44
M 1,14,2
WL 4
LO 110,18,110
LU 18,110
AW 99,46
M 1,15,3
RA 216,1,7168
HZ 29,18
LK 40,240,56,0
CF 48,48,49
LK 44,242,56,2
CF 50,50,51
v_add_f32_dpp v14, v14, v14 quad_perm:[2,3,0,1] row_mask:0xf bank_mask:0xf
GS 54
CF 248,248,249
CF 250,250,251
M 2,26,2
LL 16,88
LL 114,89
M 2,27,3
LA 16
v_add_f32_dpp v14, v14, v14 row_half_mirror row_mask:0xf bank_mask:0xf
M 3,38,2
LC_ 114
v_cvt_f64_f32 v[14:15], v14
AW 100,48
M 3,39,3
s_add_u32 s42, s94, s61
s_mov_b64 exec, s[54:55]
SP
s_mov_b64 exec, -1
AW 101,50
AW 102,248
M 4,62,2
WL 3
LO 111,19,111
LU 19,111
AW 103,250
M 4,63,3
s_addc_u32 s43, s95, 0
RA 220,1,8192
HS 29,19
LK 48,244,57,0
CF 40,40,41
LK 248,246,57,2
CF 42,42,43
GS 55
CF 44,44,45
CF 46,46,47
M 5,74,2
LL 17,90
LL 115,91
M 5,75,3
LA 17
ds_read_b64 v[14:15], v5 offset:2048
M 0,4,4
LC_ 115
SU 108
AW 104,40
M 0,5,5
s_mov_b32 m0, %[pm]
s_nop 0
DF
v_subrev_u32 v5, s39, v5
AW 105,42
AW 106,44
M 1,16,4
WL 4
LO 114,16,114
LU 16,114
AW 107,46
M 1,17,5
RA 224,1,9216
HZ 30,16
LK 40,248,58,0
CF 48,48,49
LK 44,250,58,2
CF 50,50,51
GS 56
CF 248,248,249
CF 250,250,251
M 2,28,4
LL 18,92
LL 118,93
M 2,29,5
LA 18
AW 108,48
M 3,40,4
LC_ 118
AW 109,50
M 3,41,5
s_add_u32 s42, s94, s62
AW 110,248
AW 111,250
LK 48,252,59,0
CF 40,40,41
LK 248,254,59,2
s_addc_u32 s43, s95, 0
CF 42,42,43
GS 57
CF 44,44,45
CF 46,46,47
M 4,64,4
WL 3
LO 115,17,115
LU 17,115
M 4,65,5
RA 228,1,10240
HS 30,17
M 5,76,4
LL 19,94
LL 119,95
M 5,77,5
LA 19
SU 112
M 0,6,0
LC_ 119
v_add_f32 v14, v14, v15
AW 112,40
M 0,7,1
v_add_f32_dpp v14, v14, v14 quad_perm:[1,0,3,2] row_mask:0xf bank_mask:0xf
AW 113,42
AW 114,44
M 1,18,0
WL 3
LO 118,18,118
LU 18,118
AW 115,46
RV 36,8,6912
M 1,19,1
RA 232,1,11264
HZ 31,18
GS 58
CF 48,48,49
CF 50,50,51
M 2,30,0
LL 16,96
LL 122,97
M 2,31,1
LA 16
RV 252,22,5392
CF 248,248,249
CF 250,250,251
M 3,42,0
LC_ 122
v_add_f32_dpp v14, v14, v14 quad_perm:[2,3,0,1] row_mask:0xf bank_mask:0xf
AW 116,48
AW 117,50
M 3,43,1
s_add_u32 s42, s94, s63
v_add_f32_dpp v14, v14, v14 row_half_mirror row_mask:0xf bank_mask:0xf
AW 118,248
AW 119,250
M 4,66,0
WL 5
LO 119,19,119
LU 19,119
M 4,67,1
s_addc_u32 s43, s95, 0
RA 236,1,12288
HS 31,19
GS 59
SU 116
v_cvt_f64_f32 v[14:15], v14
s_nop 5
v_cndmask_b32 v52, v52, v53, s[78:79]
M 5,78,0
LL 17,98
LL 123,99
v_cndmask_b32 v54, v54, v55, s[78:79]
M 5,79,1
LA 17
s_mov_b64 exec, s[54:55]
SP
s_mov_b64 exec, -1
v_cndmask_b32 v52, v52, v54, s[80:81]
M 0,8,2
LC_ 123
v_pk_add_f16 v31, v30, v31
v_mul_f32 v52, 0x3b2aaaab, v52
M 0,9,3
v_pk_add_f16 v29, v31, v29
C 52,52,52
M 1,20,2
WL 3
LO 122,16,122
LU 16,122
v_lshlrev_b32 v52, 16, v52
M 1,21,3
RA 216,1,13312
HZ 25,16
v_add_f32 v52, v36, v52
M 2,32,2
LL 18,100
LL 126,101
C 52,52,52
M 2,33,3
LA 18
v_pk_add_f16 v27, v29, v27
v_lshlrev_b32 v52, 16, v52
M 3,44,2
LC_ 126
HX 27
v_sqrt_f32 v52, v52
M 3,45,3
s_add_u32 s42, s44, s64
C 52,52,52
v_lshlrev_b32 v52, 16, v52
M 4,68,2
WL 3
LO 123,17,123
LU 17,123
M 4,69,3
s_addc_u32 s43, s45, 0
RA 220,1,14336
HS 25,17
M 5,80,2
LL 19,102
LL 127,103
)"
R"(v_cmp_ge_f32 vcc, v37, v52
M 5,81,3
LA 19
ST 120
v_rcp_f32 v52, v52
M 0,10,4
LC_ 127
v_mul_f32 v52, v38, v52
M 0,11,5
v_cndmask_b32 v52, 0, v52, vcc
CF 52,52,52
M 1,22,4
WL 3
LO 126,18,126
LU 18,126
M 1,23,5
RA 224,1,15360
HS 25,18
ds_bpermute_b32 v13, v28, v52
M 2,34,4
LL 16,56
LL 104,57
M 2,35,5
LA 16
AR 248,72
M 3,46,4
LC_ 104
AR 249,73
M 3,47,5
s_add_u32 s42, s44, s65
AR 250,74
AR 251,75
M 4,70,4
WL 4
LO 127,19,127
LU 19,127
M 4,71,5
s_addc_u32 s43, s45, 0
RA 228,1,16384
HS 25,19
M 5,82,4
LL 17,58
LL 105,59
M 5,83,5
LA 17
ST 124
Z 6,0,0
LC_ 105
HX 25
M 6,1,1
W1 25,1024
AR 52,76
Z 7,12,0
WL 4
LO 104,16,104
LU 16,104
M 7,13,1
RA 232,1,17408
HZ 26,16
Z 8,24,0
LL 18,60
LL 108,61
M 8,25,1
s_mul_i32 s53, s50, 25600
LA 18
W1 27,3072
Z 9,36,0
s_add_u32 s40, %[wlo], s53
LC_ 108
v_add_u32 v4, s39, v4
AR 53,77
M 9,37,1
s_addc_u32 s41, %[whi], 0
AR 54,78
AR 55,79
Z 10,60,0
WL 5
LO 105,17,105
LU 17,105
M 10,61,1
s_mov_b32 m0, s38
RA 236,1,18432
HS 26,17
XS 248
Z 11,72,0
LL 19,62
LL 109,63
XS 249
M 11,73,1
LA 19
DW 0
XS 250
M 6,2,2
LC_ 109
XS 251
M 6,3,3
AW 72,248
AW 73,249
M 7,14,2
WL 3
LO 108,18,108
LU 18,108
M 7,15,3
s_add_u32 s37, s37, 32768
RA 216,1,19456
HZ 29,18
AW 74,250
M 8,26,2
s_cmp_eq_u32 s37, s75
LL 16,64
LL 112,65
AW 75,251
M 8,27,3
s_cselect_b32 s37, s74, s37
LA 16
XS 52
M 9,38,2
s_mul_i32 s53, %[d], 393216
LC_ 112
XS 53
M 9,39,3
s_add_u32 s53, s53, s57
XS 54
XS 55
M 10,62,2
WL 3
LO 109,19,109
LU 19,109
M 10,63,3
s_add_u32 s46, %[xlo], s53
RA 220,1,20480
HS 29,19
AW 76,52
AW 77,53
M 11,74,2
LL 17,66
LL 113,67
AW 78,54
M 11,75,3
s_addc_u32 s47, %[xhi], 0
LA 17
AW 79,55
M 6,4,4
s_mov_b32 s53, 0xda43cb52
LC_ 113
v_min_u32 v35, v35, v13
M 6,5,5
s_cmp_eq_u32 %[d], 1
AR 248,80
AR 249,81
M 7,16,4
WL 3
LO 112,16,112
LU 16,112
M 7,17,5
s_cselect_b32 s53, s53, 0xfe987610
RA 224,1,21504
HZ 30,16
AR 250,82
M 8,28,4
s_bitcmp1_b64 s[92:93], 48
LL 18,68
LL 116,69
AR 251,83
M 8,29,5
s_cselect_b32 s58, 16, 0
LA 18
XS 248
M 9,40,4
s_lshr_b32 s58, s53, s58
LC_ 116
v_lshrrev_b32 v3, 6, v0
v_and_b32 v3, 12, v3
v_bfe_u32 v3, s58, v3, 4
v_mad_u32_u24 v3, v3, s97, v21
XS 249
M 9,41,5
DW 1024
XS 250
M 10,64,4
WL 3
LO 113,17,113
LU 17,113
XS 251
M 10,65,5
RA 228,1,22528
HS 30,17
AW 80,248
M 11,76,4
LL 19,70
LL 117,71
AW 81,249
M 11,77,5
LA 19
XL 60,0
AW 82,250
M 6,6,0
LC_ 117
DW 2048
AW 83,251
M 6,7,1
s_mul_i32 s53, s51, 49152
AR 52,84
AR 53,85
M 7,18,0
WL 3
LO 116,18,116
LU 18,116
M 7,19,1
s_bitcmp1_b64 s[90:91], s51
RA 232,1,23552
HZ 31,18
AR 54,86
M 8,30,0
s_cselect_b64 s[44:45], s[84:85], s[82:83]
LL 16,72
LL 120,73
AR 55,87
M 8,31,1
s_add_u32 s44, s44, s53
LA 16
s_addc_u32 s45, s45, 0
s_bitcmp1_b64 s[92:93], s51
s_cselect_b64 s[94:95], s[88:89], s[86:87]
s_add_u32 s94, s94, s53
s_addc_u32 s95, s95, 0
s_add_u32 s51, s51, %[d]
s_cmp_eq_u32 s51, %[wrap]
s_cselect_b32 s51, %[rst], s51
XL 61,64
XS 52
XS 53
M 9,42,0
s_add_u32 s40, s40, 3072
LC_ 120
s_addc_u32 s41, s41, 0
s_add_u32 m0, s38, 3072
XS 54
M 9,43,1
XL 62,128
XS 55
M 10,66,0
WL 3
LO 117,19,117
LU 19,117
AW 84,52
M 10,67,1
RA 236,1,24576
HS 31,19
AW 85,53
M 11,78,0
LL 17,74
LL 121,75
AW 86,54
M 11,79,1
LA 17
DW 0
AW 87,55
M 6,8,2
LC_ 121
XL 63,192
AR 248,88
M 6,9,3
DW 1024
v_add_u32 v1, s37, v0
AR 249,89
M 7,20,2
WL 3
LO 120,16,120
LU 16,120
AR 250,90
M 7,21,3
RA 216,1,0
HZ 24,16
AR 251,91
M 8,32,2
LL 18,76
LL 124,77
XS 248
M 8,33,3
LA 18
XL 64,256
XS 249
M 9,44,2
LC_ 124
DW 2048
XS 250
M 9,45,3
s_lshl_b32 s53, s52, 8
v_and_b32 v22, 0xfffffcff, v1
v_pk_add_f16 v31, v30, v31
XS 251
M 10,68,2
WL 3
LO 121,17,121
LU 17,121
AW 88,248
M 10,69,3
s_add_u32 s48, %[alo], s53
RA 220,1,1024
HS 24,17
AW 89,249
M 11,80,2
LL 19,78
LL 125,79
AW 90,250
M 11,81,3
s_addc_u32 s49, %[ahi], 0
LA 19
RV 252,22,5120
AW 91,251
M 6,10,4
s_add_u32 s52, s52, %[d]
LC_ 125
v_pk_add_f16 v29, v31, v29
AR 52,92
AR 53,93
M 6,11,5
s_cmp_eq_u32 s52, %[wrap]
v_pk_add_f16 v26, v29, v26
AR 54,94
M 7,22,4
WL 4
LO 124,18,124
LU 18,124
AR 55,95
M 7,23,5
s_cselect_b32 s52, %[rst], s52
RA 224,1,2048
HS 24,18
XS 52
M 8,34,4
LL 16,80
LL 106,81
HX 26
XS 53
M 8,35,5
LA 16
XL 65,320
DQ 3072
XS 54
M 9,46,4
LC_ 106
XL 66,384
XS 55
M 9,47,5
s_add_u32 s50, s50, %[d]
AW 92,52
AW 93,53
M 10,70,4
WL 3
LO 125,19,125
LU 19,125
M 10,71,5
RA 228,1,3072
HS 24,19
AW 94,54
M 11,82,4
LL 17,82
LL 107,83
AW 95,55
M 11,83,5
LA 17
HX 24
s_cmp_eq_u32 s50, %[wrap]
s_cselect_b32 s50, %[rst], s50
AR 248,96
Z 0,0,0
LC_ 107
XL 67,448
AR 249,97
M 0,1,1
s_add_u32 s38, s38, 32768
WV 12
BW 60
AR 250,98
Z 1,12,0
WL 4
LO 106,16,106
LU 16,106
AR 251,99
M 1,13,1
s_cmp_eq_u32 s38, s77
RA 232,1,4096
HZ 27,16
XS 248
Z 2,24,0
s_cselect_b32 s38, s76, s38
LL 18,84
LL 110,85
XS 249
M 2,25,1
s_barrier
LA 18
RV 40,8,6144
XS 250
Z 3,36,0
s_sub_u32 s39, 0, s39
LC_ 110
ds_read_b64 v[14:15], v5 offset:0
)"
R"(XS 251
M 3,37,1
XL 68,512
RV 44,8,6208
AW 96,248
Z 4,48,0
WL 7
LO 107,17,107
LU 17,107
AW 97,249
AW 98,250
M 4,49,1
RA 236,1,6144
HS 27,17
AW 99,251
Z 5,72,0
LL 19,86
LL 111,87
AR 52,100
M 5,73,1
LA 19
W1 24,0
AR 53,101
M 0,2,2
LC_ 111
W1 26,2048
AR 54,102
M 0,3,3
XL 69,576
RV 48,8,6272
AR 55,103
M 1,14,2
WL 8
LO 110,18,110
LU 18,110
XS 52
M 1,15,3
s_add_u32 s42, s94, s60
RA 216,1,7168
HZ 29,18
XS 53
M 2,26,2
s_addc_u32 s43, s95, 0
LL 16,88
LL 114,89
XS 54
M 2,27,3
LA 16
XL 70,640
XS 55
M 3,38,2
LC_ 114
SU 104
AW 100,52
M 3,39,3
XL 71,704
BR 60
AW 101,53
M 4,50,2
WL 6
LO 111,19,111
LU 19,111
AW 102,54
M 4,51,3
s_add_u32 s42, s94, s61
RA 220,1,8192
HS 29,19
AW 103,55
M 5,74,2
LL 17,90
LL 115,91
AR 248,104
M 5,75,3
s_addc_u32 s43, s95, 0
LA 17
SU 108
AR 249,105
M 0,4,4
LC_ 115
v_add_f32 v14, v14, v15
s_nop 1
v_add_f32_dpp v14, v14, v14 quad_perm:[1,0,3,2] row_mask:0xf bank_mask:0xf
s_nop 1
v_add_f32_dpp v14, v14, v14 quad_perm:[2,3,0,1] row_mask:0xf bank_mask:0xf
s_nop 1
v_add_f32_dpp v14, v14, v14 row_half_mirror row_mask:0xf bank_mask:0xf
AR 250,106
M 0,5,5
WV 16
BW 61
AR 251,107
M 1,16,4
WL 5
LO 114,16,114
LU 16,114
v_cvt_f64_f32 v[14:15], v14
XS 248
M 1,17,5
RA 224,1,9216
HZ 30,16
XS 249
XS 250
M 2,28,4
LL 18,92
LL 118,93
s_mov_b64 exec, s[54:55]
SP
s_mov_b64 exec, -1
XS 251
M 2,29,5
WV 16
LA 18
ds_read_b64 v[14:15], v5 offset:2048
AW 104,248
M 3,40,4
WL 7
LC_ 118
BR 61
AW 105,249
M 3,41,5
s_add_u32 s42, s94, s62
v_subrev_u32 v5, s39, v5
AW 106,250
LZ 40,60
s_addc_u32 s43, s95, 0
BW 62
M 4,52,4
WL 7
LO 115,17,115
LU 17,115
AW 107,251
M 4,53,5
RA 228,1,10240
HS 30,17
AR 52,108
M 5,76,4
LL 19,94
LL 119,95
AR 53,109
M 5,77,5
LA 19
SU 112
AR 54,110
M 0,6,0
LC_ 119
RV 40,8,6336
AR 55,111
M 0,7,1
WL 5
BR 62
XS 52
LS 44,61
WV 15
BW 63
M 1,18,0
LO 118,18,118
LU 18,118
XS 53
M 1,19,1
RA 232,1,11264
HZ 31,18
XS 54
M 2,30,0
LL 16,96
LL 122,97
XS 55
M 2,31,1
WV 13
LA 16
RV 252,22,5392
AW 108,52
M 3,42,0
WL 4
LC_ 122
RV 44,8,6400
AW 109,53
M 3,43,1
s_add_u32 s42, s94, s63
BR 63
v_add_f32 v14, v14, v15
s_nop 1
v_add_f32_dpp v14, v14, v14 quad_perm:[1,0,3,2] row_mask:0xf bank_mask:0xf
s_nop 1
v_add_f32_dpp v14, v14, v14 quad_perm:[2,3,0,1] row_mask:0xf bank_mask:0xf
AW 110,54
LS 48,62
s_addc_u32 s43, s95, 0
BW 64
M 4,54,0
LO 119,19,119
LU 19,119
AW 111,55
M 4,55,1
RA 236,1,12288
HS 31,19
v_add_f32_dpp v14, v14, v14 row_half_mirror row_mask:0xf bank_mask:0xf
AR 248,112
M 5,78,0
LL 17,98
LL 123,99
AR 249,113
M 5,79,1
LA 17
SU 116
v_cvt_f64_f32 v[14:15], v14
AR 250,114
AR 251,115
M 0,8,2
LC_ 123
s_mov_b64 exec, s[54:55]
SP
s_mov_b64 exec, -1
XS 248
M 0,9,3
WL 4
RV 48,8,6464
BR 64
XS 249
LS 40,63
WV 13
BW 65
M 1,20,2
LO 122,16,122
LU 16,122
XS 250
M 1,21,3
RA 216,1,13312
HZ 25,16
XS 251
M 2,32,2
LL 18,100
LL 126,101
AW 112,248
M 2,33,3
LA 18
RV 40,8,6528
AW 113,249
M 3,44,2
LC_ 126
BR 65
AW 114,250
M 3,45,3
s_add_u32 s42, s44, s64
v_pk_add_f16 v31, v30, v31
AW 115,251
M 4,56,2
WL 6
LO 123,17,123
LU 17,123
AR 52,116
M 4,57,3
s_addc_u32 s43, s45, 0
RA 220,1,14336
HS 25,17
AR 53,117
LS 44,64
WV 11
BW 66
M 5,80,2
LL 19,102
LL 127,103
AR 54,118
M 5,81,3
LA 19
ST 120
AR 55,119
M 0,10,4
LC_ 127
RV 44,8,6592
XS 52
M 0,11,5
WL 5
BR 66
v_pk_add_f16 v29, v31, v29
XS 53
LS 48,65
WV 11
BW 67
M 1,22,4
LO 126,18,126
LU 18,126
XS 54
M 1,23,5
RA 224,1,15360
HS 25,18
v_pk_add_f16 v27, v29, v27
XS 55
M 2,34,4
LL 16,56
LL 104,57
HX 27
AW 116,52
M 2,35,5
WV 10
LA 16
RV 48,8,6656
AW 117,53
M 3,46,4
WL 4
LC_ 104
BR 67
AW 118,54
AW 119,55
M 3,47,5
s_add_u32 s42, s44, s65
LS 40,66
s_addc_u32 s43, s45, 0
BW 68
M 4,58,4
LO 127,19,127
LU 19,127
M 4,59,5
RA 228,1,16384
HS 25,19
M 5,82,4
LL 17,58
LL 105,59
M 5,83,5
LA 17
ST 124
Z 6,0,0
LC_ 105
RV 40,8,6720
M 6,1,1
BR 68
HX 25
Z 7,12,0
WL 7
LO 104,16,104
LU 16,104
M 7,13,1
WL 6
RA 232,1,17408
HZ 26,16
LS 44,67
WV 10
BW 69
Z 8,24,0
LL 18,60
LL 108,61
M 8,25,1
s_mul_i32 s53, s50, 25600
LA 18
W1 25,1024
Z 9,36,0
s_add_u32 s40, %[wlo], s53
LC_ 108
W1 27,3072
M 9,37,1
s_addc_u32 s41, %[whi], 0
RV 44,8,6784
BR 69
Z 10,48,0
WL 8
LO 105,17,105
LU 17,105
M 10,49,1
s_mov_b32 m0, s38
RA 236,1,18432
HS 26,17
LS 48,68
WV 9
BW 70
Z 11,72,0
LL 19,62
LL 109,63
M 11,73,1
LA 19
DW 0
M 6,2,2
LC_ 109
RV 48,8,6848
M 6,3,3
RA 240,7,0
BR 70
M 7,14,2
WL 7
LO 108,18,108
LU 18,108
M 7,15,3
s_add_u32 s37, s37, 32768
RA 216,1,19456
HZ 29,18
M 8,26,2
s_cmp_eq_u32 s37, s75
LL 16,64
LL 112,65
M 8,27,3
s_cselect_b32 s37, s74, s37
LA 16
DW 1024
LS 40,69
WV 9
BW 71
M 9,38,2
LC_ 112
RA 244,7,256
M 9,39,3
BR 71
v_add_u32 v4, s39, v4
)"
R"(M 10,50,2
WL 9
LO 109,19,109
LU 19,109
M 10,51,3
RA 220,1,20480
HS 29,19
M 11,74,2
LL 17,66
LL 113,67
M 11,75,3
WL 7
LA 17
DW 2048
LS 44,70
RA 248,7,512
M 6,4,4
LC_ 113
M 6,5,5
M 7,16,4
WL 4
LO 112,16,112
LU 16,112
M 7,17,5
s_mul_i32 s53, s51, 49152
RA 224,1,21504
HZ 30,16
LS 48,71
s_bitcmp1_b64 s[90:91], s51
RA 252,7,768
M 8,28,4
s_cselect_b64 s[44:45], s[84:85], s[82:83]
LL 18,68
LL 116,69
M 8,29,5
s_add_u32 s44, s44, s53
LA 18
v_mul_f32 v36, 0xbb2aaaab, v36
M 9,40,4
s_addc_u32 s45, s45, 0
LC_ 116
v_mul_f32 v37, 0xbb2aaaab, v37
M 9,41,5
s_bitcmp1_b64 s[92:93], s51
v_mul_f32 v38, 0xbb2aaaab, v38
v_mul_f32 v39, 0xbb2aaaab, v39
M 10,52,4
WL 4
LO 113,17,113
LU 17,113
M 10,53,5
s_cselect_b64 s[94:95], s[88:89], s[86:87]
RA 228,1,22528
HS 30,17
LK 40,240,60,0
LK 44,242,60,2
s_add_u32 s94, s94, s53
RA 240,7,1024
LK 48,244,61,0
s_nop 0
CF 40,40,41
LK 248,246,61,2
s_addc_u32 s95, s95, 0
RA 244,7,1280
CF 42,42,43
M 11,76,4
LL 19,70
LL 117,71
M 11,77,5
s_add_u32 s51, s51, %[d]
LA 19
CF 44,44,45
M 6,6,0
s_cmp_eq_u32 s51, %[wrap]
LC_ 117
CF 46,46,47
M 6,7,1
s_cselect_b32 s51, %[rst], s51
AW 120,40
AW 121,42
M 7,18,0
WL 5
LO 116,18,116
LU 18,116
M 7,19,1
s_mul_i32 s53, %[d], 32768
RA 232,1,23552
HZ 31,18
M 8,30,0
s_add_u32 s64, s53, %[sb1]
LL 16,72
LL 120,73
M 8,31,1
s_add_u32 s40, s40, 3072
LA 16
AW 122,44
M 9,42,0
s_addc_u32 s41, s41, 0
LC_ 120
s_add_u32 m0, s38, 3072
M 9,43,1
DW 0
AW 123,46
LK 40,248,62,0
CF 48,48,49
LK 44,250,62,2
RA 248,7,1536
CF 50,50,51
GZ 60
CF 248,248,249
CF 250,250,251
M 10,54,0
WL 4
LO 117,19,117
LU 19,117
M 10,55,1
RA 236,1,24576
HS 31,19
M 11,78,0
LL 17,74
LL 121,75
M 11,79,1
LA 17
DW 1024
M 6,8,2
LC_ 121
DW 2048
M 6,9,3
s_lshl_b32 s53, s52, 8
v_add_u32 v1, s37, v0
v_pk_add_f16 v31, v30, v31
M 7,20,2
WL 4
LO 120,16,120
LU 16,120
M 7,21,3
s_add_u32 s48, %[alo], s53
RA 216,1,0
HZ 32,16
AW 124,48
AW 125,50
M 8,32,2
s_addc_u32 s49, %[ahi], 0
LL 18,76
LL 124,77
AW 126,248
AW 127,250
M 8,33,3
s_add_u32 s52, s52, %[d]
LA 18
v_and_b32 v22, 0xfffffcff, v1
LK 48,252,63,0
CF 40,40,41
LK 248,254,63,2
s_cmp_eq_u32 s52, %[wrap]
RA 252,7,1792
CF 42,42,43
GS 61
s_cselect_b32 s52, %[rst], s52
CF 44,44,45
CF 46,46,47
M 9,44,2
LC_ 124
DQ 3072
M 9,45,3
AW 128,40
AW 129,42
M 10,56,2
WL 4
LO 121,17,121
LU 17,121
M 10,57,3
s_add_u32 s50, s50, %[d]
RA 220,1,1024
HS 32,17
M 11,80,2
LL 19,78
LL 125,79
M 11,81,3
s_cmp_eq_u32 s50, %[wrap]
LA 19
RV 252,22,5120
AW 130,44
AW 131,46
LK 40,240,64,0
CF 48,48,49
LK 44,242,64,2
s_cselect_b32 s50, %[rst], s50
RA 240,7,2048
CF 50,50,51
GS 62
CF 248,248,249
CF 250,250,251
M 6,10,4
LC_ 125
v_pk_add_f16 v32, v29, v32
M 6,11,5
v_pk_add_f16 v32, v31, v32
AW 132,48
M 7,22,4
WL 5
LO 124,18,124
LU 18,124
M 7,23,5
s_add_u32 s38, s38, 32768
RA 224,1,2048
HZ 24,18
M 8,34,4
s_cmp_eq_u32 s38, s77
LL 16,80
LL 106,81
M 8,35,5
s_cselect_b32 s38, s76, s38
LA 16
v_pk_add_f16 v26, v32, v26
AW 133,50
AW 134,248
M 9,46,4
s_barrier
LC_ 106
HX 26
AW 135,250
M 9,47,5
s_sub_u32 s39, 0, s39
LK 48,244,65,0
CF 40,40,41
LK 248,246,65,2
RA 244,7,2304
CF 42,42,43
GS 63
CF 44,44,45
CF 46,46,47
M 10,58,4
WL 4
LO 125,19,125
LU 19,125
M 10,59,5
RA 228,1,3072
HS 24,19
M 11,82,4
LL 17,82
LL 107,83
M 11,83,5
LA 17
HX 24
AW 136,40
AW 137,42
Z 0,0,0
LC_ 107
ds_read_b64 v[14:15], v5 offset:0
AW 138,44
AW 139,46
M 0,1,1
W1 24,0
LK 40,248,66,0
CF 48,48,49
LK 44,250,66,2
RA 248,7,2560
CF 50,50,51
GS 64
CF 248,248,249
CF 250,250,251
Z 1,12,0
WL 7
LO 106,16,106
LU 16,106
M 1,13,1
RA 232,1,4096
HZ 27,16
Z 2,24,0
LL 18,84
LL 110,85
M 2,25,1
LA 18
W1 26,2048
AW 140,48
Z 3,36,0
LC_ 110
AW 141,50
AW 142,248
M 3,37,1
s_add_u32 s42, s94, s60
AW 143,250
LK 48,252,67,0
CF 40,40,41
LK 248,254,67,2
s_addc_u32 s43, s95, 0
RA 252,7,2816
CF 42,42,43
GS 65
CF 44,44,45
CF 46,46,47
Z 4,48,0
WL 8
LO 107,17,107
LU 17,107
M 4,49,1
RA 236,1,6144
HS 27,17
Z 5,72,0
LL 19,86
LL 111,87
M 5,73,1
LA 19
SU 104
M 0,2,2
WL 9
LC_ 111
v_add_f32 v14, v14, v15
AW 144,40
AW 145,42
M 0,3,3
v_add_f32_dpp v14, v14, v14 quad_perm:[1,0,3,2] row_mask:0xf bank_mask:0xf
AW 146,44
M 1,14,2
WL 4
LO 110,18,110
LU 18,110
AW 147,46
M 1,15,3
RA 216,1,7168
HZ 29,18
LK 40,240,68,0
CF 48,48,49
LK 44,242,68,2
CF 50,50,51
v_add_f32_dpp v14, v14, v14 quad_perm:[2,3,0,1] row_mask:0xf bank_mask:0xf
GS 66
CF 248,248,249
CF 250,250,251
M 2,26,2
LL 16,88
LL 114,89
M 2,27,3
LA 16
v_add_f32_dpp v14, v14, v14 row_half_mirror row_mask:0xf bank_mask:0xf
M 3,38,2
LC_ 114
v_cvt_f64_f32 v[14:15], v14
AW 148,48
M 3,39,3
s_add_u32 s42, s94, s61
s_mov_b64 exec, s[54:55]
SP
s_mov_b64 exec, -1
AW 149,50
AW 150,248
M 4,50,2
WL 3
LO 111,19,111
LU 19,111
AW 151,250
M 4,51,3
s_addc_u32 s43, s95, 0
RA 220,1,8192
HS 29,19
LK 48,244,69,0
CF 40,40,41
LK 248,246,69,2
CF 42,42,43
GS 67
CF 44,44,45
CF 46,46,47
M 5,74,2
LL 17,90
LL 115,91
M 5,75,3
LA 17
ds_read_b64 v[14:15], v5 offset:2048
M 0,4,4
LC_ 115
SU 108
AW 152,40
M 0,5,5
v_subrev_u32 v5, s39, v5
AW 153,42
AW 154,44
M 1,16,4
WL 4
LO 114,16,114
LU 16,114
AW 155,46
M 1,17,5
RA 224,1,9216
HZ 30,16
LK 40,248,70,0
CF 48,48,49
LK 44,250,70,2
CF 50,50,51
GS 68
CF 248,248,249
CF 250,250,251
M 2,28,4
LL 18,92
LL 118,93
M 2,29,5
LA 18
AW 156,48
M 3,40,4
LC_ 118
)"
R"(AW 157,50
M 3,41,5
s_add_u32 s42, s94, s62
AW 158,248
AW 159,250
LK 48,252,71,0
CF 40,40,41
LK 248,254,71,2
s_addc_u32 s43, s95, 0
CF 42,42,43
GS 69
CF 44,44,45
CF 46,46,47
M 4,52,4
WL 3
LO 115,17,115
LU 17,115
M 4,53,5
RA 228,1,10240
HS 30,17
M 5,76,4
LL 19,94
LL 119,95
M 5,77,5
LA 19
SU 112
M 0,6,0
LC_ 119
v_add_f32 v14, v14, v15
AW 160,40
M 0,7,1
v_add_f32_dpp v14, v14, v14 quad_perm:[1,0,3,2] row_mask:0xf bank_mask:0xf
AW 161,42
AW 162,44
M 1,18,0
WL 3
LO 118,18,118
LU 18,118
AW 163,46
RV 36,8,6912
M 1,19,1
RA 232,1,11264
HZ 31,18
GS 70
CF 48,48,49
CF 50,50,51
M 2,30,0
LL 16,96
LL 122,97
M 2,31,1
LA 16
RV 252,22,5392
CF 248,248,249
CF 250,250,251
M 3,42,0
LC_ 122
v_add_f32_dpp v14, v14, v14 quad_perm:[2,3,0,1] row_mask:0xf bank_mask:0xf
AW 164,48
AW 165,50
M 3,43,1
s_add_u32 s42, s94, s63
v_add_f32_dpp v14, v14, v14 row_half_mirror row_mask:0xf bank_mask:0xf
AW 166,248
AW 167,250
M 4,54,0
WL 5
LO 119,19,119
LU 19,119
M 4,55,1
s_addc_u32 s43, s95, 0
RA 236,1,12288
HS 31,19
GS 71
SU 116
v_cvt_f64_f32 v[14:15], v14
s_nop 5
v_cndmask_b32 v52, v52, v53, s[78:79]
M 5,78,0
LL 17,98
LL 123,99
v_cndmask_b32 v54, v54, v55, s[78:79]
M 5,79,1
LA 17
s_mov_b64 exec, s[54:55]
SP
s_mov_b64 exec, -1
v_cndmask_b32 v52, v52, v54, s[80:81]
M 0,8,2
LC_ 123
v_pk_add_f16 v31, v30, v31
v_mul_f32 v52, 0x3b2aaaab, v52
M 0,9,3
C 52,52,52
v_lshlrev_b32 v52, 16, v52
M 1,20,2
WL 3
LO 122,16,122
LU 16,122
M 1,21,3
RA 216,1,13312
HZ 32,16
v_add_f32 v52, v36, v52
M 2,32,2
LL 18,100
LL 126,101
C 52,52,52
M 2,33,3
LA 18
v_lshlrev_b32 v52, 16, v52
M 3,44,2
LC_ 126
v_sqrt_f32 v52, v52
M 3,45,3
s_add_u32 s42, s94, s64
C 52,52,52
v_lshlrev_b32 v52, 16, v52
M 4,56,2
WL 3
LO 123,17,123
LU 17,123
M 4,57,3
s_addc_u32 s43, s95, 0
RA 220,1,14336
HS 32,17
M 5,80,2
LL 19,102
LL 127,103
v_cmp_ge_f32 vcc, v37, v52
M 5,81,3
LA 19
SU 120
v_rcp_f32 v52, v52
M 0,10,4
LC_ 127
v_pk_add_f16 v32, v29, v32
v_mul_f32 v52, v38, v52
M 0,11,5
v_pk_add_f16 v32, v31, v32
v_cndmask_b32 v52, 0, v52, vcc
M 1,22,4
WL 3
LO 126,18,126
LU 18,126
CF 52,52,52
M 1,23,5
RA 224,1,15360
HZ 25,18
ds_bpermute_b32 v13, v28, v52
M 2,34,4
LL 16,56
LL 104,57
M 2,35,5
LA 16
v_pk_add_f16 v27, v32, v27
M 3,46,4
LC_ 104
HX 27
M 3,47,5
s_add_u32 s42, s44, s65
AR 248,120
AR 249,121
M 4,58,4
WL 4
LO 127,19,127
LU 19,127
M 4,59,5
s_addc_u32 s43, s45, 0
RA 228,1,16384
HS 25,19
M 5,82,4
LL 17,58
LL 105,59
M 5,83,5
LA 17
ST 124
Z 6,0,0
LC_ 105
HX 25
M 6,1,1
W1 25,1024
AR 250,122
Z 7,12,0
WL 4
LO 104,16,104
LU 16,104
M 7,13,1
RA 232,1,17408
HZ 26,16
Z 8,24,0
LL 18,60
LL 108,61
AR 251,123
M 8,25,1
s_mul_i32 s53, s50, 25600
LA 18
W1 27,3072
AR 52,124
Z 9,36,0
s_add_u32 s40, %[wlo], s53
LC_ 108
v_add_u32 v4, s39, v4
AR 53,125
M 9,37,1
s_addc_u32 s41, %[whi], 0
AR 54,126
AR 55,127
Z 10,48,0
WL 5
LO 105,17,105
LU 17,105
M 10,49,1
s_mov_b32 m0, s38
RA 236,1,18432
HS 26,17
XS 248
Z 11,72,0
LL 19,62
LL 109,63
XS 249
M 11,73,1
LA 19
DW 0
XS 250
M 6,2,2
LC_ 109
XS 251
M 6,3,3
AW 120,248
AW 121,249
M 7,14,2
WL 3
LO 108,18,108
LU 18,108
M 7,15,3
s_add_u32 s37, s37, 32768
RA 216,1,19456
HZ 29,18
AW 122,250
M 8,26,2
s_cmp_eq_u32 s37, s75
LL 16,64
LL 112,65
AW 123,251
M 8,27,3
s_cselect_b32 s37, s74, s37
LA 16
DW 1024
XS 52
M 9,38,2
LC_ 112
XS 53
M 9,39,3
XS 54
XS 55
M 10,50,2
WL 3
LO 109,19,109
LU 19,109
M 10,51,3
RA 220,1,20480
HS 29,19
AW 124,52
M 11,74,2
LL 17,66
LL 113,67
AW 125,53
M 11,75,3
LA 17
DW 2048
AW 126,54
M 6,4,4
LC_ 113
AW 127,55
M 6,5,5
v_min_u32 v35, v35, v13
AR 248,128
M 7,16,4
WL 3
LO 112,16,112
LU 16,112
M 7,17,5
s_mul_i32 s53, s51, 49152
RA 224,1,21504
HZ 30,16
AR 249,129
M 8,28,4
s_bitcmp1_b64 s[90:91], s51
LL 18,68
LL 116,69
AR 250,130
M 8,29,5
s_cselect_b64 s[44:45], s[84:85], s[82:83]
LA 18
AR 251,131
XS 248
M 9,40,4
s_add_u32 s44, s44, s53
LC_ 116
XS 249
M 9,41,5
s_addc_u32 s45, s45, 0
XS 250
XS 251
M 10,52,4
WL 3
LO 113,17,113
LU 17,113
M 10,53,5
s_bitcmp1_b64 s[92:93], s51
RA 228,1,22528
HS 30,17
AW 128,248
M 11,76,4
LL 19,70
LL 117,71
AW 129,249
M 11,77,5
s_cselect_b64 s[94:95], s[88:89], s[86:87]
LA 19
AW 130,250
M 6,6,0
s_add_u32 s94, s94, s53
LC_ 117
AW 131,251
M 6,7,1
s_addc_u32 s95, s95, 0
AR 52,132
AR 53,133
M 7,18,0
WL 3
LO 116,18,116
LU 18,116
M 7,19,1
s_add_u32 s51, s51, %[d]
RA 232,1,23552
HZ 31,18
AR 54,134
M 8,30,0
s_cmp_eq_u32 s51, %[wrap]
LL 16,72
LL 120,73
AR 55,135
M 8,31,1
s_cselect_b32 s51, %[rst], s51
LA 16
XS 52
M 9,42,0
s_add_u32 s40, s40, 3072
LC_ 120
s_addc_u32 s41, s41, 0
s_add_u32 m0, s38, 3072
XS 53
M 9,43,1
DW 0
XS 54
M 10,54,0
WL 3
LO 117,19,117
LU 19,117
XS 55
M 10,55,1
RA 236,1,24576
HS 31,19
AW 132,52
M 11,78,0
LL 17,74
LL 121,75
AW 133,53
M 11,79,1
LA 17
DW 1024
AW 134,54
M 6,8,2
LC_ 121
DW 2048
AW 135,55
M 6,9,3
s_lshl_b32 s53, s52, 8
v_add_u32 v1, s37, v0
v_pk_add_f16 v31, v30, v31
AR 248,136
M 7,20,2
WL 3
LO 120,16,120
LU 16,120
AR 249,137
M 7,21,3
s_add_u32 s48, %[alo], s53
RA 216,1,0
HZ 32,16
AR 250,138
M 8,32,2
s_addc_u32 s49, %[ahi], 0
LL 18,76
LL 124,77
AR 251,139
M 8,33,3
s_add_u32 s52, s52, %[d]
LA 18
v_and_b32 v22, 0xfffffcff, v1
)"
R"(XS 248
M 9,44,2
s_cmp_eq_u32 s52, %[wrap]
LC_ 124
XS 249
M 9,45,3
s_cselect_b32 s52, %[rst], s52
DQ 3072
XS 250
M 10,56,2
WL 3
LO 121,17,121
LU 17,121
XS 251
M 10,57,3
s_add_u32 s50, s50, %[d]
RA 220,1,1024
HS 32,17
AW 136,248
AW 137,249
M 11,80,2
LL 19,78
LL 125,79
AW 138,250
M 11,81,3
s_cmp_eq_u32 s50, %[wrap]
LA 19
RV 252,22,5120
AW 139,251
M 6,10,4
s_cselect_b32 s50, %[rst], s50
LC_ 125
v_pk_add_f16 v32, v29, v32
AR 52,140
M 6,11,5
v_pk_add_f16 v32, v31, v32
AR 53,141
M 7,22,4
WL 4
LO 124,18,124
LU 18,124
AR 54,142
M 7,23,5
s_add_u32 s38, s38, 32768
RA 224,1,2048
HZ 24,18
AR 55,143
M 8,34,4
s_cmp_eq_u32 s38, s77
LL 16,80
LL 106,81
XS 52
M 8,35,5
s_cselect_b32 s38, s76, s38
LA 16
v_pk_add_f16 v26, v32, v26
XS 53
M 9,46,4
s_barrier
LC_ 106
HX 26
XS 54
M 9,47,5
s_sub_u32 s39, 0, s39
XS 55
AW 140,52
M 10,58,4
WL 3
LO 125,19,125
LU 19,125
M 10,59,5
RA 228,1,3072
HS 24,19
AW 141,53
M 11,82,4
LL 17,82
LL 107,83
AW 142,54
M 11,83,5
LA 17
HX 24
AW 143,55
Z 0,0,0
LC_ 107
ds_read_b64 v[14:15], v5 offset:0
AR 248,144
M 0,1,1
W1 24,0
AR 249,145
Z 1,12,0
WL 5
LO 106,16,106
LU 16,106
AR 250,146
M 1,13,1
RA 232,1,4096
HZ 27,16
AR 251,147
Z 2,24,0
LL 18,84
LL 110,85
XS 248
M 2,25,1
LA 18
W1 26,2048
XS 249
Z 3,36,0
LC_ 110
XS 250
M 3,37,1
s_add_u32 s42, s94, s60
XS 251
AW 144,248
Z 4,48,0
WL 6
LO 107,17,107
LU 17,107
M 4,49,1
s_addc_u32 s43, s95, 0
RA 236,1,6144
HS 27,17
AW 145,249
Z 5,60,0
LL 19,86
LL 111,87
AW 146,250
M 5,61,1
LA 19
SU 104
AW 147,251
M 0,2,2
WL 7
LC_ 111
v_add_f32 v14, v14, v15
AR 52,148
M 0,3,3
v_add_f32_dpp v14, v14, v14 quad_perm:[1,0,3,2] row_mask:0xf bank_mask:0xf
AR 53,149
AR 54,150
M 1,14,2
WL 3
LO 110,18,110
LU 18,110
AR 55,151
M 1,15,3
RA 216,1,7168
HZ 29,18
XS 52
M 2,26,2
LL 16,88
LL 114,89
XS 53
M 2,27,3
LA 16
v_add_f32_dpp v14, v14, v14 quad_perm:[2,3,0,1] row_mask:0xf bank_mask:0xf
XS 54
M 3,38,2
LC_ 114
v_add_f32_dpp v14, v14, v14 row_half_mirror row_mask:0xf bank_mask:0xf
XS 55
M 3,39,3
s_add_u32 s42, s94, s61
v_cvt_f64_f32 v[14:15], v14
AW 148,52
M 4,50,2
WL 3
LO 111,19,111
LU 19,111
AW 149,53
M 4,51,3
s_addc_u32 s43, s95, 0
RA 220,1,8192
HS 29,19
AW 150,54
M 5,62,2
LL 17,90
LL 115,91
AW 151,55
M 5,63,3
LA 17
s_mov_b64 exec, s[54:55]
SP
s_mov_b64 exec, -1
AR 248,152
M 0,4,4
LC_ 115
ds_read_b64 v[14:15], v5 offset:2048
AR 249,153
M 0,5,5
SU 108
v_subrev_u32 v5, s39, v5
AR 250,154
M 1,16,4
WL 4
LO 114,16,114
LU 16,114
AR 251,155
M 1,17,5
RA 224,1,9216
HZ 30,16
XS 248
M 2,28,4
LL 18,92
LL 118,93
XS 249
M 2,29,5
LA 18
XS 250
M 3,40,4
LC_ 118
XS 251
M 3,41,5
s_add_u32 s42, s94, s62
AW 152,248
AW 153,249
M 4,52,4
WL 4
LO 115,17,115
LU 17,115
M 4,53,5
s_addc_u32 s43, s95, 0
RA 228,1,10240
HS 30,17
AW 154,250
M 5,64,4
LL 19,94
LL 119,95
AW 155,251
M 5,65,5
LA 19
SU 112
AR 52,156
M 0,6,0
WL 5
LC_ 119
v_add_f32 v14, v14, v15
AR 53,157
M 0,7,1
v_add_f32_dpp v14, v14, v14 quad_perm:[1,0,3,2] row_mask:0xf bank_mask:0xf
AR 54,158
M 1,18,0
WL 3
LO 118,18,118
LU 18,118
AR 55,159
M 1,19,1
RA 232,1,11264
HZ 31,18
XS 52
M 2,30,0
LL 16,96
LL 122,97
XS 53
M 2,31,1
LA 16
RV 252,22,5392
XS 54
XS 55
M 3,42,0
LC_ 122
v_add_f32_dpp v14, v14, v14 quad_perm:[2,3,0,1] row_mask:0xf bank_mask:0xf
AW 156,52
M 3,43,1
s_add_u32 s42, s94, s63
v_add_f32_dpp v14, v14, v14 row_half_mirror row_mask:0xf bank_mask:0xf
AW 157,53
M 4,54,0
WL 4
LO 119,19,119
LU 19,119
AW 158,54
M 4,55,1
s_addc_u32 s43, s95, 0
RA 236,1,12288
HS 31,19
AW 159,55
M 5,66,0
LL 17,98
LL 123,99
AR 248,160
M 5,67,1
LA 17
SU 116
v_cvt_f64_f32 v[14:15], v14
AR 249,161
M 0,8,2
LC_ 123
s_mov_b64 exec, s[54:55]
SP
s_mov_b64 exec, -1
AR 250,162
M 0,9,3
v_pk_add_f16 v31, v30, v31
AR 251,163
M 1,20,2
WL 3
LO 122,16,122
LU 16,122
XS 248
M 1,21,3
RA 216,1,13312
HZ 32,16
XS 249
M 2,32,2
LL 18,100
LL 126,101
XS 250
M 2,33,3
LA 18
XS 251
M 3,44,2
LC_ 126
AW 160,248
M 3,45,3
s_add_u32 s42, s94, s64
AW 161,249
AW 162,250
M 4,56,2
WL 3
LO 123,17,123
LU 17,123
M 4,57,3
s_addc_u32 s43, s95, 0
RA 220,1,14336
HS 32,17
AW 163,251
M 5,68,2
LL 19,102
LL 127,103
AR 52,164
M 5,69,3
LA 19
SU 120
AR 53,165
M 0,10,4
LC_ 127
v_pk_add_f16 v32, v29, v32
AR 54,166
M 0,11,5
v_pk_add_f16 v32, v31, v32
AR 55,167
M 1,22,4
WL 3
LO 126,18,126
LU 18,126
XS 52
M 1,23,5
RA 224,1,15360
HZ 25,18
XS 53
M 2,34,4
LL 16,56
LL 104,57
XS 54
M 2,35,5
LA 16
v_pk_add_f16 v27, v32, v27
XS 55
M 3,46,4
LC_ 104
HX 27
AW 164,52
M 3,47,5
s_add_u32 s42, s44, s65
AW 165,53
AW 166,54
M 4,58,4
WL 3
LO 127,19,127
LU 19,127
AW 167,55
M 4,59,5
s_addc_u32 s43, s45, 0
RA 228,1,16384
HS 25,19
M 5,70,4
LL 17,58
LL 105,59
M 5,71,5
LA 17
ST 124
Z 6,0,0
LC_ 105
HX 25
M 6,1,1
W1 25,1024
Z 7,12,0
WL 4
LO 104,16,104
LU 16,104
M 7,13,1
RA 232,1,17408
HZ 26,16
Z 8,24,0
LL 18,60
LL 108,61
M 8,25,1
s_mul_i32 s53, s50, 25600
LA 18
W1 27,3072
)"
R"(Z 9,36,0
s_add_u32 s40, %[wlo], s53
LC_ 108
v_add_u32 v4, s39, v4
M 9,37,1
s_addc_u32 s41, %[whi], 0
Z 10,48,0
WL 5
LO 105,17,105
LU 17,105
M 10,49,1
s_mov_b32 m0, s38
RA 236,1,18432
HS 26,17
Z 11,60,0
LL 19,62
LL 109,63
M 11,61,1
LA 19
DW 0
M 6,2,2
LC_ 109
M 6,3,3
M 7,14,2
WL 3
LO 108,18,108
LU 18,108
M 7,15,3
s_add_u32 s37, s37, 32768
RA 216,1,19456
HZ 29,18
M 8,26,2
s_cmp_eq_u32 s37, s75
LL 16,64
LL 112,65
M 8,27,3
s_cselect_b32 s37, s74, s37
LA 16
DW 1024
M 9,38,2
LC_ 112
M 9,39,3
M 10,50,2
WL 3
LO 109,19,109
LU 19,109
M 10,51,3
RA 220,1,20480
HS 29,19
M 11,62,2
LL 17,66
LL 113,67
M 11,63,3
LA 17
DW 2048
M 6,4,4
LC_ 113
M 6,5,5
M 7,16,4
WL 3
LO 112,16,112
LU 16,112
M 7,17,5
s_mul_i32 s53, s51, 49152
RA 224,1,21504
HZ 30,16
M 8,28,4
s_bitcmp1_b64 s[92:93], s51
LL 18,68
LL 116,69
M 8,29,5
s_cselect_b64 s[94:95], s[88:89], s[86:87]
LA 18
M 9,40,4
s_add_u32 s94, s94, s53
LC_ 116
M 9,41,5
s_addc_u32 s95, s95, 0
M 10,52,4
WL 3
LO 113,17,113
LU 17,113
M 10,53,5
s_add_u32 s51, s51, %[d]
RA 228,1,22528
HS 30,17
M 11,64,4
LL 19,70
LL 117,71
M 11,65,5
s_cmp_eq_u32 s51, %[wrap]
LA 19
M 6,6,0
s_cselect_b32 s51, %[rst], s51
LC_ 117
M 6,7,1
s_mul_i32 s53, %[d], 33792
M 7,18,0
WL 3
LO 116,18,116
LU 18,116
M 7,19,1
s_add_u32 s65, s53, %[sb1]
RA 232,1,23552
HZ 31,18
M 8,30,0
s_add_u32 s40, s40, 3072
LL 16,72
LL 120,73
M 8,31,1
s_addc_u32 s41, s41, 0
LA 16
M 9,42,0
s_add_u32 m0, s38, 3072
LC_ 120
DW 0
M 9,43,1
DW 1024
M 10,54,0
WL 3
LO 117,19,117
LU 19,117
M 10,55,1
RA 236,1,24576
HS 31,19
M 11,66,0
LL 17,74
LL 121,75
M 11,67,1
LA 17
DW 2048
M 6,8,2
LC_ 121
v_pk_add_f16 v31, v30, v31
M 6,9,3
s_lshl_b32 s53, s52, 8
v_add_u32 v1, s37, v0
M 7,20,2
WL 3
LO 120,16,120
LU 16,120
M 7,21,3
s_add_u32 s48, %[alo], s53
RA 216,1,0
HZ 32,16
M 8,32,2
s_addc_u32 s49, %[ahi], 0
LL 18,76
LL 124,77
M 8,33,3
s_add_u32 s52, s52, %[d]
LA 18
v_and_b32 v22, 0xfffffcff, v1
M 9,44,2
s_cmp_eq_u32 s52, %[wrap]
LC_ 124
M 9,45,3
s_cselect_b32 s52, %[rst], s52
DQ 3072
M 10,56,2
WL 3
LO 121,17,121
LU 17,121
M 10,57,3
s_add_u32 s50, s50, %[d]
RA 220,1,1024
HS 32,17
M 11,68,2
LL 19,78
LL 125,79
M 11,69,3
s_cmp_eq_u32 s50, %[wrap]
LA 19
RV 252,22,5120
M 6,10,4
s_cselect_b32 s50, %[rst], s50
LC_ 125
v_pk_add_f16 v32, v29, v32
M 6,11,5
WV 30
v_pk_add_f16 v32, v31, v32
M 7,22,4
WL 4
LO 124,18,124
LU 18,124
M 7,23,5
s_add_u32 s38, s38, 32768
RA 224,1,2048
HZ 33,18
M 8,34,4
s_cmp_eq_u32 s38, s77
LL 16,80
LL 106,81
M 8,35,5
s_cselect_b32 s38, s76, s38
LA 16
M 9,46,4
s_barrier
LC_ 106
M 9,47,5
s_sub_u32 s39, 0, s39
M 10,58,4
WL 3
LO 125,19,125
LU 19,125
M 10,59,5
RA 228,1,3072
HS 33,19
M 11,70,4
LL 17,82
LL 107,83
M 11,71,5
LA 17
v_pk_add_f16 v33, v26, v33
Z 0,0,0
LC_ 107
ds_read_b64 v[14:15], v5 offset:0
M 0,1,1
v_pk_add_f16 v26, v32, v33
Z 1,12,0
WL 4
LO 106,16,106
LU 16,106
M 1,13,1
RA 232,1,4096
HZ 27,16
Z 2,24,0
LL 18,84
LL 110,85
M 2,25,1
LA 18
HX 26
Z 3,36,0
LC_ 110
W1 26,2048
M 3,37,1
s_add_u32 s42, s94, s60
Z 4,48,0
WL 5
LO 107,17,107
LU 17,107
M 4,49,1
s_addc_u32 s43, s95, 0
RA 236,1,6144
HS 27,17
Z 5,60,0
LL 19,86
LL 111,87
M 5,61,1
LA 19
SU 104
M 0,2,2
WL 6
LC_ 111
v_add_f32 v14, v14, v15
M 0,3,3
s_nop 0
v_add_f32_dpp v14, v14, v14 quad_perm:[1,0,3,2] row_mask:0xf bank_mask:0xf
M 1,14,2
WL 4
LO 110,18,110
LU 18,110
M 1,15,3
RA 216,1,7168
HZ 29,18
M 2,26,2
LL 16,88
LL 114,89
M 2,27,3
LA 16
v_add_f32_dpp v14, v14, v14 quad_perm:[2,3,0,1] row_mask:0xf bank_mask:0xf
M 3,38,2
LC_ 114
v_add_f32_dpp v14, v14, v14 row_half_mirror row_mask:0xf bank_mask:0xf
M 3,39,3
s_add_u32 s42, s94, s61
v_cvt_f64_f32 v[14:15], v14
M 4,50,2
WL 3
LO 111,19,111
LU 19,111
M 4,51,3
s_addc_u32 s43, s95, 0
RA 220,1,8192
HS 29,19
M 5,62,2
LL 17,90
LL 115,91
M 5,63,3
LA 17
s_mov_b64 exec, s[54:55]
SP
s_mov_b64 exec, -1
M 0,4,4
LC_ 115
ds_read_b64 v[14:15], v5 offset:2048
M 0,5,5
SU 108
v_subrev_u32 v5, s39, v5
M 1,16,4
WL 4
LO 114,16,114
LU 16,114
M 1,17,5
PFN
RA 224,1,9216
HZ 30,16
M 2,28,4
s_mov_b32 m0, %[pm]
LL 18,92
LL 118,93
M 2,29,5
LA 18
DF
M 3,40,4
LC_ 118
M 3,41,5
s_add_u32 s42, s94, s62
M 4,52,4
WL 4
LO 115,17,115
LU 17,115
M 4,53,5
s_addc_u32 s43, s95, 0
RA 228,1,10240
HS 30,17
M 5,64,4
LL 19,94
LL 119,95
M 5,65,5
LA 19
SU 112
M 0,6,0
WL 5
LC_ 119
v_add_f32 v14, v14, v15
M 0,7,1
s_nop 0
v_add_f32_dpp v14, v14, v14 quad_perm:[1,0,3,2] row_mask:0xf bank_mask:0xf
M 1,18,0
WL 3
LO 118,18,118
LU 18,118
M 1,19,1
RA 232,1,11264
HZ 31,18
M 2,30,0
LL 16,96
LL 122,97
M 2,31,1
LA 16
RV 252,22,5392
M 3,42,0
LC_ 122
v_add_f32_dpp v14, v14, v14 quad_perm:[2,3,0,1] row_mask:0xf bank_mask:0xf
M 3,43,1
s_add_u32 s42, s94, s63
v_add_f32_dpp v14, v14, v14 row_half_mirror row_mask:0xf bank_mask:0xf
M 4,54,0
WL 4
LO 119,19,119
LU 19,119
M 4,55,1
s_addc_u32 s43, s95, 0
RA 236,1,12288
HS 31,19
M 5,66,0
LL 17,98
LL 123,99
M 5,67,1
LA 17
SU 116
v_cvt_f64_f32 v[14:15], v14
M 0,8,2
LC_ 123
s_mov_b64 exec, s[54:55]
SP
s_mov_b64 exec, -1
M 0,9,3
v_pk_add_f16 v31, v30, v31
M 1,20,2
WL 3
LO 122,16,122
LU 16,122
M 1,21,3
RA 216,1,13312
HZ 32,16
M 2,32,2
LL 18,100
LL 126,101
M 2,33,3
LA 18
M 3,44,2
LC_ 126
M 3,45,3
s_add_u32 s42, s94, s64
M 4,56,2
WL 3
LO 123,17,123
LU 17,123
)"
R"(M 4,57,3
s_addc_u32 s43, s95, 0
RA 220,1,14336
HS 32,17
M 5,68,2
LL 19,102
LL 127,103
M 5,69,3
LA 19
SU 120
M 0,10,4
LC_ 127
v_pk_add_f16 v32, v29, v32
M 0,11,5
v_pk_add_f16 v32, v31, v32
M 1,22,4
WL 3
LO 126,18,126
LU 18,126
M 1,23,5
RA 224,1,15360
HZ 33,18
M 2,34,4
LL 16,56
LL 104,57
M 2,35,5
LA 16
M 3,46,4
LC_ 104
M 3,47,5
s_add_u32 s42, s94, s65
M 4,58,4
WL 3
LO 127,19,127
LU 19,127
M 4,59,5
s_addc_u32 s43, s95, 0
RA 228,1,16384
HS 33,19
M 5,70,4
LL 17,58
LL 105,59
M 5,71,5
LA 17
SU 124
Z 6,0,0
LC_ 105
v_pk_add_f16 v33, v27, v33
M 6,1,1
v_pk_add_f16 v27, v32, v33
Z 7,12,0
WL 3
LO 104,16,104
LU 16,104
M 7,13,1
RA 232,1,17408
HZ 26,16
Z 8,24,0
LL 18,60
LL 108,61
M 8,25,1
s_mul_i32 s53, s50, 25600
LA 18
HX 27
Z 9,36,0
s_add_u32 s40, %[wlo], s53
LC_ 108
W1 27,3072
M 9,37,1
s_addc_u32 s41, %[whi], 0
v_add_u32 v4, s39, v4
Z 10,48,0
WL 4
LO 105,17,105
LU 17,105
M 10,49,1
s_mov_b32 m0, s38
RA 236,1,18432
HS 26,17
Z 11,60,0
LL 19,62
LL 109,63
M 11,61,1
LA 19
s_mov_b64 exec, 1
AT
s_mov_b64 exec, -1
M 6,2,2
LC_ 109
DW 0
M 6,3,3
M 7,14,2
WL 4
LO 108,18,108
LU 18,108
M 7,15,3
s_add_u32 s37, s37, 32768
RA 216,1,19456
HZ 29,18
M 8,26,2
s_cmp_eq_u32 s37, s75
LL 16,64
LL 112,65
M 8,27,3
s_cselect_b32 s37, s74, s37
LA 16
DW 1024
M 9,38,2
LC_ 112
M 9,39,3
M 10,50,2
WL 3
LO 109,19,109
LU 19,109
M 10,51,3
RA 220,1,20480
HS 29,19
M 11,62,2
LL 17,66
LL 113,67
M 11,63,3
LA 17
DW 2048
M 6,4,4
LC_ 113
M 6,5,5
M 7,16,4
WL 3
LO 112,16,112
LU 16,112
M 7,17,5
s_mul_i32 s53, s51, 49152
RA 224,1,21504
HZ 30,16
M 8,28,4
s_bitcmp1_b64 s[92:93], s51
LL 18,68
LL 116,69
M 8,29,5
s_cselect_b64 s[94:95], s[88:89], s[86:87]
LA 18
M 9,40,4
s_add_u32 s94, s94, s53
LC_ 116
M 9,41,5
s_addc_u32 s95, s95, 0
M 10,52,4
WL 3
LO 113,17,113
LU 17,113
M 10,53,5
s_add_u32 s51, s51, %[d]
RA 228,1,22528
HS 30,17
M 11,64,4
LL 19,70
LL 117,71
M 11,65,5
s_cmp_eq_u32 s51, %[wrap]
LA 19
M 6,6,0
s_cselect_b32 s51, %[rst], s51
LC_ 117
M 6,7,1
s_add_u32 s40, s40, 3072
M 7,18,0
WL 3
LO 116,18,116
LU 18,116
M 7,19,1
s_addc_u32 s41, s41, 0
RA 232,1,23552
HZ 31,18
M 8,30,0
s_add_u32 m0, s38, 3072
LL 16,72
LL 120,73
M 8,31,1
LA 16
DW 0
M 9,42,0
LC_ 120
DW 1024
M 9,43,1
M 10,54,0
WL 3
LO 117,19,117
LU 19,117
M 10,55,1
RA 236,1,24576
HS 31,19
M 11,66,0
LL 17,74
LL 121,75
M 11,67,1
LA 17
DW 2048
M 6,8,2
LC_ 121
v_pk_add_f16 v31, v30, v31
M 6,9,3
s_lshl_b32 s53, s52, 8
v_add_u32 v1, s37, v0
M 7,20,2
WL 3
LO 120,16,120
LU 16,120
M 7,21,3
s_add_u32 s48, %[alo], s53
RA 216,1,0
HZ 32,16
M 8,32,2
s_addc_u32 s49, %[ahi], 0
LL 18,76
LL 124,77
M 8,33,3
s_add_u32 s52, s52, %[d]
LA 18
v_and_b32 v22, 0xfffffcff, v1
M 9,44,2
s_cmp_eq_u32 s52, %[wrap]
LC_ 124
M 9,45,3
s_cselect_b32 s52, %[rst], s52
DQ 3072
M 10,56,2
WL 3
LO 121,17,121
LU 17,121
M 10,57,3
s_add_u32 s50, s50, %[d]
RA 220,1,1024
HS 32,17
M 11,68,2
LL 19,78
LL 125,79
M 11,69,3
s_cmp_eq_u32 s50, %[wrap]
LA 19
RV 252,22,5120
M 6,10,4
s_cselect_b32 s50, %[rst], s50
LC_ 125
v_pk_add_f16 v32, v29, v32
M 6,11,5
WV 32
v_pk_add_f16 v32, v31, v32
M 7,22,4
WL 4
LO 124,18,124
LU 18,124
M 7,23,5
s_add_u32 s38, s38, 32768
RA 224,1,2048
HZ 33,18
M 8,34,4
s_cmp_eq_u32 s38, s77
LL 16,80
LL 106,81
M 8,35,5
s_cselect_b32 s38, s76, s38
LA 16
M 9,46,4
s_barrier
LC_ 106
M 9,47,5
s_sub_u32 s39, 0, s39
M 10,58,4
WL 3
LO 125,19,125
LU 19,125
M 10,59,5
RA 228,1,3072
HS 33,19
M 11,70,4
LL 17,82
LL 107,83
M 11,71,5
LA 17
v_pk_add_f16 v33, v26, v33
Z 0,0,0
LC_ 107
ds_read_b64 v[14:15], v5 offset:2048
M 0,1,1
s_nop 0
v_subrev_u32 v5, s39, v5
v_pk_add_f16 v26, v32, v33
Z 1,12,0
WL 4
LO 106,16,106
LU 16,106
M 1,13,1
RA 232,1,4096
HZ 27,16
Z 2,24,0
LL 18,84
LL 110,85
M 2,25,1
LA 18
HX 26
Z 3,36,0
LC_ 110
W1 26,2048
M 3,37,1
s_add_u32 s42, s94, s60
Z 4,48,0
WL 5
LO 107,17,107
LU 17,107
M 4,49,1
s_addc_u32 s43, s95, 0
RA 236,1,6144
HS 27,17
Z 5,60,0
LL 19,86
LL 111,87
M 5,61,1
LA 19
SU 104
M 0,2,2
WL 6
LC_ 111
v_add_f32 v14, v14, v15
M 0,3,3
s_nop 0
v_add_f32_dpp v14, v14, v14 quad_perm:[1,0,3,2] row_mask:0xf bank_mask:0xf
M 1,14,2
WL 4
LO 110,18,110
LU 18,110
M 1,15,3
RA 216,1,7168
HZ 29,18
M 2,26,2
LL 16,88
LL 114,89
M 2,27,3
LA 16
v_add_f32_dpp v14, v14, v14 quad_perm:[2,3,0,1] row_mask:0xf bank_mask:0xf
M 3,38,2
LC_ 114
v_add_f32_dpp v14, v14, v14 row_half_mirror row_mask:0xf bank_mask:0xf
M 3,39,3
s_add_u32 s42, s94, s61
v_cvt_f64_f32 v[14:15], v14
M 4,50,2
WL 3
LO 111,19,111
LU 19,111
M 4,51,3
s_addc_u32 s43, s95, 0
RA 220,1,8192
HS 29,19
M 5,62,2
LL 17,90
LL 115,91
M 5,63,3
LA 17
s_mov_b64 exec, s[54:55]
SP
s_mov_b64 exec, -1
M 0,4,4
LC_ 115
SU 108
M 0,5,5
PFW
M 1,16,4
WL 3
LO 114,16,114
LU 16,114
M 1,17,5
s_mov_b32 m0, %[pm]
RA 224,1,9216
HZ 30,16
M 2,28,4
LL 18,92
LL 118,93
M 2,29,5
LA 18
DF
M 3,40,4
LC_ 118
M 3,41,5
s_add_u32 s42, s94, s62
M 4,52,4
WL 3
LO 115,17,115
LU 17,115
M 4,53,5
s_addc_u32 s43, s95, 0
RA 228,1,10240
HS 30,17
M 5,64,4
LL 19,94
LL 119,95
M 5,65,5
LA 19
SU 112
M 0,6,0
LC_ 119
)"
R"(M 0,7,1
M 1,18,0
WL 3
LO 118,18,118
LU 18,118
M 1,19,1
RA 232,1,11264
HZ 31,18
M 2,30,0
LL 16,96
LL 122,97
M 2,31,1
LA 16
RV 252,22,5392
M 3,42,0
LC_ 122
M 3,43,1
s_add_u32 s42, s94, s63
M 4,54,0
WL 4
LO 119,19,119
LU 19,119
M 4,55,1
s_addc_u32 s43, s95, 0
RA 236,1,12288
HS 31,19
M 5,66,0
LL 17,98
LL 123,99
M 5,67,1
LA 17
SU 116
M 0,8,2
LC_ 123
v_pk_add_f16 v31, v30, v31
M 0,9,3
M 1,20,2
WL 3
LO 122,16,122
LU 16,122
M 1,21,3
RA 216,1,13312
HZ 32,16
M 2,32,2
LL 18,100
LL 126,101
M 2,33,3
LA 18
M 3,44,2
LC_ 126
M 3,45,3
s_add_u32 s42, s94, s64
M 4,56,2
WL 3
LO 123,17,123
LU 17,123
M 4,57,3
s_addc_u32 s43, s95, 0
RA 220,1,14336
HS 32,17
M 5,68,2
LL 19,102
LL 127,103
M 5,69,3
LA 19
SU 120
M 0,10,4
LC_ 127
v_pk_add_f16 v32, v29, v32
M 0,11,5
v_pk_add_f16 v32, v31, v32
M 1,22,4
WL 3
LO 126,18,126
LU 18,126
M 1,23,5
RA 224,1,15360
HZ 33,18
M 2,34,4
LL 16,56
LL 104,57
M 2,35,5
LA 16
M 3,46,4
LC_ 104
M 3,47,5
s_add_u32 s42, s94, s65
M 4,58,4
WL 3
LO 127,19,127
LU 19,127
M 4,59,5
s_addc_u32 s43, s95, 0
RA 228,1,16384
HS 33,19
M 5,70,4
LL 17,58
LL 105,59
M 5,71,5
LA 17
SU 124
Z 6,0,0
LC_ 105
v_pk_add_f16 v33, v27, v33
M 6,1,1
v_pk_add_f16 v27, v32, v33
Z 7,12,0
WL 3
LO 104,16,104
LU 16,104
M 7,13,1
RA 232,1,17408
HZ 26,16
Z 8,24,0
WV 15
LL 18,60
LL 108,61
M 8,25,1
s_mul_i32 s53, s50, 25600
LA 18
HX 27
Z 9,36,0
s_add_u32 s40, %[wlo], s53
LC_ 108
W1 27,3072
M 9,37,1
s_addc_u32 s41, %[whi], 0
v_readfirstlane_b32 s72, v11
Z 10,48,0
WL 4
LO 105,17,105
LU 17,105
M 10,49,1
s_mov_b32 m0, s38
RA 236,1,18432
HS 26,17
Z 11,60,0
LL 19,62
LL 109,63
M 11,61,1
LA 19
DW 0
M 6,2,2
LC_ 109
v_mov_b32 v12, s72
M 6,3,3
W2 12,12,16
M 7,14,2
WL 5
LO 108,18,108
LU 18,108
M 7,15,3
s_add_u32 s37, s37, 32768
RA 216,1,19456
HZ 29,18
M 8,26,2
s_cmp_eq_u32 s37, s75
LL 16,64
LL 112,65
M 8,27,3
s_cselect_b32 s37, s74, s37
LA 16
DW 1024
M 9,38,2
LC_ 112
v_add_u32 v4, s39, v4
M 9,39,3
s_mov_b64 exec, 1
AT
s_mov_b64 exec, -1
M 10,50,2
WL 4
LO 109,19,109
LU 19,109
M 10,51,3
RA 220,1,20480
HS 29,19
M 11,62,2
LL 17,66
LL 113,67
M 11,63,3
LA 17
DW 2048
M 6,4,4
LC_ 113
M 6,5,5
M 7,16,4
WL 3
LO 112,16,112
LU 16,112
M 7,17,5
s_mul_i32 s53, s51, 49152
RA 224,1,21504
HZ 30,16
M 8,28,4
s_bitcmp1_b64 s[92:93], s51
LL 18,68
LL 116,69
M 8,29,5
s_cselect_b64 s[94:95], s[88:89], s[86:87]
LA 18
M 9,40,4
s_add_u32 s94, s94, s53
LC_ 116
M 9,41,5
s_addc_u32 s95, s95, 0
M 10,52,4
WL 3
LO 113,17,113
LU 17,113
M 10,53,5
s_add_u32 s51, s51, %[d]
RA 228,1,22528
HS 30,17
M 11,64,4
LL 19,70
LL 117,71
M 11,65,5
s_cmp_eq_u32 s51, %[wrap]
LA 19
M 6,6,0
s_cselect_b32 s51, %[rst], s51
LC_ 117
M 6,7,1
s_add_u32 s40, s40, 3072
M 7,18,0
WL 3
LO 116,18,116
LU 18,116
M 7,19,1
s_addc_u32 s41, s41, 0
RA 232,1,23552
HZ 31,18
M 8,30,0
s_add_u32 m0, s38, 3072
LL 16,72
LL 120,73
M 8,31,1
LA 16
DW 0
M 9,42,0
LC_ 120
DW 1024
M 9,43,1
M 10,54,0
WL 3
LO 117,19,117
LU 19,117
M 10,55,1
RA 236,1,24576
HS 31,19
M 11,66,0
LL 17,74
LL 121,75
M 11,67,1
LA 17
DW 2048
M 6,8,2
LC_ 121
v_pk_add_f16 v31, v30, v31
M 6,9,3
s_lshl_b32 s53, s52, 8
v_add_u32 v1, s37, v0
M 7,20,2
WL 3
LO 120,16,120
LU 16,120
M 7,21,3
s_add_u32 s48, %[alo], s53
RA 216,1,0
HZ 32,16
M 8,32,2
s_addc_u32 s49, %[ahi], 0
LL 18,76
LL 124,77
M 8,33,3
s_add_u32 s52, s52, %[d]
LA 18
v_and_b32 v22, 0xfffffcff, v1
M 9,44,2
s_cmp_eq_u32 s52, %[wrap]
LC_ 124
M 9,45,3
s_cselect_b32 s52, %[rst], s52
DQ 3072
M 10,56,2
WL 3
LO 121,17,121
LU 17,121
M 10,57,3
s_add_u32 s50, s50, %[d]
RA 220,1,1024
HS 32,17
M 11,68,2
LL 19,78
LL 125,79
M 11,69,3
s_cmp_eq_u32 s50, %[wrap]
LA 19
RV 252,22,5120
M 6,10,4
s_cselect_b32 s50, %[rst], s50
LC_ 125
v_pk_add_f16 v32, v29, v32
M 6,11,5
v_pk_add_f16 v32, v31, v32
M 7,22,4
WL 4
LO 124,18,124
LU 18,124
M 7,23,5
s_add_u32 s38, s38, 32768
RA 224,1,2048
HZ 33,18
M 8,34,4
s_cmp_eq_u32 s38, s77
LL 16,80
LL 106,81
M 8,35,5
s_cselect_b32 s38, s76, s38
LA 16
M 9,46,4
s_barrier
LC_ 106
M 9,47,5
s_sub_u32 s39, 0, s39
M 10,58,4
WL 3
LO 125,19,125
LU 19,125
M 10,59,5
RA 228,1,3072
HS 33,19
M 11,70,4
LL 17,82
LL 107,83
M 11,71,5
2:
LA 17
v_pk_add_f16 v33, v26, v33
Z 0,0,0
LC_ 107
ds_read_b32 v20, v5 offset:4096
M 0,1,1
ds_read_b64 v[14:15], v5 offset:2048
v_pk_add_f16 v26, v32, v33
Z 1,12,0
WL 5
LO 106,16,106
LU 16,106
M 1,13,1
RA 232,1,4096
HZ 27,16
Z 2,24,0
LL 18,84
LL 110,85
M 2,25,1
LA 18
v_subrev_u32 v5, s39, v5
Z 3,36,0
LC_ 110
HX 26
M 3,37,1
s_add_u32 s42, s94, s60
W1 26,2048
Z 4,48,0
WL 6
LO 107,17,107
LU 17,107
M 4,49,1
s_addc_u32 s43, s95, 0
RA 236,1,6144
HS 27,17
Z 5,60,0
LL 19,86
LL 111,87
M 5,61,1
LA 19
SU 104
M 0,2,2
WL 6
LC_ 111
v_add_f32 v14, v14, v15
M 0,3,3
s_nop 0
v_add_f32_dpp v14, v14, v14 quad_perm:[1,0,3,2] row_mask:0xf bank_mask:0xf
M 1,14,2
WL 4
LO 110,18,110
LU 18,110
M 1,15,3
RA 216,1,7168
HZ 29,18
M 2,26,2
LL 16,88
LL 114,89
M 2,27,3
LA 16
v_add_f32_dpp v14, v14, v14 quad_perm:[2,3,0,1] row_mask:0xf bank_mask:0xf
M 3,38,2
)"
R"(LC_ 114
v_add_f32_dpp v14, v14, v14 row_half_mirror row_mask:0xf bank_mask:0xf
M 3,39,3
s_add_u32 s42, s94, s61
v_cvt_f64_f32 v[14:15], v14
M 4,50,2
WL 3
LO 111,19,111
LU 19,111
M 4,51,3
s_addc_u32 s43, s95, 0
RA 220,1,8192
HS 29,19
M 5,62,2
LL 17,90
LL 115,91
M 5,63,3
LA 17
s_mov_b64 exec, s[54:55]
SP
s_mov_b64 exec, -1
M 0,4,4
LC_ 115
SU 108
M 0,5,5
v_readfirstlane_b32 s59, v20
M 1,16,4
WL 3
LO 114,16,114
LU 16,114
M 1,17,5
RA 224,1,9216
HZ 30,16
M 2,28,4
LL 18,92
LL 118,93
M 2,29,5
LA 18
M 3,40,4
LC_ 118
M 3,41,5
s_add_u32 s42, s94, s62
M 4,52,4
WL 3
LO 115,17,115
LU 17,115
M 4,53,5
s_addc_u32 s43, s95, 0
RA 228,1,10240
HS 30,17
M 5,64,4
LL 19,94
LL 119,95
M 5,65,5
LA 19
SU 112
M 0,6,0
LC_ 119
M 0,7,1
M 1,18,0
WL 3
LO 118,18,118
LU 18,118
M 1,19,1
RA 232,1,11264
HZ 31,18
M 2,30,0
LL 16,96
LL 122,97
M 2,31,1
LA 16
RV 252,22,5392
M 3,42,0
LC_ 122
M 3,43,1
s_add_u32 s42, s94, s63
M 4,54,0
WL 4
LO 119,19,119
LU 19,119
M 4,55,1
s_addc_u32 s43, s95, 0
RA 236,1,12288
HS 31,19
M 5,66,0
LL 17,98
LL 123,99
M 5,67,1
LA 17
SU 116
M 0,8,2
LC_ 123
v_pk_add_f16 v31, v30, v31
M 0,9,3
M 1,20,2
WL 3
LO 122,16,122
LU 16,122
M 1,21,3
RA 216,1,13312
HZ 32,16
M 2,32,2
LL 18,100
LL 126,101
M 2,33,3
LA 18
M 3,44,2
LC_ 126
M 3,45,3
s_add_u32 s42, s94, s64
M 4,56,2
WL 3
LO 123,17,123
LU 17,123
M 4,57,3
s_addc_u32 s43, s95, 0
RA 220,1,14336
HS 32,17
M 5,68,2
LL 19,102
LL 127,103
M 5,69,3
LA 19
SU 120
M 0,10,4
LC_ 127
v_pk_add_f16 v32, v29, v32
M 0,11,5
v_pk_add_f16 v32, v31, v32
M 1,22,4
WL 3
LO 126,18,126
LU 18,126
M 1,23,5
RA 224,1,15360
HZ 33,18
M 2,34,4
LL 16,56
LL 104,57
M 2,35,5
LA 16
M 3,46,4
LC_ 104
M 3,47,5
s_add_u32 s42, s94, s65
M 4,58,4
WL 3
LO 127,19,127
LU 19,127
M 4,59,5
s_addc_u32 s43, s95, 0
RA 228,1,16384
HS 33,19
M 5,70,4
LL 17,58
LL 105,59
M 5,71,5
LA 17
SU 124
Z 6,0,0
LC_ 105
v_pk_add_f16 v33, v27, v33
M 6,1,1
v_pk_add_f16 v27, v32, v33
Z 7,12,0
WL 3
LO 104,16,104
LU 16,104
M 7,13,1
RA 232,1,17408
HZ 26,16
Z 8,24,0
WV 12
LL 18,60
LL 108,61
M 8,25,1
s_mul_i32 s53, s50, 25600
LA 18
HX 27
Z 9,36,0
s_add_u32 s40, %[wlo], s53
LC_ 108
W1 27,3072
M 9,37,1
s_addc_u32 s41, %[whi], 0
v_readfirstlane_b32 s72, v11
Z 10,48,0
WL 4
LO 105,17,105
LU 17,105
M 10,49,1
s_mov_b32 m0, s38
RA 236,1,18432
HS 26,17
Z 11,60,0
LL 19,62
LL 109,63
M 11,61,1
LA 19
DW 0
M 6,2,2
LC_ 109
v_mov_b32 v12, s72
M 6,3,3
W2 12,12,16
M 7,14,2
WL 5
LO 108,18,108
LU 18,108
M 7,15,3
s_add_u32 s37, s37, 32768
RA 216,1,19456
HZ 29,18
M 8,26,2
s_cmp_eq_u32 s37, s75
LL 16,64
LL 112,65
M 8,27,3
s_cselect_b32 s37, s74, s37
LA 16
DW 1024
M 9,38,2
LC_ 112
v_add_u32 v4, s39, v4
M 9,39,3
s_max_u32 s59, s59, %[nb]
s_mov_b64 exec, 1
AT
s_mov_b64 exec, -1
M 10,50,2
WL 4
LO 109,19,109
LU 19,109
M 10,51,3
s_add_u32 %[nb], %[nb], 1
RA 220,1,20480
HS 29,19
M 11,62,2
LL 17,66
LL 113,67
M 11,63,3
LA 17
DW 2048
M 6,4,4
LC_ 113
M 6,5,5
M 7,16,4
WL 3
LO 112,16,112
LU 16,112
M 7,17,5
s_mul_i32 s53, s51, 49152
RA 224,1,21504
HZ 30,16
M 8,28,4
s_bitcmp1_b64 s[92:93], s51
LL 18,68
LL 116,69
M 8,29,5
s_cselect_b64 s[94:95], s[88:89], s[86:87]
LA 18
M 9,40,4
s_add_u32 s94, s94, s53
LC_ 116
M 9,41,5
s_addc_u32 s95, s95, 0
M 10,52,4
WL 3
LO 113,17,113
LU 17,113
M 10,53,5
s_add_u32 s51, s51, %[d]
RA 228,1,22528
HS 30,17
M 11,64,4
LL 19,70
LL 117,71
M 11,65,5
s_cmp_eq_u32 s51, %[wrap]
LA 19
M 6,6,0
s_cselect_b32 s51, %[rst], s51
LC_ 117
M 6,7,1
s_add_u32 s40, s40, 3072
M 7,18,0
WL 3
LO 116,18,116
LU 18,116
M 7,19,1
s_addc_u32 s41, s41, 0
RA 232,1,23552
HZ 31,18
M 8,30,0
s_add_u32 m0, s38, 3072
LL 16,72
LL 120,73
M 8,31,1
LA 16
DW 0
M 9,42,0
LC_ 120
DW 1024
M 9,43,1
M 10,54,0
WL 3
LO 117,19,117
LU 19,117
M 10,55,1
RA 236,1,24576
HS 31,19
M 11,66,0
LL 17,74
LL 121,75
M 11,67,1
LA 17
DW 2048
M 6,8,2
LC_ 121
v_pk_add_f16 v31, v30, v31
M 6,9,3
s_lshl_b32 s53, s52, 8
v_add_u32 v1, s37, v0
M 7,20,2
WL 3
LO 120,16,120
LU 16,120
M 7,21,3
s_add_u32 s48, %[alo], s53
RA 216,1,0
HZ 32,16
M 8,32,2
s_addc_u32 s49, %[ahi], 0
LL 18,76
LL 124,77
M 8,33,3
s_add_u32 s52, s52, %[d]
LA 18
v_and_b32 v22, 0xfffffcff, v1
M 9,44,2
s_cmp_eq_u32 s52, %[wrap]
LC_ 124
M 9,45,3
s_cselect_b32 s52, %[rst], s52
DQ 3072
M 10,56,2
WL 3
LO 121,17,121
LU 17,121
M 10,57,3
s_add_u32 s50, s50, %[d]
RA 220,1,1024
HS 32,17
M 11,68,2
LL 19,78
LL 125,79
M 11,69,3
s_cmp_eq_u32 s50, %[wrap]
LA 19
RV 252,22,5120
M 6,10,4
s_cselect_b32 s50, %[rst], s50
LC_ 125
v_pk_add_f16 v32, v29, v32
M 6,11,5
v_pk_add_f16 v32, v31, v32
M 7,22,4
WL 4
LO 124,18,124
LU 18,124
M 7,23,5
s_add_u32 s38, s38, 32768
RA 224,1,2048
HZ 33,18
M 8,34,4
s_cmp_eq_u32 s38, s77
LL 16,80
LL 106,81
M 8,35,5
s_cselect_b32 s38, s76, s38
LA 16
M 9,46,4
s_barrier
LC_ 106
M 9,47,5
s_sub_u32 s39, 0, s39
M 10,58,4
WL 3
LO 125,19,125
LU 19,125
M 10,59,5
RA 228,1,3072
HS 33,19
M 11,70,4
s_cmp_lt_u32 s59, 30
LL 17,82
LL 107,83
M 11,71,5
s_cbranch_scc1 2b
LA 17
)"
R"(v_pk_add_f16 v33, v26, v33
LC_ 107
ds_read_b64 v[14:15], v5 offset:2048
v_pk_add_f16 v26, v32, v33
HX 26
v_subrev_u32 v5, s39, v5
W1 26,2048
WL 0
LO 106,16,106
LU 16,106
HZ 27,16
LL 18,84
LL 110,85
LA 18
v_add_f32 v14, v14, v15
s_nop 1
v_add_f32_dpp v14, v14, v14 quad_perm:[1,0,3,2] row_mask:0xf bank_mask:0xf
s_nop 1
v_add_f32_dpp v14, v14, v14 quad_perm:[2,3,0,1] row_mask:0xf bank_mask:0xf
s_nop 1
v_add_f32_dpp v14, v14, v14 row_half_mirror row_mask:0xf bank_mask:0xf
v_cvt_f64_f32 v[14:15], v14
s_mov_b64 exec, s[54:55]
SP
s_mov_b64 exec, -1
LC_ 110
s_add_u32 s42, s94, s60
s_addc_u32 s43, s95, 0
LO 107,17,107
LU 17,107
HS 27,17
SU 104
LL 19,86
LL 111,87
LA 19
LC_ 111
WL 0
LO 110,18,110
LU 18,110
HZ 29,18
LL 16,88
LL 114,89
LA 16
LC_ 114
s_add_u32 s42, s94, s61
s_addc_u32 s43, s95, 0
LO 111,19,111
LU 19,111
HS 29,19
SU 108
LL 17,90
LL 115,91
LA 17
LC_ 115
WL 0
LO 114,16,114
LU 16,114
HZ 30,16
LL 18,92
LL 118,93
LA 18
LC_ 118
s_add_u32 s42, s94, s62
s_addc_u32 s43, s95, 0
LO 115,17,115
LU 17,115
HS 30,17
SU 112
LL 19,94
LL 119,95
LA 19
LC_ 119
WL 0
LO 118,18,118
LU 18,118
HZ 31,18
LL 16,96
LL 122,97
LA 16
LC_ 122
s_add_u32 s42, s94, s63
s_addc_u32 s43, s95, 0
LO 119,19,119
LU 19,119
HS 31,19
SU 116
LL 17,98
LL 123,99
v_pk_add_f16 v31, v30, v31
LA 17
LC_ 123
WL 0
LO 122,16,122
LU 16,122
HZ 32,16
LL 18,100
LL 126,101
LA 18
LC_ 126
s_add_u32 s42, s94, s64
s_addc_u32 s43, s95, 0
LO 123,17,123
LU 17,123
HS 32,17
SU 120
LL 19,102
LL 127,103
v_pk_add_f16 v32, v29, v32
LA 19
v_pk_add_f16 v32, v31, v32
LC_ 127
WL 0
LO 126,18,126
LU 18,126
HZ 33,18
s_add_u32 s42, s94, s65
s_addc_u32 s43, s95, 0
LO 127,19,127
LU 19,127
HS 33,19
SU 124
v_pk_add_f16 v33, v27, v33
v_pk_add_f16 v27, v32, v33
HX 27
W1 27,3072
s_nop 1
v_add_u32 v4, s39, v4
s_lshl_b32 s53, s52, 8
s_add_u32 s48, %[alo], s53
s_addc_u32 s49, %[ahi], 0
s_add_u32 s52, s52, %[d]
s_cmp_eq_u32 s52, %[wrap]
s_cselect_b32 s52, %[rst], s52
WL 0
WV 0
s_barrier
s_sub_u32 s39, 0, s39
ds_read_b64 v[14:15], v5 offset:2048
s_nop 1
v_subrev_u32 v5, s39, v5
WL 0
v_add_f32 v14, v14, v15
s_nop 1
v_add_f32_dpp v14, v14, v14 quad_perm:[1,0,3,2] row_mask:0xf bank_mask:0xf
s_nop 1
v_add_f32_dpp v14, v14, v14 quad_perm:[2,3,0,1] row_mask:0xf bank_mask:0xf
s_nop 1
v_add_f32_dpp v14, v14, v14 row_half_mirror row_mask:0xf bank_mask:0xf
v_cvt_f64_f32 v[14:15], v14
s_mov_b64 exec, s[54:55]
SP
s_mov_b64 exec, -1
GE
s_waitcnt vmcnt(0) lgkmcnt(0)
8:
.purgem M
.purgem Z
.purgem LZ
.purgem LS
.purgem LD
.purgem GZ
.purgem GS
.purgem CL
.purgem CH
.purgem XL
.purgem XN
.purgem F
.purgem U
.purgem E
.purgem A
.purgem R
.purgem C
.purgem S0
.purgem S
.purgem HC
.purgem HF
.purgem HM
.purgem HE
.purgem HG
.purgem HA
.purgem HR
.purgem HQ
.purgem HP
.purgem HY
.purgem HK
.purgem LQ
.purgem LL
.purgem LH
.purgem LA
.purgem LC_
.purgem LO
.purgem HZ
.purgem HS
.purgem HT
.purgem HX
.purgem UX
.purgem EX
.purgem AX
.purgem RX
.purgem TX
.purgem SX
.purgem AW
.purgem RA
.purgem RV
.purgem W2
.purgem W1
.purgem DW
.purgem DF
.purgem DX
.purgem ST
.purgem SU
.purgem SP
.purgem AT
.purgem PFA
.purgem PFN
.purgem PFW
.purgem PF0
.purgem WL
.purgem WV
.purgem LU
.purgem CF
.purgem XS
.purgem AR
.purgem DQ
.purgem GE
.purgem BW
.purgem BR
.purgem LK
)"
      : [nb] "=&s"(nb)
      : [xlo] "s"((unsigned)xp), [xhi] "s"((unsigned)(xp >> 32)), [hlo] "s"((unsigned)hp), [hhi] "s"((unsigned)(hp >> 32)),
        [glo] "s"((unsigned)gp), [ghi] "s"((unsigned)(gp >> 32)), [wlo] "s"((unsigned)wp), [whi] "s"((unsigned)(wp >> 32)),
        [w] "s"(w), [lds] "s"(lds0), [d] "s"(d), [wrap] "s"(wrap), [rst] "s"(rst), [pos0] "s"(pos0), [xo0] "s"(xo0), [xs1] "s"(xs1), [sb1] "s"(sb1), [r0o] "s"(r0o), [tsel] "s"(tsel), [s2] "s"(s2),
        [alo] "s"((unsigned)ap), [ahi] "s"((unsigned)(ap >> 32)), [ca0] "s"(ca0), [pm] "s"(pm), [pf] "s"(pf)
      : K1_CLOB);
  TS(6);
  MK_END(1);
}

typedef __bf16 bf16x8_t __attribute__((ext_vector_type(8)));
DI f32x4 mfma16(u32x4 a, u32x4 b, f32x4 c) {
  return __builtin_amdgcn_mfma_f32_16x16x32_bf16(__builtin_bit_cast(bf16x8_t, a), __builtin_bit_cast(bf16x8_t, b), c, 0, 0, 0);
}
DI float grn_gx(double s, float scale) { return round_bf16(__builtin_amdgcn_sqrtf(round_bf16(round_bf16((float)s * scale) + 1e-6f))); }
DI void slow_fc1(const u16* __restrict__ x, const u16* __restrict__ dw, char* __restrict__ hws, float* sacc, int w) {
  const int lane = __builtin_amdgcn_mbcnt_hi(~0u, __builtin_amdgcn_mbcnt_lo(~0u, 0u)), m = lane & 15, g = lane >> 4;
  for (int i = lane; i < HID; i += 64) sacc[i] = 0.f;
#pragma unroll 1
  for (int it = 0; it < 9; ++it) {
    const int sub = blockIdx.x * 36 + it * 4 + w;
    const unsigned px = sub_pixel(x, sub, m);
    u32x4 A[12];
    float sum = 0.f;
    const u16* dq = dw;
    asm volatile("" : "+s"(dq));
#pragma unroll
    for (int ks = 0; ks < 12; ++ks) {
      const u32x4 v = *(const u32x4*)(x + (size_t)px * CH + 32 * ks + 8 * g);
      const u32x4 d = *(const u32x4*)(dq + 32 * ks + 8 * g);
#pragma unroll
      for (int j = 0; j < 4; ++j) {
        A[ks][j] = pack_bf16(bf_lo(v[j]) * bf_lo(d[j]), bf_hi(v[j]) * bf_hi(d[j]));
        sum += bf_lo(A[ks][j]) + bf_hi(A[ks][j]);
      }
      asm volatile("" : "+v"(A[ks]));
    }
    sum += __shfl_xor(sum, 16); sum += __shfl_xor(sum, 32);
    const float mean = sum * (1.0f / CH);
    float var = 0.f;
#pragma unroll
    for (int ks = 0; ks < 12; ++ks) {
#pragma unroll
      for (int j = 0; j < 4; ++j) {
        const float a = bf_lo(A[ks][j]) - mean, b = bf_hi(A[ks][j]) - mean;
        var += a * a + b * b;
      }
      asm volatile("" : "+v"(A[ks]));
    }
    var += __shfl_xor(var, 16); var += __shfl_xor(var, 32);
    const float mb = round_bf16(mean);
    const float rd = 1.0f / round_bf16(sqrtf(round_bf16(round_bf16(var * (1.0f / CH)) + 1e-5f)));
#pragma unroll
    for (int ks = 0; ks < 12; ++ks)
#pragma unroll
      for (int j = 0; j < 4; ++j) A[ks][j] = pack_bf16((bf_lo(A[ks][j]) - mb) * rd, (bf_hi(A[ks][j]) - mb) * rd);
#pragma unroll 1
    for (int tc = 0; tc < 96; ++tc) {
      f32x4 acc = {0.f, 0.f, 0.f, 0.f};
#pragma unroll
      for (int ks = 0; ks < 12; ++ks) acc = mfma16(A[ks], *(const u32x4*)(g_buf + (size_t)(tc * 12 + ks) * 1024 + lane * 16), acc);
      float q = 0.f;
#pragma unroll
      for (int r = 0; r < 4; ++r) { acc[r] = gelu_tanh(acc[r]); q = __builtin_fmaf(acc[r], acc[r], q); }
      typedef unsigned u32x2_ __attribute__((ext_vector_type(2)));
      *(u32x2_*)(hws + h_piece(hws, sub, tc >> 1) + g * 256 + ((m + 4 * g) & 15) * 16 + (tc & 1) * 8) = (u32x2_){pack_bf16(acc[0], acc[1]), pack_bf16(acc[2], acc[3])};
      q += __shfl_xor(q, 16); q += __shfl_xor(q, 32);
      if (g == 0) sacc[tc * 16 + m] += q;
    }
  }
  __builtin_amdgcn_wave_barrier();
  __builtin_amdgcn_fence(__ATOMIC_ACQ_REL, "workgroup");
  for (int i = lane; i < HID; i += 64) atomicAdd((double*)(g_buf + G_ACC2) + i, (double)sacc[i]);
}
DI void slow_bar(unsigned* c, int w) {
  const int lane = __builtin_amdgcn_mbcnt_hi(~0u, __builtin_amdgcn_mbcnt_lo(~0u, 0u));
  asm volatile("s_waitcnt vmcnt(0)" ::: "memory");
  __syncthreads();
  if (w == 0 && lane == 0) {
    __threadfence();
    asm volatile("s_waitcnt vmcnt(0)" ::: "memory");
    __hip_atomic_fetch_add(c, 1u, __ATOMIC_RELAXED, __HIP_MEMORY_SCOPE_AGENT);
    for (int i = 0; i < (1 << 22) && __hip_atomic_load(c, __ATOMIC_RELAXED, __HIP_MEMORY_SCOPE_AGENT) < (unsigned)NWG; ++i) __builtin_amdgcn_s_sleep(32);
    __threadfence();
    asm volatile("s_waitcnt vmcnt(0)" ::: "memory");
  }
  __syncthreads();
}

#define K2_CLOB "memory", "vcc", "scc", "s48", "s49", "s50", "s51", "s52", "s53", "s54", "s55", "s56", "s57", "s58", "s59", "s60", "s61", "s62", "s63", "s64", "s65", "s66", "s67", \
  "s68", "s69", "s70", "s71", "s72", "s73", "s74", "s75", "s76", "s77", "s78", "s79", "s80", "s81", "s84", "s85", "s86", "s87", "s88", "s89", "s90", "s91", "s92", "s93", "s94", "s95", "s96", "s97", "s98", "s99", "s38", "s39", "s40", "s41", "s42", "s43", "s44", "s45", "s46", "s47", VA_CLOB
DI void slow_fc2(const u16* __restrict__ x, const char* __restrict__ hws, u16* __restrict__ out, const u16* __restrict__ grn_g, const u16* __restrict__ w2,
                 char* smem, bool hbf, int w) {
  float* sc = (float*)smem;
  float* red = sc + HID;
  u16* wf = (u16*)(smem + 8192);
  const int lane = __builtin_amdgcn_mbcnt_hi(~0u, __builtin_amdgcn_mbcnt_lo(~0u, 0u)), tid = w * 64 + lane, m = lane & 15, g = lane >> 4;
  __syncthreads();
  float part = 0.f;
  for (int c = tid; c < HID; c += 256) {
    double s = 0.0;
    if (hbf) s = ((const double*)(g_buf + G_ACC2))[c];
    else for (int r = 0; r < (int)K1_NREP; ++r) s += ((const double*)(g_buf + K1_G_ACC + r * 12288))[c];
    const float gx = grn_gx(s, hbf ? 1.0f : g_sc[2]);
    sc[c] = gx; part += gx;
  }
#pragma unroll
  for (int o = 32; o > 0; o >>= 1) part += __shfl_xor(part, o);
  if (lane == 0) red[tid >> 6] = part;
  __syncthreads();
  const float gmean = round_bf16(round_bf16(((red[0] + red[1]) + (red[2] + red[3])) * (1.0f / HID)) + 1e-6f);
  for (int c = tid; c < HID; c += 256) sc[c] = (1.0f + ld_bf16(grn_g + c) * round_bf16(sc[c] / gmean)) ;
  const float hs = g_sc[3];
#pragma unroll 1
  for (int it = 0; it < 9; ++it) {
    const int sub = blockIdx.x * 36 + it * 4 + (tid >> 6);
    f32x4 acc[24];
#pragma unroll
    for (int i = 0; i < 24; ++i) acc[i] = (f32x4){0.f, 0.f, 0.f, 0.f};
#pragma unroll 1
    for (int t = 0; t < 48; ++t) {
      __syncthreads();
      for (int e = tid; e < 32 * CH; e += 256) {
        const int k = e / CH, n = e % CH;
        wf[((n >> 4) * 64 + (k >> 3) * 16 + (n & 15)) * 8 + (k & 7)] = (u16)pack_bf16(sc[32 * t + k] * ld_bf16(w2 + (size_t)(32 * t + k) * CH + n), 0.f);
      }
      __syncthreads();
      const char* hp = hws + h_piece(hws, sub, t) + (m >> 2) * 256 + (g >> 1) * 8 + 2 * (m & 3);
      u32x4 A;
#pragma unroll
      for (int j = 0; j < 4; ++j) {
        unsigned v[2];
#pragma unroll
        for (int k = 0; k < 2; ++k) {
          v[k] = *(const u16*)(hp + ((8 * (g & 1) + 2 * j + k + (m & 12)) & 15) * 16);
          if (!hbf) v[k] = pack_bf16(hs * (float)__builtin_bit_cast(_Float16, (u16)v[k]), 0.f) & 0xffffu;
        }
        A[j] = v[0] | (v[1] << 16);
      }
#pragma unroll
      for (int i = 0; i < 24; ++i) acc[i] = mfma16(A, *(const u32x4*)(wf + (i * 64 + lane) * 8), acc[i]);
    }
#pragma unroll
    for (int i = 0; i < 24; ++i) {
      const int n = 16 * i + m, mm = n % 192;
      const f32x4 b4 = *(const f32x4*)(g_bias + (((n / 192) * 12 + 4 * (mm >> 6) + (mm & 3)) * 16 + ((mm & 63) >> 2)) * 4);
      const float bias = (b4[0] + b4[1]) + (b4[2] + b4[3]);
#pragma unroll
      for (int r = 0; r < 4; ++r) {
        const size_t o = (size_t)(sub_pixel(x, sub, 4 * g) + r) * CH + n;
        out[o] = (u16)pack_bf16(acc[i][r] + bias + ld_bf16(x + o), 0.f);
      }
    }
  }
}
__global__ __launch_bounds__(256, 1) void k2_fc2(const u16* __restrict__ x, char* __restrict__ hws, u16* __restrict__ out,
                                                 const u16* __restrict__ grn_g, const u16* __restrict__ w2, const u16* __restrict__ dw,
                                                 const u16* __restrict__ w1) {
  extern __shared__ __attribute__((aligned(16))) char smem[];
  const int w = __builtin_amdgcn_readfirstlane(threadIdx.x >> 6);
  const unsigned pg = w & 1, nh = w >> 1;
  const unsigned lds0 = lds_addr(smem);
  unsigned cl = blockIdx.x & 7;
  asm volatile("" : "+s"(cl));
  MK_START(4); TS(9);
  const uint64_t pp = uni64((uint64_t)g_prm + (blockIdx.x * 4 + w) * 256), ap = (uint64_t)g_buf + K1_G_ACC + w * 1024;
  const unsigned wm = lds0 + w * 6144, ab = lds0 + nh * 12288;
  const unsigned hb = lds0 + K2_HB + pg * 2 * K2_NU * 1024, hm = hb + nh * K2_NU * 1024;
  const unsigned bm = lds0 + K2_BIAS_OFF + w * 1536, bb = lds0 + K2_BIAS_OFF + nh * 3072;
  const unsigned pm = lds0 + K2_PF_OFF, sb = lds0 + K2_S16_OFF, sw = sb + w * 256;
  const uint64_t tb = uni64((uint64_t)g_buf + G_STMP), ta = uni64(tb + cl * 128);
  const unsigned em = __builtin_amdgcn_readfirstlane(w == 0);
  uint64_t flg;
#define K2_OPS [pp] "s"(pp), [plo] "s"((unsigned)pp), [phi] "s"((unsigned)(pp >> 32)), [cl] "s"(cl), [tb] "s"(tb), [ta] "s"(ta), [em] "s"(em), [alo] "s"((unsigned)ap), [ahi] "s"((unsigned)(ap >> 32)), [wm] "s"(wm), [hm] "s"(hm), [bm] "s"(bm), [ab] "s"(ab), [hb] "s"(hb), [bb] "s"(bb), \
         [sb] "s"(sb), [sw] "s"(sw), [pm] "s"(pm)
  asm volatile(
R"(.macro MA t,a,b
v_mfma_f32_16x16x32_f16 a[\t:\t+3], a[\b:\b+3], a[\a:\a+3], a[\t:\t+3]
.endm
.macro MV t,a,b
v_mfma_f32_16x16x32_f16 v[\t:\t+3], a[\b:\b+3], a[\a:\a+3], v[\t:\t+3]
.endm
.macro AN d
v_and_b32 v[\d], s39, v[\d]
.endm
.macro AW a,s
v_accvgpr_write_b32 a[\a], v[\s]
.endm
.macro AR d,a
v_accvgpr_read_b32 v[\d], a[\a]
.endm
.macro UL d,x
v_lshlrev_b32 v[\d], 16, v[\x]
.endm
.macro UH d,x
v_and_b32 v[\d], s38, v[\x]
.endm
.macro AD d,b
v_fmac_f32 v[\d], s53, v[\b]
.endm
.macro TR d,o
ds_read_b64_tr_b16 a[\d:\d+1], v2 offset:\o
.endm
.macro TQ d,o
ds_read_b64_tr_b16 a[\d:\d+1], v54 offset:\o
.endm
.macro RA d,o
ds_read_b128 a[\d:\d+3], v1 offset:\o
.endm
.macro RC d,o
ds_read_b128 a[\d:\d+3], v5 offset:\o
.endm
.macro BV d,o
ds_read_b128 v[\d:\d+3], v4 offset:\o
.endm
.macro BA d,o
ds_read_b128 a[\d:\d+3], v4 offset:\o
.endm
.macro PA a
s_mov_b32 m0, %[pm]
s_getpc_b64 s[46:47]
s_andn2_b32 s46, s46, 0x7f
s_add_u32 s46, s46, s81
s_addc_u32 s47, s47, 0
s_add_u32 s46, s46, \a
s_addc_u32 s47, s47, 0
.endm
.macro DF
global_load_lds_dwordx4 v0, s[46:47]
.endm
.macro DW o
global_load_lds_dwordx4 v0, s[40:41] offset:\o sc1
.endm
.macro DH o
global_load_lds_dwordx4 v0, s[40:41] offset:\o nt
.endm
.macro DS o
global_load_lds_dwordx4 v0, s[40:41] offset:\o
.endm
.macro XL d,o
global_load_dwordx2 v[\d:\d+1], v52, s[44:45] offset:\o
.endm
.macro ST d,o
global_store_dwordx2 v53, v[\d:\d+1], s[42:43] offset:\o nt sc1
.endm
.macro SU d,o
global_store_dwordx2 v53, v[\d:\d+1], s[42:43] offset:\o
.endm
.macro SA d,o
global_store_dwordx2 v53, a[\d:\d+1], s[42:43] offset:\o nt sc1
.endm
.macro CV d,a,b
v_cvt_pk_bf16_f32 v[\d], v[\a], v[\b]
.endm
.macro DR o
global_load_lds_dwordx4 v0, s[40:41] offset:\o
.endm
.macro TY d,o
ds_read_b64_tr_b16 v[\d:\d+1], v2 offset:\o
.endm
.macro TZ d,o
ds_read_b64_tr_b16 v[\d:\d+1], v54 offset:\o
.endm
.macro RS d,o
ds_read_b128 v[\d:\d+3], v46 offset:\o
.endm
.macro PM d,s
v_pk_mul_f16 v[\d], v[\d], v[\s]
.endm
.macro FS
global_store_dword v0, v10, s[96:97] sc1
.endm
.macro FL
global_load_dword v6, v7, s[98:99] sc1
.endm
.macro FB
s_cbranch_vccnz 21b
22:
.endm
.macro FG
s_or_b64 s[54:55], s[44:45], vcc
s_cbranch_scc1 23b
24:
.endm
.macro FX
s_branch 25f
21:
s_sleep 4
global_load_dword v6, v7, s[98:99] sc1
s_waitcnt vmcnt(0)
v_cmp_ne_u32 vcc, 1, v6
s_sub_u32 s80, s80, 1
s_cbranch_scc1 22f
s_nop 2
s_cbranch_vccnz 21b
s_branch 22f
23:
s_cmp_eq_u64 s[44:45], 0
s_cselect_b32 s54, 4, 0
v_mov_b32 v11, s54
v_mov_b32 v10, 1
s_mov_b64 exec, 1
global_store_dword v11, v10, s[78:79] offset:384 sc1
s_mov_b64 exec, -1
s_nop 4
s_branch 24f
25:
.endm
.macro PB
s_cbranch_vccnz 31f
32:
.endm
.macro PQ a
s_mov_b32 m0, %[pm]
s_add_u32 s46, s100, s81
s_addc_u32 s47, s101, 0
s_add_u32 s46, s46, \a
s_addc_u32 s47, s47, 0
.endm
.macro PJ
s_getpc_b64 s[100:101]
38:
s_add_u32 s92, s92, 39f-38b
s_add_u32 s100, s100, s92
s_addc_u32 s101, s101, 0
.endm
.macro WL n
s_waitcnt lgkmcnt(\n)
.endm
.macro WV n
s_waitcnt vmcnt(\n)
.endm
.macro EF d
v_fma_f32 v[\d], v[\d], s39, v55
.endm
.macro M n
.if \n < 44
v_mfma_f32_16x16x32_f16 a[72+4*(\n/2):72+4*(\n/2)+3], a[24+8*((\n/2)%%6)+4*(\n&1):24+8*((\n/2)%%6)+4*(\n&1)+3], a[8*((\n/12)%%3)+4*(\n&1):8*((\n/12)%%3)+4*(\n&1)+3], a[72+4*(\n/2):72+4*(\n/2)+3]
.else
v_mfma_f32_16x16x32_f16 v[-32+4*(\n/2):-32+4*(\n/2)+3], a[24+8*((\n/2)%%6)+4*(\n&1):24+8*((\n/2)%%6)+4*(\n&1)+3], a[8*((\n/12)%%3)+4*(\n&1):8*((\n/12)%%3)+4*(\n&1)+3], v[-32+4*(\n/2):-32+4*(\n/2)+3]
.endif
.endm
.macro R1 n
.if \n < 64
RA 8*(((\n%%24)/2)%%3)+4*(\n&1),1024*\n
.else
RC 8*(((\n%%24)/2)%%3)+4*(\n&1),1024*\n-49152
.endif
.endm
.macro R n
R1 \n
R1 (\n+1)
.endm
.macro T r,u
TR \r,1024*\u
TQ \r+2,1024*\u
.endm
.macro mw k
s_add_u32 m0, %[wm], 1024*\k
.endm
.macro mh k
s_add_u32 m0, %[hm], 1024*\k
.endm
.macro ad r,k
s_add_u32 s40, s[\r], 1024*\k
s_addc_u32 s41, s[\r+1], 0
.endm
.macro ae d,r,k
s_add_u32 s[\d], s[\r], 1024*\k
s_addc_u32 s[\d+1], s[\r+1], 0
.endm
.macro bc m,b,d,r
s_bitcmp1_b32 s[\m], \b
s_cselect_b64 s[\d:\d+1], s[\r+2:\r+3], s[\r:\r+1]
.endm
.macro W k
DW 1024*\k
.endm
.macro H k
DH 1024*\k
.endm
k2_code:
s_load_dwordx16 s[48:63], %[pp], 0x0
s_load_dwordx16 s[64:79], %[pp], 0x40
s_load_dwordx16 s[84:99], %[pp], 0x80
v_mbcnt_lo_u32_b32 v7, -1, 0
v_mbcnt_hi_u32_b32 v7, -1, v7
v_lshlrev_b32 v0, 4, v7
v_add_u32 v204, 0x1000, v0
v_add_u32 v205, 0x2000, v0
s_mov_b32 s44, %[alo]
s_mov_b32 s45, %[ahi]
global_load_dwordx4 v[56:59], v0, s[44:45]
global_load_dwordx4 v[60:63], v204, s[44:45]
global_load_dwordx4 v[72:75], v205, s[44:45]
ae 44,44,12
global_load_dwordx4 v[76:79], v0, s[44:45]
global_load_dwordx4 v[96:99], v204, s[44:45]
global_load_dwordx4 v[100:103], v205, s[44:45]
s_waitcnt lgkmcnt(0)
bc 49,0,66,60
mh 0
ad 66,0
H 0
H 1
ae 44,44,12
global_load_dwordx4 v[120:123], v0, s[44:45]
global_load_dwordx4 v[124:127], v204, s[44:45]
global_load_dwordx4 v[144:147], v205, s[44:45]
ae 44,44,12
global_load_dwordx4 v[148:151], v0, s[44:45]
global_load_dwordx4 v[152:155], v204, s[44:45]
global_load_dwordx4 v[156:159], v205, s[44:45]
v_and_b32 v8, 15, v7
v_lshrrev_b32 v9, 4, v7
global_load_dwordx4 v[224:227], v0, s[94:95]
global_load_dwordx4 v[228:231], v0, s[94:95] offset:512
v_lshlrev_b32 v10, 2, v7
global_load_dword v168, v10, s[92:93] offset:0
global_load_dword v169, v10, s[92:93] offset:1024
global_load_dword v170, v10, s[92:93] offset:2048
global_load_dwordx4 v[80:83], v0, s[88:89]
s_mov_b64 exec, 0xff
global_load_dwordx4 v[84:87], v0, s[88:89] offset:1024
s_mov_b64 exec, -1
FX
v_add_u32 v1, %[ab], v0
v_add_u32 v5, 0xc000, v1
v_and_b32 v10, 3, v7
v_lshrrev_b32 v11, 2, v7
v_lshl_add_u32 v11, v10, 2, v11
v_and_b32 v11, 15, v11
v_lshlrev_b32 v11, 4, v11
mw 0
ad 86,0
DR 0
v_lshl_add_u32 v11, v10, 8, v11
v_add_u32 v2, %[hb], v11
v_lshrrev_b32 v10, 1, v7
v_and_b32 v10, 8, v10
v_add_u32 v2, v10, v2
v_xor_b32 v54, 8, v2
v_lshlrev_b32 v4, 4, v8
DR 1024
v_add_u32 v4, %[bb], v4
s_mov_b32 s38, 0xffff0000
s_mov_b32 s80, 0x40000
s_mov_b32 s81, s52
s_mov_b32 s39, s54
v_mov_b32 v55, s54
v_mul_f32 v55, 0xb5400000, v55
v_lshl_add_u32 v46, v9, 4, %[sb]
v_add_u32 v46, 53248, v46
v_bfe_u32 v10, v7, 1, 2
v_bfe_u32 v11, v7, 3, 1
v_bfe_u32 v12, v7, 1, 1
DR 2048
v_xor_b32 v11, v11, v12
v_and_b32 v12, 1, v7
v_lshl_add_u32 v11, v11, 1, v12
v_lshl_add_u32 v10, v10, 2, v11
v_lshl_add_u32 v10, v9, 4, v10
v_lshl_add_u32 v206, v10, 2, %[sw]
v_lshl_add_u32 v207, v7, 2, %[sw]
mw 3
ad 86,3
DR 0
v_lshrrev_b32 v13, 4, v0
s_movk_i32 s42, 1366
v_add_u32 v128, s51, v13
v_lshrrev_b32 v11, 6, v128
v_mul_u32_u24 v12, s42, v11
v_lshrrev_b32 v12, 16, v12
v_and_b32 v11, 1, v11
v_lshl_add_u32 v11, v12, 1, v11
v_bfe_u32 v128, v128, 4, 2
DR 1024
v_lshl_add_u32 v128, v11, 2, v128
v_lshl_add_u32 v128, v128, 4, %[sb]
v_cmp_gt_u32 vcc, 16, v11
v_add_u32 v12, 53248, v128
s_nop 0
v_cndmask_b32 v128, v128, v12, vcc
v_add_u32 v129, s51, v13
v_add_u32 v129, 64, v129
v_lshrrev_b32 v11, 6, v129
v_mul_u32_u24 v12, s42, v11
DR 2048
v_lshrrev_b32 v12, 16, v12
v_and_b32 v11, 1, v11
v_lshl_add_u32 v11, v12, 1, v11
v_bfe_u32 v129, v129, 4, 2
v_lshl_add_u32 v129, v11, 2, v129
v_lshl_add_u32 v129, v129, 4, %[sb]
v_cmp_gt_u32 vcc, 16, v11
v_add_u32 v12, 53248, v129
s_nop 0
v_cndmask_b32 v129, v129, v12, vcc
bc 49,1,66,60
mh 2
ad 66,96
H 0
WV 13
v_add_f32 v224, v224, v225
v_add_f32 v226, v226, v227
WV 12
v_add_f32 v228, v228, v229
v_add_f32 v230, v230, v231
v_add_u32 v240, %[bm], v0
v_add_f32 v224, v224, v226
v_add_f32 v228, v228, v230
H 1
v_sub_f32 v224, v224, v55
v_sub_f32 v228, v228, v55
v_mul_f32 v224, s53, v224
v_mul_f32 v228, s53, v228
v_mov_b32 v225, v224
v_mov_b32 v229, v228
v_mov_b32 v226, v224
bc 48,1,64,56
mh 4
ad 64,96
H 0
v_mov_b32 v230, v228
v_mov_b32 v227, v224
v_mov_b32 v231, v228
ds_write_b128 v240, v[224:227] offset:0
ds_write_b128 v240, v[228:231] offset:512
v_mov_b32 v219, 0x7f800000
s_mov_b32 s44, 0x44100000
v_add_f64 v[56:57], v[56:57], v[76:77]
v_add_f64 v[60:61], v[60:61], v[96:97]
v_add_f64 v[72:73], v[72:73], v[100:101]
v_add_f64 v[58:59], v[58:59], v[78:79]
v_add_f64 v[62:63], v[62:63], v[98:99]
H 1
v_add_f64 v[74:75], v[74:75], v[102:103]
v_add_f64 v[56:57], v[56:57], v[120:121]
v_add_f64 v[60:61], v[60:61], v[124:125]
v_add_f64 v[72:73], v[72:73], v[144:145]
v_add_f64 v[58:59], v[58:59], v[122:123]
v_add_f64 v[62:63], v[62:63], v[126:127]
v_add_f64 v[74:75], v[74:75], v[146:147]
mw 24
ad 86,24
DR 0
v_add_f64 v[56:57], v[56:57], v[148:149]
v_add_f64 v[60:61], v[60:61], v[152:153]
v_add_f64 v[72:73], v[72:73], v[156:157]
v_add_f64 v[58:59], v[58:59], v[150:151]
v_add_f64 v[62:63], v[62:63], v[154:155]
v_add_f64 v[74:75], v[74:75], v[158:159]
v_cvt_f32_f64 v172, v[56:57]
v_cvt_f32_f64 v176, v[60:61]
DR 1024
v_cvt_f32_f64 v180, v[72:73]
v_cvt_f32_f64 v173, v[58:59]
v_cvt_f32_f64 v177, v[62:63]
v_cvt_f32_f64 v181, v[74:75]
v_mul_f32 v174, s55, v172
v_mul_f32 v178, s55, v176
v_mul_f32 v182, s55, v180
DR 2048
v_mul_f32 v175, s55, v173
v_mul_f32 v179, s55, v177
v_mul_f32 v183, s55, v181
CV 175,174,175
CV 179,178,179
CV 183,182,183
UL 174,175
mw 27
ad 86,27
DR 0
UL 178,179
UL 182,183
UH 175,175
)"
R"(UH 179,179
UH 183,183
v_add_f32 v174, 0x358637bd, v174
v_add_f32 v178, 0x358637bd, v178
DR 1024
v_add_f32 v182, 0x358637bd, v182
v_add_f32 v175, 0x358637bd, v175
v_add_f32 v179, 0x358637bd, v179
v_add_f32 v183, 0x358637bd, v183
CV 175,174,175
CV 179,178,179
CV 183,182,183
DR 2048
UL 174,175
UL 178,179
UL 182,183
UH 175,175
UH 179,179
UH 183,183
v_sqrt_f32 v174, v174
bc 49,2,66,60
mh 6
ad 66,192
H 0
v_sqrt_f32 v178, v178
v_sqrt_f32 v182, v182
v_sqrt_f32 v175, v175
v_sqrt_f32 v179, v179
v_sqrt_f32 v183, v183
CV 175,174,175
CV 179,178,179
H 1
CV 183,182,183
UL 174,175
UL 178,179
UL 182,183
UH 175,175
UH 179,179
UH 183,183
bc 48,2,64,56
mh 8
ad 64,192
H 0
v_cmp_le_f32 vcc, s44, v172
v_cmp_le_f32 s[54:55], s44, v173
s_nop 0
v_cndmask_b32 v172, v219, v174, vcc
v_cndmask_b32 v173, v219, v175, s[54:55]
v_add_f32 v172, v172, v173
v_cmp_le_f32 vcc, s44, v176
v_cmp_le_f32 s[54:55], s44, v177
s_nop 0
v_cndmask_b32 v176, v219, v178, vcc
v_cndmask_b32 v177, v219, v179, s[54:55]
v_add_f32 v176, v176, v177
v_cmp_le_f32 vcc, s44, v180
v_cmp_le_f32 s[54:55], s44, v181
s_nop 0
v_cndmask_b32 v180, v219, v182, vcc
v_cndmask_b32 v181, v219, v183, s[54:55]
v_add_f32 v180, v180, v181
v_add_f32 v216, v172, v176
H 1
v_add_f32 v216, v216, v180
s_nop 1
v_add_f32_dpp v216, v216, v216 row_shr:1 row_mask:0xf bank_mask:0xf
s_nop 1
v_add_f32_dpp v216, v216, v216 row_shr:2 row_mask:0xf bank_mask:0xf
s_nop 1
v_add_f32_dpp v216, v216, v216 row_shr:4 row_mask:0xf bank_mask:0xf
s_nop 1
v_add_f32_dpp v216, v216, v216 row_shr:8 row_mask:0xf bank_mask:0xf
s_nop 1
v_add_f32_dpp v216, v216, v216 row_bcast:15 row_mask:0xa bank_mask:0xf
s_nop 1
v_add_f32_dpp v216, v216, v216 row_bcast:31 row_mask:0xc bank_mask:0xf
ds_write_b32 v207, v216 offset:52224
v_mov_b32 v10, %[sb]
WL 0
s_barrier
ds_read_b32 v220, v10 offset:52476
ds_read_b32 v221, v10 offset:52732
ds_read_b32 v222, v10 offset:52988
ds_read_b32 v223, v10 offset:53244
BA 72,0
BA 76,0
BA 96,256
BA 100,256
BA 120,512
BA 124,512
BA 144,768
BA 148,768
BV 64,1024
BV 68,1024
BV 88,1280
WL 9
BV 92,1280
BV 112,1536
BV 116,1536
BV 136,1792
BV 140,1792
BV 160,2048
WL 9
BV 164,2048
BV 184,2304
BV 188,2304
BV 208,2560
BV 212,2560
BV 232,2816
WL 9
BV 236,2816
WV 24
UL 192,168
WV 23
UL 196,169
WV 22
UL 200,170
UH 193,168
UH 197,169
UH 201,170
v_mov_b32 v217, 0
H 2
v_add_f32 v220, v220, v221
v_add_f32 v222, v222, v223
v_add_f32 v220, v220, v222
v_mul_f32 v218, 0x3a2aaaab, v220
v_cmp_nlt_f32 s[44:45], v220, v219
CV 218,218,218
UL 218,218
v_add_f32 v218, 0x358637bd, v218
H 3
CV 218,218,218
UL 218,218
v_rcp_f32 v218, v218
s_nop 0
v_mul_f32 v174, v218, v174
v_mul_f32 v178, v218, v178
v_mul_f32 v182, v218, v182
v_mul_f32 v175, v218, v175
v_mul_f32 v179, v218, v179
mw 48
ad 86,48
DR 0
v_mul_f32 v183, v218, v183
CV 175,174,175
CV 179,178,179
CV 183,182,183
UL 174,175
UL 178,179
UL 182,183
DR 1024
UH 175,175
UH 179,179
UH 183,183
v_fma_f32 v174, v192, v174, 1.0
v_fma_f32 v178, v196, v178, 1.0
v_fma_f32 v182, v200, v182, 1.0
v_fma_f32 v175, v193, v175, 1.0
DR 2048
v_fma_f32 v179, v197, v179, 1.0
v_fma_f32 v183, v201, v183, 1.0
v_cvt_pk_f16_f32 v192, v174, v175
v_cvt_pk_f16_f32 v196, v178, v179
v_cvt_pk_f16_f32 v200, v182, v183
ds_write_b32 v206, v192 offset:53248
ds_write_b32 v206, v196 offset:1024
ds_write_b32 v206, v200 offset:2048
v_max3_f32 v217, |v174|, |v175|, v217
v_max3_f32 v217, |v178|, |v179|, v217
v_max3_f32 v217, |v182|, |v183|, v217
mw 51
ad 86,51
DR 0
v_cmp_lt_f32 vcc, 0x43000000, v217
WL 0
WV 18
s_barrier
RS 14,0
RS 18,64
TY 22,0
TZ 24,0
TY 26,14336
TZ 28,14336
TY 30,1024
TZ 32,1024
TY 34,15360
TZ 36,15360
R 0
R 2
R1 4
WL 9
R1 5
ds_read_b128 v[104:107], v128
ds_read_b128 v[108:111], v129
FG
PM 22,14
PM 23,15
PM 24,16
PM 25,17
PM 26,18
PM 27,19
PM 28,20
DR 1024
PM 29,21
AW 24,22
AW 25,23
AW 26,24
AW 27,25
AW 28,26
AW 29,27
DR 2048
AW 30,28
AW 31,29
WL 8
PM 30,14
PM 31,15
PM 32,16
PM 33,17
PM 34,18
PM 35,19
PM 36,20
PM 37,21
AW 32,30
AW 33,31
AW 34,32
AW 35,33
AW 36,34
AW 37,35
AW 38,36
AW 39,37
WL 1
PM 80,104
PM 81,105
PM 82,106
PM 83,107
WL 0
PM 84,108
PM 85,109
PM 86,110
PM 87,111
s_mov_b32 s42, 0x1c001c00
v_pk_mul_f16 v80, v80, s42
v_pk_mul_f16 v81, v81, s42
v_pk_mul_f16 v82, v82, s42
v_pk_mul_f16 v83, v83, s42
v_pk_mul_f16 v84, v84, s42
v_pk_mul_f16 v85, v85, s42
v_pk_mul_f16 v86, v86, s42
v_pk_mul_f16 v87, v87, s42
global_store_dwordx4 v0, v[80:83], s[90:91] sc1
s_mov_b64 exec, 0xff
global_store_dwordx4 v0, v[84:87], s[90:91] offset:1024 sc1
s_mov_b64 exec, -1
M 0
mw 72
RS 14,128
RS 18,192
M 1
TY 38,2048
TZ 40,2048
M 2
ad 86,72
DR 0
M 3
R 6
M 12
DR 1024
TY 42,16384
M 13
DR 2048
TZ 44,16384
M 14
mw 75
TY 22,3072
TZ 24,3072
M 15
R 8
M 24
ad 86,75
DR 0
M 25
DR 1024
TY 26,17408
M 26
WL 5
DR 2048
PM 38,14
M 27
R 10
M 36
PM 39,15
PM 40,16
M 37
WV 20
PM 41,17
PM 42,18
M 38
WL 0
s_barrier
PM 43,19
PM 44,20
M 39
R 24
M 48
mw 0
PM 45,21
AW 160,38
M 49
ad 86,96
DR 0
M 50
DR 1024
AW 161,39
M 51
R 26
M 60
DR 2048
AW 162,40
M 61
mw 3
AW 163,41
AW 164,42
M 62
ad 86,99
DR 0
M 63
WL 2
R 28
M 72
DR 1024
AW 165,43
M 73
DR 2048
AW 166,44
M 74
bc 49,3,66,60
AW 167,45
M 75
WL 2
R 30
M 84
mh 12
TZ 28,17408
BA 80,0
M 85
ad 66,288
H 0
M 86
H 1
BA 104,256
M 87
WL 5
R 32
M 96
BA 128,512
BA 152,768
M 97
WL 6
PM 22,14
PM 23,15
BA 84,0
M 98
PM 24,16
PM 25,17
BA 108,256
M 99
R 34
BA 132,512
M 108
PM 26,18
PM 27,19
BA 156,768
M 109
WV 16
PM 28,20
PM 29,21
M 110
WL 2
s_barrier
AW 168,22
AW 169,23
TY 30,4096
TZ 32,4096
M 111
R 48
TY 34,18432
TZ 36,18432
M 120
mw 24
AW 170,24
AW 171,25
TY 38,5120
M 121
ad 86,120
DR 0
TZ 40,5120
M 132
DR 1024
AW 172,26
TY 42,19456
)"
R"(M 133
WL 3
DR 2048
AW 173,27
TZ 44,19456
M 122
mw 27
AW 174,28
AW 175,29
PM 30,14
PM 31,15
M 123
R 50
PM 32,16
PM 33,17
M 134
ad 86,123
DR 0
PM 34,18
PM 35,19
M 135
R 52
PM 36,20
PM 37,21
MA 72,0,160
DR 1024
AW 176,30
AW 177,31
AW 178,32
MA 72,4,164
DR 2048
AW 179,33
AW 180,34
AW 181,35
MA 76,0,168
WL 4
AW 182,36
AW 183,37
PM 38,14
MA 76,4,172
PM 39,15
PM 40,16
PM 41,17
MA 80,0,176
PM 42,18
PM 43,19
PM 44,20
PM 45,21
AW 184,38
AW 185,39
AW 186,40
AW 187,41
AW 188,42
AW 189,43
AW 190,44
AW 191,45
MA 80,4,180
RS 14,256
RS 18,320
TY 22,6144
MA 84,0,184
TZ 24,6144
TY 26,20480
TZ 28,20480
MA 84,4,188
WL 6
R 54
MA 96,8,160
TY 30,7168
TZ 32,7168
MA 96,12,164
TY 34,21504
TZ 36,21504
MA 100,8,168
BV 72,1024
BV 96,1280
MA 100,12,172
WL 8
PM 22,14
PM 23,15
PM 24,16
MA 104,8,176
PM 25,17
PM 26,18
PM 27,19
MA 104,12,180
PM 28,20
PM 29,21
AW 24,22
MA 108,8,184
AW 25,23
AW 26,24
AW 27,25
MA 108,12,188
R 56
AW 28,26
MA 120,16,160
AW 29,27
AW 30,28
AW 31,29
MA 120,20,164
WL 4
PM 30,14
PM 31,15
MA 124,16,168
PM 32,16
PM 33,17
MA 124,20,172
PM 34,18
PM 35,19
MA 128,16,176
PM 36,20
PM 37,21
MA 128,20,180
AW 32,30
AW 33,31
MA 132,16,184
AW 34,32
AW 35,33
MA 132,20,188
R 58
MA 144,0,160
AW 36,34
AW 37,35
MA 144,4,164
AW 38,36
AW 39,37
MA 148,0,168
BV 120,1536
BV 144,1792
MA 148,4,172
BV 76,1024
BV 100,1280
MA 152,0,176
WV 14
BV 124,1536
BV 148,1792
MA 152,4,180
WL 6
BV 168,2048
BV 192,2304
MA 156,0,184
s_barrier
v_mov_b32 v10, 1
s_mov_b64 exec, 1
FS
s_mov_b64 exec, -1
MA 156,4,188
R 72
MV 64,8,160
mw 48
TY 38,8192
TZ 40,8192
MV 64,12,164
ad 86,144
DR 0
MV 68,8,168
TY 42,22528
TZ 44,22528
MV 68,12,172
DR 1024
TY 22,9216
MV 72,8,176
WL 9
TZ 24,9216
TY 26,23552
MV 72,12,180
DR 2048
TZ 28,23552
MV 76,8,184
BV 216,2560
BV 240,2816
MV 76,12,188
WL 6
R 74
BV 172,2048
MV 88,16,160
mw 51
PM 38,14
PM 39,15
BV 196,2304
MV 88,20,164
ad 86,147
DR 0
BV 220,2560
MV 92,16,168
PM 40,16
PM 41,17
BV 244,2816
MV 92,20,172
DR 1024
PM 42,18
MV 96,16,176
PM 43,19
PM 44,20
MV 96,20,180
DR 2048
PM 45,21
MV 100,16,184
AW 40,38
AW 41,39
MV 100,20,188
R 76
MV 112,0,160
bc 48,3,64,56
AW 42,40
MV 112,4,164
mh 0
AW 43,41
AW 44,42
MV 116,0,168
ad 64,288
H 0
MV 116,4,172
AW 45,43
AW 46,44
MV 120,0,176
H 1
AW 47,45
MV 120,4,180
WL 3
PM 22,14
PM 23,15
MV 124,0,184
H 2
PM 24,16
MV 124,4,188
R 78
MV 136,8,160
H 3
PM 25,17
MV 136,12,164
PM 26,18
PM 27,19
MV 140,8,168
bc 49,4,66,60
PM 28,20
MV 140,12,172
mh 4
PM 29,21
AW 48,22
MV 144,8,176
ad 66,384
H 0
MV 144,12,180
AW 49,23
AW 50,24
MV 148,8,184
H 1
AW 51,25
MV 148,12,188
WL 2
R 80
MV 160,16,160
AW 52,26
AW 53,27
MV 160,20,164
AW 54,28
AW 55,29
MV 164,16,168
TY 30,10240
TZ 32,10240
MV 164,20,172
TY 34,24576
TZ 36,24576
MV 168,16,176
TY 38,11264
TZ 40,11264
MV 168,20,180
TY 42,25600
TZ 44,25600
MV 172,16,184
BA 88,0
BA 112,256
MV 172,20,188
WL 8
R 82
MV 184,0,160
WL 8
PM 30,14
PM 31,15
MV 184,4,164
PM 32,16
PM 33,17
MV 188,0,168
PM 34,18
PM 35,19
MV 188,4,172
PM 36,20
PM 37,21
MV 192,0,176
WV 21
AW 56,30
AW 57,31
MV 192,4,180
WL 0
AW 58,32
AW 59,33
MV 196,0,184
s_barrier
AW 60,34
AW 61,35
MV 196,4,188
R 0
MV 208,8,160
mw 72
AW 62,36
AW 63,37
MV 208,12,164
ad 86,168
DR 0
MV 232,16,160
PM 38,14
PM 39,15
MV 232,20,164
DR 1024
PM 40,16
MV 212,8,168
PM 41,17
PM 42,18
MV 212,12,172
DR 2048
PM 43,19
MV 236,16,168
PM 44,20
PM 45,21
MV 236,20,172
mw 75
AW 64,38
AW 65,39
MV 216,8,176
ad 86,171
DR 0
MV 216,12,180
AW 66,40
AW 67,41
MV 240,16,176
DR 1024
AW 68,42
MV 240,20,180
AW 69,43
AW 70,44
MV 220,8,184
DR 2048
AW 71,45
MV 220,12,188
R 2
MV 244,16,184
bc 48,4,64,56
RS 14,384
MV 244,20,188
WL 3
R 4
M 0
mh 6
RS 18,448
BA 136,512
M 1
BV 56,768
M 2
ad 64,384
H 0
M 3
BA 92,0
BA 116,256
M 4
H 1
BA 140,512
M 5
BV 60,768
M 6
H 2
M 7
M 8
H 3
M 9
WL 3
M 10
M 11
R 6
M 12
M 13
M 14
M 15
M 16
BV 80,1024
BV 104,1280
M 17
BV 128,1536
BV 152,1792
M 18
BV 84,1024
BV 108,1280
M 19
BV 132,1536
BV 156,1792
M 20
v_lshrrev_b32 v9, 8, v0
v_bfe_u32 v8, v0, 4, 4
M 21
)"
R"(WL 12
v_lshlrev_b32 v10, 2, v9
M 22
s_mov_b32 s42, 0xcb527610
M 23
R 8
M 24
s_movk_i32 s43, 0xc00
v_lshlrev_b32 v11, 3, v8
v_bfe_u32 v3, s42, v10, 4
M 25
v_add_u32 v10, 16, v10
v_mad_u32_u24 v3, v3, s43, v11
M 26
v_bfe_u32 v50, s42, v10, 4
v_add_u32 v10, 1, v9
M 27
v_mad_u32_u24 v50, v50, s43, v11
v_and_b32 v10, 2, v10
M 28
v_sub_u32 v10, 1, v10
v_mul_i32_i24 v51, s43, v10
M 29
M 30
M 31
M 32
M 33
WL 7
M 34
M 35
R 10
M 36
M 37
M 38
M 39
M 40
M 41
M 42
M 43
M 44
WV 23
M 45
WL 0
s_barrier
TY 22,12288
TZ 24,12288
M 46
TY 26,26624
TZ 28,26624
M 47
R 24
M 48
M 49
M 50
M 51
WL 2
PM 22,14
PM 23,15
M 52
PM 24,16
PM 25,17
M 53
PM 26,18
PM 27,19
M 54
PM 28,20
PM 29,21
M 55
AW 160,22
AW 161,23
M 56
AW 162,24
AW 163,25
M 57
AW 164,26
AW 165,27
M 58
AW 166,28
AW 167,29
M 59
R 26
M 60
M 61
M 62
M 63
M 64
BV 176,2048
BV 200,2304
M 65
BV 224,2560
BV 248,2816
M 66
v_lshlrev_b32 v7, 4, v0
BV 180,2048
M 67
FL
TY 30,13312
M 68
TZ 32,13312
TY 34,27648
M 69
TZ 36,27648
BV 204,2304
M 70
WL 8
BV 228,2560
BV 252,2816
M 71
R 28
M 72
M 73
M 74
WL 5
PM 30,14
PM 31,15
M 75
PM 32,16
PM 33,17
M 76
PM 34,18
PM 35,19
M 77
PM 36,20
PM 37,21
M 78
AW 168,30
AW 169,31
M 79
AW 170,32
AW 171,33
M 80
AW 172,34
AW 173,35
M 81
AW 174,36
AW 175,37
M 82
M 83
R 30
M 84
M 85
bc 49,5,66,60
M 86
mh 10
M 87
ad 66,480
H 0
M 88
M 89
M 90
M 91
H 1
M 92
M 93
M 94
M 95
WL 2
R 32
M 96
M 97
M 98
M 99
M 100
M 101
M 102
M 103
M 104
M 105
M 106
M 107
WL 2
R 34
M 108
M 109
M 110
WV 2
v_cmp_ne_u32 vcc, 1, v6
M 111
M 112
M 113
FB
M 114
M 115
M 116
WL 0
M 117
s_barrier
TY 38,0
TZ 40,0
TY 42,14336
M 118
mw 0
TZ 44,14336
TY 22,1024
TZ 24,1024
M 119
R 48
TY 26,15360
M 120
ad 84,192
W 0
TZ 28,15360
M 121
W 1
TY 30,2048
TZ 32,2048
M 132
WL 8
W 2
TY 34,16384
TZ 36,16384
M 133
mw 3
PM 38,14
PM 39,15
PM 40,16
M 122
ad 84,195
W 0
PM 41,17
M 123
W 1
PM 42,18
PM 43,19
M 134
W 2
PM 44,20
PM 45,21
M 135
mw 24
AW 176,38
AW 177,39
AW 178,40
M 124
ad 84,216
W 0
AW 179,41
M 125
W 1
AW 180,42
AW 181,43
M 136
WL 1
W 2
AW 182,44
AW 183,45
M 137
mw 27
PM 22,14
PM 23,15
PM 24,16
M 126
ad 84,219
W 0
PM 25,17
M 127
W 1
PM 26,18
PM 27,19
M 138
W 2
PM 28,20
PM 29,21
M 139
AW 184,22
AW 185,23
AW 186,24
M 128
AW 187,25
AW 188,26
AW 189,27
M 129
WL 0
AW 190,28
AW 191,29
PM 30,14
M 140
PM 31,15
PM 32,16
PM 33,17
M 141
PM 34,18
PM 35,19
PM 36,20
M 130
PM 37,21
AW 192,30
AW 193,31
M 131
R 50
AW 194,32
M 142
AW 195,33
AW 196,34
AW 197,35
M 143
R 52
AW 198,36
MA 72,0,160
AW 199,37
TY 38,3072
TZ 40,3072
MA 72,4,164
TY 42,17408
TZ 44,17408
MA 76,0,168
MA 76,4,172
MA 80,0,176
MA 80,4,180
MA 84,0,184
WL 0
PM 38,14
PM 39,15
PM 40,16
MA 84,4,188
PM 41,17
PM 42,18
PM 43,19
MA 88,0,192
PM 44,20
PM 45,21
AW 200,38
AW 201,39
AW 202,40
AW 203,41
AW 204,42
AW 205,43
AW 206,44
AW 207,45
MA 88,4,196
MA 92,0,200
MA 92,4,204
R 54
MA 96,8,160
MA 96,12,164
MA 100,8,168
MA 100,12,172
MA 104,8,176
MA 104,12,180
MA 108,8,184
MA 108,12,188
MA 112,8,192
MA 112,12,196
MA 116,8,200
MA 116,12,204
R 56
MA 120,16,160
MA 120,20,164
MA 124,16,168
MA 124,20,172
MA 128,16,176
MA 128,20,180
MA 132,16,184
MA 132,20,188
MA 136,16,192
MA 136,20,196
MA 140,16,200
MA 140,20,204
WL 2
R 58
MA 144,0,160
MA 144,4,164
MA 148,0,168
MA 148,4,172
MA 152,0,176
MA 152,4,180
MA 156,0,184
MA 156,4,188
MV 56,0,192
MV 56,4,196
WL 0
s_barrier
MV 60,0,200
mw 48
MV 60,4,204
R 72
MV 64,8,160
ad 84,240
W 0
MV 64,12,164
v_lshrrev_b32 v8, 1, v0
s_mov_b64 exec, s[76:77]
global_load_dwordx2 v[6:7], v8, s[78:79] sc1
s_mov_b64 exec, -1
MV 68,8,168
MV 68,12,172
MV 72,8,176
W 1
MV 72,12,180
MV 76,8,184
MV 76,12,188
MV 80,8,192
W 2
MV 80,12,196
MV 84,8,200
MV 84,12,204
R 74
MV 88,16,160
mw 51
MV 88,20,164
)"
R"(ad 84,243
W 0
MV 92,16,168
MV 92,20,172
MV 96,16,176
MV 96,20,180
W 1
MV 100,16,184
MV 100,20,188
MV 104,16,192
MV 104,20,196
W 2
MV 108,16,200
MV 108,20,204
WL 2
R 76
MV 112,0,160
MV 112,4,164
bc 48,5,64,56
MV 116,0,168
mh 12
MV 116,4,172
ad 64,480
H 0
MV 120,0,176
MV 120,4,180
MV 124,0,184
MV 124,4,188
H 1
MV 128,0,192
MV 128,4,196
MV 132,0,200
MV 132,4,204
WL 2
R 78
MV 136,8,160
mh 0
MV 136,12,164
ad 64,482
H 0
MV 140,8,168
MV 140,12,172
MV 144,8,176
MV 144,12,180
H 1
MV 148,8,184
MV 148,12,188
MV 152,8,192
MV 152,12,196
bc 49,6,66,60
MV 156,8,200
mh 2
MV 156,12,204
WL 2
R 80
MV 160,16,160
ad 66,576
H 0
MV 160,20,164
MV 164,16,168
MV 164,20,172
MV 168,16,176
H 1
MV 168,20,180
MV 172,16,184
MV 172,20,188
MV 176,16,192
MV 176,20,196
MV 180,16,200
MV 180,20,204
WL 2
R 82
MV 184,0,160
MV 184,4,164
MV 188,0,168
MV 188,4,172
MV 192,0,176
MV 192,4,180
MV 196,0,184
MV 196,4,188
MV 200,0,192
WV 19
MV 200,4,196
WL 0
s_barrier
T 24,4
MV 204,0,200
mw 72
T 28,18
MV 204,4,204
R 0
MV 208,8,160
ad 84,264
W 0
MV 208,12,164
T 32,5
MV 232,16,160
T 36,19
MV 232,20,164
MV 212,8,168
W 1
MV 212,12,172
T 40,6
MV 236,16,168
T 44,20
MV 236,20,172
MV 216,8,176
W 2
MV 216,12,180
WL 8
T 48,7
MV 240,16,176
T 52,21
MV 240,20,180
ae 44,70,0
MV 220,8,184
mw 75
MV 220,12,188
T 56,8
MV 244,16,184
WL 8
T 60,22
MV 244,20,188
ad 84,267
W 0
MV 224,8,192
s_bitcmp1_b32 s50, 0
s_cselect_b32 s51, 24576, 0
v_add_u32 v52, s51, v3
MV 224,12,196
T 64,9
MV 248,16,192
T 68,23
MV 248,20,196
W 1
XL 14,0
MV 228,8,200
XL 16,768
MV 228,12,204
WL 8
R 2
MV 252,16,200
XL 18,1536
MV 252,20,204
R 4
M 0
W 2
M 1
M 2
XL 20,2304
M 3
M 4
M 5
WV 21
v_or_b32 v6, v6, v7
s_mov_b64 exec, s[76:77]
v_cmp_ne_u32 %[flg], 0, v6
s_mov_b64 exec, -1
M 6
M 7
WL 8
M 8
M 9
WL 4
M 10
M 11
WL 2
R 6
M 12
M 13
M 14
M 15
M 16
M 17
M 18
M 19
M 20
M 21
M 22
M 23
WL 2
R 8
M 24
M 25
M 26
M 27
M 28
M 29
M 30
M 31
M 32
M 33
M 34
M 35
WL 2
R 10
M 36
M 37
M 38
M 39
M 40
M 41
M 42
M 43
M 44
M 45
WL 0
s_barrier
M 46
mw 0
M 47
R 24
M 48
ad 84,288
W 0
M 49
M 50
M 51
M 52
W 1
M 53
M 54
M 55
M 56
W 2
M 57
M 58
M 59
R 26
M 60
mw 3
M 61
ad 84,291
W 0
M 62
M 63
M 64
M 65
W 1
M 66
M 67
M 68
M 69
W 2
M 70
M 71
WL 2
R 28
M 72
M 73
bc 48,6,64,56
M 74
mh 4
M 75
ad 64,576
H 0
M 76
M 77
M 78
M 79
H 1
M 80
M 81
M 82
M 83
WL 2
R 30
M 84
H 2
M 85
M 86
M 87
M 88
H 3
M 89
M 90
M 91
M 92
bc 49,7,66,60
M 93
mh 8
M 94
ad 66,672
H 0
M 95
WL 2
R 32
M 96
M 97
M 98
H 1
M 99
M 100
M 101
M 102
M 103
M 104
M 105
M 106
M 107
WL 2
R 34
M 108
M 109
M 110
M 111
M 112
M 113
M 114
M 115
M 116
WV 24
M 117
WL 0
s_barrier
M 118
mw 24
M 119
R 48
M 120
ad 84,312
W 0
M 121
M 132
M 133
T 24,10
M 122
T 28,24
M 123
W 1
M 134
M 135
T 32,11
M 124
T 36,25
M 125
W 2
M 136
ae 44,70,0
M 137
s_bfe_u32 s51, s50, 0x10001
T 40,12
M 126
T 44,26
M 127
mw 27
v_mad_i32_i24 v52, v51, s51, v50
XL 22,0
M 138
ad 84,315
W 0
M 139
WL 8
T 48,13
M 128
T 52,27
M 129
XL 24,768
M 140
W 1
M 141
T 56,0
M 130
WL 8
T 60,14
M 131
R 50
M 142
W 2
XL 26,1536
M 143
T 64,1
M 0
WL 8
T 68,15
M 1
R 52
M 2
M 3
M 4
XL 28,2304
M 5
M 6
M 7
WL 8
M 8
M 9
WL 2
M 10
M 11
R 54
M 12
M 13
M 14
M 15
M 16
M 17
M 18
M 19
M 20
)"
R"(M 21
M 22
M 23
WL 2
R 56
M 24
M 25
M 26
M 27
M 28
M 29
M 30
M 31
M 32
M 33
M 34
M 35
WL 2
R 58
M 36
M 37
M 38
M 39
M 40
WV 24
UL 6,14
UH 7,14
M 41
UL 8,15
UH 9,15
M 42
AR 10,72
AR 11,96
M 43
WV 23
AR 12,120
AR 13,144
M 44
WL 0
AD 10,6
AD 11,7
M 45
s_barrier
AD 12,8
AD 13,9
M 46
mw 48
AW 72,10
AW 96,11
M 47
R 72
M 48
ad 84,336
W 0
M 49
AW 120,12
AW 144,13
M 50
UL 6,16
UH 7,16
M 51
UL 8,17
UH 9,17
M 52
W 1
AR 10,73
M 53
AR 11,97
AR 12,121
M 54
AR 13,145
AD 10,6
M 55
AD 11,7
AD 12,8
M 56
W 2
AD 13,9
M 57
AW 73,10
AW 97,11
M 58
AW 121,12
AW 145,13
M 59
R 74
M 60
mw 51
UL 6,18
UH 7,18
M 61
ad 84,339
W 0
M 62
UL 8,19
UH 9,19
M 63
AR 10,74
AR 11,98
M 64
AR 12,122
AR 13,146
M 65
W 1
AD 10,6
M 66
AD 11,7
AD 12,8
M 67
AD 13,9
AW 74,10
M 68
AW 98,11
AW 122,12
M 69
W 2
AW 146,13
M 70
WV 28
UL 6,20
UH 7,20
M 71
WL 0
R 76
M 72
UL 8,21
UH 9,21
M 73
bc 48,7,64,56
AR 10,75
M 74
mh 10
AR 11,99
AR 12,123
M 75
ad 64,672
H 0
M 76
AR 13,147
AD 10,6
M 77
AD 11,7
AD 12,8
M 78
AD 13,9
AW 75,10
M 79
H 1
AW 99,11
M 80
AW 123,12
AW 147,13
M 81
M 82
M 83
R 78
M 84
H 2
M 85
M 86
M 87
M 88
H 3
M 89
M 90
M 91
M 92
bc 49,8,66,60
M 93
mh 0
M 94
ad 66,768
H 0
M 95
WL 2
R 80
M 96
M 97
M 98
H 1
M 99
M 100
M 101
M 102
M 103
M 104
M 105
M 106
M 107
WL 2
R 82
M 108
M 109
M 110
M 111
M 112
M 113
M 114
M 115
M 116
WV 24
M 117
WL 0
s_barrier
M 118
mw 72
M 119
R 0
M 120
ad 84,360
W 0
M 121
M 132
M 133
T 24,2
M 122
T 28,16
M 123
W 1
M 134
M 135
T 32,3
M 124
T 36,17
M 125
W 2
M 136
M 137
T 40,4
M 126
T 44,18
M 127
mw 75
M 138
ad 84,363
W 0
M 139
WL 8
T 48,5
M 128
T 52,19
M 129
ae 44,68,0
M 140
W 1
M 141
T 56,6
M 130
WL 8
T 60,20
M 131
R 2
M 142
W 2
M 143
T 64,7
M 0
WL 8
T 68,21
M 1
R 4
M 2
s_bitcmp1_b32 s50, 2
s_cselect_b32 s51, 24576, 0
M 3
v_add_u32 v52, s51, v3
XL 30,0
M 4
XL 32,768
M 5
XL 34,1536
M 6
XL 36,2304
M 7
WL 8
M 8
M 9
WL 2
M 10
M 11
R 6
M 12
M 13
M 14
M 15
M 16
M 17
M 18
M 19
M 20
M 21
M 22
M 23
WL 2
R 8
M 24
M 25
M 26
M 27
M 28
M 29
M 30
M 31
M 32
M 33
M 34
M 35
WL 2
R 10
M 36
M 37
M 38
M 39
M 40
M 41
M 42
WV 28
UL 6,22
UH 7,22
M 43
WV 24
UL 8,23
UH 9,23
M 44
WL 0
AR 10,76
AR 11,100
M 45
s_barrier
AR 12,124
AR 13,148
M 46
mw 0
AD 10,6
AD 11,7
M 47
R 24
M 48
ad 84,384
W 0
M 49
AD 12,8
AD 13,9
M 50
AW 76,10
AW 100,11
M 51
AW 124,12
AW 148,13
M 52
W 1
UL 6,24
M 53
UH 7,24
UL 8,25
M 54
UH 9,25
AR 10,77
M 55
AR 11,101
AR 12,125
M 56
W 2
AR 13,149
M 57
AD 10,6
AD 11,7
M 58
AD 12,8
AD 13,9
M 59
R 26
M 60
mw 3
AW 77,10
AW 101,11
M 61
ad 84,387
W 0
M 62
AW 125,12
AW 149,13
M 63
WV 26
UL 6,26
UH 7,26
M 64
UL 8,27
UH 9,27
M 65
W 1
AR 10,78
M 66
AR 11,102
AR 12,126
M 67
AR 13,150
AD 10,6
M 68
AD 11,7
)"
R"(AD 12,8
M 69
W 2
AD 13,9
M 70
AW 78,10
AW 102,11
M 71
WL 0
R 28
M 72
AW 126,12
AW 150,13
M 73
bc 48,8,64,56
UL 6,28
M 74
mh 2
UH 7,28
UL 8,29
M 75
ad 64,768
H 0
M 76
UH 9,29
AR 10,79
M 77
AR 11,103
AR 12,127
M 78
AR 13,151
AD 10,6
M 79
H 1
AD 11,7
M 80
AD 12,8
AD 13,9
M 81
AW 79,10
AW 103,11
M 82
AW 127,12
AW 151,13
M 83
R 30
M 84
H 2
M 85
M 86
M 87
M 88
H 3
M 89
M 90
M 91
M 92
bc 49,9,66,60
M 93
mh 6
M 94
ad 66,864
H 0
M 95
WL 2
R 32
M 96
M 97
M 98
H 1
M 99
M 100
M 101
M 102
M 103
M 104
M 105
M 106
M 107
WL 2
R 34
M 108
M 109
M 110
M 111
M 112
M 113
M 114
M 115
M 116
WV 24
M 117
WL 0
s_barrier
M 118
mw 24
M 119
R 48
M 120
ad 84,408
W 0
M 121
M 132
M 133
T 24,8
M 122
T 28,22
M 123
W 1
M 134
M 135
T 32,9
M 124
T 36,23
M 125
W 2
M 136
M 137
T 40,10
M 126
T 44,24
M 127
mw 27
M 138
ad 84,411
W 0
M 139
WL 8
T 48,11
M 128
T 52,25
M 129
ae 44,68,0
M 140
s_bfe_u32 s51, s50, 0x10003
W 1
v_mad_i32_i24 v52, v51, s51, v50
M 141
T 56,12
M 130
WL 8
T 60,26
M 131
R 50
M 142
W 2
XL 38,0
M 143
T 64,13
M 0
WL 8
T 68,27
M 1
R 52
M 2
XL 40,768
M 3
XL 42,1536
M 4
M 5
M 6
M 7
WL 8
M 8
XL 44,2304
M 9
WL 2
M 10
M 11
R 54
M 12
M 13
M 14
M 15
M 16
M 17
M 18
M 19
M 20
M 21
M 22
M 23
WL 2
R 56
M 24
M 25
M 26
M 27
M 28
M 29
M 30
M 31
M 32
M 33
M 34
M 35
WL 2
R 58
M 36
M 37
M 38
M 39
M 40
M 41
M 42
M 43
M 44
WV 22
UL 6,30
UH 7,30
M 45
WL 0
s_barrier
UL 8,31
UH 9,31
M 46
mw 48
AR 10,80
AR 11,104
M 47
R 72
M 48
ad 84,432
W 0
M 49
AR 12,128
AR 13,152
M 50
AD 10,6
AD 11,7
M 51
AD 12,8
AD 13,9
M 52
W 1
AW 80,10
M 53
AW 104,11
AW 128,12
M 54
AW 152,13
UL 6,32
M 55
UH 7,32
UL 8,33
M 56
W 2
UH 9,33
M 57
AR 10,81
AR 11,105
M 58
AR 12,129
AR 13,153
M 59
R 74
M 60
mw 51
AD 10,6
AD 11,7
M 61
ad 84,435
W 0
M 62
AD 12,8
AD 13,9
M 63
AW 81,10
AW 105,11
M 64
AW 129,12
AW 153,13
M 65
W 1
UL 6,34
M 66
UH 7,34
UL 8,35
M 67
UH 9,35
AR 10,82
M 68
AR 11,106
AR 12,130
M 69
W 2
AR 13,154
M 70
AD 10,6
AD 11,7
M 71
WL 0
R 76
M 72
AD 12,8
AD 13,9
M 73
bc 48,9,64,56
AW 82,10
M 74
mh 8
AW 106,11
AW 130,12
M 75
ad 64,864
H 0
M 76
AW 154,13
UL 6,36
M 77
UH 7,36
UL 8,37
M 78
UH 9,37
AR 10,83
M 79
H 1
AR 11,107
M 80
AR 12,131
AR 13,155
M 81
AD 10,6
AD 11,7
M 82
AD 12,8
AD 13,9
M 83
R 78
M 84
H 2
AW 83,10
M 85
AW 107,11
AW 131,12
M 86
AW 155,13
M 87
M 88
H 3
M 89
M 90
M 91
M 92
bc 49,10,66,60
M 93
mh 12
M 94
ad 66,960
H 0
M 95
WL 2
R 80
M 96
M 97
M 98
H 1
M 99
M 100
M 101
M 102
M 103
M 104
M 105
M 106
M 107
WL 2
R 82
M 108
M 109
M 110
M 111
M 112
M 113
M 114
M 115
M 116
WV 24
M 117
WL 0
s_barrier
M 118
mw 72
M 119
R 0
M 120
ad 84,456
W 0
M 121
M 132
M 133
T 24,0
M 122
T 28,14
M 123
W 1
M 134
M 135
T 32,1
M 124
)"
R"(T 36,15
M 125
W 2
M 136
M 137
T 40,2
M 126
T 44,16
M 127
mw 75
M 138
ad 84,459
W 0
M 139
WL 8
T 48,3
M 128
T 52,17
M 129
ae 44,68,48
M 140
W 1
M 141
T 56,4
M 130
WL 8
T 60,18
M 131
R 2
M 142
W 2
M 143
T 64,5
M 0
WL 8
T 68,19
M 1
R 4
M 2
s_bitcmp1_b32 s50, 4
s_cselect_b32 s51, 24576, 0
M 3
v_add_u32 v52, s51, v3
XL 46,0
M 4
XL 48,768
M 5
XL 14,1536
M 6
M 7
WL 8
M 8
M 9
WL 2
M 10
XL 16,2304
M 11
R 6
M 12
M 13
M 14
M 15
M 16
M 17
M 18
M 19
M 20
M 21
M 22
M 23
WL 2
R 8
M 24
M 25
M 26
M 27
M 28
M 29
M 30
M 31
M 32
M 33
M 34
M 35
WL 2
R 10
M 36
M 37
M 38
M 39
M 40
M 41
M 42
M 43
M 44
WV 22
M 45
WL 0
s_barrier
M 46
mw 0
UL 6,38
UH 7,38
M 47
R 24
M 48
ad 84,480
W 0
M 49
UL 8,39
UH 9,39
M 50
AR 10,84
AR 11,108
M 51
AR 12,132
AR 13,156
M 52
W 1
AD 10,6
M 53
AD 11,7
AD 12,8
M 54
AD 13,9
AW 84,10
M 55
AW 108,11
AW 132,12
M 56
W 2
AW 156,13
M 57
UL 6,40
UH 7,40
M 58
UL 8,41
UH 9,41
M 59
R 26
M 60
mw 3
AR 10,85
AR 11,109
M 61
ad 84,483
W 0
M 62
AR 12,133
AR 13,157
M 63
AD 10,6
AD 11,7
M 64
AD 12,8
AD 13,9
M 65
W 1
AW 85,10
M 66
AW 109,11
AW 133,12
M 67
AW 157,13
UL 6,42
M 68
UH 7,42
UL 8,43
M 69
W 2
UH 9,43
M 70
AR 10,86
AR 11,110
M 71
WL 0
R 28
M 72
AR 12,134
AR 13,158
M 73
bc 48,10,64,56
AD 10,6
M 74
mh 0
AD 11,7
AD 12,8
M 75
ad 64,960
H 0
M 76
AD 13,9
AW 86,10
M 77
AW 110,11
AW 134,12
M 78
AW 158,13
UL 6,44
M 79
H 1
UH 7,44
M 80
UL 8,45
UH 9,45
M 81
AR 10,87
AR 11,111
M 82
AR 12,135
AR 13,159
M 83
R 30
M 84
H 2
AD 10,6
M 85
AD 11,7
AD 12,8
M 86
AD 13,9
AW 87,10
M 87
AW 111,11
AW 135,12
M 88
H 3
AW 159,13
M 89
M 90
M 91
M 92
bc 49,11,66,60
M 93
mh 4
M 94
ad 66,1056
H 0
M 95
WL 2
R 32
M 96
M 97
M 98
H 1
M 99
M 100
M 101
M 102
M 103
M 104
M 105
M 106
M 107
WL 2
R 34
M 108
M 109
M 110
M 111
M 112
M 113
M 114
M 115
M 116
WV 24
M 117
WL 0
s_barrier
M 118
mw 24
M 119
R 48
M 120
ad 84,504
W 0
M 121
M 132
M 133
T 24,6
M 122
T 28,20
M 123
W 1
M 134
M 135
T 32,7
M 124
T 36,21
M 125
W 2
M 136
M 137
T 40,8
M 126
T 44,22
M 127
mw 27
M 138
ad 84,507
W 0
M 139
WL 8
T 48,9
M 128
T 52,23
M 129
M 140
W 1
M 141
T 56,10
M 130
WL 8
T 60,24
M 131
R 50
M 142
W 2
M 143
T 64,11
M 0
WL 8
T 68,25
M 1
R 52
M 2
ae 44,68,48
M 3
M 4
s_bfe_u32 s51, s50, 0x10005
v_mad_i32_i24 v52, v51, s51, v50
XL 18,0
M 5
XL 20,768
M 6
XL 22,1536
M 7
WL 8
M 8
M 9
WL 2
M 10
M 11
R 54
M 12
XL 24,2304
M 13
M 14
M 15
M 16
M 17
M 18
M 19
M 20
M 21
M 22
M 23
WL 2
R 56
M 24
M 25
M 26
M 27
M 28
M 29
M 30
M 31
M 32
M 33
M 34
M 35
WL 2
R 58
M 36
M 37
M 38
M 39
M 40
M 41
M 42
M 43
M 44
WV 26
M 45
WL 0
s_barrier
M 46
mw 48
M 47
R 72
M 48
ad 84,528
W 0
M 49
WV 23
UL 6,46
UH 7,46
M 50
UL 8,47
UH 9,47
)"
R"(M 51
AR 10,88
AR 11,112
M 52
W 1
AR 12,136
M 53
AD 10,6
AD 11,7
M 54
AD 12,8
AD 56,9
M 55
AW 88,10
AW 112,11
M 56
W 2
AW 136,12
M 57
UL 6,48
UH 7,48
M 58
UL 8,49
UH 9,49
M 59
R 74
M 60
mw 51
AR 10,89
AR 11,113
M 61
ad 84,531
W 0
M 62
AR 12,137
AD 10,6
M 63
AD 11,7
AD 12,8
M 64
AD 57,9
AW 89,10
M 65
W 1
AW 113,11
M 66
AW 137,12
UL 6,14
M 67
UH 7,14
UL 8,15
M 68
UH 9,15
AR 10,90
M 69
W 2
AR 11,114
M 70
AR 12,138
AD 10,6
M 71
WL 0
R 76
M 72
AD 11,7
AD 12,8
M 73
bc 48,11,64,56
AD 58,9
M 74
mh 6
AW 90,10
AW 114,11
M 75
ad 64,1056
H 0
M 76
AW 138,12
UL 6,16
M 77
UH 7,16
UL 8,17
M 78
UH 9,17
AR 10,91
M 79
H 1
AR 11,115
M 80
AR 12,139
AD 10,6
M 81
AD 11,7
AD 12,8
M 82
AD 59,9
AW 91,10
M 83
R 78
M 84
H 2
AW 115,11
M 85
AW 139,12
M 86
M 87
M 88
H 3
M 89
M 90
M 91
M 92
bc 49,12,66,60
M 93
mh 10
M 94
ad 66,1152
H 0
M 95
WL 2
R 80
M 96
M 97
M 98
H 1
M 99
M 100
M 101
M 102
M 103
M 104
M 105
M 106
M 107
WL 2
R 82
M 108
M 109
M 110
M 111
M 112
M 113
M 114
M 115
M 116
WV 24
M 117
WL 0
s_barrier
M 118
mw 72
M 119
R 0
M 120
ad 84,552
W 0
M 121
M 132
M 133
T 24,12
M 122
T 28,26
M 123
W 1
M 134
M 135
T 32,13
M 124
T 36,27
M 125
W 2
M 136
M 137
T 40,0
M 126
T 44,14
M 127
mw 75
M 138
ad 84,555
W 0
M 139
WL 8
T 48,1
M 128
T 52,15
M 129
M 140
W 1
M 141
T 56,2
M 130
WL 8
T 60,16
M 131
R 2
M 142
W 2
M 143
T 64,3
M 0
WL 8
T 68,17
M 1
R 4
M 2
M 3
M 4
M 5
M 6
M 7
WL 8
M 8
M 9
WL 2
M 10
M 11
R 6
M 12
M 13
M 14
M 15
M 16
M 17
M 18
M 19
M 20
M 21
M 22
M 23
WL 2
R 8
M 24
M 25
M 26
M 27
M 28
M 29
M 30
M 31
M 32
ae 44,70,0
M 33
s_bitcmp1_b32 s50, 0
s_cselect_b32 s51, 24576, 0
v_add_u32 v52, s51, v3
M 34
XL 26,128
M 35
WL 2
R 10
M 36
M 37
M 38
XL 28,896
M 39
M 40
M 41
M 42
M 43
WV 24
M 44
WL 0
XL 30,1664
M 45
s_barrier
M 46
mw 0
M 47
R 24
M 48
ad 84,576
W 0
M 49
M 50
WV 22
XL 32,2432
UL 6,18
M 51
UH 7,18
UL 8,19
M 52
W 1
UH 9,19
M 53
AR 10,92
AR 11,116
M 54
AR 12,140
AD 10,6
M 55
AD 11,7
AD 12,8
M 56
W 2
AD 60,9
M 57
AW 92,10
AW 116,11
M 58
AW 140,12
UL 6,20
M 59
R 26
M 60
mw 3
UH 7,20
UL 8,21
M 61
ad 84,579
W 0
M 62
UH 9,21
AR 10,93
M 63
AR 11,117
AR 12,141
M 64
AD 10,6
AD 11,7
M 65
W 1
AD 12,8
M 66
AD 61,9
AW 93,10
M 67
AW 117,11
AW 141,12
M 68
UL 6,22
UH 7,22
M 69
W 2
UL 8,23
M 70
UH 9,23
AR 10,94
M 71
WL 0
R 28
M 72
AR 11,118
AR 12,142
M 73
bc 48,12,64,56
AD 10,6
M 74
mh 12
AD 11,7
AD 12,8
M 75
ad 64,1152
H 0
M 76
AD 62,9
AW 94,10
M 77
AW 118,11
AW 142,12
M 78
UL 6,24
UH 7,24
M 79
H 1
UL 8,25
M 80
UH 9,25
AR 10,95
M 81
AR 11,119
AR 12,143
M 82
AD 10,6
AD 11,7
M 83
R 30
M 84
mh 0
AD 12,8
AD 63,9
M 85
ad 64,1154
H 0
M 86
AW 95,10
AW 119,11
M 87
AW 143,12
M 88
M 89
H 1
M 90
M 91
M 92
M 93
bc 49,13,66,60
M 94
mh 2
M 95
)"
R"(WL 2
R 32
M 96
ad 66,1248
H 0
M 97
M 98
M 99
M 100
H 1
M 101
M 102
M 103
M 104
M 105
M 106
M 107
WL 2
R 34
M 108
M 109
M 110
M 111
M 112
M 113
M 114
M 115
M 116
WV 24
M 117
WL 0
s_barrier
M 118
mw 24
M 119
R 48
M 120
ad 84,600
W 0
M 121
M 132
M 133
T 24,4
M 122
T 28,18
M 123
W 1
M 134
M 135
T 32,5
M 124
T 36,19
M 125
W 2
M 136
M 137
T 40,6
M 126
T 44,20
M 127
mw 27
M 138
ad 84,603
W 0
M 139
WL 8
T 48,7
M 128
T 52,21
M 129
M 140
W 1
M 141
T 56,8
M 130
WL 8
T 60,22
M 131
R 50
M 142
W 2
M 143
T 64,9
M 0
WL 8
T 68,23
M 1
R 52
M 2
M 3
M 4
M 5
M 6
M 7
WL 8
M 8
M 9
WL 2
M 10
M 11
R 54
M 12
M 13
M 14
M 15
M 16
M 17
M 18
M 19
M 20
M 21
M 22
M 23
WL 2
R 56
M 24
M 25
M 26
M 27
M 28
M 29
M 30
M 31
M 32
M 33
M 34
ae 44,70,0
M 35
WL 2
R 58
M 36
s_bfe_u32 s51, s50, 0x10001
v_mad_i32_i24 v52, v51, s51, v50
XL 34,128
M 37
M 38
M 39
M 40
XL 36,896
M 41
M 42
M 43
WV 24
M 44
WL 0
M 45
s_barrier
M 46
mw 48
XL 38,1664
M 47
R 72
M 48
ad 84,624
W 0
M 49
M 50
M 51
M 52
W 1
XL 40,2432
M 53
M 54
M 55
M 56
W 2
M 57
M 58
M 59
R 74
M 60
mw 51
M 61
ad 84,627
W 0
M 62
M 63
M 64
M 65
W 1
M 66
M 67
M 68
M 69
W 2
M 70
M 71
WL 2
R 76
M 72
M 73
bc 48,13,64,56
M 74
mh 4
M 75
ad 64,1248
H 0
M 76
M 77
M 78
M 79
H 1
M 80
M 81
M 82
M 83
WL 2
R 78
M 84
H 2
M 85
M 86
M 87
M 88
WV 32
H 3
UL 6,26
M 89
UH 7,26
UL 8,27
M 90
UH 9,27
AD 64,6
M 91
AD 88,7
AD 112,8
M 92
bc 49,14,66,60
AD 136,9
M 93
mh 8
UL 6,28
UH 7,28
M 94
ad 66,1344
H 0
M 95
WL 2
R 80
M 96
UL 8,29
UH 9,29
M 97
AD 65,6
AD 89,7
M 98
H 1
AD 113,8
M 99
AD 137,9
UL 6,30
M 100
UH 7,30
UL 8,31
M 101
UH 9,31
AD 66,6
M 102
AD 90,7
AD 114,8
M 103
WV 33
AD 138,9
UL 6,32
M 104
UH 7,32
UL 8,33
M 105
UH 9,33
AD 67,6
M 106
AD 91,7
AD 115,8
M 107
WL 2
R 82
M 108
AD 139,9
M 109
M 110
M 111
M 112
M 113
M 114
M 115
WV 24
M 116
WL 0
M 117
s_barrier
M 118
mw 72
M 119
R 0
M 120
ad 84,648
W 0
M 121
M 132
M 133
T 24,10
M 122
T 28,24
M 123
W 1
M 134
M 135
T 32,11
M 124
T 36,25
M 125
W 2
M 136
M 137
T 40,12
M 126
T 44,26
M 127
mw 75
M 138
ad 84,651
W 0
M 139
WL 8
T 48,13
M 128
T 52,27
M 129
M 140
W 1
M 141
T 56,0
M 130
WL 8
T 60,14
M 131
R 2
M 142
W 2
M 143
T 64,1
M 0
WL 8
T 68,15
M 1
R 4
M 2
M 3
M 4
M 5
M 6
M 7
WL 8
M 8
M 9
WL 2
M 10
M 11
R 6
M 12
M 13
M 14
M 15
M 16
M 17
M 18
M 19
M 20
M 21
M 22
M 23
WL 2
R 8
M 24
M 25
M 26
M 27
M 28
M 29
M 30
M 31
M 32
M 33
M 34
M 35
WL 2
R 10
M 36
ae 44,68,0
M 37
s_bitcmp1_b32 s50, 2
s_cselect_b32 s51, 24576, 0
v_add_u32 v52, s51, v3
M 38
XL 42,128
M 39
M 40
M 41
M 42
XL 44,896
)"
R"(M 43
WV 24
M 44
WL 0
M 45
s_barrier
M 46
mw 0
M 47
R 24
M 48
ad 84,672
W 0
M 49
XL 46,1664
M 50
M 51
M 52
W 1
M 53
M 54
XL 48,2432
M 55
M 56
W 2
M 57
M 58
M 59
R 26
M 60
mw 3
M 61
ad 84,675
W 0
M 62
M 63
M 64
M 65
W 1
M 66
M 67
M 68
M 69
W 2
M 70
M 71
WL 2
R 28
M 72
M 73
bc 48,14,64,56
M 74
mh 10
M 75
ad 64,1344
H 0
M 76
M 77
M 78
M 79
H 1
M 80
M 81
M 82
M 83
WL 2
R 30
M 84
H 2
M 85
M 86
M 87
M 88
H 3
M 89
M 90
WV 33
UL 6,34
UH 7,34
M 91
UL 8,35
UH 9,35
M 92
bc 49,15,66,60
AD 68,6
M 93
mh 0
AD 92,7
AD 116,8
M 94
ad 66,1440
H 0
M 95
WL 2
R 32
M 96
AD 140,9
UL 6,36
M 97
UH 7,36
UL 8,37
M 98
H 1
UH 9,37
M 99
AD 69,6
AD 93,7
M 100
AD 117,8
AD 141,9
M 101
UL 6,38
UH 7,38
M 102
UL 8,39
UH 9,39
M 103
AD 70,6
AD 94,7
M 104
AD 118,8
AD 142,9
M 105
WV 32
UL 6,40
UH 7,40
M 106
UL 8,41
UH 9,41
M 107
WL 2
R 34
M 108
AD 71,6
AD 95,7
M 109
AD 119,8
AD 143,9
M 110
M 111
M 112
M 113
M 114
M 115
WV 24
M 116
WL 0
M 117
s_barrier
M 118
mw 24
M 119
R 48
M 120
ad 84,696
W 0
M 121
M 132
M 133
T 24,2
M 122
T 28,16
M 123
W 1
M 134
M 135
T 32,3
M 124
T 36,17
M 125
W 2
M 136
M 137
T 40,4
M 126
T 44,18
M 127
mw 27
M 138
ad 84,699
W 0
M 139
WL 8
T 48,5
M 128
T 52,19
M 129
M 140
W 1
M 141
T 56,6
M 130
WL 8
T 60,20
M 131
R 50
M 142
W 2
M 143
T 64,7
M 0
WL 8
T 68,21
M 1
R 52
M 2
M 3
M 4
M 5
M 6
M 7
WL 8
M 8
M 9
WL 2
M 10
M 11
R 54
M 12
M 13
M 14
M 15
M 16
M 17
M 18
M 19
M 20
M 21
M 22
M 23
WL 2
R 56
M 24
M 25
M 26
M 27
M 28
M 29
M 30
M 31
M 32
M 33
M 34
M 35
WL 2
R 58
M 36
M 37
M 38
ae 44,68,0
M 39
s_bfe_u32 s51, s50, 0x10003
v_mad_i32_i24 v52, v51, s51, v50
XL 14,128
M 40
M 41
M 42
M 43
WV 23
M 44
WL 0
XL 16,896
M 45
s_barrier
M 46
mw 48
M 47
R 72
M 48
ad 84,720
W 0
M 49
M 50
XL 18,1664
M 51
M 52
W 1
M 53
M 54
M 55
M 56
W 2
XL 20,2432
M 57
M 58
M 59
R 74
M 60
mw 51
M 61
ad 84,723
W 0
M 62
M 63
M 64
M 65
W 1
M 66
M 67
M 68
M 69
W 2
M 70
M 71
WL 2
R 76
M 72
M 73
bc 48,15,64,56
M 74
mh 2
M 75
ad 64,1440
H 0
M 76
M 77
M 78
M 79
H 1
M 80
M 81
M 82
M 83
WL 2
R 78
M 84
H 2
M 85
M 86
M 87
M 88
H 3
M 89
M 90
M 91
WV 34
M 92
bc 49,16,66,60
UL 6,42
M 93
mh 6
UH 7,42
UL 8,43
M 94
ad 66,1536
H 0
M 95
WL 2
R 80
M 96
UH 9,43
AD 72,6
M 97
AD 96,7
AD 120,8
M 98
H 1
AD 144,9
M 99
UL 6,44
UH 7,44
M 100
UL 8,45
UH 9,45
M 101
AD 73,6
AD 97,7
M 102
AD 121,8
AD 145,9
M 103
WV 34
UL 6,46
UH 7,46
M 104
UL 8,47
UH 9,47
M 105
AD 74,6
AD 98,7
M 106
AD 122,8
AD 146,9
M 107
WL 2
R 82
M 108
WV 32
UL 6,48
UH 7,48
M 109
UL 8,49
UH 9,49
M 110
AD 75,6
AD 99,7
M 111
AD 123,8
AD 147,9
M 112
)"
R"(M 113
M 114
M 115
WV 24
M 116
WL 0
M 117
s_barrier
M 118
mw 72
M 119
R 0
M 120
ad 84,744
W 0
M 121
M 132
M 133
T 24,8
M 122
T 28,22
M 123
W 1
M 134
M 135
T 32,9
M 124
T 36,23
M 125
W 2
M 136
M 137
T 40,10
M 126
T 44,24
M 127
mw 75
M 138
ad 84,747
W 0
M 139
WL 8
T 48,11
M 128
T 52,25
M 129
M 140
W 1
M 141
T 56,12
M 130
WL 8
T 60,26
M 131
R 2
M 142
W 2
M 143
T 64,13
M 0
WL 8
T 68,27
M 1
R 4
M 2
M 3
M 4
M 5
M 6
M 7
WL 8
M 8
M 9
WL 2
M 10
M 11
R 6
M 12
M 13
M 14
M 15
M 16
M 17
M 18
M 19
M 20
M 21
M 22
M 23
WL 2
R 8
M 24
M 25
M 26
M 27
M 28
M 29
M 30
M 31
M 32
M 33
M 34
M 35
WL 2
R 10
M 36
M 37
M 38
M 39
M 40
ae 44,68,48
M 41
s_bitcmp1_b32 s50, 4
s_cselect_b32 s51, 24576, 0
v_add_u32 v52, s51, v3
M 42
XL 22,128
M 43
WV 23
M 44
WL 0
M 45
s_barrier
M 46
mw 0
XL 24,896
M 47
R 24
M 48
ad 84,768
W 0
M 49
M 50
M 51
M 52
W 1
XL 26,1664
M 53
M 54
M 55
M 56
W 2
M 57
M 58
XL 28,2432
M 59
R 26
M 60
mw 3
M 61
ad 84,771
W 0
M 62
M 63
M 64
M 65
W 1
M 66
M 67
M 68
M 69
W 2
M 70
M 71
WL 2
R 28
M 72
M 73
bc 48,16,64,56
M 74
mh 8
M 75
ad 64,1536
H 0
M 76
M 77
M 78
M 79
H 1
M 80
M 81
M 82
M 83
WL 2
R 30
M 84
H 2
M 85
M 86
M 87
M 88
H 3
M 89
M 90
M 91
M 92
bc 49,17,66,60
M 93
mh 12
M 94
ad 66,1632
H 0
M 95
WL 2
R 32
M 96
WV 35
UL 6,14
UH 7,14
M 97
UL 8,15
UH 9,15
M 98
H 1
AD 76,6
M 99
AD 100,7
AD 124,8
M 100
AD 148,9
UL 6,16
M 101
UH 7,16
UL 8,17
M 102
UH 9,17
AD 77,6
M 103
AD 101,7
AD 125,8
M 104
WV 34
AD 149,9
UL 6,18
M 105
UH 7,18
UL 8,19
M 106
UH 9,19
AD 78,6
M 107
WL 2
R 34
M 108
AD 102,7
AD 126,8
M 109
WV 31
AD 150,9
UL 6,20
M 110
UH 7,20
UL 8,21
M 111
UH 9,21
AD 79,6
M 112
AD 103,7
AD 127,8
M 113
AD 151,9
M 114
M 115
WV 24
M 116
WL 0
M 117
s_barrier
M 118
mw 24
M 119
R 48
M 120
ad 84,792
W 0
M 121
M 132
M 133
T 24,0
M 122
T 28,14
M 123
W 1
M 134
M 135
T 32,1
M 124
T 36,15
M 125
W 2
M 136
M 137
T 40,2
M 126
T 44,16
M 127
mw 27
M 138
ad 84,795
W 0
M 139
WL 8
T 48,3
M 128
T 52,17
M 129
M 140
W 1
M 141
T 56,4
M 130
WL 8
T 60,18
M 131
R 50
M 142
W 2
M 143
T 64,5
M 0
WL 8
T 68,19
M 1
R 52
M 2
M 3
M 4
M 5
M 6
M 7
WL 8
M 8
M 9
WL 2
M 10
M 11
R 54
M 12
M 13
M 14
M 15
M 16
M 17
M 18
M 19
M 20
M 21
M 22
M 23
WL 2
R 56
M 24
M 25
M 26
M 27
M 28
M 29
M 30
M 31
M 32
M 33
M 34
M 35
WL 2
R 58
M 36
M 37
M 38
M 39
M 40
M 41
M 42
ae 44,68,48
s_bfe_u32 s51, s50, 0x10005
M 43
WV 22
v_mad_i32_i24 v52, v51, s51, v50
XL 30,128
M 44
WL 0
M 45
s_barrier
M 46
mw 48
M 47
R 72
M 48
ad 84,816
W 0
M 49
XL 32,896
M 50
M 51
M 52
W 1
M 53
M 54
XL 34,1664
M 55
M 56
)"
R"(W 2
M 57
M 58
M 59
R 74
M 60
mw 51
XL 36,2432
M 61
ad 84,819
W 0
M 62
M 63
M 64
M 65
W 1
M 66
M 67
M 68
M 69
W 2
M 70
M 71
WL 2
R 76
M 72
M 73
bc 48,17,64,56
M 74
mh 0
M 75
ad 64,1632
H 0
M 76
M 77
M 78
M 79
H 1
M 80
M 81
M 82
M 83
WL 2
R 78
M 84
H 2
M 85
M 86
M 87
M 88
H 3
M 89
M 90
M 91
M 92
bc 49,18,66,60
M 93
mh 4
M 94
ad 66,1728
H 0
M 95
WL 2
R 80
M 96
WV 35
UL 6,22
UH 7,22
M 97
UL 8,23
UH 9,23
M 98
H 1
AD 80,6
M 99
AD 104,7
AD 128,8
M 100
AD 152,9
UL 6,24
M 101
UH 7,24
UL 8,25
M 102
UH 9,25
AD 81,6
M 103
AD 105,7
AD 129,8
M 104
WV 33
AD 153,9
UL 6,26
M 105
UH 7,26
UL 8,27
M 106
UH 9,27
AD 82,6
M 107
WL 2
R 82
M 108
AD 106,7
AD 130,8
M 109
WV 31
AD 154,9
UL 6,28
M 110
UH 7,28
UL 8,29
M 111
UH 9,29
AD 83,6
M 112
AD 107,7
AD 131,8
M 113
AD 155,9
M 114
M 115
WV 24
M 116
WL 0
M 117
s_barrier
M 118
mw 72
M 119
R 0
M 120
ad 84,840
W 0
M 121
M 132
M 133
T 24,6
M 122
T 28,20
M 123
W 1
M 134
M 135
T 32,7
M 124
T 36,21
M 125
W 2
M 136
M 137
T 40,8
M 126
T 44,22
M 127
mw 75
M 138
ad 84,843
W 0
M 139
WL 8
T 48,9
M 128
T 52,23
M 129
M 140
W 1
M 141
T 56,10
M 130
WL 8
T 60,24
M 131
R 2
M 142
W 2
M 143
T 64,11
M 0
WL 8
T 68,25
M 1
R 4
M 2
M 3
M 4
M 5
M 6
M 7
WL 8
M 8
M 9
WL 2
M 10
M 11
R 6
M 12
M 13
M 14
M 15
M 16
M 17
M 18
M 19
M 20
M 21
M 22
M 23
WL 2
R 8
M 24
M 25
M 26
M 27
M 28
M 29
M 30
M 31
M 32
M 33
M 34
M 35
WL 2
R 10
M 36
M 37
M 38
M 39
M 40
M 41
M 42
M 43
M 44
WV 22
M 45
WL 0
s_barrier
M 46
mw 0
M 47
R 24
M 48
ad 84,864
W 0
M 49
M 50
M 51
M 52
W 1
M 53
M 54
M 55
M 56
W 2
M 57
M 58
M 59
R 26
M 60
mw 3
M 61
ad 84,867
W 0
M 62
M 63
M 64
M 65
W 1
M 66
M 67
M 68
M 69
W 2
M 70
ae 44,70,0
M 71
WL 2
R 28
M 72
s_bitcmp1_b32 s50, 0
s_cselect_b32 s51, 24576, 0
v_add_u32 v52, s51, v3
M 73
bc 48,18,64,56
XL 38,256
M 74
mh 6
M 75
ad 64,1728
H 0
M 76
XL 40,1024
M 77
M 78
M 79
H 1
M 80
M 81
M 82
XL 42,1792
M 83
WL 2
R 30
M 84
H 2
M 85
M 86
M 87
M 88
H 3
XL 44,2560
M 89
M 90
M 91
M 92
bc 49,19,66,60
M 93
mh 10
M 94
ad 66,1824
H 0
M 95
WL 2
R 32
M 96
M 97
M 98
WV 36
H 1
UL 6,30
M 99
UH 7,30
UL 8,31
M 100
UH 9,31
AD 84,6
M 101
AD 108,7
AD 132,8
M 102
WV 35
AD 156,9
UL 6,32
M 103
UH 7,32
UL 8,33
M 104
UH 9,33
AD 85,6
M 105
AD 109,7
AD 133,8
M 106
WV 33
AD 157,9
UL 6,34
M 107
WL 2
R 34
M 108
UH 7,34
UL 8,35
M 109
UH 9,35
AD 86,6
M 110
AD 110,7
AD 134,8
M 111
WV 31
AD 158,9
UL 6,36
M 112
UH 7,36
UL 8,37
M 113
UH 9,37
AD 87,6
M 114
AD 111,7
AD 135,8
M 115
WV 24
AD 159,9
M 116
WL 0
M 117
s_barrier
M 118
mw 24
M 119
R 48
M 120
ad 84,888
W 0
M 121
M 132
M 133
T 24,12
M 122
)"
R"(T 28,26
M 123
W 1
M 134
M 135
T 32,13
M 124
T 36,27
M 125
W 2
M 136
M 137
T 40,0
M 126
T 44,14
M 127
mw 27
M 138
ad 84,891
W 0
M 139
WL 8
T 48,1
M 128
T 52,15
M 129
M 140
W 1
M 141
T 56,2
M 130
WL 8
T 60,16
M 131
R 50
M 142
W 2
M 143
T 64,3
M 0
WL 8
T 68,17
M 1
R 52
M 2
M 3
M 4
M 5
M 6
M 7
WL 8
M 8
M 9
WL 2
M 10
M 11
R 54
M 12
M 13
M 14
M 15
M 16
M 17
M 18
M 19
M 20
M 21
M 22
M 23
WL 2
R 56
M 24
M 25
M 26
M 27
M 28
M 29
M 30
M 31
M 32
M 33
M 34
M 35
WL 2
R 58
M 36
M 37
M 38
M 39
M 40
M 41
M 42
M 43
M 44
WV 22
M 45
WL 0
s_barrier
M 46
mw 48
M 47
R 72
M 48
ad 84,912
W 0
M 49
M 50
M 51
M 52
W 1
M 53
M 54
M 55
M 56
W 2
M 57
M 58
M 59
R 74
M 60
mw 51
M 61
ad 84,915
W 0
M 62
M 63
M 64
M 65
W 1
M 66
M 67
M 68
M 69
W 2
M 70
M 71
WL 2
R 76
M 72
M 73
bc 48,19,64,56
M 74
mh 12
M 75
ad 64,1824
H 0
M 76
ae 44,70,0
M 77
s_bfe_u32 s51, s50, 0x10001
v_mad_i32_i24 v52, v51, s51, v50
XL 46,256
M 78
M 79
H 1
M 80
XL 48,1024
M 81
M 82
M 83
WL 2
R 78
M 84
mh 0
M 85
ad 64,1826
H 0
M 86
XL 14,1792
M 87
M 88
M 89
H 1
M 90
M 91
M 92
XL 16,2560
M 93
bc 49,20,66,60
M 94
mh 2
M 95
WL 2
R 80
M 96
ad 66,1920
H 0
M 97
M 98
M 99
M 100
H 1
M 101
M 102
M 103
M 104
M 105
M 106
M 107
WL 2
R 82
M 108
M 109
M 110
M 111
M 112
M 113
M 114
M 115
M 116
WV 25
M 117
WL 0
s_barrier
M 118
mw 72
M 119
R 0
M 120
ad 84,936
W 0
M 121
M 132
M 133
T 24,4
M 122
T 28,18
M 123
W 1
M 134
UL 6,38
UH 7,38
M 135
T 32,5
M 124
T 36,19
M 125
W 2
UL 8,39
M 136
UH 9,39
AD 160,6
M 137
T 40,6
M 126
T 44,20
M 127
mw 75
AD 184,7
AD 208,8
M 138
ad 84,939
W 0
M 139
WL 8
T 48,7
M 128
T 52,21
M 129
AD 232,9
UL 6,40
M 140
W 1
UH 7,40
M 141
T 56,8
M 130
WL 8
T 60,22
M 131
R 2
M 142
W 2
UL 8,41
M 143
T 64,9
M 0
WL 8
T 68,23
M 1
R 4
M 2
UH 9,41
AD 161,6
M 3
AD 185,7
AD 209,8
M 4
AD 233,9
UL 6,42
M 5
UH 7,42
UL 8,43
M 6
UH 9,43
AD 162,6
M 7
WL 8
AD 186,7
AD 210,8
M 8
WV 30
AD 234,9
UL 6,44
M 9
WL 2
UH 7,44
UL 8,45
M 10
UH 9,45
AD 163,6
M 11
R 6
M 12
AD 187,7
AD 211,8
M 13
AD 235,9
M 14
M 15
M 16
M 17
M 18
M 19
M 20
M 21
M 22
M 23
WL 2
R 8
M 24
M 25
M 26
M 27
M 28
M 29
M 30
M 31
M 32
M 33
M 34
M 35
WL 2
R 10
M 36
M 37
M 38
M 39
M 40
M 41
M 42
M 43
M 44
WV 22
M 45
WL 0
s_barrier
M 46
mw 0
M 47
R 24
M 48
ad 84,960
W 0
M 49
M 50
M 51
M 52
W 1
M 53
M 54
M 55
M 56
W 2
M 57
M 58
M 59
R 26
M 60
mw 3
M 61
ad 84,963
W 0
M 62
M 63
M 64
M 65
W 1
M 66
M 67
M 68
M 69
W 2
M 70
M 71
WL 2
R 28
M 72
M 73
bc 48,20,64,56
)"
R"(M 74
mh 4
M 75
ad 64,1920
H 0
M 76
M 77
M 78
ae 44,68,0
M 79
H 1
M 80
s_bitcmp1_b32 s50, 2
s_cselect_b32 s51, 24576, 0
v_add_u32 v52, s51, v3
M 81
XL 18,256
M 82
M 83
WL 2
R 30
M 84
H 2
XL 20,1024
M 85
M 86
M 87
M 88
H 3
M 89
M 90
XL 22,1792
M 91
M 92
bc 49,21,66,60
M 93
mh 8
M 94
ad 66,2016
H 0
M 95
WL 2
R 32
M 96
XL 24,2560
M 97
M 98
H 1
M 99
M 100
M 101
M 102
M 103
M 104
M 105
M 106
M 107
WL 2
R 34
M 108
M 109
M 110
M 111
M 112
M 113
M 114
M 115
M 116
WV 25
M 117
WL 0
s_barrier
M 118
mw 24
M 119
R 48
M 120
ad 84,984
W 0
M 121
M 132
M 133
T 24,10
M 122
T 28,24
M 123
W 1
M 134
M 135
T 32,11
M 124
T 36,25
M 125
W 2
M 136
UL 6,46
UH 7,46
M 137
T 40,12
M 126
T 44,26
M 127
mw 27
UL 8,47
UH 9,47
M 138
ad 84,987
W 0
M 139
WL 8
T 48,13
M 128
T 52,27
M 129
AD 164,6
AD 188,7
M 140
W 1
AD 212,8
M 141
T 56,0
M 130
WL 8
T 60,14
M 131
R 50
M 142
W 2
AD 236,9
M 143
T 64,1
M 0
WL 8
T 68,15
M 1
R 52
M 2
UL 6,48
UH 7,48
M 3
UL 8,49
UH 9,49
M 4
AD 165,6
AD 189,7
M 5
AD 213,8
AD 237,9
M 6
UL 6,14
UH 7,14
M 7
WL 8
UL 8,15
UH 9,15
M 8
AD 166,6
AD 190,7
M 9
WL 2
AD 214,8
AD 238,9
M 10
WV 30
UL 6,16
UH 7,16
M 11
R 54
M 12
UL 8,17
UH 9,17
M 13
AD 167,6
AD 191,7
M 14
AD 215,8
AD 239,9
M 15
M 16
M 17
M 18
M 19
M 20
M 21
M 22
M 23
WL 2
R 56
M 24
M 25
M 26
M 27
M 28
M 29
M 30
M 31
M 32
M 33
M 34
M 35
WL 2
R 58
M 36
M 37
M 38
M 39
M 40
M 41
M 42
M 43
M 44
WV 22
M 45
WL 0
s_barrier
M 46
mw 48
M 47
R 72
M 48
ad 84,1008
W 0
M 49
M 50
M 51
M 52
W 1
M 53
M 54
M 55
M 56
W 2
M 57
M 58
M 59
R 74
M 60
mw 51
M 61
ad 84,1011
W 0
M 62
M 63
M 64
M 65
W 1
M 66
M 67
M 68
M 69
W 2
M 70
M 71
WL 2
R 76
M 72
M 73
bc 48,21,64,56
M 74
mh 10
M 75
ad 64,2016
H 0
M 76
M 77
M 78
M 79
H 1
M 80
M 81
M 82
ae 44,68,0
M 83
WL 2
R 78
M 84
s_bfe_u32 s51, s50, 0x10003
H 2
v_mad_i32_i24 v52, v51, s51, v50
M 85
XL 26,256
M 86
M 87
M 88
H 3
XL 28,1024
M 89
M 90
M 91
M 92
bc 49,22,66,60
M 93
mh 0
M 94
ad 66,2112
H 0
M 95
WL 2
R 80
M 96
XL 30,1792
M 97
M 98
H 1
M 99
M 100
XL 32,2560
M 101
M 102
M 103
M 104
M 105
M 106
M 107
WL 2
R 82
M 108
M 109
M 110
M 111
M 112
M 113
M 114
M 115
M 116
WV 26
M 117
WL 0
s_barrier
M 118
mw 72
M 119
R 0
M 120
ad 84,1032
W 0
M 121
M 132
M 133
T 24,2
M 122
T 28,16
M 123
W 1
M 134
M 135
T 32,3
M 124
T 36,17
M 125
W 2
M 136
M 137
T 40,4
M 126
T 44,18
M 127
mw 75
M 138
ad 84,1035
W 0
M 139
WL 8
T 48,5
M 128
T 52,19
M 129
UL 6,18
UH 7,18
M 140
W 1
UL 8,19
M 141
T 56,6
M 130
WL 8
T 60,20
M 131
R 2
M 142
W 2
UH 9,19
M 143
T 64,7
M 0
WL 8
T 68,21
M 1
R 4
M 2
AD 168,6
AD 192,7
M 3
AD 216,8
AD 240,9
M 4
UL 6,20
UH 7,20
M 5
UL 8,21
)"
R"(UH 9,21
M 6
AD 169,6
AD 193,7
M 7
WL 8
AD 217,8
AD 241,9
M 8
WV 31
UL 6,22
UH 7,22
M 9
WL 2
UL 8,23
UH 9,23
M 10
AD 170,6
AD 194,7
M 11
R 6
M 12
AD 218,8
AD 242,9
M 13
WV 29
UL 6,24
UH 7,24
M 14
UL 8,25
UH 9,25
M 15
AD 171,6
AD 195,7
M 16
AD 219,8
AD 243,9
M 17
M 18
M 19
M 20
M 21
M 22
M 23
WL 2
R 8
M 24
M 25
M 26
M 27
M 28
M 29
M 30
M 31
M 32
M 33
M 34
M 35
WL 2
R 10
M 36
M 37
M 38
M 39
M 40
M 41
M 42
M 43
M 44
WV 22
M 45
WL 0
s_barrier
M 46
mw 0
M 47
R 24
M 48
ad 84,1056
W 0
M 49
M 50
M 51
M 52
W 1
M 53
M 54
M 55
M 56
W 2
M 57
M 58
M 59
R 26
M 60
mw 3
M 61
ad 84,1059
W 0
M 62
M 63
M 64
M 65
W 1
M 66
M 67
M 68
M 69
W 2
M 70
M 71
WL 2
R 28
M 72
M 73
bc 48,22,64,56
M 74
mh 2
M 75
ad 64,2112
H 0
M 76
M 77
M 78
M 79
H 1
M 80
M 81
M 82
M 83
WL 2
R 30
M 84
H 2
M 85
M 86
ae 44,68,48
M 87
s_bitcmp1_b32 s50, 4
s_cselect_b32 s51, 24576, 0
v_add_u32 v52, s51, v3
M 88
H 3
XL 34,256
M 89
M 90
M 91
M 92
bc 49,23,66,60
XL 36,1024
M 93
mh 6
M 94
ad 66,2208
H 0
M 95
WL 2
R 32
M 96
M 97
M 98
H 1
XL 38,1792
M 99
M 100
M 101
M 102
M 103
M 104
XL 40,2560
M 105
M 106
M 107
WL 2
R 34
M 108
M 109
M 110
M 111
M 112
M 113
M 114
M 115
M 116
WV 27
M 117
WL 0
s_barrier
M 118
mw 24
M 119
R 48
M 120
ad 84,1080
W 0
M 121
M 132
M 133
T 24,8
M 122
T 28,22
M 123
W 1
M 134
M 135
T 32,9
M 124
T 36,23
M 125
W 2
M 136
M 137
T 40,10
M 126
T 44,24
M 127
mw 27
M 138
ad 84,1083
W 0
M 139
WL 8
T 48,11
M 128
T 52,25
M 129
M 140
W 1
UL 6,26
M 141
T 56,12
M 130
WL 8
T 60,26
M 131
R 50
M 142
W 2
UH 7,26
M 143
T 64,13
M 0
WL 8
T 68,27
M 1
R 52
M 2
UL 8,27
UH 9,27
M 3
AD 172,6
AD 196,7
M 4
AD 220,8
AD 244,9
M 5
WV 32
UL 6,28
UH 7,28
M 6
UL 8,29
UH 9,29
M 7
WL 8
AD 173,6
AD 197,7
M 8
WL 2
AD 221,8
AD 245,9
M 9
WV 30
UL 6,30
UH 7,30
M 10
UL 8,31
UH 9,31
M 11
R 54
M 12
AD 174,6
AD 198,7
M 13
AD 222,8
AD 246,9
M 14
WV 28
UL 6,32
UH 7,32
M 15
UL 8,33
UH 9,33
M 16
AD 175,6
AD 199,7
M 17
AD 223,8
AD 247,9
M 18
M 19
M 20
M 21
M 22
M 23
WL 2
R 56
M 24
M 25
M 26
M 27
M 28
M 29
M 30
M 31
M 32
M 33
M 34
M 35
WL 2
R 58
M 36
M 37
M 38
M 39
M 40
M 41
M 42
M 43
M 44
WV 22
M 45
WL 0
s_barrier
M 46
mw 48
M 47
R 72
M 48
ad 84,1104
W 0
M 49
M 50
M 51
M 52
W 1
M 53
M 54
M 55
M 56
W 2
M 57
M 58
M 59
R 74
M 60
mw 51
M 61
ad 84,1107
W 0
M 62
M 63
M 64
M 65
W 1
M 66
M 67
M 68
M 69
W 2
M 70
M 71
WL 2
R 76
M 72
M 73
bc 48,23,64,56
M 74
mh 8
M 75
ad 64,2208
H 0
M 76
M 77
M 78
M 79
H 1
M 80
M 81
M 82
M 83
WL 2
R 78
M 84
H 2
M 85
M 86
M 87
M 88
H 3
M 89
)"
R"(M 90
ae 44,68,48
M 91
s_bfe_u32 s51, s50, 0x10005
v_mad_i32_i24 v52, v51, s51, v50
XL 42,256
M 92
bc 48,0,64,56
M 93
mh 12
M 94
ad 64,0
H 0
M 95
WL 2
R 80
M 96
XL 44,1024
M 97
M 98
H 1
M 99
M 100
M 101
M 102
XL 46,1792
M 103
M 104
M 105
M 106
M 107
WL 2
R 82
M 108
XL 48,2560
M 109
M 110
M 111
M 112
M 113
M 114
M 115
WV 28
M 116
WL 0
M 117
s_barrier
M 118
mw 72
M 119
R 0
M 120
ad 84,1128
W 0
M 121
M 132
M 133
T 24,0
M 122
T 28,14
M 123
W 1
M 134
M 135
T 32,1
M 124
T 36,15
M 125
W 2
M 136
M 137
T 40,2
M 126
T 44,16
M 127
mw 75
M 138
ad 84,1131
W 0
M 139
WL 8
T 48,3
M 128
T 52,17
M 129
M 140
W 1
M 141
T 56,4
M 130
WL 8
T 60,18
M 131
R 2
M 142
WV 31
W 2
UL 6,34
M 143
T 64,5
M 0
WL 8
T 68,19
M 1
R 4
M 2
PA 12288
DF
UH 7,34
M 3
UL 8,35
UH 9,35
M 4
AD 176,6
AD 200,7
M 5
AD 224,8
AD 248,9
M 6
UL 6,36
UH 7,36
M 7
WL 8
UL 8,37
UH 9,37
M 8
AD 177,6
AD 201,7
M 9
WL 2
AD 225,8
AD 249,9
M 10
WV 29
UL 6,38
UH 7,38
M 11
R 6
M 12
UL 8,39
UH 9,39
M 13
AD 178,6
AD 202,7
M 14
AD 226,8
AD 250,9
M 15
UL 6,40
UH 7,40
M 16
UL 8,41
UH 9,41
M 17
AD 179,6
AD 203,7
M 18
AD 227,8
AD 251,9
M 19
M 20
M 21
M 22
M 23
WL 2
R 8
M 24
M 25
M 26
M 27
M 28
M 29
M 30
M 31
M 32
M 33
M 34
M 35
WL 2
R 10
M 36
M 37
M 38
M 39
M 40
M 41
M 42
M 43
M 44
WV 23
M 45
WL 0
s_barrier
M 46
mw 0
M 47
R 24
M 48
ad 84,0
W 0
M 49
M 50
M 51
M 52
W 1
M 53
M 54
M 55
M 56
W 2
M 57
M 58
M 59
R 26
M 60
mw 3
M 61
ad 84,3
W 0
M 62
M 63
M 64
M 65
W 1
M 66
M 67
M 68
M 69
W 2
M 70
M 71
WL 2
R 28
M 72
M 73
mh 0
M 74
ad 64,2
H 0
M 75
M 76
M 77
M 78
H 1
M 79
M 80
M 81
M 82
bc 49,0,66,60
M 83
WL 2
R 30
M 84
mh 2
M 85
ad 66,16
H 0
M 86
M 87
M 88
M 89
mh 3
M 90
ad 64,16
H 0
M 91
M 92
M 93
M 94
bc 48,1,64,56
M 95
WL 2
R 32
M 96
mh 4
M 97
ad 64,98
H 0
M 98
M 99
M 100
M 101
H 1
M 102
M 103
M 104
M 105
M 106
M 107
WL 2
R 34
M 108
M 109
M 110
M 111
M 112
M 113
M 114
M 115
M 116
WV 25
M 117
WL 0
s_barrier
M 118
mw 24
M 119
R 48
M 120
ad 84,24
W 0
M 121
M 132
M 133
T 24,6
M 122
T 28,20
M 123
W 1
M 134
M 135
T 32,7
M 124
T 36,21
M 125
W 2
M 136
M 137
T 40,8
M 126
T 44,22
M 127
mw 27
M 138
ad 84,27
W 0
M 139
WL 8
T 48,9
M 128
T 52,23
M 129
M 140
W 1
M 141
T 56,10
M 130
WL 8
T 60,24
M 131
R 50
M 142
W 2
M 143
T 64,11
M 0
WL 8
T 68,25
M 1
R 52
M 2
WV 30
UL 6,42
UH 7,42
M 3
UL 8,43
UH 9,43
M 4
AD 180,6
AD 204,7
M 5
AD 228,8
AD 252,9
M 6
WV 28
UL 6,44
UH 7,44
M 7
WL 8
UL 8,45
UH 9,45
M 8
AD 181,6
AD 205,7
M 9
WL 2
AD 229,8
AD 253,9
M 10
WV 25
UL 6,46
UH 7,46
M 11
R 54
M 12
UL 8,47
UH 9,47
M 13
AD 182,6
AD 206,7
)"
R"(M 14
AD 230,8
AD 254,9
M 15
UL 6,48
UH 7,48
M 16
UL 8,49
UH 9,49
M 17
AD 183,6
AD 207,7
M 18
AD 231,8
AD 255,9
M 19
M 20
M 21
M 22
M 23
WL 2
R 56
M 24
M 25
M 26
M 27
M 28
M 29
M 30
M 31
M 32
M 33
M 34
M 35
WL 2
R 58
M 36
M 37
M 38
M 39
M 40
AR 6,72
AR 7,96
M 41
AR 8,120
s_nop 0
AR 9,144
M 42
EF 6
EF 7
M 43
WV 19
EF 8
EF 9
M 44
WL 0
CV 14,6,7
CV 15,8,9
M 45
s_barrier
AR 6,73
AR 7,97
M 46
mw 48
AR 8,121
AR 9,145
M 47
R 72
M 48
ad 84,48
W 0
M 49
EF 6
EF 7
M 50
EF 8
EF 9
M 51
CV 16,6,7
CV 17,8,9
M 52
W 1
AR 6,74
M 53
AR 7,98
AR 8,122
M 54
AR 9,146
EF 6
M 55
EF 7
EF 8
M 56
W 2
EF 9
M 57
CV 18,6,7
CV 19,8,9
M 58
AR 6,75
AR 7,99
M 59
R 74
M 60
mw 51
AR 8,123
AR 9,147
M 61
ad 84,51
W 0
M 62
EF 6
EF 7
M 63
EF 8
EF 9
M 64
CV 20,6,7
CV 21,8,9
M 65
W 1
BA 72,0
M 66
BA 96,256
BA 120,512
M 67
BA 144,768
M 68
ae 42,74,0
AR 6,76
M 69
W 2
AR 7,100
M 70
s_bitcmp1_b32 s50, 0
s_cselect_b32 s52, 24576, 0
v_add_u32 v53, s52, v3
M 71
WL 4
R 76
M 72
ST 14,0
AR 8,124
M 73
bc 49,1,66,60
AR 9,148
M 74
mh 6
EF 6
EF 7
M 75
ad 66,112
H 0
M 76
EF 8
EF 9
M 77
CV 22,6,7
CV 23,8,9
M 78
AR 6,77
AR 7,101
M 79
mh 7
AR 8,125
AR 9,149
M 80
ad 64,112
H 0
M 81
EF 6
EF 7
M 82
EF 8
EF 9
M 83
R 78
M 84
H 1
CV 24,6,7
M 85
CV 25,8,9
AR 6,78
M 86
AR 7,102
AR 8,126
M 87
AR 9,150
EF 6
M 88
mh 9
EF 7
EF 8
M 89
ad 66,113
H 0
M 90
EF 9
CV 26,6,7
M 91
CV 27,8,9
AR 6,79
M 92
AR 7,103
AR 8,127
M 93
bc 49,2,66,60
AR 9,151
M 94
mh 10
EF 6
EF 7
M 95
WL 0
R 80
M 96
ad 66,208
H 0
M 97
EF 8
EF 9
M 98
CV 28,6,7
CV 29,8,9
M 99
BA 76,0
BA 100,256
M 100
BA 124,512
BA 148,768
M 101
EF 64
EF 88
M 102
EF 112
EF 136
M 103
CV 30,64,88
CV 31,112,136
M 104
EF 65
EF 89
M 105
EF 113
EF 137
M 106
CV 32,65,89
CV 33,113,137
M 107
R 82
M 108
EF 66
EF 90
M 109
EF 114
EF 138
M 110
ST 16,768
CV 34,66,90
M 111
CV 35,114,138
EF 67
M 112
EF 91
EF 115
M 113
EF 139
CV 36,67,91
M 114
CV 37,115,139
BV 64,1024
M 115
WV 21
BV 88,1280
BV 112,1536
M 116
WL 3
BV 136,1792
M 117
s_barrier
EF 68
EF 92
M 118
mw 72
EF 116
EF 140
M 119
R 0
M 120
ad 84,72
W 0
M 121
CV 38,68,92
CV 39,116,140
M 132
EF 69
EF 93
M 133
T 24,2
M 122
T 28,16
M 123
W 1
EF 117
M 134
EF 141
CV 40,69,93
M 135
T 32,3
M 124
T 36,17
M 125
W 2
CV 41,117,141
M 136
EF 70
EF 94
M 137
WL 8
T 40,12
M 126
T 44,26
M 127
mw 75
EF 118
EF 142
M 138
ad 84,75
W 0
M 139
T 48,13
M 128
WL 8
T 52,27
M 129
CV 42,70,94
CV 43,118,142
M 140
W 1
EF 71
M 141
T 56,0
M 130
T 60,14
M 131
WL 8
R 2
M 142
W 2
EF 95
M 143
T 64,1
M 4
T 68,15
M 5
WL 8
R 4
M 6
EF 119
EF 143
M 7
WL 8
CV 44,71,95
CV 45,119,143
M 8
ST 18,1536
BV 68,1024
M 9
WL 3
BV 92,1280
BV 116,1536
M 10
BV 140,1792
M 11
EF 160
EF 184
M 0
EF 208
EF 232
M 1
CV 10,160,184
CV 11,208,232
M 2
AW 160,10
AW 161,11
M 3
R 6
M 16
EF 161
EF 185
M 17
EF 209
EF 233
M 18
CV 10,161,185
CV 11,209,233
M 19
AW 162,10
AW 163,11
M 20
)"
R"(EF 162
EF 186
M 21
EF 210
EF 234
M 22
CV 10,162,186
CV 11,210,234
M 23
AW 164,10
AW 165,11
M 12
EF 163
EF 187
M 13
EF 211
EF 235
M 14
CV 10,163,187
CV 11,211,235
M 15
WL 2
R 8
M 28
AW 166,10
AW 167,11
M 29
BV 160,2048
BV 184,2304
M 30
BV 208,2560
BV 232,2816
M 31
EF 164
EF 188
M 32
EF 212
EF 236
M 33
CV 10,164,188
CV 11,212,236
M 34
AW 168,10
AW 169,11
M 35
EF 165
EF 189
M 24
EF 213
EF 237
M 25
CV 10,165,189
CV 11,213,237
M 26
AW 170,10
AW 171,11
M 27
WL 6
R 10
M 40
EF 166
EF 190
M 41
EF 214
EF 238
M 42
CV 10,166,190
CV 11,214,238
M 43
AW 172,10
AW 173,11
M 44
EF 167
EF 191
M 45
EF 215
EF 239
M 46
ST 20,2304
CV 10,167,191
M 47
WV 21
CV 11,215,239
AW 174,10
M 36
WL 0
AW 175,11
BV 164,2048
M 37
s_barrier
BV 188,2304
BV 212,2560
M 38
mw 0
BV 236,2816
M 39
R 24
M 52
ad 84,96
W 0
M 53
AR 6,80
AR 7,104
M 54
AR 8,128
AR 9,152
M 55
EF 6
EF 7
M 56
W 1
EF 8
M 57
EF 9
CV 10,6,7
M 58
CV 11,8,9
AW 176,10
M 59
AW 177,11
AR 6,81
M 48
W 2
AR 7,105
M 49
AR 8,129
AR 9,153
M 50
EF 6
EF 7
M 51
R 26
M 64
mw 3
EF 8
EF 9
M 65
ad 84,99
W 0
M 66
CV 10,6,7
CV 11,8,9
M 67
AW 178,10
AW 179,11
M 68
AR 6,82
AR 7,106
M 69
W 1
AR 8,130
M 70
AR 9,154
EF 6
M 71
EF 7
EF 8
M 60
EF 9
CV 10,6,7
M 61
W 2
CV 11,8,9
M 62
AW 180,10
AW 181,11
M 63
WL 0
R 28
M 76
AR 6,83
AR 7,107
M 77
bc 48,2,64,56
AR 8,131
M 78
mh 11
AR 9,155
EF 6
M 79
ad 64,208
H 0
M 80
EF 7
EF 8
M 81
EF 9
CV 10,6,7
M 82
CV 11,8,9
AW 182,10
M 83
H 1
AW 183,11
M 72
ae 42,74,0
BA 80,0
M 73
s_bfe_u32 s52, s50, 0x10001
v_mad_i32_i24 v53, v51, s52, v50
ST 22,0
M 74
BA 104,256
BA 128,512
M 75
R 30
M 88
mh 13
BA 152,768
M 89
ad 66,209
H 0
M 90
AR 6,84
AR 7,108
M 91
AR 8,132
AR 9,156
M 92
EF 6
EF 7
M 93
mh 0
EF 8
EF 9
M 94
ad 64,210
H 0
M 95
CV 10,6,7
CV 11,8,9
M 84
AW 184,10
AW 185,11
M 85
AR 6,85
AR 7,109
M 86
H 1
AR 8,133
M 87
WL 1
R 32
M 100
AR 9,157
EF 6
M 101
EF 7
EF 8
M 102
bc 49,3,66,60
EF 9
M 103
mh 2
CV 10,6,7
CV 11,8,9
M 104
ad 66,304
H 0
M 105
AW 186,10
AW 187,11
M 106
AR 6,86
AR 7,110
M 107
AR 8,134
AR 9,158
M 96
EF 6
EF 7
M 97
EF 8
EF 9
M 98
CV 10,6,7
CV 11,8,9
M 99
R 34
M 112
AW 188,10
AW 189,11
M 113
AR 6,87
AR 7,111
M 114
AR 8,135
AR 9,159
M 115
EF 6
EF 7
M 116
EF 8
EF 9
M 117
CV 10,6,7
CV 11,8,9
M 118
AW 190,10
AW 191,11
M 119
WV 23
BA 84,0
BA 108,256
M 108
WL 2
BA 132,512
BA 156,768
M 109
s_barrier
EF 72
EF 96
M 110
mw 24
EF 120
EF 144
M 111
R 48
M 124
ad 84,120
W 0
M 125
ST 24,768
CV 10,72,96
M 136
CV 11,120,144
AW 192,10
M 137
T 40,8
M 126
T 44,22
M 127
W 1
AW 193,11
M 138
EF 73
EF 97
M 139
T 48,9
M 128
T 52,23
M 129
W 2
EF 121
M 140
EF 145
CV 10,73,97
M 141
WL 8
T 56,4
M 130
T 60,18
M 131
mw 27
CV 11,121,145
AW 194,10
M 142
ad 84,123
W 0
M 143
T 64,5
M 120
WL 8
T 68,19
M 121
AW 195,11
EF 74
M 132
W 1
EF 98
M 133
T 24,6
M 122
T 28,20
M 123
WL 8
R 50
M 134
W 2
EF 122
M 135
T 32,7
M 8
T 36,21
M 9
WL 8
R 52
M 10
EF 146
CV 10,74,98
M 11
WL 8
CV 11,122,146
AW 196,10
M 0
AW 197,11
EF 75
M 1
WL 2
EF 99
)"
R"(EF 123
M 2
EF 147
CV 10,75,99
M 3
CV 11,123,147
AW 198,10
M 4
AW 199,11
BV 72,1024
M 5
BV 96,1280
BV 120,1536
M 6
BV 144,1792
M 7
R 54
M 20
EF 76
EF 100
M 21
EF 124
EF 148
M 22
CV 10,76,100
CV 11,124,148
M 23
ST 26,1536
AW 200,10
M 12
AW 201,11
EF 77
M 13
EF 101
EF 125
M 14
EF 149
CV 10,77,101
M 15
CV 11,125,149
AW 202,10
M 16
AW 203,11
EF 78
M 17
EF 102
EF 126
M 18
EF 150
CV 10,78,102
M 19
WL 2
R 56
M 32
CV 11,126,150
AW 204,10
M 33
AW 205,11
EF 79
M 34
EF 103
EF 127
M 35
EF 151
CV 10,79,103
M 24
CV 11,127,151
AW 206,10
M 25
AW 207,11
BV 76,1024
M 26
BV 100,1280
BV 124,1536
M 27
BV 148,1792
M 28
EF 168
EF 192
M 29
EF 216
EF 240
M 30
CV 10,168,192
CV 11,216,240
M 31
WL 6
R 58
M 44
AW 208,10
AW 209,11
M 45
EF 169
EF 193
M 46
EF 217
EF 241
M 47
CV 10,169,193
CV 11,217,241
M 36
AW 210,10
AW 211,11
M 37
EF 170
EF 194
M 38
EF 218
EF 242
M 39
WV 23
CV 10,170,194
CV 11,218,242
M 40
WL 0
AW 212,10
AW 213,11
M 41
s_barrier
EF 171
EF 195
M 42
mw 48
EF 219
EF 243
M 43
R 72
M 56
ad 84,144
W 0
M 57
CV 10,171,195
CV 11,219,243
M 58
AW 214,10
AW 215,11
M 59
BV 168,2048
BV 192,2304
M 48
W 1
BV 216,2560
M 49
ST 28,2304
BV 240,2816
M 50
ae 44,70,192
M 51
s_bitcmp1_b32 s50, 6
s_cselect_b32 s51, 24576, 0
v_add_u32 v52, s51, v3
M 52
W 2
XL 46,0
M 53
XL 48,768
EF 172
M 54
XL 14,1536
EF 196
M 55
R 74
M 68
mw 51
XL 16,2304
EF 220
M 69
ad 84,147
W 0
M 70
EF 244
CV 10,172,196
M 71
CV 11,220,244
AW 216,10
M 60
AW 217,11
EF 173
M 61
W 1
EF 197
M 62
EF 221
EF 245
M 63
CV 10,173,197
CV 11,221,245
M 64
AW 218,10
AW 219,11
M 65
W 2
EF 174
M 66
EF 198
EF 222
M 67
WL 0
R 76
M 80
EF 246
CV 10,174,198
M 81
bc 48,3,64,56
CV 11,222,246
M 82
mh 3
AW 220,10
AW 221,11
M 83
ad 64,304
H 0
M 72
EF 175
EF 199
M 73
EF 223
EF 247
M 74
CV 10,175,199
CV 11,223,247
M 75
H 1
AW 222,10
M 76
AW 223,11
BV 172,2048
M 77
BV 196,2304
BV 220,2560
M 78
BV 244,2816
M 79
R 78
M 92
mh 5
AR 6,88
AR 7,112
M 93
ad 66,305
H 0
M 94
AR 8,136
EF 6
M 95
EF 7
EF 8
M 84
EF 56
CV 10,6,7
M 85
mh 6
CV 11,8,56
AW 224,10
M 86
ad 64,306
H 0
M 87
ae 42,74,0
AW 225,11
M 88
s_bitcmp1_b32 s50, 0
s_cselect_b32 s52, 24576, 0
v_add_u32 v53, s52, v3
M 89
ST 30,128
AR 6,89
M 90
H 1
AR 7,113
M 91
WL 0
R 80
M 104
AR 8,137
EF 6
M 105
EF 7
EF 8
M 106
bc 49,4,66,60
EF 57
M 107
mh 8
CV 10,6,7
CV 11,8,57
M 96
ad 66,400
H 0
M 97
AW 226,10
AW 227,11
M 98
AR 6,90
AR 7,114
M 99
AR 8,138
EF 6
M 100
PA 12288
DF
EF 7
M 101
EF 8
EF 58
M 102
CV 10,6,7
CV 11,8,58
M 103
R 82
M 116
AW 228,10
AW 229,11
M 117
AR 6,91
AR 7,115
M 118
AR 8,139
EF 6
M 119
EF 7
EF 8
M 108
EF 59
CV 10,6,7
M 109
CV 11,8,59
AW 230,10
M 110
AW 231,11
BA 88,0
M 111
WV 28
BA 112,256
BA 136,512
M 112
WL 3
BV 56,768
M 113
s_barrier
AR 6,92
AR 7,116
AR 8,140
M 114
mw 72
EF 6
EF 7
EF 8
M 115
R 0
EF 60
M 128
ad 84,168
W 0
CV 10,6,7
M 129
CV 11,8,60
AW 232,10
AW 233,11
M 140
AR 6,93
AR 7,117
AR 8,141
M 141
T 56,0
EF 6
M 130
T 60,14
EF 7
M 131
W 1
EF 8
EF 61
M 142
CV 10,6,7
CV 11,8,61
AW 234,10
M 143
T 64,1
AW 235,11
M 120
T 68,15
AR 6,94
M 121
W 2
AR 7,118
AR 8,142
M 132
EF 6
EF 7
EF 8
M 133
WL 8
T 24,10
EF 62
M 122
T 28,24
CV 10,6,7
M 123
mw 75
)"
R"(ST 32,896
CV 11,8,62
AW 236,10
M 134
ad 84,171
W 0
AW 237,11
M 135
T 32,11
AR 6,95
M 124
WL 8
T 36,25
AR 7,119
M 125
AR 8,143
EF 6
EF 7
M 136
W 1
EF 8
EF 63
M 137
T 40,12
CV 10,6,7
M 126
T 44,26
CV 11,8,63
M 127
WL 8
R 2
AW 238,10
M 138
W 2
AW 239,11
BA 92,0
M 139
T 48,13
BA 116,256
M 0
WL 8
T 52,27
BA 140,512
M 1
R 4
BV 60,768
M 2
EF 80
EF 104
M 3
WL 12
EF 128
EF 152
M 4
CV 10,80,104
CV 11,128,152
M 5
WL 4
AW 240,10
AW 241,11
M 6
EF 81
EF 105
M 7
EF 129
EF 153
M 8
CV 10,81,105
CV 11,129,153
M 9
AW 242,10
AW 243,11
M 10
EF 82
EF 106
M 11
R 6
M 12
EF 130
EF 154
M 13
CV 10,82,106
CV 11,130,154
M 14
AW 244,10
AW 245,11
M 15
EF 83
EF 107
M 16
EF 131
EF 155
M 17
CV 10,83,107
CV 11,131,155
M 18
AW 246,10
AW 247,11
M 19
BV 80,1024
BV 104,1280
M 20
BV 128,1536
BV 152,1792
M 21
EF 84
EF 108
M 22
EF 132
EF 156
M 23
WL 6
R 8
M 24
CV 10,84,108
CV 11,132,156
M 25
AW 248,10
AW 249,11
M 26
EF 85
EF 109
M 27
ST 34,1664
EF 133
M 28
EF 157
CV 10,85,109
M 29
CV 11,133,157
AW 250,10
M 30
AW 251,11
EF 86
M 31
EF 110
EF 134
M 32
EF 158
CV 10,86,110
M 33
CV 11,134,158
AW 252,10
M 34
AW 253,11
EF 87
M 35
WL 2
R 10
M 36
EF 111
EF 135
M 37
EF 159
CV 10,87,111
M 38
CV 11,135,159
AW 254,10
M 39
AW 255,11
BV 84,1024
M 40
BV 108,1280
BV 132,1536
M 41
BV 156,1792
M 42
EF 176
EF 200
M 43
WV 28
EF 224
EF 248
M 44
WL 4
CV 18,176,200
CV 19,224,248
M 45
s_barrier
EF 177
EF 201
M 46
mw 0
EF 225
EF 249
M 47
R 24
M 48
ad 84,192
W 0
M 49
CV 20,177,201
CV 21,225,249
M 50
EF 178
EF 202
M 51
EF 226
EF 250
M 52
W 1
CV 22,178,202
M 53
CV 23,226,250
EF 179
M 54
EF 203
EF 227
M 55
EF 251
CV 24,179,203
M 56
W 2
CV 25,227,251
M 57
WL 2
BV 176,2048
BV 200,2304
M 58
BV 224,2560
BV 248,2816
M 59
R 26
M 60
mw 3
EF 180
EF 204
M 61
ad 84,195
W 0
M 62
EF 228
EF 252
M 63
CV 26,180,204
CV 27,228,252
M 64
EF 181
EF 205
M 65
W 1
ST 36,2432
M 66
EF 229
EF 253
M 67
CV 28,181,205
CV 29,229,253
M 68
EF 182
EF 206
M 69
W 2
EF 230
M 70
EF 254
CV 30,182,206
M 71
WL 0
R 28
M 72
CV 31,230,254
EF 183
M 73
bc 48,4,64,56
EF 207
M 74
mh 9
EF 231
EF 255
M 75
ad 64,400
H 0
M 76
CV 32,183,207
CV 33,231,255
M 77
BV 180,2048
BV 204,2304
M 78
BV 228,2560
BV 252,2816
M 79
WV 27
H 1
UL 6,46
M 80
ae 44,68,192
M 81
s_bitcmp1_b32 s50, 7
s_cselect_b32 s51, 24576, 0
v_add_u32 v52, s51, v3
M 82
XL 34,0
UH 7,46
M 83
R 30
M 84
mh 11
XL 36,768
UL 8,47
M 85
ad 66,401
H 0
M 86
UH 9,47
AR 10,72
M 87
AR 11,96
AR 12,120
M 88
AR 13,144
AD 10,6
M 89
mh 12
AD 11,7
AD 12,8
M 90
ad 64,402
H 0
M 91
AD 13,9
AW 72,10
M 92
AW 96,11
AW 120,12
M 93
AW 144,13
UL 6,48
M 94
H 1
UH 7,48
M 95
WL 0
R 32
M 96
UL 8,49
UH 9,49
M 97
AR 10,73
AR 11,97
M 98
bc 49,5,66,60
AR 12,121
M 99
mh 0
AR 13,145
AD 10,6
M 100
ad 66,496
H 0
M 101
AD 11,7
AD 12,8
M 102
AD 13,9
AW 73,10
M 103
ae 42,74,0
AW 97,11
M 104
s_bfe_u32 s52, s50, 0x10001
v_mad_i32_i24 v53, v51, s52, v50
ST 38,128
M 105
s_nop 0
XL 38,1536
AW 121,12
M 106
AW 145,13
UL 6,14
M 107
R 34
M 108
UH 7,14
UL 8,15
M 109
UH 9,15
AR 10,74
M 110
AR 11,98
AR 12,122
M 111
AR 13,146
AD 10,6
M 112
AD 11,7
AD 12,8
M 113
AD 13,9
AW 74,10
M 114
AW 98,11
AW 122,12
M 115
WV 27
AW 146,13
UL 6,16
M 116
WL 0
)"
R"(UH 7,16
UL 8,17
M 117
s_barrier
UH 9,17
AR 10,75
M 118
mw 24
AR 11,99
AR 12,123
M 119
R 48
M 120
ad 84,216
W 0
M 121
AR 13,147
AD 10,6
M 132
AD 11,7
AD 12,8
M 133
T 24,2
M 122
T 28,16
M 123
W 1
AD 13,9
M 134
AW 75,10
AW 99,11
M 135
T 32,3
M 124
T 36,17
M 125
W 2
AW 123,12
M 136
AW 147,13
M 137
T 40,4
M 126
T 44,18
M 127
mw 27
M 138
ad 84,219
W 0
M 139
WL 8
T 48,5
M 128
T 52,19
M 129
M 140
W 1
M 141
T 56,6
M 130
WL 8
T 60,20
M 131
R 50
M 142
W 2
ST 40,896
M 143
T 64,7
M 0
WL 8
T 68,21
M 1
R 52
M 2
XL 40,2304
M 3
M 4
ae 44,68,192
M 5
M 6
s_bfe_u32 s51, s50, 0x10008
v_mad_i32_i24 v52, v51, s51, v50
XL 46,0
M 7
WL 8
XL 48,768
M 8
XL 14,1536
M 9
WL 2
XL 16,2304
M 10
M 11
R 54
M 12
M 13
M 14
M 15
M 16
M 17
M 18
M 19
M 20
M 21
M 22
M 23
WL 2
R 56
M 24
M 25
M 26
M 27
M 28
M 29
M 30
M 31
M 32
M 33
M 34
M 35
WL 2
R 58
M 36
ST 42,1664
M 37
M 38
M 39
M 40
M 41
M 42
WV 20
UL 6,34
UH 7,34
M 43
UL 8,35
UH 9,35
M 44
WL 0
AR 10,76
AR 11,100
M 45
s_barrier
AR 12,124
AR 13,148
M 46
mw 48
AD 10,6
AD 11,7
M 47
R 72
M 48
ad 84,240
W 0
M 49
AD 12,8
AD 13,9
M 50
AW 76,10
AW 100,11
M 51
AW 124,12
AW 148,13
M 52
WV 20
W 1
UL 6,36
M 53
UH 7,36
UL 8,37
M 54
UH 9,37
AR 10,77
M 55
AR 11,101
AR 12,125
M 56
W 2
AR 13,149
M 57
AD 10,6
AD 11,7
M 58
AD 12,8
AD 13,9
M 59
R 74
M 60
mw 51
AW 77,10
AW 101,11
M 61
ad 84,243
W 0
M 62
AW 125,12
AW 149,13
M 63
WV 17
UL 6,38
UH 7,38
M 64
UL 8,39
UH 9,39
M 65
W 1
AR 10,78
M 66
AR 11,102
AR 12,126
M 67
AR 13,150
AD 10,6
M 68
AD 11,7
AD 12,8
M 69
W 2
AD 13,9
M 70
AW 78,10
AW 102,11
M 71
WL 0
R 76
M 72
WV 11
AW 126,12
AW 150,13
M 73
bc 48,5,64,56
UL 6,40
M 74
mh 1
ST 44,2432
UH 7,40
M 75
ad 64,496
H 0
M 76
UL 8,41
UH 9,41
M 77
AR 10,79
AR 11,103
M 78
AR 12,127
AR 13,151
M 79
H 1
AD 10,6
M 80
AD 11,7
AD 12,8
M 81
AD 13,9
AW 79,10
M 82
AW 103,11
AW 127,12
M 83
R 78
M 84
mh 3
AW 151,13
M 85
ad 66,497
H 0
M 86
M 87
M 88
M 89
mh 4
M 90
ad 64,498
H 0
M 91
M 92
M 93
M 94
H 1
M 95
WL 2
R 80
M 96
M 97
M 98
bc 49,6,66,60
M 99
mh 6
M 100
ad 66,592
H 0
M 101
M 102
M 103
M 104
M 105
M 106
M 107
WL 2
R 82
M 108
M 109
M 110
M 111
M 112
ae 42,74,0
M 113
s_bitcmp1_b32 s50, 0
s_cselect_b32 s52, 24576, 0
v_add_u32 v53, s52, v3
M 114
SA 160,256
M 115
M 116
WL 0
M 117
s_barrier
M 118
mw 72
M 119
R 0
M 120
ad 84,264
W 0
M 121
M 132
M 133
T 24,8
M 122
T 28,22
M 123
W 1
M 134
M 135
T 32,9
M 124
T 36,23
M 125
W 2
M 136
M 137
T 40,10
M 126
T 44,24
M 127
mw 75
M 138
ad 84,267
W 0
M 139
WL 8
T 48,11
M 128
T 52,25
M 129
ae 44,70,192
M 140
s_bfe_u32 s51, s50, 0x10009
W 1
v_mad_i32_i24 v52, v51, s51, v50
M 141
T 56,12
M 130
WL 8
T 60,26
M 131
R 2
M 142
W 2
XL 42,0
M 143
T 64,13
M 0
WL 8
T 68,27
M 1
R 4
M 2
XL 44,768
M 3
XL 34,1536
M 4
M 5
M 6
M 7
WL 8
M 8
SA 162,1024
XL 36,2304
M 9
WL 2
M 10
M 11
R 6
M 12
M 13
M 14
M 15
)"
R"(M 16
M 17
M 18
M 19
M 20
M 21
M 22
M 23
WL 2
R 8
M 24
M 25
M 26
M 27
M 28
M 29
M 30
M 31
M 32
M 33
M 34
M 35
WL 2
R 10
M 36
M 37
M 38
M 39
M 40
M 41
M 42
M 43
M 44
WV 26
UL 6,46
UH 7,46
M 45
WL 0
s_barrier
UL 8,47
UH 9,47
M 46
mw 0
SA 164,1792
AR 10,80
M 47
R 24
M 48
ad 84,288
W 0
M 49
AR 11,104
AR 12,128
M 50
AR 13,152
AD 10,6
M 51
AD 11,7
AD 12,8
M 52
W 1
AD 13,9
M 53
AW 80,10
AW 104,11
M 54
AW 128,12
AW 152,13
M 55
UL 6,48
UH 7,48
M 56
W 2
UL 8,49
M 57
UH 9,49
AR 10,81
M 58
AR 11,105
AR 12,129
M 59
R 26
M 60
mw 3
AR 13,153
AD 10,6
M 61
ad 84,291
W 0
M 62
AD 11,7
AD 12,8
M 63
AD 13,9
AW 81,10
M 64
AW 105,11
AW 129,12
M 65
W 1
AW 153,13
M 66
UL 6,14
UH 7,14
M 67
UL 8,15
UH 9,15
M 68
AR 10,82
AR 11,106
M 69
W 2
AR 12,130
M 70
AR 13,154
AD 10,6
M 71
WL 0
R 28
M 72
AD 11,7
AD 12,8
M 73
bc 48,6,64,56
AD 13,9
M 74
mh 7
AW 82,10
AW 106,11
M 75
ad 64,592
H 0
M 76
AW 130,12
AW 154,13
M 77
UL 6,16
UH 7,16
M 78
UL 8,17
UH 9,17
M 79
H 1
AR 10,83
M 80
AR 11,107
AR 12,131
M 81
AR 13,155
AD 10,6
M 82
AD 11,7
AD 12,8
M 83
R 30
M 84
mh 9
SA 166,2560
AD 13,9
M 85
ad 66,593
H 0
M 86
AW 83,10
AW 107,11
M 87
AW 131,12
AW 155,13
M 88
M 89
mh 10
M 90
ad 64,594
H 0
M 91
M 92
M 93
M 94
H 1
M 95
WL 2
R 32
M 96
M 97
M 98
bc 49,7,66,60
M 99
mh 12
M 100
ad 66,688
H 0
M 101
M 102
M 103
M 104
bc 48,7,64,56
M 105
mh 13
M 106
ad 64,688
H 0
M 107
WL 2
R 34
M 108
M 109
M 110
M 111
M 112
M 113
M 114
M 115
M 116
WV 28
M 117
WL 0
s_barrier
M 118
mw 24
M 119
R 48
M 120
ad 84,312
W 0
M 121
M 132
ae 42,74,0
M 133
s_bfe_u32 s52, s50, 0x10001
T 24,0
M 122
T 28,14
M 123
W 1
v_mad_i32_i24 v53, v51, s52, v50
M 134
SA 168,256
M 135
T 32,1
M 124
T 36,15
M 125
W 2
M 136
M 137
T 40,2
M 126
T 44,16
M 127
mw 27
M 138
ad 84,315
W 0
M 139
WL 8
T 48,3
M 128
T 52,17
M 129
ae 44,68,240
M 140
W 1
M 141
T 56,4
M 130
WL 8
T 60,18
M 131
R 50
M 142
W 2
M 143
T 64,5
M 0
WL 8
T 68,19
M 1
R 52
M 2
s_bitcmp1_b32 s50, 10
s_cselect_b32 s51, 24576, 0
M 3
v_add_u32 v52, s51, v3
XL 38,0
M 4
XL 40,768
M 5
XL 46,1536
M 6
M 7
WL 8
M 8
M 9
WL 2
M 10
XL 48,2304
M 11
R 54
M 12
M 13
M 14
M 15
M 16
M 17
M 18
M 19
M 20
SA 170,1024
M 21
M 22
M 23
WL 2
R 56
M 24
M 25
M 26
M 27
M 28
M 29
M 30
M 31
M 32
M 33
M 34
M 35
WL 2
R 58
M 36
M 37
M 38
M 39
M 40
M 41
M 42
M 43
M 44
WV 29
M 45
WL 0
s_barrier
M 46
mw 48
UL 6,42
UH 7,42
M 47
R 72
M 48
ad 84,336
W 0
M 49
UL 8,43
UH 9,43
M 50
AR 10,84
AR 11,108
M 51
AR 12,132
AR 13,156
M 52
W 1
AD 10,6
M 53
AD 11,7
AD 12,8
M 54
AD 13,9
AW 84,10
M 55
AW 108,11
AW 132,12
M 56
W 2
AW 156,13
M 57
UL 6,44
UH 7,44
M 58
SA 172,1792
UL 8,45
M 59
R 74
M 60
mw 51
UH 9,45
AR 10,85
M 61
ad 84,339
W 0
M 62
AR 11,109
AR 12,133
M 63
AR 13,157
AD 10,6
M 64
AD 11,7
AD 12,8
M 65
W 1
AD 13,9
)"
R"(M 66
AW 85,10
AW 109,11
M 67
AW 133,12
AW 157,13
M 68
UL 6,34
UH 7,34
M 69
W 2
UL 8,35
M 70
UH 9,35
AR 10,86
M 71
WL 0
R 76
M 72
AR 11,110
AR 12,134
M 73
mh 0
AR 13,158
AD 10,6
M 74
ad 64,689
H 0
M 75
AD 11,7
AD 12,8
M 76
AD 13,9
AW 86,10
M 77
WV 35
AW 110,11
AW 134,12
M 78
mh 1
AW 158,13
UL 6,36
M 79
ad 66,689
H 0
M 80
UH 7,36
UL 8,37
M 81
UH 9,37
AR 10,87
M 82
AR 11,111
AR 12,135
M 83
R 78
M 84
mh 2
AR 13,159
AD 10,6
M 85
ad 64,690
H 0
M 86
AD 11,7
AD 12,8
M 87
AD 13,9
AW 87,10
M 88
AW 111,11
AW 135,12
M 89
H 1
AW 159,13
M 90
M 91
M 92
M 93
bc 49,8,66,60
M 94
mh 4
M 95
WL 2
R 80
M 96
ad 66,784
H 0
M 97
SA 174,2560
M 98
M 99
M 100
PA 12288
DF
M 101
M 102
M 103
M 104
M 105
M 106
M 107
WL 2
R 82
M 108
M 109
M 110
M 111
M 112
M 113
M 114
M 115
M 116
WV 28
M 117
WL 0
s_barrier
M 118
mw 72
M 119
R 0
M 120
ad 84,360
W 0
M 121
M 132
M 133
T 24,6
M 122
T 28,20
M 123
W 1
M 134
M 135
T 32,7
M 124
T 36,21
M 125
W 2
M 136
M 137
T 40,8
M 126
T 44,22
M 127
mw 75
M 138
ad 84,363
W 0
M 139
WL 8
T 48,9
M 128
T 52,23
M 129
ae 42,72,0
M 140
W 1
M 141
T 56,10
M 130
WL 8
T 60,24
M 131
R 2
M 142
W 2
M 143
T 64,11
M 0
WL 8
T 68,25
M 1
R 4
M 2
s_bitcmp1_b32 s50, 2
s_cselect_b32 s52, 24576, 0
M 3
v_add_u32 v53, s52, v3
SA 176,0
M 4
ae 44,68,240
M 5
M 6
s_bfe_u32 s51, s50, 0x1000b
v_mad_i32_i24 v52, v51, s51, v50
XL 14,0
M 7
WL 8
XL 16,768
M 8
XL 42,1536
M 9
WL 2
M 10
M 11
R 6
M 12
XL 44,2304
M 13
M 14
M 15
M 16
M 17
M 18
M 19
M 20
M 21
M 22
M 23
WL 2
R 8
M 24
M 25
M 26
M 27
M 28
M 29
M 30
M 31
M 32
M 33
M 34
M 35
WL 2
R 10
M 36
M 37
M 38
M 39
M 40
M 41
SA 178,768
M 42
M 43
WV 31
M 44
WL 0
M 45
s_barrier
M 46
mw 0
M 47
R 24
M 48
ad 84,384
W 0
M 49
WV 28
UL 6,38
UH 7,38
M 50
UL 8,39
UH 9,39
M 51
AR 10,88
AR 11,112
M 52
W 1
AR 12,136
M 53
AD 10,6
AD 11,7
M 54
AD 12,8
AD 56,9
M 55
AW 88,10
AW 112,11
M 56
W 2
AW 136,12
M 57
UL 6,40
UH 7,40
M 58
UL 8,41
UH 9,41
M 59
R 26
M 60
mw 3
AR 10,89
AR 11,113
M 61
ad 84,387
W 0
M 62
AR 12,137
AD 10,6
M 63
AD 11,7
AD 12,8
M 64
AD 57,9
AW 89,10
M 65
W 1
AW 113,11
M 66
AW 137,12
UL 6,46
M 67
UH 7,46
UL 8,47
M 68
UH 9,47
AR 10,90
M 69
W 2
AR 11,114
M 70
AR 12,138
AD 10,6
M 71
WL 0
R 28
M 72
AD 11,7
AD 12,8
M 73
bc 48,8,64,56
AD 58,9
M 74
mh 5
AW 90,10
AW 114,11
M 75
ad 64,784
H 0
M 76
AW 138,12
UL 6,48
M 77
UH 7,48
UL 8,49
M 78
UH 9,49
AR 10,91
M 79
H 1
SA 180,1536
M 80
AR 11,115
AR 12,139
M 81
AD 10,6
AD 11,7
M 82
AD 12,8
AD 59,9
M 83
R 30
M 84
mh 7
AW 91,10
AW 115,11
M 85
ad 66,785
H 0
M 86
AW 139,12
M 87
M 88
M 89
mh 8
M 90
ad 64,786
H 0
M 91
M 92
M 93
M 94
H 1
M 95
WL 2
R 32
M 96
M 97
M 98
bc 49,9,66,60
M 99
mh 10
M 100
ad 66,880
H 0
M 101
M 102
M 103
M 104
M 105
M 106
M 107
WL 2
R 34
M 108
M 109
M 110
M 111
M 112
)"
R"(M 113
M 114
M 115
M 116
WV 28
M 117
WL 0
s_barrier
SA 182,2304
M 118
mw 24
M 119
R 48
M 120
ad 84,408
W 0
M 121
M 132
M 133
T 24,12
M 122
T 28,26
M 123
W 1
M 134
M 135
T 32,13
M 124
T 36,27
M 125
W 2
M 136
M 137
T 40,0
M 126
T 44,14
M 127
mw 27
M 138
ad 84,411
W 0
M 139
WL 8
T 48,1
M 128
T 52,15
M 129
M 140
W 1
M 141
T 56,2
M 130
WL 8
T 60,16
M 131
R 50
M 142
W 2
M 143
T 64,3
M 0
WL 8
T 68,17
M 1
R 52
M 2
M 3
M 4
M 5
M 6
M 7
WL 8
M 8
M 9
WL 2
M 10
M 11
R 54
M 12
ae 42,72,0
M 13
s_bfe_u32 s52, s50, 0x10003
v_mad_i32_i24 v53, v51, s52, v50
SA 184,0
M 14
M 15
M 16
M 17
M 18
M 19
M 20
M 21
M 22
M 23
WL 2
R 56
M 24
M 25
M 26
M 27
M 28
M 29
M 30
M 31
M 32
M 33
M 34
M 35
WL 2
R 58
M 36
M 37
M 38
M 39
M 40
M 41
M 42
M 43
M 44
WV 27
M 45
WL 0
s_barrier
M 46
mw 48
M 47
R 72
M 48
ad 84,432
W 0
M 49
M 50
WV 23
UL 6,14
UH 7,14
M 51
SA 186,768
UL 8,15
M 52
W 1
UH 9,15
M 53
AR 10,92
AR 11,116
M 54
AR 12,140
AD 10,6
M 55
AD 11,7
AD 12,8
M 56
W 2
AD 60,9
M 57
AW 92,10
AW 116,11
M 58
AW 140,12
UL 6,16
M 59
R 74
M 60
mw 51
UH 7,16
UL 8,17
M 61
ad 84,435
W 0
M 62
UH 9,17
AR 10,93
M 63
AR 11,117
AR 12,141
M 64
AD 10,6
AD 11,7
M 65
W 1
AD 12,8
M 66
AD 61,9
AW 93,10
M 67
AW 117,11
AW 141,12
M 68
UL 6,42
UH 7,42
M 69
W 2
UL 8,43
M 70
UH 9,43
AR 10,94
M 71
WL 0
R 76
M 72
AR 11,118
AR 12,142
M 73
bc 48,9,64,56
AD 10,6
M 74
mh 11
AD 11,7
AD 12,8
M 75
ad 64,880
H 0
M 76
AD 62,9
AW 94,10
M 77
AW 118,11
AW 142,12
M 78
UL 6,44
UH 7,44
M 79
H 1
UL 8,45
M 80
UH 9,45
AR 10,95
M 81
AR 11,119
AR 12,143
M 82
AD 10,6
AD 11,7
M 83
R 78
M 84
mh 13
AD 12,8
AD 63,9
M 85
ad 66,881
H 0
M 86
AW 95,10
AW 119,11
M 87
AW 143,12
M 88
M 89
mh 0
SA 188,1536
M 90
ad 64,882
H 0
M 91
M 92
M 93
M 94
H 1
M 95
WL 2
R 80
M 96
M 97
M 98
bc 49,10,66,60
M 99
mh 2
M 100
ad 66,976
H 0
M 101
M 102
M 103
M 104
M 105
M 106
M 107
WL 2
R 82
M 108
M 109
M 110
M 111
M 112
M 113
M 114
M 115
M 116
WV 23
M 117
WL 0
s_barrier
M 118
mw 72
M 119
R 0
M 120
ad 84,456
W 0
M 121
M 132
M 133
T 24,4
M 122
T 28,18
M 123
W 1
M 134
M 135
T 32,5
M 124
T 36,19
M 125
W 2
SA 190,2304
M 136
M 137
T 40,6
M 126
T 44,20
M 127
mw 75
M 138
ad 84,459
W 0
M 139
WL 8
T 48,7
M 128
T 52,21
M 129
M 140
W 1
M 141
T 56,8
M 130
WL 8
T 60,22
M 131
R 2
M 142
W 2
M 143
T 64,9
M 0
WL 8
T 68,23
M 1
R 4
M 2
M 3
M 4
M 5
M 6
M 7
WL 8
M 8
M 9
WL 2
M 10
M 11
R 6
M 12
M 13
M 14
M 15
M 16
M 17
M 18
M 19
M 20
M 21
M 22
M 23
WL 2
R 8
M 24
ae 42,72,0
M 25
s_bitcmp1_b32 s50, 2
s_cselect_b32 s52, 24576, 0
v_add_u32 v53, s52, v3
M 26
SA 192,128
M 27
M 28
M 29
M 30
M 31
M 32
ae 44,70,192
M 33
s_bitcmp1_b32 s50, 6
s_cselect_b32 s51, 24576, 0
v_add_u32 v52, s51, v3
M 34
XL 34,128
M 35
WL 2
R 10
)"
R"(M 36
M 37
M 38
XL 36,896
M 39
M 40
M 41
M 42
M 43
WV 25
M 44
WL 0
XL 38,1664
M 45
s_barrier
M 46
mw 0
M 47
R 24
M 48
ad 84,480
W 0
M 49
M 50
XL 40,2432
M 51
M 52
W 1
M 53
M 54
M 55
M 56
W 2
M 57
M 58
M 59
R 26
M 60
mw 3
M 61
ad 84,483
W 0
M 62
M 63
M 64
SA 194,896
M 65
W 1
M 66
M 67
M 68
M 69
W 2
M 70
M 71
WL 2
R 28
M 72
M 73
bc 48,10,64,56
M 74
mh 3
M 75
ad 64,976
H 0
M 76
M 77
M 78
M 79
H 1
M 80
M 81
M 82
M 83
WL 2
R 30
M 84
mh 5
M 85
ad 66,977
H 0
M 86
M 87
M 88
M 89
mh 6
M 90
ad 64,978
H 0
M 91
M 92
M 93
M 94
H 1
M 95
WL 2
R 32
M 96
M 97
M 98
bc 49,11,66,60
M 99
mh 8
M 100
ad 66,1072
H 0
M 101
M 102
SA 196,1664
M 103
M 104
M 105
M 106
M 107
WL 2
R 34
M 108
M 109
M 110
M 111
M 112
M 113
M 114
M 115
M 116
WV 27
M 117
WL 0
s_barrier
M 118
mw 24
M 119
R 48
M 120
ad 84,504
W 0
M 121
M 132
M 133
T 24,10
M 122
T 28,24
M 123
W 1
M 134
M 135
T 32,11
M 124
T 36,25
M 125
W 2
M 136
M 137
T 40,12
M 126
T 44,26
M 127
mw 27
M 138
ad 84,507
W 0
M 139
WL 8
T 48,13
M 128
T 52,27
M 129
M 140
W 1
M 141
T 56,0
M 130
WL 8
T 60,14
M 131
R 50
M 142
W 2
SA 198,2432
M 143
T 64,1
M 0
WL 8
T 68,15
M 1
R 52
M 2
M 3
M 4
M 5
M 6
M 7
WL 8
M 8
M 9
WL 2
M 10
M 11
R 54
M 12
M 13
M 14
M 15
M 16
M 17
M 18
M 19
M 20
M 21
M 22
M 23
WL 2
R 56
M 24
M 25
M 26
M 27
M 28
M 29
M 30
M 31
M 32
M 33
M 34
ae 44,68,192
M 35
WL 2
R 58
M 36
ae 42,72,0
M 37
s_bfe_u32 s52, s50, 0x10003
v_mad_i32_i24 v53, v51, s52, v50
SA 200,128
M 38
s_bitcmp1_b32 s50, 7
s_cselect_b32 s51, 24576, 0
v_add_u32 v52, s51, v3
M 39
XL 46,128
M 40
XL 48,896
M 41
M 42
M 43
WV 29
M 44
WL 0
M 45
s_barrier
M 46
mw 48
XL 14,1664
M 47
R 72
M 48
ad 84,528
W 0
M 49
M 50
M 51
M 52
W 1
XL 16,2432
M 53
M 54
M 55
M 56
W 2
M 57
M 58
M 59
R 74
M 60
mw 51
M 61
ad 84,531
W 0
M 62
M 63
M 64
M 65
W 1
M 66
M 67
M 68
M 69
W 2
M 70
M 71
WL 2
R 76
M 72
M 73
bc 48,11,64,56
M 74
mh 9
M 75
ad 64,1072
H 0
M 76
SA 202,896
M 77
M 78
M 79
H 1
M 80
M 81
M 82
M 83
WL 2
R 78
M 84
mh 11
M 85
ad 66,1073
H 0
M 86
M 87
M 88
WV 37
UL 6,34
UH 7,34
M 89
mh 12
UL 8,35
UH 9,35
M 90
ad 64,1074
H 0
M 91
AD 64,6
AD 88,7
M 92
AD 112,8
AD 136,9
M 93
UL 6,36
UH 7,36
M 94
H 1
UL 8,37
M 95
WL 2
R 80
M 96
UH 9,37
AD 65,6
M 97
AD 89,7
AD 113,8
M 98
bc 49,12,66,60
AD 137,9
M 99
mh 0
UL 6,38
UH 7,38
M 100
ad 66,1168
H 0
M 101
UL 8,39
UH 9,39
M 102
AD 66,6
AD 90,7
M 103
WV 38
AD 114,8
AD 138,9
M 104
PA 12288
DF
UL 6,40
M 105
UH 7,40
UL 8,41
M 106
UH 9,41
AD 67,6
M 107
WL 0
R 82
M 108
AD 91,7
AD 115,8
M 109
AD 139,9
M 110
M 111
M 112
M 113
M 114
SA 204,1664
M 115
WV 29
M 116
WL 0
M 117
)"
R"(s_barrier
M 118
mw 72
M 119
R 0
M 120
ad 84,552
W 0
M 121
M 132
M 133
T 24,2
M 122
T 28,16
M 123
W 1
M 134
M 135
T 32,3
M 124
T 36,17
M 125
W 2
M 136
M 137
T 40,4
M 126
T 44,18
M 127
mw 75
M 138
ad 84,555
W 0
M 139
WL 8
T 48,5
M 128
T 52,19
M 129
M 140
W 1
M 141
T 56,6
M 130
WL 8
T 60,20
M 131
R 2
M 142
W 2
M 143
T 64,7
M 0
WL 8
T 68,21
M 1
R 4
M 2
M 3
M 4
M 5
M 6
M 7
WL 8
M 8
SA 206,2432
M 9
WL 2
M 10
M 11
R 6
M 12
M 13
M 14
M 15
M 16
M 17
M 18
M 19
M 20
M 21
M 22
M 23
WL 2
R 8
M 24
M 25
M 26
M 27
M 28
M 29
M 30
M 31
M 32
M 33
M 34
M 35
WL 2
R 10
M 36
ae 44,68,192
M 37
s_bfe_u32 s51, s50, 0x10008
v_mad_i32_i24 v52, v51, s51, v50
XL 42,128
M 38
M 39
M 40
M 41
M 42
XL 44,896
M 43
WV 30
M 44
WL 0
M 45
s_barrier
M 46
mw 0
M 47
R 24
M 48
ad 84,576
W 0
M 49
ae 42,72,0
XL 34,1664
M 50
s_bitcmp1_b32 s50, 2
s_cselect_b32 s52, 24576, 0
v_add_u32 v53, s52, v3
M 51
SA 208,256
M 52
W 1
M 53
M 54
XL 36,2432
M 55
M 56
W 2
M 57
M 58
M 59
R 26
M 60
mw 3
M 61
ad 84,579
W 0
M 62
M 63
M 64
M 65
W 1
M 66
M 67
M 68
M 69
W 2
M 70
M 71
WL 2
R 28
M 72
M 73
bc 48,12,64,56
M 74
mh 1
M 75
ad 64,1168
H 0
M 76
M 77
M 78
M 79
H 1
M 80
M 81
M 82
M 83
WL 2
R 30
M 84
mh 3
M 85
ad 66,1169
H 0
M 86
M 87
M 88
M 89
mh 4
SA 210,1024
M 90
ad 64,1170
H 0
M 91
WV 39
UL 6,46
UH 7,46
M 92
UL 8,47
UH 9,47
M 93
AD 68,6
AD 92,7
M 94
H 1
AD 116,8
M 95
WL 2
R 32
M 96
AD 140,9
UL 6,48
M 97
UH 7,48
UL 8,49
M 98
bc 49,13,66,60
UH 9,49
M 99
mh 6
AD 69,6
AD 93,7
M 100
ad 66,1264
H 0
M 101
AD 117,8
AD 141,9
M 102
UL 6,14
UH 7,14
M 103
UL 8,15
UH 9,15
M 104
AD 70,6
AD 94,7
M 105
AD 118,8
AD 142,9
M 106
WV 38
UL 6,16
UH 7,16
M 107
WL 0
R 34
M 108
UL 8,17
UH 9,17
M 109
AD 71,6
AD 95,7
M 110
AD 119,8
AD 143,9
M 111
M 112
M 113
M 114
M 115
WV 28
M 116
WL 0
M 117
s_barrier
M 118
mw 24
M 119
R 48
M 120
ad 84,600
W 0
M 121
M 132
M 133
T 24,8
M 122
T 28,22
M 123
W 1
M 134
M 135
T 32,9
M 124
T 36,23
M 125
W 2
SA 212,1792
M 136
M 137
T 40,10
M 126
T 44,24
M 127
mw 27
M 138
ad 84,603
W 0
M 139
WL 8
T 48,11
M 128
T 52,25
M 129
M 140
W 1
M 141
T 56,12
M 130
WL 8
T 60,26
M 131
R 50
M 142
W 2
M 143
T 64,13
M 0
WL 8
T 68,27
M 1
R 52
M 2
M 3
M 4
M 5
M 6
M 7
WL 8
M 8
M 9
WL 2
M 10
M 11
R 54
M 12
M 13
M 14
M 15
M 16
M 17
M 18
M 19
M 20
M 21
M 22
M 23
WL 2
R 56
M 24
SA 214,2560
M 25
M 26
M 27
M 28
M 29
M 30
M 31
M 32
M 33
M 34
M 35
WL 2
R 58
M 36
M 37
M 38
ae 44,70,192
M 39
s_bfe_u32 s51, s50, 0x10009
v_mad_i32_i24 v52, v51, s51, v50
XL 38,128
M 40
M 41
M 42
M 43
WV 28
M 44
WL 0
XL 40,896
M 45
s_barrier
M 46
mw 48
M 47
R 72
M 48
ad 84,624
W 0
M 49
M 50
XL 46,1664
M 51
M 52
W 1
)"
R"(M 53
M 54
M 55
M 56
W 2
XL 48,2432
M 57
M 58
M 59
R 74
M 60
mw 51
M 61
ad 84,627
W 0
M 62
ae 42,72,0
M 63
s_bfe_u32 s52, s50, 0x10003
v_mad_i32_i24 v53, v51, s52, v50
SA 216,256
M 64
M 65
W 1
M 66
M 67
M 68
M 69
W 2
M 70
M 71
WL 2
R 76
M 72
M 73
bc 48,13,64,56
M 74
mh 7
M 75
ad 64,1264
H 0
M 76
M 77
M 78
M 79
H 1
M 80
M 81
M 82
M 83
WL 2
R 78
M 84
mh 9
M 85
ad 66,1265
H 0
M 86
M 87
M 88
M 89
mh 10
M 90
ad 64,1266
H 0
M 91
M 92
WV 39
UL 6,42
UH 7,42
M 93
UL 8,43
UH 9,43
M 94
H 1
AD 72,6
M 95
WL 2
R 80
M 96
AD 96,7
AD 120,8
M 97
AD 144,9
UL 6,44
M 98
bc 49,14,66,60
UH 7,44
M 99
mh 12
UL 8,45
UH 9,45
M 100
ad 66,1360
H 0
M 101
SA 218,1024
AD 73,6
M 102
AD 97,7
AD 121,8
M 103
WV 40
AD 145,9
UL 6,34
M 104
bc 48,14,64,56
UH 7,34
M 105
mh 13
UL 8,35
UH 9,35
M 106
ad 64,1360
H 0
M 107
WL 0
R 82
M 108
AD 74,6
AD 98,7
M 109
AD 122,8
AD 146,9
M 110
WV 38
UL 6,36
UH 7,36
M 111
UL 8,37
UH 9,37
M 112
AD 75,6
AD 99,7
M 113
AD 123,8
AD 147,9
M 114
M 115
WV 28
M 116
WL 0
M 117
s_barrier
M 118
mw 72
M 119
R 0
M 120
ad 84,648
W 0
M 121
M 132
M 133
T 24,0
M 122
T 28,14
M 123
W 1
M 134
M 135
T 32,1
M 124
T 36,15
M 125
W 2
M 136
M 137
T 40,2
M 126
T 44,16
M 127
mw 75
M 138
ad 84,651
W 0
M 139
WL 8
T 48,3
M 128
T 52,17
M 129
M 140
W 1
M 141
T 56,4
M 130
WL 8
T 60,18
M 131
R 2
M 142
W 2
SA 220,1792
M 143
T 64,5
M 0
WL 8
T 68,19
M 1
R 4
M 2
M 3
M 4
M 5
M 6
M 7
WL 8
M 8
M 9
WL 2
M 10
M 11
R 6
M 12
M 13
M 14
M 15
M 16
M 17
M 18
M 19
M 20
M 21
M 22
M 23
WL 2
R 8
M 24
M 25
M 26
M 27
M 28
M 29
M 30
M 31
M 32
M 33
M 34
M 35
WL 2
R 10
M 36
SA 222,2560
M 37
M 38
M 39
M 40
ae 44,68,240
M 41
s_bitcmp1_b32 s50, 10
s_cselect_b32 s51, 24576, 0
v_add_u32 v52, s51, v3
M 42
XL 14,128
M 43
WV 29
M 44
WL 0
M 45
s_barrier
M 46
mw 0
XL 16,896
M 47
R 24
M 48
ad 84,672
W 0
M 49
M 50
M 51
M 52
W 1
XL 42,1664
M 53
M 54
M 55
M 56
W 2
M 57
M 58
XL 44,2432
M 59
R 26
M 60
mw 3
M 61
ad 84,675
W 0
M 62
M 63
M 64
M 65
W 1
M 66
M 67
M 68
M 69
W 2
M 70
M 71
WL 2
R 28
M 72
M 73
mh 0
M 74
ad 64,1361
H 0
M 75
ae 42,72,48
M 76
s_bitcmp1_b32 s50, 4
s_cselect_b32 s52, 24576, 0
v_add_u32 v53, s52, v3
M 77
SA 224,0
M 78
mh 1
M 79
ad 66,1361
H 0
M 80
M 81
M 82
M 83
WL 2
R 30
M 84
mh 2
M 85
ad 64,1362
H 0
M 86
M 87
M 88
M 89
H 1
M 90
M 91
M 92
WV 40
M 93
bc 49,15,66,60
M 94
mh 4
UL 6,38
UH 7,38
M 95
WL 2
R 32
M 96
ad 66,1456
H 0
M 97
UL 8,39
UH 9,39
M 98
AD 76,6
AD 100,7
M 99
AD 124,8
AD 148,9
M 100
UL 6,40
UH 7,40
M 101
UL 8,41
UH 9,41
M 102
AD 77,6
AD 101,7
M 103
AD 125,8
AD 149,9
M 104
WV 39
UL 6,46
UH 7,46
M 105
UL 8,47
UH 9,47
M 106
AD 78,6
AD 102,7
M 107
WL 1
R 34
M 108
AD 126,8
AD 150,9
M 109
WV 36
UL 6,48
UH 7,48
M 110
UL 8,49
UH 9,49
M 111
)"
R"(AD 79,6
AD 103,7
M 112
AD 127,8
AD 151,9
M 113
M 114
M 115
WV 27
SA 226,768
M 116
WL 0
M 117
s_barrier
M 118
mw 24
M 119
R 48
M 120
ad 84,696
W 0
M 121
M 132
M 133
T 24,6
M 122
T 28,20
M 123
W 1
M 134
M 135
T 32,7
M 124
T 36,21
M 125
W 2
M 136
M 137
T 40,8
M 126
T 44,22
M 127
mw 27
M 138
ad 84,699
W 0
M 139
WL 8
T 48,9
M 128
T 52,23
M 129
M 140
W 1
M 141
T 56,10
M 130
WL 8
T 60,24
M 131
R 50
M 142
W 2
M 143
T 64,11
M 0
WL 8
T 68,25
M 1
R 52
M 2
M 3
M 4
M 5
M 6
M 7
WL 8
M 8
M 9
WL 2
SA 228,1536
M 10
M 11
R 54
M 12
M 13
M 14
M 15
M 16
M 17
M 18
M 19
M 20
M 21
M 22
M 23
WL 2
R 56
M 24
M 25
M 26
M 27
M 28
M 29
M 30
M 31
M 32
M 33
M 34
M 35
WL 2
R 58
M 36
M 37
M 38
M 39
M 40
M 41
M 42
ae 44,68,240
s_bfe_u32 s51, s50, 0x1000b
M 43
WV 26
v_mad_i32_i24 v52, v51, s51, v50
XL 34,128
M 44
WL 0
M 45
s_barrier
M 46
mw 48
M 47
R 72
M 48
ad 84,720
W 0
M 49
SA 230,2304
XL 36,896
M 50
M 51
M 52
W 1
M 53
M 54
XL 38,1664
M 55
M 56
W 2
M 57
M 58
M 59
R 74
M 60
mw 51
XL 40,2432
M 61
ad 84,723
W 0
M 62
M 63
M 64
M 65
W 1
M 66
M 67
M 68
M 69
W 2
M 70
M 71
WL 2
R 76
M 72
M 73
bc 48,15,64,56
M 74
mh 5
M 75
ad 64,1456
H 0
M 76
M 77
M 78
M 79
H 1
M 80
M 81
M 82
M 83
WL 2
R 78
M 84
mh 7
M 85
ad 66,1457
H 0
M 86
M 87
ae 42,72,48
M 88
s_bfe_u32 s52, s50, 0x10005
v_mad_i32_i24 v53, v51, s52, v50
SA 232,0
M 89
mh 8
M 90
ad 64,1458
H 0
M 91
M 92
M 93
M 94
H 1
M 95
WL 2
R 80
M 96
WV 39
UL 6,14
UH 7,14
M 97
UL 8,15
UH 9,15
M 98
bc 49,16,66,60
AD 80,6
M 99
mh 10
AD 104,7
AD 128,8
M 100
ad 66,1552
H 0
M 101
AD 152,9
UL 6,16
M 102
UH 7,16
UL 8,17
M 103
UH 9,17
AD 81,6
M 104
PA 12288
DF
AD 105,7
M 105
AD 129,8
AD 153,9
M 106
WV 38
UL 6,42
UH 7,42
M 107
WL 0
R 82
M 108
UL 8,43
UH 9,43
M 109
AD 82,6
AD 106,7
M 110
AD 130,8
AD 154,9
M 111
WV 36
UL 6,44
UH 7,44
M 112
UL 8,45
UH 9,45
M 113
AD 83,6
AD 107,7
M 114
AD 131,8
AD 155,9
M 115
WV 28
M 116
WL 0
M 117
s_barrier
M 118
mw 72
M 119
R 0
M 120
ad 84,744
W 0
M 121
M 132
M 133
T 24,12
M 122
T 28,26
M 123
W 1
M 134
SA 234,768
M 135
T 32,13
M 124
T 36,27
M 125
W 2
M 136
M 137
T 40,0
M 126
T 44,14
M 127
mw 75
M 138
ad 84,747
W 0
M 139
WL 8
T 48,1
M 128
T 52,15
M 129
M 140
W 1
M 141
T 56,2
M 130
WL 8
T 60,16
M 131
R 2
M 142
W 2
M 143
T 64,3
M 0
WL 8
T 68,17
M 1
R 4
M 2
M 3
M 4
M 5
M 6
M 7
WL 8
M 8
M 9
WL 2
M 10
M 11
R 6
M 12
M 13
M 14
M 15
M 16
M 17
M 18
M 19
M 20
SA 236,1536
M 21
M 22
M 23
WL 2
R 8
M 24
M 25
M 26
M 27
M 28
M 29
M 30
M 31
M 32
M 33
M 34
M 35
WL 2
R 10
M 36
M 37
M 38
M 39
M 40
M 41
M 42
M 43
M 44
WV 28
M 45
WL 0
s_barrier
M 46
mw 0
M 47
)"
R"(R 24
M 48
ad 84,768
W 0
M 49
M 50
M 51
M 52
W 1
M 53
M 54
M 55
M 56
W 2
M 57
M 58
SA 238,2304
M 59
R 26
M 60
mw 3
M 61
ad 84,771
W 0
M 62
M 63
M 64
M 65
W 1
M 66
M 67
M 68
M 69
W 2
M 70
M 71
WL 2
R 28
M 72
M 73
bc 48,16,64,56
M 74
mh 11
M 75
ad 64,1552
H 0
M 76
M 77
M 78
M 79
H 1
M 80
M 81
M 82
M 83
WL 2
R 30
M 84
mh 13
M 85
ad 66,1553
H 0
M 86
M 87
M 88
M 89
mh 0
M 90
ad 64,1554
H 0
M 91
M 92
M 93
M 94
H 1
M 95
WL 2
R 32
M 96
ae 42,72,48
M 97
s_bitcmp1_b32 s50, 4
s_cselect_b32 s52, 24576, 0
v_add_u32 v53, s52, v3
M 98
bc 49,17,66,60
SA 240,128
M 99
mh 2
WV 39
UL 6,34
UH 7,34
M 100
ad 66,1648
H 0
M 101
UL 8,35
UH 9,35
M 102
AD 84,6
AD 108,7
M 103
AD 132,8
AD 156,9
M 104
WV 37
UL 6,36
UH 7,36
M 105
UL 8,37
UH 9,37
M 106
AD 85,6
AD 109,7
M 107
WL 0
R 34
M 108
AD 133,8
AD 157,9
M 109
WV 35
UL 6,38
UH 7,38
M 110
UL 8,39
UH 9,39
M 111
AD 86,6
AD 110,7
M 112
AD 134,8
AD 158,9
M 113
WV 33
UL 6,40
UH 7,40
M 114
UL 8,41
UH 9,41
M 115
WV 24
AD 87,6
AD 111,7
M 116
WL 0
AD 135,8
AD 159,9
M 117
s_barrier
M 118
mw 24
M 119
R 48
M 120
ad 84,792
W 0
M 121
M 132
M 133
T 24,4
M 122
T 28,18
M 123
W 1
M 134
M 135
T 32,5
M 124
T 36,19
M 125
W 2
M 136
M 137
T 40,6
M 126
T 44,20
M 127
mw 27
M 138
ad 84,795
W 0
M 139
WL 8
T 48,7
M 128
T 52,21
M 129
SA 242,896
M 140
W 1
M 141
T 56,8
M 130
WL 8
T 60,22
M 131
R 50
M 142
W 2
M 143
T 64,9
M 0
WL 8
T 68,23
M 1
R 52
M 2
M 3
M 4
M 5
M 6
M 7
WL 8
M 8
M 9
WL 2
M 10
M 11
R 54
M 12
M 13
M 14
M 15
M 16
M 17
M 18
M 19
M 20
M 21
M 22
M 23
WL 2
R 56
M 24
M 25
M 26
M 27
M 28
M 29
M 30
M 31
SA 244,1664
M 32
M 33
M 34
M 35
WL 2
R 58
M 36
M 37
M 38
M 39
M 40
M 41
M 42
M 43
M 44
WV 23
M 45
WL 0
s_barrier
M 46
mw 48
M 47
R 72
M 48
ad 84,816
W 0
M 49
M 50
M 51
M 52
W 1
M 53
M 54
M 55
M 56
W 2
M 57
M 58
M 59
R 74
M 60
mw 51
M 61
ad 84,819
W 0
M 62
M 63
M 64
M 65
W 1
M 66
M 67
M 68
M 69
W 2
SA 246,2432
M 70
ae 44,70,192
M 71
WL 2
R 76
M 72
s_bitcmp1_b32 s50, 6
s_cselect_b32 s51, 24576, 0
v_add_u32 v52, s51, v3
M 73
bc 48,17,64,56
XL 46,256
M 74
mh 3
M 75
ad 64,1648
H 0
M 76
XL 48,1024
M 77
M 78
M 79
H 1
M 80
M 81
M 82
XL 14,1792
M 83
WL 2
R 78
M 84
mh 5
M 85
ad 66,1649
H 0
M 86
M 87
M 88
XL 16,2560
M 89
mh 6
M 90
ad 64,1650
H 0
M 91
M 92
M 93
M 94
H 1
M 95
WL 2
R 80
M 96
M 97
M 98
bc 49,18,66,60
M 99
mh 8
M 100
ad 66,1744
H 0
M 101
M 102
M 103
M 104
M 105
M 106
M 107
WL 2
R 82
M 108
ae 42,72,48
M 109
s_bfe_u32 s52, s50, 0x10005
v_mad_i32_i24 v53, v51, s52, v50
SA 248,128
M 110
M 111
M 112
M 113
M 114
M 115
WV 28
M 116
WL 0
M 117
s_barrier
M 118
mw 72
M 119
R 0
M 120
ad 84,840
W 0
M 121
M 132
M 133
T 24,10
M 122
T 28,24
M 123
)"
R"(W 1
M 134
M 135
T 32,11
M 124
T 36,25
M 125
W 2
M 136
M 137
T 40,12
M 126
T 44,26
M 127
mw 75
M 138
ad 84,843
W 0
M 139
WL 8
T 48,13
M 128
T 52,27
M 129
M 140
W 1
M 141
T 56,0
M 130
WL 8
T 60,14
M 131
R 2
M 142
W 2
M 143
T 64,1
M 0
WL 8
T 68,15
M 1
R 4
M 2
M 3
SA 250,896
M 4
M 5
M 6
M 7
WL 8
M 8
M 9
WL 2
M 10
M 11
R 6
M 12
M 13
M 14
M 15
M 16
M 17
M 18
M 19
M 20
M 21
M 22
M 23
WL 2
R 8
M 24
M 25
M 26
M 27
M 28
M 29
M 30
M 31
M 32
M 33
M 34
M 35
WL 2
R 10
M 36
M 37
M 38
M 39
M 40
M 41
SA 252,1664
M 42
M 43
WV 27
M 44
WL 0
M 45
s_barrier
M 46
mw 0
M 47
R 24
M 48
ad 84,864
W 0
M 49
M 50
M 51
M 52
W 1
M 53
M 54
M 55
M 56
W 2
M 57
M 58
M 59
R 26
M 60
mw 3
M 61
ad 84,867
W 0
M 62
M 63
M 64
M 65
W 1
M 66
M 67
M 68
M 69
W 2
M 70
M 71
WL 2
R 28
M 72
M 73
bc 48,18,64,56
M 74
mh 9
M 75
ad 64,1744
H 0
M 76
ae 44,68,192
M 77
s_bitcmp1_b32 s50, 7
s_cselect_b32 s51, 24576, 0
v_add_u32 v52, s51, v3
M 78
XL 42,256
M 79
H 1
SA 254,2432
M 80
XL 44,1024
M 81
M 82
M 83
WL 2
R 30
M 84
mh 11
M 85
ad 66,1745
H 0
M 86
XL 34,1792
M 87
M 88
M 89
mh 12
M 90
ad 64,1746
H 0
M 91
M 92
XL 36,2560
M 93
M 94
H 1
M 95
WL 2
R 32
M 96
M 97
M 98
bc 49,19,66,60
M 99
mh 0
M 100
ad 66,1840
H 0
M 101
M 102
M 103
M 104
M 105
M 106
M 107
WL 2
R 34
M 108
M 109
M 110
M 111
M 112
M 113
M 114
M 115
M 116
WV 27
M 117
WL 0
s_barrier
M 118
mw 24
M 119
R 48
M 120
ad 84,888
W 0
M 121
ae 42,72,48
M 132
s_bitcmp1_b32 s50, 4
s_cselect_b32 s52, 24576, 0
v_add_u32 v53, s52, v3
M 133
T 24,2
M 122
T 28,16
M 123
W 1
ST 18,256
M 134
UL 6,46
UH 7,46
M 135
T 32,3
M 124
T 36,17
M 125
W 2
UL 8,47
M 136
UH 9,47
AD 160,6
M 137
T 40,4
M 126
T 44,18
M 127
mw 27
AD 184,7
AD 208,8
M 138
ad 84,891
W 0
M 139
WL 8
T 48,5
M 128
T 52,19
M 129
AD 232,9
UL 6,48
M 140
W 1
UH 7,48
M 141
T 56,6
M 130
WL 8
T 60,20
M 131
R 50
M 142
W 2
UL 8,49
M 143
T 64,7
M 0
WL 8
T 68,21
M 1
R 52
M 2
UH 9,49
AD 161,6
M 3
AD 185,7
AD 209,8
M 4
AD 233,9
UL 6,14
M 5
UH 7,14
UL 8,15
M 6
UH 9,15
AD 162,6
M 7
WL 8
AD 186,7
AD 210,8
M 8
AD 234,9
UL 6,16
M 9
WL 2
UH 7,16
UL 8,17
M 10
UH 9,17
AD 163,6
M 11
R 54
M 12
AD 187,7
AD 211,8
M 13
AD 235,9
M 14
M 15
M 16
M 17
M 18
M 19
ST 20,1024
M 20
M 21
M 22
M 23
WL 2
R 56
M 24
M 25
M 26
M 27
M 28
M 29
M 30
M 31
M 32
M 33
M 34
M 35
WL 2
R 58
M 36
M 37
M 38
M 39
M 40
M 41
M 42
M 43
M 44
WV 27
M 45
WL 0
s_barrier
M 46
mw 48
M 47
R 72
M 48
ad 84,912
W 0
M 49
M 50
M 51
M 52
W 1
M 53
M 54
M 55
M 56
W 2
M 57
ST 22,1792
M 58
M 59
R 74
M 60
mw 51
M 61
ad 84,915
W 0
M 62
M 63
M 64
M 65
W 1
M 66
M 67
)"
R"(M 68
M 69
W 2
M 70
M 71
WL 2
R 76
M 72
M 73
bc 48,19,64,56
M 74
mh 1
M 75
ad 64,1840
H 0
M 76
M 77
M 78
ae 44,68,192
M 79
s_bfe_u32 s51, s50, 0x10008
H 1
v_mad_i32_i24 v52, v51, s51, v50
M 80
XL 38,256
M 81
M 82
M 83
WL 2
R 78
M 84
mh 3
XL 40,1024
M 85
ad 66,1841
H 0
M 86
M 87
M 88
M 89
mh 4
M 90
ad 64,1842
H 0
M 91
XL 18,1792
M 92
M 93
M 94
H 1
M 95
WL 2
R 80
M 96
ST 24,2560
XL 46,2560
M 97
M 98
bc 49,20,66,60
M 99
mh 6
M 100
ad 66,1936
H 0
M 101
M 102
M 103
M 104
PA 12288
DF
M 105
M 106
M 107
WL 2
R 82
M 108
M 109
M 110
M 111
M 112
M 113
M 114
M 115
M 116
WV 28
M 117
WL 0
s_barrier
M 118
mw 72
M 119
R 0
M 120
ad 84,936
W 0
M 121
M 132
M 133
T 24,8
M 122
T 28,22
M 123
W 1
M 134
M 135
T 32,9
M 124
T 36,23
M 125
W 2
M 136
UL 6,42
UH 7,42
M 137
T 40,10
M 126
T 44,24
M 127
mw 75
UL 8,43
UH 9,43
M 138
ad 84,939
W 0
M 139
WL 8
T 48,11
M 128
T 52,25
M 129
ae 42,72,48
AD 164,6
M 140
s_bfe_u32 s52, s50, 0x10005
W 1
v_mad_i32_i24 v53, v51, s52, v50
M 141
T 56,12
M 130
WL 8
T 60,26
M 131
R 2
M 142
W 2
ST 26,256
M 143
T 64,13
M 0
WL 8
T 68,27
M 1
R 4
M 2
AD 188,7
AD 212,8
M 3
AD 236,9
UL 6,44
M 4
UH 7,44
UL 8,45
M 5
UH 9,45
AD 165,6
M 6
AD 189,7
AD 213,8
M 7
WL 8
AD 237,9
UL 6,34
M 8
UH 7,34
UL 8,35
M 9
WL 2
UH 9,35
AD 166,6
M 10
AD 190,7
AD 214,8
M 11
R 6
M 12
AD 238,9
UL 6,36
M 13
UH 7,36
UL 8,37
M 14
UH 9,37
AD 167,6
M 15
AD 191,7
AD 215,8
M 16
AD 239,9
M 17
M 18
M 19
M 20
M 21
M 22
M 23
WL 2
R 8
M 24
M 25
M 26
M 27
M 28
M 29
M 30
M 31
M 32
M 33
M 34
M 35
WL 2
R 10
M 36
ST 28,1024
M 37
M 38
M 39
M 40
M 41
M 42
M 43
WV 28
M 44
WL 0
M 45
s_barrier
M 46
mw 0
M 47
R 24
M 48
ad 84,960
W 0
M 49
M 50
M 51
M 52
W 1
M 53
M 54
M 55
M 56
W 2
M 57
M 58
M 59
R 26
M 60
mw 3
M 61
ad 84,963
W 0
M 62
M 63
M 64
M 65
W 1
M 66
M 67
M 68
M 69
W 2
M 70
M 71
WL 2
R 28
M 72
M 73
bc 48,20,64,56
M 74
mh 7
ST 30,1792
M 75
ad 64,1936
H 0
M 76
M 77
M 78
M 79
H 1
M 80
M 81
M 82
ae 44,70,192
M 83
WL 2
R 30
M 84
mh 9
M 85
ad 66,1937
H 0
M 86
s_bfe_u32 s51, s50, 0x10009
v_mad_i32_i24 v52, v51, s51, v50
XL 48,256
M 87
M 88
XL 14,1024
M 89
mh 10
M 90
ad 64,1938
H 0
M 91
M 92
M 93
M 94
H 1
XL 16,1792
M 95
WL 2
R 32
M 96
M 97
M 98
bc 49,21,66,60
M 99
mh 12
M 100
ad 66,2032
H 0
M 101
XL 20,2560
M 102
M 103
M 104
bc 48,21,64,56
M 105
mh 13
M 106
ad 64,2032
H 0
M 107
WL 2
R 34
M 108
M 109
M 110
M 111
M 112
ST 32,2560
M 113
M 114
M 115
WV 31
M 116
WL 0
M 117
s_barrier
M 118
mw 24
M 119
R 48
M 120
ad 84,984
W 0
M 121
M 132
M 133
T 24,0
M 122
T 28,14
M 123
W 1
M 134
M 135
T 32,1
M 124
T 36,15
M 125
W 2
M 136
M 137
T 40,2
M 126
T 44,16
M 127
mw 27
M 138
ad 84,987
W 0
M 139
WL 8
T 48,3
M 128
T 52,17
M 129
UL 6,38
UH 7,38
M 140
)"
R"(W 1
UL 8,39
M 141
T 56,4
M 130
WL 8
T 60,18
M 131
R 50
M 142
W 2
UH 9,39
M 143
T 64,5
M 0
WL 8
T 68,19
M 1
R 52
M 2
AD 168,6
AD 192,7
M 3
AD 216,8
AD 240,9
M 4
UL 6,40
UH 7,40
M 5
UL 8,41
UH 9,41
M 6
AD 169,6
AD 193,7
M 7
WL 8
AD 217,8
AD 241,9
M 8
UL 6,18
UH 7,18
M 9
WL 2
UL 8,19
UH 9,19
M 10
AD 170,6
AD 194,7
M 11
R 54
M 12
AD 218,8
AD 242,9
M 13
WV 35
UL 6,46
UH 7,46
M 14
UL 8,47
UH 9,47
M 15
AD 171,6
AD 195,7
M 16
AD 219,8
AD 243,9
M 17
M 18
M 19
M 20
M 21
M 22
M 23
WL 2
R 56
M 24
M 25
M 26
M 27
M 28
M 29
M 30
M 31
M 32
M 33
M 34
M 35
WL 2
R 58
M 36
M 37
M 38
M 39
M 40
M 41
M 42
M 43
M 44
WV 27
M 45
WL 0
s_barrier
M 46
mw 48
M 47
R 72
M 48
ad 84,1008
W 0
M 49
M 50
M 51
M 52
W 1
M 53
M 54
M 55
M 56
W 2
M 57
M 58
M 59
R 74
M 60
mw 51
M 61
ad 84,1011
W 0
M 62
M 63
M 64
M 65
W 1
M 66
M 67
M 68
M 69
W 2
M 70
M 71
WL 2
R 76
M 72
M 73
mh 0
M 74
ad 64,2033
H 0
M 75
M 76
M 77
M 78
mh 1
M 79
ad 66,2033
H 0
M 80
M 81
M 82
M 83
WL 2
R 78
M 84
mh 2
M 85
ad 64,2034
H 0
M 86
ae 44,68,240
M 87
s_bitcmp1_b32 s50, 10
s_cselect_b32 s51, 24576, 0
v_add_u32 v52, s51, v3
M 88
XL 22,256
M 89
H 1
M 90
M 91
M 92
XL 24,1024
M 93
bc 49,22,66,60
M 94
mh 4
M 95
WL 2
R 80
M 96
ad 66,2128
H 0
M 97
M 98
XL 26,1792
M 99
M 100
M 101
M 102
M 103
M 104
XL 42,2560
M 105
M 106
M 107
WL 2
R 82
M 108
M 109
M 110
M 111
M 112
M 113
M 114
M 115
M 116
WV 26
M 117
WL 0
s_barrier
M 118
mw 72
M 119
R 0
M 120
ad 84,1032
W 0
M 121
M 132
M 133
T 24,6
M 122
T 28,20
M 123
W 1
M 134
M 135
T 32,7
M 124
T 36,21
M 125
W 2
M 136
M 137
T 40,8
M 126
T 44,22
M 127
mw 75
M 138
ad 84,1035
W 0
M 139
WL 8
T 48,9
M 128
T 52,23
M 129
M 140
W 1
UL 6,48
M 141
T 56,10
M 130
WL 8
T 60,24
M 131
R 2
M 142
W 2
UH 7,48
M 143
T 64,11
M 0
WL 8
T 68,25
M 1
R 4
M 2
UL 8,49
UH 9,49
M 3
AD 172,6
AD 196,7
M 4
AD 220,8
AD 244,9
M 5
UL 6,14
UH 7,14
M 6
UL 8,15
UH 9,15
M 7
WL 8
AD 173,6
AD 197,7
M 8
WL 2
AD 221,8
AD 245,9
M 9
WV 31
UL 6,16
UH 7,16
M 10
UL 8,17
UH 9,17
M 11
R 6
M 12
AD 174,6
AD 198,7
M 13
AD 222,8
AD 246,9
M 14
WV 29
UL 6,20
UH 7,20
M 15
UL 8,21
UH 9,21
M 16
AD 175,6
AD 199,7
M 17
AD 223,8
AD 247,9
M 18
M 19
M 20
M 21
M 22
M 23
WL 2
R 8
M 24
M 25
M 26
M 27
M 28
M 29
M 30
M 31
M 32
M 33
M 34
M 35
WL 2
R 10
M 36
M 37
M 38
M 39
M 40
M 41
M 42
M 43
M 44
WV 21
M 45
WL 0
s_barrier
M 46
mw 0
M 47
R 24
M 48
ad 84,1056
W 0
M 49
M 50
M 51
M 52
W 1
M 53
M 54
M 55
M 56
W 2
M 57
M 58
M 59
R 26
M 60
mw 3
M 61
ad 84,1059
W 0
M 62
M 63
M 64
M 65
W 1
M 66
M 67
M 68
M 69
W 2
M 70
)"
R"(M 71
WL 2
R 28
M 72
M 73
bc 48,22,64,56
M 74
mh 5
M 75
ad 64,2128
H 0
M 76
M 77
M 78
M 79
H 1
M 80
M 81
M 82
M 83
WL 2
R 30
M 84
mh 7
M 85
ad 66,2129
H 0
M 86
M 87
M 88
M 89
mh 8
M 90
ad 64,2130
H 0
M 91
ae 44,68,240
M 92
s_bfe_u32 s51, s50, 0x1000b
v_mad_i32_i24 v52, v51, s51, v50
XL 44,256
M 93
M 94
H 1
M 95
WL 2
R 32
M 96
XL 34,1024
M 97
M 98
bc 49,23,66,60
M 99
mh 10
M 100
ad 66,2224
H 0
M 101
M 102
XL 36,1792
M 103
M 104
M 105
M 106
M 107
WL 2
R 34
M 108
XL 28,2560
M 109
M 110
M 111
M 112
M 113
M 114
M 115
WV 26
M 116
WL 0
M 117
s_barrier
M 118
mw 24
M 119
R 48
M 120
ad 84,1080
W 0
M 121
M 132
M 133
T 24,12
M 122
T 28,26
M 123
W 1
M 134
M 135
T 32,13
M 124
T 36,27
M 125
W 2
M 136
M 137
T 40,0
M 126
T 44,14
M 127
mw 27
M 138
ad 84,1083
W 0
M 139
WL 8
T 48,1
M 128
T 52,15
M 129
M 140
W 1
M 141
T 56,2
M 130
WL 8
T 60,16
M 131
R 50
M 142
W 2
UL 6,22
M 143
T 64,3
M 0
WL 8
T 68,17
M 1
R 52
M 2
UH 7,22
UL 8,23
M 3
UH 9,23
AD 176,6
M 4
AD 200,7
AD 224,8
M 5
WV 31
AD 248,9
UL 6,24
M 6
UH 7,24
UL 8,25
M 7
WL 8
UH 9,25
AD 177,6
M 8
WL 2
AD 201,7
AD 225,8
M 9
WV 28
AD 249,9
UL 6,26
M 10
UH 7,26
UL 8,27
M 11
R 54
M 12
UH 9,27
AD 178,6
M 13
AD 202,7
AD 226,8
M 14
AD 250,9
UL 6,42
M 15
UH 7,42
UL 8,43
M 16
UH 9,43
AD 179,6
M 17
AD 203,7
AD 227,8
M 18
AD 251,9
M 19
M 20
M 21
M 22
M 23
WL 2
R 56
M 24
M 25
M 26
M 27
M 28
M 29
M 30
M 31
M 32
M 33
M 34
M 35
WL 2
R 58
M 36
M 37
M 38
M 39
M 40
M 41
M 42
M 43
M 44
WV 22
M 45
WL 0
s_barrier
M 46
mw 48
M 47
R 72
M 48
ad 84,1104
W 0
M 49
M 50
M 51
M 52
W 1
M 53
M 54
M 55
M 56
W 2
M 57
M 58
M 59
R 74
M 60
mw 51
M 61
ad 84,1107
W 0
M 62
M 63
M 64
M 65
W 1
M 66
M 67
M 68
M 69
W 2
M 70
M 71
WL 2
R 76
M 72
M 73
bc 48,23,64,56
M 74
mh 11
M 75
ad 64,2224
H 0
M 76
M 77
M 78
M 79
H 1
M 80
M 81
M 82
M 83
WL 2
R 78
M 84
mh 13
M 85
ad 66,2225
H 0
M 86
M 87
M 88
M 89
mh 0
M 90
ad 64,2226
H 0
M 91
M 92
M 93
M 94
H 1
M 95
WL 2
R 80
M 96
M 97
M 98
bc 48,0,64,56
M 99
mh 2
M 100
ad 64,17
H 0
M 101
M 102
M 103
M 104
bc 49,0,66,60
M 105
mh 3
M 106
ad 66,17
H 0
M 107
WL 2
R 82
M 108
M 109
M 110
PA 12288
DF
M 111
M 112
M 113
M 114
M 115
WV 24
M 116
WL 0
M 117
s_barrier
M 118
mw 72
M 119
R 0
M 120
ad 84,1128
W 0
M 121
M 132
M 133
T 24,4
M 122
T 28,18
M 123
W 1
M 134
M 135
T 32,5
M 124
T 36,19
M 125
W 2
M 136
M 137
T 40,6
M 126
T 44,20
M 127
mw 75
M 138
ad 84,1131
W 0
M 139
WL 8
T 48,7
M 128
T 52,21
M 129
M 140
W 1
M 141
T 56,8
M 130
WL 8
T 60,22
M 131
R 2
M 142
W 2
M 143
T 64,9
M 0
WL 8
T 68,23
M 1
R 4
M 2
UL 6,44
)"
R"(UH 7,44
M 3
UL 8,45
UH 9,45
M 4
AD 180,6
AD 204,7
M 5
AD 228,8
AD 252,9
M 6
WV 29
UL 6,34
UH 7,34
M 7
WL 8
UL 8,35
UH 9,35
M 8
AD 181,6
AD 205,7
M 9
WL 2
AD 229,8
AD 253,9
M 10
WV 26
UL 6,36
UH 7,36
M 11
R 6
M 12
UL 8,37
UH 9,37
M 13
AD 182,6
AD 206,7
M 14
AD 230,8
AD 254,9
M 15
UL 6,28
UH 7,28
M 16
UL 8,29
UH 9,29
M 17
AD 183,6
AD 207,7
M 18
AD 231,8
AD 255,9
M 19
M 20
M 21
M 22
M 23
WL 2
R 8
M 24
M 25
M 26
M 27
M 28
M 29
M 30
M 31
M 32
M 33
M 34
M 35
WL 2
R 10
M 36
M 37
M 38
M 39
M 40
M 41
M 42
M 43
M 44
WV 20
M 45
WL 0
s_barrier
M 46
mw 0
M 47
R 24
M 48
ad 84,0
W 0
M 49
M 50
M 51
M 52
W 1
M 53
M 54
M 55
M 56
W 2
M 57
M 58
M 59
R 26
M 60
mw 3
M 61
ad 84,3
W 0
M 62
M 63
M 64
M 65
W 1
M 66
M 67
M 68
M 69
W 2
M 70
M 71
WL 2
R 28
M 72
M 73
mh 4
M 74
ad 64,18
H 0
M 75
M 76
M 77
M 78
H 1
M 79
M 80
M 81
M 82
mh 6
M 83
WL 2
R 30
M 84
ad 66,32
H 0
M 85
M 86
M 87
M 88
mh 7
M 89
ad 64,32
H 0
M 90
M 91
M 92
M 93
bc 48,1,64,56
M 94
mh 8
M 95
WL 2
R 32
M 96
ad 64,114
H 0
M 97
M 98
M 99
M 100
H 1
M 101
M 102
M 103
M 104
M 105
M 106
M 107
WL 2
R 34
M 108
M 109
M 110
M 111
M 112
M 113
M 114
M 115
M 116
WV 21
M 117
WL 0
s_barrier
M 118
mw 24
M 119
R 48
M 120
ad 84,24
W 0
M 121
M 132
M 133
T 24,10
M 122
T 28,24
M 123
W 1
M 134
M 135
T 32,11
M 124
T 36,25
M 125
W 2
M 136
M 137
T 40,12
M 126
T 44,26
M 127
mw 27
M 138
ad 84,27
W 0
M 139
WL 8
T 48,13
M 128
T 52,27
M 129
M 140
W 1
M 141
T 56,0
M 130
WL 8
T 60,14
M 131
R 50
M 142
W 2
M 143
T 64,1
M 0
WL 8
T 68,15
M 1
R 52
M 2
M 3
M 4
M 5
M 6
M 7
WL 8
M 8
M 9
WL 2
M 10
M 11
R 54
M 12
M 13
M 14
M 15
M 16
M 17
M 18
M 19
M 20
M 21
M 22
M 23
WL 2
R 56
M 24
M 25
M 26
M 27
M 28
M 29
M 30
M 31
M 32
M 33
M 34
M 35
WL 2
R 58
M 36
M 37
M 38
M 39
M 40
AR 6,72
AR 7,96
M 41
AR 8,120
s_nop 0
AR 9,144
M 42
EF 6
EF 7
M 43
WV 18
EF 8
EF 9
M 44
WL 0
CV 30,6,7
CV 31,8,9
M 45
s_barrier
AR 6,73
AR 7,97
M 46
mw 48
AR 8,121
AR 9,145
M 47
R 72
M 48
ad 84,48
W 0
M 49
EF 6
EF 7
M 50
EF 8
EF 9
M 51
CV 32,6,7
CV 33,8,9
M 52
W 1
AR 6,74
M 53
AR 7,98
AR 8,122
M 54
AR 9,146
EF 6
M 55
EF 7
EF 8
M 56
W 2
EF 9
M 57
CV 38,6,7
CV 39,8,9
M 58
AR 6,75
AR 7,99
M 59
R 74
M 60
mw 51
AR 8,123
AR 9,147
M 61
ad 84,51
W 0
M 62
EF 6
EF 7
M 63
EF 8
EF 9
M 64
CV 40,6,7
CV 41,8,9
M 65
W 1
BA 72,0
M 66
BA 96,256
BA 120,512
M 67
BA 144,768
M 68
ae 42,74,192
AR 6,76
M 69
W 2
AR 7,100
M 70
s_bitcmp1_b32 s50, 6
s_cselect_b32 s52, 24576, 0
v_add_u32 v53, s52, v3
M 71
WL 4
R 76
M 72
ST 30,0
AR 8,124
)"
R"(M 73
bc 49,1,66,60
AR 9,148
M 74
mh 10
EF 6
EF 7
M 75
ad 66,128
H 0
M 76
EF 8
EF 9
M 77
CV 18,6,7
CV 19,8,9
M 78
AR 6,77
AR 7,101
M 79
mh 11
AR 8,125
AR 9,149
M 80
ad 64,128
H 0
M 81
EF 6
EF 7
M 82
EF 8
EF 9
M 83
R 78
M 84
H 1
CV 46,6,7
M 85
CV 47,8,9
AR 6,78
M 86
AR 7,102
AR 8,126
M 87
AR 9,150
EF 6
M 88
mh 13
EF 7
EF 8
M 89
ad 66,129
H 0
M 90
EF 9
CV 48,6,7
M 91
CV 49,8,9
AR 6,79
M 92
AR 7,103
AR 8,127
M 93
bc 49,2,66,60
AR 9,151
M 94
mh 0
EF 6
EF 7
M 95
WL 0
R 80
M 96
ad 66,224
H 0
M 97
EF 8
EF 9
M 98
CV 14,6,7
CV 15,8,9
M 99
BA 76,0
BA 100,256
M 100
BA 124,512
BA 148,768
M 101
EF 64
EF 88
M 102
EF 112
EF 136
M 103
CV 16,64,88
CV 17,112,136
M 104
EF 65
EF 89
M 105
EF 113
EF 137
M 106
CV 20,65,89
CV 21,113,137
M 107
R 82
M 108
EF 66
EF 90
M 109
EF 114
EF 138
M 110
ST 32,768
CV 22,66,90
M 111
CV 23,114,138
EF 67
M 112
EF 91
EF 115
M 113
EF 139
CV 24,67,91
M 114
CV 25,115,139
BV 64,1024
M 115
WV 21
BV 88,1280
BV 112,1536
M 116
WL 3
BV 136,1792
M 117
s_barrier
EF 68
EF 92
M 118
mw 72
EF 116
EF 140
M 119
R 0
M 120
ad 84,72
W 0
M 121
CV 26,68,92
CV 27,116,140
M 132
EF 69
EF 93
M 133
T 24,6
M 122
T 28,20
M 123
W 1
EF 117
M 134
EF 141
CV 42,69,93
M 135
T 32,7
M 124
T 36,21
M 125
W 2
CV 43,117,141
M 136
EF 70
EF 94
M 137
WL 8
T 40,2
M 126
T 44,16
M 127
mw 75
EF 118
EF 142
M 138
ad 84,75
W 0
M 139
T 48,3
M 128
WL 8
T 52,17
M 129
CV 44,70,94
CV 45,118,142
M 140
W 1
EF 71
M 141
T 56,4
M 130
T 60,18
M 131
WL 8
R 2
M 142
W 2
EF 95
M 143
T 64,5
M 4
T 68,19
M 5
WL 8
R 4
M 6
EF 119
EF 143
M 7
WL 8
CV 34,71,95
CV 35,119,143
M 8
ST 38,1536
BV 68,1024
M 9
WL 3
BV 92,1280
BV 116,1536
M 10
BV 140,1792
M 11
EF 160
EF 184
M 0
EF 208
EF 232
M 1
CV 10,160,184
CV 11,208,232
M 2
AW 160,10
AW 161,11
M 3
R 6
M 16
EF 161
EF 185
M 17
EF 209
EF 233
M 18
CV 10,161,185
CV 11,209,233
M 19
AW 162,10
AW 163,11
M 20
EF 162
EF 186
M 21
EF 210
EF 234
M 22
CV 10,162,186
CV 11,210,234
M 23
AW 164,10
AW 165,11
M 12
EF 163
EF 187
M 13
EF 211
EF 235
M 14
CV 10,163,187
CV 11,211,235
M 15
WL 2
R 8
M 28
AW 166,10
AW 167,11
M 29
BV 160,2048
BV 184,2304
M 30
BV 208,2560
BV 232,2816
M 31
EF 164
EF 188
M 32
EF 212
EF 236
M 33
CV 10,164,188
CV 11,212,236
M 34
AW 168,10
AW 169,11
M 35
EF 165
EF 189
M 24
EF 213
EF 237
M 25
CV 10,165,189
CV 11,213,237
M 26
AW 170,10
AW 171,11
M 27
WL 6
R 10
M 40
EF 166
EF 190
M 41
EF 214
EF 238
M 42
CV 10,166,190
CV 11,214,238
M 43
AW 172,10
AW 173,11
M 44
EF 167
EF 191
M 45
EF 215
EF 239
M 46
ST 40,2304
CV 10,167,191
M 47
WV 21
CV 11,215,239
AW 174,10
M 36
WL 0
AW 175,11
BV 164,2048
M 37
s_barrier
BV 188,2304
BV 212,2560
M 38
mw 0
BV 236,2816
M 39
R 24
M 52
ad 84,96
W 0
M 53
AR 6,80
AR 7,104
M 54
AR 8,128
AR 9,152
M 55
EF 6
EF 7
M 56
W 1
EF 8
M 57
EF 9
CV 10,6,7
M 58
CV 11,8,9
AW 176,10
M 59
AW 177,11
AR 6,81
M 48
W 2
AR 7,105
M 49
AR 8,129
AR 9,153
M 50
EF 6
EF 7
M 51
R 26
M 64
mw 3
EF 8
EF 9
M 65
ad 84,99
W 0
M 66
CV 10,6,7
CV 11,8,9
M 67
)"
R"(AW 178,10
AW 179,11
M 68
AR 6,82
AR 7,106
M 69
W 1
AR 8,130
M 70
AR 9,154
EF 6
M 71
EF 7
EF 8
M 60
EF 9
CV 10,6,7
M 61
W 2
CV 11,8,9
M 62
AW 180,10
AW 181,11
M 63
WL 0
R 28
M 76
AR 6,83
AR 7,107
M 77
bc 48,2,64,56
AR 8,131
M 78
mh 1
AR 9,155
EF 6
M 79
ad 64,224
H 0
M 80
EF 7
EF 8
M 81
EF 9
CV 10,6,7
M 82
CV 11,8,9
AW 182,10
M 83
H 1
AW 183,11
M 72
ae 42,72,192
BA 80,0
M 73
s_bitcmp1_b32 s50, 7
s_cselect_b32 s52, 24576, 0
v_add_u32 v53, s52, v3
M 74
ST 18,0
BA 104,256
M 75
R 30
M 88
mh 3
BA 128,512
BA 152,768
M 89
ad 66,225
H 0
M 90
AR 6,84
AR 7,108
M 91
AR 8,132
AR 9,156
M 92
EF 6
EF 7
M 93
mh 4
EF 8
EF 9
M 94
ad 64,226
H 0
M 95
CV 10,6,7
CV 11,8,9
M 84
AW 184,10
AW 185,11
M 85
AR 6,85
AR 7,109
M 86
H 1
AR 8,133
M 87
WL 2
R 32
M 100
AR 9,157
EF 6
M 101
EF 7
EF 8
M 102
bc 49,3,66,60
EF 9
M 103
mh 6
CV 10,6,7
CV 11,8,9
M 104
ad 66,320
H 0
M 105
AW 186,10
AW 187,11
M 106
AR 6,86
AR 7,110
M 107
AR 8,134
AR 9,158
M 96
EF 6
EF 7
M 97
EF 8
EF 9
M 98
CV 10,6,7
CV 11,8,9
M 99
R 34
M 112
AW 188,10
AW 189,11
M 113
AR 6,87
AR 7,111
M 114
AR 8,135
AR 9,159
M 115
EF 6
EF 7
M 116
EF 8
EF 9
M 117
CV 10,6,7
CV 11,8,9
M 118
AW 190,10
AW 191,11
M 119
WV 23
BA 84,0
BA 108,256
M 108
WL 2
BA 132,512
BA 156,768
M 109
s_barrier
EF 72
EF 96
M 110
mw 24
EF 120
EF 144
M 111
R 48
M 124
ad 84,120
W 0
M 125
ST 46,768
CV 10,72,96
M 136
CV 11,120,144
AW 192,10
M 137
T 40,12
M 126
T 44,26
M 127
W 1
AW 193,11
M 138
EF 73
EF 97
M 139
T 48,13
M 128
T 52,27
M 129
W 2
EF 121
M 140
EF 145
CV 10,73,97
M 141
WL 8
T 56,8
M 130
T 60,22
M 131
mw 27
CV 11,121,145
AW 194,10
M 142
ad 84,123
W 0
M 143
T 64,9
M 120
WL 8
T 68,23
M 121
AW 195,11
EF 74
M 132
W 1
EF 98
M 133
T 24,10
M 122
T 28,24
M 123
WL 8
R 50
M 134
W 2
EF 122
M 135
T 32,11
M 8
T 36,25
M 9
WL 8
R 52
M 10
EF 146
CV 10,74,98
M 11
WL 8
CV 11,122,146
AW 196,10
M 0
AW 197,11
EF 75
M 1
WL 2
EF 99
EF 123
M 2
EF 147
CV 10,75,99
M 3
CV 11,123,147
AW 198,10
M 4
AW 199,11
BV 72,1024
M 5
BV 96,1280
BV 120,1536
M 6
BV 144,1792
M 7
R 54
M 20
EF 76
EF 100
M 21
EF 124
EF 148
M 22
CV 10,76,100
CV 11,124,148
M 23
ST 48,1536
AW 200,10
M 12
AW 201,11
EF 77
M 13
EF 101
EF 125
M 14
EF 149
CV 10,77,101
M 15
CV 11,125,149
AW 202,10
M 16
AW 203,11
EF 78
M 17
EF 102
EF 126
M 18
EF 150
CV 10,78,102
M 19
WL 2
R 56
M 32
CV 11,126,150
AW 204,10
M 33
AW 205,11
EF 79
M 34
EF 103
EF 127
M 35
EF 151
CV 10,79,103
M 24
CV 11,127,151
AW 206,10
M 25
AW 207,11
BV 76,1024
M 26
BV 100,1280
BV 124,1536
M 27
BV 148,1792
M 28
EF 168
EF 192
M 29
EF 216
EF 240
M 30
CV 10,168,192
CV 11,216,240
M 31
WL 6
R 58
M 44
AW 208,10
AW 209,11
M 45
EF 169
EF 193
M 46
EF 217
EF 241
M 47
CV 10,169,193
CV 11,217,241
M 36
AW 210,10
AW 211,11
M 37
EF 170
EF 194
M 38
EF 218
EF 242
M 39
WV 23
CV 10,170,194
CV 11,218,242
M 40
WL 0
AW 212,10
AW 213,11
M 41
s_barrier
EF 171
EF 195
M 42
mw 48
EF 219
EF 243
M 43
R 72
M 56
ad 84,144
W 0
M 57
CV 10,171,195
CV 11,219,243
M 58
AW 214,10
AW 215,11
M 59
BV 168,2048
BV 192,2304
M 48
W 1
BV 216,2560
)"
R"(M 49
ST 14,2304
BV 240,2816
M 50
ae 44,70,384
M 51
s_bitcmp1_b32 s50, 12
s_cselect_b32 s51, 24576, 0
v_add_u32 v52, s51, v3
M 52
W 2
XL 36,0
M 53
XL 28,768
EF 172
M 54
XL 30,1536
EF 196
M 55
R 74
M 68
mw 51
XL 32,2304
EF 220
M 69
ad 84,147
W 0
M 70
EF 244
CV 10,172,196
M 71
CV 11,220,244
AW 216,10
M 60
AW 217,11
EF 173
M 61
W 1
EF 197
M 62
EF 221
EF 245
M 63
CV 10,173,197
CV 11,221,245
M 64
AW 218,10
AW 219,11
M 65
W 2
EF 174
M 66
EF 198
EF 222
M 67
WL 0
R 76
M 80
EF 246
CV 10,174,198
M 81
bc 48,3,64,56
CV 11,222,246
M 82
mh 7
AW 220,10
AW 221,11
M 83
ad 64,320
H 0
M 72
EF 175
EF 199
M 73
EF 223
EF 247
M 74
CV 10,175,199
CV 11,223,247
M 75
H 1
AW 222,10
M 76
AW 223,11
BV 172,2048
M 77
BV 196,2304
BV 220,2560
M 78
BV 244,2816
M 79
R 78
M 92
mh 9
AR 6,88
AR 7,112
M 93
ad 66,321
H 0
M 94
AR 8,136
EF 6
M 95
EF 7
EF 8
M 84
EF 56
CV 10,6,7
M 85
mh 10
CV 11,8,56
AW 224,10
M 86
ad 64,322
H 0
M 87
ae 42,74,192
AW 225,11
M 88
s_bitcmp1_b32 s50, 6
s_cselect_b32 s52, 24576, 0
v_add_u32 v53, s52, v3
M 89
ST 16,128
AR 6,89
M 90
H 1
AR 7,113
M 91
WL 0
R 80
M 104
AR 8,137
EF 6
M 105
EF 7
EF 8
M 106
bc 49,4,66,60
EF 57
M 107
mh 12
CV 10,6,7
CV 11,8,57
M 96
ad 66,416
H 0
M 97
AW 226,10
AW 227,11
M 98
AR 6,90
AR 7,114
M 99
AR 8,138
EF 6
M 100
bc 48,4,64,56
EF 7
M 101
mh 13
EF 8
EF 58
M 102
ad 64,416
H 0
M 103
R 82
M 116
CV 10,6,7
CV 11,8,58
M 117
AW 228,10
AW 229,11
M 118
PA 12288
DF
AR 6,91
M 119
AR 7,115
AR 8,139
M 108
EF 6
EF 7
M 109
EF 8
EF 59
M 110
CV 10,6,7
CV 11,8,59
M 111
WV 29
AW 230,10
AW 231,11
M 112
WL 0
BA 88,0
BA 112,256
M 113
s_barrier
BA 136,512
BV 56,768
M 114
mw 72
AR 6,92
AR 7,116
AR 8,140
M 115
R 0
EF 6
M 128
ad 84,168
W 0
EF 7
M 129
EF 8
EF 60
CV 10,6,7
M 140
CV 11,8,60
AW 232,10
AW 233,11
M 141
T 56,4
AR 6,93
M 130
T 60,18
AR 7,117
M 131
W 1
AR 8,141
EF 6
M 142
EF 7
EF 8
EF 61
M 143
T 64,5
CV 10,6,7
M 120
T 68,19
CV 11,8,61
M 121
W 2
AW 234,10
AW 235,11
M 132
AR 6,94
AR 7,118
AR 8,142
M 133
WL 8
T 24,0
EF 6
M 122
T 28,14
EF 7
M 123
mw 75
ST 20,896
EF 8
EF 62
M 134
ad 84,171
W 0
CV 10,6,7
M 135
T 32,1
CV 11,8,62
M 124
WL 8
T 36,15
AW 236,10
M 125
AW 237,11
AR 6,95
AR 7,119
M 136
W 1
AR 8,143
EF 6
M 137
T 40,2
EF 7
M 126
T 44,16
EF 8
M 127
WL 8
R 2
EF 63
M 138
W 2
CV 10,6,7
CV 11,8,63
M 139
T 48,3
AW 238,10
M 0
T 52,17
AW 239,11
M 1
WL 8
R 4
BA 92,0
M 2
BA 116,256
BA 140,512
BV 60,768
M 3
WL 12
EF 80
EF 104
M 4
EF 128
EF 152
M 5
WL 6
CV 10,80,104
CV 11,128,152
M 6
AW 240,10
AW 241,11
M 7
EF 81
EF 105
M 8
EF 129
EF 153
M 9
WL 3
CV 10,81,105
CV 11,129,153
M 10
AW 242,10
AW 243,11
M 11
R 6
M 12
EF 82
EF 106
M 13
EF 130
EF 154
M 14
CV 10,82,106
CV 11,130,154
M 15
AW 244,10
AW 245,11
M 16
EF 83
EF 107
M 17
EF 131
EF 155
M 18
CV 10,83,107
CV 11,131,155
M 19
AW 246,10
AW 247,11
M 20
BV 80,1024
BV 104,1280
M 21
WL 4
BV 128,1536
BV 152,1792
M 22
EF 84
EF 108
M 23
R 8
M 24
EF 132
EF 156
M 25
CV 10,84,108
CV 11,132,156
M 26
AW 248,10
AW 249,11
M 27
ST 22,1664
EF 85
M 28
EF 109
EF 133
M 29
EF 157
CV 10,85,109
M 30
CV 11,133,157
AW 250,10
M 31
AW 251,11
EF 86
M 32
EF 110
)"
R"(EF 134
M 33
EF 158
CV 10,86,110
M 34
CV 11,134,158
AW 252,10
M 35
WL 2
R 10
M 36
AW 253,11
EF 87
M 37
EF 111
EF 135
M 38
EF 159
CV 10,87,111
M 39
CV 11,135,159
AW 254,10
M 40
AW 255,11
BV 84,1024
M 41
BV 108,1280
BV 132,1536
M 42
BV 156,1792
M 43
WV 29
EF 176
EF 200
M 44
WL 4
EF 224
EF 248
M 45
s_barrier
CV 38,176,200
CV 39,224,248
M 46
mw 0
EF 177
EF 201
M 47
R 24
M 48
ad 84,192
W 0
M 49
EF 225
EF 249
M 50
CV 40,177,201
CV 41,225,249
M 51
EF 178
EF 202
M 52
W 1
EF 226
M 53
EF 250
CV 18,178,202
M 54
CV 19,226,250
EF 179
M 55
EF 203
EF 227
M 56
W 2
EF 251
M 57
WL 2
CV 46,179,203
CV 47,227,251
M 58
BV 176,2048
BV 200,2304
M 59
R 26
M 60
mw 3
BV 224,2560
BV 248,2816
M 61
ad 84,195
W 0
M 62
EF 180
EF 204
M 63
EF 228
EF 252
M 64
CV 48,180,204
CV 49,228,252
M 65
W 1
ST 24,2432
M 66
EF 181
EF 205
M 67
EF 229
EF 253
M 68
CV 14,181,205
CV 15,229,253
M 69
W 2
EF 182
M 70
EF 206
EF 230
M 71
WL 2
R 28
M 72
EF 254
CV 16,182,206
M 73
mh 0
CV 17,230,254
EF 183
M 74
ad 64,417
H 0
M 75
EF 207
EF 231
M 76
EF 255
CV 20,183,207
M 77
CV 21,231,255
BV 180,2048
M 78
mh 1
BV 204,2304
BV 228,2560
M 79
ad 66,417
H 0
M 80
BV 252,2816
M 81
ae 44,68,384
M 82
s_bitcmp1_b32 s50, 13
s_cselect_b32 s51, 24576, 0
v_add_u32 v52, s51, v3
M 83
WV 29
R 30
M 84
mh 2
XL 22,0
UL 6,36
M 85
ad 64,418
H 0
M 86
XL 24,768
UH 7,36
M 87
UL 8,37
UH 9,37
M 88
AR 10,72
AR 11,96
M 89
H 1
AR 12,120
M 90
AR 13,144
AD 10,6
M 91
AD 11,7
AD 12,8
M 92
AD 13,9
AW 72,10
M 93
bc 49,5,66,60
AW 96,11
M 94
mh 4
AW 120,12
AW 144,13
M 95
WL 0
R 32
M 96
ad 66,512
H 0
M 97
UL 6,28
UH 7,28
M 98
UL 8,29
UH 9,29
M 99
AR 10,73
AR 11,97
M 100
AR 12,121
AR 13,145
M 101
AD 10,6
AD 11,7
M 102
AD 12,8
AD 13,9
M 103
ae 42,72,192
AW 73,10
M 104
s_bitcmp1_b32 s50, 7
s_cselect_b32 s52, 24576, 0
v_add_u32 v53, s52, v3
M 105
ST 26,128
AW 97,11
M 106
XL 26,1536
AW 121,12
M 107
R 34
M 108
AW 145,13
UL 6,30
M 109
UH 7,30
UL 8,31
M 110
UH 9,31
AR 10,74
M 111
AR 11,98
AR 12,122
M 112
AR 13,146
AD 10,6
M 113
AD 11,7
AD 12,8
M 114
AD 13,9
AW 74,10
M 115
WV 27
AW 98,11
AW 122,12
M 116
WL 0
AW 146,13
UL 6,32
M 117
s_barrier
UH 7,32
UL 8,33
M 118
mw 24
UH 9,33
AR 10,75
M 119
R 48
M 120
ad 84,216
W 0
M 121
AR 11,99
AR 12,123
M 132
AR 13,147
AD 10,6
M 133
T 24,6
M 122
T 28,20
M 123
W 1
AD 11,7
M 134
AD 12,8
AD 13,9
M 135
T 32,7
M 124
T 36,21
M 125
W 2
AW 75,10
M 136
AW 99,11
AW 123,12
M 137
T 40,8
M 126
T 44,22
M 127
mw 27
AW 147,13
M 138
ad 84,219
W 0
M 139
WL 8
T 48,9
M 128
T 52,23
M 129
M 140
W 1
M 141
T 56,10
M 130
WL 8
T 60,24
M 131
R 50
M 142
W 2
M 143
T 64,11
M 0
WL 8
T 68,25
M 1
R 52
M 2
ST 42,896
M 3
s_nop 0
XL 42,2304
M 4
ae 44,68,384
M 5
M 6
s_bfe_u32 s51, s50, 0x1000e
v_mad_i32_i24 v52, v51, s51, v50
XL 36,0
M 7
WL 8
XL 28,768
M 8
XL 30,1536
M 9
WL 2
XL 32,2304
M 10
M 11
R 54
M 12
M 13
M 14
M 15
M 16
M 17
M 18
M 19
M 20
M 21
M 22
M 23
WL 2
R 56
M 24
M 25
M 26
M 27
M 28
M 29
M 30
M 31
M 32
M 33
M 34
M 35
WL 2
R 58
M 36
M 37
M 38
M 39
M 40
ST 44,1664
M 41
M 42
WV 19
UL 6,22
UH 7,22
M 43
UL 8,23
UH 9,23
M 44
)"
R"(WL 0
AR 10,76
AR 11,100
M 45
s_barrier
AR 12,124
AR 13,148
M 46
mw 48
AD 10,6
AD 11,7
M 47
R 72
M 48
ad 84,240
W 0
M 49
AD 12,8
AD 13,9
M 50
AW 76,10
AW 100,11
M 51
AW 124,12
AW 148,13
M 52
WV 18
W 1
UL 6,24
M 53
UH 7,24
UL 8,25
M 54
UH 9,25
AR 10,77
M 55
AR 11,101
AR 12,125
M 56
W 2
AR 13,149
M 57
AD 10,6
AD 11,7
M 58
AD 12,8
AD 13,9
M 59
R 74
M 60
mw 51
AW 77,10
AW 101,11
M 61
ad 84,243
W 0
M 62
AW 125,12
AW 149,13
M 63
WV 17
UL 6,26
UH 7,26
M 64
UL 8,27
UH 9,27
M 65
W 1
AR 10,78
M 66
AR 11,102
AR 12,126
M 67
AR 13,150
AD 10,6
M 68
AD 11,7
AD 12,8
M 69
W 2
AD 13,9
M 70
AW 78,10
AW 102,11
M 71
WL 0
R 76
M 72
WV 11
AW 126,12
AW 150,13
M 73
bc 48,5,64,56
UL 6,42
M 74
mh 5
UH 7,42
UL 8,43
M 75
ad 64,512
H 0
M 76
UH 9,43
AR 10,79
M 77
AR 11,103
AR 12,127
M 78
ST 34,2432
AR 13,151
M 79
H 1
AD 10,6
M 80
AD 11,7
AD 12,8
M 81
AD 13,9
AW 79,10
M 82
AW 103,11
AW 127,12
M 83
R 78
M 84
mh 7
AW 151,13
M 85
ad 66,513
H 0
M 86
M 87
M 88
M 89
mh 8
M 90
ad 64,514
H 0
M 91
M 92
M 93
M 94
H 1
M 95
WL 2
R 80
M 96
M 97
M 98
bc 49,6,66,60
M 99
mh 10
M 100
ad 66,608
H 0
M 101
M 102
M 103
M 104
M 105
M 106
M 107
WL 2
R 82
M 108
M 109
M 110
M 111
M 112
M 113
M 114
M 115
M 116
M 117
WL 0
s_barrier
M 118
mw 72
M 119
R 0
M 120
ad 84,264
W 0
M 121
ae 42,74,192
M 132
s_bitcmp1_b32 s50, 6
s_cselect_b32 s52, 24576, 0
v_add_u32 v53, s52, v3
M 133
T 24,12
M 122
T 28,26
M 123
W 1
SA 160,256
M 134
M 135
T 32,13
M 124
T 36,27
M 125
W 2
M 136
M 137
T 40,0
M 126
T 44,14
M 127
mw 75
M 138
ad 84,267
W 0
M 139
WL 8
T 48,1
M 128
T 52,15
M 129
ae 44,70,384
M 140
s_bfe_u32 s51, s50, 0x1000f
W 1
v_mad_i32_i24 v52, v51, s51, v50
M 141
T 56,2
M 130
WL 8
T 60,16
M 131
R 2
M 142
W 2
XL 44,0
M 143
T 64,3
M 0
WL 8
T 68,17
M 1
R 4
M 2
XL 34,768
M 3
XL 22,1536
M 4
M 5
M 6
M 7
WL 8
M 8
XL 24,2304
M 9
WL 2
M 10
M 11
R 6
M 12
M 13
M 14
M 15
M 16
M 17
M 18
M 19
SA 162,1024
M 20
M 21
M 22
M 23
WL 2
R 8
M 24
M 25
M 26
M 27
M 28
M 29
M 30
M 31
M 32
M 33
M 34
M 35
WL 2
R 10
M 36
M 37
M 38
M 39
M 40
M 41
M 42
M 43
M 44
WV 26
UL 6,36
UH 7,36
M 45
WL 0
s_barrier
UL 8,37
UH 9,37
M 46
mw 0
AR 10,80
AR 11,104
M 47
R 24
M 48
ad 84,288
W 0
M 49
AR 12,128
AR 13,152
M 50
AD 10,6
AD 11,7
M 51
AD 12,8
AD 13,9
M 52
W 1
AW 80,10
M 53
AW 104,11
AW 128,12
M 54
AW 152,13
UL 6,28
M 55
UH 7,28
UL 8,29
M 56
W 2
UH 9,29
M 57
SA 164,1792
AR 10,81
M 58
AR 11,105
AR 12,129
M 59
R 26
M 60
mw 3
AR 13,153
AD 10,6
M 61
ad 84,291
W 0
M 62
AD 11,7
AD 12,8
M 63
AD 13,9
AW 81,10
M 64
AW 105,11
AW 129,12
M 65
W 1
AW 153,13
M 66
UL 6,30
UH 7,30
M 67
UL 8,31
UH 9,31
M 68
AR 10,82
AR 11,106
M 69
W 2
AR 12,130
M 70
AR 13,154
AD 10,6
M 71
WL 0
R 28
M 72
AD 11,7
AD 12,8
M 73
bc 48,6,64,56
AD 13,9
M 74
mh 11
AW 82,10
AW 106,11
M 75
ad 64,608
H 0
M 76
AW 130,12
AW 154,13
M 77
UL 6,32
UH 7,32
)"
R"(M 78
UL 8,33
UH 9,33
M 79
H 1
AR 10,83
M 80
AR 11,107
AR 12,131
M 81
AR 13,155
AD 10,6
M 82
AD 11,7
AD 12,8
M 83
R 30
M 84
mh 13
AD 13,9
AW 83,10
M 85
ad 66,609
H 0
M 86
AW 107,11
AW 131,12
M 87
AW 155,13
M 88
M 89
mh 0
M 90
ad 64,610
H 0
M 91
M 92
M 93
M 94
H 1
M 95
WL 2
R 32
M 96
SA 166,2560
M 97
M 98
bc 49,7,66,60
M 99
mh 2
M 100
ad 66,704
H 0
M 101
M 102
M 103
M 104
M 105
M 106
M 107
WL 2
R 34
M 108
M 109
M 110
M 111
M 112
M 113
M 114
M 115
M 116
WV 27
M 117
WL 0
s_barrier
M 118
mw 24
M 119
R 48
M 120
ad 84,312
W 0
M 121
M 132
M 133
T 24,4
M 122
T 28,18
M 123
W 1
M 134
M 135
T 32,5
M 124
T 36,19
M 125
W 2
M 136
M 137
T 40,6
M 126
T 44,20
M 127
mw 27
M 138
ad 84,315
W 0
M 139
WL 8
T 48,7
M 128
T 52,21
M 129
ae 42,72,192
M 140
W 1
M 141
T 56,8
M 130
WL 8
T 60,22
M 131
R 50
M 142
W 2
M 143
T 64,9
M 0
WL 8
T 68,23
M 1
R 52
M 2
s_bitcmp1_b32 s50, 7
s_cselect_b32 s52, 24576, 0
M 3
v_add_u32 v53, s52, v3
SA 168,256
M 4
ae 44,68,432
M 5
M 6
s_bitcmp1_b32 s50, 16
s_cselect_b32 s51, 24576, 0
v_add_u32 v52, s51, v3
M 7
WL 8
XL 26,0
M 8
XL 42,768
M 9
WL 2
XL 36,1536
M 10
XL 28,2304
M 11
R 54
M 12
M 13
M 14
M 15
M 16
M 17
M 18
M 19
M 20
M 21
M 22
M 23
WL 2
R 56
M 24
M 25
M 26
M 27
M 28
M 29
M 30
M 31
M 32
M 33
M 34
M 35
WL 2
R 58
M 36
M 37
M 38
M 39
M 40
M 41
SA 170,1024
M 42
M 43
WV 27
M 44
WL 0
M 45
s_barrier
M 46
mw 48
UL 6,44
UH 7,44
M 47
R 72
M 48
ad 84,336
W 0
M 49
UL 8,45
UH 9,45
M 50
AR 10,84
AR 11,108
M 51
AR 12,132
AR 13,156
M 52
W 1
AD 10,6
M 53
AD 11,7
AD 12,8
M 54
AD 13,9
AW 84,10
M 55
AW 108,11
AW 132,12
M 56
W 2
AW 156,13
M 57
UL 6,34
UH 7,34
M 58
UL 8,35
UH 9,35
M 59
R 74
M 60
mw 51
AR 10,85
AR 11,109
M 61
ad 84,339
W 0
M 62
AR 12,133
AR 13,157
M 63
AD 10,6
AD 11,7
M 64
AD 12,8
AD 13,9
M 65
W 1
AW 85,10
M 66
AW 109,11
AW 133,12
M 67
AW 157,13
UL 6,22
M 68
UH 7,22
UL 8,23
M 69
W 2
UH 9,23
M 70
AR 10,86
AR 11,110
M 71
WL 0
R 76
M 72
AR 12,134
AR 13,158
M 73
bc 48,7,64,56
AD 10,6
M 74
mh 3
AD 11,7
AD 12,8
M 75
ad 64,704
H 0
M 76
AD 13,9
AW 86,10
M 77
AW 110,11
AW 134,12
M 78
AW 158,13
UL 6,24
M 79
H 1
SA 172,1792
M 80
UH 7,24
UL 8,25
M 81
UH 9,25
AR 10,87
M 82
AR 11,111
AR 12,135
M 83
R 78
M 84
mh 5
AR 13,159
AD 10,6
M 85
ad 66,705
H 0
M 86
AD 11,7
AD 12,8
M 87
AD 13,9
AW 87,10
M 88
AW 111,11
AW 135,12
M 89
mh 6
AW 159,13
M 90
ad 64,706
H 0
M 91
M 92
M 93
M 94
H 1
M 95
WL 2
R 80
M 96
M 97
M 98
bc 49,8,66,60
M 99
mh 8
M 100
ad 66,800
H 0
M 101
M 102
M 103
M 104
PA 12288
DF
M 105
M 106
M 107
WL 2
R 82
M 108
M 109
M 110
M 111
M 112
M 113
M 114
M 115
M 116
WV 28
M 117
WL 0
s_barrier
SA 174,2560
M 118
mw 72
M 119
R 0
M 120
ad 84,360
W 0
M 121
M 132
M 133
T 24,10
M 122
T 28,24
M 123
W 1
M 134
M 135
T 32,11
M 124
T 36,25
M 125
W 2
M 136
)"
R"(M 137
T 40,12
M 126
T 44,26
M 127
mw 75
M 138
ad 84,363
W 0
M 139
WL 8
T 48,13
M 128
T 52,27
M 129
M 140
W 1
M 141
T 56,0
M 130
WL 8
T 60,14
M 131
R 2
M 142
W 2
M 143
T 64,1
M 0
WL 8
T 68,15
M 1
R 4
M 2
ae 44,68,432
M 3
M 4
s_bfe_u32 s51, s50, 0x10011
v_mad_i32_i24 v52, v51, s51, v50
XL 30,0
M 5
XL 32,768
M 6
XL 44,1536
M 7
WL 8
M 8
M 9
WL 2
M 10
M 11
R 6
M 12
ae 42,72,192
XL 34,2304
M 13
s_bfe_u32 s52, s50, 0x10008
v_mad_i32_i24 v53, v51, s52, v50
SA 176,0
M 14
M 15
M 16
M 17
M 18
M 19
M 20
M 21
M 22
M 23
WL 2
R 8
M 24
M 25
M 26
M 27
M 28
M 29
M 30
M 31
M 32
M 33
M 34
M 35
WL 2
R 10
M 36
M 37
M 38
M 39
M 40
M 41
M 42
M 43
M 44
WV 32
M 45
WL 0
s_barrier
M 46
mw 0
M 47
R 24
M 48
ad 84,384
W 0
M 49
WV 28
UL 6,26
UH 7,26
M 50
UL 8,27
UH 9,27
M 51
SA 178,768
AR 10,88
M 52
W 1
AR 11,112
M 53
AR 12,136
AD 10,6
M 54
AD 11,7
AD 12,8
M 55
AD 56,9
AW 88,10
M 56
W 2
AW 112,11
M 57
AW 136,12
UL 6,42
M 58
UH 7,42
UL 8,43
M 59
R 26
M 60
mw 3
UH 9,43
AR 10,89
M 61
ad 84,387
W 0
M 62
AR 11,113
AR 12,137
M 63
AD 10,6
AD 11,7
M 64
AD 12,8
AD 57,9
M 65
W 1
AW 89,10
M 66
AW 113,11
AW 137,12
M 67
UL 6,36
UH 7,36
M 68
UL 8,37
UH 9,37
M 69
W 2
AR 10,90
M 70
AR 11,114
AR 12,138
M 71
WL 0
R 28
M 72
AD 10,6
AD 11,7
M 73
bc 48,8,64,56
AD 12,8
M 74
mh 9
AD 58,9
AW 90,10
M 75
ad 64,800
H 0
M 76
AW 114,11
AW 138,12
M 77
UL 6,28
UH 7,28
M 78
UL 8,29
UH 9,29
M 79
H 1
AR 10,91
M 80
AR 11,115
AR 12,139
M 81
AD 10,6
AD 11,7
M 82
AD 12,8
AD 59,9
M 83
R 30
M 84
mh 11
AW 91,10
AW 115,11
M 85
ad 66,801
H 0
M 86
AW 139,12
M 87
M 88
M 89
mh 12
SA 180,1536
M 90
ad 64,802
H 0
M 91
M 92
M 93
M 94
H 1
M 95
WL 2
R 32
M 96
M 97
M 98
bc 49,9,66,60
M 99
mh 0
M 100
ad 66,896
H 0
M 101
M 102
M 103
M 104
M 105
M 106
M 107
WL 2
R 34
M 108
M 109
M 110
M 111
M 112
M 113
M 114
M 115
M 116
WV 28
M 117
WL 0
s_barrier
M 118
mw 24
M 119
R 48
M 120
ad 84,408
W 0
M 121
M 132
M 133
T 24,2
M 122
T 28,16
M 123
W 1
M 134
M 135
T 32,3
M 124
T 36,17
M 125
W 2
SA 182,2304
M 136
M 137
T 40,4
M 126
T 44,18
M 127
mw 27
M 138
ad 84,411
W 0
M 139
WL 8
T 48,5
M 128
T 52,19
M 129
M 140
W 1
M 141
T 56,6
M 130
WL 8
T 60,20
M 131
R 50
M 142
W 2
M 143
T 64,7
M 0
WL 8
T 68,21
M 1
R 52
M 2
M 3
M 4
M 5
M 6
M 7
WL 8
M 8
M 9
WL 2
M 10
M 11
R 54
M 12
M 13
M 14
M 15
M 16
M 17
M 18
M 19
M 20
M 21
M 22
M 23
WL 2
R 56
M 24
ae 42,74,192
M 25
s_bfe_u32 s52, s50, 0x10009
v_mad_i32_i24 v53, v51, s52, v50
SA 184,0
M 26
M 27
M 28
M 29
M 30
M 31
M 32
M 33
M 34
M 35
WL 2
R 58
M 36
M 37
M 38
M 39
M 40
M 41
M 42
M 43
M 44
WV 27
M 45
WL 0
s_barrier
M 46
mw 48
M 47
R 72
M 48
ad 84,432
W 0
M 49
M 50
WV 24
UL 6,30
UH 7,30
M 51
UL 8,31
UH 9,31
M 52
W 1
AR 10,92
)"
R"(M 53
AR 11,116
AR 12,140
M 54
AD 10,6
AD 11,7
M 55
AD 12,8
AD 60,9
M 56
W 2
AW 92,10
M 57
AW 116,11
AW 140,12
M 58
UL 6,32
UH 7,32
M 59
R 74
M 60
mw 51
UL 8,33
UH 9,33
M 61
ad 84,435
W 0
M 62
AR 10,93
AR 11,117
M 63
SA 186,768
AR 12,141
M 64
AD 10,6
AD 11,7
M 65
W 1
AD 12,8
M 66
AD 61,9
AW 93,10
M 67
AW 117,11
AW 141,12
M 68
UL 6,44
UH 7,44
M 69
W 2
UL 8,45
M 70
UH 9,45
AR 10,94
M 71
WL 0
R 76
M 72
AR 11,118
AR 12,142
M 73
bc 48,9,64,56
AD 10,6
M 74
mh 1
AD 11,7
AD 12,8
M 75
ad 64,896
H 0
M 76
AD 62,9
AW 94,10
M 77
AW 118,11
AW 142,12
M 78
UL 6,34
UH 7,34
M 79
H 1
UL 8,35
M 80
UH 9,35
AR 10,95
M 81
AR 11,119
AR 12,143
M 82
AD 10,6
AD 11,7
M 83
R 78
M 84
mh 3
AD 12,8
AD 63,9
M 85
ad 66,897
H 0
M 86
AW 95,10
AW 119,11
M 87
AW 143,12
M 88
M 89
mh 4
M 90
ad 64,898
H 0
M 91
M 92
M 93
M 94
H 1
M 95
WL 2
R 80
M 96
M 97
M 98
bc 49,10,66,60
M 99
mh 6
M 100
ad 66,992
H 0
M 101
SA 188,1536
M 102
M 103
M 104
M 105
M 106
M 107
WL 2
R 82
M 108
M 109
M 110
M 111
M 112
M 113
M 114
M 115
M 116
WV 23
M 117
WL 0
s_barrier
M 118
mw 72
M 119
R 0
M 120
ad 84,456
W 0
M 121
M 132
M 133
T 24,8
M 122
T 28,22
M 123
W 1
M 134
M 135
T 32,9
M 124
T 36,23
M 125
W 2
M 136
M 137
T 40,10
M 126
T 44,24
M 127
mw 75
M 138
ad 84,459
W 0
M 139
WL 8
T 48,11
M 128
T 52,25
M 129
M 140
W 1
M 141
T 56,12
M 130
WL 8
T 60,26
M 131
R 2
M 142
W 2
SA 190,2304
M 143
T 64,13
M 0
WL 8
T 68,27
M 1
R 4
M 2
M 3
M 4
M 5
M 6
M 7
WL 8
M 8
M 9
WL 2
M 10
M 11
R 6
M 12
M 13
M 14
M 15
M 16
M 17
M 18
M 19
M 20
M 21
M 22
M 23
WL 2
R 8
M 24
M 25
M 26
M 27
M 28
M 29
M 30
M 31
M 32
ae 44,70,384
M 33
s_bitcmp1_b32 s50, 12
s_cselect_b32 s51, 24576, 0
v_add_u32 v52, s51, v3
M 34
XL 22,128
M 35
WL 2
R 10
M 36
ae 42,72,192
M 37
s_bfe_u32 s52, s50, 0x10008
v_mad_i32_i24 v53, v51, s52, v50
SA 192,128
M 38
XL 24,896
M 39
M 40
M 41
M 42
M 43
WV 25
M 44
WL 0
XL 26,1664
M 45
s_barrier
M 46
mw 0
M 47
R 24
M 48
ad 84,480
W 0
M 49
M 50
XL 42,2432
M 51
M 52
W 1
M 53
M 54
M 55
M 56
W 2
M 57
M 58
M 59
R 26
M 60
mw 3
M 61
ad 84,483
W 0
M 62
M 63
M 64
M 65
W 1
M 66
M 67
M 68
M 69
W 2
M 70
M 71
WL 2
R 28
M 72
M 73
bc 48,10,64,56
M 74
mh 7
M 75
ad 64,992
H 0
M 76
SA 194,896
M 77
M 78
M 79
H 1
M 80
M 81
M 82
M 83
WL 2
R 30
M 84
mh 9
M 85
ad 66,993
H 0
M 86
M 87
M 88
M 89
mh 10
M 90
ad 64,994
H 0
M 91
M 92
M 93
M 94
H 1
M 95
WL 2
R 32
M 96
M 97
M 98
bc 49,11,66,60
M 99
mh 12
M 100
ad 66,1088
H 0
M 101
M 102
M 103
M 104
bc 48,11,64,56
M 105
mh 13
M 106
ad 64,1088
H 0
M 107
WL 2
R 34
M 108
M 109
M 110
M 111
M 112
M 113
M 114
SA 196,1664
M 115
WV 29
M 116
WL 0
M 117
s_barrier
M 118
mw 24
M 119
R 48
M 120
ad 84,504
W 0
M 121
M 132
)"
R"(M 133
T 24,0
M 122
T 28,14
M 123
W 1
M 134
M 135
T 32,1
M 124
T 36,15
M 125
W 2
M 136
M 137
T 40,2
M 126
T 44,16
M 127
mw 27
M 138
ad 84,507
W 0
M 139
WL 8
T 48,3
M 128
T 52,17
M 129
M 140
W 1
M 141
T 56,4
M 130
WL 8
T 60,18
M 131
R 50
M 142
W 2
M 143
T 64,5
M 0
WL 8
T 68,19
M 1
R 52
M 2
M 3
M 4
M 5
M 6
M 7
WL 8
M 8
SA 198,2432
M 9
WL 2
M 10
M 11
R 54
M 12
M 13
M 14
M 15
M 16
M 17
M 18
M 19
M 20
M 21
M 22
M 23
WL 2
R 56
M 24
M 25
M 26
M 27
M 28
M 29
M 30
M 31
M 32
M 33
M 34
ae 44,68,384
M 35
WL 2
R 58
M 36
s_bitcmp1_b32 s50, 13
s_cselect_b32 s51, 24576, 0
v_add_u32 v52, s51, v3
M 37
XL 36,128
M 38
M 39
M 40
XL 28,896
M 41
M 42
M 43
WV 30
M 44
WL 0
M 45
s_barrier
M 46
mw 48
XL 30,1664
M 47
R 72
M 48
ad 84,528
W 0
M 49
ae 42,74,192
M 50
s_bfe_u32 s52, s50, 0x10009
v_mad_i32_i24 v53, v51, s52, v50
SA 200,128
M 51
M 52
W 1
XL 32,2432
M 53
M 54
M 55
M 56
W 2
M 57
M 58
M 59
R 74
M 60
mw 51
M 61
ad 84,531
W 0
M 62
M 63
M 64
M 65
W 1
M 66
M 67
M 68
M 69
W 2
M 70
M 71
WL 2
R 76
M 72
M 73
mh 0
M 74
ad 64,1089
H 0
M 75
M 76
M 77
M 78
mh 1
M 79
ad 66,1089
H 0
M 80
M 81
M 82
M 83
WL 2
R 78
M 84
mh 2
M 85
ad 64,1090
H 0
M 86
M 87
M 88
WV 40
SA 202,896
UL 6,22
M 89
H 1
UH 7,22
M 90
UL 8,23
UH 9,23
M 91
AD 64,6
AD 88,7
M 92
WV 39
AD 112,8
AD 136,9
M 93
bc 49,12,66,60
UL 6,24
M 94
mh 4
UH 7,24
UL 8,25
M 95
WL 2
R 80
M 96
ad 66,1184
H 0
M 97
UH 9,25
AD 65,6
M 98
AD 89,7
AD 113,8
M 99
AD 137,9
UL 6,26
M 100
UH 7,26
UL 8,27
M 101
UH 9,27
AD 66,6
M 102
AD 90,7
AD 114,8
M 103
WV 38
AD 138,9
UL 6,42
M 104
UH 7,42
UL 8,43
M 105
UH 9,43
AD 67,6
M 106
AD 91,7
AD 115,8
M 107
WL 1
R 82
M 108
AD 139,9
M 109
M 110
M 111
M 112
M 113
M 114
M 115
WV 27
M 116
WL 0
M 117
s_barrier
M 118
mw 72
M 119
R 0
M 120
ad 84,552
W 0
M 121
M 132
M 133
T 24,6
M 122
T 28,20
M 123
W 1
M 134
SA 204,1664
M 135
T 32,7
M 124
T 36,21
M 125
W 2
M 136
M 137
T 40,8
M 126
T 44,22
M 127
mw 75
M 138
ad 84,555
W 0
M 139
WL 8
T 48,9
M 128
T 52,23
M 129
M 140
W 1
M 141
T 56,10
M 130
WL 8
T 60,24
M 131
R 2
M 142
W 2
M 143
T 64,11
M 0
WL 8
T 68,25
M 1
R 4
M 2
M 3
M 4
M 5
M 6
M 7
WL 8
M 8
M 9
WL 2
M 10
M 11
R 6
M 12
M 13
M 14
M 15
M 16
M 17
M 18
M 19
M 20
SA 206,2432
M 21
M 22
M 23
WL 2
R 8
M 24
M 25
M 26
M 27
M 28
M 29
M 30
M 31
M 32
M 33
M 34
M 35
WL 2
R 10
M 36
ae 44,68,384
M 37
s_bfe_u32 s51, s50, 0x1000e
v_mad_i32_i24 v52, v51, s51, v50
XL 44,128
M 38
M 39
M 40
M 41
M 42
XL 34,896
M 43
WV 28
M 44
WL 0
M 45
s_barrier
M 46
mw 0
M 47
R 24
M 48
ad 84,576
W 0
M 49
XL 22,1664
M 50
M 51
M 52
W 1
M 53
M 54
XL 24,2432
M 55
M 56
W 2
M 57
M 58
ae 42,72,192
M 59
R 26
)"
R"(M 60
mw 3
M 61
ad 84,579
W 0
M 62
s_bfe_u32 s52, s50, 0x10008
v_mad_i32_i24 v53, v51, s52, v50
SA 208,256
M 63
M 64
M 65
W 1
M 66
M 67
M 68
M 69
W 2
M 70
M 71
WL 2
R 28
M 72
M 73
bc 48,12,64,56
M 74
mh 5
M 75
ad 64,1184
H 0
M 76
M 77
M 78
M 79
H 1
M 80
M 81
M 82
M 83
WL 2
R 30
M 84
mh 7
M 85
ad 66,1185
H 0
M 86
M 87
M 88
M 89
mh 8
M 90
ad 64,1186
H 0
M 91
WV 37
UL 6,36
UH 7,36
M 92
UL 8,37
UH 9,37
M 93
AD 68,6
AD 92,7
M 94
H 1
AD 116,8
M 95
WL 2
R 32
M 96
AD 140,9
UL 6,28
M 97
UH 7,28
UL 8,29
M 98
bc 49,13,66,60
UH 9,29
M 99
mh 10
AD 69,6
AD 93,7
M 100
ad 66,1280
H 0
M 101
SA 210,1024
AD 117,8
M 102
AD 141,9
UL 6,30
M 103
UH 7,30
UL 8,31
M 104
UH 9,31
AD 70,6
M 105
AD 94,7
AD 118,8
M 106
WV 36
AD 142,9
UL 6,32
M 107
WL 0
R 34
M 108
UH 7,32
UL 8,33
M 109
UH 9,33
AD 71,6
M 110
AD 95,7
AD 119,8
M 111
AD 143,9
M 112
M 113
M 114
M 115
WV 27
M 116
WL 0
M 117
s_barrier
M 118
mw 24
M 119
R 48
M 120
ad 84,600
W 0
M 121
M 132
M 133
T 24,12
M 122
T 28,26
M 123
W 1
M 134
M 135
T 32,13
M 124
T 36,27
M 125
W 2
M 136
M 137
T 40,0
M 126
T 44,14
M 127
mw 27
M 138
ad 84,603
W 0
M 139
WL 8
T 48,1
M 128
T 52,15
M 129
M 140
W 1
M 141
T 56,2
M 130
WL 8
T 60,16
M 131
R 50
M 142
W 2
SA 212,1792
M 143
T 64,3
M 0
WL 8
T 68,17
M 1
R 52
M 2
M 3
M 4
M 5
M 6
M 7
WL 8
M 8
M 9
WL 2
M 10
M 11
R 54
M 12
M 13
M 14
M 15
M 16
M 17
M 18
M 19
M 20
M 21
M 22
M 23
WL 2
R 56
M 24
M 25
M 26
M 27
M 28
M 29
M 30
M 31
M 32
M 33
M 34
M 35
WL 2
R 58
M 36
SA 214,2560
M 37
M 38
ae 44,70,384
M 39
s_bfe_u32 s51, s50, 0x1000f
v_mad_i32_i24 v52, v51, s51, v50
XL 26,128
M 40
M 41
M 42
M 43
WV 28
M 44
WL 0
XL 42,896
M 45
s_barrier
M 46
mw 48
M 47
R 72
M 48
ad 84,624
W 0
M 49
M 50
XL 36,1664
M 51
M 52
W 1
M 53
M 54
M 55
M 56
W 2
XL 28,2432
M 57
M 58
M 59
R 74
M 60
mw 51
M 61
ad 84,627
W 0
M 62
M 63
M 64
M 65
W 1
M 66
M 67
M 68
M 69
W 2
M 70
M 71
WL 2
R 76
M 72
M 73
bc 48,13,64,56
M 74
mh 11
M 75
ad 64,1280
H 0
M 76
ae 42,74,192
M 77
s_bfe_u32 s52, s50, 0x10009
v_mad_i32_i24 v53, v51, s52, v50
SA 216,256
M 78
M 79
H 1
M 80
M 81
M 82
M 83
WL 2
R 78
M 84
mh 13
M 85
ad 66,1281
H 0
M 86
M 87
M 88
M 89
mh 0
M 90
ad 64,1282
H 0
M 91
M 92
WV 39
UL 6,44
UH 7,44
M 93
UL 8,45
UH 9,45
M 94
H 1
AD 72,6
M 95
WL 2
R 80
M 96
AD 96,7
AD 120,8
M 97
AD 144,9
UL 6,34
M 98
bc 49,14,66,60
UH 7,34
M 99
mh 2
UL 8,35
UH 9,35
M 100
ad 66,1376
H 0
M 101
AD 73,6
AD 97,7
M 102
AD 121,8
AD 145,9
M 103
WV 39
UL 6,22
UH 7,22
M 104
UL 8,23
UH 9,23
M 105
AD 74,6
AD 98,7
M 106
AD 122,8
AD 146,9
M 107
WL 0
R 82
M 108
WV 37
UL 6,24
UH 7,24
M 109
UL 8,25
UH 9,25
M 110
AD 75,6
AD 99,7
M 111
AD 123,8
AD 147,9
M 112
M 113
M 114
M 115
WV 27
SA 218,1024
M 116
WL 0
M 117
s_barrier
M 118
mw 72
)"
R"(M 119
R 0
M 120
ad 84,648
W 0
M 121
M 132
M 133
T 24,4
M 122
T 28,18
M 123
W 1
M 134
M 135
T 32,5
M 124
T 36,19
M 125
W 2
M 136
M 137
T 40,6
M 126
T 44,20
M 127
mw 75
M 138
ad 84,651
W 0
M 139
WL 8
T 48,7
M 128
T 52,21
M 129
M 140
W 1
M 141
T 56,8
M 130
WL 8
T 60,22
M 131
R 2
M 142
W 2
M 143
T 64,9
M 0
WL 8
T 68,23
M 1
R 4
M 2
M 3
M 4
M 5
M 6
M 7
WL 8
M 8
M 9
WL 2
SA 220,1792
M 10
M 11
R 6
M 12
M 13
M 14
M 15
M 16
M 17
M 18
M 19
M 20
M 21
M 22
M 23
WL 2
R 8
M 24
M 25
M 26
M 27
M 28
M 29
M 30
M 31
M 32
M 33
M 34
M 35
WL 2
R 10
M 36
M 37
M 38
M 39
M 40
ae 44,68,432
M 41
s_bitcmp1_b32 s50, 16
s_cselect_b32 s51, 24576, 0
v_add_u32 v52, s51, v3
M 42
XL 30,128
M 43
WV 28
M 44
WL 0
M 45
s_barrier
M 46
mw 0
XL 32,896
M 47
R 24
M 48
ad 84,672
W 0
M 49
SA 222,2560
M 50
M 51
M 52
W 1
XL 44,1664
M 53
M 54
M 55
M 56
W 2
M 57
M 58
XL 34,2432
M 59
R 26
M 60
mw 3
M 61
ad 84,675
W 0
M 62
M 63
M 64
M 65
W 1
M 66
M 67
M 68
M 69
W 2
M 70
M 71
WL 2
R 28
M 72
M 73
bc 48,14,64,56
M 74
mh 3
M 75
ad 64,1376
H 0
M 76
M 77
M 78
M 79
H 1
M 80
M 81
M 82
M 83
WL 2
R 30
M 84
mh 5
M 85
ad 66,1377
H 0
M 86
M 87
ae 42,72,240
M 88
s_bitcmp1_b32 s50, 10
s_cselect_b32 s52, 24576, 0
v_add_u32 v53, s52, v3
M 89
mh 6
SA 224,0
M 90
ad 64,1378
H 0
M 91
M 92
M 93
M 94
WV 39
H 1
UL 6,26
M 95
WL 2
R 32
M 96
UH 7,26
UL 8,27
M 97
UH 9,27
AD 76,6
M 98
bc 49,15,66,60
AD 100,7
M 99
mh 8
AD 124,8
AD 148,9
M 100
ad 66,1472
H 0
M 101
UL 6,42
UH 7,42
M 102
UL 8,43
UH 9,43
M 103
AD 77,6
AD 101,7
M 104
AD 125,8
AD 149,9
M 105
WV 39
UL 6,36
UH 7,36
M 106
UL 8,37
UH 9,37
M 107
WL 0
R 34
M 108
AD 78,6
AD 102,7
M 109
AD 126,8
AD 150,9
M 110
WV 36
UL 6,28
UH 7,28
M 111
UL 8,29
UH 9,29
M 112
AD 79,6
AD 103,7
M 113
AD 127,8
AD 151,9
M 114
M 115
WV 27
M 116
WL 0
M 117
s_barrier
M 118
mw 24
M 119
R 48
M 120
ad 84,696
W 0
M 121
M 132
M 133
T 24,10
M 122
T 28,24
M 123
W 1
M 134
M 135
T 32,11
M 124
T 36,25
M 125
W 2
SA 226,768
M 136
M 137
T 40,12
M 126
T 44,26
M 127
mw 27
M 138
ad 84,699
W 0
M 139
WL 8
T 48,13
M 128
T 52,27
M 129
M 140
W 1
M 141
T 56,0
M 130
WL 8
T 60,14
M 131
R 50
M 142
W 2
M 143
T 64,1
M 0
WL 8
T 68,15
M 1
R 52
M 2
M 3
M 4
M 5
M 6
M 7
WL 8
M 8
M 9
WL 2
M 10
M 11
R 54
M 12
M 13
M 14
M 15
M 16
M 17
M 18
M 19
M 20
M 21
M 22
M 23
WL 2
R 56
M 24
SA 228,1536
M 25
M 26
M 27
M 28
M 29
M 30
M 31
M 32
M 33
M 34
M 35
WL 2
R 58
M 36
M 37
M 38
M 39
M 40
M 41
M 42
ae 44,68,432
s_bfe_u32 s51, s50, 0x10011
M 43
WV 27
v_mad_i32_i24 v52, v51, s51, v50
XL 22,128
M 44
WL 0
M 45
s_barrier
M 46
mw 48
M 47
R 72
M 48
ad 84,720
W 0
M 49
XL 24,896
M 50
M 51
M 52
W 1
M 53
M 54
)"
R"(XL 26,1664
M 55
M 56
W 2
M 57
M 58
M 59
R 74
M 60
mw 51
XL 42,2432
M 61
ad 84,723
W 0
M 62
SA 230,2304
M 63
M 64
M 65
W 1
M 66
M 67
M 68
M 69
W 2
M 70
M 71
WL 2
R 76
M 72
M 73
bc 48,15,64,56
M 74
mh 9
M 75
ad 64,1472
H 0
M 76
M 77
M 78
M 79
H 1
M 80
M 81
M 82
M 83
WL 2
R 78
M 84
mh 11
M 85
ad 66,1473
H 0
M 86
M 87
M 88
M 89
mh 12
M 90
ad 64,1474
H 0
M 91
M 92
M 93
M 94
H 1
M 95
WL 2
R 80
M 96
WV 40
UL 6,30
UH 7,30
M 97
UL 8,31
UH 9,31
M 98
bc 49,16,66,60
AD 80,6
M 99
mh 0
AD 104,7
AD 128,8
M 100
ad 66,1568
H 0
M 101
ae 42,72,240
AD 152,9
M 102
s_bfe_u32 s52, s50, 0x1000b
v_mad_i32_i24 v53, v51, s52, v50
SA 232,0
M 103
UL 6,32
UH 7,32
M 104
UL 8,33
UH 9,33
M 105
AD 81,6
AD 105,7
M 106
AD 129,8
AD 153,9
M 107
WL 0
R 82
M 108
WV 38
UL 6,44
UH 7,44
M 109
UL 8,45
UH 9,45
M 110
AD 82,6
AD 106,7
M 111
AD 130,8
AD 154,9
M 112
WV 36
UL 6,34
UH 7,34
M 113
UL 8,35
UH 9,35
M 114
AD 83,6
AD 107,7
M 115
WV 27
AD 131,8
AD 155,9
M 116
WL 0
M 117
s_barrier
M 118
mw 72
M 119
R 0
M 120
ad 84,744
W 0
M 121
M 132
M 133
T 24,2
M 122
T 28,16
M 123
W 1
M 134
M 135
T 32,3
M 124
T 36,17
M 125
W 2
M 136
M 137
T 40,4
M 126
T 44,18
M 127
mw 75
M 138
ad 84,747
W 0
M 139
WL 8
T 48,5
M 128
T 52,19
M 129
M 140
W 1
M 141
T 56,6
M 130
WL 8
T 60,20
M 131
R 2
M 142
W 2
SA 234,768
M 143
T 64,7
M 0
WL 8
T 68,21
M 1
R 4
M 2
M 3
M 4
M 5
M 6
M 7
WL 8
M 8
M 9
WL 2
M 10
M 11
R 6
M 12
M 13
M 14
M 15
M 16
M 17
M 18
M 19
M 20
M 21
M 22
M 23
WL 2
R 8
M 24
M 25
M 26
M 27
M 28
M 29
M 30
M 31
M 32
M 33
M 34
M 35
WL 2
R 10
M 36
SA 236,1536
M 37
M 38
M 39
M 40
M 41
M 42
M 43
WV 27
M 44
WL 0
M 45
s_barrier
M 46
mw 0
M 47
R 24
M 48
ad 84,768
W 0
M 49
M 50
M 51
M 52
W 1
M 53
M 54
M 55
M 56
W 2
M 57
M 58
M 59
R 26
M 60
mw 3
M 61
ad 84,771
W 0
M 62
M 63
M 64
M 65
W 1
M 66
M 67
M 68
M 69
W 2
M 70
M 71
WL 2
R 28
M 72
M 73
bc 48,16,64,56
M 74
mh 1
SA 238,2304
M 75
ad 64,1568
H 0
M 76
M 77
M 78
M 79
H 1
M 80
M 81
M 82
M 83
WL 2
R 30
M 84
mh 3
M 85
ad 66,1569
H 0
M 86
M 87
M 88
M 89
mh 4
M 90
ad 64,1570
H 0
M 91
M 92
M 93
M 94
H 1
M 95
WL 2
R 32
M 96
M 97
WV 37
M 98
bc 49,17,66,60
UL 6,22
M 99
mh 6
UH 7,22
UL 8,23
M 100
ad 66,1664
H 0
M 101
UH 9,23
AD 84,6
M 102
AD 108,7
AD 132,8
M 103
WV 36
AD 156,9
UL 6,24
M 104
UH 7,24
UL 8,25
M 105
UH 9,25
AD 85,6
M 106
AD 109,7
AD 133,8
M 107
WL 2
R 34
M 108
WV 34
AD 157,9
UL 6,26
M 109
UH 7,26
UL 8,27
M 110
UH 9,27
AD 86,6
M 111
AD 110,7
AD 134,8
M 112
ae 42,72,240
AD 158,9
M 113
s_bitcmp1_b32 s50, 10
s_cselect_b32 s52, 24576, 0
v_add_u32 v53, s52, v3
M 114
WV 32
SA 240,128
UL 6,42
M 115
WV 24
UH 7,42
UL 8,43
M 116
WL 0
UH 9,43
AD 87,6
M 117
)"
R"(s_barrier
AD 111,7
AD 135,8
M 118
mw 24
AD 159,9
M 119
R 48
M 120
ad 84,792
W 0
M 121
M 132
M 133
T 24,8
M 122
T 28,22
M 123
W 1
M 134
M 135
T 32,9
M 124
T 36,23
M 125
W 2
M 136
M 137
T 40,10
M 126
T 44,24
M 127
mw 27
M 138
ad 84,795
W 0
M 139
WL 8
T 48,11
M 128
T 52,25
M 129
M 140
W 1
M 141
T 56,12
M 130
WL 8
T 60,26
M 131
R 50
M 142
W 2
M 143
T 64,13
M 0
WL 8
T 68,27
M 1
R 52
M 2
M 3
M 4
M 5
M 6
M 7
WL 8
M 8
SA 242,896
M 9
WL 2
M 10
M 11
R 54
M 12
M 13
M 14
M 15
M 16
M 17
M 18
M 19
M 20
M 21
M 22
M 23
WL 2
R 56
M 24
M 25
M 26
M 27
M 28
M 29
M 30
M 31
M 32
M 33
M 34
M 35
WL 2
R 58
M 36
M 37
M 38
M 39
M 40
M 41
M 42
M 43
M 44
WV 23
M 45
WL 0
s_barrier
M 46
mw 48
SA 244,1664
M 47
R 72
M 48
ad 84,816
W 0
M 49
M 50
M 51
M 52
W 1
M 53
M 54
M 55
M 56
W 2
M 57
M 58
M 59
R 74
M 60
mw 51
M 61
ad 84,819
W 0
M 62
M 63
M 64
M 65
W 1
M 66
M 67
M 68
M 69
W 2
M 70
ae 44,70,384
M 71
WL 2
R 76
M 72
s_bitcmp1_b32 s50, 12
s_cselect_b32 s51, 24576, 0
v_add_u32 v52, s51, v3
M 73
bc 48,17,64,56
XL 36,256
M 74
mh 7
M 75
ad 64,1664
H 0
M 76
XL 28,1024
M 77
M 78
M 79
H 1
M 80
M 81
M 82
XL 30,1792
M 83
WL 2
R 78
M 84
mh 9
SA 246,2432
M 85
ad 66,1665
H 0
M 86
M 87
M 88
XL 32,2560
M 89
mh 10
M 90
ad 64,1666
H 0
M 91
M 92
M 93
M 94
H 1
M 95
WL 2
R 80
M 96
M 97
M 98
bc 49,18,66,60
M 99
mh 12
M 100
ad 66,1760
H 0
M 101
M 102
M 103
M 104
bc 48,18,64,56
M 105
mh 13
M 106
ad 64,1760
H 0
M 107
WL 2
R 82
M 108
M 109
M 110
M 111
M 112
M 113
M 114
M 115
M 116
WV 28
M 117
WL 0
s_barrier
M 118
mw 72
M 119
R 0
M 120
ad 84,840
W 0
M 121
M 132
ae 42,72,240
M 133
s_bfe_u32 s52, s50, 0x1000b
T 24,0
M 122
T 28,14
M 123
W 1
v_mad_i32_i24 v53, v51, s52, v50
M 134
SA 248,128
M 135
T 32,1
M 124
T 36,15
M 125
W 2
M 136
M 137
T 40,2
M 126
T 44,16
M 127
mw 75
M 138
ad 84,843
W 0
M 139
WL 8
T 48,3
M 128
T 52,17
M 129
M 140
W 1
M 141
T 56,4
M 130
WL 8
T 60,18
M 131
R 2
M 142
W 2
M 143
T 64,5
M 0
WL 8
T 68,19
M 1
R 4
M 2
M 3
M 4
M 5
M 6
M 7
WL 8
M 8
M 9
WL 2
M 10
M 11
R 6
M 12
M 13
M 14
M 15
M 16
M 17
M 18
M 19
M 20
SA 250,896
M 21
M 22
M 23
WL 2
R 8
M 24
M 25
M 26
M 27
M 28
M 29
M 30
M 31
M 32
M 33
M 34
M 35
WL 2
R 10
M 36
M 37
M 38
M 39
M 40
M 41
M 42
M 43
M 44
WV 28
M 45
WL 0
s_barrier
M 46
mw 0
M 47
R 24
M 48
ad 84,864
W 0
M 49
M 50
M 51
M 52
W 1
M 53
M 54
M 55
M 56
W 2
M 57
M 58
SA 252,1664
M 59
R 26
M 60
mw 3
M 61
ad 84,867
W 0
M 62
M 63
M 64
M 65
W 1
M 66
M 67
M 68
M 69
W 2
M 70
M 71
WL 2
R 28
M 72
M 73
mh 0
M 74
)"
R"(ad 64,1761
H 0
M 75
ae 44,68,384
M 76
s_bitcmp1_b32 s50, 13
s_cselect_b32 s51, 24576, 0
v_add_u32 v52, s51, v3
M 77
XL 44,256
M 78
mh 1
M 79
ad 66,1761
H 0
M 80
XL 34,1024
M 81
M 82
M 83
WL 2
R 30
M 84
mh 2
M 85
ad 64,1762
H 0
M 86
XL 22,1792
M 87
M 88
M 89
H 1
M 90
M 91
M 92
XL 24,2560
M 93
bc 49,19,66,60
M 94
mh 4
M 95
WL 2
R 32
M 96
ad 66,1856
H 0
M 97
SA 254,2432
M 98
M 99
M 100
M 101
M 102
M 103
M 104
M 105
M 106
M 107
WL 2
R 34
M 108
M 109
M 110
M 111
M 112
M 113
M 114
M 115
M 116
WV 27
M 117
WL 0
s_barrier
M 118
mw 24
M 119
R 48
M 120
ad 84,888
W 0
M 121
M 132
M 133
T 24,6
M 122
T 28,20
M 123
W 1
M 134
UL 6,36
UH 7,36
M 135
T 32,7
M 124
T 36,21
M 125
W 2
UL 8,37
M 136
UH 9,37
AD 160,6
M 137
T 40,8
M 126
T 44,22
M 127
mw 27
AD 184,7
AD 208,8
M 138
ad 84,891
W 0
M 139
WL 8
T 48,9
M 128
T 52,23
M 129
ae 42,72,240
AD 232,9
M 140
W 1
UL 6,28
M 141
T 56,10
M 130
WL 8
T 60,24
M 131
R 50
M 142
W 2
UH 7,28
M 143
T 64,11
M 0
WL 8
T 68,25
M 1
R 52
M 2
s_bitcmp1_b32 s50, 10
s_cselect_b32 s52, 24576, 0
M 3
v_add_u32 v53, s52, v3
ST 38,256
M 4
UL 8,29
UH 9,29
M 5
AD 161,6
AD 185,7
M 6
AD 209,8
AD 233,9
M 7
WL 8
UL 6,30
UH 7,30
M 8
UL 8,31
UH 9,31
M 9
WL 2
AD 162,6
AD 186,7
M 10
AD 210,8
AD 234,9
M 11
R 54
M 12
UL 6,32
UH 7,32
M 13
UL 8,33
UH 9,33
M 14
AD 163,6
AD 187,7
M 15
AD 211,8
AD 235,9
M 16
M 17
M 18
M 19
M 20
M 21
M 22
M 23
WL 2
R 56
M 24
M 25
M 26
M 27
M 28
M 29
M 30
M 31
M 32
M 33
M 34
M 35
WL 2
R 58
M 36
M 37
M 38
M 39
M 40
M 41
ST 40,1024
M 42
M 43
WV 26
M 44
WL 0
M 45
s_barrier
M 46
mw 48
M 47
R 72
M 48
ad 84,912
W 0
M 49
M 50
M 51
M 52
W 1
M 53
M 54
M 55
M 56
W 2
M 57
M 58
M 59
R 74
M 60
mw 51
M 61
ad 84,915
W 0
M 62
M 63
M 64
M 65
W 1
M 66
M 67
M 68
M 69
W 2
M 70
M 71
WL 2
R 76
M 72
M 73
bc 48,19,64,56
M 74
mh 5
M 75
ad 64,1856
H 0
M 76
M 77
M 78
ae 44,68,384
M 79
s_bfe_u32 s51, s50, 0x1000e
H 1
ST 18,1792
M 80
v_mad_i32_i24 v52, v51, s51, v50
XL 26,256
M 81
M 82
M 83
WL 2
R 78
M 84
mh 7
XL 42,1024
M 85
ad 66,1857
H 0
M 86
M 87
M 88
M 89
mh 8
M 90
ad 64,1858
H 0
M 91
XL 38,1792
M 92
M 93
M 94
H 1
M 95
WL 2
R 80
M 96
XL 36,2560
M 97
M 98
bc 49,20,66,60
M 99
mh 10
M 100
ad 66,1952
H 0
M 101
M 102
M 103
M 104
M 105
M 106
M 107
WL 2
R 82
M 108
M 109
M 110
M 111
M 112
M 113
M 114
M 115
M 116
WV 28
M 117
WL 0
s_barrier
ST 46,2560
M 118
mw 72
M 119
R 0
M 120
ad 84,936
W 0
M 121
M 132
M 133
T 24,12
M 122
T 28,26
M 123
W 1
M 134
M 135
T 32,13
M 124
T 36,27
M 125
W 2
M 136
UL 6,44
UH 7,44
M 137
T 40,0
M 126
T 44,14
M 127
mw 75
UL 8,45
UH 9,45
M 138
ad 84,939
W 0
M 139
WL 8
T 48,1
M 128
T 52,15
M 129
AD 164,6
AD 188,7
M 140
W 1
AD 212,8
M 141
T 56,2
M 130
WL 8
T 60,16
M 131
R 2
M 142
W 2
AD 236,9
M 143
)"
R"(T 64,3
M 0
WL 8
T 68,17
M 1
R 4
M 2
UL 6,34
UH 7,34
M 3
UL 8,35
UH 9,35
M 4
AD 165,6
AD 189,7
M 5
AD 213,8
AD 237,9
M 6
UL 6,22
UH 7,22
M 7
WL 8
UL 8,23
UH 9,23
M 8
AD 166,6
AD 190,7
M 9
WL 2
AD 214,8
AD 238,9
M 10
WV 34
UL 6,24
UH 7,24
M 11
R 6
M 12
ae 42,72,240
UL 8,25
M 13
s_bfe_u32 s52, s50, 0x1000b
v_mad_i32_i24 v53, v51, s52, v50
ST 48,256
M 14
UH 9,25
AD 167,6
M 15
AD 191,7
AD 215,8
M 16
AD 239,9
M 17
M 18
M 19
M 20
M 21
M 22
M 23
WL 2
R 8
M 24
M 25
M 26
M 27
M 28
M 29
M 30
M 31
M 32
M 33
M 34
M 35
WL 2
R 10
M 36
M 37
M 38
M 39
M 40
M 41
M 42
M 43
M 44
WV 27
M 45
WL 0
s_barrier
M 46
mw 0
M 47
R 24
M 48
ad 84,960
W 0
M 49
M 50
M 51
ST 14,1024
M 52
W 1
M 53
M 54
M 55
M 56
W 2
M 57
M 58
M 59
R 26
M 60
mw 3
M 61
ad 84,963
W 0
M 62
M 63
M 64
M 65
W 1
M 66
M 67
M 68
M 69
W 2
M 70
M 71
WL 2
R 28
M 72
M 73
bc 48,20,64,56
M 74
mh 11
M 75
ad 64,1952
H 0
M 76
M 77
M 78
M 79
H 1
M 80
M 81
M 82
ae 44,70,384
M 83
WL 2
R 30
M 84
mh 13
M 85
ad 66,1953
H 0
M 86
s_bfe_u32 s51, s50, 0x1000f
v_mad_i32_i24 v52, v51, s51, v50
XL 28,256
M 87
M 88
XL 30,1024
M 89
mh 0
ST 16,1792
M 90
ad 64,1954
H 0
M 91
M 92
M 93
M 94
H 1
XL 32,1792
M 95
WL 2
R 32
M 96
M 97
M 98
bc 49,21,66,60
M 99
mh 2
M 100
ad 66,2048
H 0
M 101
XL 40,2560
M 102
M 103
M 104
M 105
M 106
M 107
WL 2
R 34
M 108
M 109
M 110
M 111
M 112
M 113
M 114
M 115
M 116
WV 28
M 117
WL 0
s_barrier
M 118
mw 24
M 119
R 48
M 120
ad 84,984
W 0
M 121
M 132
M 133
T 24,4
M 122
T 28,18
M 123
W 1
M 134
M 135
T 32,5
M 124
T 36,19
M 125
W 2
ST 20,2560
M 136
M 137
T 40,6
M 126
T 44,20
M 127
mw 27
M 138
ad 84,987
W 0
M 139
WL 8
T 48,7
M 128
T 52,21
M 129
UL 6,26
UH 7,26
M 140
W 1
UL 8,27
M 141
T 56,8
M 130
WL 8
T 60,22
M 131
R 50
M 142
W 2
UH 9,27
M 143
T 64,9
M 0
WL 8
T 68,23
M 1
R 52
M 2
AD 168,6
AD 192,7
M 3
AD 216,8
AD 240,9
M 4
UL 6,42
UH 7,42
M 5
UL 8,43
UH 9,43
M 6
AD 169,6
AD 193,7
M 7
WL 8
AD 217,8
AD 241,9
M 8
UL 6,38
UH 7,38
M 9
WL 2
UL 8,39
UH 9,39
M 10
AD 170,6
AD 194,7
M 11
R 54
M 12
AD 218,8
AD 242,9
M 13
WV 34
UL 6,36
UH 7,36
M 14
UL 8,37
UH 9,37
M 15
AD 171,6
AD 195,7
M 16
AD 219,8
AD 243,9
M 17
M 18
M 19
M 20
M 21
M 22
M 23
WL 2
R 56
M 24
M 25
M 26
M 27
M 28
M 29
M 30
M 31
M 32
M 33
M 34
M 35
WL 2
R 58
M 36
M 37
M 38
M 39
M 40
M 41
M 42
M 43
M 44
WV 26
M 45
WL 0
s_barrier
M 46
mw 48
M 47
R 72
M 48
ad 84,1008
W 0
M 49
M 50
M 51
M 52
W 1
M 53
M 54
M 55
M 56
W 2
M 57
M 58
M 59
R 74
M 60
mw 51
M 61
ad 84,1011
W 0
M 62
M 63
M 64
M 65
W 1
M 66
M 67
M 68
M 69
W 2
M 70
M 71
WL 2
R 76
M 72
M 73
bc 48,21,64,56
M 74
mh 3
M 75
ad 64,2048
)"
R"(H 0
M 76
M 77
M 78
M 79
H 1
M 80
M 81
M 82
M 83
WL 2
R 78
M 84
mh 5
M 85
ad 66,2049
H 0
M 86
ae 44,68,432
M 87
s_bitcmp1_b32 s50, 16
s_cselect_b32 s51, 24576, 0
v_add_u32 v52, s51, v3
M 88
XL 18,256
M 89
mh 6
M 90
ad 64,2050
H 0
M 91
M 92
XL 46,1024
M 93
M 94
H 1
M 95
WL 2
R 80
M 96
M 97
M 98
bc 49,22,66,60
XL 48,1792
M 99
mh 8
M 100
ad 66,2144
H 0
M 101
M 102
M 103
M 104
XL 44,2560
M 105
M 106
M 107
WL 2
R 82
M 108
M 109
M 110
M 111
M 112
M 113
M 114
M 115
M 116
WV 26
M 117
WL 0
s_barrier
M 118
mw 72
M 119
R 0
M 120
ad 84,1032
W 0
M 121
M 132
M 133
T 24,10
M 122
T 28,24
M 123
W 1
M 134
M 135
T 32,11
M 124
T 36,25
M 125
W 2
M 136
M 137
T 40,12
M 126
T 44,26
M 127
mw 75
M 138
ad 84,1035
W 0
M 139
WL 8
T 48,13
M 128
T 52,27
M 129
M 140
W 1
UL 6,28
M 141
T 56,0
M 130
WL 8
T 60,14
M 131
R 2
M 142
W 2
UH 7,28
M 143
T 64,1
M 0
WL 8
T 68,15
M 1
R 4
M 2
UL 8,29
UH 9,29
M 3
AD 172,6
AD 196,7
M 4
AD 220,8
AD 244,9
M 5
UL 6,30
UH 7,30
M 6
UL 8,31
UH 9,31
M 7
WL 8
AD 173,6
AD 197,7
M 8
WL 2
AD 221,8
AD 245,9
M 9
WV 31
UL 6,32
UH 7,32
M 10
UL 8,33
UH 9,33
M 11
R 6
M 12
AD 174,6
AD 198,7
M 13
AD 222,8
AD 246,9
M 14
WV 29
UL 6,40
UH 7,40
M 15
UL 8,41
UH 9,41
M 16
AD 175,6
AD 199,7
M 17
AD 223,8
AD 247,9
M 18
M 19
M 20
M 21
M 22
M 23
WL 2
R 8
M 24
M 25
M 26
M 27
M 28
M 29
M 30
M 31
M 32
M 33
M 34
M 35
WL 2
R 10
M 36
M 37
M 38
M 39
M 40
M 41
M 42
M 43
M 44
WV 22
M 45
WL 0
s_barrier
M 46
mw 0
M 47
R 24
M 48
ad 84,1056
W 0
M 49
M 50
M 51
M 52
W 1
M 53
M 54
M 55
M 56
W 2
M 57
M 58
M 59
R 26
M 60
mw 3
M 61
ad 84,1059
W 0
M 62
M 63
M 64
M 65
W 1
M 66
M 67
M 68
M 69
W 2
M 70
M 71
WL 2
R 28
M 72
M 73
bc 48,22,64,56
M 74
mh 9
M 75
ad 64,2144
H 0
M 76
M 77
M 78
M 79
H 1
M 80
M 81
M 82
M 83
WL 2
R 30
M 84
mh 11
M 85
ad 66,2145
H 0
M 86
M 87
M 88
M 89
mh 12
M 90
ad 64,2146
H 0
M 91
ae 44,68,432
M 92
s_bfe_u32 s51, s50, 0x10011
v_mad_i32_i24 v52, v51, s51, v50
XL 34,256
M 93
M 94
H 1
M 95
WL 2
R 32
M 96
XL 22,1024
M 97
M 98
bc 49,23,66,60
M 99
mh 0
M 100
ad 66,2240
H 0
M 101
M 102
XL 24,1792
M 103
M 104
M 105
M 106
M 107
WL 2
R 34
M 108
XL 14,2560
M 109
M 110
M 111
M 112
M 113
M 114
M 115
WV 25
M 116
WL 0
M 117
s_barrier
M 118
mw 24
M 119
R 48
M 120
ad 84,1080
W 0
M 121
M 132
M 133
T 24,2
M 122
T 28,16
M 123
W 1
M 134
M 135
T 32,3
M 124
T 36,17
M 125
W 2
M 136
M 137
T 40,4
M 126
T 44,18
M 127
mw 27
M 138
ad 84,1083
W 0
M 139
WL 8
T 48,5
M 128
T 52,19
M 129
M 140
W 1
M 141
T 56,6
M 130
WL 8
T 60,20
M 131
R 50
M 142
W 2
UL 6,18
M 143
T 64,7
M 0
WL 8
T 68,21
M 1
R 52
M 2
UH 7,18
UL 8,19
M 3
UH 9,19
AD 176,6
M 4
AD 200,7
AD 224,8
)"
R"(M 5
AD 248,9
UL 6,46
M 6
UH 7,46
UL 8,47
M 7
WL 8
UH 9,47
AD 177,6
M 8
WL 2
AD 201,7
AD 225,8
M 9
WV 30
AD 249,9
UL 6,48
M 10
UH 7,48
UL 8,49
M 11
R 54
M 12
UH 9,49
AD 178,6
M 13
AD 202,7
AD 226,8
M 14
WV 28
AD 250,9
UL 6,44
M 15
UH 7,44
UL 8,45
M 16
UH 9,45
AD 179,6
M 17
AD 203,7
AD 227,8
M 18
AD 251,9
M 19
M 20
M 21
M 22
M 23
WL 2
R 56
M 24
M 25
M 26
M 27
M 28
M 29
M 30
M 31
M 32
M 33
M 34
M 35
WL 2
R 58
M 36
M 37
M 38
M 39
M 40
M 41
M 42
M 43
M 44
WV 22
M 45
WL 0
s_barrier
M 46
mw 48
M 47
R 72
M 48
ad 84,1104
W 0
M 49
M 50
M 51
M 52
W 1
M 53
M 54
M 55
M 56
W 2
M 57
M 58
M 59
R 74
M 60
mw 51
M 61
ad 84,1107
W 0
M 62
M 63
M 64
M 65
W 1
M 66
M 67
M 68
M 69
W 2
M 70
M 71
WL 2
R 76
M 72
M 73
bc 48,23,64,56
M 74
mh 1
M 75
ad 64,2240
H 0
M 76
M 77
M 78
M 79
H 1
M 80
M 81
M 82
M 83
WL 2
R 78
M 84
mh 3
M 85
ad 66,2241
H 0
M 86
M 87
M 88
M 89
mh 4
M 90
ad 64,2242
H 0
M 91
M 92
M 93
M 94
H 1
M 95
WL 2
R 80
M 96
M 97
M 98
bc 48,0,64,56
M 99
mh 6
M 100
ad 64,33
H 0
M 101
M 102
M 103
M 104
M 105
M 106
M 107
WL 2
R 82
M 108
M 109
M 110
M 111
M 112
M 113
M 114
M 115
M 116
WV 22
M 117
WL 0
s_barrier
M 118
mw 72
M 119
R 0
M 120
ad 84,1128
W 0
M 121
M 132
M 133
T 24,8
M 122
T 28,22
M 123
W 1
M 134
M 135
T 32,9
M 124
T 36,23
M 125
W 2
M 136
M 137
T 40,10
M 126
T 44,24
M 127
mw 75
M 138
ad 84,1131
W 0
M 139
WL 8
T 48,11
M 128
T 52,25
M 129
M 140
W 1
M 141
T 56,12
M 130
WL 8
T 60,26
M 131
R 2
M 142
W 2
M 143
T 64,13
M 0
WL 8
T 68,27
M 1
R 4
M 2
bc 49,0,66,60
UL 6,34
M 3
UH 7,34
UL 8,35
M 4
mh 7
UH 9,35
AD 180,6
M 5
WL 8
AD 204,7
AD 228,8
M 6
ad 66,33
H 0
M 7
WV 28
AD 252,9
UL 6,22
M 8
UH 7,22
UL 8,23
M 9
WL 2
UH 9,23
AD 181,6
M 10
AD 205,7
AD 229,8
M 11
R 6
M 12
WV 25
AD 253,9
UL 6,24
M 13
UH 7,24
UL 8,25
M 14
UH 9,25
AD 182,6
M 15
AD 206,7
AD 230,8
M 16
AD 254,9
UL 6,14
M 17
UH 7,14
UL 8,15
M 18
UH 9,15
AD 183,6
M 19
AD 207,7
AD 231,8
M 20
AD 255,9
M 21
M 22
M 23
WL 2
R 8
M 24
M 25
M 26
M 27
M 28
M 29
M 30
M 31
M 32
M 33
M 34
M 35
WL 2
R 10
M 36
M 37
M 38
M 39
M 40
M 41
M 42
M 43
M 44
WV 19
M 45
WL 0
s_barrier
M 46
mw 0
M 47
R 24
M 48
ad 84,0
W 0
M 49
M 50
M 51
M 52
W 1
M 53
M 54
M 55
M 56
W 2
M 57
M 58
M 59
R 26
M 60
mw 3
M 61
ad 84,3
W 0
M 62
M 63
M 64
M 65
W 1
M 66
M 67
M 68
M 69
W 2
M 70
M 71
WL 2
R 28
M 72
M 73
mh 8
M 74
ad 64,34
H 0
M 75
M 76
M 77
M 78
H 1
M 79
M 80
M 81
M 82
bc 48,1,64,56
M 83
WL 2
R 30
M 84
mh 10
M 85
ad 64,130
)"
R"(H 0
M 86
M 87
M 88
M 89
H 1
M 90
M 91
M 92
M 93
M 94
M 95
WL 2
R 32
M 96
M 97
M 98
M 99
M 100
M 101
M 102
M 103
M 104
M 105
M 106
M 107
WL 2
R 34
M 108
M 109
M 110
M 111
M 112
M 113
M 114
M 115
M 116
WV 18
M 117
WL 0
s_barrier
M 118
mw 24
M 119
R 48
M 120
ad 84,24
W 0
M 121
M 132
M 133
T 24,0
M 122
T 28,14
M 123
W 1
M 134
M 135
T 32,1
M 124
T 36,15
M 125
W 2
M 136
M 137
T 40,2
M 126
T 44,16
M 127
mw 27
M 138
ad 84,27
W 0
M 139
WL 8
T 48,3
M 128
T 52,17
M 129
M 140
W 1
M 141
T 56,4
M 130
WL 8
T 60,18
M 131
R 50
M 142
W 2
M 143
T 64,5
M 0
WL 8
T 68,19
M 1
R 52
M 2
M 3
M 4
M 5
M 6
M 7
WL 8
M 8
M 9
WL 2
M 10
M 11
R 54
M 12
M 13
M 14
M 15
M 16
M 17
M 18
M 19
M 20
M 21
M 22
M 23
WL 2
R 56
M 24
M 25
M 26
M 27
M 28
M 29
M 30
M 31
M 32
M 33
M 34
M 35
WL 2
R 58
M 36
M 37
M 38
M 39
M 40
AR 6,72
AR 7,96
M 41
AR 8,120
s_nop 0
AR 9,144
M 42
EF 6
EF 7
M 43
WV 17
EF 8
EF 9
M 44
WL 0
CV 16,6,7
CV 17,8,9
M 45
s_barrier
AR 6,73
AR 7,97
M 46
mw 48
AR 8,121
AR 9,145
M 47
R 72
M 48
ad 84,48
W 0
M 49
EF 6
EF 7
M 50
EF 8
EF 9
M 51
CV 20,6,7
CV 21,8,9
M 52
W 1
AR 6,74
M 53
AR 7,98
AR 8,122
M 54
AR 9,146
EF 6
M 55
EF 7
EF 8
M 56
W 2
EF 9
M 57
CV 26,6,7
CV 27,8,9
M 58
AR 6,75
AR 7,99
M 59
R 74
M 60
mw 51
AR 8,123
AR 9,147
M 61
ad 84,51
W 0
M 62
EF 6
EF 7
M 63
EF 8
EF 9
M 64
CV 42,6,7
CV 43,8,9
M 65
W 1
AR 6,76
M 66
ae 42,74,384
AR 7,100
M 67
s_bitcmp1_b32 s50, 12
s_cselect_b32 s52, 24576, 0
v_add_u32 v53, s52, v3
M 68
ST 16,0
AR 8,124
M 69
W 2
AR 9,148
M 70
EF 6
EF 7
M 71
WL 0
R 76
M 72
ST 20,768
EF 8
M 73
EF 9
CV 38,6,7
M 74
CV 39,8,9
AR 6,77
M 75
AR 7,101
AR 8,125
M 76
ST 26,1536
AR 9,149
M 77
EF 6
EF 7
M 78
EF 8
EF 9
M 79
CV 36,6,7
CV 37,8,9
M 80
ST 42,2304
AR 6,78
M 81
AR 7,102
AR 8,126
M 82
AR 9,150
EF 6
M 83
R 78
M 84
EF 7
EF 8
M 85
EF 9
CV 28,6,7
M 86
CV 29,8,9
AR 6,79
M 87
AR 7,103
AR 8,127
M 88
AR 9,151
EF 6
M 89
EF 7
EF 8
M 90
EF 9
CV 30,6,7
M 91
CV 31,8,9
M 92
ae 42,72,384
EF 64
M 93
s_bitcmp1_b32 s50, 13
s_cselect_b32 s52, 24576, 0
v_add_u32 v53, s52, v3
M 94
ST 38,0
EF 88
M 95
WL 1
R 80
M 96
EF 112
EF 136
M 97
CV 32,64,88
CV 33,112,136
M 98
ST 36,768
EF 65
M 99
EF 89
EF 113
M 100
EF 137
CV 40,65,89
M 101
CV 41,113,137
EF 66
M 102
ST 28,1536
EF 90
M 103
EF 114
EF 138
M 104
CV 18,66,90
CV 19,114,138
M 105
EF 67
EF 91
M 106
ST 30,2304
EF 115
M 107
WL 2
R 82
M 108
EF 139
CV 46,67,91
M 109
CV 47,115,139
M 110
ae 42,74,384
EF 68
M 111
s_bitcmp1_b32 s50, 12
s_cselect_b32 s52, 24576, 0
v_add_u32 v53, s52, v3
M 112
ST 32,128
EF 92
M 113
EF 116
EF 140
M 114
CV 48,68,92
CV 49,116,140
M 115
WV 23
EF 69
EF 93
M 116
WL 0
ST 40,896
EF 117
M 117
s_barrier
EF 141
CV 44,69,93
M 118
mw 72
CV 45,117,141
EF 70
M 119
R 0
M 120
ad 84,72
W 0
M 121
ST 18,1664
EF 94
M 132
EF 118
EF 142
M 133
CV 34,70,94
)"
R"(CV 35,118,142
M 122
W 1
EF 71
M 123
ST 46,2432
EF 95
M 134
EF 119
EF 143
M 135
CV 22,71,95
CV 23,119,143
M 124
W 2
EF 160
M 125
ae 42,72,384
EF 184
M 136
s_bitcmp1_b32 s50, 13
s_cselect_b32 s52, 24576, 0
v_add_u32 v53, s52, v3
M 137
T 40,6
M 126
mw 75
T 44,20
M 127
ad 84,75
W 0
M 138
ST 48,128
EF 208
M 139
T 48,7
M 128
T 52,21
M 129
W 1
EF 232
M 140
ST 44,896
CV 24,160,184
M 141
T 56,8
M 130
T 60,22
M 131
WL 8
R 2
M 142
W 2
ST 34,1664
M 143
T 64,9
M 4
T 68,23
M 5
WL 8
R 4
M 6
ST 22,2432
CV 25,208,232
M 7
WL 8
EF 161
EF 185
M 8
EF 209
EF 233
M 9
WL 2
CV 14,161,185
CV 15,209,233
M 10
EF 162
EF 186
M 11
R 6
M 16
EF 210
EF 234
M 17
CV 16,162,186
CV 17,210,234
M 18
EF 163
EF 187
M 19
EF 211
EF 235
M 20
CV 20,163,187
CV 21,211,235
M 21
ae 42,74,384
EF 164
M 22
s_bitcmp1_b32 s50, 12
s_cselect_b32 s52, 24576, 0
v_add_u32 v53, s52, v3
M 23
WL 2
R 8
M 28
ST 24,256
EF 188
M 29
EF 212
EF 236
M 30
CV 26,164,188
CV 27,212,236
M 31
EF 165
EF 189
M 32
ST 14,1024
EF 213
M 33
EF 237
CV 42,165,189
M 34
CV 43,213,237
EF 166
M 35
WL 2
R 10
M 40
ST 16,1792
EF 190
M 41
EF 214
EF 238
M 42
CV 38,166,190
CV 39,214,238
M 43
EF 167
EF 191
M 44
WV 31
ST 20,2560
EF 215
M 45
WL 0
EF 239
CV 36,167,191
M 46
s_barrier
CV 37,215,239
M 47
R 24
M 52
ae 42,72,384
AR 6,80
M 53
s_bitcmp1_b32 s50, 13
s_cselect_b32 s52, 24576, 0
v_add_u32 v53, s52, v3
M 54
ST 26,256
AR 7,104
M 55
AR 8,128
AR 9,152
M 56
EF 6
EF 7
M 57
EF 8
EF 9
M 58
ST 42,1024
CV 28,6,7
M 59
R 26
M 64
CV 29,8,9
AR 6,81
M 65
AR 7,105
AR 8,129
M 66
ST 38,1792
AR 9,153
M 67
EF 6
EF 7
M 68
EF 8
EF 9
M 69
CV 30,6,7
CV 31,8,9
M 70
ST 36,2560
AR 6,82
M 71
WL 2
R 28
M 76
AR 7,106
AR 8,130
M 77
AR 9,154
EF 6
M 78
EF 7
EF 8
M 79
EF 9
CV 32,6,7
M 80
CV 33,8,9
AR 6,83
M 81
AR 7,107
AR 8,131
M 82
AR 9,155
EF 6
M 83
WL 2
R 30
M 88
EF 7
EF 8
M 89
EF 9
CV 40,6,7
M 90
CV 41,8,9
M 91
ae 42,72,384
AR 6,84
M 92
s_bfe_u32 s52, s50, 0x1000e
v_mad_i32_i24 v53, v51, s52, v50
ST 28,0
M 93
AR 7,108
AR 8,132
M 94
AR 9,156
EF 6
M 95
WL 2
R 32
M 100
ST 30,768
EF 7
M 101
EF 8
EF 9
M 102
CV 18,6,7
CV 19,8,9
M 103
AR 6,85
AR 7,109
M 104
ST 32,1536
AR 8,133
M 105
AR 9,157
EF 6
M 106
EF 7
EF 8
M 107
WL 2
R 34
M 112
ST 40,2304
EF 9
M 113
CV 46,6,7
CV 47,8,9
M 114
AR 6,86
AR 7,110
M 115
AR 8,134
AR 9,158
M 116
WV 33
EF 6
EF 7
M 117
WL 0
EF 8
EF 9
M 118
s_barrier
CV 48,6,7
CV 49,8,9
M 119
R 48
M 124
AR 6,87
AR 7,111
M 125
AR 8,135
AR 9,159
M 136
EF 6
EF 7
M 137
EF 8
EF 9
M 126
CV 44,6,7
CV 45,8,9
M 127
ae 42,74,384
EF 72
M 138
s_bfe_u32 s52, s50, 0x1000f
v_mad_i32_i24 v53, v51, s52, v50
ST 18,0
M 139
EF 96
EF 120
M 128
EF 144
CV 34,72,96
M 129
CV 35,120,144
EF 73
M 140
ST 46,768
EF 97
M 141
T 56,10
M 130
T 60,24
M 131
R 50
M 142
ST 48,1536
EF 121
M 143
T 64,11
WL 4
M 8
T 68,25
M 9
R 52
WL 2
M 10
ST 44,2304
EF 145
M 11
R 54
M 20
CV 22,73,97
CV 23,121,145
M 21
EF 74
EF 98
M 22
EF 122
EF 146
M 23
WL 2
R 56
M 32
CV 24,74,98
CV 25,122,146
M 33
EF 75
EF 99
M 34
EF 123
EF 147
M 35
WL 2
R 58
M 44
CV 14,75,99
CV 15,123,147
M 45
WV 18
EF 76
EF 100
M 46
WL 0
s_barrier
EF 124
EF 148
M 47
R 72
M 56
ae 42,72,384
CV 16,76,100
M 57
s_bfe_u32 s52, s50, 0x1000e
v_mad_i32_i24 v53, v51, s52, v50
)"
R"(ST 34,128
M 58
CV 17,124,148
EF 77
M 59
R 74
M 68
EF 101
EF 125
M 69
ST 22,896
EF 149
M 70
CV 20,77,101
CV 21,125,149
M 71
WL 2
R 76
M 80
EF 78
EF 102
M 81
ST 24,1664
EF 126
M 82
EF 150
CV 26,78,102
M 83
WL 2
R 78
M 92
CV 27,126,150
EF 79
M 93
ST 14,2432
EF 103
M 94
EF 127
EF 151
M 95
WL 2
R 80
M 104
CV 42,79,103
CV 43,127,151
M 105
ae 42,74,384
EF 168
EF 192
M 106
s_bfe_u32 s52, s50, 0x1000f
v_mad_i32_i24 v53, v51, s52, v50
ST 16,128
EF 216
M 107
WL 2
R 82
EF 240
M 116
CV 38,168,192
CV 39,216,240
EF 169
M 117
EF 193
EF 217
EF 241
M 118
ST 20,896
CV 36,169,193
CV 37,217,241
M 119
WL 0
EF 170
EF 194
EF 218
M 128
EF 242
CV 28,170,194
CV 29,218,242
M 129
EF 171
EF 195
EF 219
M 140
ST 26,1664
EF 243
CV 30,171,195
M 141
CV 31,219,243
M 130
EF 172
EF 196
EF 220
EF 244
CV 32,172,196
CV 33,220,244
M 131
EF 173
EF 197
EF 221
EF 245
CV 40,173,197
CV 41,221,245
M 142
ST 42,2432
EF 174
EF 198
EF 222
EF 246
CV 18,174,198
M 143
ae 42,72,384
s_bfe_u32 s52, s50, 0x1000e
v_mad_i32_i24 v53, v51, s52, v50
ST 38,256
ST 36,1024
ST 28,1792
ST 30,2560
CV 19,222,246
EF 175
EF 199
EF 223
EF 247
CV 46,175,199
CV 47,223,247
ae 42,74,384
s_bfe_u32 s52, s50, 0x1000f
v_mad_i32_i24 v53, v51, s52, v50
ST 32,256
ST 40,1024
ST 18,1792
ST 46,2560
AR 6,88
AR 7,112
AR 8,136
EF 6
EF 7
EF 8
EF 56
CV 48,6,7
CV 49,8,56
AR 6,89
AR 7,113
AR 8,137
EF 6
EF 7
EF 8
EF 57
CV 44,6,7
CV 45,8,57
AR 6,90
AR 7,114
AR 8,138
EF 6
EF 7
EF 8
EF 58
CV 34,6,7
CV 35,8,58
AR 6,91
AR 7,115
AR 8,139
EF 6
EF 7
EF 8
EF 59
CV 22,6,7
CV 23,8,59
ae 42,72,432
s_bitcmp1_b32 s50, 16
s_cselect_b32 s52, 24576, 0
v_add_u32 v53, s52, v3
ST 48,0
ST 44,768
ST 34,1536
ST 22,2304
AR 6,92
AR 7,116
AR 8,140
EF 6
EF 7
EF 8
EF 60
CV 24,6,7
CV 25,8,60
AR 6,93
AR 7,117
AR 8,141
EF 6
EF 7
EF 8
EF 61
CV 14,6,7
CV 15,8,61
AR 6,94
AR 7,118
AR 8,142
EF 6
EF 7
EF 8
EF 62
CV 16,6,7
CV 17,8,62
AR 6,95
AR 7,119
AR 8,143
EF 6
EF 7
EF 8
EF 63
CV 20,6,7
CV 21,8,63
ae 42,72,432
s_bfe_u32 s52, s50, 0x10011
v_mad_i32_i24 v53, v51, s52, v50
ST 24,0
ST 14,768
ST 16,1536
ST 20,2304
EF 80
EF 104
EF 128
EF 152
CV 26,80,104
CV 27,128,152
EF 81
EF 105
EF 129
EF 153
CV 42,81,105
CV 43,129,153
EF 82
EF 106
EF 130
EF 154
CV 38,82,106
CV 39,130,154
EF 83
EF 107
EF 131
EF 155
CV 36,83,107
CV 37,131,155
ae 42,72,432
s_bitcmp1_b32 s50, 16
s_cselect_b32 s52, 24576, 0
v_add_u32 v53, s52, v3
ST 26,128
ST 42,896
ST 38,1664
ST 36,2432
EF 84
EF 108
EF 132
EF 156
CV 28,84,108
CV 29,132,156
EF 85
EF 109
EF 133
EF 157
CV 30,85,109
CV 31,133,157
EF 86
EF 110
EF 134
EF 158
CV 32,86,110
CV 33,134,158
EF 87
EF 111
EF 135
EF 159
CV 40,87,111
CV 41,135,159
ae 42,72,432
s_bfe_u32 s52, s50, 0x10011
v_mad_i32_i24 v53, v51, s52, v50
ST 28,128
ST 30,896
ST 32,1664
ST 40,2432
EF 176
EF 200
EF 224
EF 248
CV 18,176,200
CV 19,224,248
EF 177
EF 201
EF 225
EF 249
CV 46,177,201
CV 47,225,249
EF 178
EF 202
EF 226
EF 250
CV 48,178,202
CV 49,226,250
EF 179
EF 203
EF 227
EF 251
CV 44,179,203
CV 45,227,251
ae 42,72,432
s_bitcmp1_b32 s50, 16
s_cselect_b32 s52, 24576, 0
v_add_u32 v53, s52, v3
ST 18,256
ST 46,1024
ST 48,1792
ST 44,2560
EF 180
EF 204
EF 228
EF 252
CV 34,180,204
CV 35,228,252
EF 181
EF 205
EF 229
EF 253
CV 22,181,205
CV 23,229,253
EF 182
EF 206
EF 230
EF 254
CV 24,182,206
CV 25,230,254
EF 183
EF 207
EF 231
EF 255
CV 14,183,207
CV 15,231,255
ae 42,72,432
s_bfe_u32 s52, s50, 0x10011
v_mad_i32_i24 v53, v51, s52, v50
ST 34,256
ST 22,1024
ST 24,1792
ST 14,2560
s_waitcnt vmcnt(0) lgkmcnt(0)
.purgem MA
.purgem MV
.purgem AN
.purgem AW
.purgem AR
.purgem UL
.purgem UH
.purgem AD
.purgem TR
.purgem TQ
.purgem RA
.purgem RC
.purgem BV
.purgem BA
.purgem PA
.purgem DF
.purgem DW
.purgem DH
.purgem DS
.purgem XL
.purgem ST
.purgem SU
.purgem SA
.purgem CV
.purgem DR
.purgem TY
.purgem TZ
.purgem RS
.purgem PM
.purgem FS
.purgem FL
.purgem FB
.purgem FG
.purgem FX
.purgem PB
.purgem PQ
.purgem PJ
.purgem WL
.purgem WV
.purgem EF
.purgem M
.purgem R1
.purgem R
.purgem T
.purgem mw
.purgem mh
.purgem ad
.purgem ae
.purgem bc
.purgem W
.purgem H
)"
      : [flg] "=&s"(flg) : K2_OPS : K2_CLOB);
  if (flg) {
    const unsigned lane = __builtin_amdgcn_mbcnt_hi(~0u, __builtin_amdgcn_mbcnt_lo(~0u, 0u));
    const bool hbf = __ballot(lane < 26 && lane != 1 && __hip_atomic_load((const unsigned*)(g_buf + G_FLAG) + 96 + min(lane, 25u), __ATOMIC_RELAXED, __HIP_MEMORY_SCOPE_AGENT) != 0) != 0;
    const u16* const volatile __attribute__((address_space(4)))* ka = (const u16* const volatile __attribute__((address_space(4)))*)__builtin_amdgcn_kernarg_segment_ptr();
    if (hbf) {
      const u16* w1p = ka[6];
#pragma unroll 1
      for (unsigned f = blockIdx.x * 4 + w; f < 1152; f += 1024) {
        const u16* p = w1p + (32 * (f % 12) + 8 * (lane >> 4)) * HID + 16 * (f / 12) + (lane & 15);
        u32x4 v;
#pragma unroll
        for (int j = 0; j < 4; ++j) v[j] = p[2 * j * HID] | (unsigned)p[(2 * j + 1) * HID] << 16;
        *(u32x4*)(g_buf + f * 1024 + lane * 16) = v;
      }
    }
    slow_bar((unsigned*)(g_buf + G_BAR), w);
    if (hbf) {
      slow_fc1(ka[0], ka[5], (char*)ka[1], (float*)smem + w * HID, w);
      slow_bar((unsigned*)(g_buf + G_BAR + 64), w);
    }
    slow_fc2(ka[0], (const char*)ka[1], (u16*)ka[2], ka[3], ka[4], smem, hbf, w);
  }
  TS(15);
  MK_END(4);
}

extern "C" void kernel_launch(void* const* d_in, const int* in_sizes, int n_in,
                              void* d_out, int out_size, void* d_ws, size_t ws_size,
                              hipStream_t stream) {
  const u16* x = (const u16*)d_in[0];
  const u16* dw = (const u16*)d_in[1];
  const u16* w1 = (const u16*)d_in[2];
  const u16* w2 = (const u16*)d_in[3];
  const u16* grn_g = (const u16*)d_in[4];
  const u16* grn_b = (const u16*)d_in[5];
  char* ws = (char*)d_ws;
  if (ws_size < H_BYTES) return;
  (void)hipFuncSetAttribute((const void*)k1_fc1, hipFuncAttributeMaxDynamicSharedMemorySize, K1_LDS);
  (void)hipFuncSetAttribute((const void*)k2_fc2, hipFuncAttributeMaxDynamicSharedMemorySize, K2_LDS);

  k0_prep<<<8 + K0_NB + NPAGE + NPAGX, 256, 0, stream>>>(w1, dw, ws, (const char*)x, grn_b, w2);
  k1_fc1<<<NWG + 8, 256, K1_LDS, stream>>>(x, ws, (const u16*)d_out, grn_g);
  k2_fc2<<<NWG, 256, K2_LDS, stream>>>(x, ws, (u16*)d_out, grn_g, w2, dw, w1);
}
```

```cpp
#include <hip/hip_runtime.h>
#include <hip/hip_bf16.h>
#include <stdint.h>
#include <stdio.h>

typedef short bf16x8 __attribute__((ext_vector_type(8)));
typedef short s16x4 __attribute__((ext_vector_type(4)));
typedef short v4i16_t __attribute__((ext_vector_type(4)));
typedef float f32x16 __attribute__((ext_vector_type(16)));
typedef float f32x4 __attribute__((ext_vector_type(4)));
typedef float f32x2 __attribute__((ext_vector_type(2)));
typedef unsigned u32x4 __attribute__((ext_vector_type(4)));
typedef __bf16 bf16x2_t __attribute__((ext_vector_type(2)));
typedef unsigned short u16;

constexpr float TZ = 175.f, TH = 175.f, TW2 = 175.f;
#define DI __device__ __forceinline__
#define LAS __attribute__((address_space(3)))

constexpr int NPIX = 147456;
constexpr int CH = 384;
constexpr int HID = 1536;
constexpr int NTILE = NPIX / 32;
constexpr int NWG = 256;
constexpr int TPW = NTILE / NWG;

constexpr size_t H_BYTES = (size_t)NPIX * HID * 2;
constexpr unsigned K1_LDS = 163840, K1_PF_OFF = 130048, K1_SMAX = 10, K1_NREP = 4, K1_G_TAB = 1228800, K1_G_CNT = 1239040, K1_G_LOC = 1236992, K1_G_DUM = 1255424,
  K1_G_ACC = 1320960, K1_G_LUT = 1382400, K1_G_BYTES = 1398784;
constexpr bool K1_LUT = true;
constexpr float K1_ZS = 16.0f;
constexpr unsigned K1_GST = 25600, K1_WCH = 6400, K1_ZC = 1365;
constexpr bool K1_F2 = true;
constexpr unsigned K2_SHT = 49923;
constexpr int K2_LDS = 163840, K2_HB = 98304, K2_NU = 14, K2_BIAS_OFF = 155648, K2_PF_OFF = 161792;
#define K2_ROLES 0
constexpr int K2_TS = 1, K2_S16_OFF = 109568;

__device__ __attribute__((aligned(256))) char g_buf[K1_G_BYTES];
__device__ __attribute__((aligned(256))) float g_bias[2048];
__device__ __attribute__((aligned(256))) char g_w2r[96 * 12 * 1024];
__device__ __attribute__((aligned(256))) char g_w2f[96 * 12 * 1024];
__device__ __attribute__((aligned(256))) char g_w2q[16 * 12 * 1024];
__device__ __attribute__((aligned(256))) float g_sc[64];
constexpr unsigned G_FLAG = K1_G_CNT + 14336;
constexpr unsigned G_PUB = K1_G_DUM;
__device__ __attribute__((aligned(256))) unsigned g_prm[1024 * 64];
constexpr unsigned G_STMP = K1_G_CNT + 12288;
constexpr unsigned G_BAR = K1_G_CNT + 13312;
constexpr unsigned H_TS = 49152, H_RS = 48 * H_TS, NPAGE = 217, NPAGX = 55;
#define TS(i) do { } while (0)
#define KST(i) do { } while (0)
#define MK_START(i) do { } while (0)
#define MK_END(i) do { } while (0)

DI unsigned pack_bf16(float a, float b) {
  f32x2 v = {a, b};
  bf16x2_t r = __builtin_convertvector(v, bf16x2_t);
  return __builtin_bit_cast(unsigned, r);
}
typedef _Float16 f16x2_t __attribute__((ext_vector_type(2)));
DI unsigned pack_f16(float a, float b) {
  f32x2 v = {a, b};
  f16x2_t r = __builtin_convertvector(v, f16x2_t);
  return __builtin_bit_cast(unsigned, r);
}
DI float bf_lo(unsigned u) { return __uint_as_float(u << 16); }
DI float bf_hi(unsigned u) { return __uint_as_float(u & 0xffff0000u); }
DI float round_bf16(float a) { return bf_lo(pack_bf16(a, 0.f)); }
DI float dot2acc(unsigned a, unsigned b, float c) {
  return __builtin_amdgcn_fdot2_f32_bf16(__builtin_bit_cast(bf16x2_t, a), __builtin_bit_cast(bf16x2_t, b), c, false);
}
DI float ld_bf16(const u16* p) { return __uint_as_float((unsigned)(*p) << 16); }

DI s16x4 tr_read(const char* p) {
  return __builtin_bit_cast(s16x4, __builtin_amdgcn_ds_read_tr16_b64_v4i16((LAS v4i16_t*)p));
}

#define MFMA32(a, b, c) __builtin_amdgcn_mfma_f32_32x32x16_bf16((a), (b), (c), 0, 0, 0)
#define BAR() __builtin_amdgcn_s_barrier()

DI void dma16(unsigned voff, const char* sbase, unsigned lds_dst) {
  unsigned keep;
  asm volatile("s_mov_b32 %0, m0\n\ts_mov_b32 m0, %3\n\ts_nop 0\n\tglobal_load_lds_dwordx4 %1, %2\n\ts_mov_b32 m0, %0"
               : "=&s"(keep) : "v"(voff), "s"(sbase), "s"(lds_dst) : "memory");
}
DI unsigned lds_addr(const char* p) { return (unsigned)(size_t)(const LAS char*)p; }

DI float gelu_tanh(float x) {
  const float c0 = -2.0f * 0.7978845608028654f * 1.4426950408889634f;
  const float c1 = c0 * 0.044715f;
  float x2 = x * x;
  float t = __builtin_fmaf(x2, c1, c0);
  float e = __builtin_amdgcn_exp2f(x * t);
  float r = __builtin_amdgcn_rcpf(1.0f + e);
  return x * r;
}

DI void gload_b128_asm(u32x4& dst, const char* sbase, unsigned voff) {
  asm volatile("global_load_dwordx4 %0, %1, %2" : "=v"(dst) : "v"(voff), "s"(sbase) : "memory");
}
DI void lds_rd128(u32x4& dst, unsigned addr, int off) {
  asm volatile("ds_read_b128 %0, %1 offset:%2" : "=v"(dst) : "v"(addr), "i"(off));
}
DI void lds_rd128_far(u32x4& dst, unsigned voff, unsigned sbase, int imm) {
  unsigned t;
  asm volatile("v_add_u32 %1, %3, %2\n\tv_add_u32 %1, %4, %1\n\tds_read_b128 %0, %1" : "=v"(dst), "=&v"(t) : "v"(voff), "s"(sbase), "i"(imm));
}
DI void gload16i(u32x4& dst, const char* sbase, unsigned voff, int imm) {
  unsigned t;
  asm volatile("v_add_u32 %1, %4, %2\n\tglobal_load_dwordx4 %0, %1, %3" : "=v"(dst), "=&v"(t) : "v"(voff), "s"(sbase), "i"(imm) : "memory");
}
DI void dma16i(const char* sbase, unsigned voff, int immv, unsigned lbase, int imml) {
  unsigned t, keep;
  asm volatile("s_mov_b32 %1, m0\n\ts_add_u32 m0, %4, %6\n\tv_add_u32 %0, %5, %2\n\tglobal_load_lds_dwordx4 %0, %3\n\ts_mov_b32 m0, %1"
               : "=&v"(t), "=&s"(keep) : "v"(voff), "s"(sbase), "s"(lbase), "i"(immv), "i"(imml) : "memory", "scc");
}

DI void st16_wt(void* p, u32x4 v) { asm volatile("global_store_dwordx4 %0, %1, off\n\ts_nop 1" :: "v"(p), "v"(v) : "memory"); }
template <int I> struct IC { static constexpr int value = I; };
template <int N, int I = 0, typename F> DI void static_for(F&& f) {
  if constexpr (I < N) { f(IC<I>{}); static_for<N, I + 1>(f); }
}
template <int C, int M> DI float dpp_add(float v) { return v + __int_as_float(__builtin_amdgcn_update_dpp(0, __float_as_int(v), C, M, 0xf, false)); }
DI float wave_sum63(float v) {
  v = dpp_add<0x111, 0xf>(v); v = dpp_add<0x112, 0xf>(v); v = dpp_add<0x114, 0xf>(v); v = dpp_add<0x118, 0xf>(v);
  v = dpp_add<0x142, 0xa>(v); return dpp_add<0x143, 0xc>(v);
}
DI uint64_t uni64(uint64_t v) { return (uint64_t)__builtin_amdgcn_readfirstlane(v >> 32) << 32 | (unsigned)__builtin_amdgcn_readfirstlane(v); }
DI unsigned wg_side(unsigned b) { return (((const unsigned*)(g_buf + K1_G_LOC))[b & 3] ^ (b >> 2)) & 1; }
DI unsigned h_flip(const char* ws, unsigned region, unsigned t) { return __popc(((const unsigned*)(g_buf + K1_G_LOC + 64))[(((unsigned)(size_t)ws & 0x1fffffu) + region * H_RS + t * H_TS) >> 21]) > 2; }
DI unsigned x_flip(const u16* x, unsigned region) { return __popc(((const unsigned*)(g_buf + K1_G_LOC + 1024))[(((unsigned)(size_t)x & 0x1fffffu) + (12 * region + 6) * 49152u) >> 21]) > 2; }
DI unsigned sub_pixel(const u16* x, unsigned sub, unsigned m) {
  const unsigned blk = sub / 24, u = sub % 24;
  const unsigned region = blk < 256 ? (blk >> 3) * 4 + (blk & 3) : 128 + ((blk - 256) >> 1), side = blk < 256 ? wg_side(blk) : blk & 1;
  return (12 * region + (u >> 1)) * 64 + ((0xda43cb52fe987610ull >> (32 * (u & 1) + 16 * (side ^ x_flip(x, region)) + (m & 12))) & 15) * 4 + (m & 3);
}
DI unsigned h_piece(const char* ws, unsigned sub, unsigned t) {
  const unsigned blk = sub / 24, u = sub % 24;
  const unsigned region = blk < 256 ? (blk >> 3) * 4 + (blk & 3) : 128 + ((blk - 256) >> 1), side = blk < 256 ? wg_side(blk) : blk & 1;
  return region * H_RS + t * H_TS + (u >> 3) * 16384 + (u & 7) * 1024 + ((side ^ h_flip(ws, region, t)) << 13);
}
constexpr unsigned K0_NB = 145 + 24 + 96 + 16 + 48;
constexpr float U24 = 0x1p-24f, EPS = 1e-5f;
DI float wave_all(float v) { return __int_as_float(__builtin_amdgcn_readlane(__float_as_int(wave_sum63(v)), 63)); }
DI float pow2f(float v) { return __uint_as_float(__float_as_uint(v) & 0x7f800000u); }
DI u32x4 w_load(const u16* w) { return *(const u32x4*)(w + ((threadIdx.x >> 3) & 7) * 73408 + 36800 + (threadIdx.x & 7) * 8); }
DI float w_ms(u32x4 a) {
  float q = 0.f;
#pragma unroll
  for (int i = 0; i < 4; ++i) { const float lo = bf_lo(a[i]), hi = bf_hi(a[i]); q = __builtin_fmaf(lo, lo, __builtin_fmaf(hi, hi, q)); }
  return wave_all(q) * (1.0f / 512);
}
DI float w1_scale(float ms) { return pow2f(300.f * U24 * __builtin_amdgcn_rsqf(ms)); }
DI float w2_scale(float ms) { return TW2 * U24 * __builtin_amdgcn_rsqf(ms); }
struct Sc { float w1, zs, g, h, vlo, vhi; };
DI Sc scales(const u16* w1, const u16* x, const u16* dw) {
  const u32x4 a = w_load(w1);
  const unsigned l = threadIdx.x & 63;
  float s = 0.f, q = 0.f;
#pragma unroll
  for (int k = 0; k < 6; ++k) {
    const u32x4 v = *(const u32x4*)(x + (9001 + 18397 * (l >> 3)) * CH + 64 * k + 8 * (l & 7)), d = *(const u32x4*)(dw + 64 * k + 8 * (l & 7));
#pragma unroll
    for (int i = 0; i < 4; ++i) { const float lo = bf_lo(v[i]) * bf_lo(d[i]), hi = bf_hi(v[i]) * bf_hi(d[i]); s += lo + hi; q = __builtin_fmaf(lo, lo, __builtin_fmaf(hi, hi, q)); }
  }
  s = dpp_add<0x141, 0xf>(dpp_add<0x4e, 0xf>(dpp_add<0xb1, 0xf>(s))); q = dpp_add<0x141, 0xf>(dpp_add<0x4e, 0xf>(dpp_add<0xb1, 0xf>(q)));
  const float var = fmaxf(q * (1.0f / CH) - s * s * (1.0f / (CH * CH)), 0.f);
  const float r2 = wave_all(var * __builtin_amdgcn_rcpf(var + EPS)) * (1.0f / 64), rden = __builtin_amdgcn_exp2f(wave_all(__builtin_amdgcn_logf(var + EPS)) * (-1.0f / 128));
  const float s1 = w_ms(a), p2 = r2 * CH * s1;
  Sc c;
  c.w1 = w1_scale(s1);
  c.g = TZ * U24 * __builtin_amdgcn_rsqf(r2);
  c.h = TH * U24 * __builtin_amdgcn_rsqf(p2 * (0.25f + 0.5f * p2 * __builtin_amdgcn_rcpf(1.0f + 2.0f * p2)));
  c.zs = fminf(pow2f(c.g * 2530.f), pow2f(c.g * 0x1p12f * rden));
  const float e = EPS * c.zs * c.zs, z2 = r2 * (660.f * 660.f / (TZ * TZ));
  c.vlo = 4.2f * c.g * c.g - e;
  c.vhi = fminf(0x1.ep29f * c.g * c.g - e, z2 < 1.0f ? e * z2 * __builtin_amdgcn_rcpf(1.0f - z2) : 0x1p120f);
  return c;
}
__global__ __launch_bounds__(256) void k0_prep(const u16* __restrict__ w1, const u16* __restrict__ dw, const char* __restrict__ ws, const char* __restrict__ x,
                                                const u16* __restrict__ grn_b, const u16* __restrict__ w2) {
  MK_START(0);
  uint64_t pc; int d1;
  asm volatile("s_getpc_b64 %0\n1:\n\ts_mov_b32 %1, k1_code-1b" : "=s"(pc), "=s"(d1));
  if (threadIdx.x >= 192) pc += (int64_t)d1 + ((int)(blockIdx.x >> 3) - 3) * 8192;
  unsigned pfd = 0;
  if (blockIdx.x < 64 && (threadIdx.x < 150 || (blockIdx.x < 48 && threadIdx.x >= 192))) pfd = *(const __attribute__((address_space(1))) unsigned*)((pc & ~127ull) + threadIdx.x * 128);
#define K0_END() do { asm volatile("" :: "v"(pfd)); MK_END(0); } while (0)
  unsigned xcc;
  asm volatile("s_getreg_b32 %0, hwreg(HW_REG_XCC_ID)" : "=s"(xcc));
  xcc = (xcc >> 2) & 1;
  if (blockIdx.x < 4 && threadIdx.x == 0) ((unsigned*)(g_buf + K1_G_LOC))[blockIdx.x] = 7 * xcc;
  constexpr unsigned NP = NPAGE + NPAGX;
  __shared__ Sc ssc;
  __shared__ float ss2;
  const bool sw = threadIdx.x >= 192;
  const unsigned bi = blockIdx.x;
  const unsigned bx = bi < NP ? bi + K0_NB : bi - NP;
  if (bx >= K0_NB) {
    __shared__ unsigned lat[8];
    const unsigned pg = bx - K0_NB, w = threadIdx.x >> 6, lane = threadIdx.x & 63;
    auto gran = [&](unsigned p) {
      const bool h = p < NPAGE;
      const char* buf = h ? ws : x;
      const int o0 = (int)((h ? p : p - NPAGE) << 21) - (int)((unsigned)(size_t)buf & 0x1fffffu);
      const unsigned al = h ? 49152 : 12 * 49152;
      const unsigned q = ((((unsigned)max(o0, 0) + al - 1) >> (h ? 14 : 16)) * (h ? 87382u : 29128u)) >> 18;
      return buf + min(q * al, (unsigned)(h ? H_BYTES : H_BYTES / 4) - 16384u);
    };
    if (lane < 5) {
      const char* a = gran(lane < 4 ? min(pg + NP - 1 - (lane >> 1), pg - 1 - (lane >> 1)) : pg) + (lane & 1) * 8192 + w * 2048 + (lane < 4 ? 1024 : 0);
      const char* b = a;
      const char* own = (const char*)uni64(__builtin_amdgcn_readlane((uint64_t)a >> 32, 4) * 4294967296ull + (unsigned)__builtin_amdgcn_readlane((unsigned)(uint64_t)a, 4));
      unsigned long long t0, t1, t2;
      int n = 2;
      asm volatile("" : "+s"(n));
#pragma unroll 1
      do {
        unsigned v;
        asm volatile("s_cmp_eq_u32 %6, 2\n\ts_cbranch_scc1 1f\n\ts_sleep 4\n1:\n\ts_memtime %0\n\ts_waitcnt lgkmcnt(0)\n\tglobal_load_dword %3, %4, off sc1\n\ts_waitcnt vmcnt(0)\n\ts_memtime %1\n\ts_waitcnt lgkmcnt(0)\n\t"
                     "global_load_dword %3, %5, off sc1\n\ts_waitcnt vmcnt(0)\n\ts_memtime %2\n\ts_waitcnt lgkmcnt(0)" : "=&s"(t0), "=&s"(t1), "=&s"(t2), "=&v"(v) : "v"(a), "v"(b), "s"(n) : "memory", "scc");
        a = own + 1024 + (w & 1) * 8192; b = a + 8192 - (w & 1) * 16384;
      } while (--n);
      lat[w + 4 * (w & 1)] = (unsigned)(t1 - t0); lat[w + 4 - 4 * (w & 1)] = (unsigned)(t2 - t1);
    }
    __syncthreads();
    if (threadIdx.x == 0) {
      auto fastest = [&](const unsigned* l) { unsigned m = 9999; for (int i = 0; i < 4; ++i) m = l[i] < 300 ? m : min(m, l[i]); return (int)m; };
      const int df = fastest(lat) - fastest(lat + 4);
      const unsigned v = xcc ^ (df > 0), sure = abs(df) > 80;
      char* e = g_buf + K1_G_LOC + 64 + 4 * (pg < NPAGE ? pg : pg + 240 - NPAGE);
      *(u16*)(e + 2) = v ? 3 + 4 * sure : 1 - sure;
      if (pg == 0 || pg == NPAGE) e[0] = v; else e[-3] = v ^ 1;
      if (pg == NPAGE - 1 || pg == NP - 1) e[1] = v; else e[4] = v ^ 1;
    }
    K0_END();
    return;
  }
  if (bx >= 145 + 24 + 96 + 16) {
    __shared__ __attribute__((aligned(16))) float red[2][16][32];
    const int t = bx - 281, c8 = (threadIdx.x & 3) * 8;
    u32x4 sm, v[6];
    if (sw) sm = w_load(w1);
#pragma unroll
    for (int k = 0; k < 6; ++k) v[k] = *(const u32x4*)(w1 + (size_t)(64 * k + (threadIdx.x >> 2)) * HID + 32 * t + c8);
    if (sw) ss2 = w1_scale(w_ms(sm));
    __syncthreads();
    const float s1 = ss2;
    float a[8], q[8];
#pragma unroll
    for (int i = 0; i < 8; ++i) {
      a[i] = q[i] = 0.f;
#pragma unroll
      for (int k = 0; k < 6; ++k) {
        const float f = (float)(_Float16)(s1 * (i & 1 ? bf_hi(v[k][i >> 1]) : bf_lo(v[k][i >> 1]))) * 0x1p24f;
        a[i] += f; q[i] = __builtin_fmaf(f, f, q[i]);
      }
      a[i] = dpp_add<0x118, 0xf>(dpp_add<0x114, 0xf>(a[i])); q[i] = dpp_add<0x118, 0xf>(dpp_add<0x114, 0xf>(q[i]));
    }
    if ((threadIdx.x & 12) == 12) {
#pragma unroll
      for (int i = 0; i < 2; ++i) {
        *(f32x4*)&red[0][threadIdx.x >> 4][c8 + 4 * i] = (f32x4){a[4 * i], a[4 * i + 1], a[4 * i + 2], a[4 * i + 3]};
        *(f32x4*)&red[1][threadIdx.x >> 4][c8 + 4 * i] = (f32x4){q[4 * i], q[4 * i + 1], q[4 * i + 2], q[4 * i + 3]};
      }
    }
    __syncthreads();
    if (threadIdx.x < 64) {
      const int n = threadIdx.x & 31;
      float s = 0.f;
#pragma unroll
      for (int i = 0; i < 16; ++i) s += red[threadIdx.x >> 5][i][n];
      const float c = (0.f - (float)K1_ZC * s) * 0x1p-48f;
      char* p = g_buf + t * K1_GST + 5120;
      if (threadIdx.x < 32) *(f32x4*)(p + 272 * (n >> 4) + (n & 15) * 16) = (f32x4){c, c, c, c};
      if (threadIdx.x == 32) *(u32x4*)(p + 256) = (u32x4){0u, 0u, 0u, 0u};
      const bool any = __ballot(threadIdx.x >= 32 && !(s >= 384.f * 64 * 64 && s <= 384.f * 1024 * 1024)) != 0;
      if (threadIdx.x == 0) g_buf[G_FLAG + 4 * 98 + t] = any;
    }
    K0_END();
    return;
  }
  if (bx >= 145 + 24 + 96) {
    if (sw) ssc = scales(w1, (const u16*)x, dw);
    __syncthreads();
    const Sc sc = ssc;
    const unsigned e = (bx - 265) * 256 + threadIdx.x;
    const float u = 1.0f / (sc.g * sc.w1);
    float r[2];
#pragma unroll
    for (int i = 0; i < 2; ++i) {
      const unsigned c = 2 * e + i;
      r[i] = sc.h * gelu_tanh(u * __uint_as_float((c >> 12) << 31 | ((c & 4095) + 79 * 128) << 16 | 0x8000u));
    }
    *(unsigned*)(g_buf + K1_G_LUT + 4 * e) = pack_f16(r[0], r[1]);
    K0_END();
    return;
  }
  if (bx >= 145 + 24) {
    __shared__ __attribute__((aligned(16))) u16 tw[32][200];
    const int kappa = (bx - 169) >> 1, nh = (bx - 169) & 1;
    u32x4 sm, v[3];
    if (sw) sm = w_load(w2);
#pragma unroll
    for (int i = 0; i < 3; ++i) { const int e = threadIdx.x + 256 * i; v[i] = *(const u32x4*)(w2 + (size_t)(32 * kappa + e / 24) * CH + 192 * nh + (e % 24) * 8); }
    if (sw) ss2 = w2_scale(w_ms(sm));
#pragma unroll
    for (int i = 0; i < 3; ++i) { const int e = threadIdx.x + 256 * i; *(u32x4*)&tw[e / 24][(e % 24) * 8] = v[i]; }
    __syncthreads();
    const int lane = threadIdx.x & 63, g = lane >> 4;
    const float s2 = 256.f * ss2;
    bool bad = false;
#pragma unroll
    for (int i = 0; i < 3; ++i) {
      const int fi = (threadIdx.x >> 6) + 4 * i, n = 64 * (fi >> 2) + 4 * (lane & 15) + (fi & 3);
      float wv[8];
      unsigned bits = 0;
#pragma unroll
      for (int j = 0; j < 8; ++j) {
        wv[j] = ld_bf16(&tw[16 * ((j >> 2) ^ (g & K2_TS)) + 4 * g + (j & 3)][n]);
        bits |= wv[j] != 0.f ? 256u << j : 0u;
        wv[j] *= s2;
        bits |= fabsf(wv[j]) >= 48 * 256 * U24 ? 1u << j : 0u;
        bad |= !(fabsf(wv[j]) <= 500.f);
      }
      const unsigned o = (((kappa & ~1) + fi / 6) * 24 + nh * 12 + (fi % 6) * 2 + (kappa & 1)) * 1024 + lane * 16;
      st16_wt(g_w2r + o, (u32x4){pack_f16(wv[0], wv[1]), pack_f16(wv[2], wv[3]), pack_f16(wv[4], wv[5]), pack_f16(wv[6], wv[7])});
      constexpr float q = 1.0f / 256;
      if (kappa < 8) st16_wt(g_w2q + o, (u32x4){pack_f16(q * wv[0], q * wv[1]), pack_f16(q * wv[2], q * wv[3]), pack_f16(q * wv[4], q * wv[5]), pack_f16(q * wv[6], q * wv[7])});
      static_for<4>([&](auto ic) { bits |= __builtin_amdgcn_update_dpp(0u, bits, 0x120 + (1 << decltype(ic)::value), 0xf, 0xf, false); });
      bad |= ((bits >> 8) & ~bits & 255u) != 0;
    }
    const bool any = __ballot(bad) != 0;
    if (lane == 0) ((char*)(g_buf + G_FLAG))[4 * (bx - 169) + (threadIdx.x >> 6)] = any;
    K0_END();
    return;
  }
  if (bx >= 145) {
    __shared__ __attribute__((aligned(16))) float red[16][64];
    const int c0 = 64 * ((bx - 145) >> 2), rg = (bx - 145) & 3, c8 = (threadIdx.x & 7) * 8;
    float a[8];
#pragma unroll
    for (int i = 0; i < 8; ++i) a[i] = 0.f;
#pragma unroll
    for (int k = 0; k < 12; ++k) {
      const int ch = 384 * rg + 32 * k + (threadIdx.x >> 3);
      const float b = ld_bf16(grn_b + ch);
      const u32x4 v = *(const u32x4*)(w2 + (size_t)ch * CH + c0 + c8);
#pragma unroll
      for (int i = 0; i < 4; ++i) { a[2 * i] = __builtin_fmaf(b, bf_lo(v[i]), a[2 * i]); a[2 * i + 1] = __builtin_fmaf(b, bf_hi(v[i]), a[2 * i + 1]); }
    }
#pragma unroll
    for (int i = 0; i < 8; ++i) a[i] = dpp_add<0x118, 0xf>(a[i]);
    if (threadIdx.x & 8) {
      *(f32x4*)&red[threadIdx.x >> 4][c8] = (f32x4){a[0], a[1], a[2], a[3]};
      *(f32x4*)&red[threadIdx.x >> 4][c8 + 4] = (f32x4){a[4], a[5], a[6], a[7]};
    }
    __syncthreads();
    if (threadIdx.x < 64) {
      const int n = c0 + threadIdx.x, m = n % 192;
      float s = 0.f;
#pragma unroll
      for (int i = 0; i < 16; ++i) s += red[i][threadIdx.x];
      g_bias[(((n / 192) * 12 + 4 * (m >> 6) + (m & 3)) * 16 + ((m & 63) >> 2)) * 4 + rg] = s;
    }
    K0_END();
    return;
  }
  const unsigned bid = bx;
  if (bid == 0) {
    if (sw) ssc = scales(w1, (const u16*)x, dw);
    if ((threadIdx.x >> 6) == 2) ss2 = w2_scale(w_ms(w_load(w2)));
#pragma unroll
    for (int i = 0; i < 4; ++i) if (threadIdx.x + 256 * i < 896) *(u32x4*)(g_buf + K1_G_CNT + (threadIdx.x + 256 * i) * 16) = (u32x4){0u, 0u, 0u, 0u};
    *(unsigned*)(g_buf + G_PUB + threadIdx.x * 256) = 0u;
    const int e = threadIdx.x, e2 = e < 192 ? 32 * (e >> 4) + 8 * ((e & 15) >> 2) : 8 * min(e - 192, 47);
    const u32x4 d = *(const u32x4*)(dw + e2);
    __syncthreads();
    const Sc sc = ssc;
    const float s2 = ss2;
    u32x4 v;
#pragma unroll
    for (int k = 0; k < 4; ++k) v[k] = pack_bf16(sc.zs * bf_lo(d[k]), sc.zs * bf_hi(d[k]));
    if (e < 192) {
#pragma unroll
      for (int k = 0; k < 4; ++k) v[k] &= (k & 1) != ((e >> 1) & 1) ? 0u : e & 1 ? 0xffff0000u : 0xffffu;
    }
    if (e >= 240) v = (u32x4){__float_as_uint(EPS * sc.zs * sc.zs), __float_as_uint(sc.vhi), __float_as_uint(sc.g), __float_as_uint(sc.vlo)};
    *(u32x4*)(g_buf + K1_G_TAB + (e < 192 ? e : e + 192) * 16) = v;
    auto sane = [](float v) { return v >= 0x1p-45f && v <= 0x1p20f; };
    if (threadIdx.x < 2) ((unsigned*)(g_buf + G_FLAG))[96 + threadIdx.x] = !(sane(sc.w1) && sane(sc.zs) && sane(sc.g) && sane(sc.h) && sane(s2));
    if (threadIdx.x == 0) *(f32x4*)g_sc = (f32x4){sc.h * s2, 1.0f / (sc.h * s2), 0x1p-28f / (sc.h * sc.h), 1.0f / sc.h};
    K0_END();
    return;
  }
  __shared__ __attribute__((aligned(16))) u16 tl[32][136];
  const int e = (bid - 1) * 256 + threadIdx.x, ks = (bid - 1) % 12, cb = (bid - 1) / 12;
  u32x4 sm, v[2];
  if (sw) sm = w_load(w1);
#pragma unroll
  for (int i = 0; i < 2; ++i) v[i] = *(const u32x4*)(w1 + (size_t)(32 * ks + (threadIdx.x >> 4) + 16 * i) * HID + 128 * cb + (threadIdx.x & 15) * 8);
  if (sw) ss2 = w1_scale(w_ms(sm));
#pragma unroll
  for (int i = 0; i < 2; ++i) *(u32x4*)&tl[(threadIdx.x >> 4) + 16 * i][(threadIdx.x & 15) * 8] = v[i];
  if (e < (int)(K1_NREP + 1) * 768) *(u32x4*)(g_buf + K1_G_ACC + e * 16) = (u32x4){0u, 0u, 0u, 0u};
  __syncthreads();
  const float s1 = ss2;
#pragma unroll
  for (int i = 0; i < 2; ++i) {
    const int lane = threadIdx.x & 63, f = (threadIdx.x >> 6) + 4 * i, n = 16 * f + (lane & 15), k0 = 8 * (lane >> 4);
    u32x4 P;
#pragma unroll
    for (int jj = 0; jj < 4; ++jj) P[jj] = pack_f16(s1 * ld_bf16(&tl[k0 + 2 * jj][n]), s1 * ld_bf16(&tl[k0 + 2 * jj + 1][n]));
    const int fs = (f & 1) * 12 + ks;
    st16_wt(g_buf + (4 * cb + (f >> 1)) * K1_GST + (fs + (fs >= 5)) * 1024 + lane * 16, P);
  }
  K0_END();
}

constexpr unsigned G_ACC2 = K1_G_ACC + K1_NREP * 12288;

#define VA_CLOB \
  "v0", "v1", "v2", "v3", "v4", "v5", "v6", "v7", "v8", "v9", "v10", "v11", "v12", "v13", "v14", "v15", "v16", "v17", "v18", "v19", "v20", "v21", "v22", "v23", "v24", "v25", "v26", "v27", "v28", "v29", "v30", "v31", \
  "v32", "v33", "v34", "v35", "v36", "v37", "v38", "v39", "v40", "v41", "v42", "v43", "v44", "v45", "v46", "v47", "v48", "v49", "v50", "v51", "v52", "v53", "v54", "v55", "v56", "v57", "v58", "v59", "v60", "v61", "v62", "v63", \
  "v64", "v65", "v66", "v67", "v68", "v69", "v70", "v71", "v72", "v73", "v74", "v75", "v76", "v77", "v78", "v79", "v80", "v81", "v82", "v83", "v84", "v85", "v86", "v87", "v88", "v89", "v90", "v91", "v92", "v93", "v94", "v95", \
  "v96", "v97", "v98", "v99", "v100", "v101", "v102", "v103", "v104", "v105", "v106", "v107", "v108", "v109", "v110", "v111", "v112", "v113", "v114", "v115", "v116", "v117", "v118", "v119", "v120", "v121", "v122", "v123", "v124", "v125", "v126", "v127", \
  "v128", "v129", "v130", "v131", "v132", "v133", "v134", "v135", "v136", "v137", "v138", "v139", "v140", "v141", "v142", "v143", "v144", "v145", "v146", "v147", "v148", "v149", "v150", "v151", "v152", "v153", "v154", "v155", "v156", "v157", "v158", "v159", \
  "v160", "v161", "v162", "v163", "v164", "v165", "v166", "v167", "v168", "v169", "v170", "v171", "v172", "v173", "v174", "v175", "v176", "v177", "v178", "v179", "v180", "v181", "v182", "v183", "v184", "v185", "v186", "v187", "v188", "v189", "v190", "v191", \
  "v192", "v193", "v194", "v195", "v196", "v197", "v198", "v199", "v200", "v201", "v202", "v203", "v204", "v205", "v206", "v207", "v208", "v209", "v210", "v211", "v212", "v213", "v214", "v215", "v216", "v217", "v218", "v219", "v220", "v221", "v222", "v223", \
  "v224", "v225", "v226", "v227", "v228", "v229", "v230", "v231", "v232", "v233", "v234", "v235", "v236", "v237", "v238", "v239", "v240", "v241", "v242", "v243", "v244", "v245", "v246", "v247", "v248", "v249", "v250", "v251", "v252", "v253", "v254", "v255", \
  "a0", "a1", "a2", "a3", "a4", "a5", "a6", "a7", "a8", "a9", "a10", "a11", "a12", "a13", "a14", "a15", "a16", "a17", "a18", "a19", "a20", "a21", "a22", "a23", "a24", "a25", "a26", "a27", "a28", "a29", "a30", "a31", \
  "a32", "a33", "a34", "a35", "a36", "a37", "a38", "a39", "a40", "a41", "a42", "a43", "a44", "a45", "a46", "a47", "a48", "a49", "a50", "a51", "a52", "a53", "a54", "a55", "a56", "a57", "a58", "a59", "a60", "a61", "a62", "a63", \
  "a64", "a65", "a66", "a67", "a68", "a69", "a70", "a71", "a72", "a73", "a74", "a75", "a76", "a77", "a78", "a79", "a80", "a81", "a82", "a83", "a84", "a85", "a86", "a87", "a88", "a89", "a90", "a91", "a92", "a93", "a94", "a95", \
  "a96", "a97", "a98", "a99", "a100", "a101", "a102", "a103", "a104", "a105", "a106", "a107", "a108", "a109", "a110", "a111", "a112", "a113", "a114", "a115", "a116", "a117", "a118", "a119", "a120", "a121", "a122", "a123", "a124", "a125", "a126", "a127", \
  "a128", "a129", "a130", "a131", "a132", "a133", "a134", "a135", "a136", "a137", "a138", "a139", "a140", "a141", "a142", "a143", "a144", "a145", "a146", "a147", "a148", "a149", "a150", "a151", "a152", "a153", "a154", "a155", "a156", "a157", "a158", "a159", \
  "a160", "a161", "a162", "a163", "a164", "a165", "a166", "a167", "a168", "a169", "a170", "a171", "a172", "a173", "a174", "a175", "a176", "a177", "a178", "a179", "a180", "a181", "a182", "a183", "a184", "a185", "a186", "a187", "a188", "a189", "a190", "a191", \
  "a192", "a193", "a194", "a195", "a196", "a197", "a198", "a199", "a200", "a201", "a202", "a203", "a204", "a205", "a206", "a207", "a208", "a209", "a210", "a211", "a212", "a213", "a214", "a215", "a216", "a217", "a218", "a219", "a220", "a221", "a222", "a223", \
  "a224", "a225", "a226", "a227", "a228", "a229", "a230", "a231", "a232", "a233", "a234", "a235", "a236", "a237", "a238", "a239", "a240", "a241", "a242", "a243", "a244", "a245", "a246", "a247", "a248", "a249", "a250", "a251", "a252", "a253", "a254", "a255"
#define K1_CLOB "memory", "vcc", "scc", "s36", "s37", "s38", "s39", "s40", "s41", "s42", "s43", "s44", "s45", "s46", "s47", "s48", "s49", "s50", "s51", "s52", "s53", "s54", "s55", "s56", "s57", "s58", "s59", "s60", "s61", "s62", "s63", "s64", "s65", "s66", "s67", "s68", "s69", "s70", "s71", "s72", "s73", "s74", "s75", "s76", "s77", "s78", "s79", "s80", "s81", "s82", "s83", "s84", "s85", "s86", "s87", "s88", "s89", "s90", "s91", "s92", "s93", "s94", "s95", "s96", "s97", VA_CLOB
DI void k2_params(const u16* x, const char* hws, const u16* out, const u16* grn_g, unsigned tid) {
  const unsigned e = (tid >> 2) * 32 + blockIdx.x * 4 + (tid & 3);
  const unsigned b = e >> 2, w = e & 3, pg = w & 1, nh = w >> 1, q = (b >> 3) * 4 + (b & 3), rs = 128 + (q >> 1), hf = q & 1, side = wg_side(b);
  auto fmask = [&](unsigned region) {
    const unsigned o = ((unsigned)(size_t)hws & 0x1fffffu) + region * H_RS + nh * H_TS;
    const unsigned* p = (const unsigned*)(g_buf + K1_G_LOC + 64) + (o >> 21);
    const unsigned r = 0x200000u + 2 * H_TS - 1 - (o & 0x1fffffu), d1 = ((r >> 15) * 171) >> 9, d2 = (((r + 0x200000u) >> 15) * 171) >> 9;
    const unsigned a1 = ~0u << d1, a2 = d2 < 32 ? ~0u << d2 : 0u;
    return ((__popc(p[0]) > 2 ? ~a1 : 0u) | (__popc(p[1]) > 2 ? a1 & ~a2 : 0u) | (__popc(p[2]) > 2 ? a2 : 0u)) ^ (0u - side);
  };
  const unsigned f0 = fmask(q), f1 = fmask(rs);
  const unsigned fx = ((side ^ x_flip(x, q)) ? ~K2_SHT : 0u) | ((side ^ x_flip(x, rs)) ? K2_SHT : 0u);
  const uint64_t h0 = (uint64_t)hws + q * H_RS + nh * H_TS + pg * 4096, h1 = (uint64_t)hws + rs * H_RS + nh * H_TS + hf * 4096 + pg * 2048;
  const unsigned p0 = (12 * q + 2 * pg) * 49152 + nh * 384, p1 = (12 * rs + 2 * hf + pg) * 49152 + nh * 384, e0 = 288 * b + 72 * w;
  typedef uint64_t u64x2 __attribute__((ext_vector_type(2)));
  u64x2* P = (u64x2*)(g_prm + 64 * e);
  P[0] = (u64x2){f0 | (uint64_t)f1 << 32, fx | (uint64_t)e0 << 32};
  const u32x4 sc = *(const u32x4*)g_sc;
  P[1] = (u64x2){(((b >> 3) * 4 + w) & 7) * 1024 | (uint64_t)sc[0] << 32, sc[1] | (uint64_t)sc[2] << 32};
  P[2] = (u64x2){h0, h0 + 8192};
  P[3] = (u64x2){h1, h1 + 8192};
  P[4] = (u64x2){0, 0};
  P[5] = (u64x2){(uint64_t)x + p0, (uint64_t)x + p1};
  P[6] = (u64x2){(uint64_t)out + p0, (uint64_t)out + p1};
  P[7] = (u64x2){0x7fffffffffffffull, (uint64_t)g_buf + G_FLAG};
  P[8] = (u64x2){(uint64_t)g_w2f + w * 6144, (uint64_t)g_w2q + w * 6144};
  P[9] = (u64x2){(uint64_t)g_w2r + e0 * 16, (uint64_t)g_w2f + e0 * 16};
  P[10] = (u64x2){(uint64_t)grn_g + w * 256, (uint64_t)g_bias + w * 1536};
  P[11] = (u64x2){(uint64_t)g_buf + G_PUB + b * 256, (uint64_t)g_buf + G_PUB + w * 16384};
  P[12] = (u64x2){(uint64_t)g_w2f + w * 6144, 0};
  P[13] = (u64x2){h0, h0 + 8192};
  P[14] = (u64x2){(uint64_t)x + p0, (uint64_t)out + p0};
  P[15] = (u64x2){f0, fx};
}
__global__ __launch_bounds__(256, 1) void k1_fc1(const u16* __restrict__ x, char* __restrict__ hws, const u16* __restrict__ out, const u16* __restrict__ grn_g) {
  extern __shared__ __attribute__((aligned(16))) char smem[];
  const unsigned w = __builtin_amdgcn_readfirstlane(threadIdx.x >> 6);
  if (blockIdx.x < 8) {
    if (w < 2) k2_params(x, hws, out, grn_g, w * 64 + __builtin_amdgcn_mbcnt_hi(~0u, __builtin_amdgcn_mbcnt_lo(~0u, 0u)));
    return;
  }
  const unsigned lds0 = lds_addr(smem);
  const unsigned b = blockIdx.x - 8;
  const unsigned dir = (b >> 3) & 1;
  TS(0); MK_START(1);
  const uint64_t xp = (uint64_t)x, hp = (uint64_t)hws, gp = (uint64_t)g_buf, wp = gp + w * K1_WCH;
  const unsigned d = dir ? ~0u : 1u, wrap = dir ? ~0u : 48u, rst = dir ? 47u : 0u, pos0 = dir ? K1_SMAX - 1 : 0u;
  const unsigned xo0 = (12 * ((b >> 3) * 4 + (b & 3)) + w) * 49152, xs1 = (1536 + w + (dir ? 8 : 0)) * 49152, sb1 = 2048 * w + (dir ? 33792 : 0);
  const unsigned r0o = ((b >> 3) * 4 + (b & 3)) * H_RS, tsel = 4 * (b & 3), s2 = (b >> 2) & 1;
  const uint64_t ap = gp + K1_G_ACC + (b % K1_NREP) * 12288;
  const unsigned ca0 = w == 0 ? dir * 64 : K1_G_DUM - K1_G_CNT + (4 * b + w) * 64;
  const unsigned pm = lds0 + K1_PF_OFF, pf = ((b >> 3) * 4 + w) * 1024;
  unsigned nb;
  asm volatile(
R"(.macro M t,f,w
.if \f < 30
v_mfma_f32_16x16x32_f16 v[56+4*\t:56+4*\t+3], v[128+4*\f:128+4*\f+3], a[216+4*\w:216+4*\w+3], v[56+4*\t:56+4*\t+3]
.else
v_mfma_f32_16x16x32_f16 v[56+4*\t:56+4*\t+3], a[4*\f-4*30:4*\f-4*30+3], a[216+4*\w:216+4*\w+3], v[56+4*\t:56+4*\t+3]
.endif
.endm
.macro Z t,f,w
.if \f < 30
v_mfma_f32_16x16x32_f16 v[56+4*\t:56+4*\t+3], v[128+4*\f:128+4*\f+3], a[216+4*\w:216+4*\w+3], v[252:255]
.else
v_mfma_f32_16x16x32_f16 v[56+4*\t:56+4*\t+3], a[4*\f-4*30:4*\f-4*30+3], a[216+4*\w:216+4*\w+3], v[252:255]
.endif
.endm
.macro LZ a,f
.if \f < 30
v_mfma_f32_16x16x32_bf16 v[36:36+3], v[\a:\a+3], v[128+4*\f:128+4*\f+3], 0
.else
v_mfma_f32_16x16x32_bf16 v[36:36+3], v[\a:\a+3], a[4*\f-4*30:4*\f-4*30+3], 0
.endif
.endm
.macro LS a,f
.if \f < 30
v_mfma_f32_16x16x32_bf16 v[36:36+3], v[\a:\a+3], v[128+4*\f:128+4*\f+3], v[36:36+3]
.else
v_mfma_f32_16x16x32_bf16 v[36:36+3], v[\a:\a+3], a[4*\f-4*30:4*\f-4*30+3], v[36:36+3]
.endif
.endm
.macro LD d,a,f
.if \f < 30
v_mfma_f32_16x16x32_bf16 v[\d:\d+3], a[\a:\a+3], v[128+4*\f:128+4*\f+3], v[36:36+3]
.else
v_mfma_f32_16x16x32_bf16 v[\d:\d+3], a[\a:\a+3], a[4*\f-4*30:4*\f-4*30+3], v[36:36+3]
.endif
.endm
.macro GZ f
.if \f < 30
v_mfma_f32_16x16x32_f16 v[52:55], v[128+4*\f:128+4*\f+3], v[128+4*\f:128+4*\f+3], 0
.else
v_mfma_f32_16x16x32_f16 v[52:55], a[4*\f-4*30:4*\f-4*30+3], a[4*\f-4*30:4*\f-4*30+3], 0
.endif
.endm
.macro GS f
.if \f < 30
v_mfma_f32_16x16x32_f16 v[52:55], v[128+4*\f:128+4*\f+3], v[128+4*\f:128+4*\f+3], v[52:55]
.else
v_mfma_f32_16x16x32_f16 v[52:55], a[4*\f-4*30:4*\f-4*30+3], a[4*\f-4*30:4*\f-4*30+3], v[52:55]
.endif
.endm
.macro CL p,z,s
v_fma_mixlo_f16 v[\p], v[\z], v[\s], 0 op_sel_hi:[0,1,0]
.endm
.macro CH p,z,s
v_fma_mixhi_f16 v[\p], v[\z], v[\s], 0 op_sel:[0,1,0] op_sel_hi:[0,1,0]
.endm
.macro XL f,o
.if \f < 30
global_load_dwordx4 v[128+4*\f:128+4*\f+3], v3, s[46:47] offset:\o
.else
global_load_dwordx4 a[4*\f-4*30:4*\f-4*30+3], v3, s[46:47] offset:\o
.endif
.endm
.macro XN f,o
.if \f < 30
global_load_dwordx4 v[128+4*\f:128+4*\f+3], v3, s[46:47] offset:\o nt
.else
global_load_dwordx4 a[4*\f-4*30:4*\f-4*30+3], v3, s[46:47] offset:\o nt
.endif
.endm
.macro F t,z
v_fma_f32 v[\t], -v[\z], v[\z], s36
.endm
.macro U t,z
v_mul_f32 v[\t], v[\z], v[\t]
.endm
.macro E t
v_exp_f32 v[\t], v[\t]
.endm
.macro A t
v_add_f32 v[\t], 1.0, v[\t]
.endm
.macro R t
v_rcp_f32 v[\t], v[\t]
.endm
.macro C d,a,b
v_cvt_pk_bf16_f32 v[\d], v[\a], v[\b]
.endm
.macro S0 q,t
v_mul_f32 v[\q], v[\t], v[\t]
.endm
.macro S q,t
v_fmac_f32 v[\q], v[\t], v[\t]
.endm
.macro HC d,z
v_cvt_pk_f16_f32 v[\d], v[\z], v[\z+1]
.endm
.macro HF t,p
v_pk_fma_f16 v[\t], v[\p], v[\p], s36 neg_lo:[1,0,0] neg_hi:[1,0,0]
.endm
.macro HM t,p
v_pk_mul_f16 v[\t], v[\p], v[\t]
.endm
.macro HE d,s
v_exp_f16 v[\d], v[\s]
.endm
.macro HG d,s
v_exp_f16_sdwa v[\d], v[\s] dst_sel:WORD_1 dst_unused:UNUSED_PRESERVE src0_sel:WORD_1
.endm
.macro HA t
v_pk_add_f16 v[\t], v[\t], 1.0 op_sel_hi:[1,0]
.endm
.macro HR d,s
v_rcp_f16 v[\d], v[\s]
.endm
.macro HQ d,s
v_rcp_f16_sdwa v[\d], v[\s] dst_sel:WORD_1 dst_unused:UNUSED_PRESERVE src0_sel:WORD_1
.endm
.macro HP d,s
v_rcp_f16_sdwa v[\d], v[\s] dst_sel:DWORD dst_unused:UNUSED_PAD src0_sel:WORD_1
.endm
.macro HY t,z
v_fma_mix_f32 v[\t], v[\z], v[\t], 0 op_sel_hi:[0,1,0]
.endm
.macro HK p
v_and_b32 v[\p], s56, v[\p]
.endm
.macro LQ p
v_lshrrev_b32 v[\p], 2, v[\p]
.endm
.macro LL t,z
v_lshlrev_b32_sdwa v[\t], 1, v[\z] dst_sel:DWORD dst_unused:UNUSED_PAD src0_sel:DWORD src1_sel:WORD_1
.endm
.macro LH t,p
v_and_b32_sdwa v[\t], s56, v[\p] dst_sel:DWORD dst_unused:UNUSED_PAD src0_sel:DWORD src1_sel:WORD_1
.endm
.macro LA t
ds_read_u16 v[\t], v[\t] offset:5376
.endm
.macro LC_ t
ds_read_u16_d16_hi v[\t], v[\t] offset:5376
.endm
.macro LO p,a,b
v_bfi_b32 v[\p], s36, v[\a], v[\b]
.endm
.macro HZ q,p
v_pk_mul_f16 v[\q], v[\p], v[\p]
.endm
.macro HS q,p
v_pk_fma_f16 v[\q], v[\p], v[\p], v[\q]
.endm
.macro HT q,p
v_fma_mix_f32 v[\q], v[\p], v[\p], v[\q] op_sel:[1,1,0] op_sel_hi:[1,1,0]
.endm
.macro HX q
v_fma_mix_f32 v[\q], v[\q], 1.0, v[\q] op_sel:[0,0,1] op_sel_hi:[1,0,1]
.endm
.macro UX t,z
v_mul_f32_e64 v[\t], v[\z], v[\t]
.endm
.macro EX t
v_exp_f32_e64 v[\t], v[\t]
.endm
.macro AX t
v_add_f32_e64 v[\t], 1.0, v[\t]
.endm
.macro RX t
v_rcp_f32_e64 v[\t], v[\t]
.endm
.macro TX q,t
v_mul_f32_e64 v[\q], v[\t], v[\t]
.endm
.macro SX q,t
v_fmac_f32_e64 v[\q], v[\t], v[\t]
.endm
.macro AW a,s
v_accvgpr_write_b32 a[\a], v[\s]
.endm
.macro RA d,v,o
ds_read_b128 a[\d:\d+3], v[\v] offset:\o
.endm
.macro RV d,v,o
ds_read_b128 v[\d:\d+3], v[\v] offset:\o
.endm
.macro W2 a,b,o
ds_write2st64_b32 v4, v[\a], v[\b] offset0:\o offset1:\o+4
.endm
.macro W1 a,o
ds_write_b32 v4, v[\a] offset:\o
.endm
.macro DW o
global_load_lds_dwordx4 v0, s[40:41] offset:\o
.endm
.macro DF
global_load_lds_dwordx4 v0, s[70:71]
.endm
.macro DX
global_load_lds_dword v3, s[46:47]
.endm
.macro ST d
global_store_dwordx4 v2, v[\d:\d+3], s[42:43] nt sc1
.endm
.macro SU d
global_store_dwordx4 v2, v[\d:\d+3], s[42:43] sc1
.endm
.macro SP
global_atomic_add_f64 v6, v[14:15], s[48:49]
.endm
.macro AT
global_atomic_add v11, v9, v10, s[68:69] sc0
.endm
.macro PFA
s_getpc_b64 s[70:71]
s_add_u32 s70, s70, 7f-.
s_addc_u32 s71, s71, 0
s_andn2_b32 s70, s70, 0x7f
s_min_u32 s53, %[pf], 8f-7f-1152
s_andn2_b32 s53, s53, 0x7f
s_add_u32 s70, s70, s53
s_addc_u32 s71, s71, 0
.endm
.macro PFN
s_getpc_b64 s[70:71]
s_add_u32 s70, s70, k2_code@rel32@lo-1020
s_addc_u32 s71, s71, k2_code@rel32@hi-1012
s_add_u32 s70, s70, %[pf]
s_addc_u32 s71, s71, 0
s_andn2_b32 s70, s70, 0x7f
.endm
.macro PFW
s_getpc_b64 s[70:71]
s_add_u32 s70, s70, g_w2q@rel32@lo+4
s_addc_u32 s71, s71, g_w2q@rel32@hi+12
s_add_u32 s70, s70, %[pf]
s_addc_u32 s71, s71, 0
.endm
.macro PF0
s_getpc_b64 s[70:71]
s_andn2_b32 s70, s70, 0x7f
s_min_u32 s53, %[pf], 7f-.-1152
s_andn2_b32 s53, s53, 0x7f
s_add_u32 s70, s70, s53
s_addc_u32 s71, s71, 0
.endm
.macro WL n
s_waitcnt lgkmcnt(\n)
.endm
.macro WV n
s_waitcnt vmcnt(\n)
.endm
.macro LU t,p
v_pk_mul_f16 v[\t], v[\p], v34
.endm
.macro CF d,a,b
v_cvt_pk_f16_f32 v[\d], v[\a], v[\b]
.endm
.macro XS d
v_pk_fma_f16 v[\d], v[\d], v13, s56
.endm
.macro AR d,a
v_accvgpr_read_b32 v[\d], a[\a]
.endm
.macro DQ o
global_load_lds_dword v23, s[40:41] offset:\o
.endm
.macro GE
v_cmp_eq_u32 vcc, 0, v35
s_nop 4
s_cbranch_vccz 9f
v_mov_b32 v35, 14720
s_mov_b64 exec, 1
global_store_dword v35, v10, s[68:69] sc1
s_mov_b64 exec, -1
9:
.endm
.macro BW f
.if \f < 30
ds_write_b128 v12, v[128+4*\f:128+4*\f+3]
.else
ds_write_b128 v12, a[4*\f-120:4*\f-120+3]
.endif
.endm
.macro BR f
.if \f < 30
ds_read_b128 v[128+4*\f:128+4*\f+3], v20
.else
ds_read_b128 a[4*\f-120:4*\f-120+3], v20
.endif
.endm
.macro LK d,a,f,h
.if \f < 30
v_mfma_f32_4x4x4_16b_bf16 v[\d:\d+3], a[\a:\a+1], v[128+4*\f+\h:128+4*\f+\h+1], v[36:39]
.else
v_mfma_f32_4x4x4_16b_bf16 v[\d:\d+3], a[\a:\a+1], a[4*\f-120+\h:4*\f-120+\h+1], v[36:39]
.endif
.endm
k1_code:
v_mbcnt_lo_u32_b32 v16, -1, 0
v_mbcnt_hi_u32_b32 v16, -1, v16
v_lshlrev_b32 v0, 4, v16
v_and_b32 v17, 15, v16
v_lshrrev_b32 v18, 4, v16
s_add_u32 s68, %[glo], 1239040
s_addc_u32 s69, %[ghi], 0
s_mov_b32 s53, %[r0o]
s_add_u32 s82, %[hlo], s53
s_addc_u32 s83, %[hhi], 0
s_add_u32 s84, s82, 8192
s_addc_u32 s85, s83, 0
s_and_b32 s90, %[hlo], 0x1fffff
s_add_u32 s90, s90, s53
s_mov_b32 s91, 3072
v_mov_b32 v11, s90
v_mad_u32_u24 v11, v0, s91, v11
v_lshrrev_b32 v11, 21, v11
v_lshl_add_u32 v11, v11, 2, 64
v_writelane_b32 v11, %[tsel], 63
s_sub_u32 s53, s82, %[hlo]
s_lshr_b32 s53, s53, 2
s_and_b32 s90, %[xlo], 0x1fffff
s_add_u32 s53, s53, s90
s_add_u32 s53, s53, 0x20048000
s_lshr_b32 s53, s53, 21
s_lshl_b32 s90, s53, 2
v_writelane_b32 v11, s90, 48
global_load_dword v11, v11, s[68:69] offset:-2048
v_lshlrev_b32 v19, 1, v18
s_movk_i32 s53, 0x78
v_bfe_u32 v21, v16, 2, 2
v_lshrrev_b32 v19, v19, s53
v_mul_u32_u24 v21, 0x300, v21
v_xor_b32 v19, v19, v16
v_and_b32 v19, 3, v19
v_lshl_add_u32 v21, v19, 4, v21
s_mov_b32 s97, 3072
s_mov_b32 s50, %[pos0]
s_mov_b32 s51, %[pos0]
s_mov_b32 s52, %[pos0]
s_add_u32 s74, %[lds], 0
s_add_u32 s75, s74, 163840
s_mov_b32 s37, s74
s_mul_i32 s53, %[w], 6400
s_add_u32 s76, s74, s53
s_add_u32 s77, s76, 163840
s_mov_b32 s38, s76
s_lshl_b32 s60, %[w], 11
s_add_u32 s61, s60, 1024
s_add_u32 s62, s60, 16384
s_add_u32 s63, s60, 17408
s_add_u32 s64, s60, 32768
s_add_u32 s65, s60, 33792
v_lshlrev_b32 v23, 2, v16
s_lshl_b32 s53, %[w], 11
s_min_u32 s53, s53, 5120
s_add_u32 s72, s53, 1228800
s_add_u32 s40, %[glo], s72
s_addc_u32 s41, %[ghi], 0
s_add_u32 s53, s53, %[lds]
s_add_u32 m0, s53, 58368
s_nop 0
DW 0
DW 1024
s_nop 0 ;PS
s_mul_i32 s53, s50, 25600
s_add_u32 s40, %[wlo], s53
s_addc_u32 s41, %[whi], 0
s_mov_b32 m0, s38
s_nop 0
DW 0
DW 1024
DW 2048
s_add_u32 s40, s40, 3072
s_addc_u32 s41, s41, 0
s_add_u32 m0, s38, 3072
s_nop 0
DW 0
DW 1024
DW 2048
DQ 3072
s_add_u32 s50, s50, %[d]
s_cmp_eq_u32 s50, %[wrap]
s_cselect_b32 s50, %[rst], s50
s_add_u32 s38, s38, 32768
s_cmp_eq_u32 s38, s77
s_cselect_b32 s38, s76, s38
s_mul_i32 s53, s50, 25600
s_add_u32 s40, %[wlo], s53
s_addc_u32 s41, %[whi], 0
s_mov_b32 m0, s38
s_nop 0
DW 0
DW 1024
DW 2048
s_add_u32 s40, s40, 3072
s_addc_u32 s41, s41, 0
s_add_u32 m0, s38, 3072
s_nop 0
DW 0
DW 1024
DW 2048
DQ 3072
)"
R"(s_add_u32 s50, s50, %[d]
s_cmp_eq_u32 s50, %[wrap]
s_cselect_b32 s50, %[rst], s50
s_add_u32 s38, s38, 32768
s_cmp_eq_u32 s38, s77
s_cselect_b32 s38, s76, s38
s_and_b32 s53, %[w], 1
s_mul_i32 s53, s53, 3072
s_lshr_b32 s72, %[w], 1
s_lshl_b32 s40, s72, 16
s_add_u32 s40, s40, s53
s_add_u32 m0, s40, 25600
s_lshl_b32 s72, s72, 13
s_add_u32 s53, s53, s72
s_add_u32 s53, s53, 1382400
s_add_u32 s40, %[glo], s53
s_addc_u32 s41, %[ghi], 0
DW 0
DW 1024
DW 2048
DW 3072
v_add_u32 v1, s37, v0
v_lshl_add_u32 v19, v18, 2, v17
v_and_b32 v19, 15, v19
v_lshlrev_b32 v19, 4, v19
v_lshl_add_u32 v2, v18, 8, v19
s_lshl_b32 s53, %[w], 2
s_add_u32 s53, s53, %[lds]
s_add_u32 s53, s53, 123904
s_add_u32 s53, %[lds], 123904
v_bfe_u32 v4, v17, 1, 2
v_and_b32 v5, 8, v17
v_add3_u32 v4, v4, v5, %[w]
v_lshl_add_u32 v4, v18, 2, v4
v_and_b32 v4, 15, v4
v_lshl_add_u32 v4, v17, 4, v4
v_lshl_add_u32 v4, v4, 2, s53
s_lshl_b32 s53, %[w], 9
s_add_u32 s53, s53, %[lds]
s_add_u32 s53, s53, 123904
v_lshlrev_b32 v5, 3, v16
v_add_u32 v5, s53, v5
s_lshl_b32 s53, %[w], 6
v_and_b32 v6, 0x38, v16
v_add_u32 v6, s53, v6
s_add_u32 s53, %[lds], 58368
v_and_b32 v19, 3, v16
v_lshl_add_u32 v19, v18, 2, v19
v_lshl_add_u32 v7, v19, 4, s53
v_lshlrev_b32 v8, 4, v18
v_add_u32 v8, s53, v8
v_mov_b32 v10, 1
s_mov_b32 s36, 0xffff
s_mov_b32 s56, 0x5550555
s_mov_b32 s39, 32768
s_mov_b32 s54, 0x01010101
s_mov_b32 s55, 0x01010101
s_mov_b32 %[nb], 0
v_and_b32 v28, 12, v17
v_lshl_add_u32 v28, v28, 2, v17
v_lshlrev_b32 v28, 2, v28
v_mov_b32 v34, 0x74007400
v_mov_b32 v35, -1
v_and_b32 v22, 0xfffffcff, v1
s_mov_b32 s78, 0xaaaaaaaa
s_mov_b32 s79, 0xaaaaaaaa
s_mov_b32 s80, 0xcccccccc
s_mov_b32 s81, 0xcccccccc
s_and_b32 s53, %[w], 1
s_lshl_b32 s53, s53, 10
s_lshr_b32 s72, %[w], 1
s_mul_i32 s72, s72, 32768
s_add_u32 s72, s72, s53
s_add_u32 s72, s72, 128000
s_movk_i32 s53, 0x78
v_lshrrev_b32 v12, 5, v0
v_and_b32 v12, 6, v12
v_bfe_u32 v12, s53, v12, 2
v_lshrrev_b32 v20, 8, v0
v_xor_b32 v20, v12, v20
v_lshl_add_u32 v20, v20, 4, s72
v_and_b32 v12, 0xf0, v0
v_lshl_add_u32 v20, v12, 2, v20
v_add_u32 v12, s72, v0
WV 20
v_bcnt_u32_b32 v11, v11, 0
v_cmp_lt_u32 s[90:91], 2, v11
s_nop 0
s_lshr_b32 s96, s91, 31
s_xor_b32 s96, s96, %[s2]
s_sub_u32 s53, 0, s96
s_xor_b32 s90, s90, s53
s_xor_b32 s91, s91, s53
s_add_u32 s53, %[xo0], 0
s_add_u32 s46, %[xlo], s53
s_addc_u32 s47, %[xhi], 0
s_bitcmp1_b64 s[90:91], 48
s_cselect_b32 s58, 16, 0
s_lshr_b32 s58, 0xfe987610, s58
v_lshrrev_b32 v3, 6, v0
v_and_b32 v3, 12, v3
v_bfe_u32 v3, s58, v3, 4
v_mad_u32_u24 v3, v3, s97, v21
XL 12,0
XL 13,64
XL 14,128
XL 15,192
XL 16,256
XL 17,320
XL 18,384
XL 19,448
XL 20,512
XL 21,576
XL 22,640
XL 23,704
s_mul_i32 s53, s96, 8320
s_cmp_lt_u32 %[ca0], 128
s_cselect_b32 s53, s53, 0
s_add_u32 s73, s53, %[ca0]
v_mov_b32 v9, s73
WV 30
s_barrier
WV 11
BW 12
RV 40,8,6144
RV 44,8,6208
RV 48,8,6272
RV 248,8,6336
BR 12
WV 10
BW 13
s_nop 1
BR 13
WL 2
LZ 40,12
WV 9
BW 14
RV 40,8,6400
s_nop 0
BR 14
PF0
s_mov_b32 m0, %[pm]
s_nop 0
DF
PFA
WL 3
LS 44,13
WV 9
BW 15
RV 44,8,6464
s_nop 0
BR 15
WL 3
LS 48,14
WV 8
BW 16
RV 48,8,6528
s_nop 0
BR 16
s_add_u32 s53, %[xo0], 0
s_add_u32 s46, %[xlo], s53
s_addc_u32 s47, %[xhi], 0
s_bitcmp1_b64 s[90:91], 48
s_cselect_b32 s58, 16, 0
s_lshr_b32 s58, 0xda43cb52, s58
v_lshrrev_b32 v3, 6, v0
v_and_b32 v3, 12, v3
v_bfe_u32 v3, s58, v3, 4
v_mad_u32_u24 v3, v3, s97, v21
WL 3
LS 248,15
WV 7
BW 17
RV 248,8,6592
s_nop 0
BR 17
WL 3
LS 40,16
WV 6
BW 18
RV 40,8,6656
s_nop 0
BR 18
XL 24,0
WL 3
LS 44,17
WV 6
BW 19
RV 44,8,6720
s_nop 0
BR 19
WL 3
LS 48,18
WV 5
BW 20
RV 48,8,6784
s_nop 0
BR 20
XL 25,64
WL 3
LS 248,19
WV 5
BW 21
RV 248,8,6848
s_nop 0
BR 21
XL 26,128
WL 3
LS 40,20
WV 5
BW 22
RA 240,7,0
s_nop 0
BR 22
WL 3
LS 44,21
WV 4
BW 23
RA 244,7,256
s_nop 0
BR 23
XL 27,192
WL 3
LS 48,22
RA 248,7,512
WL 1
LS 248,23
RA 252,7,768
s_nop 6
v_mul_f32 v36, 0xbb2aaaab, v36
v_mul_f32 v37, 0xbb2aaaab, v37
v_mul_f32 v38, 0xbb2aaaab, v38
v_mul_f32 v39, 0xbb2aaaab, v39
XL 28,256
s_nop 0
LK 40,240,12,0
LK 44,242,12,2
RA 240,7,1024
LK 48,244,13,0
s_nop 1
CF 176,40,41
LK 248,246,13,2
WL 2
RA 244,7,1280
CF 177,42,43
CF 178,44,45
CF 179,46,47
XL 29,320
LK 40,248,14,0
CF 180,48,49
LK 44,250,14,2
RA 248,7,1536
CF 181,50,51
GZ 12
WL 3
XL 30,384
CF 182,248,249
CF 183,250,251
LK 48,252,15,0
CF 184,40,41
LK 248,254,15,2
RA 252,7,1792
CF 185,42,43
XL 31,448
GS 13
WL 3
CF 186,44,45
CF 187,46,47
LK 40,240,16,0
CF 188,48,49
LK 44,242,16,2
RA 240,7,2048
CF 189,50,51
XL 32,512
GS 14
WL 3
CF 190,248,249
CF 191,250,251
LK 48,244,17,0
CF 192,40,41
LK 248,246,17,2
RA 244,7,2304
CF 193,42,43
XL 33,576
GS 15
WL 3
CF 194,44,45
CF 195,46,47
LK 40,248,18,0
CF 196,48,49
LK 44,250,18,2
RA 248,7,2560
CF 197,50,51
XL 34,640
GS 16
WL 3
CF 198,248,249
CF 199,250,251
LK 48,252,19,0
CF 200,40,41
LK 248,254,19,2
WL 2
RA 252,7,2816
CF 201,42,43
XL 35,704
GS 17
s_mul_i32 s53, s50, 25600
s_add_u32 s40, %[wlo], s53
s_addc_u32 s41, %[whi], 0
s_mov_b32 m0, s38
CF 202,44,45
CF 203,46,47
LK 40,240,20,0
CF 204,48,49
LK 44,242,20,2
CF 205,50,51
GS 18
WL 2
DW 0
CF 206,248,249
CF 207,250,251
LK 48,244,21,0
CF 208,40,41
LK 248,246,21,2
CF 209,42,43
GS 19
WL 1
DW 1024
CF 210,44,45
CF 211,46,47
LK 40,248,22,0
CF 212,48,49
LK 44,250,22,2
CF 213,50,51
GS 20
WL 0
DW 2048
CF 214,248,249
CF 215,250,251
LK 48,252,23,0
CF 216,40,41
LK 248,254,23,2
CF 217,42,43
GS 21
s_add_u32 s40, s40, 3072
s_addc_u32 s41, s41, 0
s_add_u32 m0, s38, 3072
CF 218,44,45
CF 219,46,47
RV 36,8,6912
s_nop 0
GS 22
DW 0
CF 220,48,49
CF 221,50,51
CF 222,248,249
CF 223,250,251
s_nop 1
GS 23
s_nop 7
v_cndmask_b32 v52, v52, v53, s[78:79]
v_cndmask_b32 v54, v54, v55, s[78:79]
v_cndmask_b32 v52, v52, v54, s[80:81]
v_mul_f32 v52, 0x3b2aaaab, v52
WL 0
v_med3_f32 v53, v52, v39, v37
DW 1024
s_barrier
RA 216,1,0
RA 220,1,1024
RA 224,1,2048
RA 228,1,3072
RV 252,22,5120
v_cmp_eq_f32 vcc, v52, v53
C 52,52,52
v_lshlrev_b32 v52, 16, v52
v_add_f32 v52, v36, v52
C 52,52,52
v_lshlrev_b32 v52, 16, v52
v_sqrt_f32 v52, v52
s_nop 0
C 52,52,52
v_lshlrev_b32 v52, 16, v52
v_rcp_f32 v52, v52
DW 2048
v_mul_f32 v52, v38, v52
v_cndmask_b32 v52, 0, v52, vcc
CF 52,52,52
ds_bpermute_b32 v13, v28, v52
WL 0
XS 176
XS 177
XS 178
XS 179
XS 180
XS 181
DQ 3072
XS 182
XS 183
v_min_u32 v35, v35, v13
)"
R"(XS 184
XS 185
XS 186
XS 187
XS 188
XS 189
XS 190
s_add_u32 s50, s50, %[d]
s_cmp_eq_u32 s50, %[wrap]
s_cselect_b32 s50, %[rst], s50
XS 191
s_add_u32 s38, s38, 32768
s_cmp_eq_u32 s38, s77
s_cselect_b32 s38, s76, s38
Z 0,12,0
XS 192
XS 193
XS 194
XS 195
XS 196
XS 197
XS 198
XS 199
XS 200
XS 201
XS 202
XS 203
XS 204
M 0,13,1
s_mul_i32 s53, s50, 25600
RA 232,1,4096
XS 205
XS 206
XS 207
M 0,14,2
s_add_u32 s40, %[wlo], s53
RA 236,1,6144
s_addc_u32 s41, %[whi], 0
s_mov_b32 m0, s38
XS 208
XS 209
M 0,15,3
WL 1
RA 216,1,7168
DW 0
XS 210
XS 211
M 0,16,4
RA 220,1,8192
DW 1024
XS 212
XS 213
WL 2
M 0,17,5
RA 224,1,9216
DW 2048
XS 214
XS 215
XS 216
WL 2
M 0,18,0
s_add_u32 s40, s40, 3072
RA 228,1,10240
s_addc_u32 s41, s41, 0
s_add_u32 m0, s38, 3072
XS 217
XS 218
WL 2
M 0,19,1
WL 1
RA 232,1,11264
DW 0
XS 219
XS 220
M 0,20,2
WV 22
RV 252,22,5392
RA 236,1,12288
DW 1024
XS 221
BW 24
RV 40,8,6144
XS 222
RV 44,8,6208
XS 223
WL 6
M 0,21,3
WV 22
RA 216,1,13312
RV 48,8,6272
DW 2048
BR 24
BW 25
WL 9
M 0,22,4
WL 7
RA 220,1,14336
BR 25
DQ 3072
M 0,23,5
s_add_u32 s50, s50, %[d]
RA 224,1,15360
s_cmp_eq_u32 s50, %[wrap]
s_cselect_b32 s50, %[rst], s50
WL 4
LZ 40,24
WV 23
BW 26
RV 40,8,6336
s_nop 0
BR 26
WL 4
LS 44,25
WV 22
BW 27
Z 6,12,0
s_add_u32 s38, s38, 32768
RA 228,1,16384
RV 44,8,6400
s_cmp_eq_u32 s38, s77
s_cselect_b32 s38, s76, s38
BR 27
M 6,13,1
s_add_u32 s53, %[xo0], 196608
RA 232,1,17408
s_add_u32 s46, %[xlo], s53
s_addc_u32 s47, %[xhi], 0
s_bitcmp1_b64 s[90:91], 48
s_cselect_b32 s58, 16, 0
s_lshr_b32 s58, 0xfe987610, s58
v_lshrrev_b32 v3, 6, v0
v_and_b32 v3, 12, v3
v_bfe_u32 v3, s58, v3, 4
v_mad_u32_u24 v3, v3, s97, v21
WL 5
LS 48,26
WV 21
BW 28
RV 48,8,6464
M 6,14,2
WL 3
LL 16,56
LL 104,57
RA 236,1,18432
XL 36,0
BR 28
M 6,15,3
s_add_u32 s37, s37, 32768
LA 16
LC_ 104
s_cmp_eq_u32 s37, s75
s_cselect_b32 s37, s74, s37
XL 37,64
LS 40,27
WV 22
BW 29
RV 40,8,6528
s_nop 0
BR 29
WL 5
LS 44,28
WV 21
BW 30
M 6,16,4
RA 216,1,19456
RV 44,8,6592
RA 220,1,20480
XL 38,128
BR 30
M 6,17,5
WV 21
LL 17,58
LL 105,59
XL 39,192
WL 5
LS 48,29
s_mul_i32 s53, s51, 49152
BW 31
RV 48,8,6656
s_nop 0
BR 31
WL 6
M 6,18,0
s_bitcmp1_b64 s[90:91], s51
LA 17
LC_ 105
RA 224,1,21504
RA 228,1,22528
s_cselect_b64 s[44:45], s[84:85], s[82:83]
s_add_u32 s44, s44, s53
s_addc_u32 s45, s45, 0
s_add_u32 s51, s51, %[d]
s_cmp_eq_u32 s51, %[wrap]
s_cselect_b32 s51, %[rst], s51
XL 40,256
WL 8
M 6,19,1
WL 4
LO 104,16,104
LU 16,104
HZ 24,16
LS 40,30
WV 22
BW 32
RV 40,8,6720
s_nop 0
BR 32
LS 44,31
WV 21
BW 33
WL 5
M 6,20,2
WL 4
RA 232,1,23552
RV 44,8,6784
RA 236,1,24576
s_nop 1
v_add_u32 v1, s37, v0
XL 41,320
BR 33
M 6,21,3
WL 1
WV 13
s_barrier
RA 216,1,0
v_and_b32 v22, 0xfffffcff, v1
LS 48,32
BW 34
RV 48,8,6848
RA 240,7,0
M 6,22,4
LO 105,17,105
LU 17,105
HS 24,17
RA 220,1,1024
BR 34
M 6,23,5
RV 252,22,5120
HX 24
WL 0
Z 0,12,0
RA 224,1,2048
RA 228,1,3072
M 0,13,1
s_mul_i32 s53, s50, 25600
RA 232,1,4096
W1 24,0
LS 40,33
s_add_u32 s40, %[wlo], s53
BW 35
RA 244,7,256
s_nop 0
BR 35
LS 44,34
s_addc_u32 s41, %[whi], 0
RA 248,7,512
WL 1
LS 48,35
s_mov_b32 m0, s38
RA 252,7,768
s_nop 5
v_mul_f32 v36, 0xbb2aaaab, v36
v_mul_f32 v37, 0xbb2aaaab, v37
v_mul_f32 v38, 0xbb2aaaab, v38
v_mul_f32 v39, 0xbb2aaaab, v39
s_nop 1
LK 40,240,24,0
LK 44,242,24,2
RA 240,7,1024
LK 48,244,25,0
s_nop 1
CF 224,40,41
LK 248,246,25,2
RA 244,7,1280
CF 225,42,43
M 0,14,2
LL 18,80
LL 106,81
RA 236,1,6144
CF 226,44,45
CF 227,46,47
M 0,15,3
WL 4
LA 18
LC_ 106
DW 0
LK 40,248,26,0
CF 228,48,49
LK 44,250,26,2
RA 248,7,1536
CF 229,50,51
GZ 24
WL 6
CF 230,248,249
CF 231,250,251
LK 48,252,27,0
CF 232,40,41
LK 248,254,27,2
RA 252,7,1792
CF 233,42,43
GS 25
CF 234,44,45
CF 235,46,47
M 0,16,4
WL 4
RA 216,1,7168
RA 220,1,8192
DW 1024
M 0,17,5
LL 19,82
LL 107,83
DW 2048
LK 40,240,28,0
CF 236,48,49
LK 44,242,28,2
RA 240,7,2048
CF 237,50,51
GS 26
CF 238,248,249
CF 239,250,251
LK 48,244,29,0
CF 240,40,41
LK 248,246,29,2
RA 244,7,2304
CF 241,42,43
GS 27
WL 5
CF 242,44,45
CF 243,46,47
LK 40,248,30,0
CF 244,48,49
LK 44,250,30,2
RA 248,7,2560
CF 245,50,51
GS 28
WL 5
CF 246,248,249
CF 247,250,251
LK 48,252,31,0
CF 152,40,41
LK 248,254,31,2
RA 252,7,2816
CF 153,42,43
GS 29
WL 4
CF 154,44,45
CF 155,46,47
M 0,18,0
s_add_u32 s40, s40, 3072
LA 19
LC_ 107
RA 224,1,9216
RA 228,1,10240
s_addc_u32 s41, s41, 0
s_add_u32 m0, s38, 3072
M 0,19,1
WL 7
LO 106,18,106
LU 18,106
HZ 25,18
DW 0
LK 40,240,32,0
CF 156,48,49
LK 44,242,32,2
CF 157,50,51
GS 6
WL 6
CF 158,248,249
CF 159,250,251
LK 48,244,33,0
CF 160,40,41
LK 248,246,33,2
CF 161,42,43
GS 7
WL 1
CF 162,44,45
CF 163,46,47
M 0,20,2
WL 0
RA 232,1,11264
DW 1024
M 0,21,3
RV 252,22,5392
RA 236,1,12288
DW 2048
LK 40,248,34,0
CF 164,48,49
LK 44,250,34,2
s_add_u32 s42, s44, s60
CF 165,50,51
GS 8
s_addc_u32 s43, s45, 0
CF 166,248,249
CF 167,250,251
LK 48,252,35,0
CF 168,40,41
LK 248,254,35,2
CF 169,42,43
GS 9
WL 2
)"
R"(CF 170,44,45
CF 171,46,47
M 0,22,4
WL 0
LO 107,19,107
LU 19,107
HS 25,19
RA 216,1,13312
RA 220,1,14336
M 0,23,5
RA 224,1,15360
RV 36,8,6912
DQ 3072
GS 10
CF 172,48,49
CF 173,50,51
WL 3
Z 6,12,0
RA 228,1,16384
HX 25
ST 104
s_add_u32 s50, s50, %[d]
s_cmp_eq_u32 s50, %[wrap]
s_cselect_b32 s50, %[rst], s50
CF 174,248,249
CF 175,250,251
WL 3
M 6,13,1
s_add_u32 s38, s38, 32768
RA 232,1,17408
W1 25,1024
s_cmp_eq_u32 s38, s77
s_cselect_b32 s38, s76, s38
GS 11
WL 4
RA 236,1,18432
s_nop 5
v_cndmask_b32 v52, v52, v53, s[78:79]
v_cndmask_b32 v54, v54, v55, s[78:79]
v_cndmask_b32 v52, v52, v54, s[80:81]
v_mul_f32 v52, 0x3b2aaaab, v52
M 6,14,2
WL 3
LL 16,56
LL 104,57
XL 42,384
v_med3_f32 v53, v52, v39, v37
v_cmp_eq_f32 vcc, v52, v53
C 52,52,52
v_lshlrev_b32 v52, 16, v52
M 6,15,3
s_add_u32 s37, s37, 32768
LA 16
LC_ 104
v_add_u32 v4, s39, v4
s_cmp_eq_u32 s37, s75
s_cselect_b32 s37, s74, s37
XL 43,448
v_add_f32 v52, v36, v52
C 52,52,52
v_lshlrev_b32 v52, 16, v52
v_sqrt_f32 v52, v52
WL 4
M 6,16,4
WL 2
RA 216,1,19456
RA 220,1,20480
XL 44,512
C 52,52,52
v_lshlrev_b32 v52, 16, v52
v_rcp_f32 v52, v52
s_nop 0
v_mul_f32 v52, v38, v52
M 6,17,5
WV 17
LL 17,58
LL 105,59
XL 45,576
v_cndmask_b32 v52, 0, v52, vcc
CF 52,52,52
ds_bpermute_b32 v13, v28, v52
WL 2
M 6,18,0
s_mul_i32 s53, s51, 49152
LA 17
LC_ 105
RA 224,1,21504
RA 228,1,22528
s_bitcmp1_b64 s[90:91], s51
s_cselect_b64 s[44:45], s[84:85], s[82:83]
s_add_u32 s44, s44, s53
s_addc_u32 s45, s45, 0
s_add_u32 s51, s51, %[d]
s_cmp_eq_u32 s51, %[wrap]
s_cselect_b32 s51, %[rst], s51
XL 46,640
WL 5
M 6,19,1
WL 1
LO 104,16,104
LU 16,104
HZ 24,16
M 6,20,2
s_lshl_b32 s53, s52, 8
RA 232,1,23552
RA 236,1,24576
s_nop 1
v_add_u32 v1, s37, v0
s_add_u32 s48, %[alo], s53
s_addc_u32 s49, %[ahi], 0
s_add_u32 s52, s52, %[d]
s_cmp_eq_u32 s52, %[wrap]
s_cselect_b32 s52, %[rst], s52
XL 47,704
XS 224
XS 225
WL 2
M 6,21,3
WL 0
s_barrier
RA 216,1,0
v_and_b32 v22, 0xfffffcff, v1
XS 226
XS 227
XS 228
M 6,22,4
LO 105,17,105
LU 17,105
HS 24,17
RA 220,1,1024
s_sub_u32 s39, 0, s39
XS 229
XS 230
XS 231
M 6,23,5
RV 252,22,5120
ds_read_b64 v[14:15], v5 offset:0
HX 24
v_min_u32 v35, v35, v13
XS 232
XS 233
WL 1
Z 0,12,0
RA 224,1,2048
v_subrev_u32 v5, s39, v5
XS 234
XS 235
XS 236
M 0,13,1
s_mul_i32 s53, s50, 25600
RA 228,1,3072
XS 237
XS 238
XS 239
Z 1,24,0
s_add_u32 s40, %[wlo], s53
LL 18,80
LL 106,81
XS 240
XS 241
XS 242
M 1,25,1
s_addc_u32 s41, %[whi], 0
LA 18
LC_ 106
W1 24,0
XS 243
XS 244
XS 245
WL 4
M 0,14,2
s_mov_b32 m0, s38
RA 232,1,4096
v_add_f32 v14, v14, v15
XS 246
XS 247
WL 4
M 0,15,3
LL 19,82
LL 107,83
XS 152
XS 153
XS 154
M 1,26,2
LA 19
LC_ 107
RA 236,1,6144
DW 0
XS 155
AW 0,152
AW 1,153
M 1,27,3
WL 3
LO 106,18,106
LU 18,106
HZ 25,18
v_add_f32_dpp v14, v14, v14 quad_perm:[1,0,3,2] row_mask:0xf bank_mask:0xf
AW 2,154
AW 3,155
XS 156
M 0,16,4
WL 0
RA 216,1,7168
v_add_f32_dpp v14, v14, v14 quad_perm:[2,3,0,1] row_mask:0xf bank_mask:0xf
DW 1024
XS 157
XS 158
XS 159
M 0,17,5
RA 220,1,8192
v_add_f32_dpp v14, v14, v14 row_half_mirror row_mask:0xf bank_mask:0xf
AW 4,156
AW 5,157
AW 6,158
M 1,28,4
LO 107,19,107
LU 19,107
HS 25,19
v_cvt_f64_f32 v[14:15], v14
AW 7,159
XS 160
XS 161
M 1,29,5
WL 0
RA 224,1,9216
s_mov_b64 exec, s[54:55]
SP
s_mov_b64 exec, -1
HX 25
DW 2048
XS 162
XS 163
AW 8,160
M 0,18,0
s_add_u32 s42, s44, s60
RA 228,1,10240
s_addc_u32 s43, s45, 0
AW 9,161
AW 10,162
AW 11,163
M 0,19,1
s_add_u32 s40, s40, 3072
RA 232,1,11264
s_addc_u32 s41, s41, 0
s_add_u32 m0, s38, 3072
XS 164
XS 165
XS 166
M 1,30,0
WL 2
RV 252,22,5392
ST 104
XS 167
AW 12,164
AW 13,165
M 1,31,1
WV 24
RA 236,1,12288
W1 25,1024
AW 14,166
BW 36
AW 15,167
XS 168
M 0,20,2
WL 5
DW 0
RV 40,8,6144
XS 169
XS 170
RV 44,8,6208
XS 171
M 0,21,3
RA 216,1,13312
RV 48,8,6272
AW 16,168
AW 17,169
AW 18,170
BR 36
M 1,32,2
WV 24
DW 1024
v_add_u32 v4, s39, v4
AW 19,171
XS 172
BW 37
XS 173
M 1,33,3
RA 220,1,14336
BR 37
XS 174
XS 175
AW 20,172
WL 3
LZ 40,36
WV 24
BW 38
M 0,22,4
DW 2048
RV 40,8,6336
AW 21,173
AW 22,174
AW 23,175
M 0,23,5
RA 224,1,15360
BR 38
M 1,34,4
DQ 3072
M 1,35,5
WL 4
RA 228,1,16384
LS 44,37
WV 23
BW 39
RV 44,8,6400
Z 6,12,0
s_add_u32 s50, s50, %[d]
LL 16,56
LL 104,57
BR 39
M 6,13,1
s_cmp_eq_u32 s50, %[wrap]
LA 16
RA 232,1,17408
WL 6
LS 48,38
s_cselect_b32 s50, %[rst], s50
BW 40
Z 7,24,0
s_add_u32 s38, s38, 32768
LC_ 104
RV 48,8,6464
M 7,25,1
s_cmp_eq_u32 s38, s77
RA 236,1,18432
BR 40
WL 7
LS 40,39
s_cselect_b32 s38, s76, s38
BW 41
M 6,14,2
s_add_u32 s37, s37, 32768
RV 40,8,6528
BR 41
M 6,15,3
s_cmp_eq_u32 s37, s75
RA 216,1,19456
M 7,26,2
s_cselect_b32 s37, s74, s37
LL 17,58
LL 105,59
M 7,27,3
s_add_u32 s53, %[xo0], 196608
LA 17
RA 220,1,20480
s_add_u32 s46, %[xlo], s53
s_addc_u32 s47, %[xhi], 0
s_bitcmp1_b64 s[90:91], 48
s_cselect_b32 s58, 16, 0
s_lshr_b32 s58, 0xda43cb52, s58
v_lshrrev_b32 v3, 6, v0
v_and_b32 v3, 12, v3
v_bfe_u32 v3, s58, v3, 4
v_mad_u32_u24 v3, v3, s97, v21
WL 6
LS 44,40
WV 13
BW 42
RV 44,8,6592
M 6,16,4
WL 5
LC_ 105
XL 48,0
BR 42
M 6,17,5
s_mul_i32 s53, s51, 49152
RA 224,1,21504
LS 48,41
s_bitcmp1_b64 s[90:91], s51
BW 43
M 7,28,4
WV 13
LO 104,16,104
LU 16,104
RV 48,8,6656
BR 43
M 7,29,5
s_cselect_b64 s[44:45], s[84:85], s[82:83]
RA 228,1,22528
HZ 24,16
WL 5
LS 40,42
s_add_u32 s44, s44, s53
BW 44
M 6,18,0
s_addc_u32 s45, s45, 0
LL 18,60
LL 108,61
s_add_u32 s51, s51, %[d]
s_cmp_eq_u32 s51, %[wrap]
s_cselect_b32 s51, %[rst], s51
)"
R"(XL 49,64
RV 40,8,6720
M 6,19,1
LA 18
RA 232,1,23552
BR 44
M 7,30,0
LC_ 108
XL 50,128
M 7,31,1
WL 7
RA 236,1,24576
s_nop 1
v_add_u32 v1, s37, v0
LS 44,43
WV 14
BW 45
RV 44,8,6784
s_nop 0
BR 45
M 6,20,2
WL 10
LO 105,17,105
LU 17,105
M 6,21,3
WL 5
RA 216,1,0
HS 24,17
XL 51,192
LS 48,44
WV 14
BW 46
RV 48,8,6848
M 7,32,2
WV 13
LL 19,62
LL 109,63
RA 240,7,0
M 7,33,3
WL 4
LA 19
XL 52,256
BR 46
LS 40,45
s_lshl_b32 s53, s52, 8
BW 47
M 6,22,4
s_add_u32 s48, %[alo], s53
LC_ 109
RA 244,7,256
v_and_b32 v22, 0xfffffcff, v1
M 6,23,5
s_addc_u32 s49, %[ahi], 0
RA 220,1,1024
BR 47
M 7,34,4
LO 108,18,108
LU 18,108
s_add_u32 s52, s52, %[d]
s_cmp_eq_u32 s52, %[wrap]
s_cselect_b32 s52, %[rst], s52
XL 53,320
M 7,35,5
s_barrier
RV 252,22,5120
HS 24,18
WL 0
Z 0,12,0
s_sub_u32 s39, 0, s39
LL 16,80
LL 106,81
RA 224,1,2048
M 0,13,1
s_mul_i32 s53, s50, 25600
LA 16
RA 228,1,3072
LS 44,46
s_add_u32 s40, %[wlo], s53
RA 248,7,512
LS 48,47
s_addc_u32 s41, %[whi], 0
RA 252,7,768
s_nop 5
v_mul_f32 v36, 0xbb2aaaab, v36
v_mul_f32 v37, 0xbb2aaaab, v37
v_mul_f32 v38, 0xbb2aaaab, v38
v_mul_f32 v39, 0xbb2aaaab, v39
s_nop 1
LK 40,240,36,0
LK 44,242,36,2
s_mov_b32 m0, s38
RA 240,7,1024
LK 48,244,37,0
s_nop 0
CF 128,40,41
LK 248,246,37,2
RA 244,7,1280
CF 129,42,43
Z 1,24,0
LC_ 106
RA 232,1,4096
M 1,25,1
WL 8
RA 236,1,6144
ds_read_b64 v[14:15], v5 offset:0
CF 130,44,45
M 0,14,2
WL 8
LO 109,19,109
LU 19,109
CF 131,46,47
M 0,15,3
WL 7
RA 216,1,7168
HS 24,19
LK 40,248,38,0
CF 132,48,49
LK 44,250,38,2
RA 248,7,1536
CF 133,50,51
GZ 0
WL 8
CF 134,248,249
CF 135,250,251
LK 48,252,39,0
CF 136,40,41
LK 248,254,39,2
RA 252,7,1792
CF 137,42,43
GS 1
WL 8
CF 138,44,45
CF 139,46,47
LK 40,240,40,0
CF 140,48,49
LK 44,242,40,2
RA 240,7,2048
CF 141,50,51
GS 2
WL 8
CF 142,248,249
CF 143,250,251
LK 48,244,41,0
CF 144,40,41
LK 248,246,41,2
RA 244,7,2304
CF 145,42,43
GS 3
CF 146,44,45
CF 147,46,47
M 1,26,2
LL 17,82
LL 107,83
DW 0
M 1,27,3
WL 3
LA 17
RA 220,1,8192
v_subrev_u32 v5, s39, v5
LK 40,248,42,0
CF 148,48,49
LK 44,250,42,2
RA 248,7,2560
CF 149,50,51
GS 4
CF 150,248,249
CF 151,250,251
M 0,16,4
LC_ 107
DW 1024
M 0,17,5
WL 6
RA 224,1,9216
v_add_f32 v14, v14, v15
s_nop 1
v_add_f32_dpp v14, v14, v14 quad_perm:[1,0,3,2] row_mask:0xf bank_mask:0xf
s_nop 1
v_add_f32_dpp v14, v14, v14 quad_perm:[2,3,0,1] row_mask:0xf bank_mask:0xf
s_nop 1
v_add_f32_dpp v14, v14, v14 row_half_mirror row_mask:0xf bank_mask:0xf
HX 24
LK 48,252,43,0
CF 152,40,41
LK 248,254,43,2
RA 252,7,2816
CF 153,42,43
GS 5
CF 154,44,45
CF 155,46,47
M 1,28,4
LO 106,16,106
LU 16,106
v_cvt_f64_f32 v[14:15], v14
W1 24,0
M 1,29,5
WL 8
RA 228,1,10240
HZ 25,16
s_mov_b64 exec, s[54:55]
SP
s_mov_b64 exec, -1
DW 2048
LK 40,240,44,0
CF 156,48,49
LK 44,242,44,2
CF 157,50,51
GS 6
WL 6
CF 158,248,249
CF 159,250,251
M 0,18,0
s_add_u32 s40, s40, 3072
LL 18,84
LL 110,85
M 0,19,1
s_addc_u32 s41, s41, 0
LA 18
RA 232,1,11264
LK 48,244,45,0
CF 160,40,41
LK 248,246,45,2
s_add_u32 m0, s38, 3072
CF 161,42,43
GS 7
CF 162,44,45
CF 163,46,47
M 1,30,0
LC_ 110
RV 252,22,5392
M 1,31,1
WL 9
RA 236,1,12288
LK 40,248,46,0
CF 164,48,49
LK 44,250,46,2
CF 165,50,51
GS 8
WL 8
CF 166,248,249
CF 167,250,251
M 0,20,2
WL 5
LO 107,17,107
LU 17,107
M 0,21,3
RA 216,1,13312
HS 25,17
DW 0
LK 48,252,47,0
CF 168,40,41
LK 248,254,47,2
s_add_u32 s42, s44, s60
CF 169,42,43
GS 9
s_addc_u32 s43, s45, 0
CF 170,44,45
CF 171,46,47
M 1,32,2
LL 19,86
LL 111,87
M 1,33,3
WL 4
LA 19
RA 220,1,14336
DW 1024
M 0,22,4
WL 3
LC_ 111
ST 104
M 0,23,5
RA 224,1,15360
RV 36,8,6912
GS 10
CF 172,48,49
CF 173,50,51
M 1,34,4
LO 110,18,110
LU 18,110
DW 2048
M 1,35,5
WL 5
RA 228,1,16384
HS 25,18
Z 6,12,0
WL 4
LL 16,56
LL 104,57
CF 174,248,249
CF 175,250,251
M 6,13,1
WL 1
LA 16
RA 232,1,17408
DQ 3072
GS 11
s_add_u32 s50, s50, %[d]
s_nop 6
v_cndmask_b32 v52, v52, v53, s[78:79]
v_cndmask_b32 v54, v54, v55, s[78:79]
Z 7,24,0
s_cmp_eq_u32 s50, %[wrap]
LC_ 104
v_cndmask_b32 v52, v52, v54, s[80:81]
v_mul_f32 v52, 0x3b2aaaab, v52
M 7,25,1
s_cselect_b32 s50, %[rst], s50
RA 236,1,18432
v_med3_f32 v53, v52, v39, v37
v_cmp_eq_f32 vcc, v52, v53
M 6,14,2
WL 2
LO 111,19,111
LU 19,111
C 52,52,52
v_lshlrev_b32 v52, 16, v52
M 6,15,3
s_add_u32 s42, s44, s61
RA 216,1,19456
HS 25,19
v_add_f32 v52, v36, v52
C 52,52,52
M 7,26,2
s_addc_u32 s43, s45, 0
LL 17,58
LL 105,59
s_add_u32 s38, s38, 32768
s_cmp_eq_u32 s38, s77
s_cselect_b32 s38, s76, s38
v_lshlrev_b32 v52, 16, v52
v_sqrt_f32 v52, v52
M 7,27,3
s_add_u32 s37, s37, 32768
LA 17
RA 220,1,20480
s_cmp_eq_u32 s37, s75
s_cselect_b32 s37, s74, s37
C 52,52,52
v_lshlrev_b32 v52, 16, v52
M 6,16,4
WL 3
LC_ 105
ST 108
XL 54,384
v_rcp_f32 v52, v52
s_nop 0
v_mul_f32 v52, v38, v52
M 6,17,5
RA 224,1,21504
HX 25
v_cndmask_b32 v52, 0, v52, vcc
CF 52,52,52
M 7,28,4
LO 104,16,104
LU 16,104
W1 25,1024
ds_bpermute_b32 v13, v28, v52
M 7,29,5
WL 4
RA 228,1,22528
HZ 24,16
XL 55,448
M 6,18,0
s_mul_i32 s53, s51, 49152
LL 18,60
LL 108,61
s_bitcmp1_b64 s[90:91], s51
s_cselect_b64 s[44:45], s[84:85], s[82:83]
s_add_u32 s44, s44, s53
s_addc_u32 s45, s45, 0
s_add_u32 s51, s51, %[d]
s_cmp_eq_u32 s51, %[wrap]
s_cselect_b32 s51, %[rst], s51
M 6,19,1
LA 18
RA 232,1,23552
M 7,30,0
LC_ 108
XL 56,512
v_add_u32 v4, s39, v4
M 7,31,1
WL 6
RA 236,1,24576
s_nop 1
v_add_u32 v1, s37, v0
M 6,20,2
WL 4
LO 105,17,105
LU 17,105
XL 57,576
M 6,21,3
RA 216,1,0
HS 24,17
M 7,32,2
LL 19,62
LL 109,63
M 7,33,3
WL 1
LA 19
XL 58,640
XS 128
)"
R"(XS 129
M 6,22,4
s_lshl_b32 s53, s52, 8
LC_ 109
v_and_b32 v22, 0xfffffcff, v1
XS 130
XS 131
M 6,23,5
s_add_u32 s48, %[alo], s53
RA 220,1,1024
AW 24,128
AW 25,129
AW 26,130
M 7,34,4
WV 21
LO 108,18,108
LU 18,108
s_addc_u32 s49, %[ahi], 0
s_add_u32 s52, s52, %[d]
s_cmp_eq_u32 s52, %[wrap]
s_cselect_b32 s52, %[rst], s52
XL 59,704
AW 27,131
XS 132
M 7,35,5
s_barrier
RV 252,22,5120
HS 24,18
XS 133
XS 134
XS 135
WL 0
Z 0,12,0
s_sub_u32 s39, 0, s39
LL 16,80
LL 106,81
AW 28,132
AW 29,133
M 0,13,1
s_mul_i32 s53, s50, 25600
LA 16
RA 224,1,2048
AW 30,134
AW 31,135
v_min_u32 v35, v35, v13
Z 1,24,0
s_add_u32 s40, %[wlo], s53
LC_ 106
RA 228,1,3072
XS 136
XS 137
M 1,25,1
s_addc_u32 s41, %[whi], 0
RA 232,1,4096
XS 138
XS 139
AW 32,136
Z 2,36,0
WL 1
LO 109,19,109
LU 19,109
AW 33,137
AW 34,138
AW 35,139
M 2,37,1
s_mov_b32 m0, s38
RA 236,1,6144
HS 24,19
ds_read_b64 v[14:15], v5 offset:0
XS 140
XS 141
M 0,14,2
LL 17,82
LL 107,83
XS 142
XS 143
AW 36,140
M 0,15,3
LA 17
DW 0
AW 37,141
AW 38,142
M 1,26,2
LC_ 107
v_subrev_u32 v5, s39, v5
AW 39,143
XS 144
XS 145
M 1,27,3
RA 216,1,7168
HX 24
XS 146
XS 147
M 2,38,2
WL 4
LO 106,16,106
LU 16,106
AW 40,144
AW 41,145
AW 42,146
M 2,39,3
RA 220,1,8192
HZ 25,16
AW 43,147
XS 148
XS 149
M 0,16,4
LL 18,84
LL 110,85
W1 24,0
XS 150
XS 151
M 0,17,5
LA 18
DW 1024
AW 44,148
AW 45,149
AW 46,150
M 1,28,4
WL 6
LC_ 110
DW 2048
AW 47,151
XS 152
M 1,29,5
s_add_u32 s42, s44, s60
RA 224,1,9216
v_add_f32 v14, v14, v15
s_nop 1
v_add_f32_dpp v14, v14, v14 quad_perm:[1,0,3,2] row_mask:0xf bank_mask:0xf
XS 153
XS 154
XS 155
M 2,40,4
WL 4
LO 107,17,107
LU 17,107
AW 48,152
AW 49,153
M 2,41,5
s_addc_u32 s43, s45, 0
RA 228,1,10240
HS 25,17
v_add_f32_dpp v14, v14, v14 quad_perm:[2,3,0,1] row_mask:0xf bank_mask:0xf
AW 50,154
AW 51,155
XS 156
M 0,18,0
s_add_u32 s40, s40, 3072
LL 19,86
LL 111,87
XS 157
XS 158
XS 159
M 0,19,1
s_addc_u32 s41, s41, 0
LA 19
v_add_f32_dpp v14, v14, v14 row_half_mirror row_mask:0xf bank_mask:0xf
AW 52,156
AW 53,157
M 1,30,0
s_add_u32 m0, s38, 3072
LC_ 111
ST 104
AW 54,158
AW 55,159
XS 160
M 1,31,1
WV 25
RA 232,1,11264
v_cvt_f64_f32 v[14:15], v14
XS 161
XS 162
M 2,42,0
WL 3
LO 110,18,110
LU 18,110
XS 163
AW 56,160
BW 48
AW 57,161
M 2,43,1
RV 252,22,5392
HS 25,18
s_mov_b64 exec, s[54:55]
SP
s_mov_b64 exec, -1
AW 58,162
RV 40,8,6144
AW 59,163
M 0,20,2
RA 236,1,12288
RV 44,8,6208
XS 164
XS 165
XS 166
M 0,21,3
DW 0
RV 48,8,6272
XS 167
AW 60,164
AW 61,165
M 1,32,2
WV 26
DW 1024
BR 48
AW 62,166
AW 63,167
M 1,33,3
s_add_u32 s42, s44, s61
RA 216,1,13312
XS 168
BW 49
XS 169
XS 170
M 2,44,2
WL 5
LO 111,19,111
LU 19,111
XS 171
BR 49
AW 64,168
M 2,45,3
s_addc_u32 s43, s45, 0
RA 220,1,14336
HS 25,19
AW 65,169
AW 66,170
AW 67,171
WL 4
LZ 40,48
WV 26
BW 50
M 0,22,4
DW 2048
RV 40,8,6336
XS 172
XS 173
M 0,23,5
ST 108
BR 50
XS 174
XS 175
AW 68,172
M 1,34,4
HX 25
AW 69,173
AW 70,174
AW 71,175
M 1,35,5
WL 4
RA 224,1,15360
LS 44,49
WV 27
BW 51
M 2,46,4
WL 5
LL 16,56
LL 104,57
M 2,47,5
LA 16
RA 228,1,16384
RV 44,8,6400
Z 6,12,0
WV 26
LC_ 104
W1 25,1024
BR 51
M 6,13,1
WL 8
DQ 3072
v_add_u32 v4, s39, v4
LS 48,50
s_add_u32 s50, s50, %[d]
BW 52
Z 7,24,0
s_cmp_eq_u32 s50, %[wrap]
RV 48,8,6464
BR 52
M 7,25,1
s_cselect_b32 s50, %[rst], s50
RA 232,1,17408
Z 8,36,0
WL 4
LL 17,58
LL 105,59
M 8,37,1
s_add_u32 s38, s38, 32768
LA 17
RA 236,1,18432
LS 40,51
s_cmp_eq_u32 s38, s77
WV 26
BW 53
M 6,14,2
s_cselect_b32 s38, s76, s38
LC_ 105
RV 40,8,6528
M 6,15,3
s_add_u32 s37, s37, 32768
BR 53
M 7,26,2
WL 7
LO 104,16,104
LU 16,104
M 7,27,3
s_cmp_eq_u32 s37, s75
RA 216,1,19456
HZ 24,16
LS 44,52
s_cselect_b32 s37, s74, s37
WV 15
BW 54
M 8,38,2
WL 6
LL 18,60
LL 108,61
RV 44,8,6592
M 8,39,3
s_add_u32 s53, %[xo0], 393216
LA 18
RA 220,1,20480
BR 54
M 6,16,4
s_add_u32 s46, %[xlo], s53
LC_ 108
s_addc_u32 s47, %[xhi], 0
s_bitcmp1_b64 s[90:91], 48
s_cselect_b32 s58, 16, 0
s_lshr_b32 s58, 0xfe987610, s58
v_lshrrev_b32 v3, 6, v0
v_and_b32 v3, 12, v3
v_bfe_u32 v3, s58, v3, 4
v_mad_u32_u24 v3, v3, s97, v21
M 6,17,5
WL 7
XL 60,0
LS 48,53
s_lshl_b32 s53, s52, 8
WV 15
BW 55
M 7,28,4
WV 14
LO 105,17,105
LU 17,105
RV 48,8,6656
M 7,29,5
s_add_u32 s48, %[alo], s53
RA 224,1,21504
HS 24,17
BR 55
M 8,40,4
WL 5
LL 19,62
LL 109,63
M 8,41,5
s_addc_u32 s49, %[ahi], 0
LA 19
RA 228,1,22528
LS 40,54
s_add_u32 s52, s52, %[d]
BW 56
M 6,18,0
s_cmp_eq_u32 s52, %[wrap]
LC_ 109
RV 40,8,6720
s_cselect_b32 s52, %[rst], s52
s_mul_i32 s53, s51, 49152
s_bitcmp1_b64 s[90:91], s51
s_cselect_b64 s[44:45], s[84:85], s[82:83]
s_add_u32 s44, s44, s53
s_addc_u32 s45, s45, 0
s_add_u32 s51, s51, %[d]
s_cmp_eq_u32 s51, %[wrap]
s_cselect_b32 s51, %[rst], s51
M 6,19,1
XL 61,64
BR 56
M 7,30,0
WL 10
LO 108,18,108
LU 18,108
M 7,31,1
WL 6
RA 232,1,23552
HS 24,18
LS 44,55
WV 14
BW 57
M 8,42,0
WL 6
LL 16,64
LL 112,65
RV 44,8,6784
M 8,43,1
LA 16
RA 236,1,24576
BR 57
M 6,20,2
LC_ 112
)"
R"(XL 62,128
M 6,21,3
WL 7
XL 63,192
v_add_u32 v1, s37, v0
LS 48,56
WV 15
BW 58
M 7,32,2
LO 109,19,109
LU 19,109
RV 48,8,6848
M 7,33,3
WV 14
RA 216,1,0
HS 24,19
RA 240,7,0
M 8,44,2
WL 6
LL 17,66
LL 113,67
BR 58
M 8,45,3
WL 6
LA 17
XL 64,256
LS 40,57
s_barrier
BW 59
M 6,22,4
s_sub_u32 s39, 0, s39
LC_ 113
ds_read_b64 v[14:15], v5 offset:0
M 6,23,5
XL 65,320
RA 244,7,256
M 7,34,4
WL 10
LO 112,16,112
LU 16,112
v_and_b32 v22, 0xfffffcff, v1
BR 59
M 7,35,5
WL 6
RA 220,1,1024
HS 24,16
LS 44,58
RA 248,7,512
v_subrev_u32 v5, s39, v5
M 8,46,4
WL 1
LL 18,80
LL 106,81
M 8,47,5
LA 18
RV 252,22,5120
WL 0
Z 0,12,0
LC_ 106
RA 224,1,2048
M 0,13,1
s_mul_i32 s53, s50, 25600
RA 228,1,3072
LS 48,59
s_add_u32 s40, %[wlo], s53
RA 252,7,768
s_nop 5
v_mul_f32 v36, 0xbb2aaaab, v36
v_mul_f32 v37, 0xbb2aaaab, v37
v_mul_f32 v38, 0xbb2aaaab, v38
Z 1,24,0
LO 113,17,113
LU 17,113
v_mul_f32 v39, 0xbb2aaaab, v39
M 1,25,1
s_addc_u32 s41, %[whi], 0
RA 232,1,4096
HS 24,17
LK 40,240,48,0
LK 44,242,48,2
s_mov_b32 m0, s38
RA 240,7,1024
LK 48,244,49,0
s_nop 0
CF 128,40,41
LK 248,246,49,2
RA 244,7,1280
CF 129,42,43
Z 2,36,0
WL 4
LL 19,82
LL 107,83
M 2,37,1
LA 19
RA 236,1,6144
CF 130,44,45
CF 131,46,47
LK 40,248,50,0
CF 132,48,49
LK 44,250,50,2
RA 248,7,1536
CF 133,50,51
GZ 0
WL 6
CF 134,248,249
CF 135,250,251
LK 48,252,51,0
CF 136,40,41
LK 248,254,51,2
RA 252,7,1792
CF 137,42,43
GS 1
CF 138,44,45
CF 139,46,47
M 0,14,2
LC_ 107
DW 0
M 0,15,3
WL 6
v_add_f32 v14, v14, v15
HX 24
LK 40,240,52,0
CF 140,48,49
LK 44,242,52,2
RA 240,7,2048
CF 141,50,51
GS 2
WL 6
CF 142,248,249
CF 143,250,251
LK 48,244,53,0
CF 144,40,41
LK 248,246,53,2
RA 244,7,2304
CF 145,42,43
GS 3
CF 146,44,45
CF 147,46,47
M 1,26,2
LO 106,18,106
LU 18,106
M 1,27,3
RA 216,1,7168
HZ 25,18
M 2,38,2
WL 6
LL 16,84
LL 110,85
v_add_f32_dpp v14, v14, v14 quad_perm:[1,0,3,2] row_mask:0xf bank_mask:0xf
W1 24,0
M 2,39,3
WL 6
LA 16
RA 220,1,8192
LK 40,248,54,0
CF 148,48,49
LK 44,250,54,2
RA 248,7,2560
CF 149,50,51
GS 4
CF 150,248,249
CF 151,250,251
M 0,16,4
WL 8
LC_ 110
DW 1024
v_add_f32_dpp v14, v14, v14 quad_perm:[2,3,0,1] row_mask:0xf bank_mask:0xf
M 0,17,5
s_add_u32 s42, s44, s60
v_add_f32_dpp v14, v14, v14 row_half_mirror row_mask:0xf bank_mask:0xf
LK 48,252,55,0
CF 152,40,41
LK 248,254,55,2
s_addc_u32 s43, s45, 0
RA 252,7,2816
CF 153,42,43
GS 5
CF 154,44,45
CF 155,46,47
M 1,28,4
WL 9
LO 107,19,107
LU 19,107
M 1,29,5
RA 224,1,9216
HS 25,19
M 2,40,4
WL 4
LL 17,86
LL 111,87
v_cvt_f64_f32 v[14:15], v14
M 2,41,5
LA 17
RA 228,1,10240
LK 40,240,56,0
CF 156,48,49
LK 44,242,56,2
CF 157,50,51
GS 6
CF 158,248,249
CF 159,250,251
M 0,18,0
LC_ 111
s_mov_b64 exec, s[54:55]
SP
s_mov_b64 exec, -1
DW 2048
M 0,19,1
ST 104
LK 48,244,57,0
CF 160,40,41
LK 248,246,57,2
s_add_u32 s40, s40, 3072
CF 161,42,43
GS 7
s_addc_u32 s41, s41, 0
CF 162,44,45
CF 163,46,47
M 1,30,0
WL 5
LO 110,16,110
LU 16,110
M 1,31,1
s_add_u32 m0, s38, 3072
RA 232,1,11264
HS 25,16
M 2,42,0
WL 2
LL 18,88
LL 114,89
M 2,43,1
LA 18
RV 252,22,5392
LK 40,248,58,0
CF 164,48,49
LK 44,250,58,2
CF 165,50,51
GS 8
CF 166,248,249
CF 167,250,251
M 0,20,2
LC_ 114
RA 236,1,12288
M 0,21,3
DW 0
LK 48,252,59,0
CF 168,40,41
LK 248,254,59,2
s_add_u32 s42, s44, s61
CF 169,42,43
GS 9
s_addc_u32 s43, s45, 0
CF 170,44,45
CF 171,46,47
M 1,32,2
WL 5
LO 111,17,111
LU 17,111
M 1,33,3
RA 216,1,13312
HS 25,17
M 2,44,2
WL 1
LL 19,90
LL 115,91
M 2,45,3
LA 19
RA 220,1,14336
M 0,22,4
LC_ 115
DW 1024
M 0,23,5
ST 108
RV 36,8,6912
GS 10
CF 172,48,49
CF 173,50,51
M 1,34,4
LO 114,18,114
LU 18,114
M 1,35,5
RA 224,1,15360
HS 25,18
M 2,46,4
WL 3
LL 16,56
LL 104,57
DW 2048
M 2,47,5
LA 16
RA 228,1,16384
CF 174,248,249
Z 6,12,0
LC_ 104
DQ 3072
CF 175,250,251
M 6,13,1
s_add_u32 s42, s44, s62
GS 11
s_addc_u32 s43, s45, 0
s_nop 6
v_cndmask_b32 v52, v52, v53, s[78:79]
v_cndmask_b32 v54, v54, v55, s[78:79]
Z 7,24,0
WL 5
LO 115,19,115
LU 19,115
v_cndmask_b32 v52, v52, v54, s[80:81]
M 7,25,1
s_add_u32 s50, s50, %[d]
RA 232,1,17408
HS 25,19
v_mul_f32 v52, 0x3b2aaaab, v52
Z 8,36,0
WL 2
LL 17,58
LL 105,59
v_med3_f32 v53, v52, v39, v37
v_cmp_eq_f32 vcc, v52, v53
M 8,37,1
s_cmp_eq_u32 s50, %[wrap]
LA 17
RA 236,1,18432
C 52,52,52
M 6,14,2
s_cselect_b32 s50, %[rst], s50
LC_ 105
ST 112
v_lshlrev_b32 v52, 16, v52
M 6,15,3
s_add_u32 s38, s38, 32768
HX 25
v_add_f32 v52, v36, v52
C 52,52,52
M 7,26,2
WL 4
LO 104,16,104
LU 16,104
v_lshlrev_b32 v52, 16, v52
M 7,27,3
s_cmp_eq_u32 s38, s77
RA 216,1,19456
HZ 24,16
v_sqrt_f32 v52, v52
M 8,38,2
WL 2
LL 18,60
LL 108,61
W1 25,1024
C 52,52,52
v_lshlrev_b32 v52, 16, v52
M 8,39,3
s_cselect_b32 s38, s76, s38
LA 18
RA 220,1,20480
v_rcp_f32 v52, v52
M 6,16,4
s_add_u32 s37, s37, 32768
LC_ 108
v_add_u32 v4, s39, v4
s_cmp_eq_u32 s37, s75
s_cselect_b32 s37, s74, s37
v_mul_f32 v52, v38, v52
M 6,17,5
XL 66,384
v_cndmask_b32 v52, 0, v52, vcc
CF 52,52,52
M 7,28,4
WL 5
LO 105,17,105
LU 17,105
ds_bpermute_b32 v13, v28, v52
M 7,29,5
s_lshl_b32 s53, s52, 8
RA 224,1,21504
HS 24,17
M 8,40,4
WL 3
LL 19,62
LL 109,63
M 8,41,5
s_add_u32 s48, %[alo], s53
LA 19
RA 228,1,22528
M 6,18,0
s_addc_u32 s49, %[ahi], 0
LC_ 109
s_add_u32 s52, s52, %[d]
s_cmp_eq_u32 s52, %[wrap]
s_cselect_b32 s52, %[rst], s52
s_mul_i32 s53, s51, 49152
s_bitcmp1_b64 s[90:91], s51
s_cselect_b64 s[44:45], s[84:85], s[82:83]
s_add_u32 s44, s44, s53
s_addc_u32 s45, s45, 0
s_add_u32 s51, s51, %[d]
s_cmp_eq_u32 s51, %[wrap]
s_cselect_b32 s51, %[rst], s51
M 6,19,1
XL 67,448
)"
R"(M 7,30,0
WL 5
LO 108,18,108
LU 18,108
M 7,31,1
RA 232,1,23552
HS 24,18
M 8,42,0
WL 2
LL 16,64
LL 112,65
M 8,43,1
LA 16
RA 236,1,24576
XL 68,512
M 6,20,2
LC_ 112
XL 69,576
XS 128
XS 129
M 6,21,3
v_add_u32 v1, s37, v0
XS 130
M 7,32,2
WL 4
LO 109,19,109
LU 19,109
XS 131
AW 72,128
M 7,33,3
RA 216,1,0
HS 24,19
AW 73,129
AW 74,130
M 8,44,2
WL 2
LL 17,66
LL 113,67
AW 75,131
M 8,45,3
WV 21
LA 17
XL 70,640
XS 132
XS 133
M 6,22,4
s_barrier
LC_ 113
v_and_b32 v22, 0xfffffcff, v1
XS 134
XS 135
M 6,23,5
s_sub_u32 s39, 0, s39
RA 220,1,1024
AW 76,132
M 7,34,4
WL 4
LO 112,16,112
LU 16,112
AW 77,133
AW 78,134
M 7,35,5
RV 252,22,5120
HS 24,16
AW 79,135
v_min_u32 v35, v35, v13
M 8,46,4
WL 1
LL 18,80
LL 106,81
XS 136
M 8,47,5
LA 18
RA 224,1,2048
XS 137
XS 138
WL 2
Z 0,12,0
LC_ 106
RA 228,1,3072
XS 139
AW 80,136
M 0,13,1
ds_read_b64 v[14:15], v5 offset:0
AW 81,137
AW 82,138
Z 1,24,0
LO 113,17,113
LU 17,113
XL 71,704
AW 83,139
M 1,25,1
s_mul_i32 s53, s50, 25600
RA 232,1,4096
HS 24,17
XS 140
XS 141
Z 2,36,0
s_add_u32 s40, %[wlo], s53
LL 19,82
LL 107,83
XS 142
XS 143
M 2,37,1
s_addc_u32 s41, %[whi], 0
LA 19
v_subrev_u32 v5, s39, v5
AW 84,140
Z 3,48,0
WL 3
LC_ 107
HX 24
AW 85,141
AW 86,142
M 3,49,1
s_mov_b32 m0, s38
RA 236,1,6144
AW 87,143
XS 144
M 0,14,2
LO 106,18,106
LU 18,106
XS 145
M 0,15,3
W1 24,0
HZ 25,18
XS 146
XS 147
M 1,26,2
LL 16,84
LL 110,85
AW 88,144
AW 89,145
M 1,27,3
LA 16
RA 216,1,7168
AW 90,146
AW 91,147
M 2,38,2
LC_ 110
DW 0
XS 148
M 2,39,3
WL 8
DW 1024
v_add_f32 v14, v14, v15
s_nop 1
v_add_f32_dpp v14, v14, v14 quad_perm:[1,0,3,2] row_mask:0xf bank_mask:0xf
XS 149
XS 150
M 3,50,2
WL 4
LO 107,19,107
LU 19,107
v_add_f32_dpp v14, v14, v14 quad_perm:[2,3,0,1] row_mask:0xf bank_mask:0xf
XS 151
AW 92,148
M 3,51,3
s_add_u32 s42, s44, s60
RA 220,1,8192
HS 25,19
AW 93,149
M 0,16,4
s_addc_u32 s43, s45, 0
LL 17,86
LL 111,87
v_add_f32_dpp v14, v14, v14 row_half_mirror row_mask:0xf bank_mask:0xf
AW 94,150
AW 95,151
M 0,17,5
LA 17
ST 104
XS 152
XS 153
M 1,28,4
LC_ 111
DW 2048
v_cvt_f64_f32 v[14:15], v14
XS 154
M 1,29,5
RA 224,1,9216
XS 155
AW 96,152
M 2,40,4
WL 4
LO 110,16,110
LU 16,110
s_mov_b64 exec, s[54:55]
SP
s_mov_b64 exec, -1
AW 97,153
AW 98,154
M 2,41,5
WV 27
HS 25,16
AW 99,155
M 3,52,4
WL 3
LL 18,88
LL 114,89
XS 156
XS 157
M 3,53,5
s_add_u32 s40, s40, 3072
LA 18
RA 228,1,10240
XS 158
XS 159
M 0,18,0
s_addc_u32 s41, s41, 0
LC_ 114
AW 100,156
AW 101,157
M 0,19,1
s_add_u32 m0, s38, 3072
BW 60
AW 102,158
M 1,30,0
WL 5
LO 111,17,111
LU 17,111
AW 103,159
XS 160
M 1,31,1
s_add_u32 s42, s44, s61
RA 232,1,11264
HS 25,17
XS 161
XS 162
M 2,42,0
s_addc_u32 s43, s45, 0
LL 19,90
LL 115,91
XS 163
M 2,43,1
LA 19
RV 252,22,5392
AW 104,160
AW 105,161
M 3,54,0
WL 5
LC_ 115
ST 108
AW 106,162
AW 107,163
M 3,55,1
RA 236,1,12288
RV 40,8,6144
XS 164
M 0,20,2
WL 7
LO 114,18,114
LU 18,114
XS 165
RV 44,8,6208
XS 166
M 0,21,3
DW 0
HS 25,18
XS 167
RV 48,8,6272
AW 108,164
M 1,32,2
DW 1024
BR 60
AW 109,165
AW 110,166
M 1,33,3
RA 216,1,13312
AW 111,167
M 2,44,2
WV 29
BW 61
XS 168
XS 169
M 2,45,3
s_add_u32 s42, s44, s62
BR 61
XS 170
XS 171
M 3,56,2
WL 3
LO 115,19,115
LU 19,115
AW 112,168
M 3,57,3
s_addc_u32 s43, s45, 0
RA 220,1,14336
HS 25,19
AW 113,169
AW 114,170
LZ 40,60
WV 28
BW 62
M 0,22,4
DW 2048
RV 40,8,6336
AW 115,171
XS 172
M 0,23,5
ST 112
BR 62
XS 173
M 1,34,4
HX 25
XS 174
XS 175
M 1,35,5
WL 4
RA 224,1,15360
AW 116,172
AW 117,173
LS 44,61
WV 29
BW 63
M 2,46,4
LL 16,56
LL 104,57
AW 118,174
AW 119,175
M 2,47,5
LA 16
W1 25,1024
M 3,58,4
WL 7
LC_ 104
DQ 3072
RV 44,8,6400
M 3,59,5
WV 29
RA 228,1,16384
BR 63
Z 6,12,0
WL 8
v_add_u32 v4, s39, v4
M 6,13,1
s_add_u32 s50, s50, %[d]
LS 48,62
s_cmp_eq_u32 s50, %[wrap]
BW 64
Z 7,24,0
s_cselect_b32 s50, %[rst], s50
LL 17,58
LL 105,59
M 7,25,1
LA 17
RA 232,1,17408
RV 48,8,6464
Z 8,36,0
LC_ 105
BR 64
M 8,37,1
WV 28
Z 9,48,0
WL 6
LO 104,16,104
LU 16,104
M 9,49,1
s_add_u32 s38, s38, 32768
RA 236,1,18432
HZ 24,16
LS 40,63
s_cmp_eq_u32 s38, s77
BW 65
M 6,14,2
s_cselect_b32 s38, s76, s38
LL 18,60
LL 108,61
RV 40,8,6528
M 6,15,3
s_add_u32 s37, s37, 32768
LA 18
BR 65
M 7,26,2
s_cmp_eq_u32 s37, s75
LC_ 108
M 7,27,3
s_cselect_b32 s37, s74, s37
RA 216,1,19456
WL 7
LS 44,64
WV 16
BW 66
M 8,38,2
WV 15
LO 105,17,105
LU 17,105
M 8,39,3
s_add_u32 s53, %[xo0], 393216
RV 44,8,6592
HS 24,17
M 9,50,2
WL 4
LL 19,62
LL 109,63
M 9,51,3
s_add_u32 s46, %[xlo], s53
LA 19
RA 220,1,20480
BR 66
)"
R"(M 6,16,4
s_addc_u32 s47, %[xhi], 0
LC_ 109
M 6,17,5
s_bitcmp1_b64 s[90:91], 48
LS 48,65
s_cselect_b32 s58, 16, 0
BW 67
M 7,28,4
WL 8
LO 108,18,108
LU 18,108
s_lshr_b32 s58, 0xda43cb52, s58
v_lshrrev_b32 v3, 6, v0
v_and_b32 v3, 12, v3
v_bfe_u32 v3, s58, v3, 4
v_mad_u32_u24 v3, v3, s97, v21
M 7,29,5
RA 224,1,21504
HS 24,18
RV 48,8,6656
M 8,40,4
WV 14
LL 16,64
LL 112,65
BR 67
M 8,41,5
WL 5
LA 16
XL 72,0
LS 40,66
s_mul_i32 s53, s51, 49152
BW 68
M 9,52,4
LC_ 112
RV 40,8,6720
M 9,53,5
s_bitcmp1_b64 s[90:91], s51
RA 228,1,22528
BR 68
M 6,18,0
WL 10
LO 109,19,109
LU 19,109
M 6,19,1
s_cselect_b64 s[44:45], s[84:85], s[82:83]
HS 24,19
s_add_u32 s44, s44, s53
s_addc_u32 s45, s45, 0
s_add_u32 s51, s51, %[d]
s_cmp_eq_u32 s51, %[wrap]
s_cselect_b32 s51, %[rst], s51
M 7,30,0
LL 17,66
LL 113,67
M 7,31,1
WL 6
LA 17
RA 232,1,23552
LS 44,67
WV 14
BW 69
M 8,42,0
WV 13
LC_ 113
XL 73,64
M 8,43,1
s_lshl_b32 s53, s52, 8
RV 44,8,6784
BR 69
M 9,54,0
WL 6
LO 112,16,112
LU 16,112
M 9,55,1
s_add_u32 s48, %[alo], s53
RA 236,1,24576
HS 24,16
LS 48,68
s_addc_u32 s49, %[ahi], 0
BW 70
M 6,20,2
s_add_u32 s52, s52, %[d]
LL 18,68
LL 116,69
s_cmp_eq_u32 s52, %[wrap]
s_cselect_b32 s52, %[rst], s52
M 6,21,3
LA 18
XL 74,128
M 7,32,2
LC_ 116
XL 75,192
RV 48,8,6848
M 7,33,3
RA 240,7,0
BR 70
M 8,44,2
WL 9
LO 113,17,113
LU 17,113
M 8,45,3
WL 7
HS 24,17
v_add_u32 v1, s37, v0
LS 40,69
WV 15
BW 71
M 9,56,2
WL 7
LL 19,70
LL 117,71
M 9,57,3
LA 19
RA 216,1,0
M 6,22,4
LC_ 117
XL 76,256
M 6,23,5
s_barrier
RA 244,7,256
BR 71
M 7,34,4
WL 6
LO 116,18,116
LU 18,116
v_and_b32 v22, 0xfffffcff, v1
M 7,35,5
s_sub_u32 s39, 0, s39
RA 220,1,1024
HS 24,18
LS 44,70
RA 248,7,512
M 8,46,4
LL 16,80
LL 106,81
M 8,47,5
LA 16
RV 252,22,5120
M 9,58,4
WL 3
LC_ 106
RA 224,1,2048
M 9,59,5
RA 228,1,3072
WL 3
Z 0,12,0
LO 117,19,117
LU 19,117
M 0,13,1
ds_read_b64 v[14:15], v5 offset:0
HS 24,19
LS 48,71
RA 252,7,768
s_nop 6
v_mul_f32 v36, 0xbb2aaaab, v36
v_mul_f32 v37, 0xbb2aaaab, v37
v_mul_f32 v38, 0xbb2aaaab, v38
Z 1,24,0
LL 17,82
LL 107,83
XL 77,320
v_mul_f32 v39, 0xbb2aaaab, v39
M 1,25,1
s_mul_i32 s53, s50, 25600
LA 17
RA 232,1,4096
LK 40,240,60,0
LK 44,242,60,2
s_add_u32 s40, %[wlo], s53
RA 240,7,1024
v_subrev_u32 v5, s39, v5
LK 48,244,61,0
CF 128,40,41
LK 248,246,61,2
s_addc_u32 s41, %[whi], 0
RA 244,7,1280
CF 129,42,43
Z 2,36,0
s_mov_b32 m0, s38
LC_ 107
HX 24
M 2,37,1
W1 24,0
CF 130,44,45
Z 3,48,0
WL 8
LO 106,16,106
LU 16,106
M 3,49,1
RA 236,1,6144
HZ 25,16
CF 131,46,47
LK 40,248,62,0
CF 132,48,49
LK 44,250,62,2
RA 248,7,1536
CF 133,50,51
GZ 0
WL 8
CF 134,248,249
CF 135,250,251
LK 48,252,63,0
CF 136,40,41
LK 248,254,63,2
RA 252,7,1792
CF 137,42,43
GS 1
WL 6
CF 138,44,45
CF 139,46,47
LK 40,240,64,0
CF 140,48,49
LK 44,242,64,2
RA 240,7,2048
CF 141,50,51
GS 2
CF 142,248,249
CF 143,250,251
M 0,14,2
LL 18,84
LL 110,85
M 0,15,3
LA 18
DW 0
M 1,26,2
WL 7
LC_ 110
M 1,27,3
s_add_u32 s42, s44, s60
RA 216,1,7168
v_add_f32 v14, v14, v15
LK 48,244,65,0
CF 144,40,41
LK 248,246,65,2
s_addc_u32 s43, s45, 0
RA 244,7,2304
CF 145,42,43
GS 3
CF 146,44,45
CF 147,46,47
M 2,38,2
WL 9
LO 107,17,107
LU 17,107
v_add_f32_dpp v14, v14, v14 quad_perm:[1,0,3,2] row_mask:0xf bank_mask:0xf
M 2,39,3
DW 1024
HS 25,17
M 3,50,2
WL 7
LL 19,86
LL 111,87
v_add_f32_dpp v14, v14, v14 quad_perm:[2,3,0,1] row_mask:0xf bank_mask:0xf
M 3,51,3
WL 6
LA 19
RA 220,1,8192
LK 40,248,66,0
CF 148,48,49
LK 44,250,66,2
RA 248,7,2560
CF 149,50,51
GS 4
CF 150,248,249
CF 151,250,251
M 0,16,4
LC_ 111
ST 104
v_add_f32_dpp v14, v14, v14 row_half_mirror row_mask:0xf bank_mask:0xf
M 0,17,5
v_cvt_f64_f32 v[14:15], v14
M 1,28,4
WL 6
LO 110,18,110
LU 18,110
M 1,29,5
RA 224,1,9216
HS 25,18
LK 48,252,67,0
CF 152,40,41
LK 248,254,67,2
RA 252,7,2816
CF 153,42,43
GS 5
CF 154,44,45
CF 155,46,47
M 2,40,4
LL 16,88
LL 114,89
s_mov_b64 exec, s[54:55]
SP
s_mov_b64 exec, -1
M 2,41,5
LA 16
DW 2048
M 3,52,4
WL 5
LC_ 114
M 3,53,5
s_add_u32 s42, s44, s61
RA 228,1,10240
LK 40,240,68,0
CF 156,48,49
LK 44,242,68,2
s_addc_u32 s43, s45, 0
CF 157,50,51
GS 6
CF 158,248,249
CF 159,250,251
M 0,18,0
WL 5
LO 111,19,111
LU 19,111
M 0,19,1
s_add_u32 s40, s40, 3072
HS 25,19
LK 48,244,69,0
CF 160,40,41
LK 248,246,69,2
s_addc_u32 s41, s41, 0
CF 161,42,43
GS 7
s_add_u32 m0, s38, 3072
CF 162,44,45
CF 163,46,47
M 1,30,0
LL 17,90
LL 115,91
M 1,31,1
LA 17
RA 232,1,11264
M 2,42,0
LC_ 115
RV 252,22,5392
M 2,43,1
ST 108
LK 40,248,70,0
CF 164,48,49
LK 44,250,70,2
CF 165,50,51
GS 8
CF 166,248,249
CF 167,250,251
M 3,54,0
WL 4
LO 114,16,114
LU 16,114
M 3,55,1
RA 236,1,12288
HS 25,16
M 0,20,2
LL 18,92
LL 118,93
M 0,21,3
LA 18
DW 0
LK 48,252,71,0
CF 168,40,41
LK 248,254,71,2
CF 169,42,43
GS 9
CF 170,44,45
CF 171,46,47
M 1,32,2
LC_ 118
RV 36,8,6912
M 1,33,3
RA 216,1,13312
M 2,44,2
WL 6
LO 115,17,115
LU 17,115
M 2,45,3
DW 1024
HS 25,17
GS 10
s_add_u32 s42, s44, s62
CF 172,48,49
CF 173,50,51
M 3,56,2
WL 4
LL 19,94
LL 119,95
M 3,57,3
s_addc_u32 s43, s45, 0
LA 19
RA 220,1,14336
M 0,22,4
LC_ 119
ST 112
M 0,23,5
DW 2048
CF 174,248,249
M 1,34,4
WL 5
LO 118,18,118
LU 18,118
M 1,35,5
RA 224,1,15360
)"
R"(HS 25,18
M 2,46,4
LL 16,56
LL 104,57
M 2,47,5
LA 16
CF 175,250,251
M 3,58,4
WL 3
LC_ 104
DQ 3072
M 3,59,5
s_add_u32 s42, s44, s63
RA 228,1,16384
GS 11
s_addc_u32 s43, s45, 0
Z 6,12,0
WL 4
LO 119,19,119
LU 19,119
s_nop 2
v_cndmask_b32 v52, v52, v53, s[78:79]
M 6,13,1
ST 116
HS 25,19
Z 7,24,0
LL 17,58
LL 105,59
v_cndmask_b32 v54, v54, v55, s[78:79]
M 7,25,1
LA 17
RA 232,1,17408
v_cndmask_b32 v52, v52, v54, s[80:81]
Z 8,36,0
s_add_u32 s50, s50, %[d]
LC_ 105
HX 25
v_mul_f32 v52, 0x3b2aaaab, v52
M 8,37,1
s_cmp_eq_u32 s50, %[wrap]
W1 25,1024
v_med3_f32 v53, v52, v39, v37
Z 9,48,0
WL 4
LO 104,16,104
LU 16,104
v_cmp_eq_f32 vcc, v52, v53
M 9,49,1
s_cselect_b32 s50, %[rst], s50
RA 236,1,18432
HZ 24,16
C 52,52,52
M 6,14,2
LL 18,60
LL 108,61
v_lshlrev_b32 v52, 16, v52
M 6,15,3
s_add_u32 s37, s37, 32768
LA 18
v_add_u32 v4, s39, v4
v_add_f32 v52, v36, v52
M 7,26,2
s_cmp_eq_u32 s37, s75
LC_ 108
C 52,52,52
M 7,27,3
s_cselect_b32 s37, s74, s37
RA 216,1,19456
v_lshlrev_b32 v52, 16, v52
M 8,38,2
WL 5
LO 105,17,105
LU 17,105
v_sqrt_f32 v52, v52
M 8,39,3
s_add_u32 s38, s38, 32768
HS 24,17
C 52,52,52
M 9,50,2
WL 3
LL 19,62
LL 109,63
v_lshlrev_b32 v52, 16, v52
M 9,51,3
s_cmp_eq_u32 s38, s77
LA 19
RA 220,1,20480
v_rcp_f32 v52, v52
M 6,16,4
s_cselect_b32 s38, s76, s38
LC_ 109
XL 78,384
v_mul_f32 v52, v38, v52
M 6,17,5
v_cndmask_b32 v52, 0, v52, vcc
CF 52,52,52
M 7,28,4
WL 4
LO 108,18,108
LU 18,108
M 7,29,5
s_mul_i32 s53, s51, 49152
RA 224,1,21504
HS 24,18
ds_bpermute_b32 v13, v28, v52
M 8,40,4
s_bitcmp1_b64 s[90:91], s51
LL 16,64
LL 112,65
M 8,41,5
s_cselect_b64 s[44:45], s[84:85], s[82:83]
LA 16
M 9,52,4
WL 4
LC_ 112
M 9,53,5
s_add_u32 s44, s44, s53
RA 228,1,22528
M 6,18,0
WL 5
LO 109,19,109
LU 19,109
M 6,19,1
s_addc_u32 s45, s45, 0
HS 24,19
s_add_u32 s51, s51, %[d]
s_cmp_eq_u32 s51, %[wrap]
s_cselect_b32 s51, %[rst], s51
M 7,30,0
LL 17,66
LL 113,67
M 7,31,1
WL 3
LA 17
RA 232,1,23552
XL 79,448
M 8,42,0
s_lshl_b32 s53, s52, 8
LC_ 113
XS 128
M 8,43,1
s_add_u32 s48, %[alo], s53
XS 129
XS 130
M 9,54,0
WL 3
LO 112,16,112
LU 16,112
M 9,55,1
s_addc_u32 s49, %[ahi], 0
RA 236,1,24576
HS 24,16
XS 131
M 6,20,2
s_add_u32 s52, s52, %[d]
LL 18,68
LL 116,69
s_cmp_eq_u32 s52, %[wrap]
s_cselect_b32 s52, %[rst], s52
AW 120,128
M 6,21,3
LA 18
XL 80,512
AW 121,129
AW 122,130
M 7,32,2
LC_ 116
XL 81,576
AW 123,131
M 7,33,3
v_add_u32 v1, s37, v0
XS 132
M 8,44,2
WL 3
LO 113,17,113
LU 17,113
XS 133
M 8,45,3
RA 216,1,0
HS 24,17
XS 134
XS 135
M 9,56,2
WL 3
LL 19,70
LL 117,71
AW 124,132
M 9,57,3
WV 22
LA 19
XL 82,640
AW 125,133
M 6,22,4
s_barrier
LC_ 117
v_and_b32 v22, 0xfffffcff, v1
AW 126,134
M 6,23,5
s_sub_u32 s39, 0, s39
RA 220,1,1024
AW 127,135
v_min_u32 v35, v35, v13
M 7,34,4
WL 4
LO 116,18,116
LU 18,116
XS 136
M 7,35,5
RV 252,22,5120
HS 24,18
XS 137
M 8,46,4
LL 16,80
LL 106,81
XS 138
XS 139
M 8,47,5
LA 16
RA 224,1,2048
AW 128,136
M 9,58,4
WL 3
LC_ 106
ds_read_b64 v[14:15], v5 offset:0
AW 129,137
M 9,59,5
WL 4
RA 228,1,3072
v_subrev_u32 v5, s39, v5
AW 130,138
Z 0,12,0
LO 117,19,117
LU 19,117
AW 131,139
XS 140
M 0,13,1
XL 83,704
HS 24,19
XS 141
Z 1,24,0
s_mul_i32 s53, s50, 25600
LL 17,82
LL 107,83
XS 142
M 1,25,1
s_add_u32 s40, %[wlo], s53
LA 17
RA 232,1,4096
XS 143
Z 2,36,0
s_addc_u32 s41, %[whi], 0
LC_ 107
HX 24
AW 132,140
AW 133,141
M 2,37,1
s_mov_b32 m0, s38
W1 24,0
AW 134,142
Z 3,48,0
WL 6
LO 106,16,106
LU 16,106
AW 135,143
M 3,49,1
HZ 25,16
XS 144
Z 4,60,0
WL 4
LL 18,84
LL 110,85
XS 145
XS 146
M 4,61,1
LA 18
RA 236,1,6144
XS 147
M 0,14,2
LC_ 110
v_add_f32 v14, v14, v15
AW 136,144
M 0,15,3
s_add_u32 s42, s44, s60
v_add_f32_dpp v14, v14, v14 quad_perm:[1,0,3,2] row_mask:0xf bank_mask:0xf
AW 137,145
AW 138,146
M 1,26,2
WL 4
LO 107,17,107
LU 17,107
AW 139,147
M 1,27,3
s_addc_u32 s43, s45, 0
RA 216,1,7168
HS 25,17
XS 148
M 2,38,2
LL 19,86
LL 111,87
XS 149
M 2,39,3
LA 19
DW 0
XS 150
XS 151
M 3,50,2
LC_ 111
ST 104
v_add_f32_dpp v14, v14, v14 quad_perm:[2,3,0,1] row_mask:0xf bank_mask:0xf
AW 140,148
M 3,51,3
v_add_f32_dpp v14, v14, v14 row_half_mirror row_mask:0xf bank_mask:0xf
AW 141,149
M 4,62,2
WL 3
LO 110,18,110
LU 18,110
AW 142,150
M 4,63,3
RA 220,1,8192
HS 25,18
AW 143,151
XS 152
M 0,16,4
LL 16,88
LL 114,89
v_cvt_f64_f32 v[14:15], v14
XS 153
M 0,17,5
LA 16
s_mov_b64 exec, s[54:55]
SP
s_mov_b64 exec, -1
XS 154
M 1,28,4
LC_ 114
XS 155
M 1,29,5
s_add_u32 s42, s44, s61
RA 224,1,9216
AW 144,152
AW 145,153
M 2,40,4
WL 4
LO 111,19,111
LU 19,111
AW 146,154
M 2,41,5
s_addc_u32 s43, s45, 0
DW 1024
HS 25,19
AW 147,155
M 3,52,4
LL 17,90
LL 115,91
XS 156
XS 157
M 3,53,5
LA 17
ST 108
XS 158
M 4,64,4
WL 4
LC_ 115
XS 159
M 4,65,5
RA 228,1,10240
AW 148,156
M 0,18,0
WL 4
LO 114,16,114
LU 16,114
AW 149,157
AW 150,158
M 0,19,1
HS 25,16
AW 151,159
M 1,30,0
LL 18,92
LL 118,93
XS 160
M 1,31,1
LA 18
RA 232,1,11264
XS 161
M 2,42,0
WV 28
LC_ 118
RV 252,22,5392
XS 162
XS 163
M 2,43,1
s_add_u32 s42, s44, s62
BW 72
AW 152,160
M 3,54,0
WL 6
LO 115,17,115
LU 17,115
AW 153,161
)"
R"(M 3,55,1
s_addc_u32 s43, s45, 0
DW 2048
HS 25,17
AW 154,162
M 4,66,0
WL 5
LL 19,94
LL 119,95
AW 155,163
RV 40,8,6144
XS 164
M 4,67,1
LA 19
RA 236,1,12288
XS 165
M 0,20,2
LC_ 119
ST 112
RV 44,8,6208
XS 166
M 0,21,3
RV 48,8,6272
BR 72
XS 167
AW 156,164
M 1,32,2
WL 9
LO 118,18,118
LU 18,118
AW 157,165
M 1,33,3
RA 216,1,13312
HS 25,18
AW 158,166
M 2,44,2
WV 29
BW 73
AW 159,167
M 2,45,3
BR 73
XS 168
XS 169
M 3,56,2
WV 28
XS 170
XS 171
M 3,57,3
s_add_u32 s40, s40, 3072
AW 160,168
AW 161,169
M 4,68,2
WL 3
LO 119,19,119
LU 19,119
M 4,69,3
s_addc_u32 s41, s41, 0
RA 220,1,14336
HS 25,19
AW 162,170
LZ 40,72
s_add_u32 m0, s38, 3072
BW 74
M 0,22,4
s_add_u32 s42, s44, s63
RV 40,8,6336
BR 74
AW 163,171
M 0,23,5
s_addc_u32 s43, s45, 0
ST 116
HX 25
XS 172
M 1,34,4
W1 25,1024
XS 173
M 1,35,5
WL 5
RA 224,1,15360
v_add_u32 v4, s39, v4
XS 174
XS 175
LS 44,73
WV 28
BW 75
M 2,46,4
LL 16,56
LL 104,57
AW 164,172
M 2,47,5
LA 16
RV 44,8,6400
AW 165,173
M 3,58,4
LC_ 104
BR 75
AW 166,174
AW 167,175
M 3,59,5
WL 7
LS 48,74
WV 27
BW 76
M 4,70,4
DW 0
RV 48,8,6464
M 4,71,5
RA 228,1,16384
BR 76
Z 6,12,0
LL 17,58
LL 105,59
M 6,13,1
LA 17
Z 7,24,0
LC_ 105
M 7,25,1
RA 232,1,17408
Z 8,36,0
WL 8
LO 104,16,104
LU 16,104
M 8,37,1
WL 7
HZ 24,16
LS 40,75
WV 27
BW 77
Z 9,48,0
LL 18,60
LL 108,61
M 9,49,1
LA 18
RV 40,8,6528
Z 10,60,0
WL 7
LC_ 108
BR 77
M 10,61,1
WL 8
RA 236,1,18432
LS 44,76
WV 14
BW 78
M 6,14,2
WL 8
LO 105,17,105
LU 17,105
M 6,15,3
DW 1024
HS 24,17
M 7,26,2
s_add_u32 s37, s37, 32768
LL 19,62
LL 109,63
M 7,27,3
s_cmp_eq_u32 s37, s75
LA 19
RA 216,1,19456
M 8,38,2
s_cselect_b32 s37, s74, s37
LC_ 109
RV 44,8,6592
M 8,39,3
BR 78
M 9,50,2
WL 8
LO 108,18,108
LU 18,108
M 9,51,3
WL 7
HS 24,18
LS 48,77
WV 14
BW 79
M 10,62,2
WL 7
LL 16,64
LL 112,65
M 10,63,3
LA 16
RA 220,1,20480
M 6,16,4
LC_ 112
RV 48,8,6656
M 6,17,5
WV 13
DW 2048
BR 79
M 7,28,4
WL 6
LO 109,19,109
LU 19,109
M 7,29,5
s_mul_i32 s53, s51, 49152
RA 224,1,21504
HS 24,19
LS 40,78
s_bitcmp1_b64 s[90:91], s51
BW 80
M 8,40,4
s_cselect_b64 s[44:45], s[84:85], s[82:83]
LL 17,66
LL 113,67
M 8,41,5
s_add_u32 s44, s44, s53
LA 17
RV 40,8,6720
M 9,52,4
s_addc_u32 s45, s45, 0
LC_ 113
BR 80
M 9,53,5
s_add_u32 s51, s51, %[d]
M 10,64,4
WL 8
LO 112,16,112
LU 16,112
M 10,65,5
s_cmp_eq_u32 s51, %[wrap]
RA 228,1,22528
HS 24,16
M 6,18,0
s_cselect_b32 s51, %[rst], s51
LL 18,68
LL 116,69
M 6,19,1
WL 7
LA 18
LS 44,79
WV 13
BW 81
M 7,30,0
LC_ 116
RV 44,8,6784
M 7,31,1
RA 232,1,23552
BR 81
M 8,42,0
WL 8
LO 113,17,113
LU 17,113
M 8,43,1
WL 7
DQ 3072
HS 24,17
LS 48,80
WV 13
BW 82
M 9,54,0
LL 19,70
LL 117,71
M 9,55,1
s_lshl_b32 s53, s52, 8
LA 19
RV 48,8,6848
M 10,66,0
WL 9
LC_ 117
RA 240,7,0
M 10,67,1
s_add_u32 s48, %[alo], s53
RA 236,1,24576
BR 82
M 6,20,2
WL 7
LO 116,18,116
LU 18,116
M 6,21,3
s_addc_u32 s49, %[ahi], 0
HS 24,18
v_add_u32 v1, s37, v0
M 7,32,2
s_add_u32 s52, s52, %[d]
LL 16,72
LL 120,73
M 7,33,3
s_cmp_eq_u32 s52, %[wrap]
LA 16
RA 216,1,0
LS 40,81
s_cselect_b32 s52, %[rst], s52
WV 12
BW 83
M 8,44,2
s_add_u32 s50, s50, %[d]
LC_ 120
RA 244,7,256
M 8,45,3
s_cmp_eq_u32 s50, %[wrap]
BR 83
v_and_b32 v22, 0xfffffcff, v1
M 9,56,2
WL 9
LO 117,19,117
LU 19,117
M 9,57,3
s_cselect_b32 s50, %[rst], s50
HS 24,19
M 10,68,2
WL 7
LL 17,74
LL 121,75
M 10,69,3
WL 6
LA 17
RA 220,1,1024
LS 44,82
RA 248,7,512
M 6,22,4
LC_ 121
RV 252,22,5120
M 6,23,5
M 7,34,4
WL 5
LO 120,16,120
LU 16,120
M 7,35,5
s_barrier
RA 224,1,2048
HS 24,16
LS 48,83
s_sub_u32 s39, 0, s39
RA 252,7,768
M 8,46,4
LL 18,80
LL 106,81
M 8,47,5
s_add_u32 s38, s38, 32768
LA 18
ds_read_b64 v[14:15], v5 offset:0
M 9,58,4
s_cmp_eq_u32 s38, s77
LC_ 106
v_subrev_u32 v5, s39, v5
M 9,59,5
s_cselect_b32 s38, s76, s38
v_mul_f32 v36, 0xbb2aaaab, v36
v_mul_f32 v37, 0xbb2aaaab, v37
M 10,70,4
WL 6
LO 121,17,121
LU 17,121
M 10,71,5
WL 5
RA 228,1,3072
HS 24,17
Z 0,12,0
LL 19,82
LL 107,83
M 0,13,1
s_mul_i32 s53, s50, 25600
LA 19
HX 24
Z 1,24,0
s_add_u32 s40, %[wlo], s53
LC_ 107
W1 24,0
v_mul_f32 v38, 0xbb2aaaab, v38
v_mul_f32 v39, 0xbb2aaaab, v39
M 1,25,1
s_addc_u32 s41, %[whi], 0
RA 232,1,4096
LK 40,240,72,0
LK 44,242,72,2
s_mov_b32 m0, s38
RA 240,7,1024
LK 48,244,73,0
s_nop 0
CF 128,40,41
LK 248,246,73,2
RA 244,7,1280
CF 129,42,43
Z 2,36,0
WL 7
LO 106,18,106
LU 18,106
M 2,37,1
HZ 25,18
v_add_f32 v14, v14, v15
Z 3,48,0
LL 16,84
LL 110,85
M 3,49,1
LA 16
DW 0
Z 4,60,0
WL 7
LC_ 110
v_add_f32_dpp v14, v14, v14 quad_perm:[1,0,3,2] row_mask:0xf bank_mask:0xf
CF 130,44,45
M 4,61,1
s_add_u32 s42, s44, s60
RA 236,1,6144
v_add_f32_dpp v14, v14, v14 quad_perm:[2,3,0,1] row_mask:0xf bank_mask:0xf
CF 131,46,47
LK 40,248,74,0
CF 132,48,49
LK 44,250,74,2
s_addc_u32 s43, s45, 0
)"
R"(RA 248,7,1536
CF 133,50,51
GZ 0
CF 134,248,249
CF 135,250,251
LK 48,252,75,0
CF 136,40,41
LK 248,254,75,2
RA 252,7,1792
CF 137,42,43
GS 1
WL 6
CF 138,44,45
CF 139,46,47
LK 40,240,76,0
CF 140,48,49
LK 44,242,76,2
RA 240,7,2048
CF 141,50,51
GS 2
CF 142,248,249
CF 143,250,251
M 0,14,2
LO 107,19,107
LU 19,107
M 0,15,3
ST 104
HS 25,19
M 1,26,2
LL 17,86
LL 111,87
M 1,27,3
WL 6
LA 17
RA 216,1,7168
LK 48,244,77,0
CF 144,40,41
LK 248,246,77,2
RA 244,7,2304
CF 145,42,43
GS 3
CF 146,44,45
CF 147,46,47
M 2,38,2
LC_ 111
DW 1024
M 2,39,3
v_add_f32_dpp v14, v14, v14 row_half_mirror row_mask:0xf bank_mask:0xf
M 3,50,2
WL 8
LO 110,16,110
LU 16,110
M 3,51,3
WL 6
HS 25,16
v_cvt_f64_f32 v[14:15], v14
LK 40,248,78,0
CF 148,48,49
LK 44,250,78,2
RA 248,7,2560
CF 149,50,51
GS 4
CF 150,248,249
CF 151,250,251
M 4,62,2
LL 18,88
LL 114,89
M 4,63,3
LA 18
RA 220,1,8192
M 0,16,4
LC_ 114
s_mov_b64 exec, s[54:55]
SP
s_mov_b64 exec, -1
M 0,17,5
DW 2048
M 1,28,4
WL 4
LO 111,17,111
LU 17,111
M 1,29,5
s_add_u32 s42, s44, s61
RA 224,1,9216
HS 25,17
LK 48,252,79,0
CF 152,40,41
LK 248,254,79,2
s_addc_u32 s43, s45, 0
RA 252,7,2816
CF 153,42,43
GS 5
s_add_u32 s40, s40, 3072
CF 154,44,45
CF 155,46,47
M 2,40,4
s_addc_u32 s41, s41, 0
LL 19,90
LL 115,91
M 2,41,5
s_add_u32 m0, s38, 3072
LA 19
ST 108
M 3,52,4
LC_ 115
M 3,53,5
LK 40,240,80,0
CF 156,48,49
LK 44,242,80,2
CF 157,50,51
GS 6
CF 158,248,249
CF 159,250,251
M 4,64,4
WL 4
LO 114,18,114
LU 18,114
M 4,65,5
RA 228,1,10240
HS 25,18
M 0,18,0
LL 16,92
LL 118,93
M 0,19,1
LA 16
DW 0
LK 48,244,81,0
CF 160,40,41
LK 248,246,81,2
CF 161,42,43
GS 7
CF 162,44,45
CF 163,46,47
M 1,30,0
LC_ 118
M 1,31,1
s_add_u32 s42, s44, s62
RA 232,1,11264
M 2,42,0
WL 4
LO 115,19,115
LU 19,115
M 2,43,1
s_addc_u32 s43, s45, 0
RV 252,22,5392
HS 25,19
M 3,54,0
LL 17,94
LL 119,95
M 3,55,1
LA 17
DW 1024
LK 40,248,82,0
CF 164,48,49
LK 44,250,82,2
CF 165,50,51
GS 8
CF 166,248,249
CF 167,250,251
M 4,66,0
WL 5
LC_ 119
ST 112
M 4,67,1
RA 236,1,12288
M 0,20,2
WL 5
LO 118,16,118
LU 16,118
M 0,21,3
DW 2048
HS 25,16
LK 48,252,83,0
CF 168,40,41
LK 248,254,83,2
CF 169,42,43
GS 9
CF 170,44,45
CF 171,46,47
M 1,32,2
LL 18,96
LL 122,97
M 1,33,3
LA 18
RA 216,1,13312
M 2,44,2
LC_ 122
RV 36,8,6912
M 2,45,3
DQ 3072
GS 10
s_add_u32 s42, s44, s63
CF 172,48,49
M 3,56,2
WL 5
LO 119,17,119
LU 17,119
M 3,57,3
s_addc_u32 s43, s45, 0
ST 116
HS 25,17
M 4,68,2
WL 4
LL 19,98
LL 123,99
M 4,69,3
LA 19
RA 220,1,14336
M 0,22,4
s_add_u32 s50, s50, %[d]
LC_ 123
CF 173,50,51
M 0,23,5
s_cmp_eq_u32 s50, %[wrap]
CF 174,248,249
CF 175,250,251
M 1,34,4
WL 4
LO 122,18,122
LU 18,122
M 1,35,5
s_cselect_b32 s50, %[rst], s50
RA 224,1,15360
HS 25,18
GS 11
M 2,46,4
LL 16,56
LL 104,57
M 2,47,5
LA 16
s_nop 2
v_cndmask_b32 v52, v52, v53, s[78:79]
M 3,58,4
s_add_u32 s38, s38, 32768
LC_ 104
v_cndmask_b32 v54, v54, v55, s[78:79]
M 3,59,5
s_cmp_eq_u32 s38, s77
v_cndmask_b32 v52, v52, v54, s[80:81]
v_mul_f32 v52, 0x3b2aaaab, v52
M 4,70,4
WL 3
LO 123,19,123
LU 19,123
M 4,71,5
s_cselect_b32 s38, s76, s38
RA 228,1,16384
HS 25,19
Z 6,12,0
s_add_u32 s42, s44, s64
LL 17,58
LL 105,59
M 6,13,1
s_addc_u32 s43, s45, 0
LA 17
ST 120
Z 7,24,0
s_mul_i32 s53, s50, 25600
LC_ 105
HX 25
M 7,25,1
s_add_u32 s40, %[wlo], s53
RA 232,1,17408
v_med3_f32 v53, v52, v39, v37
Z 8,36,0
WL 4
LO 104,16,104
LU 16,104
M 8,37,1
s_addc_u32 s41, %[whi], 0
W1 25,1024
HZ 24,16
Z 9,48,0
s_mov_b32 m0, s38
LL 18,60
LL 108,61
M 9,49,1
LA 18
DW 0
v_cmp_eq_f32 vcc, v52, v53
Z 10,60,0
WL 5
LC_ 108
v_add_u32 v4, s39, v4
C 52,52,52
M 10,61,1
RA 236,1,18432
v_lshlrev_b32 v52, 16, v52
M 6,14,2
WL 5
LO 105,17,105
LU 17,105
M 6,15,3
s_add_u32 s37, s37, 32768
HS 24,17
v_add_f32 v52, v36, v52
M 7,26,2
s_cmp_eq_u32 s37, s75
LL 19,62
LL 109,63
C 52,52,52
M 7,27,3
s_cselect_b32 s37, s74, s37
LA 19
RA 216,1,19456
v_lshlrev_b32 v52, 16, v52
M 8,38,2
LC_ 109
DW 1024
v_sqrt_f32 v52, v52
M 8,39,3
C 52,52,52
v_lshlrev_b32 v52, 16, v52
M 9,50,2
WL 4
LO 108,18,108
LU 18,108
M 9,51,3
DW 2048
HS 24,18
M 10,62,2
WL 3
LL 16,64
LL 112,65
v_rcp_f32 v52, v52
M 10,63,3
LA 16
RA 220,1,20480
v_mul_f32 v52, v38, v52
M 6,16,4
LC_ 112
v_cndmask_b32 v52, 0, v52, vcc
M 6,17,5
CF 52,52,52
M 7,28,4
WL 3
LO 109,19,109
LU 19,109
M 7,29,5
s_mul_i32 s53, s51, 49152
RA 224,1,21504
HS 24,19
ds_bpermute_b32 v13, v28, v52
M 8,40,4
s_bitcmp1_b64 s[90:91], s51
LL 17,66
LL 113,67
M 8,41,5
s_cselect_b64 s[44:45], s[84:85], s[82:83]
LA 17
M 9,52,4
s_add_u32 s44, s44, s53
LC_ 113
M 9,53,5
s_addc_u32 s45, s45, 0
M 10,64,4
WL 4
LO 112,16,112
LU 16,112
M 10,65,5
s_add_u32 s51, s51, %[d]
RA 228,1,22528
HS 24,16
M 6,18,0
s_cmp_eq_u32 s51, %[wrap]
LL 18,68
LL 116,69
M 6,19,1
s_cselect_b32 s51, %[rst], s51
LA 18
s_add_u32 s40, s40, 3072
s_addc_u32 s41, s41, 0
s_add_u32 m0, s38, 3072
M 7,30,0
LC_ 116
DW 0
M 7,31,1
WL 5
RA 232,1,23552
XS 128
M 8,42,0
WL 4
LO 113,17,113
LU 17,113
XS 129
M 8,43,1
DW 1024
HS 24,17
XS 130
M 9,54,0
LL 19,70
LL 117,71
XS 131
M 9,55,1
s_lshl_b32 s53, s52, 8
LA 19
AW 168,128
M 10,66,0
WL 4
LC_ 117
)"
R"(AW 169,129
M 10,67,1
s_add_u32 s48, %[alo], s53
RA 236,1,24576
AW 170,130
M 6,20,2
WL 4
LO 116,18,116
LU 18,116
AW 171,131
M 6,21,3
s_addc_u32 s49, %[ahi], 0
HS 24,18
v_add_u32 v1, s37, v0
XS 132
M 7,32,2
s_add_u32 s52, s52, %[d]
LL 16,72
LL 120,73
XS 133
M 7,33,3
s_cmp_eq_u32 s52, %[wrap]
LA 16
RA 216,1,0
XS 134
M 8,44,2
s_cselect_b32 s52, %[rst], s52
LC_ 120
DW 2048
XS 135
M 8,45,3
DQ 3072
v_and_b32 v22, 0xfffffcff, v1
AW 172,132
M 9,56,2
WL 4
LO 117,19,117
LU 19,117
AW 173,133
M 9,57,3
HS 24,19
AW 174,134
M 10,68,2
WL 3
LL 17,74
LL 121,75
AW 175,135
M 10,69,3
WV 20
LA 17
RA 220,1,1024
v_min_u32 v35, v35, v13
M 6,22,4
s_add_u32 s50, s50, %[d]
LC_ 121
RV 252,22,5120
XS 136
M 6,23,5
s_cmp_eq_u32 s50, %[wrap]
XS 137
XS 138
M 7,34,4
WL 4
LO 120,16,120
LU 16,120
M 7,35,5
s_cselect_b32 s50, %[rst], s50
RA 224,1,2048
HS 24,16
XS 139
M 8,46,4
s_barrier
LL 18,80
LL 106,81
AW 176,136
M 8,47,5
s_sub_u32 s39, 0, s39
LA 18
ds_read_b64 v[14:15], v5 offset:0
AW 177,137
M 9,58,4
s_add_u32 s38, s38, 32768
LC_ 106
v_subrev_u32 v5, s39, v5
AW 178,138
M 9,59,5
s_cmp_eq_u32 s38, s77
AW 179,139
XS 140
M 10,70,4
WL 4
LO 121,17,121
LU 17,121
M 10,71,5
s_cselect_b32 s38, s76, s38
RA 228,1,3072
HS 24,17
XS 141
Z 0,12,0
LL 19,82
LL 107,83
XS 142
M 0,13,1
LA 19
HX 24
XS 143
Z 1,24,0
LC_ 107
W1 24,0
AW 180,140
M 1,25,1
RA 232,1,4096
AW 181,141
Z 2,36,0
WL 5
LO 106,18,106
LU 18,106
AW 182,142
M 2,37,1
HZ 25,18
v_add_f32 v14, v14, v15
AW 183,143
Z 3,48,0
LL 16,84
LL 110,85
XS 144
M 3,49,1
LA 16
v_add_f32_dpp v14, v14, v14 quad_perm:[1,0,3,2] row_mask:0xf bank_mask:0xf
XS 145
Z 4,60,0
LC_ 110
v_add_f32_dpp v14, v14, v14 quad_perm:[2,3,0,1] row_mask:0xf bank_mask:0xf
XS 146
M 4,61,1
s_add_u32 s42, s44, s60
RA 236,1,6144
v_add_f32_dpp v14, v14, v14 row_half_mirror row_mask:0xf bank_mask:0xf
XS 147
Z 5,72,0
WL 5
LO 107,19,107
LU 19,107
AW 184,144
M 5,73,1
s_addc_u32 s43, s45, 0
ST 104
HS 25,19
AW 185,145
M 0,14,2
LL 17,86
LL 111,87
AW 186,146
M 0,15,3
LA 17
v_cvt_f64_f32 v[14:15], v14
AW 187,147
M 1,26,2
LC_ 111
s_mov_b64 exec, s[54:55]
SP
s_mov_b64 exec, -1
XS 148
M 1,27,3
RA 216,1,7168
XS 149
M 2,38,2
WL 4
LO 110,16,110
LU 16,110
XS 150
M 2,39,3
HS 25,16
XS 151
M 3,50,2
LL 18,88
LL 114,89
AW 188,148
M 3,51,3
LA 18
AW 189,149
M 4,62,2
LC_ 114
AW 190,150
M 4,63,3
s_add_u32 s42, s44, s61
RA 220,1,8192
AW 191,151
M 5,74,2
WL 4
LO 111,17,111
LU 17,111
XS 152
M 5,75,3
s_addc_u32 s43, s45, 0
HS 25,17
XS 153
M 0,16,4
s_mov_b32 m0, %[pm]
LL 19,90
LL 115,91
XS 154
M 0,17,5
LA 19
DF
XS 155
M 1,28,4
LC_ 115
ST 108
AW 192,152
M 1,29,5
RA 224,1,9216
AW 193,153
M 2,40,4
WL 4
LO 114,18,114
LU 18,114
AW 194,154
M 2,41,5
HS 25,18
AW 195,155
M 3,52,4
LL 16,92
LL 118,93
XS 156
M 3,53,5
LA 16
XS 157
M 4,64,4
LC_ 118
XS 158
M 4,65,5
s_add_u32 s42, s44, s62
RA 228,1,10240
XS 159
M 5,76,4
WL 4
LO 115,19,115
LU 19,115
AW 196,156
M 5,77,5
s_addc_u32 s43, s45, 0
ST 112
HS 25,19
AW 197,157
M 0,18,0
LL 17,94
LL 119,95
AW 198,158
M 0,19,1
LA 17
AW 199,159
M 1,30,0
LC_ 119
XS 160
M 1,31,1
RA 232,1,11264
XS 161
M 2,42,0
WL 4
LO 118,16,118
LU 16,118
XS 162
M 2,43,1
RV 252,22,5392
HS 25,16
XS 163
M 3,54,0
LL 18,96
LL 122,97
AW 200,160
M 3,55,1
LA 18
AW 201,161
M 4,66,0
LC_ 122
AW 202,162
M 4,67,1
s_add_u32 s42, s44, s63
RA 236,1,12288
AW 203,163
M 5,78,0
WL 5
LO 119,17,119
LU 17,119
XS 164
M 5,79,1
s_addc_u32 s43, s45, 0
ST 116
HS 25,17
XS 165
M 0,20,2
LL 19,98
LL 123,99
XS 166
M 0,21,3
LA 19
XS 167
M 1,32,2
LC_ 123
AW 204,164
M 1,33,3
RA 216,1,13312
AW 205,165
M 2,44,2
WL 4
LO 122,18,122
LU 18,122
AW 206,166
M 2,45,3
HS 25,18
AW 207,167
M 3,56,2
XS 168
XS 169
M 3,57,3
XS 170
XS 171
M 4,68,2
AW 208,168
AW 209,169
M 4,69,3
s_add_u32 s42, s44, s64
RA 220,1,14336
AW 210,170
M 5,80,2
WL 2
LO 123,19,123
LU 19,123
M 5,81,3
s_addc_u32 s43, s45, 0
ST 120
HS 25,19
M 0,22,4
HX 25
AW 211,171
M 0,23,5
W1 25,1024
XS 172
M 1,34,4
v_add_u32 v4, s39, v4
XS 173
M 1,35,5
RA 224,1,15360
XS 174
M 2,46,4
LL 16,56
LL 104,57
M 2,47,5
LA 16
XS 175
M 3,58,4
LC_ 104
AW 212,172
M 3,59,5
AW 213,173
AW 214,174
M 4,70,4
AW 215,175
M 4,71,5
RA 228,1,16384
M 5,82,4
WL 5
LL 17,58
LL 105,59
M 5,83,5
LA 17
Z 6,12,0
LC_ 105
M 6,13,1
Z 7,24,0
WL 3
LO 104,16,104
LU 16,104
M 7,25,1
RA 232,1,17408
HZ 24,16
Z 8,36,0
LL 18,60
LL 108,61
M 8,37,1
s_mul_i32 s53, s50, 25600
LA 18
Z 9,48,0
s_add_u32 s40, %[wlo], s53
LC_ 108
M 9,49,1
s_addc_u32 s41, %[whi], 0
Z 10,60,0
WL 3
LO 105,17,105
LU 17,105
M 10,61,1
s_mov_b32 m0, s38
RA 236,1,18432
HS 24,17
Z 11,72,0
LL 19,62
LL 109,63
M 11,73,1
)"
R"(LA 19
DW 0
M 6,14,2
LC_ 109
M 6,15,3
M 7,26,2
WL 3
LO 108,18,108
LU 18,108
M 7,27,3
s_add_u32 s37, s37, 32768
RA 216,1,19456
HS 24,18
M 8,38,2
s_cmp_eq_u32 s37, s75
LL 16,64
LL 112,65
M 8,39,3
s_cselect_b32 s37, s74, s37
LA 16
DW 1024
M 9,50,2
LC_ 112
M 9,51,3
M 10,62,2
WL 3
LO 109,19,109
LU 19,109
M 10,63,3
RA 220,1,20480
HS 24,19
M 11,74,2
LL 17,66
LL 113,67
M 11,75,3
LA 17
DW 2048
M 6,16,4
LC_ 113
M 6,17,5
M 7,28,4
WL 3
LO 112,16,112
LU 16,112
M 7,29,5
s_mul_i32 s53, s51, 49152
RA 224,1,21504
HS 24,16
M 8,40,4
s_bitcmp1_b64 s[90:91], s51
LL 18,68
LL 116,69
M 8,41,5
s_cselect_b64 s[44:45], s[84:85], s[82:83]
LA 18
M 9,52,4
s_add_u32 s44, s44, s53
LC_ 116
M 9,53,5
s_addc_u32 s45, s45, 0
M 10,64,4
WL 3
LO 113,17,113
LU 17,113
M 10,65,5
s_add_u32 s51, s51, %[d]
RA 228,1,22528
HS 24,17
M 11,76,4
LL 19,70
LL 117,71
M 11,77,5
s_cmp_eq_u32 s51, %[wrap]
LA 19
M 6,18,0
s_cselect_b32 s51, %[rst], s51
LC_ 117
M 6,19,1
s_add_u32 s40, s40, 3072
M 7,30,0
WL 3
LO 116,18,116
LU 18,116
M 7,31,1
s_addc_u32 s41, s41, 0
RA 232,1,23552
HS 24,18
M 8,42,0
s_add_u32 m0, s38, 3072
LL 16,72
LL 120,73
M 8,43,1
LA 16
DW 0
M 9,54,0
LC_ 120
DW 1024
M 9,55,1
M 10,66,0
WL 3
LO 117,19,117
LU 19,117
M 10,67,1
RA 236,1,24576
HS 24,19
M 11,78,0
LL 17,74
LL 121,75
M 11,79,1
LA 17
DW 2048
M 6,20,2
LC_ 121
M 6,21,3
s_lshl_b32 s53, s52, 8
v_add_u32 v1, s37, v0
M 7,32,2
WL 3
LO 120,16,120
LU 16,120
M 7,33,3
s_add_u32 s48, %[alo], s53
RA 216,1,0
HS 24,16
M 8,44,2
s_addc_u32 s49, %[ahi], 0
LL 18,76
LL 124,77
M 8,45,3
s_add_u32 s52, s52, %[d]
LA 18
v_and_b32 v22, 0xfffffcff, v1
M 9,56,2
s_cmp_eq_u32 s52, %[wrap]
LC_ 124
M 9,57,3
s_cselect_b32 s52, %[rst], s52
DQ 3072
M 10,68,2
WL 3
LO 121,17,121
LU 17,121
M 10,69,3
s_add_u32 s50, s50, %[d]
RA 220,1,1024
HS 24,17
M 11,80,2
LL 19,78
LL 125,79
M 11,81,3
s_cmp_eq_u32 s50, %[wrap]
LA 19
RV 252,22,5120
M 6,22,4
s_cselect_b32 s50, %[rst], s50
LC_ 125
M 6,23,5
WV 23
M 7,34,4
WL 4
LO 124,18,124
LU 18,124
M 7,35,5
s_add_u32 s38, s38, 32768
RA 224,1,2048
HS 24,18
M 8,46,4
s_cmp_eq_u32 s38, s77
LL 16,80
LL 106,81
M 8,47,5
s_cselect_b32 s38, s76, s38
LA 16
M 9,58,4
s_barrier
LC_ 106
M 9,59,5
s_sub_u32 s39, 0, s39
M 10,70,4
WL 3
LO 125,19,125
LU 19,125
M 10,71,5
RA 228,1,3072
HS 24,19
M 11,82,4
LL 17,82
LL 107,83
M 11,83,5
LA 17
HX 24
Z 0,12,0
LC_ 107
ds_read_b64 v[14:15], v5 offset:0
M 0,13,1
W1 24,0
v_subrev_u32 v5, s39, v5
Z 1,24,0
WL 5
LO 106,16,106
LU 16,106
M 1,25,1
RA 232,1,4096
HZ 25,16
Z 2,36,0
LL 18,84
LL 110,85
M 2,37,1
LA 18
Z 3,48,0
LC_ 110
M 3,49,1
s_add_u32 s42, s44, s60
Z 4,60,0
WL 5
LO 107,17,107
LU 17,107
M 4,61,1
s_addc_u32 s43, s45, 0
RA 236,1,6144
HS 25,17
Z 5,72,0
LL 19,86
LL 111,87
M 5,73,1
LA 19
ST 104
M 0,14,2
WL 6
LC_ 111
v_add_f32 v14, v14, v15
M 0,15,3
s_nop 0
v_add_f32_dpp v14, v14, v14 quad_perm:[1,0,3,2] row_mask:0xf bank_mask:0xf
M 1,26,2
WL 3
LO 110,18,110
LU 18,110
M 1,27,3
RA 216,1,7168
HS 25,18
M 2,38,2
LL 16,88
LL 114,89
M 2,39,3
LA 16
v_add_f32_dpp v14, v14, v14 quad_perm:[2,3,0,1] row_mask:0xf bank_mask:0xf
M 3,50,2
LC_ 114
v_add_f32_dpp v14, v14, v14 row_half_mirror row_mask:0xf bank_mask:0xf
M 3,51,3
s_add_u32 s42, s44, s61
v_cvt_f64_f32 v[14:15], v14
M 4,62,2
WL 3
LO 111,19,111
LU 19,111
M 4,63,3
s_addc_u32 s43, s45, 0
RA 220,1,8192
HS 25,19
M 5,74,2
LL 17,90
LL 115,91
M 5,75,3
LA 17
s_mov_b64 exec, s[54:55]
SP
s_mov_b64 exec, -1
M 0,16,4
LC_ 115
ST 108
M 0,17,5
s_mov_b32 m0, %[pm]
s_nop 0
DF
M 1,28,4
WL 3
LO 114,16,114
LU 16,114
M 1,29,5
RA 224,1,9216
HS 25,16
M 2,40,4
LL 18,92
LL 118,93
M 2,41,5
LA 18
M 3,52,4
LC_ 118
M 3,53,5
s_add_u32 s42, s44, s62
M 4,64,4
WL 3
LO 115,17,115
LU 17,115
M 4,65,5
s_addc_u32 s43, s45, 0
RA 228,1,10240
HS 25,17
M 5,76,4
LL 19,94
LL 119,95
M 5,77,5
LA 19
ST 112
M 0,18,0
LC_ 119
M 0,19,1
M 1,30,0
WL 3
LO 118,18,118
LU 18,118
M 1,31,1
RA 232,1,11264
HS 25,18
M 2,42,0
LL 16,96
LL 122,97
M 2,43,1
LA 16
RV 252,22,5392
M 3,54,0
LC_ 122
M 3,55,1
s_add_u32 s42, s44, s63
M 4,66,0
WL 4
LO 119,19,119
LU 19,119
M 4,67,1
s_addc_u32 s43, s45, 0
RA 236,1,12288
HS 25,19
M 5,78,0
LL 17,98
LL 123,99
M 5,79,1
LA 17
ST 116
M 0,20,2
LC_ 123
M 0,21,3
M 1,32,2
WL 3
LO 122,16,122
LU 16,122
M 1,33,3
RA 216,1,13312
HS 25,16
M 2,44,2
LL 18,100
LL 126,101
M 2,45,3
LA 18
M 3,56,2
LC_ 126
M 3,57,3
s_add_u32 s42, s44, s64
M 4,68,2
WL 3
LO 123,17,123
LU 17,123
M 4,69,3
s_addc_u32 s43, s45, 0
RA 220,1,14336
HS 25,17
M 5,80,2
LL 19,102
LL 127,103
M 5,81,3
LA 19
ST 120
M 0,22,4
LC_ 127
M 0,23,5
M 1,34,4
WL 3
LO 126,18,126
LU 18,126
M 1,35,5
RA 224,1,15360
HS 25,18
M 2,46,4
LL 16,56
LL 104,57
M 2,47,5
LA 16
M 3,58,4
LC_ 104
M 3,59,5
s_add_u32 s42, s44, s65
M 4,70,4
WL 3
LO 127,19,127
LU 19,127
M 4,71,5
s_addc_u32 s43, s45, 0
RA 228,1,16384
HS 25,19
)"
R"(M 5,82,4
LL 17,58
LL 105,59
M 5,83,5
LA 17
ST 124
Z 6,12,0
LC_ 105
HX 25
M 6,13,1
W1 25,1024
Z 7,24,0
WL 4
LO 104,16,104
LU 16,104
M 7,25,1
RA 232,1,17408
HZ 24,16
Z 8,36,0
LL 18,60
LL 108,61
M 8,37,1
s_mul_i32 s53, s50, 25600
LA 18
v_add_u32 v4, s39, v4
Z 9,48,0
s_add_u32 s40, %[wlo], s53
LC_ 108
M 9,49,1
s_addc_u32 s41, %[whi], 0
Z 10,60,0
WL 4
LO 105,17,105
LU 17,105
M 10,61,1
s_mov_b32 m0, s38
RA 236,1,18432
HS 24,17
Z 11,72,0
LL 19,62
LL 109,63
M 11,73,1
LA 19
DW 0
M 6,14,2
LC_ 109
M 6,15,3
M 7,26,2
WL 3
LO 108,18,108
LU 18,108
M 7,27,3
s_add_u32 s37, s37, 32768
RA 216,1,19456
HS 24,18
M 8,38,2
s_cmp_eq_u32 s37, s75
LL 16,64
LL 112,65
M 8,39,3
s_cselect_b32 s37, s74, s37
LA 16
DW 1024
M 9,50,2
LC_ 112
M 9,51,3
M 10,62,2
WL 3
LO 109,19,109
LU 19,109
M 10,63,3
RA 220,1,20480
HS 24,19
M 11,74,2
LL 17,66
LL 113,67
M 11,75,3
LA 17
DW 2048
M 6,16,4
LC_ 113
M 6,17,5
M 7,28,4
WL 3
LO 112,16,112
LU 16,112
M 7,29,5
s_mul_i32 s53, s51, 49152
RA 224,1,21504
HS 24,16
M 8,40,4
s_bitcmp1_b64 s[90:91], s51
LL 18,68
LL 116,69
M 8,41,5
s_cselect_b64 s[44:45], s[84:85], s[82:83]
LA 18
M 9,52,4
s_add_u32 s44, s44, s53
LC_ 116
M 9,53,5
s_addc_u32 s45, s45, 0
M 10,64,4
WL 3
LO 113,17,113
LU 17,113
M 10,65,5
s_add_u32 s51, s51, %[d]
RA 228,1,22528
HS 24,17
M 11,76,4
LL 19,70
LL 117,71
M 11,77,5
s_cmp_eq_u32 s51, %[wrap]
LA 19
M 6,18,0
s_cselect_b32 s51, %[rst], s51
LC_ 117
M 6,19,1
s_add_u32 s40, s40, 3072
M 7,30,0
WL 3
LO 116,18,116
LU 18,116
M 7,31,1
s_addc_u32 s41, s41, 0
RA 232,1,23552
HS 24,18
M 8,42,0
s_add_u32 m0, s38, 3072
LL 16,72
LL 120,73
M 8,43,1
LA 16
DW 0
M 9,54,0
LC_ 120
DW 1024
M 9,55,1
M 10,66,0
WL 3
LO 117,19,117
LU 19,117
M 10,67,1
RA 236,1,24576
HS 24,19
M 11,78,0
LL 17,74
LL 121,75
M 11,79,1
LA 17
DW 2048
M 6,20,2
LC_ 121
M 6,21,3
s_lshl_b32 s53, s52, 8
v_add_u32 v1, s37, v0
M 7,32,2
WL 3
LO 120,16,120
LU 16,120
M 7,33,3
s_add_u32 s48, %[alo], s53
RA 216,1,0
HS 24,16
M 8,44,2
s_addc_u32 s49, %[ahi], 0
LL 18,76
LL 124,77
M 8,45,3
s_add_u32 s52, s52, %[d]
LA 18
v_and_b32 v22, 0xfffffcff, v1
M 9,56,2
s_cmp_eq_u32 s52, %[wrap]
LC_ 124
M 9,57,3
s_cselect_b32 s52, %[rst], s52
DQ 3072
M 10,68,2
WL 3
LO 121,17,121
LU 17,121
M 10,69,3
s_add_u32 s50, s50, %[d]
RA 220,1,1024
HS 24,17
M 11,80,2
LL 19,78
LL 125,79
M 11,81,3
s_cmp_eq_u32 s50, %[wrap]
LA 19
RV 252,22,5120
M 6,22,4
s_cselect_b32 s50, %[rst], s50
LC_ 125
M 6,23,5
WV 29
M 7,34,4
WL 4
LO 124,18,124
LU 18,124
M 7,35,5
s_add_u32 s38, s38, 32768
RA 224,1,2048
HS 24,18
M 8,46,4
s_cmp_eq_u32 s38, s77
LL 16,80
LL 106,81
M 8,47,5
s_cselect_b32 s38, s76, s38
LA 16
M 9,58,4
s_barrier
LC_ 106
M 9,59,5
s_sub_u32 s39, 0, s39
M 10,70,4
WL 3
LO 125,19,125
LU 19,125
M 10,71,5
RA 228,1,3072
HS 24,19
M 11,82,4
LL 17,82
LL 107,83
M 11,83,5
s_mov_b32 s66, 28
1:
LA 17
HX 24
Z 0,12,0
LC_ 107
ds_read_b64 v[14:15], v5 offset:0
M 0,13,1
W1 24,0
v_subrev_u32 v5, s39, v5
Z 1,24,0
WL 5
LO 106,16,106
LU 16,106
M 1,25,1
RA 232,1,4096
HZ 25,16
Z 2,36,0
LL 18,84
LL 110,85
M 2,37,1
LA 18
Z 3,48,0
LC_ 110
M 3,49,1
s_add_u32 s42, s44, s60
Z 4,60,0
WL 5
LO 107,17,107
LU 17,107
M 4,61,1
s_addc_u32 s43, s45, 0
RA 236,1,6144
HS 25,17
Z 5,72,0
LL 19,86
LL 111,87
M 5,73,1
LA 19
ST 104
M 0,14,2
WL 6
LC_ 111
v_add_f32 v14, v14, v15
M 0,15,3
s_nop 0
v_add_f32_dpp v14, v14, v14 quad_perm:[1,0,3,2] row_mask:0xf bank_mask:0xf
M 1,26,2
WL 3
LO 110,18,110
LU 18,110
M 1,27,3
RA 216,1,7168
HS 25,18
M 2,38,2
LL 16,88
LL 114,89
M 2,39,3
LA 16
v_add_f32_dpp v14, v14, v14 quad_perm:[2,3,0,1] row_mask:0xf bank_mask:0xf
M 3,50,2
LC_ 114
v_add_f32_dpp v14, v14, v14 row_half_mirror row_mask:0xf bank_mask:0xf
M 3,51,3
s_add_u32 s42, s44, s61
v_cvt_f64_f32 v[14:15], v14
M 4,62,2
WL 3
LO 111,19,111
LU 19,111
M 4,63,3
s_addc_u32 s43, s45, 0
RA 220,1,8192
HS 25,19
M 5,74,2
LL 17,90
LL 115,91
M 5,75,3
LA 17
s_mov_b64 exec, s[54:55]
SP
s_mov_b64 exec, -1
M 0,16,4
LC_ 115
ST 108
M 0,17,5
s_mov_b32 m0, %[pm]
s_nop 0
DF
M 1,28,4
WL 3
LO 114,16,114
LU 16,114
M 1,29,5
RA 224,1,9216
HS 25,16
M 2,40,4
LL 18,92
LL 118,93
M 2,41,5
LA 18
M 3,52,4
LC_ 118
M 3,53,5
s_add_u32 s42, s44, s62
M 4,64,4
WL 3
LO 115,17,115
LU 17,115
M 4,65,5
s_addc_u32 s43, s45, 0
RA 228,1,10240
HS 25,17
M 5,76,4
LL 19,94
LL 119,95
M 5,77,5
LA 19
ST 112
M 0,18,0
LC_ 119
M 0,19,1
M 1,30,0
WL 3
LO 118,18,118
LU 18,118
M 1,31,1
RA 232,1,11264
HS 25,18
M 2,42,0
LL 16,96
LL 122,97
M 2,43,1
LA 16
RV 252,22,5392
M 3,54,0
LC_ 122
M 3,55,1
s_add_u32 s42, s44, s63
M 4,66,0
WL 4
LO 119,19,119
LU 19,119
M 4,67,1
s_addc_u32 s43, s45, 0
RA 236,1,12288
HS 25,19
M 5,78,0
LL 17,98
LL 123,99
M 5,79,1
LA 17
ST 116
M 0,20,2
LC_ 123
M 0,21,3
M 1,32,2
WL 3
LO 122,16,122
LU 16,122
M 1,33,3
RA 216,1,13312
HS 25,16
M 2,44,2
LL 18,100
LL 126,101
M 2,45,3
LA 18
M 3,56,2
)"
R"(LC_ 126
M 3,57,3
s_add_u32 s42, s44, s64
M 4,68,2
WL 3
LO 123,17,123
LU 17,123
M 4,69,3
s_addc_u32 s43, s45, 0
RA 220,1,14336
HS 25,17
M 5,80,2
LL 19,102
LL 127,103
M 5,81,3
LA 19
ST 120
M 0,22,4
LC_ 127
M 0,23,5
M 1,34,4
WL 3
LO 126,18,126
LU 18,126
M 1,35,5
RA 224,1,15360
HS 25,18
M 2,46,4
LL 16,56
LL 104,57
M 2,47,5
LA 16
M 3,58,4
LC_ 104
M 3,59,5
s_add_u32 s42, s44, s65
M 4,70,4
WL 3
LO 127,19,127
LU 19,127
M 4,71,5
s_addc_u32 s43, s45, 0
RA 228,1,16384
HS 25,19
M 5,82,4
LL 17,58
LL 105,59
M 5,83,5
LA 17
ST 124
Z 6,12,0
LC_ 105
HX 25
M 6,13,1
W1 25,1024
Z 7,24,0
WL 4
LO 104,16,104
LU 16,104
M 7,25,1
RA 232,1,17408
HZ 24,16
Z 8,36,0
LL 18,60
LL 108,61
M 8,37,1
s_mul_i32 s53, s50, 25600
LA 18
v_add_u32 v4, s39, v4
Z 9,48,0
s_add_u32 s40, %[wlo], s53
LC_ 108
M 9,49,1
s_addc_u32 s41, %[whi], 0
Z 10,60,0
WL 4
LO 105,17,105
LU 17,105
M 10,61,1
s_mov_b32 m0, s38
RA 236,1,18432
HS 24,17
Z 11,72,0
LL 19,62
LL 109,63
M 11,73,1
LA 19
DW 0
M 6,14,2
LC_ 109
M 6,15,3
M 7,26,2
WL 3
LO 108,18,108
LU 18,108
M 7,27,3
s_add_u32 s37, s37, 32768
RA 216,1,19456
HS 24,18
M 8,38,2
s_cmp_eq_u32 s37, s75
LL 16,64
LL 112,65
M 8,39,3
s_cselect_b32 s37, s74, s37
LA 16
DW 1024
M 9,50,2
LC_ 112
M 9,51,3
M 10,62,2
WL 3
LO 109,19,109
LU 19,109
M 10,63,3
RA 220,1,20480
HS 24,19
M 11,74,2
LL 17,66
LL 113,67
M 11,75,3
LA 17
DW 2048
M 6,16,4
LC_ 113
M 6,17,5
M 7,28,4
WL 3
LO 112,16,112
LU 16,112
M 7,29,5
s_mul_i32 s53, s51, 49152
RA 224,1,21504
HS 24,16
M 8,40,4
s_bitcmp1_b64 s[90:91], s51
LL 18,68
LL 116,69
M 8,41,5
s_cselect_b64 s[44:45], s[84:85], s[82:83]
LA 18
M 9,52,4
s_add_u32 s44, s44, s53
LC_ 116
M 9,53,5
s_addc_u32 s45, s45, 0
M 10,64,4
WL 3
LO 113,17,113
LU 17,113
M 10,65,5
s_add_u32 s51, s51, %[d]
RA 228,1,22528
HS 24,17
M 11,76,4
LL 19,70
LL 117,71
M 11,77,5
s_cmp_eq_u32 s51, %[wrap]
LA 19
M 6,18,0
s_cselect_b32 s51, %[rst], s51
LC_ 117
M 6,19,1
s_add_u32 s40, s40, 3072
M 7,30,0
WL 3
LO 116,18,116
LU 18,116
M 7,31,1
s_addc_u32 s41, s41, 0
RA 232,1,23552
HS 24,18
M 8,42,0
s_add_u32 m0, s38, 3072
LL 16,72
LL 120,73
M 8,43,1
LA 16
DW 0
M 9,54,0
LC_ 120
DW 1024
M 9,55,1
M 10,66,0
WL 3
LO 117,19,117
LU 19,117
M 10,67,1
RA 236,1,24576
HS 24,19
M 11,78,0
LL 17,74
LL 121,75
M 11,79,1
LA 17
DW 2048
M 6,20,2
LC_ 121
M 6,21,3
s_lshl_b32 s53, s52, 8
v_add_u32 v1, s37, v0
M 7,32,2
WL 3
LO 120,16,120
LU 16,120
M 7,33,3
s_add_u32 s48, %[alo], s53
RA 216,1,0
HS 24,16
M 8,44,2
s_addc_u32 s49, %[ahi], 0
LL 18,76
LL 124,77
M 8,45,3
s_add_u32 s52, s52, %[d]
LA 18
v_and_b32 v22, 0xfffffcff, v1
M 9,56,2
s_cmp_eq_u32 s52, %[wrap]
LC_ 124
M 9,57,3
s_cselect_b32 s52, %[rst], s52
DQ 3072
M 10,68,2
WL 3
LO 121,17,121
LU 17,121
M 10,69,3
s_add_u32 s50, s50, %[d]
RA 220,1,1024
HS 24,17
M 11,80,2
LL 19,78
LL 125,79
M 11,81,3
s_cmp_eq_u32 s50, %[wrap]
LA 19
RV 252,22,5120
M 6,22,4
s_cselect_b32 s50, %[rst], s50
LC_ 125
M 6,23,5
WV 30
M 7,34,4
WL 4
LO 124,18,124
LU 18,124
M 7,35,5
s_add_u32 s38, s38, 32768
RA 224,1,2048
HS 24,18
M 8,46,4
s_cmp_eq_u32 s38, s77
LL 16,80
LL 106,81
M 8,47,5
s_cselect_b32 s38, s76, s38
LA 16
M 9,58,4
s_barrier
LC_ 106
M 9,59,5
s_sub_u32 s39, 0, s39
M 10,70,4
WL 3
LO 125,19,125
LU 19,125
M 10,71,5
RA 228,1,3072
HS 24,19
M 11,82,4
s_sub_u32 s66, s66, 1
LL 17,82
LL 107,83
M 11,83,5
s_cbranch_scc0 1b
7:
LA 17
HX 24
Z 0,12,0
LC_ 107
ds_read_b64 v[14:15], v5 offset:0
M 0,13,1
W1 24,0
v_subrev_u32 v5, s39, v5
Z 1,24,0
WL 5
LO 106,16,106
LU 16,106
M 1,25,1
RA 232,1,4096
HZ 25,16
Z 2,36,0
LL 18,84
LL 110,85
M 2,37,1
LA 18
Z 3,48,0
LC_ 110
M 3,49,1
s_add_u32 s42, s44, s60
Z 4,60,0
WL 5
LO 107,17,107
LU 17,107
M 4,61,1
s_addc_u32 s43, s45, 0
RA 236,1,6144
HS 25,17
Z 5,72,0
LL 19,86
LL 111,87
M 5,73,1
LA 19
ST 104
M 0,14,2
WL 6
LC_ 111
v_add_f32 v14, v14, v15
M 0,15,3
s_nop 0
v_add_f32_dpp v14, v14, v14 quad_perm:[1,0,3,2] row_mask:0xf bank_mask:0xf
M 1,26,2
WL 3
LO 110,18,110
LU 18,110
M 1,27,3
RA 216,1,7168
HS 25,18
M 2,38,2
LL 16,88
LL 114,89
M 2,39,3
LA 16
v_add_f32_dpp v14, v14, v14 quad_perm:[2,3,0,1] row_mask:0xf bank_mask:0xf
M 3,50,2
LC_ 114
v_add_f32_dpp v14, v14, v14 row_half_mirror row_mask:0xf bank_mask:0xf
M 3,51,3
s_add_u32 s42, s44, s61
v_cvt_f64_f32 v[14:15], v14
M 4,62,2
WL 3
LO 111,19,111
LU 19,111
M 4,63,3
s_addc_u32 s43, s45, 0
RA 220,1,8192
HS 25,19
M 5,74,2
LL 17,90
LL 115,91
M 5,75,3
LA 17
s_mov_b64 exec, s[54:55]
SP
s_mov_b64 exec, -1
M 0,16,4
LC_ 115
ST 108
M 0,17,5
s_mov_b32 m0, %[pm]
s_nop 0
DF
M 1,28,4
WL 3
LO 114,16,114
LU 16,114
M 1,29,5
RA 224,1,9216
HS 25,16
M 2,40,4
LL 18,92
LL 118,93
M 2,41,5
LA 18
M 3,52,4
LC_ 118
M 3,53,5
s_add_u32 s42, s44, s62
M 4,64,4
WL 3
LO 115,17,115
LU 17,115
M 4,65,5
s_addc_u32 s43, s45, 0
RA 228,1,10240
HS 25,17
M 5,76,4
LL 19,94
LL 119,95
M 5,77,5
LA 19
ST 112
M 0,18,0
LC_ 119
M 0,19,1
M 1,30,0
WL 3
)"
R"(LO 118,18,118
LU 18,118
M 1,31,1
RA 232,1,11264
HS 25,18
M 2,42,0
LL 16,96
LL 122,97
M 2,43,1
LA 16
RV 252,22,5392
M 3,54,0
LC_ 122
M 3,55,1
s_add_u32 s42, s44, s63
M 4,66,0
WL 4
LO 119,19,119
LU 19,119
M 4,67,1
s_addc_u32 s43, s45, 0
RA 236,1,12288
HS 25,19
M 5,78,0
LL 17,98
LL 123,99
M 5,79,1
LA 17
ST 116
M 0,20,2
LC_ 123
M 0,21,3
M 1,32,2
WL 3
LO 122,16,122
LU 16,122
M 1,33,3
RA 216,1,13312
HS 25,16
M 2,44,2
LL 18,100
LL 126,101
M 2,45,3
LA 18
M 3,56,2
LC_ 126
M 3,57,3
s_add_u32 s42, s44, s64
M 4,68,2
WL 3
LO 123,17,123
LU 17,123
M 4,69,3
s_addc_u32 s43, s45, 0
RA 220,1,14336
HS 25,17
M 5,80,2
LL 19,102
LL 127,103
M 5,81,3
LA 19
ST 120
M 0,22,4
LC_ 127
M 0,23,5
M 1,34,4
WL 3
LO 126,18,126
LU 18,126
M 1,35,5
RA 224,1,15360
HS 25,18
M 2,46,4
LL 16,56
LL 104,57
M 2,47,5
LA 16
M 3,58,4
LC_ 104
M 3,59,5
s_add_u32 s42, s44, s65
M 4,70,4
WL 3
LO 127,19,127
LU 19,127
M 4,71,5
s_addc_u32 s43, s45, 0
RA 228,1,16384
HS 25,19
M 5,82,4
LL 17,58
LL 105,59
M 5,83,5
LA 17
ST 124
Z 6,12,0
LC_ 105
HX 25
M 6,13,1
W1 25,1024
Z 7,24,0
WL 4
LO 104,16,104
LU 16,104
M 7,25,1
RA 232,1,17408
HZ 24,16
Z 8,36,0
LL 18,60
LL 108,61
M 8,37,1
s_mul_i32 s53, s50, 25600
LA 18
v_add_u32 v4, s39, v4
Z 9,48,0
s_add_u32 s40, %[wlo], s53
LC_ 108
s_mov_b64 exec, 1
AT
s_mov_b64 exec, -1
M 9,49,1
s_addc_u32 s41, %[whi], 0
Z 10,60,0
WL 4
LO 105,17,105
LU 17,105
M 10,61,1
s_mov_b32 m0, s38
RA 236,1,18432
HS 24,17
Z 11,72,0
LL 19,62
LL 109,63
M 11,73,1
LA 19
DW 0
M 6,14,2
LC_ 109
M 6,15,3
M 7,26,2
WL 3
LO 108,18,108
LU 18,108
M 7,27,3
s_add_u32 s37, s37, 32768
RA 216,1,19456
HS 24,18
M 8,38,2
s_cmp_eq_u32 s37, s75
LL 16,64
LL 112,65
M 8,39,3
s_cselect_b32 s37, s74, s37
LA 16
DW 1024
M 9,50,2
LC_ 112
M 9,51,3
M 10,62,2
WL 3
LO 109,19,109
LU 19,109
M 10,63,3
RA 220,1,20480
HS 24,19
M 11,74,2
LL 17,66
LL 113,67
M 11,75,3
LA 17
DW 2048
M 6,16,4
LC_ 113
M 6,17,5
M 7,28,4
WL 3
LO 112,16,112
LU 16,112
M 7,29,5
s_mul_i32 s53, s51, 49152
RA 224,1,21504
HS 24,16
M 8,40,4
s_bitcmp1_b64 s[90:91], s51
LL 18,68
LL 116,69
M 8,41,5
s_cselect_b64 s[44:45], s[84:85], s[82:83]
LA 18
M 9,52,4
s_add_u32 s44, s44, s53
LC_ 116
M 9,53,5
s_addc_u32 s45, s45, 0
M 10,64,4
WL 3
LO 113,17,113
LU 17,113
M 10,65,5
s_add_u32 s51, s51, %[d]
RA 228,1,22528
HS 24,17
M 11,76,4
LL 19,70
LL 117,71
M 11,77,5
s_cmp_eq_u32 s51, %[wrap]
LA 19
M 6,18,0
s_cselect_b32 s51, %[rst], s51
LC_ 117
M 6,19,1
s_add_u32 s40, s40, 3072
M 7,30,0
WL 3
LO 116,18,116
LU 18,116
M 7,31,1
s_addc_u32 s41, s41, 0
RA 232,1,23552
HS 24,18
M 8,42,0
s_add_u32 m0, s38, 3072
LL 16,72
LL 120,73
M 8,43,1
LA 16
DW 0
M 9,54,0
LC_ 120
DW 1024
M 9,55,1
M 10,66,0
WL 3
LO 117,19,117
LU 19,117
M 10,67,1
RA 236,1,24576
HS 24,19
M 11,78,0
LL 17,74
LL 121,75
M 11,79,1
LA 17
DW 2048
M 6,20,2
LC_ 121
M 6,21,3
s_lshl_b32 s53, s52, 8
v_add_u32 v1, s37, v0
M 7,32,2
WL 3
LO 120,16,120
LU 16,120
M 7,33,3
s_add_u32 s48, %[alo], s53
RA 216,1,0
HS 24,16
M 8,44,2
s_addc_u32 s49, %[ahi], 0
LL 18,76
LL 124,77
M 8,45,3
s_add_u32 s52, s52, %[d]
LA 18
v_and_b32 v22, 0xfffffcff, v1
M 9,56,2
s_cmp_eq_u32 s52, %[wrap]
LC_ 124
M 9,57,3
s_cselect_b32 s52, %[rst], s52
DQ 3072
M 10,68,2
WL 3
LO 121,17,121
LU 17,121
M 10,69,3
s_add_u32 s50, s50, %[d]
RA 220,1,1024
HS 24,17
M 11,80,2
LL 19,78
LL 125,79
M 11,81,3
s_cmp_eq_u32 s50, %[wrap]
LA 19
RV 252,22,5120
M 6,22,4
s_cselect_b32 s50, %[rst], s50
LC_ 125
M 6,23,5
WV 31
M 7,34,4
WL 4
LO 124,18,124
LU 18,124
M 7,35,5
s_add_u32 s38, s38, 32768
RA 224,1,2048
HS 24,18
M 8,46,4
s_cmp_eq_u32 s38, s77
LL 16,80
LL 106,81
M 8,47,5
s_cselect_b32 s38, s76, s38
LA 16
M 9,58,4
s_barrier
LC_ 106
M 9,59,5
s_sub_u32 s39, 0, s39
M 10,70,4
WL 3
LO 125,19,125
LU 19,125
M 10,71,5
RA 228,1,3072
HS 24,19
M 11,82,4
LL 17,82
LL 107,83
M 11,83,5
LA 17
HX 24
Z 0,12,0
LC_ 107
ds_read_b64 v[14:15], v5 offset:0
M 0,13,1
W1 24,0
v_subrev_u32 v5, s39, v5
Z 1,24,0
WL 5
LO 106,16,106
LU 16,106
M 1,25,1
RA 232,1,4096
HZ 25,16
Z 2,36,0
LL 18,84
LL 110,85
M 2,37,1
LA 18
Z 3,48,0
LC_ 110
M 3,49,1
s_add_u32 s42, s44, s60
Z 4,60,0
WL 5
LO 107,17,107
LU 17,107
M 4,61,1
s_addc_u32 s43, s45, 0
RA 236,1,6144
HS 25,17
Z 5,72,0
LL 19,86
LL 111,87
M 5,73,1
LA 19
ST 104
M 0,14,2
WL 6
LC_ 111
v_add_f32 v14, v14, v15
M 0,15,3
s_nop 0
v_add_f32_dpp v14, v14, v14 quad_perm:[1,0,3,2] row_mask:0xf bank_mask:0xf
M 1,26,2
WL 3
LO 110,18,110
LU 18,110
M 1,27,3
RA 216,1,7168
HS 25,18
M 2,38,2
LL 16,88
LL 114,89
M 2,39,3
LA 16
v_add_f32_dpp v14, v14, v14 quad_perm:[2,3,0,1] row_mask:0xf bank_mask:0xf
M 3,50,2
LC_ 114
v_add_f32_dpp v14, v14, v14 row_half_mirror row_mask:0xf bank_mask:0xf
M 3,51,3
s_add_u32 s42, s44, s61
v_cvt_f64_f32 v[14:15], v14
M 4,62,2
WL 3
LO 111,19,111
LU 19,111
M 4,63,3
s_addc_u32 s43, s45, 0
RA 220,1,8192
HS 25,19
M 5,74,2
LL 17,90
LL 115,91
M 5,75,3
LA 17
)"
R"(s_mov_b64 exec, s[54:55]
SP
s_mov_b64 exec, -1
M 0,16,4
LC_ 115
ST 108
M 0,17,5
s_mov_b32 m0, %[pm]
s_nop 0
DF
M 1,28,4
WL 3
LO 114,16,114
LU 16,114
M 1,29,5
RA 224,1,9216
HS 25,16
M 2,40,4
LL 18,92
LL 118,93
M 2,41,5
LA 18
M 3,52,4
LC_ 118
M 3,53,5
s_add_u32 s42, s44, s62
M 4,64,4
WL 3
LO 115,17,115
LU 17,115
M 4,65,5
s_addc_u32 s43, s45, 0
RA 228,1,10240
HS 25,17
M 5,76,4
LL 19,94
LL 119,95
M 5,77,5
LA 19
ST 112
M 0,18,0
LC_ 119
M 0,19,1
M 1,30,0
WL 3
LO 118,18,118
LU 18,118
M 1,31,1
RA 232,1,11264
HS 25,18
M 2,42,0
LL 16,96
LL 122,97
M 2,43,1
LA 16
RV 252,22,5392
M 3,54,0
LC_ 122
M 3,55,1
s_add_u32 s42, s44, s63
M 4,66,0
WL 4
LO 119,19,119
LU 19,119
M 4,67,1
s_addc_u32 s43, s45, 0
RA 236,1,12288
HS 25,19
M 5,78,0
LL 17,98
LL 123,99
M 5,79,1
LA 17
ST 116
M 0,20,2
LC_ 123
M 0,21,3
M 1,32,2
WL 3
LO 122,16,122
LU 16,122
M 1,33,3
RA 216,1,13312
HS 25,16
M 2,44,2
LL 18,100
LL 126,101
M 2,45,3
LA 18
M 3,56,2
LC_ 126
M 3,57,3
s_add_u32 s42, s44, s64
M 4,68,2
WL 3
LO 123,17,123
LU 17,123
M 4,69,3
s_addc_u32 s43, s45, 0
RA 220,1,14336
HS 25,17
M 5,80,2
LL 19,102
LL 127,103
M 5,81,3
LA 19
ST 120
M 0,22,4
LC_ 127
M 0,23,5
M 1,34,4
WL 3
LO 126,18,126
LU 18,126
M 1,35,5
RA 224,1,15360
HS 25,18
M 2,46,4
LL 16,56
LL 104,57
M 2,47,5
LA 16
M 3,58,4
LC_ 104
M 3,59,5
s_add_u32 s42, s44, s65
M 4,70,4
WL 3
LO 127,19,127
LU 19,127
M 4,71,5
s_addc_u32 s43, s45, 0
RA 228,1,16384
HS 25,19
M 5,82,4
LL 17,58
LL 105,59
M 5,83,5
LA 17
ST 124
Z 6,12,0
LC_ 105
HX 25
M 6,13,1
WV 15
W1 25,1024
v_readfirstlane_b32 s72, v11
Z 7,24,0
WL 4
LO 104,16,104
LU 16,104
M 7,25,1
RA 232,1,17408
HZ 24,16
Z 8,36,0
LL 18,60
LL 108,61
M 8,37,1
s_mul_i32 s53, s50, 25600
LA 18
v_mov_b32 v12, s72
Z 9,48,0
s_add_u32 s40, %[wlo], s53
LC_ 108
W2 12,12,16
M 9,49,1
s_addc_u32 s41, %[whi], 0
v_add_u32 v4, s39, v4
Z 10,60,0
WL 5
LO 105,17,105
LU 17,105
M 10,61,1
s_mov_b32 m0, s38
RA 236,1,18432
HS 24,17
Z 11,72,0
LL 19,62
LL 109,63
M 11,73,1
LA 19
DW 0
M 6,14,2
LC_ 109
M 6,15,3
M 7,26,2
WL 4
LO 108,18,108
LU 18,108
M 7,27,3
s_add_u32 s37, s37, 32768
RA 216,1,19456
HS 24,18
M 8,38,2
s_cmp_eq_u32 s37, s75
LL 16,64
LL 112,65
M 8,39,3
s_cselect_b32 s37, s74, s37
LA 16
DW 1024
M 9,50,2
LC_ 112
M 9,51,3
M 10,62,2
WL 3
LO 109,19,109
LU 19,109
M 10,63,3
RA 220,1,20480
HS 24,19
M 11,74,2
LL 17,66
LL 113,67
M 11,75,3
LA 17
DW 2048
M 6,16,4
LC_ 113
M 6,17,5
M 7,28,4
WL 3
LO 112,16,112
LU 16,112
M 7,29,5
s_mul_i32 s53, s51, 49152
RA 224,1,21504
HS 24,16
M 8,40,4
s_bitcmp1_b64 s[90:91], s51
LL 18,68
LL 116,69
M 8,41,5
s_cselect_b64 s[44:45], s[84:85], s[82:83]
LA 18
M 9,52,4
s_add_u32 s44, s44, s53
LC_ 116
M 9,53,5
s_addc_u32 s45, s45, 0
M 10,64,4
WL 3
LO 113,17,113
LU 17,113
M 10,65,5
s_add_u32 s51, s51, %[d]
RA 228,1,22528
HS 24,17
M 11,76,4
LL 19,70
LL 117,71
M 11,77,5
s_cmp_eq_u32 s51, %[wrap]
LA 19
M 6,18,0
s_cselect_b32 s51, %[rst], s51
LC_ 117
M 6,19,1
s_add_u32 s40, s40, 3072
M 7,30,0
WL 3
LO 116,18,116
LU 18,116
M 7,31,1
s_addc_u32 s41, s41, 0
RA 232,1,23552
HS 24,18
M 8,42,0
s_add_u32 m0, s38, 3072
LL 16,72
LL 120,73
M 8,43,1
LA 16
DW 0
M 9,54,0
LC_ 120
DW 1024
M 9,55,1
M 10,66,0
WL 3
LO 117,19,117
LU 19,117
M 10,67,1
RA 236,1,24576
HS 24,19
M 11,78,0
LL 17,74
LL 121,75
M 11,79,1
LA 17
DW 2048
M 6,20,2
LC_ 121
M 6,21,3
s_lshl_b32 s53, s52, 8
v_add_u32 v1, s37, v0
M 7,32,2
WL 3
LO 120,16,120
LU 16,120
M 7,33,3
s_add_u32 s48, %[alo], s53
RA 216,1,0
HS 24,16
M 8,44,2
s_addc_u32 s49, %[ahi], 0
LL 18,76
LL 124,77
M 8,45,3
s_add_u32 s52, s52, %[d]
LA 18
v_and_b32 v22, 0xfffffcff, v1
M 9,56,2
s_cmp_eq_u32 s52, %[wrap]
LC_ 124
M 9,57,3
s_cselect_b32 s52, %[rst], s52
DQ 3072
M 10,68,2
WL 3
LO 121,17,121
LU 17,121
M 10,69,3
s_add_u32 s50, s50, %[d]
RA 220,1,1024
HS 24,17
M 11,80,2
LL 19,78
LL 125,79
M 11,81,3
s_cmp_eq_u32 s50, %[wrap]
LA 19
RV 252,22,5120
M 6,22,4
s_cselect_b32 s50, %[rst], s50
LC_ 125
M 6,23,5
M 7,34,4
WL 4
LO 124,18,124
LU 18,124
M 7,35,5
s_add_u32 s38, s38, 32768
RA 224,1,2048
HS 24,18
M 8,46,4
s_cmp_eq_u32 s38, s77
LL 16,80
LL 106,81
M 8,47,5
s_cselect_b32 s38, s76, s38
LA 16
M 9,58,4
s_barrier
LC_ 106
M 9,59,5
s_sub_u32 s39, 0, s39
M 10,70,4
WL 3
LO 125,19,125
LU 19,125
M 10,71,5
RA 228,1,3072
HS 24,19
M 11,82,4
LL 17,82
LL 107,83
M 11,83,5
LA 17
HX 24
Z 0,12,0
LC_ 107
ds_read_b32 v20, v5 offset:4096
M 0,13,1
ds_read_b64 v[14:15], v5 offset:0
Z 1,24,0
WL 5
LO 106,16,106
LU 16,106
M 1,25,1
RA 232,1,4096
HZ 25,16
Z 2,36,0
LL 18,84
LL 110,85
M 2,37,1
LA 18
W1 24,0
Z 3,48,0
LC_ 110
v_subrev_u32 v5, s39, v5
M 3,49,1
s_add_u32 s42, s44, s60
Z 4,60,0
WL 6
LO 107,17,107
LU 17,107
M 4,61,1
s_addc_u32 s43, s45, 0
RA 236,1,6144
)"
R"(HS 25,17
Z 5,72,0
LL 19,86
LL 111,87
M 5,73,1
LA 19
ST 104
M 0,14,2
WL 6
LC_ 111
v_add_f32 v14, v14, v15
M 0,15,3
s_nop 0
v_add_f32_dpp v14, v14, v14 quad_perm:[1,0,3,2] row_mask:0xf bank_mask:0xf
M 1,26,2
WL 3
LO 110,18,110
LU 18,110
M 1,27,3
RA 216,1,7168
HS 25,18
M 2,38,2
LL 16,88
LL 114,89
M 2,39,3
LA 16
v_add_f32_dpp v14, v14, v14 quad_perm:[2,3,0,1] row_mask:0xf bank_mask:0xf
M 3,50,2
LC_ 114
v_add_f32_dpp v14, v14, v14 row_half_mirror row_mask:0xf bank_mask:0xf
M 3,51,3
s_add_u32 s42, s44, s61
v_cvt_f64_f32 v[14:15], v14
M 4,62,2
WL 3
LO 111,19,111
LU 19,111
M 4,63,3
s_addc_u32 s43, s45, 0
RA 220,1,8192
HS 25,19
M 5,74,2
LL 17,90
LL 115,91
M 5,75,3
LA 17
s_mov_b64 exec, s[54:55]
SP
s_mov_b64 exec, -1
M 0,16,4
LC_ 115
ST 108
M 0,17,5
s_mov_b32 m0, %[pm]
s_nop 0
DF
v_readfirstlane_b32 s72, v20
M 1,28,4
WL 3
LO 114,16,114
LU 16,114
M 1,29,5
s_sub_u32 s53, 63, s72
RA 224,1,9216
HS 25,16
M 2,40,4
s_cmp_eq_u32 %[d], 1
LL 18,92
LL 118,93
M 2,41,5
s_cselect_b32 s72, s72, s53
LA 18
M 3,52,4
s_and_b32 s72, s72, 63
LC_ 118
M 3,53,5
s_add_u32 s53, s72, 128
M 4,64,4
WL 3
LO 115,17,115
LU 17,115
M 4,65,5
s_mul_i32 s53, s53, 2359296
RA 228,1,10240
HS 25,17
M 5,76,4
LL 19,94
LL 119,95
M 5,77,5
s_add_u32 s86, %[hlo], s53
LA 19
M 0,18,0
s_addc_u32 s87, %[hhi], 0
LC_ 119
M 0,19,1
s_add_u32 s88, s86, 8192
M 1,30,0
WL 3
LO 118,18,118
LU 18,118
M 1,31,1
s_addc_u32 s89, s87, 0
RA 232,1,11264
HS 25,18
M 2,42,0
s_and_b32 s92, %[hlo], 0x1fffff
LL 16,96
LL 122,97
M 2,43,1
s_add_u32 s92, s92, s53
LA 16
RV 252,22,5392
M 3,54,0
s_mov_b32 s93, 3072
LC_ 122
M 3,55,1
s_add_u32 s42, s44, s62
M 4,66,0
WL 4
LO 119,19,119
LU 19,119
M 4,67,1
s_addc_u32 s43, s45, 0
RA 236,1,12288
HS 25,19
M 5,78,0
LL 17,98
LL 123,99
M 5,79,1
s_mul_i32 s57, s72, 589824
LA 17
s_add_u32 s57, s57, %[xs1]
s_lshl_b32 s72, s72, 1
s_add_u32 s72, s72, s96
M 0,20,2
LC_ 123
ST 112
M 0,21,3
s_lshl_b32 s53, s72, 6
M 1,32,2
WL 3
LO 122,16,122
LU 16,122
s_add_u32 s53, s53, 128
s_cmp_lt_u32 %[ca0], 128
s_cselect_b32 s73, s53, %[ca0]
M 1,33,3
RA 216,1,13312
HS 25,16
M 2,44,2
LL 18,100
LL 126,101
M 2,45,3
LA 18
v_mov_b32 v9, s73
M 3,56,2
LC_ 126
v_mov_b32 v11, s92
M 3,57,3
s_add_u32 s42, s44, s63
M 4,68,2
WL 3
LO 123,17,123
LU 17,123
M 4,69,3
s_addc_u32 s43, s45, 0
RA 220,1,14336
HS 25,17
M 5,80,2
LL 19,102
LL 127,103
M 5,81,3
LA 19
v_mad_u32_u24 v11, v0, s93, v11
M 0,22,4
LC_ 127
ST 116
M 0,23,5
v_lshrrev_b32 v11, 21, v11
M 1,34,4
WL 3
LO 126,18,126
LU 18,126
M 1,35,5
RA 224,1,15360
HS 25,18
M 2,46,4
LL 16,56
LL 104,57
M 2,47,5
LA 16
v_lshl_add_u32 v11, v11, 2, 64
M 3,58,4
LC_ 104
v_writelane_b32 v11, %[tsel], 63
M 3,59,5
s_add_u32 s42, s44, s64
M 4,70,4
WL 3
LO 127,19,127
LU 19,127
M 4,71,5
s_addc_u32 s43, s45, 0
RA 228,1,16384
HS 25,19
M 5,82,4
LL 17,58
LL 105,59
M 5,83,5
s_sub_u32 s53, s86, %[hlo]
LA 17
HX 25
s_lshr_b32 s53, s53, 2
s_and_b32 s92, %[xlo], 0x1fffff
s_add_u32 s53, s53, s92
s_add_u32 s53, s53, 0x20048000
s_lshr_b32 s53, s53, 21
s_lshl_b32 s92, s53, 2
Z 6,12,0
LC_ 105
ST 120
M 6,13,1
W1 25,1024
v_writelane_b32 v11, s92, 48
Z 7,24,0
WL 4
LO 104,16,104
LU 16,104
M 7,25,1
RA 232,1,17408
HZ 24,16
Z 8,36,0
LL 18,60
LL 108,61
M 8,37,1
LA 18
global_load_dword v11, v11, s[68:69] offset:-2048
Z 9,48,0
s_add_u32 s42, s44, s65
LC_ 108
v_add_u32 v4, s39, v4
M 9,49,1
s_addc_u32 s43, s45, 0
ST 124
Z 10,60,0
WL 4
LO 105,17,105
LU 17,105
M 10,61,1
s_mul_i32 s53, s50, 25600
RA 236,1,18432
HS 24,17
Z 11,72,0
LL 19,62
LL 109,63
M 11,73,1
s_add_u32 s40, %[wlo], s53
LA 19
M 6,14,2
s_addc_u32 s41, %[whi], 0
LC_ 109
M 6,15,3
s_mov_b32 m0, s38
s_nop 0
DW 0
M 7,26,2
WL 3
LO 108,18,108
LU 18,108
M 7,27,3
s_add_u32 s37, s37, 32768
RA 216,1,19456
HS 24,18
M 8,38,2
s_cmp_eq_u32 s37, s75
LL 16,64
LL 112,65
M 8,39,3
s_cselect_b32 s37, s74, s37
LA 16
DW 1024
M 9,50,2
LC_ 112
M 9,51,3
M 10,62,2
WL 3
LO 109,19,109
LU 19,109
M 10,63,3
RA 220,1,20480
HS 24,19
M 11,74,2
LL 17,66
LL 113,67
M 11,75,3
LA 17
DW 2048
M 6,16,4
LC_ 113
M 6,17,5
M 7,28,4
WL 3
LO 112,16,112
LU 16,112
M 7,29,5
s_mul_i32 s53, s51, 49152
RA 224,1,21504
HS 24,16
M 8,40,4
s_bitcmp1_b64 s[90:91], s51
LL 18,68
LL 116,69
M 8,41,5
s_cselect_b64 s[44:45], s[84:85], s[82:83]
LA 18
M 9,52,4
s_add_u32 s44, s44, s53
LC_ 116
M 9,53,5
s_addc_u32 s45, s45, 0
M 10,64,4
WL 3
LO 113,17,113
LU 17,113
M 10,65,5
s_add_u32 s51, s51, %[d]
RA 228,1,22528
HS 24,17
M 11,76,4
LL 19,70
LL 117,71
M 11,77,5
s_cmp_eq_u32 s51, %[wrap]
LA 19
M 6,18,0
s_cselect_b32 s51, %[rst], s51
LC_ 117
M 6,19,1
s_add_u32 s40, s40, 3072
M 7,30,0
WL 3
LO 116,18,116
LU 18,116
M 7,31,1
s_addc_u32 s41, s41, 0
RA 232,1,23552
HS 24,18
M 8,42,0
s_add_u32 m0, s38, 3072
LL 16,72
LL 120,73
M 8,43,1
LA 16
DW 0
M 9,54,0
LC_ 120
DW 1024
M 9,55,1
M 10,66,0
WL 3
LO 117,19,117
LU 19,117
M 10,67,1
RA 236,1,24576
HS 24,19
M 11,78,0
LL 17,74
LL 121,75
M 11,79,1
LA 17
DW 2048
M 6,20,2
LC_ 121
M 6,21,3
s_lshl_b32 s53, s52, 8
v_add_u32 v1, s37, v0
M 7,32,2
WL 3
LO 120,16,120
LU 16,120
M 7,33,3
s_add_u32 s48, %[alo], s53
RA 216,1,0
HS 24,16
M 8,44,2
s_addc_u32 s49, %[ahi], 0
LL 18,76
LL 124,77
M 8,45,3
s_add_u32 s52, s52, %[d]
LA 18
v_and_b32 v22, 0xfffffcff, v1
M 9,56,2
s_cmp_eq_u32 s52, %[wrap]
LC_ 124
M 9,57,3
s_cselect_b32 s52, %[rst], s52
DQ 3072
M 10,68,2
WL 3
)"
R"(LO 121,17,121
LU 17,121
M 10,69,3
s_add_u32 s50, s50, %[d]
RA 220,1,1024
HS 24,17
M 11,80,2
LL 19,78
LL 125,79
M 11,81,3
s_cmp_eq_u32 s50, %[wrap]
LA 19
RV 252,22,5120
M 6,22,4
s_cselect_b32 s50, %[rst], s50
LC_ 125
M 6,23,5
WV 31
M 7,34,4
WL 4
LO 124,18,124
LU 18,124
M 7,35,5
s_add_u32 s38, s38, 32768
RA 224,1,2048
HS 24,18
M 8,46,4
s_cmp_eq_u32 s38, s77
LL 16,80
LL 106,81
M 8,47,5
s_cselect_b32 s38, s76, s38
LA 16
M 9,58,4
s_barrier
LC_ 106
M 9,59,5
s_sub_u32 s39, 0, s39
M 10,70,4
WL 3
LO 125,19,125
LU 19,125
M 10,71,5
RA 228,1,3072
HS 24,19
M 11,82,4
LL 17,82
LL 107,83
M 11,83,5
LA 17
HX 24
Z 0,12,0
LC_ 107
ds_read_b64 v[14:15], v5 offset:0
M 0,13,1
W1 24,0
v_subrev_u32 v5, s39, v5
Z 1,24,0
WL 5
LO 106,16,106
LU 16,106
M 1,25,1
RA 232,1,4096
HZ 25,16
Z 2,36,0
LL 18,84
LL 110,85
M 2,37,1
LA 18
Z 3,48,0
LC_ 110
M 3,49,1
s_add_u32 s42, s44, s60
Z 4,60,0
WL 5
LO 107,17,107
LU 17,107
M 4,61,1
s_addc_u32 s43, s45, 0
RA 236,1,6144
HS 25,17
Z 5,72,0
LL 19,86
LL 111,87
M 5,73,1
LA 19
ST 104
M 0,14,2
WL 6
LC_ 111
v_add_f32 v14, v14, v15
M 0,15,3
s_nop 0
v_add_f32_dpp v14, v14, v14 quad_perm:[1,0,3,2] row_mask:0xf bank_mask:0xf
M 1,26,2
WL 3
LO 110,18,110
LU 18,110
M 1,27,3
RA 216,1,7168
HS 25,18
M 2,38,2
LL 16,88
LL 114,89
M 2,39,3
LA 16
v_add_f32_dpp v14, v14, v14 quad_perm:[2,3,0,1] row_mask:0xf bank_mask:0xf
M 3,50,2
LC_ 114
v_add_f32_dpp v14, v14, v14 row_half_mirror row_mask:0xf bank_mask:0xf
M 3,51,3
s_add_u32 s42, s44, s61
v_cvt_f64_f32 v[14:15], v14
M 4,62,2
WL 3
LO 111,19,111
LU 19,111
M 4,63,3
s_addc_u32 s43, s45, 0
RA 220,1,8192
HS 25,19
M 5,74,2
LL 17,90
LL 115,91
M 5,75,3
LA 17
s_mov_b64 exec, s[54:55]
SP
s_mov_b64 exec, -1
M 0,16,4
LC_ 115
ST 108
M 0,17,5
s_mov_b32 m0, %[pm]
s_nop 0
DF
M 1,28,4
WL 3
LO 114,16,114
LU 16,114
M 1,29,5
RA 224,1,9216
HS 25,16
M 2,40,4
LL 18,92
LL 118,93
M 2,41,5
LA 18
M 3,52,4
LC_ 118
M 3,53,5
s_add_u32 s42, s44, s62
M 4,64,4
WL 3
LO 115,17,115
LU 17,115
M 4,65,5
s_addc_u32 s43, s45, 0
RA 228,1,10240
HS 25,17
M 5,76,4
LL 19,94
LL 119,95
M 5,77,5
LA 19
ST 112
M 0,18,0
LC_ 119
M 0,19,1
M 1,30,0
WL 3
LO 118,18,118
LU 18,118
M 1,31,1
RA 232,1,11264
HS 25,18
M 2,42,0
LL 16,96
LL 122,97
M 2,43,1
LA 16
RV 252,22,5392
M 3,54,0
LC_ 122
M 3,55,1
s_add_u32 s42, s44, s63
M 4,66,0
WL 4
LO 119,19,119
LU 19,119
M 4,67,1
s_addc_u32 s43, s45, 0
RA 236,1,12288
HS 25,19
M 5,78,0
LL 17,98
LL 123,99
M 5,79,1
LA 17
ST 116
M 0,20,2
LC_ 123
M 0,21,3
M 1,32,2
WL 3
LO 122,16,122
LU 16,122
M 1,33,3
RA 216,1,13312
HS 25,16
M 2,44,2
LL 18,100
LL 126,101
M 2,45,3
LA 18
M 3,56,2
LC_ 126
M 3,57,3
s_add_u32 s42, s44, s64
M 4,68,2
WL 3
LO 123,17,123
LU 17,123
M 4,69,3
s_addc_u32 s43, s45, 0
RA 220,1,14336
HS 25,17
M 5,80,2
LL 19,102
LL 127,103
M 5,81,3
LA 19
ST 120
M 0,22,4
LC_ 127
M 0,23,5
M 1,34,4
WL 3
LO 126,18,126
LU 18,126
M 1,35,5
RA 224,1,15360
HS 25,18
M 2,46,4
LL 16,56
LL 104,57
M 2,47,5
LA 16
M 3,58,4
LC_ 104
M 3,59,5
s_add_u32 s42, s44, s65
M 4,70,4
WL 3
LO 127,19,127
LU 19,127
M 4,71,5
s_addc_u32 s43, s45, 0
RA 228,1,16384
HS 25,19
M 5,82,4
LL 17,58
LL 105,59
M 5,83,5
LA 17
ST 124
Z 6,12,0
s_and_b32 s53, %[w], 1
LC_ 105
HX 25
M 6,13,1
s_lshl_b32 s53, s53, 10
W1 25,1024
Z 7,24,0
WL 4
LO 104,16,104
LU 16,104
M 7,25,1
s_lshr_b32 s72, %[w], 1
RA 232,1,17408
HZ 24,16
Z 8,36,0
s_mul_i32 s72, s72, 32768
LL 18,60
LL 108,61
M 8,37,1
s_add_u32 s72, s72, s53
LA 18
v_add_u32 v4, s39, v4
Z 9,48,0
s_add_u32 s72, s72, 128000
LC_ 108
M 9,49,1
s_movk_i32 s53, 0x78
v_lshrrev_b32 v12, 5, v0
Z 10,60,0
WL 4
LO 105,17,105
LU 17,105
M 10,61,1
RA 236,1,18432
HS 24,17
Z 11,72,0
LL 19,62
LL 109,63
M 11,73,1
LA 19
v_and_b32 v12, 6, v12
M 6,14,2
LC_ 109
v_bfe_u32 v12, s53, v12, 2
M 6,15,3
v_lshrrev_b32 v20, 8, v0
M 7,26,2
WL 3
LO 108,18,108
LU 18,108
v_xor_b32 v20, v12, v20
v_lshl_add_u32 v20, v20, 4, s72
v_and_b32 v12, 0xf0, v0
v_lshl_add_u32 v20, v12, 2, v20
v_add_u32 v12, s72, v0
M 7,27,3
s_mul_i32 s53, s50, 25600
RA 216,1,19456
HS 24,18
M 8,38,2
s_add_u32 s40, %[wlo], s53
LL 16,64
LL 112,65
M 8,39,3
s_addc_u32 s41, %[whi], 0
LA 16
M 9,50,2
s_mov_b32 m0, s38
LC_ 112
DW 0
M 9,51,3
s_add_u32 s37, s37, 32768
M 10,62,2
WL 3
LO 109,19,109
LU 19,109
M 10,63,3
s_cmp_eq_u32 s37, s75
RA 220,1,20480
HS 24,19
M 11,74,2
LL 17,66
LL 113,67
M 11,75,3
s_cselect_b32 s37, s74, s37
LA 17
DW 1024
M 6,16,4
LC_ 113
DW 2048
M 6,17,5
M 7,28,4
WL 3
LO 112,16,112
LU 16,112
M 7,29,5
s_mul_i32 s53, s51, 49152
RA 224,1,21504
HS 24,16
M 8,40,4
s_bitcmp1_b64 s[90:91], s51
LL 18,68
LL 116,69
M 8,41,5
s_cselect_b64 s[44:45], s[84:85], s[82:83]
LA 18
M 9,52,4
s_add_u32 s44, s44, s53
LC_ 116
M 9,53,5
s_addc_u32 s45, s45, 0
M 10,64,4
WL 3
LO 113,17,113
LU 17,113
M 10,65,5
s_add_u32 s51, s51, %[d]
RA 228,1,22528
HS 24,17
M 11,76,4
LL 19,70
LL 117,71
M 11,77,5
s_cmp_eq_u32 s51, %[wrap]
LA 19
M 6,18,0
s_cselect_b32 s51, %[rst], s51
LC_ 117
M 6,19,1
s_add_u32 s40, s40, 3072
M 7,30,0
WL 3
LO 116,18,116
)"
R"(LU 18,116
M 7,31,1
s_addc_u32 s41, s41, 0
RA 232,1,23552
HS 24,18
M 8,42,0
s_add_u32 m0, s38, 3072
LL 16,72
LL 120,73
M 8,43,1
LA 16
DW 0
M 9,54,0
LC_ 120
DW 1024
M 9,55,1
M 10,66,0
WL 3
LO 117,19,117
LU 19,117
M 10,67,1
RA 236,1,24576
HS 24,19
M 11,78,0
LL 17,74
LL 121,75
M 11,79,1
LA 17
DW 2048
M 6,20,2
LC_ 121
M 6,21,3
WV 22
v_add_u32 v1, s37, v0
v_bcnt_u32_b32 v11, v11, 0
M 7,32,2
WL 3
LO 120,16,120
LU 16,120
M 7,33,3
s_lshl_b32 s53, s52, 8
RA 216,1,0
HS 24,16
M 8,44,2
s_add_u32 s48, %[alo], s53
LL 18,76
LL 124,77
M 8,45,3
s_addc_u32 s49, %[ahi], 0
LA 18
v_and_b32 v22, 0xfffffcff, v1
M 9,56,2
s_add_u32 s52, s52, %[d]
LC_ 124
M 9,57,3
s_cmp_eq_u32 s52, %[wrap]
M 10,68,2
WL 3
LO 121,17,121
LU 17,121
M 10,69,3
s_cselect_b32 s52, %[rst], s52
RA 220,1,1024
HS 24,17
M 11,80,2
LL 19,78
LL 125,79
M 11,81,3
LA 19
RV 252,22,5120
M 6,22,4
LC_ 125
DQ 3072
M 6,23,5
v_cmp_lt_u32 s[92:93], 2, v11
M 7,34,4
WL 4
LO 124,18,124
LU 18,124
M 7,35,5
s_nop 0
RA 224,1,2048
HS 24,18
M 8,46,4
s_sub_u32 s53, 0, s96
LL 16,80
LL 106,81
M 8,47,5
s_xor_b32 s92, s92, s53
LA 16
M 9,58,4
s_xor_b32 s93, s93, s53
LC_ 106
M 9,59,5
s_add_u32 s50, s50, %[d]
M 10,70,4
WL 3
LO 125,19,125
LU 19,125
M 10,71,5
RA 228,1,3072
HS 24,19
M 11,82,4
LL 17,82
LL 107,83
M 11,83,5
LA 17
HX 24
s_cmp_eq_u32 s50, %[wrap]
s_cselect_b32 s50, %[rst], s50
Z 0,12,0
s_add_u32 s38, s38, 32768
LC_ 107
M 0,13,1
s_cmp_eq_u32 s38, s77
Z 1,24,0
WL 3
LO 106,16,106
LU 16,106
M 1,25,1
s_cselect_b32 s38, s76, s38
RA 232,1,4096
HZ 25,16
Z 2,36,0
s_barrier
LL 18,84
LL 110,85
M 2,37,1
s_sub_u32 s39, 0, s39
LA 18
ds_read_b64 v[14:15], v5 offset:0
Z 3,48,0
LC_ 110
W1 24,0
M 3,49,1
s_add_u32 s42, s44, s60
v_subrev_u32 v5, s39, v5
Z 4,60,0
WL 5
LO 107,17,107
LU 17,107
M 4,61,1
s_addc_u32 s43, s45, 0
RA 236,1,6144
HS 25,17
Z 5,72,0
LL 19,86
LL 111,87
M 5,73,1
LA 19
ST 104
M 0,14,2
LC_ 111
M 0,15,3
M 1,26,2
WL 4
LO 110,18,110
LU 18,110
M 1,27,3
RA 216,1,7168
HS 25,18
M 2,38,2
LL 16,88
LL 114,89
M 2,39,3
LA 16
v_add_f32 v14, v14, v15
M 3,50,2
LC_ 114
v_add_f32_dpp v14, v14, v14 quad_perm:[1,0,3,2] row_mask:0xf bank_mask:0xf
M 3,51,3
s_add_u32 s42, s44, s61
v_add_f32_dpp v14, v14, v14 quad_perm:[2,3,0,1] row_mask:0xf bank_mask:0xf
M 4,62,2
WL 3
LO 111,19,111
LU 19,111
M 4,63,3
s_addc_u32 s43, s45, 0
RA 220,1,8192
HS 25,19
M 5,74,2
LL 17,90
LL 115,91
M 5,75,3
LA 17
ST 108
M 0,16,4
s_mov_b32 m0, %[pm]
LC_ 115
DF
v_add_f32_dpp v14, v14, v14 row_half_mirror row_mask:0xf bank_mask:0xf
M 0,17,5
v_cvt_f64_f32 v[14:15], v14
M 1,28,4
WL 3
LO 114,16,114
LU 16,114
M 1,29,5
RA 224,1,9216
HS 25,16
M 2,40,4
LL 18,92
LL 118,93
s_mov_b64 exec, s[54:55]
SP
s_mov_b64 exec, -1
M 2,41,5
LA 18
M 3,52,4
LC_ 118
M 3,53,5
s_add_u32 s42, s44, s62
M 4,64,4
WL 3
LO 115,17,115
LU 17,115
M 4,65,5
s_addc_u32 s43, s45, 0
RA 228,1,10240
HS 25,17
M 5,76,4
LL 19,94
LL 119,95
M 5,77,5
LA 19
ST 112
M 0,18,0
LC_ 119
M 0,19,1
M 1,30,0
WL 3
LO 118,18,118
LU 18,118
M 1,31,1
RA 232,1,11264
HS 25,18
M 2,42,0
LL 16,96
LL 122,97
M 2,43,1
LA 16
RV 252,22,5392
M 3,54,0
LC_ 122
M 3,55,1
s_add_u32 s42, s44, s63
M 4,66,0
WL 4
LO 119,19,119
LU 19,119
M 4,67,1
s_addc_u32 s43, s45, 0
RA 236,1,12288
HS 25,19
M 5,78,0
LL 17,98
LL 123,99
M 5,79,1
LA 17
ST 116
M 0,20,2
LC_ 123
M 0,21,3
M 1,32,2
WL 3
LO 122,16,122
LU 16,122
M 1,33,3
RA 216,1,13312
HS 25,16
M 2,44,2
LL 18,100
LL 126,101
M 2,45,3
LA 18
M 3,56,2
LC_ 126
M 3,57,3
s_add_u32 s42, s44, s64
M 4,68,2
WL 3
LO 123,17,123
LU 17,123
M 4,69,3
s_addc_u32 s43, s45, 0
RA 220,1,14336
HS 25,17
M 5,80,2
LL 19,102
LL 127,103
M 5,81,3
LA 19
ST 120
M 0,22,4
LC_ 127
M 0,23,5
M 1,34,4
WL 3
LO 126,18,126
LU 18,126
M 1,35,5
RA 224,1,15360
HS 25,18
M 2,46,4
LL 16,56
LL 104,57
M 2,47,5
LA 16
M 3,58,4
LC_ 104
M 3,59,5
s_add_u32 s42, s44, s65
M 4,70,4
WL 3
LO 127,19,127
LU 19,127
M 4,71,5
s_addc_u32 s43, s45, 0
RA 228,1,16384
HS 25,19
M 5,82,4
LL 17,58
LL 105,59
M 5,83,5
LA 17
ST 124
Z 6,12,0
LC_ 105
HX 25
M 6,13,1
W1 25,1024
Z 7,24,0
WL 4
LO 104,16,104
LU 16,104
M 7,25,1
RA 232,1,17408
HZ 24,16
Z 8,36,0
LL 18,60
LL 108,61
M 8,37,1
s_mul_i32 s53, s50, 25600
LA 18
v_add_u32 v4, s39, v4
Z 9,48,0
s_add_u32 s40, %[wlo], s53
LC_ 108
M 9,49,1
s_addc_u32 s41, %[whi], 0
Z 10,60,0
WL 4
LO 105,17,105
LU 17,105
M 10,61,1
s_mov_b32 m0, s38
RA 236,1,18432
HS 24,17
Z 11,72,0
LL 19,62
LL 109,63
M 11,73,1
LA 19
DW 0
M 6,14,2
LC_ 109
M 6,15,3
M 7,26,2
WL 3
LO 108,18,108
LU 18,108
M 7,27,3
s_add_u32 s37, s37, 32768
RA 216,1,19456
HS 24,18
M 8,38,2
s_cmp_eq_u32 s37, s75
LL 16,64
LL 112,65
M 8,39,3
s_cselect_b32 s37, s74, s37
LA 16
M 9,50,2
s_mul_i32 s53, %[d], 0
LC_ 112
M 9,51,3
s_add_u32 s53, s53, s57
M 10,62,2
WL 3
LO 109,19,109
LU 19,109
M 10,63,3
s_add_u32 s46, %[xlo], s53
RA 220,1,20480
HS 24,19
M 11,74,2
LL 17,66
LL 113,67
M 11,75,3
)"
R"(s_addc_u32 s47, %[xhi], 0
LA 17
M 6,16,4
s_mov_b32 s53, 0xfe987610
LC_ 113
M 6,17,5
s_cmp_eq_u32 %[d], 1
M 7,28,4
WL 3
LO 112,16,112
LU 16,112
M 7,29,5
s_cselect_b32 s53, s53, 0xda43cb52
RA 224,1,21504
HS 24,16
M 8,40,4
s_bitcmp1_b64 s[92:93], 48
LL 18,68
LL 116,69
M 8,41,5
s_cselect_b32 s58, 16, 0
LA 18
M 9,52,4
s_lshr_b32 s58, s53, s58
LC_ 116
v_lshrrev_b32 v3, 6, v0
v_and_b32 v3, 12, v3
v_bfe_u32 v3, s58, v3, 4
v_mad_u32_u24 v3, v3, s97, v21
M 9,53,5
DW 1024
M 10,64,4
WL 3
LO 113,17,113
LU 17,113
M 10,65,5
RA 228,1,22528
HS 24,17
M 11,76,4
LL 19,70
LL 117,71
M 11,77,5
LA 19
XL 0,0
M 6,18,0
LC_ 117
DW 2048
M 6,19,1
s_mul_i32 s53, s51, 49152
M 7,30,0
WL 3
LO 116,18,116
LU 18,116
M 7,31,1
s_bitcmp1_b64 s[90:91], s51
RA 232,1,23552
HS 24,18
M 8,42,0
s_cselect_b64 s[44:45], s[84:85], s[82:83]
LL 16,72
LL 120,73
M 8,43,1
s_add_u32 s44, s44, s53
LA 16
s_addc_u32 s45, s45, 0
s_add_u32 s51, s51, %[d]
s_cmp_eq_u32 s51, %[wrap]
s_cselect_b32 s51, %[rst], s51
XL 1,64
M 9,54,0
s_add_u32 s40, s40, 3072
LC_ 120
s_addc_u32 s41, s41, 0
s_add_u32 m0, s38, 3072
M 9,55,1
XL 2,128
M 10,66,0
WL 3
LO 117,19,117
LU 19,117
M 10,67,1
RA 236,1,24576
HS 24,19
M 11,78,0
LL 17,74
LL 121,75
M 11,79,1
LA 17
DW 0
M 6,20,2
LC_ 121
XL 3,192
M 6,21,3
DW 1024
v_add_u32 v1, s37, v0
M 7,32,2
WL 3
LO 120,16,120
LU 16,120
M 7,33,3
RA 216,1,0
HS 24,16
M 8,44,2
LL 18,76
LL 124,77
M 8,45,3
LA 18
XL 4,256
M 9,56,2
LC_ 124
DW 2048
M 9,57,3
s_lshl_b32 s53, s52, 8
v_and_b32 v22, 0xfffffcff, v1
M 10,68,2
WL 3
LO 121,17,121
LU 17,121
M 10,69,3
s_add_u32 s48, %[alo], s53
RA 220,1,1024
HS 24,17
M 11,80,2
LL 19,78
LL 125,79
M 11,81,3
s_addc_u32 s49, %[ahi], 0
LA 19
RV 252,22,5120
M 6,22,4
s_add_u32 s52, s52, %[d]
LC_ 125
M 6,23,5
s_cmp_eq_u32 s52, %[wrap]
M 7,34,4
WL 4
LO 124,18,124
LU 18,124
M 7,35,5
s_cselect_b32 s52, %[rst], s52
RA 224,1,2048
HS 24,18
M 8,46,4
LL 16,80
LL 106,81
M 8,47,5
LA 16
XL 5,320
DQ 3072
M 9,58,4
LC_ 106
XL 6,384
M 9,59,5
s_add_u32 s50, s50, %[d]
M 10,70,4
WL 3
LO 125,19,125
LU 19,125
M 10,71,5
RA 228,1,3072
HS 24,19
M 11,82,4
LL 17,82
LL 107,83
M 11,83,5
LA 17
HX 24
s_cmp_eq_u32 s50, %[wrap]
s_cselect_b32 s50, %[rst], s50
Z 0,12,0
LC_ 107
XL 7,448
M 0,13,1
s_add_u32 s38, s38, 32768
WV 12
BW 0
Z 1,24,0
WL 4
LO 106,16,106
LU 16,106
M 1,25,1
s_cmp_eq_u32 s38, s77
RA 232,1,4096
HZ 25,16
Z 2,36,0
s_cselect_b32 s38, s76, s38
LL 18,84
LL 110,85
M 2,37,1
s_barrier
LA 18
RV 40,8,6144
Z 3,48,0
s_sub_u32 s39, 0, s39
LC_ 110
ds_read_b64 v[14:15], v5 offset:0
M 3,49,1
XL 8,512
RV 44,8,6208
Z 4,60,0
WL 7
LO 107,17,107
LU 17,107
M 4,61,1
RA 236,1,6144
HS 25,17
Z 5,72,0
LL 19,86
LL 111,87
M 5,73,1
LA 19
W1 24,0
M 0,14,2
LC_ 111
XL 9,576
M 0,15,3
s_add_u32 s42, s44, s60
RV 48,8,6272
BR 0
M 1,26,2
WL 8
LO 110,18,110
LU 18,110
M 1,27,3
s_addc_u32 s43, s45, 0
RA 216,1,7168
HS 25,18
M 2,38,2
LL 16,88
LL 114,89
M 2,39,3
LA 16
XL 10,640
M 3,50,2
LC_ 114
ST 104
M 3,51,3
XL 11,704
v_subrev_u32 v5, s39, v5
M 4,62,2
WL 5
LO 111,19,111
LU 19,111
M 4,63,3
s_add_u32 s42, s44, s61
RA 220,1,8192
HS 25,19
M 5,74,2
LL 17,90
LL 115,91
M 5,75,3
s_addc_u32 s43, s45, 0
LA 17
ST 108
M 0,16,4
s_mov_b32 m0, %[pm]
LC_ 115
DF
M 0,17,5
WV 17
BW 1
M 1,28,4
WL 4
LO 114,16,114
LU 16,114
M 1,29,5
RA 224,1,9216
HS 25,16
M 2,40,4
LL 18,92
LL 118,93
M 2,41,5
LA 18
BR 1
LZ 40,0
WV 16
BW 2
M 3,52,4
LC_ 118
RV 40,8,6336
M 3,53,5
s_add_u32 s42, s44, s62
BR 2
v_add_f32 v14, v14, v15
s_nop 1
v_add_f32_dpp v14, v14, v14 quad_perm:[1,0,3,2] row_mask:0xf bank_mask:0xf
s_nop 1
v_add_f32_dpp v14, v14, v14 quad_perm:[2,3,0,1] row_mask:0xf bank_mask:0xf
s_nop 1
v_add_f32_dpp v14, v14, v14 row_half_mirror row_mask:0xf bank_mask:0xf
v_cvt_f64_f32 v[14:15], v14
s_mov_b64 exec, s[54:55]
SP
s_mov_b64 exec, -1
M 4,64,4
WL 8
LO 115,17,115
LU 17,115
M 4,65,5
s_addc_u32 s43, s45, 0
RA 228,1,10240
HS 25,17
M 5,76,4
LL 19,94
LL 119,95
M 5,77,5
LA 19
ST 112
M 0,18,0
LC_ 119
M 0,19,1
WL 7
LS 44,1
WV 16
BW 3
M 1,30,0
WL 6
LO 118,18,118
LU 18,118
M 1,31,1
RA 232,1,11264
HS 25,18
M 2,42,0
LL 16,96
LL 122,97
M 2,43,1
WV 14
LA 16
RV 252,22,5392
M 3,54,0
WL 7
LC_ 122
RV 44,8,6400
M 3,55,1
s_add_u32 s42, s44, s63
BR 3
LS 48,2
s_addc_u32 s43, s45, 0
BW 4
M 4,66,0
WL 8
LO 119,19,119
LU 19,119
M 4,67,1
RA 236,1,12288
HS 25,19
M 5,78,0
LL 17,98
LL 123,99
M 5,79,1
LA 17
ST 116
M 0,20,2
LC_ 123
RV 48,8,6464
M 0,21,3
BR 4
M 1,32,2
WL 8
LO 122,16,122
LU 16,122
M 1,33,3
WL 6
RA 216,1,13312
HS 25,16
LS 40,3
WV 13
BW 5
M 2,44,2
LL 18,100
LL 126,101
M 2,45,3
LA 18
RV 40,8,6528
M 3,56,2
LC_ 126
BR 5
M 3,57,3
s_add_u32 s42, s44, s64
M 4,68,2
WL 6
LO 123,17,123
LU 17,123
M 4,69,3
s_addc_u32 s43, s45, 0
RA 220,1,14336
HS 25,17
LS 44,4
WV 11
BW 6
M 5,80,2
LL 19,102
LL 127,103
M 5,81,3
LA 19
ST 120
M 0,22,4
LC_ 127
RV 44,8,6592
M 0,23,5
)"
R"(BR 6
M 1,34,4
WL 7
LO 126,18,126
LU 18,126
M 1,35,5
WL 6
RA 224,1,15360
HS 25,18
LS 48,5
WV 11
BW 7
M 2,46,4
LL 16,56
LL 104,57
M 2,47,5
LA 16
RV 48,8,6656
M 3,58,4
LC_ 104
BR 7
M 3,59,5
s_add_u32 s42, s44, s65
M 4,70,4
WL 6
LO 127,19,127
LU 19,127
M 4,71,5
s_addc_u32 s43, s45, 0
RA 228,1,16384
HS 25,19
LS 40,6
WV 10
BW 8
M 5,82,4
LL 17,58
LL 105,59
M 5,83,5
LA 17
ST 124
Z 6,12,0
LC_ 105
RV 40,8,6720
M 6,13,1
BR 8
HX 25
Z 7,24,0
WL 7
LO 104,16,104
LU 16,104
M 7,25,1
WL 6
RA 232,1,17408
HZ 24,16
LS 44,7
WV 10
BW 9
Z 8,36,0
LL 18,60
LL 108,61
M 8,37,1
s_mul_i32 s53, s50, 25600
LA 18
W1 25,1024
Z 9,48,0
s_add_u32 s40, %[wlo], s53
LC_ 108
RV 44,8,6784
M 9,49,1
s_addc_u32 s41, %[whi], 0
BR 9
v_add_u32 v4, s39, v4
Z 10,60,0
WL 7
LO 105,17,105
LU 17,105
M 10,61,1
s_mov_b32 m0, s38
RA 236,1,18432
HS 24,17
LS 48,8
WV 9
BW 10
Z 11,72,0
LL 19,62
LL 109,63
M 11,73,1
LA 19
DW 0
M 6,14,2
LC_ 109
RV 48,8,6848
M 6,15,3
WV 8
RA 240,7,0
BR 10
M 7,26,2
WL 7
LO 108,18,108
LU 18,108
M 7,27,3
s_add_u32 s37, s37, 32768
RA 216,1,19456
HS 24,18
LS 40,9
s_cmp_eq_u32 s37, s75
BW 11
M 8,38,2
s_cselect_b32 s37, s74, s37
LL 16,64
LL 112,65
M 8,39,3
LA 16
DW 1024
M 9,50,2
LC_ 112
RA 244,7,256
M 9,51,3
BR 11
M 10,62,2
WL 9
LO 109,19,109
LU 19,109
M 10,63,3
RA 220,1,20480
HS 24,19
M 11,74,2
LL 17,66
LL 113,67
M 11,75,3
WL 7
LA 17
DW 2048
LS 44,10
RA 248,7,512
M 6,16,4
LC_ 113
M 6,17,5
M 7,28,4
WL 4
LO 112,16,112
LU 16,112
M 7,29,5
s_mul_i32 s53, s51, 49152
RA 224,1,21504
HS 24,16
LS 48,11
s_bitcmp1_b64 s[90:91], s51
RA 252,7,768
M 8,40,4
s_cselect_b64 s[44:45], s[84:85], s[82:83]
LL 18,68
LL 116,69
M 8,41,5
s_add_u32 s44, s44, s53
LA 18
v_mul_f32 v36, 0xbb2aaaab, v36
M 9,52,4
s_addc_u32 s45, s45, 0
LC_ 116
v_mul_f32 v37, 0xbb2aaaab, v37
M 9,53,5
s_add_u32 s51, s51, %[d]
v_mul_f32 v38, 0xbb2aaaab, v38
v_mul_f32 v39, 0xbb2aaaab, v39
M 10,64,4
WL 4
LO 113,17,113
LU 17,113
M 10,65,5
s_cmp_eq_u32 s51, %[wrap]
RA 228,1,22528
HS 24,17
LK 40,240,0,0
LK 44,242,0,2
s_cselect_b32 s51, %[rst], s51
RA 240,7,1024
LK 48,244,1,0
s_nop 0
CF 128,40,41
LK 248,246,1,2
s_add_u32 s40, s40, 3072
RA 244,7,1280
CF 129,42,43
M 11,76,4
LL 19,70
LL 117,71
M 11,77,5
s_addc_u32 s41, s41, 0
LA 19
CF 130,44,45
M 6,18,0
s_add_u32 m0, s38, 3072
LC_ 117
DW 0
M 6,19,1
CF 131,46,47
LK 40,248,2,0
CF 132,48,49
LK 44,250,2,2
RA 248,7,1536
CF 133,50,51
GZ 0
WL 8
CF 134,248,249
CF 135,250,251
LK 48,252,3,0
CF 136,40,41
LK 248,254,3,2
RA 252,7,1792
CF 137,42,43
GS 1
WL 5
CF 138,44,45
CF 139,46,47
LK 40,240,4,0
CF 140,48,49
LK 44,242,4,2
RA 240,7,2048
CF 141,50,51
GS 2
WL 5
CF 142,248,249
CF 143,250,251
LK 48,244,5,0
CF 144,40,41
LK 248,246,5,2
RA 244,7,2304
CF 145,42,43
GS 3
CF 146,44,45
CF 147,46,47
M 7,30,0
LO 116,18,116
LU 18,116
M 7,31,1
WL 3
RA 232,1,23552
HS 24,18
LK 40,248,6,0
CF 148,48,49
LK 44,250,6,2
RA 248,7,2560
CF 149,50,51
GS 4
WL 4
CF 150,248,249
CF 151,250,251
LK 48,252,7,0
CF 152,40,41
LK 248,254,7,2
RA 252,7,2816
CF 153,42,43
GS 5
WL 4
CF 154,44,45
CF 155,46,47
LK 40,240,8,0
CF 156,48,49
LK 44,242,8,2
CF 157,50,51
GS 6
CF 158,248,249
CF 159,250,251
M 8,42,0
LL 16,72
LL 120,73
M 8,43,1
LA 16
DW 1024
M 9,54,0
LC_ 120
M 9,55,1
WL 5
LK 48,244,9,0
CF 160,40,41
LK 248,246,9,2
CF 161,42,43
GS 7
CF 162,44,45
CF 163,46,47
M 10,66,0
LO 117,19,117
LU 19,117
M 10,67,1
RA 236,1,24576
HS 24,19
M 11,78,0
LL 17,74
LL 121,75
M 11,79,1
LA 17
DW 2048
M 6,20,2
WL 5
LC_ 121
M 6,21,3
s_lshl_b32 s53, s52, 8
v_add_u32 v1, s37, v0
LK 40,248,10,0
CF 164,48,49
LK 44,250,10,2
s_add_u32 s48, %[alo], s53
CF 165,50,51
GS 8
s_addc_u32 s49, %[ahi], 0
CF 166,248,249
CF 167,250,251
M 7,32,2
WL 3
LO 120,16,120
LU 16,120
M 7,33,3
s_add_u32 s52, s52, %[d]
RA 216,1,0
HS 24,16
M 8,44,2
s_cmp_eq_u32 s52, %[wrap]
LL 18,76
LL 124,77
M 8,45,3
s_cselect_b32 s52, %[rst], s52
LA 18
DQ 3072
M 9,56,2
LC_ 124
v_and_b32 v22, 0xfffffcff, v1
M 9,57,3
LK 48,252,11,0
CF 168,40,41
LK 248,254,11,2
CF 169,42,43
GS 9
CF 170,44,45
CF 171,46,47
M 10,68,2
WL 3
LO 121,17,121
LU 17,121
M 10,69,3
s_add_u32 s50, s50, %[d]
RA 220,1,1024
HS 24,17
M 11,80,2
LL 19,78
LL 125,79
M 11,81,3
s_cmp_eq_u32 s50, %[wrap]
LA 19
RV 252,22,5120
M 6,22,4
s_cselect_b32 s50, %[rst], s50
LC_ 125
RV 36,8,6912
M 6,23,5
M 7,34,4
WL 5
LO 124,18,124
LU 18,124
M 7,35,5
s_add_u32 s38, s38, 32768
RA 224,1,2048
HS 24,18
GS 10
s_cmp_eq_u32 s38, s77
CF 172,48,49
CF 173,50,51
M 8,46,4
s_cselect_b32 s38, s76, s38
LL 16,80
LL 106,81
M 8,47,5
s_barrier
LA 16
CF 174,248,249
M 9,58,4
s_sub_u32 s39, 0, s39
LC_ 106
ds_read_b64 v[14:15], v5 offset:0
M 9,59,5
s_nop 0
v_subrev_u32 v5, s39, v5
CF 175,250,251
M 10,70,4
WL 5
LO 125,19,125
LU 19,125
M 10,71,5
RA 228,1,3072
HS 24,19
GS 11
M 11,82,4
LL 17,82
LL 107,83
M 11,83,5
LA 17
HX 24
Z 0,12,0
LC_ 107
W1 24,0
)"
R"(M 0,13,1
v_cndmask_b32 v52, v52, v53, s[78:79]
v_cndmask_b32 v54, v54, v55, s[78:79]
Z 1,24,0
WL 5
LO 106,16,106
LU 16,106
M 1,25,1
RA 232,1,4096
HZ 25,16
Z 2,36,0
LL 18,84
LL 110,85
M 2,37,1
WL 5
LA 18
v_add_f32 v14, v14, v15
Z 3,48,0
LC_ 110
v_add_f32_dpp v14, v14, v14 quad_perm:[1,0,3,2] row_mask:0xf bank_mask:0xf
M 3,49,1
s_add_u32 s42, s44, s60
v_add_f32_dpp v14, v14, v14 quad_perm:[2,3,0,1] row_mask:0xf bank_mask:0xf
v_cndmask_b32 v52, v52, v54, s[80:81]
Z 4,60,0
WL 4
LO 107,17,107
LU 17,107
M 4,61,1
s_addc_u32 s43, s45, 0
RA 236,1,6144
HS 25,17
Z 5,72,0
LL 19,86
LL 111,87
M 5,73,1
LA 19
ST 104
M 0,14,2
LC_ 111
v_add_f32_dpp v14, v14, v14 row_half_mirror row_mask:0xf bank_mask:0xf
M 0,15,3
v_cvt_f64_f32 v[14:15], v14
v_mul_f32 v52, 0x3b2aaaab, v52
M 1,26,2
WL 3
LO 110,18,110
LU 18,110
M 1,27,3
RA 216,1,7168
HS 25,18
M 2,38,2
LL 16,88
LL 114,89
M 2,39,3
LA 16
s_mov_b64 exec, s[54:55]
SP
s_mov_b64 exec, -1
M 3,50,2
LC_ 114
v_med3_f32 v53, v52, v39, v37
M 3,51,3
s_add_u32 s42, s44, s61
v_cmp_eq_f32 vcc, v52, v53
C 52,52,52
M 4,62,2
WL 3
LO 111,19,111
LU 19,111
M 4,63,3
s_addc_u32 s43, s45, 0
RA 220,1,8192
HS 25,19
M 5,74,2
LL 17,90
LL 115,91
M 5,75,3
LA 17
ST 108
M 0,16,4
s_mov_b32 m0, %[pm]
LC_ 115
DF
M 0,17,5
v_lshlrev_b32 v52, 16, v52
v_add_f32 v52, v36, v52
M 1,28,4
WL 3
LO 114,16,114
LU 16,114
M 1,29,5
RA 224,1,9216
HS 25,16
M 2,40,4
LL 18,92
LL 118,93
M 2,41,5
LA 18
C 52,52,52
M 3,52,4
LC_ 118
v_lshlrev_b32 v52, 16, v52
M 3,53,5
s_add_u32 s42, s44, s62
v_sqrt_f32 v52, v52
M 4,64,4
WL 3
LO 115,17,115
LU 17,115
M 4,65,5
s_addc_u32 s43, s45, 0
RA 228,1,10240
HS 25,17
M 5,76,4
LL 19,94
LL 119,95
M 5,77,5
LA 19
ST 112
M 0,18,0
LC_ 119
C 52,52,52
M 0,19,1
v_lshlrev_b32 v52, 16, v52
M 1,30,0
WL 3
LO 118,18,118
LU 18,118
M 1,31,1
RA 232,1,11264
HS 25,18
M 2,42,0
LL 16,96
LL 122,97
M 2,43,1
LA 16
RV 252,22,5392
M 3,54,0
LC_ 122
M 3,55,1
s_add_u32 s42, s44, s63
v_rcp_f32 v52, v52
M 4,66,0
WL 4
LO 119,19,119
LU 19,119
M 4,67,1
s_addc_u32 s43, s45, 0
RA 236,1,12288
HS 25,19
M 5,78,0
LL 17,98
LL 123,99
M 5,79,1
LA 17
ST 116
M 0,20,2
LC_ 123
v_mul_f32 v52, v38, v52
M 0,21,3
v_cndmask_b32 v52, 0, v52, vcc
CF 52,52,52
M 1,32,2
WL 3
LO 122,16,122
LU 16,122
M 1,33,3
RA 216,1,13312
HS 25,16
M 2,44,2
LL 18,100
LL 126,101
M 2,45,3
LA 18
ds_bpermute_b32 v13, v28, v52
M 3,56,2
LC_ 126
M 3,57,3
s_add_u32 s42, s44, s64
M 4,68,2
WL 4
LO 123,17,123
LU 17,123
M 4,69,3
s_addc_u32 s43, s45, 0
RA 220,1,14336
HS 25,17
M 5,80,2
LL 19,102
LL 127,103
M 5,81,3
LA 19
ST 120
M 0,22,4
LC_ 127
M 0,23,5
M 1,34,4
WL 3
LO 126,18,126
LU 18,126
M 1,35,5
RA 224,1,15360
HS 25,18
M 2,46,4
LL 16,56
LL 104,57
M 2,47,5
LA 16
XS 128
M 3,58,4
LC_ 104
XS 129
M 3,59,5
s_add_u32 s42, s44, s65
XS 130
XS 131
M 4,70,4
WL 3
LO 127,19,127
LU 19,127
M 4,71,5
s_addc_u32 s43, s45, 0
RA 228,1,16384
HS 25,19
M 5,82,4
LL 17,58
LL 105,59
M 5,83,5
LA 17
ST 124
Z 6,12,0
LC_ 105
HX 25
M 6,13,1
W1 25,1024
XS 132
Z 7,24,0
WL 4
LO 104,16,104
LU 16,104
M 7,25,1
RA 232,1,17408
HZ 24,16
Z 8,36,0
LL 18,60
LL 108,61
M 8,37,1
s_mul_i32 s53, s50, 25600
LA 18
v_add_u32 v4, s39, v4
Z 9,48,0
s_add_u32 s40, %[wlo], s53
LC_ 108
XS 133
M 9,49,1
s_addc_u32 s41, %[whi], 0
XS 134
XS 135
Z 10,60,0
WL 4
LO 105,17,105
LU 17,105
M 10,61,1
s_mov_b32 m0, s38
RA 236,1,18432
HS 24,17
Z 11,72,0
LL 19,62
LL 109,63
M 11,73,1
LA 19
DW 0
M 6,14,2
LC_ 109
v_min_u32 v35, v35, v13
M 6,15,3
XS 136
XS 137
M 7,26,2
WL 3
LO 108,18,108
LU 18,108
M 7,27,3
s_add_u32 s37, s37, 32768
RA 216,1,19456
HS 24,18
M 8,38,2
s_cmp_eq_u32 s37, s75
LL 16,64
LL 112,65
M 8,39,3
s_cselect_b32 s37, s74, s37
LA 16
XS 138
M 9,50,2
s_mul_i32 s53, %[d], 0
LC_ 112
XS 139
M 9,51,3
s_add_u32 s53, s53, s57
XS 140
XS 141
M 10,62,2
WL 3
LO 109,19,109
LU 19,109
M 10,63,3
s_add_u32 s46, %[xlo], s53
RA 220,1,20480
HS 24,19
M 11,74,2
LL 17,66
LL 113,67
M 11,75,3
s_addc_u32 s47, %[xhi], 0
LA 17
XS 142
M 6,16,4
s_mov_b32 s53, 0xda43cb52
LC_ 113
XS 143
M 6,17,5
s_cmp_eq_u32 %[d], 1
XS 144
XS 145
M 7,28,4
WL 3
LO 112,16,112
LU 16,112
M 7,29,5
s_cselect_b32 s53, s53, 0xfe987610
RA 224,1,21504
HS 24,16
M 8,40,4
s_bitcmp1_b64 s[92:93], 48
LL 18,68
LL 116,69
M 8,41,5
s_cselect_b32 s58, 16, 0
LA 18
XS 146
M 9,52,4
s_lshr_b32 s58, s53, s58
LC_ 116
v_lshrrev_b32 v3, 6, v0
v_and_b32 v3, 12, v3
v_bfe_u32 v3, s58, v3, 4
v_mad_u32_u24 v3, v3, s97, v21
M 9,53,5
DW 1024
XS 147
M 10,64,4
WL 3
LO 113,17,113
LU 17,113
M 10,65,5
RA 228,1,22528
HS 24,17
M 11,76,4
LL 19,70
LL 117,71
M 11,77,5
LA 19
XL 12,0
M 6,18,0
LC_ 117
DW 2048
M 6,19,1
s_mul_i32 s53, s51, 49152
XS 148
XS 149
M 7,30,0
WL 3
LO 116,18,116
LU 18,116
M 7,31,1
s_bitcmp1_b64 s[90:91], s51
RA 232,1,23552
HS 24,18
M 8,42,0
s_cselect_b64 s[44:45], s[84:85], s[82:83]
LL 16,72
LL 120,73
M 8,43,1
s_add_u32 s44, s44, s53
LA 16
s_addc_u32 s45, s45, 0
s_add_u32 s51, s51, %[d]
s_cmp_eq_u32 s51, %[wrap]
s_cselect_b32 s51, %[rst], s51
XL 13,64
M 9,54,0
s_add_u32 s40, s40, 3072
LC_ 120
s_addc_u32 s41, s41, 0
s_add_u32 m0, s38, 3072
M 9,55,1
XL 14,128
XS 150
)"
R"(M 10,66,0
WL 3
LO 117,19,117
LU 19,117
M 10,67,1
RA 236,1,24576
HS 24,19
M 11,78,0
LL 17,74
LL 121,75
M 11,79,1
LA 17
DW 0
M 6,20,2
LC_ 121
XL 15,192
M 6,21,3
DW 1024
v_add_u32 v1, s37, v0
M 7,32,2
WL 3
LO 120,16,120
LU 16,120
M 7,33,3
RA 216,1,0
HS 24,16
M 8,44,2
LL 18,76
LL 124,77
M 8,45,3
LA 18
XL 16,256
M 9,56,2
LC_ 124
DW 2048
M 9,57,3
s_lshl_b32 s53, s52, 8
v_and_b32 v22, 0xfffffcff, v1
XS 151
M 10,68,2
WL 3
LO 121,17,121
LU 17,121
M 10,69,3
s_add_u32 s48, %[alo], s53
RA 220,1,1024
HS 24,17
M 11,80,2
LL 19,78
LL 125,79
M 11,81,3
s_addc_u32 s49, %[ahi], 0
LA 19
RV 252,22,5120
M 6,22,4
s_add_u32 s52, s52, %[d]
LC_ 125
XS 152
M 6,23,5
s_cmp_eq_u32 s52, %[wrap]
XS 153
XS 154
M 7,34,4
WL 4
LO 124,18,124
LU 18,124
M 7,35,5
s_cselect_b32 s52, %[rst], s52
RA 224,1,2048
HS 24,18
M 8,46,4
LL 16,80
LL 106,81
M 8,47,5
LA 16
XL 17,320
DQ 3072
M 9,58,4
LC_ 106
XL 18,384
M 9,59,5
s_add_u32 s50, s50, %[d]
XS 155
XS 156
M 10,70,4
WL 3
LO 125,19,125
LU 19,125
M 10,71,5
RA 228,1,3072
HS 24,19
M 11,82,4
LL 17,82
LL 107,83
M 11,83,5
LA 17
HX 24
s_cmp_eq_u32 s50, %[wrap]
s_cselect_b32 s50, %[rst], s50
Z 0,0,0
LC_ 107
XL 19,448
M 0,1,1
s_add_u32 s38, s38, 32768
WV 12
BW 12
Z 1,24,0
WL 4
LO 106,16,106
LU 16,106
M 1,25,1
s_cmp_eq_u32 s38, s77
RA 232,1,4096
HZ 25,16
Z 2,36,0
s_cselect_b32 s38, s76, s38
LL 18,84
LL 110,85
M 2,37,1
s_barrier
LA 18
RV 40,8,6144
Z 3,48,0
s_sub_u32 s39, 0, s39
LC_ 110
ds_read_b64 v[14:15], v5 offset:0
M 3,49,1
XL 20,512
RV 44,8,6208
Z 4,60,0
WL 7
LO 107,17,107
LU 17,107
M 4,61,1
RA 236,1,6144
HS 25,17
Z 5,72,0
LL 19,86
LL 111,87
XS 157
M 5,73,1
LA 19
W1 24,0
M 0,2,2
LC_ 111
XL 21,576
M 0,3,3
s_add_u32 s42, s44, s60
RV 48,8,6272
BR 12
XS 158
M 1,26,2
WL 8
LO 110,18,110
LU 18,110
M 1,27,3
s_addc_u32 s43, s45, 0
RA 216,1,7168
HS 25,18
XS 159
M 2,38,2
LL 16,88
LL 114,89
M 2,39,3
LA 16
XL 22,640
M 3,50,2
LC_ 114
ST 104
XS 160
M 3,51,3
XL 23,704
v_subrev_u32 v5, s39, v5
M 4,62,2
WL 5
LO 111,19,111
LU 19,111
M 4,63,3
s_add_u32 s42, s44, s61
RA 220,1,8192
HS 25,19
XS 161
M 5,74,2
LL 17,90
LL 115,91
M 5,75,3
s_addc_u32 s43, s45, 0
LA 17
ST 108
M 0,4,4
s_mov_b32 m0, %[pm]
LC_ 115
DF
XS 162
M 0,5,5
WV 17
BW 13
M 1,28,4
WL 4
LO 114,16,114
LU 16,114
XS 163
M 1,29,5
RA 224,1,9216
HS 25,16
M 2,40,4
LL 18,92
LL 118,93
M 2,41,5
LA 18
BR 13
XS 164
LZ 40,12
WV 16
BW 14
M 3,52,4
LC_ 118
RV 40,8,6336
M 3,53,5
s_add_u32 s42, s44, s62
BR 14
v_add_f32 v14, v14, v15
s_nop 1
v_add_f32_dpp v14, v14, v14 quad_perm:[1,0,3,2] row_mask:0xf bank_mask:0xf
s_nop 1
v_add_f32_dpp v14, v14, v14 quad_perm:[2,3,0,1] row_mask:0xf bank_mask:0xf
s_nop 1
v_add_f32_dpp v14, v14, v14 row_half_mirror row_mask:0xf bank_mask:0xf
v_cvt_f64_f32 v[14:15], v14
s_mov_b64 exec, s[54:55]
SP
s_mov_b64 exec, -1
M 4,64,4
WL 8
LO 115,17,115
LU 17,115
XS 165
M 4,65,5
s_addc_u32 s43, s45, 0
RA 228,1,10240
HS 25,17
M 5,76,4
LL 19,94
LL 119,95
XS 166
M 5,77,5
WL 5
LA 19
ST 112
LS 44,13
WV 16
BW 15
M 0,6,0
LC_ 119
RV 44,8,6400
M 0,7,1
WL 5
BR 15
XS 167
LS 48,14
WV 14
BW 16
M 1,30,0
LO 118,18,118
LU 18,118
M 1,31,1
RA 232,1,11264
HS 25,18
M 2,42,0
LL 16,96
LL 122,97
XS 168
M 2,43,1
LA 16
RV 252,22,5392
M 3,54,0
LC_ 122
RV 48,8,6464
XS 169
M 3,55,1
s_add_u32 s42, s44, s63
BR 16
XS 170
M 4,66,0
WL 7
LO 119,19,119
LU 19,119
M 4,67,1
s_addc_u32 s43, s45, 0
RA 236,1,12288
HS 25,19
LS 40,15
WV 12
BW 17
M 5,78,0
LL 17,98
LL 123,99
M 5,79,1
LA 17
ST 116
M 0,8,2
LC_ 123
RV 40,8,6528
XS 171
M 0,9,3
BR 17
XS 172
M 1,32,2
WL 8
LO 122,16,122
LU 16,122
M 1,33,3
WL 6
RA 216,1,13312
HS 25,16
LS 44,16
WV 11
BW 18
M 2,44,2
LL 18,100
LL 126,101
M 2,45,3
LA 18
RV 44,8,6592
XS 173
M 3,56,2
LC_ 126
BR 18
M 3,57,3
s_add_u32 s42, s44, s64
XS 174
XS 175
M 4,68,2
WL 6
LO 123,17,123
LU 17,123
M 4,69,3
s_addc_u32 s43, s45, 0
RA 220,1,14336
HS 25,17
LS 48,17
WV 10
BW 19
M 5,80,2
LL 19,102
LL 127,103
M 5,81,3
LA 19
ST 120
M 0,10,4
LC_ 127
RV 48,8,6656
M 0,11,5
BR 19
M 1,34,4
WL 7
LO 126,18,126
LU 18,126
M 1,35,5
WL 6
RA 224,1,15360
HS 25,18
LS 40,18
WV 10
BW 20
M 2,46,4
LL 16,56
LL 104,57
M 2,47,5
LA 16
RV 40,8,6720
M 3,58,4
LC_ 104
BR 20
M 3,59,5
s_add_u32 s42, s44, s65
M 4,70,4
WL 6
LO 127,19,127
LU 19,127
M 4,71,5
s_addc_u32 s43, s45, 0
RA 228,1,16384
HS 25,19
LS 44,19
WV 9
BW 21
M 5,82,4
LL 17,58
LL 105,59
M 5,83,5
LA 17
ST 124
Z 6,0,0
LC_ 105
RV 44,8,6784
M 6,1,1
BR 21
HX 25
Z 7,24,0
)"
R"(WL 7
LO 104,16,104
LU 16,104
M 7,25,1
WL 6
RA 232,1,17408
HZ 26,16
LS 48,20
WV 9
BW 22
Z 8,36,0
LL 18,60
LL 108,61
M 8,37,1
s_mul_i32 s53, s50, 25600
LA 18
W1 25,1024
Z 9,48,0
s_add_u32 s40, %[wlo], s53
LC_ 108
RV 48,8,6848
M 9,49,1
s_addc_u32 s41, %[whi], 0
RA 240,7,0
BR 22
Z 10,60,0
WL 8
LO 105,17,105
LU 17,105
M 10,61,1
s_mov_b32 m0, s38
RA 236,1,18432
HS 26,17
LS 40,21
WV 7
BW 23
Z 11,72,0
LL 19,62
LL 109,63
M 11,73,1
LA 19
DW 0
M 6,2,2
LC_ 109
RA 244,7,256
M 6,3,3
BR 23
v_add_u32 v4, s39, v4
M 7,26,2
WL 6
LO 108,18,108
LU 18,108
M 7,27,3
s_add_u32 s37, s37, 32768
RA 216,1,19456
HZ 24,18
M 8,38,2
s_cmp_eq_u32 s37, s75
LL 16,64
LL 112,65
M 8,39,3
s_cselect_b32 s37, s74, s37
LA 16
DW 1024
LS 44,22
RA 248,7,512
HX 26
M 9,50,2
LC_ 112
M 9,51,3
M 10,62,2
WL 6
LO 109,19,109
LU 19,109
M 10,63,3
WL 4
RA 220,1,20480
HS 24,19
LS 48,23
RA 252,7,768
M 11,74,2
LL 17,66
LL 113,67
M 11,75,3
LA 17
DW 2048
M 6,4,4
LC_ 113
v_mul_f32 v36, 0xbb2aaaab, v36
M 6,5,5
v_mul_f32 v37, 0xbb2aaaab, v37
v_mul_f32 v38, 0xbb2aaaab, v38
M 7,28,4
WL 4
LO 112,16,112
LU 16,112
M 7,29,5
s_mul_i32 s53, s51, 49152
RA 224,1,21504
HS 24,16
M 8,40,4
s_bitcmp1_b64 s[90:91], s51
LL 18,68
LL 116,69
M 8,41,5
s_cselect_b64 s[44:45], s[84:85], s[82:83]
LA 18
v_mul_f32 v39, 0xbb2aaaab, v39
M 9,52,4
s_add_u32 s44, s44, s53
LC_ 116
M 9,53,5
s_addc_u32 s45, s45, 0
LK 40,240,12,0
LK 44,242,12,2
s_bitcmp1_b64 s[92:93], s51
RA 240,7,1024
LK 48,244,13,0
s_nop 0
CF 176,40,41
LK 248,246,13,2
s_cselect_b64 s[94:95], s[88:89], s[86:87]
RA 244,7,1280
CF 177,42,43
M 10,64,4
WL 5
LO 113,17,113
LU 17,113
M 10,65,5
s_add_u32 s94, s94, s53
RA 228,1,22528
HS 24,17
M 11,76,4
LL 19,70
LL 117,71
M 11,77,5
s_addc_u32 s95, s95, 0
LA 19
CF 178,44,45
M 6,6,0
s_add_u32 s51, s51, %[d]
LC_ 117
CF 179,46,47
M 6,7,1
s_cmp_eq_u32 s51, %[wrap]
LK 40,248,14,0
CF 180,48,49
LK 44,250,14,2
s_cselect_b32 s51, %[rst], s51
RA 248,7,1536
CF 181,50,51
GZ 12
s_mul_i32 s53, %[d], 0
CF 182,248,249
CF 183,250,251
LK 48,252,15,0
WL 5
CF 184,40,41
LK 248,254,15,2
s_add_u32 s60, s53, %[sb1]
RA 252,7,1792
CF 185,42,43
GS 13
s_add_u32 s40, s40, 3072
CF 186,44,45
CF 187,46,47
LK 40,240,16,0
WL 5
CF 188,48,49
LK 44,242,16,2
s_addc_u32 s41, s41, 0
RA 240,7,2048
CF 189,50,51
GS 14
s_add_u32 m0, s38, 3072
CF 190,248,249
CF 191,250,251
LK 48,244,17,0
CF 192,40,41
LK 248,246,17,2
RA 244,7,2304
CF 193,42,43
GS 15
CF 194,44,45
CF 195,46,47
M 7,30,0
LO 116,18,116
LU 18,116
M 7,31,1
WL 3
RA 232,1,23552
HS 24,18
LK 40,248,18,0
CF 196,48,49
LK 44,250,18,2
RA 248,7,2560
CF 197,50,51
GS 16
WL 4
CF 198,248,249
CF 199,250,251
LK 48,252,19,0
CF 200,40,41
LK 248,254,19,2
RA 252,7,2816
CF 201,42,43
GS 17
WL 4
CF 202,44,45
CF 203,46,47
LK 40,240,20,0
CF 204,48,49
LK 44,242,20,2
CF 205,50,51
GS 18
CF 206,248,249
CF 207,250,251
M 8,42,0
LL 16,72
LL 120,73
M 8,43,1
LA 16
DW 0
M 9,54,0
LC_ 120
DW 1024
M 9,55,1
WL 5
LK 48,244,21,0
CF 208,40,41
LK 248,246,21,2
CF 209,42,43
GS 19
CF 210,44,45
CF 211,46,47
M 10,66,0
LO 117,19,117
LU 19,117
M 10,67,1
RA 236,1,24576
HS 24,19
M 11,78,0
LL 17,74
LL 121,75
M 11,79,1
LA 17
DW 2048
M 6,8,2
WL 5
LC_ 121
M 6,9,3
s_lshl_b32 s53, s52, 8
v_add_u32 v1, s37, v0
LK 40,248,22,0
CF 212,48,49
LK 44,250,22,2
s_add_u32 s48, %[alo], s53
CF 213,50,51
GS 20
s_addc_u32 s49, %[ahi], 0
CF 214,248,249
CF 215,250,251
M 7,32,2
WL 3
LO 120,16,120
LU 16,120
M 7,33,3
s_add_u32 s52, s52, %[d]
RA 216,1,0
HS 24,16
M 8,44,2
s_cmp_eq_u32 s52, %[wrap]
LL 18,76
LL 124,77
M 8,45,3
s_cselect_b32 s52, %[rst], s52
LA 18
DQ 3072
M 9,56,2
LC_ 124
v_and_b32 v22, 0xfffffcff, v1
M 9,57,3
LK 48,252,23,0
CF 216,40,41
LK 248,254,23,2
CF 217,42,43
GS 21
CF 218,44,45
CF 219,46,47
M 10,68,2
WL 3
LO 121,17,121
LU 17,121
M 10,69,3
s_add_u32 s50, s50, %[d]
RA 220,1,1024
HS 24,17
M 11,80,2
LL 19,78
LL 125,79
M 11,81,3
s_cmp_eq_u32 s50, %[wrap]
LA 19
RV 252,22,5120
M 6,10,4
s_cselect_b32 s50, %[rst], s50
LC_ 125
RV 36,8,6912
M 6,11,5
M 7,34,4
WL 5
LO 124,18,124
LU 18,124
M 7,35,5
s_add_u32 s38, s38, 32768
RA 224,1,2048
HS 24,18
GS 22
s_cmp_eq_u32 s38, s77
CF 220,48,49
CF 221,50,51
M 8,46,4
s_cselect_b32 s38, s76, s38
LL 16,80
LL 106,81
M 8,47,5
s_barrier
LA 16
CF 222,248,249
M 9,58,4
s_sub_u32 s39, 0, s39
LC_ 106
ds_read_b64 v[14:15], v5 offset:0
M 9,59,5
s_nop 0
v_subrev_u32 v5, s39, v5
CF 223,250,251
M 10,70,4
WL 5
LO 125,19,125
LU 19,125
M 10,71,5
RA 228,1,3072
HS 24,19
GS 23
M 11,82,4
LL 17,82
LL 107,83
M 11,83,5
LA 17
HX 24
Z 0,0,0
LC_ 107
W1 24,0
M 0,1,1
W1 26,2048
v_cndmask_b32 v52, v52, v53, s[78:79]
Z 1,24,0
WL 6
LO 106,16,106
LU 16,106
M 1,25,1
RA 232,1,4096
HZ 27,16
Z 2,36,0
LL 18,84
LL 110,85
M 2,37,1
WL 6
LA 18
v_add_f32 v14, v14, v15
Z 3,48,0
LC_ 110
v_add_f32_dpp v14, v14, v14 quad_perm:[1,0,3,2] row_mask:0xf bank_mask:0xf
M 3,49,1
s_add_u32 s42, s94, s60
v_add_f32_dpp v14, v14, v14 quad_perm:[2,3,0,1] row_mask:0xf bank_mask:0xf
v_cndmask_b32 v54, v54, v55, s[78:79]
Z 4,60,0
WL 5
LO 107,17,107
LU 17,107
M 4,61,1
s_addc_u32 s43, s95, 0
RA 236,1,6144
HS 27,17
Z 5,72,0
LL 19,86
LL 111,87
M 5,73,1
LA 19
SU 104
M 0,2,2
LC_ 111
v_add_f32_dpp v14, v14, v14 row_half_mirror row_mask:0xf bank_mask:0xf
M 0,3,3
v_cvt_f64_f32 v[14:15], v14
HX 27
M 1,26,2
WL 3
)"
R"(LO 110,18,110
LU 18,110
M 1,27,3
RA 216,1,7168
HZ 25,18
M 2,38,2
LL 16,88
LL 114,89
M 2,39,3
LA 16
s_mov_b64 exec, s[54:55]
SP
s_mov_b64 exec, -1
M 3,50,2
LC_ 114
v_cndmask_b32 v52, v52, v54, s[80:81]
M 3,51,3
s_add_u32 s42, s44, s61
v_mul_f32 v52, 0x3b2aaaab, v52
v_med3_f32 v53, v52, v39, v37
M 4,62,2
WL 3
LO 111,19,111
LU 19,111
M 4,63,3
s_addc_u32 s43, s45, 0
RA 220,1,8192
HS 25,19
M 5,74,2
LL 17,90
LL 115,91
M 5,75,3
LA 17
ST 108
M 0,4,4
s_mov_b32 m0, %[pm]
LC_ 115
DF
M 0,5,5
v_cmp_eq_f32 vcc, v52, v53
C 52,52,52
M 1,28,4
WL 3
LO 114,16,114
LU 16,114
M 1,29,5
RA 224,1,9216
HS 25,16
M 2,40,4
LL 18,92
LL 118,93
M 2,41,5
LA 18
v_lshlrev_b32 v52, 16, v52
M 3,52,4
LC_ 118
v_add_f32 v52, v36, v52
M 3,53,5
s_add_u32 s42, s44, s62
C 52,52,52
v_lshlrev_b32 v52, 16, v52
M 4,64,4
WL 3
LO 115,17,115
LU 17,115
M 4,65,5
s_addc_u32 s43, s45, 0
RA 228,1,10240
HS 25,17
M 5,76,4
LL 19,94
LL 119,95
M 5,77,5
LA 19
ST 112
M 0,6,0
LC_ 119
M 0,7,1
v_sqrt_f32 v52, v52
M 1,30,0
WL 3
LO 118,18,118
LU 18,118
M 1,31,1
RA 232,1,11264
HS 25,18
M 2,42,0
LL 16,96
LL 122,97
M 2,43,1
LA 16
RV 252,22,5392
M 3,54,0
LC_ 122
C 52,52,52
M 3,55,1
s_add_u32 s42, s44, s63
v_lshlrev_b32 v52, 16, v52
M 4,66,0
WL 4
LO 119,19,119
LU 19,119
M 4,67,1
s_addc_u32 s43, s45, 0
RA 236,1,12288
HS 25,19
M 5,78,0
LL 17,98
LL 123,99
M 5,79,1
LA 17
ST 116
M 0,8,2
LC_ 123
M 0,9,3
v_rcp_f32 v52, v52
M 1,32,2
WL 3
LO 122,16,122
LU 16,122
M 1,33,3
RA 216,1,13312
HS 25,16
M 2,44,2
LL 18,100
LL 126,101
M 2,45,3
LA 18
v_mul_f32 v52, v38, v52
M 3,56,2
LC_ 126
v_cndmask_b32 v52, 0, v52, vcc
M 3,57,3
s_add_u32 s42, s44, s64
CF 52,52,52
M 4,68,2
WL 3
LO 123,17,123
LU 17,123
M 4,69,3
s_addc_u32 s43, s45, 0
RA 220,1,14336
HS 25,17
M 5,80,2
LL 19,102
LL 127,103
M 5,81,3
LA 19
ST 120
M 0,10,4
LC_ 127
ds_bpermute_b32 v13, v28, v52
M 0,11,5
M 1,34,4
WL 4
LO 126,18,126
LU 18,126
M 1,35,5
RA 224,1,15360
HS 25,18
M 2,46,4
LL 16,56
LL 104,57
M 2,47,5
LA 16
M 3,58,4
LC_ 104
M 3,59,5
s_add_u32 s42, s44, s65
M 4,70,4
WL 4
LO 127,19,127
LU 19,127
M 4,71,5
s_addc_u32 s43, s45, 0
RA 228,1,16384
HS 25,19
M 5,82,4
LL 17,58
LL 105,59
M 5,83,5
LA 17
ST 124
Z 6,0,0
LC_ 105
HX 25
M 6,1,1
WL 6
W1 25,1024
XS 176
Z 7,24,0
WL 4
LO 104,16,104
LU 16,104
M 7,25,1
RA 232,1,17408
HZ 26,16
Z 8,36,0
LL 18,60
LL 108,61
XS 177
M 8,37,1
s_mul_i32 s53, s50, 25600
LA 18
W1 27,3072
Z 9,48,0
s_add_u32 s40, %[wlo], s53
LC_ 108
v_add_u32 v4, s39, v4
M 9,49,1
s_addc_u32 s41, %[whi], 0
XS 178
XS 179
Z 10,60,0
WL 5
LO 105,17,105
LU 17,105
M 10,61,1
s_mov_b32 m0, s38
RA 236,1,18432
HS 26,17
Z 11,72,0
LL 19,62
LL 109,63
M 11,73,1
LA 19
DW 0
M 6,2,2
LC_ 109
HX 26
XS 180
M 6,3,3
XS 181
XS 182
M 7,26,2
WL 3
LO 108,18,108
LU 18,108
M 7,27,3
s_add_u32 s37, s37, 32768
RA 216,1,19456
HZ 24,18
M 8,38,2
s_cmp_eq_u32 s37, s75
LL 16,64
LL 112,65
M 8,39,3
s_cselect_b32 s37, s74, s37
LA 16
XS 183
M 9,50,2
s_mul_i32 s53, %[d], 196608
LC_ 112
v_min_u32 v35, v35, v13
M 9,51,3
s_add_u32 s53, s53, s57
XS 184
XS 185
M 10,62,2
WL 3
LO 109,19,109
LU 19,109
M 10,63,3
s_add_u32 s46, %[xlo], s53
RA 220,1,20480
HS 24,19
M 11,74,2
LL 17,66
LL 113,67
M 11,75,3
s_addc_u32 s47, %[xhi], 0
LA 17
XS 186
M 6,4,4
s_mov_b32 s53, 0xfe987610
LC_ 113
XS 187
M 6,5,5
s_cmp_eq_u32 %[d], 1
XS 188
XS 189
M 7,28,4
WL 3
LO 112,16,112
LU 16,112
M 7,29,5
s_cselect_b32 s53, s53, 0xda43cb52
RA 224,1,21504
HS 24,16
M 8,40,4
s_bitcmp1_b64 s[92:93], 48
LL 18,68
LL 116,69
M 8,41,5
s_cselect_b32 s58, 16, 0
LA 18
XS 190
M 9,52,4
s_lshr_b32 s58, s53, s58
LC_ 116
v_lshrrev_b32 v3, 6, v0
v_and_b32 v3, 12, v3
v_bfe_u32 v3, s58, v3, 4
v_mad_u32_u24 v3, v3, s97, v21
M 9,53,5
DW 1024
XS 191
M 10,64,4
WL 3
LO 113,17,113
LU 17,113
M 10,65,5
RA 228,1,22528
HS 24,17
M 11,76,4
LL 19,70
LL 117,71
M 11,77,5
LA 19
XL 24,0
M 6,6,0
LC_ 117
DW 2048
M 6,7,1
s_mul_i32 s53, s51, 49152
XS 192
XS 193
M 7,30,0
WL 3
LO 116,18,116
LU 18,116
M 7,31,1
s_bitcmp1_b64 s[90:91], s51
RA 232,1,23552
HS 24,18
M 8,42,0
s_cselect_b64 s[44:45], s[84:85], s[82:83]
LL 16,72
LL 120,73
M 8,43,1
s_add_u32 s44, s44, s53
LA 16
s_addc_u32 s45, s45, 0
s_bitcmp1_b64 s[92:93], s51
s_cselect_b64 s[94:95], s[88:89], s[86:87]
s_add_u32 s94, s94, s53
s_addc_u32 s95, s95, 0
s_add_u32 s51, s51, %[d]
s_cmp_eq_u32 s51, %[wrap]
s_cselect_b32 s51, %[rst], s51
XL 25,64
M 9,54,0
s_add_u32 s40, s40, 3072
LC_ 120
s_addc_u32 s41, s41, 0
s_add_u32 m0, s38, 3072
M 9,55,1
XL 26,128
XS 194
M 10,66,0
WL 3
LO 117,19,117
LU 19,117
M 10,67,1
RA 236,1,24576
HS 24,19
M 11,78,0
LL 17,74
LL 121,75
M 11,79,1
LA 17
DW 0
M 6,8,2
LC_ 121
XL 27,192
M 6,9,3
DW 1024
v_add_u32 v1, s37, v0
M 7,32,2
WL 3
LO 120,16,120
LU 16,120
M 7,33,3
RA 216,1,0
HS 24,16
M 8,44,2
LL 18,76
LL 124,77
M 8,45,3
LA 18
XL 28,256
M 9,56,2
LC_ 124
DW 2048
M 9,57,3
s_lshl_b32 s53, s52, 8
v_and_b32 v22, 0xfffffcff, v1
XS 195
M 10,68,2
WL 3
)"
R"(LO 121,17,121
LU 17,121
M 10,69,3
s_add_u32 s48, %[alo], s53
RA 220,1,1024
HS 24,17
M 11,80,2
LL 19,78
LL 125,79
M 11,81,3
s_addc_u32 s49, %[ahi], 0
LA 19
RV 252,22,5120
XS 196
M 6,10,4
s_add_u32 s52, s52, %[d]
LC_ 125
XS 197
M 6,11,5
s_cmp_eq_u32 s52, %[wrap]
XS 198
XS 199
M 7,34,4
WL 4
LO 124,18,124
LU 18,124
M 7,35,5
s_cselect_b32 s52, %[rst], s52
RA 224,1,2048
HS 24,18
M 8,46,4
LL 16,80
LL 106,81
M 8,47,5
LA 16
XL 29,320
DQ 3072
M 9,58,4
LC_ 106
XL 30,384
M 9,59,5
s_add_u32 s50, s50, %[d]
XS 200
XS 201
M 10,70,4
WL 3
LO 125,19,125
LU 19,125
M 10,71,5
RA 228,1,3072
HS 24,19
M 11,82,4
LL 17,82
LL 107,83
M 11,83,5
LA 17
HX 24
s_cmp_eq_u32 s50, %[wrap]
s_cselect_b32 s50, %[rst], s50
Z 0,0,0
LC_ 107
XL 31,448
M 0,1,1
s_add_u32 s38, s38, 32768
WV 12
BW 24
Z 1,12,0
WL 4
LO 106,16,106
LU 16,106
M 1,13,1
s_cmp_eq_u32 s38, s77
RA 232,1,4096
HZ 27,16
XS 202
Z 2,36,0
s_cselect_b32 s38, s76, s38
LL 18,84
LL 110,85
M 2,37,1
s_barrier
LA 18
RV 40,8,6144
Z 3,48,0
s_sub_u32 s39, 0, s39
LC_ 110
ds_read_b64 v[14:15], v5 offset:0
XS 203
M 3,49,1
XL 32,512
RV 44,8,6208
Z 4,60,0
WL 7
LO 107,17,107
LU 17,107
M 4,61,1
RA 236,1,6144
HS 27,17
XS 204
Z 5,72,0
LL 19,86
LL 111,87
M 5,73,1
LA 19
W1 24,0
XS 205
M 0,2,2
LC_ 111
W1 26,2048
M 0,3,3
XL 33,576
RV 48,8,6272
M 1,14,2
WL 8
LO 110,18,110
LU 18,110
XS 206
M 1,15,3
s_add_u32 s42, s94, s60
RA 216,1,7168
HZ 25,18
M 2,38,2
s_addc_u32 s43, s95, 0
LL 16,88
LL 114,89
M 2,39,3
LA 16
XL 34,640
XS 207
M 3,50,2
LC_ 114
SU 104
M 3,51,3
XL 35,704
BR 24
M 4,62,2
WL 6
LO 111,19,111
LU 19,111
XS 208
M 4,63,3
s_add_u32 s42, s44, s61
RA 220,1,8192
HS 25,19
M 5,74,2
LL 17,90
LL 115,91
XS 209
M 5,75,3
s_addc_u32 s43, s45, 0
LA 17
ST 108
M 0,4,4
s_mov_b32 m0, %[pm]
LC_ 115
DF
M 0,5,5
WV 17
BW 25
HX 27
XS 210
M 1,16,4
WL 5
LO 114,16,114
LU 16,114
M 1,17,5
RA 224,1,9216
HS 25,16
M 2,40,4
LL 18,92
LL 118,93
XS 211
M 2,41,5
WL 5
LA 18
BR 25
LZ 40,24
WV 16
BW 26
M 3,52,4
LC_ 118
RV 40,8,6336
XS 212
M 3,53,5
s_add_u32 s42, s44, s62
BR 26
v_add_f32 v14, v14, v15
s_nop 1
v_add_f32_dpp v14, v14, v14 quad_perm:[1,0,3,2] row_mask:0xf bank_mask:0xf
s_nop 1
v_add_f32_dpp v14, v14, v14 quad_perm:[2,3,0,1] row_mask:0xf bank_mask:0xf
s_nop 1
v_add_f32_dpp v14, v14, v14 row_half_mirror row_mask:0xf bank_mask:0xf
v_cvt_f64_f32 v[14:15], v14
s_mov_b64 exec, s[54:55]
SP
s_mov_b64 exec, -1
s_nop 0
ds_read_b64 v[14:15], v5 offset:2048
M 4,64,4
WL 9
LO 115,17,115
LU 17,115
M 4,65,5
s_addc_u32 s43, s45, 0
RA 228,1,10240
HS 25,17
XS 213
M 5,76,4
LL 19,94
LL 119,95
M 5,77,5
WL 6
LA 19
ST 112
LS 44,25
WV 16
BW 27
M 0,6,0
LC_ 119
RV 44,8,6400
XS 214
M 0,7,1
WL 6
BR 27
v_subrev_u32 v5, s39, v5
LS 48,26
WV 14
BW 28
M 1,18,0
LO 118,18,118
LU 18,118
M 1,19,1
RA 232,1,11264
HS 25,18
XS 215
M 2,42,0
LL 16,96
LL 122,97
M 2,43,1
LA 16
RV 252,22,5392
XS 216
M 3,54,0
WL 10
LC_ 122
RV 48,8,6464
M 3,55,1
s_add_u32 s42, s44, s63
BR 28
v_add_f32 v14, v14, v15
s_nop 1
v_add_f32_dpp v14, v14, v14 quad_perm:[1,0,3,2] row_mask:0xf bank_mask:0xf
s_nop 1
v_add_f32_dpp v14, v14, v14 quad_perm:[2,3,0,1] row_mask:0xf bank_mask:0xf
M 4,66,0
WL 7
LO 119,19,119
LU 19,119
XS 217
M 4,67,1
s_addc_u32 s43, s45, 0
RA 236,1,12288
HS 25,19
v_add_f32_dpp v14, v14, v14 row_half_mirror row_mask:0xf bank_mask:0xf
LS 40,27
WV 12
BW 29
M 5,78,0
LL 17,98
LL 123,99
M 5,79,1
LA 17
ST 116
v_cvt_f64_f32 v[14:15], v14
XS 218
M 0,8,2
LC_ 123
s_mov_b64 exec, s[54:55]
SP
s_mov_b64 exec, -1
M 0,9,3
WL 4
RV 40,8,6528
BR 29
LS 44,28
WV 12
BW 30
M 1,20,2
LO 122,16,122
LU 16,122
XS 219
M 1,21,3
RA 216,1,13312
HS 25,16
M 2,44,2
LL 18,100
LL 126,101
XS 220
M 2,45,3
LA 18
RV 44,8,6592
M 3,56,2
LC_ 126
BR 30
M 3,57,3
s_add_u32 s42, s44, s64
XS 221
XS 222
M 4,68,2
WL 6
LO 123,17,123
LU 17,123
M 4,69,3
s_addc_u32 s43, s45, 0
RA 220,1,14336
HS 25,17
LS 48,29
WV 11
BW 31
M 5,80,2
LL 19,102
LL 127,103
M 5,81,3
LA 19
ST 120
M 0,10,4
LC_ 127
RV 48,8,6656
XS 223
M 0,11,5
BR 31
M 1,22,4
WL 7
LO 126,18,126
LU 18,126
M 1,23,5
WL 6
RA 224,1,15360
HS 25,18
LS 40,30
WV 11
BW 32
M 2,46,4
LL 16,56
LL 104,57
M 2,47,5
LA 16
RV 40,8,6720
M 3,58,4
LC_ 104
BR 32
M 3,59,5
s_add_u32 s42, s44, s65
M 4,70,4
WL 6
LO 127,19,127
LU 19,127
M 4,71,5
s_addc_u32 s43, s45, 0
RA 228,1,16384
HS 25,19
LS 44,31
WV 10
BW 33
M 5,82,4
LL 17,58
LL 105,59
M 5,83,5
LA 17
ST 124
Z 6,0,0
LC_ 105
RV 44,8,6784
M 6,1,1
BR 33
HX 25
Z 7,12,0
WL 7
LO 104,16,104
LU 16,104
M 7,13,1
WL 6
RA 232,1,17408
HZ 26,16
LS 48,32
WV 10
BW 34
Z 8,36,0
LL 18,60
LL 108,61
M 8,37,1
s_mul_i32 s53, s50, 25600
LA 18
W1 25,1024
Z 9,48,0
s_add_u32 s40, %[wlo], s53
LC_ 108
W1 27,3072
M 9,49,1
)"
R"(s_addc_u32 s41, %[whi], 0
RV 48,8,6848
RA 240,7,0
Z 10,60,0
WL 10
LO 105,17,105
LU 17,105
M 10,61,1
s_mov_b32 m0, s38
RA 236,1,18432
HS 26,17
Z 11,72,0
LL 19,62
LL 109,63
M 11,73,1
LA 19
DW 0
M 6,2,2
LC_ 109
BR 34
M 6,3,3
WL 12
v_add_u32 v4, s39, v4
LS 40,33
WV 9
BW 35
M 7,14,2
WL 8
LO 108,18,108
LU 18,108
M 7,15,3
s_add_u32 s37, s37, 32768
RA 216,1,19456
HZ 29,18
M 8,38,2
s_cmp_eq_u32 s37, s75
LL 16,64
LL 112,65
M 8,39,3
s_cselect_b32 s37, s74, s37
LA 16
DW 1024
M 9,50,2
LC_ 112
RA 244,7,256
M 9,51,3
BR 35
M 10,62,2
WL 7
LO 109,19,109
LU 19,109
M 10,63,3
WL 6
RA 220,1,20480
HS 29,19
LS 44,34
RA 248,7,512
M 11,74,2
LL 17,66
LL 113,67
M 11,75,3
LA 17
DW 2048
M 6,4,4
LC_ 113
v_pk_add_f16 v26, v29, v26
M 6,5,5
HX 26
M 7,16,4
WL 4
LO 112,16,112
LU 16,112
M 7,17,5
s_mul_i32 s53, s51, 49152
RA 224,1,21504
HZ 24,16
LS 48,35
s_bitcmp1_b64 s[90:91], s51
RA 252,7,768
M 8,40,4
s_cselect_b64 s[44:45], s[84:85], s[82:83]
LL 18,68
LL 116,69
M 8,41,5
s_add_u32 s44, s44, s53
LA 18
v_mul_f32 v36, 0xbb2aaaab, v36
M 9,52,4
s_addc_u32 s45, s45, 0
LC_ 116
v_mul_f32 v37, 0xbb2aaaab, v37
M 9,53,5
s_bitcmp1_b64 s[92:93], s51
v_mul_f32 v38, 0xbb2aaaab, v38
v_mul_f32 v39, 0xbb2aaaab, v39
M 10,64,4
WL 4
LO 113,17,113
LU 17,113
M 10,65,5
s_cselect_b64 s[94:95], s[88:89], s[86:87]
RA 228,1,22528
HS 24,17
LK 40,240,24,0
LK 44,242,24,2
s_add_u32 s94, s94, s53
RA 240,7,1024
LK 48,244,25,0
s_nop 0
CF 224,40,41
LK 248,246,25,2
s_addc_u32 s95, s95, 0
RA 244,7,1280
CF 225,42,43
M 11,76,4
LL 19,70
LL 117,71
M 11,77,5
s_add_u32 s51, s51, %[d]
LA 19
CF 226,44,45
M 6,6,0
s_cmp_eq_u32 s51, %[wrap]
LC_ 117
CF 227,46,47
M 6,7,1
s_cselect_b32 s51, %[rst], s51
LK 40,248,26,0
WL 7
CF 228,48,49
LK 44,250,26,2
s_mul_i32 s53, %[d], 1024
RA 248,7,1536
CF 229,50,51
GZ 24
s_add_u32 s61, s53, %[sb1]
CF 230,248,249
CF 231,250,251
LK 48,252,27,0
WL 4
CF 232,40,41
LK 248,254,27,2
s_add_u32 s40, s40, 3072
RA 252,7,1792
CF 233,42,43
GS 25
s_addc_u32 s41, s41, 0
CF 234,44,45
CF 235,46,47
LK 40,240,28,0
CF 236,48,49
LK 44,242,28,2
s_add_u32 m0, s38, 3072
RA 240,7,2048
CF 237,50,51
GS 26
WL 5
CF 238,248,249
CF 239,250,251
LK 48,244,29,0
CF 240,40,41
LK 248,246,29,2
RA 244,7,2304
CF 241,42,43
GS 27
CF 242,44,45
CF 243,46,47
M 7,18,0
LO 116,18,116
LU 18,116
M 7,19,1
WL 3
RA 232,1,23552
HS 24,18
LK 40,248,30,0
CF 244,48,49
LK 44,250,30,2
RA 248,7,2560
CF 245,50,51
GS 28
WL 4
CF 246,248,249
CF 247,250,251
LK 48,252,31,0
CF 40,40,41
LK 248,254,31,2
RA 252,7,2816
CF 42,42,43
GS 29
CF 44,44,45
CF 46,46,47
M 8,42,0
LL 16,72
LL 120,73
M 8,43,1
LA 16
DW 0
M 9,54,0
LC_ 120
DW 1024
M 9,55,1
AW 0,40
AW 1,42
M 10,66,0
LO 117,19,117
LU 19,117
M 10,67,1
RA 236,1,24576
HS 24,19
M 11,78,0
LL 17,74
LL 121,75
M 11,79,1
LA 17
DW 2048
M 6,8,2
WL 8
LC_ 121
AW 2,44
M 6,9,3
s_lshl_b32 s53, s52, 8
v_add_u32 v1, s37, v0
AW 3,46
LK 40,240,32,0
CF 48,48,49
LK 44,242,32,2
s_add_u32 s48, %[alo], s53
CF 50,50,51
GS 30
s_addc_u32 s49, %[ahi], 0
CF 248,248,249
CF 250,250,251
M 7,20,2
WL 3
LO 120,16,120
LU 16,120
M 7,21,3
s_add_u32 s52, s52, %[d]
RA 216,1,0
HS 24,16
M 8,44,2
s_cmp_eq_u32 s52, %[wrap]
LL 18,76
LL 124,77
M 8,45,3
s_cselect_b32 s52, %[rst], s52
LA 18
DQ 3072
M 9,56,2
LC_ 124
v_and_b32 v22, 0xfffffcff, v1
M 9,57,3
AW 4,48
AW 5,50
M 10,68,2
WL 3
LO 121,17,121
LU 17,121
M 10,69,3
s_add_u32 s50, s50, %[d]
RA 220,1,1024
HS 24,17
M 11,80,2
LL 19,78
LL 125,79
M 11,81,3
s_cmp_eq_u32 s50, %[wrap]
LA 19
RV 252,22,5120
M 6,10,4
s_cselect_b32 s50, %[rst], s50
LC_ 125
AW 6,248
M 6,11,5
AW 7,250
LK 48,244,33,0
CF 40,40,41
LK 248,246,33,2
CF 42,42,43
GS 31
CF 44,44,45
CF 46,46,47
M 7,22,4
WL 4
LO 124,18,124
LU 18,124
M 7,23,5
s_add_u32 s38, s38, 32768
RA 224,1,2048
HS 24,18
M 8,46,4
s_cmp_eq_u32 s38, s77
LL 16,80
LL 106,81
M 8,47,5
s_cselect_b32 s38, s76, s38
LA 16
AW 8,40
M 9,58,4
s_barrier
LC_ 106
AW 9,42
M 9,59,5
s_sub_u32 s39, 0, s39
AW 10,44
AW 11,46
LK 40,248,34,0
CF 48,48,49
LK 44,250,34,2
CF 50,50,51
GS 32
CF 248,248,249
CF 250,250,251
M 10,70,4
WL 3
LO 125,19,125
LU 19,125
M 10,71,5
RA 228,1,3072
HS 24,19
M 11,82,4
LL 17,82
LL 107,83
M 11,83,5
LA 17
HX 24
Z 0,0,0
LC_ 107
ds_read_b64 v[14:15], v5 offset:0
M 0,1,1
W1 24,0
AW 12,48
Z 1,12,0
WL 5
LO 106,16,106
LU 16,106
M 1,13,1
RA 232,1,4096
HZ 27,16
Z 2,36,0
LL 18,84
LL 110,85
M 2,37,1
LA 18
W1 26,2048
Z 3,48,0
LC_ 110
AW 13,50
M 3,49,1
s_add_u32 s42, s94, s60
AW 14,248
AW 15,250
LK 48,252,35,0
CF 40,40,41
LK 248,254,35,2
s_addc_u32 s43, s95, 0
CF 42,42,43
GS 33
CF 44,44,45
CF 46,46,47
Z 4,60,0
WL 6
LO 107,17,107
LU 17,107
M 4,61,1
RA 236,1,6144
HS 27,17
Z 5,72,0
LL 19,86
LL 111,87
M 5,73,1
LA 19
SU 104
M 0,2,2
WL 7
LC_ 111
v_add_f32 v14, v14, v15
M 0,3,3
s_nop 0
v_add_f32_dpp v14, v14, v14 quad_perm:[1,0,3,2] row_mask:0xf bank_mask:0xf
AW 16,40
M 1,14,2
WL 3
LO 110,18,110
LU 18,110
M 1,15,3
RA 216,1,7168
HZ 29,18
M 2,38,2
LL 16,88
LL 114,89
M 2,39,3
LA 16
v_add_f32_dpp v14, v14, v14 quad_perm:[2,3,0,1] row_mask:0xf bank_mask:0xf
M 3,50,2
LC_ 114
v_add_f32_dpp v14, v14, v14 row_half_mirror row_mask:0xf bank_mask:0xf
M 3,51,3
s_add_u32 s42, s94, s61
v_cvt_f64_f32 v[14:15], v14
)"
R"(AW 17,42
M 4,62,2
WL 3
LO 111,19,111
LU 19,111
M 4,63,3
s_addc_u32 s43, s95, 0
RA 220,1,8192
HS 29,19
M 5,74,2
LL 17,90
LL 115,91
M 5,75,3
LA 17
s_mov_b64 exec, s[54:55]
SP
s_mov_b64 exec, -1
M 0,4,4
LC_ 115
ds_read_b64 v[14:15], v5 offset:2048
M 0,5,5
SU 108
v_subrev_u32 v5, s39, v5
M 1,16,4
WL 4
LO 114,16,114
LU 16,114
M 1,17,5
s_mov_b32 m0, %[pm]
RA 224,1,9216
HZ 25,16
M 2,40,4
LL 18,92
LL 118,93
M 2,41,5
LA 18
DF
M 3,52,4
LC_ 118
v_pk_add_f16 v27, v29, v27
M 3,53,5
s_add_u32 s42, s44, s62
HX 27
AW 18,44
M 4,64,4
WL 4
LO 115,17,115
LU 17,115
M 4,65,5
s_addc_u32 s43, s45, 0
RA 228,1,10240
HS 25,17
M 5,76,4
LL 19,94
LL 119,95
M 5,77,5
LA 19
ST 112
M 0,6,0
WL 5
LC_ 119
v_add_f32 v14, v14, v15
M 0,7,1
s_nop 0
v_add_f32_dpp v14, v14, v14 quad_perm:[1,0,3,2] row_mask:0xf bank_mask:0xf
AW 19,46
RV 36,8,6912
M 1,18,0
WL 4
LO 118,18,118
LU 18,118
M 1,19,1
RA 232,1,11264
HS 25,18
GS 34
CF 48,48,49
CF 50,50,51
M 2,42,0
LL 16,96
LL 122,97
CF 248,248,249
M 2,43,1
LA 16
RV 252,22,5392
CF 250,250,251
AW 20,48
M 3,54,0
LC_ 122
v_add_f32_dpp v14, v14, v14 quad_perm:[2,3,0,1] row_mask:0xf bank_mask:0xf
AW 21,50
M 3,55,1
s_add_u32 s42, s44, s63
v_add_f32_dpp v14, v14, v14 row_half_mirror row_mask:0xf bank_mask:0xf
AW 22,248
AW 23,250
M 4,66,0
WL 5
LO 119,19,119
LU 19,119
M 4,67,1
s_addc_u32 s43, s45, 0
RA 236,1,12288
HS 25,19
GS 35
ST 116
v_cvt_f64_f32 v[14:15], v14
s_nop 5
v_cndmask_b32 v52, v52, v53, s[78:79]
M 5,78,0
LL 17,98
LL 123,99
v_cndmask_b32 v54, v54, v55, s[78:79]
M 5,79,1
LA 17
s_mov_b64 exec, s[54:55]
SP
s_mov_b64 exec, -1
v_cndmask_b32 v52, v52, v54, s[80:81]
M 0,8,2
LC_ 123
v_mul_f32 v52, 0x3b2aaaab, v52
M 0,9,3
WL 7
v_med3_f32 v53, v52, v39, v37
v_cmp_eq_f32 vcc, v52, v53
M 1,20,2
WL 3
LO 122,16,122
LU 16,122
M 1,21,3
RA 216,1,13312
HS 25,16
C 52,52,52
M 2,44,2
LL 18,100
LL 126,101
v_lshlrev_b32 v52, 16, v52
M 2,45,3
LA 18
v_add_f32 v52, v36, v52
M 3,56,2
LC_ 126
C 52,52,52
M 3,57,3
s_add_u32 s42, s44, s64
v_lshlrev_b32 v52, 16, v52
M 4,68,2
WL 3
LO 123,17,123
LU 17,123
v_sqrt_f32 v52, v52
M 4,69,3
s_addc_u32 s43, s45, 0
RA 220,1,14336
HS 25,17
C 52,52,52
M 5,80,2
LL 19,102
LL 127,103
v_lshlrev_b32 v52, 16, v52
M 5,81,3
LA 19
ST 120
v_rcp_f32 v52, v52
M 0,10,4
LC_ 127
v_mul_f32 v52, v38, v52
M 0,11,5
v_cndmask_b32 v52, 0, v52, vcc
CF 52,52,52
M 1,22,4
WL 3
LO 126,18,126
LU 18,126
M 1,23,5
RA 224,1,15360
HS 25,18
ds_bpermute_b32 v13, v28, v52
M 2,46,4
LL 16,56
LL 104,57
M 2,47,5
LA 16
AR 248,0
M 3,58,4
LC_ 104
AR 249,1
M 3,59,5
s_add_u32 s42, s44, s65
AR 250,2
AR 251,3
M 4,70,4
WL 4
LO 127,19,127
LU 19,127
M 4,71,5
s_addc_u32 s43, s45, 0
RA 228,1,16384
HS 25,19
M 5,82,4
LL 17,58
LL 105,59
M 5,83,5
LA 17
ST 124
Z 6,0,0
LC_ 105
HX 25
M 6,1,1
W1 25,1024
AR 52,4
Z 7,12,0
WL 4
LO 104,16,104
LU 16,104
M 7,13,1
RA 232,1,17408
HZ 26,16
Z 8,36,0
LL 18,60
LL 108,61
M 8,37,1
s_mul_i32 s53, s50, 25600
LA 18
W1 27,3072
Z 9,48,0
s_add_u32 s40, %[wlo], s53
LC_ 108
v_add_u32 v4, s39, v4
M 9,49,1
s_addc_u32 s41, %[whi], 0
AR 53,5
AR 54,6
Z 10,60,0
WL 5
LO 105,17,105
LU 17,105
M 10,61,1
s_mov_b32 m0, s38
RA 236,1,18432
HS 26,17
Z 11,72,0
LL 19,62
LL 109,63
M 11,73,1
LA 19
DW 0
AR 55,7
M 6,2,2
LC_ 109
XS 224
M 6,3,3
XS 225
XS 226
M 7,14,2
WL 3
LO 108,18,108
LU 18,108
M 7,15,3
s_add_u32 s37, s37, 32768
RA 216,1,19456
HZ 29,18
M 8,38,2
s_cmp_eq_u32 s37, s75
LL 16,64
LL 112,65
M 8,39,3
s_cselect_b32 s37, s74, s37
LA 16
XS 227
M 9,50,2
s_mul_i32 s53, %[d], 196608
LC_ 112
XS 228
M 9,51,3
s_add_u32 s53, s53, s57
XS 229
XS 230
M 10,62,2
WL 3
LO 109,19,109
LU 19,109
M 10,63,3
s_add_u32 s46, %[xlo], s53
RA 220,1,20480
HS 29,19
M 11,74,2
LL 17,66
LL 113,67
XS 231
M 11,75,3
s_addc_u32 s47, %[xhi], 0
LA 17
v_pk_add_f16 v26, v29, v26
M 6,4,4
s_mov_b32 s53, 0xda43cb52
LC_ 113
HX 26
v_min_u32 v35, v35, v13
M 6,5,5
s_cmp_eq_u32 %[d], 1
XS 232
XS 233
M 7,16,4
WL 3
LO 112,16,112
LU 16,112
M 7,17,5
s_cselect_b32 s53, s53, 0xfe987610
RA 224,1,21504
HZ 24,16
M 8,40,4
s_bitcmp1_b64 s[92:93], 48
LL 18,68
LL 116,69
XS 234
M 8,41,5
s_cselect_b32 s58, 16, 0
LA 18
XS 235
M 9,52,4
s_lshr_b32 s58, s53, s58
LC_ 116
v_lshrrev_b32 v3, 6, v0
v_and_b32 v3, 12, v3
v_bfe_u32 v3, s58, v3, 4
v_mad_u32_u24 v3, v3, s97, v21
M 9,53,5
DW 1024
XS 236
M 10,64,4
WL 3
LO 113,17,113
LU 17,113
XS 237
M 10,65,5
RA 228,1,22528
HS 24,17
XS 238
M 11,76,4
LL 19,70
LL 117,71
M 11,77,5
LA 19
XL 36,0
XS 239
M 6,6,0
LC_ 117
DW 2048
XS 240
M 6,7,1
s_mul_i32 s53, s51, 49152
XS 241
XS 242
M 7,18,0
WL 3
LO 116,18,116
LU 18,116
M 7,19,1
s_bitcmp1_b64 s[90:91], s51
RA 232,1,23552
HS 24,18
M 8,42,0
s_cselect_b64 s[44:45], s[84:85], s[82:83]
LL 16,72
LL 120,73
XS 243
M 8,43,1
s_add_u32 s44, s44, s53
LA 16
s_addc_u32 s45, s45, 0
s_bitcmp1_b64 s[92:93], s51
s_cselect_b64 s[94:95], s[88:89], s[86:87]
s_add_u32 s94, s94, s53
s_addc_u32 s95, s95, 0
s_add_u32 s51, s51, %[d]
s_cmp_eq_u32 s51, %[wrap]
s_cselect_b32 s51, %[rst], s51
XL 37,64
M 9,54,0
s_add_u32 s40, s40, 3072
LC_ 120
s_addc_u32 s41, s41, 0
s_add_u32 m0, s38, 3072
XS 244
M 9,55,1
XL 38,128
XS 245
M 10,66,0
WL 3
LO 117,19,117
LU 19,117
XS 246
M 10,67,1
RA 236,1,24576
HS 24,19
M 11,78,0
LL 17,74
LL 121,75
XS 247
)"
R"(M 11,79,1
LA 17
DW 0
XS 248
M 6,8,2
LC_ 121
XL 39,192
M 6,9,3
DW 1024
v_add_u32 v1, s37, v0
XS 249
M 7,20,2
WL 3
LO 120,16,120
LU 16,120
XS 250
M 7,21,3
RA 216,1,0
HS 24,16
XS 251
M 8,44,2
LL 18,76
LL 124,77
M 8,45,3
LA 18
XL 40,256
AW 0,248
M 9,56,2
LC_ 124
DW 2048
AW 1,249
M 9,57,3
s_lshl_b32 s53, s52, 8
v_and_b32 v22, 0xfffffcff, v1
AW 2,250
M 10,68,2
WL 3
LO 121,17,121
LU 17,121
M 10,69,3
s_add_u32 s48, %[alo], s53
RA 220,1,1024
HS 24,17
AW 3,251
M 11,80,2
LL 19,78
LL 125,79
XS 52
M 11,81,3
s_addc_u32 s49, %[ahi], 0
LA 19
RV 252,22,5120
M 6,10,4
s_add_u32 s52, s52, %[d]
LC_ 125
XS 53
M 6,11,5
s_cmp_eq_u32 s52, %[wrap]
XS 54
XS 55
M 7,22,4
WL 4
LO 124,18,124
LU 18,124
M 7,23,5
s_cselect_b32 s52, %[rst], s52
RA 224,1,2048
HS 24,18
M 8,46,4
LL 16,80
LL 106,81
AW 4,52
M 8,47,5
LA 16
XL 41,320
DQ 3072
AW 5,53
M 9,58,4
LC_ 106
XL 42,384
M 9,59,5
s_add_u32 s50, s50, %[d]
AW 6,54
AW 7,55
M 10,70,4
WL 3
LO 125,19,125
LU 19,125
M 10,71,5
RA 228,1,3072
HS 24,19
AR 248,8
M 11,82,4
LL 17,82
LL 107,83
M 11,83,5
LA 17
HX 24
s_cmp_eq_u32 s50, %[wrap]
s_cselect_b32 s50, %[rst], s50
AR 249,9
Z 0,0,0
LC_ 107
XL 43,448
AR 250,10
M 0,1,1
s_add_u32 s38, s38, 32768
WV 12
BW 36
Z 1,12,0
WL 4
LO 106,16,106
LU 16,106
AR 251,11
M 1,13,1
s_cmp_eq_u32 s38, s77
RA 232,1,4096
HZ 27,16
XS 248
Z 2,24,0
s_cselect_b32 s38, s76, s38
LL 18,84
LL 110,85
XS 249
M 2,25,1
s_barrier
LA 18
RV 40,8,6144
Z 3,48,0
s_sub_u32 s39, 0, s39
LC_ 110
ds_read_b64 v[14:15], v5 offset:0
XS 250
M 3,49,1
XL 44,512
RV 44,8,6208
XS 251
Z 4,60,0
WL 7
LO 107,17,107
LU 17,107
AW 8,248
M 4,61,1
RA 236,1,6144
HS 27,17
Z 5,72,0
LL 19,86
LL 111,87
AW 9,249
M 5,73,1
LA 19
W1 24,0
AW 10,250
M 0,2,2
LC_ 111
W1 26,2048
M 0,3,3
XL 45,576
RV 48,8,6272
AW 11,251
M 1,14,2
WL 8
LO 110,18,110
LU 18,110
AR 52,12
M 1,15,3
s_add_u32 s42, s94, s60
RA 216,1,7168
HZ 29,18
AR 53,13
M 2,26,2
s_addc_u32 s43, s95, 0
LL 16,88
LL 114,89
M 2,27,3
LA 16
XL 46,640
AR 54,14
M 3,50,2
LC_ 114
SU 104
AR 55,15
M 3,51,3
XL 47,704
BR 36
M 4,62,2
WL 6
LO 111,19,111
LU 19,111
XS 52
M 4,63,3
s_add_u32 s42, s94, s61
RA 220,1,8192
HS 29,19
XS 53
M 5,74,2
LL 17,90
LL 115,91
XS 54
M 5,75,3
s_addc_u32 s43, s95, 0
LA 17
SU 108
M 0,4,4
s_mov_b32 m0, %[pm]
LC_ 115
DF
XS 55
M 0,5,5
WV 17
BW 37
AW 12,52
M 1,16,4
WL 5
LO 114,16,114
LU 16,114
AW 13,53
M 1,17,5
RA 224,1,9216
HZ 25,16
M 2,28,4
LL 18,92
LL 118,93
AW 14,54
M 2,29,5
WL 5
LA 18
BR 37
AW 15,55
LZ 40,36
WV 16
BW 38
M 3,52,4
LC_ 118
RV 40,8,6336
M 3,53,5
s_add_u32 s42, s44, s62
BR 38
v_add_f32 v14, v14, v15
s_nop 1
v_add_f32_dpp v14, v14, v14 quad_perm:[1,0,3,2] row_mask:0xf bank_mask:0xf
s_nop 1
v_add_f32_dpp v14, v14, v14 quad_perm:[2,3,0,1] row_mask:0xf bank_mask:0xf
s_nop 1
v_add_f32_dpp v14, v14, v14 row_half_mirror row_mask:0xf bank_mask:0xf
v_cvt_f64_f32 v[14:15], v14
s_mov_b64 exec, s[54:55]
SP
s_mov_b64 exec, -1
s_nop 0
ds_read_b64 v[14:15], v5 offset:2048
AR 248,16
M 4,64,4
WL 9
LO 115,17,115
LU 17,115
AR 249,17
M 4,65,5
s_addc_u32 s43, s45, 0
RA 228,1,10240
HS 25,17
AR 250,18
M 5,76,4
LL 19,94
LL 119,95
M 5,77,5
WL 6
LA 19
ST 112
AR 251,19
LS 44,37
WV 16
BW 39
M 0,6,0
LC_ 119
RV 44,8,6400
XS 248
M 0,7,1
WL 6
BR 39
v_subrev_u32 v5, s39, v5
v_pk_add_f16 v27, v29, v27
LS 48,38
WV 14
BW 40
M 1,18,0
LO 118,18,118
LU 18,118
HX 27
XS 249
M 1,19,1
RA 232,1,11264
HS 25,18
XS 250
M 2,30,0
LL 16,96
LL 122,97
XS 251
M 2,31,1
WV 12
LA 16
RV 252,22,5392
M 3,54,0
WL 4
LC_ 122
RV 48,8,6464
AW 16,248
M 3,55,1
s_add_u32 s42, s44, s63
BR 40
v_add_f32 v14, v14, v15
s_nop 1
v_add_f32_dpp v14, v14, v14 quad_perm:[1,0,3,2] row_mask:0xf bank_mask:0xf
s_nop 1
v_add_f32_dpp v14, v14, v14 quad_perm:[2,3,0,1] row_mask:0xf bank_mask:0xf
AW 17,249
LS 40,39
s_addc_u32 s43, s45, 0
BW 41
M 4,66,0
LO 119,19,119
LU 19,119
AW 18,250
M 4,67,1
RA 236,1,12288
HS 25,19
v_add_f32_dpp v14, v14, v14 row_half_mirror row_mask:0xf bank_mask:0xf
M 5,78,0
LL 17,98
LL 123,99
AW 19,251
M 5,79,1
LA 17
ST 116
v_cvt_f64_f32 v[14:15], v14
AR 52,20
M 0,8,2
LC_ 123
s_mov_b64 exec, s[54:55]
SP
s_mov_b64 exec, -1
M 0,9,3
WL 4
RV 40,8,6528
BR 41
AR 53,21
LS 44,40
WV 12
BW 42
M 1,20,2
LO 122,16,122
LU 16,122
AR 54,22
M 1,21,3
RA 216,1,13312
HS 25,16
AR 55,23
M 2,32,2
LL 18,100
LL 126,101
M 2,33,3
LA 18
RV 44,8,6592
XS 52
M 3,56,2
LC_ 126
BR 42
XS 53
M 3,57,3
s_add_u32 s42, s44, s64
XS 54
XS 55
M 4,68,2
WL 6
LO 123,17,123
LU 17,123
M 4,69,3
s_addc_u32 s43, s45, 0
RA 220,1,14336
HS 25,17
LS 48,41
WV 11
BW 43
M 5,80,2
LL 19,102
LL 127,103
AW 20,52
M 5,81,3
LA 19
ST 120
M 0,10,4
LC_ 127
RV 48,8,6656
AW 21,53
M 0,11,5
BR 43
AW 22,54
M 1,22,4
WL 7
LO 126,18,126
)"
R"(LU 18,126
AW 23,55
M 1,23,5
WL 6
RA 224,1,15360
HS 25,18
LS 40,42
WV 11
BW 44
M 2,34,4
LL 16,56
LL 104,57
M 2,35,5
LA 16
RV 40,8,6720
M 3,58,4
LC_ 104
BR 44
M 3,59,5
s_add_u32 s42, s44, s65
M 4,70,4
WL 6
LO 127,19,127
LU 19,127
M 4,71,5
s_addc_u32 s43, s45, 0
RA 228,1,16384
HS 25,19
LS 44,43
WV 10
BW 45
M 5,82,4
LL 17,58
LL 105,59
M 5,83,5
LA 17
ST 124
Z 6,0,0
LC_ 105
RV 44,8,6784
M 6,1,1
BR 45
HX 25
Z 7,12,0
WL 7
LO 104,16,104
LU 16,104
M 7,13,1
WL 6
RA 232,1,17408
HZ 26,16
LS 48,44
WV 10
BW 46
Z 8,24,0
LL 18,60
LL 108,61
M 8,25,1
s_mul_i32 s53, s50, 25600
LA 18
W1 25,1024
Z 9,48,0
s_add_u32 s40, %[wlo], s53
LC_ 108
W1 27,3072
M 9,49,1
s_addc_u32 s41, %[whi], 0
RV 48,8,6848
RA 240,7,0
Z 10,60,0
WL 10
LO 105,17,105
LU 17,105
M 10,61,1
s_mov_b32 m0, s38
RA 236,1,18432
HS 26,17
Z 11,72,0
LL 19,62
LL 109,63
M 11,73,1
LA 19
DW 0
M 6,2,2
LC_ 109
BR 46
M 6,3,3
WL 12
v_add_u32 v4, s39, v4
LS 40,45
WV 9
BW 47
M 7,14,2
WL 8
LO 108,18,108
LU 18,108
M 7,15,3
s_add_u32 s37, s37, 32768
RA 216,1,19456
HZ 29,18
M 8,26,2
s_cmp_eq_u32 s37, s75
LL 16,64
LL 112,65
M 8,27,3
s_cselect_b32 s37, s74, s37
LA 16
DW 1024
M 9,50,2
LC_ 112
RA 244,7,256
M 9,51,3
BR 47
M 10,62,2
WL 7
LO 109,19,109
LU 19,109
M 10,63,3
WL 6
RA 220,1,20480
HS 29,19
LS 44,46
RA 248,7,512
M 11,74,2
LL 17,66
LL 113,67
M 11,75,3
LA 17
DW 2048
M 6,4,4
LC_ 113
M 6,5,5
M 7,16,4
WL 4
LO 112,16,112
LU 16,112
M 7,17,5
s_mul_i32 s53, s51, 49152
RA 224,1,21504
HZ 30,16
LS 48,47
s_bitcmp1_b64 s[90:91], s51
RA 252,7,768
M 8,28,4
s_cselect_b64 s[44:45], s[84:85], s[82:83]
LL 18,68
LL 116,69
M 8,29,5
s_add_u32 s44, s44, s53
LA 18
v_mul_f32 v36, 0xbb2aaaab, v36
M 9,52,4
s_addc_u32 s45, s45, 0
LC_ 116
v_mul_f32 v37, 0xbb2aaaab, v37
M 9,53,5
s_bitcmp1_b64 s[92:93], s51
v_mul_f32 v38, 0xbb2aaaab, v38
v_mul_f32 v39, 0xbb2aaaab, v39
M 10,64,4
WL 4
LO 113,17,113
LU 17,113
M 10,65,5
s_cselect_b64 s[94:95], s[88:89], s[86:87]
RA 228,1,22528
HS 30,17
LK 40,240,36,0
LK 44,242,36,2
s_add_u32 s94, s94, s53
RA 240,7,1024
LK 48,244,37,0
s_nop 0
CF 40,40,41
LK 248,246,37,2
s_addc_u32 s95, s95, 0
RA 244,7,1280
CF 42,42,43
M 11,76,4
LL 19,70
LL 117,71
M 11,77,5
s_add_u32 s51, s51, %[d]
LA 19
v_pk_add_f16 v29, v30, v29
M 6,6,0
s_cmp_eq_u32 s51, %[wrap]
LC_ 117
v_pk_add_f16 v26, v29, v26
M 6,7,1
s_cselect_b32 s51, %[rst], s51
HX 26
CF 44,44,45
M 7,18,0
WL 5
LO 116,18,116
LU 18,116
M 7,19,1
s_mul_i32 s53, %[d], 16384
RA 232,1,23552
HZ 24,18
M 8,30,0
s_add_u32 s62, s53, %[sb1]
LL 16,72
LL 120,73
M 8,31,1
s_add_u32 s40, s40, 3072
LA 16
CF 46,46,47
M 9,54,0
s_addc_u32 s41, s41, 0
LC_ 120
s_add_u32 m0, s38, 3072
M 9,55,1
DW 0
AW 24,40
M 10,66,0
WL 3
LO 117,19,117
LU 19,117
M 10,67,1
RA 236,1,24576
HS 24,19
AW 25,42
AW 26,44
M 11,78,0
LL 17,74
LL 121,75
AW 27,46
M 11,79,1
LA 17
DW 1024
LK 40,248,38,0
CF 48,48,49
LK 44,250,38,2
RA 248,7,1536
CF 50,50,51
GZ 36
CF 248,248,249
CF 250,250,251
M 6,8,2
LC_ 121
DW 2048
M 6,9,3
s_lshl_b32 s53, s52, 8
v_add_u32 v1, s37, v0
AW 28,48
M 7,20,2
WL 4
LO 120,16,120
LU 16,120
M 7,21,3
s_add_u32 s48, %[alo], s53
RA 216,1,0
HS 24,16
AW 29,50
M 8,32,2
s_addc_u32 s49, %[ahi], 0
LL 18,76
LL 124,77
AW 30,248
AW 31,250
M 8,33,3
s_add_u32 s52, s52, %[d]
LA 18
v_and_b32 v22, 0xfffffcff, v1
LK 48,252,39,0
CF 40,40,41
LK 248,254,39,2
s_cmp_eq_u32 s52, %[wrap]
RA 252,7,1792
CF 42,42,43
GS 37
s_cselect_b32 s52, %[rst], s52
CF 44,44,45
CF 46,46,47
M 9,56,2
LC_ 124
DQ 3072
M 9,57,3
AW 32,40
AW 33,42
M 10,68,2
WL 4
LO 121,17,121
LU 17,121
M 10,69,3
s_add_u32 s50, s50, %[d]
RA 220,1,1024
HS 24,17
M 11,80,2
LL 19,78
LL 125,79
M 11,81,3
s_cmp_eq_u32 s50, %[wrap]
LA 19
RV 252,22,5120
AW 34,44
AW 35,46
LK 40,240,40,0
CF 48,48,49
LK 44,242,40,2
s_cselect_b32 s50, %[rst], s50
RA 240,7,2048
CF 50,50,51
GS 38
CF 248,248,249
CF 250,250,251
M 6,10,4
LC_ 125
AW 36,48
M 6,11,5
AW 37,50
AW 38,248
M 7,22,4
WL 5
LO 124,18,124
LU 18,124
M 7,23,5
s_add_u32 s38, s38, 32768
RA 224,1,2048
HS 24,18
M 8,34,4
s_cmp_eq_u32 s38, s77
LL 16,80
LL 106,81
M 8,35,5
s_cselect_b32 s38, s76, s38
LA 16
AW 39,250
LK 48,244,41,0
CF 40,40,41
LK 248,246,41,2
s_barrier
RA 244,7,2304
CF 42,42,43
GS 39
s_sub_u32 s39, 0, s39
CF 44,44,45
CF 46,46,47
M 9,58,4
LC_ 106
ds_read_b64 v[14:15], v5 offset:0
M 9,59,5
AW 40,40
AW 41,42
M 10,70,4
WL 5
LO 125,19,125
LU 19,125
M 10,71,5
RA 228,1,3072
HS 24,19
M 11,82,4
LL 17,82
LL 107,83
M 11,83,5
LA 17
HX 24
Z 0,0,0
LC_ 107
W1 24,0
AW 42,44
AW 43,46
M 0,1,1
W1 26,2048
LK 40,248,42,0
CF 48,48,49
LK 44,250,42,2
RA 248,7,2560
CF 50,50,51
GS 40
CF 248,248,249
CF 250,250,251
Z 1,12,0
WL 7
LO 106,16,106
LU 16,106
M 1,13,1
RA 232,1,4096
HZ 27,16
Z 2,24,0
LL 18,84
LL 110,85
M 2,25,1
WL 7
LA 18
v_add_f32 v14, v14, v15
AW 44,48
Z 3,48,0
LC_ 110
v_add_f32_dpp v14, v14, v14 quad_perm:[1,0,3,2] row_mask:0xf bank_mask:0xf
AW 45,50
AW 46,248
M 3,49,1
s_add_u32 s42, s94, s60
v_add_f32_dpp v14, v14, v14 quad_perm:[2,3,0,1] row_mask:0xf bank_mask:0xf
AW 47,250
LK 48,252,43,0
CF 40,40,41
)"
R"(LK 248,254,43,2
s_addc_u32 s43, s95, 0
RA 252,7,2816
CF 42,42,43
GS 41
CF 44,44,45
CF 46,46,47
Z 4,60,0
WL 7
LO 107,17,107
LU 17,107
M 4,61,1
RA 236,1,6144
HS 27,17
Z 5,72,0
LL 19,86
LL 111,87
M 5,73,1
LA 19
SU 104
M 0,2,2
LC_ 111
v_add_f32_dpp v14, v14, v14 row_half_mirror row_mask:0xf bank_mask:0xf
AW 48,40
AW 49,42
M 0,3,3
v_cvt_f64_f32 v[14:15], v14
AW 50,44
M 1,14,2
WL 4
LO 110,18,110
LU 18,110
AW 51,46
M 1,15,3
RA 216,1,7168
HZ 29,18
LK 40,240,44,0
CF 48,48,49
LK 44,242,44,2
CF 50,50,51
GS 42
CF 248,248,249
CF 250,250,251
M 2,26,2
LL 16,88
LL 114,89
M 2,27,3
LA 16
s_mov_b64 exec, s[54:55]
SP
s_mov_b64 exec, -1
M 3,50,2
LC_ 114
ds_read_b64 v[14:15], v5 offset:2048
AW 52,48
M 3,51,3
s_add_u32 s42, s94, s61
v_subrev_u32 v5, s39, v5
AW 53,50
AW 54,248
M 4,62,2
WL 4
LO 111,19,111
LU 19,111
AW 55,250
M 4,63,3
s_addc_u32 s43, s95, 0
RA 220,1,8192
HS 29,19
LK 48,244,45,0
CF 40,40,41
LK 248,246,45,2
CF 42,42,43
GS 43
CF 44,44,45
CF 46,46,47
M 5,74,2
LL 17,90
LL 115,91
M 5,75,3
LA 17
SU 108
M 0,4,4
s_mov_b32 m0, %[pm]
LC_ 115
DF
AW 56,40
M 0,5,5
AW 57,42
AW 58,44
M 1,16,4
WL 4
LO 114,16,114
LU 16,114
AW 59,46
M 1,17,5
RA 224,1,9216
HZ 30,16
LK 40,248,46,0
CF 48,48,49
LK 44,250,46,2
CF 50,50,51
GS 44
CF 248,248,249
CF 250,250,251
M 2,28,4
LL 18,92
LL 118,93
M 2,29,5
WL 4
LA 18
v_add_f32 v14, v14, v15
M 3,52,4
LC_ 118
v_add_f32_dpp v14, v14, v14 quad_perm:[1,0,3,2] row_mask:0xf bank_mask:0xf
AW 60,48
M 3,53,5
s_add_u32 s42, s94, s62
v_add_f32_dpp v14, v14, v14 quad_perm:[2,3,0,1] row_mask:0xf bank_mask:0xf
AW 61,50
AW 62,248
M 4,64,4
WL 3
LO 115,17,115
LU 17,115
AW 63,250
M 4,65,5
s_addc_u32 s43, s95, 0
RA 228,1,10240
HS 30,17
LK 48,252,47,0
CF 40,40,41
LK 248,254,47,2
CF 42,42,43
v_add_f32_dpp v14, v14, v14 row_half_mirror row_mask:0xf bank_mask:0xf
GS 45
CF 44,44,45
CF 46,46,47
M 5,76,4
LL 19,94
LL 119,95
M 5,77,5
LA 19
SU 112
M 0,6,0
LC_ 119
v_cvt_f64_f32 v[14:15], v14
AW 64,40
M 0,7,1
s_mov_b64 exec, s[54:55]
SP
s_mov_b64 exec, -1
v_pk_add_f16 v29, v30, v29
AW 65,42
AW 66,44
M 1,18,0
WL 3
LO 118,18,118
LU 18,118
AW 67,46
RV 36,8,6912
M 1,19,1
RA 232,1,11264
HZ 25,18
GS 46
CF 48,48,49
CF 50,50,51
M 2,30,0
LL 16,96
LL 122,97
M 2,31,1
LA 16
RV 252,22,5392
CF 248,248,249
CF 250,250,251
M 3,54,0
LC_ 122
v_pk_add_f16 v27, v29, v27
AW 68,48
AW 69,50
M 3,55,1
s_add_u32 s42, s44, s63
HX 27
AW 70,248
AW 71,250
M 4,66,0
WL 5
LO 119,19,119
LU 19,119
M 4,67,1
s_addc_u32 s43, s45, 0
RA 236,1,12288
HS 25,19
GS 47
ST 116
s_nop 6
v_cndmask_b32 v52, v52, v53, s[78:79]
M 5,78,0
LL 17,98
LL 123,99
v_cndmask_b32 v54, v54, v55, s[78:79]
M 5,79,1
LA 17
v_cndmask_b32 v52, v52, v54, s[80:81]
M 0,8,2
LC_ 123
v_mul_f32 v52, 0x3b2aaaab, v52
M 0,9,3
WL 7
v_med3_f32 v53, v52, v39, v37
v_cmp_eq_f32 vcc, v52, v53
M 1,20,2
WL 3
LO 122,16,122
LU 16,122
M 1,21,3
RA 216,1,13312
HS 25,16
C 52,52,52
M 2,32,2
LL 18,100
LL 126,101
v_lshlrev_b32 v52, 16, v52
M 2,33,3
LA 18
v_add_f32 v52, v36, v52
M 3,56,2
LC_ 126
C 52,52,52
M 3,57,3
s_add_u32 s42, s44, s64
v_lshlrev_b32 v52, 16, v52
M 4,68,2
WL 3
LO 123,17,123
LU 17,123
v_sqrt_f32 v52, v52
M 4,69,3
s_addc_u32 s43, s45, 0
RA 220,1,14336
HS 25,17
C 52,52,52
M 5,80,2
LL 19,102
LL 127,103
v_lshlrev_b32 v52, 16, v52
M 5,81,3
LA 19
ST 120
v_rcp_f32 v52, v52
M 0,10,4
LC_ 127
v_mul_f32 v52, v38, v52
M 0,11,5
v_cndmask_b32 v52, 0, v52, vcc
CF 52,52,52
M 1,22,4
WL 3
LO 126,18,126
LU 18,126
M 1,23,5
RA 224,1,15360
HS 25,18
ds_bpermute_b32 v13, v28, v52
M 2,34,4
LL 16,56
LL 104,57
M 2,35,5
LA 16
AR 248,24
M 3,58,4
LC_ 104
AR 249,25
M 3,59,5
s_add_u32 s42, s44, s65
AR 250,26
AR 251,27
M 4,70,4
WL 4
LO 127,19,127
LU 19,127
M 4,71,5
s_addc_u32 s43, s45, 0
RA 228,1,16384
HS 25,19
M 5,82,4
LL 17,58
LL 105,59
M 5,83,5
LA 17
ST 124
Z 6,0,0
LC_ 105
HX 25
M 6,1,1
W1 25,1024
AR 52,28
Z 7,12,0
WL 4
LO 104,16,104
LU 16,104
M 7,13,1
RA 232,1,17408
HZ 26,16
Z 8,24,0
LL 18,60
LL 108,61
M 8,25,1
s_mul_i32 s53, s50, 25600
LA 18
W1 27,3072
Z 9,48,0
s_add_u32 s40, %[wlo], s53
LC_ 108
v_add_u32 v4, s39, v4
AR 53,29
M 9,49,1
s_addc_u32 s41, %[whi], 0
AR 54,30
AR 55,31
Z 10,60,0
WL 5
LO 105,17,105
LU 17,105
M 10,61,1
s_mov_b32 m0, s38
RA 236,1,18432
HS 26,17
XS 248
Z 11,72,0
LL 19,62
LL 109,63
XS 249
M 11,73,1
LA 19
DW 0
XS 250
M 6,2,2
LC_ 109
XS 251
M 6,3,3
AW 24,248
AW 25,249
M 7,14,2
WL 3
LO 108,18,108
LU 18,108
M 7,15,3
s_add_u32 s37, s37, 32768
RA 216,1,19456
HZ 29,18
AW 26,250
M 8,26,2
s_cmp_eq_u32 s37, s75
LL 16,64
LL 112,65
AW 27,251
XS 52
M 8,27,3
s_cselect_b32 s37, s74, s37
LA 16
XS 53
M 9,50,2
s_mul_i32 s53, %[d], 393216
LC_ 112
XS 54
M 9,51,3
s_add_u32 s53, s53, s57
XS 55
AW 28,52
M 10,62,2
WL 3
LO 109,19,109
LU 19,109
M 10,63,3
s_add_u32 s46, %[xlo], s53
RA 220,1,20480
HS 29,19
AW 29,53
M 11,74,2
LL 17,66
LL 113,67
AW 30,54
M 11,75,3
s_addc_u32 s47, %[xhi], 0
LA 17
AW 31,55
M 6,4,4
s_mov_b32 s53, 0xfe987610
LC_ 113
v_min_u32 v35, v35, v13
M 6,5,5
s_cmp_eq_u32 %[d], 1
AR 248,32
AR 249,33
M 7,16,4
WL 3
LO 112,16,112
LU 16,112
M 7,17,5
)"
R"(s_cselect_b32 s53, s53, 0xda43cb52
RA 224,1,21504
HZ 30,16
AR 250,34
M 8,28,4
s_bitcmp1_b64 s[92:93], 48
LL 18,68
LL 116,69
AR 251,35
M 8,29,5
s_cselect_b32 s58, 16, 0
LA 18
XS 248
M 9,52,4
s_lshr_b32 s58, s53, s58
LC_ 116
v_lshrrev_b32 v3, 6, v0
v_and_b32 v3, 12, v3
v_bfe_u32 v3, s58, v3, 4
v_mad_u32_u24 v3, v3, s97, v21
XS 249
M 9,53,5
DW 1024
XS 250
XS 251
M 10,64,4
WL 3
LO 113,17,113
LU 17,113
AW 32,248
M 10,65,5
RA 228,1,22528
HS 30,17
AW 33,249
M 11,76,4
LL 19,70
LL 117,71
AW 34,250
M 11,77,5
LA 19
XL 48,0
AW 35,251
M 6,6,0
LC_ 117
DW 2048
AR 52,36
M 6,7,1
s_mul_i32 s53, s51, 49152
v_pk_add_f16 v29, v30, v29
AR 53,37
M 7,18,0
WL 3
LO 116,18,116
LU 18,116
AR 54,38
M 7,19,1
s_bitcmp1_b64 s[90:91], s51
RA 232,1,23552
HZ 24,18
AR 55,39
M 8,30,0
s_cselect_b64 s[44:45], s[84:85], s[82:83]
LL 16,72
LL 120,73
XS 52
M 8,31,1
s_add_u32 s44, s44, s53
LA 16
v_pk_add_f16 v26, v29, v26
s_addc_u32 s45, s45, 0
s_bitcmp1_b64 s[92:93], s51
s_cselect_b64 s[94:95], s[88:89], s[86:87]
s_add_u32 s94, s94, s53
s_addc_u32 s95, s95, 0
s_add_u32 s51, s51, %[d]
s_cmp_eq_u32 s51, %[wrap]
s_cselect_b32 s51, %[rst], s51
XL 49,64
XS 53
M 9,54,0
s_add_u32 s40, s40, 3072
LC_ 120
HX 26
s_addc_u32 s41, s41, 0
s_add_u32 m0, s38, 3072
XS 54
M 9,55,1
XL 50,128
XS 55
M 10,66,0
WL 3
LO 117,19,117
LU 19,117
AW 36,52
M 10,67,1
RA 236,1,24576
HS 24,19
AW 37,53
M 11,78,0
LL 17,74
LL 121,75
AW 38,54
AW 39,55
M 11,79,1
LA 17
DW 0
AR 248,40
M 6,8,2
LC_ 121
XL 51,192
AR 249,41
M 6,9,3
DW 1024
v_add_u32 v1, s37, v0
AR 250,42
M 7,20,2
WL 3
LO 120,16,120
LU 16,120
AR 251,43
M 7,21,3
RA 216,1,0
HS 24,16
XS 248
M 8,32,2
LL 18,76
LL 124,77
XS 249
M 8,33,3
LA 18
XL 52,256
XS 250
M 9,56,2
LC_ 124
DW 2048
XS 251
M 9,57,3
s_lshl_b32 s53, s52, 8
v_and_b32 v22, 0xfffffcff, v1
AW 40,248
M 10,68,2
WL 3
LO 121,17,121
LU 17,121
AW 41,249
M 10,69,3
s_add_u32 s48, %[alo], s53
RA 220,1,1024
HS 24,17
AW 42,250
M 11,80,2
LL 19,78
LL 125,79
AW 43,251
M 11,81,3
s_addc_u32 s49, %[ahi], 0
LA 19
RV 252,22,5120
AR 52,44
M 6,10,4
s_add_u32 s52, s52, %[d]
LC_ 125
AR 53,45
M 6,11,5
s_cmp_eq_u32 s52, %[wrap]
AR 54,46
AR 55,47
M 7,22,4
WL 4
LO 124,18,124
LU 18,124
XS 52
M 7,23,5
s_cselect_b32 s52, %[rst], s52
RA 224,1,2048
HS 24,18
XS 53
M 8,34,4
LL 16,80
LL 106,81
XS 54
M 8,35,5
LA 16
XL 53,320
DQ 3072
XS 55
M 9,58,4
LC_ 106
XL 54,384
AW 44,52
M 9,59,5
s_add_u32 s50, s50, %[d]
AW 45,53
AW 46,54
M 10,70,4
WL 3
LO 125,19,125
LU 19,125
M 10,71,5
RA 228,1,3072
HS 24,19
AW 47,55
M 11,82,4
LL 17,82
LL 107,83
AR 248,48
M 11,83,5
LA 17
HX 24
s_cmp_eq_u32 s50, %[wrap]
s_cselect_b32 s50, %[rst], s50
AR 249,49
Z 0,0,0
LC_ 107
XL 55,448
AR 250,50
M 0,1,1
s_add_u32 s38, s38, 32768
WV 12
BW 48
AR 251,51
Z 1,12,0
WL 4
LO 106,16,106
LU 16,106
XS 248
M 1,13,1
s_cmp_eq_u32 s38, s77
RA 232,1,4096
HZ 27,16
XS 249
Z 2,24,0
s_cselect_b32 s38, s76, s38
LL 18,84
LL 110,85
XS 250
XS 251
M 2,25,1
s_barrier
LA 18
RV 40,8,6144
AW 48,248
Z 3,36,0
s_sub_u32 s39, 0, s39
LC_ 110
ds_read_b64 v[14:15], v5 offset:0
AW 49,249
M 3,37,1
XL 56,512
RV 44,8,6208
AW 50,250
Z 4,60,0
WL 7
LO 107,17,107
LU 17,107
AW 51,251
M 4,61,1
RA 236,1,6144
HS 27,17
AR 52,52
Z 5,72,0
LL 19,86
LL 111,87
AR 53,53
M 5,73,1
LA 19
W1 24,0
AR 54,54
M 0,2,2
LC_ 111
W1 26,2048
AR 55,55
M 0,3,3
XL 57,576
RV 48,8,6272
XS 52
M 1,14,2
WL 8
LO 110,18,110
LU 18,110
XS 53
M 1,15,3
s_add_u32 s42, s94, s60
RA 216,1,7168
HZ 29,18
XS 54
M 2,26,2
s_addc_u32 s43, s95, 0
LL 16,88
LL 114,89
XS 55
M 2,27,3
LA 16
XL 58,640
AW 52,52
M 3,38,2
LC_ 114
SU 104
AW 53,53
M 3,39,3
XL 59,704
BR 48
AW 54,54
AW 55,55
M 4,62,2
WL 6
LO 111,19,111
LU 19,111
AR 248,56
M 4,63,3
s_add_u32 s42, s94, s61
RA 220,1,8192
HS 29,19
AR 249,57
M 5,74,2
LL 17,90
LL 115,91
AR 250,58
M 5,75,3
s_addc_u32 s43, s95, 0
LA 17
SU 108
AR 251,59
M 0,4,4
s_mov_b32 m0, %[pm]
LC_ 115
DF
XS 248
M 0,5,5
WV 17
BW 49
XS 249
M 1,16,4
WL 5
LO 114,16,114
LU 16,114
XS 250
M 1,17,5
RA 224,1,9216
HZ 30,16
XS 251
M 2,28,4
LL 18,92
LL 118,93
AW 56,248
M 2,29,5
WL 5
LA 18
BR 49
AW 57,249
LZ 40,48
WV 16
BW 50
M 3,40,4
LC_ 118
RV 40,8,6336
AW 58,250
M 3,41,5
s_add_u32 s42, s94, s62
BR 50
v_add_f32 v14, v14, v15
s_nop 1
v_add_f32_dpp v14, v14, v14 quad_perm:[1,0,3,2] row_mask:0xf bank_mask:0xf
s_nop 1
v_add_f32_dpp v14, v14, v14 quad_perm:[2,3,0,1] row_mask:0xf bank_mask:0xf
s_nop 1
v_add_f32_dpp v14, v14, v14 row_half_mirror row_mask:0xf bank_mask:0xf
v_cvt_f64_f32 v[14:15], v14
s_mov_b64 exec, s[54:55]
SP
s_mov_b64 exec, -1
s_nop 0
ds_read_b64 v[14:15], v5 offset:2048
AW 59,251
M 4,64,4
WL 9
LO 115,17,115
LU 17,115
AR 52,60
M 4,65,5
s_addc_u32 s43, s95, 0
RA 228,1,10240
HS 30,17
AR 53,61
M 5,76,4
LL 19,94
LL 119,95
AR 54,62
AR 55,63
M 5,77,5
WL 6
LA 19
SU 112
XS 52
LS 44,49
WV 16
BW 51
M 0,6,0
LC_ 119
RV 44,8,6400
)"
R"(XS 53
M 0,7,1
WL 6
BR 51
v_subrev_u32 v5, s39, v5
XS 54
LS 48,50
WV 14
BW 52
M 1,18,0
LO 118,18,118
LU 18,118
XS 55
M 1,19,1
RA 232,1,11264
HZ 25,18
AW 60,52
M 2,30,0
LL 16,96
LL 122,97
AW 61,53
M 2,31,1
WV 12
LA 16
RV 252,22,5392
AW 62,54
M 3,42,0
WL 4
LC_ 122
RV 48,8,6464
AW 63,55
M 3,43,1
s_add_u32 s42, s44, s63
BR 52
v_add_f32 v14, v14, v15
s_nop 1
v_add_f32_dpp v14, v14, v14 quad_perm:[1,0,3,2] row_mask:0xf bank_mask:0xf
s_nop 1
v_add_f32_dpp v14, v14, v14 quad_perm:[2,3,0,1] row_mask:0xf bank_mask:0xf
AR 248,64
LS 40,51
s_addc_u32 s43, s45, 0
BW 53
M 4,66,0
LO 119,19,119
LU 19,119
AR 249,65
M 4,67,1
RA 236,1,12288
HS 25,19
v_add_f32_dpp v14, v14, v14 row_half_mirror row_mask:0xf bank_mask:0xf
AR 250,66
M 5,78,0
LL 17,98
LL 123,99
AR 251,67
M 5,79,1
LA 17
ST 116
v_cvt_f64_f32 v[14:15], v14
XS 248
M 0,8,2
LC_ 123
s_mov_b64 exec, s[54:55]
SP
s_mov_b64 exec, -1
XS 249
M 0,9,3
WL 4
RV 40,8,6528
BR 53
v_pk_add_f16 v29, v30, v29
XS 250
XS 251
LS 44,52
WV 12
BW 54
M 1,20,2
LO 122,16,122
LU 16,122
v_pk_add_f16 v27, v29, v27
AW 64,248
M 1,21,3
RA 216,1,13312
HS 25,16
HX 27
AW 65,249
M 2,32,2
LL 18,100
LL 126,101
AW 66,250
M 2,33,3
WV 11
LA 18
RV 44,8,6592
AW 67,251
M 3,44,2
WL 4
LC_ 126
BR 54
AR 52,68
M 3,45,3
s_add_u32 s42, s44, s64
AR 53,69
AR 54,70
LS 48,53
s_addc_u32 s43, s45, 0
BW 55
M 4,68,2
LO 123,17,123
LU 17,123
M 4,69,3
RA 220,1,14336
HS 25,17
AR 55,71
M 5,80,2
LL 19,102
LL 127,103
XS 52
M 5,81,3
LA 19
ST 120
XS 53
M 0,10,4
LC_ 127
RV 48,8,6656
XS 54
M 0,11,5
WL 5
BR 55
XS 55
LS 40,54
WV 11
BW 56
M 1,22,4
LO 126,18,126
LU 18,126
AW 68,52
M 1,23,5
RA 224,1,15360
HS 25,18
AW 69,53
M 2,34,4
LL 16,56
LL 104,57
AW 70,54
AW 71,55
M 2,35,5
LA 16
RV 40,8,6720
M 3,46,4
LC_ 104
BR 56
M 3,47,5
s_add_u32 s42, s44, s65
M 4,70,4
WL 6
LO 127,19,127
LU 19,127
M 4,71,5
s_addc_u32 s43, s45, 0
RA 228,1,16384
HS 25,19
LS 44,55
WV 10
BW 57
M 5,82,4
LL 17,58
LL 105,59
M 5,83,5
LA 17
ST 124
Z 6,0,0
LC_ 105
RV 44,8,6784
M 6,1,1
BR 57
HX 25
Z 7,12,0
WL 7
LO 104,16,104
LU 16,104
M 7,13,1
WL 6
RA 232,1,17408
HZ 26,16
LS 48,56
WV 10
BW 58
Z 8,24,0
LL 18,60
LL 108,61
M 8,25,1
s_mul_i32 s53, s50, 25600
LA 18
W1 25,1024
Z 9,36,0
s_add_u32 s40, %[wlo], s53
LC_ 108
W1 27,3072
M 9,37,1
s_addc_u32 s41, %[whi], 0
RV 48,8,6848
RA 240,7,0
Z 10,60,0
WL 10
LO 105,17,105
LU 17,105
M 10,61,1
s_mov_b32 m0, s38
RA 236,1,18432
HS 26,17
Z 11,72,0
LL 19,62
LL 109,63
M 11,73,1
LA 19
DW 0
M 6,2,2
LC_ 109
BR 58
M 6,3,3
WL 12
v_add_u32 v4, s39, v4
LS 40,57
WV 9
BW 59
M 7,14,2
WL 8
LO 108,18,108
LU 18,108
M 7,15,3
s_add_u32 s37, s37, 32768
RA 216,1,19456
HZ 29,18
M 8,26,2
s_cmp_eq_u32 s37, s75
LL 16,64
LL 112,65
M 8,27,3
s_cselect_b32 s37, s74, s37
LA 16
DW 1024
M 9,38,2
LC_ 112
RA 244,7,256
M 9,39,3
BR 59
M 10,62,2
WL 7
LO 109,19,109
LU 19,109
M 10,63,3
WL 6
RA 220,1,20480
HS 29,19
LS 44,58
RA 248,7,512
M 11,74,2
LL 17,66
LL 113,67
M 11,75,3
LA 17
DW 2048
M 6,4,4
LC_ 113
M 6,5,5
M 7,16,4
WL 4
LO 112,16,112
LU 16,112
M 7,17,5
s_mul_i32 s53, s51, 49152
RA 224,1,21504
HZ 30,16
LS 48,59
s_bitcmp1_b64 s[90:91], s51
RA 252,7,768
M 8,28,4
s_cselect_b64 s[44:45], s[84:85], s[82:83]
LL 18,68
LL 116,69
M 8,29,5
s_add_u32 s44, s44, s53
LA 18
v_mul_f32 v36, 0xbb2aaaab, v36
M 9,40,4
s_addc_u32 s45, s45, 0
LC_ 116
v_mul_f32 v37, 0xbb2aaaab, v37
M 9,41,5
s_bitcmp1_b64 s[92:93], s51
v_mul_f32 v38, 0xbb2aaaab, v38
v_mul_f32 v39, 0xbb2aaaab, v39
M 10,64,4
WL 4
LO 113,17,113
LU 17,113
M 10,65,5
s_cselect_b64 s[94:95], s[88:89], s[86:87]
RA 228,1,22528
HS 30,17
LK 40,240,48,0
LK 44,242,48,2
s_add_u32 s94, s94, s53
RA 240,7,1024
LK 48,244,49,0
s_nop 0
CF 40,40,41
LK 248,246,49,2
s_addc_u32 s95, s95, 0
RA 244,7,1280
CF 42,42,43
M 11,76,4
LL 19,70
LL 117,71
M 11,77,5
s_add_u32 s51, s51, %[d]
LA 19
CF 44,44,45
M 6,6,0
s_cmp_eq_u32 s51, %[wrap]
LC_ 117
CF 46,46,47
M 6,7,1
s_cselect_b32 s51, %[rst], s51
AW 72,40
AW 73,42
M 7,18,0
WL 5
LO 116,18,116
LU 18,116
M 7,19,1
s_mul_i32 s53, %[d], 17408
RA 232,1,23552
HZ 31,18
M 8,30,0
s_add_u32 s63, s53, %[sb1]
LL 16,72
LL 120,73
M 8,31,1
s_add_u32 s40, s40, 3072
LA 16
AW 74,44
M 9,42,0
s_addc_u32 s41, s41, 0
LC_ 120
s_add_u32 m0, s38, 3072
M 9,43,1
DW 0
AW 75,46
LK 40,248,50,0
CF 48,48,49
LK 44,250,50,2
RA 248,7,1536
CF 50,50,51
GZ 48
CF 248,248,249
CF 250,250,251
M 10,66,0
WL 4
LO 117,19,117
LU 19,117
M 10,67,1
RA 236,1,24576
HS 31,19
M 11,78,0
LL 17,74
LL 121,75
M 11,79,1
LA 17
DW 1024
M 6,8,2
LC_ 121
DW 2048
M 6,9,3
s_lshl_b32 s53, s52, 8
v_add_u32 v1, s37, v0
v_pk_add_f16 v31, v30, v31
M 7,20,2
WL 4
LO 120,16,120
LU 16,120
M 7,21,3
s_add_u32 s48, %[alo], s53
RA 216,1,0
HZ 24,16
AW 76,48
AW 77,50
M 8,32,2
s_addc_u32 s49, %[ahi], 0
LL 18,76
LL 124,77
AW 78,248
AW 79,250
M 8,33,3
s_add_u32 s52, s52, %[d]
LA 18
v_and_b32 v22, 0xfffffcff, v1
LK 48,252,51,0
)"
R"(CF 40,40,41
LK 248,254,51,2
s_cmp_eq_u32 s52, %[wrap]
RA 252,7,1792
CF 42,42,43
GS 49
s_cselect_b32 s52, %[rst], s52
CF 44,44,45
CF 46,46,47
M 9,44,2
LC_ 124
DQ 3072
M 9,45,3
v_pk_add_f16 v29, v31, v29
AW 80,40
M 10,68,2
WL 4
LO 121,17,121
LU 17,121
M 10,69,3
s_add_u32 s50, s50, %[d]
RA 220,1,1024
HS 24,17
M 11,80,2
LL 19,78
LL 125,79
AW 81,42
M 11,81,3
s_cmp_eq_u32 s50, %[wrap]
LA 19
RV 252,22,5120
AW 82,44
AW 83,46
LK 40,240,52,0
CF 48,48,49
LK 44,242,52,2
s_cselect_b32 s50, %[rst], s50
RA 240,7,2048
CF 50,50,51
GS 50
CF 248,248,249
CF 250,250,251
M 6,10,4
LC_ 125
v_pk_add_f16 v26, v29, v26
M 6,11,5
HX 26
AW 84,48
M 7,22,4
WL 5
LO 124,18,124
LU 18,124
M 7,23,5
s_add_u32 s38, s38, 32768
RA 224,1,2048
HS 24,18
M 8,34,4
s_cmp_eq_u32 s38, s77
LL 16,80
LL 106,81
M 8,35,5
s_cselect_b32 s38, s76, s38
LA 16
AW 85,50
AW 86,248
M 9,46,4
s_barrier
LC_ 106
AW 87,250
M 9,47,5
s_sub_u32 s39, 0, s39
LK 48,244,53,0
CF 40,40,41
LK 248,246,53,2
RA 244,7,2304
CF 42,42,43
GS 51
CF 44,44,45
CF 46,46,47
M 10,70,4
WL 4
LO 125,19,125
LU 19,125
M 10,71,5
RA 228,1,3072
HS 24,19
M 11,82,4
LL 17,82
LL 107,83
M 11,83,5
LA 17
HX 24
AW 88,40
AW 89,42
Z 0,0,0
LC_ 107
ds_read_b64 v[14:15], v5 offset:0
AW 90,44
AW 91,46
M 0,1,1
W1 24,0
LK 40,248,54,0
CF 48,48,49
LK 44,250,54,2
RA 248,7,2560
CF 50,50,51
GS 52
CF 248,248,249
CF 250,250,251
Z 1,12,0
WL 7
LO 106,16,106
LU 16,106
M 1,13,1
RA 232,1,4096
HZ 27,16
Z 2,24,0
LL 18,84
LL 110,85
M 2,25,1
LA 18
W1 26,2048
AW 92,48
Z 3,36,0
LC_ 110
AW 93,50
AW 94,248
M 3,37,1
s_add_u32 s42, s94, s60
AW 95,250
LK 48,252,55,0
CF 40,40,41
LK 248,254,55,2
s_addc_u32 s43, s95, 0
RA 252,7,2816
CF 42,42,43
GS 53
CF 44,44,45
CF 46,46,47
Z 4,60,0
WL 8
LO 107,17,107
LU 17,107
M 4,61,1
RA 236,1,6144
HS 27,17
Z 5,72,0
LL 19,86
LL 111,87
M 5,73,1
LA 19
SU 104
M 0,2,2
WL 9
LC_ 111
v_add_f32 v14, v14, v15
AW 96,40
AW 97,42
M 0,3,3
v_add_f32_dpp v14, v14, v14 quad_perm:[1,0,3,2] row_mask:0xf bank_mask:0xf
AW 98,44
M 1,14,2
WL 4
LO 110,18,110
LU 18,110
AW 99,46
M 1,15,3
RA 216,1,7168
HZ 29,18
LK 40,240,56,0
CF 48,48,49
LK 44,242,56,2
CF 50,50,51
v_add_f32_dpp v14, v14, v14 quad_perm:[2,3,0,1] row_mask:0xf bank_mask:0xf
GS 54
CF 248,248,249
CF 250,250,251
M 2,26,2
LL 16,88
LL 114,89
M 2,27,3
LA 16
v_add_f32_dpp v14, v14, v14 row_half_mirror row_mask:0xf bank_mask:0xf
M 3,38,2
LC_ 114
v_cvt_f64_f32 v[14:15], v14
AW 100,48
M 3,39,3
s_add_u32 s42, s94, s61
s_mov_b64 exec, s[54:55]
SP
s_mov_b64 exec, -1
AW 101,50
AW 102,248
M 4,62,2
WL 3
LO 111,19,111
LU 19,111
AW 103,250
M 4,63,3
s_addc_u32 s43, s95, 0
RA 220,1,8192
HS 29,19
LK 48,244,57,0
CF 40,40,41
LK 248,246,57,2
CF 42,42,43
GS 55
CF 44,44,45
CF 46,46,47
M 5,74,2
LL 17,90
LL 115,91
M 5,75,3
LA 17
ds_read_b64 v[14:15], v5 offset:2048
M 0,4,4
LC_ 115
SU 108
AW 104,40
M 0,5,5
s_mov_b32 m0, %[pm]
s_nop 0
DF
v_subrev_u32 v5, s39, v5
AW 105,42
AW 106,44
M 1,16,4
WL 4
LO 114,16,114
LU 16,114
AW 107,46
M 1,17,5
RA 224,1,9216
HZ 30,16
LK 40,248,58,0
CF 48,48,49
LK 44,250,58,2
CF 50,50,51
GS 56
CF 248,248,249
CF 250,250,251
M 2,28,4
LL 18,92
LL 118,93
M 2,29,5
LA 18
AW 108,48
M 3,40,4
LC_ 118
AW 109,50
M 3,41,5
s_add_u32 s42, s94, s62
AW 110,248
AW 111,250
LK 48,252,59,0
CF 40,40,41
LK 248,254,59,2
s_addc_u32 s43, s95, 0
CF 42,42,43
GS 57
CF 44,44,45
CF 46,46,47
M 4,64,4
WL 3
LO 115,17,115
LU 17,115
M 4,65,5
RA 228,1,10240
HS 30,17
M 5,76,4
LL 19,94
LL 119,95
M 5,77,5
LA 19
SU 112
M 0,6,0
LC_ 119
v_add_f32 v14, v14, v15
AW 112,40
M 0,7,1
v_add_f32_dpp v14, v14, v14 quad_perm:[1,0,3,2] row_mask:0xf bank_mask:0xf
AW 113,42
AW 114,44
M 1,18,0
WL 3
LO 118,18,118
LU 18,118
AW 115,46
RV 36,8,6912
M 1,19,1
RA 232,1,11264
HZ 31,18
GS 58
CF 48,48,49
CF 50,50,51
M 2,30,0
LL 16,96
LL 122,97
M 2,31,1
LA 16
RV 252,22,5392
CF 248,248,249
CF 250,250,251
M 3,42,0
LC_ 122
v_add_f32_dpp v14, v14, v14 quad_perm:[2,3,0,1] row_mask:0xf bank_mask:0xf
AW 116,48
AW 117,50
M 3,43,1
s_add_u32 s42, s94, s63
v_add_f32_dpp v14, v14, v14 row_half_mirror row_mask:0xf bank_mask:0xf
AW 118,248
AW 119,250
M 4,66,0
WL 5
LO 119,19,119
LU 19,119
M 4,67,1
s_addc_u32 s43, s95, 0
RA 236,1,12288
HS 31,19
GS 59
SU 116
v_cvt_f64_f32 v[14:15], v14
s_nop 5
v_cndmask_b32 v52, v52, v53, s[78:79]
M 5,78,0
LL 17,98
LL 123,99
v_cndmask_b32 v54, v54, v55, s[78:79]
M 5,79,1
LA 17
s_mov_b64 exec, s[54:55]
SP
s_mov_b64 exec, -1
v_cndmask_b32 v52, v52, v54, s[80:81]
M 0,8,2
LC_ 123
v_pk_add_f16 v31, v30, v31
v_mul_f32 v52, 0x3b2aaaab, v52
M 0,9,3
WL 7
v_pk_add_f16 v29, v31, v29
v_med3_f32 v53, v52, v39, v37
M 1,20,2
WL 3
LO 122,16,122
LU 16,122
v_cmp_eq_f32 vcc, v52, v53
M 1,21,3
RA 216,1,13312
HZ 25,16
C 52,52,52
M 2,32,2
LL 18,100
LL 126,101
v_lshlrev_b32 v52, 16, v52
M 2,33,3
LA 18
v_pk_add_f16 v27, v29, v27
v_add_f32 v52, v36, v52
M 3,44,2
LC_ 126
HX 27
C 52,52,52
M 3,45,3
s_add_u32 s42, s44, s64
v_lshlrev_b32 v52, 16, v52
M 4,68,2
WL 3
LO 123,17,123
LU 17,123
v_sqrt_f32 v52, v52
M 4,69,3
s_addc_u32 s43, s45, 0
RA 220,1,14336
HS 25,17
C 52,52,52
M 5,80,2
LL 19,102
LL 127,103
v_lshlrev_b32 v52, 16, v52
M 5,81,3
LA 19
ST 120
v_rcp_f32 v52, v52
M 0,10,4
LC_ 127
v_mul_f32 v52, v38, v52
M 0,11,5
v_cndmask_b32 v52, 0, v52, vcc
CF 52,52,52
M 1,22,4
WL 3
LO 126,18,126
LU 18,126
M 1,23,5
RA 224,1,15360
HS 25,18
)"
R"(ds_bpermute_b32 v13, v28, v52
M 2,34,4
LL 16,56
LL 104,57
M 2,35,5
LA 16
AR 248,72
M 3,46,4
LC_ 104
AR 249,73
M 3,47,5
s_add_u32 s42, s44, s65
AR 250,74
AR 251,75
M 4,70,4
WL 4
LO 127,19,127
LU 19,127
M 4,71,5
s_addc_u32 s43, s45, 0
RA 228,1,16384
HS 25,19
M 5,82,4
LL 17,58
LL 105,59
M 5,83,5
LA 17
ST 124
Z 6,0,0
LC_ 105
HX 25
M 6,1,1
W1 25,1024
AR 52,76
Z 7,12,0
WL 4
LO 104,16,104
LU 16,104
M 7,13,1
RA 232,1,17408
HZ 26,16
Z 8,24,0
LL 18,60
LL 108,61
M 8,25,1
s_mul_i32 s53, s50, 25600
LA 18
W1 27,3072
Z 9,36,0
s_add_u32 s40, %[wlo], s53
LC_ 108
v_add_u32 v4, s39, v4
AR 53,77
M 9,37,1
s_addc_u32 s41, %[whi], 0
AR 54,78
AR 55,79
Z 10,60,0
WL 5
LO 105,17,105
LU 17,105
M 10,61,1
s_mov_b32 m0, s38
RA 236,1,18432
HS 26,17
XS 248
Z 11,72,0
LL 19,62
LL 109,63
XS 249
M 11,73,1
LA 19
DW 0
XS 250
M 6,2,2
LC_ 109
XS 251
M 6,3,3
AW 72,248
AW 73,249
M 7,14,2
WL 3
LO 108,18,108
LU 18,108
M 7,15,3
s_add_u32 s37, s37, 32768
RA 216,1,19456
HZ 29,18
AW 74,250
M 8,26,2
s_cmp_eq_u32 s37, s75
LL 16,64
LL 112,65
AW 75,251
M 8,27,3
s_cselect_b32 s37, s74, s37
LA 16
XS 52
M 9,38,2
s_mul_i32 s53, %[d], 393216
LC_ 112
XS 53
M 9,39,3
s_add_u32 s53, s53, s57
XS 54
XS 55
M 10,62,2
WL 3
LO 109,19,109
LU 19,109
M 10,63,3
s_add_u32 s46, %[xlo], s53
RA 220,1,20480
HS 29,19
AW 76,52
AW 77,53
M 11,74,2
LL 17,66
LL 113,67
AW 78,54
M 11,75,3
s_addc_u32 s47, %[xhi], 0
LA 17
AW 79,55
M 6,4,4
s_mov_b32 s53, 0xda43cb52
LC_ 113
v_min_u32 v35, v35, v13
M 6,5,5
s_cmp_eq_u32 %[d], 1
AR 248,80
AR 249,81
M 7,16,4
WL 3
LO 112,16,112
LU 16,112
M 7,17,5
s_cselect_b32 s53, s53, 0xfe987610
RA 224,1,21504
HZ 30,16
AR 250,82
M 8,28,4
s_bitcmp1_b64 s[92:93], 48
LL 18,68
LL 116,69
AR 251,83
M 8,29,5
s_cselect_b32 s58, 16, 0
LA 18
XS 248
M 9,40,4
s_lshr_b32 s58, s53, s58
LC_ 116
v_lshrrev_b32 v3, 6, v0
v_and_b32 v3, 12, v3
v_bfe_u32 v3, s58, v3, 4
v_mad_u32_u24 v3, v3, s97, v21
XS 249
M 9,41,5
DW 1024
XS 250
M 10,64,4
WL 3
LO 113,17,113
LU 17,113
XS 251
M 10,65,5
RA 228,1,22528
HS 30,17
AW 80,248
M 11,76,4
LL 19,70
LL 117,71
AW 81,249
M 11,77,5
LA 19
XL 60,0
AW 82,250
M 6,6,0
LC_ 117
DW 2048
AW 83,251
M 6,7,1
s_mul_i32 s53, s51, 49152
AR 52,84
AR 53,85
M 7,18,0
WL 3
LO 116,18,116
LU 18,116
M 7,19,1
s_bitcmp1_b64 s[90:91], s51
RA 232,1,23552
HZ 31,18
AR 54,86
M 8,30,0
s_cselect_b64 s[44:45], s[84:85], s[82:83]
LL 16,72
LL 120,73
AR 55,87
M 8,31,1
s_add_u32 s44, s44, s53
LA 16
s_addc_u32 s45, s45, 0
s_bitcmp1_b64 s[92:93], s51
s_cselect_b64 s[94:95], s[88:89], s[86:87]
s_add_u32 s94, s94, s53
s_addc_u32 s95, s95, 0
s_add_u32 s51, s51, %[d]
s_cmp_eq_u32 s51, %[wrap]
s_cselect_b32 s51, %[rst], s51
XL 61,64
XS 52
XS 53
M 9,42,0
s_add_u32 s40, s40, 3072
LC_ 120
s_addc_u32 s41, s41, 0
s_add_u32 m0, s38, 3072
XS 54
M 9,43,1
XL 62,128
XS 55
M 10,66,0
WL 3
LO 117,19,117
LU 19,117
AW 84,52
M 10,67,1
RA 236,1,24576
HS 31,19
AW 85,53
M 11,78,0
LL 17,74
LL 121,75
AW 86,54
M 11,79,1
LA 17
DW 0
AW 87,55
M 6,8,2
LC_ 121
XL 63,192
AR 248,88
M 6,9,3
DW 1024
v_add_u32 v1, s37, v0
AR 249,89
M 7,20,2
WL 3
LO 120,16,120
LU 16,120
AR 250,90
M 7,21,3
RA 216,1,0
HZ 24,16
AR 251,91
M 8,32,2
LL 18,76
LL 124,77
XS 248
M 8,33,3
LA 18
XL 64,256
XS 249
M 9,44,2
LC_ 124
DW 2048
XS 250
M 9,45,3
s_lshl_b32 s53, s52, 8
v_and_b32 v22, 0xfffffcff, v1
v_pk_add_f16 v31, v30, v31
XS 251
M 10,68,2
WL 3
LO 121,17,121
LU 17,121
AW 88,248
M 10,69,3
s_add_u32 s48, %[alo], s53
RA 220,1,1024
HS 24,17
AW 89,249
M 11,80,2
LL 19,78
LL 125,79
AW 90,250
M 11,81,3
s_addc_u32 s49, %[ahi], 0
LA 19
RV 252,22,5120
AW 91,251
M 6,10,4
s_add_u32 s52, s52, %[d]
LC_ 125
v_pk_add_f16 v29, v31, v29
AR 52,92
AR 53,93
M 6,11,5
s_cmp_eq_u32 s52, %[wrap]
v_pk_add_f16 v26, v29, v26
AR 54,94
M 7,22,4
WL 4
LO 124,18,124
LU 18,124
AR 55,95
M 7,23,5
s_cselect_b32 s52, %[rst], s52
RA 224,1,2048
HS 24,18
XS 52
M 8,34,4
LL 16,80
LL 106,81
HX 26
XS 53
M 8,35,5
LA 16
XL 65,320
DQ 3072
XS 54
M 9,46,4
LC_ 106
XL 66,384
XS 55
M 9,47,5
s_add_u32 s50, s50, %[d]
AW 92,52
AW 93,53
M 10,70,4
WL 3
LO 125,19,125
LU 19,125
M 10,71,5
RA 228,1,3072
HS 24,19
AW 94,54
M 11,82,4
LL 17,82
LL 107,83
AW 95,55
M 11,83,5
LA 17
HX 24
s_cmp_eq_u32 s50, %[wrap]
s_cselect_b32 s50, %[rst], s50
AR 248,96
Z 0,0,0
LC_ 107
XL 67,448
AR 249,97
M 0,1,1
s_add_u32 s38, s38, 32768
WV 12
BW 60
AR 250,98
Z 1,12,0
WL 4
LO 106,16,106
LU 16,106
AR 251,99
M 1,13,1
s_cmp_eq_u32 s38, s77
RA 232,1,4096
HZ 27,16
XS 248
Z 2,24,0
s_cselect_b32 s38, s76, s38
LL 18,84
LL 110,85
XS 249
M 2,25,1
s_barrier
LA 18
RV 40,8,6144
XS 250
Z 3,36,0
s_sub_u32 s39, 0, s39
LC_ 110
ds_read_b64 v[14:15], v5 offset:0
XS 251
M 3,37,1
XL 68,512
RV 44,8,6208
AW 96,248
Z 4,48,0
WL 7
LO 107,17,107
LU 17,107
AW 97,249
AW 98,250
M 4,49,1
RA 236,1,6144
HS 27,17
AW 99,251
Z 5,72,0
LL 19,86
LL 111,87
)"
R"(AR 52,100
M 5,73,1
LA 19
W1 24,0
AR 53,101
M 0,2,2
LC_ 111
W1 26,2048
AR 54,102
M 0,3,3
XL 69,576
RV 48,8,6272
AR 55,103
M 1,14,2
WL 8
LO 110,18,110
LU 18,110
XS 52
M 1,15,3
s_add_u32 s42, s94, s60
RA 216,1,7168
HZ 29,18
XS 53
M 2,26,2
s_addc_u32 s43, s95, 0
LL 16,88
LL 114,89
XS 54
M 2,27,3
LA 16
XL 70,640
XS 55
M 3,38,2
LC_ 114
SU 104
AW 100,52
M 3,39,3
XL 71,704
BR 60
AW 101,53
M 4,50,2
WL 6
LO 111,19,111
LU 19,111
AW 102,54
M 4,51,3
s_add_u32 s42, s94, s61
RA 220,1,8192
HS 29,19
AW 103,55
M 5,74,2
LL 17,90
LL 115,91
AR 248,104
M 5,75,3
s_addc_u32 s43, s95, 0
LA 17
SU 108
AR 249,105
M 0,4,4
LC_ 115
v_add_f32 v14, v14, v15
s_nop 1
v_add_f32_dpp v14, v14, v14 quad_perm:[1,0,3,2] row_mask:0xf bank_mask:0xf
s_nop 1
v_add_f32_dpp v14, v14, v14 quad_perm:[2,3,0,1] row_mask:0xf bank_mask:0xf
s_nop 1
v_add_f32_dpp v14, v14, v14 row_half_mirror row_mask:0xf bank_mask:0xf
AR 250,106
M 0,5,5
WV 16
BW 61
AR 251,107
M 1,16,4
WL 5
LO 114,16,114
LU 16,114
v_cvt_f64_f32 v[14:15], v14
XS 248
M 1,17,5
RA 224,1,9216
HZ 30,16
XS 249
XS 250
M 2,28,4
LL 18,92
LL 118,93
s_mov_b64 exec, s[54:55]
SP
s_mov_b64 exec, -1
XS 251
M 2,29,5
WV 16
LA 18
ds_read_b64 v[14:15], v5 offset:2048
AW 104,248
M 3,40,4
WL 7
LC_ 118
BR 61
AW 105,249
M 3,41,5
s_add_u32 s42, s94, s62
v_subrev_u32 v5, s39, v5
AW 106,250
LZ 40,60
s_addc_u32 s43, s95, 0
BW 62
M 4,52,4
WL 7
LO 115,17,115
LU 17,115
AW 107,251
M 4,53,5
RA 228,1,10240
HS 30,17
AR 52,108
M 5,76,4
LL 19,94
LL 119,95
AR 53,109
M 5,77,5
LA 19
SU 112
AR 54,110
M 0,6,0
LC_ 119
RV 40,8,6336
AR 55,111
M 0,7,1
WL 5
BR 62
XS 52
LS 44,61
WV 15
BW 63
M 1,18,0
LO 118,18,118
LU 18,118
XS 53
M 1,19,1
RA 232,1,11264
HZ 31,18
XS 54
M 2,30,0
LL 16,96
LL 122,97
XS 55
M 2,31,1
WV 13
LA 16
RV 252,22,5392
AW 108,52
M 3,42,0
WL 4
LC_ 122
RV 44,8,6400
AW 109,53
M 3,43,1
s_add_u32 s42, s94, s63
BR 63
v_add_f32 v14, v14, v15
s_nop 1
v_add_f32_dpp v14, v14, v14 quad_perm:[1,0,3,2] row_mask:0xf bank_mask:0xf
s_nop 1
v_add_f32_dpp v14, v14, v14 quad_perm:[2,3,0,1] row_mask:0xf bank_mask:0xf
AW 110,54
LS 48,62
s_addc_u32 s43, s95, 0
BW 64
M 4,54,0
LO 119,19,119
LU 19,119
AW 111,55
M 4,55,1
RA 236,1,12288
HS 31,19
v_add_f32_dpp v14, v14, v14 row_half_mirror row_mask:0xf bank_mask:0xf
AR 248,112
M 5,78,0
LL 17,98
LL 123,99
AR 249,113
M 5,79,1
LA 17
SU 116
v_cvt_f64_f32 v[14:15], v14
AR 250,114
AR 251,115
M 0,8,2
LC_ 123
s_mov_b64 exec, s[54:55]
SP
s_mov_b64 exec, -1
XS 248
M 0,9,3
WL 4
RV 48,8,6464
BR 64
XS 249
LS 40,63
WV 13
BW 65
M 1,20,2
LO 122,16,122
LU 16,122
XS 250
M 1,21,3
RA 216,1,13312
HZ 25,16
XS 251
M 2,32,2
LL 18,100
LL 126,101
AW 112,248
M 2,33,3
LA 18
RV 40,8,6528
AW 113,249
M 3,44,2
LC_ 126
BR 65
AW 114,250
M 3,45,3
s_add_u32 s42, s44, s64
v_pk_add_f16 v31, v30, v31
AW 115,251
M 4,56,2
WL 6
LO 123,17,123
LU 17,123
AR 52,116
M 4,57,3
s_addc_u32 s43, s45, 0
RA 220,1,14336
HS 25,17
AR 53,117
LS 44,64
WV 11
BW 66
M 5,80,2
LL 19,102
LL 127,103
AR 54,118
M 5,81,3
LA 19
ST 120
AR 55,119
M 0,10,4
LC_ 127
RV 44,8,6592
XS 52
M 0,11,5
WL 5
BR 66
v_pk_add_f16 v29, v31, v29
XS 53
LS 48,65
WV 11
BW 67
M 1,22,4
LO 126,18,126
LU 18,126
XS 54
M 1,23,5
RA 224,1,15360
HS 25,18
v_pk_add_f16 v27, v29, v27
XS 55
M 2,34,4
LL 16,56
LL 104,57
HX 27
AW 116,52
M 2,35,5
WV 10
LA 16
RV 48,8,6656
AW 117,53
M 3,46,4
WL 4
LC_ 104
BR 67
AW 118,54
AW 119,55
M 3,47,5
s_add_u32 s42, s44, s65
LS 40,66
s_addc_u32 s43, s45, 0
BW 68
M 4,58,4
LO 127,19,127
LU 19,127
M 4,59,5
RA 228,1,16384
HS 25,19
M 5,82,4
LL 17,58
LL 105,59
M 5,83,5
LA 17
ST 124
Z 6,0,0
LC_ 105
RV 40,8,6720
M 6,1,1
BR 68
HX 25
Z 7,12,0
WL 7
LO 104,16,104
LU 16,104
M 7,13,1
WL 6
RA 232,1,17408
HZ 26,16
LS 44,67
WV 10
BW 69
Z 8,24,0
LL 18,60
LL 108,61
M 8,25,1
s_mul_i32 s53, s50, 25600
LA 18
W1 25,1024
Z 9,36,0
s_add_u32 s40, %[wlo], s53
LC_ 108
W1 27,3072
M 9,37,1
s_addc_u32 s41, %[whi], 0
RV 44,8,6784
BR 69
Z 10,48,0
WL 8
LO 105,17,105
LU 17,105
M 10,49,1
s_mov_b32 m0, s38
RA 236,1,18432
HS 26,17
LS 48,68
WV 9
BW 70
Z 11,72,0
LL 19,62
LL 109,63
M 11,73,1
LA 19
DW 0
M 6,2,2
LC_ 109
RV 48,8,6848
M 6,3,3
RA 240,7,0
BR 70
M 7,14,2
WL 7
LO 108,18,108
LU 18,108
M 7,15,3
s_add_u32 s37, s37, 32768
RA 216,1,19456
HZ 29,18
M 8,26,2
s_cmp_eq_u32 s37, s75
LL 16,64
LL 112,65
M 8,27,3
s_cselect_b32 s37, s74, s37
LA 16
DW 1024
LS 40,69
WV 9
BW 71
M 9,38,2
LC_ 112
RA 244,7,256
M 9,39,3
BR 71
v_add_u32 v4, s39, v4
M 10,50,2
WL 9
LO 109,19,109
LU 19,109
M 10,51,3
RA 220,1,20480
HS 29,19
M 11,74,2
LL 17,66
LL 113,67
M 11,75,3
WL 7
LA 17
DW 2048
LS 44,70
RA 248,7,512
M 6,4,4
LC_ 113
)"
R"(M 6,5,5
M 7,16,4
WL 4
LO 112,16,112
LU 16,112
M 7,17,5
s_mul_i32 s53, s51, 49152
RA 224,1,21504
HZ 30,16
LS 48,71
s_bitcmp1_b64 s[90:91], s51
RA 252,7,768
M 8,28,4
s_cselect_b64 s[44:45], s[84:85], s[82:83]
LL 18,68
LL 116,69
M 8,29,5
s_add_u32 s44, s44, s53
LA 18
v_mul_f32 v36, 0xbb2aaaab, v36
M 9,40,4
s_addc_u32 s45, s45, 0
LC_ 116
v_mul_f32 v37, 0xbb2aaaab, v37
M 9,41,5
s_bitcmp1_b64 s[92:93], s51
v_mul_f32 v38, 0xbb2aaaab, v38
v_mul_f32 v39, 0xbb2aaaab, v39
M 10,52,4
WL 4
LO 113,17,113
LU 17,113
M 10,53,5
s_cselect_b64 s[94:95], s[88:89], s[86:87]
RA 228,1,22528
HS 30,17
LK 40,240,60,0
LK 44,242,60,2
s_add_u32 s94, s94, s53
RA 240,7,1024
LK 48,244,61,0
s_nop 0
CF 40,40,41
LK 248,246,61,2
s_addc_u32 s95, s95, 0
RA 244,7,1280
CF 42,42,43
M 11,76,4
LL 19,70
LL 117,71
M 11,77,5
s_add_u32 s51, s51, %[d]
LA 19
CF 44,44,45
M 6,6,0
s_cmp_eq_u32 s51, %[wrap]
LC_ 117
CF 46,46,47
M 6,7,1
s_cselect_b32 s51, %[rst], s51
AW 120,40
AW 121,42
M 7,18,0
WL 5
LO 116,18,116
LU 18,116
M 7,19,1
s_mul_i32 s53, %[d], 32768
RA 232,1,23552
HZ 31,18
M 8,30,0
s_add_u32 s64, s53, %[sb1]
LL 16,72
LL 120,73
M 8,31,1
s_add_u32 s40, s40, 3072
LA 16
AW 122,44
M 9,42,0
s_addc_u32 s41, s41, 0
LC_ 120
s_add_u32 m0, s38, 3072
M 9,43,1
DW 0
AW 123,46
LK 40,248,62,0
CF 48,48,49
LK 44,250,62,2
RA 248,7,1536
CF 50,50,51
GZ 60
CF 248,248,249
CF 250,250,251
M 10,54,0
WL 4
LO 117,19,117
LU 19,117
M 10,55,1
RA 236,1,24576
HS 31,19
M 11,78,0
LL 17,74
LL 121,75
M 11,79,1
LA 17
DW 1024
M 6,8,2
LC_ 121
DW 2048
M 6,9,3
s_lshl_b32 s53, s52, 8
v_add_u32 v1, s37, v0
v_pk_add_f16 v31, v30, v31
M 7,20,2
WL 4
LO 120,16,120
LU 16,120
M 7,21,3
s_add_u32 s48, %[alo], s53
RA 216,1,0
HZ 32,16
AW 124,48
AW 125,50
M 8,32,2
s_addc_u32 s49, %[ahi], 0
LL 18,76
LL 124,77
AW 126,248
AW 127,250
M 8,33,3
s_add_u32 s52, s52, %[d]
LA 18
v_and_b32 v22, 0xfffffcff, v1
LK 48,252,63,0
CF 40,40,41
LK 248,254,63,2
s_cmp_eq_u32 s52, %[wrap]
RA 252,7,1792
CF 42,42,43
GS 61
s_cselect_b32 s52, %[rst], s52
CF 44,44,45
CF 46,46,47
M 9,44,2
LC_ 124
DQ 3072
M 9,45,3
AW 128,40
AW 129,42
M 10,56,2
WL 4
LO 121,17,121
LU 17,121
M 10,57,3
s_add_u32 s50, s50, %[d]
RA 220,1,1024
HS 32,17
M 11,80,2
LL 19,78
LL 125,79
M 11,81,3
s_cmp_eq_u32 s50, %[wrap]
LA 19
RV 252,22,5120
AW 130,44
AW 131,46
LK 40,240,64,0
CF 48,48,49
LK 44,242,64,2
s_cselect_b32 s50, %[rst], s50
RA 240,7,2048
CF 50,50,51
GS 62
CF 248,248,249
CF 250,250,251
M 6,10,4
LC_ 125
v_pk_add_f16 v32, v29, v32
M 6,11,5
v_pk_add_f16 v32, v31, v32
AW 132,48
M 7,22,4
WL 5
LO 124,18,124
LU 18,124
M 7,23,5
s_add_u32 s38, s38, 32768
RA 224,1,2048
HZ 24,18
M 8,34,4
s_cmp_eq_u32 s38, s77
LL 16,80
LL 106,81
M 8,35,5
s_cselect_b32 s38, s76, s38
LA 16
v_pk_add_f16 v26, v32, v26
AW 133,50
AW 134,248
M 9,46,4
s_barrier
LC_ 106
HX 26
AW 135,250
M 9,47,5
s_sub_u32 s39, 0, s39
LK 48,244,65,0
CF 40,40,41
LK 248,246,65,2
RA 244,7,2304
CF 42,42,43
GS 63
CF 44,44,45
CF 46,46,47
M 10,58,4
WL 4
LO 125,19,125
LU 19,125
M 10,59,5
RA 228,1,3072
HS 24,19
M 11,82,4
LL 17,82
LL 107,83
M 11,83,5
LA 17
HX 24
AW 136,40
AW 137,42
Z 0,0,0
LC_ 107
ds_read_b64 v[14:15], v5 offset:0
AW 138,44
AW 139,46
M 0,1,1
W1 24,0
LK 40,248,66,0
CF 48,48,49
LK 44,250,66,2
RA 248,7,2560
CF 50,50,51
GS 64
CF 248,248,249
CF 250,250,251
Z 1,12,0
WL 7
LO 106,16,106
LU 16,106
M 1,13,1
RA 232,1,4096
HZ 27,16
Z 2,24,0
LL 18,84
LL 110,85
M 2,25,1
LA 18
W1 26,2048
AW 140,48
Z 3,36,0
LC_ 110
AW 141,50
AW 142,248
M 3,37,1
s_add_u32 s42, s94, s60
AW 143,250
LK 48,252,67,0
CF 40,40,41
LK 248,254,67,2
s_addc_u32 s43, s95, 0
RA 252,7,2816
CF 42,42,43
GS 65
CF 44,44,45
CF 46,46,47
Z 4,48,0
WL 8
LO 107,17,107
LU 17,107
M 4,49,1
RA 236,1,6144
HS 27,17
Z 5,72,0
LL 19,86
LL 111,87
M 5,73,1
LA 19
SU 104
M 0,2,2
WL 9
LC_ 111
v_add_f32 v14, v14, v15
AW 144,40
AW 145,42
M 0,3,3
v_add_f32_dpp v14, v14, v14 quad_perm:[1,0,3,2] row_mask:0xf bank_mask:0xf
AW 146,44
M 1,14,2
WL 4
LO 110,18,110
LU 18,110
AW 147,46
M 1,15,3
RA 216,1,7168
HZ 29,18
LK 40,240,68,0
CF 48,48,49
LK 44,242,68,2
CF 50,50,51
v_add_f32_dpp v14, v14, v14 quad_perm:[2,3,0,1] row_mask:0xf bank_mask:0xf
GS 66
CF 248,248,249
CF 250,250,251
M 2,26,2
LL 16,88
LL 114,89
M 2,27,3
LA 16
v_add_f32_dpp v14, v14, v14 row_half_mirror row_mask:0xf bank_mask:0xf
M 3,38,2
LC_ 114
v_cvt_f64_f32 v[14:15], v14
AW 148,48
M 3,39,3
s_add_u32 s42, s94, s61
s_mov_b64 exec, s[54:55]
SP
s_mov_b64 exec, -1
AW 149,50
AW 150,248
M 4,50,2
WL 3
LO 111,19,111
LU 19,111
AW 151,250
M 4,51,3
s_addc_u32 s43, s95, 0
RA 220,1,8192
HS 29,19
LK 48,244,69,0
CF 40,40,41
LK 248,246,69,2
CF 42,42,43
GS 67
CF 44,44,45
CF 46,46,47
M 5,74,2
LL 17,90
LL 115,91
M 5,75,3
LA 17
ds_read_b64 v[14:15], v5 offset:2048
M 0,4,4
LC_ 115
SU 108
AW 152,40
M 0,5,5
v_subrev_u32 v5, s39, v5
AW 153,42
AW 154,44
M 1,16,4
WL 4
LO 114,16,114
LU 16,114
AW 155,46
M 1,17,5
RA 224,1,9216
HZ 30,16
LK 40,248,70,0
CF 48,48,49
LK 44,250,70,2
CF 50,50,51
GS 68
CF 248,248,249
CF 250,250,251
M 2,28,4
LL 18,92
LL 118,93
M 2,29,5
LA 18
AW 156,48
M 3,40,4
LC_ 118
AW 157,50
M 3,41,5
s_add_u32 s42, s94, s62
AW 158,248
AW 159,250
LK 48,252,71,0
CF 40,40,41
LK 248,254,71,2
s_addc_u32 s43, s95, 0
CF 42,42,43
GS 69
CF 44,44,45
CF 46,46,47
M 4,52,4
WL 3
LO 115,17,115
LU 17,115
M 4,53,5
)"
R"(RA 228,1,10240
HS 30,17
M 5,76,4
LL 19,94
LL 119,95
M 5,77,5
LA 19
SU 112
M 0,6,0
LC_ 119
v_add_f32 v14, v14, v15
AW 160,40
M 0,7,1
v_add_f32_dpp v14, v14, v14 quad_perm:[1,0,3,2] row_mask:0xf bank_mask:0xf
AW 161,42
AW 162,44
M 1,18,0
WL 3
LO 118,18,118
LU 18,118
AW 163,46
RV 36,8,6912
M 1,19,1
RA 232,1,11264
HZ 31,18
GS 70
CF 48,48,49
CF 50,50,51
M 2,30,0
LL 16,96
LL 122,97
M 2,31,1
LA 16
RV 252,22,5392
CF 248,248,249
CF 250,250,251
M 3,42,0
LC_ 122
v_add_f32_dpp v14, v14, v14 quad_perm:[2,3,0,1] row_mask:0xf bank_mask:0xf
AW 164,48
AW 165,50
M 3,43,1
s_add_u32 s42, s94, s63
v_add_f32_dpp v14, v14, v14 row_half_mirror row_mask:0xf bank_mask:0xf
AW 166,248
AW 167,250
M 4,54,0
WL 5
LO 119,19,119
LU 19,119
M 4,55,1
s_addc_u32 s43, s95, 0
RA 236,1,12288
HS 31,19
GS 71
SU 116
v_cvt_f64_f32 v[14:15], v14
s_nop 5
v_cndmask_b32 v52, v52, v53, s[78:79]
M 5,78,0
LL 17,98
LL 123,99
v_cndmask_b32 v54, v54, v55, s[78:79]
M 5,79,1
LA 17
s_mov_b64 exec, s[54:55]
SP
s_mov_b64 exec, -1
v_cndmask_b32 v52, v52, v54, s[80:81]
M 0,8,2
LC_ 123
v_pk_add_f16 v31, v30, v31
v_mul_f32 v52, 0x3b2aaaab, v52
M 0,9,3
WL 7
v_med3_f32 v53, v52, v39, v37
v_cmp_eq_f32 vcc, v52, v53
M 1,20,2
WL 3
LO 122,16,122
LU 16,122
M 1,21,3
RA 216,1,13312
HZ 32,16
C 52,52,52
M 2,32,2
LL 18,100
LL 126,101
v_lshlrev_b32 v52, 16, v52
M 2,33,3
LA 18
v_add_f32 v52, v36, v52
M 3,44,2
LC_ 126
C 52,52,52
M 3,45,3
s_add_u32 s42, s94, s64
v_lshlrev_b32 v52, 16, v52
M 4,56,2
WL 3
LO 123,17,123
LU 17,123
v_sqrt_f32 v52, v52
M 4,57,3
s_addc_u32 s43, s95, 0
RA 220,1,14336
HS 32,17
C 52,52,52
M 5,80,2
LL 19,102
LL 127,103
v_lshlrev_b32 v52, 16, v52
M 5,81,3
LA 19
SU 120
v_rcp_f32 v52, v52
M 0,10,4
LC_ 127
v_pk_add_f16 v32, v29, v32
v_mul_f32 v52, v38, v52
M 0,11,5
v_pk_add_f16 v32, v31, v32
v_cndmask_b32 v52, 0, v52, vcc
M 1,22,4
WL 3
LO 126,18,126
LU 18,126
CF 52,52,52
M 1,23,5
RA 224,1,15360
HZ 25,18
ds_bpermute_b32 v13, v28, v52
M 2,34,4
LL 16,56
LL 104,57
M 2,35,5
LA 16
v_pk_add_f16 v27, v32, v27
M 3,46,4
LC_ 104
HX 27
M 3,47,5
s_add_u32 s42, s44, s65
AR 248,120
AR 249,121
M 4,58,4
WL 4
LO 127,19,127
LU 19,127
M 4,59,5
s_addc_u32 s43, s45, 0
RA 228,1,16384
HS 25,19
M 5,82,4
LL 17,58
LL 105,59
M 5,83,5
LA 17
ST 124
Z 6,0,0
LC_ 105
HX 25
M 6,1,1
W1 25,1024
AR 250,122
Z 7,12,0
WL 4
LO 104,16,104
LU 16,104
M 7,13,1
RA 232,1,17408
HZ 26,16
Z 8,24,0
LL 18,60
LL 108,61
AR 251,123
M 8,25,1
s_mul_i32 s53, s50, 25600
LA 18
W1 27,3072
AR 52,124
Z 9,36,0
s_add_u32 s40, %[wlo], s53
LC_ 108
v_add_u32 v4, s39, v4
AR 53,125
M 9,37,1
s_addc_u32 s41, %[whi], 0
AR 54,126
AR 55,127
Z 10,48,0
WL 5
LO 105,17,105
LU 17,105
M 10,49,1
s_mov_b32 m0, s38
RA 236,1,18432
HS 26,17
XS 248
Z 11,72,0
LL 19,62
LL 109,63
XS 249
M 11,73,1
LA 19
DW 0
XS 250
M 6,2,2
LC_ 109
XS 251
M 6,3,3
AW 120,248
AW 121,249
M 7,14,2
WL 3
LO 108,18,108
LU 18,108
M 7,15,3
s_add_u32 s37, s37, 32768
RA 216,1,19456
HZ 29,18
AW 122,250
M 8,26,2
s_cmp_eq_u32 s37, s75
LL 16,64
LL 112,65
AW 123,251
M 8,27,3
s_cselect_b32 s37, s74, s37
LA 16
DW 1024
XS 52
M 9,38,2
LC_ 112
XS 53
M 9,39,3
XS 54
XS 55
M 10,50,2
WL 3
LO 109,19,109
LU 19,109
M 10,51,3
RA 220,1,20480
HS 29,19
AW 124,52
M 11,74,2
LL 17,66
LL 113,67
AW 125,53
M 11,75,3
LA 17
DW 2048
AW 126,54
M 6,4,4
LC_ 113
AW 127,55
M 6,5,5
v_min_u32 v35, v35, v13
AR 248,128
M 7,16,4
WL 3
LO 112,16,112
LU 16,112
M 7,17,5
s_mul_i32 s53, s51, 49152
RA 224,1,21504
HZ 30,16
AR 249,129
M 8,28,4
s_bitcmp1_b64 s[90:91], s51
LL 18,68
LL 116,69
AR 250,130
M 8,29,5
s_cselect_b64 s[44:45], s[84:85], s[82:83]
LA 18
AR 251,131
XS 248
M 9,40,4
s_add_u32 s44, s44, s53
LC_ 116
XS 249
M 9,41,5
s_addc_u32 s45, s45, 0
XS 250
XS 251
M 10,52,4
WL 3
LO 113,17,113
LU 17,113
M 10,53,5
s_bitcmp1_b64 s[92:93], s51
RA 228,1,22528
HS 30,17
AW 128,248
M 11,76,4
LL 19,70
LL 117,71
AW 129,249
M 11,77,5
s_cselect_b64 s[94:95], s[88:89], s[86:87]
LA 19
AW 130,250
M 6,6,0
s_add_u32 s94, s94, s53
LC_ 117
AW 131,251
M 6,7,1
s_addc_u32 s95, s95, 0
AR 52,132
AR 53,133
M 7,18,0
WL 3
LO 116,18,116
LU 18,116
M 7,19,1
s_add_u32 s51, s51, %[d]
RA 232,1,23552
HZ 31,18
AR 54,134
M 8,30,0
s_cmp_eq_u32 s51, %[wrap]
LL 16,72
LL 120,73
AR 55,135
M 8,31,1
s_cselect_b32 s51, %[rst], s51
LA 16
XS 52
M 9,42,0
s_add_u32 s40, s40, 3072
LC_ 120
s_addc_u32 s41, s41, 0
s_add_u32 m0, s38, 3072
XS 53
M 9,43,1
DW 0
XS 54
M 10,54,0
WL 3
LO 117,19,117
LU 19,117
XS 55
M 10,55,1
RA 236,1,24576
HS 31,19
AW 132,52
M 11,78,0
LL 17,74
LL 121,75
AW 133,53
M 11,79,1
LA 17
DW 1024
AW 134,54
M 6,8,2
LC_ 121
DW 2048
AW 135,55
M 6,9,3
s_lshl_b32 s53, s52, 8
v_add_u32 v1, s37, v0
v_pk_add_f16 v31, v30, v31
AR 248,136
M 7,20,2
WL 3
LO 120,16,120
LU 16,120
AR 249,137
M 7,21,3
s_add_u32 s48, %[alo], s53
RA 216,1,0
HZ 32,16
AR 250,138
M 8,32,2
s_addc_u32 s49, %[ahi], 0
LL 18,76
LL 124,77
AR 251,139
M 8,33,3
s_add_u32 s52, s52, %[d]
LA 18
v_and_b32 v22, 0xfffffcff, v1
XS 248
M 9,44,2
s_cmp_eq_u32 s52, %[wrap]
LC_ 124
XS 249
M 9,45,3
s_cselect_b32 s52, %[rst], s52
DQ 3072
XS 250
M 10,56,2
WL 3
LO 121,17,121
LU 17,121
XS 251
M 10,57,3
s_add_u32 s50, s50, %[d]
)"
R"(RA 220,1,1024
HS 32,17
AW 136,248
AW 137,249
M 11,80,2
LL 19,78
LL 125,79
AW 138,250
M 11,81,3
s_cmp_eq_u32 s50, %[wrap]
LA 19
RV 252,22,5120
AW 139,251
M 6,10,4
s_cselect_b32 s50, %[rst], s50
LC_ 125
v_pk_add_f16 v32, v29, v32
AR 52,140
M 6,11,5
v_pk_add_f16 v32, v31, v32
AR 53,141
M 7,22,4
WL 4
LO 124,18,124
LU 18,124
AR 54,142
M 7,23,5
s_add_u32 s38, s38, 32768
RA 224,1,2048
HZ 24,18
AR 55,143
M 8,34,4
s_cmp_eq_u32 s38, s77
LL 16,80
LL 106,81
XS 52
M 8,35,5
s_cselect_b32 s38, s76, s38
LA 16
v_pk_add_f16 v26, v32, v26
XS 53
M 9,46,4
s_barrier
LC_ 106
HX 26
XS 54
M 9,47,5
s_sub_u32 s39, 0, s39
XS 55
AW 140,52
M 10,58,4
WL 3
LO 125,19,125
LU 19,125
M 10,59,5
RA 228,1,3072
HS 24,19
AW 141,53
M 11,82,4
LL 17,82
LL 107,83
AW 142,54
M 11,83,5
LA 17
HX 24
AW 143,55
Z 0,0,0
LC_ 107
ds_read_b64 v[14:15], v5 offset:0
AR 248,144
M 0,1,1
W1 24,0
AR 249,145
Z 1,12,0
WL 5
LO 106,16,106
LU 16,106
AR 250,146
M 1,13,1
RA 232,1,4096
HZ 27,16
AR 251,147
Z 2,24,0
LL 18,84
LL 110,85
XS 248
M 2,25,1
LA 18
W1 26,2048
XS 249
Z 3,36,0
LC_ 110
XS 250
M 3,37,1
s_add_u32 s42, s94, s60
XS 251
AW 144,248
Z 4,48,0
WL 6
LO 107,17,107
LU 17,107
M 4,49,1
s_addc_u32 s43, s95, 0
RA 236,1,6144
HS 27,17
AW 145,249
Z 5,60,0
LL 19,86
LL 111,87
AW 146,250
M 5,61,1
LA 19
SU 104
AW 147,251
M 0,2,2
WL 7
LC_ 111
v_add_f32 v14, v14, v15
AR 52,148
M 0,3,3
v_add_f32_dpp v14, v14, v14 quad_perm:[1,0,3,2] row_mask:0xf bank_mask:0xf
AR 53,149
AR 54,150
M 1,14,2
WL 3
LO 110,18,110
LU 18,110
AR 55,151
M 1,15,3
RA 216,1,7168
HZ 29,18
XS 52
M 2,26,2
LL 16,88
LL 114,89
XS 53
M 2,27,3
LA 16
v_add_f32_dpp v14, v14, v14 quad_perm:[2,3,0,1] row_mask:0xf bank_mask:0xf
XS 54
M 3,38,2
LC_ 114
v_add_f32_dpp v14, v14, v14 row_half_mirror row_mask:0xf bank_mask:0xf
XS 55
M 3,39,3
s_add_u32 s42, s94, s61
v_cvt_f64_f32 v[14:15], v14
AW 148,52
M 4,50,2
WL 3
LO 111,19,111
LU 19,111
AW 149,53
M 4,51,3
s_addc_u32 s43, s95, 0
RA 220,1,8192
HS 29,19
AW 150,54
M 5,62,2
LL 17,90
LL 115,91
AW 151,55
M 5,63,3
LA 17
s_mov_b64 exec, s[54:55]
SP
s_mov_b64 exec, -1
AR 248,152
M 0,4,4
LC_ 115
ds_read_b64 v[14:15], v5 offset:2048
AR 249,153
M 0,5,5
SU 108
v_subrev_u32 v5, s39, v5
AR 250,154
M 1,16,4
WL 4
LO 114,16,114
LU 16,114
AR 251,155
M 1,17,5
RA 224,1,9216
HZ 30,16
XS 248
M 2,28,4
LL 18,92
LL 118,93
XS 249
M 2,29,5
LA 18
XS 250
M 3,40,4
LC_ 118
XS 251
M 3,41,5
s_add_u32 s42, s94, s62
AW 152,248
AW 153,249
M 4,52,4
WL 4
LO 115,17,115
LU 17,115
M 4,53,5
s_addc_u32 s43, s95, 0
RA 228,1,10240
HS 30,17
AW 154,250
M 5,64,4
LL 19,94
LL 119,95
AW 155,251
M 5,65,5
LA 19
SU 112
AR 52,156
M 0,6,0
WL 5
LC_ 119
v_add_f32 v14, v14, v15
AR 53,157
M 0,7,1
v_add_f32_dpp v14, v14, v14 quad_perm:[1,0,3,2] row_mask:0xf bank_mask:0xf
AR 54,158
M 1,18,0
WL 3
LO 118,18,118
LU 18,118
AR 55,159
M 1,19,1
RA 232,1,11264
HZ 31,18
XS 52
M 2,30,0
LL 16,96
LL 122,97
XS 53
M 2,31,1
LA 16
RV 252,22,5392
XS 54
XS 55
M 3,42,0
LC_ 122
v_add_f32_dpp v14, v14, v14 quad_perm:[2,3,0,1] row_mask:0xf bank_mask:0xf
AW 156,52
M 3,43,1
s_add_u32 s42, s94, s63
v_add_f32_dpp v14, v14, v14 row_half_mirror row_mask:0xf bank_mask:0xf
AW 157,53
M 4,54,0
WL 4
LO 119,19,119
LU 19,119
AW 158,54
M 4,55,1
s_addc_u32 s43, s95, 0
RA 236,1,12288
HS 31,19
AW 159,55
M 5,66,0
LL 17,98
LL 123,99
AR 248,160
M 5,67,1
LA 17
SU 116
v_cvt_f64_f32 v[14:15], v14
AR 249,161
M 0,8,2
LC_ 123
s_mov_b64 exec, s[54:55]
SP
s_mov_b64 exec, -1
AR 250,162
M 0,9,3
v_pk_add_f16 v31, v30, v31
AR 251,163
M 1,20,2
WL 3
LO 122,16,122
LU 16,122
XS 248
M 1,21,3
RA 216,1,13312
HZ 32,16
XS 249
M 2,32,2
LL 18,100
LL 126,101
XS 250
M 2,33,3
LA 18
XS 251
M 3,44,2
LC_ 126
AW 160,248
M 3,45,3
s_add_u32 s42, s94, s64
AW 161,249
AW 162,250
M 4,56,2
WL 3
LO 123,17,123
LU 17,123
M 4,57,3
s_addc_u32 s43, s95, 0
RA 220,1,14336
HS 32,17
AW 163,251
M 5,68,2
LL 19,102
LL 127,103
AR 52,164
M 5,69,3
LA 19
SU 120
AR 53,165
M 0,10,4
LC_ 127
v_pk_add_f16 v32, v29, v32
AR 54,166
M 0,11,5
v_pk_add_f16 v32, v31, v32
AR 55,167
M 1,22,4
WL 3
LO 126,18,126
LU 18,126
XS 52
M 1,23,5
RA 224,1,15360
HZ 25,18
XS 53
M 2,34,4
LL 16,56
LL 104,57
XS 54
M 2,35,5
LA 16
v_pk_add_f16 v27, v32, v27
XS 55
M 3,46,4
LC_ 104
HX 27
AW 164,52
M 3,47,5
s_add_u32 s42, s44, s65
AW 165,53
AW 166,54
M 4,58,4
WL 3
LO 127,19,127
LU 19,127
AW 167,55
M 4,59,5
s_addc_u32 s43, s45, 0
RA 228,1,16384
HS 25,19
M 5,70,4
LL 17,58
LL 105,59
M 5,71,5
LA 17
ST 124
Z 6,0,0
LC_ 105
HX 25
M 6,1,1
W1 25,1024
Z 7,12,0
WL 4
LO 104,16,104
LU 16,104
M 7,13,1
RA 232,1,17408
HZ 26,16
Z 8,24,0
LL 18,60
LL 108,61
M 8,25,1
s_mul_i32 s53, s50, 25600
LA 18
W1 27,3072
Z 9,36,0
s_add_u32 s40, %[wlo], s53
LC_ 108
v_add_u32 v4, s39, v4
M 9,37,1
s_addc_u32 s41, %[whi], 0
Z 10,48,0
WL 5
LO 105,17,105
LU 17,105
M 10,49,1
s_mov_b32 m0, s38
RA 236,1,18432
HS 26,17
Z 11,60,0
LL 19,62
)"
R"(LL 109,63
M 11,61,1
LA 19
DW 0
M 6,2,2
LC_ 109
M 6,3,3
M 7,14,2
WL 3
LO 108,18,108
LU 18,108
M 7,15,3
s_add_u32 s37, s37, 32768
RA 216,1,19456
HZ 29,18
M 8,26,2
s_cmp_eq_u32 s37, s75
LL 16,64
LL 112,65
M 8,27,3
s_cselect_b32 s37, s74, s37
LA 16
DW 1024
M 9,38,2
LC_ 112
M 9,39,3
M 10,50,2
WL 3
LO 109,19,109
LU 19,109
M 10,51,3
RA 220,1,20480
HS 29,19
M 11,62,2
LL 17,66
LL 113,67
M 11,63,3
LA 17
DW 2048
M 6,4,4
LC_ 113
M 6,5,5
M 7,16,4
WL 3
LO 112,16,112
LU 16,112
M 7,17,5
s_mul_i32 s53, s51, 49152
RA 224,1,21504
HZ 30,16
M 8,28,4
s_bitcmp1_b64 s[92:93], s51
LL 18,68
LL 116,69
M 8,29,5
s_cselect_b64 s[94:95], s[88:89], s[86:87]
LA 18
M 9,40,4
s_add_u32 s94, s94, s53
LC_ 116
M 9,41,5
s_addc_u32 s95, s95, 0
M 10,52,4
WL 3
LO 113,17,113
LU 17,113
M 10,53,5
s_add_u32 s51, s51, %[d]
RA 228,1,22528
HS 30,17
M 11,64,4
LL 19,70
LL 117,71
M 11,65,5
s_cmp_eq_u32 s51, %[wrap]
LA 19
M 6,6,0
s_cselect_b32 s51, %[rst], s51
LC_ 117
M 6,7,1
s_mul_i32 s53, %[d], 33792
M 7,18,0
WL 3
LO 116,18,116
LU 18,116
M 7,19,1
s_add_u32 s65, s53, %[sb1]
RA 232,1,23552
HZ 31,18
M 8,30,0
s_add_u32 s40, s40, 3072
LL 16,72
LL 120,73
M 8,31,1
s_addc_u32 s41, s41, 0
LA 16
M 9,42,0
s_add_u32 m0, s38, 3072
LC_ 120
DW 0
M 9,43,1
DW 1024
M 10,54,0
WL 3
LO 117,19,117
LU 19,117
M 10,55,1
RA 236,1,24576
HS 31,19
M 11,66,0
LL 17,74
LL 121,75
M 11,67,1
LA 17
DW 2048
M 6,8,2
LC_ 121
v_pk_add_f16 v31, v30, v31
M 6,9,3
s_lshl_b32 s53, s52, 8
v_add_u32 v1, s37, v0
M 7,20,2
WL 3
LO 120,16,120
LU 16,120
M 7,21,3
s_add_u32 s48, %[alo], s53
RA 216,1,0
HZ 32,16
M 8,32,2
s_addc_u32 s49, %[ahi], 0
LL 18,76
LL 124,77
M 8,33,3
s_add_u32 s52, s52, %[d]
LA 18
v_and_b32 v22, 0xfffffcff, v1
M 9,44,2
s_cmp_eq_u32 s52, %[wrap]
LC_ 124
M 9,45,3
s_cselect_b32 s52, %[rst], s52
DQ 3072
M 10,56,2
WL 3
LO 121,17,121
LU 17,121
M 10,57,3
s_add_u32 s50, s50, %[d]
RA 220,1,1024
HS 32,17
M 11,68,2
LL 19,78
LL 125,79
M 11,69,3
s_cmp_eq_u32 s50, %[wrap]
LA 19
RV 252,22,5120
M 6,10,4
s_cselect_b32 s50, %[rst], s50
LC_ 125
v_pk_add_f16 v32, v29, v32
M 6,11,5
WV 30
v_pk_add_f16 v32, v31, v32
M 7,22,4
WL 4
LO 124,18,124
LU 18,124
M 7,23,5
s_add_u32 s38, s38, 32768
RA 224,1,2048
HZ 33,18
M 8,34,4
s_cmp_eq_u32 s38, s77
LL 16,80
LL 106,81
M 8,35,5
s_cselect_b32 s38, s76, s38
LA 16
M 9,46,4
s_barrier
LC_ 106
M 9,47,5
s_sub_u32 s39, 0, s39
M 10,58,4
WL 3
LO 125,19,125
LU 19,125
M 10,59,5
RA 228,1,3072
HS 33,19
M 11,70,4
LL 17,82
LL 107,83
M 11,71,5
LA 17
v_pk_add_f16 v33, v26, v33
Z 0,0,0
LC_ 107
ds_read_b64 v[14:15], v5 offset:0
M 0,1,1
v_pk_add_f16 v26, v32, v33
Z 1,12,0
WL 4
LO 106,16,106
LU 16,106
M 1,13,1
RA 232,1,4096
HZ 27,16
Z 2,24,0
LL 18,84
LL 110,85
M 2,25,1
LA 18
HX 26
Z 3,36,0
LC_ 110
W1 26,2048
M 3,37,1
s_add_u32 s42, s94, s60
Z 4,48,0
WL 5
LO 107,17,107
LU 17,107
M 4,49,1
s_addc_u32 s43, s95, 0
RA 236,1,6144
HS 27,17
Z 5,60,0
LL 19,86
LL 111,87
M 5,61,1
LA 19
SU 104
M 0,2,2
WL 6
LC_ 111
v_add_f32 v14, v14, v15
M 0,3,3
s_nop 0
v_add_f32_dpp v14, v14, v14 quad_perm:[1,0,3,2] row_mask:0xf bank_mask:0xf
M 1,14,2
WL 4
LO 110,18,110
LU 18,110
M 1,15,3
RA 216,1,7168
HZ 29,18
M 2,26,2
LL 16,88
LL 114,89
M 2,27,3
LA 16
v_add_f32_dpp v14, v14, v14 quad_perm:[2,3,0,1] row_mask:0xf bank_mask:0xf
M 3,38,2
LC_ 114
v_add_f32_dpp v14, v14, v14 row_half_mirror row_mask:0xf bank_mask:0xf
M 3,39,3
s_add_u32 s42, s94, s61
v_cvt_f64_f32 v[14:15], v14
M 4,50,2
WL 3
LO 111,19,111
LU 19,111
M 4,51,3
s_addc_u32 s43, s95, 0
RA 220,1,8192
HS 29,19
M 5,62,2
LL 17,90
LL 115,91
M 5,63,3
LA 17
s_mov_b64 exec, s[54:55]
SP
s_mov_b64 exec, -1
M 0,4,4
LC_ 115
ds_read_b64 v[14:15], v5 offset:2048
M 0,5,5
SU 108
v_subrev_u32 v5, s39, v5
M 1,16,4
WL 4
LO 114,16,114
LU 16,114
M 1,17,5
PFN
RA 224,1,9216
HZ 30,16
M 2,28,4
s_mov_b32 m0, %[pm]
LL 18,92
LL 118,93
M 2,29,5
LA 18
DF
M 3,40,4
LC_ 118
M 3,41,5
s_add_u32 s42, s94, s62
M 4,52,4
WL 4
LO 115,17,115
LU 17,115
M 4,53,5
s_addc_u32 s43, s95, 0
RA 228,1,10240
HS 30,17
M 5,64,4
LL 19,94
LL 119,95
M 5,65,5
LA 19
SU 112
M 0,6,0
WL 5
LC_ 119
v_add_f32 v14, v14, v15
M 0,7,1
s_nop 0
v_add_f32_dpp v14, v14, v14 quad_perm:[1,0,3,2] row_mask:0xf bank_mask:0xf
M 1,18,0
WL 3
LO 118,18,118
LU 18,118
M 1,19,1
RA 232,1,11264
HZ 31,18
M 2,30,0
LL 16,96
LL 122,97
M 2,31,1
LA 16
RV 252,22,5392
M 3,42,0
LC_ 122
v_add_f32_dpp v14, v14, v14 quad_perm:[2,3,0,1] row_mask:0xf bank_mask:0xf
M 3,43,1
s_add_u32 s42, s94, s63
v_add_f32_dpp v14, v14, v14 row_half_mirror row_mask:0xf bank_mask:0xf
M 4,54,0
WL 4
LO 119,19,119
LU 19,119
M 4,55,1
s_addc_u32 s43, s95, 0
RA 236,1,12288
HS 31,19
M 5,66,0
LL 17,98
LL 123,99
M 5,67,1
LA 17
SU 116
v_cvt_f64_f32 v[14:15], v14
M 0,8,2
LC_ 123
s_mov_b64 exec, s[54:55]
SP
s_mov_b64 exec, -1
M 0,9,3
v_pk_add_f16 v31, v30, v31
M 1,20,2
WL 3
LO 122,16,122
LU 16,122
M 1,21,3
RA 216,1,13312
HZ 32,16
M 2,32,2
LL 18,100
LL 126,101
M 2,33,3
LA 18
M 3,44,2
LC_ 126
M 3,45,3
s_add_u32 s42, s94, s64
M 4,56,2
WL 3
LO 123,17,123
LU 17,123
M 4,57,3
s_addc_u32 s43, s95, 0
RA 220,1,14336
HS 32,17
M 5,68,2
LL 19,102
LL 127,103
M 5,69,3
LA 19
SU 120
M 0,10,4
LC_ 127
v_pk_add_f16 v32, v29, v32
M 0,11,5
v_pk_add_f16 v32, v31, v32
M 1,22,4
)"
R"(WL 3
LO 126,18,126
LU 18,126
M 1,23,5
RA 224,1,15360
HZ 33,18
M 2,34,4
LL 16,56
LL 104,57
M 2,35,5
LA 16
M 3,46,4
LC_ 104
M 3,47,5
s_add_u32 s42, s94, s65
M 4,58,4
WL 3
LO 127,19,127
LU 19,127
M 4,59,5
s_addc_u32 s43, s95, 0
RA 228,1,16384
HS 33,19
M 5,70,4
LL 17,58
LL 105,59
M 5,71,5
LA 17
SU 124
Z 6,0,0
LC_ 105
v_pk_add_f16 v33, v27, v33
M 6,1,1
v_pk_add_f16 v27, v32, v33
Z 7,12,0
WL 3
LO 104,16,104
LU 16,104
M 7,13,1
RA 232,1,17408
HZ 26,16
Z 8,24,0
LL 18,60
LL 108,61
M 8,25,1
s_mul_i32 s53, s50, 25600
LA 18
HX 27
Z 9,36,0
s_add_u32 s40, %[wlo], s53
LC_ 108
W1 27,3072
M 9,37,1
s_addc_u32 s41, %[whi], 0
v_add_u32 v4, s39, v4
Z 10,48,0
WL 4
LO 105,17,105
LU 17,105
M 10,49,1
s_mov_b32 m0, s38
RA 236,1,18432
HS 26,17
Z 11,60,0
LL 19,62
LL 109,63
M 11,61,1
LA 19
s_mov_b64 exec, 1
AT
s_mov_b64 exec, -1
M 6,2,2
LC_ 109
DW 0
M 6,3,3
M 7,14,2
WL 4
LO 108,18,108
LU 18,108
M 7,15,3
s_add_u32 s37, s37, 32768
RA 216,1,19456
HZ 29,18
M 8,26,2
s_cmp_eq_u32 s37, s75
LL 16,64
LL 112,65
M 8,27,3
s_cselect_b32 s37, s74, s37
LA 16
DW 1024
M 9,38,2
LC_ 112
M 9,39,3
M 10,50,2
WL 3
LO 109,19,109
LU 19,109
M 10,51,3
RA 220,1,20480
HS 29,19
M 11,62,2
LL 17,66
LL 113,67
M 11,63,3
LA 17
DW 2048
M 6,4,4
LC_ 113
M 6,5,5
M 7,16,4
WL 3
LO 112,16,112
LU 16,112
M 7,17,5
s_mul_i32 s53, s51, 49152
RA 224,1,21504
HZ 30,16
M 8,28,4
s_bitcmp1_b64 s[92:93], s51
LL 18,68
LL 116,69
M 8,29,5
s_cselect_b64 s[94:95], s[88:89], s[86:87]
LA 18
M 9,40,4
s_add_u32 s94, s94, s53
LC_ 116
M 9,41,5
s_addc_u32 s95, s95, 0
M 10,52,4
WL 3
LO 113,17,113
LU 17,113
M 10,53,5
s_add_u32 s51, s51, %[d]
RA 228,1,22528
HS 30,17
M 11,64,4
LL 19,70
LL 117,71
M 11,65,5
s_cmp_eq_u32 s51, %[wrap]
LA 19
M 6,6,0
s_cselect_b32 s51, %[rst], s51
LC_ 117
M 6,7,1
s_add_u32 s40, s40, 3072
M 7,18,0
WL 3
LO 116,18,116
LU 18,116
M 7,19,1
s_addc_u32 s41, s41, 0
RA 232,1,23552
HZ 31,18
M 8,30,0
s_add_u32 m0, s38, 3072
LL 16,72
LL 120,73
M 8,31,1
LA 16
DW 0
M 9,42,0
LC_ 120
DW 1024
M 9,43,1
M 10,54,0
WL 3
LO 117,19,117
LU 19,117
M 10,55,1
RA 236,1,24576
HS 31,19
M 11,66,0
LL 17,74
LL 121,75
M 11,67,1
LA 17
DW 2048
M 6,8,2
LC_ 121
v_pk_add_f16 v31, v30, v31
M 6,9,3
s_lshl_b32 s53, s52, 8
v_add_u32 v1, s37, v0
M 7,20,2
WL 3
LO 120,16,120
LU 16,120
M 7,21,3
s_add_u32 s48, %[alo], s53
RA 216,1,0
HZ 32,16
M 8,32,2
s_addc_u32 s49, %[ahi], 0
LL 18,76
LL 124,77
M 8,33,3
s_add_u32 s52, s52, %[d]
LA 18
v_and_b32 v22, 0xfffffcff, v1
M 9,44,2
s_cmp_eq_u32 s52, %[wrap]
LC_ 124
M 9,45,3
s_cselect_b32 s52, %[rst], s52
DQ 3072
M 10,56,2
WL 3
LO 121,17,121
LU 17,121
M 10,57,3
s_add_u32 s50, s50, %[d]
RA 220,1,1024
HS 32,17
M 11,68,2
LL 19,78
LL 125,79
M 11,69,3
s_cmp_eq_u32 s50, %[wrap]
LA 19
RV 252,22,5120
M 6,10,4
s_cselect_b32 s50, %[rst], s50
LC_ 125
v_pk_add_f16 v32, v29, v32
M 6,11,5
WV 32
v_pk_add_f16 v32, v31, v32
M 7,22,4
WL 4
LO 124,18,124
LU 18,124
M 7,23,5
s_add_u32 s38, s38, 32768
RA 224,1,2048
HZ 33,18
M 8,34,4
s_cmp_eq_u32 s38, s77
LL 16,80
LL 106,81
M 8,35,5
s_cselect_b32 s38, s76, s38
LA 16
M 9,46,4
s_barrier
LC_ 106
M 9,47,5
s_sub_u32 s39, 0, s39
M 10,58,4
WL 3
LO 125,19,125
LU 19,125
M 10,59,5
RA 228,1,3072
HS 33,19
M 11,70,4
LL 17,82
LL 107,83
M 11,71,5
LA 17
v_pk_add_f16 v33, v26, v33
Z 0,0,0
LC_ 107
ds_read_b64 v[14:15], v5 offset:2048
M 0,1,1
s_nop 0
v_subrev_u32 v5, s39, v5
v_pk_add_f16 v26, v32, v33
Z 1,12,0
WL 4
LO 106,16,106
LU 16,106
M 1,13,1
RA 232,1,4096
HZ 27,16
Z 2,24,0
LL 18,84
LL 110,85
M 2,25,1
LA 18
HX 26
Z 3,36,0
LC_ 110
W1 26,2048
M 3,37,1
s_add_u32 s42, s94, s60
Z 4,48,0
WL 5
LO 107,17,107
LU 17,107
M 4,49,1
s_addc_u32 s43, s95, 0
RA 236,1,6144
HS 27,17
Z 5,60,0
LL 19,86
LL 111,87
M 5,61,1
LA 19
SU 104
M 0,2,2
WL 6
LC_ 111
v_add_f32 v14, v14, v15
M 0,3,3
s_nop 0
v_add_f32_dpp v14, v14, v14 quad_perm:[1,0,3,2] row_mask:0xf bank_mask:0xf
M 1,14,2
WL 4
LO 110,18,110
LU 18,110
M 1,15,3
RA 216,1,7168
HZ 29,18
M 2,26,2
LL 16,88
LL 114,89
M 2,27,3
LA 16
v_add_f32_dpp v14, v14, v14 quad_perm:[2,3,0,1] row_mask:0xf bank_mask:0xf
M 3,38,2
LC_ 114
v_add_f32_dpp v14, v14, v14 row_half_mirror row_mask:0xf bank_mask:0xf
M 3,39,3
s_add_u32 s42, s94, s61
v_cvt_f64_f32 v[14:15], v14
M 4,50,2
WL 3
LO 111,19,111
LU 19,111
M 4,51,3
s_addc_u32 s43, s95, 0
RA 220,1,8192
HS 29,19
M 5,62,2
LL 17,90
LL 115,91
M 5,63,3
LA 17
s_mov_b64 exec, s[54:55]
SP
s_mov_b64 exec, -1
M 0,4,4
LC_ 115
SU 108
M 0,5,5
PFW
M 1,16,4
WL 3
LO 114,16,114
LU 16,114
M 1,17,5
s_mov_b32 m0, %[pm]
RA 224,1,9216
HZ 30,16
M 2,28,4
LL 18,92
LL 118,93
M 2,29,5
LA 18
DF
M 3,40,4
LC_ 118
M 3,41,5
s_add_u32 s42, s94, s62
M 4,52,4
WL 3
LO 115,17,115
LU 17,115
M 4,53,5
s_addc_u32 s43, s95, 0
RA 228,1,10240
HS 30,17
M 5,64,4
LL 19,94
LL 119,95
M 5,65,5
LA 19
SU 112
M 0,6,0
LC_ 119
M 0,7,1
M 1,18,0
WL 3
LO 118,18,118
LU 18,118
M 1,19,1
RA 232,1,11264
HZ 31,18
M 2,30,0
LL 16,96
LL 122,97
M 2,31,1
LA 16
RV 252,22,5392
M 3,42,0
LC_ 122
)"
R"(M 3,43,1
s_add_u32 s42, s94, s63
M 4,54,0
WL 4
LO 119,19,119
LU 19,119
M 4,55,1
s_addc_u32 s43, s95, 0
RA 236,1,12288
HS 31,19
M 5,66,0
LL 17,98
LL 123,99
M 5,67,1
LA 17
SU 116
M 0,8,2
LC_ 123
v_pk_add_f16 v31, v30, v31
M 0,9,3
M 1,20,2
WL 3
LO 122,16,122
LU 16,122
M 1,21,3
RA 216,1,13312
HZ 32,16
M 2,32,2
LL 18,100
LL 126,101
M 2,33,3
LA 18
M 3,44,2
LC_ 126
M 3,45,3
s_add_u32 s42, s94, s64
M 4,56,2
WL 3
LO 123,17,123
LU 17,123
M 4,57,3
s_addc_u32 s43, s95, 0
RA 220,1,14336
HS 32,17
M 5,68,2
LL 19,102
LL 127,103
M 5,69,3
LA 19
SU 120
M 0,10,4
LC_ 127
v_pk_add_f16 v32, v29, v32
M 0,11,5
v_pk_add_f16 v32, v31, v32
M 1,22,4
WL 3
LO 126,18,126
LU 18,126
M 1,23,5
RA 224,1,15360
HZ 33,18
M 2,34,4
LL 16,56
LL 104,57
M 2,35,5
LA 16
M 3,46,4
LC_ 104
M 3,47,5
s_add_u32 s42, s94, s65
M 4,58,4
WL 3
LO 127,19,127
LU 19,127
M 4,59,5
s_addc_u32 s43, s95, 0
RA 228,1,16384
HS 33,19
M 5,70,4
LL 17,58
LL 105,59
M 5,71,5
LA 17
SU 124
Z 6,0,0
LC_ 105
v_pk_add_f16 v33, v27, v33
M 6,1,1
v_pk_add_f16 v27, v32, v33
Z 7,12,0
WL 3
LO 104,16,104
LU 16,104
M 7,13,1
RA 232,1,17408
HZ 26,16
Z 8,24,0
WV 15
LL 18,60
LL 108,61
M 8,25,1
s_mul_i32 s53, s50, 25600
LA 18
HX 27
Z 9,36,0
s_add_u32 s40, %[wlo], s53
LC_ 108
W1 27,3072
M 9,37,1
s_addc_u32 s41, %[whi], 0
v_readfirstlane_b32 s72, v11
Z 10,48,0
WL 4
LO 105,17,105
LU 17,105
M 10,49,1
s_mov_b32 m0, s38
RA 236,1,18432
HS 26,17
Z 11,60,0
LL 19,62
LL 109,63
M 11,61,1
LA 19
DW 0
M 6,2,2
LC_ 109
v_mov_b32 v12, s72
M 6,3,3
W2 12,12,16
M 7,14,2
WL 5
LO 108,18,108
LU 18,108
M 7,15,3
s_add_u32 s37, s37, 32768
RA 216,1,19456
HZ 29,18
M 8,26,2
s_cmp_eq_u32 s37, s75
LL 16,64
LL 112,65
M 8,27,3
s_cselect_b32 s37, s74, s37
LA 16
DW 1024
M 9,38,2
LC_ 112
v_add_u32 v4, s39, v4
M 9,39,3
s_mov_b64 exec, 1
AT
s_mov_b64 exec, -1
M 10,50,2
WL 4
LO 109,19,109
LU 19,109
M 10,51,3
RA 220,1,20480
HS 29,19
M 11,62,2
LL 17,66
LL 113,67
M 11,63,3
LA 17
DW 2048
M 6,4,4
LC_ 113
M 6,5,5
M 7,16,4
WL 3
LO 112,16,112
LU 16,112
M 7,17,5
s_mul_i32 s53, s51, 49152
RA 224,1,21504
HZ 30,16
M 8,28,4
s_bitcmp1_b64 s[92:93], s51
LL 18,68
LL 116,69
M 8,29,5
s_cselect_b64 s[94:95], s[88:89], s[86:87]
LA 18
M 9,40,4
s_add_u32 s94, s94, s53
LC_ 116
M 9,41,5
s_addc_u32 s95, s95, 0
M 10,52,4
WL 3
LO 113,17,113
LU 17,113
M 10,53,5
s_add_u32 s51, s51, %[d]
RA 228,1,22528
HS 30,17
M 11,64,4
LL 19,70
LL 117,71
M 11,65,5
s_cmp_eq_u32 s51, %[wrap]
LA 19
M 6,6,0
s_cselect_b32 s51, %[rst], s51
LC_ 117
M 6,7,1
s_add_u32 s40, s40, 3072
M 7,18,0
WL 3
LO 116,18,116
LU 18,116
M 7,19,1
s_addc_u32 s41, s41, 0
RA 232,1,23552
HZ 31,18
M 8,30,0
s_add_u32 m0, s38, 3072
LL 16,72
LL 120,73
M 8,31,1
LA 16
DW 0
M 9,42,0
LC_ 120
DW 1024
M 9,43,1
M 10,54,0
WL 3
LO 117,19,117
LU 19,117
M 10,55,1
RA 236,1,24576
HS 31,19
M 11,66,0
LL 17,74
LL 121,75
M 11,67,1
LA 17
DW 2048
M 6,8,2
LC_ 121
v_pk_add_f16 v31, v30, v31
M 6,9,3
s_lshl_b32 s53, s52, 8
v_add_u32 v1, s37, v0
M 7,20,2
WL 3
LO 120,16,120
LU 16,120
M 7,21,3
s_add_u32 s48, %[alo], s53
RA 216,1,0
HZ 32,16
M 8,32,2
s_addc_u32 s49, %[ahi], 0
LL 18,76
LL 124,77
M 8,33,3
s_add_u32 s52, s52, %[d]
LA 18
v_and_b32 v22, 0xfffffcff, v1
M 9,44,2
s_cmp_eq_u32 s52, %[wrap]
LC_ 124
M 9,45,3
s_cselect_b32 s52, %[rst], s52
DQ 3072
M 10,56,2
WL 3
LO 121,17,121
LU 17,121
M 10,57,3
s_add_u32 s50, s50, %[d]
RA 220,1,1024
HS 32,17
M 11,68,2
LL 19,78
LL 125,79
M 11,69,3
s_cmp_eq_u32 s50, %[wrap]
LA 19
RV 252,22,5120
M 6,10,4
s_cselect_b32 s50, %[rst], s50
LC_ 125
v_pk_add_f16 v32, v29, v32
M 6,11,5
v_pk_add_f16 v32, v31, v32
M 7,22,4
WL 4
LO 124,18,124
LU 18,124
M 7,23,5
s_add_u32 s38, s38, 32768
RA 224,1,2048
HZ 33,18
M 8,34,4
s_cmp_eq_u32 s38, s77
LL 16,80
LL 106,81
M 8,35,5
s_cselect_b32 s38, s76, s38
LA 16
M 9,46,4
s_barrier
LC_ 106
M 9,47,5
s_sub_u32 s39, 0, s39
M 10,58,4
WL 3
LO 125,19,125
LU 19,125
M 10,59,5
RA 228,1,3072
HS 33,19
M 11,70,4
LL 17,82
LL 107,83
M 11,71,5
2:
LA 17
v_pk_add_f16 v33, v26, v33
Z 0,0,0
LC_ 107
ds_read_b32 v20, v5 offset:4096
M 0,1,1
ds_read_b64 v[14:15], v5 offset:2048
v_pk_add_f16 v26, v32, v33
Z 1,12,0
WL 5
LO 106,16,106
LU 16,106
M 1,13,1
RA 232,1,4096
HZ 27,16
Z 2,24,0
LL 18,84
LL 110,85
M 2,25,1
LA 18
v_subrev_u32 v5, s39, v5
Z 3,36,0
LC_ 110
HX 26
M 3,37,1
s_add_u32 s42, s94, s60
W1 26,2048
Z 4,48,0
WL 6
LO 107,17,107
LU 17,107
M 4,49,1
s_addc_u32 s43, s95, 0
RA 236,1,6144
HS 27,17
Z 5,60,0
LL 19,86
LL 111,87
M 5,61,1
LA 19
SU 104
M 0,2,2
WL 6
LC_ 111
v_add_f32 v14, v14, v15
M 0,3,3
s_nop 0
v_add_f32_dpp v14, v14, v14 quad_perm:[1,0,3,2] row_mask:0xf bank_mask:0xf
M 1,14,2
WL 4
LO 110,18,110
LU 18,110
M 1,15,3
RA 216,1,7168
HZ 29,18
M 2,26,2
LL 16,88
LL 114,89
M 2,27,3
LA 16
v_add_f32_dpp v14, v14, v14 quad_perm:[2,3,0,1] row_mask:0xf bank_mask:0xf
M 3,38,2
LC_ 114
v_add_f32_dpp v14, v14, v14 row_half_mirror row_mask:0xf bank_mask:0xf
M 3,39,3
s_add_u32 s42, s94, s61
v_cvt_f64_f32 v[14:15], v14
M 4,50,2
WL 3
LO 111,19,111
LU 19,111
M 4,51,3
s_addc_u32 s43, s95, 0
RA 220,1,8192
HS 29,19
M 5,62,2
LL 17,90
LL 115,91
)"
R"(M 5,63,3
LA 17
s_mov_b64 exec, s[54:55]
SP
s_mov_b64 exec, -1
M 0,4,4
LC_ 115
SU 108
M 0,5,5
v_readfirstlane_b32 s59, v20
M 1,16,4
WL 3
LO 114,16,114
LU 16,114
M 1,17,5
RA 224,1,9216
HZ 30,16
M 2,28,4
LL 18,92
LL 118,93
M 2,29,5
LA 18
M 3,40,4
LC_ 118
M 3,41,5
s_add_u32 s42, s94, s62
M 4,52,4
WL 3
LO 115,17,115
LU 17,115
M 4,53,5
s_addc_u32 s43, s95, 0
RA 228,1,10240
HS 30,17
M 5,64,4
LL 19,94
LL 119,95
M 5,65,5
LA 19
SU 112
M 0,6,0
LC_ 119
M 0,7,1
M 1,18,0
WL 3
LO 118,18,118
LU 18,118
M 1,19,1
RA 232,1,11264
HZ 31,18
M 2,30,0
LL 16,96
LL 122,97
M 2,31,1
LA 16
RV 252,22,5392
M 3,42,0
LC_ 122
M 3,43,1
s_add_u32 s42, s94, s63
M 4,54,0
WL 4
LO 119,19,119
LU 19,119
M 4,55,1
s_addc_u32 s43, s95, 0
RA 236,1,12288
HS 31,19
M 5,66,0
LL 17,98
LL 123,99
M 5,67,1
LA 17
SU 116
M 0,8,2
LC_ 123
v_pk_add_f16 v31, v30, v31
M 0,9,3
M 1,20,2
WL 3
LO 122,16,122
LU 16,122
M 1,21,3
RA 216,1,13312
HZ 32,16
M 2,32,2
LL 18,100
LL 126,101
M 2,33,3
LA 18
M 3,44,2
LC_ 126
M 3,45,3
s_add_u32 s42, s94, s64
M 4,56,2
WL 3
LO 123,17,123
LU 17,123
M 4,57,3
s_addc_u32 s43, s95, 0
RA 220,1,14336
HS 32,17
M 5,68,2
LL 19,102
LL 127,103
M 5,69,3
LA 19
SU 120
M 0,10,4
LC_ 127
v_pk_add_f16 v32, v29, v32
M 0,11,5
v_pk_add_f16 v32, v31, v32
M 1,22,4
WL 3
LO 126,18,126
LU 18,126
M 1,23,5
RA 224,1,15360
HZ 33,18
M 2,34,4
LL 16,56
LL 104,57
M 2,35,5
LA 16
M 3,46,4
LC_ 104
M 3,47,5
s_add_u32 s42, s94, s65
M 4,58,4
WL 3
LO 127,19,127
LU 19,127
M 4,59,5
s_addc_u32 s43, s95, 0
RA 228,1,16384
HS 33,19
M 5,70,4
LL 17,58
LL 105,59
M 5,71,5
LA 17
SU 124
Z 6,0,0
LC_ 105
v_pk_add_f16 v33, v27, v33
M 6,1,1
v_pk_add_f16 v27, v32, v33
Z 7,12,0
WL 3
LO 104,16,104
LU 16,104
M 7,13,1
RA 232,1,17408
HZ 26,16
Z 8,24,0
WV 12
LL 18,60
LL 108,61
M 8,25,1
s_mul_i32 s53, s50, 25600
LA 18
HX 27
Z 9,36,0
s_add_u32 s40, %[wlo], s53
LC_ 108
W1 27,3072
M 9,37,1
s_addc_u32 s41, %[whi], 0
v_readfirstlane_b32 s72, v11
Z 10,48,0
WL 4
LO 105,17,105
LU 17,105
M 10,49,1
s_mov_b32 m0, s38
RA 236,1,18432
HS 26,17
Z 11,60,0
LL 19,62
LL 109,63
M 11,61,1
LA 19
DW 0
M 6,2,2
LC_ 109
v_mov_b32 v12, s72
M 6,3,3
W2 12,12,16
M 7,14,2
WL 5
LO 108,18,108
LU 18,108
M 7,15,3
s_add_u32 s37, s37, 32768
RA 216,1,19456
HZ 29,18
M 8,26,2
s_cmp_eq_u32 s37, s75
LL 16,64
LL 112,65
M 8,27,3
s_cselect_b32 s37, s74, s37
LA 16
DW 1024
M 9,38,2
LC_ 112
v_add_u32 v4, s39, v4
M 9,39,3
s_max_u32 s59, s59, %[nb]
s_mov_b64 exec, 1
AT
s_mov_b64 exec, -1
M 10,50,2
WL 4
LO 109,19,109
LU 19,109
M 10,51,3
s_add_u32 %[nb], %[nb], 1
RA 220,1,20480
HS 29,19
M 11,62,2
LL 17,66
LL 113,67
M 11,63,3
LA 17
DW 2048
M 6,4,4
LC_ 113
M 6,5,5
M 7,16,4
WL 3
LO 112,16,112
LU 16,112
M 7,17,5
s_mul_i32 s53, s51, 49152
RA 224,1,21504
HZ 30,16
M 8,28,4
s_bitcmp1_b64 s[92:93], s51
LL 18,68
LL 116,69
M 8,29,5
s_cselect_b64 s[94:95], s[88:89], s[86:87]
LA 18
M 9,40,4
s_add_u32 s94, s94, s53
LC_ 116
M 9,41,5
s_addc_u32 s95, s95, 0
M 10,52,4
WL 3
LO 113,17,113
LU 17,113
M 10,53,5
s_add_u32 s51, s51, %[d]
RA 228,1,22528
HS 30,17
M 11,64,4
LL 19,70
LL 117,71
M 11,65,5
s_cmp_eq_u32 s51, %[wrap]
LA 19
M 6,6,0
s_cselect_b32 s51, %[rst], s51
LC_ 117
M 6,7,1
s_add_u32 s40, s40, 3072
M 7,18,0
WL 3
LO 116,18,116
LU 18,116
M 7,19,1
s_addc_u32 s41, s41, 0
RA 232,1,23552
HZ 31,18
M 8,30,0
s_add_u32 m0, s38, 3072
LL 16,72
LL 120,73
M 8,31,1
LA 16
DW 0
M 9,42,0
LC_ 120
DW 1024
M 9,43,1
M 10,54,0
WL 3
LO 117,19,117
LU 19,117
M 10,55,1
RA 236,1,24576
HS 31,19
M 11,66,0
LL 17,74
LL 121,75
M 11,67,1
LA 17
DW 2048
M 6,8,2
LC_ 121
v_pk_add_f16 v31, v30, v31
M 6,9,3
s_lshl_b32 s53, s52, 8
v_add_u32 v1, s37, v0
M 7,20,2
WL 3
LO 120,16,120
LU 16,120
M 7,21,3
s_add_u32 s48, %[alo], s53
RA 216,1,0
HZ 32,16
M 8,32,2
s_addc_u32 s49, %[ahi], 0
LL 18,76
LL 124,77
M 8,33,3
s_add_u32 s52, s52, %[d]
LA 18
v_and_b32 v22, 0xfffffcff, v1
M 9,44,2
s_cmp_eq_u32 s52, %[wrap]
LC_ 124
M 9,45,3
s_cselect_b32 s52, %[rst], s52
DQ 3072
M 10,56,2
WL 3
LO 121,17,121
LU 17,121
M 10,57,3
s_add_u32 s50, s50, %[d]
RA 220,1,1024
HS 32,17
M 11,68,2
LL 19,78
LL 125,79
M 11,69,3
s_cmp_eq_u32 s50, %[wrap]
LA 19
RV 252,22,5120
M 6,10,4
s_cselect_b32 s50, %[rst], s50
LC_ 125
v_pk_add_f16 v32, v29, v32
M 6,11,5
v_pk_add_f16 v32, v31, v32
M 7,22,4
WL 4
LO 124,18,124
LU 18,124
M 7,23,5
s_add_u32 s38, s38, 32768
RA 224,1,2048
HZ 33,18
M 8,34,4
s_cmp_eq_u32 s38, s77
LL 16,80
LL 106,81
M 8,35,5
s_cselect_b32 s38, s76, s38
LA 16
M 9,46,4
s_barrier
LC_ 106
M 9,47,5
s_sub_u32 s39, 0, s39
M 10,58,4
WL 3
LO 125,19,125
LU 19,125
M 10,59,5
RA 228,1,3072
HS 33,19
M 11,70,4
s_cmp_lt_u32 s59, 30
LL 17,82
LL 107,83
M 11,71,5
s_cbranch_scc1 2b
LA 17
v_pk_add_f16 v33, v26, v33
LC_ 107
ds_read_b64 v[14:15], v5 offset:2048
v_pk_add_f16 v26, v32, v33
HX 26
v_subrev_u32 v5, s39, v5
W1 26,2048
WL 0
LO 106,16,106
LU 16,106
HZ 27,16
LL 18,84
LL 110,85
LA 18
v_add_f32 v14, v14, v15
s_nop 1
)"
R"(v_add_f32_dpp v14, v14, v14 quad_perm:[1,0,3,2] row_mask:0xf bank_mask:0xf
s_nop 1
v_add_f32_dpp v14, v14, v14 quad_perm:[2,3,0,1] row_mask:0xf bank_mask:0xf
s_nop 1
v_add_f32_dpp v14, v14, v14 row_half_mirror row_mask:0xf bank_mask:0xf
v_cvt_f64_f32 v[14:15], v14
s_mov_b64 exec, s[54:55]
SP
s_mov_b64 exec, -1
LC_ 110
s_add_u32 s42, s94, s60
s_addc_u32 s43, s95, 0
LO 107,17,107
LU 17,107
HS 27,17
SU 104
LL 19,86
LL 111,87
LA 19
LC_ 111
WL 0
LO 110,18,110
LU 18,110
HZ 29,18
LL 16,88
LL 114,89
LA 16
LC_ 114
s_add_u32 s42, s94, s61
s_addc_u32 s43, s95, 0
LO 111,19,111
LU 19,111
HS 29,19
SU 108
LL 17,90
LL 115,91
LA 17
LC_ 115
WL 0
LO 114,16,114
LU 16,114
HZ 30,16
LL 18,92
LL 118,93
LA 18
LC_ 118
s_add_u32 s42, s94, s62
s_addc_u32 s43, s95, 0
LO 115,17,115
LU 17,115
HS 30,17
SU 112
LL 19,94
LL 119,95
LA 19
LC_ 119
WL 0
LO 118,18,118
LU 18,118
HZ 31,18
LL 16,96
LL 122,97
LA 16
LC_ 122
s_add_u32 s42, s94, s63
s_addc_u32 s43, s95, 0
LO 119,19,119
LU 19,119
HS 31,19
SU 116
LL 17,98
LL 123,99
v_pk_add_f16 v31, v30, v31
LA 17
LC_ 123
WL 0
LO 122,16,122
LU 16,122
HZ 32,16
LL 18,100
LL 126,101
LA 18
LC_ 126
s_add_u32 s42, s94, s64
s_addc_u32 s43, s95, 0
LO 123,17,123
LU 17,123
HS 32,17
SU 120
LL 19,102
LL 127,103
v_pk_add_f16 v32, v29, v32
LA 19
v_pk_add_f16 v32, v31, v32
LC_ 127
WL 0
LO 126,18,126
LU 18,126
HZ 33,18
s_add_u32 s42, s94, s65
s_addc_u32 s43, s95, 0
LO 127,19,127
LU 19,127
HS 33,19
SU 124
v_pk_add_f16 v33, v27, v33
v_pk_add_f16 v27, v32, v33
HX 27
W1 27,3072
s_nop 1
v_add_u32 v4, s39, v4
s_lshl_b32 s53, s52, 8
s_add_u32 s48, %[alo], s53
s_addc_u32 s49, %[ahi], 0
s_add_u32 s52, s52, %[d]
s_cmp_eq_u32 s52, %[wrap]
s_cselect_b32 s52, %[rst], s52
WL 0
WV 0
s_barrier
s_sub_u32 s39, 0, s39
ds_read_b64 v[14:15], v5 offset:2048
s_nop 1
v_subrev_u32 v5, s39, v5
WL 0
v_add_f32 v14, v14, v15
s_nop 1
v_add_f32_dpp v14, v14, v14 quad_perm:[1,0,3,2] row_mask:0xf bank_mask:0xf
s_nop 1
v_add_f32_dpp v14, v14, v14 quad_perm:[2,3,0,1] row_mask:0xf bank_mask:0xf
s_nop 1
v_add_f32_dpp v14, v14, v14 row_half_mirror row_mask:0xf bank_mask:0xf
v_cvt_f64_f32 v[14:15], v14
s_mov_b64 exec, s[54:55]
SP
s_mov_b64 exec, -1
GE
s_waitcnt vmcnt(0) lgkmcnt(0)
8:
.purgem M
.purgem Z
.purgem LZ
.purgem LS
.purgem LD
.purgem GZ
.purgem GS
.purgem CL
.purgem CH
.purgem XL
.purgem XN
.purgem F
.purgem U
.purgem E
.purgem A
.purgem R
.purgem C
.purgem S0
.purgem S
.purgem HC
.purgem HF
.purgem HM
.purgem HE
.purgem HG
.purgem HA
.purgem HR
.purgem HQ
.purgem HP
.purgem HY
.purgem HK
.purgem LQ
.purgem LL
.purgem LH
.purgem LA
.purgem LC_
.purgem LO
.purgem HZ
.purgem HS
.purgem HT
.purgem HX
.purgem UX
.purgem EX
.purgem AX
.purgem RX
.purgem TX
.purgem SX
.purgem AW
.purgem RA
.purgem RV
.purgem W2
.purgem W1
.purgem DW
.purgem DF
.purgem DX
.purgem ST
.purgem SU
.purgem SP
.purgem AT
.purgem PFA
.purgem PFN
.purgem PFW
.purgem PF0
.purgem WL
.purgem WV
.purgem LU
.purgem CF
.purgem XS
.purgem AR
.purgem DQ
.purgem GE
.purgem BW
.purgem BR
.purgem LK
)"
      : [nb] "=&s"(nb)
      : [xlo] "s"((unsigned)xp), [xhi] "s"((unsigned)(xp >> 32)), [hlo] "s"((unsigned)hp), [hhi] "s"((unsigned)(hp >> 32)),
        [glo] "s"((unsigned)gp), [ghi] "s"((unsigned)(gp >> 32)), [wlo] "s"((unsigned)wp), [whi] "s"((unsigned)(wp >> 32)),
        [w] "s"(w), [lds] "s"(lds0), [d] "s"(d), [wrap] "s"(wrap), [rst] "s"(rst), [pos0] "s"(pos0), [xo0] "s"(xo0), [xs1] "s"(xs1), [sb1] "s"(sb1), [r0o] "s"(r0o), [tsel] "s"(tsel), [s2] "s"(s2),
        [alo] "s"((unsigned)ap), [ahi] "s"((unsigned)(ap >> 32)), [ca0] "s"(ca0), [pm] "s"(pm), [pf] "s"(pf)
      : K1_CLOB);
  TS(6);
  MK_END(1);
}

typedef __bf16 bf16x8_t __attribute__((ext_vector_type(8)));
DI f32x4 mfma16(u32x4 a, u32x4 b, f32x4 c) {
  return __builtin_amdgcn_mfma_f32_16x16x32_bf16(__builtin_bit_cast(bf16x8_t, a), __builtin_bit_cast(bf16x8_t, b), c, 0, 0, 0);
}
DI float grn_gx(double s, float scale) { return round_bf16(__builtin_amdgcn_sqrtf(round_bf16(round_bf16((float)s * scale) + 1e-6f))); }
DI void slow_fc1(const u16* __restrict__ x, const u16* __restrict__ dw, char* __restrict__ hws, float* sacc, int w) {
  const int lane = __builtin_amdgcn_mbcnt_hi(~0u, __builtin_amdgcn_mbcnt_lo(~0u, 0u)), m = lane & 15, g = lane >> 4;
  for (int i = lane; i < HID; i += 64) sacc[i] = 0.f;
#pragma unroll 1
  for (int it = 0; it < 9; ++it) {
    const int sub = blockIdx.x * 36 + it * 4 + w;
    const unsigned px = sub_pixel(x, sub, m);
    u32x4 A[12];
    float sum = 0.f;
    const u16* dq = dw;
    asm volatile("" : "+s"(dq));
#pragma unroll
    for (int ks = 0; ks < 12; ++ks) {
      const u32x4 v = *(const u32x4*)(x + (size_t)px * CH + 32 * ks + 8 * g);
      const u32x4 d = *(const u32x4*)(dq + 32 * ks + 8 * g);
#pragma unroll
      for (int j = 0; j < 4; ++j) {
        A[ks][j] = pack_bf16(bf_lo(v[j]) * bf_lo(d[j]), bf_hi(v[j]) * bf_hi(d[j]));
        sum += bf_lo(A[ks][j]) + bf_hi(A[ks][j]);
      }
      asm volatile("" : "+v"(A[ks]));
    }
    sum += __shfl_xor(sum, 16); sum += __shfl_xor(sum, 32);
    const float mean = sum * (1.0f / CH);
    float var = 0.f;
#pragma unroll
    for (int ks = 0; ks < 12; ++ks) {
#pragma unroll
      for (int j = 0; j < 4; ++j) {
        const float a = bf_lo(A[ks][j]) - mean, b = bf_hi(A[ks][j]) - mean;
        var += a * a + b * b;
      }
      asm volatile("" : "+v"(A[ks]));
    }
    var += __shfl_xor(var, 16); var += __shfl_xor(var, 32);
    const float mb = round_bf16(mean);
    const float rd = 1.0f / round_bf16(sqrtf(round_bf16(round_bf16(var * (1.0f / CH)) + 1e-5f)));
#pragma unroll
    for (int ks = 0; ks < 12; ++ks)
#pragma unroll
      for (int j = 0; j < 4; ++j) A[ks][j] = pack_bf16((bf_lo(A[ks][j]) - mb) * rd, (bf_hi(A[ks][j]) - mb) * rd);
#pragma unroll 1
    for (int tc = 0; tc < 96; ++tc) {
      f32x4 acc = {0.f, 0.f, 0.f, 0.f};
#pragma unroll
      for (int ks = 0; ks < 12; ++ks) acc = mfma16(A[ks], *(const u32x4*)(g_buf + (size_t)(tc * 12 + ks) * 1024 + lane * 16), acc);
      float q = 0.f;
#pragma unroll
      for (int r = 0; r < 4; ++r) { acc[r] = gelu_tanh(acc[r]); q = __builtin_fmaf(acc[r], acc[r], q); }
      typedef unsigned u32x2_ __attribute__((ext_vector_type(2)));
      *(u32x2_*)(hws + h_piece(hws, sub, tc >> 1) + g * 256 + ((m + 4 * g) & 15) * 16 + (tc & 1) * 8) = (u32x2_){pack_bf16(acc[0], acc[1]), pack_bf16(acc[2], acc[3])};
      q += __shfl_xor(q, 16); q += __shfl_xor(q, 32);
      if (g == 0) sacc[tc * 16 + m] += q;
    }
  }
  __builtin_amdgcn_wave_barrier();
  __builtin_amdgcn_fence(__ATOMIC_ACQ_REL, "workgroup");
  for (int i = lane; i < HID; i += 64) atomicAdd((double*)(g_buf + G_ACC2) + i, (double)sacc[i]);
}
DI void slow_bar(unsigned* c, int w) {
  const int lane = __builtin_amdgcn_mbcnt_hi(~0u, __builtin_amdgcn_mbcnt_lo(~0u, 0u));
  asm volatile("s_waitcnt vmcnt(0)" ::: "memory");
  __syncthreads();
  if (w == 0 && lane == 0) {
    __threadfence();
    asm volatile("s_waitcnt vmcnt(0)" ::: "memory");
    __hip_atomic_fetch_add(c, 1u, __ATOMIC_RELAXED, __HIP_MEMORY_SCOPE_AGENT);
    for (int i = 0; i < (1 << 22) && __hip_atomic_load(c, __ATOMIC_RELAXED, __HIP_MEMORY_SCOPE_AGENT) < (unsigned)NWG; ++i) __builtin_amdgcn_s_sleep(32);
    __threadfence();
    asm volatile("s_waitcnt vmcnt(0)" ::: "memory");
  }
  __syncthreads();
}

#define K2_CLOB "memory", "vcc", "scc", "s48", "s49", "s50", "s51", "s52", "s53", "s54", "s55", "s56", "s57", "s58", "s59", "s60", "s61", "s62", "s63", "s64", "s65", "s66", "s67", \
  "s68", "s69", "s70", "s71", "s72", "s73", "s74", "s75", "s76", "s77", "s78", "s79", "s80", "s81", "s84", "s85", "s86", "s87", "s88", "s89", "s90", "s91", "s92", "s93", "s94", "s95", "s96", "s97", "s98", "s99", "s38", "s39", "s40", "s41", "s42", "s43", "s44", "s45", "s46", "s47", VA_CLOB
DI void slow_fc2(const u16* __restrict__ x, const char* __restrict__ hws, u16* __restrict__ out, const u16* __restrict__ grn_g, const u16* __restrict__ w2,
                 char* smem, bool hbf, int w) {
  float* sc = (float*)smem;
  float* red = sc + HID;
  u16* wf = (u16*)(smem + 8192);
  const int lane = __builtin_amdgcn_mbcnt_hi(~0u, __builtin_amdgcn_mbcnt_lo(~0u, 0u)), tid = w * 64 + lane, m = lane & 15, g = lane >> 4;
  __syncthreads();
  float part = 0.f;
  for (int c = tid; c < HID; c += 256) {
    double s = 0.0;
    if (hbf) s = ((const double*)(g_buf + G_ACC2))[c];
    else for (int r = 0; r < (int)K1_NREP; ++r) s += ((const double*)(g_buf + K1_G_ACC + r * 12288))[c];
    const float gx = grn_gx(s, hbf ? 1.0f : g_sc[2]);
    sc[c] = gx; part += gx;
  }
#pragma unroll
  for (int o = 32; o > 0; o >>= 1) part += __shfl_xor(part, o);
  if (lane == 0) red[tid >> 6] = part;
  __syncthreads();
  const float gmean = round_bf16(round_bf16(((red[0] + red[1]) + (red[2] + red[3])) * (1.0f / HID)) + 1e-6f);
  for (int c = tid; c < HID; c += 256) sc[c] = (1.0f + ld_bf16(grn_g + c) * round_bf16(sc[c] / gmean)) ;
  const float hs = g_sc[3];
#pragma unroll 1
  for (int it = 0; it < 9; ++it) {
    const int sub = blockIdx.x * 36 + it * 4 + (tid >> 6);
    f32x4 acc[24];
#pragma unroll
    for (int i = 0; i < 24; ++i) acc[i] = (f32x4){0.f, 0.f, 0.f, 0.f};
#pragma unroll 1
    for (int t = 0; t < 48; ++t) {
      __syncthreads();
      for (int e = tid; e < 32 * CH; e += 256) {
        const int k = e / CH, n = e % CH;
        wf[((n >> 4) * 64 + (k >> 3) * 16 + (n & 15)) * 8 + (k & 7)] = (u16)pack_bf16(sc[32 * t + k] * ld_bf16(w2 + (size_t)(32 * t + k) * CH + n), 0.f);
      }
      __syncthreads();
      const char* hp = hws + h_piece(hws, sub, t) + (m >> 2) * 256 + (g >> 1) * 8 + 2 * (m & 3);
      u32x4 A;
#pragma unroll
      for (int j = 0; j < 4; ++j) {
        unsigned v[2];
#pragma unroll
        for (int k = 0; k < 2; ++k) {
          v[k] = *(const u16*)(hp + ((8 * (g & 1) + 2 * j + k + (m & 12)) & 15) * 16);
          if (!hbf) v[k] = pack_bf16(hs * (float)__builtin_bit_cast(_Float16, (u16)v[k]), 0.f) & 0xffffu;
        }
        A[j] = v[0] | (v[1] << 16);
      }
#pragma unroll
      for (int i = 0; i < 24; ++i) acc[i] = mfma16(A, *(const u32x4*)(wf + (i * 64 + lane) * 8), acc[i]);
    }
#pragma unroll
    for (int i = 0; i < 24; ++i) {
      const int n = 16 * i + m, mm = n % 192;
      const f32x4 b4 = *(const f32x4*)(g_bias + (((n / 192) * 12 + 4 * (mm >> 6) + (mm & 3)) * 16 + ((mm & 63) >> 2)) * 4);
      const float bias = (b4[0] + b4[1]) + (b4[2] + b4[3]);
#pragma unroll
      for (int r = 0; r < 4; ++r) {
        const size_t o = (size_t)(sub_pixel(x, sub, 4 * g) + r) * CH + n;
        out[o] = (u16)pack_bf16(acc[i][r] + bias + ld_bf16(x + o), 0.f);
      }
    }
  }
}
__global__ __launch_bounds__(256, 1) void k2_fc2(const u16* __restrict__ x, char* __restrict__ hws, u16* __restrict__ out,
                                                 const u16* __restrict__ grn_g, const u16* __restrict__ w2, const u16* __restrict__ dw,
                                                 const u16* __restrict__ w1) {
  extern __shared__ __attribute__((aligned(16))) char smem[];
  const int w = __builtin_amdgcn_readfirstlane(threadIdx.x >> 6);
  const unsigned pg = w & 1, nh = w >> 1;
  const unsigned lds0 = lds_addr(smem);
  unsigned cl = blockIdx.x & 7;
  asm volatile("" : "+s"(cl));
  MK_START(4); TS(9);
  const uint64_t pp = uni64((uint64_t)g_prm + (blockIdx.x * 4 + w) * 256), ap = (uint64_t)g_buf + K1_G_ACC + w * 1024;
  const unsigned wm = lds0 + w * 6144, ab = lds0 + nh * 12288;
  const unsigned hb = lds0 + K2_HB + pg * 2 * K2_NU * 1024, hm = hb + nh * K2_NU * 1024;
  const unsigned bm = lds0 + K2_BIAS_OFF + w * 1536, bb = lds0 + K2_BIAS_OFF + nh * 3072;
  const unsigned pm = lds0 + K2_PF_OFF, sb = lds0 + K2_S16_OFF, sw = sb + w * 256;
  const uint64_t tb = uni64((uint64_t)g_buf + G_STMP), ta = uni64(tb + cl * 128);
  const unsigned em = __builtin_amdgcn_readfirstlane(w == 0);
  uint64_t flg;
#define K2_OPS [pp] "s"(pp), [plo] "s"((unsigned)pp), [phi] "s"((unsigned)(pp >> 32)), [cl] "s"(cl), [tb] "s"(tb), [ta] "s"(ta), [em] "s"(em), [alo] "s"((unsigned)ap), [ahi] "s"((unsigned)(ap >> 32)), [wm] "s"(wm), [hm] "s"(hm), [bm] "s"(bm), [ab] "s"(ab), [hb] "s"(hb), [bb] "s"(bb), \
         [sb] "s"(sb), [sw] "s"(sw), [pm] "s"(pm)
  asm volatile(
R"(.macro MA t,a,b
v_mfma_f32_16x16x32_f16 a[\t:\t+3], a[\b:\b+3], a[\a:\a+3], a[\t:\t+3]
.endm
.macro MV t,a,b
v_mfma_f32_16x16x32_f16 v[\t:\t+3], a[\b:\b+3], a[\a:\a+3], v[\t:\t+3]
.endm
.macro AN d
v_and_b32 v[\d], s39, v[\d]
.endm
.macro AW a,s
v_accvgpr_write_b32 a[\a], v[\s]
.endm
.macro AR d,a
v_accvgpr_read_b32 v[\d], a[\a]
.endm
.macro UL d,x
v_lshlrev_b32 v[\d], 16, v[\x]
.endm
.macro UH d,x
v_and_b32 v[\d], s38, v[\x]
.endm
.macro AD d,b
v_fmac_f32 v[\d], s53, v[\b]
.endm
.macro TR d,o
ds_read_b64_tr_b16 a[\d:\d+1], v2 offset:\o
.endm
.macro TQ d,o
ds_read_b64_tr_b16 a[\d:\d+1], v54 offset:\o
.endm
.macro RA d,o
ds_read_b128 a[\d:\d+3], v1 offset:\o
.endm
.macro RC d,o
ds_read_b128 a[\d:\d+3], v5 offset:\o
.endm
.macro BV d,o
ds_read_b128 v[\d:\d+3], v4 offset:\o
.endm
.macro BA d,o
ds_read_b128 a[\d:\d+3], v4 offset:\o
.endm
.macro PA a
s_mov_b32 m0, %[pm]
s_getpc_b64 s[46:47]
s_andn2_b32 s46, s46, 0x7f
s_add_u32 s46, s46, s81
s_addc_u32 s47, s47, 0
s_add_u32 s46, s46, \a
s_addc_u32 s47, s47, 0
.endm
.macro DF
global_load_lds_dwordx4 v0, s[46:47]
.endm
.macro DW o
global_load_lds_dwordx4 v0, s[40:41] offset:\o sc1
.endm
.macro DH o
global_load_lds_dwordx4 v0, s[40:41] offset:\o nt
.endm
.macro DS o
global_load_lds_dwordx4 v0, s[40:41] offset:\o
.endm
.macro XL d,o
global_load_dwordx2 v[\d:\d+1], v52, s[44:45] offset:\o
.endm
.macro ST d,o
global_store_dwordx2 v53, v[\d:\d+1], s[42:43] offset:\o nt sc1
.endm
.macro SU d,o
global_store_dwordx2 v53, v[\d:\d+1], s[42:43] offset:\o
.endm
.macro SA d,o
global_store_dwordx2 v53, a[\d:\d+1], s[42:43] offset:\o nt sc1
.endm
.macro CV d,a,b
v_cvt_pk_bf16_f32 v[\d], v[\a], v[\b]
.endm
.macro DR o
global_load_lds_dwordx4 v0, s[40:41] offset:\o
.endm
.macro TY d,o
ds_read_b64_tr_b16 v[\d:\d+1], v2 offset:\o
.endm
.macro TZ d,o
ds_read_b64_tr_b16 v[\d:\d+1], v54 offset:\o
.endm
.macro RS d,o
ds_read_b128 v[\d:\d+3], v46 offset:\o
.endm
.macro PM d,s
v_pk_mul_f16 v[\d], v[\d], v[\s]
.endm
.macro FS
global_store_dword v0, v10, s[96:97] sc1
.endm
.macro FL
global_load_dword v6, v7, s[98:99] sc1
.endm
.macro FB
s_cbranch_vccnz 21b
22:
.endm
.macro FG
s_or_b64 s[54:55], s[44:45], vcc
s_cbranch_scc1 23b
24:
.endm
.macro FX
s_branch 25f
21:
s_sleep 4
global_load_dword v6, v7, s[98:99] sc1
s_waitcnt vmcnt(0)
v_cmp_ne_u32 vcc, 1, v6
s_sub_u32 s80, s80, 1
s_cbranch_scc1 22f
s_nop 2
s_cbranch_vccnz 21b
s_branch 22f
23:
s_cmp_eq_u64 s[44:45], 0
s_cselect_b32 s54, 4, 0
v_mov_b32 v11, s54
v_mov_b32 v10, 1
s_mov_b64 exec, 1
global_store_dword v11, v10, s[78:79] offset:384 sc1
s_mov_b64 exec, -1
s_nop 4
s_branch 24f
25:
.endm
.macro PB
s_cbranch_vccnz 31f
32:
.endm
.macro PQ a
s_mov_b32 m0, %[pm]
s_add_u32 s46, s100, s81
s_addc_u32 s47, s101, 0
s_add_u32 s46, s46, \a
s_addc_u32 s47, s47, 0
.endm
.macro PJ
s_getpc_b64 s[100:101]
38:
s_add_u32 s92, s92, 39f-38b
s_add_u32 s100, s100, s92
s_addc_u32 s101, s101, 0
.endm
.macro WL n
s_waitcnt lgkmcnt(\n)
.endm
.macro WV n
s_waitcnt vmcnt(\n)
.endm
.macro EF d
v_fma_f32 v[\d], v[\d], s39, v55
.endm
.macro DP
global_load_lds_dword v54, s[46:47]
.endm
.macro M n
.if \n < 44
v_mfma_f32_16x16x32_f16 a[72+4*(\n/2):72+4*(\n/2)+3], a[24+8*((\n/2)%%6)+4*(\n&1):24+8*((\n/2)%%6)+4*(\n&1)+3], a[8*((\n/12)%%3)+4*(\n&1):8*((\n/12)%%3)+4*(\n&1)+3], a[72+4*(\n/2):72+4*(\n/2)+3]
.else
v_mfma_f32_16x16x32_f16 v[-32+4*(\n/2):-32+4*(\n/2)+3], a[24+8*((\n/2)%%6)+4*(\n&1):24+8*((\n/2)%%6)+4*(\n&1)+3], a[8*((\n/12)%%3)+4*(\n&1):8*((\n/12)%%3)+4*(\n&1)+3], v[-32+4*(\n/2):-32+4*(\n/2)+3]
.endif
.endm
.macro R1 n
.if \n < 64
RA 8*(((\n%%24)/2)%%3)+4*(\n&1),1024*\n
.else
RC 8*(((\n%%24)/2)%%3)+4*(\n&1),1024*\n-49152
.endif
.endm
.macro R n
R1 \n
R1 (\n+1)
.endm
.macro T r,u
TR \r,1024*\u
TQ \r+2,1024*\u
.endm
.macro mw k
s_add_u32 m0, %[wm], 1024*\k
.endm
.macro mh k
s_add_u32 m0, %[hm], 1024*\k
.endm
.macro ad r,k
s_add_u32 s40, s[\r], 1024*\k
s_addc_u32 s41, s[\r+1], 0
.endm
.macro ae d,r,k
s_add_u32 s[\d], s[\r], 1024*\k
s_addc_u32 s[\d+1], s[\r+1], 0
.endm
.macro bc m,b,d,r
s_bitcmp1_b32 s[\m], \b
s_cselect_b64 s[\d:\d+1], s[\r+2:\r+3], s[\r:\r+1]
.endm
.macro W k
DW 1024*\k
.endm
.macro H k
DH 1024*\k
.endm
k2_code:
s_load_dwordx16 s[48:63], %[pp], 0x0
s_load_dwordx16 s[64:79], %[pp], 0x40
s_load_dwordx16 s[84:99], %[pp], 0x80
v_mbcnt_lo_u32_b32 v7, -1, 0
v_mbcnt_hi_u32_b32 v7, -1, v7
v_lshlrev_b32 v0, 4, v7
v_add_u32 v204, 0x1000, v0
v_add_u32 v205, 0x2000, v0
s_mov_b32 s44, %[alo]
s_mov_b32 s45, %[ahi]
global_load_dwordx4 v[56:59], v0, s[44:45]
global_load_dwordx4 v[60:63], v204, s[44:45]
global_load_dwordx4 v[72:75], v205, s[44:45]
ae 44,44,12
global_load_dwordx4 v[76:79], v0, s[44:45]
global_load_dwordx4 v[96:99], v204, s[44:45]
global_load_dwordx4 v[100:103], v205, s[44:45]
s_waitcnt lgkmcnt(0)
bc 49,0,66,60
mh 0
ad 66,0
H 0
H 1
ae 44,44,12
global_load_dwordx4 v[120:123], v0, s[44:45]
global_load_dwordx4 v[124:127], v204, s[44:45]
global_load_dwordx4 v[144:147], v205, s[44:45]
ae 44,44,12
global_load_dwordx4 v[148:151], v0, s[44:45]
global_load_dwordx4 v[152:155], v204, s[44:45]
global_load_dwordx4 v[156:159], v205, s[44:45]
v_and_b32 v8, 15, v7
v_lshrrev_b32 v9, 4, v7
global_load_dwordx4 v[224:227], v0, s[94:95]
global_load_dwordx4 v[228:231], v0, s[94:95] offset:512
v_lshlrev_b32 v10, 2, v7
global_load_dword v168, v10, s[92:93] offset:0
global_load_dword v169, v10, s[92:93] offset:1024
global_load_dword v170, v10, s[92:93] offset:2048
global_load_dwordx4 v[80:83], v0, s[88:89]
s_mov_b64 exec, 0xff
global_load_dwordx4 v[84:87], v0, s[88:89] offset:1024
s_mov_b64 exec, -1
FX
v_add_u32 v1, %[ab], v0
v_add_u32 v5, 0xc000, v1
v_and_b32 v10, 3, v7
v_lshrrev_b32 v11, 2, v7
v_lshl_add_u32 v11, v10, 2, v11
v_and_b32 v11, 15, v11
v_lshlrev_b32 v11, 4, v11
mw 0
ad 86,0
DR 0
v_lshl_add_u32 v11, v10, 8, v11
v_add_u32 v2, %[hb], v11
v_lshrrev_b32 v10, 1, v7
v_and_b32 v10, 8, v10
v_add_u32 v2, v10, v2
v_xor_b32 v54, 8, v2
v_lshlrev_b32 v4, 4, v8
DR 1024
v_add_u32 v4, %[bb], v4
s_mov_b32 s38, 0xffff0000
s_mov_b32 s80, 0x40000
s_mov_b32 s81, s52
s_mov_b32 s39, s54
v_mov_b32 v55, s54
v_mul_f32 v55, 0xb5400000, v55
v_lshl_add_u32 v46, v9, 4, %[sb]
v_add_u32 v46, 53248, v46
v_bfe_u32 v10, v7, 1, 2
v_bfe_u32 v11, v7, 3, 1
v_bfe_u32 v12, v7, 1, 1
DR 2048
v_xor_b32 v11, v11, v12
v_and_b32 v12, 1, v7
v_lshl_add_u32 v11, v11, 1, v12
v_lshl_add_u32 v10, v10, 2, v11
v_lshl_add_u32 v10, v9, 4, v10
v_lshl_add_u32 v206, v10, 2, %[sw]
v_lshl_add_u32 v207, v7, 2, %[sw]
mw 3
ad 86,3
DR 0
v_lshrrev_b32 v13, 4, v0
s_movk_i32 s42, 1366
v_add_u32 v128, s51, v13
v_lshrrev_b32 v11, 6, v128
v_mul_u32_u24 v12, s42, v11
v_lshrrev_b32 v12, 16, v12
v_and_b32 v11, 1, v11
v_lshl_add_u32 v11, v12, 1, v11
v_bfe_u32 v128, v128, 4, 2
DR 1024
v_lshl_add_u32 v128, v11, 2, v128
v_lshl_add_u32 v128, v128, 4, %[sb]
v_cmp_gt_u32 vcc, 16, v11
v_add_u32 v12, 53248, v128
s_nop 0
v_cndmask_b32 v128, v128, v12, vcc
v_add_u32 v129, s51, v13
v_add_u32 v129, 64, v129
v_lshrrev_b32 v11, 6, v129
v_mul_u32_u24 v12, s42, v11
DR 2048
v_lshrrev_b32 v12, 16, v12
v_and_b32 v11, 1, v11
v_lshl_add_u32 v11, v12, 1, v11
v_bfe_u32 v129, v129, 4, 2
v_lshl_add_u32 v129, v11, 2, v129
v_lshl_add_u32 v129, v129, 4, %[sb]
v_cmp_gt_u32 vcc, 16, v11
v_add_u32 v12, 53248, v129
s_nop 0
v_cndmask_b32 v129, v129, v12, vcc
bc 49,1,66,60
mh 2
ad 66,96
H 0
WV 13
v_add_f32 v224, v224, v225
v_add_f32 v226, v226, v227
WV 12
v_add_f32 v228, v228, v229
v_add_f32 v230, v230, v231
v_add_u32 v240, %[bm], v0
v_add_f32 v224, v224, v226
v_add_f32 v228, v228, v230
H 1
v_sub_f32 v224, v224, v55
v_sub_f32 v228, v228, v55
v_mul_f32 v224, s53, v224
v_mul_f32 v228, s53, v228
v_mov_b32 v225, v224
v_mov_b32 v229, v228
v_mov_b32 v226, v224
bc 48,1,64,56
mh 4
ad 64,96
H 0
v_mov_b32 v230, v228
v_mov_b32 v227, v224
v_mov_b32 v231, v228
ds_write_b128 v240, v[224:227] offset:0
ds_write_b128 v240, v[228:231] offset:512
v_mov_b32 v219, 0x7f800000
s_mov_b32 s44, 0x44100000
v_add_f64 v[56:57], v[56:57], v[76:77]
v_add_f64 v[60:61], v[60:61], v[96:97]
v_add_f64 v[72:73], v[72:73], v[100:101]
v_add_f64 v[58:59], v[58:59], v[78:79]
v_add_f64 v[62:63], v[62:63], v[98:99]
H 1
v_add_f64 v[74:75], v[74:75], v[102:103]
v_add_f64 v[56:57], v[56:57], v[120:121]
v_add_f64 v[60:61], v[60:61], v[124:125]
v_add_f64 v[72:73], v[72:73], v[144:145]
v_add_f64 v[58:59], v[58:59], v[122:123]
v_add_f64 v[62:63], v[62:63], v[126:127]
v_add_f64 v[74:75], v[74:75], v[146:147]
mw 24
ad 86,24
DR 0
v_add_f64 v[56:57], v[56:57], v[148:149]
v_add_f64 v[60:61], v[60:61], v[152:153]
v_add_f64 v[72:73], v[72:73], v[156:157]
v_add_f64 v[58:59], v[58:59], v[150:151]
v_add_f64 v[62:63], v[62:63], v[154:155]
v_add_f64 v[74:75], v[74:75], v[158:159]
v_cvt_f32_f64 v172, v[56:57]
v_cvt_f32_f64 v176, v[60:61]
DR 1024
v_cvt_f32_f64 v180, v[72:73]
v_cvt_f32_f64 v173, v[58:59]
v_cvt_f32_f64 v177, v[62:63]
v_cvt_f32_f64 v181, v[74:75]
v_mul_f32 v174, s55, v172
v_mul_f32 v178, s55, v176
v_mul_f32 v182, s55, v180
DR 2048
v_mul_f32 v175, s55, v173
v_mul_f32 v179, s55, v177
v_mul_f32 v183, s55, v181
CV 175,174,175
CV 179,178,179
CV 183,182,183
UL 174,175
mw 27
ad 86,27
DR 0
)"
R"(UL 178,179
UL 182,183
UH 175,175
UH 179,179
UH 183,183
v_add_f32 v174, 0x358637bd, v174
v_add_f32 v178, 0x358637bd, v178
DR 1024
v_add_f32 v182, 0x358637bd, v182
v_add_f32 v175, 0x358637bd, v175
v_add_f32 v179, 0x358637bd, v179
v_add_f32 v183, 0x358637bd, v183
CV 175,174,175
CV 179,178,179
CV 183,182,183
DR 2048
UL 174,175
UL 178,179
UL 182,183
UH 175,175
UH 179,179
UH 183,183
v_sqrt_f32 v174, v174
bc 49,2,66,60
mh 6
ad 66,192
H 0
v_sqrt_f32 v178, v178
v_sqrt_f32 v182, v182
v_sqrt_f32 v175, v175
v_sqrt_f32 v179, v179
v_sqrt_f32 v183, v183
CV 175,174,175
CV 179,178,179
H 1
CV 183,182,183
UL 174,175
UL 178,179
UL 182,183
UH 175,175
UH 179,179
UH 183,183
bc 48,2,64,56
mh 8
ad 64,192
H 0
v_cmp_le_f32 vcc, s44, v172
v_cmp_le_f32 s[54:55], s44, v173
s_nop 0
v_cndmask_b32 v172, v219, v174, vcc
v_cndmask_b32 v173, v219, v175, s[54:55]
v_add_f32 v172, v172, v173
v_cmp_le_f32 vcc, s44, v176
v_cmp_le_f32 s[54:55], s44, v177
s_nop 0
v_cndmask_b32 v176, v219, v178, vcc
v_cndmask_b32 v177, v219, v179, s[54:55]
v_add_f32 v176, v176, v177
v_cmp_le_f32 vcc, s44, v180
v_cmp_le_f32 s[54:55], s44, v181
s_nop 0
v_cndmask_b32 v180, v219, v182, vcc
v_cndmask_b32 v181, v219, v183, s[54:55]
v_add_f32 v180, v180, v181
v_add_f32 v216, v172, v176
H 1
v_add_f32 v216, v216, v180
s_nop 1
v_add_f32_dpp v216, v216, v216 row_shr:1 row_mask:0xf bank_mask:0xf
s_nop 1
v_add_f32_dpp v216, v216, v216 row_shr:2 row_mask:0xf bank_mask:0xf
s_nop 1
v_add_f32_dpp v216, v216, v216 row_shr:4 row_mask:0xf bank_mask:0xf
s_nop 1
v_add_f32_dpp v216, v216, v216 row_shr:8 row_mask:0xf bank_mask:0xf
s_nop 1
v_add_f32_dpp v216, v216, v216 row_bcast:15 row_mask:0xa bank_mask:0xf
s_nop 1
v_add_f32_dpp v216, v216, v216 row_bcast:31 row_mask:0xc bank_mask:0xf
ds_write_b32 v207, v216 offset:52224
v_mov_b32 v10, %[sb]
WL 0
s_barrier
ds_read_b32 v220, v10 offset:52476
ds_read_b32 v221, v10 offset:52732
ds_read_b32 v222, v10 offset:52988
ds_read_b32 v223, v10 offset:53244
BA 72,0
BA 76,0
BA 96,256
BA 100,256
BA 120,512
BA 124,512
BA 144,768
BA 148,768
BV 64,1024
BV 68,1024
BV 88,1280
WL 9
BV 92,1280
BV 112,1536
BV 116,1536
BV 136,1792
BV 140,1792
BV 160,2048
WL 9
BV 164,2048
BV 184,2304
BV 188,2304
BV 208,2560
BV 212,2560
BV 232,2816
WL 9
BV 236,2816
WV 24
UL 192,168
WV 23
UL 196,169
WV 22
UL 200,170
UH 193,168
UH 197,169
UH 201,170
v_mov_b32 v217, 0
H 2
v_add_f32 v220, v220, v221
v_add_f32 v222, v222, v223
v_add_f32 v220, v220, v222
v_mul_f32 v218, 0x3a2aaaab, v220
v_cmp_nlt_f32 s[44:45], v220, v219
CV 218,218,218
UL 218,218
v_add_f32 v218, 0x358637bd, v218
H 3
CV 218,218,218
UL 218,218
v_rcp_f32 v218, v218
s_nop 0
v_mul_f32 v174, v218, v174
v_mul_f32 v178, v218, v178
v_mul_f32 v182, v218, v182
v_mul_f32 v175, v218, v175
v_mul_f32 v179, v218, v179
mw 48
ad 86,48
DR 0
v_mul_f32 v183, v218, v183
CV 175,174,175
CV 179,178,179
CV 183,182,183
UL 174,175
UL 178,179
UL 182,183
DR 1024
UH 175,175
UH 179,179
UH 183,183
v_fma_f32 v174, v192, v174, 1.0
v_fma_f32 v178, v196, v178, 1.0
v_fma_f32 v182, v200, v182, 1.0
v_fma_f32 v175, v193, v175, 1.0
DR 2048
v_fma_f32 v179, v197, v179, 1.0
v_fma_f32 v183, v201, v183, 1.0
v_cvt_pk_f16_f32 v192, v174, v175
v_cvt_pk_f16_f32 v196, v178, v179
v_cvt_pk_f16_f32 v200, v182, v183
ds_write_b32 v206, v192 offset:53248
ds_write_b32 v206, v196 offset:1024
ds_write_b32 v206, v200 offset:2048
v_max3_f32 v217, |v174|, |v175|, v217
v_max3_f32 v217, |v178|, |v179|, v217
v_max3_f32 v217, |v182|, |v183|, v217
mw 51
ad 86,51
DR 0
v_cmp_lt_f32 vcc, 0x43000000, v217
WL 0
WV 18
s_barrier
RS 14,0
RS 18,64
TY 22,0
TZ 24,0
TY 26,14336
TZ 28,14336
TY 30,1024
TZ 32,1024
TY 34,15360
TZ 36,15360
R 0
R 2
R1 4
WL 9
R1 5
ds_read_b128 v[104:107], v128
ds_read_b128 v[108:111], v129
FG
PM 22,14
PM 23,15
PM 24,16
PM 25,17
PM 26,18
PM 27,19
PM 28,20
DR 1024
PM 29,21
AW 24,22
AW 25,23
AW 26,24
AW 27,25
AW 28,26
AW 29,27
DR 2048
AW 30,28
AW 31,29
WL 8
PM 30,14
PM 31,15
PM 32,16
PM 33,17
PM 34,18
PM 35,19
PM 36,20
PM 37,21
AW 32,30
AW 33,31
AW 34,32
AW 35,33
AW 36,34
AW 37,35
AW 38,36
AW 39,37
WL 1
PM 80,104
PM 81,105
PM 82,106
PM 83,107
WL 0
PM 84,108
PM 85,109
PM 86,110
PM 87,111
s_mov_b32 s42, 0x1c001c00
v_pk_mul_f16 v80, v80, s42
v_pk_mul_f16 v81, v81, s42
v_pk_mul_f16 v82, v82, s42
v_pk_mul_f16 v83, v83, s42
v_pk_mul_f16 v84, v84, s42
v_pk_mul_f16 v85, v85, s42
v_pk_mul_f16 v86, v86, s42
v_pk_mul_f16 v87, v87, s42
global_store_dwordx4 v0, v[80:83], s[90:91] sc1
s_mov_b64 exec, 0xff
global_store_dwordx4 v0, v[84:87], s[90:91] offset:1024 sc1
s_mov_b64 exec, -1
M 0
mw 72
RS 14,128
RS 18,192
M 1
TY 38,2048
TZ 40,2048
M 2
ad 86,72
DR 0
M 3
R 6
M 12
DR 1024
TY 42,16384
M 13
DR 2048
TZ 44,16384
M 14
mw 75
TY 22,3072
TZ 24,3072
M 15
R 8
M 24
ad 86,75
DR 0
M 25
DR 1024
TY 26,17408
M 26
WL 5
DR 2048
PM 38,14
M 27
R 10
M 36
PM 39,15
PM 40,16
M 37
WV 20
PM 41,17
PM 42,18
M 38
WL 0
s_barrier
PM 43,19
PM 44,20
M 39
R 24
M 48
mw 0
PM 45,21
AW 160,38
M 49
ad 86,96
DR 0
M 50
DR 1024
AW 161,39
M 51
R 26
M 60
DR 2048
AW 162,40
M 61
mw 3
AW 163,41
AW 164,42
M 62
ad 86,99
DR 0
M 63
WL 2
R 28
M 72
DR 1024
AW 165,43
M 73
DR 2048
AW 166,44
M 74
bc 49,3,66,60
AW 167,45
M 75
WL 2
R 30
M 84
mh 12
TZ 28,17408
BA 80,0
M 85
ad 66,288
H 0
M 86
H 1
BA 104,256
M 87
WL 5
R 32
M 96
BA 128,512
BA 152,768
M 97
WL 6
PM 22,14
PM 23,15
BA 84,0
M 98
PM 24,16
PM 25,17
BA 108,256
M 99
R 34
BA 132,512
M 108
PM 26,18
PM 27,19
BA 156,768
M 109
WV 16
PM 28,20
PM 29,21
M 110
WL 2
s_barrier
AW 168,22
AW 169,23
TY 30,4096
TZ 32,4096
M 111
R 48
TY 34,18432
TZ 36,18432
M 120
mw 24
AW 170,24
AW 171,25
TY 38,5120
M 121
ad 86,120
DR 0
TZ 40,5120
M 132
)"
R"(DR 1024
AW 172,26
TY 42,19456
M 133
WL 3
DR 2048
AW 173,27
TZ 44,19456
M 122
mw 27
AW 174,28
AW 175,29
PM 30,14
PM 31,15
M 123
R 50
PM 32,16
PM 33,17
M 134
ad 86,123
DR 0
PM 34,18
PM 35,19
M 135
R 52
PM 36,20
PM 37,21
MA 72,0,160
DR 1024
AW 176,30
AW 177,31
AW 178,32
MA 72,4,164
DR 2048
AW 179,33
AW 180,34
AW 181,35
MA 76,0,168
WL 4
AW 182,36
AW 183,37
PM 38,14
MA 76,4,172
PM 39,15
PM 40,16
PM 41,17
MA 80,0,176
PM 42,18
PM 43,19
PM 44,20
PM 45,21
AW 184,38
AW 185,39
AW 186,40
AW 187,41
AW 188,42
AW 189,43
AW 190,44
AW 191,45
MA 80,4,180
RS 14,256
RS 18,320
TY 22,6144
MA 84,0,184
TZ 24,6144
TY 26,20480
TZ 28,20480
MA 84,4,188
WL 6
R 54
MA 96,8,160
TY 30,7168
TZ 32,7168
MA 96,12,164
TY 34,21504
TZ 36,21504
MA 100,8,168
BV 72,1024
BV 96,1280
MA 100,12,172
WL 8
PM 22,14
PM 23,15
PM 24,16
MA 104,8,176
PM 25,17
PM 26,18
PM 27,19
MA 104,12,180
PM 28,20
PM 29,21
AW 24,22
MA 108,8,184
AW 25,23
AW 26,24
AW 27,25
MA 108,12,188
R 56
AW 28,26
MA 120,16,160
AW 29,27
AW 30,28
AW 31,29
MA 120,20,164
WL 4
PM 30,14
PM 31,15
MA 124,16,168
PM 32,16
PM 33,17
MA 124,20,172
PM 34,18
PM 35,19
MA 128,16,176
PM 36,20
PM 37,21
MA 128,20,180
AW 32,30
AW 33,31
MA 132,16,184
AW 34,32
AW 35,33
MA 132,20,188
R 58
MA 144,0,160
AW 36,34
AW 37,35
MA 144,4,164
AW 38,36
AW 39,37
MA 148,0,168
BV 120,1536
BV 144,1792
MA 148,4,172
BV 76,1024
BV 100,1280
MA 152,0,176
WV 14
BV 124,1536
BV 148,1792
MA 152,4,180
WL 6
BV 168,2048
BV 192,2304
MA 156,0,184
s_barrier
v_mov_b32 v10, 1
s_mov_b64 exec, 1
FS
s_mov_b64 exec, -1
MA 156,4,188
R 72
MV 64,8,160
mw 48
TY 38,8192
TZ 40,8192
MV 64,12,164
ad 86,144
DR 0
MV 68,8,168
TY 42,22528
TZ 44,22528
MV 68,12,172
DR 1024
TY 22,9216
MV 72,8,176
WL 9
TZ 24,9216
TY 26,23552
MV 72,12,180
DR 2048
TZ 28,23552
MV 76,8,184
BV 216,2560
BV 240,2816
MV 76,12,188
WL 6
R 74
BV 172,2048
MV 88,16,160
mw 51
PM 38,14
PM 39,15
BV 196,2304
MV 88,20,164
ad 86,147
DR 0
BV 220,2560
MV 92,16,168
PM 40,16
PM 41,17
BV 244,2816
MV 92,20,172
DR 1024
PM 42,18
MV 96,16,176
PM 43,19
PM 44,20
MV 96,20,180
DR 2048
PM 45,21
MV 100,16,184
AW 40,38
AW 41,39
MV 100,20,188
R 76
MV 112,0,160
bc 48,3,64,56
AW 42,40
MV 112,4,164
mh 0
AW 43,41
AW 44,42
MV 116,0,168
ad 64,288
H 0
MV 116,4,172
AW 45,43
AW 46,44
MV 120,0,176
H 1
AW 47,45
MV 120,4,180
WL 3
PM 22,14
PM 23,15
MV 124,0,184
H 2
PM 24,16
MV 124,4,188
R 78
MV 136,8,160
H 3
PM 25,17
MV 136,12,164
PM 26,18
PM 27,19
MV 140,8,168
bc 49,4,66,60
PM 28,20
MV 140,12,172
mh 4
PM 29,21
AW 48,22
MV 144,8,176
ad 66,384
H 0
MV 144,12,180
AW 49,23
AW 50,24
MV 148,8,184
H 1
AW 51,25
MV 148,12,188
WL 2
R 80
MV 160,16,160
AW 52,26
AW 53,27
MV 160,20,164
AW 54,28
AW 55,29
MV 164,16,168
TY 30,10240
TZ 32,10240
MV 164,20,172
TY 34,24576
TZ 36,24576
MV 168,16,176
TY 38,11264
TZ 40,11264
MV 168,20,180
TY 42,25600
TZ 44,25600
MV 172,16,184
BA 88,0
BA 112,256
MV 172,20,188
WL 8
R 82
MV 184,0,160
WL 8
PM 30,14
PM 31,15
MV 184,4,164
PM 32,16
PM 33,17
MV 188,0,168
PM 34,18
PM 35,19
MV 188,4,172
PM 36,20
PM 37,21
MV 192,0,176
WV 21
AW 56,30
AW 57,31
MV 192,4,180
WL 0
AW 58,32
AW 59,33
MV 196,0,184
s_barrier
AW 60,34
AW 61,35
MV 196,4,188
R 0
MV 208,8,160
mw 72
AW 62,36
AW 63,37
MV 208,12,164
ad 86,168
DR 0
MV 232,16,160
PM 38,14
PM 39,15
MV 232,20,164
DR 1024
PM 40,16
MV 212,8,168
PM 41,17
PM 42,18
MV 212,12,172
DR 2048
PM 43,19
MV 236,16,168
PM 44,20
PM 45,21
MV 236,20,172
mw 75
AW 64,38
AW 65,39
MV 216,8,176
ad 86,171
DR 0
MV 216,12,180
AW 66,40
AW 67,41
MV 240,16,176
DR 1024
AW 68,42
MV 240,20,180
AW 69,43
AW 70,44
MV 220,8,184
DR 2048
AW 71,45
MV 220,12,188
R 2
MV 244,16,184
bc 48,4,64,56
RS 14,384
MV 244,20,188
WL 3
R 4
M 0
mh 6
RS 18,448
BA 136,512
M 1
BV 56,768
M 2
ad 64,384
H 0
M 3
BA 92,0
BA 116,256
M 4
H 1
BA 140,512
M 5
BV 60,768
M 6
H 2
M 7
M 8
H 3
M 9
WL 3
M 10
M 11
R 6
M 12
M 13
M 14
M 15
M 16
BV 80,1024
BV 104,1280
M 17
BV 128,1536
BV 152,1792
M 18
BV 84,1024
BV 108,1280
M 19
BV 132,1536
BV 156,1792
M 20
)"
R"(v_lshrrev_b32 v9, 8, v0
v_bfe_u32 v8, v0, 4, 4
M 21
WL 12
v_lshlrev_b32 v10, 2, v9
M 22
s_mov_b32 s42, 0xcb527610
M 23
R 8
M 24
s_movk_i32 s43, 0xc00
v_lshlrev_b32 v11, 3, v8
v_bfe_u32 v3, s42, v10, 4
M 25
v_add_u32 v10, 16, v10
v_mad_u32_u24 v3, v3, s43, v11
M 26
v_bfe_u32 v50, s42, v10, 4
v_add_u32 v10, 1, v9
M 27
v_mad_u32_u24 v50, v50, s43, v11
v_and_b32 v10, 2, v10
M 28
v_sub_u32 v10, 1, v10
v_mul_i32_i24 v51, s43, v10
M 29
M 30
M 31
M 32
M 33
WL 7
M 34
M 35
R 10
M 36
M 37
M 38
M 39
M 40
M 41
M 42
M 43
M 44
WV 23
M 45
WL 0
s_barrier
TY 22,12288
TZ 24,12288
M 46
TY 26,26624
TZ 28,26624
M 47
R 24
M 48
M 49
M 50
M 51
WL 2
PM 22,14
PM 23,15
M 52
PM 24,16
PM 25,17
M 53
PM 26,18
PM 27,19
M 54
PM 28,20
PM 29,21
M 55
AW 160,22
AW 161,23
M 56
AW 162,24
AW 163,25
M 57
AW 164,26
AW 165,27
M 58
AW 166,28
AW 167,29
M 59
R 26
M 60
M 61
M 62
M 63
M 64
BV 176,2048
BV 200,2304
M 65
BV 224,2560
BV 248,2816
M 66
v_lshlrev_b32 v7, 4, v0
BV 180,2048
M 67
FL
TY 30,13312
M 68
TZ 32,13312
TY 34,27648
M 69
TZ 36,27648
BV 204,2304
M 70
WL 8
BV 228,2560
BV 252,2816
M 71
R 28
M 72
M 73
M 74
WL 5
PM 30,14
PM 31,15
M 75
PM 32,16
PM 33,17
M 76
PM 34,18
PM 35,19
M 77
PM 36,20
PM 37,21
M 78
AW 168,30
AW 169,31
M 79
AW 170,32
AW 171,33
M 80
AW 172,34
AW 173,35
M 81
AW 174,36
AW 175,37
M 82
M 83
R 30
M 84
M 85
bc 49,5,66,60
M 86
mh 10
M 87
ad 66,480
H 0
M 88
M 89
M 90
M 91
H 1
M 92
M 93
M 94
M 95
WL 2
R 32
M 96
M 97
M 98
M 99
M 100
M 101
M 102
M 103
M 104
M 105
M 106
M 107
WL 2
R 34
M 108
M 109
M 110
WV 2
v_cmp_ne_u32 vcc, 1, v6
M 111
M 112
M 113
FB
M 114
M 115
M 116
WL 0
M 117
s_barrier
TY 38,0
TZ 40,0
TY 42,14336
M 118
mw 0
TZ 44,14336
TY 22,1024
TZ 24,1024
M 119
R 48
TY 26,15360
M 120
ad 84,192
W 0
TZ 28,15360
M 121
W 1
TY 30,2048
TZ 32,2048
M 132
WL 8
W 2
TY 34,16384
TZ 36,16384
M 133
mw 3
PM 38,14
PM 39,15
PM 40,16
M 122
ad 84,195
W 0
PM 41,17
M 123
W 1
PM 42,18
PM 43,19
M 134
W 2
PM 44,20
PM 45,21
M 135
mw 24
AW 176,38
AW 177,39
AW 178,40
M 124
ad 84,216
W 0
AW 179,41
M 125
W 1
AW 180,42
AW 181,43
M 136
WL 1
W 2
AW 182,44
AW 183,45
M 137
mw 27
PM 22,14
PM 23,15
PM 24,16
M 126
ad 84,219
W 0
PM 25,17
M 127
W 1
PM 26,18
PM 27,19
M 138
W 2
PM 28,20
PM 29,21
M 139
AW 184,22
AW 185,23
AW 186,24
M 128
AW 187,25
AW 188,26
AW 189,27
M 129
WL 0
AW 190,28
AW 191,29
PM 30,14
M 140
PM 31,15
PM 32,16
PM 33,17
M 141
PM 34,18
PM 35,19
PM 36,20
M 130
PM 37,21
AW 192,30
AW 193,31
M 131
R 50
AW 194,32
M 142
AW 195,33
AW 196,34
AW 197,35
M 143
R 52
AW 198,36
MA 72,0,160
AW 199,37
TY 38,3072
TZ 40,3072
MA 72,4,164
TY 42,17408
TZ 44,17408
MA 76,0,168
MA 76,4,172
MA 80,0,176
MA 80,4,180
MA 84,0,184
WL 0
PM 38,14
PM 39,15
PM 40,16
MA 84,4,188
PM 41,17
PM 42,18
PM 43,19
MA 88,0,192
PM 44,20
PM 45,21
AW 200,38
AW 201,39
AW 202,40
AW 203,41
AW 204,42
AW 205,43
AW 206,44
AW 207,45
MA 88,4,196
MA 92,0,200
MA 92,4,204
R 54
MA 96,8,160
MA 96,12,164
MA 100,8,168
MA 100,12,172
MA 104,8,176
MA 104,12,180
MA 108,8,184
MA 108,12,188
MA 112,8,192
MA 112,12,196
MA 116,8,200
MA 116,12,204
R 56
MA 120,16,160
MA 120,20,164
MA 124,16,168
MA 124,20,172
MA 128,16,176
MA 128,20,180
MA 132,16,184
MA 132,20,188
MA 136,16,192
MA 136,20,196
MA 140,16,200
MA 140,20,204
WL 2
R 58
MA 144,0,160
MA 144,4,164
MA 148,0,168
MA 148,4,172
MA 152,0,176
MA 152,4,180
MA 156,0,184
MA 156,4,188
MV 56,0,192
MV 56,4,196
WL 0
s_barrier
MV 60,0,200
mw 48
MV 60,4,204
R 72
MV 64,8,160
ad 84,240
W 0
MV 64,12,164
v_lshrrev_b32 v8, 1, v0
s_mov_b64 exec, s[76:77]
global_load_dwordx2 v[6:7], v8, s[78:79] sc1
s_mov_b64 exec, -1
MV 68,8,168
MV 68,12,172
MV 72,8,176
W 1
MV 72,12,180
MV 76,8,184
MV 76,12,188
MV 80,8,192
W 2
MV 80,12,196
MV 84,8,200
MV 84,12,204
R 74
)"
R"(MV 88,16,160
mw 51
MV 88,20,164
ad 84,243
W 0
MV 92,16,168
MV 92,20,172
MV 96,16,176
MV 96,20,180
W 1
MV 100,16,184
MV 100,20,188
MV 104,16,192
MV 104,20,196
W 2
MV 108,16,200
MV 108,20,204
WL 2
R 76
MV 112,0,160
MV 112,4,164
bc 48,5,64,56
MV 116,0,168
mh 12
MV 116,4,172
ad 64,480
H 0
MV 120,0,176
MV 120,4,180
MV 124,0,184
MV 124,4,188
H 1
MV 128,0,192
MV 128,4,196
MV 132,0,200
MV 132,4,204
WL 2
R 78
MV 136,8,160
mh 0
MV 136,12,164
ad 64,482
H 0
MV 140,8,168
MV 140,12,172
MV 144,8,176
MV 144,12,180
H 1
MV 148,8,184
MV 148,12,188
MV 152,8,192
MV 152,12,196
bc 49,6,66,60
MV 156,8,200
mh 2
MV 156,12,204
WL 2
R 80
MV 160,16,160
ad 66,576
H 0
MV 160,20,164
MV 164,16,168
MV 164,20,172
MV 168,16,176
H 1
MV 168,20,180
MV 172,16,184
MV 172,20,188
MV 176,16,192
MV 176,20,196
MV 180,16,200
MV 180,20,204
WL 2
R 82
MV 184,0,160
MV 184,4,164
MV 188,0,168
MV 188,4,172
MV 192,0,176
MV 192,4,180
MV 196,0,184
MV 196,4,188
MV 200,0,192
WV 19
MV 200,4,196
WL 0
s_barrier
T 24,4
MV 204,0,200
mw 72
T 28,18
MV 204,4,204
R 0
MV 208,8,160
ad 84,264
W 0
MV 208,12,164
T 32,5
MV 232,16,160
T 36,19
MV 232,20,164
MV 212,8,168
W 1
MV 212,12,172
T 40,6
MV 236,16,168
T 44,20
MV 236,20,172
MV 216,8,176
W 2
MV 216,12,180
WL 8
T 48,7
MV 240,16,176
T 52,21
MV 240,20,180
ae 44,70,0
MV 220,8,184
mw 75
MV 220,12,188
T 56,8
MV 244,16,184
WL 8
T 60,22
MV 244,20,188
ad 84,267
W 0
MV 224,8,192
s_bitcmp1_b32 s50, 0
s_cselect_b32 s51, 24576, 0
v_add_u32 v52, s51, v3
MV 224,12,196
T 64,9
MV 248,16,192
T 68,23
MV 248,20,196
W 1
XL 14,0
MV 228,8,200
XL 16,768
MV 228,12,204
WL 8
R 2
MV 252,16,200
XL 18,1536
MV 252,20,204
R 4
M 0
W 2
M 1
M 2
XL 20,2304
M 3
M 4
M 5
WV 21
v_or_b32 v6, v6, v7
s_mov_b64 exec, s[76:77]
v_cmp_ne_u32 %[flg], 0, v6
s_mov_b64 exec, -1
M 6
M 7
WL 8
M 8
M 9
WL 4
M 10
M 11
WL 2
R 6
M 12
M 13
M 14
M 15
M 16
M 17
M 18
M 19
M 20
M 21
M 22
M 23
WL 2
R 8
M 24
M 25
M 26
M 27
M 28
M 29
M 30
M 31
M 32
M 33
M 34
M 35
WL 2
R 10
M 36
M 37
M 38
M 39
M 40
M 41
M 42
M 43
M 44
M 45
WL 0
s_barrier
M 46
mw 0
M 47
R 24
M 48
ad 84,288
W 0
M 49
M 50
M 51
M 52
W 1
M 53
M 54
M 55
M 56
W 2
M 57
M 58
M 59
R 26
M 60
mw 3
M 61
ad 84,291
W 0
M 62
M 63
M 64
M 65
W 1
M 66
M 67
M 68
M 69
W 2
M 70
M 71
WL 2
R 28
M 72
M 73
bc 48,6,64,56
M 74
mh 4
M 75
ad 64,576
H 0
M 76
M 77
M 78
M 79
H 1
M 80
M 81
M 82
M 83
WL 2
R 30
M 84
H 2
M 85
M 86
M 87
M 88
H 3
M 89
M 90
M 91
M 92
bc 49,7,66,60
M 93
mh 8
M 94
ad 66,672
H 0
M 95
WL 2
R 32
M 96
M 97
M 98
H 1
M 99
M 100
M 101
M 102
M 103
M 104
M 105
M 106
M 107
WL 2
R 34
M 108
M 109
M 110
M 111
M 112
M 113
M 114
M 115
M 116
WV 24
M 117
WL 0
s_barrier
M 118
mw 24
M 119
R 48
M 120
ad 84,312
W 0
M 121
M 132
M 133
T 24,10
M 122
T 28,24
M 123
W 1
M 134
M 135
T 32,11
M 124
T 36,25
M 125
W 2
M 136
ae 44,70,0
M 137
s_bfe_u32 s51, s50, 0x10001
T 40,12
M 126
T 44,26
M 127
mw 27
v_mad_i32_i24 v52, v51, s51, v50
XL 22,0
M 138
ad 84,315
W 0
M 139
WL 8
T 48,13
M 128
T 52,27
M 129
XL 24,768
M 140
W 1
M 141
T 56,0
M 130
WL 8
T 60,14
M 131
R 50
M 142
W 2
XL 26,1536
M 143
T 64,1
M 0
WL 8
T 68,15
M 1
R 52
M 2
M 3
M 4
XL 28,2304
M 5
M 6
M 7
WL 8
M 8
M 9
WL 2
M 10
M 11
R 54
M 12
M 13
M 14
M 15
M 16
M 17
)"
R"(M 18
M 19
M 20
M 21
M 22
M 23
WL 2
R 56
M 24
M 25
M 26
M 27
M 28
M 29
M 30
M 31
M 32
M 33
M 34
M 35
WL 2
R 58
M 36
M 37
M 38
M 39
M 40
WV 24
UL 6,14
UH 7,14
M 41
UL 8,15
UH 9,15
M 42
AR 10,72
AR 11,96
M 43
WV 23
AR 12,120
AR 13,144
M 44
WL 0
AD 10,6
AD 11,7
M 45
s_barrier
AD 12,8
AD 13,9
M 46
mw 48
AW 72,10
AW 96,11
M 47
R 72
M 48
ad 84,336
W 0
M 49
AW 120,12
AW 144,13
M 50
UL 6,16
UH 7,16
M 51
UL 8,17
UH 9,17
M 52
W 1
AR 10,73
M 53
AR 11,97
AR 12,121
M 54
AR 13,145
AD 10,6
M 55
AD 11,7
AD 12,8
M 56
W 2
AD 13,9
M 57
AW 73,10
AW 97,11
M 58
AW 121,12
AW 145,13
M 59
R 74
M 60
mw 51
UL 6,18
UH 7,18
M 61
ad 84,339
W 0
M 62
UL 8,19
UH 9,19
M 63
AR 10,74
AR 11,98
M 64
AR 12,122
AR 13,146
M 65
W 1
AD 10,6
M 66
AD 11,7
AD 12,8
M 67
AD 13,9
AW 74,10
M 68
AW 98,11
AW 122,12
M 69
W 2
AW 146,13
M 70
WV 28
UL 6,20
UH 7,20
M 71
WL 0
R 76
M 72
UL 8,21
UH 9,21
M 73
bc 48,7,64,56
AR 10,75
M 74
mh 10
AR 11,99
AR 12,123
M 75
ad 64,672
H 0
M 76
AR 13,147
AD 10,6
M 77
AD 11,7
AD 12,8
M 78
AD 13,9
AW 75,10
M 79
H 1
AW 99,11
M 80
AW 123,12
AW 147,13
M 81
M 82
M 83
R 78
M 84
H 2
M 85
M 86
M 87
M 88
H 3
M 89
M 90
M 91
M 92
bc 49,8,66,60
M 93
mh 0
M 94
ad 66,768
H 0
M 95
WL 2
R 80
M 96
M 97
M 98
H 1
M 99
M 100
M 101
M 102
M 103
M 104
M 105
M 106
M 107
WL 2
R 82
M 108
M 109
M 110
M 111
M 112
M 113
M 114
M 115
M 116
WV 24
M 117
WL 0
s_barrier
M 118
mw 72
M 119
R 0
M 120
ad 84,360
W 0
M 121
M 132
M 133
T 24,2
M 122
T 28,16
M 123
W 1
M 134
M 135
T 32,3
M 124
T 36,17
M 125
W 2
M 136
M 137
T 40,4
M 126
T 44,18
M 127
mw 75
M 138
ad 84,363
W 0
M 139
WL 8
T 48,5
M 128
T 52,19
M 129
ae 44,68,0
M 140
W 1
M 141
T 56,6
M 130
WL 8
T 60,20
M 131
R 2
M 142
W 2
M 143
T 64,7
M 0
WL 8
T 68,21
M 1
R 4
M 2
s_bitcmp1_b32 s50, 2
s_cselect_b32 s51, 24576, 0
M 3
v_add_u32 v52, s51, v3
XL 30,0
M 4
XL 32,768
M 5
XL 34,1536
M 6
XL 36,2304
M 7
WL 8
M 8
M 9
WL 2
M 10
M 11
R 6
M 12
M 13
M 14
M 15
M 16
M 17
M 18
M 19
M 20
M 21
M 22
M 23
WL 2
R 8
M 24
M 25
M 26
M 27
M 28
M 29
M 30
M 31
M 32
M 33
M 34
M 35
WL 2
R 10
M 36
M 37
M 38
M 39
M 40
M 41
M 42
WV 28
UL 6,22
UH 7,22
M 43
WV 24
UL 8,23
UH 9,23
M 44
WL 0
AR 10,76
AR 11,100
M 45
s_barrier
AR 12,124
AR 13,148
M 46
mw 0
AD 10,6
AD 11,7
M 47
R 24
M 48
ad 84,384
W 0
M 49
AD 12,8
AD 13,9
M 50
AW 76,10
AW 100,11
M 51
AW 124,12
AW 148,13
M 52
W 1
UL 6,24
M 53
UH 7,24
UL 8,25
M 54
UH 9,25
AR 10,77
M 55
AR 11,101
AR 12,125
M 56
W 2
AR 13,149
M 57
AD 10,6
AD 11,7
M 58
AD 12,8
AD 13,9
M 59
R 26
M 60
mw 3
AW 77,10
AW 101,11
M 61
ad 84,387
W 0
M 62
AW 125,12
AW 149,13
M 63
WV 26
UL 6,26
UH 7,26
M 64
UL 8,27
UH 9,27
M 65
W 1
AR 10,78
M 66
AR 11,102
AR 12,126
M 67
AR 13,150
)"
R"(AD 10,6
M 68
AD 11,7
AD 12,8
M 69
W 2
AD 13,9
M 70
AW 78,10
AW 102,11
M 71
WL 0
R 28
M 72
AW 126,12
AW 150,13
M 73
bc 48,8,64,56
UL 6,28
M 74
mh 2
UH 7,28
UL 8,29
M 75
ad 64,768
H 0
M 76
UH 9,29
AR 10,79
M 77
AR 11,103
AR 12,127
M 78
AR 13,151
AD 10,6
M 79
H 1
AD 11,7
M 80
AD 12,8
AD 13,9
M 81
AW 79,10
AW 103,11
M 82
AW 127,12
AW 151,13
M 83
R 30
M 84
H 2
M 85
M 86
M 87
M 88
H 3
M 89
M 90
M 91
M 92
bc 49,9,66,60
M 93
mh 6
M 94
ad 66,864
H 0
M 95
WL 2
R 32
M 96
M 97
M 98
H 1
M 99
M 100
M 101
M 102
M 103
M 104
M 105
M 106
M 107
WL 2
R 34
M 108
M 109
M 110
M 111
M 112
M 113
M 114
M 115
M 116
WV 24
M 117
WL 0
s_barrier
M 118
mw 24
M 119
R 48
M 120
ad 84,408
W 0
M 121
M 132
M 133
T 24,8
M 122
T 28,22
M 123
W 1
M 134
M 135
T 32,9
M 124
T 36,23
M 125
W 2
M 136
M 137
T 40,10
M 126
T 44,24
M 127
mw 27
M 138
ad 84,411
W 0
M 139
WL 8
T 48,11
M 128
T 52,25
M 129
ae 44,68,0
M 140
s_bfe_u32 s51, s50, 0x10003
W 1
v_mad_i32_i24 v52, v51, s51, v50
M 141
T 56,12
M 130
WL 8
T 60,26
M 131
R 50
M 142
W 2
XL 38,0
M 143
T 64,13
M 0
WL 8
T 68,27
M 1
R 52
M 2
XL 40,768
M 3
XL 42,1536
M 4
M 5
M 6
M 7
WL 8
M 8
XL 44,2304
M 9
WL 2
M 10
M 11
R 54
M 12
M 13
M 14
M 15
M 16
M 17
M 18
M 19
M 20
M 21
M 22
M 23
WL 2
R 56
M 24
M 25
M 26
M 27
M 28
M 29
M 30
M 31
M 32
M 33
M 34
M 35
WL 2
R 58
M 36
M 37
M 38
M 39
M 40
M 41
M 42
M 43
M 44
WV 22
UL 6,30
UH 7,30
M 45
WL 0
s_barrier
UL 8,31
UH 9,31
M 46
mw 48
AR 10,80
AR 11,104
M 47
R 72
M 48
ad 84,432
W 0
M 49
AR 12,128
AR 13,152
M 50
AD 10,6
AD 11,7
M 51
AD 12,8
AD 13,9
M 52
W 1
AW 80,10
M 53
AW 104,11
AW 128,12
M 54
AW 152,13
UL 6,32
M 55
UH 7,32
UL 8,33
M 56
W 2
UH 9,33
M 57
AR 10,81
AR 11,105
M 58
AR 12,129
AR 13,153
M 59
R 74
M 60
mw 51
AD 10,6
AD 11,7
M 61
ad 84,435
W 0
M 62
AD 12,8
AD 13,9
M 63
AW 81,10
AW 105,11
M 64
AW 129,12
AW 153,13
M 65
W 1
UL 6,34
M 66
UH 7,34
UL 8,35
M 67
UH 9,35
AR 10,82
M 68
AR 11,106
AR 12,130
M 69
W 2
AR 13,154
M 70
AD 10,6
AD 11,7
M 71
WL 0
R 76
M 72
AD 12,8
AD 13,9
M 73
bc 48,9,64,56
AW 82,10
M 74
mh 8
AW 106,11
AW 130,12
M 75
ad 64,864
H 0
M 76
AW 154,13
UL 6,36
M 77
UH 7,36
UL 8,37
M 78
UH 9,37
AR 10,83
M 79
H 1
AR 11,107
M 80
AR 12,131
AR 13,155
M 81
AD 10,6
AD 11,7
M 82
AD 12,8
AD 13,9
M 83
R 78
M 84
H 2
AW 83,10
M 85
AW 107,11
AW 131,12
M 86
AW 155,13
M 87
M 88
H 3
M 89
M 90
M 91
M 92
bc 49,10,66,60
M 93
mh 12
M 94
ad 66,960
H 0
M 95
WL 2
R 80
M 96
M 97
M 98
H 1
M 99
M 100
M 101
M 102
M 103
M 104
M 105
M 106
M 107
WL 2
R 82
M 108
M 109
M 110
M 111
M 112
M 113
M 114
M 115
M 116
WV 24
M 117
WL 0
s_barrier
M 118
mw 72
M 119
R 0
M 120
ad 84,456
W 0
M 121
M 132
M 133
T 24,0
M 122
T 28,14
M 123
W 1
M 134
)"
R"(M 135
T 32,1
M 124
T 36,15
M 125
W 2
M 136
M 137
T 40,2
M 126
T 44,16
M 127
mw 75
M 138
ad 84,459
W 0
M 139
WL 8
T 48,3
M 128
T 52,17
M 129
ae 44,68,48
M 140
W 1
M 141
T 56,4
M 130
WL 8
T 60,18
M 131
R 2
M 142
W 2
M 143
T 64,5
M 0
WL 8
T 68,19
M 1
R 4
M 2
s_bitcmp1_b32 s50, 4
s_cselect_b32 s51, 24576, 0
M 3
v_add_u32 v52, s51, v3
XL 46,0
M 4
XL 48,768
M 5
XL 14,1536
M 6
M 7
WL 8
M 8
M 9
WL 2
M 10
XL 16,2304
M 11
R 6
M 12
M 13
M 14
M 15
M 16
M 17
M 18
M 19
M 20
M 21
M 22
M 23
WL 2
R 8
M 24
M 25
M 26
M 27
M 28
M 29
M 30
M 31
M 32
M 33
M 34
M 35
WL 2
R 10
M 36
M 37
M 38
M 39
M 40
M 41
M 42
M 43
M 44
WV 22
M 45
WL 0
s_barrier
M 46
mw 0
UL 6,38
UH 7,38
M 47
R 24
M 48
ad 84,480
W 0
M 49
UL 8,39
UH 9,39
M 50
AR 10,84
AR 11,108
M 51
AR 12,132
AR 13,156
M 52
W 1
AD 10,6
M 53
AD 11,7
AD 12,8
M 54
AD 13,9
AW 84,10
M 55
AW 108,11
AW 132,12
M 56
W 2
AW 156,13
M 57
UL 6,40
UH 7,40
M 58
UL 8,41
UH 9,41
M 59
R 26
M 60
mw 3
AR 10,85
AR 11,109
M 61
ad 84,483
W 0
M 62
AR 12,133
AR 13,157
M 63
AD 10,6
AD 11,7
M 64
AD 12,8
AD 13,9
M 65
W 1
AW 85,10
M 66
AW 109,11
AW 133,12
M 67
AW 157,13
UL 6,42
M 68
UH 7,42
UL 8,43
M 69
W 2
UH 9,43
M 70
AR 10,86
AR 11,110
M 71
WL 0
R 28
M 72
AR 12,134
AR 13,158
M 73
bc 48,10,64,56
AD 10,6
M 74
mh 0
AD 11,7
AD 12,8
M 75
ad 64,960
H 0
M 76
AD 13,9
AW 86,10
M 77
AW 110,11
AW 134,12
M 78
AW 158,13
UL 6,44
M 79
H 1
UH 7,44
M 80
UL 8,45
UH 9,45
M 81
AR 10,87
AR 11,111
M 82
AR 12,135
AR 13,159
M 83
R 30
M 84
H 2
AD 10,6
M 85
AD 11,7
AD 12,8
M 86
AD 13,9
AW 87,10
M 87
AW 111,11
AW 135,12
M 88
H 3
AW 159,13
M 89
M 90
M 91
M 92
bc 49,11,66,60
M 93
mh 4
M 94
ad 66,1056
H 0
M 95
WL 2
R 32
M 96
M 97
M 98
H 1
M 99
M 100
M 101
M 102
M 103
M 104
M 105
M 106
M 107
WL 2
R 34
M 108
M 109
M 110
M 111
M 112
M 113
M 114
M 115
M 116
WV 24
M 117
WL 0
s_barrier
M 118
mw 24
M 119
R 48
M 120
ad 84,504
W 0
M 121
M 132
M 133
T 24,6
M 122
T 28,20
M 123
W 1
M 134
M 135
T 32,7
M 124
T 36,21
M 125
W 2
M 136
M 137
T 40,8
M 126
T 44,22
M 127
mw 27
M 138
ad 84,507
W 0
M 139
WL 8
T 48,9
M 128
T 52,23
M 129
M 140
W 1
M 141
T 56,10
M 130
WL 8
T 60,24
M 131
R 50
M 142
W 2
M 143
T 64,11
M 0
WL 8
T 68,25
M 1
R 52
M 2
ae 44,68,48
M 3
M 4
s_bfe_u32 s51, s50, 0x10005
v_mad_i32_i24 v52, v51, s51, v50
XL 18,0
M 5
XL 20,768
M 6
XL 22,1536
M 7
WL 8
M 8
M 9
WL 2
M 10
M 11
R 54
M 12
XL 24,2304
M 13
M 14
M 15
M 16
M 17
M 18
M 19
M 20
M 21
M 22
M 23
WL 2
R 56
M 24
M 25
M 26
M 27
M 28
M 29
M 30
M 31
M 32
M 33
M 34
M 35
WL 2
R 58
M 36
M 37
M 38
M 39
M 40
M 41
M 42
M 43
M 44
WV 26
M 45
WL 0
s_barrier
M 46
mw 48
M 47
R 72
M 48
ad 84,528
W 0
M 49
WV 23
UL 6,46
UH 7,46
)"
R"(M 50
UL 8,47
UH 9,47
M 51
AR 10,88
AR 11,112
M 52
W 1
AR 12,136
M 53
AD 10,6
AD 11,7
M 54
AD 12,8
AD 56,9
M 55
AW 88,10
AW 112,11
M 56
W 2
AW 136,12
M 57
UL 6,48
UH 7,48
M 58
UL 8,49
UH 9,49
M 59
R 74
M 60
mw 51
AR 10,89
AR 11,113
M 61
ad 84,531
W 0
M 62
AR 12,137
AD 10,6
M 63
AD 11,7
AD 12,8
M 64
AD 57,9
AW 89,10
M 65
W 1
AW 113,11
M 66
AW 137,12
UL 6,14
M 67
UH 7,14
UL 8,15
M 68
UH 9,15
AR 10,90
M 69
W 2
AR 11,114
M 70
AR 12,138
AD 10,6
M 71
WL 0
R 76
M 72
AD 11,7
AD 12,8
M 73
bc 48,11,64,56
AD 58,9
M 74
mh 6
AW 90,10
AW 114,11
M 75
ad 64,1056
H 0
M 76
AW 138,12
UL 6,16
M 77
UH 7,16
UL 8,17
M 78
UH 9,17
AR 10,91
M 79
H 1
AR 11,115
M 80
AR 12,139
AD 10,6
M 81
AD 11,7
AD 12,8
M 82
AD 59,9
AW 91,10
M 83
R 78
M 84
H 2
AW 115,11
M 85
AW 139,12
M 86
M 87
M 88
H 3
M 89
M 90
M 91
M 92
bc 49,12,66,60
M 93
mh 10
M 94
ad 66,1152
H 0
M 95
WL 2
R 80
M 96
M 97
M 98
H 1
M 99
M 100
M 101
M 102
M 103
M 104
M 105
M 106
M 107
WL 2
R 82
M 108
M 109
M 110
M 111
M 112
M 113
M 114
M 115
M 116
WV 24
M 117
WL 0
s_barrier
M 118
mw 72
M 119
R 0
M 120
ad 84,552
W 0
M 121
M 132
M 133
T 24,12
M 122
T 28,26
M 123
W 1
M 134
M 135
T 32,13
M 124
T 36,27
M 125
W 2
M 136
M 137
T 40,0
M 126
T 44,14
M 127
mw 75
M 138
ad 84,555
W 0
M 139
WL 8
T 48,1
M 128
T 52,15
M 129
M 140
W 1
M 141
T 56,2
M 130
WL 8
T 60,16
M 131
R 2
M 142
W 2
M 143
T 64,3
M 0
WL 8
T 68,17
M 1
R 4
M 2
M 3
M 4
M 5
M 6
M 7
WL 8
M 8
M 9
WL 2
M 10
M 11
R 6
M 12
M 13
M 14
M 15
M 16
M 17
M 18
M 19
M 20
M 21
M 22
M 23
WL 2
R 8
M 24
M 25
M 26
M 27
M 28
M 29
M 30
M 31
M 32
ae 44,70,0
M 33
s_bitcmp1_b32 s50, 0
s_cselect_b32 s51, 24576, 0
v_add_u32 v52, s51, v3
M 34
XL 26,128
M 35
WL 2
R 10
M 36
M 37
M 38
XL 28,896
M 39
M 40
M 41
M 42
M 43
WV 24
M 44
WL 0
XL 30,1664
M 45
s_barrier
M 46
mw 0
M 47
R 24
M 48
ad 84,576
W 0
M 49
M 50
WV 22
XL 32,2432
UL 6,18
M 51
UH 7,18
UL 8,19
M 52
W 1
UH 9,19
M 53
AR 10,92
AR 11,116
M 54
AR 12,140
AD 10,6
M 55
AD 11,7
AD 12,8
M 56
W 2
AD 60,9
M 57
AW 92,10
AW 116,11
M 58
AW 140,12
UL 6,20
M 59
R 26
M 60
mw 3
UH 7,20
UL 8,21
M 61
ad 84,579
W 0
M 62
UH 9,21
AR 10,93
M 63
AR 11,117
AR 12,141
M 64
AD 10,6
AD 11,7
M 65
W 1
AD 12,8
M 66
AD 61,9
AW 93,10
M 67
AW 117,11
AW 141,12
M 68
UL 6,22
UH 7,22
M 69
W 2
UL 8,23
M 70
UH 9,23
AR 10,94
M 71
WL 0
R 28
M 72
AR 11,118
AR 12,142
M 73
bc 48,12,64,56
AD 10,6
M 74
mh 12
AD 11,7
AD 12,8
M 75
ad 64,1152
H 0
M 76
AD 62,9
AW 94,10
M 77
AW 118,11
AW 142,12
M 78
UL 6,24
UH 7,24
M 79
H 1
UL 8,25
M 80
UH 9,25
AR 10,95
M 81
AR 11,119
AR 12,143
M 82
AD 10,6
AD 11,7
M 83
R 30
M 84
mh 0
AD 12,8
AD 63,9
M 85
ad 64,1154
H 0
M 86
AW 95,10
AW 119,11
M 87
AW 143,12
M 88
M 89
H 1
M 90
M 91
M 92
M 93
bc 49,13,66,60
)"
R"(M 94
mh 2
M 95
WL 2
R 32
M 96
ad 66,1248
H 0
M 97
M 98
M 99
M 100
H 1
M 101
M 102
M 103
M 104
M 105
M 106
M 107
WL 2
R 34
M 108
M 109
M 110
M 111
M 112
M 113
M 114
M 115
M 116
WV 24
M 117
WL 0
s_barrier
M 118
mw 24
M 119
R 48
M 120
ad 84,600
W 0
M 121
M 132
M 133
T 24,4
M 122
T 28,18
M 123
W 1
M 134
M 135
T 32,5
M 124
T 36,19
M 125
W 2
M 136
M 137
T 40,6
M 126
T 44,20
M 127
mw 27
M 138
ad 84,603
W 0
M 139
WL 8
T 48,7
M 128
T 52,21
M 129
M 140
W 1
M 141
T 56,8
M 130
WL 8
T 60,22
M 131
R 50
M 142
W 2
M 143
T 64,9
M 0
WL 8
T 68,23
M 1
R 52
M 2
M 3
M 4
M 5
M 6
M 7
WL 8
M 8
M 9
WL 2
M 10
M 11
R 54
M 12
M 13
M 14
M 15
M 16
M 17
M 18
M 19
M 20
M 21
M 22
M 23
WL 2
R 56
M 24
M 25
M 26
M 27
M 28
M 29
M 30
M 31
M 32
M 33
M 34
ae 44,70,0
M 35
WL 2
R 58
M 36
s_bfe_u32 s51, s50, 0x10001
v_mad_i32_i24 v52, v51, s51, v50
XL 34,128
M 37
M 38
M 39
M 40
XL 36,896
M 41
M 42
M 43
WV 24
M 44
WL 0
M 45
s_barrier
M 46
mw 48
XL 38,1664
M 47
R 72
M 48
ad 84,624
W 0
M 49
M 50
M 51
M 52
W 1
XL 40,2432
M 53
M 54
M 55
M 56
W 2
M 57
M 58
M 59
R 74
M 60
mw 51
M 61
ad 84,627
W 0
M 62
M 63
M 64
M 65
W 1
M 66
M 67
M 68
M 69
W 2
M 70
M 71
WL 2
R 76
M 72
M 73
bc 48,13,64,56
M 74
mh 4
M 75
ad 64,1248
H 0
M 76
M 77
M 78
M 79
H 1
M 80
M 81
M 82
M 83
WL 2
R 78
M 84
H 2
M 85
M 86
M 87
M 88
WV 32
H 3
UL 6,26
M 89
UH 7,26
UL 8,27
M 90
UH 9,27
AD 64,6
M 91
AD 88,7
AD 112,8
M 92
bc 49,14,66,60
AD 136,9
M 93
mh 8
UL 6,28
UH 7,28
M 94
ad 66,1344
H 0
M 95
WL 2
R 80
M 96
UL 8,29
UH 9,29
M 97
AD 65,6
AD 89,7
M 98
H 1
AD 113,8
M 99
AD 137,9
UL 6,30
M 100
UH 7,30
UL 8,31
M 101
UH 9,31
AD 66,6
M 102
AD 90,7
AD 114,8
M 103
WV 33
AD 138,9
UL 6,32
M 104
UH 7,32
UL 8,33
M 105
UH 9,33
AD 67,6
M 106
AD 91,7
AD 115,8
M 107
WL 2
R 82
M 108
AD 139,9
M 109
M 110
M 111
M 112
M 113
M 114
M 115
WV 24
M 116
WL 0
M 117
s_barrier
M 118
mw 72
M 119
R 0
M 120
ad 84,648
W 0
M 121
M 132
M 133
T 24,10
M 122
T 28,24
M 123
W 1
M 134
M 135
T 32,11
M 124
T 36,25
M 125
W 2
M 136
M 137
T 40,12
M 126
T 44,26
M 127
mw 75
M 138
ad 84,651
W 0
M 139
WL 8
T 48,13
M 128
T 52,27
M 129
M 140
W 1
M 141
T 56,0
M 130
WL 8
T 60,14
M 131
R 2
M 142
W 2
M 143
T 64,1
M 0
WL 8
T 68,15
M 1
R 4
M 2
M 3
M 4
M 5
M 6
M 7
WL 8
M 8
M 9
WL 2
M 10
M 11
R 6
M 12
M 13
M 14
M 15
M 16
M 17
M 18
M 19
M 20
M 21
M 22
M 23
WL 2
R 8
M 24
M 25
M 26
M 27
M 28
M 29
M 30
M 31
M 32
M 33
M 34
M 35
WL 2
R 10
M 36
ae 44,68,0
M 37
s_bitcmp1_b32 s50, 2
s_cselect_b32 s51, 24576, 0
v_add_u32 v52, s51, v3
M 38
XL 42,128
M 39
M 40
)"
R"(M 41
M 42
XL 44,896
M 43
WV 24
M 44
WL 0
M 45
s_barrier
M 46
mw 0
M 47
R 24
M 48
ad 84,672
W 0
M 49
XL 46,1664
M 50
M 51
M 52
W 1
M 53
M 54
XL 48,2432
M 55
M 56
W 2
M 57
M 58
M 59
R 26
M 60
mw 3
M 61
ad 84,675
W 0
M 62
M 63
M 64
M 65
W 1
M 66
M 67
M 68
M 69
W 2
M 70
M 71
WL 2
R 28
M 72
M 73
bc 48,14,64,56
M 74
mh 10
M 75
ad 64,1344
H 0
M 76
M 77
M 78
M 79
H 1
M 80
M 81
M 82
M 83
WL 2
R 30
M 84
H 2
M 85
M 86
M 87
M 88
H 3
M 89
M 90
WV 33
UL 6,34
UH 7,34
M 91
UL 8,35
UH 9,35
M 92
bc 49,15,66,60
AD 68,6
M 93
mh 0
AD 92,7
AD 116,8
M 94
ad 66,1440
H 0
M 95
WL 2
R 32
M 96
AD 140,9
UL 6,36
M 97
UH 7,36
UL 8,37
M 98
H 1
UH 9,37
M 99
AD 69,6
AD 93,7
M 100
AD 117,8
AD 141,9
M 101
UL 6,38
UH 7,38
M 102
UL 8,39
UH 9,39
M 103
AD 70,6
AD 94,7
M 104
AD 118,8
AD 142,9
M 105
WV 32
UL 6,40
UH 7,40
M 106
UL 8,41
UH 9,41
M 107
WL 2
R 34
M 108
AD 71,6
AD 95,7
M 109
AD 119,8
AD 143,9
M 110
M 111
M 112
M 113
M 114
M 115
WV 24
M 116
WL 0
M 117
s_barrier
M 118
mw 24
M 119
R 48
M 120
ad 84,696
W 0
M 121
M 132
M 133
T 24,2
M 122
T 28,16
M 123
W 1
M 134
M 135
T 32,3
M 124
T 36,17
M 125
W 2
M 136
M 137
T 40,4
M 126
T 44,18
M 127
mw 27
M 138
ad 84,699
W 0
M 139
WL 8
T 48,5
M 128
T 52,19
M 129
M 140
W 1
M 141
T 56,6
M 130
WL 8
T 60,20
M 131
R 50
M 142
W 2
M 143
T 64,7
M 0
WL 8
T 68,21
M 1
R 52
M 2
M 3
M 4
M 5
M 6
M 7
WL 8
M 8
M 9
WL 2
M 10
M 11
R 54
M 12
M 13
M 14
M 15
M 16
M 17
M 18
M 19
M 20
M 21
M 22
M 23
WL 2
R 56
M 24
M 25
M 26
M 27
M 28
M 29
M 30
M 31
M 32
M 33
M 34
M 35
WL 2
R 58
M 36
M 37
M 38
ae 44,68,0
M 39
s_bfe_u32 s51, s50, 0x10003
v_mad_i32_i24 v52, v51, s51, v50
XL 14,128
M 40
M 41
M 42
M 43
WV 23
M 44
WL 0
XL 16,896
M 45
s_barrier
M 46
mw 48
M 47
R 72
M 48
ad 84,720
W 0
M 49
M 50
XL 18,1664
M 51
M 52
W 1
M 53
M 54
M 55
M 56
W 2
XL 20,2432
M 57
M 58
M 59
R 74
M 60
mw 51
M 61
ad 84,723
W 0
M 62
M 63
M 64
M 65
W 1
M 66
M 67
M 68
M 69
W 2
M 70
M 71
WL 2
R 76
M 72
M 73
bc 48,15,64,56
M 74
mh 2
M 75
ad 64,1440
H 0
M 76
M 77
M 78
M 79
H 1
M 80
M 81
M 82
M 83
WL 2
R 78
M 84
H 2
M 85
M 86
M 87
M 88
H 3
M 89
M 90
M 91
WV 34
M 92
bc 49,16,66,60
UL 6,42
M 93
mh 6
UH 7,42
UL 8,43
M 94
ad 66,1536
H 0
M 95
WL 2
R 80
M 96
UH 9,43
AD 72,6
M 97
AD 96,7
AD 120,8
M 98
H 1
AD 144,9
M 99
UL 6,44
UH 7,44
M 100
UL 8,45
UH 9,45
M 101
AD 73,6
AD 97,7
M 102
AD 121,8
AD 145,9
M 103
WV 34
UL 6,46
UH 7,46
M 104
UL 8,47
UH 9,47
M 105
AD 74,6
AD 98,7
M 106
AD 122,8
AD 146,9
M 107
WL 2
R 82
M 108
WV 32
UL 6,48
UH 7,48
M 109
UL 8,49
UH 9,49
M 110
AD 75,6
AD 99,7
M 111
)"
R"(AD 123,8
AD 147,9
M 112
M 113
M 114
M 115
WV 24
M 116
WL 0
M 117
s_barrier
M 118
mw 72
M 119
R 0
M 120
ad 84,744
W 0
M 121
M 132
M 133
T 24,8
M 122
T 28,22
M 123
W 1
M 134
M 135
T 32,9
M 124
T 36,23
M 125
W 2
M 136
M 137
T 40,10
M 126
T 44,24
M 127
mw 75
M 138
ad 84,747
W 0
M 139
WL 8
T 48,11
M 128
T 52,25
M 129
M 140
W 1
M 141
T 56,12
M 130
WL 8
T 60,26
M 131
R 2
M 142
W 2
M 143
T 64,13
M 0
WL 8
T 68,27
M 1
R 4
M 2
M 3
M 4
M 5
M 6
M 7
WL 8
M 8
M 9
WL 2
M 10
M 11
R 6
M 12
M 13
M 14
M 15
M 16
M 17
M 18
M 19
M 20
M 21
M 22
M 23
WL 2
R 8
M 24
M 25
M 26
M 27
M 28
M 29
M 30
M 31
M 32
M 33
M 34
M 35
WL 2
R 10
M 36
M 37
M 38
M 39
M 40
ae 44,68,48
M 41
s_bitcmp1_b32 s50, 4
s_cselect_b32 s51, 24576, 0
v_add_u32 v52, s51, v3
M 42
XL 22,128
M 43
WV 23
M 44
WL 0
M 45
s_barrier
M 46
mw 0
XL 24,896
M 47
R 24
M 48
ad 84,768
W 0
M 49
M 50
M 51
M 52
W 1
XL 26,1664
M 53
M 54
M 55
M 56
W 2
M 57
M 58
XL 28,2432
M 59
R 26
M 60
mw 3
M 61
ad 84,771
W 0
M 62
M 63
M 64
M 65
W 1
M 66
M 67
M 68
M 69
W 2
M 70
M 71
WL 2
R 28
M 72
M 73
bc 48,16,64,56
M 74
mh 8
M 75
ad 64,1536
H 0
M 76
M 77
M 78
M 79
H 1
M 80
M 81
M 82
M 83
WL 2
R 30
M 84
H 2
M 85
M 86
M 87
M 88
H 3
M 89
M 90
M 91
M 92
bc 49,17,66,60
M 93
mh 12
M 94
ad 66,1632
H 0
M 95
WL 2
R 32
M 96
WV 35
UL 6,14
UH 7,14
M 97
UL 8,15
UH 9,15
M 98
H 1
AD 76,6
M 99
AD 100,7
AD 124,8
M 100
AD 148,9
UL 6,16
M 101
UH 7,16
UL 8,17
M 102
UH 9,17
AD 77,6
M 103
AD 101,7
AD 125,8
M 104
WV 34
AD 149,9
UL 6,18
M 105
UH 7,18
UL 8,19
M 106
UH 9,19
AD 78,6
M 107
WL 2
R 34
M 108
AD 102,7
AD 126,8
M 109
WV 31
AD 150,9
UL 6,20
M 110
UH 7,20
UL 8,21
M 111
UH 9,21
AD 79,6
M 112
AD 103,7
AD 127,8
M 113
AD 151,9
M 114
M 115
WV 24
M 116
WL 0
M 117
s_barrier
M 118
mw 24
M 119
R 48
M 120
ad 84,792
W 0
M 121
M 132
M 133
T 24,0
M 122
T 28,14
M 123
W 1
M 134
M 135
T 32,1
M 124
T 36,15
M 125
W 2
M 136
M 137
T 40,2
M 126
T 44,16
M 127
mw 27
M 138
ad 84,795
W 0
M 139
WL 8
T 48,3
M 128
T 52,17
M 129
M 140
W 1
M 141
T 56,4
M 130
WL 8
T 60,18
M 131
R 50
M 142
W 2
M 143
T 64,5
M 0
WL 8
T 68,19
M 1
R 52
M 2
M 3
M 4
M 5
M 6
M 7
WL 8
M 8
M 9
WL 2
M 10
M 11
R 54
M 12
M 13
M 14
M 15
M 16
M 17
M 18
M 19
M 20
M 21
M 22
M 23
WL 2
R 56
M 24
M 25
M 26
M 27
M 28
M 29
M 30
M 31
M 32
M 33
M 34
M 35
WL 2
R 58
M 36
M 37
M 38
M 39
M 40
M 41
M 42
ae 44,68,48
s_bfe_u32 s51, s50, 0x10005
M 43
WV 22
v_mad_i32_i24 v52, v51, s51, v50
XL 30,128
M 44
WL 0
M 45
s_barrier
M 46
mw 48
M 47
R 72
M 48
ad 84,816
W 0
M 49
XL 32,896
M 50
M 51
M 52
W 1
M 53
M 54
)"
R"(XL 34,1664
M 55
M 56
W 2
M 57
M 58
M 59
R 74
M 60
mw 51
XL 36,2432
M 61
ad 84,819
W 0
M 62
M 63
M 64
M 65
W 1
M 66
M 67
M 68
M 69
W 2
M 70
M 71
WL 2
R 76
M 72
M 73
bc 48,17,64,56
M 74
mh 0
M 75
ad 64,1632
H 0
M 76
M 77
M 78
M 79
H 1
M 80
M 81
M 82
M 83
WL 2
R 78
M 84
H 2
M 85
M 86
M 87
M 88
H 3
M 89
M 90
M 91
M 92
bc 49,18,66,60
M 93
mh 4
M 94
ad 66,1728
H 0
M 95
WL 2
R 80
M 96
WV 35
UL 6,22
UH 7,22
M 97
UL 8,23
UH 9,23
M 98
H 1
AD 80,6
M 99
AD 104,7
AD 128,8
M 100
AD 152,9
UL 6,24
M 101
UH 7,24
UL 8,25
M 102
UH 9,25
AD 81,6
M 103
AD 105,7
AD 129,8
M 104
WV 33
AD 153,9
UL 6,26
M 105
UH 7,26
UL 8,27
M 106
UH 9,27
AD 82,6
M 107
WL 2
R 82
M 108
AD 106,7
AD 130,8
M 109
WV 31
AD 154,9
UL 6,28
M 110
UH 7,28
UL 8,29
M 111
UH 9,29
AD 83,6
M 112
AD 107,7
AD 131,8
M 113
AD 155,9
M 114
M 115
WV 24
M 116
WL 0
M 117
s_barrier
M 118
mw 72
M 119
R 0
M 120
ad 84,840
W 0
M 121
M 132
M 133
T 24,6
M 122
T 28,20
M 123
W 1
M 134
M 135
T 32,7
M 124
T 36,21
M 125
W 2
M 136
M 137
T 40,8
M 126
T 44,22
M 127
mw 75
M 138
ad 84,843
W 0
M 139
WL 8
T 48,9
M 128
T 52,23
M 129
M 140
W 1
M 141
T 56,10
M 130
WL 8
T 60,24
M 131
R 2
M 142
W 2
M 143
T 64,11
M 0
WL 8
T 68,25
M 1
R 4
M 2
M 3
M 4
M 5
M 6
M 7
WL 8
M 8
M 9
WL 2
M 10
M 11
R 6
M 12
M 13
M 14
M 15
M 16
M 17
M 18
M 19
M 20
M 21
M 22
M 23
WL 2
R 8
M 24
M 25
M 26
M 27
M 28
M 29
M 30
M 31
M 32
M 33
M 34
M 35
WL 2
R 10
M 36
M 37
M 38
M 39
M 40
M 41
M 42
M 43
M 44
WV 22
M 45
WL 0
s_barrier
M 46
mw 0
M 47
R 24
M 48
ad 84,864
W 0
M 49
M 50
M 51
M 52
W 1
M 53
M 54
M 55
M 56
W 2
M 57
M 58
M 59
R 26
M 60
mw 3
M 61
ad 84,867
W 0
M 62
M 63
M 64
M 65
W 1
M 66
M 67
M 68
M 69
W 2
M 70
ae 44,70,0
M 71
WL 2
R 28
M 72
s_bitcmp1_b32 s50, 0
s_cselect_b32 s51, 24576, 0
v_add_u32 v52, s51, v3
M 73
bc 48,18,64,56
XL 38,256
M 74
mh 6
M 75
ad 64,1728
H 0
M 76
XL 40,1024
M 77
M 78
M 79
H 1
M 80
M 81
M 82
XL 42,1792
M 83
WL 2
R 30
M 84
H 2
M 85
M 86
M 87
M 88
H 3
XL 44,2560
M 89
M 90
M 91
M 92
bc 49,19,66,60
M 93
mh 10
M 94
ad 66,1824
H 0
M 95
WL 2
R 32
M 96
M 97
M 98
WV 36
H 1
UL 6,30
M 99
UH 7,30
UL 8,31
M 100
UH 9,31
AD 84,6
M 101
AD 108,7
AD 132,8
M 102
WV 35
AD 156,9
UL 6,32
M 103
UH 7,32
UL 8,33
M 104
UH 9,33
AD 85,6
M 105
AD 109,7
AD 133,8
M 106
WV 33
AD 157,9
UL 6,34
M 107
WL 2
R 34
M 108
UH 7,34
UL 8,35
M 109
UH 9,35
AD 86,6
M 110
AD 110,7
AD 134,8
M 111
WV 31
AD 158,9
UL 6,36
M 112
UH 7,36
UL 8,37
M 113
UH 9,37
AD 87,6
M 114
AD 111,7
AD 135,8
M 115
WV 24
AD 159,9
M 116
WL 0
M 117
s_barrier
M 118
mw 24
M 119
R 48
M 120
ad 84,888
W 0
M 121
M 132
)"
R"(M 133
T 24,12
M 122
T 28,26
M 123
W 1
M 134
M 135
T 32,13
M 124
T 36,27
M 125
W 2
M 136
M 137
T 40,0
M 126
T 44,14
M 127
mw 27
M 138
ad 84,891
W 0
M 139
WL 8
T 48,1
M 128
T 52,15
M 129
M 140
W 1
M 141
T 56,2
M 130
WL 8
T 60,16
M 131
R 50
M 142
W 2
M 143
T 64,3
M 0
WL 8
T 68,17
M 1
R 52
M 2
M 3
M 4
M 5
M 6
M 7
WL 8
M 8
M 9
WL 2
M 10
M 11
R 54
M 12
M 13
M 14
M 15
M 16
M 17
M 18
M 19
M 20
M 21
M 22
M 23
WL 2
R 56
M 24
M 25
M 26
M 27
M 28
M 29
M 30
M 31
M 32
M 33
M 34
M 35
WL 2
R 58
M 36
M 37
M 38
M 39
M 40
M 41
M 42
M 43
M 44
WV 22
M 45
WL 0
s_barrier
M 46
mw 48
M 47
R 72
M 48
ad 84,912
W 0
M 49
M 50
M 51
M 52
W 1
M 53
M 54
M 55
M 56
W 2
M 57
M 58
M 59
R 74
M 60
mw 51
M 61
ad 84,915
W 0
M 62
M 63
M 64
M 65
W 1
M 66
M 67
M 68
M 69
W 2
M 70
M 71
WL 2
R 76
M 72
M 73
bc 48,19,64,56
M 74
mh 12
M 75
ad 64,1824
H 0
M 76
ae 44,70,0
M 77
s_bfe_u32 s51, s50, 0x10001
v_mad_i32_i24 v52, v51, s51, v50
XL 46,256
M 78
M 79
H 1
M 80
XL 48,1024
M 81
M 82
M 83
WL 2
R 78
M 84
mh 0
M 85
ad 64,1826
H 0
M 86
XL 14,1792
M 87
M 88
M 89
H 1
M 90
M 91
M 92
XL 16,2560
M 93
bc 49,20,66,60
M 94
mh 2
M 95
WL 2
R 80
M 96
ad 66,1920
H 0
M 97
M 98
M 99
M 100
H 1
M 101
M 102
M 103
M 104
M 105
M 106
M 107
WL 2
R 82
M 108
M 109
M 110
M 111
M 112
M 113
M 114
M 115
M 116
WV 25
M 117
WL 0
s_barrier
M 118
mw 72
M 119
R 0
M 120
ad 84,936
W 0
M 121
M 132
M 133
T 24,4
M 122
T 28,18
M 123
W 1
M 134
UL 6,38
UH 7,38
M 135
T 32,5
M 124
T 36,19
M 125
W 2
UL 8,39
M 136
UH 9,39
AD 160,6
M 137
T 40,6
M 126
T 44,20
M 127
mw 75
AD 184,7
AD 208,8
M 138
ad 84,939
W 0
M 139
WL 8
T 48,7
M 128
T 52,21
M 129
AD 232,9
UL 6,40
M 140
W 1
UH 7,40
M 141
T 56,8
M 130
WL 8
T 60,22
M 131
R 2
M 142
W 2
UL 8,41
M 143
T 64,9
M 0
WL 8
T 68,23
M 1
R 4
M 2
UH 9,41
AD 161,6
M 3
AD 185,7
AD 209,8
M 4
AD 233,9
UL 6,42
M 5
UH 7,42
UL 8,43
M 6
UH 9,43
AD 162,6
M 7
WL 8
AD 186,7
AD 210,8
M 8
WV 30
AD 234,9
UL 6,44
M 9
WL 2
UH 7,44
UL 8,45
M 10
UH 9,45
AD 163,6
M 11
R 6
M 12
AD 187,7
AD 211,8
M 13
AD 235,9
M 14
M 15
M 16
M 17
M 18
M 19
M 20
M 21
M 22
M 23
WL 2
R 8
M 24
M 25
M 26
M 27
M 28
M 29
M 30
M 31
M 32
M 33
M 34
M 35
WL 2
R 10
M 36
M 37
M 38
M 39
M 40
M 41
M 42
M 43
M 44
WV 22
M 45
WL 0
s_barrier
M 46
mw 0
M 47
R 24
M 48
ad 84,960
W 0
M 49
M 50
M 51
M 52
W 1
M 53
M 54
M 55
M 56
W 2
M 57
M 58
M 59
R 26
M 60
mw 3
M 61
ad 84,963
W 0
M 62
M 63
M 64
M 65
W 1
M 66
M 67
M 68
M 69
W 2
M 70
M 71
WL 2
R 28
)"
R"(M 72
M 73
bc 48,20,64,56
M 74
mh 4
M 75
ad 64,1920
H 0
M 76
M 77
M 78
ae 44,68,0
M 79
H 1
M 80
s_bitcmp1_b32 s50, 2
s_cselect_b32 s51, 24576, 0
v_add_u32 v52, s51, v3
M 81
XL 18,256
M 82
M 83
WL 2
R 30
M 84
H 2
XL 20,1024
M 85
M 86
M 87
M 88
H 3
M 89
M 90
XL 22,1792
M 91
M 92
bc 49,21,66,60
M 93
mh 8
M 94
ad 66,2016
H 0
M 95
WL 2
R 32
M 96
XL 24,2560
M 97
M 98
H 1
M 99
M 100
M 101
M 102
M 103
M 104
M 105
M 106
M 107
WL 2
R 34
M 108
M 109
M 110
M 111
M 112
M 113
M 114
M 115
M 116
WV 25
M 117
WL 0
s_barrier
M 118
mw 24
M 119
R 48
M 120
ad 84,984
W 0
M 121
M 132
M 133
T 24,10
M 122
T 28,24
M 123
W 1
M 134
M 135
T 32,11
M 124
T 36,25
M 125
W 2
M 136
UL 6,46
UH 7,46
M 137
T 40,12
M 126
T 44,26
M 127
mw 27
UL 8,47
UH 9,47
M 138
ad 84,987
W 0
M 139
WL 8
T 48,13
M 128
T 52,27
M 129
AD 164,6
AD 188,7
M 140
W 1
AD 212,8
M 141
T 56,0
M 130
WL 8
T 60,14
M 131
R 50
M 142
W 2
AD 236,9
M 143
T 64,1
M 0
WL 8
T 68,15
M 1
R 52
M 2
UL 6,48
UH 7,48
M 3
UL 8,49
UH 9,49
M 4
AD 165,6
AD 189,7
M 5
AD 213,8
AD 237,9
M 6
UL 6,14
UH 7,14
M 7
WL 8
UL 8,15
UH 9,15
M 8
AD 166,6
AD 190,7
M 9
WL 2
AD 214,8
AD 238,9
M 10
WV 30
UL 6,16
UH 7,16
M 11
R 54
M 12
UL 8,17
UH 9,17
M 13
AD 167,6
AD 191,7
M 14
AD 215,8
AD 239,9
M 15
M 16
M 17
M 18
M 19
M 20
M 21
M 22
M 23
WL 2
R 56
M 24
M 25
M 26
M 27
M 28
M 29
M 30
M 31
M 32
M 33
M 34
M 35
WL 2
R 58
M 36
M 37
M 38
M 39
M 40
M 41
M 42
M 43
M 44
WV 22
M 45
WL 0
s_barrier
M 46
mw 48
M 47
R 72
M 48
ad 84,1008
W 0
M 49
M 50
M 51
M 52
W 1
M 53
M 54
M 55
M 56
W 2
M 57
M 58
M 59
R 74
M 60
mw 51
M 61
ad 84,1011
W 0
M 62
M 63
M 64
M 65
W 1
M 66
M 67
M 68
M 69
W 2
M 70
M 71
WL 2
R 76
M 72
M 73
bc 48,21,64,56
M 74
mh 10
M 75
ad 64,2016
H 0
M 76
M 77
M 78
M 79
H 1
M 80
M 81
M 82
ae 44,68,0
M 83
WL 2
R 78
M 84
s_bfe_u32 s51, s50, 0x10003
H 2
v_mad_i32_i24 v52, v51, s51, v50
M 85
XL 26,256
M 86
M 87
M 88
H 3
XL 28,1024
M 89
M 90
M 91
M 92
bc 49,22,66,60
M 93
mh 0
M 94
ad 66,2112
H 0
M 95
WL 2
R 80
M 96
XL 30,1792
M 97
M 98
H 1
M 99
M 100
XL 32,2560
M 101
M 102
M 103
M 104
M 105
M 106
M 107
WL 2
R 82
M 108
M 109
M 110
M 111
M 112
M 113
M 114
M 115
M 116
WV 26
M 117
WL 0
s_barrier
M 118
mw 72
M 119
R 0
M 120
ad 84,1032
W 0
M 121
M 132
M 133
T 24,2
M 122
T 28,16
M 123
W 1
M 134
M 135
T 32,3
M 124
T 36,17
M 125
W 2
M 136
M 137
T 40,4
M 126
T 44,18
M 127
mw 75
M 138
ad 84,1035
W 0
M 139
WL 8
T 48,5
M 128
T 52,19
M 129
UL 6,18
UH 7,18
M 140
W 1
UL 8,19
M 141
T 56,6
M 130
WL 8
T 60,20
M 131
R 2
M 142
W 2
UH 9,19
M 143
T 64,7
M 0
WL 8
T 68,21
M 1
R 4
M 2
AD 168,6
AD 192,7
M 3
AD 216,8
AD 240,9
M 4
UL 6,20
)"
R"(UH 7,20
M 5
UL 8,21
UH 9,21
M 6
AD 169,6
AD 193,7
M 7
WL 8
AD 217,8
AD 241,9
M 8
WV 31
UL 6,22
UH 7,22
M 9
WL 2
UL 8,23
UH 9,23
M 10
AD 170,6
AD 194,7
M 11
R 6
M 12
AD 218,8
AD 242,9
M 13
WV 29
UL 6,24
UH 7,24
M 14
UL 8,25
UH 9,25
M 15
AD 171,6
AD 195,7
M 16
AD 219,8
AD 243,9
M 17
M 18
M 19
M 20
M 21
M 22
M 23
WL 2
R 8
M 24
M 25
M 26
M 27
M 28
M 29
M 30
M 31
M 32
M 33
M 34
M 35
WL 2
R 10
M 36
M 37
M 38
M 39
M 40
M 41
M 42
M 43
M 44
WV 22
M 45
WL 0
s_barrier
M 46
mw 0
M 47
R 24
M 48
ad 84,1056
W 0
M 49
M 50
M 51
M 52
W 1
M 53
M 54
M 55
M 56
W 2
M 57
M 58
M 59
R 26
M 60
mw 3
M 61
ad 84,1059
W 0
M 62
M 63
M 64
M 65
W 1
M 66
M 67
M 68
M 69
W 2
M 70
M 71
WL 2
R 28
M 72
M 73
bc 48,22,64,56
M 74
mh 2
M 75
ad 64,2112
H 0
M 76
M 77
M 78
M 79
H 1
M 80
M 81
M 82
M 83
WL 2
R 30
M 84
H 2
M 85
M 86
ae 44,68,48
M 87
s_bitcmp1_b32 s50, 4
s_cselect_b32 s51, 24576, 0
v_add_u32 v52, s51, v3
M 88
H 3
XL 34,256
M 89
M 90
M 91
M 92
bc 49,23,66,60
XL 36,1024
M 93
mh 6
M 94
ad 66,2208
H 0
M 95
WL 2
R 32
M 96
M 97
M 98
H 1
XL 38,1792
M 99
M 100
M 101
M 102
M 103
M 104
XL 40,2560
M 105
M 106
M 107
WL 2
R 34
M 108
M 109
M 110
M 111
M 112
M 113
M 114
M 115
M 116
WV 27
M 117
WL 0
s_barrier
M 118
mw 24
M 119
R 48
M 120
ad 84,1080
W 0
M 121
M 132
M 133
T 24,8
M 122
T 28,22
M 123
W 1
M 134
M 135
T 32,9
M 124
T 36,23
M 125
W 2
M 136
M 137
T 40,10
M 126
T 44,24
M 127
mw 27
M 138
ad 84,1083
W 0
M 139
WL 8
T 48,11
M 128
T 52,25
M 129
M 140
W 1
UL 6,26
M 141
T 56,12
M 130
WL 8
T 60,26
M 131
R 50
M 142
W 2
UH 7,26
M 143
T 64,13
M 0
WL 8
T 68,27
M 1
R 52
M 2
UL 8,27
UH 9,27
M 3
AD 172,6
AD 196,7
M 4
AD 220,8
AD 244,9
M 5
WV 32
UL 6,28
UH 7,28
M 6
UL 8,29
UH 9,29
M 7
WL 8
AD 173,6
AD 197,7
M 8
WL 2
AD 221,8
AD 245,9
M 9
WV 30
UL 6,30
UH 7,30
M 10
UL 8,31
UH 9,31
M 11
R 54
M 12
AD 174,6
AD 198,7
M 13
AD 222,8
AD 246,9
M 14
WV 28
UL 6,32
UH 7,32
M 15
UL 8,33
UH 9,33
M 16
AD 175,6
AD 199,7
M 17
AD 223,8
AD 247,9
M 18
M 19
M 20
M 21
M 22
M 23
WL 2
R 56
M 24
M 25
M 26
M 27
M 28
M 29
M 30
M 31
M 32
M 33
M 34
M 35
WL 2
R 58
M 36
M 37
M 38
M 39
M 40
M 41
M 42
M 43
M 44
WV 22
M 45
WL 0
s_barrier
M 46
mw 48
M 47
R 72
M 48
ad 84,1104
W 0
M 49
M 50
M 51
M 52
W 1
M 53
M 54
M 55
M 56
W 2
M 57
M 58
M 59
R 74
M 60
mw 51
M 61
ad 84,1107
W 0
M 62
M 63
M 64
M 65
W 1
M 66
M 67
M 68
M 69
W 2
M 70
M 71
WL 2
R 76
M 72
M 73
bc 48,23,64,56
M 74
mh 8
M 75
ad 64,2208
H 0
M 76
M 77
M 78
M 79
H 1
M 80
M 81
M 82
M 83
WL 2
R 78
M 84
H 2
M 85
M 86
M 87
)"
R"(M 88
H 3
M 89
M 90
ae 44,68,48
M 91
s_bfe_u32 s51, s50, 0x10005
v_mad_i32_i24 v52, v51, s51, v50
XL 42,256
M 92
bc 48,0,64,56
M 93
mh 12
M 94
ad 64,0
H 0
M 95
WL 2
R 80
M 96
XL 44,1024
M 97
M 98
H 1
M 99
M 100
M 101
M 102
XL 46,1792
M 103
M 104
M 105
M 106
M 107
WL 2
R 82
M 108
XL 48,2560
M 109
M 110
M 111
M 112
M 113
M 114
M 115
WV 28
M 116
WL 0
M 117
s_barrier
M 118
mw 72
M 119
R 0
M 120
ad 84,1128
W 0
M 121
M 132
M 133
T 24,0
M 122
T 28,14
M 123
W 1
M 134
M 135
T 32,1
M 124
T 36,15
M 125
W 2
M 136
M 137
T 40,2
M 126
T 44,16
M 127
mw 75
M 138
ad 84,1131
W 0
M 139
WL 8
T 48,3
M 128
T 52,17
M 129
M 140
W 1
M 141
T 56,4
M 130
WL 8
T 60,18
M 131
R 2
M 142
WV 31
W 2
UL 6,34
M 143
T 64,5
M 0
WL 8
T 68,19
M 1
R 4
M 2
PA 12288
DF
UH 7,34
M 3
UL 8,35
UH 9,35
M 4
AD 176,6
AD 200,7
M 5
AD 224,8
AD 248,9
M 6
UL 6,36
UH 7,36
M 7
WL 8
UL 8,37
UH 9,37
M 8
AD 177,6
AD 201,7
M 9
WL 2
AD 225,8
AD 249,9
M 10
WV 29
UL 6,38
UH 7,38
M 11
R 6
M 12
UL 8,39
UH 9,39
M 13
AD 178,6
AD 202,7
M 14
AD 226,8
AD 250,9
M 15
UL 6,40
UH 7,40
M 16
UL 8,41
UH 9,41
M 17
AD 179,6
AD 203,7
M 18
AD 227,8
AD 251,9
M 19
M 20
M 21
M 22
M 23
WL 2
R 8
M 24
M 25
M 26
M 27
M 28
M 29
M 30
M 31
M 32
M 33
M 34
M 35
WL 2
R 10
M 36
M 37
M 38
M 39
M 40
M 41
M 42
M 43
M 44
WV 23
M 45
WL 0
s_barrier
M 46
mw 0
M 47
R 24
M 48
ad 84,0
W 0
M 49
M 50
M 51
M 52
W 1
M 53
M 54
M 55
M 56
W 2
M 57
M 58
M 59
R 26
M 60
mw 3
M 61
ad 84,3
W 0
M 62
M 63
M 64
M 65
W 1
M 66
M 67
M 68
M 69
W 2
M 70
M 71
WL 2
R 28
M 72
M 73
mh 0
M 74
ad 64,2
H 0
M 75
M 76
M 77
M 78
H 1
M 79
M 80
M 81
M 82
mh 2
M 83
WL 2
R 30
M 84
ad 64,16
H 0
M 85
M 86
M 87
M 88
H 1
M 89
M 90
M 91
M 92
bc 48,1,64,56
M 93
mh 4
M 94
ad 64,98
H 0
M 95
WL 2
R 32
M 96
M 97
M 98
H 1
M 99
M 100
M 101
M 102
M 103
M 104
M 105
M 106
M 107
WL 2
R 34
M 108
M 109
M 110
M 111
M 112
M 113
M 114
M 115
M 116
WV 25
M 117
WL 0
s_barrier
M 118
mw 24
M 119
R 48
M 120
ad 84,24
W 0
M 121
M 132
M 133
T 24,6
M 122
T 28,20
M 123
W 1
M 134
M 135
T 32,7
M 124
T 36,21
M 125
W 2
M 136
M 137
T 40,8
M 126
T 44,22
M 127
mw 27
M 138
ad 84,27
W 0
M 139
WL 8
T 48,9
M 128
T 52,23
M 129
M 140
W 1
M 141
T 56,10
M 130
WL 8
T 60,24
M 131
R 50
M 142
W 2
M 143
T 64,11
M 0
WL 8
T 68,25
M 1
R 52
M 2
WV 30
UL 6,42
UH 7,42
M 3
UL 8,43
UH 9,43
M 4
AD 180,6
AD 204,7
M 5
AD 228,8
AD 252,9
M 6
WV 28
UL 6,44
UH 7,44
M 7
WL 8
UL 8,45
UH 9,45
M 8
AD 181,6
AD 205,7
M 9
WL 2
AD 229,8
AD 253,9
M 10
WV 25
UL 6,46
UH 7,46
M 11
R 54
M 12
UL 8,47
UH 9,47
M 13
AD 182,6
AD 206,7
)"
R"(M 14
AD 230,8
AD 254,9
M 15
UL 6,48
UH 7,48
M 16
UL 8,49
UH 9,49
M 17
AD 183,6
AD 207,7
M 18
AD 231,8
AD 255,9
M 19
M 20
M 21
M 22
M 23
WL 2
R 56
M 24
M 25
M 26
M 27
M 28
M 29
M 30
M 31
M 32
M 33
M 34
M 35
WL 2
R 58
M 36
M 37
M 38
M 39
M 40
AR 6,72
AR 7,96
M 41
AR 8,120
s_nop 0
AR 9,144
M 42
EF 6
EF 7
M 43
WV 19
EF 8
EF 9
M 44
WL 0
CV 14,6,7
CV 15,8,9
M 45
s_barrier
AR 6,73
AR 7,97
M 46
mw 48
AR 8,121
AR 9,145
M 47
R 72
M 48
ad 84,48
W 0
M 49
EF 6
EF 7
M 50
EF 8
EF 9
M 51
CV 16,6,7
CV 17,8,9
M 52
W 1
AR 6,74
M 53
AR 7,98
AR 8,122
M 54
AR 9,146
EF 6
M 55
EF 7
EF 8
M 56
W 2
EF 9
M 57
CV 18,6,7
CV 19,8,9
M 58
AR 6,75
AR 7,99
M 59
R 74
M 60
mw 51
AR 8,123
AR 9,147
M 61
ad 84,51
W 0
M 62
EF 6
EF 7
M 63
EF 8
EF 9
M 64
CV 20,6,7
CV 21,8,9
M 65
W 1
BA 72,0
M 66
BA 96,256
BA 120,512
M 67
BA 144,768
M 68
ae 42,74,0
AR 6,76
M 69
W 2
AR 7,100
M 70
s_bitcmp1_b32 s50, 0
s_cselect_b32 s52, 24576, 0
v_add_u32 v53, s52, v3
M 71
WL 4
R 76
M 72
ST 14,0
AR 8,124
M 73
mh 6
AR 9,148
EF 6
M 74
ad 64,112
H 0
M 75
EF 7
EF 8
M 76
EF 9
CV 22,6,7
M 77
CV 23,8,9
AR 6,77
M 78
H 1
AR 7,101
M 79
AR 8,125
AR 9,149
M 80
EF 6
EF 7
M 81
EF 8
EF 9
M 82
bc 49,1,66,60
CV 24,6,7
M 83
R 78
M 84
mh 8
CV 25,8,9
AR 6,78
M 85
ad 66,112
H 0
M 86
AR 7,102
AR 8,126
M 87
AR 9,150
EF 6
M 88
EF 7
EF 8
M 89
H 1
EF 9
M 90
CV 26,6,7
CV 27,8,9
M 91
AR 6,79
AR 7,103
M 92
AR 8,127
AR 9,151
M 93
bc 48,2,64,56
EF 6
M 94
mh 10
EF 7
EF 8
M 95
WL 0
R 80
M 96
ad 64,208
H 0
M 97
EF 9
CV 28,6,7
M 98
CV 29,8,9
BA 76,0
M 99
BA 100,256
BA 124,512
M 100
H 1
BA 148,768
M 101
EF 64
EF 88
M 102
EF 112
EF 136
M 103
CV 30,64,88
CV 31,112,136
M 104
EF 65
EF 89
M 105
EF 113
EF 137
M 106
CV 32,65,89
CV 33,113,137
M 107
R 82
M 108
EF 66
EF 90
M 109
EF 114
EF 138
M 110
ST 16,768
CV 34,66,90
M 111
CV 35,114,138
EF 67
M 112
EF 91
EF 115
M 113
EF 139
CV 36,67,91
M 114
CV 37,115,139
BV 64,1024
M 115
WV 22
BV 88,1280
BV 112,1536
M 116
WL 3
BV 136,1792
M 117
s_barrier
EF 68
EF 92
M 118
mw 72
EF 116
EF 140
M 119
R 0
M 120
ad 84,72
W 0
M 121
CV 38,68,92
CV 39,116,140
M 132
EF 69
EF 93
M 133
T 24,2
M 122
T 28,16
M 123
W 1
EF 117
M 134
EF 141
CV 40,69,93
M 135
T 32,3
M 124
T 36,17
M 125
W 2
CV 41,117,141
M 136
EF 70
EF 94
M 137
WL 8
T 40,12
M 126
T 44,26
M 127
mw 75
EF 118
EF 142
M 138
ad 84,75
W 0
M 139
T 48,13
M 128
WL 8
T 52,27
M 129
CV 42,70,94
CV 43,118,142
M 140
W 1
EF 71
M 141
T 56,0
M 130
T 60,14
M 131
WL 8
R 2
M 142
W 2
EF 95
M 143
T 64,1
M 4
T 68,15
M 5
WL 8
R 4
M 6
EF 119
EF 143
M 7
WL 8
CV 44,71,95
CV 45,119,143
M 8
ST 18,1536
BV 68,1024
M 9
WL 3
BV 92,1280
BV 116,1536
M 10
BV 140,1792
M 11
EF 160
EF 184
M 0
EF 208
EF 232
M 1
CV 10,160,184
CV 11,208,232
M 2
AW 160,10
AW 161,11
M 3
R 6
M 16
EF 161
EF 185
M 17
EF 209
EF 233
M 18
CV 10,161,185
CV 11,209,233
M 19
AW 162,10
AW 163,11
M 20
EF 162
)"
R"(EF 186
M 21
EF 210
EF 234
M 22
CV 10,162,186
CV 11,210,234
M 23
AW 164,10
AW 165,11
M 12
EF 163
EF 187
M 13
EF 211
EF 235
M 14
CV 10,163,187
CV 11,211,235
M 15
WL 2
R 8
M 28
AW 166,10
AW 167,11
M 29
BV 160,2048
BV 184,2304
M 30
BV 208,2560
BV 232,2816
M 31
EF 164
EF 188
M 32
EF 212
EF 236
M 33
CV 10,164,188
CV 11,212,236
M 34
AW 168,10
AW 169,11
M 35
EF 165
EF 189
M 24
EF 213
EF 237
M 25
CV 10,165,189
CV 11,213,237
M 26
AW 170,10
AW 171,11
M 27
WL 6
R 10
M 40
EF 166
EF 190
M 41
EF 214
EF 238
M 42
CV 10,166,190
CV 11,214,238
M 43
AW 172,10
AW 173,11
M 44
EF 167
EF 191
M 45
EF 215
EF 239
M 46
ST 20,2304
CV 10,167,191
M 47
WV 22
CV 11,215,239
AW 174,10
M 36
WL 0
AW 175,11
BV 164,2048
M 37
s_barrier
BV 188,2304
BV 212,2560
M 38
mw 0
BV 236,2816
M 39
R 24
M 52
ad 84,96
W 0
M 53
AR 6,80
AR 7,104
M 54
AR 8,128
AR 9,152
M 55
EF 6
EF 7
M 56
W 1
EF 8
M 57
EF 9
CV 10,6,7
M 58
CV 11,8,9
AW 176,10
M 59
AW 177,11
AR 6,81
M 48
W 2
AR 7,105
M 49
AR 8,129
AR 9,153
M 50
EF 6
EF 7
M 51
R 26
M 64
mw 3
EF 8
EF 9
M 65
ad 84,99
W 0
M 66
CV 10,6,7
CV 11,8,9
M 67
AW 178,10
AW 179,11
M 68
AR 6,82
AR 7,106
M 69
W 1
AR 8,130
M 70
AR 9,154
EF 6
M 71
EF 7
EF 8
M 60
EF 9
CV 10,6,7
M 61
W 2
CV 11,8,9
M 62
AW 180,10
AW 181,11
M 63
WL 0
R 28
M 76
AR 6,83
AR 7,107
M 77
bc 49,2,66,60
AR 8,131
M 78
mh 12
AR 9,155
EF 6
M 79
ad 66,208
H 0
M 80
EF 7
EF 8
M 81
EF 9
CV 10,6,7
M 82
CV 11,8,9
AW 182,10
M 83
H 1
AW 183,11
M 72
ae 42,74,0
BA 80,0
M 73
s_bfe_u32 s52, s50, 0x10001
v_mad_i32_i24 v53, v51, s52, v50
ST 22,0
M 74
BA 104,256
BA 128,512
M 75
R 30
M 88
mh 0
BA 152,768
M 89
ad 64,210
H 0
M 90
AR 6,84
AR 7,108
M 91
AR 8,132
AR 9,156
M 92
EF 6
EF 7
M 93
H 1
EF 8
M 94
EF 9
CV 10,6,7
M 95
CV 11,8,9
AW 184,10
M 84
AW 185,11
AR 6,85
M 85
bc 48,3,64,56
AR 7,109
M 86
mh 2
AR 8,133
AR 9,157
M 87
WL 1
R 32
M 100
ad 64,304
H 0
M 101
EF 6
EF 7
M 102
EF 8
EF 9
M 103
CV 10,6,7
CV 11,8,9
M 104
H 1
AW 186,10
M 105
AW 187,11
AR 6,86
M 106
AR 7,110
AR 8,134
M 107
AR 9,158
EF 6
M 96
EF 7
EF 8
M 97
EF 9
CV 10,6,7
M 98
CV 11,8,9
AW 188,10
M 99
R 34
M 112
AW 189,11
AR 6,87
M 113
AR 7,111
AR 8,135
M 114
AR 9,159
EF 6
M 115
EF 7
EF 8
M 116
EF 9
CV 10,6,7
M 117
CV 11,8,9
AW 190,10
M 118
AW 191,11
BA 84,0
M 119
WV 24
BA 108,256
BA 132,512
M 108
WL 3
BA 156,768
M 109
s_barrier
EF 72
EF 96
M 110
mw 24
EF 120
EF 144
M 111
R 48
M 124
ad 84,120
W 0
M 125
ST 24,768
CV 10,72,96
M 136
CV 11,120,144
AW 192,10
M 137
T 40,8
M 126
T 44,22
M 127
W 1
AW 193,11
M 138
EF 73
EF 97
M 139
T 48,9
M 128
T 52,23
M 129
W 2
EF 121
M 140
EF 145
CV 10,73,97
M 141
WL 8
T 56,4
M 130
T 60,18
M 131
mw 27
CV 11,121,145
AW 194,10
M 142
ad 84,123
W 0
M 143
T 64,5
M 120
WL 8
T 68,19
M 121
AW 195,11
EF 74
M 132
W 1
EF 98
M 133
T 24,6
M 122
T 28,20
M 123
WL 8
R 50
M 134
W 2
EF 122
M 135
T 32,7
M 8
T 36,21
M 9
WL 8
R 52
M 10
EF 146
CV 10,74,98
M 11
WL 8
CV 11,122,146
AW 196,10
M 0
AW 197,11
EF 75
M 1
WL 2
EF 99
EF 123
M 2
EF 147
)"
R"(CV 10,75,99
M 3
CV 11,123,147
AW 198,10
M 4
AW 199,11
BV 72,1024
M 5
BV 96,1280
BV 120,1536
M 6
BV 144,1792
M 7
R 54
M 20
EF 76
EF 100
M 21
EF 124
EF 148
M 22
CV 10,76,100
CV 11,124,148
M 23
ST 26,1536
AW 200,10
M 12
AW 201,11
EF 77
M 13
EF 101
EF 125
M 14
EF 149
CV 10,77,101
M 15
CV 11,125,149
AW 202,10
M 16
AW 203,11
EF 78
M 17
EF 102
EF 126
M 18
EF 150
CV 10,78,102
M 19
WL 2
R 56
M 32
CV 11,126,150
AW 204,10
M 33
AW 205,11
EF 79
M 34
EF 103
EF 127
M 35
EF 151
CV 10,79,103
M 24
CV 11,127,151
AW 206,10
M 25
AW 207,11
BV 76,1024
M 26
BV 100,1280
BV 124,1536
M 27
BV 148,1792
M 28
EF 168
EF 192
M 29
EF 216
EF 240
M 30
CV 10,168,192
CV 11,216,240
M 31
WL 6
R 58
M 44
AW 208,10
AW 209,11
M 45
EF 169
EF 193
M 46
EF 217
EF 241
M 47
CV 10,169,193
CV 11,217,241
M 36
AW 210,10
AW 211,11
M 37
EF 170
EF 194
M 38
EF 218
EF 242
M 39
WV 23
CV 10,170,194
CV 11,218,242
M 40
WL 0
AW 212,10
AW 213,11
M 41
s_barrier
EF 171
EF 195
M 42
mw 48
EF 219
EF 243
M 43
R 72
M 56
ad 84,144
W 0
M 57
CV 10,171,195
CV 11,219,243
M 58
AW 214,10
AW 215,11
M 59
BV 168,2048
BV 192,2304
M 48
W 1
BV 216,2560
M 49
ST 28,2304
BV 240,2816
M 50
ae 44,68,192
M 51
s_bitcmp1_b32 s50, 6
s_cselect_b32 s51, 24576, 0
v_add_u32 v52, s51, v3
M 52
W 2
XL 46,0
M 53
XL 48,768
EF 172
M 54
XL 14,1536
EF 196
M 55
R 74
M 68
mw 51
XL 16,2304
EF 220
M 69
ad 84,147
W 0
M 70
EF 244
CV 10,172,196
M 71
CV 11,220,244
AW 216,10
M 60
AW 217,11
EF 173
M 61
W 1
EF 197
M 62
EF 221
EF 245
M 63
CV 10,173,197
CV 11,221,245
M 64
AW 218,10
AW 219,11
M 65
W 2
EF 174
M 66
EF 198
EF 222
M 67
WL 0
R 76
M 80
EF 246
CV 10,174,198
M 81
bc 49,3,66,60
CV 11,222,246
M 82
mh 4
AW 220,10
AW 221,11
M 83
ad 66,304
H 0
M 72
EF 175
EF 199
M 73
EF 223
EF 247
M 74
CV 10,175,199
CV 11,223,247
M 75
H 1
AW 222,10
M 76
AW 223,11
BV 172,2048
M 77
BV 196,2304
BV 220,2560
M 78
BV 244,2816
M 79
R 78
M 92
mh 6
AR 6,88
AR 7,112
M 93
ad 64,306
H 0
M 94
AR 8,136
EF 6
M 95
EF 7
EF 8
M 84
EF 56
CV 10,6,7
M 85
H 1
CV 11,8,56
M 86
AW 224,10
AW 225,11
M 87
ae 42,74,0
AR 6,89
M 88
s_bitcmp1_b32 s50, 0
s_cselect_b32 s52, 24576, 0
v_add_u32 v53, s52, v3
M 89
bc 48,4,64,56
ST 30,128
M 90
mh 8
AR 7,113
AR 8,137
M 91
WL 0
R 80
M 104
ad 64,400
H 0
M 105
EF 6
EF 7
M 106
EF 8
EF 57
M 107
CV 10,6,7
CV 11,8,57
M 96
H 1
AW 226,10
M 97
AW 227,11
AR 6,90
M 98
AR 7,114
AR 8,138
M 99
EF 6
EF 7
M 100
PA 12288
DF
EF 8
M 101
EF 58
CV 10,6,7
M 102
CV 11,8,58
AW 228,10
M 103
R 82
M 116
AW 229,11
AR 6,91
M 117
AR 7,115
AR 8,139
M 118
EF 6
EF 7
M 119
EF 8
EF 59
M 108
CV 10,6,7
CV 11,8,59
M 109
AW 230,10
AW 231,11
M 110
BA 88,0
BA 112,256
M 111
WV 29
BA 136,512
BV 56,768
M 112
WL 4
AR 6,92
AR 7,116
AR 8,140
M 113
s_barrier
EF 6
EF 7
EF 8
M 114
mw 72
EF 60
CV 10,6,7
CV 11,8,60
M 115
R 0
AW 232,10
M 128
ad 84,168
W 0
AW 233,11
M 129
AR 6,93
AR 7,117
AR 8,141
M 140
EF 6
EF 7
EF 8
M 141
T 56,0
EF 61
M 130
T 60,14
CV 10,6,7
M 131
W 1
CV 11,8,61
AW 234,10
M 142
AW 235,11
AR 6,94
AR 7,118
M 143
T 64,1
AR 8,142
M 120
T 68,15
EF 6
M 121
W 2
EF 7
EF 8
M 132
EF 62
CV 10,6,7
CV 11,8,62
M 133
WL 8
T 24,10
AW 236,10
M 122
T 28,24
AW 237,11
M 123
mw 75
ST 32,896
AR 6,95
)"
R"(AR 7,119
M 134
ad 84,171
W 0
AR 8,143
M 135
T 32,11
EF 6
M 124
WL 8
T 36,25
EF 7
M 125
EF 8
EF 63
CV 10,6,7
M 136
W 1
CV 11,8,63
AW 238,10
M 137
T 40,12
AW 239,11
M 126
T 44,26
BA 92,0
M 127
WL 9
R 2
BA 116,256
M 138
W 2
BA 140,512
BV 60,768
M 139
WL 8
T 48,13
M 0
T 52,27
M 1
R 4
M 2
EF 80
EF 104
M 3
WL 12
EF 128
EF 152
M 4
CV 10,80,104
CV 11,128,152
M 5
WL 2
AW 240,10
AW 241,11
M 6
EF 81
EF 105
M 7
EF 129
EF 153
M 8
CV 10,81,105
CV 11,129,153
M 9
AW 242,10
AW 243,11
M 10
EF 82
EF 106
M 11
R 6
M 12
EF 130
EF 154
M 13
CV 10,82,106
CV 11,130,154
M 14
AW 244,10
AW 245,11
M 15
EF 83
EF 107
M 16
EF 131
EF 155
M 17
CV 10,83,107
CV 11,131,155
M 18
AW 246,10
AW 247,11
M 19
BV 80,1024
BV 104,1280
M 20
BV 128,1536
BV 152,1792
M 21
EF 84
EF 108
M 22
EF 132
EF 156
M 23
WL 6
R 8
M 24
CV 10,84,108
CV 11,132,156
M 25
AW 248,10
AW 249,11
M 26
EF 85
EF 109
M 27
ST 34,1664
EF 133
M 28
EF 157
CV 10,85,109
M 29
CV 11,133,157
AW 250,10
M 30
AW 251,11
EF 86
M 31
EF 110
EF 134
M 32
EF 158
CV 10,86,110
M 33
CV 11,134,158
AW 252,10
M 34
AW 253,11
EF 87
M 35
WL 2
R 10
M 36
EF 111
EF 135
M 37
EF 159
CV 10,87,111
M 38
CV 11,135,159
AW 254,10
M 39
AW 255,11
BV 84,1024
M 40
BV 108,1280
BV 132,1536
M 41
BV 156,1792
M 42
EF 176
EF 200
M 43
WV 28
EF 224
EF 248
M 44
WL 4
CV 18,176,200
CV 19,224,248
M 45
s_barrier
EF 177
EF 201
M 46
mw 0
EF 225
EF 249
M 47
R 24
M 48
ad 84,192
W 0
M 49
CV 20,177,201
CV 21,225,249
M 50
EF 178
EF 202
M 51
EF 226
EF 250
M 52
W 1
CV 22,178,202
M 53
CV 23,226,250
EF 179
M 54
EF 203
EF 227
M 55
EF 251
CV 24,179,203
M 56
W 2
CV 25,227,251
M 57
WL 2
BV 176,2048
BV 200,2304
M 58
BV 224,2560
BV 248,2816
M 59
R 26
M 60
mw 3
EF 180
EF 204
M 61
ad 84,195
W 0
M 62
EF 228
EF 252
M 63
CV 26,180,204
CV 27,228,252
M 64
EF 181
EF 205
M 65
W 1
ST 36,2432
M 66
EF 229
EF 253
M 67
CV 28,181,205
CV 29,229,253
M 68
EF 182
EF 206
M 69
W 2
EF 230
M 70
EF 254
CV 30,182,206
M 71
WL 0
R 28
M 72
CV 31,230,254
EF 183
M 73
bc 49,4,66,60
EF 207
M 74
mh 10
EF 231
EF 255
M 75
ad 66,400
H 0
M 76
CV 32,183,207
CV 33,231,255
M 77
BV 180,2048
BV 204,2304
M 78
BV 228,2560
BV 252,2816
M 79
WV 27
H 1
UL 6,46
M 80
ae 44,68,192
M 81
s_bfe_u32 s51, s50, 0x10007
v_mad_i32_i24 v52, v51, s51, v50
XL 34,0
M 82
XL 36,768
UH 7,46
M 83
R 30
M 84
mh 12
UL 8,47
UH 9,47
M 85
ad 64,402
H 0
M 86
AR 10,72
AR 11,96
M 87
AR 12,120
AR 13,144
M 88
AD 10,6
AD 11,7
M 89
H 1
AD 12,8
M 90
AD 13,9
AW 72,10
M 91
AW 96,11
AW 120,12
M 92
AW 144,13
UL 6,48
M 93
bc 48,5,64,56
UH 7,48
M 94
mh 0
UL 8,49
UH 9,49
M 95
WL 0
R 32
M 96
ad 64,496
H 0
M 97
AR 10,73
AR 11,97
M 98
AR 12,121
AR 13,145
M 99
AD 10,6
AD 11,7
M 100
H 1
AD 12,8
M 101
AD 13,9
AW 73,10
M 102
AW 97,11
AW 121,12
M 103
ae 42,74,0
AW 145,13
M 104
s_bfe_u32 s52, s50, 0x10001
v_mad_i32_i24 v53, v51, s52, v50
ST 38,128
M 105
s_nop 0
XL 38,1536
UL 6,14
M 106
UH 7,14
UL 8,15
M 107
R 34
M 108
UH 9,15
AR 10,74
M 109
AR 11,98
AR 12,122
M 110
AR 13,146
AD 10,6
M 111
AD 11,7
AD 12,8
M 112
AD 13,9
AW 74,10
M 113
AW 98,11
AW 122,12
M 114
AW 146,13
UL 6,16
M 115
WV 29
UH 7,16
UL 8,17
M 116
WL 0
UH 9,17
AR 10,75
M 117
s_barrier
AR 11,99
AR 12,123
)"
R"(M 118
mw 24
AR 13,147
AD 10,6
M 119
R 48
M 120
ad 84,216
W 0
M 121
AD 11,7
AD 12,8
M 132
AD 13,9
AW 75,10
M 133
T 24,2
M 122
T 28,16
M 123
W 1
AW 99,11
M 134
AW 123,12
AW 147,13
M 135
T 32,3
M 124
T 36,17
M 125
W 2
M 136
M 137
T 40,4
M 126
T 44,18
M 127
mw 27
M 138
ad 84,219
W 0
M 139
WL 8
T 48,5
M 128
T 52,19
M 129
M 140
W 1
M 141
T 56,6
M 130
WL 8
T 60,20
M 131
R 50
M 142
W 2
ST 40,896
M 143
T 64,7
M 0
WL 8
T 68,21
M 1
R 52
M 2
XL 40,2304
M 3
M 4
ae 44,70,192
M 5
M 6
s_bitcmp1_b32 s50, 8
s_cselect_b32 s51, 24576, 0
v_add_u32 v52, s51, v3
M 7
WL 8
XL 46,0
M 8
XL 48,768
M 9
WL 2
XL 14,1536
M 10
XL 16,2304
M 11
R 54
M 12
M 13
M 14
M 15
M 16
M 17
M 18
M 19
M 20
M 21
M 22
M 23
WL 2
R 56
M 24
M 25
M 26
M 27
M 28
M 29
M 30
M 31
M 32
M 33
M 34
M 35
WL 2
R 58
M 36
ST 42,1664
M 37
M 38
M 39
M 40
M 41
M 42
WV 20
UL 6,34
UH 7,34
M 43
UL 8,35
UH 9,35
M 44
WL 0
AR 10,76
AR 11,100
M 45
s_barrier
AR 12,124
AR 13,148
M 46
mw 48
AD 10,6
AD 11,7
M 47
R 72
M 48
ad 84,240
W 0
M 49
AD 12,8
AD 13,9
M 50
AW 76,10
AW 100,11
M 51
AW 124,12
AW 148,13
M 52
WV 20
W 1
UL 6,36
M 53
UH 7,36
UL 8,37
M 54
UH 9,37
AR 10,77
M 55
AR 11,101
AR 12,125
M 56
W 2
AR 13,149
M 57
AD 10,6
AD 11,7
M 58
AD 12,8
AD 13,9
M 59
R 74
M 60
mw 51
AW 77,10
AW 101,11
M 61
ad 84,243
W 0
M 62
AW 125,12
AW 149,13
M 63
WV 17
UL 6,38
UH 7,38
M 64
UL 8,39
UH 9,39
M 65
W 1
AR 10,78
M 66
AR 11,102
AR 12,126
M 67
AR 13,150
AD 10,6
M 68
AD 11,7
AD 12,8
M 69
W 2
AD 13,9
M 70
AW 78,10
AW 102,11
M 71
WL 0
R 76
M 72
WV 11
AW 126,12
AW 150,13
M 73
bc 49,5,66,60
UL 6,40
M 74
mh 2
ST 44,2432
UH 7,40
M 75
ad 66,496
H 0
M 76
UL 8,41
UH 9,41
M 77
AR 10,79
AR 11,103
M 78
AR 12,127
AR 13,151
M 79
H 1
AD 10,6
M 80
AD 11,7
AD 12,8
M 81
AD 13,9
AW 79,10
M 82
AW 103,11
AW 127,12
M 83
R 78
M 84
mh 4
AW 151,13
M 85
ad 64,498
H 0
M 86
M 87
M 88
M 89
H 1
M 90
M 91
M 92
M 93
bc 48,6,64,56
M 94
mh 6
M 95
WL 2
R 80
M 96
ad 64,592
H 0
M 97
M 98
M 99
M 100
H 1
M 101
M 102
M 103
M 104
M 105
M 106
M 107
WL 2
R 82
M 108
M 109
M 110
M 111
M 112
ae 42,74,0
M 113
s_bitcmp1_b32 s50, 0
s_cselect_b32 s52, 24576, 0
v_add_u32 v53, s52, v3
M 114
SA 160,256
M 115
M 116
WL 0
M 117
s_barrier
M 118
mw 72
M 119
R 0
M 120
ad 84,264
W 0
M 121
M 132
M 133
T 24,8
M 122
T 28,22
M 123
W 1
M 134
M 135
T 32,9
M 124
T 36,23
M 125
W 2
M 136
M 137
T 40,10
M 126
T 44,24
M 127
mw 75
M 138
ad 84,267
W 0
M 139
WL 8
T 48,11
M 128
T 52,25
M 129
ae 44,70,192
M 140
s_bfe_u32 s51, s50, 0x10009
W 1
v_mad_i32_i24 v52, v51, s51, v50
M 141
T 56,12
M 130
WL 8
T 60,26
M 131
R 2
M 142
W 2
XL 42,0
M 143
T 64,13
M 0
WL 8
T 68,27
M 1
R 4
M 2
XL 44,768
M 3
XL 34,1536
M 4
M 5
M 6
M 7
WL 8
M 8
SA 162,1024
XL 36,2304
M 9
WL 2
M 10
M 11
R 6
M 12
M 13
M 14
M 15
M 16
M 17
M 18
M 19
M 20
M 21
M 22
M 23
WL 2
)"
R"(R 8
M 24
M 25
M 26
M 27
M 28
M 29
M 30
M 31
M 32
M 33
M 34
M 35
WL 2
R 10
M 36
M 37
M 38
M 39
M 40
M 41
M 42
M 43
M 44
WV 26
UL 6,46
UH 7,46
M 45
WL 0
s_barrier
UL 8,47
UH 9,47
M 46
mw 0
SA 164,1792
AR 10,80
M 47
R 24
M 48
ad 84,288
W 0
M 49
AR 11,104
AR 12,128
M 50
AR 13,152
AD 10,6
M 51
AD 11,7
AD 12,8
M 52
W 1
AD 13,9
M 53
AW 80,10
AW 104,11
M 54
AW 128,12
AW 152,13
M 55
UL 6,48
UH 7,48
M 56
W 2
UL 8,49
M 57
UH 9,49
AR 10,81
M 58
AR 11,105
AR 12,129
M 59
R 26
M 60
mw 3
AR 13,153
AD 10,6
M 61
ad 84,291
W 0
M 62
AD 11,7
AD 12,8
M 63
AD 13,9
AW 81,10
M 64
AW 105,11
AW 129,12
M 65
W 1
AW 153,13
M 66
UL 6,14
UH 7,14
M 67
UL 8,15
UH 9,15
M 68
AR 10,82
AR 11,106
M 69
W 2
AR 12,130
M 70
AR 13,154
AD 10,6
M 71
WL 0
R 28
M 72
AD 11,7
AD 12,8
M 73
bc 49,6,66,60
AD 13,9
M 74
mh 8
AW 82,10
AW 106,11
M 75
ad 66,592
H 0
M 76
AW 130,12
AW 154,13
M 77
UL 6,16
UH 7,16
M 78
UL 8,17
UH 9,17
M 79
H 1
AR 10,83
M 80
AR 11,107
AR 12,131
M 81
AR 13,155
AD 10,6
M 82
AD 11,7
AD 12,8
M 83
R 30
M 84
mh 10
SA 166,2560
AD 13,9
M 85
ad 64,594
H 0
M 86
AW 83,10
AW 107,11
M 87
AW 131,12
AW 155,13
M 88
M 89
H 1
M 90
M 91
M 92
M 93
bc 48,7,64,56
M 94
mh 12
M 95
WL 2
R 32
M 96
ad 64,688
H 0
M 97
M 98
M 99
M 100
H 1
M 101
M 102
M 103
M 104
M 105
M 106
M 107
WL 2
R 34
M 108
M 109
M 110
M 111
M 112
M 113
M 114
M 115
M 116
WV 28
M 117
WL 0
s_barrier
M 118
mw 24
M 119
R 48
M 120
ad 84,312
W 0
M 121
M 132
ae 42,74,0
M 133
s_bfe_u32 s52, s50, 0x10001
T 24,0
M 122
T 28,14
M 123
W 1
v_mad_i32_i24 v53, v51, s52, v50
M 134
SA 168,256
M 135
T 32,1
M 124
T 36,15
M 125
W 2
M 136
M 137
T 40,2
M 126
T 44,16
M 127
mw 27
M 138
ad 84,315
W 0
M 139
WL 8
T 48,3
M 128
T 52,17
M 129
ae 44,68,240
M 140
W 1
M 141
T 56,4
M 130
WL 8
T 60,18
M 131
R 50
M 142
W 2
M 143
T 64,5
M 0
WL 8
T 68,19
M 1
R 52
M 2
s_bitcmp1_b32 s50, 10
s_cselect_b32 s51, 24576, 0
M 3
v_add_u32 v52, s51, v3
XL 38,0
M 4
XL 40,768
M 5
XL 46,1536
M 6
M 7
WL 8
M 8
M 9
WL 2
M 10
XL 48,2304
M 11
R 54
M 12
M 13
M 14
M 15
M 16
M 17
M 18
M 19
M 20
SA 170,1024
M 21
M 22
M 23
WL 2
R 56
M 24
M 25
M 26
M 27
M 28
M 29
M 30
M 31
M 32
M 33
M 34
M 35
WL 2
R 58
M 36
M 37
M 38
M 39
M 40
M 41
M 42
M 43
M 44
WV 28
M 45
WL 0
s_barrier
M 46
mw 48
UL 6,42
UH 7,42
M 47
R 72
M 48
ad 84,336
W 0
M 49
UL 8,43
UH 9,43
M 50
AR 10,84
AR 11,108
M 51
AR 12,132
AR 13,156
M 52
W 1
AD 10,6
M 53
AD 11,7
AD 12,8
M 54
AD 13,9
AW 84,10
M 55
AW 108,11
AW 132,12
M 56
W 2
AW 156,13
M 57
UL 6,44
UH 7,44
M 58
SA 172,1792
UL 8,45
M 59
R 74
M 60
mw 51
UH 9,45
AR 10,85
M 61
ad 84,339
W 0
M 62
AR 11,109
AR 12,133
M 63
AR 13,157
AD 10,6
M 64
AD 11,7
AD 12,8
M 65
W 1
AD 13,9
M 66
AW 85,10
AW 109,11
M 67
AW 133,12
AW 157,13
M 68
UL 6,34
UH 7,34
M 69
W 2
UL 8,35
M 70
UH 9,35
AR 10,86
)"
R"(M 71
WL 0
R 76
M 72
AR 11,110
AR 12,134
M 73
bc 49,7,66,60
AR 13,158
M 74
mh 0
AD 10,6
AD 11,7
M 75
ad 66,688
H 0
M 76
AD 12,8
AD 13,9
M 77
AW 86,10
AW 110,11
M 78
AW 134,12
AW 158,13
M 79
WV 34
H 1
UL 6,36
M 80
UH 7,36
UL 8,37
M 81
UH 9,37
AR 10,87
M 82
AR 11,111
AR 12,135
M 83
R 78
M 84
mh 2
AR 13,159
AD 10,6
M 85
ad 64,690
H 0
M 86
AD 11,7
AD 12,8
M 87
AD 13,9
AW 87,10
M 88
AW 111,11
AW 135,12
M 89
H 1
AW 159,13
M 90
M 91
M 92
M 93
bc 48,8,64,56
M 94
mh 4
M 95
WL 2
R 80
M 96
ad 64,784
H 0
M 97
SA 174,2560
M 98
M 99
M 100
H 1
M 101
M 102
M 103
M 104
PA 12288
DF
M 105
M 106
M 107
WL 2
R 82
M 108
M 109
M 110
M 111
M 112
M 113
M 114
M 115
M 116
WV 29
M 117
WL 0
s_barrier
M 118
mw 72
M 119
R 0
M 120
ad 84,360
W 0
M 121
M 132
M 133
T 24,6
M 122
T 28,20
M 123
W 1
M 134
M 135
T 32,7
M 124
T 36,21
M 125
W 2
M 136
M 137
T 40,8
M 126
T 44,22
M 127
mw 75
M 138
ad 84,363
W 0
M 139
WL 8
T 48,9
M 128
T 52,23
M 129
ae 42,72,0
M 140
W 1
M 141
T 56,10
M 130
WL 8
T 60,24
M 131
R 2
M 142
W 2
M 143
T 64,11
M 0
WL 8
T 68,25
M 1
R 4
M 2
s_bitcmp1_b32 s50, 2
s_cselect_b32 s52, 24576, 0
M 3
v_add_u32 v53, s52, v3
SA 176,0
M 4
ae 44,68,240
M 5
M 6
s_bfe_u32 s51, s50, 0x1000b
v_mad_i32_i24 v52, v51, s51, v50
XL 14,0
M 7
WL 8
XL 16,768
M 8
XL 42,1536
M 9
WL 2
M 10
M 11
R 6
M 12
XL 44,2304
M 13
M 14
M 15
M 16
M 17
M 18
M 19
M 20
M 21
M 22
M 23
WL 2
R 8
M 24
M 25
M 26
M 27
M 28
M 29
M 30
M 31
M 32
M 33
M 34
M 35
WL 2
R 10
M 36
M 37
M 38
M 39
M 40
M 41
SA 178,768
M 42
M 43
WV 32
M 44
WL 0
M 45
s_barrier
M 46
mw 0
M 47
R 24
M 48
ad 84,384
W 0
M 49
WV 29
UL 6,38
UH 7,38
M 50
UL 8,39
UH 9,39
M 51
AR 10,88
AR 11,112
M 52
W 1
AR 12,136
M 53
AD 10,6
AD 11,7
M 54
AD 12,8
AD 56,9
M 55
AW 88,10
AW 112,11
M 56
W 2
AW 136,12
M 57
UL 6,40
UH 7,40
M 58
UL 8,41
UH 9,41
M 59
R 26
M 60
mw 3
AR 10,89
AR 11,113
M 61
ad 84,387
W 0
M 62
AR 12,137
AD 10,6
M 63
AD 11,7
AD 12,8
M 64
AD 57,9
AW 89,10
M 65
W 1
AW 113,11
M 66
AW 137,12
UL 6,46
M 67
UH 7,46
UL 8,47
M 68
UH 9,47
AR 10,90
M 69
W 2
AR 11,114
M 70
AR 12,138
AD 10,6
M 71
WL 0
R 28
M 72
AD 11,7
AD 12,8
M 73
bc 49,8,66,60
AD 58,9
M 74
mh 6
AW 90,10
AW 114,11
M 75
ad 66,784
H 0
M 76
AW 138,12
UL 6,48
M 77
UH 7,48
UL 8,49
M 78
UH 9,49
AR 10,91
M 79
H 1
SA 180,1536
M 80
AR 11,115
AR 12,139
M 81
AD 10,6
AD 11,7
M 82
AD 12,8
AD 59,9
M 83
R 30
M 84
mh 8
AW 91,10
AW 115,11
M 85
ad 64,786
H 0
M 86
AW 139,12
M 87
M 88
M 89
H 1
M 90
M 91
M 92
M 93
bc 48,9,64,56
M 94
mh 10
M 95
WL 2
R 32
M 96
ad 64,880
H 0
M 97
M 98
M 99
M 100
H 1
M 101
M 102
M 103
M 104
M 105
M 106
M 107
WL 2
R 34
M 108
M 109
M 110
M 111
M 112
M 113
M 114
M 115
M 116
WV 29
M 117
WL 0
s_barrier
SA 182,2304
M 118
mw 24
M 119
R 48
M 120
ad 84,408
W 0
M 121
)"
R"(M 132
M 133
T 24,12
M 122
T 28,26
M 123
W 1
M 134
M 135
T 32,13
M 124
T 36,27
M 125
W 2
M 136
M 137
T 40,0
M 126
T 44,14
M 127
mw 27
M 138
ad 84,411
W 0
M 139
WL 8
T 48,1
M 128
T 52,15
M 129
M 140
W 1
M 141
T 56,2
M 130
WL 8
T 60,16
M 131
R 50
M 142
W 2
M 143
T 64,3
M 0
WL 8
T 68,17
M 1
R 52
M 2
M 3
M 4
M 5
M 6
M 7
WL 8
M 8
M 9
WL 2
M 10
M 11
R 54
M 12
ae 42,72,0
M 13
s_bfe_u32 s52, s50, 0x10003
v_mad_i32_i24 v53, v51, s52, v50
SA 184,0
M 14
M 15
M 16
M 17
M 18
M 19
M 20
M 21
M 22
M 23
WL 2
R 56
M 24
M 25
M 26
M 27
M 28
M 29
M 30
M 31
M 32
M 33
M 34
M 35
WL 2
R 58
M 36
M 37
M 38
M 39
M 40
M 41
M 42
M 43
M 44
WV 27
M 45
WL 0
s_barrier
M 46
mw 48
M 47
R 72
M 48
ad 84,432
W 0
M 49
M 50
WV 23
UL 6,14
UH 7,14
M 51
SA 186,768
UL 8,15
M 52
W 1
UH 9,15
M 53
AR 10,92
AR 11,116
M 54
AR 12,140
AD 10,6
M 55
AD 11,7
AD 12,8
M 56
W 2
AD 60,9
M 57
AW 92,10
AW 116,11
M 58
AW 140,12
UL 6,16
M 59
R 74
M 60
mw 51
UH 7,16
UL 8,17
M 61
ad 84,435
W 0
M 62
UH 9,17
AR 10,93
M 63
AR 11,117
AR 12,141
M 64
AD 10,6
AD 11,7
M 65
W 1
AD 12,8
M 66
AD 61,9
AW 93,10
M 67
AW 117,11
AW 141,12
M 68
UL 6,42
UH 7,42
M 69
W 2
UL 8,43
M 70
UH 9,43
AR 10,94
M 71
WL 0
R 76
M 72
AR 11,118
AR 12,142
M 73
bc 49,9,66,60
AD 10,6
M 74
mh 12
AD 11,7
AD 12,8
M 75
ad 66,880
H 0
M 76
AD 62,9
AW 94,10
M 77
AW 118,11
AW 142,12
M 78
UL 6,44
UH 7,44
M 79
H 1
UL 8,45
M 80
UH 9,45
AR 10,95
M 81
AR 11,119
AR 12,143
M 82
AD 10,6
AD 11,7
M 83
R 78
M 84
mh 0
AD 12,8
AD 63,9
M 85
ad 64,882
H 0
M 86
AW 95,10
AW 119,11
M 87
AW 143,12
M 88
M 89
H 1
SA 188,1536
M 90
M 91
M 92
M 93
bc 48,10,64,56
M 94
mh 2
M 95
WL 2
R 80
M 96
ad 64,976
H 0
M 97
M 98
M 99
M 100
H 1
M 101
M 102
M 103
M 104
M 105
M 106
M 107
WL 2
R 82
M 108
M 109
M 110
M 111
M 112
M 113
M 114
M 115
M 116
WV 24
M 117
WL 0
s_barrier
M 118
mw 72
M 119
R 0
M 120
ad 84,456
W 0
M 121
M 132
M 133
T 24,4
M 122
T 28,18
M 123
W 1
M 134
M 135
T 32,5
M 124
T 36,19
M 125
W 2
SA 190,2304
M 136
M 137
T 40,6
M 126
T 44,20
M 127
mw 75
M 138
ad 84,459
W 0
M 139
WL 8
T 48,7
M 128
T 52,21
M 129
M 140
W 1
M 141
T 56,8
M 130
WL 8
T 60,22
M 131
R 2
M 142
W 2
M 143
T 64,9
M 0
WL 8
T 68,23
M 1
R 4
M 2
M 3
M 4
M 5
M 6
M 7
WL 8
M 8
M 9
WL 2
M 10
M 11
R 6
M 12
M 13
M 14
M 15
M 16
M 17
M 18
M 19
M 20
M 21
M 22
M 23
WL 2
R 8
M 24
ae 42,72,0
M 25
s_bitcmp1_b32 s50, 2
s_cselect_b32 s52, 24576, 0
v_add_u32 v53, s52, v3
M 26
SA 192,128
M 27
M 28
M 29
M 30
M 31
M 32
ae 44,68,192
M 33
s_bitcmp1_b32 s50, 6
s_cselect_b32 s51, 24576, 0
v_add_u32 v52, s51, v3
M 34
XL 34,128
M 35
WL 2
R 10
M 36
M 37
M 38
XL 36,896
M 39
M 40
M 41
M 42
M 43
WV 25
M 44
WL 0
XL 38,1664
M 45
s_barrier
M 46
mw 0
M 47
R 24
)"
R"(M 48
ad 84,480
W 0
M 49
M 50
XL 40,2432
M 51
M 52
W 1
M 53
M 54
M 55
M 56
W 2
M 57
M 58
M 59
R 26
M 60
mw 3
M 61
ad 84,483
W 0
M 62
M 63
M 64
SA 194,896
M 65
W 1
M 66
M 67
M 68
M 69
W 2
M 70
M 71
WL 2
R 28
M 72
M 73
bc 49,10,66,60
M 74
mh 4
M 75
ad 66,976
H 0
M 76
M 77
M 78
M 79
H 1
M 80
M 81
M 82
M 83
WL 2
R 30
M 84
mh 6
M 85
ad 64,978
H 0
M 86
M 87
M 88
M 89
H 1
M 90
M 91
M 92
M 93
bc 48,11,64,56
M 94
mh 8
M 95
WL 2
R 32
M 96
ad 64,1072
H 0
M 97
M 98
M 99
M 100
H 1
M 101
M 102
SA 196,1664
M 103
M 104
M 105
M 106
M 107
WL 2
R 34
M 108
M 109
M 110
M 111
M 112
M 113
M 114
M 115
M 116
WV 29
M 117
WL 0
s_barrier
M 118
mw 24
M 119
R 48
M 120
ad 84,504
W 0
M 121
M 132
M 133
T 24,10
M 122
T 28,24
M 123
W 1
M 134
M 135
T 32,11
M 124
T 36,25
M 125
W 2
M 136
M 137
T 40,12
M 126
T 44,26
M 127
mw 27
M 138
ad 84,507
W 0
M 139
WL 8
T 48,13
M 128
T 52,27
M 129
M 140
W 1
M 141
T 56,0
M 130
WL 8
T 60,14
M 131
R 50
M 142
W 2
SA 198,2432
M 143
T 64,1
M 0
WL 8
T 68,15
M 1
R 52
M 2
M 3
M 4
M 5
M 6
M 7
WL 8
M 8
M 9
WL 2
M 10
M 11
R 54
M 12
M 13
M 14
M 15
M 16
M 17
M 18
M 19
M 20
M 21
M 22
M 23
WL 2
R 56
M 24
M 25
M 26
M 27
M 28
M 29
M 30
M 31
M 32
M 33
M 34
ae 44,68,192
M 35
WL 2
R 58
M 36
ae 42,72,0
M 37
s_bfe_u32 s52, s50, 0x10003
v_mad_i32_i24 v53, v51, s52, v50
SA 200,128
M 38
s_bfe_u32 s51, s50, 0x10007
v_mad_i32_i24 v52, v51, s51, v50
XL 46,128
M 39
M 40
XL 48,896
M 41
M 42
M 43
WV 29
M 44
WL 0
M 45
s_barrier
M 46
mw 48
XL 14,1664
M 47
R 72
M 48
ad 84,528
W 0
M 49
M 50
M 51
M 52
W 1
XL 16,2432
M 53
M 54
M 55
M 56
W 2
M 57
M 58
M 59
R 74
M 60
mw 51
M 61
ad 84,531
W 0
M 62
M 63
M 64
M 65
W 1
M 66
M 67
M 68
M 69
W 2
M 70
M 71
WL 2
R 76
M 72
M 73
bc 49,11,66,60
M 74
mh 10
M 75
ad 66,1072
H 0
M 76
SA 202,896
M 77
M 78
M 79
H 1
M 80
M 81
M 82
M 83
WL 2
R 78
M 84
mh 12
M 85
ad 64,1074
H 0
M 86
M 87
M 88
WV 37
UL 6,34
UH 7,34
M 89
H 1
UL 8,35
M 90
UH 9,35
AD 64,6
M 91
AD 88,7
AD 112,8
M 92
AD 136,9
UL 6,36
M 93
bc 48,12,64,56
UH 7,36
M 94
mh 0
UL 8,37
UH 9,37
M 95
WL 2
R 80
M 96
ad 64,1168
H 0
M 97
AD 65,6
AD 89,7
M 98
AD 113,8
AD 137,9
M 99
UL 6,38
UH 7,38
M 100
H 1
UL 8,39
M 101
UH 9,39
AD 66,6
M 102
AD 90,7
AD 114,8
M 103
WV 38
AD 138,9
UL 6,40
M 104
PA 12288
DF
UH 7,40
M 105
UL 8,41
UH 9,41
M 106
AD 67,6
AD 91,7
M 107
WL 0
R 82
M 108
AD 115,8
AD 139,9
M 109
M 110
M 111
M 112
M 113
M 114
SA 204,1664
M 115
WV 30
M 116
WL 0
M 117
s_barrier
M 118
mw 72
M 119
R 0
M 120
ad 84,552
W 0
M 121
M 132
M 133
T 24,2
M 122
T 28,16
M 123
W 1
M 134
M 135
T 32,3
M 124
T 36,17
M 125
W 2
M 136
)"
R"(M 137
T 40,4
M 126
T 44,18
M 127
mw 75
M 138
ad 84,555
W 0
M 139
WL 8
T 48,5
M 128
T 52,19
M 129
M 140
W 1
M 141
T 56,6
M 130
WL 8
T 60,20
M 131
R 2
M 142
W 2
M 143
T 64,7
M 0
WL 8
T 68,21
M 1
R 4
M 2
M 3
M 4
M 5
M 6
M 7
WL 8
M 8
SA 206,2432
M 9
WL 2
M 10
M 11
R 6
M 12
M 13
M 14
M 15
M 16
M 17
M 18
M 19
M 20
M 21
M 22
M 23
WL 2
R 8
M 24
M 25
M 26
M 27
M 28
M 29
M 30
M 31
M 32
M 33
M 34
M 35
WL 2
R 10
M 36
ae 44,70,192
M 37
s_bitcmp1_b32 s50, 8
s_cselect_b32 s51, 24576, 0
v_add_u32 v52, s51, v3
M 38
XL 42,128
M 39
M 40
M 41
M 42
XL 44,896
M 43
WV 30
M 44
WL 0
M 45
s_barrier
M 46
mw 0
M 47
R 24
M 48
ad 84,576
W 0
M 49
ae 42,72,0
XL 34,1664
M 50
s_bitcmp1_b32 s50, 2
s_cselect_b32 s52, 24576, 0
v_add_u32 v53, s52, v3
M 51
SA 208,256
M 52
W 1
M 53
M 54
XL 36,2432
M 55
M 56
W 2
M 57
M 58
M 59
R 26
M 60
mw 3
M 61
ad 84,579
W 0
M 62
M 63
M 64
M 65
W 1
M 66
M 67
M 68
M 69
W 2
M 70
M 71
WL 2
R 28
M 72
M 73
bc 49,12,66,60
M 74
mh 2
M 75
ad 66,1168
H 0
M 76
M 77
M 78
M 79
H 1
M 80
M 81
M 82
M 83
WL 2
R 30
M 84
mh 4
M 85
ad 64,1170
H 0
M 86
M 87
M 88
M 89
H 1
SA 210,1024
M 90
WV 39
UL 6,46
UH 7,46
M 91
UL 8,47
UH 9,47
M 92
AD 68,6
AD 92,7
M 93
bc 48,13,64,56
AD 116,8
M 94
mh 6
AD 140,9
UL 6,48
M 95
WL 2
R 32
M 96
ad 64,1264
H 0
M 97
UH 7,48
UL 8,49
M 98
UH 9,49
AD 69,6
M 99
AD 93,7
AD 117,8
M 100
H 1
AD 141,9
M 101
UL 6,14
UH 7,14
M 102
UL 8,15
UH 9,15
M 103
AD 70,6
AD 94,7
M 104
AD 118,8
AD 142,9
M 105
WV 38
UL 6,16
UH 7,16
M 106
UL 8,17
UH 9,17
M 107
WL 1
R 34
M 108
AD 71,6
AD 95,7
M 109
AD 119,8
AD 143,9
M 110
M 111
M 112
M 113
M 114
M 115
WV 29
M 116
WL 0
M 117
s_barrier
M 118
mw 24
M 119
R 48
M 120
ad 84,600
W 0
M 121
M 132
M 133
T 24,8
M 122
T 28,22
M 123
W 1
M 134
M 135
T 32,9
M 124
T 36,23
M 125
W 2
SA 212,1792
M 136
M 137
T 40,10
M 126
T 44,24
M 127
mw 27
M 138
ad 84,603
W 0
M 139
WL 8
T 48,11
M 128
T 52,25
M 129
M 140
W 1
M 141
T 56,12
M 130
WL 8
T 60,26
M 131
R 50
M 142
W 2
M 143
T 64,13
M 0
WL 8
T 68,27
M 1
R 52
M 2
M 3
M 4
M 5
M 6
M 7
WL 8
M 8
M 9
WL 2
M 10
M 11
R 54
M 12
M 13
M 14
M 15
M 16
M 17
M 18
M 19
M 20
M 21
M 22
M 23
WL 2
R 56
M 24
SA 214,2560
M 25
M 26
M 27
M 28
M 29
M 30
M 31
M 32
M 33
M 34
M 35
WL 2
R 58
M 36
M 37
M 38
ae 44,70,192
M 39
s_bfe_u32 s51, s50, 0x10009
v_mad_i32_i24 v52, v51, s51, v50
XL 38,128
M 40
M 41
M 42
M 43
WV 28
M 44
WL 0
XL 40,896
M 45
s_barrier
M 46
mw 48
M 47
R 72
M 48
ad 84,624
W 0
M 49
M 50
XL 46,1664
M 51
M 52
W 1
M 53
M 54
M 55
M 56
W 2
XL 48,2432
M 57
M 58
M 59
R 74
M 60
mw 51
M 61
ad 84,627
W 0
M 62
ae 42,72,0
M 63
s_bfe_u32 s52, s50, 0x10003
v_mad_i32_i24 v53, v51, s52, v50
SA 216,256
M 64
M 65
W 1
M 66
)"
R"(M 67
M 68
M 69
W 2
M 70
M 71
WL 2
R 76
M 72
M 73
bc 49,13,66,60
M 74
mh 8
M 75
ad 66,1264
H 0
M 76
M 77
M 78
M 79
H 1
M 80
M 81
M 82
M 83
WL 2
R 78
M 84
mh 10
M 85
ad 64,1266
H 0
M 86
M 87
M 88
M 89
H 1
M 90
M 91
M 92
WV 39
UL 6,42
UH 7,42
M 93
bc 48,14,64,56
UL 8,43
M 94
mh 12
UH 9,43
AD 72,6
M 95
WL 2
R 80
M 96
ad 64,1360
H 0
M 97
AD 96,7
AD 120,8
M 98
AD 144,9
UL 6,44
M 99
UH 7,44
UL 8,45
M 100
H 1
UH 9,45
M 101
SA 218,1024
AD 73,6
M 102
AD 97,7
AD 121,8
M 103
WV 40
AD 145,9
UL 6,34
M 104
UH 7,34
UL 8,35
M 105
UH 9,35
AD 74,6
M 106
AD 98,7
AD 122,8
M 107
WL 1
R 82
M 108
WV 37
AD 146,9
UL 6,36
M 109
UH 7,36
UL 8,37
M 110
UH 9,37
AD 75,6
M 111
AD 99,7
AD 123,8
M 112
AD 147,9
M 113
M 114
M 115
WV 29
M 116
WL 0
M 117
s_barrier
M 118
mw 72
M 119
R 0
M 120
ad 84,648
W 0
M 121
M 132
M 133
T 24,0
M 122
T 28,14
M 123
W 1
M 134
M 135
T 32,1
M 124
T 36,15
M 125
W 2
M 136
M 137
T 40,2
M 126
T 44,16
M 127
mw 75
M 138
ad 84,651
W 0
M 139
WL 8
T 48,3
M 128
T 52,17
M 129
M 140
W 1
M 141
T 56,4
M 130
WL 8
T 60,18
M 131
R 2
M 142
W 2
SA 220,1792
M 143
T 64,5
M 0
WL 8
T 68,19
M 1
R 4
M 2
M 3
M 4
M 5
M 6
M 7
WL 8
M 8
M 9
WL 2
M 10
M 11
R 6
M 12
M 13
M 14
M 15
M 16
M 17
M 18
M 19
M 20
M 21
M 22
M 23
WL 2
R 8
M 24
M 25
M 26
M 27
M 28
M 29
M 30
M 31
M 32
M 33
M 34
M 35
WL 2
R 10
M 36
SA 222,2560
M 37
M 38
M 39
M 40
ae 44,68,240
M 41
s_bitcmp1_b32 s50, 10
s_cselect_b32 s51, 24576, 0
v_add_u32 v52, s51, v3
M 42
XL 14,128
M 43
WV 28
M 44
WL 0
M 45
s_barrier
M 46
mw 0
XL 16,896
M 47
R 24
M 48
ad 84,672
W 0
M 49
M 50
M 51
M 52
W 1
XL 42,1664
M 53
M 54
M 55
M 56
W 2
M 57
M 58
XL 44,2432
M 59
R 26
M 60
mw 3
M 61
ad 84,675
W 0
M 62
M 63
M 64
M 65
W 1
M 66
M 67
M 68
M 69
W 2
M 70
M 71
WL 2
R 28
M 72
M 73
bc 49,14,66,60
M 74
mh 0
M 75
ad 66,1360
H 0
M 76
ae 42,72,48
M 77
s_bitcmp1_b32 s50, 4
s_cselect_b32 s52, 24576, 0
v_add_u32 v53, s52, v3
M 78
SA 224,0
M 79
H 1
M 80
M 81
M 82
M 83
WL 2
R 30
M 84
mh 2
M 85
ad 64,1362
H 0
M 86
M 87
M 88
M 89
H 1
M 90
M 91
M 92
WV 39
M 93
bc 48,15,64,56
M 94
mh 4
UL 6,38
UH 7,38
M 95
WL 2
R 32
M 96
ad 64,1456
H 0
M 97
UL 8,39
UH 9,39
M 98
AD 76,6
AD 100,7
M 99
AD 124,8
AD 148,9
M 100
H 1
UL 6,40
M 101
UH 7,40
UL 8,41
M 102
UH 9,41
AD 77,6
M 103
AD 101,7
AD 125,8
M 104
WV 39
AD 149,9
UL 6,46
M 105
UH 7,46
UL 8,47
M 106
UH 9,47
AD 78,6
M 107
WL 1
R 34
M 108
AD 102,7
AD 126,8
M 109
WV 36
AD 150,9
UL 6,48
M 110
UH 7,48
UL 8,49
M 111
UH 9,49
AD 79,6
M 112
AD 103,7
AD 127,8
M 113
AD 151,9
M 114
M 115
WV 28
M 116
WL 0
SA 226,768
M 117
s_barrier
M 118
mw 24
M 119
R 48
M 120
ad 84,696
W 0
M 121
M 132
M 133
T 24,6
M 122
T 28,20
M 123
W 1
M 134
M 135
)"
R"(T 32,7
M 124
T 36,21
M 125
W 2
M 136
M 137
T 40,8
M 126
T 44,22
M 127
mw 27
M 138
ad 84,699
W 0
M 139
WL 8
T 48,9
M 128
T 52,23
M 129
M 140
W 1
M 141
T 56,10
M 130
WL 8
T 60,24
M 131
R 50
M 142
W 2
M 143
T 64,11
M 0
WL 8
T 68,25
M 1
R 52
M 2
M 3
M 4
M 5
M 6
M 7
WL 8
M 8
M 9
WL 2
M 10
SA 228,1536
M 11
R 54
M 12
M 13
M 14
M 15
M 16
M 17
M 18
M 19
M 20
M 21
M 22
M 23
WL 2
R 56
M 24
M 25
M 26
M 27
M 28
M 29
M 30
M 31
M 32
M 33
M 34
M 35
WL 2
R 58
M 36
M 37
M 38
M 39
M 40
M 41
M 42
ae 44,68,240
s_bfe_u32 s51, s50, 0x1000b
M 43
WV 27
v_mad_i32_i24 v52, v51, s51, v50
XL 34,128
M 44
WL 0
M 45
s_barrier
M 46
mw 48
M 47
R 72
M 48
ad 84,720
W 0
M 49
SA 230,2304
XL 36,896
M 50
M 51
M 52
W 1
M 53
M 54
XL 38,1664
M 55
M 56
W 2
M 57
M 58
M 59
R 74
M 60
mw 51
XL 40,2432
M 61
ad 84,723
W 0
M 62
M 63
M 64
M 65
W 1
M 66
M 67
M 68
M 69
W 2
M 70
M 71
WL 2
R 76
M 72
M 73
bc 49,15,66,60
M 74
mh 6
M 75
ad 66,1456
H 0
M 76
M 77
M 78
M 79
H 1
M 80
M 81
M 82
M 83
WL 2
R 78
M 84
mh 8
M 85
ad 64,1458
H 0
M 86
M 87
ae 42,72,48
M 88
s_bfe_u32 s52, s50, 0x10005
v_mad_i32_i24 v53, v51, s52, v50
SA 232,0
M 89
H 1
M 90
M 91
M 92
M 93
bc 48,16,64,56
M 94
mh 10
M 95
WL 2
R 80
M 96
ad 64,1552
H 0
M 97
WV 40
UL 6,14
UH 7,14
M 98
UL 8,15
UH 9,15
M 99
AD 80,6
AD 104,7
M 100
H 1
AD 128,8
M 101
AD 152,9
UL 6,16
M 102
UH 7,16
UL 8,17
M 103
UH 9,17
AD 81,6
M 104
PA 12288
DF
AD 105,7
M 105
AD 129,8
AD 153,9
M 106
WV 39
UL 6,42
UH 7,42
M 107
WL 0
R 82
M 108
UL 8,43
UH 9,43
M 109
AD 82,6
AD 106,7
M 110
AD 130,8
AD 154,9
M 111
WV 37
UL 6,44
UH 7,44
M 112
UL 8,45
UH 9,45
M 113
AD 83,6
AD 107,7
M 114
AD 131,8
AD 155,9
M 115
WV 29
M 116
WL 0
M 117
s_barrier
M 118
mw 72
M 119
R 0
M 120
ad 84,744
W 0
M 121
M 132
M 133
T 24,12
M 122
T 28,26
M 123
W 1
M 134
SA 234,768
M 135
T 32,13
M 124
T 36,27
M 125
W 2
M 136
M 137
T 40,0
M 126
T 44,14
M 127
mw 75
M 138
ad 84,747
W 0
M 139
WL 8
T 48,1
M 128
T 52,15
M 129
M 140
W 1
M 141
T 56,2
M 130
WL 8
T 60,16
M 131
R 2
M 142
W 2
M 143
T 64,3
M 0
WL 8
T 68,17
M 1
R 4
M 2
M 3
M 4
M 5
M 6
M 7
WL 8
M 8
M 9
WL 2
M 10
M 11
R 6
M 12
M 13
M 14
M 15
M 16
M 17
M 18
M 19
M 20
SA 236,1536
M 21
M 22
M 23
WL 2
R 8
M 24
M 25
M 26
M 27
M 28
M 29
M 30
M 31
M 32
M 33
M 34
M 35
WL 2
R 10
M 36
M 37
M 38
M 39
M 40
M 41
M 42
M 43
M 44
WV 28
M 45
WL 0
s_barrier
M 46
mw 0
M 47
R 24
M 48
ad 84,768
W 0
M 49
M 50
M 51
M 52
W 1
M 53
M 54
M 55
M 56
W 2
M 57
M 58
SA 238,2304
M 59
R 26
M 60
mw 3
M 61
ad 84,771
W 0
M 62
M 63
M 64
M 65
W 1
M 66
M 67
M 68
M 69
)"
R"(W 2
M 70
M 71
WL 2
R 28
M 72
M 73
bc 49,16,66,60
M 74
mh 12
M 75
ad 66,1552
H 0
M 76
M 77
M 78
M 79
H 1
M 80
M 81
M 82
M 83
WL 2
R 30
M 84
mh 0
M 85
ad 64,1554
H 0
M 86
M 87
M 88
M 89
H 1
M 90
M 91
M 92
M 93
bc 48,17,64,56
M 94
mh 2
M 95
WL 2
R 32
M 96
ad 64,1648
H 0
M 97
ae 42,72,48
M 98
s_bitcmp1_b32 s50, 4
s_cselect_b32 s52, 24576, 0
v_add_u32 v53, s52, v3
M 99
WV 38
SA 240,128
UL 6,34
M 100
H 1
UH 7,34
M 101
UL 8,35
UH 9,35
M 102
AD 84,6
AD 108,7
M 103
AD 132,8
AD 156,9
M 104
WV 37
UL 6,36
UH 7,36
M 105
UL 8,37
UH 9,37
M 106
AD 85,6
AD 109,7
M 107
WL 0
R 34
M 108
AD 133,8
AD 157,9
M 109
WV 35
UL 6,38
UH 7,38
M 110
UL 8,39
UH 9,39
M 111
AD 86,6
AD 110,7
M 112
AD 134,8
AD 158,9
M 113
WV 33
UL 6,40
UH 7,40
M 114
UL 8,41
UH 9,41
M 115
WV 25
AD 87,6
AD 111,7
M 116
WL 0
AD 135,8
AD 159,9
M 117
s_barrier
M 118
mw 24
M 119
R 48
M 120
ad 84,792
W 0
M 121
M 132
M 133
T 24,4
M 122
T 28,18
M 123
W 1
M 134
M 135
T 32,5
M 124
T 36,19
M 125
W 2
M 136
M 137
T 40,6
M 126
T 44,20
M 127
mw 27
M 138
ad 84,795
W 0
M 139
WL 8
T 48,7
M 128
T 52,21
M 129
SA 242,896
M 140
W 1
M 141
T 56,8
M 130
WL 8
T 60,22
M 131
R 50
M 142
W 2
M 143
T 64,9
M 0
WL 8
T 68,23
M 1
R 52
M 2
M 3
M 4
M 5
M 6
M 7
WL 8
M 8
M 9
WL 2
M 10
M 11
R 54
M 12
M 13
M 14
M 15
M 16
M 17
M 18
M 19
M 20
M 21
M 22
M 23
WL 2
R 56
M 24
M 25
M 26
M 27
M 28
M 29
M 30
M 31
SA 244,1664
M 32
M 33
M 34
M 35
WL 2
R 58
M 36
M 37
M 38
M 39
M 40
M 41
M 42
M 43
M 44
WV 23
M 45
WL 0
s_barrier
M 46
mw 48
M 47
R 72
M 48
ad 84,816
W 0
M 49
M 50
M 51
M 52
W 1
M 53
M 54
M 55
M 56
W 2
M 57
M 58
M 59
R 74
M 60
mw 51
M 61
ad 84,819
W 0
M 62
M 63
M 64
M 65
W 1
M 66
M 67
M 68
M 69
W 2
SA 246,2432
M 70
ae 44,68,192
M 71
WL 2
R 76
M 72
s_bitcmp1_b32 s50, 6
s_cselect_b32 s51, 24576, 0
v_add_u32 v52, s51, v3
M 73
bc 49,17,66,60
XL 46,256
M 74
mh 4
M 75
ad 66,1648
H 0
M 76
XL 48,1024
M 77
M 78
M 79
H 1
M 80
M 81
M 82
XL 14,1792
M 83
WL 2
R 78
M 84
mh 6
M 85
ad 64,1650
H 0
M 86
M 87
M 88
XL 16,2560
M 89
H 1
M 90
M 91
M 92
M 93
bc 48,18,64,56
M 94
mh 8
M 95
WL 2
R 80
M 96
ad 64,1744
H 0
M 97
M 98
M 99
M 100
H 1
M 101
M 102
M 103
M 104
M 105
M 106
M 107
WL 2
R 82
M 108
ae 42,72,48
M 109
s_bfe_u32 s52, s50, 0x10005
v_mad_i32_i24 v53, v51, s52, v50
SA 248,128
M 110
M 111
M 112
M 113
M 114
M 115
WV 29
M 116
WL 0
M 117
s_barrier
M 118
mw 72
M 119
R 0
M 120
ad 84,840
W 0
M 121
M 132
M 133
T 24,10
M 122
T 28,24
M 123
W 1
M 134
M 135
T 32,11
M 124
T 36,25
M 125
W 2
M 136
M 137
T 40,12
M 126
T 44,26
M 127
mw 75
M 138
ad 84,843
W 0
M 139
WL 8
T 48,13
M 128
T 52,27
M 129
M 140
W 1
M 141
T 56,0
M 130
WL 8
T 60,14
M 131
R 2
M 142
W 2
M 143
T 64,1
)"
R"(M 0
WL 8
T 68,15
M 1
R 4
M 2
M 3
SA 250,896
M 4
M 5
M 6
M 7
WL 8
M 8
M 9
WL 2
M 10
M 11
R 6
M 12
M 13
M 14
M 15
M 16
M 17
M 18
M 19
M 20
M 21
M 22
M 23
WL 2
R 8
M 24
M 25
M 26
M 27
M 28
M 29
M 30
M 31
M 32
M 33
M 34
M 35
WL 2
R 10
M 36
M 37
M 38
M 39
M 40
M 41
SA 252,1664
M 42
M 43
WV 27
M 44
WL 0
M 45
s_barrier
M 46
mw 0
M 47
R 24
M 48
ad 84,864
W 0
M 49
M 50
M 51
M 52
W 1
M 53
M 54
M 55
M 56
W 2
M 57
M 58
M 59
R 26
M 60
mw 3
M 61
ad 84,867
W 0
M 62
M 63
M 64
M 65
W 1
M 66
M 67
M 68
M 69
W 2
M 70
M 71
WL 2
R 28
M 72
M 73
bc 49,18,66,60
M 74
mh 10
M 75
ad 66,1744
H 0
M 76
ae 44,68,192
M 77
s_bfe_u32 s51, s50, 0x10007
v_mad_i32_i24 v52, v51, s51, v50
XL 42,256
M 78
M 79
H 1
SA 254,2432
M 80
XL 44,1024
M 81
M 82
M 83
WL 2
R 30
M 84
mh 12
M 85
ad 64,1746
H 0
M 86
XL 34,1792
M 87
M 88
M 89
H 1
M 90
M 91
M 92
XL 36,2560
M 93
bc 48,19,64,56
M 94
mh 0
M 95
WL 2
R 32
M 96
ad 64,1840
H 0
M 97
M 98
M 99
M 100
H 1
M 101
M 102
M 103
M 104
M 105
M 106
M 107
WL 2
R 34
M 108
M 109
M 110
M 111
M 112
M 113
M 114
M 115
M 116
WV 28
M 117
WL 0
s_barrier
M 118
mw 24
M 119
R 48
M 120
ad 84,888
W 0
M 121
ae 42,72,48
M 132
s_bitcmp1_b32 s50, 4
s_cselect_b32 s52, 24576, 0
v_add_u32 v53, s52, v3
M 133
T 24,2
M 122
T 28,16
M 123
W 1
ST 18,256
M 134
UL 6,46
UH 7,46
M 135
T 32,3
M 124
T 36,17
M 125
W 2
UL 8,47
M 136
UH 9,47
AD 160,6
M 137
T 40,4
M 126
T 44,18
M 127
mw 27
AD 184,7
AD 208,8
M 138
ad 84,891
W 0
M 139
WL 8
T 48,5
M 128
T 52,19
M 129
AD 232,9
UL 6,48
M 140
W 1
UH 7,48
M 141
T 56,6
M 130
WL 8
T 60,20
M 131
R 50
M 142
W 2
UL 8,49
M 143
T 64,7
M 0
WL 8
T 68,21
M 1
R 52
M 2
UH 9,49
AD 161,6
M 3
AD 185,7
AD 209,8
M 4
AD 233,9
UL 6,14
M 5
UH 7,14
UL 8,15
M 6
UH 9,15
AD 162,6
M 7
WL 8
AD 186,7
AD 210,8
M 8
AD 234,9
UL 6,16
M 9
WL 2
UH 7,16
UL 8,17
M 10
UH 9,17
AD 163,6
M 11
R 54
M 12
AD 187,7
AD 211,8
M 13
AD 235,9
M 14
M 15
M 16
M 17
M 18
M 19
ST 20,1024
M 20
M 21
M 22
M 23
WL 2
R 56
M 24
M 25
M 26
M 27
M 28
M 29
M 30
M 31
M 32
M 33
M 34
M 35
WL 2
R 58
M 36
M 37
M 38
M 39
M 40
M 41
M 42
M 43
M 44
WV 27
M 45
WL 0
s_barrier
M 46
mw 48
M 47
R 72
M 48
ad 84,912
W 0
M 49
M 50
M 51
M 52
W 1
M 53
M 54
M 55
M 56
W 2
M 57
ST 22,1792
M 58
M 59
R 74
M 60
mw 51
M 61
ad 84,915
W 0
M 62
M 63
M 64
M 65
W 1
M 66
M 67
M 68
M 69
W 2
M 70
M 71
WL 2
R 76
M 72
M 73
bc 49,19,66,60
M 74
mh 2
M 75
ad 66,1840
H 0
M 76
M 77
M 78
ae 44,70,192
M 79
H 1
M 80
s_bitcmp1_b32 s50, 8
s_cselect_b32 s51, 24576, 0
v_add_u32 v52, s51, v3
M 81
XL 38,256
M 82
M 83
WL 2
R 78
M 84
mh 4
XL 40,1024
M 85
ad 64,1842
H 0
M 86
M 87
M 88
)"
R"(M 89
H 1
M 90
XL 18,1792
M 91
M 92
M 93
bc 48,20,64,56
M 94
mh 6
M 95
WL 2
R 80
M 96
ad 64,1936
H 0
M 97
ST 24,2560
XL 46,2560
M 98
M 99
M 100
H 1
M 101
M 102
M 103
M 104
PA 12288
DF
M 105
M 106
M 107
WL 2
R 82
M 108
M 109
M 110
M 111
M 112
M 113
M 114
M 115
M 116
WV 30
M 117
WL 0
s_barrier
M 118
mw 72
M 119
R 0
M 120
ad 84,936
W 0
M 121
M 132
M 133
T 24,8
M 122
T 28,22
M 123
W 1
M 134
M 135
T 32,9
M 124
T 36,23
M 125
W 2
M 136
UL 6,42
UH 7,42
M 137
T 40,10
M 126
T 44,24
M 127
mw 75
UL 8,43
UH 9,43
M 138
ad 84,939
W 0
M 139
WL 8
T 48,11
M 128
T 52,25
M 129
ae 42,72,48
AD 164,6
M 140
s_bfe_u32 s52, s50, 0x10005
W 1
v_mad_i32_i24 v53, v51, s52, v50
M 141
T 56,12
M 130
WL 8
T 60,26
M 131
R 2
M 142
W 2
ST 26,256
M 143
T 64,13
M 0
WL 8
T 68,27
M 1
R 4
M 2
AD 188,7
AD 212,8
M 3
AD 236,9
UL 6,44
M 4
UH 7,44
UL 8,45
M 5
UH 9,45
AD 165,6
M 6
AD 189,7
AD 213,8
M 7
WL 8
AD 237,9
UL 6,34
M 8
UH 7,34
UL 8,35
M 9
WL 2
UH 9,35
AD 166,6
M 10
AD 190,7
AD 214,8
M 11
R 6
M 12
WV 36
AD 238,9
UL 6,36
M 13
UH 7,36
UL 8,37
M 14
UH 9,37
AD 167,6
M 15
AD 191,7
AD 215,8
M 16
AD 239,9
M 17
M 18
M 19
M 20
M 21
M 22
M 23
WL 2
R 8
M 24
M 25
M 26
M 27
M 28
M 29
M 30
M 31
M 32
M 33
M 34
M 35
WL 2
R 10
M 36
ST 28,1024
M 37
M 38
M 39
M 40
M 41
M 42
M 43
WV 28
M 44
WL 0
M 45
s_barrier
M 46
mw 0
M 47
R 24
M 48
ad 84,960
W 0
M 49
M 50
M 51
M 52
W 1
M 53
M 54
M 55
M 56
W 2
M 57
M 58
M 59
R 26
M 60
mw 3
M 61
ad 84,963
W 0
M 62
M 63
M 64
M 65
W 1
M 66
M 67
M 68
M 69
W 2
M 70
M 71
WL 2
R 28
M 72
M 73
bc 49,20,66,60
M 74
mh 8
ST 30,1792
M 75
ad 66,1936
H 0
M 76
M 77
M 78
M 79
H 1
M 80
M 81
M 82
ae 44,70,192
M 83
WL 2
R 30
M 84
mh 10
M 85
ad 64,1938
H 0
M 86
s_bfe_u32 s51, s50, 0x10009
v_mad_i32_i24 v52, v51, s51, v50
XL 48,256
M 87
M 88
XL 14,1024
M 89
H 1
M 90
M 91
M 92
M 93
bc 48,21,64,56
M 94
mh 12
XL 16,1792
M 95
WL 2
R 32
M 96
ad 64,2032
H 0
M 97
M 98
M 99
M 100
H 1
XL 20,2560
M 101
M 102
M 103
M 104
M 105
M 106
M 107
WL 2
R 34
M 108
M 109
M 110
M 111
M 112
ST 32,2560
M 113
M 114
M 115
WV 32
M 116
WL 0
M 117
s_barrier
M 118
mw 24
M 119
R 48
M 120
ad 84,984
W 0
M 121
M 132
M 133
T 24,0
M 122
T 28,14
M 123
W 1
M 134
M 135
T 32,1
M 124
T 36,15
M 125
W 2
M 136
M 137
T 40,2
M 126
T 44,16
M 127
mw 27
M 138
ad 84,987
W 0
M 139
WL 8
T 48,3
M 128
T 52,17
M 129
UL 6,38
UH 7,38
M 140
W 1
UL 8,39
M 141
T 56,4
M 130
WL 8
T 60,18
M 131
R 50
M 142
W 2
UH 9,39
M 143
T 64,5
M 0
WL 8
T 68,19
M 1
R 52
M 2
AD 168,6
AD 192,7
M 3
AD 216,8
AD 240,9
M 4
UL 6,40
UH 7,40
M 5
UL 8,41
UH 9,41
M 6
AD 169,6
AD 193,7
M 7
WL 8
AD 217,8
AD 241,9
M 8
WV 37
UL 6,18
UH 7,18
M 9
WL 2
UL 8,19
UH 9,19
)"
R"(M 10
AD 170,6
AD 194,7
M 11
R 54
M 12
AD 218,8
AD 242,9
M 13
WV 34
UL 6,46
UH 7,46
M 14
UL 8,47
UH 9,47
M 15
AD 171,6
AD 195,7
M 16
AD 219,8
AD 243,9
M 17
M 18
M 19
M 20
M 21
M 22
M 23
WL 2
R 56
M 24
M 25
M 26
M 27
M 28
M 29
M 30
M 31
M 32
M 33
M 34
M 35
WL 2
R 58
M 36
M 37
M 38
M 39
M 40
M 41
M 42
M 43
M 44
WV 26
M 45
WL 0
s_barrier
M 46
mw 48
M 47
R 72
M 48
ad 84,1008
W 0
M 49
M 50
M 51
M 52
W 1
M 53
M 54
M 55
M 56
W 2
M 57
M 58
M 59
R 74
M 60
mw 51
M 61
ad 84,1011
W 0
M 62
M 63
M 64
M 65
W 1
M 66
M 67
M 68
M 69
W 2
M 70
M 71
WL 2
R 76
M 72
M 73
bc 49,21,66,60
M 74
mh 0
M 75
ad 66,2032
H 0
M 76
M 77
M 78
M 79
H 1
M 80
M 81
M 82
M 83
WL 2
R 78
M 84
mh 2
M 85
ad 64,2034
H 0
M 86
ae 44,68,240
M 87
s_bitcmp1_b32 s50, 10
s_cselect_b32 s51, 24576, 0
v_add_u32 v52, s51, v3
M 88
XL 22,256
M 89
H 1
M 90
M 91
M 92
XL 24,1024
M 93
bc 48,22,64,56
M 94
mh 4
M 95
WL 2
R 80
M 96
ad 64,2128
H 0
M 97
M 98
XL 26,1792
M 99
M 100
H 1
M 101
M 102
M 103
M 104
XL 42,2560
M 105
M 106
M 107
WL 2
R 82
M 108
M 109
M 110
M 111
M 112
M 113
M 114
M 115
M 116
WV 27
M 117
WL 0
s_barrier
M 118
mw 72
M 119
R 0
M 120
ad 84,1032
W 0
M 121
M 132
M 133
T 24,6
M 122
T 28,20
M 123
W 1
M 134
M 135
T 32,7
M 124
T 36,21
M 125
W 2
M 136
M 137
T 40,8
M 126
T 44,22
M 127
mw 75
M 138
ad 84,1035
W 0
M 139
WL 8
T 48,9
M 128
T 52,23
M 129
M 140
W 1
UL 6,48
M 141
T 56,10
M 130
WL 8
T 60,24
M 131
R 2
M 142
W 2
UH 7,48
M 143
T 64,11
M 0
WL 8
T 68,25
M 1
R 4
M 2
UL 8,49
UH 9,49
M 3
AD 172,6
AD 196,7
M 4
AD 220,8
AD 244,9
M 5
UL 6,14
UH 7,14
M 6
UL 8,15
UH 9,15
M 7
WL 8
AD 173,6
AD 197,7
M 8
WL 2
AD 221,8
AD 245,9
M 9
WV 32
UL 6,16
UH 7,16
M 10
UL 8,17
UH 9,17
M 11
R 6
M 12
AD 174,6
AD 198,7
M 13
AD 222,8
AD 246,9
M 14
WV 29
UL 6,20
UH 7,20
M 15
UL 8,21
UH 9,21
M 16
AD 175,6
AD 199,7
M 17
AD 223,8
AD 247,9
M 18
M 19
M 20
M 21
M 22
M 23
WL 2
R 8
M 24
M 25
M 26
M 27
M 28
M 29
M 30
M 31
M 32
M 33
M 34
M 35
WL 2
R 10
M 36
M 37
M 38
M 39
M 40
M 41
M 42
M 43
M 44
WV 22
M 45
WL 0
s_barrier
M 46
mw 0
M 47
R 24
M 48
ad 84,1056
W 0
M 49
M 50
M 51
M 52
W 1
M 53
M 54
M 55
M 56
W 2
M 57
M 58
M 59
R 26
M 60
mw 3
M 61
ad 84,1059
W 0
M 62
M 63
M 64
M 65
W 1
M 66
M 67
M 68
M 69
W 2
M 70
M 71
WL 2
R 28
M 72
M 73
bc 49,22,66,60
M 74
mh 6
M 75
ad 66,2128
H 0
M 76
M 77
M 78
M 79
H 1
M 80
M 81
M 82
M 83
WL 2
R 30
M 84
mh 8
M 85
ad 64,2130
H 0
M 86
M 87
M 88
M 89
H 1
M 90
ae 44,68,240
M 91
s_bfe_u32 s51, s50, 0x1000b
v_mad_i32_i24 v52, v51, s51, v50
XL 44,256
M 92
M 93
bc 48,23,64,56
M 94
mh 10
M 95
WL 2
)"
R"(R 32
M 96
ad 64,2224
H 0
M 97
XL 34,1024
M 98
M 99
M 100
H 1
M 101
M 102
XL 36,1792
M 103
M 104
M 105
M 106
M 107
WL 2
R 34
M 108
XL 28,2560
M 109
M 110
M 111
M 112
M 113
M 114
M 115
WV 27
M 116
WL 0
M 117
s_barrier
M 118
mw 24
M 119
R 48
M 120
ad 84,1080
W 0
M 121
M 132
M 133
T 24,12
M 122
T 28,26
M 123
W 1
M 134
M 135
T 32,13
M 124
T 36,27
M 125
W 2
M 136
M 137
T 40,0
M 126
T 44,14
M 127
mw 27
M 138
ad 84,1083
W 0
M 139
WL 8
T 48,1
M 128
T 52,15
M 129
M 140
W 1
M 141
T 56,2
M 130
WL 8
T 60,16
M 131
R 50
M 142
W 2
UL 6,22
M 143
T 64,3
M 0
WL 8
T 68,17
M 1
R 52
M 2
UH 7,22
UL 8,23
M 3
UH 9,23
AD 176,6
M 4
AD 200,7
AD 224,8
M 5
WV 32
AD 248,9
UL 6,24
M 6
UH 7,24
UL 8,25
M 7
WL 8
UH 9,25
AD 177,6
M 8
WL 2
AD 201,7
AD 225,8
M 9
WV 30
AD 249,9
UL 6,26
M 10
UH 7,26
UL 8,27
M 11
R 54
M 12
UH 9,27
AD 178,6
M 13
AD 202,7
AD 226,8
M 14
WV 28
AD 250,9
UL 6,42
M 15
UH 7,42
UL 8,43
M 16
UH 9,43
AD 179,6
M 17
AD 203,7
AD 227,8
M 18
AD 251,9
M 19
M 20
M 21
M 22
M 23
WL 2
R 56
M 24
M 25
M 26
M 27
M 28
M 29
M 30
M 31
M 32
M 33
M 34
M 35
WL 2
R 58
M 36
M 37
M 38
M 39
M 40
M 41
M 42
M 43
M 44
WV 22
M 45
WL 0
s_barrier
M 46
mw 48
M 47
R 72
M 48
ad 84,1104
W 0
M 49
M 50
M 51
M 52
W 1
M 53
M 54
M 55
M 56
W 2
M 57
M 58
M 59
R 74
M 60
mw 51
M 61
ad 84,1107
W 0
M 62
M 63
M 64
M 65
W 1
M 66
M 67
M 68
M 69
W 2
M 70
M 71
WL 2
R 76
M 72
M 73
bc 49,23,66,60
M 74
mh 12
M 75
ad 66,2224
H 0
M 76
M 77
M 78
M 79
H 1
M 80
M 81
M 82
M 83
WL 2
R 78
M 84
mh 0
M 85
ad 64,2226
H 0
M 86
M 87
M 88
M 89
H 1
M 90
M 91
M 92
M 93
bc 49,0,66,60
M 94
mh 2
M 95
WL 2
R 80
M 96
ad 66,16
H 0
M 97
M 98
M 99
M 100
H 1
M 101
M 102
M 103
M 104
PA 12288
DF
M 105
M 106
M 107
WL 2
R 82
M 108
M 109
M 110
M 111
M 112
M 113
M 114
M 115
M 116
WV 25
M 117
WL 0
s_barrier
M 118
mw 72
M 119
R 0
M 120
ad 84,1128
W 0
M 121
M 132
M 133
T 24,4
M 122
T 28,18
M 123
W 1
M 134
M 135
T 32,5
M 124
T 36,19
M 125
W 2
M 136
M 137
T 40,6
M 126
T 44,20
M 127
mw 75
M 138
ad 84,1131
W 0
M 139
WL 8
T 48,7
M 128
T 52,21
M 129
M 140
W 1
M 141
T 56,8
M 130
WL 8
T 60,22
M 131
R 2
M 142
W 2
M 143
T 64,9
M 0
WL 8
T 68,23
M 1
R 4
M 2
WV 30
UL 6,44
UH 7,44
M 3
UL 8,45
UH 9,45
M 4
AD 180,6
AD 204,7
M 5
AD 228,8
AD 252,9
M 6
WV 28
UL 6,34
UH 7,34
M 7
WL 8
UL 8,35
UH 9,35
M 8
AD 181,6
AD 205,7
M 9
WL 2
AD 229,8
AD 253,9
M 10
WV 25
UL 6,36
UH 7,36
M 11
R 6
M 12
UL 8,37
UH 9,37
M 13
AD 182,6
AD 206,7
M 14
AD 230,8
AD 254,9
M 15
UL 6,28
UH 7,28
M 16
UL 8,29
UH 9,29
M 17
AD 183,6
AD 207,7
M 18
AD 231,8
)"
R"(AD 255,9
M 19
M 20
M 21
M 22
M 23
WL 2
R 8
M 24
M 25
M 26
M 27
M 28
M 29
M 30
M 31
M 32
M 33
M 34
M 35
WL 2
R 10
M 36
M 37
M 38
M 39
M 40
M 41
M 42
M 43
M 44
WV 19
M 45
WL 0
s_barrier
M 46
mw 0
M 47
R 24
M 48
ad 84,0
W 0
M 49
M 50
M 51
M 52
W 1
M 53
M 54
M 55
M 56
W 2
M 57
M 58
M 59
R 26
M 60
mw 3
M 61
ad 84,3
W 0
M 62
M 63
M 64
M 65
W 1
M 66
M 67
M 68
M 69
W 2
M 70
M 71
WL 2
R 28
M 72
M 73
bc 48,0,64,56
M 74
mh 4
M 75
ad 64,18
H 0
M 76
M 77
M 78
M 79
H 1
M 80
M 81
M 82
M 83
WL 2
R 30
M 84
mh 6
M 85
ad 64,32
H 0
M 86
M 87
M 88
M 89
H 1
M 90
M 91
M 92
M 93
bc 48,1,64,56
M 94
mh 8
M 95
WL 2
R 32
M 96
ad 64,114
H 0
M 97
M 98
M 99
M 100
H 1
M 101
M 102
M 103
M 104
M 105
M 106
M 107
WL 2
R 34
M 108
M 109
M 110
M 111
M 112
M 113
M 114
M 115
M 116
WV 21
M 117
WL 0
s_barrier
M 118
mw 24
M 119
R 48
M 120
ad 84,24
W 0
M 121
M 132
M 133
T 24,10
M 122
T 28,24
M 123
W 1
M 134
M 135
T 32,11
M 124
T 36,25
M 125
W 2
M 136
M 137
T 40,12
M 126
T 44,26
M 127
mw 27
M 138
ad 84,27
W 0
M 139
WL 8
T 48,13
M 128
T 52,27
M 129
M 140
W 1
M 141
T 56,0
M 130
WL 8
T 60,14
M 131
R 50
M 142
W 2
M 143
T 64,1
M 0
WL 8
T 68,15
M 1
R 52
M 2
M 3
M 4
M 5
M 6
M 7
WL 8
M 8
M 9
WL 2
M 10
M 11
R 54
M 12
M 13
M 14
M 15
M 16
M 17
M 18
M 19
M 20
M 21
M 22
M 23
WL 2
R 56
M 24
M 25
M 26
M 27
M 28
M 29
M 30
M 31
M 32
M 33
M 34
M 35
WL 2
R 58
M 36
M 37
M 38
M 39
M 40
AR 6,72
AR 7,96
M 41
AR 8,120
s_nop 0
AR 9,144
M 42
EF 6
EF 7
M 43
WV 18
EF 8
EF 9
M 44
WL 0
CV 30,6,7
CV 31,8,9
M 45
s_barrier
AR 6,73
AR 7,97
M 46
mw 48
AR 8,121
AR 9,145
M 47
R 72
M 48
ad 84,48
W 0
M 49
EF 6
EF 7
M 50
EF 8
EF 9
M 51
CV 32,6,7
CV 33,8,9
M 52
W 1
AR 6,74
M 53
AR 7,98
AR 8,122
M 54
AR 9,146
EF 6
M 55
EF 7
EF 8
M 56
W 2
EF 9
M 57
CV 38,6,7
CV 39,8,9
M 58
AR 6,75
AR 7,99
M 59
R 74
M 60
mw 51
AR 8,123
AR 9,147
M 61
ad 84,51
W 0
M 62
EF 6
EF 7
M 63
EF 8
EF 9
M 64
CV 40,6,7
CV 41,8,9
M 65
W 1
BA 72,0
M 66
BA 96,256
BA 120,512
M 67
BA 144,768
M 68
ae 42,72,192
AR 6,76
M 69
W 2
AR 7,100
M 70
s_bitcmp1_b32 s50, 6
s_cselect_b32 s52, 24576, 0
v_add_u32 v53, s52, v3
M 71
WL 4
R 76
M 72
ST 30,0
AR 8,124
M 73
mh 10
AR 9,148
EF 6
M 74
ad 64,128
H 0
M 75
EF 7
EF 8
M 76
EF 9
CV 18,6,7
M 77
CV 19,8,9
AR 6,77
M 78
H 1
AR 7,101
M 79
AR 8,125
AR 9,149
M 80
EF 6
EF 7
M 81
EF 8
EF 9
M 82
bc 49,1,66,60
CV 46,6,7
M 83
R 78
M 84
mh 12
CV 47,8,9
AR 6,78
M 85
ad 66,128
H 0
M 86
AR 7,102
AR 8,126
M 87
AR 9,150
EF 6
M 88
EF 7
EF 8
M 89
H 1
EF 9
)"
R"(M 90
CV 48,6,7
CV 49,8,9
M 91
AR 6,79
AR 7,103
M 92
AR 8,127
AR 9,151
M 93
bc 48,2,64,56
EF 6
M 94
mh 0
EF 7
EF 8
M 95
WL 0
R 80
M 96
ad 64,224
H 0
M 97
EF 9
CV 14,6,7
M 98
CV 15,8,9
BA 76,0
M 99
BA 100,256
BA 124,512
M 100
H 1
BA 148,768
M 101
EF 64
EF 88
M 102
EF 112
EF 136
M 103
CV 16,64,88
CV 17,112,136
M 104
EF 65
EF 89
M 105
EF 113
EF 137
M 106
CV 20,65,89
CV 21,113,137
M 107
R 82
M 108
EF 66
EF 90
M 109
EF 114
EF 138
M 110
ST 32,768
CV 22,66,90
M 111
CV 23,114,138
EF 67
M 112
EF 91
EF 115
M 113
EF 139
CV 24,67,91
M 114
CV 25,115,139
BV 64,1024
M 115
WV 22
BV 88,1280
BV 112,1536
M 116
WL 3
BV 136,1792
M 117
s_barrier
EF 68
EF 92
M 118
mw 72
EF 116
EF 140
M 119
R 0
M 120
ad 84,72
W 0
M 121
CV 26,68,92
CV 27,116,140
M 132
EF 69
EF 93
M 133
T 24,6
M 122
T 28,20
M 123
W 1
EF 117
M 134
EF 141
CV 42,69,93
M 135
T 32,7
M 124
T 36,21
M 125
W 2
CV 43,117,141
M 136
EF 70
EF 94
M 137
WL 8
T 40,2
M 126
T 44,16
M 127
mw 75
EF 118
EF 142
M 138
ad 84,75
W 0
M 139
T 48,3
M 128
WL 8
T 52,17
M 129
CV 44,70,94
CV 45,118,142
M 140
W 1
EF 71
M 141
T 56,4
M 130
T 60,18
M 131
WL 8
R 2
M 142
W 2
EF 95
M 143
T 64,5
M 4
T 68,19
M 5
WL 8
R 4
M 6
EF 119
EF 143
M 7
WL 8
CV 34,71,95
CV 35,119,143
M 8
ST 38,1536
BV 68,1024
M 9
WL 3
BV 92,1280
BV 116,1536
M 10
BV 140,1792
M 11
EF 160
EF 184
M 0
EF 208
EF 232
M 1
CV 10,160,184
CV 11,208,232
M 2
AW 160,10
AW 161,11
M 3
R 6
M 16
EF 161
EF 185
M 17
EF 209
EF 233
M 18
CV 10,161,185
CV 11,209,233
M 19
AW 162,10
AW 163,11
M 20
EF 162
EF 186
M 21
EF 210
EF 234
M 22
CV 10,162,186
CV 11,210,234
M 23
AW 164,10
AW 165,11
M 12
EF 163
EF 187
M 13
EF 211
EF 235
M 14
CV 10,163,187
CV 11,211,235
M 15
WL 2
R 8
M 28
AW 166,10
AW 167,11
M 29
BV 160,2048
BV 184,2304
M 30
BV 208,2560
BV 232,2816
M 31
EF 164
EF 188
M 32
EF 212
EF 236
M 33
CV 10,164,188
CV 11,212,236
M 34
AW 168,10
AW 169,11
M 35
EF 165
EF 189
M 24
EF 213
EF 237
M 25
CV 10,165,189
CV 11,213,237
M 26
AW 170,10
AW 171,11
M 27
WL 6
R 10
M 40
EF 166
EF 190
M 41
EF 214
EF 238
M 42
CV 10,166,190
CV 11,214,238
M 43
AW 172,10
AW 173,11
M 44
EF 167
EF 191
M 45
EF 215
EF 239
M 46
ST 40,2304
CV 10,167,191
M 47
WV 22
CV 11,215,239
AW 174,10
M 36
WL 0
AW 175,11
BV 164,2048
M 37
s_barrier
BV 188,2304
BV 212,2560
M 38
mw 0
BV 236,2816
M 39
R 24
M 52
ad 84,96
W 0
M 53
AR 6,80
AR 7,104
M 54
AR 8,128
AR 9,152
M 55
EF 6
EF 7
M 56
W 1
EF 8
M 57
EF 9
CV 10,6,7
M 58
CV 11,8,9
AW 176,10
M 59
AW 177,11
AR 6,81
M 48
W 2
AR 7,105
M 49
AR 8,129
AR 9,153
M 50
EF 6
EF 7
M 51
R 26
M 64
mw 3
EF 8
EF 9
M 65
ad 84,99
W 0
M 66
CV 10,6,7
CV 11,8,9
M 67
AW 178,10
AW 179,11
M 68
AR 6,82
AR 7,106
M 69
W 1
AR 8,130
M 70
AR 9,154
EF 6
M 71
EF 7
EF 8
M 60
EF 9
CV 10,6,7
M 61
W 2
CV 11,8,9
M 62
AW 180,10
AW 181,11
M 63
WL 0
R 28
M 76
AR 6,83
AR 7,107
M 77
bc 49,2,66,60
AR 8,131
M 78
mh 2
AR 9,155
EF 6
M 79
ad 66,224
H 0
M 80
EF 7
EF 8
M 81
EF 9
CV 10,6,7
M 82
CV 11,8,9
AW 182,10
M 83
H 1
AW 183,11
M 72
ae 42,72,192
)"
R"(BA 80,0
M 73
s_bfe_u32 s52, s50, 0x10007
v_mad_i32_i24 v53, v51, s52, v50
ST 18,0
M 74
BA 104,256
BA 128,512
M 75
R 30
M 88
mh 4
BA 152,768
M 89
ad 64,226
H 0
M 90
AR 6,84
AR 7,108
M 91
AR 8,132
AR 9,156
M 92
EF 6
EF 7
M 93
H 1
EF 8
M 94
EF 9
CV 10,6,7
M 95
CV 11,8,9
AW 184,10
M 84
AW 185,11
AR 6,85
M 85
bc 48,3,64,56
AR 7,109
M 86
mh 6
AR 8,133
AR 9,157
M 87
WL 1
R 32
M 100
ad 64,320
H 0
M 101
EF 6
EF 7
M 102
EF 8
EF 9
M 103
CV 10,6,7
CV 11,8,9
M 104
H 1
AW 186,10
M 105
AW 187,11
AR 6,86
M 106
AR 7,110
AR 8,134
M 107
AR 9,158
EF 6
M 96
EF 7
EF 8
M 97
EF 9
CV 10,6,7
M 98
CV 11,8,9
AW 188,10
M 99
R 34
M 112
AW 189,11
AR 6,87
M 113
AR 7,111
AR 8,135
M 114
AR 9,159
EF 6
M 115
EF 7
EF 8
M 116
EF 9
CV 10,6,7
M 117
CV 11,8,9
AW 190,10
M 118
AW 191,11
BA 84,0
M 119
WV 24
BA 108,256
BA 132,512
M 108
WL 3
BA 156,768
M 109
s_barrier
EF 72
EF 96
M 110
mw 24
EF 120
EF 144
M 111
R 48
M 124
ad 84,120
W 0
M 125
ST 46,768
CV 10,72,96
M 136
CV 11,120,144
AW 192,10
M 137
T 40,12
M 126
T 44,26
M 127
W 1
AW 193,11
M 138
EF 73
EF 97
M 139
T 48,13
M 128
T 52,27
M 129
W 2
EF 121
M 140
EF 145
CV 10,73,97
M 141
WL 8
T 56,8
M 130
T 60,22
M 131
mw 27
CV 11,121,145
AW 194,10
M 142
ad 84,123
W 0
M 143
T 64,9
M 120
WL 8
T 68,23
M 121
AW 195,11
EF 74
M 132
W 1
EF 98
M 133
T 24,10
M 122
T 28,24
M 123
WL 8
R 50
M 134
W 2
EF 122
M 135
T 32,11
M 8
T 36,25
M 9
WL 8
R 52
M 10
EF 146
CV 10,74,98
M 11
WL 8
CV 11,122,146
AW 196,10
M 0
AW 197,11
EF 75
M 1
WL 2
EF 99
EF 123
M 2
EF 147
CV 10,75,99
M 3
CV 11,123,147
AW 198,10
M 4
AW 199,11
BV 72,1024
M 5
BV 96,1280
BV 120,1536
M 6
BV 144,1792
M 7
R 54
M 20
EF 76
EF 100
M 21
EF 124
EF 148
M 22
CV 10,76,100
CV 11,124,148
M 23
ST 48,1536
AW 200,10
M 12
AW 201,11
EF 77
M 13
EF 101
EF 125
M 14
EF 149
CV 10,77,101
M 15
CV 11,125,149
AW 202,10
M 16
AW 203,11
EF 78
M 17
EF 102
EF 126
M 18
EF 150
CV 10,78,102
M 19
WL 2
R 56
M 32
CV 11,126,150
AW 204,10
M 33
AW 205,11
EF 79
M 34
EF 103
EF 127
M 35
EF 151
CV 10,79,103
M 24
CV 11,127,151
AW 206,10
M 25
AW 207,11
BV 76,1024
M 26
BV 100,1280
BV 124,1536
M 27
BV 148,1792
M 28
EF 168
EF 192
M 29
EF 216
EF 240
M 30
CV 10,168,192
CV 11,216,240
M 31
WL 6
R 58
M 44
AW 208,10
AW 209,11
M 45
EF 169
EF 193
M 46
EF 217
EF 241
M 47
CV 10,169,193
CV 11,217,241
M 36
AW 210,10
AW 211,11
M 37
EF 170
EF 194
M 38
EF 218
EF 242
M 39
WV 23
CV 10,170,194
CV 11,218,242
M 40
WL 0
AW 212,10
AW 213,11
M 41
s_barrier
EF 171
EF 195
M 42
mw 48
EF 219
EF 243
M 43
R 72
M 56
ad 84,144
W 0
M 57
CV 10,171,195
CV 11,219,243
M 58
AW 214,10
AW 215,11
M 59
BV 168,2048
BV 192,2304
M 48
W 1
BV 216,2560
M 49
ST 14,2304
BV 240,2816
M 50
ae 44,68,384
M 51
s_bitcmp1_b32 s50, 12
s_cselect_b32 s51, 24576, 0
v_add_u32 v52, s51, v3
M 52
W 2
XL 36,0
M 53
XL 28,768
EF 172
M 54
XL 30,1536
EF 196
M 55
R 74
M 68
mw 51
XL 32,2304
EF 220
M 69
ad 84,147
W 0
M 70
EF 244
CV 10,172,196
M 71
CV 11,220,244
AW 216,10
M 60
AW 217,11
EF 173
M 61
W 1
EF 197
M 62
EF 221
EF 245
M 63
CV 10,173,197
CV 11,221,245
M 64
AW 218,10
AW 219,11
M 65
W 2
EF 174
M 66
EF 198
EF 222
M 67
WL 0
)"
R"(R 76
M 80
EF 246
CV 10,174,198
M 81
bc 49,3,66,60
CV 11,222,246
M 82
mh 8
AW 220,10
AW 221,11
M 83
ad 66,320
H 0
M 72
EF 175
EF 199
M 73
EF 223
EF 247
M 74
CV 10,175,199
CV 11,223,247
M 75
H 1
AW 222,10
M 76
AW 223,11
BV 172,2048
M 77
BV 196,2304
BV 220,2560
M 78
BV 244,2816
M 79
R 78
M 92
mh 10
AR 6,88
AR 7,112
M 93
ad 64,322
H 0
M 94
AR 8,136
EF 6
M 95
EF 7
EF 8
M 84
EF 56
CV 10,6,7
M 85
H 1
CV 11,8,56
M 86
AW 224,10
AW 225,11
M 87
ae 42,72,192
AR 6,89
M 88
s_bitcmp1_b32 s50, 6
s_cselect_b32 s52, 24576, 0
v_add_u32 v53, s52, v3
M 89
bc 48,4,64,56
ST 16,128
M 90
mh 12
AR 7,113
AR 8,137
M 91
WL 0
R 80
M 104
ad 64,416
H 0
M 105
EF 6
EF 7
M 106
EF 8
EF 57
M 107
CV 10,6,7
CV 11,8,57
M 96
H 1
AW 226,10
M 97
AW 227,11
AR 6,90
M 98
AR 7,114
AR 8,138
M 99
EF 6
EF 7
M 100
PA 12288
DF
EF 8
M 101
EF 58
CV 10,6,7
M 102
CV 11,8,58
AW 228,10
M 103
R 82
M 116
AW 229,11
AR 6,91
M 117
AR 7,115
AR 8,139
M 118
EF 6
EF 7
M 119
EF 8
EF 59
M 108
CV 10,6,7
CV 11,8,59
M 109
AW 230,10
AW 231,11
M 110
BA 88,0
BA 112,256
M 111
WV 29
BA 136,512
BV 56,768
M 112
WL 4
AR 6,92
AR 7,116
AR 8,140
M 113
s_barrier
EF 6
EF 7
EF 8
M 114
mw 72
EF 60
CV 10,6,7
CV 11,8,60
M 115
R 0
AW 232,10
M 128
ad 84,168
W 0
AW 233,11
M 129
AR 6,93
AR 7,117
AR 8,141
M 140
EF 6
EF 7
EF 8
M 141
T 56,4
EF 61
M 130
T 60,18
CV 10,6,7
M 131
W 1
CV 11,8,61
AW 234,10
M 142
AW 235,11
AR 6,94
AR 7,118
M 143
T 64,5
AR 8,142
M 120
T 68,19
EF 6
M 121
W 2
EF 7
EF 8
M 132
EF 62
CV 10,6,7
CV 11,8,62
M 133
WL 8
T 24,0
AW 236,10
M 122
T 28,14
AW 237,11
M 123
mw 75
ST 20,896
AR 6,95
AR 7,119
M 134
ad 84,171
W 0
AR 8,143
M 135
T 32,1
EF 6
M 124
WL 8
T 36,15
EF 7
M 125
EF 8
EF 63
CV 10,6,7
M 136
W 1
CV 11,8,63
AW 238,10
M 137
T 40,2
AW 239,11
M 126
T 44,16
BA 92,0
M 127
WL 9
R 2
BA 116,256
M 138
W 2
BA 140,512
BV 60,768
M 139
WL 8
T 48,3
M 0
T 52,17
M 1
R 4
M 2
EF 80
EF 104
M 3
WL 12
EF 128
EF 152
M 4
CV 10,80,104
CV 11,128,152
M 5
WL 2
AW 240,10
AW 241,11
M 6
EF 81
EF 105
M 7
EF 129
EF 153
M 8
CV 10,81,105
CV 11,129,153
M 9
AW 242,10
AW 243,11
M 10
EF 82
EF 106
M 11
R 6
M 12
EF 130
EF 154
M 13
CV 10,82,106
CV 11,130,154
M 14
AW 244,10
AW 245,11
M 15
EF 83
EF 107
M 16
EF 131
EF 155
M 17
CV 10,83,107
CV 11,131,155
M 18
AW 246,10
AW 247,11
M 19
BV 80,1024
BV 104,1280
M 20
BV 128,1536
BV 152,1792
M 21
EF 84
EF 108
M 22
EF 132
EF 156
M 23
WL 6
R 8
M 24
CV 10,84,108
CV 11,132,156
M 25
AW 248,10
AW 249,11
M 26
EF 85
EF 109
M 27
ST 22,1664
EF 133
M 28
EF 157
CV 10,85,109
M 29
CV 11,133,157
AW 250,10
M 30
AW 251,11
EF 86
M 31
EF 110
EF 134
M 32
EF 158
CV 10,86,110
M 33
CV 11,134,158
AW 252,10
M 34
AW 253,11
EF 87
M 35
WL 2
R 10
M 36
EF 111
EF 135
M 37
EF 159
CV 10,87,111
M 38
CV 11,135,159
AW 254,10
M 39
AW 255,11
BV 84,1024
M 40
BV 108,1280
BV 132,1536
M 41
BV 156,1792
M 42
EF 176
EF 200
M 43
WV 28
EF 224
EF 248
M 44
WL 4
CV 38,176,200
CV 39,224,248
M 45
s_barrier
EF 177
EF 201
M 46
mw 0
EF 225
EF 249
M 47
R 24
M 48
ad 84,192
W 0
M 49
CV 40,177,201
CV 41,225,249
M 50
EF 178
EF 202
M 51
EF 226
EF 250
M 52
)"
R"(W 1
CV 18,178,202
M 53
CV 19,226,250
EF 179
M 54
EF 203
EF 227
M 55
EF 251
CV 46,179,203
M 56
W 2
CV 47,227,251
M 57
WL 2
BV 176,2048
BV 200,2304
M 58
BV 224,2560
BV 248,2816
M 59
R 26
M 60
mw 3
EF 180
EF 204
M 61
ad 84,195
W 0
M 62
EF 228
EF 252
M 63
CV 48,180,204
CV 49,228,252
M 64
EF 181
EF 205
M 65
W 1
ST 24,2432
M 66
EF 229
EF 253
M 67
CV 14,181,205
CV 15,229,253
M 68
EF 182
EF 206
M 69
W 2
EF 230
M 70
EF 254
CV 16,182,206
M 71
WL 0
R 28
M 72
CV 17,230,254
EF 183
M 73
bc 49,4,66,60
EF 207
M 74
mh 0
EF 231
EF 255
M 75
ad 66,416
H 0
M 76
CV 20,183,207
CV 21,231,255
M 77
BV 180,2048
BV 204,2304
M 78
BV 228,2560
BV 252,2816
M 79
WV 27
H 1
UL 6,36
M 80
ae 44,68,384
M 81
s_bfe_u32 s51, s50, 0x1000d
v_mad_i32_i24 v52, v51, s51, v50
XL 22,0
M 82
XL 24,768
UH 7,36
M 83
R 30
M 84
mh 2
UL 8,37
UH 9,37
M 85
ad 64,418
H 0
M 86
AR 10,72
AR 11,96
M 87
AR 12,120
AR 13,144
M 88
AD 10,6
AD 11,7
M 89
H 1
AD 12,8
M 90
AD 13,9
AW 72,10
M 91
AW 96,11
AW 120,12
M 92
AW 144,13
UL 6,28
M 93
bc 48,5,64,56
UH 7,28
M 94
mh 4
UL 8,29
UH 9,29
M 95
WL 0
R 32
M 96
ad 64,512
H 0
M 97
AR 10,73
AR 11,97
M 98
AR 12,121
AR 13,145
M 99
AD 10,6
AD 11,7
M 100
H 1
AD 12,8
M 101
AD 13,9
AW 73,10
M 102
AW 97,11
AW 121,12
M 103
ae 42,72,192
AW 145,13
M 104
s_bfe_u32 s52, s50, 0x10007
v_mad_i32_i24 v53, v51, s52, v50
ST 26,128
M 105
s_nop 0
XL 26,1536
UL 6,30
M 106
UH 7,30
UL 8,31
M 107
R 34
M 108
UH 9,31
AR 10,74
M 109
AR 11,98
AR 12,122
M 110
AR 13,146
AD 10,6
M 111
AD 11,7
AD 12,8
M 112
AD 13,9
AW 74,10
M 113
AW 98,11
AW 122,12
M 114
AW 146,13
UL 6,32
M 115
WV 29
UH 7,32
UL 8,33
M 116
WL 0
UH 9,33
AR 10,75
M 117
s_barrier
AR 11,99
AR 12,123
M 118
mw 24
AR 13,147
AD 10,6
M 119
R 48
M 120
ad 84,216
W 0
M 121
AD 11,7
AD 12,8
M 132
AD 13,9
AW 75,10
M 133
T 24,6
M 122
T 28,20
M 123
W 1
AW 99,11
M 134
AW 123,12
AW 147,13
M 135
T 32,7
M 124
T 36,21
M 125
W 2
M 136
M 137
T 40,8
M 126
T 44,22
M 127
mw 27
M 138
ad 84,219
W 0
M 139
WL 8
T 48,9
M 128
T 52,23
M 129
M 140
W 1
M 141
T 56,10
M 130
WL 8
T 60,24
M 131
R 50
M 142
W 2
ST 42,896
M 143
T 64,11
M 0
WL 8
T 68,25
M 1
R 52
M 2
XL 42,2304
M 3
M 4
ae 44,70,384
M 5
M 6
s_bitcmp1_b32 s50, 14
s_cselect_b32 s51, 24576, 0
v_add_u32 v52, s51, v3
M 7
WL 8
XL 36,0
M 8
XL 28,768
M 9
WL 2
XL 30,1536
M 10
XL 32,2304
M 11
R 54
M 12
M 13
M 14
M 15
M 16
M 17
M 18
M 19
M 20
M 21
M 22
M 23
WL 2
R 56
M 24
M 25
M 26
M 27
M 28
M 29
M 30
M 31
M 32
M 33
M 34
M 35
WL 2
R 58
M 36
ST 44,1664
M 37
M 38
M 39
M 40
M 41
M 42
WV 20
UL 6,22
UH 7,22
M 43
UL 8,23
UH 9,23
M 44
WL 0
AR 10,76
AR 11,100
M 45
s_barrier
AR 12,124
AR 13,148
M 46
mw 48
AD 10,6
AD 11,7
M 47
R 72
M 48
ad 84,240
W 0
M 49
AD 12,8
AD 13,9
M 50
AW 76,10
AW 100,11
M 51
AW 124,12
AW 148,13
M 52
WV 20
W 1
UL 6,24
M 53
UH 7,24
UL 8,25
M 54
UH 9,25
AR 10,77
M 55
AR 11,101
AR 12,125
M 56
W 2
AR 13,149
M 57
AD 10,6
AD 11,7
M 58
AD 12,8
AD 13,9
M 59
R 74
M 60
mw 51
AW 77,10
AW 101,11
M 61
ad 84,243
W 0
M 62
AW 125,12
AW 149,13
M 63
WV 17
UL 6,26
UH 7,26
M 64
)"
R"(UL 8,27
UH 9,27
M 65
W 1
AR 10,78
M 66
AR 11,102
AR 12,126
M 67
AR 13,150
AD 10,6
M 68
AD 11,7
AD 12,8
M 69
W 2
AD 13,9
M 70
AW 78,10
AW 102,11
M 71
WL 0
R 76
M 72
WV 11
AW 126,12
AW 150,13
M 73
bc 49,5,66,60
UL 6,42
M 74
mh 6
ST 34,2432
UH 7,42
M 75
ad 66,512
H 0
M 76
UL 8,43
UH 9,43
M 77
AR 10,79
AR 11,103
M 78
AR 12,127
AR 13,151
M 79
H 1
AD 10,6
M 80
AD 11,7
AD 12,8
M 81
AD 13,9
AW 79,10
M 82
AW 103,11
AW 127,12
M 83
R 78
M 84
mh 8
AW 151,13
M 85
ad 64,514
H 0
M 86
M 87
M 88
M 89
H 1
M 90
M 91
M 92
M 93
bc 48,6,64,56
M 94
mh 10
M 95
WL 2
R 80
M 96
ad 64,608
H 0
M 97
M 98
M 99
M 100
H 1
M 101
M 102
M 103
M 104
M 105
M 106
M 107
WL 2
R 82
M 108
M 109
M 110
M 111
M 112
ae 42,72,192
M 113
s_bitcmp1_b32 s50, 6
s_cselect_b32 s52, 24576, 0
v_add_u32 v53, s52, v3
M 114
SA 160,256
M 115
M 116
WL 0
M 117
s_barrier
M 118
mw 72
M 119
R 0
M 120
ad 84,264
W 0
M 121
M 132
M 133
T 24,12
M 122
T 28,26
M 123
W 1
M 134
M 135
T 32,13
M 124
T 36,27
M 125
W 2
M 136
M 137
T 40,0
M 126
T 44,14
M 127
mw 75
M 138
ad 84,267
W 0
M 139
WL 8
T 48,1
M 128
T 52,15
M 129
ae 44,70,384
M 140
s_bfe_u32 s51, s50, 0x1000f
W 1
v_mad_i32_i24 v52, v51, s51, v50
M 141
T 56,2
M 130
WL 8
T 60,16
M 131
R 2
M 142
W 2
XL 44,0
M 143
T 64,3
M 0
WL 8
T 68,17
M 1
R 4
M 2
XL 34,768
M 3
XL 22,1536
M 4
M 5
M 6
M 7
WL 8
M 8
SA 162,1024
XL 24,2304
M 9
WL 2
M 10
M 11
R 6
M 12
M 13
M 14
M 15
M 16
M 17
M 18
M 19
M 20
M 21
M 22
M 23
WL 2
R 8
M 24
M 25
M 26
M 27
M 28
M 29
M 30
M 31
M 32
M 33
M 34
M 35
WL 2
R 10
M 36
M 37
M 38
M 39
M 40
M 41
M 42
M 43
M 44
WV 26
UL 6,36
UH 7,36
M 45
WL 0
s_barrier
UL 8,37
UH 9,37
M 46
mw 0
SA 164,1792
AR 10,80
M 47
R 24
M 48
ad 84,288
W 0
M 49
AR 11,104
AR 12,128
M 50
AR 13,152
AD 10,6
M 51
AD 11,7
AD 12,8
M 52
W 1
AD 13,9
M 53
AW 80,10
AW 104,11
M 54
AW 128,12
AW 152,13
M 55
UL 6,28
UH 7,28
M 56
W 2
UL 8,29
M 57
UH 9,29
AR 10,81
M 58
AR 11,105
AR 12,129
M 59
R 26
M 60
mw 3
AR 13,153
AD 10,6
M 61
ad 84,291
W 0
M 62
AD 11,7
AD 12,8
M 63
AD 13,9
AW 81,10
M 64
AW 105,11
AW 129,12
M 65
W 1
AW 153,13
M 66
UL 6,30
UH 7,30
M 67
UL 8,31
UH 9,31
M 68
AR 10,82
AR 11,106
M 69
W 2
AR 12,130
M 70
AR 13,154
AD 10,6
M 71
WL 0
R 28
M 72
AD 11,7
AD 12,8
M 73
bc 49,6,66,60
AD 13,9
M 74
mh 12
AW 82,10
AW 106,11
M 75
ad 66,608
H 0
M 76
AW 130,12
AW 154,13
M 77
UL 6,32
UH 7,32
M 78
UL 8,33
UH 9,33
M 79
H 1
AR 10,83
M 80
AR 11,107
AR 12,131
M 81
AR 13,155
AD 10,6
M 82
AD 11,7
AD 12,8
M 83
R 30
M 84
mh 0
SA 166,2560
AD 13,9
M 85
ad 64,610
H 0
M 86
AW 83,10
AW 107,11
M 87
AW 131,12
AW 155,13
M 88
M 89
H 1
M 90
M 91
M 92
M 93
bc 48,7,64,56
M 94
mh 2
M 95
WL 2
R 32
M 96
ad 64,704
H 0
M 97
M 98
M 99
M 100
H 1
M 101
M 102
M 103
M 104
M 105
M 106
M 107
WL 2
R 34
M 108
M 109
M 110
M 111
M 112
M 113
)"
R"(M 114
M 115
M 116
WV 28
M 117
WL 0
s_barrier
M 118
mw 24
M 119
R 48
M 120
ad 84,312
W 0
M 121
M 132
ae 42,72,192
M 133
s_bfe_u32 s52, s50, 0x10007
T 24,4
M 122
T 28,18
M 123
W 1
v_mad_i32_i24 v53, v51, s52, v50
M 134
SA 168,256
M 135
T 32,5
M 124
T 36,19
M 125
W 2
M 136
M 137
T 40,6
M 126
T 44,20
M 127
mw 27
M 138
ad 84,315
W 0
M 139
WL 8
T 48,7
M 128
T 52,21
M 129
ae 44,68,432
M 140
W 1
M 141
T 56,8
M 130
WL 8
T 60,22
M 131
R 50
M 142
W 2
M 143
T 64,9
M 0
WL 8
T 68,23
M 1
R 52
M 2
s_bitcmp1_b32 s50, 16
s_cselect_b32 s51, 24576, 0
M 3
v_add_u32 v52, s51, v3
XL 26,0
M 4
XL 42,768
M 5
XL 36,1536
M 6
M 7
WL 8
M 8
M 9
WL 2
M 10
XL 28,2304
M 11
R 54
M 12
M 13
M 14
M 15
M 16
M 17
M 18
M 19
M 20
SA 170,1024
M 21
M 22
M 23
WL 2
R 56
M 24
M 25
M 26
M 27
M 28
M 29
M 30
M 31
M 32
M 33
M 34
M 35
WL 2
R 58
M 36
M 37
M 38
M 39
M 40
M 41
M 42
M 43
M 44
WV 28
M 45
WL 0
s_barrier
M 46
mw 48
UL 6,44
UH 7,44
M 47
R 72
M 48
ad 84,336
W 0
M 49
UL 8,45
UH 9,45
M 50
AR 10,84
AR 11,108
M 51
AR 12,132
AR 13,156
M 52
W 1
AD 10,6
M 53
AD 11,7
AD 12,8
M 54
AD 13,9
AW 84,10
M 55
AW 108,11
AW 132,12
M 56
W 2
AW 156,13
M 57
UL 6,34
UH 7,34
M 58
SA 172,1792
UL 8,35
M 59
R 74
M 60
mw 51
UH 9,35
AR 10,85
M 61
ad 84,339
W 0
M 62
AR 11,109
AR 12,133
M 63
AR 13,157
AD 10,6
M 64
AD 11,7
AD 12,8
M 65
W 1
AD 13,9
M 66
AW 85,10
AW 109,11
M 67
AW 133,12
AW 157,13
M 68
UL 6,22
UH 7,22
M 69
W 2
UL 8,23
M 70
UH 9,23
AR 10,86
M 71
WL 0
R 76
M 72
AR 11,110
AR 12,134
M 73
bc 49,7,66,60
AR 13,158
M 74
mh 4
AD 10,6
AD 11,7
M 75
ad 66,704
H 0
M 76
AD 12,8
AD 13,9
M 77
AW 86,10
AW 110,11
M 78
AW 134,12
AW 158,13
M 79
WV 34
H 1
UL 6,24
M 80
UH 7,24
UL 8,25
M 81
UH 9,25
AR 10,87
M 82
AR 11,111
AR 12,135
M 83
R 78
M 84
mh 6
AR 13,159
AD 10,6
M 85
ad 64,706
H 0
M 86
AD 11,7
AD 12,8
M 87
AD 13,9
AW 87,10
M 88
AW 111,11
AW 135,12
M 89
H 1
AW 159,13
M 90
M 91
M 92
M 93
bc 48,8,64,56
M 94
mh 8
M 95
WL 2
R 80
M 96
ad 64,800
H 0
M 97
SA 174,2560
M 98
M 99
M 100
H 1
M 101
M 102
M 103
M 104
PA 12288
DF
M 105
M 106
M 107
WL 2
R 82
M 108
M 109
M 110
M 111
M 112
M 113
M 114
M 115
M 116
WV 29
M 117
WL 0
s_barrier
M 118
mw 72
M 119
R 0
M 120
ad 84,360
W 0
M 121
M 132
M 133
T 24,10
M 122
T 28,24
M 123
W 1
M 134
M 135
T 32,11
M 124
T 36,25
M 125
W 2
M 136
M 137
T 40,12
M 126
T 44,26
M 127
mw 75
M 138
ad 84,363
W 0
M 139
WL 8
T 48,13
M 128
T 52,27
M 129
ae 42,74,192
M 140
W 1
M 141
T 56,0
M 130
WL 8
T 60,14
M 131
R 2
M 142
W 2
M 143
T 64,1
M 0
WL 8
T 68,15
M 1
R 4
M 2
s_bitcmp1_b32 s50, 8
s_cselect_b32 s52, 24576, 0
M 3
v_add_u32 v53, s52, v3
SA 176,0
M 4
ae 44,68,432
M 5
M 6
s_bfe_u32 s51, s50, 0x10011
v_mad_i32_i24 v52, v51, s51, v50
XL 30,0
M 7
WL 8
XL 32,768
M 8
XL 44,1536
M 9
WL 2
M 10
M 11
R 6
M 12
XL 34,2304
M 13
M 14
M 15
M 16
M 17
M 18
M 19
M 20
M 21
M 22
M 23
)"
R"(WL 2
R 8
M 24
M 25
M 26
M 27
M 28
M 29
M 30
M 31
M 32
M 33
M 34
M 35
WL 2
R 10
M 36
M 37
M 38
M 39
M 40
M 41
SA 178,768
M 42
M 43
WV 32
M 44
WL 0
M 45
s_barrier
M 46
mw 0
M 47
R 24
M 48
ad 84,384
W 0
M 49
WV 29
UL 6,26
UH 7,26
M 50
UL 8,27
UH 9,27
M 51
AR 10,88
AR 11,112
M 52
W 1
AR 12,136
M 53
AD 10,6
AD 11,7
M 54
AD 12,8
AD 56,9
M 55
AW 88,10
AW 112,11
M 56
W 2
AW 136,12
M 57
UL 6,42
UH 7,42
M 58
UL 8,43
UH 9,43
M 59
R 26
M 60
mw 3
AR 10,89
AR 11,113
M 61
ad 84,387
W 0
M 62
AR 12,137
AD 10,6
M 63
AD 11,7
AD 12,8
M 64
AD 57,9
AW 89,10
M 65
W 1
AW 113,11
M 66
AW 137,12
UL 6,36
M 67
UH 7,36
UL 8,37
M 68
UH 9,37
AR 10,90
M 69
W 2
AR 11,114
M 70
AR 12,138
AD 10,6
M 71
WL 0
R 28
M 72
AD 11,7
AD 12,8
M 73
bc 49,8,66,60
AD 58,9
M 74
mh 10
AW 90,10
AW 114,11
M 75
ad 66,800
H 0
M 76
AW 138,12
UL 6,28
M 77
UH 7,28
UL 8,29
M 78
UH 9,29
AR 10,91
M 79
H 1
SA 180,1536
M 80
AR 11,115
AR 12,139
M 81
AD 10,6
AD 11,7
M 82
AD 12,8
AD 59,9
M 83
R 30
M 84
mh 12
AW 91,10
AW 115,11
M 85
ad 64,802
H 0
M 86
AW 139,12
M 87
M 88
M 89
H 1
M 90
M 91
M 92
M 93
bc 48,9,64,56
M 94
mh 0
M 95
WL 2
R 32
M 96
ad 64,896
H 0
M 97
M 98
M 99
M 100
H 1
M 101
M 102
M 103
M 104
M 105
M 106
M 107
WL 2
R 34
M 108
M 109
M 110
M 111
M 112
M 113
M 114
M 115
M 116
WV 29
M 117
WL 0
s_barrier
SA 182,2304
M 118
mw 24
M 119
R 48
M 120
ad 84,408
W 0
M 121
M 132
M 133
T 24,2
M 122
T 28,16
M 123
W 1
M 134
M 135
T 32,3
M 124
T 36,17
M 125
W 2
M 136
M 137
T 40,4
M 126
T 44,18
M 127
mw 27
M 138
ad 84,411
W 0
M 139
WL 8
T 48,5
M 128
T 52,19
M 129
M 140
W 1
M 141
T 56,6
M 130
WL 8
T 60,20
M 131
R 50
M 142
W 2
M 143
T 64,7
M 0
WL 8
T 68,21
M 1
R 52
M 2
M 3
M 4
M 5
M 6
M 7
WL 8
M 8
M 9
WL 2
M 10
M 11
R 54
M 12
ae 42,74,192
M 13
s_bfe_u32 s52, s50, 0x10009
v_mad_i32_i24 v53, v51, s52, v50
SA 184,0
M 14
M 15
M 16
M 17
M 18
M 19
M 20
M 21
M 22
M 23
WL 2
R 56
M 24
M 25
M 26
M 27
M 28
M 29
M 30
M 31
M 32
M 33
M 34
M 35
WL 2
R 58
M 36
M 37
M 38
M 39
M 40
M 41
M 42
M 43
M 44
WV 27
M 45
WL 0
s_barrier
M 46
mw 48
M 47
R 72
M 48
ad 84,432
W 0
M 49
M 50
WV 23
UL 6,30
UH 7,30
M 51
SA 186,768
UL 8,31
M 52
W 1
UH 9,31
M 53
AR 10,92
AR 11,116
M 54
AR 12,140
AD 10,6
M 55
AD 11,7
AD 12,8
M 56
W 2
AD 60,9
M 57
AW 92,10
AW 116,11
M 58
AW 140,12
UL 6,32
M 59
R 74
M 60
mw 51
UH 7,32
UL 8,33
M 61
ad 84,435
W 0
M 62
UH 9,33
AR 10,93
M 63
AR 11,117
AR 12,141
M 64
AD 10,6
AD 11,7
M 65
W 1
AD 12,8
M 66
AD 61,9
AW 93,10
M 67
AW 117,11
AW 141,12
M 68
UL 6,44
UH 7,44
M 69
W 2
UL 8,45
M 70
UH 9,45
AR 10,94
M 71
WL 0
R 76
M 72
AR 11,118
AR 12,142
M 73
bc 49,9,66,60
AD 10,6
M 74
mh 2
AD 11,7
AD 12,8
M 75
ad 66,896
H 0
M 76
)"
R"(AD 62,9
AW 94,10
M 77
AW 118,11
AW 142,12
M 78
UL 6,34
UH 7,34
M 79
H 1
UL 8,35
M 80
UH 9,35
AR 10,95
M 81
AR 11,119
AR 12,143
M 82
AD 10,6
AD 11,7
M 83
R 78
M 84
mh 4
AD 12,8
AD 63,9
M 85
ad 64,898
H 0
M 86
AW 95,10
AW 119,11
M 87
AW 143,12
M 88
M 89
H 1
SA 188,1536
M 90
M 91
M 92
M 93
bc 48,10,64,56
M 94
mh 6
M 95
WL 2
R 80
M 96
ad 64,992
H 0
M 97
M 98
M 99
M 100
H 1
M 101
M 102
M 103
M 104
M 105
M 106
M 107
WL 2
R 82
M 108
M 109
M 110
M 111
M 112
M 113
M 114
M 115
M 116
WV 24
M 117
WL 0
s_barrier
M 118
mw 72
M 119
R 0
M 120
ad 84,456
W 0
M 121
M 132
M 133
T 24,8
M 122
T 28,22
M 123
W 1
M 134
M 135
T 32,9
M 124
T 36,23
M 125
W 2
SA 190,2304
M 136
M 137
T 40,10
M 126
T 44,24
M 127
mw 75
M 138
ad 84,459
W 0
M 139
WL 8
T 48,11
M 128
T 52,25
M 129
M 140
W 1
M 141
T 56,12
M 130
WL 8
T 60,26
M 131
R 2
M 142
W 2
M 143
T 64,13
M 0
WL 8
T 68,27
M 1
R 4
M 2
M 3
M 4
M 5
M 6
M 7
WL 8
M 8
M 9
WL 2
M 10
M 11
R 6
M 12
M 13
M 14
M 15
M 16
M 17
M 18
M 19
M 20
M 21
M 22
M 23
WL 2
R 8
M 24
ae 42,74,192
M 25
s_bitcmp1_b32 s50, 8
s_cselect_b32 s52, 24576, 0
v_add_u32 v53, s52, v3
M 26
SA 192,128
M 27
M 28
M 29
M 30
M 31
M 32
ae 44,68,384
M 33
s_bitcmp1_b32 s50, 12
s_cselect_b32 s51, 24576, 0
v_add_u32 v52, s51, v3
M 34
XL 22,128
M 35
WL 2
R 10
M 36
M 37
M 38
XL 24,896
M 39
M 40
M 41
M 42
M 43
WV 25
M 44
WL 0
XL 26,1664
M 45
s_barrier
M 46
mw 0
M 47
R 24
M 48
ad 84,480
W 0
M 49
M 50
XL 42,2432
M 51
M 52
W 1
M 53
M 54
M 55
M 56
W 2
M 57
M 58
M 59
R 26
M 60
mw 3
M 61
ad 84,483
W 0
M 62
M 63
M 64
SA 194,896
M 65
W 1
M 66
M 67
M 68
M 69
W 2
M 70
M 71
WL 2
R 28
M 72
M 73
bc 49,10,66,60
M 74
mh 8
M 75
ad 66,992
H 0
M 76
M 77
M 78
M 79
H 1
M 80
M 81
M 82
M 83
WL 2
R 30
M 84
mh 10
M 85
ad 64,994
H 0
M 86
M 87
M 88
M 89
H 1
M 90
M 91
M 92
M 93
bc 48,11,64,56
M 94
mh 12
M 95
WL 2
R 32
M 96
ad 64,1088
H 0
M 97
M 98
M 99
M 100
H 1
M 101
M 102
SA 196,1664
M 103
M 104
M 105
M 106
M 107
WL 2
R 34
M 108
M 109
M 110
M 111
M 112
M 113
M 114
M 115
M 116
WV 29
M 117
WL 0
s_barrier
M 118
mw 24
M 119
R 48
M 120
ad 84,504
W 0
M 121
M 132
M 133
T 24,0
M 122
T 28,14
M 123
W 1
M 134
M 135
T 32,1
M 124
T 36,15
M 125
W 2
M 136
M 137
T 40,2
M 126
T 44,16
M 127
mw 27
M 138
ad 84,507
W 0
M 139
WL 8
T 48,3
M 128
T 52,17
M 129
M 140
W 1
M 141
T 56,4
M 130
WL 8
T 60,18
M 131
R 50
M 142
W 2
SA 198,2432
M 143
T 64,5
M 0
WL 8
T 68,19
M 1
R 52
M 2
M 3
M 4
M 5
M 6
M 7
WL 8
M 8
M 9
WL 2
M 10
M 11
R 54
M 12
M 13
M 14
M 15
M 16
M 17
M 18
M 19
M 20
M 21
M 22
M 23
WL 2
R 56
M 24
M 25
M 26
)"
R"(M 27
M 28
M 29
M 30
M 31
M 32
M 33
M 34
ae 44,68,384
M 35
WL 2
R 58
M 36
ae 42,74,192
M 37
s_bfe_u32 s52, s50, 0x10009
v_mad_i32_i24 v53, v51, s52, v50
SA 200,128
M 38
s_bfe_u32 s51, s50, 0x1000d
v_mad_i32_i24 v52, v51, s51, v50
XL 36,128
M 39
M 40
XL 28,896
M 41
M 42
M 43
WV 29
M 44
WL 0
M 45
s_barrier
M 46
mw 48
XL 30,1664
M 47
R 72
M 48
ad 84,528
W 0
M 49
M 50
M 51
M 52
W 1
XL 32,2432
M 53
M 54
M 55
M 56
W 2
M 57
M 58
M 59
R 74
M 60
mw 51
M 61
ad 84,531
W 0
M 62
M 63
M 64
M 65
W 1
M 66
M 67
M 68
M 69
W 2
M 70
M 71
WL 2
R 76
M 72
M 73
bc 49,11,66,60
M 74
mh 0
M 75
ad 66,1088
H 0
M 76
SA 202,896
M 77
M 78
M 79
H 1
M 80
M 81
M 82
M 83
WL 2
R 78
M 84
mh 2
M 85
ad 64,1090
H 0
M 86
M 87
M 88
WV 37
UL 6,22
UH 7,22
M 89
H 1
UL 8,23
M 90
UH 9,23
AD 64,6
M 91
AD 88,7
AD 112,8
M 92
AD 136,9
UL 6,24
M 93
bc 48,12,64,56
UH 7,24
M 94
mh 4
UL 8,25
UH 9,25
M 95
WL 2
R 80
M 96
ad 64,1184
H 0
M 97
AD 65,6
AD 89,7
M 98
AD 113,8
AD 137,9
M 99
UL 6,26
UH 7,26
M 100
H 1
UL 8,27
M 101
UH 9,27
AD 66,6
M 102
AD 90,7
AD 114,8
M 103
WV 38
AD 138,9
UL 6,42
M 104
UH 7,42
UL 8,43
M 105
UH 9,43
AD 67,6
M 106
AD 91,7
AD 115,8
M 107
WL 1
R 82
M 108
AD 139,9
M 109
M 110
M 111
M 112
M 113
M 114
SA 204,1664
M 115
WV 29
M 116
WL 0
M 117
s_barrier
M 118
mw 72
M 119
R 0
M 120
ad 84,552
W 0
M 121
M 132
M 133
T 24,6
M 122
T 28,20
M 123
W 1
M 134
M 135
T 32,7
M 124
T 36,21
M 125
W 2
M 136
M 137
T 40,8
M 126
T 44,22
M 127
mw 75
M 138
ad 84,555
W 0
M 139
WL 8
T 48,9
M 128
T 52,23
M 129
M 140
W 1
M 141
T 56,10
M 130
WL 8
T 60,24
M 131
R 2
M 142
W 2
M 143
T 64,11
M 0
WL 8
T 68,25
M 1
R 4
M 2
M 3
M 4
M 5
M 6
M 7
WL 8
M 8
SA 206,2432
M 9
WL 2
M 10
M 11
R 6
M 12
M 13
M 14
M 15
M 16
M 17
M 18
M 19
M 20
M 21
M 22
M 23
WL 2
R 8
M 24
M 25
M 26
M 27
M 28
M 29
M 30
M 31
M 32
M 33
M 34
M 35
WL 2
R 10
M 36
ae 44,70,384
M 37
s_bitcmp1_b32 s50, 14
s_cselect_b32 s51, 24576, 0
v_add_u32 v52, s51, v3
M 38
XL 44,128
M 39
M 40
M 41
M 42
XL 34,896
M 43
WV 29
M 44
WL 0
M 45
s_barrier
M 46
mw 0
M 47
R 24
M 48
ad 84,576
W 0
M 49
ae 42,74,192
XL 22,1664
M 50
s_bitcmp1_b32 s50, 8
s_cselect_b32 s52, 24576, 0
v_add_u32 v53, s52, v3
M 51
SA 208,256
M 52
W 1
M 53
M 54
XL 24,2432
M 55
M 56
W 2
M 57
M 58
M 59
R 26
M 60
mw 3
M 61
ad 84,579
W 0
M 62
M 63
M 64
M 65
W 1
M 66
M 67
M 68
M 69
W 2
M 70
M 71
WL 2
R 28
M 72
M 73
bc 49,12,66,60
M 74
mh 6
M 75
ad 66,1184
H 0
M 76
M 77
M 78
M 79
H 1
M 80
M 81
M 82
M 83
WL 2
R 30
M 84
mh 8
M 85
ad 64,1186
H 0
M 86
M 87
M 88
M 89
H 1
SA 210,1024
M 90
WV 38
UL 6,36
UH 7,36
M 91
UL 8,37
UH 9,37
M 92
AD 68,6
AD 92,7
M 93
bc 48,13,64,56
AD 116,8
M 94
mh 10
AD 140,9
UL 6,28
M 95
WL 2
R 32
M 96
ad 64,1280
H 0
M 97
UH 7,28
UL 8,29
)"
R"(M 98
UH 9,29
AD 69,6
M 99
AD 93,7
AD 117,8
M 100
H 1
AD 141,9
M 101
UL 6,30
UH 7,30
M 102
UL 8,31
UH 9,31
M 103
AD 70,6
AD 94,7
M 104
AD 118,8
AD 142,9
M 105
WV 37
UL 6,32
UH 7,32
M 106
UL 8,33
UH 9,33
M 107
WL 1
R 34
M 108
AD 71,6
AD 95,7
M 109
AD 119,8
AD 143,9
M 110
M 111
M 112
M 113
M 114
M 115
WV 28
M 116
WL 0
M 117
s_barrier
M 118
mw 24
M 119
R 48
M 120
ad 84,600
W 0
M 121
M 132
M 133
T 24,12
M 122
T 28,26
M 123
W 1
M 134
M 135
T 32,13
M 124
T 36,27
M 125
W 2
SA 212,1792
M 136
M 137
T 40,0
M 126
T 44,14
M 127
mw 27
M 138
ad 84,603
W 0
M 139
WL 8
T 48,1
M 128
T 52,15
M 129
M 140
W 1
M 141
T 56,2
M 130
WL 8
T 60,16
M 131
R 50
M 142
W 2
M 143
T 64,3
M 0
WL 8
T 68,17
M 1
R 52
M 2
M 3
M 4
M 5
M 6
M 7
WL 8
M 8
M 9
WL 2
M 10
M 11
R 54
M 12
M 13
M 14
M 15
M 16
M 17
M 18
M 19
M 20
M 21
M 22
M 23
WL 2
R 56
M 24
SA 214,2560
M 25
M 26
M 27
M 28
M 29
M 30
M 31
M 32
M 33
M 34
M 35
WL 2
R 58
M 36
M 37
M 38
ae 44,70,384
M 39
s_bfe_u32 s51, s50, 0x1000f
v_mad_i32_i24 v52, v51, s51, v50
XL 26,128
M 40
M 41
M 42
M 43
WV 28
M 44
WL 0
XL 42,896
M 45
s_barrier
M 46
mw 48
M 47
R 72
M 48
ad 84,624
W 0
M 49
M 50
XL 36,1664
M 51
M 52
W 1
M 53
M 54
M 55
M 56
W 2
XL 28,2432
M 57
M 58
M 59
R 74
M 60
mw 51
M 61
ad 84,627
W 0
M 62
ae 42,74,192
M 63
s_bfe_u32 s52, s50, 0x10009
v_mad_i32_i24 v53, v51, s52, v50
SA 216,256
M 64
M 65
W 1
M 66
M 67
M 68
M 69
W 2
M 70
M 71
WL 2
R 76
M 72
M 73
bc 49,13,66,60
M 74
mh 12
M 75
ad 66,1280
H 0
M 76
M 77
M 78
M 79
H 1
M 80
M 81
M 82
M 83
WL 2
R 78
M 84
mh 0
M 85
ad 64,1282
H 0
M 86
M 87
M 88
M 89
H 1
M 90
M 91
M 92
WV 39
UL 6,44
UH 7,44
M 93
bc 48,14,64,56
UL 8,45
M 94
mh 2
UH 9,45
AD 72,6
M 95
WL 2
R 80
M 96
ad 64,1376
H 0
M 97
AD 96,7
AD 120,8
M 98
AD 144,9
UL 6,34
M 99
UH 7,34
UL 8,35
M 100
H 1
UH 9,35
M 101
SA 218,1024
AD 73,6
M 102
AD 97,7
AD 121,8
M 103
WV 40
AD 145,9
UL 6,22
M 104
UH 7,22
UL 8,23
M 105
UH 9,23
AD 74,6
M 106
AD 98,7
AD 122,8
M 107
WL 1
R 82
M 108
WV 37
AD 146,9
UL 6,24
M 109
UH 7,24
UL 8,25
M 110
UH 9,25
AD 75,6
M 111
AD 99,7
AD 123,8
M 112
AD 147,9
M 113
M 114
M 115
WV 29
M 116
WL 0
M 117
s_barrier
M 118
mw 72
M 119
R 0
M 120
ad 84,648
W 0
M 121
M 132
M 133
T 24,4
M 122
T 28,18
M 123
W 1
M 134
M 135
T 32,5
M 124
T 36,19
M 125
W 2
M 136
M 137
T 40,6
M 126
T 44,20
M 127
mw 75
M 138
ad 84,651
W 0
M 139
WL 8
T 48,7
M 128
T 52,21
M 129
M 140
W 1
M 141
T 56,8
M 130
WL 8
T 60,22
M 131
R 2
M 142
W 2
SA 220,1792
M 143
T 64,9
M 0
WL 8
T 68,23
M 1
R 4
M 2
M 3
M 4
M 5
M 6
M 7
WL 8
M 8
M 9
WL 2
M 10
M 11
R 6
M 12
M 13
M 14
M 15
M 16
M 17
M 18
M 19
M 20
M 21
M 22
M 23
WL 2
R 8
)"
R"(M 24
M 25
M 26
M 27
M 28
M 29
M 30
M 31
M 32
M 33
M 34
M 35
WL 2
R 10
M 36
SA 222,2560
M 37
M 38
M 39
M 40
ae 44,68,432
M 41
s_bitcmp1_b32 s50, 16
s_cselect_b32 s51, 24576, 0
v_add_u32 v52, s51, v3
M 42
XL 30,128
M 43
WV 28
M 44
WL 0
M 45
s_barrier
M 46
mw 0
XL 32,896
M 47
R 24
M 48
ad 84,672
W 0
M 49
M 50
M 51
M 52
W 1
XL 44,1664
M 53
M 54
M 55
M 56
W 2
M 57
M 58
XL 34,2432
M 59
R 26
M 60
mw 3
M 61
ad 84,675
W 0
M 62
M 63
M 64
M 65
W 1
M 66
M 67
M 68
M 69
W 2
M 70
M 71
WL 2
R 28
M 72
M 73
bc 49,14,66,60
M 74
mh 4
M 75
ad 66,1376
H 0
M 76
ae 42,72,240
M 77
s_bitcmp1_b32 s50, 10
s_cselect_b32 s52, 24576, 0
v_add_u32 v53, s52, v3
M 78
SA 224,0
M 79
H 1
M 80
M 81
M 82
M 83
WL 2
R 30
M 84
mh 6
M 85
ad 64,1378
H 0
M 86
M 87
M 88
M 89
H 1
M 90
M 91
M 92
WV 39
M 93
bc 48,15,64,56
M 94
mh 8
UL 6,26
UH 7,26
M 95
WL 2
R 32
M 96
ad 64,1472
H 0
M 97
UL 8,27
UH 9,27
M 98
AD 76,6
AD 100,7
M 99
AD 124,8
AD 148,9
M 100
H 1
UL 6,42
M 101
UH 7,42
UL 8,43
M 102
UH 9,43
AD 77,6
M 103
AD 101,7
AD 125,8
M 104
WV 39
AD 149,9
UL 6,36
M 105
UH 7,36
UL 8,37
M 106
UH 9,37
AD 78,6
M 107
WL 1
R 34
M 108
AD 102,7
AD 126,8
M 109
WV 36
AD 150,9
UL 6,28
M 110
UH 7,28
UL 8,29
M 111
UH 9,29
AD 79,6
M 112
AD 103,7
AD 127,8
M 113
AD 151,9
M 114
M 115
WV 28
M 116
WL 0
SA 226,768
M 117
s_barrier
M 118
mw 24
M 119
R 48
M 120
ad 84,696
W 0
M 121
M 132
M 133
T 24,10
M 122
T 28,24
M 123
W 1
M 134
M 135
T 32,11
M 124
T 36,25
M 125
W 2
M 136
M 137
T 40,12
M 126
T 44,26
M 127
mw 27
M 138
ad 84,699
W 0
M 139
WL 8
T 48,13
M 128
T 52,27
M 129
M 140
W 1
M 141
T 56,0
M 130
WL 8
T 60,14
M 131
R 50
M 142
W 2
M 143
T 64,1
M 0
WL 8
T 68,15
M 1
R 52
M 2
M 3
M 4
M 5
M 6
M 7
WL 8
M 8
M 9
WL 2
M 10
SA 228,1536
M 11
R 54
M 12
M 13
M 14
M 15
M 16
M 17
M 18
M 19
M 20
M 21
M 22
M 23
WL 2
R 56
M 24
M 25
M 26
M 27
M 28
M 29
M 30
M 31
M 32
M 33
M 34
M 35
WL 2
R 58
M 36
M 37
M 38
M 39
M 40
M 41
M 42
ae 44,68,432
s_bfe_u32 s51, s50, 0x10011
M 43
WV 27
v_mad_i32_i24 v52, v51, s51, v50
XL 22,128
M 44
WL 0
M 45
s_barrier
M 46
mw 48
M 47
R 72
M 48
ad 84,720
W 0
M 49
SA 230,2304
XL 24,896
M 50
M 51
M 52
W 1
M 53
M 54
XL 26,1664
M 55
M 56
W 2
M 57
M 58
M 59
R 74
M 60
mw 51
XL 42,2432
M 61
ad 84,723
W 0
M 62
M 63
M 64
M 65
W 1
M 66
M 67
M 68
M 69
W 2
M 70
M 71
WL 2
R 76
M 72
M 73
bc 49,15,66,60
M 74
mh 10
M 75
ad 66,1472
H 0
M 76
M 77
M 78
M 79
H 1
M 80
M 81
M 82
M 83
WL 2
R 78
M 84
mh 12
M 85
ad 64,1474
H 0
M 86
M 87
ae 42,72,240
M 88
s_bfe_u32 s52, s50, 0x1000b
v_mad_i32_i24 v53, v51, s52, v50
SA 232,0
M 89
H 1
M 90
M 91
M 92
M 93
bc 48,16,64,56
M 94
mh 0
M 95
WL 2
R 80
M 96
ad 64,1568
H 0
M 97
WV 40
UL 6,30
UH 7,30
M 98
UL 8,31
UH 9,31
M 99
AD 80,6
)"
R"(AD 104,7
M 100
H 1
AD 128,8
M 101
AD 152,9
UL 6,32
M 102
UH 7,32
UL 8,33
M 103
UH 9,33
AD 81,6
M 104
AD 105,7
AD 129,8
M 105
WV 38
AD 153,9
UL 6,44
M 106
UH 7,44
UL 8,45
M 107
WL 1
R 82
M 108
UH 9,45
AD 82,6
M 109
AD 106,7
AD 130,8
M 110
WV 36
AD 154,9
UL 6,34
M 111
UH 7,34
UL 8,35
M 112
UH 9,35
AD 83,6
M 113
AD 107,7
AD 131,8
M 114
AD 155,9
M 115
WV 28
M 116
WL 0
M 117
s_barrier
M 118
mw 72
M 119
R 0
M 120
ad 84,744
W 0
M 121
M 132
M 133
T 24,2
M 122
T 28,16
M 123
W 1
M 134
SA 234,768
M 135
T 32,3
M 124
T 36,17
M 125
W 2
M 136
M 137
T 40,4
M 126
T 44,18
M 127
mw 75
M 138
ad 84,747
W 0
M 139
WL 8
T 48,5
M 128
T 52,19
M 129
M 140
W 1
M 141
T 56,6
M 130
WL 8
T 60,20
M 131
R 2
M 142
W 2
M 143
T 64,7
M 0
WL 8
T 68,21
M 1
R 4
M 2
M 3
M 4
M 5
M 6
M 7
WL 8
M 8
M 9
WL 2
M 10
M 11
R 6
M 12
M 13
M 14
M 15
M 16
M 17
M 18
M 19
M 20
SA 236,1536
M 21
M 22
M 23
WL 2
R 8
M 24
M 25
M 26
M 27
M 28
M 29
M 30
M 31
M 32
M 33
M 34
M 35
WL 2
R 10
M 36
M 37
M 38
M 39
M 40
M 41
M 42
M 43
M 44
WV 27
M 45
WL 0
s_barrier
M 46
mw 0
M 47
R 24
M 48
ad 84,768
W 0
M 49
M 50
M 51
M 52
W 1
M 53
M 54
M 55
M 56
W 2
M 57
M 58
SA 238,2304
M 59
R 26
M 60
mw 3
M 61
ad 84,771
W 0
M 62
M 63
M 64
M 65
W 1
M 66
M 67
M 68
M 69
W 2
M 70
M 71
WL 2
R 28
M 72
M 73
bc 49,16,66,60
M 74
mh 2
M 75
ad 66,1568
H 0
M 76
M 77
M 78
M 79
H 1
M 80
M 81
M 82
M 83
WL 2
R 30
M 84
mh 4
M 85
ad 64,1570
H 0
M 86
M 87
M 88
M 89
H 1
M 90
M 91
M 92
M 93
bc 48,17,64,56
M 94
mh 6
M 95
WL 2
R 32
M 96
ad 64,1664
H 0
M 97
ae 42,72,240
M 98
s_bitcmp1_b32 s50, 10
s_cselect_b32 s52, 24576, 0
v_add_u32 v53, s52, v3
M 99
WV 37
SA 240,128
UL 6,22
M 100
H 1
UH 7,22
M 101
UL 8,23
UH 9,23
M 102
AD 84,6
AD 108,7
M 103
AD 132,8
AD 156,9
M 104
WV 36
UL 6,24
UH 7,24
M 105
UL 8,25
UH 9,25
M 106
AD 85,6
AD 109,7
M 107
WL 0
R 34
M 108
AD 133,8
AD 157,9
M 109
WV 34
UL 6,26
UH 7,26
M 110
UL 8,27
UH 9,27
M 111
AD 86,6
AD 110,7
M 112
AD 134,8
AD 158,9
M 113
WV 32
UL 6,42
UH 7,42
M 114
UL 8,43
UH 9,43
M 115
WV 24
AD 87,6
AD 111,7
M 116
WL 0
AD 135,8
AD 159,9
M 117
s_barrier
M 118
mw 24
M 119
R 48
M 120
ad 84,792
W 0
M 121
M 132
M 133
T 24,8
M 122
T 28,22
M 123
W 1
M 134
M 135
T 32,9
M 124
T 36,23
M 125
W 2
M 136
M 137
T 40,10
M 126
T 44,24
M 127
mw 27
M 138
ad 84,795
W 0
M 139
WL 8
T 48,11
M 128
T 52,25
M 129
SA 242,896
M 140
W 1
M 141
T 56,12
M 130
WL 8
T 60,26
M 131
R 50
M 142
W 2
M 143
T 64,13
M 0
WL 8
T 68,27
M 1
R 52
M 2
M 3
M 4
M 5
M 6
M 7
WL 8
M 8
M 9
WL 2
M 10
M 11
R 54
M 12
M 13
M 14
M 15
M 16
M 17
M 18
M 19
M 20
M 21
M 22
M 23
WL 2
R 56
)"
R"(M 24
M 25
M 26
M 27
M 28
M 29
M 30
M 31
SA 244,1664
M 32
M 33
M 34
M 35
WL 2
R 58
M 36
M 37
M 38
M 39
M 40
M 41
M 42
M 43
M 44
WV 23
M 45
WL 0
s_barrier
M 46
mw 48
M 47
R 72
M 48
ad 84,816
W 0
M 49
M 50
M 51
M 52
W 1
M 53
M 54
M 55
M 56
W 2
M 57
M 58
M 59
R 74
M 60
mw 51
M 61
ad 84,819
W 0
M 62
M 63
M 64
M 65
W 1
M 66
M 67
M 68
M 69
W 2
SA 246,2432
M 70
ae 44,68,384
M 71
WL 2
R 76
M 72
s_bitcmp1_b32 s50, 12
s_cselect_b32 s51, 24576, 0
v_add_u32 v52, s51, v3
M 73
bc 49,17,66,60
XL 36,256
M 74
mh 8
M 75
ad 66,1664
H 0
M 76
XL 28,1024
M 77
M 78
M 79
H 1
M 80
M 81
M 82
XL 30,1792
M 83
WL 2
R 78
M 84
mh 10
M 85
ad 64,1666
H 0
M 86
M 87
M 88
XL 32,2560
M 89
H 1
M 90
M 91
M 92
M 93
bc 48,18,64,56
M 94
mh 12
M 95
WL 2
R 80
M 96
ad 64,1760
H 0
M 97
M 98
M 99
M 100
H 1
M 101
M 102
M 103
M 104
M 105
M 106
M 107
WL 2
R 82
M 108
ae 42,72,240
M 109
s_bfe_u32 s52, s50, 0x1000b
v_mad_i32_i24 v53, v51, s52, v50
SA 248,128
M 110
M 111
M 112
M 113
M 114
M 115
WV 29
M 116
WL 0
M 117
s_barrier
M 118
mw 72
M 119
R 0
M 120
ad 84,840
W 0
M 121
M 132
M 133
T 24,0
M 122
T 28,14
M 123
W 1
M 134
M 135
T 32,1
M 124
T 36,15
M 125
W 2
M 136
M 137
T 40,2
M 126
T 44,16
M 127
mw 75
M 138
ad 84,843
W 0
M 139
WL 8
T 48,3
M 128
T 52,17
M 129
M 140
W 1
M 141
T 56,4
M 130
WL 8
T 60,18
M 131
R 2
M 142
W 2
M 143
T 64,5
M 0
WL 8
T 68,19
M 1
R 4
M 2
M 3
SA 250,896
M 4
M 5
M 6
M 7
WL 8
M 8
M 9
WL 2
M 10
M 11
R 6
M 12
M 13
M 14
M 15
M 16
M 17
M 18
M 19
M 20
M 21
M 22
M 23
WL 2
R 8
M 24
M 25
M 26
M 27
M 28
M 29
M 30
M 31
M 32
M 33
M 34
M 35
WL 2
R 10
M 36
M 37
M 38
M 39
M 40
M 41
SA 252,1664
M 42
M 43
WV 27
M 44
WL 0
M 45
s_barrier
M 46
mw 0
M 47
R 24
M 48
ad 84,864
W 0
M 49
M 50
M 51
M 52
W 1
M 53
M 54
M 55
M 56
W 2
M 57
M 58
M 59
R 26
M 60
mw 3
M 61
ad 84,867
W 0
M 62
M 63
M 64
M 65
W 1
M 66
M 67
M 68
M 69
W 2
M 70
M 71
WL 2
R 28
M 72
M 73
bc 49,18,66,60
M 74
mh 0
M 75
ad 66,1760
H 0
M 76
ae 44,68,384
M 77
s_bfe_u32 s51, s50, 0x1000d
v_mad_i32_i24 v52, v51, s51, v50
XL 44,256
M 78
M 79
H 1
SA 254,2432
M 80
XL 34,1024
M 81
M 82
M 83
WL 2
R 30
M 84
mh 2
M 85
ad 64,1762
H 0
M 86
XL 22,1792
M 87
M 88
M 89
H 1
M 90
M 91
M 92
XL 24,2560
M 93
bc 48,19,64,56
M 94
mh 4
M 95
WL 2
R 32
M 96
ad 64,1856
H 0
M 97
M 98
M 99
M 100
H 1
M 101
M 102
M 103
M 104
M 105
M 106
M 107
WL 2
R 34
M 108
M 109
M 110
M 111
M 112
M 113
M 114
M 115
M 116
WV 28
M 117
WL 0
s_barrier
M 118
mw 24
M 119
R 48
M 120
ad 84,888
W 0
M 121
ae 42,72,240
M 132
s_bitcmp1_b32 s50, 10
s_cselect_b32 s52, 24576, 0
v_add_u32 v53, s52, v3
M 133
T 24,6
M 122
T 28,20
M 123
W 1
ST 38,256
M 134
)"
R"(UL 6,36
UH 7,36
M 135
T 32,7
M 124
T 36,21
M 125
W 2
UL 8,37
M 136
UH 9,37
AD 160,6
M 137
T 40,8
M 126
T 44,22
M 127
mw 27
AD 184,7
AD 208,8
M 138
ad 84,891
W 0
M 139
WL 8
T 48,9
M 128
T 52,23
M 129
AD 232,9
UL 6,28
M 140
W 1
UH 7,28
M 141
T 56,10
M 130
WL 8
T 60,24
M 131
R 50
M 142
W 2
UL 8,29
M 143
T 64,11
M 0
WL 8
T 68,25
M 1
R 52
M 2
UH 9,29
AD 161,6
M 3
AD 185,7
AD 209,8
M 4
AD 233,9
UL 6,30
M 5
UH 7,30
UL 8,31
M 6
UH 9,31
AD 162,6
M 7
WL 8
AD 186,7
AD 210,8
M 8
AD 234,9
UL 6,32
M 9
WL 2
UH 7,32
UL 8,33
M 10
UH 9,33
AD 163,6
M 11
R 54
M 12
AD 187,7
AD 211,8
M 13
AD 235,9
M 14
M 15
M 16
M 17
M 18
M 19
ST 40,1024
M 20
M 21
M 22
M 23
WL 2
R 56
M 24
M 25
M 26
M 27
M 28
M 29
M 30
M 31
M 32
M 33
M 34
M 35
WL 2
R 58
M 36
M 37
M 38
M 39
M 40
M 41
M 42
M 43
M 44
WV 27
M 45
WL 0
s_barrier
M 46
mw 48
M 47
R 72
M 48
ad 84,912
W 0
M 49
M 50
M 51
M 52
W 1
M 53
M 54
M 55
M 56
W 2
M 57
ST 18,1792
M 58
M 59
R 74
M 60
mw 51
M 61
ad 84,915
W 0
M 62
M 63
M 64
M 65
W 1
M 66
M 67
M 68
M 69
W 2
M 70
M 71
WL 2
R 76
M 72
M 73
bc 49,19,66,60
M 74
mh 6
M 75
ad 66,1856
H 0
M 76
M 77
M 78
ae 44,70,384
M 79
H 1
M 80
s_bitcmp1_b32 s50, 14
s_cselect_b32 s51, 24576, 0
v_add_u32 v52, s51, v3
M 81
XL 26,256
M 82
M 83
WL 2
R 78
M 84
mh 8
XL 42,1024
M 85
ad 64,1858
H 0
M 86
M 87
M 88
M 89
H 1
M 90
XL 38,1792
M 91
M 92
M 93
bc 48,20,64,56
M 94
mh 10
M 95
WL 2
R 80
M 96
ad 64,1952
H 0
M 97
ST 46,2560
XL 36,2560
M 98
M 99
M 100
H 1
M 101
M 102
M 103
M 104
M 105
M 106
M 107
WL 2
R 82
M 108
M 109
M 110
M 111
M 112
M 113
M 114
M 115
M 116
WV 29
M 117
WL 0
s_barrier
M 118
mw 72
M 119
R 0
M 120
ad 84,936
W 0
M 121
M 132
M 133
T 24,12
M 122
T 28,26
M 123
W 1
M 134
M 135
T 32,13
M 124
T 36,27
M 125
W 2
M 136
UL 6,44
UH 7,44
M 137
T 40,0
M 126
T 44,14
M 127
mw 75
UL 8,45
UH 9,45
M 138
ad 84,939
W 0
M 139
WL 8
T 48,1
M 128
T 52,15
M 129
ae 42,72,240
AD 164,6
M 140
s_bfe_u32 s52, s50, 0x1000b
W 1
v_mad_i32_i24 v53, v51, s52, v50
M 141
T 56,2
M 130
WL 8
T 60,16
M 131
R 2
M 142
W 2
ST 48,256
M 143
T 64,3
M 0
WL 8
T 68,17
M 1
R 4
M 2
AD 188,7
AD 212,8
M 3
AD 236,9
UL 6,34
M 4
UH 7,34
UL 8,35
M 5
UH 9,35
AD 165,6
M 6
AD 189,7
AD 213,8
M 7
WL 8
AD 237,9
UL 6,22
M 8
UH 7,22
UL 8,23
M 9
WL 2
UH 9,23
AD 166,6
M 10
AD 190,7
AD 214,8
M 11
R 6
M 12
WV 35
AD 238,9
UL 6,24
M 13
UH 7,24
UL 8,25
M 14
UH 9,25
AD 167,6
M 15
AD 191,7
AD 215,8
M 16
AD 239,9
M 17
M 18
M 19
M 20
M 21
M 22
M 23
WL 2
R 8
M 24
M 25
M 26
M 27
M 28
M 29
M 30
M 31
M 32
M 33
M 34
M 35
WL 2
R 10
M 36
ST 14,1024
M 37
M 38
M 39
M 40
M 41
M 42
M 43
WV 27
M 44
WL 0
M 45
s_barrier
M 46
mw 0
M 47
R 24
M 48
ad 84,960
)"
R"(W 0
M 49
M 50
M 51
M 52
W 1
M 53
M 54
M 55
M 56
W 2
M 57
M 58
M 59
R 26
M 60
mw 3
M 61
ad 84,963
W 0
M 62
M 63
M 64
M 65
W 1
M 66
M 67
M 68
M 69
W 2
M 70
M 71
WL 2
R 28
M 72
M 73
bc 49,20,66,60
M 74
mh 12
ST 16,1792
M 75
ad 66,1952
H 0
M 76
M 77
M 78
M 79
H 1
M 80
M 81
M 82
ae 44,70,384
M 83
WL 2
R 30
M 84
mh 0
M 85
ad 64,1954
H 0
M 86
s_bfe_u32 s51, s50, 0x1000f
v_mad_i32_i24 v52, v51, s51, v50
XL 28,256
M 87
M 88
XL 30,1024
M 89
H 1
M 90
M 91
M 92
M 93
bc 48,21,64,56
M 94
mh 2
XL 32,1792
M 95
WL 2
R 32
M 96
ad 64,2048
H 0
M 97
M 98
M 99
M 100
H 1
XL 40,2560
M 101
M 102
M 103
M 104
M 105
M 106
M 107
WL 2
R 34
M 108
M 109
M 110
M 111
M 112
ST 20,2560
M 113
M 114
M 115
WV 31
M 116
WL 0
M 117
s_barrier
M 118
mw 24
M 119
R 48
M 120
ad 84,984
W 0
M 121
M 132
M 133
T 24,4
M 122
T 28,18
M 123
W 1
M 134
M 135
T 32,5
M 124
T 36,19
M 125
W 2
M 136
M 137
T 40,6
M 126
T 44,20
M 127
mw 27
M 138
ad 84,987
W 0
M 139
WL 8
T 48,7
M 128
T 52,21
M 129
UL 6,26
UH 7,26
M 140
W 1
UL 8,27
M 141
T 56,8
M 130
WL 8
T 60,22
M 131
R 50
M 142
W 2
UH 9,27
M 143
T 64,9
M 0
WL 8
T 68,23
M 1
R 52
M 2
AD 168,6
AD 192,7
M 3
AD 216,8
AD 240,9
M 4
UL 6,42
UH 7,42
M 5
UL 8,43
UH 9,43
M 6
AD 169,6
AD 193,7
M 7
WL 8
AD 217,8
AD 241,9
M 8
WV 36
UL 6,38
UH 7,38
M 9
WL 2
UL 8,39
UH 9,39
M 10
AD 170,6
AD 194,7
M 11
R 54
M 12
AD 218,8
AD 242,9
M 13
WV 33
UL 6,36
UH 7,36
M 14
UL 8,37
UH 9,37
M 15
AD 171,6
AD 195,7
M 16
AD 219,8
AD 243,9
M 17
M 18
M 19
M 20
M 21
M 22
M 23
WL 2
R 56
M 24
M 25
M 26
M 27
M 28
M 29
M 30
M 31
M 32
M 33
M 34
M 35
WL 2
R 58
M 36
M 37
M 38
M 39
M 40
M 41
M 42
M 43
M 44
WV 26
M 45
WL 0
s_barrier
M 46
mw 48
M 47
R 72
M 48
ad 84,1008
W 0
M 49
M 50
M 51
M 52
W 1
M 53
M 54
M 55
M 56
W 2
M 57
M 58
M 59
R 74
M 60
mw 51
M 61
ad 84,1011
W 0
M 62
M 63
M 64
M 65
W 1
M 66
M 67
M 68
M 69
W 2
M 70
M 71
WL 2
R 76
M 72
M 73
bc 49,21,66,60
M 74
mh 4
M 75
ad 66,2048
H 0
M 76
M 77
M 78
M 79
H 1
M 80
M 81
M 82
M 83
WL 2
R 78
M 84
mh 6
M 85
ad 64,2050
H 0
M 86
ae 44,68,432
M 87
s_bitcmp1_b32 s50, 16
s_cselect_b32 s51, 24576, 0
v_add_u32 v52, s51, v3
M 88
XL 18,256
M 89
H 1
M 90
M 91
M 92
XL 46,1024
M 93
bc 48,22,64,56
M 94
mh 8
M 95
WL 2
R 80
M 96
ad 64,2144
H 0
M 97
M 98
XL 48,1792
M 99
M 100
H 1
M 101
M 102
M 103
M 104
XL 44,2560
M 105
M 106
M 107
WL 2
R 82
M 108
M 109
M 110
M 111
M 112
M 113
M 114
M 115
M 116
WV 27
M 117
WL 0
s_barrier
M 118
mw 72
M 119
R 0
M 120
ad 84,1032
W 0
M 121
M 132
M 133
T 24,10
M 122
T 28,24
M 123
W 1
M 134
M 135
T 32,11
M 124
T 36,25
M 125
W 2
M 136
M 137
T 40,12
M 126
T 44,26
)"
R"(M 127
mw 75
M 138
ad 84,1035
W 0
M 139
WL 8
T 48,13
M 128
T 52,27
M 129
M 140
W 1
UL 6,28
M 141
T 56,0
M 130
WL 8
T 60,14
M 131
R 2
M 142
W 2
UH 7,28
M 143
T 64,1
M 0
WL 8
T 68,15
M 1
R 4
M 2
UL 8,29
UH 9,29
M 3
AD 172,6
AD 196,7
M 4
AD 220,8
AD 244,9
M 5
UL 6,30
UH 7,30
M 6
UL 8,31
UH 9,31
M 7
WL 8
AD 173,6
AD 197,7
M 8
WL 2
AD 221,8
AD 245,9
M 9
WV 32
UL 6,32
UH 7,32
M 10
UL 8,33
UH 9,33
M 11
R 6
M 12
AD 174,6
AD 198,7
M 13
AD 222,8
AD 246,9
M 14
WV 29
UL 6,40
UH 7,40
M 15
UL 8,41
UH 9,41
M 16
AD 175,6
AD 199,7
M 17
AD 223,8
AD 247,9
M 18
M 19
M 20
M 21
M 22
M 23
WL 2
R 8
M 24
M 25
M 26
M 27
M 28
M 29
M 30
M 31
M 32
M 33
M 34
M 35
WL 2
R 10
M 36
M 37
M 38
M 39
M 40
M 41
M 42
M 43
M 44
WV 22
M 45
WL 0
s_barrier
M 46
mw 0
M 47
R 24
M 48
ad 84,1056
W 0
M 49
M 50
M 51
M 52
W 1
M 53
M 54
M 55
M 56
W 2
M 57
M 58
M 59
R 26
M 60
mw 3
M 61
ad 84,1059
W 0
M 62
M 63
M 64
M 65
W 1
M 66
M 67
M 68
M 69
W 2
M 70
M 71
WL 2
R 28
M 72
M 73
bc 49,22,66,60
M 74
mh 10
M 75
ad 66,2144
H 0
M 76
M 77
M 78
M 79
H 1
M 80
M 81
M 82
M 83
WL 2
R 30
M 84
mh 12
M 85
ad 64,2146
H 0
M 86
M 87
M 88
M 89
H 1
M 90
ae 44,68,432
M 91
s_bfe_u32 s51, s50, 0x10011
v_mad_i32_i24 v52, v51, s51, v50
XL 34,256
M 92
M 93
bc 48,23,64,56
M 94
mh 0
M 95
WL 2
R 32
M 96
ad 64,2240
H 0
M 97
XL 22,1024
M 98
M 99
M 100
H 1
M 101
M 102
XL 24,1792
M 103
M 104
M 105
M 106
M 107
WL 2
R 34
M 108
XL 14,2560
M 109
M 110
M 111
M 112
M 113
M 114
M 115
WV 27
M 116
WL 0
M 117
s_barrier
M 118
mw 24
M 119
R 48
M 120
ad 84,1080
W 0
M 121
M 132
M 133
T 24,2
M 122
T 28,16
M 123
W 1
M 134
M 135
T 32,3
M 124
T 36,17
M 125
W 2
M 136
M 137
T 40,4
M 126
T 44,18
M 127
mw 27
M 138
ad 84,1083
W 0
M 139
WL 8
T 48,5
M 128
T 52,19
M 129
M 140
W 1
M 141
T 56,6
M 130
WL 8
T 60,20
M 131
R 50
M 142
W 2
UL 6,18
M 143
T 64,7
M 0
WL 8
T 68,21
M 1
R 52
M 2
UH 7,18
UL 8,19
M 3
UH 9,19
AD 176,6
M 4
AD 200,7
AD 224,8
M 5
WV 32
AD 248,9
UL 6,46
M 6
UH 7,46
UL 8,47
M 7
WL 8
UH 9,47
AD 177,6
M 8
WL 2
AD 201,7
AD 225,8
M 9
WV 30
AD 249,9
UL 6,48
M 10
UH 7,48
UL 8,49
M 11
R 54
M 12
UH 9,49
AD 178,6
M 13
AD 202,7
AD 226,8
M 14
WV 28
AD 250,9
UL 6,44
M 15
UH 7,44
UL 8,45
M 16
UH 9,45
AD 179,6
M 17
AD 203,7
AD 227,8
M 18
AD 251,9
M 19
M 20
M 21
M 22
M 23
WL 2
R 56
M 24
M 25
M 26
M 27
M 28
M 29
M 30
M 31
M 32
M 33
M 34
M 35
WL 2
R 58
M 36
M 37
M 38
M 39
M 40
M 41
M 42
M 43
M 44
WV 22
M 45
WL 0
s_barrier
M 46
mw 48
M 47
R 72
M 48
ad 84,1104
W 0
M 49
M 50
M 51
M 52
W 1
M 53
M 54
M 55
M 56
W 2
M 57
M 58
M 59
R 74
M 60
)"
R"(mw 51
M 61
ad 84,1107
W 0
M 62
M 63
M 64
M 65
W 1
M 66
M 67
M 68
M 69
W 2
M 70
M 71
WL 2
R 76
M 72
M 73
bc 49,23,66,60
M 74
mh 2
M 75
ad 66,2240
H 0
M 76
M 77
M 78
M 79
H 1
M 80
M 81
M 82
M 83
WL 2
R 78
M 84
mh 4
M 85
ad 64,2242
H 0
M 86
M 87
M 88
M 89
H 1
M 90
M 91
M 92
M 93
bc 49,0,66,60
M 94
mh 6
M 95
WL 2
R 80
M 96
ad 66,32
H 0
M 97
M 98
M 99
M 100
H 1
M 101
M 102
M 103
M 104
M 105
M 106
M 107
WL 2
R 82
M 108
M 109
M 110
M 111
M 112
M 113
M 114
M 115
M 116
WV 24
M 117
WL 0
s_barrier
M 118
mw 72
M 119
R 0
M 120
ad 84,1128
W 0
M 121
M 132
M 133
T 24,8
M 122
T 28,22
M 123
W 1
M 134
M 135
T 32,9
M 124
T 36,23
M 125
W 2
M 136
M 137
T 40,10
M 126
T 44,24
M 127
mw 75
M 138
ad 84,1131
W 0
M 139
WL 8
T 48,11
M 128
T 52,25
M 129
M 140
W 1
M 141
T 56,12
M 130
WL 8
T 60,26
M 131
R 2
M 142
W 2
M 143
T 64,13
M 0
WL 8
T 68,27
M 1
R 4
M 2
WV 29
UL 6,34
UH 7,34
M 3
UL 8,35
UH 9,35
M 4
AD 180,6
AD 204,7
M 5
AD 228,8
AD 252,9
M 6
WV 27
UL 6,22
UH 7,22
M 7
WL 8
UL 8,23
UH 9,23
M 8
AD 181,6
AD 205,7
M 9
WL 2
AD 229,8
AD 253,9
M 10
WV 24
UL 6,24
UH 7,24
M 11
R 6
M 12
UL 8,25
UH 9,25
M 13
AD 182,6
AD 206,7
M 14
AD 230,8
AD 254,9
M 15
UL 6,14
UH 7,14
M 16
UL 8,15
UH 9,15
M 17
AD 183,6
AD 207,7
M 18
AD 231,8
AD 255,9
M 19
M 20
M 21
M 22
M 23
WL 2
R 8
M 24
M 25
M 26
M 27
M 28
M 29
M 30
M 31
M 32
M 33
M 34
M 35
WL 2
R 10
M 36
M 37
M 38
M 39
M 40
M 41
M 42
M 43
M 44
WV 18
M 45
WL 0
s_barrier
M 46
mw 0
M 47
R 24
M 48
ad 84,0
W 0
M 49
M 50
M 51
M 52
W 1
M 53
M 54
M 55
M 56
W 2
M 57
M 58
M 59
R 26
M 60
mw 3
M 61
ad 84,3
W 0
M 62
M 63
M 64
M 65
W 1
M 66
M 67
M 68
M 69
W 2
M 70
M 71
WL 2
R 28
M 72
M 73
bc 48,0,64,56
M 74
mh 8
M 75
ad 64,34
H 0
M 76
M 77
M 78
M 79
H 1
M 80
M 81
M 82
M 83
WL 2
R 30
M 84
bc 48,1,64,56
M 85
mh 10
M 86
ad 64,130
H 0
M 87
M 88
M 89
M 90
H 1
M 91
M 92
M 93
M 94
M 95
WL 2
R 32
M 96
M 97
M 98
M 99
M 100
M 101
M 102
M 103
M 104
M 105
M 106
M 107
WL 2
R 34
M 108
M 109
M 110
M 111
M 112
M 113
M 114
M 115
M 116
WV 18
M 117
WL 0
s_barrier
M 118
mw 24
M 119
R 48
M 120
ad 84,24
W 0
M 121
M 132
M 133
T 24,0
M 122
T 28,14
M 123
W 1
M 134
M 135
T 32,1
M 124
T 36,15
M 125
W 2
M 136
M 137
T 40,2
M 126
T 44,16
M 127
mw 27
M 138
ad 84,27
W 0
M 139
WL 8
T 48,3
M 128
T 52,17
M 129
M 140
W 1
M 141
T 56,4
M 130
WL 8
T 60,18
M 131
R 50
M 142
W 2
M 143
T 64,5
M 0
WL 8
T 68,19
M 1
R 52
M 2
M 3
M 4
M 5
M 6
M 7
WL 8
)"
R"(M 8
M 9
WL 2
M 10
M 11
R 54
M 12
M 13
M 14
M 15
M 16
M 17
M 18
M 19
M 20
M 21
M 22
M 23
WL 2
R 56
M 24
M 25
M 26
M 27
M 28
M 29
M 30
M 31
M 32
M 33
M 34
M 35
WL 2
R 58
M 36
M 37
M 38
M 39
M 40
AR 6,72
AR 7,96
M 41
AR 8,120
s_nop 0
AR 9,144
M 42
EF 6
EF 7
M 43
WV 16
EF 8
EF 9
M 44
WL 0
CV 16,6,7
CV 17,8,9
M 45
s_barrier
AR 6,73
AR 7,97
M 46
mw 48
AR 8,121
AR 9,145
M 47
R 72
M 48
ad 84,48
W 0
M 49
EF 6
EF 7
M 50
EF 8
EF 9
M 51
CV 20,6,7
CV 21,8,9
M 52
W 1
AR 6,74
M 53
AR 7,98
AR 8,122
M 54
AR 9,146
EF 6
M 55
EF 7
EF 8
M 56
W 2
EF 9
M 57
CV 26,6,7
CV 27,8,9
M 58
AR 6,75
AR 7,99
M 59
R 74
M 60
mw 51
AR 8,123
AR 9,147
M 61
ad 84,51
W 0
M 62
EF 6
EF 7
M 63
EF 8
EF 9
M 64
CV 42,6,7
CV 43,8,9
M 65
W 1
AR 6,76
M 66
ae 42,72,384
AR 7,100
M 67
s_bitcmp1_b32 s50, 12
s_cselect_b32 s52, 24576, 0
v_add_u32 v53, s52, v3
M 68
ST 16,0
AR 8,124
M 69
W 2
AR 9,148
M 70
EF 6
EF 7
M 71
WL 0
R 76
M 72
ST 20,768
EF 8
M 73
EF 9
CV 38,6,7
M 74
CV 39,8,9
AR 6,77
M 75
AR 7,101
AR 8,125
M 76
ST 26,1536
AR 9,149
M 77
EF 6
EF 7
M 78
EF 8
EF 9
M 79
CV 36,6,7
CV 37,8,9
M 80
ST 42,2304
AR 6,78
M 81
AR 7,102
AR 8,126
M 82
AR 9,150
EF 6
M 83
R 78
M 84
EF 7
EF 8
M 85
EF 9
CV 28,6,7
M 86
CV 29,8,9
AR 6,79
M 87
AR 7,103
AR 8,127
M 88
AR 9,151
EF 6
M 89
EF 7
EF 8
M 90
EF 9
CV 30,6,7
M 91
CV 31,8,9
M 92
ae 42,72,384
EF 64
M 93
s_bfe_u32 s52, s50, 0x1000d
v_mad_i32_i24 v53, v51, s52, v50
ST 38,0
M 94
EF 88
EF 112
M 95
WL 1
R 80
M 96
EF 136
CV 32,64,88
M 97
ST 36,768
CV 33,112,136
M 98
EF 65
EF 89
M 99
EF 113
EF 137
M 100
CV 40,65,89
CV 41,113,137
M 101
ST 28,1536
EF 66
M 102
EF 90
EF 114
M 103
EF 138
CV 18,66,90
M 104
CV 19,114,138
EF 67
M 105
ST 30,2304
EF 91
M 106
EF 115
EF 139
M 107
WL 2
R 82
M 108
CV 46,67,91
CV 47,115,139
M 109
ae 42,72,384
EF 68
M 110
s_bitcmp1_b32 s50, 12
s_cselect_b32 s52, 24576, 0
v_add_u32 v53, s52, v3
M 111
ST 32,128
EF 92
M 112
EF 116
EF 140
M 113
CV 48,68,92
CV 49,116,140
M 114
EF 69
EF 93
M 115
WV 23
ST 40,896
EF 117
M 116
WL 0
EF 141
CV 44,69,93
M 117
s_barrier
CV 45,117,141
EF 70
M 118
mw 72
EF 94
EF 118
M 119
R 0
M 120
ad 84,72
W 0
M 121
ST 18,1664
EF 142
M 132
CV 34,70,94
CV 35,118,142
M 133
EF 71
EF 95
M 122
W 1
EF 119
M 123
ST 46,2432
EF 143
M 134
CV 22,71,95
CV 23,119,143
M 135
EF 160
EF 184
M 124
W 2
EF 208
M 125
ae 42,72,384
EF 232
M 136
s_bfe_u32 s52, s50, 0x1000d
v_mad_i32_i24 v53, v51, s52, v50
ST 48,128
M 137
T 40,6
M 126
mw 75
T 44,20
M 127
ad 84,75
W 0
M 138
ST 44,896
CV 24,160,184
M 139
T 48,7
M 128
T 52,21
M 129
W 1
CV 25,208,232
M 140
ST 34,1664
EF 161
M 141
T 56,8
M 130
T 60,22
M 131
WL 8
R 2
M 142
W 2
ST 22,2432
M 143
T 64,9
M 4
T 68,23
M 5
WL 8
R 4
M 6
EF 185
EF 209
M 7
WL 8
EF 233
CV 14,161,185
M 8
CV 15,209,233
EF 162
M 9
WL 2
EF 186
EF 210
M 10
EF 234
CV 16,162,186
M 11
R 6
M 16
CV 17,210,234
EF 163
M 17
EF 187
EF 211
M 18
EF 235
CV 20,163,187
M 19
CV 21,211,235
M 20
ae 42,72,384
EF 164
M 21
s_bitcmp1_b32 s50, 12
s_cselect_b32 s52, 24576, 0
v_add_u32 v53, s52, v3
M 22
ST 24,256
EF 188
M 23
WL 2
)"
R"(R 8
M 28
EF 212
EF 236
M 29
CV 26,164,188
CV 27,212,236
M 30
ST 14,1024
EF 165
M 31
EF 189
EF 213
M 32
EF 237
CV 42,165,189
M 33
CV 43,213,237
EF 166
M 34
ST 16,1792
EF 190
M 35
WL 2
R 10
M 40
EF 214
EF 238
M 41
CV 38,166,190
CV 39,214,238
M 42
ST 20,2560
EF 167
M 43
EF 191
EF 215
M 44
WV 32
EF 239
CV 36,167,191
M 45
WL 0
CV 37,215,239
M 46
s_barrier
AR 6,80
AR 7,104
M 47
R 24
M 52
ae 42,72,384
AR 8,128
M 53
s_bfe_u32 s52, s50, 0x1000d
v_mad_i32_i24 v53, v51, s52, v50
ST 26,256
M 54
AR 9,152
EF 6
M 55
EF 7
EF 8
M 56
EF 9
CV 28,6,7
M 57
ST 42,1024
CV 29,8,9
M 58
AR 6,81
AR 7,105
M 59
R 26
M 64
AR 8,129
AR 9,153
M 65
ST 38,1792
EF 6
M 66
EF 7
EF 8
M 67
EF 9
CV 30,6,7
M 68
CV 31,8,9
AR 6,82
M 69
ST 36,2560
AR 7,106
M 70
AR 8,130
AR 9,154
M 71
WL 2
R 28
M 76
EF 6
EF 7
M 77
EF 8
EF 9
M 78
CV 32,6,7
CV 33,8,9
M 79
AR 6,83
AR 7,107
M 80
AR 8,131
AR 9,155
M 81
EF 6
EF 7
M 82
EF 8
EF 9
M 83
WL 2
R 30
M 88
CV 40,6,7
CV 41,8,9
M 89
ae 42,74,384
AR 6,84
M 90
s_bitcmp1_b32 s50, 14
s_cselect_b32 s52, 24576, 0
v_add_u32 v53, s52, v3
M 91
ST 28,0
AR 7,108
M 92
AR 8,132
AR 9,156
M 93
EF 6
EF 7
M 94
EF 8
EF 9
M 95
WL 2
R 32
M 100
ST 30,768
CV 18,6,7
M 101
CV 19,8,9
AR 6,85
M 102
AR 7,109
AR 8,133
M 103
AR 9,157
EF 6
M 104
ST 32,1536
EF 7
M 105
EF 8
EF 9
M 106
CV 46,6,7
CV 47,8,9
M 107
WL 2
R 34
M 112
ST 40,2304
AR 6,86
M 113
AR 7,110
AR 8,134
M 114
AR 9,158
EF 6
M 115
EF 7
EF 8
M 116
WV 33
EF 9
CV 48,6,7
M 117
WL 0
CV 49,8,9
AR 6,87
M 118
s_barrier
AR 7,111
AR 8,135
M 119
R 48
M 124
AR 9,159
EF 6
M 125
EF 7
EF 8
M 136
EF 9
CV 44,6,7
M 137
CV 45,8,9
M 126
ae 42,74,384
EF 72
M 127
s_bfe_u32 s52, s50, 0x1000f
v_mad_i32_i24 v53, v51, s52, v50
ST 18,0
M 138
EF 96
EF 120
M 139
EF 144
CV 34,72,96
M 128
CV 35,120,144
EF 73
M 129
ST 46,768
EF 97
M 140
EF 121
EF 145
M 141
T 56,10
M 130
T 60,24
M 131
R 50
M 142
ST 48,1536
CV 22,73,97
M 143
T 64,11
WL 4
M 8
T 68,25
M 9
R 52
WL 2
M 10
ST 44,2304
CV 23,121,145
M 11
R 54
M 20
EF 74
EF 98
M 21
EF 122
EF 146
M 22
CV 24,74,98
CV 25,122,146
M 23
WL 2
R 56
M 32
EF 75
EF 99
M 33
EF 123
EF 147
M 34
CV 14,75,99
CV 15,123,147
M 35
WL 2
R 58
M 44
EF 76
EF 100
M 45
WV 17
EF 124
EF 148
M 46
WL 0
s_barrier
CV 16,76,100
CV 17,124,148
M 47
R 72
M 56
ae 42,74,384
EF 77
M 57
s_bitcmp1_b32 s50, 14
s_cselect_b32 s52, 24576, 0
v_add_u32 v53, s52, v3
M 58
ST 34,128
EF 101
M 59
R 74
M 68
EF 125
EF 149
M 69
CV 20,77,101
CV 21,125,149
M 70
ST 22,896
EF 78
M 71
WL 2
R 76
M 80
EF 102
EF 126
M 81
EF 150
CV 26,78,102
M 82
ST 24,1664
CV 27,126,150
M 83
WL 2
R 78
M 92
EF 79
EF 103
M 93
EF 127
EF 151
M 94
ST 14,2432
CV 42,79,103
M 95
WL 2
R 80
M 104
CV 43,127,151
M 105
EF 168
EF 192
EF 216
M 106
ae 42,74,384
EF 240
CV 38,168,192
M 107
WL 2
R 82
CV 39,216,240
M 116
s_bfe_u32 s52, s50, 0x1000f
v_mad_i32_i24 v53, v51, s52, v50
ST 16,128
EF 169
M 117
EF 193
EF 217
EF 241
M 118
CV 36,169,193
CV 37,217,241
EF 170
M 119
WL 0
EF 194
EF 218
EF 242
M 128
ST 20,896
CV 28,170,194
CV 29,218,242
M 129
EF 171
EF 195
EF 219
M 140
EF 243
CV 30,171,195
CV 31,219,243
M 141
EF 172
EF 196
EF 220
EF 244
CV 32,172,196
CV 33,220,244
M 130
ST 26,1664
EF 173
EF 197
EF 221
EF 245
CV 40,173,197
M 131
CV 41,221,245
EF 174
EF 198
)"
R"(EF 222
EF 246
CV 18,174,198
M 142
CV 19,222,246
EF 175
EF 199
EF 223
EF 247
CV 46,175,199
M 143
ST 42,2432
ae 42,74,384
s_bitcmp1_b32 s50, 14
s_cselect_b32 s52, 24576, 0
v_add_u32 v53, s52, v3
ST 38,256
ST 36,1024
ST 28,1792
ST 30,2560
CV 47,223,247
ae 42,74,384
s_bfe_u32 s52, s50, 0x1000f
v_mad_i32_i24 v53, v51, s52, v50
ST 32,256
ST 40,1024
ST 18,1792
ST 46,2560
AR 6,88
AR 7,112
AR 8,136
EF 6
EF 7
EF 8
EF 56
CV 48,6,7
CV 49,8,56
AR 6,89
AR 7,113
AR 8,137
EF 6
EF 7
EF 8
EF 57
CV 44,6,7
CV 45,8,57
AR 6,90
AR 7,114
AR 8,138
EF 6
EF 7
EF 8
EF 58
CV 34,6,7
CV 35,8,58
AR 6,91
AR 7,115
AR 8,139
EF 6
EF 7
EF 8
EF 59
CV 22,6,7
CV 23,8,59
ae 42,72,432
s_bitcmp1_b32 s50, 16
s_cselect_b32 s52, 24576, 0
v_add_u32 v53, s52, v3
ST 48,0
ST 44,768
ST 34,1536
ST 22,2304
AR 6,92
AR 7,116
AR 8,140
EF 6
EF 7
EF 8
EF 60
CV 24,6,7
CV 25,8,60
AR 6,93
AR 7,117
AR 8,141
EF 6
EF 7
EF 8
EF 61
CV 14,6,7
CV 15,8,61
AR 6,94
AR 7,118
AR 8,142
EF 6
EF 7
EF 8
EF 62
CV 16,6,7
CV 17,8,62
AR 6,95
AR 7,119
AR 8,143
EF 6
EF 7
EF 8
EF 63
CV 20,6,7
CV 21,8,63
ae 42,72,432
s_bfe_u32 s52, s50, 0x10011
v_mad_i32_i24 v53, v51, s52, v50
ST 24,0
ST 14,768
ST 16,1536
ST 20,2304
EF 80
EF 104
EF 128
EF 152
CV 26,80,104
CV 27,128,152
EF 81
EF 105
EF 129
EF 153
CV 42,81,105
CV 43,129,153
EF 82
EF 106
EF 130
EF 154
CV 38,82,106
CV 39,130,154
EF 83
EF 107
EF 131
EF 155
CV 36,83,107
CV 37,131,155
ae 42,72,432
s_bitcmp1_b32 s50, 16
s_cselect_b32 s52, 24576, 0
v_add_u32 v53, s52, v3
ST 26,128
ST 42,896
ST 38,1664
ST 36,2432
EF 84
EF 108
EF 132
EF 156
CV 28,84,108
CV 29,132,156
EF 85
EF 109
EF 133
EF 157
CV 30,85,109
CV 31,133,157
EF 86
EF 110
EF 134
EF 158
CV 32,86,110
CV 33,134,158
EF 87
EF 111
EF 135
EF 159
CV 40,87,111
CV 41,135,159
ae 42,72,432
s_bfe_u32 s52, s50, 0x10011
v_mad_i32_i24 v53, v51, s52, v50
ST 28,128
ST 30,896
ST 32,1664
ST 40,2432
EF 176
EF 200
EF 224
EF 248
CV 18,176,200
CV 19,224,248
EF 177
EF 201
EF 225
EF 249
CV 46,177,201
CV 47,225,249
EF 178
EF 202
EF 226
EF 250
CV 48,178,202
CV 49,226,250
EF 179
EF 203
EF 227
EF 251
CV 44,179,203
CV 45,227,251
ae 42,72,432
s_bitcmp1_b32 s50, 16
s_cselect_b32 s52, 24576, 0
v_add_u32 v53, s52, v3
ST 18,256
ST 46,1024
ST 48,1792
ST 44,2560
EF 180
EF 204
EF 228
EF 252
CV 34,180,204
CV 35,228,252
EF 181
EF 205
EF 229
EF 253
CV 22,181,205
CV 23,229,253
EF 182
EF 206
EF 230
EF 254
CV 24,182,206
CV 25,230,254
EF 183
EF 207
EF 231
EF 255
CV 14,183,207
CV 15,231,255
ae 42,72,432
s_bfe_u32 s52, s50, 0x10011
v_mad_i32_i24 v53, v51, s52, v50
ST 34,256
ST 22,1024
ST 24,1792
ST 14,2560
s_waitcnt vmcnt(0) lgkmcnt(0)
.purgem MA
.purgem MV
.purgem AN
.purgem AW
.purgem AR
.purgem UL
.purgem UH
.purgem AD
.purgem TR
.purgem TQ
.purgem RA
.purgem RC
.purgem BV
.purgem BA
.purgem PA
.purgem DF
.purgem DW
.purgem DH
.purgem DS
.purgem XL
.purgem ST
.purgem SU
.purgem SA
.purgem CV
.purgem DR
.purgem TY
.purgem TZ
.purgem RS
.purgem PM
.purgem FS
.purgem FL
.purgem FB
.purgem FG
.purgem FX
.purgem PB
.purgem PQ
.purgem PJ
.purgem WL
.purgem WV
.purgem EF
.purgem DP
.purgem M
.purgem R1
.purgem R
.purgem T
.purgem mw
.purgem mh
.purgem ad
.purgem ae
.purgem bc
.purgem W
.purgem H
)"
      : [flg] "=&s"(flg) : K2_OPS : K2_CLOB);
  if (flg) {
    const unsigned lane = __builtin_amdgcn_mbcnt_hi(~0u, __builtin_amdgcn_mbcnt_lo(~0u, 0u));
    const bool hbf = __ballot(lane != 1 && __hip_atomic_load((const unsigned*)(g_buf + G_FLAG) + 96 + min(lane, 13u), __ATOMIC_RELAXED, __HIP_MEMORY_SCOPE_AGENT) != 0) != 0;
    const u16* const volatile __attribute__((address_space(4)))* ka = (const u16* const volatile __attribute__((address_space(4)))*)__builtin_amdgcn_kernarg_segment_ptr();
    if (hbf) {
      const u16* w1p = ka[6];
#pragma unroll 1
      for (unsigned f = blockIdx.x * 4 + w; f < 1152; f += 1024) {
        const u16* p = w1p + (32 * (f % 12) + 8 * (lane >> 4)) * HID + 16 * (f / 12) + (lane & 15);
        u32x4 v;
#pragma unroll
        for (int j = 0; j < 4; ++j) v[j] = p[2 * j * HID] | (unsigned)p[(2 * j + 1) * HID] << 16;
        *(u32x4*)(g_buf + f * 1024 + lane * 16) = v;
      }
    }
    slow_bar((unsigned*)(g_buf + G_BAR), w);
    if (hbf) {
      slow_fc1(ka[0], ka[5], (char*)ka[1], (float*)smem + w * HID, w);
      slow_bar((unsigned*)(g_buf + G_BAR + 64), w);
    }
    slow_fc2(ka[0], (const char*)ka[1], (u16*)ka[2], ka[3], ka[4], smem, hbf, w);
  }
  TS(15);
  MK_END(4);
}

extern "C" void kernel_launch(void* const* d_in, const int* in_sizes, int n_in,
                              void* d_out, int out_size, void* d_ws, size_t ws_size,
                              hipStream_t stream) {
  const u16* x = (const u16*)d_in[0];
  const u16* dw = (const u16*)d_in[1];
  const u16* w1 = (const u16*)d_in[2];
  const u16* w2 = (const u16*)d_in[3];
  const u16* grn_g = (const u16*)d_in[4];
  const u16* grn_b = (const u16*)d_in[5];
  char* ws = (char*)d_ws;
  if (ws_size < H_BYTES) return;
  (void)hipFuncSetAttribute((const void*)k1_fc1, hipFuncAttributeMaxDynamicSharedMemorySize, K1_LDS);
  (void)hipFuncSetAttribute((const void*)k2_fc2, hipFuncAttributeMaxDynamicSharedMemorySize, K2_LDS);

  k0_prep<<<K0_NB + NPAGE + NPAGX, 256, 0, stream>>>(w1, dw, ws, (const char*)x, grn_b, w2);
  k1_fc1<<<NWG + 8, 256, K1_LDS, stream>>>(x, ws, (const u16*)d_out, grn_g);
  k2_fc2<<<NWG, 256, K2_LDS, stream>>>(x, ws, (u16*)d_out, grn_g, w2, dw, w1);
}
```

```cpp
#include <hip/hip_runtime.h>
#include <hip/hip_bf16.h>
#include <stdint.h>
#include <stdio.h>

typedef short bf16x8 __attribute__((ext_vector_type(8)));
typedef short s16x4 __attribute__((ext_vector_type(4)));
typedef short v4i16_t __attribute__((ext_vector_type(4)));
typedef float f32x16 __attribute__((ext_vector_type(16)));
typedef float f32x4 __attribute__((ext_vector_type(4)));
typedef float f32x2 __attribute__((ext_vector_type(2)));
typedef unsigned u32x4 __attribute__((ext_vector_type(4)));
typedef __bf16 bf16x2_t __attribute__((ext_vector_type(2)));
typedef unsigned short u16;

constexpr float TZ = 175.f, TH = 175.f, TW2 = 175.f;
#define DI __device__ __forceinline__
#define LAS __attribute__((address_space(3)))

constexpr int NPIX = 147456;
constexpr int CH = 384;
constexpr int HID = 1536;
constexpr int NTILE = NPIX / 32;
constexpr int NWG = 256;
constexpr int TPW = NTILE / NWG;

constexpr size_t H_BYTES = (size_t)NPIX * HID * 2;
constexpr unsigned K1_LDS = 163840, K1_PF_OFF = 130048, K1_SMAX = 12, K1_NREP = 4, K1_G_TAB = 1228800, K1_G_CNT = 1239040, K1_G_LOC = 1236992, K1_G_DUM = 1255424,
  K1_G_ACC = 1320960, K1_G_LUT = 1382400, K1_G_BYTES = 1398784;
constexpr bool K1_LUT = true;
constexpr float K1_ZS = 16.0f;
constexpr unsigned K1_GST = 25600, K1_WCH = 6400, K1_ZC = 1365;
constexpr unsigned K1_SB1 = 32768;
constexpr bool K1_F2 = true;
constexpr unsigned K2_SHT = 12483;
constexpr int K2_LDS = 162816, K2_HB = 98304, K2_RSZ = 13824, K2_E_OFF = 153600, K2_PF_OFF = 159744, K2_S16_OFF = 152576, K2_S16_D0 = 8192, K2_NAT = 8;

__device__ __attribute__((aligned(256))) char g_buf[K1_G_BYTES];
__device__ __attribute__((aligned(256))) float g_cs[8 * 384];
__device__ __attribute__((aligned(256))) char g_w2r[96 * 12 * 1024];
__device__ __attribute__((aligned(256))) char g_w2f[96 * 12 * 1024];
__device__ __attribute__((aligned(256))) char g_w2q[16 * 12 * 1024];
__device__ __attribute__((aligned(256))) float g_sc[64];
constexpr unsigned G_FLAG = K1_G_CNT + 14336;
constexpr unsigned G_PUB = K1_G_DUM;
__device__ __attribute__((aligned(256))) unsigned g_prm[1024 * 64];
constexpr unsigned G_STMP = K1_G_CNT + 12288;
constexpr unsigned G_BAR = K1_G_CNT + 13312;
constexpr unsigned H_TS = 49152, H_RS = 48 * H_TS, NPAGE = 217, NPAGX = 55;
#define TS(i) do { } while (0)
#define KST(i) do { } while (0)
#define MK_START(i) do { } while (0)
#define MK_END(i) do { } while (0)

DI unsigned pack_bf16(float a, float b) {
  f32x2 v = {a, b};
  bf16x2_t r = __builtin_convertvector(v, bf16x2_t);
  return __builtin_bit_cast(unsigned, r);
}
typedef _Float16 f16x2_t __attribute__((ext_vector_type(2)));
DI unsigned pack_f16(float a, float b) {
  f32x2 v = {a, b};
  f16x2_t r = __builtin_convertvector(v, f16x2_t);
  return __builtin_bit_cast(unsigned, r);
}
DI float bf_lo(unsigned u) { return __uint_as_float(u << 16); }
DI float bf_hi(unsigned u) { return __uint_as_float(u & 0xffff0000u); }
DI float round_bf16(float a) { return bf_lo(pack_bf16(a, 0.f)); }
DI float dot2acc(unsigned a, unsigned b, float c) {
  return __builtin_amdgcn_fdot2_f32_bf16(__builtin_bit_cast(bf16x2_t, a), __builtin_bit_cast(bf16x2_t, b), c, false);
}
DI float ld_bf16(const u16* p) { return __uint_as_float((unsigned)(*p) << 16); }

DI s16x4 tr_read(const char* p) {
  return __builtin_bit_cast(s16x4, __builtin_amdgcn_ds_read_tr16_b64_v4i16((LAS v4i16_t*)p));
}

#define MFMA32(a, b, c) __builtin_amdgcn_mfma_f32_32x32x16_bf16((a), (b), (c), 0, 0, 0)
#define BAR() __builtin_amdgcn_s_barrier()

DI void dma16(unsigned voff, const char* sbase, unsigned lds_dst) {
  unsigned keep;
  asm volatile("s_mov_b32 %0, m0\n\ts_mov_b32 m0, %3\n\ts_nop 0\n\tglobal_load_lds_dwordx4 %1, %2\n\ts_mov_b32 m0, %0"
               : "=&s"(keep) : "v"(voff), "s"(sbase), "s"(lds_dst) : "memory");
}
DI unsigned lds_addr(const char* p) { return (unsigned)(size_t)(const LAS char*)p; }

DI float gelu_tanh(float x) {
  const float c0 = -2.0f * 0.7978845608028654f * 1.4426950408889634f;
  const float c1 = c0 * 0.044715f;
  float x2 = x * x;
  float t = __builtin_fmaf(x2, c1, c0);
  float e = __builtin_amdgcn_exp2f(x * t);
  float r = __builtin_amdgcn_rcpf(1.0f + e);
  return x * r;
}

DI void gload_b128_asm(u32x4& dst, const char* sbase, unsigned voff) {
  asm volatile("global_load_dwordx4 %0, %1, %2" : "=v"(dst) : "v"(voff), "s"(sbase) : "memory");
}
DI void lds_rd128(u32x4& dst, unsigned addr, int off) {
  asm volatile("ds_read_b128 %0, %1 offset:%2" : "=v"(dst) : "v"(addr), "i"(off));
}
DI void lds_rd128_far(u32x4& dst, unsigned voff, unsigned sbase, int imm) {
  unsigned t;
  asm volatile("v_add_u32 %1, %3, %2\n\tv_add_u32 %1, %4, %1\n\tds_read_b128 %0, %1" : "=v"(dst), "=&v"(t) : "v"(voff), "s"(sbase), "i"(imm));
}
DI void gload16i(u32x4& dst, const char* sbase, unsigned voff, int imm) {
  unsigned t;
  asm volatile("v_add_u32 %1, %4, %2\n\tglobal_load_dwordx4 %0, %1, %3" : "=v"(dst), "=&v"(t) : "v"(voff), "s"(sbase), "i"(imm) : "memory");
}
DI void dma16i(const char* sbase, unsigned voff, int immv, unsigned lbase, int imml) {
  unsigned t, keep;
  asm volatile("s_mov_b32 %1, m0\n\ts_add_u32 m0, %4, %6\n\tv_add_u32 %0, %5, %2\n\tglobal_load_lds_dwordx4 %0, %3\n\ts_mov_b32 m0, %1"
               : "=&v"(t), "=&s"(keep) : "v"(voff), "s"(sbase), "s"(lbase), "i"(immv), "i"(imml) : "memory", "scc");
}

DI void st16_wt(void* p, u32x4 v) { asm volatile("global_store_dwordx4 %0, %1, off\n\ts_nop 1" :: "v"(p), "v"(v) : "memory"); }
template <int I> struct IC { static constexpr int value = I; };
template <int N, int I = 0, typename F> DI void static_for(F&& f) {
  if constexpr (I < N) { f(IC<I>{}); static_for<N, I + 1>(f); }
}
template <int C, int M> DI float dpp_add(float v) { return v + __int_as_float(__builtin_amdgcn_update_dpp(0, __float_as_int(v), C, M, 0xf, false)); }
DI float wave_sum63(float v) {
  v = dpp_add<0x111, 0xf>(v); v = dpp_add<0x112, 0xf>(v); v = dpp_add<0x114, 0xf>(v); v = dpp_add<0x118, 0xf>(v);
  v = dpp_add<0x142, 0xa>(v); return dpp_add<0x143, 0xc>(v);
}
DI unsigned cs_index(unsigned n) { return (n & ~63u) + (n & 3) * 16 + ((n >> 2) & 15); }
DI uint64_t uni64(uint64_t v) { return (uint64_t)__builtin_amdgcn_readfirstlane(v >> 32) << 32 | (unsigned)__builtin_amdgcn_readfirstlane(v); }
DI unsigned wg_side(unsigned b) { return (((const unsigned*)(g_buf + K1_G_LOC))[b & 3] ^ (b >> 2)) & 1; }
DI unsigned h_flip(const char* ws, unsigned region, unsigned t) { return __popc(((const unsigned*)(g_buf + K1_G_LOC + 64))[(((unsigned)(size_t)ws & 0x1fffffu) + region * H_RS + t * H_TS) >> 21]) > 2; }
DI unsigned x_flip(const u16* x, unsigned region) { return __popc(((const unsigned*)(g_buf + K1_G_LOC + 1024))[(((unsigned)(size_t)x & 0x1fffffu) + (12 * region + 6) * 49152u) >> 21]) > 2; }
DI unsigned sub_pixel(const u16* x, unsigned sub, unsigned m) {
  const unsigned blk = sub / 24, u = sub % 24;
  const unsigned region = blk < 256 ? (blk >> 3) * 4 + (blk & 3) : 128 + ((blk - 256) >> 1), side = blk < 256 ? wg_side(blk) : blk & 1;
  return (12 * region + (u >> 1)) * 64 + ((0xda43cb52fe987610ull >> (32 * (u & 1) + 16 * (side ^ x_flip(x, region)) + (m & 12))) & 15) * 4 + (m & 3);
}
DI unsigned h_piece(const char* ws, unsigned sub, unsigned t) {
  const unsigned blk = sub / 24, u = sub % 24;
  const unsigned region = blk < 256 ? (blk >> 3) * 4 + (blk & 3) : 128 + ((blk - 256) >> 1), side = blk < 256 ? wg_side(blk) : blk & 1;
  return region * H_RS + t * H_TS + (u >> 3) * 16384 + (u & 7) * 1024 + ((side ^ h_flip(ws, region, t)) << 13);
}
constexpr unsigned K0_NB = 145 + 24 + 96 + 16 + 48;
constexpr float U24 = 0x1p-24f, EPS = 1e-5f;
DI float wave_all(float v) { return __int_as_float(__builtin_amdgcn_readlane(__float_as_int(wave_sum63(v)), 63)); }
DI float pow2f(float v) { return __uint_as_float(__float_as_uint(v) & 0x7f800000u); }
DI u32x4 w_load(const u16* w) { return *(const u32x4*)(w + ((threadIdx.x >> 3) & 7) * 73408 + 36800 + (threadIdx.x & 7) * 8); }
DI float w_ms(u32x4 a) {
  float q = 0.f;
#pragma unroll
  for (int i = 0; i < 4; ++i) { const float lo = bf_lo(a[i]), hi = bf_hi(a[i]); q = __builtin_fmaf(lo, lo, __builtin_fmaf(hi, hi, q)); }
  return wave_all(q) * (1.0f / 512);
}
DI float w1_scale(float ms) { return pow2f(300.f * U24 * __builtin_amdgcn_rsqf(ms)); }
DI float w2_scale(float ms) { return TW2 * U24 * __builtin_amdgcn_rsqf(ms); }
struct Sc { float w1, zs, g, h, vlo, vhi, c; };
DI Sc scales(const u16* w1, const u16* x, const u16* dw) {
  const u32x4 a = w_load(w1);
  const unsigned l = threadIdx.x & 63;
  float s = 0.f, q = 0.f;
#pragma unroll
  for (int k = 0; k < 6; ++k) {
    const u32x4 v = *(const u32x4*)(x + (9001 + 18397 * (l >> 3)) * CH + 64 * k + 8 * (l & 7)), d = *(const u32x4*)(dw + 64 * k + 8 * (l & 7));
#pragma unroll
    for (int i = 0; i < 4; ++i) { const float lo = bf_lo(v[i]) * bf_lo(d[i]), hi = bf_hi(v[i]) * bf_hi(d[i]); s += lo + hi; q = __builtin_fmaf(lo, lo, __builtin_fmaf(hi, hi, q)); }
  }
  s = dpp_add<0x141, 0xf>(dpp_add<0x4e, 0xf>(dpp_add<0xb1, 0xf>(s))); q = dpp_add<0x141, 0xf>(dpp_add<0x4e, 0xf>(dpp_add<0xb1, 0xf>(q)));
  const float var = fmaxf(q * (1.0f / CH) - s * s * (1.0f / (CH * CH)), 0.f);
  const float r2 = wave_all(var * __builtin_amdgcn_rcpf(var + EPS)) * (1.0f / 64), rden = __builtin_amdgcn_exp2f(wave_all(__builtin_amdgcn_logf(var + EPS)) * (-1.0f / 128));
  const float s1 = w_ms(a), p2 = r2 * CH * s1;
  Sc c;
  c.w1 = w1_scale(s1);
  c.g = TZ * U24 * __builtin_amdgcn_rsqf(r2);
  c.h = TH * U24 * __builtin_amdgcn_rsqf(p2 * (0.25f + 0.5f * p2 * __builtin_amdgcn_rcpf(1.0f + 2.0f * p2)));
  c.c = fminf(__builtin_ceilf(0.1701f * 0x1p24f * c.h) + 1.0f, 1400.f);
  c.zs = fminf(pow2f(c.g * 2530.f), pow2f(c.g * 0x1p12f * rden));
  const float e = EPS * c.zs * c.zs, z2 = r2 * (660.f * 660.f / (TZ * TZ));
  c.vlo = 4.2f * c.g * c.g - e;
  c.vhi = fminf(0x1.ep29f * c.g * c.g - e, z2 < 1.0f ? e * z2 * __builtin_amdgcn_rcpf(1.0f - z2) : 0x1p120f);
  return c;
}
__global__ __launch_bounds__(256) void k0_prep(const u16* __restrict__ w1, const u16* __restrict__ dw, const char* __restrict__ ws, const char* __restrict__ x,
                                                const u16* __restrict__ grn_b, const u16* __restrict__ w2) {
  MK_START(0);
  uint64_t pc; int d1;
  asm volatile("s_getpc_b64 %0\n1:\n\ts_mov_b32 %1, k1_code-1b" : "=s"(pc), "=s"(d1));
  if (threadIdx.x >= 192) pc += (int64_t)d1 + ((int)(blockIdx.x >> 3) - 3) * 8192;
  unsigned pfd = 0;
  if (blockIdx.x < 64 && (threadIdx.x < 150 || (blockIdx.x < 48 && threadIdx.x >= 192))) pfd = *(const __attribute__((address_space(1))) unsigned*)((pc & ~127ull) + threadIdx.x * 128);
#define K0_END() do { asm volatile("" :: "v"(pfd)); MK_END(0); } while (0)
  unsigned xcc;
  asm volatile("s_getreg_b32 %0, hwreg(HW_REG_XCC_ID)" : "=s"(xcc));
  xcc = (xcc >> 2) & 1;
  if (blockIdx.x < 4 && threadIdx.x == 0) ((unsigned*)(g_buf + K1_G_LOC))[blockIdx.x] = 7 * xcc;
  constexpr unsigned NP = NPAGE + NPAGX;
  __shared__ Sc ssc;
  __shared__ float ss2;
  const bool sw = threadIdx.x >= 192;
  const unsigned bi = blockIdx.x;
  const unsigned bx = bi < NP ? bi + K0_NB : bi - NP;
  if (bx >= K0_NB) {
    __shared__ unsigned lat[8];
    const unsigned pg = bx - K0_NB, w = threadIdx.x >> 6, lane = threadIdx.x & 63;
    auto gran = [&](unsigned p) {
      const bool h = p < NPAGE;
      const char* buf = h ? ws : x;
      const int o0 = (int)((h ? p : p - NPAGE) << 21) - (int)((unsigned)(size_t)buf & 0x1fffffu);
      const unsigned al = h ? 49152 : 12 * 49152;
      const unsigned q = ((((unsigned)max(o0, 0) + al - 1) >> (h ? 14 : 16)) * (h ? 87382u : 29128u)) >> 18;
      return buf + min(q * al, (unsigned)(h ? H_BYTES : H_BYTES / 4) - 16384u);
    };
    if (lane < 5) {
      const char* a = gran(lane < 4 ? min(pg + NP - 1 - (lane >> 1), pg - 1 - (lane >> 1)) : pg) + (lane & 1) * 8192 + w * 2048 + (lane < 4 ? 1024 : 0);
      const char* b = a;
      const char* own = (const char*)uni64(__builtin_amdgcn_readlane((uint64_t)a >> 32, 4) * 4294967296ull + (unsigned)__builtin_amdgcn_readlane((unsigned)(uint64_t)a, 4));
      unsigned long long t0, t1, t2;
      int n = 2;
      asm volatile("" : "+s"(n));
#pragma unroll 1
      do {
        unsigned v;
        asm volatile("s_cmp_eq_u32 %6, 2\n\ts_cbranch_scc1 1f\n\ts_sleep 4\n1:\n\ts_memtime %0\n\ts_waitcnt lgkmcnt(0)\n\tglobal_load_dword %3, %4, off sc1\n\ts_waitcnt vmcnt(0)\n\ts_memtime %1\n\ts_waitcnt lgkmcnt(0)\n\t"
                     "global_load_dword %3, %5, off sc1\n\ts_waitcnt vmcnt(0)\n\ts_memtime %2\n\ts_waitcnt lgkmcnt(0)" : "=&s"(t0), "=&s"(t1), "=&s"(t2), "=&v"(v) : "v"(a), "v"(b), "s"(n) : "memory", "scc");
        a = own + 1024 + (w & 1) * 8192; b = a + 8192 - (w & 1) * 16384;
      } while (--n);
      lat[w + 4 * (w & 1)] = (unsigned)(t1 - t0); lat[w + 4 - 4 * (w & 1)] = (unsigned)(t2 - t1);
    }
    __syncthreads();
    if (threadIdx.x == 0) {
      auto fastest = [&](const unsigned* l) { unsigned m = 9999; for (int i = 0; i < 4; ++i) m = l[i] < 300 ? m : min(m, l[i]); return (int)m; };
      const int df = fastest(lat) - fastest(lat + 4);
      const unsigned v = xcc ^ (df > 0), sure = abs(df) > 80;
      char* e = g_buf + K1_G_LOC + 64 + 4 * (pg < NPAGE ? pg : pg + 240 - NPAGE);
      *(u16*)(e + 2) = v ? 3 + 4 * sure : 1 - sure;
      if (pg == 0 || pg == NPAGE) e[0] = v; else e[-3] = v ^ 1;
      if (pg == NPAGE - 1 || pg == NP - 1) e[1] = v; else e[4] = v ^ 1;
    }
    K0_END();
    return;
  }
  if (bx >= 145 + 24 + 96 + 16) {
    __shared__ __attribute__((aligned(16))) float red[2][16][32];
    const int t = bx - 281, c8 = (threadIdx.x & 3) * 8;
    u32x4 sm, v[6];
    if (sw) sm = w_load(w1);
#pragma unroll
    for (int k = 0; k < 6; ++k) v[k] = *(const u32x4*)(w1 + (size_t)(64 * k + (threadIdx.x >> 2)) * HID + 32 * t + c8);
    if (sw) ss2 = w1_scale(w_ms(sm));
    __syncthreads();
    const float s1 = ss2;
    float a[8], q[8];
#pragma unroll
    for (int i = 0; i < 8; ++i) {
      a[i] = q[i] = 0.f;
#pragma unroll
      for (int k = 0; k < 6; ++k) {
        const float f = (float)(_Float16)(s1 * (i & 1 ? bf_hi(v[k][i >> 1]) : bf_lo(v[k][i >> 1]))) * 0x1p24f;
        a[i] += f; q[i] = __builtin_fmaf(f, f, q[i]);
      }
      a[i] = dpp_add<0x118, 0xf>(dpp_add<0x114, 0xf>(a[i])); q[i] = dpp_add<0x118, 0xf>(dpp_add<0x114, 0xf>(q[i]));
    }
    if ((threadIdx.x & 12) == 12) {
#pragma unroll
      for (int i = 0; i < 2; ++i) {
        *(f32x4*)&red[0][threadIdx.x >> 4][c8 + 4 * i] = (f32x4){a[4 * i], a[4 * i + 1], a[4 * i + 2], a[4 * i + 3]};
        *(f32x4*)&red[1][threadIdx.x >> 4][c8 + 4 * i] = (f32x4){q[4 * i], q[4 * i + 1], q[4 * i + 2], q[4 * i + 3]};
      }
    }
    __syncthreads();
    if (threadIdx.x < 64) {
      const int n = threadIdx.x & 31;
      float s = 0.f;
#pragma unroll
      for (int i = 0; i < 16; ++i) s += red[threadIdx.x >> 5][i][n];
      const float c = (0.f - (float)K1_ZC * s) * 0x1p-48f;
      char* p = g_buf + t * K1_GST + 5120;
      if (threadIdx.x < 32) *(f32x4*)(p + 272 * (n >> 4) + (n & 15) * 16) = (f32x4){c, c, c, c};
      const bool any = __ballot(threadIdx.x >= 32 && !(s >= 384.f * 64 * 64 && s <= 384.f * 1024 * 1024)) != 0;
      if (threadIdx.x == 0) g_buf[G_FLAG + 4 * 98 + t] = any;
    }
    K0_END();
    return;
  }
  if (bx >= 145 + 24 + 96) {
    if (sw) ssc = scales(w1, (const u16*)x, dw);
    __syncthreads();
    const Sc sc = ssc;
    const unsigned e = (bx - 265) * 256 + threadIdx.x;
    const float u = 1.0f / (sc.g * sc.w1);
    float r[2];
#pragma unroll
    for (int i = 0; i < 2; ++i) {
      const unsigned c = 2 * e + i;
      r[i] = __builtin_fmaf(sc.h, gelu_tanh(u * __uint_as_float((c >> 12) << 31 | ((c & 4095) + 79 * 128) << 16 | 0x8000u)), sc.c * U24);
      if (!(r[i] >= 0.f && r[i] < 8189.f * U24)) r[i] = __builtin_inff();
    }
    *(unsigned*)(g_buf + K1_G_LUT + 4 * e) = pack_f16(r[0], r[1]);
    K0_END();
    return;
  }
  if (bx >= 145 + 24) {
    __shared__ __attribute__((aligned(16))) u16 tw[32][200];
    const int kappa = (bx - 169) >> 1, nh = (bx - 169) & 1;
    u32x4 sm, v[3];
    if (sw) sm = w_load(w2);
#pragma unroll
    for (int i = 0; i < 3; ++i) { const int e = threadIdx.x + 256 * i; v[i] = *(const u32x4*)(w2 + (size_t)(32 * kappa + e / 24) * CH + 192 * nh + (e % 24) * 8); }
    if (sw) ss2 = w2_scale(w_ms(sm));
#pragma unroll
    for (int i = 0; i < 3; ++i) { const int e = threadIdx.x + 256 * i; *(u32x4*)&tw[e / 24][(e % 24) * 8] = v[i]; }
    __syncthreads();
    const int lane = threadIdx.x & 63, g = lane >> 4;
    const float s2 = 256.f * ss2;
    bool bad = false;
#pragma unroll
    for (int i = 0; i < 3; ++i) {
      const int fi = (threadIdx.x >> 6) + 4 * i, n = 64 * (fi >> 2) + 4 * (lane & 15) + (fi & 3);
      float wv[8];
      unsigned bits = 0;
#pragma unroll
      for (int j = 0; j < 8; ++j) {
        wv[j] = ld_bf16(&tw[16 * (j >> 2) + 4 * g + (j & 3)][n]);
        bits |= wv[j] != 0.f ? 256u << j : 0u;
        wv[j] *= s2;
        bits |= fabsf(wv[j]) >= 48 * 256 * U24 ? 1u << j : 0u;
        bad |= !(fabsf(wv[j]) <= 500.f);
      }
      const unsigned o = (((kappa & ~1) + fi / 6) * 24 + nh * 12 + (fi % 6) * 2 + (kappa & 1)) * 1024 + lane * 16;
      st16_wt(g_w2r + o, (u32x4){pack_f16(wv[0], wv[1]), pack_f16(wv[2], wv[3]), pack_f16(wv[4], wv[5]), pack_f16(wv[6], wv[7])});
      constexpr float q = 1.0f / 256;
      if (kappa < 8) st16_wt(g_w2q + o, (u32x4){pack_f16(q * wv[0], q * wv[1]), pack_f16(q * wv[2], q * wv[3]), pack_f16(q * wv[4], q * wv[5]), pack_f16(q * wv[6], q * wv[7])});
      static_for<4>([&](auto ic) { bits |= __builtin_amdgcn_update_dpp(0u, bits, 0x120 + (1 << decltype(ic)::value), 0xf, 0xf, false); });
      bad |= ((bits >> 8) & ~bits & 255u) != 0;
    }
    const bool any = __ballot(bad) != 0;
    if (lane == 0) ((char*)(g_buf + G_FLAG))[4 * (bx - 169) + (threadIdx.x >> 6)] = any;
    K0_END();
    return;
  }
  if (bx >= 145) {
    __shared__ __attribute__((aligned(16))) float red[16][64];
    const int c0 = 64 * ((bx - 145) >> 2), rg = (bx - 145) & 3, c8 = (threadIdx.x & 7) * 8;
    float a[8];
#pragma unroll
    for (int i = 0; i < 8; ++i) a[i] = 0.f;
#pragma unroll
    for (int k = 0; k < 12; ++k) {
      const int ch = 384 * rg + 32 * k + (threadIdx.x >> 3);
      const float b = ld_bf16(grn_b + ch);
      const u32x4 v = *(const u32x4*)(w2 + (size_t)ch * CH + c0 + c8);
#pragma unroll
      for (int i = 0; i < 4; ++i) { a[2 * i] = __builtin_fmaf(b, bf_lo(v[i]), a[2 * i]); a[2 * i + 1] = __builtin_fmaf(b, bf_hi(v[i]), a[2 * i + 1]); }
    }
#pragma unroll
    for (int i = 0; i < 8; ++i) a[i] = dpp_add<0x118, 0xf>(a[i]);
    if (threadIdx.x & 8) {
      *(f32x4*)&red[threadIdx.x >> 4][c8] = (f32x4){a[0], a[1], a[2], a[3]};
      *(f32x4*)&red[threadIdx.x >> 4][c8 + 4] = (f32x4){a[4], a[5], a[6], a[7]};
    }
    __syncthreads();
    if (threadIdx.x < 64) {
      float s = 0.f;
#pragma unroll
      for (int i = 0; i < 16; ++i) s += red[i][threadIdx.x];
      g_cs[(4 + rg) * 384 + cs_index(c0 + threadIdx.x)] = s;
      g_cs[64 * (bx - 145) + threadIdx.x] = 0.f;
    }
    K0_END();
    return;
  }
  const unsigned bid = bx;
  if (bid == 0) {
    if (sw) ssc = scales(w1, (const u16*)x, dw);
    if ((threadIdx.x >> 6) == 2) ss2 = w2_scale(w_ms(w_load(w2)));
#pragma unroll
    for (int i = 0; i < 4; ++i) if (threadIdx.x + 256 * i < 896) *(u32x4*)(g_buf + K1_G_CNT + (threadIdx.x + 256 * i) * 16) = (u32x4){0u, 0u, 0u, 0u};
    *(unsigned*)(g_buf + G_PUB + threadIdx.x * 256) = 0u;
    const int e = threadIdx.x, e2 = e < 192 ? 32 * (e >> 4) + 8 * ((e & 15) >> 2) : 8 * min(e - 192, 47);
    const u32x4 d = *(const u32x4*)(dw + e2);
    __syncthreads();
    const Sc sc = ssc;
    const float s2 = ss2;
    u32x4 v;
#pragma unroll
    for (int k = 0; k < 4; ++k) v[k] = pack_bf16(sc.zs * bf_lo(d[k]), sc.zs * bf_hi(d[k]));
    if (e < 192) {
#pragma unroll
      for (int k = 0; k < 4; ++k) v[k] &= (k & 1) != ((e >> 1) & 1) ? 0u : e & 1 ? 0xffff0000u : 0xffffu;
    }
    if (e >= 240) v = (u32x4){__float_as_uint(EPS * sc.zs * sc.zs), __float_as_uint(sc.vhi), __float_as_uint(sc.g), __float_as_uint(sc.vlo)};
    *(u32x4*)(g_buf + K1_G_TAB + (e < 192 ? e : e + 192) * 16) = v;
    auto sane = [](float v) { return v >= 0x1p-45f && v <= 0x1p20f; };
    if (threadIdx.x < 2) ((unsigned*)(g_buf + G_FLAG))[96 + threadIdx.x] = !(sane(sc.w1) && sane(sc.zs) && sane(sc.g) && sane(sc.h) && sane(s2));
    if (threadIdx.x == 0) {
      const float ik = 1.0f / (sc.h * s2);
      *(f32x4*)g_sc = (f32x4){sc.h * s2, ik, 0x1p-28f / (sc.h * sc.h), -sc.c * U24 * ik};
      g_sc[4] = __uint_as_float(pack_f16(-sc.c * U24, -sc.c * U24));
      *(unsigned*)(g_buf + K1_G_CNT + 16320) = pack_f16(-sc.c * 0x1p-10f, -sc.c * 0x1p-10f);
    }
    if (threadIdx.x < 48) *(u32x4*)(g_buf + threadIdx.x * K1_GST + 5376) = (u32x4){pack_f16(sc.c * U24, 0.f), 0u, 0u, 0u};
    K0_END();
    return;
  }
  __shared__ __attribute__((aligned(16))) u16 tl[32][136];
  const int e = (bid - 1) * 256 + threadIdx.x, ks = (bid - 1) % 12, cb = (bid - 1) / 12;
  u32x4 sm, v[2];
  if (sw) sm = w_load(w1);
#pragma unroll
  for (int i = 0; i < 2; ++i) v[i] = *(const u32x4*)(w1 + (size_t)(32 * ks + (threadIdx.x >> 4) + 16 * i) * HID + 128 * cb + (threadIdx.x & 15) * 8);
  if (sw) ss2 = w1_scale(w_ms(sm));
#pragma unroll
  for (int i = 0; i < 2; ++i) *(u32x4*)&tl[(threadIdx.x >> 4) + 16 * i][(threadIdx.x & 15) * 8] = v[i];
  if (e < (int)(K1_NREP + 1) * 768) *(u32x4*)(g_buf + K1_G_ACC + e * 16) = (u32x4){0u, 0u, 0u, 0u};
  __syncthreads();
  const float s1 = ss2;
#pragma unroll
  for (int i = 0; i < 2; ++i) {
    const int lane = threadIdx.x & 63, f = (threadIdx.x >> 6) + 4 * i, n = 16 * f + (lane & 15), k0 = 8 * (lane >> 4);
    u32x4 P;
#pragma unroll
    for (int jj = 0; jj < 4; ++jj) P[jj] = pack_f16(s1 * ld_bf16(&tl[k0 + 2 * jj][n]), s1 * ld_bf16(&tl[k0 + 2 * jj + 1][n]));
    const int fs = (f & 1) * 12 + ks;
    st16_wt(g_buf + (4 * cb + (f >> 1)) * K1_GST + (fs + (fs >= 5)) * 1024 + lane * 16, P);
  }
  K0_END();
}

constexpr unsigned G_ACC2 = K1_G_ACC + K1_NREP * 12288;

#define VA_CLOB \
  "v0", "v1", "v2", "v3", "v4", "v5", "v6", "v7", "v8", "v9", "v10", "v11", "v12", "v13", "v14", "v15", "v16", "v17", "v18", "v19", "v20", "v21", "v22", "v23", "v24", "v25", "v26", "v27", "v28", "v29", "v30", "v31", \
  "v32", "v33", "v34", "v35", "v36", "v37", "v38", "v39", "v40", "v41", "v42", "v43", "v44", "v45", "v46", "v47", "v48", "v49", "v50", "v51", "v52", "v53", "v54", "v55", "v56", "v57", "v58", "v59", "v60", "v61", "v62", "v63", \
  "v64", "v65", "v66", "v67", "v68", "v69", "v70", "v71", "v72", "v73", "v74", "v75", "v76", "v77", "v78", "v79", "v80", "v81", "v82", "v83", "v84", "v85", "v86", "v87", "v88", "v89", "v90", "v91", "v92", "v93", "v94", "v95", \
  "v96", "v97", "v98", "v99", "v100", "v101", "v102", "v103", "v104", "v105", "v106", "v107", "v108", "v109", "v110", "v111", "v112", "v113", "v114", "v115", "v116", "v117", "v118", "v119", "v120", "v121", "v122", "v123", "v124", "v125", "v126", "v127", \
  "v128", "v129", "v130", "v131", "v132", "v133", "v134", "v135", "v136", "v137", "v138", "v139", "v140", "v141", "v142", "v143", "v144", "v145", "v146", "v147", "v148", "v149", "v150", "v151", "v152", "v153", "v154", "v155", "v156", "v157", "v158", "v159", \
  "v160", "v161", "v162", "v163", "v164", "v165", "v166", "v167", "v168", "v169", "v170", "v171", "v172", "v173", "v174", "v175", "v176", "v177", "v178", "v179", "v180", "v181", "v182", "v183", "v184", "v185", "v186", "v187", "v188", "v189", "v190", "v191", \
  "v192", "v193", "v194", "v195", "v196", "v197", "v198", "v199", "v200", "v201", "v202", "v203", "v204", "v205", "v206", "v207", "v208", "v209", "v210", "v211", "v212", "v213", "v214", "v215", "v216", "v217", "v218", "v219", "v220", "v221", "v222", "v223", \
  "v224", "v225", "v226", "v227", "v228", "v229", "v230", "v231", "v232", "v233", "v234", "v235", "v236", "v237", "v238", "v239", "v240", "v241", "v242", "v243", "v244", "v245", "v246", "v247", "v248", "v249", "v250", "v251", "v252", "v253", "v254", "v255", \
  "a0", "a1", "a2", "a3", "a4", "a5", "a6", "a7", "a8", "a9", "a10", "a11", "a12", "a13", "a14", "a15", "a16", "a17", "a18", "a19", "a20", "a21", "a22", "a23", "a24", "a25", "a26", "a27", "a28", "a29", "a30", "a31", \
  "a32", "a33", "a34", "a35", "a36", "a37", "a38", "a39", "a40", "a41", "a42", "a43", "a44", "a45", "a46", "a47", "a48", "a49", "a50", "a51", "a52", "a53", "a54", "a55", "a56", "a57", "a58", "a59", "a60", "a61", "a62", "a63", \
  "a64", "a65", "a66", "a67", "a68", "a69", "a70", "a71", "a72", "a73", "a74", "a75", "a76", "a77", "a78", "a79", "a80", "a81", "a82", "a83", "a84", "a85", "a86", "a87", "a88", "a89", "a90", "a91", "a92", "a93", "a94", "a95", \
  "a96", "a97", "a98", "a99", "a100", "a101", "a102", "a103", "a104", "a105", "a106", "a107", "a108", "a109", "a110", "a111", "a112", "a113", "a114", "a115", "a116", "a117", "a118", "a119", "a120", "a121", "a122", "a123", "a124", "a125", "a126", "a127", \
  "a128", "a129", "a130", "a131", "a132", "a133", "a134", "a135", "a136", "a137", "a138", "a139", "a140", "a141", "a142", "a143", "a144", "a145", "a146", "a147", "a148", "a149", "a150", "a151", "a152", "a153", "a154", "a155", "a156", "a157", "a158", "a159", \
  "a160", "a161", "a162", "a163", "a164", "a165", "a166", "a167", "a168", "a169", "a170", "a171", "a172", "a173", "a174", "a175", "a176", "a177", "a178", "a179", "a180", "a181", "a182", "a183", "a184", "a185", "a186", "a187", "a188", "a189", "a190", "a191", \
  "a192", "a193", "a194", "a195", "a196", "a197", "a198", "a199", "a200", "a201", "a202", "a203", "a204", "a205", "a206", "a207", "a208", "a209", "a210", "a211", "a212", "a213", "a214", "a215", "a216", "a217", "a218", "a219", "a220", "a221", "a222", "a223", \
  "a224", "a225", "a226", "a227", "a228", "a229", "a230", "a231", "a232", "a233", "a234", "a235", "a236", "a237", "a238", "a239", "a240", "a241", "a242", "a243", "a244", "a245", "a246", "a247", "a248", "a249", "a250", "a251", "a252", "a253", "a254", "a255"
#define K1_CLOB "memory", "vcc", "scc", "s36", "s37", "s38", "s39", "s40", "s41", "s42", "s43", "s44", "s45", "s46", "s47", "s48", "s49", "s50", "s51", "s52", "s53", "s54", "s55", "s56", "s57", "s58", "s59", "s60", "s61", "s62", "s63", "s64", "s65", "s66", "s67", "s68", "s69", "s70", "s71", "s72", "s73", "s74", "s75", "s76", "s77", "s78", "s79", "s80", "s81", "s82", "s83", "s84", "s85", "s86", "s87", "s88", "s89", "s90", "s91", "s92", "s93", "s94", "s95", "s96", "s97", VA_CLOB
DI void k2_params(const u16* x, const char* hws, const u16* out, const u16* grn_g, unsigned tid) {
  const unsigned e = (tid >> 2) * 32 + blockIdx.x * 4 + (tid & 3);
  const unsigned b = e >> 2, w = e & 3, pg = w & 1, nh = w >> 1, q = (b >> 3) * 4 + (b & 3), rs = 128 + (q >> 1), hf = q & 1, side = wg_side(b);
  auto fmask = [&](unsigned region) {
    const unsigned o = ((unsigned)(size_t)hws & 0x1fffffu) + region * H_RS + nh * H_TS;
    const unsigned* p = (const unsigned*)(g_buf + K1_G_LOC + 64) + (o >> 21);
    const unsigned r = 0x200000u + 2 * H_TS - 1 - (o & 0x1fffffu), d1 = ((r >> 15) * 171) >> 9, d2 = (((r + 0x200000u) >> 15) * 171) >> 9;
    const unsigned a1 = ~0u << d1, a2 = d2 < 32 ? ~0u << d2 : 0u;
    return ((__popc(p[0]) > 2 ? ~a1 : 0u) | (__popc(p[1]) > 2 ? a1 & ~a2 : 0u) | (__popc(p[2]) > 2 ? a2 : 0u)) ^ (0u - side);
  };
  const unsigned f0 = fmask(q), f1 = fmask(rs);
  const unsigned fx = ((side ^ x_flip(x, q)) ? ~K2_SHT : 0u) | ((side ^ x_flip(x, rs)) ? K2_SHT : 0u);
  const uint64_t h0 = (uint64_t)hws + q * H_RS + nh * H_TS + pg * 3072, h1 = (uint64_t)hws + rs * H_RS + nh * H_TS + hf * 3072 + pg * 1536;
  const unsigned p0 = (12 * q + 2 * pg) * 49152 + nh * 384, p1 = (12 * rs + 2 * hf + pg) * 49152 + nh * 384;
  const unsigned e1 = 1024 + (e >> 3);
  auto kap = [](unsigned f) { return f / 48 * 2 + (f & 1); };
  auto sco = [&](unsigned f) { return kap(f) * 64 + (kap(f) < 16 ? K2_S16_D0 : 0); };
  auto cso = [&](unsigned f) { const unsigned r = f % 48; return ((f < 192 ? min(f / 48, 2u) : 3) * 384 + (r / 12 % 2 * 12 + r / 24 * 6 + r % 12 / 2) * 16) * 4; };
  typedef uint64_t u64x2 __attribute__((ext_vector_type(2)));
  u64x2* P = (u64x2*)(g_prm + 64 * e);
  P[0] = (u64x2){f0 | (uint64_t)f1 << 32, fx | (uint64_t)(sco(e) | (sco(e1) + (e & 6) * 8) << 16) << 32};
  const u32x4 sc = *(const u32x4*)g_sc;
  P[1] = (u64x2){(((b >> 3) * 4 + w) & 7) * 1024 | (uint64_t)sc[0] << 32, sc[1] | (uint64_t)sc[2] << 32};
  P[2] = (u64x2){h0, h0 + 8192};
  P[3] = (u64x2){h1, h1 + 8192};
  P[4] = (u64x2){__float_as_uint(g_sc[4]) | (uint64_t)((e1 - e) * 1024 + (e & 7) * 128) << 32, 0};
  P[5] = (u64x2){(uint64_t)x + p0, (uint64_t)x + p1};
  P[6] = (u64x2){(uint64_t)out + p0, (uint64_t)out + p1};
  P[7] = (u64x2){0x7fffffffffffffull, (uint64_t)g_buf + G_FLAG};
  P[8] = (u64x2){(uint64_t)g_w2f + w * 6144, (uint64_t)g_w2q + w * 6144};
  P[9] = (u64x2){(uint64_t)g_w2r + e * 1024, (uint64_t)g_w2f + e * 1024};
  P[10] = (u64x2){(uint64_t)grn_g + w * 256, (uint64_t)g_cs + w * 384 + 2048};
  P[11] = (u64x2){(uint64_t)g_buf + G_PUB + b * 256, (uint64_t)g_buf + G_PUB + w * 16384};
  P[12] = (u64x2){cso(e) | (uint64_t)(cso(e1) + (e & 1) * 32) << 32, (uint64_t)g_cs};
  P[13] = (u64x2){sc[3] | (uint64_t)1 << (32 + (b >> 3) % K2_NAT), 0};
}
__global__ __launch_bounds__(256, 1) void k1_fc1(const u16* __restrict__ x, char* __restrict__ hws, const u16* __restrict__ out, const u16* __restrict__ grn_g) {
  extern __shared__ __attribute__((aligned(16))) char smem[];
  const unsigned w = __builtin_amdgcn_readfirstlane(threadIdx.x >> 6);
  if (blockIdx.x < 8) {
    if (w < 2) k2_params(x, hws, out, grn_g, w * 64 + __builtin_amdgcn_mbcnt_hi(~0u, __builtin_amdgcn_mbcnt_lo(~0u, 0u)));
    return;
  }
  const unsigned lds0 = lds_addr(smem);
  const unsigned b = blockIdx.x - 8;
  const unsigned dir = (b >> 3) & 1;
  TS(0); MK_START(1);
  const uint64_t xp = (uint64_t)x, hp = (uint64_t)hws, gp = (uint64_t)g_buf, wp = gp + w * K1_WCH;
  const unsigned d = dir ? ~0u : 1u, wrap = dir ? ~0u : 48u, rst = dir ? 47u : 0u, pos0 = dir ? K1_SMAX - 1 : 0u;
  const unsigned xo0 = (12 * ((b >> 3) * 4 + (b & 3)) + w) * 49152, xs1 = (1536 + w + (dir ? 8 : 0)) * 49152, sb1 = 1536 * w + (dir ? K1_SB1 : 0);
  const unsigned r0o = ((b >> 3) * 4 + (b & 3)) * H_RS, tsel = 4 * (b & 3), s2 = (b >> 2) & 1;
  const uint64_t ap = gp + K1_G_ACC + (b % K1_NREP) * 12288;
  const unsigned ca0 = w == 0 ? dir * 64 : K1_G_DUM - K1_G_CNT + (4 * b + w) * 64;
  const unsigned pm = lds0 + K1_PF_OFF, pf = ((b >> 3) * 4 + w) * 1024;
  unsigned nb;
  asm volatile(
R"(.macro M t,f,w
.if \f < 30
v_mfma_f32_16x16x32_f16 v[56+4*\t:56+4*\t+3], v[128+4*\f:128+4*\f+3], a[216+4*\w:216+4*\w+3], v[56+4*\t:56+4*\t+3]
.else
v_mfma_f32_16x16x32_f16 v[56+4*\t:56+4*\t+3], a[4*\f-4*30:4*\f-4*30+3], a[216+4*\w:216+4*\w+3], v[56+4*\t:56+4*\t+3]
.endif
.endm
.macro Z t,f,w
.if \f < 30
v_mfma_f32_16x16x32_f16 v[56+4*\t:56+4*\t+3], v[128+4*\f:128+4*\f+3], a[216+4*\w:216+4*\w+3], v[252:255]
.else
v_mfma_f32_16x16x32_f16 v[56+4*\t:56+4*\t+3], a[4*\f-4*30:4*\f-4*30+3], a[216+4*\w:216+4*\w+3], v[252:255]
.endif
.endm
.macro LZ a,f
.if \f < 30
v_mfma_f32_16x16x32_bf16 v[36:36+3], v[\a:\a+3], v[128+4*\f:128+4*\f+3], 0
.else
v_mfma_f32_16x16x32_bf16 v[36:36+3], v[\a:\a+3], a[4*\f-4*30:4*\f-4*30+3], 0
.endif
.endm
.macro LS a,f
.if \f < 30
v_mfma_f32_16x16x32_bf16 v[36:36+3], v[\a:\a+3], v[128+4*\f:128+4*\f+3], v[36:36+3]
.else
v_mfma_f32_16x16x32_bf16 v[36:36+3], v[\a:\a+3], a[4*\f-4*30:4*\f-4*30+3], v[36:36+3]
.endif
.endm
.macro LD d,a,f
.if \f < 30
v_mfma_f32_16x16x32_bf16 v[\d:\d+3], a[\a:\a+3], v[128+4*\f:128+4*\f+3], v[36:36+3]
.else
v_mfma_f32_16x16x32_bf16 v[\d:\d+3], a[\a:\a+3], a[4*\f-4*30:4*\f-4*30+3], v[36:36+3]
.endif
.endm
.macro GZ f
.if \f < 30
v_mfma_f32_16x16x32_f16 v[52:55], v[128+4*\f:128+4*\f+3], v[128+4*\f:128+4*\f+3], 0
.else
v_mfma_f32_16x16x32_f16 v[52:55], a[4*\f-4*30:4*\f-4*30+3], a[4*\f-4*30:4*\f-4*30+3], 0
.endif
.endm
.macro GS f
.if \f < 30
v_mfma_f32_16x16x32_f16 v[52:55], v[128+4*\f:128+4*\f+3], v[128+4*\f:128+4*\f+3], v[52:55]
.else
v_mfma_f32_16x16x32_f16 v[52:55], a[4*\f-4*30:4*\f-4*30+3], a[4*\f-4*30:4*\f-4*30+3], v[52:55]
.endif
.endm
.macro CL p,z,s
v_fma_mixlo_f16 v[\p], v[\z], v[\s], 0 op_sel_hi:[0,1,0]
.endm
.macro CH p,z,s
v_fma_mixhi_f16 v[\p], v[\z], v[\s], 0 op_sel:[0,1,0] op_sel_hi:[0,1,0]
.endm
.macro XL f,o
.if \f < 30
global_load_dwordx4 v[128+4*\f:128+4*\f+3], v3, s[46:47] offset:\o
.else
global_load_dwordx4 a[4*\f-4*30:4*\f-4*30+3], v3, s[46:47] offset:\o
.endif
.endm
.macro XN f,o
.if \f < 30
global_load_dwordx4 v[128+4*\f:128+4*\f+3], v3, s[46:47] offset:\o nt
.else
global_load_dwordx4 a[4*\f-4*30:4*\f-4*30+3], v3, s[46:47] offset:\o nt
.endif
.endm
.macro F t,z
v_fma_f32 v[\t], -v[\z], v[\z], s36
.endm
.macro U t,z
v_mul_f32 v[\t], v[\z], v[\t]
.endm
.macro E t
v_exp_f32 v[\t], v[\t]
.endm
.macro A t
v_add_f32 v[\t], 1.0, v[\t]
.endm
.macro R t
v_rcp_f32 v[\t], v[\t]
.endm
.macro C d,a,b
v_cvt_pk_bf16_f32 v[\d], v[\a], v[\b]
.endm
.macro S0 q,t
v_mul_f32 v[\q], v[\t], v[\t]
.endm
.macro S q,t
v_fmac_f32 v[\q], v[\t], v[\t]
.endm
.macro HC d,z
v_cvt_pk_f16_f32 v[\d], v[\z], v[\z+1]
.endm
.macro HF t,p
v_pk_fma_f16 v[\t], v[\p], v[\p], s36 neg_lo:[1,0,0] neg_hi:[1,0,0]
.endm
.macro HM t,p
v_pk_mul_f16 v[\t], v[\p], v[\t]
.endm
.macro HE d,s
v_exp_f16 v[\d], v[\s]
.endm
.macro HG d,s
v_exp_f16_sdwa v[\d], v[\s] dst_sel:WORD_1 dst_unused:UNUSED_PRESERVE src0_sel:WORD_1
.endm
.macro HA t
v_pk_add_f16 v[\t], v[\t], 1.0 op_sel_hi:[1,0]
.endm
.macro HR d,s
v_rcp_f16 v[\d], v[\s]
.endm
.macro HQ d,s
v_rcp_f16_sdwa v[\d], v[\s] dst_sel:WORD_1 dst_unused:UNUSED_PRESERVE src0_sel:WORD_1
.endm
.macro HP d,s
v_rcp_f16_sdwa v[\d], v[\s] dst_sel:DWORD dst_unused:UNUSED_PAD src0_sel:WORD_1
.endm
.macro HY t,z
v_fma_mix_f32 v[\t], v[\z], v[\t], 0 op_sel_hi:[0,1,0]
.endm
.macro HK p
v_and_b32 v[\p], s56, v[\p]
.endm
.macro LQ p
v_lshrrev_b32 v[\p], 2, v[\p]
.endm
.macro LL t,z
v_lshlrev_b32_sdwa v[\t], 1, v[\z] dst_sel:DWORD dst_unused:UNUSED_PAD src0_sel:DWORD src1_sel:WORD_1
.endm
.macro LH t,p
v_and_b32_sdwa v[\t], s56, v[\p] dst_sel:DWORD dst_unused:UNUSED_PAD src0_sel:DWORD src1_sel:WORD_1
.endm
.macro LA t
ds_read_u16 v[\t], v[\t] offset:5376
.endm
.macro LC_ t
ds_read_u16_d16_hi v[\t], v[\t] offset:5376
.endm
.macro LO p,a,b
v_bfi_b32 v[\p], s36, v[\a], v[\b]
.endm
.macro HZ q,p
v_pk_mul_f16 v[\q], v[\p], v[\p]
.endm
.macro HS q,p
v_pk_fma_f16 v[\q], v[\p], v[\p], v[\q]
.endm
.macro HT q,p
v_fma_mix_f32 v[\q], v[\p], v[\p], v[\q] op_sel:[1,1,0] op_sel_hi:[1,1,0]
.endm
.macro HX q
v_fma_mix_f32 v[\q], v[\q], 1.0, v[\q] op_sel:[0,0,1] op_sel_hi:[1,0,1]
.endm
.macro UX t,z
v_mul_f32_e64 v[\t], v[\z], v[\t]
.endm
.macro EX t
v_exp_f32_e64 v[\t], v[\t]
.endm
.macro AX t
v_add_f32_e64 v[\t], 1.0, v[\t]
.endm
.macro RX t
v_rcp_f32_e64 v[\t], v[\t]
.endm
.macro TX q,t
v_mul_f32_e64 v[\q], v[\t], v[\t]
.endm
.macro SX q,t
v_fmac_f32_e64 v[\q], v[\t], v[\t]
.endm
.macro AW a,s
v_accvgpr_write_b32 a[\a], v[\s]
.endm
.macro RA d,v,o
ds_read_b128 a[\d:\d+3], v[\v] offset:\o
.endm
.macro RV d,v,o
ds_read_b128 v[\d:\d+3], v[\v] offset:\o
.endm
.macro W2 a,b,o
ds_write2st64_b32 v4, v[\a], v[\b] offset0:\o offset1:\o+4
.endm
.macro W1 a,o
ds_write_b32 v4, v[\a] offset:\o
.endm
.macro DW o
global_load_lds_dwordx4 v0, s[40:41] offset:\o
.endm
.macro DF
global_load_lds_dwordx4 v0, s[70:71]
.endm
.macro DX
global_load_lds_dword v3, s[46:47]
.endm
.macro ST d
.if ((\d - 104) / 4) & 1
global_store_dwordx4 v2, v[\d:\d+3], s[42:43] nt sc1
.else
global_store_dwordx2 v31, v[\d:\d+1], s[42:43] nt sc1
.endif
.endm
.macro SU d
.if ((\d - 104) / 4) & 1
global_store_dwordx4 v2, v[\d:\d+3], s[42:43]  sc1
.else
global_store_dwordx2 v31, v[\d:\d+1], s[42:43]  sc1
.endif
.endm
.macro SP
global_atomic_add_f64 v6, v[14:15], s[48:49]
.endm
.macro AT
global_atomic_add v11, v9, v10, s[68:69] sc0
.endm
.macro PFA
s_getpc_b64 s[70:71]
s_add_u32 s70, s70, 7f-.
s_addc_u32 s71, s71, 0
s_andn2_b32 s70, s70, 0x7f
s_min_u32 s53, %[pf], 8f-7f-1152
s_andn2_b32 s53, s53, 0x7f
s_add_u32 s70, s70, s53
s_addc_u32 s71, s71, 0
.endm
.macro PFN
s_getpc_b64 s[70:71]
s_add_u32 s70, s70, k2_code@rel32@lo-1020
s_addc_u32 s71, s71, k2_code@rel32@hi-1012
s_add_u32 s70, s70, %[pf]
s_addc_u32 s71, s71, 0
s_andn2_b32 s70, s70, 0x7f
.endm
.macro PFW
s_getpc_b64 s[70:71]
s_add_u32 s70, s70, g_w2q@rel32@lo+4
s_addc_u32 s71, s71, g_w2q@rel32@hi+12
s_add_u32 s70, s70, %[pf]
s_addc_u32 s71, s71, 0
.endm
.macro PF0
s_getpc_b64 s[70:71]
s_andn2_b32 s70, s70, 0x7f
s_min_u32 s53, %[pf], 7f-.-1152
s_andn2_b32 s53, s53, 0x7f
s_add_u32 s70, s70, s53
s_addc_u32 s71, s71, 0
.endm
.macro WL n
s_waitcnt lgkmcnt(\n)
.endm
.macro WV n
s_waitcnt vmcnt(\n)
.endm
.macro LU t,p
v_pk_fma_f16 v[\t], v[\p], v34, s67
.endm
.macro CF d,a,b
v_cvt_pk_f16_f32 v[\d], v[\a], v[\b]
.endm
.macro XS d
v_pk_fma_f16 v[\d], v[\d], v13, s56
.endm
.macro AR d,a
v_accvgpr_read_b32 v[\d], a[\a]
.endm
.macro DQ o
global_load_lds_dword v23, s[40:41] offset:\o
.endm
.macro GE
v_cmp_eq_u32 vcc, 0, v35
s_nop 4
s_cbranch_vccz 9f
v_mov_b32 v35, 14720
s_mov_b64 exec, 1
global_store_dword v35, v10, s[68:69] sc1
s_mov_b64 exec, -1
9:
.endm
.macro BW f
.if \f < 30
ds_write_b128 v12, v[128+4*\f:128+4*\f+3]
.else
ds_write_b128 v12, a[4*\f-120:4*\f-120+3]
.endif
.endm
.macro BR f
.if \f < 30
ds_read_b128 v[128+4*\f:128+4*\f+3], v20
.else
ds_read_b128 a[4*\f-120:4*\f-120+3], v20
.endif
.endm
.macro LK d,a,f,h
.if \f < 30
v_mfma_f32_4x4x4_16b_bf16 v[\d:\d+3], a[\a:\a+1], v[128+4*\f+\h:128+4*\f+\h+1], v[36:39]
.else
v_mfma_f32_4x4x4_16b_bf16 v[\d:\d+3], a[\a:\a+1], a[4*\f-120+\h:4*\f-120+\h+1], v[36:39]
.endif
.endm
.macro PK d,a,b,s
v_perm_b32 v[\d], v[\a], v[\b], v[\s]
.endm
.macro PO d
v_lshl_or_b32 v[\d], v[\d], 4, v[\d-4]
.endm
k1_code:
v_mbcnt_lo_u32_b32 v16, -1, 0
v_mbcnt_hi_u32_b32 v16, -1, v16
v_lshlrev_b32 v0, 4, v16
v_and_b32 v17, 15, v16
v_lshrrev_b32 v18, 4, v16
s_add_u32 s68, %[glo], 1239040
s_addc_u32 s69, %[ghi], 0
s_mov_b32 s53, %[r0o]
s_load_dword s67, s[68:69], 16320
s_add_u32 s82, %[hlo], s53
s_addc_u32 s83, %[hhi], 0
s_add_u32 s84, s82, 8192
s_addc_u32 s85, s83, 0
s_and_b32 s90, %[hlo], 0x1fffff
s_add_u32 s90, s90, s53
s_mov_b32 s91, 3072
v_mov_b32 v11, s90
v_mad_u32_u24 v11, v0, s91, v11
v_lshrrev_b32 v11, 21, v11
v_lshl_add_u32 v11, v11, 2, 64
v_writelane_b32 v11, %[tsel], 63
s_sub_u32 s53, s82, %[hlo]
s_lshr_b32 s53, s53, 2
s_and_b32 s90, %[xlo], 0x1fffff
s_add_u32 s53, s53, s90
s_add_u32 s53, s53, 0x20048000
s_lshr_b32 s53, s53, 21
s_lshl_b32 s90, s53, 2
v_writelane_b32 v11, s90, 48
global_load_dword v11, v11, s[68:69] offset:-2048
v_lshlrev_b32 v19, 1, v18
s_movk_i32 s53, 0x78
v_bfe_u32 v21, v16, 2, 2
v_lshrrev_b32 v19, v19, s53
v_mul_u32_u24 v21, 0x300, v21
v_xor_b32 v19, v19, v16
v_and_b32 v19, 3, v19
v_lshl_add_u32 v21, v19, 4, v21
s_mov_b32 s97, 3072
s_mov_b32 s50, %[pos0]
s_mov_b32 s51, %[pos0]
s_mov_b32 s52, %[pos0]
s_add_u32 s74, %[lds], 0
s_add_u32 s75, s74, 163840
s_mov_b32 s37, s74
s_mul_i32 s53, %[w], 6400
s_add_u32 s76, s74, s53
s_add_u32 s77, s76, 163840
s_mov_b32 s38, s76
s_mul_i32 s60, %[w], 1536
s_add_u32 s61, s60, 512
s_add_u32 s62, s60, 16384
s_add_u32 s63, s60, 16896
s_add_u32 s64, s60, 32768
s_add_u32 s65, s60, 33280
v_lshlrev_b32 v23, 2, v16
s_lshl_b32 s53, %[w], 11
s_min_u32 s53, s53, 5120
s_add_u32 s72, s53, 1228800
s_add_u32 s40, %[glo], s72
s_addc_u32 s41, %[ghi], 0
s_add_u32 s53, s53, %[lds]
s_add_u32 m0, s53, 58368
s_nop 0
DW 0
DW 1024
s_nop 0 ;PS
s_mul_i32 s53, s50, 25600
s_add_u32 s40, %[wlo], s53
s_addc_u32 s41, %[whi], 0
s_mov_b32 m0, s38
s_nop 0
DW 0
DW 1024
DW 2048
s_add_u32 s40, s40, 3072
s_addc_u32 s41, s41, 0
s_add_u32 m0, s38, 3072
s_nop 0
DW 0
DW 1024
DW 2048
DQ 3072
s_add_u32 s50, s50, %[d]
s_cmp_eq_u32 s50, %[wrap]
s_cselect_b32 s50, %[rst], s50
s_add_u32 s38, s38, 32768
s_cmp_eq_u32 s38, s77
s_cselect_b32 s38, s76, s38
s_mul_i32 s53, s50, 25600
)"
R"(s_add_u32 s40, %[wlo], s53
s_addc_u32 s41, %[whi], 0
s_mov_b32 m0, s38
s_nop 0
DW 0
DW 1024
DW 2048
s_add_u32 s40, s40, 3072
s_addc_u32 s41, s41, 0
s_add_u32 m0, s38, 3072
s_nop 0
DW 0
DW 1024
DW 2048
DQ 3072
s_add_u32 s50, s50, %[d]
s_cmp_eq_u32 s50, %[wrap]
s_cselect_b32 s50, %[rst], s50
s_add_u32 s38, s38, 32768
s_cmp_eq_u32 s38, s77
s_cselect_b32 s38, s76, s38
s_and_b32 s53, %[w], 1
s_mul_i32 s53, s53, 3072
s_lshr_b32 s72, %[w], 1
s_lshl_b32 s40, s72, 16
s_add_u32 s40, s40, s53
s_add_u32 m0, s40, 25600
s_lshl_b32 s72, s72, 13
s_add_u32 s53, s53, s72
s_add_u32 s53, s53, 1382400
s_add_u32 s40, %[glo], s53
s_addc_u32 s41, %[ghi], 0
DW 0
DW 1024
DW 2048
DW 3072
v_add_u32 v1, s37, v0
v_lshl_add_u32 v19, v18, 2, v17
v_and_b32 v19, 15, v19
v_lshlrev_b32 v19, 4, v19
v_lshl_add_u32 v2, v18, 8, v19
s_lshl_b32 s53, %[w], 2
s_add_u32 s53, s53, %[lds]
s_add_u32 s53, s53, 123904
s_add_u32 s53, %[lds], 123904
v_bfe_u32 v4, v17, 1, 2
v_and_b32 v5, 8, v17
v_add3_u32 v4, v4, v5, %[w]
v_lshl_add_u32 v4, v18, 2, v4
v_and_b32 v4, 15, v4
v_lshl_add_u32 v4, v17, 4, v4
v_lshl_add_u32 v4, v4, 2, s53
s_lshl_b32 s53, %[w], 9
s_add_u32 s53, s53, %[lds]
s_add_u32 s53, s53, 123904
v_lshlrev_b32 v5, 3, v16
v_add_u32 v5, s53, v5
s_lshl_b32 s53, %[w], 6
v_and_b32 v6, 0x38, v16
v_add_u32 v6, s53, v6
s_add_u32 s53, %[lds], 58368
v_and_b32 v19, 3, v16
v_lshl_add_u32 v19, v18, 2, v19
v_lshl_add_u32 v7, v19, 4, s53
v_lshlrev_b32 v8, 4, v18
v_add_u32 v8, s53, v8
v_mov_b32 v10, 1
s_mov_b32 s36, 0xffff
s_mov_b32 s56, 0x5550555
s_mov_b32 s39, 32768
s_mov_b32 s54, 0x01010101
s_mov_b32 s55, 0x01010101
s_mov_b32 %[nb], 0
v_and_b32 v28, 12, v17
v_lshl_add_u32 v28, v28, 2, v17
v_lshlrev_b32 v28, 2, v28
v_mov_b32 v34, 0x74007400
v_mov_b32 v35, -1
v_and_b32 v22, 0xfffffcff, v1
v_lshrrev_b32 v31, 1, v2
v_mov_b32 v32, 0x06020400
v_mov_b32 v33, 0x07030501
s_waitcnt lgkmcnt(0)
s_mov_b32 s78, 0xaaaaaaaa
s_mov_b32 s79, 0xaaaaaaaa
s_mov_b32 s80, 0xcccccccc
s_mov_b32 s81, 0xcccccccc
s_and_b32 s53, %[w], 1
s_lshl_b32 s53, s53, 10
s_lshr_b32 s72, %[w], 1
s_mul_i32 s72, s72, 32768
s_add_u32 s72, s72, s53
s_add_u32 s72, s72, 128000
s_movk_i32 s53, 0x78
v_lshrrev_b32 v12, 5, v0
v_and_b32 v12, 6, v12
v_bfe_u32 v12, s53, v12, 2
v_lshrrev_b32 v20, 8, v0
v_xor_b32 v20, v12, v20
v_lshl_add_u32 v20, v20, 4, s72
v_and_b32 v12, 0xf0, v0
v_lshl_add_u32 v20, v12, 2, v20
v_add_u32 v12, s72, v0
WV 20
v_bcnt_u32_b32 v11, v11, 0
v_cmp_lt_u32 s[90:91], 2, v11
s_nop 0
s_lshr_b32 s96, s91, 31
s_xor_b32 s96, s96, %[s2]
s_sub_u32 s53, 0, s96
s_xor_b32 s90, s90, s53
s_xor_b32 s91, s91, s53
s_add_u32 s53, %[xo0], 0
s_add_u32 s46, %[xlo], s53
s_addc_u32 s47, %[xhi], 0
s_bitcmp1_b64 s[90:91], 48
s_cselect_b32 s58, 16, 0
s_lshr_b32 s58, 0xfe987610, s58
v_lshrrev_b32 v3, 6, v0
v_and_b32 v3, 12, v3
v_bfe_u32 v3, s58, v3, 4
v_mad_u32_u24 v3, v3, s97, v21
XL 12,0
XL 13,64
XL 14,128
XL 15,192
XL 16,256
XL 17,320
XL 18,384
XL 19,448
XL 20,512
XL 21,576
XL 22,640
XL 23,704
s_mul_i32 s53, s96, 8320
s_cmp_lt_u32 %[ca0], 128
s_cselect_b32 s53, s53, 0
s_add_u32 s73, s53, %[ca0]
v_mov_b32 v9, s73
WV 30
s_barrier
WV 11
BW 12
RV 40,8,6144
RV 44,8,6208
RV 48,8,6272
RV 248,8,6336
BR 12
WV 10
BW 13
s_nop 1
BR 13
WL 2
LZ 40,12
WV 9
BW 14
RV 40,8,6400
s_nop 0
BR 14
WL 3
LS 44,13
WV 8
BW 15
RV 44,8,6464
s_nop 0
BR 15
s_add_u32 s53, %[xo0], 0
s_add_u32 s46, %[xlo], s53
s_addc_u32 s47, %[xhi], 0
s_bitcmp1_b64 s[90:91], 48
s_cselect_b32 s58, 16, 0
s_lshr_b32 s58, 0xda43cb52, s58
v_lshrrev_b32 v3, 6, v0
v_and_b32 v3, 12, v3
v_bfe_u32 v3, s58, v3, 4
v_mad_u32_u24 v3, v3, s97, v21
WL 3
LS 48,14
WV 7
BW 16
RV 48,8,6528
s_nop 0
BR 16
WL 3
LS 248,15
WV 6
BW 17
RV 248,8,6592
s_nop 0
BR 17
XL 24,0
WL 3
LS 40,16
WV 6
BW 18
RV 40,8,6656
s_nop 0
BR 18
WL 3
LS 44,17
WV 5
BW 19
RV 44,8,6720
s_nop 0
BR 19
XL 25,64
WL 3
LS 48,18
WV 5
BW 20
RV 48,8,6784
s_nop 0
BR 20
WL 3
LS 248,19
WV 4
BW 21
RV 248,8,6848
s_nop 0
BR 21
XL 26,128
WL 3
LS 40,20
WV 4
BW 22
RA 240,7,0
s_nop 0
BR 22
WL 3
LS 44,21
WV 3
BW 23
RA 244,7,256
s_nop 0
BR 23
XL 27,192
WL 3
LS 48,22
RA 248,7,512
WL 1
LS 248,23
RA 252,7,768
s_nop 6
v_mul_f32 v36, 0xbb2aaaab, v36
v_mul_f32 v37, 0xbb2aaaab, v37
v_mul_f32 v38, 0xbb2aaaab, v38
v_mul_f32 v39, 0xbb2aaaab, v39
XL 28,256
s_nop 0
LK 40,240,12,0
LK 44,242,12,2
RA 240,7,1024
LK 48,244,13,0
s_nop 1
CF 176,40,41
LK 248,246,13,2
WL 2
RA 244,7,1280
CF 177,42,43
CF 178,44,45
CF 179,46,47
XL 29,320
LK 40,248,14,0
CF 180,48,49
LK 44,250,14,2
RA 248,7,1536
CF 181,50,51
GZ 12
WL 3
CF 182,248,249
CF 183,250,251
LK 48,252,15,0
CF 184,40,41
LK 248,254,15,2
RA 252,7,1792
CF 185,42,43
XL 30,384
GS 13
WL 3
CF 186,44,45
CF 187,46,47
LK 40,240,16,0
CF 188,48,49
LK 44,242,16,2
RA 240,7,2048
CF 189,50,51
XL 31,448
GS 14
WL 3
CF 190,248,249
CF 191,250,251
LK 48,244,17,0
CF 192,40,41
LK 248,246,17,2
RA 244,7,2304
CF 193,42,43
XL 32,512
GS 15
WL 3
CF 194,44,45
CF 195,46,47
LK 40,248,18,0
CF 196,48,49
LK 44,250,18,2
RA 248,7,2560
CF 197,50,51
XL 33,576
GS 16
WL 3
CF 198,248,249
CF 199,250,251
LK 48,252,19,0
CF 200,40,41
LK 248,254,19,2
RA 252,7,2816
CF 201,42,43
XL 34,640
GS 17
WL 3
CF 202,44,45
CF 203,46,47
LK 40,240,20,0
CF 204,48,49
LK 44,242,20,2
CF 205,50,51
XL 35,704
GS 18
WL 2
CF 206,248,249
CF 207,250,251
LK 48,244,21,0
CF 208,40,41
LK 248,246,21,2
CF 209,42,43
PF0
s_mov_b32 m0, %[pm]
s_nop 0
DF
PFA
GS 19
WL 1
CF 210,44,45
CF 211,46,47
LK 40,248,22,0
CF 212,48,49
LK 44,250,22,2
CF 213,50,51
s_mul_i32 s53, s50, 25600
s_add_u32 s40, %[wlo], s53
s_addc_u32 s41, %[whi], 0
s_mov_b32 m0, s38
GS 20
WL 0
CF 214,248,249
CF 215,250,251
LK 48,252,23,0
CF 216,40,41
LK 248,254,23,2
CF 217,42,43
GS 21
DW 0
CF 218,44,45
CF 219,46,47
RV 36,8,6912
s_nop 0
GS 22
CF 220,48,49
CF 221,50,51
CF 222,248,249
CF 223,250,251
s_nop 1
GS 23
DW 1024
s_nop 6
v_cndmask_b32 v52, v52, v53, s[78:79]
v_cndmask_b32 v54, v54, v55, s[78:79]
v_cndmask_b32 v52, v52, v54, s[80:81]
v_mul_f32 v52, 0x3b2aaaab, v52
WL 0
v_med3_f32 v53, v52, v39, v37
v_cmp_eq_f32 vcc, v52, v53
C 52,52,52
v_lshlrev_b32 v52, 16, v52
v_add_f32 v52, v36, v52
C 52,52,52
v_lshlrev_b32 v52, 16, v52
v_sqrt_f32 v52, v52
s_nop 0
C 52,52,52
v_lshlrev_b32 v52, 16, v52
v_rcp_f32 v52, v52
s_nop 0
v_mul_f32 v52, v38, v52
v_cndmask_b32 v52, 0, v52, vcc
CF 52,52,52
ds_bpermute_b32 v13, v28, v52
WV 14
BW 24
RV 40,8,6144
RV 44,8,6208
)"
R"(RV 48,8,6272
BR 24
WV 13
BW 25
s_nop 1
BR 25
WL 7
XS 176
WL 2
LZ 40,24
XS 177
WV 12
BW 26
XS 178
RV 40,8,6336
XS 179
BR 26
XS 180
WL 3
LS 44,25
DW 2048
XS 181
WV 12
BW 27
XS 182
RV 44,8,6400
XS 183
BR 27
v_min_u32 v35, v35, v13
WL 3
LS 48,26
XS 184
WV 11
BW 28
XS 185
RV 48,8,6464
XS 186
BR 28
XS 187
WL 3
LS 40,27
s_add_u32 s40, s40, 3072
s_addc_u32 s41, s41, 0
s_add_u32 m0, s38, 3072
XS 188
WV 10
BW 29
XS 189
RV 40,8,6528
XS 190
XS 191
BR 29
XS 192
WL 3
LS 44,28
XS 193
WV 9
BW 30
XS 194
RV 44,8,6592
XS 195
BR 30
XS 196
WL 3
LS 48,29
DW 0
XS 197
WV 9
BW 31
XS 198
RV 48,8,6656
XS 199
BR 31
XS 200
WL 3
LS 40,30
XS 201
WV 8
BW 32
XS 202
RV 40,8,6720
XS 203
BR 32
XS 204
WL 3
LS 44,31
DW 1024
XS 205
WV 8
BW 33
XS 206
XS 207
RV 44,8,6784
XS 208
BR 33
XS 209
WL 3
LS 48,32
XS 210
WV 7
BW 34
XS 211
RV 48,8,6848
XS 212
RA 240,7,0
XS 213
BR 34
XS 214
WL 4
LS 40,33
DW 2048
XS 215
WV 7
BW 35
XS 216
RA 244,7,256
XS 217
BR 35
XS 218
WL 3
LS 44,34
XS 219
RA 248,7,512
XS 220
WL 1
LS 48,35
XS 221
RA 252,7,768
XS 222
XS 223
s_nop 3
v_mul_f32 v36, 0xbb2aaaab, v36
v_mul_f32 v37, 0xbb2aaaab, v37
v_mul_f32 v38, 0xbb2aaaab, v38
v_mul_f32 v39, 0xbb2aaaab, v39
s_nop 1
LK 40,240,24,0
LK 44,242,24,2
RA 240,7,1024
DQ 3072
LK 48,244,25,0
s_nop 0
CF 224,40,41
LK 248,246,25,2
WL 2
RA 244,7,1280
CF 225,42,43
CF 226,44,45
CF 227,46,47
LK 40,248,26,0
CF 228,48,49
LK 44,250,26,2
RA 248,7,1536
CF 229,50,51
s_add_u32 s50, s50, %[d]
s_cmp_eq_u32 s50, %[wrap]
s_cselect_b32 s50, %[rst], s50
GZ 24
WL 3
CF 230,248,249
CF 231,250,251
LK 48,252,27,0
CF 232,40,41
LK 248,254,27,2
RA 252,7,1792
CF 233,42,43
s_add_u32 s38, s38, 32768
s_cmp_eq_u32 s38, s77
s_cselect_b32 s38, s76, s38
GS 25
WL 3
CF 234,44,45
CF 235,46,47
LK 40,240,28,0
CF 236,48,49
LK 44,242,28,2
RA 240,7,2048
CF 237,50,51
s_add_u32 s53, %[xo0], 196608
s_add_u32 s46, %[xlo], s53
s_addc_u32 s47, %[xhi], 0
s_bitcmp1_b64 s[90:91], 48
s_cselect_b32 s58, 16, 0
s_lshr_b32 s58, 0xfe987610, s58
v_lshrrev_b32 v3, 6, v0
v_and_b32 v3, 12, v3
v_bfe_u32 v3, s58, v3, 4
v_mad_u32_u24 v3, v3, s97, v21
GS 26
WL 3
CF 238,248,249
CF 239,250,251
LK 48,244,29,0
CF 240,40,41
LK 248,246,29,2
RA 244,7,2304
CF 241,42,43
XL 36,0
GS 27
WL 3
CF 242,44,45
CF 243,46,47
LK 40,248,30,0
CF 244,48,49
LK 44,250,30,2
RA 248,7,2560
CF 245,50,51
XL 37,64
GS 28
WL 3
CF 246,248,249
CF 247,250,251
LK 48,252,31,0
CF 152,40,41
LK 248,254,31,2
RA 252,7,2816
CF 153,42,43
XL 38,128
GS 29
WL 3
CF 154,44,45
CF 155,46,47
LK 40,240,32,0
CF 156,48,49
LK 44,242,32,2
CF 157,50,51
XL 39,192
GS 6
WL 2
CF 158,248,249
CF 159,250,251
LK 48,244,33,0
CF 160,40,41
LK 248,246,33,2
CF 161,42,43
GS 7
WL 1
XL 40,256
CF 162,44,45
CF 163,46,47
LK 40,248,34,0
CF 164,48,49
LK 44,250,34,2
CF 165,50,51
GS 8
WL 0
XL 41,320
CF 166,248,249
CF 167,250,251
LK 48,252,35,0
CF 168,40,41
LK 248,254,35,2
CF 169,42,43
GS 9
XL 42,384
CF 170,44,45
CF 171,46,47
RV 36,8,6912
s_nop 0
GS 10
CF 172,48,49
CF 173,50,51
CF 174,248,249
CF 175,250,251
s_nop 1
GS 11
XL 43,448
s_nop 6
v_cndmask_b32 v52, v52, v53, s[78:79]
v_cndmask_b32 v54, v54, v55, s[78:79]
v_cndmask_b32 v52, v52, v54, s[80:81]
v_mul_f32 v52, 0x3b2aaaab, v52
WL 0
v_med3_f32 v53, v52, v39, v37
s_barrier
RA 216,1,0
RA 220,1,1024
RA 224,1,2048
RA 228,1,3072
RV 252,22,5120
v_cmp_eq_f32 vcc, v52, v53
C 52,52,52
v_lshlrev_b32 v52, 16, v52
v_add_f32 v52, v36, v52
C 52,52,52
XL 44,512
v_lshlrev_b32 v52, 16, v52
v_sqrt_f32 v52, v52
s_nop 0
C 52,52,52
v_lshlrev_b32 v52, 16, v52
v_rcp_f32 v52, v52
s_nop 0
v_mul_f32 v52, v38, v52
v_cndmask_b32 v52, 0, v52, vcc
CF 52,52,52
ds_bpermute_b32 v13, v28, v52
WL 0
XS 224
XL 45,576
XS 225
XS 226
XS 227
XS 228
XS 229
XS 230
XS 231
v_min_u32 v35, v35, v13
XS 232
XS 233
XL 46,640
XS 234
XS 235
XS 236
XS 237
XS 238
XS 239
XL 47,704
Z 0,12,0
XS 240
XS 241
XS 242
XS 243
XS 244
XS 245
XS 246
XS 247
XS 152
XS 153
XS 154
XS 155
M 0,13,1
s_mul_i32 s53, s50, 25600
RA 232,1,4096
AW 0,152
AW 1,153
Z 1,24,0
s_add_u32 s40, %[wlo], s53
AW 2,154
AW 3,155
M 1,25,1
s_addc_u32 s41, %[whi], 0
RA 236,1,6144
XS 156
XS 157
M 0,14,2
s_mov_b32 m0, s38
s_nop 0
DW 0
XS 158
XS 159
AW 4,156
M 0,15,3
RA 216,1,7168
AW 5,157
AW 6,158
M 1,26,2
DW 1024
AW 7,159
XS 160
M 1,27,3
WL 2
RA 220,1,8192
XS 161
XS 162
M 0,16,4
WL 2
XS 163
AW 8,160
M 0,17,5
RA 224,1,9216
AW 9,161
AW 10,162
AW 11,163
M 1,28,4
WL 1
DW 2048
XS 164
XS 165
M 1,29,5
s_add_u32 s40, s40, 3072
RA 228,1,10240
XS 166
XS 167
M 0,18,0
s_addc_u32 s41, s41, 0
WV 14
BW 36
AW 12,164
AW 13,165
M 0,19,1
s_add_u32 m0, s38, 3072
RA 232,1,11264
RV 40,8,6144
AW 14,166
AW 15,167
M 1,30,0
RV 252,22,5392
RV 44,8,6208
XS 168
)"
R"(XS 169
M 1,31,1
WL 6
RA 236,1,12288
RV 48,8,6272
XS 170
XS 171
AW 16,168
M 0,20,2
WL 7
DW 0
BR 36
AW 17,169
AW 18,170
M 0,21,3
RA 216,1,13312
AW 19,171
XS 172
M 1,32,2
DW 1024
XS 173
XS 174
M 1,33,3
WL 7
RA 220,1,14336
XS 175
WV 15
BW 37
AW 20,172
s_nop 0
BR 37
M 0,22,4
WL 6
DW 2048
AW 21,173
AW 22,174
AW 23,175
M 0,23,5
WL 4
RA 224,1,15360
LZ 40,36
WV 15
BW 38
RV 40,8,6336
s_nop 0
BR 38
M 1,34,4
DQ 3072
M 1,35,5
WL 4
RA 228,1,16384
LS 44,37
WV 15
BW 39
RV 44,8,6400
Z 6,12,0
s_add_u32 s50, s50, %[d]
LL 16,56
LL 104,57
BR 39
M 6,13,1
s_cmp_eq_u32 s50, %[wrap]
LA 16
RA 232,1,17408
WL 6
LS 48,38
s_cselect_b32 s50, %[rst], s50
WV 14
BW 40
RV 48,8,6464
Z 7,24,0
s_add_u32 s38, s38, 32768
LC_ 104
BR 40
M 7,25,1
s_cmp_eq_u32 s38, s77
RA 236,1,18432
WL 7
LS 40,39
s_cselect_b32 s38, s76, s38
WV 13
BW 41
RV 40,8,6528
M 6,14,2
s_add_u32 s37, s37, 32768
BR 41
M 6,15,3
s_cmp_eq_u32 s37, s75
RA 216,1,19456
WL 5
LS 44,40
s_cselect_b32 s37, s74, s37
WV 12
BW 42
M 7,26,2
s_add_u32 s53, %[xo0], 196608
LL 17,58
LL 105,59
RV 44,8,6592
BR 42
M 7,27,3
s_add_u32 s46, %[xlo], s53
LA 17
RA 220,1,20480
s_addc_u32 s47, %[xhi], 0
s_bitcmp1_b64 s[90:91], 48
s_cselect_b32 s58, 16, 0
s_lshr_b32 s58, 0xda43cb52, s58
v_lshrrev_b32 v3, 6, v0
v_and_b32 v3, 12, v3
v_bfe_u32 v3, s58, v3, 4
v_mad_u32_u24 v3, v3, s97, v21
WL 6
LS 48,41
WV 10
BW 43
M 6,16,4
WL 3
LC_ 105
XL 48,0
RV 48,8,6656
BR 43
M 6,17,5
s_mul_i32 s53, s51, 49152
RA 224,1,21504
LS 40,42
s_bitcmp1_b64 s[90:91], s51
BW 44
M 7,28,4
WV 10
LO 104,16,104
LU 16,104
RV 40,8,6720
M 7,29,5
s_cselect_b64 s[44:45], s[84:85], s[82:83]
RA 228,1,22528
HZ 24,16
BR 44
WL 5
LS 44,43
s_add_u32 s44, s44, s53
BW 45
M 6,18,0
s_addc_u32 s45, s45, 0
LL 18,60
LL 108,61
s_add_u32 s51, s51, %[d]
s_cmp_eq_u32 s51, %[wrap]
s_cselect_b32 s51, %[rst], s51
XL 49,64
RV 44,8,6784
M 6,19,1
WL 2
LA 18
RA 232,1,23552
BR 45
LS 48,44
WV 10
BW 46
M 7,30,0
LC_ 108
XL 50,128
M 7,31,1
RA 236,1,24576
RV 48,8,6848
RA 240,7,0
M 6,20,2
LO 105,17,105
LU 17,105
BR 46
M 6,21,3
WL 6
XL 51,192
HS 24,17
LS 40,45
WV 11
BW 47
RA 244,7,256
M 7,32,2
LL 19,62
LL 109,63
v_add_u32 v1, s37, v0
BR 47
M 7,33,3
WL 3
LA 19
RA 216,1,0
XL 52,256
LS 44,46
RA 248,7,512
v_and_b32 v22, 0xfffffcff, v1
M 6,22,4
LC_ 109
RA 220,1,1024
M 6,23,5
WL 5
RV 252,22,5120
LS 48,47
RA 252,7,768
s_nop 6
v_mul_f32 v36, 0xbb2aaaab, v36
v_mul_f32 v37, 0xbb2aaaab, v37
v_mul_f32 v38, 0xbb2aaaab, v38
M 7,34,4
LO 108,18,108
LU 18,108
XL 53,320
v_mul_f32 v39, 0xbb2aaaab, v39
M 7,35,5
s_barrier
RA 224,1,2048
HS 24,18
WL 2
Z 0,12,0
LL 16,80
LL 106,81
M 0,13,1
s_mul_i32 s53, s50, 25600
LA 16
RA 228,1,3072
LK 40,240,36,0
LK 44,242,36,2
s_add_u32 s40, %[wlo], s53
RA 240,7,1024
LK 48,244,37,0
s_nop 0
CF 128,40,41
LK 248,246,37,2
s_addc_u32 s41, %[whi], 0
RA 244,7,1280
CF 129,42,43
CF 130,44,45
CF 131,46,47
LK 40,248,38,0
CF 132,48,49
LK 44,250,38,2
s_mov_b32 m0, s38
RA 248,7,1536
CF 133,50,51
GZ 0
WL 6
CF 134,248,249
CF 135,250,251
LK 48,252,39,0
CF 136,40,41
LK 248,254,39,2
RA 252,7,1792
CF 137,42,43
GS 1
CF 138,44,45
CF 139,46,47
Z 1,24,0
LC_ 106
RA 232,1,4096
M 1,25,1
WL 8
RA 236,1,6144
M 0,14,2
WL 7
LO 109,19,109
LU 19,109
M 0,15,3
WL 6
RA 216,1,7168
HS 24,19
LK 40,240,40,0
CF 140,48,49
LK 44,242,40,2
RA 240,7,2048
CF 141,50,51
GS 2
WL 7
CF 142,248,249
CF 143,250,251
LK 48,244,41,0
CF 144,40,41
LK 248,246,41,2
RA 244,7,2304
CF 145,42,43
GS 3
WL 7
CF 146,44,45
CF 147,46,47
LK 40,248,42,0
CF 148,48,49
LK 44,250,42,2
RA 248,7,2560
CF 149,50,51
GS 4
CF 150,248,249
CF 151,250,251
M 1,26,2
LL 17,82
LL 107,83
DW 0
M 1,27,3
WL 7
LA 17
RA 220,1,8192
LK 48,252,43,0
CF 152,40,41
LK 248,254,43,2
RA 252,7,2816
CF 153,42,43
GS 5
WL 8
CF 154,44,45
CF 155,46,47
M 0,16,4
WL 7
LC_ 107
DW 1024
M 0,17,5
WL 6
RA 224,1,9216
HX 24
LK 40,240,44,0
CF 156,48,49
LK 44,242,44,2
CF 157,50,51
GS 6
CF 158,248,249
CF 159,250,251
M 1,28,4
LO 16,16,106
LU 106,16
W1 24,0
M 1,29,5
WL 7
RA 228,1,10240
HZ 25,106
DW 2048
LK 48,244,45,0
CF 160,40,41
LK 248,246,45,2
CF 161,42,43
GS 7
WL 5
CF 162,44,45
CF 163,46,47
M 0,18,0
s_add_u32 s40, s40, 3072
PK 106,16,104,33
PK 104,16,104,32
LL 18,84
LL 110,85
M 0,19,1
s_addc_u32 s41, s41, 0
LA 18
RA 232,1,11264
LK 40,248,46,0
CF 164,48,49
LK 44,250,46,2
s_add_u32 m0, s38, 3072
CF 165,50,51
GS 8
CF 166,248,249
CF 167,250,251
M 1,30,0
LC_ 110
RV 252,22,5392
M 1,31,1
WL 6
RA 236,1,12288
M 0,20,2
WL 5
LO 17,17,107
LU 107,17
M 0,21,3
RA 216,1,13312
HS 25,107
DW 0
LK 48,252,47,0
CF 168,40,41
LK 248,254,47,2
CF 169,42,43
GS 9
CF 170,44,45
CF 171,46,47
M 1,32,2
PK 107,17,105,33
PK 105,17,105,32
LL 19,86
LL 111,87
M 1,33,3
WL 1
LA 19
RA 220,1,14336
DW 1024
M 0,22,4
s_add_u32 s42, s44, s60
LC_ 111
RV 36,8,6912
s_addc_u32 s43, s45, 0
M 0,23,5
RA 224,1,15360
GS 10
CF 172,48,49
CF 173,50,51
M 1,34,4
)"
R"(LO 18,18,110
LU 110,18
ST 104
DW 2048
M 1,35,5
WL 5
RA 228,1,16384
HS 25,110
Z 6,12,0
WL 4
PK 110,18,108,33
PK 108,18,108,32
LL 16,56
LL 104,57
M 6,13,1
WL 1
LA 16
PO 110
DQ 3072
CF 174,248,249
CF 175,250,251
s_nop 1
GS 11
s_add_u32 s50, s50, %[d]
RA 232,1,17408
s_nop 5
v_cndmask_b32 v52, v52, v53, s[78:79]
Z 7,24,0
s_cmp_eq_u32 s50, %[wrap]
LC_ 104
v_cndmask_b32 v54, v54, v55, s[78:79]
v_cndmask_b32 v52, v52, v54, s[80:81]
M 7,25,1
s_cselect_b32 s50, %[rst], s50
RA 236,1,18432
v_mul_f32 v52, 0x3b2aaaab, v52
v_med3_f32 v53, v52, v39, v37
M 6,14,2
WL 2
LO 19,19,111
LU 111,19
v_cmp_eq_f32 vcc, v52, v53
C 52,52,52
M 6,15,3
s_add_u32 s38, s38, 32768
RA 216,1,19456
HS 25,111
v_lshlrev_b32 v52, 16, v52
M 7,26,2
s_cmp_eq_u32 s38, s77
PK 111,19,109,33
PK 109,19,109,32
LL 17,58
LL 105,59
s_cselect_b32 s38, s76, s38
v_add_f32 v52, v36, v52
C 52,52,52
M 7,27,3
s_add_u32 s37, s37, 32768
LA 17
PO 111
s_cmp_eq_u32 s37, s75
s_cselect_b32 s37, s74, s37
v_lshlrev_b32 v52, 16, v52
v_sqrt_f32 v52, v52
M 6,16,4
WL 2
LC_ 105
RA 220,1,20480
XL 54,384
C 52,52,52
v_lshlrev_b32 v52, 16, v52
M 6,17,5
s_add_u32 s42, s44, s61
RA 224,1,21504
HX 25
s_addc_u32 s43, s45, 0
v_rcp_f32 v52, v52
s_nop 0
v_mul_f32 v52, v38, v52
M 7,28,4
LO 104,16,104
LU 16,104
W1 25,1024
v_cndmask_b32 v52, 0, v52, vcc
CF 52,52,52
M 7,29,5
WL 2
RA 228,1,22528
HZ 24,16
ST 108
XL 55,448
ds_bpermute_b32 v13, v28, v52
M 6,18,0
s_mul_i32 s53, s51, 49152
LL 18,60
LL 108,61
s_bitcmp1_b64 s[90:91], s51
s_cselect_b64 s[44:45], s[84:85], s[82:83]
s_add_u32 s44, s44, s53
s_addc_u32 s45, s45, 0
s_add_u32 s51, s51, %[d]
s_cmp_eq_u32 s51, %[wrap]
s_cselect_b32 s51, %[rst], s51
M 6,19,1
LA 18
RA 232,1,23552
M 7,30,0
WL 5
LC_ 108
XL 56,512
v_add_u32 v4, s39, v4
M 7,31,1
WV 17
RA 236,1,24576
s_nop 1
v_add_u32 v1, s37, v0
BW 48
M 6,20,2
WL 6
LO 105,17,105
LU 17,105
XL 57,576
RV 40,8,6144
RV 44,8,6208
M 6,21,3
RA 216,1,0
HS 24,17
RV 48,8,6272
M 7,32,2
WL 5
LL 19,62
LL 109,63
BR 48
M 7,33,3
WV 16
LA 19
XL 58,640
BW 49
s_nop 1
BR 49
M 6,22,4
s_lshl_b32 s53, s52, 8
LC_ 109
v_and_b32 v22, 0xfffffcff, v1
XS 128
M 6,23,5
s_add_u32 s48, %[alo], s53
RA 220,1,1024
XS 129
XS 130
XS 131
WL 5
LZ 40,48
s_addc_u32 s49, %[ahi], 0
BW 50
M 7,34,4
WV 16
LO 108,18,108
LU 18,108
s_add_u32 s52, s52, %[d]
s_cmp_eq_u32 s52, %[wrap]
s_cselect_b32 s52, %[rst], s52
XL 59,704
RV 40,8,6336
AW 24,128
BR 50
AW 25,129
M 7,35,5
s_barrier
RV 252,22,5120
HS 24,18
AW 26,130
AW 27,131
WL 0
Z 0,12,0
s_sub_u32 s39, 0, s39
LL 16,80
LL 106,81
RA 224,1,2048
XS 132
XS 133
M 0,13,1
s_mul_i32 s53, s50, 25600
LA 16
RA 228,1,3072
XS 134
XS 135
AW 28,132
LS 44,49
s_add_u32 s40, %[wlo], s53
BW 51
RV 44,8,6400
s_nop 0
BR 51
Z 1,24,0
s_addc_u32 s41, %[whi], 0
LC_ 106
RA 232,1,4096
AW 29,133
AW 30,134
M 1,25,1
s_mov_b32 m0, s38
RA 236,1,6144
ds_read_b64 v[14:15], v5 offset:0
AW 31,135
v_min_u32 v35, v35, v13
LS 48,50
WV 16
BW 52
RV 48,8,6464
WL 11
M 0,14,2
WL 9
LO 109,19,109
LU 19,109
XS 136
XS 137
BR 52
M 0,15,3
WL 7
RA 216,1,7168
HS 24,19
XS 138
XS 139
AW 32,136
LS 40,51
WV 15
BW 53
M 1,26,2
LL 17,82
LL 107,83
DW 0
AW 33,137
AW 34,138
RV 40,8,6528
M 1,27,3
WL 8
LA 17
RA 220,1,8192
v_subrev_u32 v5, s39, v5
AW 35,139
BR 53
XS 140
M 0,16,4
WL 10
LC_ 107
DW 1024
XS 141
XS 142
M 0,17,5
WL 7
RA 224,1,9216
v_add_f32 v14, v14, v15
s_nop 1
v_add_f32_dpp v14, v14, v14 quad_perm:[1,0,3,2] row_mask:0xf bank_mask:0xf
s_nop 1
v_add_f32_dpp v14, v14, v14 quad_perm:[2,3,0,1] row_mask:0xf bank_mask:0xf
s_nop 1
v_add_f32_dpp v14, v14, v14 row_half_mirror row_mask:0xf bank_mask:0xf
HX 24
XS 143
AW 36,140
AW 37,141
LS 44,52
WV 8
BW 54
RV 44,8,6592
M 1,28,4
LO 16,16,106
LU 106,16
v_cvt_f64_f32 v[14:15], v14
W1 24,0
AW 38,142
BR 54
AW 39,143
M 1,29,5
WL 6
RA 228,1,10240
HZ 25,106
s_mov_b64 exec, s[54:55]
SP
s_mov_b64 exec, -1
DW 2048
XS 144
XS 145
LS 48,53
s_add_u32 s40, s40, 3072
WV 8
BW 55
M 0,18,0
s_addc_u32 s41, s41, 0
PK 106,16,104,33
PK 104,16,104,32
LL 18,84
LL 110,85
XS 146
XS 147
RV 48,8,6656
M 0,19,1
s_add_u32 m0, s38, 3072
LA 18
RA 232,1,11264
AW 40,144
AW 41,145
BR 55
AW 42,146
M 1,30,0
LC_ 110
RV 252,22,5392
AW 43,147
XS 148
M 1,31,1
WL 8
RA 236,1,12288
XS 149
XS 150
LS 40,54
WV 7
BW 56
RV 40,8,6720
M 0,20,2
WL 10
LO 17,17,107
LU 107,17
DW 0
XS 151
BR 56
AW 44,148
M 0,21,3
WL 6
RA 216,1,13312
HS 25,107
AW 45,149
AW 46,150
AW 47,151
LS 44,55
WV 7
BW 57
M 1,32,2
WL 5
PK 107,17,105,33
PK 105,17,105,32
LL 19,86
LL 111,87
DW 1024
XS 152
RV 44,8,6784
XS 153
M 1,33,3
s_add_u32 s42, s44, s60
LA 19
RA 220,1,14336
XS 154
BR 57
XS 155
M 0,22,4
s_addc_u32 s43, s45, 0
LC_ 111
DW 2048
AW 48,152
AW 49,153
M 0,23,5
WL 7
RA 224,1,15360
AW 50,154
AW 51,155
XS 156
LS 48,56
WV 8
BW 58
RV 48,8,6848
M 1,34,4
LO 18,18,110
LU 110,18
ST 104
RA 240,7,0
XS 157
XS 158
M 1,35,5
WL 5
RA 228,1,16384
HS 25,110
DQ 3072
BR 58
XS 159
AW 52,156
LS 40,57
WV 9
BW 59
Z 6,12,0
s_add_u32 s50, s50, %[d]
PK 110,18,108,33
PK 108,18,108,32
LL 16,56
LL 104,57
AW 53,157
AW 54,158
M 6,13,1
s_cmp_eq_u32 s50, %[wrap]
)"
R"(LA 16
PO 110
AW 55,159
RA 244,7,256
XS 160
Z 7,24,0
s_cselect_b32 s50, %[rst], s50
LC_ 104
RA 232,1,17408
XS 161
BR 59
XS 162
XS 163
M 7,25,1
s_add_u32 s38, s38, 32768
RA 236,1,18432
AW 56,160
AW 57,161
WL 7
LS 44,58
s_cmp_eq_u32 s38, s77
RA 248,7,512
AW 58,162
M 6,14,2
WL 2
LO 19,19,111
LU 111,19
s_cselect_b32 s38, s76, s38
AW 59,163
M 6,15,3
s_add_u32 s37, s37, 32768
RA 216,1,19456
HS 25,111
XS 164
XS 165
LS 48,59
s_cmp_eq_u32 s37, s75
RA 252,7,768
XS 166
M 7,26,2
s_cselect_b32 s37, s74, s37
PK 111,19,109,33
PK 109,19,109,32
LL 17,58
LL 105,59
XS 167
AW 60,164
M 7,27,3
s_add_u32 s53, %[xo0], 393216
LA 17
PO 111
s_add_u32 s46, %[xlo], s53
s_addc_u32 s47, %[xhi], 0
s_bitcmp1_b64 s[90:91], 48
s_cselect_b32 s58, 16, 0
s_lshr_b32 s58, 0xfe987610, s58
v_lshrrev_b32 v3, 6, v0
v_and_b32 v3, 12, v3
v_bfe_u32 v3, s58, v3, 4
v_mad_u32_u24 v3, v3, s97, v21
AW 61,165
AW 62,166
M 6,16,4
s_add_u32 s42, s44, s61
LC_ 105
RA 220,1,20480
AW 63,167
XS 168
WL 6
M 6,17,5
s_addc_u32 s43, s45, 0
RA 224,1,21504
HX 25
XL 60,0
XS 169
XS 170
M 7,28,4
LO 104,16,104
LU 16,104
W1 25,1024
XS 171
AW 64,168
AW 65,169
M 7,29,5
WL 2
RA 228,1,22528
HZ 24,16
ST 108
AW 66,170
AW 67,171
M 6,18,0
s_mul_i32 s53, s51, 49152
LL 18,60
LL 108,61
s_bitcmp1_b64 s[90:91], s51
s_cselect_b64 s[44:45], s[84:85], s[82:83]
s_add_u32 s44, s44, s53
s_addc_u32 s45, s45, 0
s_add_u32 s51, s51, %[d]
s_cmp_eq_u32 s51, %[wrap]
s_cselect_b32 s51, %[rst], s51
XL 61,64
XS 172
XS 173
M 6,19,1
LA 18
RA 232,1,23552
XS 174
XS 175
M 7,30,0
LC_ 108
XL 62,128
v_add_u32 v4, s39, v4
AW 68,172
AW 69,173
AW 70,174
M 7,31,1
RA 236,1,24576
s_nop 1
v_add_u32 v1, s37, v0
AW 71,175
v_mul_f32 v36, 0xbb2aaaab, v36
v_mul_f32 v37, 0xbb2aaaab, v37
v_mul_f32 v38, 0xbb2aaaab, v38
v_mul_f32 v39, 0xbb2aaaab, v39
s_nop 1
LK 40,240,48,0
LK 44,242,48,2
RA 240,7,1024
LK 48,244,49,0
s_nop 1
CF 128,40,41
LK 248,246,49,2
RA 244,7,1280
CF 129,42,43
CF 130,44,45
CF 131,46,47
LK 40,248,50,0
CF 132,48,49
LK 44,250,50,2
RA 248,7,1536
CF 133,50,51
GZ 0
CF 134,248,249
CF 135,250,251
LK 48,252,51,0
CF 136,40,41
LK 248,254,51,2
RA 252,7,1792
CF 137,42,43
GS 1
WL 8
CF 138,44,45
CF 139,46,47
M 6,20,2
LO 105,17,105
LU 17,105
M 6,21,3
WL 3
RA 216,1,0
HS 24,17
XL 63,192
LK 40,240,52,0
CF 140,48,49
LK 44,242,52,2
RA 240,7,2048
CF 141,50,51
GS 2
WL 4
CF 142,248,249
CF 143,250,251
LK 48,244,53,0
CF 144,40,41
LK 248,246,53,2
RA 244,7,2304
CF 145,42,43
GS 3
WL 4
CF 146,44,45
CF 147,46,47
LK 40,248,54,0
CF 148,48,49
LK 44,250,54,2
RA 248,7,2560
CF 149,50,51
GS 4
CF 150,248,249
CF 151,250,251
M 7,32,2
LL 19,62
LL 109,63
M 7,33,3
WL 4
LA 19
XL 64,256
LK 48,252,55,0
CF 152,40,41
LK 248,254,55,2
s_lshl_b32 s53, s52, 8
RA 252,7,2816
CF 153,42,43
GS 5
s_add_u32 s48, %[alo], s53
CF 154,44,45
CF 155,46,47
M 6,22,4
s_addc_u32 s49, %[ahi], 0
LC_ 109
v_and_b32 v22, 0xfffffcff, v1
M 6,23,5
s_add_u32 s52, s52, %[d]
RA 220,1,1024
M 7,34,4
LO 108,18,108
LU 18,108
s_cmp_eq_u32 s52, %[wrap]
s_cselect_b32 s52, %[rst], s52
XL 65,320
M 7,35,5
s_barrier
RV 252,22,5120
HS 24,18
WL 0
Z 0,12,0
s_sub_u32 s39, 0, s39
LL 16,80
LL 106,81
RA 224,1,2048
M 0,13,1
s_mul_i32 s53, s50, 25600
LA 16
RA 228,1,3072
LK 40,240,56,0
CF 156,48,49
LK 44,242,56,2
s_add_u32 s40, %[wlo], s53
CF 157,50,51
GS 6
s_addc_u32 s41, %[whi], 0
CF 158,248,249
CF 159,250,251
LK 48,244,57,0
CF 160,40,41
LK 248,246,57,2
s_mov_b32 m0, s38
CF 161,42,43
GS 7
CF 162,44,45
CF 163,46,47
Z 1,24,0
LC_ 106
RA 232,1,4096
M 1,25,1
RA 236,1,6144
ds_read_b64 v[14:15], v5 offset:0
LK 40,248,58,0
CF 164,48,49
LK 44,250,58,2
CF 165,50,51
GS 8
WL 6
CF 166,248,249
CF 167,250,251
M 0,14,2
WL 4
LO 109,19,109
LU 19,109
M 0,15,3
RA 216,1,7168
HS 24,19
LK 48,252,59,0
CF 168,40,41
LK 248,254,59,2
CF 169,42,43
v_subrev_u32 v5, s39, v5
GS 9
CF 170,44,45
CF 171,46,47
M 1,26,2
LL 17,82
LL 107,83
DW 0
M 1,27,3
WL 3
LA 17
RA 220,1,8192
M 0,16,4
WL 4
LC_ 107
DW 1024
M 0,17,5
RA 224,1,9216
RV 36,8,6912
HX 24
GS 10
CF 172,48,49
CF 173,50,51
M 1,28,4
LO 16,16,106
LU 106,16
W1 24,0
M 1,29,5
WL 4
RA 228,1,10240
HZ 25,106
v_add_f32 v14, v14, v15
s_nop 1
v_add_f32_dpp v14, v14, v14 quad_perm:[1,0,3,2] row_mask:0xf bank_mask:0xf
s_nop 1
v_add_f32_dpp v14, v14, v14 quad_perm:[2,3,0,1] row_mask:0xf bank_mask:0xf
s_nop 1
v_add_f32_dpp v14, v14, v14 row_half_mirror row_mask:0xf bank_mask:0xf
v_cvt_f64_f32 v[14:15], v14
s_mov_b64 exec, s[54:55]
SP
s_mov_b64 exec, -1
DW 2048
M 0,18,0
s_add_u32 s40, s40, 3072
PK 106,16,104,33
PK 104,16,104,32
LL 18,84
LL 110,85
M 0,19,1
s_addc_u32 s41, s41, 0
LA 18
RA 232,1,11264
M 1,30,0
s_add_u32 m0, s38, 3072
LC_ 110
RV 252,22,5392
M 1,31,1
WL 7
RA 236,1,12288
CF 174,248,249
CF 175,250,251
M 0,20,2
WL 5
LO 17,17,107
LU 107,17
M 0,21,3
RA 216,1,13312
HS 25,107
DW 0
GS 11
WL 4
DW 1024
s_nop 5
v_cndmask_b32 v52, v52, v53, s[78:79]
v_cndmask_b32 v54, v54, v55, s[78:79]
v_cndmask_b32 v52, v52, v54, s[80:81]
M 1,32,2
s_add_u32 s42, s44, s60
PK 107,17,105,33
PK 105,17,105,32
LL 19,86
LL 111,87
v_mul_f32 v52, 0x3b2aaaab, v52
M 1,33,3
s_addc_u32 s43, s45, 0
LA 19
RA 220,1,14336
v_med3_f32 v53, v52, v39, v37
v_cmp_eq_f32 vcc, v52, v53
M 0,22,4
WL 3
LC_ 111
ST 104
C 52,52,52
M 0,23,5
RA 224,1,15360
v_lshlrev_b32 v52, 16, v52
M 1,34,4
LO 18,18,110
LU 110,18
DW 2048
v_add_f32 v52, v36, v52
C 52,52,52
M 1,35,5
WL 4
RA 228,1,16384
HS 25,110
v_lshlrev_b32 v52, 16, v52
Z 6,12,0
WL 3
PK 110,18,108,33
PK 108,18,108,32
LL 16,56
LL 104,57
v_sqrt_f32 v52, v52
s_nop 0
C 52,52,52
M 6,13,1
WL 1
LA 16
PO 110
DQ 3072
v_lshlrev_b32 v52, 16, v52
Z 7,24,0
s_add_u32 s50, s50, %[d]
)"
R"(LC_ 104
RA 232,1,17408
v_rcp_f32 v52, v52
s_nop 0
v_mul_f32 v52, v38, v52
M 7,25,1
s_cmp_eq_u32 s50, %[wrap]
RA 236,1,18432
s_cselect_b32 s50, %[rst], s50
v_cndmask_b32 v52, 0, v52, vcc
M 6,14,2
WL 4
LO 19,19,111
LU 111,19
CF 52,52,52
M 6,15,3
s_add_u32 s38, s38, 32768
RA 216,1,19456
HS 25,111
ds_bpermute_b32 v13, v28, v52
M 7,26,2
s_cmp_eq_u32 s38, s77
PK 111,19,109,33
PK 109,19,109,32
LL 17,58
LL 105,59
s_cselect_b32 s38, s76, s38
M 7,27,3
s_add_u32 s37, s37, 32768
LA 17
PO 111
s_cmp_eq_u32 s37, s75
s_cselect_b32 s37, s74, s37
WL 4
M 6,16,4
WL 3
LC_ 105
RA 220,1,20480
XL 66,384
M 6,17,5
s_add_u32 s42, s44, s61
RA 224,1,21504
HX 25
s_addc_u32 s43, s45, 0
M 7,28,4
LO 104,16,104
LU 16,104
W1 25,1024
M 7,29,5
WL 2
RA 228,1,22528
HZ 24,16
ST 108
XL 67,448
M 6,18,0
s_mul_i32 s53, s51, 49152
LL 18,60
LL 108,61
s_bitcmp1_b64 s[90:91], s51
s_cselect_b64 s[44:45], s[84:85], s[82:83]
s_add_u32 s44, s44, s53
s_addc_u32 s45, s45, 0
s_add_u32 s51, s51, %[d]
s_cmp_eq_u32 s51, %[wrap]
s_cselect_b32 s51, %[rst], s51
M 6,19,1
LA 18
RA 232,1,23552
M 7,30,0
LC_ 108
XL 68,512
v_add_u32 v4, s39, v4
XS 128
M 7,31,1
WL 5
RA 236,1,24576
s_nop 1
v_add_u32 v1, s37, v0
XS 129
XS 130
M 6,20,2
WL 4
LO 105,17,105
LU 17,105
XL 69,576
XS 131
M 6,21,3
RA 216,1,0
HS 24,17
AW 72,128
AW 73,129
M 7,32,2
LL 19,62
LL 109,63
AW 74,130
AW 75,131
M 7,33,3
WL 1
LA 19
XL 70,640
XS 132
XS 133
M 6,22,4
s_lshl_b32 s53, s52, 8
LC_ 109
v_and_b32 v22, 0xfffffcff, v1
XS 134
M 6,23,5
s_add_u32 s48, %[alo], s53
RA 220,1,1024
XS 135
AW 76,132
M 7,34,4
WV 22
LO 108,18,108
LU 18,108
s_addc_u32 s49, %[ahi], 0
s_add_u32 s52, s52, %[d]
s_cmp_eq_u32 s52, %[wrap]
s_cselect_b32 s52, %[rst], s52
XL 71,704
AW 77,133
AW 78,134
M 7,35,5
s_barrier
RV 252,22,5120
HS 24,18
AW 79,135
v_min_u32 v35, v35, v13
WL 0
Z 0,12,0
s_sub_u32 s39, 0, s39
LL 16,80
LL 106,81
XS 136
XS 137
M 0,13,1
s_mul_i32 s53, s50, 25600
LA 16
RA 224,1,2048
XS 138
Z 1,24,0
s_add_u32 s40, %[wlo], s53
LC_ 106
RA 228,1,3072
XS 139
AW 80,136
M 1,25,1
s_addc_u32 s41, %[whi], 0
RA 232,1,4096
AW 81,137
AW 82,138
Z 2,36,0
LO 109,19,109
LU 19,109
AW 83,139
XS 140
M 2,37,1
s_mov_b32 m0, s38
ds_read_b64 v[14:15], v5 offset:0
HS 24,19
XS 141
Z 3,48,0
WL 2
LL 17,82
LL 107,83
XS 142
XS 143
M 3,49,1
LA 17
RA 236,1,6144
AW 84,140
AW 85,141
M 0,14,2
LC_ 107
DW 0
AW 86,142
AW 87,143
M 0,15,3
v_subrev_u32 v5, s39, v5
HX 24
XS 144
XS 145
M 1,26,2
LO 16,16,106
LU 106,16
XS 146
M 1,27,3
RA 216,1,7168
HZ 25,106
XS 147
AW 88,144
M 2,38,2
PK 106,16,104,33
PK 104,16,104,32
LL 18,84
LL 110,85
AW 89,145
AW 90,146
M 2,39,3
LA 18
W1 24,0
AW 91,147
XS 148
M 3,50,2
WL 4
LC_ 110
DW 1024
XS 149
M 3,51,3
RA 220,1,8192
v_add_f32 v14, v14, v15
XS 150
XS 151
M 0,16,4
WL 5
LO 17,17,107
LU 107,17
AW 92,148
AW 93,149
M 0,17,5
DW 2048
HS 25,107
v_add_f32_dpp v14, v14, v14 quad_perm:[1,0,3,2] row_mask:0xf bank_mask:0xf
AW 94,150
AW 95,151
M 1,28,4
s_add_u32 s42, s44, s60
PK 107,17,105,33
PK 105,17,105,32
LL 19,86
LL 111,87
XS 152
M 1,29,5
s_addc_u32 s43, s45, 0
LA 19
RA 224,1,9216
v_add_f32_dpp v14, v14, v14 quad_perm:[2,3,0,1] row_mask:0xf bank_mask:0xf
XS 153
XS 154
M 2,40,4
LC_ 111
ST 104
XS 155
AW 96,152
M 2,41,5
WV 26
BW 60
v_add_f32_dpp v14, v14, v14 row_half_mirror row_mask:0xf bank_mask:0xf
AW 97,153
AW 98,154
M 3,52,4
WL 4
LO 18,18,110
LU 110,18
AW 99,155
XS 156
M 3,53,5
s_add_u32 s40, s40, 3072
RA 228,1,10240
HS 25,110
v_cvt_f64_f32 v[14:15], v14
XS 157
M 0,18,0
s_addc_u32 s41, s41, 0
PK 110,18,108,33
PK 108,18,108,32
XS 158
XS 159
M 0,19,1
s_add_u32 m0, s38, 3072
s_mov_b64 exec, s[54:55]
SP
s_mov_b64 exec, -1
PO 110
AW 100,156
AW 101,157
M 1,30,0
RV 40,8,6144
RV 44,8,6208
AW 102,158
AW 103,159
M 1,31,1
RA 232,1,11264
RV 48,8,6272
XS 160
M 2,42,0
WL 6
LO 19,19,111
LU 111,19
XS 161
XS 162
M 2,43,1
RV 252,22,5392
HS 25,111
XS 163
AW 104,160
M 3,54,0
WL 5
PK 111,19,109,33
PK 109,19,109,32
AW 105,161
AW 106,162
M 3,55,1
RA 236,1,12288
PO 111
AW 107,163
M 0,20,2
WV 25
DW 0
BR 60
XS 164
XS 165
M 0,21,3
s_add_u32 s42, s44, s61
BW 61
XS 166
XS 167
M 1,32,2
s_addc_u32 s43, s45, 0
ST 108
BR 61
AW 108,164
AW 109,165
M 1,33,3
RA 216,1,13312
HX 25
AW 110,166
AW 111,167
M 2,44,2
W1 25,1024
XS 168
M 2,45,3
DW 1024
v_add_u32 v4, s39, v4
XS 169
XS 170
M 3,56,2
WL 5
XS 171
AW 112,168
M 3,57,3
WL 4
RA 220,1,14336
AW 113,169
AW 114,170
LZ 40,60
WV 27
BW 62
M 0,22,4
DW 2048
RV 40,8,6336
AW 115,171
M 0,23,5
WL 5
BR 62
XS 172
XS 173
LS 44,61
WV 27
BW 63
M 1,34,4
RV 44,8,6400
BR 63
XS 174
XS 175
M 1,35,5
RA 224,1,15360
AW 116,172
AW 117,173
M 2,46,4
LL 16,56
LL 104,57
AW 118,174
AW 119,175
M 2,47,5
LA 16
DQ 3072
M 3,58,4
WL 8
LC_ 104
M 3,59,5
WV 27
RA 228,1,16384
Z 6,12,0
WL 7
M 6,13,1
s_add_u32 s50, s50, %[d]
LS 48,62
s_cmp_eq_u32 s50, %[wrap]
BW 64
Z 7,24,0
s_cselect_b32 s50, %[rst], s50
LL 17,58
LL 105,59
RV 48,8,6464
M 7,25,1
WL 6
LA 17
RA 232,1,17408
)"
R"(BR 64
LS 40,63
WV 26
BW 65
Z 8,36,0
LC_ 105
RV 40,8,6528
M 8,37,1
WV 16
BR 65
Z 9,48,0
WL 9
LO 104,16,104
LU 16,104
M 9,49,1
s_add_u32 s38, s38, 32768
RA 236,1,18432
HZ 24,16
M 6,14,2
s_cmp_eq_u32 s38, s77
LL 18,60
LL 108,61
M 6,15,3
s_cselect_b32 s38, s76, s38
LA 18
WL 6
LS 44,64
s_add_u32 s37, s37, 32768
BW 66
M 7,26,2
s_cmp_eq_u32 s37, s75
LC_ 108
RV 44,8,6592
M 7,27,3
s_cselect_b32 s37, s74, s37
RA 216,1,19456
BR 66
M 8,38,2
WL 7
LO 105,17,105
LU 17,105
M 8,39,3
s_add_u32 s53, %[xo0], 393216
HS 24,17
LS 48,65
s_add_u32 s46, %[xlo], s53
WV 14
BW 67
M 9,50,2
WL 7
LL 19,62
LL 109,63
M 9,51,3
s_addc_u32 s47, %[xhi], 0
LA 19
RA 220,1,20480
RV 48,8,6656
M 6,16,4
s_bitcmp1_b64 s[90:91], 48
LC_ 109
BR 67
M 6,17,5
s_cselect_b32 s58, 16, 0
M 7,28,4
WL 9
LO 108,18,108
LU 18,108
s_lshr_b32 s58, 0xda43cb52, s58
v_lshrrev_b32 v3, 6, v0
v_and_b32 v3, 12, v3
v_bfe_u32 v3, s58, v3, 4
v_mad_u32_u24 v3, v3, s97, v21
M 7,29,5
WL 6
RA 224,1,21504
HS 24,18
LS 40,66
WV 13
BW 68
M 8,40,4
LL 16,64
LL 112,65
RV 40,8,6720
M 8,41,5
WV 12
LA 16
XL 72,0
BR 68
M 9,52,4
WL 5
LC_ 112
M 9,53,5
s_mul_i32 s53, s51, 49152
RA 228,1,22528
LS 44,67
s_bitcmp1_b64 s[90:91], s51
BW 69
M 6,18,0
LO 109,19,109
LU 19,109
RV 44,8,6784
M 6,19,1
s_cselect_b64 s[44:45], s[84:85], s[82:83]
BR 69
HS 24,19
s_add_u32 s44, s44, s53
s_addc_u32 s45, s45, 0
s_add_u32 s51, s51, %[d]
s_cmp_eq_u32 s51, %[wrap]
s_cselect_b32 s51, %[rst], s51
M 7,30,0
LL 17,66
LL 113,67
M 7,31,1
WL 5
LA 17
RA 232,1,23552
LS 48,68
WV 12
BW 70
M 8,42,0
LC_ 113
XL 73,64
M 8,43,1
s_lshl_b32 s53, s52, 8
RV 48,8,6848
RA 240,7,0
M 9,54,0
WL 9
LO 112,16,112
LU 16,112
M 9,55,1
s_add_u32 s48, %[alo], s53
RA 236,1,24576
HS 24,16
BR 70
M 6,20,2
s_addc_u32 s49, %[ahi], 0
LL 18,68
LL 116,69
s_add_u32 s52, s52, %[d]
s_cmp_eq_u32 s52, %[wrap]
s_cselect_b32 s52, %[rst], s52
M 6,21,3
WL 8
LA 18
XL 74,128
LS 40,69
WV 13
BW 71
M 7,32,2
LC_ 116
XL 75,192
RA 244,7,256
M 7,33,3
BR 71
v_add_u32 v1, s37, v0
M 8,44,2
WL 9
LO 113,17,113
LU 17,113
M 8,45,3
WL 5
RA 216,1,0
HS 24,17
LS 44,70
RA 248,7,512
v_and_b32 v22, 0xfffffcff, v1
M 9,56,2
LL 19,70
LL 117,71
M 9,57,3
WL 2
LA 19
RA 220,1,1024
LS 48,71
RA 252,7,768
M 6,22,4
LC_ 117
RV 252,22,5120
M 6,23,5
XL 76,256
s_nop 1
v_mul_f32 v36, 0xbb2aaaab, v36
v_mul_f32 v37, 0xbb2aaaab, v37
M 7,34,4
LO 116,18,116
LU 18,116
v_mul_f32 v38, 0xbb2aaaab, v38
M 7,35,5
s_barrier
RA 224,1,2048
HS 24,18
v_mul_f32 v39, 0xbb2aaaab, v39
s_nop 1
LK 40,240,60,0
LK 44,242,60,2
s_sub_u32 s39, 0, s39
RA 240,7,1024
LK 48,244,61,0
s_nop 0
CF 128,40,41
LK 248,246,61,2
RA 244,7,1280
CF 129,42,43
M 8,46,4
LL 16,80
LL 106,81
M 8,47,5
LA 16
ds_read_b64 v[14:15], v5 offset:0
M 9,58,4
WL 8
LC_ 106
XL 77,320
M 9,59,5
WL 6
RA 228,1,3072
v_subrev_u32 v5, s39, v5
Z 0,12,0
LO 117,19,117
LU 19,117
CF 130,44,45
M 0,13,1
s_mul_i32 s53, s50, 25600
HS 24,19
CF 131,46,47
LK 40,248,62,0
CF 132,48,49
LK 44,250,62,2
s_add_u32 s40, %[wlo], s53
RA 248,7,1536
CF 133,50,51
GZ 0
s_addc_u32 s41, %[whi], 0
CF 134,248,249
CF 135,250,251
LK 48,252,63,0
CF 136,40,41
LK 248,254,63,2
s_mov_b32 m0, s38
RA 252,7,1792
CF 137,42,43
GS 1
WL 7
CF 138,44,45
CF 139,46,47
LK 40,240,64,0
CF 140,48,49
LK 44,242,64,2
RA 240,7,2048
CF 141,50,51
GS 2
CF 142,248,249
CF 143,250,251
Z 1,24,0
LL 17,82
LL 107,83
M 1,25,1
LA 17
RA 232,1,4096
Z 2,36,0
LC_ 107
HX 24
M 2,37,1
WL 10
W1 24,0
LK 48,244,65,0
CF 144,40,41
LK 248,246,65,2
RA 244,7,2304
CF 145,42,43
GS 3
CF 146,44,45
CF 147,46,47
Z 3,48,0
WL 8
LO 16,16,106
LU 106,16
M 3,49,1
RA 236,1,6144
HZ 25,106
M 0,14,2
LL 18,84
LL 110,85
M 0,15,3
WL 8
LA 18
PK 106,16,104,33
LK 40,248,66,0
CF 148,48,49
LK 44,250,66,2
RA 248,7,2560
CF 149,50,51
GS 4
CF 150,248,249
CF 151,250,251
M 1,26,2
LC_ 110
PK 104,16,104,32
M 1,27,3
RA 216,1,7168
v_add_f32 v14, v14, v15
M 2,38,2
WL 7
LO 17,17,107
LU 107,17
v_add_f32_dpp v14, v14, v14 quad_perm:[1,0,3,2] row_mask:0xf bank_mask:0xf
M 2,39,3
DW 0
HS 25,107
LK 48,252,67,0
CF 152,40,41
LK 248,254,67,2
RA 252,7,2816
CF 153,42,43
GS 5
CF 154,44,45
CF 155,46,47
M 3,50,2
WL 5
LL 19,86
LL 111,87
v_add_f32_dpp v14, v14, v14 quad_perm:[2,3,0,1] row_mask:0xf bank_mask:0xf
M 3,51,3
LA 19
PK 107,17,105,33
M 0,16,4
LC_ 111
PK 105,17,105,32
v_add_f32_dpp v14, v14, v14 row_half_mirror row_mask:0xf bank_mask:0xf
M 0,17,5
RA 220,1,8192
v_cvt_f64_f32 v[14:15], v14
M 1,28,4
WL 5
LO 18,18,110
LU 110,18
M 1,29,5
RA 224,1,9216
HS 25,110
DW 1024
LK 40,240,68,0
CF 156,48,49
LK 44,242,68,2
s_add_u32 s42, s44, s60
CF 157,50,51
GS 6
s_addc_u32 s43, s45, 0
CF 158,248,249
CF 159,250,251
M 2,40,4
LL 16,88
LL 114,89
s_mov_b64 exec, s[54:55]
SP
s_mov_b64 exec, -1
M 2,41,5
LA 16
PK 110,18,108,33
M 3,52,4
WL 2
LC_ 114
PK 108,18,108,32
M 3,53,5
RA 228,1,10240
PO 110
LK 48,244,69,0
CF 160,40,41
LK 248,246,69,2
CF 161,42,43
GS 7
CF 162,44,45
CF 163,46,47
M 0,18,0
LO 19,19,111
LU 111,19
ST 104
DW 2048
M 0,19,1
s_add_u32 s40, s40, 3072
HS 25,111
M 1,30,0
s_addc_u32 s41, s41, 0
LL 17,90
LL 115,91
M 1,31,1
s_add_u32 m0, s38, 3072
LA 17
PK 111,19,109,33
LK 40,248,70,0
CF 164,48,49
LK 44,250,70,2
CF 165,50,51
GS 8
CF 166,248,249
CF 167,250,251
M 2,42,0
s_add_u32 s42, s44, s61
LC_ 115
)"
R"(PK 109,19,109,32
M 2,43,1
s_addc_u32 s43, s45, 0
RA 232,1,11264
PO 111
M 3,54,0
WL 3
LO 16,16,114
LU 114,16
M 3,55,1
RV 252,22,5392
HS 25,114
LK 48,252,71,0
CF 168,40,41
LK 248,254,71,2
CF 169,42,43
GS 9
CF 170,44,45
CF 171,46,47
M 0,20,2
LL 18,92
LL 118,93
M 0,21,3
LA 18
PK 114,16,112,33
M 1,32,2
LC_ 118
PK 112,16,112,32
M 1,33,3
RA 236,1,12288
RV 36,8,6912
GS 10
RA 216,1,13312
M 2,44,2
WL 7
LO 17,17,115
LU 115,17
DW 0
M 2,45,3
ST 108
HS 25,115
M 3,56,2
WL 2
LL 19,94
LL 119,95
M 3,57,3
LA 19
PK 115,17,113,33
M 0,22,4
LC_ 119
PK 113,17,113,32
M 0,23,5
RA 220,1,14336
DW 1024
M 1,34,4
LO 18,18,118
LU 118,18
M 1,35,5
s_add_u32 s42, s44, s62
RA 224,1,15360
HS 25,118
M 2,46,4
s_addc_u32 s43, s45, 0
LL 16,56
LL 104,57
M 2,47,5
LA 16
PK 118,18,116,33
M 3,58,4
WL 2
LC_ 104
PK 116,18,116,32
M 3,59,5
RA 228,1,16384
PO 118
DW 2048
CF 172,48,49
Z 6,12,0
LO 19,19,119
LU 119,19
ST 112
CF 173,50,51
M 6,13,1
DQ 3072
HS 25,119
CF 174,248,249
CF 175,250,251
Z 7,24,0
LL 17,58
LL 105,59
M 7,25,1
LA 17
PK 119,19,117,33
GS 11
RA 232,1,17408
HX 25
s_nop 5
v_cndmask_b32 v52, v52, v53, s[78:79]
v_cndmask_b32 v54, v54, v55, s[78:79]
Z 8,36,0
s_add_u32 s50, s50, %[d]
LC_ 105
PK 117,19,117,32
v_cndmask_b32 v52, v52, v54, s[80:81]
M 8,37,1
s_cmp_eq_u32 s50, %[wrap]
W1 25,1024
PO 119
v_mul_f32 v52, 0x3b2aaaab, v52
Z 9,48,0
WL 4
LO 104,16,104
LU 16,104
v_med3_f32 v53, v52, v39, v37
M 9,49,1
s_cselect_b32 s50, %[rst], s50
RA 236,1,18432
HZ 24,16
v_cmp_eq_f32 vcc, v52, v53
M 6,14,2
s_add_u32 s42, s44, s63
LL 18,60
LL 108,61
C 52,52,52
M 6,15,3
s_addc_u32 s43, s45, 0
LA 18
ST 116
v_lshlrev_b32 v52, 16, v52
M 7,26,2
s_add_u32 s37, s37, 32768
LC_ 108
v_add_u32 v4, s39, v4
M 7,27,3
s_cmp_eq_u32 s37, s75
RA 216,1,19456
v_add_f32 v52, v36, v52
M 8,38,2
WL 5
LO 105,17,105
LU 17,105
C 52,52,52
M 8,39,3
s_cselect_b32 s37, s74, s37
HS 24,17
v_lshlrev_b32 v52, 16, v52
M 9,50,2
WL 3
LL 19,62
LL 109,63
v_sqrt_f32 v52, v52
M 9,51,3
s_add_u32 s38, s38, 32768
LA 19
RA 220,1,20480
C 52,52,52
M 6,16,4
s_cmp_eq_u32 s38, s77
LC_ 109
s_cselect_b32 s38, s76, s38
v_lshlrev_b32 v52, 16, v52
M 6,17,5
XL 78,384
v_rcp_f32 v52, v52
M 7,28,4
WL 4
LO 108,18,108
LU 18,108
v_mul_f32 v52, v38, v52
M 7,29,5
s_mul_i32 s53, s51, 49152
RA 224,1,21504
HS 24,18
v_cndmask_b32 v52, 0, v52, vcc
M 8,40,4
s_bitcmp1_b64 s[90:91], s51
LL 16,64
LL 112,65
CF 52,52,52
M 8,41,5
s_cselect_b64 s[44:45], s[84:85], s[82:83]
LA 16
ds_bpermute_b32 v13, v28, v52
M 9,52,4
WL 4
LC_ 112
M 9,53,5
s_add_u32 s44, s44, s53
RA 228,1,22528
M 6,18,0
WL 5
LO 109,19,109
LU 19,109
M 6,19,1
s_addc_u32 s45, s45, 0
HS 24,19
s_add_u32 s51, s51, %[d]
s_cmp_eq_u32 s51, %[wrap]
s_cselect_b32 s51, %[rst], s51
M 7,30,0
LL 17,66
LL 113,67
M 7,31,1
WV 18
LA 17
RA 232,1,23552
XL 79,448
M 8,42,0
s_lshl_b32 s53, s52, 8
LC_ 113
M 8,43,1
s_add_u32 s48, %[alo], s53
BW 72
M 9,54,0
WL 4
LO 112,16,112
LU 16,112
M 9,55,1
s_addc_u32 s49, %[ahi], 0
RA 236,1,24576
HS 24,16
XS 128
RV 40,8,6144
M 6,20,2
s_add_u32 s52, s52, %[d]
LL 18,68
LL 116,69
s_cmp_eq_u32 s52, %[wrap]
s_cselect_b32 s52, %[rst], s52
XS 129
RV 44,8,6208
M 6,21,3
LA 18
XL 80,512
XS 130
M 7,32,2
LC_ 116
XL 81,576
XS 131
RV 48,8,6272
M 7,33,3
BR 72
v_add_u32 v1, s37, v0
AW 120,128
M 8,44,2
WL 8
LO 113,17,113
LU 17,113
AW 121,129
M 8,45,3
WV 20
RA 216,1,0
HS 24,17
AW 122,130
BW 73
M 9,56,2
WL 8
LL 19,70
LL 117,71
AW 123,131
BR 73
M 9,57,3
WL 3
LA 19
XL 82,640
XS 132
XS 133
M 6,22,4
s_barrier
LC_ 117
v_and_b32 v22, 0xfffffcff, v1
XS 134
M 6,23,5
s_sub_u32 s39, 0, s39
RA 220,1,1024
XS 135
LZ 40,72
WV 20
BW 74
M 7,34,4
LO 116,18,116
LU 18,116
AW 124,132
M 7,35,5
RV 252,22,5120
HS 24,18
RV 40,8,6336
AW 125,133
M 8,46,4
LL 16,80
LL 106,81
BR 74
AW 126,134
M 8,47,5
WL 7
LA 16
RA 224,1,2048
AW 127,135
LS 44,73
WV 19
BW 75
M 9,58,4
WL 7
LC_ 106
ds_read_b64 v[14:15], v5 offset:0
v_min_u32 v35, v35, v13
M 9,59,5
WL 7
RA 228,1,3072
RV 44,8,6400
XS 136
Z 0,12,0
WV 18
LO 117,19,117
LU 19,117
XS 137
M 0,13,1
WL 7
XL 83,704
HS 24,19
XS 138
Z 1,24,0
s_mul_i32 s53, s50, 25600
LL 17,82
LL 107,83
BR 75
XS 139
M 1,25,1
s_add_u32 s40, %[wlo], s53
LA 17
RA 232,1,4096
AW 128,136
LS 48,74
s_addc_u32 s41, %[whi], 0
BW 76
Z 2,36,0
s_mov_b32 m0, s38
LC_ 107
RV 48,8,6464
AW 129,137
M 2,37,1
BR 76
v_subrev_u32 v5, s39, v5
AW 130,138
Z 3,48,0
WL 8
LO 16,16,106
LU 106,16
AW 131,139
M 3,49,1
RA 236,1,6144
HZ 25,106
XS 140
M 0,14,2
LL 18,84
LL 110,85
XS 141
M 0,15,3
WL 7
LA 18
PK 106,16,104,33
HX 24
XS 142
LS 40,75
WV 18
BW 77
M 1,26,2
LC_ 110
PK 104,16,104,32
W1 24,0
XS 143
M 1,27,3
RA 216,1,7168
RV 40,8,6528
AW 132,140
M 2,38,2
WL 9
LO 17,17,107
LU 107,17
DW 0
AW 133,141
M 2,39,3
DW 1024
HS 25,107
AW 134,142
M 3,50,2
WL 6
LL 19,86
LL 111,87
BR 77
AW 135,143
M 3,51,3
LA 19
PK 107,17,105,33
v_add_f32 v14, v14, v15
s_nop 1
v_add_f32_dpp v14, v14, v14 quad_perm:[1,0,3,2] row_mask:0xf bank_mask:0xf
s_nop 1
v_add_f32_dpp v14, v14, v14 quad_perm:[2,3,0,1] row_mask:0xf bank_mask:0xf
XS 144
)"
R"(LS 44,76
WV 7
BW 78
M 0,16,4
LC_ 111
PK 105,17,105,32
v_add_f32_dpp v14, v14, v14 row_half_mirror row_mask:0xf bank_mask:0xf
XS 145
M 0,17,5
RA 220,1,8192
RV 44,8,6592
XS 146
M 1,28,4
WL 9
LO 18,18,110
LU 110,18
v_cvt_f64_f32 v[14:15], v14
XS 147
M 1,29,5
RA 224,1,9216
HS 25,110
AW 136,144
M 2,40,4
WL 6
LL 16,88
LL 114,89
s_mov_b64 exec, s[54:55]
SP
s_mov_b64 exec, -1
DW 2048
BR 78
AW 137,145
M 2,41,5
s_add_u32 s42, s44, s60
LA 16
PK 110,18,108,33
AW 138,146
LS 48,77
s_addc_u32 s43, s45, 0
WV 8
BW 79
M 3,52,4
WL 5
LC_ 114
PK 108,18,108,32
AW 139,147
M 3,53,5
RA 228,1,10240
PO 110
XS 148
M 0,18,0
LO 19,19,111
LU 111,19
ST 104
XS 149
RV 48,8,6656
M 0,19,1
s_add_u32 s40, s40, 3072
BR 79
HS 25,111
s_addc_u32 s41, s41, 0
s_add_u32 m0, s38, 3072
XS 150
M 1,30,0
LL 17,90
LL 115,91
XS 151
M 1,31,1
WL 6
LA 17
PK 111,19,109,33
AW 140,148
LS 40,78
WV 8
BW 80
M 2,42,0
LC_ 115
PK 109,19,109,32
AW 141,149
M 2,43,1
RA 232,1,11264
PO 111
DW 0
AW 142,150
M 3,54,0
WL 6
LO 16,16,114
LU 114,16
AW 143,151
RV 40,8,6720
M 3,55,1
RV 252,22,5392
HS 25,114
XS 152
M 0,20,2
WV 8
LL 18,92
LL 118,93
BR 80
XS 153
M 0,21,3
WL 7
LA 18
PK 114,16,112,33
DW 1024
XS 154
LS 44,79
s_add_u32 s42, s44, s61
BW 81
M 1,32,2
s_addc_u32 s43, s45, 0
LC_ 118
PK 112,16,112,32
XS 155
M 1,33,3
RA 236,1,12288
RV 44,8,6784
AW 144,152
M 2,44,2
WL 9
LO 17,17,115
LU 115,17
AW 145,153
M 2,45,3
RA 216,1,13312
HS 25,115
ST 108
AW 146,154
BR 81
M 3,56,2
WL 3
LL 19,94
LL 119,95
DW 2048
AW 147,155
M 3,57,3
LA 19
PK 115,17,113,33
XS 156
LS 48,80
WV 10
BW 82
M 0,22,4
LC_ 119
PK 113,17,113,32
XS 157
M 0,23,5
WV 9
RA 220,1,14336
RV 48,8,6848
XS 158
M 1,34,4
WL 5
LO 18,18,118
LU 118,18
DQ 3072
XS 159
M 1,35,5
s_add_u32 s42, s44, s62
RA 224,1,15360
HS 25,118
AW 148,156
RA 240,7,0
M 2,46,4
s_addc_u32 s43, s45, 0
LL 16,56
LL 104,57
AW 149,157
M 2,47,5
s_add_u32 s50, s50, %[d]
LA 16
PK 118,18,116,33
AW 150,158
BR 82
LS 40,81
s_cmp_eq_u32 s50, %[wrap]
BW 83
M 3,58,4
WL 6
LC_ 104
PK 116,18,116,32
s_cselect_b32 s50, %[rst], s50
AW 151,159
M 3,59,5
RA 228,1,16384
PO 118
XS 160
Z 6,12,0
WL 3
LO 19,19,119
LU 119,19
ST 112
XS 161
M 6,13,1
s_add_u32 s38, s38, 32768
RA 244,7,256
HS 25,119
XS 162
Z 7,24,0
s_cmp_eq_u32 s38, s77
LL 17,58
LL 105,59
s_cselect_b32 s38, s76, s38
XS 163
M 7,25,1
s_mul_i32 s53, s50, 25600
LA 17
PK 119,19,117,33
AW 152,160
BR 83
LS 44,82
s_add_u32 s40, %[wlo], s53
RA 248,7,512
AW 153,161
Z 8,36,0
s_addc_u32 s41, %[whi], 0
LC_ 105
PK 117,19,117,32
M 8,37,1
s_mov_b32 m0, s38
RA 232,1,17408
PO 119
AW 154,162
Z 9,48,0
WL 3
LO 104,16,104
LU 16,104
AW 155,163
M 9,49,1
s_add_u32 s42, s44, s63
RA 236,1,18432
HZ 24,16
XS 164
LS 48,83
s_addc_u32 s43, s45, 0
RA 252,7,768
XS 165
M 6,14,2
LL 18,60
LL 108,61
M 6,15,3
LA 18
DW 0
HX 25
XS 166
M 7,26,2
LC_ 108
W1 25,1024
XS 167
M 7,27,3
RA 216,1,19456
v_add_u32 v4, s39, v4
AW 156,164
M 8,38,2
WL 7
LO 105,17,105
LU 17,105
AW 157,165
M 8,39,3
ST 116
HS 24,17
AW 158,166
M 9,50,2
WL 5
LL 19,62
LL 109,63
AW 159,167
M 9,51,3
LA 19
RA 220,1,20480
DW 1024
XS 168
XS 169
M 6,16,4
s_add_u32 s37, s37, 32768
LC_ 109
s_cmp_eq_u32 s37, s75
s_cselect_b32 s37, s74, s37
XS 170
M 6,17,5
DW 2048
XS 171
M 7,28,4
WL 5
LO 108,18,108
LU 18,108
AW 160,168
M 7,29,5
s_add_u32 s40, s40, 3072
RA 224,1,21504
HS 24,18
AW 161,169
M 8,40,4
s_addc_u32 s41, s41, 0
LL 16,64
LL 112,65
AW 162,170
M 8,41,5
s_add_u32 m0, s38, 3072
LA 16
AW 163,171
M 9,52,4
WL 3
LC_ 112
XS 172
M 9,53,5
s_mul_i32 s53, s51, 49152
RA 228,1,22528
XS 173
M 6,18,0
WL 4
LO 109,19,109
LU 19,109
XS 174
M 6,19,1
s_bitcmp1_b64 s[90:91], s51
HS 24,19
s_cselect_b64 s[44:45], s[84:85], s[82:83]
s_add_u32 s44, s44, s53
s_addc_u32 s45, s45, 0
s_add_u32 s51, s51, %[d]
s_cmp_eq_u32 s51, %[wrap]
s_cselect_b32 s51, %[rst], s51
XS 175
M 7,30,0
LL 17,66
LL 113,67
AW 164,172
M 7,31,1
LA 17
RA 232,1,23552
AW 165,173
M 8,42,0
LC_ 113
DW 0
AW 166,174
M 8,43,1
s_lshl_b32 s53, s52, 8
AW 167,175
v_mul_f32 v36, 0xbb2aaaab, v36
v_mul_f32 v37, 0xbb2aaaab, v37
v_mul_f32 v38, 0xbb2aaaab, v38
v_mul_f32 v39, 0xbb2aaaab, v39
s_nop 1
LK 40,240,72,0
LK 44,242,72,2
s_add_u32 s48, %[alo], s53
RA 240,7,1024
LK 48,244,73,0
s_nop 0
CF 128,40,41
LK 248,246,73,2
s_addc_u32 s49, %[ahi], 0
RA 244,7,1280
CF 129,42,43
CF 130,44,45
CF 131,46,47
LK 40,248,74,0
CF 132,48,49
LK 44,250,74,2
s_add_u32 s52, s52, %[d]
RA 248,7,1536
CF 133,50,51
GZ 0
s_cmp_eq_u32 s52, %[wrap]
CF 134,248,249
CF 135,250,251
LK 48,252,75,0
CF 136,40,41
LK 248,254,75,2
s_cselect_b32 s52, %[rst], s52
RA 252,7,1792
CF 137,42,43
GS 1
CF 138,44,45
CF 139,46,47
M 9,54,0
WL 7
LO 112,16,112
LU 16,112
M 9,55,1
WL 3
RA 236,1,24576
HS 24,16
LK 40,240,76,0
CF 140,48,49
LK 44,242,76,2
RA 240,7,2048
CF 141,50,51
GS 2
WL 4
CF 142,248,249
CF 143,250,251
LK 48,244,77,0
CF 144,40,41
LK 248,246,77,2
RA 244,7,2304
CF 145,42,43
GS 3
WL 4
)"
R"(CF 146,44,45
CF 147,46,47
LK 40,248,78,0
CF 148,48,49
LK 44,250,78,2
RA 248,7,2560
CF 149,50,51
GS 4
CF 150,248,249
CF 151,250,251
M 6,20,2
LL 18,68
LL 116,69
DW 1024
M 6,21,3
LA 18
DW 2048
M 7,32,2
LC_ 116
v_add_u32 v1, s37, v0
M 7,33,3
RA 216,1,0
v_and_b32 v22, 0xfffffcff, v1
M 8,44,2
LO 113,17,113
LU 17,113
M 8,45,3
WL 7
DQ 3072
HS 24,17
LK 48,252,79,0
CF 152,40,41
LK 248,254,79,2
RA 252,7,2816
CF 153,42,43
GS 5
CF 154,44,45
CF 155,46,47
M 9,56,2
WL 7
LL 19,70
LL 117,71
M 9,57,3
s_add_u32 s50, s50, %[d]
LA 19
RA 220,1,1024
M 6,22,4
s_cmp_eq_u32 s50, %[wrap]
LC_ 117
RV 252,22,5120
M 6,23,5
s_cselect_b32 s50, %[rst], s50
M 7,34,4
WL 6
LO 116,18,116
LU 18,116
M 7,35,5
s_barrier
RA 224,1,2048
HS 24,18
LK 40,240,80,0
CF 156,48,49
LK 44,242,80,2
s_sub_u32 s39, 0, s39
CF 157,50,51
GS 6
s_add_u32 s38, s38, 32768
CF 158,248,249
CF 159,250,251
M 8,46,4
s_cmp_eq_u32 s38, s77
LL 16,80
LL 106,81
M 8,47,5
s_cselect_b32 s38, s76, s38
LA 16
ds_read_b64 v[14:15], v5 offset:0
M 9,58,4
WL 5
LC_ 106
v_subrev_u32 v5, s39, v5
M 9,59,5
WL 4
RA 228,1,3072
Z 0,12,0
LO 117,19,117
LU 19,117
M 0,13,1
s_mul_i32 s53, s50, 25600
HS 24,19
LK 48,244,81,0
CF 160,40,41
LK 248,246,81,2
s_add_u32 s40, %[wlo], s53
CF 161,42,43
GS 7
s_addc_u32 s41, %[whi], 0
CF 162,44,45
CF 163,46,47
Z 1,24,0
s_mov_b32 m0, s38
LL 17,82
LL 107,83
M 1,25,1
LA 17
RA 232,1,4096
Z 2,36,0
LC_ 107
HX 24
M 2,37,1
W1 24,0
LK 40,248,82,0
CF 164,48,49
LK 44,250,82,2
CF 165,50,51
GS 8
CF 166,248,249
CF 167,250,251
Z 3,48,0
WL 4
LO 16,16,106
LU 106,16
M 3,49,1
RA 236,1,6144
HZ 25,106
M 0,14,2
LL 18,84
LL 110,85
M 0,15,3
LA 18
PK 106,16,104,33
M 1,26,2
LC_ 110
PK 104,16,104,32
M 1,27,3
RA 216,1,7168
v_add_f32 v14, v14, v15
LK 48,252,83,0
CF 168,40,41
LK 248,254,83,2
CF 169,42,43
v_add_f32_dpp v14, v14, v14 quad_perm:[1,0,3,2] row_mask:0xf bank_mask:0xf
GS 9
CF 170,44,45
CF 171,46,47
M 2,38,2
WL 5
LO 17,17,107
LU 107,17
M 2,39,3
DW 0
HS 25,107
M 3,50,2
WL 3
LL 19,86
LL 111,87
v_add_f32_dpp v14, v14, v14 quad_perm:[2,3,0,1] row_mask:0xf bank_mask:0xf
M 3,51,3
s_add_u32 s42, s44, s60
LA 19
PK 107,17,105,33
M 0,16,4
s_addc_u32 s43, s45, 0
LC_ 111
PK 105,17,105,32
v_add_f32_dpp v14, v14, v14 row_half_mirror row_mask:0xf bank_mask:0xf
M 0,17,5
RA 220,1,8192
RV 36,8,6912
GS 10
CF 172,48,49
CF 173,50,51
M 1,28,4
WL 5
LO 18,18,110
LU 110,18
v_cvt_f64_f32 v[14:15], v14
M 1,29,5
RA 224,1,9216
HS 25,110
M 2,40,4
LL 16,88
LL 114,89
s_mov_b64 exec, s[54:55]
SP
s_mov_b64 exec, -1
M 2,41,5
LA 16
PK 110,18,108,33
M 3,52,4
WL 3
LC_ 114
PK 108,18,108,32
M 3,53,5
RA 228,1,10240
PO 110
M 0,18,0
LO 19,19,111
LU 111,19
ST 104
M 0,19,1
DW 1024
HS 25,111
M 1,30,0
LL 17,90
LL 115,91
M 1,31,1
LA 17
PK 111,19,109,33
M 2,42,0
s_add_u32 s42, s44, s61
LC_ 115
PK 109,19,109,32
M 2,43,1
s_addc_u32 s43, s45, 0
RA 232,1,11264
PO 111
CF 174,248,249
M 3,54,0
WL 3
LO 16,16,114
LU 114,16
CF 175,250,251
M 3,55,1
RV 252,22,5392
HS 25,114
GS 11
RA 236,1,12288
s_nop 6
v_cndmask_b32 v52, v52, v53, s[78:79]
M 0,20,2
LL 18,92
LL 118,93
M 0,21,3
LA 18
PK 114,16,112,33
v_cndmask_b32 v54, v54, v55, s[78:79]
M 1,32,2
LC_ 118
PK 112,16,112,32
v_cndmask_b32 v52, v52, v54, s[80:81]
M 1,33,3
RA 216,1,13312
DW 2048
v_mul_f32 v52, 0x3b2aaaab, v52
M 2,44,2
WL 6
LO 17,17,115
LU 115,17
M 2,45,3
ST 108
HS 25,115
v_med3_f32 v53, v52, v39, v37
M 3,56,2
WL 3
LL 19,94
LL 119,95
v_cmp_eq_f32 vcc, v52, v53
M 3,57,3
s_add_u32 s40, s40, 3072
LA 19
PK 115,17,113,33
M 0,22,4
s_addc_u32 s41, s41, 0
LC_ 119
PK 113,17,113,32
C 52,52,52
M 0,23,5
s_add_u32 m0, s38, 3072
RA 220,1,14336
v_lshlrev_b32 v52, 16, v52
M 1,34,4
WL 4
LO 18,18,118
LU 118,18
M 1,35,5
s_add_u32 s42, s44, s62
RA 224,1,15360
HS 25,118
v_add_f32 v52, v36, v52
M 2,46,4
s_addc_u32 s43, s45, 0
LL 16,56
LL 104,57
C 52,52,52
M 2,47,5
LA 16
PK 118,18,116,33
v_lshlrev_b32 v52, 16, v52
M 3,58,4
WL 2
LC_ 104
PK 116,18,116,32
M 3,59,5
RA 228,1,16384
PO 118
v_sqrt_f32 v52, v52
Z 6,12,0
LO 19,19,119
LU 119,19
ST 112
C 52,52,52
M 6,13,1
DW 0
HS 25,119
Z 7,24,0
LL 17,58
LL 105,59
v_lshlrev_b32 v52, 16, v52
M 7,25,1
LA 17
PK 119,19,117,33
v_rcp_f32 v52, v52
Z 8,36,0
s_add_u32 s42, s44, s63
LC_ 105
PK 117,19,117,32
v_mul_f32 v52, v38, v52
M 8,37,1
s_addc_u32 s43, s45, 0
RA 232,1,17408
PO 119
Z 9,48,0
WL 3
LO 104,16,104
LU 16,104
v_cndmask_b32 v52, 0, v52, vcc
M 9,49,1
RA 236,1,18432
HZ 24,16
CF 52,52,52
M 6,14,2
LL 18,60
LL 108,61
M 6,15,3
LA 18
ST 116
HX 25
ds_bpermute_b32 v13, v28, v52
M 7,26,2
LC_ 108
W1 25,1024
M 7,27,3
RA 216,1,19456
v_add_u32 v4, s39, v4
M 8,38,2
WL 7
LO 105,17,105
LU 17,105
M 8,39,3
DW 1024
HS 24,17
M 9,50,2
WL 5
LL 19,62
LL 109,63
M 9,51,3
s_add_u32 s37, s37, 32768
LA 19
RA 220,1,20480
M 6,16,4
s_cmp_eq_u32 s37, s75
LC_ 109
s_cselect_b32 s37, s74, s37
M 6,17,5
DW 2048
M 7,28,4
WL 5
LO 108,18,108
LU 18,108
M 7,29,5
s_mul_i32 s53, s51, 49152
RA 224,1,21504
HS 24,18
XS 128
M 8,40,4
s_bitcmp1_b64 s[90:91], s51
LL 16,64
LL 112,65
XS 129
M 8,41,5
s_cselect_b64 s[44:45], s[84:85], s[82:83]
LA 16
XS 130
M 9,52,4
WL 3
LC_ 112
XS 131
M 9,53,5
s_add_u32 s44, s44, s53
RA 228,1,22528
AW 168,128
M 6,18,0
WL 4
LO 109,19,109
LU 19,109
AW 169,129
M 6,19,1
s_addc_u32 s45, s45, 0
HS 24,19
)"
R"(s_add_u32 s51, s51, %[d]
s_cmp_eq_u32 s51, %[wrap]
s_cselect_b32 s51, %[rst], s51
AW 170,130
M 7,30,0
s_lshl_b32 s53, s52, 8
LL 17,66
LL 113,67
AW 171,131
M 7,31,1
s_add_u32 s48, %[alo], s53
LA 17
RA 232,1,23552
XS 132
M 8,42,0
s_addc_u32 s49, %[ahi], 0
LC_ 113
XS 133
M 8,43,1
s_add_u32 s52, s52, %[d]
XS 134
XS 135
M 9,54,0
WL 3
LO 112,16,112
LU 16,112
M 9,55,1
s_cmp_eq_u32 s52, %[wrap]
RA 236,1,24576
HS 24,16
AW 172,132
M 6,20,2
s_cselect_b32 s52, %[rst], s52
LL 18,68
LL 116,69
DQ 3072
AW 173,133
M 6,21,3
LA 18
v_add_u32 v1, s37, v0
AW 174,134
M 7,32,2
s_add_u32 s50, s50, %[d]
LC_ 116
AW 175,135
M 7,33,3
s_cmp_eq_u32 s50, %[wrap]
RA 216,1,0
v_and_b32 v22, 0xfffffcff, v1
v_min_u32 v35, v35, v13
M 8,44,2
WL 4
LO 113,17,113
LU 17,113
XS 136
M 8,45,3
s_cselect_b32 s50, %[rst], s50
HS 24,17
XS 137
M 9,56,2
WL 3
LL 19,70
LL 117,71
XS 138
M 9,57,3
WV 21
LA 19
RA 220,1,1024
XS 139
M 6,22,4
s_barrier
LC_ 117
RV 252,22,5120
AW 176,136
M 6,23,5
s_sub_u32 s39, 0, s39
AW 177,137
AW 178,138
M 7,34,4
WL 5
LO 116,18,116
LU 18,116
M 7,35,5
s_add_u32 s38, s38, 32768
RA 224,1,2048
HS 24,18
AW 179,139
M 8,46,4
s_cmp_eq_u32 s38, s77
LL 16,80
LL 106,81
XS 140
M 8,47,5
s_cselect_b32 s38, s76, s38
LA 16
XS 141
M 9,58,4
WL 4
LC_ 106
XS 142
M 9,59,5
WL 3
RA 228,1,3072
XS 143
Z 0,12,0
LO 117,19,117
LU 19,117
M 0,13,1
HS 24,19
AW 180,140
Z 1,24,0
LL 17,82
LL 107,83
AW 181,141
M 1,25,1
LA 17
RA 232,1,4096
AW 182,142
Z 2,36,0
LC_ 107
HX 24
AW 183,143
M 2,37,1
W1 24,0
XS 144
Z 3,48,0
WL 5
LO 16,16,106
LU 106,16
XS 145
M 3,49,1
HZ 25,106
XS 146
Z 4,60,0
LL 18,84
LL 110,85
XS 147
M 4,61,1
LA 18
PK 106,16,104,33
AW 184,144
Z 5,72,0
WL 5
LC_ 110
PK 104,16,104,32
AW 185,145
M 5,73,1
RA 236,1,6144
AW 186,146
M 0,14,2
WL 4
LO 17,17,107
LU 107,17
AW 187,147
M 0,15,3
HS 25,107
XS 148
M 1,26,2
LL 19,86
LL 111,87
XS 149
M 1,27,3
s_add_u32 s42, s44, s60
LA 19
PK 107,17,105,33
XS 150
M 2,38,2
s_addc_u32 s43, s45, 0
LC_ 111
PK 105,17,105,32
XS 151
M 2,39,3
RA 216,1,7168
AW 188,148
M 3,50,2
WL 4
LO 18,18,110
LU 110,18
AW 189,149
M 3,51,3
ST 104
HS 25,110
AW 190,150
M 4,62,2
LL 16,88
LL 114,89
AW 191,151
M 4,63,3
LA 16
PK 110,18,108,33
XS 152
M 5,74,2
WL 4
LC_ 114
PK 108,18,108,32
XS 153
M 5,75,3
s_mov_b32 m0, %[pm]
RA 220,1,8192
PO 110
XS 154
M 0,16,4
WL 4
LO 19,19,111
LU 111,19
XS 155
M 0,17,5
DF
HS 25,111
AW 192,152
M 1,28,4
LL 17,90
LL 115,91
AW 193,153
M 1,29,5
LA 17
PK 111,19,109,33
AW 194,154
M 2,40,4
s_add_u32 s42, s44, s61
LC_ 115
PK 109,19,109,32
AW 195,155
M 2,41,5
s_addc_u32 s43, s45, 0
RA 224,1,9216
PO 111
XS 156
M 3,52,4
WL 4
LO 16,16,114
LU 114,16
XS 157
M 3,53,5
ST 108
HS 25,114
XS 158
M 4,64,4
LL 18,92
LL 118,93
XS 159
M 4,65,5
LA 18
PK 114,16,112,33
AW 196,156
M 5,76,4
WL 4
LC_ 118
PK 112,16,112,32
AW 197,157
M 5,77,5
RA 228,1,10240
AW 198,158
M 0,18,0
WL 4
LO 17,17,115
LU 115,17
AW 199,159
M 0,19,1
HS 25,115
XS 160
M 1,30,0
LL 19,94
LL 119,95
XS 161
M 1,31,1
s_add_u32 s42, s44, s62
LA 19
PK 115,17,113,33
XS 162
M 2,42,0
s_addc_u32 s43, s45, 0
LC_ 119
PK 113,17,113,32
XS 163
M 2,43,1
RA 232,1,11264
AW 200,160
M 3,54,0
WL 4
LO 18,18,118
LU 118,18
AW 201,161
M 3,55,1
RV 252,22,5392
HS 25,118
AW 202,162
M 4,66,0
ST 112
AW 203,163
M 4,67,1
RA 236,1,12288
PK 118,18,116,33
XS 164
M 5,78,0
WL 5
PK 116,18,116,32
XS 165
M 5,79,1
PO 118
XS 166
M 0,20,2
WL 3
LO 19,19,119
LU 119,19
XS 167
M 0,21,3
HS 25,119
AW 204,164
M 1,32,2
HX 25
AW 205,165
M 1,33,3
RA 216,1,13312
PK 119,19,117,33
AW 206,166
M 2,44,2
s_add_u32 s42, s44, s63
W1 25,1024
PK 117,19,117,32
AW 207,167
M 2,45,3
s_addc_u32 s43, s45, 0
PO 119
v_add_u32 v4, s39, v4
XS 168
M 3,56,2
ST 116
XS 169
M 3,57,3
XS 170
XS 171
M 4,68,2
AW 208,168
AW 209,169
M 4,69,3
RA 220,1,14336
AW 210,170
M 5,80,2
WL 3
AW 211,171
XS 172
M 5,81,3
XS 173
XS 174
M 0,22,4
XS 175
AW 212,172
M 0,23,5
AW 213,173
AW 214,174
M 1,34,4
AW 215,175
M 1,35,5
RA 224,1,15360
M 2,46,4
s_nop 0
LL 16,56
LL 104,57
M 2,47,5
LA 16
ds_read_b64 v[14:15], v5 offset:0
M 3,58,4
LC_ 104
v_subrev_u32 v5, s39, v5
M 3,59,5
M 4,70,4
M 4,71,5
RA 228,1,16384
M 5,82,4
WL 5
LL 17,58
LL 105,59
M 5,83,5
LA 17
Z 6,12,0
LC_ 105
M 6,13,1
Z 7,24,0
WL 3
LO 104,16,104
LU 16,104
M 7,25,1
RA 232,1,17408
HZ 24,16
Z 8,36,0
LL 18,60
LL 108,61
M 8,37,1
s_mul_i32 s53, s50, 25600
LA 18
v_add_f32 v14, v14, v15
Z 9,48,0
s_add_u32 s40, %[wlo], s53
LC_ 108
v_add_f32_dpp v14, v14, v14 quad_perm:[1,0,3,2] row_mask:0xf bank_mask:0xf
M 9,49,1
s_addc_u32 s41, %[whi], 0
v_add_f32_dpp v14, v14, v14 quad_perm:[2,3,0,1] row_mask:0xf bank_mask:0xf
Z 10,60,0
)"
R"(WL 3
LO 105,17,105
LU 17,105
M 10,61,1
s_mov_b32 m0, s38
RA 236,1,18432
HS 24,17
Z 11,72,0
LL 19,62
LL 109,63
M 11,73,1
LA 19
DW 0
M 6,14,2
LC_ 109
v_add_f32_dpp v14, v14, v14 row_half_mirror row_mask:0xf bank_mask:0xf
M 6,15,3
v_cvt_f64_f32 v[14:15], v14
M 7,26,2
WL 3
LO 108,18,108
LU 18,108
M 7,27,3
s_add_u32 s37, s37, 32768
RA 216,1,19456
HS 24,18
M 8,38,2
s_cmp_eq_u32 s37, s75
LL 16,64
LL 112,65
M 8,39,3
s_cselect_b32 s37, s74, s37
LA 16
s_mov_b64 exec, s[54:55]
SP
s_mov_b64 exec, -1
M 9,50,2
LC_ 112
DW 1024
M 9,51,3
M 10,62,2
WL 3
LO 109,19,109
LU 19,109
M 10,63,3
RA 220,1,20480
HS 24,19
M 11,74,2
LL 17,66
LL 113,67
M 11,75,3
LA 17
DW 2048
M 6,16,4
LC_ 113
M 6,17,5
M 7,28,4
WL 3
LO 112,16,112
LU 16,112
M 7,29,5
s_mul_i32 s53, s51, 49152
RA 224,1,21504
HS 24,16
M 8,40,4
s_bitcmp1_b64 s[90:91], s51
LL 18,68
LL 116,69
M 8,41,5
s_cselect_b64 s[44:45], s[84:85], s[82:83]
LA 18
M 9,52,4
s_add_u32 s44, s44, s53
LC_ 116
M 9,53,5
s_addc_u32 s45, s45, 0
M 10,64,4
WL 3
LO 113,17,113
LU 17,113
M 10,65,5
s_add_u32 s51, s51, %[d]
RA 228,1,22528
HS 24,17
M 11,76,4
LL 19,70
LL 117,71
M 11,77,5
s_cmp_eq_u32 s51, %[wrap]
LA 19
M 6,18,0
s_cselect_b32 s51, %[rst], s51
LC_ 117
M 6,19,1
s_add_u32 s40, s40, 3072
M 7,30,0
WL 3
LO 116,18,116
LU 18,116
M 7,31,1
s_addc_u32 s41, s41, 0
RA 232,1,23552
HS 24,18
M 8,42,0
s_add_u32 m0, s38, 3072
LL 16,72
LL 120,73
M 8,43,1
LA 16
DW 0
M 9,54,0
LC_ 120
DW 1024
M 9,55,1
M 10,66,0
WL 3
LO 117,19,117
LU 19,117
M 10,67,1
RA 236,1,24576
HS 24,19
M 11,78,0
LL 17,74
LL 121,75
M 11,79,1
LA 17
DW 2048
M 6,20,2
LC_ 121
M 6,21,3
s_lshl_b32 s53, s52, 8
v_add_u32 v1, s37, v0
M 7,32,2
WL 3
LO 120,16,120
LU 16,120
M 7,33,3
s_add_u32 s48, %[alo], s53
RA 216,1,0
HS 24,16
M 8,44,2
s_addc_u32 s49, %[ahi], 0
LL 18,76
LL 124,77
M 8,45,3
s_add_u32 s52, s52, %[d]
LA 18
v_and_b32 v22, 0xfffffcff, v1
M 9,56,2
s_cmp_eq_u32 s52, %[wrap]
LC_ 124
M 9,57,3
s_cselect_b32 s52, %[rst], s52
DQ 3072
M 10,68,2
WL 3
LO 121,17,121
LU 17,121
M 10,69,3
s_add_u32 s50, s50, %[d]
RA 220,1,1024
HS 24,17
M 11,80,2
LL 19,78
LL 125,79
M 11,81,3
s_cmp_eq_u32 s50, %[wrap]
LA 19
RV 252,22,5120
M 6,22,4
s_cselect_b32 s50, %[rst], s50
LC_ 125
M 6,23,5
WV 25
M 7,34,4
WL 4
LO 124,18,124
LU 18,124
M 7,35,5
s_add_u32 s38, s38, 32768
RA 224,1,2048
HS 24,18
M 8,46,4
s_cmp_eq_u32 s38, s77
LL 16,80
LL 106,81
M 8,47,5
s_cselect_b32 s38, s76, s38
LA 16
M 9,58,4
s_barrier
LC_ 106
M 9,59,5
s_sub_u32 s39, 0, s39
M 10,70,4
WL 3
LO 125,19,125
LU 19,125
M 10,71,5
RA 228,1,3072
HS 24,19
M 11,82,4
LL 17,82
LL 107,83
M 11,83,5
LA 17
HX 24
Z 0,12,0
LC_ 107
W1 24,0
M 0,13,1
Z 1,24,0
WL 4
LO 16,16,106
LU 106,16
M 1,25,1
RA 232,1,4096
HZ 25,106
Z 2,36,0
LL 18,84
LL 110,85
M 2,37,1
LA 18
PK 106,16,104,33
Z 3,48,0
LC_ 110
PK 104,16,104,32
M 3,49,1
Z 4,60,0
WL 4
LO 17,17,107
LU 107,17
M 4,61,1
RA 236,1,6144
HS 25,107
Z 5,72,0
LL 19,86
LL 111,87
M 5,73,1
s_add_u32 s42, s44, s60
LA 19
PK 107,17,105,33
M 0,14,2
s_addc_u32 s43, s45, 0
LC_ 111
PK 105,17,105,32
M 0,15,3
ST 104
M 1,26,2
WL 3
LO 18,18,110
LU 110,18
M 1,27,3
RA 216,1,7168
HS 25,110
M 2,38,2
LL 16,88
LL 114,89
M 2,39,3
LA 16
PK 110,18,108,33
M 3,50,2
LC_ 114
PK 108,18,108,32
M 3,51,3
PO 110
M 4,62,2
WL 3
LO 19,19,111
LU 111,19
M 4,63,3
RA 220,1,8192
HS 25,111
M 5,74,2
LL 17,90
LL 115,91
M 5,75,3
s_mov_b32 m0, %[pm]
LA 17
PK 111,19,109,33
M 0,16,4
LC_ 115
PK 109,19,109,32
M 0,17,5
DF
PO 111
M 1,28,4
WL 3
LO 16,16,114
LU 114,16
M 1,29,5
s_add_u32 s42, s44, s61
RA 224,1,9216
HS 25,114
M 2,40,4
s_addc_u32 s43, s45, 0
LL 18,92
LL 118,93
M 2,41,5
LA 18
PK 114,16,112,33
M 3,52,4
LC_ 118
PK 112,16,112,32
M 3,53,5
ST 108
M 4,64,4
WL 3
LO 17,17,115
LU 115,17
M 4,65,5
RA 228,1,10240
HS 25,115
M 5,76,4
LL 19,94
LL 119,95
M 5,77,5
s_add_u32 s42, s44, s62
LA 19
PK 115,17,113,33
M 0,18,0
s_addc_u32 s43, s45, 0
LC_ 119
PK 113,17,113,32
M 0,19,1
ST 112
M 1,30,0
WL 3
LO 18,18,118
LU 118,18
M 1,31,1
RA 232,1,11264
HS 25,118
M 2,42,0
LL 16,96
LL 122,97
M 2,43,1
LA 16
PK 118,18,116,33
M 3,54,0
LC_ 122
PK 116,18,116,32
M 3,55,1
RV 252,22,5392
PO 118
M 4,66,0
WL 4
LO 19,19,119
LU 119,19
M 4,67,1
RA 236,1,12288
HS 25,119
M 5,78,0
LL 17,98
LL 123,99
M 5,79,1
LA 17
PK 119,19,117,33
M 0,20,2
s_add_u32 s42, s44, s63
LC_ 123
PK 117,19,117,32
M 0,21,3
s_addc_u32 s43, s45, 0
PO 119
M 1,32,2
WL 4
LO 16,16,122
LU 122,16
M 1,33,3
RA 216,1,13312
HS 25,122
M 2,44,2
LL 18,100
LL 126,101
M 2,45,3
LA 18
PK 122,16,120,33
M 3,56,2
LC_ 126
PK 120,16,120,32
M 3,57,3
ST 116
M 4,68,2
WL 3
LO 17,17,123
LU 123,17
M 4,69,3
RA 220,1,14336
HS 25,123
M 5,80,2
LL 19,102
LL 127,103
M 5,81,3
s_add_u32 s42, s44, s64
LA 19
PK 123,17,121,33
M 0,22,4
s_addc_u32 s43, s45, 0
LC_ 127
)"
R"(PK 121,17,121,32
M 0,23,5
ST 120
M 1,34,4
WL 3
LO 18,18,126
LU 126,18
M 1,35,5
RA 224,1,15360
HS 25,126
M 2,46,4
LL 16,56
LL 104,57
M 2,47,5
LA 16
PK 126,18,124,33
M 3,58,4
LC_ 104
PK 124,18,124,32
M 3,59,5
ds_read_b64 v[14:15], v5 offset:0
PO 126
M 4,70,4
WL 4
LO 19,19,127
LU 127,19
M 4,71,5
RA 228,1,16384
HS 25,127
M 5,82,4
LL 17,58
LL 105,59
M 5,83,5
LA 17
PK 127,19,125,33
Z 6,12,0
s_add_u32 s42, s44, s65
LC_ 105
PK 125,19,125,32
M 6,13,1
s_addc_u32 s43, s45, 0
PO 127
v_subrev_u32 v5, s39, v5
Z 7,24,0
WL 4
LO 104,16,104
LU 16,104
M 7,25,1
RA 232,1,17408
HZ 24,16
Z 8,36,0
LL 18,60
LL 108,61
M 8,37,1
WL 4
LA 18
ST 124
Z 9,48,0
s_mul_i32 s53, s50, 25600
LC_ 108
v_add_f32 v14, v14, v15
M 9,49,1
s_add_u32 s40, %[wlo], s53
v_add_f32_dpp v14, v14, v14 quad_perm:[1,0,3,2] row_mask:0xf bank_mask:0xf
HX 25
Z 10,60,0
WL 3
LO 105,17,105
LU 17,105
M 10,61,1
s_addc_u32 s41, %[whi], 0
RA 236,1,18432
HS 24,17
Z 11,72,0
LL 19,62
LL 109,63
M 11,73,1
s_mov_b32 m0, s38
LA 19
W1 25,1024
M 6,14,2
LC_ 109
DW 0
M 6,15,3
v_add_f32_dpp v14, v14, v14 quad_perm:[2,3,0,1] row_mask:0xf bank_mask:0xf
v_add_u32 v4, s39, v4
M 7,26,2
WL 4
LO 108,18,108
LU 18,108
M 7,27,3
s_add_u32 s37, s37, 32768
RA 216,1,19456
HS 24,18
M 8,38,2
s_cmp_eq_u32 s37, s75
LL 16,64
LL 112,65
M 8,39,3
s_cselect_b32 s37, s74, s37
LA 16
DW 1024
M 9,50,2
LC_ 112
v_add_f32_dpp v14, v14, v14 row_half_mirror row_mask:0xf bank_mask:0xf
M 9,51,3
v_cvt_f64_f32 v[14:15], v14
M 10,62,2
WL 3
LO 109,19,109
LU 19,109
M 10,63,3
RA 220,1,20480
HS 24,19
M 11,74,2
LL 17,66
LL 113,67
M 11,75,3
LA 17
s_mov_b64 exec, s[54:55]
SP
s_mov_b64 exec, -1
M 6,16,4
LC_ 113
DW 2048
M 6,17,5
M 7,28,4
WL 3
LO 112,16,112
LU 16,112
M 7,29,5
s_mul_i32 s53, s51, 49152
RA 224,1,21504
HS 24,16
M 8,40,4
s_bitcmp1_b64 s[90:91], s51
LL 18,68
LL 116,69
M 8,41,5
s_cselect_b64 s[44:45], s[84:85], s[82:83]
LA 18
M 9,52,4
s_add_u32 s44, s44, s53
LC_ 116
M 9,53,5
s_addc_u32 s45, s45, 0
M 10,64,4
WL 3
LO 113,17,113
LU 17,113
M 10,65,5
s_add_u32 s51, s51, %[d]
RA 228,1,22528
HS 24,17
M 11,76,4
LL 19,70
LL 117,71
M 11,77,5
s_cmp_eq_u32 s51, %[wrap]
LA 19
M 6,18,0
s_cselect_b32 s51, %[rst], s51
LC_ 117
M 6,19,1
s_add_u32 s40, s40, 3072
M 7,30,0
WL 3
LO 116,18,116
LU 18,116
M 7,31,1
s_addc_u32 s41, s41, 0
RA 232,1,23552
HS 24,18
M 8,42,0
s_add_u32 m0, s38, 3072
LL 16,72
LL 120,73
M 8,43,1
LA 16
DW 0
M 9,54,0
LC_ 120
DW 1024
M 9,55,1
M 10,66,0
WL 3
LO 117,19,117
LU 19,117
M 10,67,1
RA 236,1,24576
HS 24,19
M 11,78,0
LL 17,74
LL 121,75
M 11,79,1
LA 17
DW 2048
M 6,20,2
LC_ 121
M 6,21,3
s_lshl_b32 s53, s52, 8
v_add_u32 v1, s37, v0
M 7,32,2
WL 3
LO 120,16,120
LU 16,120
M 7,33,3
s_add_u32 s48, %[alo], s53
RA 216,1,0
HS 24,16
M 8,44,2
s_addc_u32 s49, %[ahi], 0
LL 18,76
LL 124,77
M 8,45,3
s_add_u32 s52, s52, %[d]
LA 18
v_and_b32 v22, 0xfffffcff, v1
M 9,56,2
s_cmp_eq_u32 s52, %[wrap]
LC_ 124
M 9,57,3
s_cselect_b32 s52, %[rst], s52
DQ 3072
M 10,68,2
WL 3
LO 121,17,121
LU 17,121
M 10,69,3
s_add_u32 s50, s50, %[d]
RA 220,1,1024
HS 24,17
M 11,80,2
LL 19,78
LL 125,79
M 11,81,3
s_cmp_eq_u32 s50, %[wrap]
LA 19
RV 252,22,5120
M 6,22,4
s_cselect_b32 s50, %[rst], s50
LC_ 125
M 6,23,5
WV 28
M 7,34,4
WL 4
LO 124,18,124
LU 18,124
M 7,35,5
s_add_u32 s38, s38, 32768
RA 224,1,2048
HS 24,18
M 8,46,4
s_cmp_eq_u32 s38, s77
LL 16,80
LL 106,81
M 8,47,5
s_cselect_b32 s38, s76, s38
LA 16
M 9,58,4
s_barrier
LC_ 106
M 9,59,5
s_sub_u32 s39, 0, s39
M 10,70,4
WL 3
LO 125,19,125
LU 19,125
M 10,71,5
RA 228,1,3072
HS 24,19
M 11,82,4
LL 17,82
LL 107,83
M 11,83,5
s_mov_b32 s66, 30
1:
LA 17
HX 24
Z 0,12,0
LC_ 107
W1 24,0
M 0,13,1
Z 1,24,0
WL 4
LO 16,16,106
LU 106,16
M 1,25,1
RA 232,1,4096
HZ 25,106
Z 2,36,0
LL 18,84
LL 110,85
M 2,37,1
LA 18
PK 106,16,104,33
Z 3,48,0
LC_ 110
PK 104,16,104,32
M 3,49,1
Z 4,60,0
WL 4
LO 17,17,107
LU 107,17
M 4,61,1
RA 236,1,6144
HS 25,107
Z 5,72,0
LL 19,86
LL 111,87
M 5,73,1
s_add_u32 s42, s44, s60
LA 19
PK 107,17,105,33
M 0,14,2
s_addc_u32 s43, s45, 0
LC_ 111
PK 105,17,105,32
M 0,15,3
ST 104
M 1,26,2
WL 3
LO 18,18,110
LU 110,18
M 1,27,3
RA 216,1,7168
HS 25,110
M 2,38,2
LL 16,88
LL 114,89
M 2,39,3
LA 16
PK 110,18,108,33
M 3,50,2
LC_ 114
PK 108,18,108,32
M 3,51,3
PO 110
M 4,62,2
WL 3
LO 19,19,111
LU 111,19
M 4,63,3
RA 220,1,8192
HS 25,111
M 5,74,2
LL 17,90
LL 115,91
M 5,75,3
s_mov_b32 m0, %[pm]
LA 17
PK 111,19,109,33
M 0,16,4
LC_ 115
PK 109,19,109,32
M 0,17,5
DF
PO 111
M 1,28,4
WL 3
LO 16,16,114
LU 114,16
M 1,29,5
s_add_u32 s42, s44, s61
RA 224,1,9216
HS 25,114
M 2,40,4
s_addc_u32 s43, s45, 0
LL 18,92
LL 118,93
M 2,41,5
LA 18
PK 114,16,112,33
M 3,52,4
LC_ 118
PK 112,16,112,32
M 3,53,5
ST 108
M 4,64,4
WL 3
LO 17,17,115
LU 115,17
M 4,65,5
RA 228,1,10240
HS 25,115
M 5,76,4
LL 19,94
LL 119,95
M 5,77,5
s_add_u32 s42, s44, s62
LA 19
PK 115,17,113,33
M 0,18,0
s_addc_u32 s43, s45, 0
LC_ 119
PK 113,17,113,32
M 0,19,1
ST 112
M 1,30,0
WL 3
)"
R"(LO 18,18,118
LU 118,18
M 1,31,1
RA 232,1,11264
HS 25,118
M 2,42,0
LL 16,96
LL 122,97
M 2,43,1
LA 16
PK 118,18,116,33
M 3,54,0
LC_ 122
PK 116,18,116,32
M 3,55,1
RV 252,22,5392
PO 118
M 4,66,0
WL 4
LO 19,19,119
LU 119,19
M 4,67,1
RA 236,1,12288
HS 25,119
M 5,78,0
LL 17,98
LL 123,99
M 5,79,1
LA 17
PK 119,19,117,33
M 0,20,2
s_add_u32 s42, s44, s63
LC_ 123
PK 117,19,117,32
M 0,21,3
s_addc_u32 s43, s45, 0
PO 119
M 1,32,2
WL 4
LO 16,16,122
LU 122,16
M 1,33,3
RA 216,1,13312
HS 25,122
M 2,44,2
LL 18,100
LL 126,101
M 2,45,3
LA 18
PK 122,16,120,33
M 3,56,2
LC_ 126
PK 120,16,120,32
M 3,57,3
ST 116
M 4,68,2
WL 3
LO 17,17,123
LU 123,17
M 4,69,3
RA 220,1,14336
HS 25,123
M 5,80,2
LL 19,102
LL 127,103
M 5,81,3
s_add_u32 s42, s44, s64
LA 19
PK 123,17,121,33
M 0,22,4
s_addc_u32 s43, s45, 0
LC_ 127
PK 121,17,121,32
M 0,23,5
ST 120
M 1,34,4
WL 3
LO 18,18,126
LU 126,18
M 1,35,5
RA 224,1,15360
HS 25,126
M 2,46,4
LL 16,56
LL 104,57
M 2,47,5
LA 16
PK 126,18,124,33
M 3,58,4
LC_ 104
PK 124,18,124,32
M 3,59,5
ds_read_b64 v[14:15], v5 offset:0
PO 126
M 4,70,4
WL 4
LO 19,19,127
LU 127,19
M 4,71,5
RA 228,1,16384
HS 25,127
M 5,82,4
LL 17,58
LL 105,59
M 5,83,5
LA 17
PK 127,19,125,33
Z 6,12,0
s_add_u32 s42, s44, s65
LC_ 105
PK 125,19,125,32
M 6,13,1
s_addc_u32 s43, s45, 0
PO 127
v_subrev_u32 v5, s39, v5
Z 7,24,0
WL 4
LO 104,16,104
LU 16,104
M 7,25,1
RA 232,1,17408
HZ 24,16
Z 8,36,0
LL 18,60
LL 108,61
M 8,37,1
WL 4
LA 18
ST 124
Z 9,48,0
s_mul_i32 s53, s50, 25600
LC_ 108
v_add_f32 v14, v14, v15
M 9,49,1
s_add_u32 s40, %[wlo], s53
v_add_f32_dpp v14, v14, v14 quad_perm:[1,0,3,2] row_mask:0xf bank_mask:0xf
HX 25
Z 10,60,0
WL 3
LO 105,17,105
LU 17,105
M 10,61,1
s_addc_u32 s41, %[whi], 0
RA 236,1,18432
HS 24,17
Z 11,72,0
LL 19,62
LL 109,63
M 11,73,1
s_mov_b32 m0, s38
LA 19
W1 25,1024
M 6,14,2
LC_ 109
DW 0
M 6,15,3
v_add_f32_dpp v14, v14, v14 quad_perm:[2,3,0,1] row_mask:0xf bank_mask:0xf
v_add_u32 v4, s39, v4
M 7,26,2
WL 4
LO 108,18,108
LU 18,108
M 7,27,3
s_add_u32 s37, s37, 32768
RA 216,1,19456
HS 24,18
M 8,38,2
s_cmp_eq_u32 s37, s75
LL 16,64
LL 112,65
M 8,39,3
s_cselect_b32 s37, s74, s37
LA 16
DW 1024
M 9,50,2
LC_ 112
v_add_f32_dpp v14, v14, v14 row_half_mirror row_mask:0xf bank_mask:0xf
M 9,51,3
v_cvt_f64_f32 v[14:15], v14
M 10,62,2
WL 3
LO 109,19,109
LU 19,109
M 10,63,3
RA 220,1,20480
HS 24,19
M 11,74,2
LL 17,66
LL 113,67
M 11,75,3
LA 17
s_mov_b64 exec, s[54:55]
SP
s_mov_b64 exec, -1
M 6,16,4
LC_ 113
DW 2048
M 6,17,5
M 7,28,4
WL 3
LO 112,16,112
LU 16,112
M 7,29,5
s_mul_i32 s53, s51, 49152
RA 224,1,21504
HS 24,16
M 8,40,4
s_bitcmp1_b64 s[90:91], s51
LL 18,68
LL 116,69
M 8,41,5
s_cselect_b64 s[44:45], s[84:85], s[82:83]
LA 18
M 9,52,4
s_add_u32 s44, s44, s53
LC_ 116
M 9,53,5
s_addc_u32 s45, s45, 0
M 10,64,4
WL 3
LO 113,17,113
LU 17,113
M 10,65,5
s_add_u32 s51, s51, %[d]
RA 228,1,22528
HS 24,17
M 11,76,4
LL 19,70
LL 117,71
M 11,77,5
s_cmp_eq_u32 s51, %[wrap]
LA 19
M 6,18,0
s_cselect_b32 s51, %[rst], s51
LC_ 117
M 6,19,1
s_add_u32 s40, s40, 3072
M 7,30,0
WL 3
LO 116,18,116
LU 18,116
M 7,31,1
s_addc_u32 s41, s41, 0
RA 232,1,23552
HS 24,18
M 8,42,0
s_add_u32 m0, s38, 3072
LL 16,72
LL 120,73
M 8,43,1
LA 16
DW 0
M 9,54,0
LC_ 120
DW 1024
M 9,55,1
M 10,66,0
WL 3
LO 117,19,117
LU 19,117
M 10,67,1
RA 236,1,24576
HS 24,19
M 11,78,0
LL 17,74
LL 121,75
M 11,79,1
LA 17
DW 2048
M 6,20,2
LC_ 121
M 6,21,3
s_lshl_b32 s53, s52, 8
v_add_u32 v1, s37, v0
M 7,32,2
WL 3
LO 120,16,120
LU 16,120
M 7,33,3
s_add_u32 s48, %[alo], s53
RA 216,1,0
HS 24,16
M 8,44,2
s_addc_u32 s49, %[ahi], 0
LL 18,76
LL 124,77
M 8,45,3
s_add_u32 s52, s52, %[d]
LA 18
v_and_b32 v22, 0xfffffcff, v1
M 9,56,2
s_cmp_eq_u32 s52, %[wrap]
LC_ 124
M 9,57,3
s_cselect_b32 s52, %[rst], s52
DQ 3072
M 10,68,2
WL 3
LO 121,17,121
LU 17,121
M 10,69,3
s_add_u32 s50, s50, %[d]
RA 220,1,1024
HS 24,17
M 11,80,2
LL 19,78
LL 125,79
M 11,81,3
s_cmp_eq_u32 s50, %[wrap]
LA 19
RV 252,22,5120
M 6,22,4
s_cselect_b32 s50, %[rst], s50
LC_ 125
M 6,23,5
WV 30
M 7,34,4
WL 4
LO 124,18,124
LU 18,124
M 7,35,5
s_add_u32 s38, s38, 32768
RA 224,1,2048
HS 24,18
M 8,46,4
s_cmp_eq_u32 s38, s77
LL 16,80
LL 106,81
M 8,47,5
s_cselect_b32 s38, s76, s38
LA 16
M 9,58,4
s_barrier
LC_ 106
M 9,59,5
s_sub_u32 s39, 0, s39
M 10,70,4
WL 3
LO 125,19,125
LU 19,125
M 10,71,5
RA 228,1,3072
HS 24,19
M 11,82,4
s_sub_u32 s66, s66, 1
LL 17,82
LL 107,83
M 11,83,5
s_cbranch_scc0 1b
7:
LA 17
HX 24
Z 0,12,0
LC_ 107
W1 24,0
M 0,13,1
Z 1,24,0
WL 4
LO 16,16,106
LU 106,16
M 1,25,1
RA 232,1,4096
HZ 25,106
Z 2,36,0
LL 18,84
LL 110,85
M 2,37,1
LA 18
PK 106,16,104,33
Z 3,48,0
LC_ 110
PK 104,16,104,32
M 3,49,1
Z 4,60,0
WL 4
LO 17,17,107
LU 107,17
M 4,61,1
RA 236,1,6144
HS 25,107
Z 5,72,0
LL 19,86
LL 111,87
M 5,73,1
s_add_u32 s42, s44, s60
LA 19
PK 107,17,105,33
M 0,14,2
s_addc_u32 s43, s45, 0
LC_ 111
PK 105,17,105,32
M 0,15,3
ST 104
M 1,26,2
WL 3
LO 18,18,110
LU 110,18
M 1,27,3
RA 216,1,7168
HS 25,110
)"
R"(M 2,38,2
LL 16,88
LL 114,89
M 2,39,3
LA 16
PK 110,18,108,33
M 3,50,2
LC_ 114
PK 108,18,108,32
M 3,51,3
PO 110
M 4,62,2
WL 3
LO 19,19,111
LU 111,19
M 4,63,3
RA 220,1,8192
HS 25,111
M 5,74,2
LL 17,90
LL 115,91
M 5,75,3
s_mov_b32 m0, %[pm]
LA 17
PK 111,19,109,33
M 0,16,4
LC_ 115
PK 109,19,109,32
M 0,17,5
DF
PO 111
M 1,28,4
WL 3
LO 16,16,114
LU 114,16
M 1,29,5
s_add_u32 s42, s44, s61
RA 224,1,9216
HS 25,114
M 2,40,4
s_addc_u32 s43, s45, 0
LL 18,92
LL 118,93
M 2,41,5
LA 18
PK 114,16,112,33
M 3,52,4
LC_ 118
PK 112,16,112,32
M 3,53,5
ST 108
M 4,64,4
WL 3
LO 17,17,115
LU 115,17
M 4,65,5
RA 228,1,10240
HS 25,115
M 5,76,4
LL 19,94
LL 119,95
M 5,77,5
s_add_u32 s42, s44, s62
LA 19
PK 115,17,113,33
M 0,18,0
s_addc_u32 s43, s45, 0
LC_ 119
PK 113,17,113,32
M 0,19,1
ST 112
M 1,30,0
WL 3
LO 18,18,118
LU 118,18
M 1,31,1
RA 232,1,11264
HS 25,118
M 2,42,0
LL 16,96
LL 122,97
M 2,43,1
LA 16
PK 118,18,116,33
M 3,54,0
LC_ 122
PK 116,18,116,32
M 3,55,1
RV 252,22,5392
PO 118
M 4,66,0
WL 4
LO 19,19,119
LU 119,19
M 4,67,1
RA 236,1,12288
HS 25,119
M 5,78,0
LL 17,98
LL 123,99
M 5,79,1
LA 17
PK 119,19,117,33
M 0,20,2
s_add_u32 s42, s44, s63
LC_ 123
PK 117,19,117,32
M 0,21,3
s_addc_u32 s43, s45, 0
PO 119
M 1,32,2
WL 4
LO 16,16,122
LU 122,16
M 1,33,3
RA 216,1,13312
HS 25,122
M 2,44,2
LL 18,100
LL 126,101
M 2,45,3
LA 18
PK 122,16,120,33
M 3,56,2
LC_ 126
PK 120,16,120,32
M 3,57,3
ST 116
M 4,68,2
WL 3
LO 17,17,123
LU 123,17
M 4,69,3
RA 220,1,14336
HS 25,123
M 5,80,2
LL 19,102
LL 127,103
M 5,81,3
s_add_u32 s42, s44, s64
LA 19
PK 123,17,121,33
M 0,22,4
s_addc_u32 s43, s45, 0
LC_ 127
PK 121,17,121,32
M 0,23,5
ST 120
M 1,34,4
WL 3
LO 18,18,126
LU 126,18
M 1,35,5
RA 224,1,15360
HS 25,126
M 2,46,4
LL 16,56
LL 104,57
M 2,47,5
LA 16
PK 126,18,124,33
M 3,58,4
LC_ 104
PK 124,18,124,32
M 3,59,5
ds_read_b64 v[14:15], v5 offset:0
PO 126
M 4,70,4
WL 4
LO 19,19,127
LU 127,19
M 4,71,5
RA 228,1,16384
HS 25,127
M 5,82,4
LL 17,58
LL 105,59
M 5,83,5
LA 17
PK 127,19,125,33
Z 6,12,0
s_add_u32 s42, s44, s65
LC_ 105
PK 125,19,125,32
M 6,13,1
s_addc_u32 s43, s45, 0
PO 127
v_subrev_u32 v5, s39, v5
Z 7,24,0
WL 4
LO 104,16,104
LU 16,104
M 7,25,1
RA 232,1,17408
HZ 24,16
Z 8,36,0
LL 18,60
LL 108,61
M 8,37,1
WL 4
LA 18
ST 124
Z 9,48,0
s_mul_i32 s53, s50, 25600
LC_ 108
v_add_f32 v14, v14, v15
M 9,49,1
s_add_u32 s40, %[wlo], s53
v_add_f32_dpp v14, v14, v14 quad_perm:[1,0,3,2] row_mask:0xf bank_mask:0xf
HX 25
Z 10,60,0
WL 3
LO 105,17,105
LU 17,105
M 10,61,1
s_addc_u32 s41, %[whi], 0
RA 236,1,18432
HS 24,17
Z 11,72,0
LL 19,62
LL 109,63
M 11,73,1
s_mov_b32 m0, s38
LA 19
W1 25,1024
M 6,14,2
LC_ 109
DW 0
M 6,15,3
v_add_f32_dpp v14, v14, v14 quad_perm:[2,3,0,1] row_mask:0xf bank_mask:0xf
v_add_u32 v4, s39, v4
M 7,26,2
WL 4
LO 108,18,108
LU 18,108
M 7,27,3
s_add_u32 s37, s37, 32768
RA 216,1,19456
HS 24,18
M 8,38,2
s_cmp_eq_u32 s37, s75
LL 16,64
LL 112,65
s_mov_b64 exec, 1
AT
s_mov_b64 exec, -1
M 8,39,3
s_cselect_b32 s37, s74, s37
LA 16
DW 1024
M 9,50,2
LC_ 112
v_add_f32_dpp v14, v14, v14 row_half_mirror row_mask:0xf bank_mask:0xf
M 9,51,3
v_cvt_f64_f32 v[14:15], v14
M 10,62,2
WL 3
LO 109,19,109
LU 19,109
M 10,63,3
RA 220,1,20480
HS 24,19
M 11,74,2
LL 17,66
LL 113,67
M 11,75,3
LA 17
s_mov_b64 exec, s[54:55]
SP
s_mov_b64 exec, -1
M 6,16,4
LC_ 113
DW 2048
M 6,17,5
M 7,28,4
WL 3
LO 112,16,112
LU 16,112
M 7,29,5
s_mul_i32 s53, s51, 49152
RA 224,1,21504
HS 24,16
M 8,40,4
s_bitcmp1_b64 s[90:91], s51
LL 18,68
LL 116,69
M 8,41,5
s_cselect_b64 s[44:45], s[84:85], s[82:83]
LA 18
M 9,52,4
s_add_u32 s44, s44, s53
LC_ 116
M 9,53,5
s_addc_u32 s45, s45, 0
M 10,64,4
WL 3
LO 113,17,113
LU 17,113
M 10,65,5
s_add_u32 s51, s51, %[d]
RA 228,1,22528
HS 24,17
M 11,76,4
LL 19,70
LL 117,71
M 11,77,5
s_cmp_eq_u32 s51, %[wrap]
LA 19
M 6,18,0
s_cselect_b32 s51, %[rst], s51
LC_ 117
M 6,19,1
s_add_u32 s40, s40, 3072
M 7,30,0
WL 3
LO 116,18,116
LU 18,116
M 7,31,1
s_addc_u32 s41, s41, 0
RA 232,1,23552
HS 24,18
M 8,42,0
s_add_u32 m0, s38, 3072
LL 16,72
LL 120,73
M 8,43,1
LA 16
DW 0
M 9,54,0
LC_ 120
DW 1024
M 9,55,1
M 10,66,0
WL 3
LO 117,19,117
LU 19,117
M 10,67,1
RA 236,1,24576
HS 24,19
M 11,78,0
LL 17,74
LL 121,75
M 11,79,1
LA 17
DW 2048
M 6,20,2
LC_ 121
M 6,21,3
s_lshl_b32 s53, s52, 8
v_add_u32 v1, s37, v0
M 7,32,2
WL 3
LO 120,16,120
LU 16,120
M 7,33,3
s_add_u32 s48, %[alo], s53
RA 216,1,0
HS 24,16
M 8,44,2
s_addc_u32 s49, %[ahi], 0
LL 18,76
LL 124,77
M 8,45,3
s_add_u32 s52, s52, %[d]
LA 18
v_and_b32 v22, 0xfffffcff, v1
M 9,56,2
s_cmp_eq_u32 s52, %[wrap]
LC_ 124
M 9,57,3
s_cselect_b32 s52, %[rst], s52
DQ 3072
M 10,68,2
WL 3
LO 121,17,121
LU 17,121
M 10,69,3
s_add_u32 s50, s50, %[d]
RA 220,1,1024
HS 24,17
M 11,80,2
LL 19,78
LL 125,79
M 11,81,3
s_cmp_eq_u32 s50, %[wrap]
LA 19
RV 252,22,5120
M 6,22,4
s_cselect_b32 s50, %[rst], s50
LC_ 125
M 6,23,5
WV 31
M 7,34,4
WL 4
LO 124,18,124
LU 18,124
M 7,35,5
s_add_u32 s38, s38, 32768
RA 224,1,2048
HS 24,18
)"
R"(M 8,46,4
s_cmp_eq_u32 s38, s77
LL 16,80
LL 106,81
M 8,47,5
s_cselect_b32 s38, s76, s38
LA 16
M 9,58,4
s_barrier
LC_ 106
M 9,59,5
s_sub_u32 s39, 0, s39
M 10,70,4
WL 3
LO 125,19,125
LU 19,125
M 10,71,5
RA 228,1,3072
HS 24,19
M 11,82,4
LL 17,82
LL 107,83
M 11,83,5
LA 17
HX 24
Z 0,12,0
LC_ 107
W1 24,0
M 0,13,1
Z 1,24,0
WL 4
LO 16,16,106
LU 106,16
M 1,25,1
RA 232,1,4096
HZ 25,106
Z 2,36,0
LL 18,84
LL 110,85
M 2,37,1
LA 18
PK 106,16,104,33
Z 3,48,0
LC_ 110
PK 104,16,104,32
M 3,49,1
Z 4,60,0
WL 4
LO 17,17,107
LU 107,17
M 4,61,1
RA 236,1,6144
HS 25,107
Z 5,72,0
LL 19,86
LL 111,87
M 5,73,1
s_add_u32 s42, s44, s60
LA 19
PK 107,17,105,33
M 0,14,2
s_addc_u32 s43, s45, 0
LC_ 111
PK 105,17,105,32
M 0,15,3
ST 104
M 1,26,2
WL 3
LO 18,18,110
LU 110,18
M 1,27,3
RA 216,1,7168
HS 25,110
M 2,38,2
LL 16,88
LL 114,89
M 2,39,3
LA 16
PK 110,18,108,33
M 3,50,2
LC_ 114
PK 108,18,108,32
M 3,51,3
PO 110
M 4,62,2
WL 3
LO 19,19,111
LU 111,19
M 4,63,3
RA 220,1,8192
HS 25,111
M 5,74,2
LL 17,90
LL 115,91
M 5,75,3
s_mov_b32 m0, %[pm]
LA 17
PK 111,19,109,33
M 0,16,4
LC_ 115
PK 109,19,109,32
M 0,17,5
DF
PO 111
M 1,28,4
WL 3
LO 16,16,114
LU 114,16
M 1,29,5
s_add_u32 s42, s44, s61
RA 224,1,9216
HS 25,114
M 2,40,4
s_addc_u32 s43, s45, 0
LL 18,92
LL 118,93
M 2,41,5
LA 18
PK 114,16,112,33
M 3,52,4
LC_ 118
PK 112,16,112,32
M 3,53,5
ST 108
M 4,64,4
WL 3
LO 17,17,115
LU 115,17
M 4,65,5
RA 228,1,10240
HS 25,115
M 5,76,4
LL 19,94
LL 119,95
M 5,77,5
s_add_u32 s42, s44, s62
LA 19
PK 115,17,113,33
M 0,18,0
s_addc_u32 s43, s45, 0
LC_ 119
PK 113,17,113,32
M 0,19,1
ST 112
M 1,30,0
WL 3
LO 18,18,118
LU 118,18
M 1,31,1
RA 232,1,11264
HS 25,118
M 2,42,0
LL 16,96
LL 122,97
M 2,43,1
LA 16
PK 118,18,116,33
M 3,54,0
LC_ 122
PK 116,18,116,32
M 3,55,1
RV 252,22,5392
PO 118
M 4,66,0
WL 4
LO 19,19,119
LU 119,19
M 4,67,1
RA 236,1,12288
HS 25,119
M 5,78,0
LL 17,98
LL 123,99
M 5,79,1
LA 17
PK 119,19,117,33
M 0,20,2
s_add_u32 s42, s44, s63
LC_ 123
PK 117,19,117,32
M 0,21,3
s_addc_u32 s43, s45, 0
PO 119
M 1,32,2
WL 4
LO 16,16,122
LU 122,16
M 1,33,3
RA 216,1,13312
HS 25,122
M 2,44,2
LL 18,100
LL 126,101
M 2,45,3
LA 18
PK 122,16,120,33
M 3,56,2
LC_ 126
PK 120,16,120,32
M 3,57,3
ST 116
M 4,68,2
WL 3
LO 17,17,123
LU 123,17
M 4,69,3
RA 220,1,14336
HS 25,123
M 5,80,2
LL 19,102
LL 127,103
M 5,81,3
s_add_u32 s42, s44, s64
LA 19
PK 123,17,121,33
M 0,22,4
s_addc_u32 s43, s45, 0
LC_ 127
PK 121,17,121,32
M 0,23,5
ST 120
M 1,34,4
WL 3
LO 18,18,126
LU 126,18
M 1,35,5
RA 224,1,15360
HS 25,126
M 2,46,4
LL 16,56
LL 104,57
M 2,47,5
LA 16
PK 126,18,124,33
M 3,58,4
LC_ 104
PK 124,18,124,32
M 3,59,5
ds_read_b64 v[14:15], v5 offset:0
PO 126
M 4,70,4
WL 4
LO 19,19,127
LU 127,19
M 4,71,5
RA 228,1,16384
HS 25,127
M 5,82,4
LL 17,58
LL 105,59
M 5,83,5
LA 17
PK 127,19,125,33
Z 6,12,0
s_add_u32 s42, s44, s65
LC_ 105
PK 125,19,125,32
M 6,13,1
s_addc_u32 s43, s45, 0
PO 127
v_subrev_u32 v5, s39, v5
Z 7,24,0
WL 4
LO 104,16,104
LU 16,104
M 7,25,1
RA 232,1,17408
HZ 24,16
Z 8,36,0
LL 18,60
LL 108,61
M 8,37,1
WL 4
LA 18
ST 124
Z 9,48,0
s_mul_i32 s53, s50, 25600
LC_ 108
v_add_f32 v14, v14, v15
M 9,49,1
s_add_u32 s40, %[wlo], s53
v_add_f32_dpp v14, v14, v14 quad_perm:[1,0,3,2] row_mask:0xf bank_mask:0xf
HX 25
Z 10,60,0
WL 3
LO 105,17,105
LU 17,105
M 10,61,1
s_addc_u32 s41, %[whi], 0
RA 236,1,18432
HS 24,17
Z 11,72,0
LL 19,62
LL 109,63
M 11,73,1
s_mov_b32 m0, s38
LA 19
W1 25,1024
M 6,14,2
LC_ 109
DW 0
M 6,15,3
WV 15
v_add_f32_dpp v14, v14, v14 quad_perm:[2,3,0,1] row_mask:0xf bank_mask:0xf
v_readfirstlane_b32 s72, v11
M 7,26,2
WL 4
LO 108,18,108
LU 18,108
M 7,27,3
s_add_u32 s37, s37, 32768
RA 216,1,19456
HS 24,18
M 8,38,2
s_cmp_eq_u32 s37, s75
LL 16,64
LL 112,65
v_mov_b32 v12, s72
M 8,39,3
s_cselect_b32 s37, s74, s37
LA 16
W2 12,12,16
M 9,50,2
LC_ 112
DW 1024
M 9,51,3
v_add_f32_dpp v14, v14, v14 row_half_mirror row_mask:0xf bank_mask:0xf
v_add_u32 v4, s39, v4
M 10,62,2
WL 4
LO 109,19,109
LU 19,109
M 10,63,3
RA 220,1,20480
HS 24,19
M 11,74,2
LL 17,66
LL 113,67
M 11,75,3
LA 17
DW 2048
M 6,16,4
LC_ 113
v_cvt_f64_f32 v[14:15], v14
M 6,17,5
s_mov_b64 exec, s[54:55]
SP
s_mov_b64 exec, -1
M 7,28,4
WL 3
LO 112,16,112
LU 16,112
M 7,29,5
s_mul_i32 s53, s51, 49152
RA 224,1,21504
HS 24,16
M 8,40,4
s_bitcmp1_b64 s[90:91], s51
LL 18,68
LL 116,69
M 8,41,5
s_cselect_b64 s[44:45], s[84:85], s[82:83]
LA 18
M 9,52,4
s_add_u32 s44, s44, s53
LC_ 116
M 9,53,5
s_addc_u32 s45, s45, 0
M 10,64,4
WL 3
LO 113,17,113
LU 17,113
M 10,65,5
s_add_u32 s51, s51, %[d]
RA 228,1,22528
HS 24,17
M 11,76,4
LL 19,70
LL 117,71
M 11,77,5
s_cmp_eq_u32 s51, %[wrap]
LA 19
M 6,18,0
s_cselect_b32 s51, %[rst], s51
LC_ 117
M 6,19,1
s_add_u32 s40, s40, 3072
M 7,30,0
WL 3
LO 116,18,116
LU 18,116
M 7,31,1
s_addc_u32 s41, s41, 0
RA 232,1,23552
HS 24,18
M 8,42,0
s_add_u32 m0, s38, 3072
LL 16,72
LL 120,73
M 8,43,1
)"
R"(LA 16
DW 0
M 9,54,0
LC_ 120
DW 1024
M 9,55,1
M 10,66,0
WL 3
LO 117,19,117
LU 19,117
M 10,67,1
RA 236,1,24576
HS 24,19
M 11,78,0
LL 17,74
LL 121,75
M 11,79,1
LA 17
DW 2048
M 6,20,2
LC_ 121
M 6,21,3
s_lshl_b32 s53, s52, 8
v_add_u32 v1, s37, v0
M 7,32,2
WL 3
LO 120,16,120
LU 16,120
M 7,33,3
s_add_u32 s48, %[alo], s53
RA 216,1,0
HS 24,16
M 8,44,2
s_addc_u32 s49, %[ahi], 0
LL 18,76
LL 124,77
M 8,45,3
s_add_u32 s52, s52, %[d]
LA 18
v_and_b32 v22, 0xfffffcff, v1
M 9,56,2
s_cmp_eq_u32 s52, %[wrap]
LC_ 124
M 9,57,3
s_cselect_b32 s52, %[rst], s52
DQ 3072
M 10,68,2
WL 3
LO 121,17,121
LU 17,121
M 10,69,3
s_add_u32 s50, s50, %[d]
RA 220,1,1024
HS 24,17
M 11,80,2
LL 19,78
LL 125,79
M 11,81,3
s_cmp_eq_u32 s50, %[wrap]
LA 19
RV 252,22,5120
M 6,22,4
s_cselect_b32 s50, %[rst], s50
LC_ 125
M 6,23,5
M 7,34,4
WL 4
LO 124,18,124
LU 18,124
M 7,35,5
s_add_u32 s38, s38, 32768
RA 224,1,2048
HS 24,18
M 8,46,4
s_cmp_eq_u32 s38, s77
LL 16,80
LL 106,81
M 8,47,5
s_cselect_b32 s38, s76, s38
LA 16
M 9,58,4
s_barrier
LC_ 106
M 9,59,5
s_sub_u32 s39, 0, s39
M 10,70,4
WL 3
LO 125,19,125
LU 19,125
M 10,71,5
RA 228,1,3072
HS 24,19
M 11,82,4
LL 17,82
LL 107,83
M 11,83,5
LA 17
HX 24
Z 0,12,0
LC_ 107
ds_read_b32 v20, v5 offset:4096
M 0,13,1
W1 24,0
Z 1,24,0
WL 5
LO 16,16,106
LU 106,16
M 1,25,1
RA 232,1,4096
HZ 25,106
Z 2,36,0
LL 18,84
LL 110,85
M 2,37,1
LA 18
PK 106,16,104,33
Z 3,48,0
LC_ 110
PK 104,16,104,32
M 3,49,1
Z 4,60,0
WL 5
LO 17,17,107
LU 107,17
M 4,61,1
RA 236,1,6144
HS 25,107
Z 5,72,0
LL 19,86
LL 111,87
M 5,73,1
s_add_u32 s42, s44, s60
LA 19
PK 107,17,105,33
M 0,14,2
s_addc_u32 s43, s45, 0
LC_ 111
PK 105,17,105,32
M 0,15,3
WL 7
ST 104
v_readfirstlane_b32 s72, v20
M 1,26,2
WL 3
LO 18,18,110
LU 110,18
M 1,27,3
s_sub_u32 s53, 63, s72
RA 216,1,7168
HS 25,110
M 2,38,2
s_cmp_eq_u32 %[d], 1
LL 16,88
LL 114,89
M 2,39,3
s_cselect_b32 s72, s72, s53
LA 16
PK 110,18,108,33
M 3,50,2
s_and_b32 s72, s72, 63
LC_ 114
PK 108,18,108,32
M 3,51,3
s_add_u32 s53, s72, 128
PO 110
M 4,62,2
WL 3
LO 19,19,111
LU 111,19
M 4,63,3
s_mul_i32 s53, s53, 2359296
RA 220,1,8192
HS 25,111
M 5,74,2
LL 17,90
LL 115,91
M 5,75,3
s_add_u32 s86, %[hlo], s53
LA 17
PK 111,19,109,33
M 0,16,4
s_addc_u32 s87, %[hhi], 0
LC_ 115
PK 109,19,109,32
M 0,17,5
s_add_u32 s88, s86, 8192
PO 111
M 1,28,4
WL 3
LO 16,16,114
LU 114,16
M 1,29,5
s_addc_u32 s89, s87, 0
RA 224,1,9216
HS 25,114
M 2,40,4
s_and_b32 s92, %[hlo], 0x1fffff
LL 18,92
LL 118,93
M 2,41,5
s_add_u32 s92, s92, s53
LA 18
PK 114,16,112,33
M 3,52,4
s_mov_b32 s93, 3072
LC_ 118
PK 112,16,112,32
M 3,53,5
s_mul_i32 s57, s72, 589824
M 4,64,4
WL 3
LO 17,17,115
LU 115,17
M 4,65,5
s_add_u32 s57, s57, %[xs1]
RA 228,1,10240
HS 25,115
M 5,76,4
LL 19,94
LL 119,95
M 5,77,5
s_lshl_b32 s72, s72, 1
LA 19
PK 115,17,113,33
M 0,18,0
s_add_u32 s72, s72, s96
LC_ 119
PK 113,17,113,32
M 0,19,1
s_mov_b32 m0, %[pm]
s_nop 0
DF
M 1,30,0
WL 3
LO 18,18,118
LU 118,18
M 1,31,1
s_add_u32 s42, s44, s61
RA 232,1,11264
HS 25,118
s_addc_u32 s43, s45, 0
s_lshl_b32 s53, s72, 6
s_add_u32 s53, s53, 128
s_cmp_lt_u32 %[ca0], 128
s_cselect_b32 s73, s53, %[ca0]
M 2,42,0
LL 16,96
LL 122,97
M 2,43,1
LA 16
PK 118,18,116,33
ST 108
M 3,54,0
LC_ 122
PK 116,18,116,32
v_mov_b32 v9, s73
M 3,55,1
RV 252,22,5392
PO 118
M 4,66,0
WL 4
LO 19,19,119
LU 119,19
M 4,67,1
RA 236,1,12288
HS 25,119
v_mov_b32 v11, s92
M 5,78,0
LL 17,98
LL 123,99
M 5,79,1
LA 17
PK 119,19,117,33
M 0,20,2
LC_ 123
PK 117,19,117,32
v_mad_u32_u24 v11, v0, s93, v11
M 0,21,3
s_add_u32 s42, s44, s62
PO 119
M 1,32,2
WL 4
LO 16,16,122
LU 122,16
s_addc_u32 s43, s45, 0
M 1,33,3
RA 216,1,13312
HS 25,122
v_lshrrev_b32 v11, 21, v11
M 2,44,2
LL 18,100
LL 126,101
ST 112
M 2,45,3
LA 18
PK 122,16,120,33
M 3,56,2
LC_ 126
PK 120,16,120,32
v_lshl_add_u32 v11, v11, 2, 64
M 3,57,3
v_writelane_b32 v11, %[tsel], 63
M 4,68,2
WL 3
LO 17,17,123
LU 123,17
M 4,69,3
s_sub_u32 s53, s86, %[hlo]
RA 220,1,14336
HS 25,123
M 5,80,2
LL 19,102
LL 127,103
M 5,81,3
s_lshr_b32 s53, s53, 2
LA 19
PK 123,17,121,33
M 0,22,4
s_and_b32 s92, %[xlo], 0x1fffff
LC_ 127
PK 121,17,121,32
s_add_u32 s53, s53, s92
s_add_u32 s53, s53, 0x20048000
s_lshr_b32 s53, s53, 21
s_lshl_b32 s92, s53, 2
M 0,23,5
s_add_u32 s42, s44, s63
M 1,34,4
WL 3
LO 18,18,126
LU 126,18
M 1,35,5
s_addc_u32 s43, s45, 0
RA 224,1,15360
HS 25,126
v_writelane_b32 v11, s92, 48
M 2,46,4
LL 16,56
LL 104,57
M 2,47,5
LA 16
PK 126,18,124,33
ST 116
M 3,58,4
LC_ 104
PK 124,18,124,32
global_load_dword v11, v11, s[68:69] offset:-2048
M 3,59,5
s_add_u32 s42, s44, s64
ds_read_b64 v[14:15], v5 offset:0
PO 126
M 4,70,4
WL 4
LO 19,19,127
LU 127,19
M 4,71,5
s_addc_u32 s43, s45, 0
RA 228,1,16384
HS 25,127
M 5,82,4
LL 17,58
LL 105,59
M 5,83,5
LA 17
PK 127,19,125,33
Z 6,12,0
LC_ 105
PK 125,19,125,32
M 6,13,1
ST 120
PO 127
Z 7,24,0
WL 4
LO 104,16,104
LU 16,104
M 7,25,1
s_add_u32 s42, s44, s65
RA 232,1,17408
HZ 24,16
Z 8,36,0
s_addc_u32 s43, s45, 0
LL 18,60
LL 108,61
M 8,37,1
WL 4
LA 18
ST 124
Z 9,48,0
s_mul_i32 s53, s50, 25600
LC_ 108
v_subrev_u32 v5, s39, v5
M 9,49,1
s_add_u32 s40, %[wlo], s53
v_add_f32 v14, v14, v15
HX 25
Z 10,60,0
WL 3
LO 105,17,105
LU 17,105
M 10,61,1
)"
R"(s_addc_u32 s41, %[whi], 0
RA 236,1,18432
HS 24,17
Z 11,72,0
LL 19,62
LL 109,63
M 11,73,1
s_mov_b32 m0, s38
LA 19
W1 25,1024
M 6,14,2
LC_ 109
DW 0
M 6,15,3
v_add_f32_dpp v14, v14, v14 quad_perm:[1,0,3,2] row_mask:0xf bank_mask:0xf
v_add_u32 v4, s39, v4
M 7,26,2
WL 4
LO 108,18,108
LU 18,108
M 7,27,3
s_add_u32 s37, s37, 32768
RA 216,1,19456
HS 24,18
M 8,38,2
s_cmp_eq_u32 s37, s75
LL 16,64
LL 112,65
M 8,39,3
s_cselect_b32 s37, s74, s37
LA 16
DW 1024
M 9,50,2
LC_ 112
v_add_f32_dpp v14, v14, v14 quad_perm:[2,3,0,1] row_mask:0xf bank_mask:0xf
M 9,51,3
s_nop 0
v_add_f32_dpp v14, v14, v14 row_half_mirror row_mask:0xf bank_mask:0xf
M 10,62,2
WL 3
LO 109,19,109
LU 19,109
M 10,63,3
RA 220,1,20480
HS 24,19
M 11,74,2
LL 17,66
LL 113,67
M 11,75,3
LA 17
DW 2048
M 6,16,4
LC_ 113
v_cvt_f64_f32 v[14:15], v14
M 6,17,5
s_mov_b64 exec, s[54:55]
SP
s_mov_b64 exec, -1
M 7,28,4
WL 3
LO 112,16,112
LU 16,112
M 7,29,5
s_mul_i32 s53, s51, 49152
RA 224,1,21504
HS 24,16
M 8,40,4
s_bitcmp1_b64 s[90:91], s51
LL 18,68
LL 116,69
M 8,41,5
s_cselect_b64 s[44:45], s[84:85], s[82:83]
LA 18
M 9,52,4
s_add_u32 s44, s44, s53
LC_ 116
M 9,53,5
s_addc_u32 s45, s45, 0
M 10,64,4
WL 3
LO 113,17,113
LU 17,113
M 10,65,5
s_add_u32 s51, s51, %[d]
RA 228,1,22528
HS 24,17
M 11,76,4
LL 19,70
LL 117,71
M 11,77,5
s_cmp_eq_u32 s51, %[wrap]
LA 19
M 6,18,0
s_cselect_b32 s51, %[rst], s51
LC_ 117
M 6,19,1
s_add_u32 s40, s40, 3072
M 7,30,0
WL 3
LO 116,18,116
LU 18,116
M 7,31,1
s_addc_u32 s41, s41, 0
RA 232,1,23552
HS 24,18
M 8,42,0
s_add_u32 m0, s38, 3072
LL 16,72
LL 120,73
M 8,43,1
LA 16
DW 0
M 9,54,0
LC_ 120
DW 1024
M 9,55,1
M 10,66,0
WL 3
LO 117,19,117
LU 19,117
M 10,67,1
RA 236,1,24576
HS 24,19
M 11,78,0
LL 17,74
LL 121,75
M 11,79,1
LA 17
DW 2048
M 6,20,2
LC_ 121
M 6,21,3
s_lshl_b32 s53, s52, 8
v_add_u32 v1, s37, v0
M 7,32,2
WL 3
LO 120,16,120
LU 16,120
M 7,33,3
s_add_u32 s48, %[alo], s53
RA 216,1,0
HS 24,16
M 8,44,2
s_addc_u32 s49, %[ahi], 0
LL 18,76
LL 124,77
M 8,45,3
s_add_u32 s52, s52, %[d]
LA 18
v_and_b32 v22, 0xfffffcff, v1
M 9,56,2
s_cmp_eq_u32 s52, %[wrap]
LC_ 124
M 9,57,3
s_cselect_b32 s52, %[rst], s52
DQ 3072
M 10,68,2
WL 3
LO 121,17,121
LU 17,121
M 10,69,3
s_add_u32 s50, s50, %[d]
RA 220,1,1024
HS 24,17
M 11,80,2
LL 19,78
LL 125,79
M 11,81,3
s_cmp_eq_u32 s50, %[wrap]
LA 19
RV 252,22,5120
M 6,22,4
s_cselect_b32 s50, %[rst], s50
LC_ 125
M 6,23,5
WV 31
M 7,34,4
WL 4
LO 124,18,124
LU 18,124
M 7,35,5
s_add_u32 s38, s38, 32768
RA 224,1,2048
HS 24,18
M 8,46,4
s_cmp_eq_u32 s38, s77
LL 16,80
LL 106,81
M 8,47,5
s_cselect_b32 s38, s76, s38
LA 16
M 9,58,4
s_barrier
LC_ 106
M 9,59,5
s_sub_u32 s39, 0, s39
M 10,70,4
WL 3
LO 125,19,125
LU 19,125
M 10,71,5
RA 228,1,3072
HS 24,19
M 11,82,4
LL 17,82
LL 107,83
M 11,83,5
LA 17
HX 24
Z 0,12,0
LC_ 107
W1 24,0
M 0,13,1
Z 1,24,0
WL 4
LO 16,16,106
LU 106,16
M 1,25,1
RA 232,1,4096
HZ 25,106
Z 2,36,0
LL 18,84
LL 110,85
M 2,37,1
LA 18
PK 106,16,104,33
Z 3,48,0
LC_ 110
PK 104,16,104,32
M 3,49,1
Z 4,60,0
WL 4
LO 17,17,107
LU 107,17
M 4,61,1
RA 236,1,6144
HS 25,107
Z 5,72,0
LL 19,86
LL 111,87
M 5,73,1
s_add_u32 s42, s44, s60
LA 19
PK 107,17,105,33
M 0,14,2
s_addc_u32 s43, s45, 0
LC_ 111
PK 105,17,105,32
M 0,15,3
ST 104
M 1,26,2
WL 3
LO 18,18,110
LU 110,18
M 1,27,3
RA 216,1,7168
HS 25,110
M 2,38,2
LL 16,88
LL 114,89
M 2,39,3
LA 16
PK 110,18,108,33
M 3,50,2
LC_ 114
PK 108,18,108,32
M 3,51,3
PO 110
M 4,62,2
WL 3
LO 19,19,111
LU 111,19
M 4,63,3
RA 220,1,8192
HS 25,111
M 5,74,2
LL 17,90
LL 115,91
M 5,75,3
s_mov_b32 m0, %[pm]
LA 17
PK 111,19,109,33
M 0,16,4
LC_ 115
PK 109,19,109,32
M 0,17,5
DF
PO 111
M 1,28,4
WL 3
LO 16,16,114
LU 114,16
M 1,29,5
s_add_u32 s42, s44, s61
RA 224,1,9216
HS 25,114
M 2,40,4
s_addc_u32 s43, s45, 0
LL 18,92
LL 118,93
M 2,41,5
LA 18
PK 114,16,112,33
M 3,52,4
LC_ 118
PK 112,16,112,32
M 3,53,5
ST 108
M 4,64,4
WL 3
LO 17,17,115
LU 115,17
M 4,65,5
RA 228,1,10240
HS 25,115
M 5,76,4
LL 19,94
LL 119,95
M 5,77,5
s_add_u32 s42, s44, s62
LA 19
PK 115,17,113,33
M 0,18,0
s_addc_u32 s43, s45, 0
LC_ 119
PK 113,17,113,32
M 0,19,1
ST 112
M 1,30,0
WL 3
LO 18,18,118
LU 118,18
M 1,31,1
RA 232,1,11264
HS 25,118
M 2,42,0
LL 16,96
LL 122,97
M 2,43,1
LA 16
PK 118,18,116,33
M 3,54,0
LC_ 122
PK 116,18,116,32
M 3,55,1
RV 252,22,5392
PO 118
M 4,66,0
WL 4
LO 19,19,119
LU 119,19
M 4,67,1
RA 236,1,12288
HS 25,119
M 5,78,0
LL 17,98
LL 123,99
M 5,79,1
LA 17
PK 119,19,117,33
M 0,20,2
s_add_u32 s42, s44, s63
LC_ 123
PK 117,19,117,32
M 0,21,3
s_addc_u32 s43, s45, 0
PO 119
M 1,32,2
WL 4
LO 16,16,122
LU 122,16
M 1,33,3
RA 216,1,13312
HS 25,122
M 2,44,2
LL 18,100
LL 126,101
M 2,45,3
LA 18
PK 122,16,120,33
M 3,56,2
LC_ 126
PK 120,16,120,32
M 3,57,3
ST 116
M 4,68,2
WL 3
LO 17,17,123
LU 123,17
M 4,69,3
RA 220,1,14336
HS 25,123
M 5,80,2
LL 19,102
LL 127,103
M 5,81,3
s_add_u32 s42, s44, s64
LA 19
PK 123,17,121,33
M 0,22,4
)"
R"(s_addc_u32 s43, s45, 0
LC_ 127
PK 121,17,121,32
M 0,23,5
s_and_b32 s53, %[w], 1
M 1,34,4
WL 3
LO 18,18,126
LU 126,18
M 1,35,5
s_lshl_b32 s53, s53, 10
RA 224,1,15360
HS 25,126
M 2,46,4
s_lshr_b32 s72, %[w], 1
LL 16,56
LL 104,57
M 2,47,5
s_mul_i32 s72, s72, 32768
LA 16
PK 126,18,124,33
M 3,58,4
s_add_u32 s72, s72, s53
LC_ 104
PK 124,18,124,32
M 3,59,5
s_add_u32 s72, s72, 128000
ds_read_b64 v[14:15], v5 offset:0
PO 126
M 4,70,4
WL 4
LO 19,19,127
LU 127,19
M 4,71,5
s_movk_i32 s53, 0x78
RA 228,1,16384
HS 25,127
M 5,82,4
LL 17,58
LL 105,59
M 5,83,5
LA 17
PK 127,19,125,33
Z 6,12,0
LC_ 105
PK 125,19,125,32
M 6,13,1
PO 127
v_subrev_u32 v5, s39, v5
Z 7,24,0
WL 4
LO 104,16,104
LU 16,104
M 7,25,1
RA 232,1,17408
HZ 24,16
v_lshrrev_b32 v12, 5, v0
v_and_b32 v12, 6, v12
v_bfe_u32 v12, s53, v12, 2
v_lshrrev_b32 v20, 8, v0
v_xor_b32 v20, v12, v20
v_lshl_add_u32 v20, v20, 4, s72
v_and_b32 v12, 0xf0, v0
v_lshl_add_u32 v20, v12, 2, v20
v_add_u32 v12, s72, v0
Z 8,36,0
WL 4
LL 18,60
LL 108,61
ST 120
M 8,37,1
s_add_u32 s42, s44, s65
LA 18
v_add_f32 v14, v14, v15
Z 9,48,0
s_addc_u32 s43, s45, 0
LC_ 108
ST 124
M 9,49,1
s_mul_i32 s53, s50, 25600
v_add_f32_dpp v14, v14, v14 quad_perm:[1,0,3,2] row_mask:0xf bank_mask:0xf
HX 25
Z 10,60,0
WL 3
LO 105,17,105
LU 17,105
M 10,61,1
s_add_u32 s40, %[wlo], s53
RA 236,1,18432
HS 24,17
Z 11,72,0
LL 19,62
LL 109,63
M 11,73,1
s_addc_u32 s41, %[whi], 0
LA 19
W1 25,1024
M 6,14,2
s_mov_b32 m0, s38
LC_ 109
DW 0
M 6,15,3
v_add_f32_dpp v14, v14, v14 quad_perm:[2,3,0,1] row_mask:0xf bank_mask:0xf
v_add_u32 v4, s39, v4
M 7,26,2
WL 4
LO 108,18,108
LU 18,108
M 7,27,3
s_add_u32 s37, s37, 32768
RA 216,1,19456
HS 24,18
M 8,38,2
s_cmp_eq_u32 s37, s75
LL 16,64
LL 112,65
M 8,39,3
s_cselect_b32 s37, s74, s37
LA 16
DW 1024
M 9,50,2
LC_ 112
v_add_f32_dpp v14, v14, v14 row_half_mirror row_mask:0xf bank_mask:0xf
M 9,51,3
v_cvt_f64_f32 v[14:15], v14
M 10,62,2
WL 3
LO 109,19,109
LU 19,109
M 10,63,3
RA 220,1,20480
HS 24,19
M 11,74,2
LL 17,66
LL 113,67
M 11,75,3
LA 17
s_mov_b64 exec, s[54:55]
SP
s_mov_b64 exec, -1
M 6,16,4
LC_ 113
DW 2048
M 6,17,5
M 7,28,4
WL 3
LO 112,16,112
LU 16,112
M 7,29,5
s_mul_i32 s53, s51, 49152
RA 224,1,21504
HS 24,16
M 8,40,4
s_bitcmp1_b64 s[90:91], s51
LL 18,68
LL 116,69
M 8,41,5
s_cselect_b64 s[44:45], s[84:85], s[82:83]
LA 18
M 9,52,4
s_add_u32 s44, s44, s53
LC_ 116
M 9,53,5
s_addc_u32 s45, s45, 0
M 10,64,4
WL 3
LO 113,17,113
LU 17,113
M 10,65,5
s_add_u32 s51, s51, %[d]
RA 228,1,22528
HS 24,17
M 11,76,4
LL 19,70
LL 117,71
M 11,77,5
s_cmp_eq_u32 s51, %[wrap]
LA 19
M 6,18,0
s_cselect_b32 s51, %[rst], s51
LC_ 117
M 6,19,1
s_add_u32 s40, s40, 3072
M 7,30,0
WL 3
LO 116,18,116
LU 18,116
M 7,31,1
s_addc_u32 s41, s41, 0
RA 232,1,23552
HS 24,18
M 8,42,0
s_add_u32 m0, s38, 3072
LL 16,72
LL 120,73
M 8,43,1
LA 16
DW 0
M 9,54,0
WV 22
LC_ 120
v_bcnt_u32_b32 v11, v11, 0
M 9,55,1
DW 1024
v_cmp_lt_u32 s[92:93], 2, v11
M 10,66,0
WL 3
LO 117,19,117
LU 19,117
M 10,67,1
s_nop 0
RA 236,1,24576
HS 24,19
M 11,78,0
LL 17,74
LL 121,75
M 11,79,1
s_sub_u32 s53, 0, s96
LA 17
M 6,20,2
s_xor_b32 s92, s92, s53
LC_ 121
M 6,21,3
s_xor_b32 s93, s93, s53
DW 2048
v_add_u32 v1, s37, v0
M 7,32,2
WL 3
LO 120,16,120
LU 16,120
M 7,33,3
s_lshl_b32 s53, s52, 8
RA 216,1,0
HS 24,16
M 8,44,2
s_add_u32 s48, %[alo], s53
LL 18,76
LL 124,77
M 8,45,3
s_addc_u32 s49, %[ahi], 0
LA 18
v_and_b32 v22, 0xfffffcff, v1
M 9,56,2
s_add_u32 s52, s52, %[d]
LC_ 124
M 9,57,3
s_cmp_eq_u32 s52, %[wrap]
M 10,68,2
WL 3
LO 121,17,121
LU 17,121
M 10,69,3
s_cselect_b32 s52, %[rst], s52
RA 220,1,1024
HS 24,17
M 11,80,2
LL 19,78
LL 125,79
M 11,81,3
LA 19
RV 252,22,5120
M 6,22,4
LC_ 125
DQ 3072
M 6,23,5
s_add_u32 s50, s50, %[d]
M 7,34,4
WL 4
LO 124,18,124
LU 18,124
M 7,35,5
s_cmp_eq_u32 s50, %[wrap]
RA 224,1,2048
HS 24,18
M 8,46,4
s_cselect_b32 s50, %[rst], s50
LL 16,80
LL 106,81
M 8,47,5
s_add_u32 s38, s38, 32768
LA 16
M 9,58,4
LC_ 106
M 9,59,5
M 10,70,4
WL 3
LO 125,19,125
LU 19,125
M 10,71,5
RA 228,1,3072
HS 24,19
M 11,82,4
LL 17,82
LL 107,83
M 11,83,5
LA 17
HX 24
Z 0,12,0
s_cmp_eq_u32 s38, s77
LC_ 107
M 0,13,1
s_cselect_b32 s38, s76, s38
Z 1,24,0
WL 3
LO 16,16,106
LU 106,16
M 1,25,1
s_barrier
RA 232,1,4096
HZ 25,106
Z 2,36,0
s_sub_u32 s39, 0, s39
LL 18,84
LL 110,85
M 2,37,1
s_mul_i32 s53, %[d], 0
LA 18
PK 106,16,104,33
Z 3,48,0
s_add_u32 s53, s53, s57
LC_ 110
PK 104,16,104,32
M 3,49,1
s_add_u32 s46, %[xlo], s53
W1 24,0
Z 4,60,0
WL 4
LO 17,17,107
LU 107,17
M 4,61,1
s_addc_u32 s47, %[xhi], 0
RA 236,1,6144
HS 25,107
Z 5,72,0
LL 19,86
LL 111,87
M 5,73,1
s_bitcmp1_b64 s[92:93], 48
LA 19
PK 107,17,105,33
M 0,14,2
s_cselect_b32 s58, 16, 0
LC_ 111
PK 105,17,105,32
M 0,15,3
s_lshr_b32 s58, 0xfe987610, s58
v_lshrrev_b32 v3, 6, v0
M 1,26,2
WL 4
LO 18,18,110
LU 110,18
M 1,27,3
RA 216,1,7168
HS 25,110
M 2,38,2
LL 16,88
LL 114,89
M 2,39,3
LA 16
PK 110,18,108,33
v_and_b32 v3, 12, v3
v_bfe_u32 v3, s58, v3, 4
v_mad_u32_u24 v3, v3, s97, v21
M 3,50,2
LC_ 114
PK 108,18,108,32
M 3,51,3
XL 0,0
PO 110
M 4,62,2
WL 3
LO 19,19,111
LU 111,19
M 4,63,3
s_add_u32 s42, s44, s60
RA 220,1,8192
HS 25,111
M 5,74,2
LL 17,90
LL 115,91
M 5,75,3
s_addc_u32 s43, s45, 0
LA 17
PK 111,19,109,33
M 0,16,4
LC_ 115
PK 109,19,109,32
M 0,17,5
ST 104
PO 111
M 1,28,4
WL 3
LO 16,16,114
LU 114,16
M 1,29,5
RA 224,1,9216
HS 25,114
M 2,40,4
LL 18,92
LL 118,93
)"
R"(M 2,41,5
LA 18
PK 114,16,112,33
M 3,52,4
LC_ 118
PK 112,16,112,32
M 3,53,5
XL 1,64
M 4,64,4
WL 3
LO 17,17,115
LU 115,17
M 4,65,5
s_mov_b32 m0, %[pm]
RA 228,1,10240
HS 25,115
M 5,76,4
LL 19,94
LL 119,95
M 5,77,5
LA 19
PK 115,17,113,33
M 0,18,0
LC_ 119
PK 113,17,113,32
M 0,19,1
DF
M 1,30,0
WL 3
LO 18,18,118
LU 118,18
M 1,31,1
s_add_u32 s42, s44, s61
RA 232,1,11264
HS 25,118
s_addc_u32 s43, s45, 0
M 2,42,0
LL 16,96
LL 122,97
M 2,43,1
LA 16
PK 118,18,116,33
ST 108
M 3,54,0
LC_ 122
PK 116,18,116,32
XL 2,128
M 3,55,1
s_add_u32 s42, s44, s62
RV 252,22,5392
PO 118
M 4,66,0
WL 4
LO 19,19,119
LU 119,19
M 4,67,1
s_addc_u32 s43, s45, 0
RA 236,1,12288
HS 25,119
M 5,78,0
LL 17,98
LL 123,99
M 5,79,1
LA 17
PK 119,19,117,33
M 0,20,2
LC_ 123
PK 117,19,117,32
M 0,21,3
XL 3,192
PO 119
M 1,32,2
WL 4
LO 16,16,122
LU 122,16
M 1,33,3
RA 216,1,13312
HS 25,122
M 2,44,2
LL 18,100
LL 126,101
ST 112
M 2,45,3
LA 18
PK 122,16,120,33
M 3,56,2
LC_ 126
PK 120,16,120,32
M 3,57,3
XL 4,256
M 4,68,2
WL 3
LO 17,17,123
LU 123,17
M 4,69,3
s_add_u32 s42, s44, s63
RA 220,1,14336
HS 25,123
M 5,80,2
LL 19,102
LL 127,103
M 5,81,3
s_addc_u32 s43, s45, 0
LA 19
PK 123,17,121,33
M 0,22,4
LC_ 127
PK 121,17,121,32
M 0,23,5
ST 116
M 1,34,4
WL 3
LO 18,18,126
LU 126,18
M 1,35,5
RA 224,1,15360
HS 25,126
M 2,46,4
LL 16,56
LL 104,57
M 2,47,5
LA 16
PK 126,18,124,33
M 3,58,4
LC_ 104
PK 124,18,124,32
M 3,59,5
XL 5,320
PO 126
M 4,70,4
WL 3
LO 19,19,127
LU 127,19
M 4,71,5
s_add_u32 s42, s44, s64
RA 228,1,16384
HS 25,127
M 5,82,4
LL 17,58
LL 105,59
M 5,83,5
s_addc_u32 s43, s45, 0
LA 17
PK 127,19,125,33
Z 6,12,0
LC_ 105
PK 125,19,125,32
M 6,13,1
XL 6,384
PO 127
Z 7,24,0
WL 3
LO 104,16,104
LU 16,104
M 7,25,1
RA 232,1,17408
HZ 24,16
Z 8,36,0
LL 18,60
LL 108,61
ST 120
M 8,37,1
LA 18
ds_read_b64 v[14:15], v5 offset:0
Z 9,48,0
LC_ 108
XL 7,448
M 9,49,1
s_add_u32 s42, s44, s65
v_subrev_u32 v5, s39, v5
HX 25
Z 10,60,0
WL 4
LO 105,17,105
LU 17,105
M 10,61,1
s_addc_u32 s43, s45, 0
RA 236,1,18432
HS 24,17
Z 11,72,0
LL 19,62
LL 109,63
M 11,73,1
LA 19
W1 25,1024
M 6,14,2
LC_ 109
ST 124
M 6,15,3
s_mul_i32 s53, s50, 25600
v_add_u32 v4, s39, v4
M 7,26,2
WL 4
LO 108,18,108
LU 18,108
M 7,27,3
s_add_u32 s40, %[wlo], s53
RA 216,1,19456
HS 24,18
M 8,38,2
s_addc_u32 s41, %[whi], 0
LL 16,64
LL 112,65
M 8,39,3
s_mov_b32 m0, s38
LA 16
XL 8,512
M 9,50,2
LC_ 112
DW 0
M 9,51,3
XL 9,576
v_add_f32 v14, v14, v15
s_nop 1
v_add_f32_dpp v14, v14, v14 quad_perm:[1,0,3,2] row_mask:0xf bank_mask:0xf
M 10,62,2
WL 3
LO 109,19,109
LU 19,109
M 10,63,3
s_add_u32 s37, s37, 32768
RA 220,1,20480
HS 24,19
v_add_f32_dpp v14, v14, v14 quad_perm:[2,3,0,1] row_mask:0xf bank_mask:0xf
M 11,74,2
LL 17,66
LL 113,67
M 11,75,3
s_cmp_eq_u32 s37, s75
LA 17
v_add_f32_dpp v14, v14, v14 row_half_mirror row_mask:0xf bank_mask:0xf
M 6,16,4
s_cselect_b32 s37, s74, s37
LC_ 113
DW 1024
M 6,17,5
XL 10,640
v_cvt_f64_f32 v[14:15], v14
M 7,28,4
WL 3
LO 112,16,112
LU 16,112
M 7,29,5
RA 224,1,21504
HS 24,16
M 8,40,4
LL 18,68
LL 116,69
M 8,41,5
LA 18
s_mov_b64 exec, s[54:55]
SP
s_mov_b64 exec, -1
M 9,52,4
LC_ 116
DW 2048
M 9,53,5
s_mul_i32 s53, s51, 49152
M 10,64,4
WL 3
LO 113,17,113
LU 17,113
M 10,65,5
s_bitcmp1_b64 s[90:91], s51
RA 228,1,22528
HS 24,17
M 11,76,4
LL 19,70
LL 117,71
M 11,77,5
s_cselect_b64 s[44:45], s[84:85], s[82:83]
LA 19
M 6,18,0
s_add_u32 s44, s44, s53
LC_ 117
M 6,19,1
s_addc_u32 s45, s45, 0
M 7,30,0
WL 3
LO 116,18,116
LU 18,116
M 7,31,1
s_add_u32 s51, s51, %[d]
RA 232,1,23552
HS 24,18
M 8,42,0
s_cmp_eq_u32 s51, %[wrap]
LL 16,72
LL 120,73
M 8,43,1
s_cselect_b32 s51, %[rst], s51
LA 16
M 9,54,0
s_add_u32 s40, s40, 3072
LC_ 120
s_addc_u32 s41, s41, 0
s_add_u32 m0, s38, 3072
M 9,55,1
XL 11,704
M 10,66,0
WL 3
LO 117,19,117
LU 19,117
M 10,67,1
RA 236,1,24576
HS 24,19
M 11,78,0
LL 17,74
LL 121,75
M 11,79,1
LA 17
DW 0
M 6,20,2
LC_ 121
DW 1024
M 6,21,3
DW 2048
v_add_u32 v1, s37, v0
M 7,32,2
WL 3
LO 120,16,120
LU 16,120
M 7,33,3
s_lshl_b32 s53, s52, 8
RA 216,1,0
HS 24,16
M 8,44,2
s_add_u32 s48, %[alo], s53
LL 18,76
LL 124,77
M 8,45,3
s_addc_u32 s49, %[ahi], 0
LA 18
v_and_b32 v22, 0xfffffcff, v1
M 9,56,2
s_add_u32 s52, s52, %[d]
LC_ 124
M 9,57,3
s_cmp_eq_u32 s52, %[wrap]
M 10,68,2
WL 3
LO 121,17,121
LU 17,121
M 10,69,3
s_cselect_b32 s52, %[rst], s52
RA 220,1,1024
HS 24,17
M 11,80,2
LL 19,78
LL 125,79
M 11,81,3
LA 19
RV 252,22,5120
M 6,22,4
LC_ 125
DQ 3072
M 6,23,5
s_add_u32 s50, s50, %[d]
M 7,34,4
WL 4
LO 124,18,124
LU 18,124
M 7,35,5
s_cmp_eq_u32 s50, %[wrap]
RA 224,1,2048
HS 24,18
M 8,46,4
s_cselect_b32 s50, %[rst], s50
LL 16,80
LL 106,81
M 8,47,5
s_add_u32 s38, s38, 32768
LA 16
M 9,58,4
LC_ 106
M 9,59,5
M 10,70,4
WL 3
LO 125,19,125
LU 19,125
M 10,71,5
RA 228,1,3072
HS 24,19
M 11,82,4
LL 17,82
LL 107,83
M 11,83,5
LA 17
HX 24
Z 0,12,0
s_cmp_eq_u32 s38, s77
LC_ 107
M 0,13,1
s_cselect_b32 s38, s76, s38
Z 1,24,0
WL 3
LO 16,16,106
LU 106,16
M 1,25,1
WV 42
)"
R"(s_barrier
RA 232,1,4096
HZ 25,106
Z 2,36,0
s_sub_u32 s39, 0, s39
LL 18,84
LL 110,85
M 2,37,1
LA 18
PK 106,16,104,33
Z 3,48,0
LC_ 110
PK 104,16,104,32
M 3,49,1
W1 24,0
Z 4,60,0
WL 4
LO 17,17,107
LU 107,17
M 4,61,1
RA 236,1,6144
HS 25,107
Z 5,72,0
LL 19,86
LL 111,87
M 5,73,1
s_add_u32 s42, s44, s60
LA 19
PK 107,17,105,33
M 0,14,2
s_addc_u32 s43, s45, 0
LC_ 111
PK 105,17,105,32
M 0,15,3
ST 104
M 1,26,2
WL 4
LO 18,18,110
LU 110,18
M 1,27,3
RA 216,1,7168
HS 25,110
M 2,38,2
LL 16,88
LL 114,89
M 2,39,3
LA 16
PK 110,18,108,33
M 3,50,2
LC_ 114
PK 108,18,108,32
M 3,51,3
PO 110
M 4,62,2
WL 3
LO 19,19,111
LU 111,19
M 4,63,3
RA 220,1,8192
HS 25,111
M 5,74,2
LL 17,90
LL 115,91
M 5,75,3
s_mov_b32 m0, %[pm]
LA 17
PK 111,19,109,33
M 0,16,4
LC_ 115
PK 109,19,109,32
M 0,17,5
DF
PO 111
M 1,28,4
WL 3
LO 16,16,114
LU 114,16
M 1,29,5
s_add_u32 s42, s44, s61
RA 224,1,9216
HS 25,114
M 2,40,4
s_addc_u32 s43, s45, 0
LL 18,92
LL 118,93
M 2,41,5
LA 18
PK 114,16,112,33
M 3,52,4
LC_ 118
PK 112,16,112,32
M 3,53,5
ST 108
M 4,64,4
WL 3
LO 17,17,115
LU 115,17
M 4,65,5
RA 228,1,10240
HS 25,115
M 5,76,4
LL 19,94
LL 119,95
M 5,77,5
s_add_u32 s42, s44, s62
LA 19
PK 115,17,113,33
M 0,18,0
s_addc_u32 s43, s45, 0
LC_ 119
PK 113,17,113,32
M 0,19,1
ST 112
M 1,30,0
WL 3
LO 18,18,118
LU 118,18
M 1,31,1
RA 232,1,11264
HS 25,118
M 2,42,0
LL 16,96
LL 122,97
M 2,43,1
LA 16
PK 118,18,116,33
M 3,54,0
LC_ 122
PK 116,18,116,32
M 3,55,1
RV 252,22,5392
PO 118
M 4,66,0
WL 4
LO 19,19,119
LU 119,19
M 4,67,1
RA 236,1,12288
HS 25,119
M 5,78,0
LL 17,98
LL 123,99
M 5,79,1
WV 30
LA 17
PK 119,19,117,33
M 0,20,2
s_add_u32 s42, s44, s63
LC_ 123
PK 117,19,117,32
BW 0
M 0,21,3
s_addc_u32 s43, s45, 0
RV 40,8,6144
PO 119
M 1,32,2
WL 6
LO 16,16,122
LU 122,16
M 1,33,3
RA 216,1,13312
HS 25,122
RV 44,8,6208
M 2,44,2
LL 18,100
LL 126,101
M 2,45,3
LA 18
PK 122,16,120,33
RV 48,8,6272
M 3,56,2
LC_ 126
PK 120,16,120,32
BR 0
M 3,57,3
WV 28
ST 116
BW 1
M 4,68,2
WL 9
LO 17,17,123
LU 123,17
M 4,69,3
WV 26
RA 220,1,14336
HS 25,123
BR 1
M 5,80,2
WL 3
LL 19,102
LL 127,103
M 5,81,3
s_add_u32 s42, s44, s64
LA 19
PK 123,17,121,33
LZ 40,0
s_addc_u32 s43, s45, 0
BW 2
M 0,22,4
LC_ 127
PK 121,17,121,32
M 0,23,5
ST 120
RV 40,8,6336
M 1,34,4
LO 18,18,126
LU 126,18
BR 2
M 1,35,5
WL 5
RA 224,1,15360
HS 25,126
LS 44,1
WV 26
BW 3
M 2,46,4
LL 16,56
LL 104,57
M 2,47,5
LA 16
PK 126,18,124,33
M 3,58,4
LC_ 104
PK 124,18,124,32
RV 44,8,6400
M 3,59,5
ds_read_b64 v[14:15], v5 offset:0
PO 126
BR 3
M 4,70,4
WL 9
LO 19,19,127
LU 127,19
M 4,71,5
WL 7
RA 228,1,16384
HS 25,127
LS 48,2
WV 24
BW 4
M 5,82,4
LL 17,58
LL 105,59
M 5,83,5
LA 17
PK 127,19,125,33
RV 48,8,6464
Z 6,12,0
s_add_u32 s42, s44, s65
LC_ 105
PK 125,19,125,32
M 6,13,1
s_addc_u32 s43, s45, 0
BR 4
PO 127
Z 7,24,0
WL 9
LO 104,16,104
LU 16,104
M 7,25,1
WL 6
RA 232,1,17408
HZ 24,16
LS 40,3
WV 22
BW 5
Z 8,36,0
WV 21
LL 18,60
LL 108,61
M 8,37,1
WL 2
LA 18
ST 124
RV 40,8,6528
Z 9,48,0
s_mul_i32 s53, s50, 25600
LC_ 108
BR 5
M 9,49,1
s_add_u32 s40, %[wlo], s53
v_subrev_u32 v5, s39, v5
HX 25
LS 44,4
s_addc_u32 s41, %[whi], 0
BW 6
Z 10,60,0
LO 105,17,105
LU 17,105
M 10,61,1
s_mov_b32 m0, s38
RA 236,1,18432
HS 24,17
Z 11,72,0
LL 19,62
LL 109,63
RV 44,8,6592
M 11,73,1
LA 19
W1 25,1024
BR 6
M 6,14,2
LC_ 109
DW 0
M 6,15,3
WL 7
v_add_f32 v14, v14, v15
v_add_u32 v4, s39, v4
LS 48,5
WV 21
BW 7
M 7,26,2
LO 108,18,108
LU 18,108
M 7,27,3
s_add_u32 s37, s37, 32768
RA 216,1,19456
HS 24,18
M 8,38,2
s_cmp_eq_u32 s37, s75
LL 16,64
LL 112,65
RV 48,8,6656
M 8,39,3
s_cselect_b32 s37, s74, s37
LA 16
DW 1024
BR 7
M 9,50,2
LC_ 112
v_add_f32_dpp v14, v14, v14 quad_perm:[1,0,3,2] row_mask:0xf bank_mask:0xf
M 9,51,3
WL 7
v_add_f32_dpp v14, v14, v14 quad_perm:[2,3,0,1] row_mask:0xf bank_mask:0xf
LS 40,6
WV 20
BW 8
M 10,62,2
WL 7
LO 109,19,109
LU 19,109
M 10,63,3
RA 220,1,20480
HS 24,19
RV 40,8,6720
M 11,74,2
LL 17,66
LL 113,67
BR 8
M 11,75,3
LA 17
DW 2048
M 6,16,4
LC_ 113
v_add_f32_dpp v14, v14, v14 row_half_mirror row_mask:0xf bank_mask:0xf
M 6,17,5
WL 7
v_cvt_f64_f32 v[14:15], v14
LS 44,7
WV 17
BW 9
M 7,28,4
WL 3
LO 112,16,112
LU 16,112
RV 44,8,6784
M 7,29,5
s_mul_i32 s53, s51, 49152
RA 224,1,21504
HS 24,16
M 8,40,4
s_bitcmp1_b64 s[90:91], s51
LL 18,68
LL 116,69
BR 9
M 8,41,5
s_cselect_b64 s[44:45], s[84:85], s[82:83]
LA 18
s_mov_b64 exec, s[54:55]
SP
s_mov_b64 exec, -1
LS 48,8
s_add_u32 s44, s44, s53
BW 10
M 9,52,4
s_addc_u32 s45, s45, 0
LC_ 116
RV 48,8,6848
M 9,53,5
s_add_u32 s51, s51, %[d]
RA 240,7,0
BR 10
M 10,64,4
WL 10
LO 113,17,113
LU 17,113
M 10,65,5
s_cmp_eq_u32 s51, %[wrap]
RA 228,1,22528
HS 24,17
)"
R"(M 11,76,4
WL 7
LL 19,70
LL 117,71
M 11,77,5
s_cselect_b32 s51, %[rst], s51
LA 19
M 6,18,0
s_add_u32 s40, s40, 3072
LC_ 117
M 6,19,1
s_addc_u32 s41, s41, 0
LS 40,9
s_add_u32 m0, s38, 3072
WV 15
BW 11
M 7,30,0
WL 7
LO 116,18,116
LU 18,116
RA 244,7,256
M 7,31,1
RA 232,1,23552
HS 24,18
M 8,42,0
LL 16,72
LL 120,73
BR 11
M 8,43,1
WL 7
LA 16
DW 0
LS 44,10
RA 248,7,512
M 9,54,0
LC_ 120
DW 1024
M 9,55,1
M 10,66,0
WL 7
LO 117,19,117
LU 19,117
M 10,67,1
WL 3
RA 236,1,24576
HS 24,19
LS 48,11
RA 252,7,768
M 11,78,0
LL 17,74
LL 121,75
s_nop 3
v_mul_f32 v36, 0xbb2aaaab, v36
M 11,79,1
LA 17
DW 2048
v_mul_f32 v37, 0xbb2aaaab, v37
M 6,20,2
LC_ 121
v_mul_f32 v38, 0xbb2aaaab, v38
v_mul_f32 v39, 0xbb2aaaab, v39
M 6,21,3
s_lshl_b32 s53, s52, 8
v_add_u32 v1, s37, v0
LK 40,240,0,0
LK 44,242,0,2
s_add_u32 s48, %[alo], s53
RA 240,7,1024
LK 48,244,1,0
s_nop 0
CF 128,40,41
LK 248,246,1,2
s_addc_u32 s49, %[ahi], 0
RA 244,7,1280
CF 129,42,43
M 7,32,2
WL 6
LO 120,16,120
LU 16,120
M 7,33,3
s_add_u32 s52, s52, %[d]
RA 216,1,0
HS 24,16
M 8,44,2
s_cmp_eq_u32 s52, %[wrap]
LL 18,76
LL 124,77
M 8,45,3
s_cselect_b32 s52, %[rst], s52
LA 18
DQ 3072
M 9,56,2
LC_ 124
v_and_b32 v22, 0xfffffcff, v1
CF 130,44,45
M 9,57,3
CF 131,46,47
LK 40,248,2,0
CF 132,48,49
LK 44,250,2,2
RA 248,7,1536
CF 133,50,51
GZ 0
WL 8
CF 134,248,249
CF 135,250,251
LK 48,252,3,0
CF 136,40,41
LK 248,254,3,2
RA 252,7,1792
CF 137,42,43
GS 1
WL 6
CF 138,44,45
CF 139,46,47
LK 40,240,4,0
CF 140,48,49
LK 44,242,4,2
RA 240,7,2048
CF 141,50,51
GS 2
WL 6
CF 142,248,249
CF 143,250,251
LK 48,244,5,0
CF 144,40,41
LK 248,246,5,2
RA 244,7,2304
CF 145,42,43
GS 3
CF 146,44,45
CF 147,46,47
M 10,68,2
WL 3
LO 121,17,121
LU 17,121
M 10,69,3
s_add_u32 s50, s50, %[d]
RA 220,1,1024
HS 24,17
LK 40,248,6,0
WL 3
CF 148,48,49
LK 44,250,6,2
s_cmp_eq_u32 s50, %[wrap]
RA 248,7,2560
CF 149,50,51
GS 4
s_cselect_b32 s50, %[rst], s50
CF 150,248,249
CF 151,250,251
LK 48,252,7,0
CF 152,40,41
LK 248,254,7,2
RA 252,7,2816
CF 153,42,43
GS 5
WL 4
CF 154,44,45
CF 155,46,47
LK 40,240,8,0
CF 156,48,49
LK 44,242,8,2
CF 157,50,51
GS 6
CF 158,248,249
CF 159,250,251
M 11,80,2
LL 19,78
LL 125,79
M 11,81,3
WL 3
LA 19
RV 252,22,5120
LK 48,244,9,0
CF 160,40,41
LK 248,246,9,2
CF 161,42,43
GS 7
CF 162,44,45
CF 163,46,47
M 6,22,4
LC_ 125
M 6,23,5
M 7,34,4
WL 4
LO 124,18,124
LU 18,124
M 7,35,5
s_add_u32 s38, s38, 32768
RA 224,1,2048
HS 24,18
M 8,46,4
s_cmp_eq_u32 s38, s77
LL 16,80
LL 106,81
M 8,47,5
s_cselect_b32 s38, s76, s38
LA 16
LK 40,248,10,0
CF 164,48,49
LK 44,250,10,2
s_barrier
CF 165,50,51
GS 8
s_sub_u32 s39, 0, s39
CF 166,248,249
CF 167,250,251
M 9,58,4
LC_ 106
M 9,59,5
M 10,70,4
WL 3
LO 125,19,125
LU 19,125
M 10,71,5
RA 228,1,3072
HS 24,19
LK 48,252,11,0
CF 168,40,41
LK 248,254,11,2
CF 169,42,43
GS 9
CF 170,44,45
CF 171,46,47
M 11,82,4
LL 17,82
LL 107,83
M 11,83,5
LA 17
HX 24
Z 0,12,0
LC_ 107
W1 24,0
M 0,13,1
RV 36,8,6912
GS 10
CF 172,48,49
Z 1,24,0
WL 5
LO 16,16,106
LU 106,16
M 1,25,1
RA 232,1,4096
HZ 25,106
Z 2,36,0
LL 18,84
LL 110,85
M 2,37,1
LA 18
PK 106,16,104,33
Z 3,48,0
LC_ 110
PK 104,16,104,32
M 3,49,1
CF 173,50,51
CF 174,248,249
Z 4,60,0
WL 5
LO 17,17,107
LU 107,17
M 4,61,1
RA 236,1,6144
HS 25,107
Z 5,72,0
LL 19,86
LL 111,87
M 5,73,1
s_add_u32 s42, s44, s60
LA 19
PK 107,17,105,33
M 0,14,2
s_addc_u32 s43, s45, 0
LC_ 111
PK 105,17,105,32
M 0,15,3
ST 104
CF 175,250,251
M 1,26,2
WL 3
LO 18,18,110
LU 110,18
M 1,27,3
RA 216,1,7168
HS 25,110
GS 11
M 2,38,2
LL 16,88
LL 114,89
M 2,39,3
LA 16
PK 110,18,108,33
M 3,50,2
LC_ 114
PK 108,18,108,32
M 3,51,3
PO 110
v_cndmask_b32 v52, v52, v53, s[78:79]
M 4,62,2
WL 3
LO 19,19,111
LU 111,19
M 4,63,3
RA 220,1,8192
HS 25,111
M 5,74,2
LL 17,90
LL 115,91
M 5,75,3
s_mov_b32 m0, %[pm]
LA 17
PK 111,19,109,33
M 0,16,4
LC_ 115
PK 109,19,109,32
M 0,17,5
DF
PO 111
M 1,28,4
WL 3
LO 16,16,114
LU 114,16
M 1,29,5
s_add_u32 s42, s44, s61
RA 224,1,9216
HS 25,114
M 2,40,4
s_addc_u32 s43, s45, 0
LL 18,92
LL 118,93
M 2,41,5
LA 18
PK 114,16,112,33
M 3,52,4
LC_ 118
PK 112,16,112,32
M 3,53,5
ST 108
v_cndmask_b32 v54, v54, v55, s[78:79]
M 4,64,4
WL 3
LO 17,17,115
LU 115,17
M 4,65,5
RA 228,1,10240
HS 25,115
M 5,76,4
LL 19,94
LL 119,95
M 5,77,5
s_add_u32 s42, s44, s62
LA 19
PK 115,17,113,33
M 0,18,0
s_addc_u32 s43, s45, 0
LC_ 119
PK 113,17,113,32
M 0,19,1
ST 112
v_cndmask_b32 v52, v52, v54, s[80:81]
M 1,30,0
WL 3
LO 18,18,118
LU 118,18
v_mul_f32 v52, 0x3b2aaaab, v52
M 1,31,1
RA 232,1,11264
HS 25,118
v_med3_f32 v53, v52, v39, v37
M 2,42,0
LL 16,96
LL 122,97
v_cmp_eq_f32 vcc, v52, v53
M 2,43,1
LA 16
PK 118,18,116,33
C 52,52,52
M 3,54,0
LC_ 122
PK 116,18,116,32
v_lshlrev_b32 v52, 16, v52
M 3,55,1
RV 252,22,5392
PO 118
v_add_f32 v52, v36, v52
M 4,66,0
WL 4
LO 19,19,119
LU 119,19
C 52,52,52
M 4,67,1
RA 236,1,12288
HS 25,119
v_lshlrev_b32 v52, 16, v52
M 5,78,0
LL 17,98
LL 123,99
v_sqrt_f32 v52, v52
M 5,79,1
LA 17
)"
R"(PK 119,19,117,33
C 52,52,52
M 0,20,2
s_add_u32 s42, s44, s63
LC_ 123
PK 117,19,117,32
v_lshlrev_b32 v52, 16, v52
M 0,21,3
s_addc_u32 s43, s45, 0
PO 119
v_rcp_f32 v52, v52
M 1,32,2
WL 4
LO 16,16,122
LU 122,16
v_mul_f32 v52, v38, v52
M 1,33,3
RA 216,1,13312
HS 25,122
v_cndmask_b32 v52, 0, v52, vcc
M 2,44,2
LL 18,100
LL 126,101
CF 52,52,52
M 2,45,3
LA 18
PK 122,16,120,33
ds_bpermute_b32 v13, v28, v52
M 3,56,2
LC_ 126
PK 120,16,120,32
M 3,57,3
ST 116
M 4,68,2
WL 4
LO 17,17,123
LU 123,17
M 4,69,3
RA 220,1,14336
HS 25,123
M 5,80,2
LL 19,102
LL 127,103
M 5,81,3
s_add_u32 s42, s44, s64
LA 19
PK 123,17,121,33
M 0,22,4
s_addc_u32 s43, s45, 0
LC_ 127
PK 121,17,121,32
M 0,23,5
ST 120
M 1,34,4
WL 3
LO 18,18,126
LU 126,18
M 1,35,5
RA 224,1,15360
HS 25,126
XS 128
M 2,46,4
LL 16,56
LL 104,57
M 2,47,5
LA 16
PK 126,18,124,33
M 3,58,4
LC_ 104
PK 124,18,124,32
XS 129
M 3,59,5
ds_read_b64 v[14:15], v5 offset:0
PO 126
M 4,70,4
WL 4
LO 19,19,127
LU 127,19
M 4,71,5
RA 228,1,16384
HS 25,127
XS 130
M 5,82,4
LL 17,58
LL 105,59
M 5,83,5
LA 17
PK 127,19,125,33
Z 6,12,0
s_add_u32 s42, s44, s65
LC_ 105
PK 125,19,125,32
XS 131
M 6,13,1
s_addc_u32 s43, s45, 0
PO 127
v_subrev_u32 v5, s39, v5
Z 7,24,0
WL 4
LO 104,16,104
LU 16,104
XS 132
M 7,25,1
RA 232,1,17408
HZ 24,16
Z 8,36,0
LL 18,60
LL 108,61
M 8,37,1
WL 4
LA 18
ST 124
XS 133
Z 9,48,0
s_mul_i32 s53, s50, 25600
LC_ 108
v_add_f32 v14, v14, v15
M 9,49,1
s_add_u32 s40, %[wlo], s53
v_add_f32_dpp v14, v14, v14 quad_perm:[1,0,3,2] row_mask:0xf bank_mask:0xf
HX 25
Z 10,60,0
WL 3
LO 105,17,105
LU 17,105
XS 134
M 10,61,1
s_addc_u32 s41, %[whi], 0
RA 236,1,18432
HS 24,17
Z 11,72,0
LL 19,62
LL 109,63
M 11,73,1
s_mov_b32 m0, s38
LA 19
W1 25,1024
XS 135
M 6,14,2
LC_ 109
DW 0
M 6,15,3
v_add_f32_dpp v14, v14, v14 quad_perm:[2,3,0,1] row_mask:0xf bank_mask:0xf
v_add_u32 v4, s39, v4
v_min_u32 v35, v35, v13
M 7,26,2
WL 4
LO 108,18,108
LU 18,108
M 7,27,3
s_add_u32 s37, s37, 32768
RA 216,1,19456
HS 24,18
M 8,38,2
s_cmp_eq_u32 s37, s75
LL 16,64
LL 112,65
XS 136
M 8,39,3
s_cselect_b32 s37, s74, s37
LA 16
v_add_f32_dpp v14, v14, v14 row_half_mirror row_mask:0xf bank_mask:0xf
M 9,50,2
s_mul_i32 s53, %[d], 0
LC_ 112
v_cvt_f64_f32 v[14:15], v14
M 9,51,3
s_add_u32 s53, s53, s57
s_mov_b64 exec, s[54:55]
SP
s_mov_b64 exec, -1
XS 137
M 10,62,2
WL 3
LO 109,19,109
LU 19,109
M 10,63,3
s_add_u32 s46, %[xlo], s53
RA 220,1,20480
HS 24,19
M 11,74,2
LL 17,66
LL 113,67
XS 138
M 11,75,3
s_addc_u32 s47, %[xhi], 0
LA 17
XS 139
M 6,16,4
s_bitcmp1_b64 s[92:93], 48
LC_ 113
XS 140
M 6,17,5
s_cselect_b32 s58, 16, 0
XS 141
XS 142
M 7,28,4
WL 3
LO 112,16,112
LU 16,112
M 7,29,5
s_lshr_b32 s58, 0xda43cb52, s58
RA 224,1,21504
HS 24,16
M 8,40,4
LL 18,68
LL 116,69
M 8,41,5
LA 18
v_lshrrev_b32 v3, 6, v0
M 9,52,4
LC_ 116
v_and_b32 v3, 12, v3
v_bfe_u32 v3, s58, v3, 4
v_mad_u32_u24 v3, v3, s97, v21
M 9,53,5
DW 1024
XS 143
M 10,64,4
WL 3
LO 113,17,113
LU 17,113
M 10,65,5
RA 228,1,22528
HS 24,17
M 11,76,4
LL 19,70
LL 117,71
M 11,77,5
LA 19
XL 12,0
M 6,18,0
LC_ 117
DW 2048
M 6,19,1
XL 13,64
XS 144
M 7,30,0
WL 3
LO 116,18,116
LU 18,116
M 7,31,1
s_mul_i32 s53, s51, 49152
RA 232,1,23552
HS 24,18
M 8,42,0
s_bitcmp1_b64 s[90:91], s51
LL 16,72
LL 120,73
M 8,43,1
s_cselect_b64 s[44:45], s[84:85], s[82:83]
LA 16
s_add_u32 s44, s44, s53
s_addc_u32 s45, s45, 0
s_add_u32 s51, s51, %[d]
s_cmp_eq_u32 s51, %[wrap]
s_cselect_b32 s51, %[rst], s51
XS 145
M 9,54,0
s_add_u32 s40, s40, 3072
LC_ 120
s_addc_u32 s41, s41, 0
s_add_u32 m0, s38, 3072
M 9,55,1
XL 14,128
XS 146
M 10,66,0
WL 3
LO 117,19,117
LU 19,117
M 10,67,1
RA 236,1,24576
HS 24,19
M 11,78,0
LL 17,74
LL 121,75
XS 147
M 11,79,1
LA 17
DW 0
M 6,20,2
LC_ 121
XL 15,192
M 6,21,3
DW 1024
v_add_u32 v1, s37, v0
XS 148
M 7,32,2
WL 3
LO 120,16,120
LU 16,120
M 7,33,3
RA 216,1,0
HS 24,16
M 8,44,2
LL 18,76
LL 124,77
XS 149
M 8,45,3
LA 18
XL 16,256
M 9,56,2
LC_ 124
DW 2048
XS 150
M 9,57,3
XL 17,320
v_and_b32 v22, 0xfffffcff, v1
M 10,68,2
WL 3
LO 121,17,121
LU 17,121
M 10,69,3
s_lshl_b32 s53, s52, 8
RA 220,1,1024
HS 24,17
XS 151
M 11,80,2
LL 19,78
LL 125,79
M 11,81,3
s_add_u32 s48, %[alo], s53
LA 19
RV 252,22,5120
M 6,22,4
s_addc_u32 s49, %[ahi], 0
LC_ 125
XS 152
M 6,23,5
s_add_u32 s52, s52, %[d]
XS 153
XS 154
M 7,34,4
WL 4
LO 124,18,124
LU 18,124
M 7,35,5
s_cmp_eq_u32 s52, %[wrap]
RA 224,1,2048
HS 24,18
M 8,46,4
s_cselect_b32 s52, %[rst], s52
LL 16,80
LL 106,81
M 8,47,5
LA 16
DQ 3072
M 9,58,4
LC_ 106
XL 18,384
M 9,59,5
s_add_u32 s50, s50, %[d]
XS 155
XS 156
M 10,70,4
WL 3
LO 125,19,125
LU 19,125
M 10,71,5
RA 228,1,3072
HS 24,19
M 11,82,4
LL 17,82
LL 107,83
M 11,83,5
LA 17
HX 24
s_cmp_eq_u32 s50, %[wrap]
s_cselect_b32 s50, %[rst], s50
XL 19,448
Z 2,36,0
WV 38
LC_ 107
XL 20,512
s_add_u32 s38, s38, 32768
s_cmp_eq_u32 s38, s77
s_cselect_b32 s38, s76, s38
s_barrier
s_sub_u32 s39, 0, s39
M 2,37,1
ds_read_b64 v[14:15], v5 offset:0
XL 21,576
XS 157
Z 3,48,0
WL 4
LO 16,16,106
LU 106,16
M 3,49,1
RA 232,1,4096
HZ 25,106
Z 4,60,0
LL 18,84
LL 110,85
XS 158
M 4,61,1
LA 18
PK 106,16,104,33
Z 5,72,0
WL 5
LC_ 110
)"
R"(PK 104,16,104,32
XL 22,640
M 5,73,1
RA 236,1,6144
v_subrev_u32 v5, s39, v5
W1 24,0
XS 159
M 2,38,2
WL 6
LO 17,17,107
LU 107,17
M 2,39,3
XL 23,704
HS 25,107
M 3,50,2
LL 19,86
LL 111,87
XS 160
M 3,51,3
s_add_u32 s42, s44, s60
LA 19
PK 107,17,105,33
M 4,62,2
s_addc_u32 s43, s45, 0
LC_ 111
PK 105,17,105,32
XS 161
M 4,63,3
WL 7
RA 216,1,7168
v_add_f32 v14, v14, v15
s_nop 1
v_add_f32_dpp v14, v14, v14 quad_perm:[1,0,3,2] row_mask:0xf bank_mask:0xf
M 5,74,2
WL 4
LO 18,18,110
LU 110,18
v_add_f32_dpp v14, v14, v14 quad_perm:[2,3,0,1] row_mask:0xf bank_mask:0xf
M 5,75,3
RA 220,1,8192
HS 25,110
XS 162
M 2,40,4
LL 16,88
LL 114,89
v_add_f32_dpp v14, v14, v14 row_half_mirror row_mask:0xf bank_mask:0xf
M 2,41,5
LA 16
PK 110,18,108,33
M 3,52,4
LC_ 114
PK 108,18,108,32
v_cvt_f64_f32 v[14:15], v14
XS 163
M 3,53,5
RA 224,1,9216
PO 110
M 4,64,4
WL 5
LO 19,19,111
LU 111,19
s_mov_b64 exec, s[54:55]
SP
s_mov_b64 exec, -1
ST 104
XS 164
M 4,65,5
HS 25,111
XS 165
M 5,76,4
WL 3
LL 17,90
LL 115,91
M 5,77,5
WV 18
LA 17
PK 111,19,109,33
M 2,42,0
s_add_u32 s42, s44, s61
LC_ 115
PK 109,19,109,32
M 2,43,1
s_addc_u32 s43, s45, 0
RA 228,1,10240
PO 111
BW 12
M 3,54,0
WL 5
LO 16,16,114
LU 114,16
XS 166
M 3,55,1
RA 232,1,11264
HS 25,114
RV 40,8,6144
M 4,66,0
LL 18,92
LL 118,93
RV 44,8,6208
M 4,67,1
LA 18
PK 114,16,112,33
XS 167
RV 48,8,6272
M 5,78,0
WL 6
LC_ 118
PK 112,16,112,32
BR 12
M 5,79,1
WV 16
RV 252,22,5392
XS 168
BW 13
M 2,44,2
LO 17,17,115
LU 115,17
BR 13
M 2,45,3
RA 236,1,12288
HS 25,115
ST 108
WL 4
LZ 40,12
WV 16
BW 14
M 3,56,2
LL 19,94
LL 119,95
XS 169
M 3,57,3
s_add_u32 s42, s44, s62
LA 19
PK 115,17,113,33
RV 40,8,6336
M 4,68,2
s_addc_u32 s43, s45, 0
LC_ 119
PK 113,17,113,32
BR 14
M 4,69,3
WL 6
RA 216,1,13312
XS 170
LS 44,13
WV 14
BW 15
M 5,80,2
WL 7
LO 18,18,118
LU 118,18
M 5,81,3
RA 220,1,14336
HS 25,118
RV 44,8,6400
M 2,46,4
LL 16,96
LL 122,97
XS 171
BR 15
M 2,47,5
WL 5
LA 16
PK 118,18,116,33
LS 48,14
WV 12
BW 16
M 3,58,4
LC_ 122
PK 116,18,116,32
XS 172
M 3,59,5
RA 224,1,15360
PO 118
RV 48,8,6464
M 4,70,4
LO 19,19,119
LU 119,19
ST 112
M 4,71,5
BR 16
HS 25,119
XS 173
M 5,82,4
WL 8
LL 17,98
LL 123,99
M 5,83,5
WL 6
LA 17
PK 119,19,117,33
LS 40,15
WV 11
BW 17
Z 8,36,0
s_add_u32 s42, s44, s63
LC_ 123
PK 117,19,117,32
RV 40,8,6528
XS 174
M 8,37,1
s_addc_u32 s43, s45, 0
RA 228,1,16384
PO 119
BR 17
Z 9,48,0
WL 9
LO 16,16,122
LU 122,16
XS 175
M 9,49,1
WL 6
RA 232,1,17408
HS 25,122
LS 44,16
WV 9
BW 18
Z 10,60,0
LL 18,100
LL 126,101
RV 44,8,6592
M 10,61,1
LA 18
PK 122,16,120,33
BR 18
Z 0,72,0
WL 6
LC_ 126
PK 120,16,120,32
M 0,73,1
WL 6
RA 236,1,18432
LS 48,17
WV 8
BW 19
M 8,38,2
LO 17,17,123
LU 123,17
RV 48,8,6656
M 8,39,3
ST 116
HS 25,123
s_mul_i32 s53, %[d], 196608
s_add_u32 s53, s53, s57
s_add_u32 s46, %[xlo], s53
s_addc_u32 s47, %[xhi], 0
s_bitcmp1_b64 s[92:93], 48
s_cselect_b32 s58, 16, 0
s_lshr_b32 s58, 0xfe987610, s58
v_lshrrev_b32 v3, 6, v0
v_and_b32 v3, 12, v3
v_bfe_u32 v3, s58, v3, 4
v_mad_u32_u24 v3, v3, s97, v21
BR 19
M 9,50,2
s_mul_i32 s53, s50, 25600
LL 19,102
LL 127,103
s_add_u32 s40, %[wlo], s53
s_addc_u32 s41, %[whi], 0
s_mov_b32 m0, s38
M 9,51,3
WL 5
LA 19
PK 123,17,121,33
LS 40,18
WV 8
BW 20
M 10,62,2
LC_ 127
PK 121,17,121,32
RV 40,8,6720
M 10,63,3
WV 7
RA 216,1,19456
BR 20
DW 0
M 0,74,2
WL 6
LO 18,18,126
LU 126,18
M 0,75,3
s_add_u32 s37, s37, 32768
RA 220,1,20480
HS 25,126
LS 44,19
s_cmp_eq_u32 s37, s75
BW 21
M 8,40,4
s_cselect_b32 s37, s74, s37
LL 16,64
LL 112,65
M 8,41,5
LA 16
PK 126,18,124,33
XL 24,0
DW 1024
RV 44,8,6784
M 9,52,4
s_add_u32 s42, s44, s64
LC_ 112
PK 124,18,124,32
s_addc_u32 s43, s45, 0
BR 21
M 9,53,5
WL 6
RA 224,1,21504
PO 126
LS 48,20
WV 9
BW 22
M 10,64,4
LO 19,19,127
LU 127,19
ST 120
M 10,65,5
DW 2048
HS 25,127
RV 48,8,6848
M 0,76,4
WL 8
LL 17,66
LL 113,67
RA 240,7,0
M 0,77,5
s_add_u32 s40, s40, 3072
LA 17
PK 127,19,125,33
BR 22
M 8,42,0
s_addc_u32 s41, s41, 0
LC_ 113
PK 125,19,125,32
s_add_u32 m0, s38, 3072
M 8,43,1
WL 7
RA 228,1,22528
PO 127
XL 25,64
LS 40,21
WV 11
BW 23
M 9,54,0
LO 112,16,112
LU 16,112
RA 244,7,256
M 9,55,1
RA 232,1,23552
HZ 24,16
BR 23
M 10,66,0
WL 6
LL 18,68
LL 116,69
DW 0
M 10,67,1
s_add_u32 s42, s44, s65
LA 18
HX 25
LS 44,22
s_addc_u32 s43, s45, 0
RA 248,7,512
M 0,78,0
WL 2
LC_ 116
W1 25,1024
M 0,79,1
s_lshl_b32 s53, s52, 8
RA 236,1,24576
v_add_u32 v4, s39, v4
LS 48,23
s_add_u32 s48, %[alo], s53
RA 252,7,768
M 8,44,2
LO 113,17,113
LU 17,113
s_addc_u32 s49, %[ahi], 0
s_add_u32 s52, s52, %[d]
s_cmp_eq_u32 s52, %[wrap]
s_cselect_b32 s52, %[rst], s52
DW 1024
v_mul_f32 v36, 0xbb2aaaab, v36
M 8,45,3
ST 124
HS 24,17
v_mul_f32 v37, 0xbb2aaaab, v37
M 9,56,2
s_mul_i32 s53, s51, 49152
LL 19,70
LL 117,71
s_bitcmp1_b64 s[90:91], s51
s_cselect_b64 s[44:45], s[84:85], s[82:83]
s_add_u32 s44, s44, s53
s_addc_u32 s45, s45, 0
s_add_u32 s51, s51, %[d]
s_cmp_eq_u32 s51, %[wrap]
s_cselect_b32 s51, %[rst], s51
XL 26,128
v_mul_f32 v38, 0xbb2aaaab, v38
v_mul_f32 v39, 0xbb2aaaab, v39
)"
R"(M 9,57,3
LA 19
DW 2048
LK 40,240,12,0
LK 44,242,12,2
RA 240,7,1024
v_add_u32 v1, s37, v0
LK 48,244,13,0
s_nop 0
CF 176,40,41
LK 248,246,13,2
RA 244,7,1280
CF 177,42,43
M 10,68,2
LC_ 117
RA 216,1,0
M 10,69,3
DQ 3072
v_and_b32 v22, 0xfffffcff, v1
M 0,80,2
WL 6
LO 116,18,116
LU 18,116
M 0,81,3
RA 220,1,1024
HS 24,18
CF 178,44,45
M 8,46,4
LL 16,72
LL 120,73
CF 179,46,47
M 8,47,5
LA 16
RV 252,22,5120
LK 40,248,14,0
CF 180,48,49
LK 44,250,14,2
RA 248,7,1536
CF 181,50,51
GZ 12
WL 9
CF 182,248,249
CF 183,250,251
LK 48,252,15,0
CF 184,40,41
LK 248,254,15,2
RA 252,7,1792
CF 185,42,43
GS 13
WL 8
CF 186,44,45
CF 187,46,47
LK 40,240,16,0
CF 188,48,49
LK 44,242,16,2
RA 240,7,2048
CF 189,50,51
GS 14
WL 8
CF 190,248,249
CF 191,250,251
LK 48,244,17,0
CF 192,40,41
LK 248,246,17,2
RA 244,7,2304
CF 193,42,43
GS 15
CF 194,44,45
CF 195,46,47
M 9,58,4
WL 3
LC_ 120
XL 27,192
M 9,59,5
s_add_u32 s50, s50, %[d]
RA 224,1,2048
s_cmp_eq_u32 s50, %[wrap]
s_cselect_b32 s50, %[rst], s50
LK 40,248,18,0
WL 4
CF 196,48,49
LK 44,250,18,2
s_add_u32 s38, s38, 32768
RA 248,7,2560
CF 197,50,51
GS 16
s_cmp_eq_u32 s38, s77
CF 198,248,249
CF 199,250,251
LK 48,252,19,0
WL 4
CF 200,40,41
LK 248,254,19,2
s_cselect_b32 s38, s76, s38
RA 252,7,2816
CF 201,42,43
GS 17
s_barrier
CF 202,44,45
CF 203,46,47
LK 40,240,20,0
CF 204,48,49
LK 44,242,20,2
s_sub_u32 s39, 0, s39
CF 205,50,51
GS 18
CF 206,248,249
CF 207,250,251
M 10,70,4
LO 117,19,117
LU 19,117
M 10,71,5
ds_read_b64 v[14:15], v5 offset:0
HS 24,19
M 0,82,4
LL 17,74
LL 121,75
M 0,83,5
LA 17
RA 228,1,3072
Z 2,36,0
LC_ 121
XL 28,256
M 2,37,1
WL 8
v_subrev_u32 v5, s39, v5
LK 48,244,21,0
CF 208,40,41
LK 248,246,21,2
CF 209,42,43
GS 19
CF 210,44,45
CF 211,46,47
Z 3,48,0
WL 7
LO 120,16,120
LU 16,120
M 3,49,1
WL 5
RA 232,1,4096
HS 24,16
LK 40,248,22,0
CF 212,48,49
LK 44,250,22,2
CF 213,50,51
GS 20
CF 214,248,249
CF 215,250,251
Z 4,60,0
LL 18,76
LL 124,77
M 4,61,1
LA 18
XL 29,320
Z 6,72,0
WL 3
LC_ 124
M 6,73,1
RA 236,1,6144
v_add_f32 v14, v14, v15
LK 48,252,23,0
CF 216,40,41
LK 248,254,23,2
CF 217,42,43
v_add_f32_dpp v14, v14, v14 quad_perm:[1,0,3,2] row_mask:0xf bank_mask:0xf
GS 21
CF 218,44,45
CF 219,46,47
M 2,38,2
WL 4
LO 121,17,121
LU 17,121
M 2,39,3
RV 36,8,6912
HS 24,17
M 3,50,2
LL 19,78
LL 125,79
M 3,51,3
LA 19
RA 216,1,7168
GS 22
v_add_f32_dpp v14, v14, v14 quad_perm:[2,3,0,1] row_mask:0xf bank_mask:0xf
M 4,62,2
LC_ 125
v_add_f32_dpp v14, v14, v14 row_half_mirror row_mask:0xf bank_mask:0xf
M 4,63,3
XL 30,384
v_cvt_f64_f32 v[14:15], v14
M 6,74,2
WL 4
LO 124,18,124
LU 18,124
M 6,75,3
RA 220,1,8192
HS 24,18
M 2,40,4
LL 16,88
LL 114,89
M 2,41,5
LA 16
s_mov_b64 exec, s[54:55]
SP
s_mov_b64 exec, -1
M 3,52,4
LC_ 114
CF 220,48,49
M 3,53,5
RA 224,1,9216
CF 221,50,51
M 4,64,4
WL 4
LO 125,19,125
LU 19,125
M 4,65,5
XL 31,448
HS 24,19
M 6,76,4
WL 3
LL 17,90
LL 115,91
M 6,77,5
LA 17
RA 228,1,10240
M 2,42,0
LC_ 115
HX 24
M 2,43,1
W1 24,0
CF 222,248,249
M 3,54,0
WL 5
LO 16,16,114
LU 114,16
M 3,55,1
RA 232,1,11264
HZ 25,114
M 4,66,0
LL 18,92
LL 118,93
M 4,67,1
LA 18
PK 114,16,112,33
M 6,78,0
WL 4
LC_ 118
PK 112,16,112,32
M 6,79,1
RV 252,22,5392
CF 223,250,251
M 2,44,2
WL 5
LO 17,17,115
LU 115,17
M 2,45,3
RA 236,1,12288
HS 25,115
GS 23
XL 32,512
M 3,56,2
LL 19,94
LL 119,95
M 3,57,3
s_add_u32 s42, s44, s62
LA 19
PK 115,17,113,33
M 4,68,2
s_addc_u32 s43, s45, 0
LC_ 119
PK 113,17,113,32
M 4,69,3
RA 216,1,13312
v_cndmask_b32 v52, v52, v53, s[78:79]
M 6,80,2
WL 3
LO 18,18,118
LU 118,18
M 6,81,3
RA 220,1,14336
HS 25,118
M 2,46,4
LL 16,96
LL 122,97
v_cndmask_b32 v54, v54, v55, s[78:79]
M 2,47,5
LA 16
PK 118,18,116,33
v_cndmask_b32 v52, v52, v54, s[80:81]
M 3,58,4
LC_ 122
PK 116,18,116,32
v_mul_f32 v52, 0x3b2aaaab, v52
M 3,59,5
RA 224,1,15360
PO 118
v_med3_f32 v53, v52, v39, v37
v_cmp_eq_f32 vcc, v52, v53
M 4,70,4
WL 5
LO 19,19,119
LU 119,19
ST 112
XL 33,576
C 52,52,52
M 4,71,5
HS 25,119
v_lshlrev_b32 v52, 16, v52
M 6,82,4
WL 3
LL 17,98
LL 123,99
v_add_f32 v52, v36, v52
M 6,83,5
LA 17
PK 119,19,117,33
C 52,52,52
v_lshlrev_b32 v52, 16, v52
Z 8,36,0
LC_ 123
PK 117,19,117,32
v_sqrt_f32 v52, v52
M 8,37,1
RA 228,1,16384
PO 119
C 52,52,52
Z 9,48,0
WL 4
LO 16,16,122
LU 122,16
v_lshlrev_b32 v52, 16, v52
M 9,49,1
RA 232,1,17408
HS 25,122
v_rcp_f32 v52, v52
s_nop 0
v_mul_f32 v52, v38, v52
Z 10,60,0
LL 18,56
LL 126,57
XL 34,640
v_cndmask_b32 v52, 0, v52, vcc
M 10,61,1
s_add_u32 s42, s44, s63
LA 18
PK 122,16,120,33
CF 52,52,52
Z 11,72,0
WL 2
LC_ 126
PK 120,16,120,32
ds_bpermute_b32 v13, v28, v52
M 11,73,1
s_addc_u32 s43, s45, 0
RA 236,1,18432
M 8,38,2
LO 17,17,123
LU 123,17
M 8,39,3
ST 116
HS 25,123
M 9,50,2
s_mul_i32 s53, s50, 25600
LL 19,58
LL 127,59
s_add_u32 s40, %[wlo], s53
s_addc_u32 s41, %[whi], 0
s_mov_b32 m0, s38
M 9,51,3
LA 19
PK 123,17,121,33
M 10,62,2
LC_ 127
PK 121,17,121,32
M 10,63,3
RA 216,1,19456
XL 35,704
DW 0
M 11,74,2
WL 3
LO 18,18,126
LU 126,18
M 11,75,3
s_add_u32 s37, s37, 32768
RA 220,1,20480
HS 25,126
XS 176
M 8,40,4
s_cmp_eq_u32 s37, s75
LL 16,64
LL 112,65
s_cselect_b32 s37, s74, s37
M 8,41,5
LA 16
PK 126,18,124,33
DW 1024
XS 177
M 9,52,4
s_add_u32 s42, s44, s64
LC_ 112
PK 124,18,124,32
)"
R"(s_addc_u32 s43, s45, 0
M 9,53,5
RA 224,1,21504
PO 126
XS 178
M 10,64,4
WL 5
LO 19,19,127
LU 127,19
ST 120
M 10,65,5
DW 2048
HS 25,127
XS 179
M 11,76,4
WL 3
LL 17,66
LL 113,67
M 11,77,5
s_add_u32 s40, s40, 3072
LA 17
PK 127,19,125,33
XS 180
M 8,42,0
s_addc_u32 s41, s41, 0
LC_ 113
PK 125,19,125,32
s_add_u32 m0, s38, 3072
M 8,43,1
RA 228,1,22528
PO 127
XS 181
M 9,54,0
WL 4
LO 112,16,112
LU 16,112
M 9,55,1
RA 232,1,23552
HZ 24,16
XS 182
M 10,66,0
LL 18,68
LL 116,69
DW 0
M 10,67,1
s_add_u32 s42, s44, s65
LA 18
HX 25
XS 183
M 11,78,0
WL 2
LC_ 116
W1 25,1024
M 11,79,1
s_addc_u32 s43, s45, 0
RA 236,1,24576
v_add_u32 v4, s39, v4
v_min_u32 v35, v35, v13
M 8,44,2
LO 113,17,113
LU 17,113
s_lshl_b32 s53, s52, 8
s_add_u32 s48, %[alo], s53
s_addc_u32 s49, %[ahi], 0
s_add_u32 s52, s52, %[d]
s_cmp_eq_u32 s52, %[wrap]
s_cselect_b32 s52, %[rst], s52
DW 1024
M 8,45,3
ST 124
HS 24,17
XS 184
M 9,56,2
s_mul_i32 s53, s51, 49152
LL 19,70
LL 117,71
s_bitcmp1_b64 s[90:91], s51
s_cselect_b64 s[44:45], s[84:85], s[82:83]
s_add_u32 s44, s44, s53
s_addc_u32 s45, s45, 0
s_add_u32 s51, s51, %[d]
s_cmp_eq_u32 s51, %[wrap]
s_cselect_b32 s51, %[rst], s51
M 9,57,3
LA 19
DW 2048
XS 185
M 10,68,2
LC_ 117
DQ 3072
M 10,69,3
s_add_u32 s50, s50, %[d]
v_add_u32 v1, s37, v0
XS 186
M 11,80,2
WL 2
LO 116,18,116
LU 18,116
M 11,81,3
s_cmp_eq_u32 s50, %[wrap]
RA 216,1,0
HS 24,18
XS 187
M 8,46,4
s_cselect_b32 s50, %[rst], s50
LL 16,72
LL 120,73
M 8,47,5
s_add_u32 s38, s38, 32768
LA 16
v_and_b32 v22, 0xfffffcff, v1
XS 188
M 9,58,4
s_cmp_eq_u32 s38, s77
LC_ 120
RA 220,1,1024
M 9,59,5
s_cselect_b32 s38, s76, s38
RV 252,22,5120
XS 189
M 10,70,4
WL 5
LO 117,19,117
LU 19,117
M 10,71,5
s_barrier
RA 224,1,2048
HS 24,19
XS 190
M 11,82,4
WL 2
LL 17,74
LL 121,75
XS 191
M 11,83,5
s_sub_u32 s39, 0, s39
LA 17
RA 228,1,3072
WL 3
Z 0,0,0
LC_ 121
XS 192
M 0,1,1
XS 193
XS 194
Z 1,12,0
LO 120,16,120
LU 16,120
M 1,13,1
RA 232,1,4096
HS 24,16
Z 2,36,0
LL 18,80
LL 124,81
M 2,37,1
LA 18
XS 195
Z 3,48,0
LC_ 124
XS 196
M 3,49,1
XS 197
XS 198
Z 4,60,0
WL 3
LO 121,17,121
LU 17,121
M 4,61,1
RA 236,1,6144
HS 24,17
Z 5,72,0
LL 19,82
LL 125,83
M 5,73,1
LA 19
XS 199
M 0,2,2
LC_ 125
XS 200
M 0,3,3
XS 201
XS 202
M 1,14,2
WL 3
LO 124,18,124
LU 18,124
M 1,15,3
RA 216,1,7168
HS 24,18
M 2,38,2
LL 16,88
LL 114,89
M 2,39,3
LA 16
XS 203
M 3,50,2
LC_ 114
XS 204
M 3,51,3
WV 31
BW 24
M 4,62,2
WL 4
LO 125,19,125
LU 19,125
M 4,63,3
RA 220,1,8192
HS 24,19
M 5,74,2
LL 17,90
LL 115,91
M 5,75,3
s_mov_b32 m0, %[pm]
LA 17
DF
M 0,4,4
LC_ 115
RV 40,8,6144
M 0,5,5
RV 44,8,6208
RV 48,8,6272
M 1,16,4
WL 7
LO 16,16,114
LU 114,16
XS 205
M 1,17,5
RA 224,1,9216
HZ 25,114
M 2,40,4
LL 18,92
LL 118,93
XS 206
M 2,41,5
LA 18
PK 114,16,112,33
XS 207
M 3,52,4
LC_ 118
PK 112,16,112,32
M 3,53,5
BR 24
HX 24
XS 208
M 4,64,4
WL 7
LO 17,17,115
LU 115,17
M 4,65,5
RA 228,1,10240
HS 25,115
XS 209
M 5,76,4
LL 19,94
LL 119,95
M 5,77,5
s_add_u32 s42, s44, s62
LA 19
PK 115,17,113,33
XS 210
M 0,6,0
s_addc_u32 s43, s45, 0
LC_ 119
PK 113,17,113,32
M 0,7,1
W1 24,0
XS 211
M 1,18,0
WL 5
LO 18,18,118
LU 118,18
M 1,19,1
RA 232,1,11264
HS 25,118
XS 212
M 2,42,0
LL 16,96
LL 122,97
M 2,43,1
LA 16
PK 118,18,116,33
XS 213
M 3,54,0
LC_ 122
PK 116,18,116,32
M 3,55,1
RV 252,22,5392
PO 118
XS 214
M 4,66,0
WL 5
LO 19,19,119
LU 119,19
M 4,67,1
RA 236,1,12288
HS 25,119
XS 215
M 5,78,0
LL 17,98
LL 123,99
M 5,79,1
LA 17
PK 119,19,117,33
XS 216
M 0,8,2
LC_ 123
PK 117,19,117,32
M 0,9,3
ST 112
PO 119
XS 217
M 1,20,2
WL 4
LO 16,16,122
LU 122,16
M 1,21,3
s_add_u32 s42, s44, s63
RA 216,1,13312
HS 25,122
XS 218
M 2,44,2
s_addc_u32 s43, s45, 0
LL 18,100
LL 126,101
M 2,45,3
LA 18
PK 122,16,120,33
XS 219
M 3,56,2
LC_ 126
PK 120,16,120,32
M 3,57,3
WV 29
ST 116
XS 220
BW 25
M 4,68,2
WL 4
LO 17,17,123
LU 123,17
M 4,69,3
RA 220,1,14336
HS 25,123
BR 25
XS 221
LZ 40,24
WV 26
BW 26
M 5,80,2
LL 19,102
LL 127,103
M 5,81,3
s_add_u32 s42, s44, s64
LA 19
PK 123,17,121,33
XS 222
M 0,10,4
s_addc_u32 s43, s45, 0
LC_ 127
PK 121,17,121,32
XS 223
M 0,11,5
ST 120
RV 40,8,6336
M 1,22,4
WL 7
LO 18,18,126
LU 126,18
BR 26
M 1,23,5
WL 5
RA 224,1,15360
HS 25,126
LS 44,25
WV 24
BW 27
M 2,46,4
LL 16,56
LL 104,57
M 2,47,5
LA 16
PK 126,18,124,33
M 3,58,4
LC_ 104
PK 124,18,124,32
RV 44,8,6400
M 3,59,5
ds_read_b64 v[14:15], v5 offset:0
PO 126
BR 27
M 4,70,4
WL 9
LO 19,19,127
LU 127,19
M 4,71,5
WL 7
RA 228,1,16384
HS 25,127
LS 48,26
WV 23
)"
R"(BW 28
M 5,82,4
LL 17,58
LL 105,59
M 5,83,5
LA 17
PK 127,19,125,33
RV 48,8,6464
Z 6,0,0
s_add_u32 s42, s44, s65
LC_ 105
PK 125,19,125,32
M 6,1,1
s_addc_u32 s43, s45, 0
BR 28
PO 127
Z 7,12,0
WL 9
LO 104,16,104
LU 16,104
M 7,13,1
WL 6
RA 232,1,17408
HZ 26,16
LS 40,27
WV 22
BW 29
Z 8,36,0
WV 21
LL 18,60
LL 108,61
M 8,37,1
WL 2
LA 18
ST 124
RV 40,8,6528
Z 9,48,0
s_mul_i32 s53, s50, 25600
LC_ 108
BR 29
M 9,49,1
s_add_u32 s40, %[wlo], s53
v_subrev_u32 v5, s39, v5
HX 25
LS 44,28
s_addc_u32 s41, %[whi], 0
BW 30
Z 10,60,0
LO 105,17,105
LU 17,105
M 10,61,1
s_mov_b32 m0, s38
RA 236,1,18432
HS 26,17
Z 11,72,0
LL 19,62
LL 109,63
RV 44,8,6592
M 11,73,1
LA 19
W1 25,1024
BR 30
M 6,2,2
LC_ 109
DW 0
M 6,3,3
WL 7
v_add_f32 v14, v14, v15
v_add_u32 v4, s39, v4
LS 48,29
WV 21
BW 31
M 7,14,2
LO 108,18,108
LU 18,108
M 7,15,3
s_add_u32 s37, s37, 32768
RA 216,1,19456
HS 26,18
M 8,38,2
s_cmp_eq_u32 s37, s75
LL 16,64
LL 112,65
RV 48,8,6656
M 8,39,3
s_cselect_b32 s37, s74, s37
LA 16
DW 1024
BR 31
M 9,50,2
LC_ 112
v_add_f32_dpp v14, v14, v14 quad_perm:[1,0,3,2] row_mask:0xf bank_mask:0xf
M 9,51,3
WL 7
v_add_f32_dpp v14, v14, v14 quad_perm:[2,3,0,1] row_mask:0xf bank_mask:0xf
LS 40,30
WV 21
BW 32
M 10,62,2
WL 7
LO 109,19,109
LU 19,109
M 10,63,3
RA 220,1,20480
HS 26,19
RV 40,8,6720
M 11,74,2
LL 17,66
LL 113,67
BR 32
M 11,75,3
LA 17
DW 2048
M 6,4,4
LC_ 113
v_add_f32_dpp v14, v14, v14 row_half_mirror row_mask:0xf bank_mask:0xf
M 6,5,5
WL 7
v_cvt_f64_f32 v[14:15], v14
HX 26
LS 44,31
WV 19
BW 33
M 7,16,4
WL 3
LO 112,16,112
LU 16,112
RV 44,8,6784
M 7,17,5
s_mul_i32 s53, s51, 49152
RA 224,1,21504
HZ 24,16
M 8,40,4
s_bitcmp1_b64 s[90:91], s51
LL 18,68
LL 116,69
BR 33
M 8,41,5
s_cselect_b64 s[44:45], s[84:85], s[82:83]
LA 18
s_mov_b64 exec, s[54:55]
SP
s_mov_b64 exec, -1
LS 48,32
s_add_u32 s44, s44, s53
BW 34
M 9,52,4
s_addc_u32 s45, s45, 0
LC_ 116
RV 48,8,6848
M 9,53,5
s_bitcmp1_b64 s[92:93], s51
RA 240,7,0
BR 34
M 10,64,4
WL 10
LO 113,17,113
LU 17,113
M 10,65,5
s_cselect_b64 s[94:95], s[88:89], s[86:87]
RA 228,1,22528
HS 24,17
M 11,76,4
WL 7
LL 19,70
LL 117,71
M 11,77,5
s_add_u32 s94, s94, s53
LA 19
M 6,6,0
s_addc_u32 s95, s95, 0
LC_ 117
M 6,7,1
s_add_u32 s51, s51, %[d]
LS 40,33
s_cmp_eq_u32 s51, %[wrap]
WV 18
BW 35
M 7,18,0
WL 4
LO 116,18,116
LU 18,116
RA 244,7,256
M 7,19,1
s_cselect_b32 s51, %[rst], s51
RA 232,1,23552
HS 24,18
M 8,42,0
s_mul_i32 s53, %[d], 0
LL 16,72
LL 120,73
BR 35
M 8,43,1
s_add_u32 s60, s53, %[sb1]
LA 16
s_add_u32 s61, s60, 512
LS 44,34
s_add_u32 s40, s40, 3072
RA 248,7,512
M 9,54,0
s_addc_u32 s41, s41, 0
LC_ 120
s_add_u32 m0, s38, 3072
M 9,55,1
DW 0
M 10,66,0
WL 7
LO 117,19,117
LU 19,117
M 10,67,1
WL 3
RA 236,1,24576
HS 24,19
LS 48,35
RA 252,7,768
M 11,78,0
LL 17,74
LL 121,75
s_nop 3
v_mul_f32 v36, 0xbb2aaaab, v36
M 11,79,1
LA 17
DW 1024
v_mul_f32 v37, 0xbb2aaaab, v37
v_mul_f32 v38, 0xbb2aaaab, v38
M 6,8,2
LC_ 121
DW 2048
v_mul_f32 v39, 0xbb2aaaab, v39
M 6,9,3
s_lshl_b32 s53, s52, 8
v_add_u32 v1, s37, v0
LK 40,240,24,0
LK 44,242,24,2
s_add_u32 s48, %[alo], s53
RA 240,7,1024
LK 48,244,25,0
s_nop 0
CF 224,40,41
LK 248,246,25,2
s_addc_u32 s49, %[ahi], 0
RA 244,7,1280
CF 225,42,43
M 7,20,2
WL 6
LO 120,16,120
LU 16,120
M 7,21,3
s_add_u32 s52, s52, %[d]
RA 216,1,0
HS 24,16
M 8,44,2
s_cmp_eq_u32 s52, %[wrap]
LL 18,76
LL 124,77
CF 226,44,45
M 8,45,3
s_cselect_b32 s52, %[rst], s52
LA 18
DQ 3072
CF 227,46,47
LK 40,248,26,0
CF 228,48,49
LK 44,250,26,2
RA 248,7,1536
CF 229,50,51
GZ 24
WL 7
CF 230,248,249
CF 231,250,251
LK 48,252,27,0
CF 232,40,41
LK 248,254,27,2
RA 252,7,1792
CF 233,42,43
GS 25
WL 5
CF 234,44,45
CF 235,46,47
LK 40,240,28,0
CF 236,48,49
LK 44,242,28,2
RA 240,7,2048
CF 237,50,51
GS 26
WL 5
CF 238,248,249
CF 239,250,251
LK 48,244,29,0
CF 240,40,41
LK 248,246,29,2
RA 244,7,2304
CF 241,42,43
GS 27
CF 242,44,45
CF 243,46,47
M 9,56,2
LC_ 124
v_and_b32 v22, 0xfffffcff, v1
M 9,57,3
WL 4
LK 40,248,30,0
CF 244,48,49
LK 44,250,30,2
RA 248,7,2560
CF 245,50,51
GS 28
WL 4
CF 246,248,249
CF 247,250,251
LK 48,252,31,0
CF 40,40,41
LK 248,254,31,2
RA 252,7,2816
CF 42,42,43
GS 29
CF 44,44,45
CF 46,46,47
M 10,68,2
LO 121,17,121
LU 17,121
M 10,69,3
s_add_u32 s50, s50, %[d]
RA 220,1,1024
HS 24,17
M 11,80,2
LL 19,78
LL 125,79
M 11,81,3
s_cmp_eq_u32 s50, %[wrap]
LA 19
RV 252,22,5120
M 6,10,4
s_cselect_b32 s50, %[rst], s50
LC_ 125
AW 0,40
M 6,11,5
AW 1,42
AW 2,44
M 7,22,4
WL 6
LO 124,18,124
LU 18,124
M 7,23,5
s_add_u32 s38, s38, 32768
RA 224,1,2048
HS 24,18
M 8,46,4
s_cmp_eq_u32 s38, s77
LL 16,80
LL 106,81
M 8,47,5
s_cselect_b32 s38, s76, s38
LA 16
AW 3,46
LK 40,240,32,0
CF 48,48,49
LK 44,242,32,2
s_barrier
CF 50,50,51
GS 30
s_sub_u32 s39, 0, s39
CF 248,248,249
CF 250,250,251
M 9,58,4
LC_ 106
AW 4,48
M 9,59,5
AW 5,50
AW 6,248
M 10,70,4
WL 3
LO 125,19,125
LU 19,125
M 10,71,5
RA 228,1,3072
HS 24,19
M 11,82,4
LL 17,82
LL 107,83
M 11,83,5
LA 17
HX 24
Z 0,0,0
LC_ 107
W1 24,0
M 0,1,1
W1 26,2048
AW 7,250
LK 48,244,33,0
CF 40,40,41
LK 248,246,33,2
CF 42,42,43
GS 31
CF 44,44,45
CF 46,46,47
Z 1,12,0
WL 5
LO 16,16,106
LU 106,16
)"
R"(M 1,13,1
RA 232,1,4096
HZ 27,106
Z 2,36,0
LL 18,84
LL 110,85
M 2,37,1
LA 18
PK 106,16,104,33
Z 3,48,0
LC_ 110
PK 104,16,104,32
M 3,49,1
AW 8,40
AW 9,42
Z 4,60,0
WL 5
LO 17,17,107
LU 107,17
M 4,61,1
RA 236,1,6144
HS 27,107
Z 5,72,0
LL 19,86
LL 111,87
M 5,73,1
s_add_u32 s42, s94, s60
LA 19
PK 107,17,105,33
M 0,2,2
s_addc_u32 s43, s95, 0
LC_ 111
PK 105,17,105,32
M 0,3,3
SU 104
AW 10,44
M 1,14,2
WL 3
LO 18,18,110
LU 110,18
M 1,15,3
RA 216,1,7168
HS 27,110
M 2,38,2
LL 16,88
LL 114,89
M 2,39,3
LA 16
PK 110,18,108,33
M 3,50,2
LC_ 114
PK 108,18,108,32
AW 11,46
M 3,51,3
PO 110
LK 40,248,34,0
CF 48,48,49
LK 44,250,34,2
CF 50,50,51
GS 32
CF 248,248,249
CF 250,250,251
M 4,62,2
WL 3
LO 19,19,111
LU 111,19
M 4,63,3
RA 220,1,8192
HS 27,111
M 5,74,2
LL 17,90
LL 115,91
AW 12,48
AW 13,50
M 5,75,3
s_mov_b32 m0, %[pm]
LA 17
PK 111,19,109,33
AW 14,248
AW 15,250
LK 48,252,35,0
CF 40,40,41
LK 248,254,35,2
CF 42,42,43
HX 27
GS 33
CF 44,44,45
CF 46,46,47
M 0,4,4
LC_ 115
PK 109,19,109,32
M 0,5,5
DF
PO 111
M 1,16,4
WL 3
LO 16,16,114
LU 114,16
M 1,17,5
s_add_u32 s42, s94, s61
RA 224,1,9216
HZ 25,114
AW 16,40
M 2,40,4
s_addc_u32 s43, s95, 0
LL 18,92
LL 118,93
AW 17,42
AW 18,44
M 2,41,5
LA 18
PK 114,16,112,33
AW 19,46
RV 36,8,6912
M 3,52,4
LC_ 118
PK 112,16,112,32
M 3,53,5
SU 108
GS 34
CF 48,48,49
CF 50,50,51
CF 248,248,249
M 4,64,4
WL 4
LO 17,17,115
LU 115,17
CF 250,250,251
AW 20,48
M 4,65,5
RA 228,1,10240
HS 25,115
AW 21,50
AW 22,248
M 5,76,4
LL 19,94
LL 119,95
AW 23,250
M 5,77,5
s_add_u32 s42, s44, s62
LA 19
PK 115,17,113,33
GS 35
s_addc_u32 s43, s45, 0
s_nop 6
v_cndmask_b32 v52, v52, v53, s[78:79]
M 0,6,0
LC_ 119
PK 113,17,113,32
v_cndmask_b32 v54, v54, v55, s[78:79]
M 0,7,1
ST 112
v_cndmask_b32 v52, v52, v54, s[80:81]
M 1,18,0
WL 3
LO 18,18,118
LU 118,18
v_mul_f32 v52, 0x3b2aaaab, v52
M 1,19,1
RA 232,1,11264
HS 25,118
v_med3_f32 v53, v52, v39, v37
M 2,42,0
LL 16,96
LL 122,97
v_cmp_eq_f32 vcc, v52, v53
M 2,43,1
LA 16
PK 118,18,116,33
C 52,52,52
M 3,54,0
LC_ 122
PK 116,18,116,32
v_lshlrev_b32 v52, 16, v52
M 3,55,1
RV 252,22,5392
PO 118
v_add_f32 v52, v36, v52
M 4,66,0
WL 4
LO 19,19,119
LU 119,19
C 52,52,52
M 4,67,1
RA 236,1,12288
HS 25,119
v_lshlrev_b32 v52, 16, v52
M 5,78,0
LL 17,98
LL 123,99
v_sqrt_f32 v52, v52
M 5,79,1
LA 17
PK 119,19,117,33
C 52,52,52
M 0,8,2
s_add_u32 s42, s44, s63
LC_ 123
PK 117,19,117,32
v_lshlrev_b32 v52, 16, v52
M 0,9,3
s_addc_u32 s43, s45, 0
PO 119
v_rcp_f32 v52, v52
M 1,20,2
WL 4
LO 16,16,122
LU 122,16
v_mul_f32 v52, v38, v52
M 1,21,3
RA 216,1,13312
HS 25,122
v_cndmask_b32 v52, 0, v52, vcc
M 2,44,2
LL 18,100
LL 126,101
CF 52,52,52
M 2,45,3
LA 18
PK 122,16,120,33
ds_bpermute_b32 v13, v28, v52
M 3,56,2
LC_ 126
PK 120,16,120,32
M 3,57,3
ST 116
AR 248,0
M 4,68,2
WL 4
LO 17,17,123
LU 123,17
M 4,69,3
RA 220,1,14336
HS 25,123
M 5,80,2
LL 19,102
LL 127,103
M 5,81,3
s_add_u32 s42, s44, s64
LA 19
PK 123,17,121,33
M 0,10,4
s_addc_u32 s43, s45, 0
LC_ 127
PK 121,17,121,32
M 0,11,5
ST 120
AR 249,1
M 1,22,4
WL 3
LO 18,18,126
LU 126,18
M 1,23,5
RA 224,1,15360
HS 25,126
M 2,46,4
LL 16,56
LL 104,57
M 2,47,5
LA 16
PK 126,18,124,33
M 3,58,4
LC_ 104
PK 124,18,124,32
AR 250,2
M 3,59,5
ds_read_b64 v[14:15], v5 offset:0
PO 126
M 4,70,4
WL 4
LO 19,19,127
LU 127,19
AR 251,3
M 4,71,5
RA 228,1,16384
HS 25,127
AR 52,4
M 5,82,4
LL 17,58
LL 105,59
AR 53,5
M 5,83,5
LA 17
PK 127,19,125,33
Z 6,0,0
s_add_u32 s42, s44, s65
LC_ 105
PK 125,19,125,32
AR 54,6
M 6,1,1
s_addc_u32 s43, s45, 0
PO 127
v_subrev_u32 v5, s39, v5
AR 55,7
Z 7,12,0
WL 4
LO 104,16,104
LU 16,104
XS 224
M 7,13,1
RA 232,1,17408
HZ 26,16
Z 8,36,0
LL 18,60
LL 108,61
XS 225
M 8,37,1
WL 4
LA 18
ST 124
XS 226
Z 9,48,0
s_mul_i32 s53, s50, 25600
LC_ 108
v_add_f32 v14, v14, v15
M 9,49,1
s_add_u32 s40, %[wlo], s53
v_add_f32_dpp v14, v14, v14 quad_perm:[1,0,3,2] row_mask:0xf bank_mask:0xf
HX 25
XS 227
Z 10,60,0
WL 3
LO 105,17,105
LU 17,105
XS 228
M 10,61,1
s_addc_u32 s41, %[whi], 0
RA 236,1,18432
HS 26,17
XS 229
Z 11,72,0
LL 19,62
LL 109,63
M 11,73,1
s_mov_b32 m0, s38
LA 19
W1 25,1024
XS 230
M 6,2,2
LC_ 109
W1 27,3072
XS 231
M 6,3,3
DW 0
v_add_f32_dpp v14, v14, v14 quad_perm:[2,3,0,1] row_mask:0xf bank_mask:0xf
v_min_u32 v35, v35, v13
M 7,14,2
WL 5
LO 108,18,108
LU 18,108
M 7,15,3
s_add_u32 s37, s37, 32768
RA 216,1,19456
HS 26,18
XS 232
M 8,38,2
s_cmp_eq_u32 s37, s75
LL 16,64
LL 112,65
XS 233
M 8,39,3
s_cselect_b32 s37, s74, s37
LA 16
v_add_f32_dpp v14, v14, v14 row_half_mirror row_mask:0xf bank_mask:0xf
XS 234
M 9,50,2
s_mul_i32 s53, %[d], 196608
LC_ 112
v_cvt_f64_f32 v[14:15], v14
M 9,51,3
s_add_u32 s53, s53, s57
s_mov_b64 exec, s[54:55]
SP
s_mov_b64 exec, -1
v_add_u32 v4, s39, v4
XS 235
M 10,62,2
WL 4
LO 109,19,109
LU 19,109
XS 236
M 10,63,3
s_add_u32 s46, %[xlo], s53
RA 220,1,20480
HS 26,19
M 11,74,2
LL 17,66
LL 113,67
XS 237
M 11,75,3
s_addc_u32 s47, %[xhi], 0
LA 17
HX 26
XS 238
M 6,4,4
s_bitcmp1_b64 s[92:93], 48
LC_ 113
XS 239
M 6,5,5
s_cselect_b32 s58, 16, 0
)"
R"(XS 240
XS 241
M 7,16,4
WL 3
LO 112,16,112
LU 16,112
M 7,17,5
s_lshr_b32 s58, 0xda43cb52, s58
RA 224,1,21504
HZ 24,16
M 8,40,4
LL 18,68
LL 116,69
XS 242
M 8,41,5
LA 18
v_lshrrev_b32 v3, 6, v0
M 9,52,4
LC_ 116
v_and_b32 v3, 12, v3
v_bfe_u32 v3, s58, v3, 4
v_mad_u32_u24 v3, v3, s97, v21
XS 243
M 9,53,5
DW 1024
XS 244
M 10,64,4
WL 3
LO 113,17,113
LU 17,113
XS 245
M 10,65,5
RA 228,1,22528
HS 24,17
M 11,76,4
LL 19,70
LL 117,71
XS 246
M 11,77,5
LA 19
XL 36,0
XS 247
M 6,6,0
LC_ 117
DW 2048
M 6,7,1
XL 37,64
XS 248
M 7,18,0
WL 3
LO 116,18,116
LU 18,116
XS 249
M 7,19,1
s_mul_i32 s53, s51, 49152
RA 232,1,23552
HS 24,18
XS 250
M 8,42,0
s_bitcmp1_b64 s[90:91], s51
LL 16,72
LL 120,73
M 8,43,1
s_cselect_b64 s[44:45], s[84:85], s[82:83]
LA 16
s_add_u32 s44, s44, s53
s_addc_u32 s45, s45, 0
s_bitcmp1_b64 s[92:93], s51
s_cselect_b64 s[94:95], s[88:89], s[86:87]
s_add_u32 s94, s94, s53
s_addc_u32 s95, s95, 0
s_add_u32 s51, s51, %[d]
s_cmp_eq_u32 s51, %[wrap]
s_cselect_b32 s51, %[rst], s51
XS 251
M 9,54,0
s_add_u32 s40, s40, 3072
LC_ 120
s_addc_u32 s41, s41, 0
s_add_u32 m0, s38, 3072
AW 0,248
M 9,55,1
XL 38,128
AW 1,249
M 10,66,0
WL 3
LO 117,19,117
LU 19,117
M 10,67,1
RA 236,1,24576
HS 24,19
AW 2,250
M 11,78,0
LL 17,74
LL 121,75
AW 3,251
M 11,79,1
LA 17
DW 0
XS 52
M 6,8,2
LC_ 121
XL 39,192
M 6,9,3
DW 1024
v_add_u32 v1, s37, v0
XS 53
M 7,20,2
WL 3
LO 120,16,120
LU 16,120
XS 54
M 7,21,3
RA 216,1,0
HS 24,16
M 8,44,2
LL 18,76
LL 124,77
XS 55
M 8,45,3
LA 18
XL 40,256
AW 4,52
M 9,56,2
LC_ 124
DW 2048
AW 5,53
M 9,57,3
XL 41,320
v_and_b32 v22, 0xfffffcff, v1
M 10,68,2
WL 3
LO 121,17,121
LU 17,121
AW 6,54
M 10,69,3
s_lshl_b32 s53, s52, 8
RA 220,1,1024
HS 24,17
AW 7,55
M 11,80,2
LL 19,78
LL 125,79
AR 248,8
M 11,81,3
s_add_u32 s48, %[alo], s53
LA 19
RV 252,22,5120
M 6,10,4
s_addc_u32 s49, %[ahi], 0
LC_ 125
AR 249,9
M 6,11,5
s_add_u32 s52, s52, %[d]
AR 250,10
AR 251,11
M 7,22,4
WL 4
LO 124,18,124
LU 18,124
M 7,23,5
s_cmp_eq_u32 s52, %[wrap]
RA 224,1,2048
HS 24,18
M 8,46,4
s_cselect_b32 s52, %[rst], s52
LL 16,80
LL 106,81
XS 248
M 8,47,5
LA 16
DQ 3072
XS 249
M 9,58,4
LC_ 106
XL 42,384
M 9,59,5
s_add_u32 s50, s50, %[d]
XS 250
XS 251
M 10,70,4
WL 3
LO 125,19,125
LU 19,125
M 10,71,5
RA 228,1,3072
HS 24,19
AW 8,248
M 11,82,4
LL 17,82
LL 107,83
M 11,83,5
LA 17
HX 24
s_cmp_eq_u32 s50, %[wrap]
s_cselect_b32 s50, %[rst], s50
XL 43,448
AW 9,249
Z 0,0,0
WV 36
LC_ 107
XL 44,512
s_add_u32 s38, s38, 32768
s_cmp_eq_u32 s38, s77
s_cselect_b32 s38, s76, s38
s_barrier
s_sub_u32 s39, 0, s39
AW 10,250
M 0,1,1
ds_read_b64 v[14:15], v5 offset:0
XL 45,576
AW 11,251
Z 1,12,0
WL 4
LO 16,16,106
LU 106,16
M 1,13,1
RA 232,1,4096
HZ 27,106
AR 52,12
Z 4,60,0
LL 18,84
LL 110,85
AR 53,13
M 4,61,1
LA 18
PK 106,16,104,33
AR 54,14
Z 5,72,0
WL 5
LC_ 110
PK 104,16,104,32
XL 46,640
M 5,73,1
RA 236,1,6144
W1 24,0
W1 26,2048
AR 55,15
M 0,2,2
WL 7
LO 17,17,107
LU 107,17
XS 52
M 0,3,3
XL 47,704
HS 27,107
M 1,14,2
LL 19,86
LL 111,87
XS 53
M 1,15,3
s_add_u32 s42, s94, s60
LA 19
PK 107,17,105,33
XS 54
M 4,62,2
s_addc_u32 s43, s95, 0
LC_ 111
PK 105,17,105,32
XS 55
M 4,63,3
WL 8
RA 216,1,7168
v_add_f32 v14, v14, v15
s_nop 1
v_add_f32_dpp v14, v14, v14 quad_perm:[1,0,3,2] row_mask:0xf bank_mask:0xf
M 5,74,2
WL 5
LO 18,18,110
LU 110,18
v_add_f32_dpp v14, v14, v14 quad_perm:[2,3,0,1] row_mask:0xf bank_mask:0xf
AW 12,52
M 5,75,3
RA 220,1,8192
HS 27,110
AW 13,53
M 0,4,4
LL 16,88
LL 114,89
v_add_f32_dpp v14, v14, v14 row_half_mirror row_mask:0xf bank_mask:0xf
AW 14,54
M 0,5,5
LA 16
PK 110,18,108,33
M 1,16,4
LC_ 114
PK 108,18,108,32
v_cvt_f64_f32 v[14:15], v14
AW 15,55
M 1,17,5
RA 224,1,9216
PO 110
AR 248,16
M 4,64,4
WL 5
LO 19,19,111
LU 111,19
s_mov_b64 exec, s[54:55]
SP
s_mov_b64 exec, -1
SU 104
AR 249,17
M 4,65,5
ds_read_b64 v[14:15], v5 offset:2048
HS 27,111
M 5,76,4
WL 4
LL 17,90
LL 115,91
AR 250,18
M 5,77,5
WV 18
LA 17
PK 111,19,109,33
AR 251,19
M 0,6,0
s_add_u32 s42, s94, s61
LC_ 115
PK 109,19,109,32
M 0,7,1
s_addc_u32 s43, s95, 0
RA 228,1,10240
PO 111
XS 248
BW 36
M 1,18,0
WL 6
LO 16,16,114
LU 114,16
XS 249
M 1,19,1
RA 232,1,11264
HZ 25,114
RV 40,8,6144
XS 250
M 4,66,0
LL 18,92
LL 118,93
RV 44,8,6208
M 4,67,1
LA 18
PK 114,16,112,33
v_subrev_u32 v5, s39, v5
HX 27
XS 251
RV 48,8,6272
M 5,78,0
WL 6
LC_ 118
PK 112,16,112,32
BR 36
AW 16,248
M 5,79,1
WV 16
RV 252,22,5392
v_add_f32 v14, v14, v15
BW 37
AW 17,249
M 0,8,2
LO 17,17,115
LU 115,17
BR 37
M 0,9,3
WL 3
RA 236,1,12288
HS 25,115
v_add_f32_dpp v14, v14, v14 quad_perm:[1,0,3,2] row_mask:0xf bank_mask:0xf
SU 108
AW 18,250
LZ 40,36
WV 16
BW 38
M 1,20,2
LL 19,94
LL 119,95
AW 19,251
M 1,21,3
s_add_u32 s42, s44, s62
LA 19
PK 115,17,113,33
v_add_f32_dpp v14, v14, v14 quad_perm:[2,3,0,1] row_mask:0xf bank_mask:0xf
RV 40,8,6336
AR 52,20
M 4,68,2
s_addc_u32 s43, s45, 0
LC_ 119
PK 113,17,113,32
BR 38
M 4,69,3
WL 6
RA 216,1,13312
v_add_f32_dpp v14, v14, v14 row_half_mirror row_mask:0xf bank_mask:0xf
AR 53,21
LS 44,37
WV 14
BW 39
M 5,80,2
WL 7
LO 18,18,118
LU 118,18
)"
R"(AR 54,22
M 5,81,3
RA 220,1,14336
HS 25,118
v_cvt_f64_f32 v[14:15], v14
RV 44,8,6400
M 0,10,4
LL 16,96
LL 122,97
AR 55,23
BR 39
M 0,11,5
WL 5
LA 16
PK 118,18,116,33
s_mov_b64 exec, s[54:55]
SP
s_mov_b64 exec, -1
XS 52
LS 48,38
WV 13
BW 40
M 1,22,4
LC_ 122
PK 116,18,116,32
XS 53
M 1,23,5
RA 224,1,15360
PO 118
RV 48,8,6464
M 4,70,4
LO 19,19,119
LU 119,19
ST 112
XS 54
M 4,71,5
BR 40
HS 25,119
XS 55
M 5,82,4
WL 8
LL 17,98
LL 123,99
AW 20,52
M 5,83,5
WL 6
LA 17
PK 119,19,117,33
LS 40,39
WV 12
BW 41
Z 6,0,0
s_add_u32 s42, s44, s63
LC_ 123
PK 117,19,117,32
RV 40,8,6528
AW 21,53
M 6,1,1
s_addc_u32 s43, s45, 0
RA 228,1,16384
PO 119
BR 41
AW 22,54
Z 7,12,0
WL 9
LO 16,16,122
LU 122,16
AW 23,55
M 7,13,1
WL 6
RA 232,1,17408
HS 25,122
LS 44,40
WV 10
BW 42
Z 10,60,0
LL 18,100
LL 126,101
RV 44,8,6592
M 10,61,1
LA 18
PK 122,16,120,33
BR 42
Z 2,72,0
WL 6
LC_ 126
PK 120,16,120,32
M 2,73,1
WL 6
RA 236,1,18432
LS 48,41
WV 9
BW 43
M 6,2,2
LO 17,17,123
LU 123,17
RV 48,8,6656
M 6,3,3
ST 116
HS 25,123
s_mul_i32 s53, %[d], 393216
s_add_u32 s53, s53, s57
s_add_u32 s46, %[xlo], s53
s_addc_u32 s47, %[xhi], 0
s_bitcmp1_b64 s[92:93], 48
s_cselect_b32 s58, 16, 0
s_lshr_b32 s58, 0xfe987610, s58
v_lshrrev_b32 v3, 6, v0
v_and_b32 v3, 12, v3
v_bfe_u32 v3, s58, v3, 4
v_mad_u32_u24 v3, v3, s97, v21
BR 43
M 7,14,2
s_mul_i32 s53, s50, 25600
LL 19,102
LL 127,103
s_add_u32 s40, %[wlo], s53
s_addc_u32 s41, %[whi], 0
s_mov_b32 m0, s38
M 7,15,3
WL 5
LA 19
PK 123,17,121,33
LS 40,42
WV 9
BW 44
M 10,62,2
LC_ 127
PK 121,17,121,32
RV 40,8,6720
M 10,63,3
WV 8
RA 216,1,19456
BR 44
DW 0
M 2,74,2
WL 6
LO 18,18,126
LU 126,18
M 2,75,3
s_add_u32 s37, s37, 32768
RA 220,1,20480
HS 25,126
LS 44,43
s_cmp_eq_u32 s37, s75
BW 45
M 6,4,4
s_cselect_b32 s37, s74, s37
LL 16,56
LL 104,57
M 6,5,5
LA 16
PK 126,18,124,33
XL 48,0
DW 1024
RV 44,8,6784
M 7,16,4
s_add_u32 s42, s44, s64
LC_ 104
PK 124,18,124,32
s_addc_u32 s43, s45, 0
BR 45
M 7,17,5
WL 6
RA 224,1,21504
PO 126
LS 48,44
WV 10
BW 46
M 10,64,4
LO 19,19,127
LU 127,19
ST 120
M 10,65,5
DW 2048
HS 25,127
RV 48,8,6848
M 2,76,4
WL 8
LL 17,58
LL 105,59
RA 240,7,0
M 2,77,5
s_add_u32 s40, s40, 3072
LA 17
PK 127,19,125,33
BR 46
M 6,6,0
s_addc_u32 s41, s41, 0
LC_ 105
PK 125,19,125,32
s_add_u32 m0, s38, 3072
M 6,7,1
WL 7
RA 228,1,22528
PO 127
XL 49,64
LS 40,45
WV 12
BW 47
M 7,18,0
LO 104,16,104
LU 16,104
RA 244,7,256
M 7,19,1
RA 232,1,23552
HZ 26,16
BR 47
M 10,66,0
WL 6
LL 18,60
LL 108,61
DW 0
M 10,67,1
s_add_u32 s42, s44, s65
LA 18
HX 25
LS 44,46
s_addc_u32 s43, s45, 0
RA 248,7,512
M 2,78,0
WL 2
LC_ 108
W1 25,1024
W1 27,3072
M 2,79,1
s_lshl_b32 s53, s52, 8
RA 236,1,24576
v_add_u32 v4, s39, v4
LS 48,47
s_add_u32 s48, %[alo], s53
RA 252,7,768
M 6,8,2
LO 105,17,105
LU 17,105
s_addc_u32 s49, %[ahi], 0
s_add_u32 s52, s52, %[d]
s_cmp_eq_u32 s52, %[wrap]
s_cselect_b32 s52, %[rst], s52
DW 1024
v_mul_f32 v36, 0xbb2aaaab, v36
M 6,9,3
ST 124
HS 26,17
v_mul_f32 v37, 0xbb2aaaab, v37
v_mul_f32 v38, 0xbb2aaaab, v38
M 7,20,2
s_mul_i32 s53, s51, 49152
LL 19,62
LL 109,63
s_bitcmp1_b64 s[90:91], s51
s_cselect_b64 s[44:45], s[84:85], s[82:83]
s_add_u32 s44, s44, s53
s_addc_u32 s45, s45, 0
s_bitcmp1_b64 s[92:93], s51
s_cselect_b64 s[94:95], s[88:89], s[86:87]
s_add_u32 s94, s94, s53
s_addc_u32 s95, s95, 0
s_add_u32 s51, s51, %[d]
s_cmp_eq_u32 s51, %[wrap]
s_cselect_b32 s51, %[rst], s51
XL 50,128
v_mul_f32 v39, 0xbb2aaaab, v39
M 7,21,3
LA 19
DW 2048
LK 40,240,36,0
LK 44,242,36,2
RA 240,7,1024
v_add_u32 v1, s37, v0
LK 48,244,37,0
s_nop 0
CF 40,40,41
LK 248,246,37,2
RA 244,7,1280
CF 42,42,43
M 10,68,2
LC_ 109
RA 216,1,0
M 10,69,3
DQ 3072
v_and_b32 v22, 0xfffffcff, v1
CF 44,44,45
CF 46,46,47
M 2,80,2
WL 6
LO 108,18,108
LU 18,108
AW 24,40
AW 25,42
M 2,81,3
RA 220,1,1024
HS 26,18
AW 26,44
AW 27,46
LK 40,248,38,0
CF 48,48,49
LK 44,250,38,2
RA 248,7,1536
CF 50,50,51
GZ 36
CF 248,248,249
CF 250,250,251
M 6,10,4
LL 16,72
LL 120,73
M 6,11,5
LA 16
RV 252,22,5120
M 7,22,4
WL 9
LC_ 120
XL 51,192
AW 28,48
M 7,23,5
s_add_u32 s50, s50, %[d]
RA 224,1,2048
s_cmp_eq_u32 s50, %[wrap]
s_cselect_b32 s50, %[rst], s50
AW 29,50
AW 30,248
AW 31,250
LK 48,252,39,0
CF 40,40,41
LK 248,254,39,2
s_add_u32 s38, s38, 32768
RA 252,7,1792
CF 42,42,43
GS 37
s_cmp_eq_u32 s38, s77
CF 44,44,45
CF 46,46,47
M 10,70,4
WL 8
LO 109,19,109
LU 19,109
M 10,71,5
s_cselect_b32 s38, s76, s38
HS 26,19
AW 32,40
M 2,82,4
WL 3
LL 17,74
LL 121,75
M 2,83,5
s_barrier
LA 17
RA 228,1,3072
s_sub_u32 s39, 0, s39
AW 33,42
Z 0,0,0
LC_ 121
ds_read_b64 v[14:15], v5 offset:0
AW 34,44
AW 35,46
M 0,1,1
XL 52,256
HX 26
LK 40,240,40,0
CF 48,48,49
LK 44,242,40,2
RA 240,7,2048
CF 50,50,51
GS 38
CF 248,248,249
CF 250,250,251
Z 1,12,0
WL 7
LO 120,16,120
LU 16,120
M 1,13,1
RA 232,1,4096
HZ 24,16
AW 36,48
Z 4,60,0
LL 18,76
LL 124,77
AW 37,50
AW 38,248
M 4,61,1
LA 18
XL 53,320
AW 39,250
LK 48,244,41,0
CF 40,40,41
LK 248,246,41,2
RA 244,7,2304
CF 42,42,43
GS 39
CF 44,44,45
CF 46,46,47
Z 8,72,0
WL 6
LC_ 124
AW 40,40
M 8,73,1
RA 236,1,6144
AW 41,42
M 0,2,2
WL 7
)"
R"(LO 121,17,121
LU 17,121
M 0,3,3
HS 24,17
AW 42,44
AW 43,46
LK 40,248,42,0
CF 48,48,49
LK 44,250,42,2
RA 248,7,2560
CF 50,50,51
GS 40
CF 248,248,249
CF 250,250,251
M 1,14,2
LL 19,78
LL 125,79
M 1,15,3
LA 19
RA 216,1,7168
M 4,62,2
WL 9
LC_ 125
v_add_f32 v14, v14, v15
s_nop 1
v_add_f32_dpp v14, v14, v14 quad_perm:[1,0,3,2] row_mask:0xf bank_mask:0xf
M 4,63,3
XL 54,384
v_add_f32_dpp v14, v14, v14 quad_perm:[2,3,0,1] row_mask:0xf bank_mask:0xf
AW 44,48
AW 45,50
AW 46,248
M 8,74,2
WL 4
LO 124,18,124
LU 18,124
AW 47,250
M 8,75,3
RA 220,1,8192
HS 24,18
LK 48,252,43,0
CF 40,40,41
LK 248,254,43,2
RA 252,7,2816
CF 42,42,43
GS 41
CF 44,44,45
CF 46,46,47
M 0,4,4
LL 16,80
LL 106,81
v_add_f32_dpp v14, v14, v14 row_half_mirror row_mask:0xf bank_mask:0xf
M 0,5,5
LA 16
v_cvt_f64_f32 v[14:15], v14
AW 48,40
AW 49,42
M 1,16,4
LC_ 106
s_mov_b64 exec, s[54:55]
SP
s_mov_b64 exec, -1
AW 50,44
AW 51,46
M 1,17,5
RA 224,1,9216
LK 40,240,44,0
CF 48,48,49
LK 44,242,44,2
CF 50,50,51
GS 42
CF 248,248,249
CF 250,250,251
M 4,64,4
WL 5
LO 125,19,125
LU 19,125
M 4,65,5
ds_read_b64 v[14:15], v5 offset:2048
HS 24,19
AW 52,48
M 8,76,4
WL 5
LL 17,82
LL 107,83
AW 53,50
AW 54,248
AW 55,250
M 8,77,5
LA 17
RA 228,1,10240
LK 48,244,45,0
CF 40,40,41
LK 248,246,45,2
CF 42,42,43
v_subrev_u32 v5, s39, v5
GS 43
CF 44,44,45
CF 46,46,47
M 0,6,0
LC_ 107
XL 55,448
M 0,7,1
HX 24
AW 56,40
M 1,18,0
WL 5
LO 16,16,106
LU 106,16
AW 57,42
AW 58,44
M 1,19,1
RA 232,1,11264
HZ 27,106
AW 59,46
LK 40,248,46,0
CF 48,48,49
LK 44,250,46,2
CF 50,50,51
GS 44
CF 248,248,249
CF 250,250,251
M 4,66,0
LL 18,84
LL 110,85
M 4,67,1
LA 18
PK 106,16,104,33
M 8,78,0
WL 3
LC_ 110
PK 104,16,104,32
W1 24,0
W1 26,2048
AW 60,48
AW 61,50
AW 62,248
M 8,79,1
RV 252,22,5392
v_add_f32 v14, v14, v15
AW 63,250
LK 48,252,47,0
CF 40,40,41
LK 248,254,47,2
CF 42,42,43
v_add_f32_dpp v14, v14, v14 quad_perm:[1,0,3,2] row_mask:0xf bank_mask:0xf
GS 45
CF 44,44,45
CF 46,46,47
M 0,8,2
WL 6
LO 17,17,107
LU 107,17
M 0,9,3
RA 236,1,12288
HS 27,107
M 1,20,2
LL 19,86
LL 111,87
AW 64,40
AW 65,42
M 1,21,3
LA 19
PK 107,17,105,33
v_add_f32_dpp v14, v14, v14 quad_perm:[2,3,0,1] row_mask:0xf bank_mask:0xf
AW 66,44
AW 67,46
RV 36,8,6912
M 4,68,2
LC_ 111
PK 105,17,105,32
XL 56,512
M 4,69,3
s_add_u32 s42, s94, s60
RA 216,1,13312
v_add_f32_dpp v14, v14, v14 row_half_mirror row_mask:0xf bank_mask:0xf
GS 46
s_addc_u32 s43, s95, 0
CF 48,48,49
CF 50,50,51
CF 248,248,249
CF 250,250,251
M 8,80,2
WL 4
LO 18,18,110
LU 110,18
AW 68,48
AW 69,50
AW 70,248
M 8,81,3
RA 220,1,14336
HS 27,110
v_cvt_f64_f32 v[14:15], v14
AW 71,250
s_nop 1
GS 47
s_mov_b64 exec, s[54:55]
SP
s_mov_b64 exec, -1
s_nop 4
v_cndmask_b32 v52, v52, v53, s[78:79]
M 0,10,4
LL 16,96
LL 122,97
v_cndmask_b32 v54, v54, v55, s[78:79]
M 0,11,5
LA 16
PK 110,18,108,33
v_cndmask_b32 v52, v52, v54, s[80:81]
M 1,22,4
LC_ 122
PK 108,18,108,32
v_mul_f32 v52, 0x3b2aaaab, v52
M 1,23,5
WL 5
RA 224,1,15360
PO 110
v_med3_f32 v53, v52, v39, v37
v_cmp_eq_f32 vcc, v52, v53
M 4,70,4
WL 5
LO 19,19,111
LU 111,19
SU 104
XL 57,576
C 52,52,52
M 4,71,5
HS 27,111
v_lshlrev_b32 v52, 16, v52
M 8,82,4
WL 3
LL 17,98
LL 123,99
v_add_f32 v52, v36, v52
M 8,83,5
LA 17
PK 111,19,109,33
C 52,52,52
v_lshlrev_b32 v52, 16, v52
Z 6,0,0
LC_ 123
PK 109,19,109,32
v_sqrt_f32 v52, v52
M 6,1,1
RA 228,1,16384
PO 111
C 52,52,52
Z 7,12,0
WL 4
LO 16,16,122
LU 122,16
v_lshlrev_b32 v52, 16, v52
M 7,13,1
RA 232,1,17408
HZ 25,122
v_rcp_f32 v52, v52
s_nop 0
v_mul_f32 v52, v38, v52
Z 10,60,0
LL 18,64
LL 126,65
XL 58,640
v_cndmask_b32 v52, 0, v52, vcc
M 10,61,1
s_add_u32 s42, s94, s61
LA 18
PK 122,16,120,33
HX 27
CF 52,52,52
Z 11,72,0
WL 2
LC_ 126
PK 120,16,120,32
ds_bpermute_b32 v13, v28, v52
M 11,73,1
s_addc_u32 s43, s95, 0
RA 236,1,18432
AR 248,24
M 6,2,2
LO 17,17,123
LU 123,17
M 6,3,3
SU 108
HS 25,123
M 7,14,2
s_mul_i32 s53, s50, 25600
LL 19,66
LL 127,67
s_add_u32 s40, %[wlo], s53
s_addc_u32 s41, %[whi], 0
s_mov_b32 m0, s38
M 7,15,3
LA 19
PK 123,17,121,33
M 10,62,2
LC_ 127
PK 121,17,121,32
M 10,63,3
RA 216,1,19456
XL 59,704
DW 0
M 11,74,2
WL 3
LO 18,18,126
LU 126,18
M 11,75,3
s_add_u32 s37, s37, 32768
RA 220,1,20480
HS 25,126
M 6,4,4
s_cmp_eq_u32 s37, s75
LL 16,56
LL 104,57
s_cselect_b32 s37, s74, s37
AR 249,25
M 6,5,5
LA 16
PK 126,18,124,33
DW 1024
AR 250,26
M 7,16,4
s_add_u32 s42, s44, s64
LC_ 104
PK 124,18,124,32
s_addc_u32 s43, s45, 0
AR 251,27
AR 52,28
M 7,17,5
RA 224,1,21504
PO 126
AR 53,29
M 10,64,4
WL 5
LO 19,19,127
LU 127,19
ST 120
AR 54,30
AR 55,31
M 10,65,5
DW 2048
HS 25,127
XS 248
M 11,76,4
WL 3
LL 17,58
LL 105,59
XS 249
XS 250
M 11,77,5
s_add_u32 s40, s40, 3072
LA 17
PK 127,19,125,33
XS 251
M 6,6,0
s_addc_u32 s41, s41, 0
LC_ 105
PK 125,19,125,32
s_add_u32 m0, s38, 3072
AW 24,248
AW 25,249
M 6,7,1
RA 228,1,22528
PO 127
AW 26,250
M 7,18,0
WL 4
LO 104,16,104
LU 16,104
AW 27,251
XS 52
M 7,19,1
RA 232,1,23552
HZ 26,16
XS 53
M 10,66,0
LL 18,60
LL 108,61
DW 0
XS 54
XS 55
M 10,67,1
s_add_u32 s42, s44, s65
LA 18
HX 25
AW 28,52
M 11,78,0
WL 2
LC_ 108
W1 25,1024
W1 27,3072
AW 29,53
AW 30,54
M 11,79,1
s_addc_u32 s43, s45, 0
RA 236,1,24576
v_add_u32 v4, s39, v4
AW 31,55
M 6,8,2
LO 105,17,105
LU 17,105
)"
R"(s_lshl_b32 s53, s52, 8
s_add_u32 s48, %[alo], s53
s_addc_u32 s49, %[ahi], 0
s_add_u32 s52, s52, %[d]
s_cmp_eq_u32 s52, %[wrap]
s_cselect_b32 s52, %[rst], s52
DW 1024
v_min_u32 v35, v35, v13
AR 248,32
M 6,9,3
ST 124
HS 26,17
AR 249,33
M 7,20,2
s_mul_i32 s53, s51, 49152
LL 19,62
LL 109,63
s_bitcmp1_b64 s[90:91], s51
s_cselect_b64 s[44:45], s[84:85], s[82:83]
s_add_u32 s44, s44, s53
s_addc_u32 s45, s45, 0
s_bitcmp1_b64 s[92:93], s51
s_cselect_b64 s[94:95], s[88:89], s[86:87]
s_add_u32 s94, s94, s53
s_addc_u32 s95, s95, 0
s_add_u32 s51, s51, %[d]
s_cmp_eq_u32 s51, %[wrap]
s_cselect_b32 s51, %[rst], s51
AR 250,34
AR 251,35
M 7,21,3
LA 19
DW 2048
XS 248
M 10,68,2
LC_ 109
DQ 3072
XS 249
XS 250
M 10,69,3
s_add_u32 s50, s50, %[d]
v_add_u32 v1, s37, v0
XS 251
M 11,80,2
WL 2
LO 108,18,108
LU 18,108
AW 32,248
AW 33,249
M 11,81,3
s_cmp_eq_u32 s50, %[wrap]
RA 216,1,0
HS 26,18
AW 34,250
M 6,10,4
s_cselect_b32 s50, %[rst], s50
LL 16,72
LL 120,73
AW 35,251
AR 52,36
M 6,11,5
s_add_u32 s38, s38, 32768
LA 16
v_and_b32 v22, 0xfffffcff, v1
AR 53,37
M 7,22,4
s_cmp_eq_u32 s38, s77
LC_ 120
RA 220,1,1024
AR 54,38
AR 55,39
M 7,23,5
s_cselect_b32 s38, s76, s38
RV 252,22,5120
XS 52
M 10,70,4
WL 5
LO 109,19,109
LU 19,109
XS 53
XS 54
M 10,71,5
s_barrier
RA 224,1,2048
HS 26,19
XS 55
M 11,82,4
WL 1
LL 17,74
LL 121,75
AW 36,52
AW 37,53
M 11,83,5
s_sub_u32 s39, 0, s39
LA 17
RA 228,1,3072
AW 38,54
Z 0,0,0
LC_ 121
HX 26
AW 39,55
M 0,1,1
AR 248,40
AR 249,41
Z 1,12,0
LO 120,16,120
LU 16,120
AR 250,42
M 1,13,1
RA 232,1,4096
HZ 24,16
AR 251,43
XS 248
Z 2,24,0
LL 18,88
LL 124,89
XS 249
M 2,25,1
LA 18
XS 250
XS 251
Z 3,36,0
LC_ 124
AW 40,248
M 3,37,1
AW 41,249
AW 42,250
Z 4,60,0
WL 3
LO 121,17,121
LU 17,121
AW 43,251
M 4,61,1
RA 236,1,6144
HS 24,17
AR 52,44
AR 53,45
Z 5,72,0
LL 19,90
LL 125,91
AR 54,46
M 5,73,1
LA 19
AR 55,47
XS 52
M 0,2,2
LC_ 125
XS 53
M 0,3,3
XS 54
XS 55
M 1,14,2
WL 3
LO 124,18,124
LU 18,124
AW 44,52
M 1,15,3
RA 216,1,7168
HS 24,18
AW 45,53
AW 46,54
M 2,26,2
LL 16,80
LL 106,81
AW 47,55
M 2,27,3
LA 16
AR 248,48
AR 249,49
M 3,38,2
LC_ 106
AR 250,50
M 3,39,3
WV 32
BW 48
AR 251,51
XS 248
M 4,62,2
WL 4
LO 125,19,125
LU 19,125
XS 249
M 4,63,3
RA 220,1,8192
HS 24,19
XS 250
XS 251
M 5,74,2
LL 17,82
LL 107,83
AW 48,248
M 5,75,3
s_mov_b32 m0, %[pm]
LA 17
DF
AW 49,249
AW 50,250
M 0,4,4
LC_ 107
RV 40,8,6144
AW 51,251
M 0,5,5
RV 44,8,6208
RV 48,8,6272
AR 52,52
AR 53,53
M 1,16,4
WL 7
LO 16,16,106
LU 106,16
AR 54,54
M 1,17,5
RA 224,1,9216
HZ 27,106
AR 55,55
XS 52
M 2,28,4
LL 18,84
LL 110,85
XS 53
M 2,29,5
LA 18
PK 106,16,104,33
XS 54
XS 55
M 3,40,4
LC_ 110
PK 104,16,104,32
AW 52,52
M 3,41,5
BR 48
HX 24
AW 53,53
AW 54,54
M 4,64,4
WL 7
LO 17,17,107
LU 107,17
AW 55,55
M 4,65,5
RA 228,1,10240
HS 27,107
AR 248,56
M 5,76,4
LL 19,86
LL 111,87
AR 249,57
AR 250,58
M 5,77,5
s_add_u32 s42, s94, s60
LA 19
PK 107,17,105,33
AR 251,59
M 0,6,0
s_addc_u32 s43, s95, 0
LC_ 111
PK 105,17,105,32
XS 248
XS 249
M 0,7,1
W1 24,0
XS 250
M 1,18,0
WL 5
LO 18,18,110
LU 110,18
W1 26,2048
XS 251
AW 56,248
M 1,19,1
RA 232,1,11264
HS 27,110
AW 57,249
M 2,30,0
LL 16,96
LL 122,97
AW 58,250
AW 59,251
M 2,31,1
LA 16
PK 110,18,108,33
AR 52,60
M 3,42,0
LC_ 122
PK 108,18,108,32
AR 53,61
AR 54,62
M 3,43,1
RV 252,22,5392
PO 110
AR 55,63
M 4,66,0
WL 6
LO 19,19,111
LU 111,19
XS 52
XS 53
M 4,67,1
RA 236,1,12288
HS 27,111
XS 54
M 5,78,0
LL 17,98
LL 123,99
XS 55
AW 60,52
M 5,79,1
LA 17
PK 111,19,109,33
AW 61,53
M 0,8,2
LC_ 123
PK 109,19,109,32
AW 62,54
AW 63,55
M 0,9,3
SU 104
PO 111
AR 248,64
M 1,20,2
WL 4
LO 16,16,122
LU 122,16
AR 249,65
AR 250,66
M 1,21,3
s_add_u32 s42, s94, s61
RA 216,1,13312
HZ 25,122
AR 251,67
M 2,32,2
s_addc_u32 s43, s95, 0
LL 18,100
LL 126,101
XS 248
XS 249
M 2,33,3
LA 18
PK 122,16,120,33
XS 250
M 3,44,2
LC_ 126
PK 120,16,120,32
XS 251
AW 64,248
M 3,45,3
WV 30
SU 108
HX 27
AW 65,249
BW 49
M 4,68,2
WL 4
LO 17,17,123
LU 123,17
AW 66,250
AW 67,251
M 4,69,3
RA 220,1,14336
HS 25,123
BR 49
AR 52,68
LZ 40,48
WV 27
BW 50
M 5,80,2
LL 19,102
LL 127,103
AR 53,69
AR 54,70
M 5,81,3
s_add_u32 s42, s44, s64
LA 19
PK 123,17,121,33
AR 55,71
M 0,10,4
s_addc_u32 s43, s45, 0
LC_ 127
PK 121,17,121,32
XS 52
XS 53
M 0,11,5
ST 120
RV 40,8,6336
XS 54
M 1,22,4
WL 7
LO 18,18,126
LU 126,18
XS 55
BR 50
AW 68,52
M 1,23,5
WL 5
RA 224,1,15360
HS 25,126
AW 69,53
LS 44,49
WV 25
BW 51
)"
R"(M 2,34,4
LL 16,56
LL 104,57
AW 70,54
AW 71,55
M 2,35,5
LA 16
PK 126,18,124,33
M 3,46,4
LC_ 104
PK 124,18,124,32
RV 44,8,6400
M 3,47,5
ds_read_b64 v[14:15], v5 offset:0
PO 126
BR 51
M 4,70,4
WL 9
LO 19,19,127
LU 127,19
M 4,71,5
WL 7
RA 228,1,16384
HS 25,127
LS 48,50
WV 24
BW 52
M 5,82,4
LL 17,58
LL 105,59
M 5,83,5
LA 17
PK 127,19,125,33
RV 48,8,6464
Z 6,0,0
s_add_u32 s42, s44, s65
LC_ 105
PK 125,19,125,32
M 6,1,1
s_addc_u32 s43, s45, 0
BR 52
PO 127
Z 7,12,0
WL 9
LO 104,16,104
LU 16,104
M 7,13,1
WL 6
RA 232,1,17408
HZ 26,16
LS 40,51
WV 23
BW 53
Z 8,24,0
WV 22
LL 18,60
LL 108,61
M 8,25,1
WL 2
LA 18
ST 124
RV 40,8,6528
Z 9,36,0
s_mul_i32 s53, s50, 25600
LC_ 108
BR 53
M 9,37,1
s_add_u32 s40, %[wlo], s53
v_add_f32 v14, v14, v15
HX 25
LS 44,52
s_addc_u32 s41, %[whi], 0
BW 54
Z 10,60,0
LO 105,17,105
LU 17,105
M 10,61,1
s_mov_b32 m0, s38
RA 236,1,18432
HS 26,17
Z 11,72,0
LL 19,62
LL 109,63
RV 44,8,6592
M 11,73,1
LA 19
W1 25,1024
BR 54
M 6,2,2
LC_ 109
W1 27,3072
M 6,3,3
WL 8
DW 0
v_add_f32_dpp v14, v14, v14 quad_perm:[1,0,3,2] row_mask:0xf bank_mask:0xf
LS 48,53
WV 22
BW 55
M 7,14,2
LO 108,18,108
LU 18,108
M 7,15,3
s_add_u32 s37, s37, 32768
RA 216,1,19456
HS 26,18
M 8,26,2
s_cmp_eq_u32 s37, s75
LL 16,64
LL 112,65
RV 48,8,6656
M 8,27,3
s_cselect_b32 s37, s74, s37
LA 16
DW 1024
BR 55
M 9,38,2
LC_ 112
v_add_f32_dpp v14, v14, v14 quad_perm:[2,3,0,1] row_mask:0xf bank_mask:0xf
M 9,39,3
WL 8
v_add_f32_dpp v14, v14, v14 row_half_mirror row_mask:0xf bank_mask:0xf
v_add_u32 v4, s39, v4
LS 40,54
WV 22
BW 56
M 10,62,2
WL 8
LO 109,19,109
LU 19,109
M 10,63,3
RA 220,1,20480
HS 26,19
RV 40,8,6720
M 11,74,2
LL 17,66
LL 113,67
BR 56
M 11,75,3
LA 17
DW 2048
M 6,4,4
WL 6
LC_ 113
v_cvt_f64_f32 v[14:15], v14
M 6,5,5
s_mov_b64 exec, s[54:55]
SP
s_mov_b64 exec, -1
LS 44,55
WV 20
BW 57
M 7,16,4
WL 3
LO 112,16,112
LU 16,112
RV 44,8,6784
M 7,17,5
s_mul_i32 s53, s51, 49152
RA 224,1,21504
HZ 29,16
M 8,28,4
s_bitcmp1_b64 s[90:91], s51
LL 18,68
LL 116,69
BR 57
M 8,29,5
s_cselect_b64 s[44:45], s[84:85], s[82:83]
LA 18
ds_read_b64 v[14:15], v5 offset:2048
LS 48,56
s_add_u32 s44, s44, s53
BW 58
M 9,40,4
s_addc_u32 s45, s45, 0
LC_ 116
RV 48,8,6848
M 9,41,5
s_bitcmp1_b64 s[92:93], s51
RA 240,7,0
BR 58
M 10,64,4
WL 11
LO 113,17,113
LU 17,113
M 10,65,5
s_cselect_b64 s[94:95], s[88:89], s[86:87]
RA 228,1,22528
HS 29,17
M 11,76,4
WL 8
LL 19,70
LL 117,71
M 11,77,5
s_add_u32 s94, s94, s53
LA 19
v_subrev_u32 v5, s39, v5
M 6,6,0
s_addc_u32 s95, s95, 0
LC_ 117
M 6,7,1
s_add_u32 s51, s51, %[d]
LS 40,57
s_cmp_eq_u32 s51, %[wrap]
WV 18
BW 59
M 7,18,0
WL 4
LO 116,18,116
LU 18,116
RA 244,7,256
M 7,19,1
s_cselect_b32 s51, %[rst], s51
RA 232,1,23552
HS 29,18
M 8,30,0
s_mul_i32 s53, %[d], 16384
LL 16,72
LL 120,73
BR 59
M 8,31,1
s_add_u32 s62, s53, %[sb1]
LA 16
v_add_f32 v14, v14, v15
s_add_u32 s63, s62, 512
LS 44,58
s_add_u32 s40, s40, 3072
RA 248,7,512
v_add_f32_dpp v14, v14, v14 quad_perm:[1,0,3,2] row_mask:0xf bank_mask:0xf
M 9,42,0
s_addc_u32 s41, s41, 0
LC_ 120
v_add_f32_dpp v14, v14, v14 quad_perm:[2,3,0,1] row_mask:0xf bank_mask:0xf
s_add_u32 m0, s38, 3072
M 9,43,1
DW 0
v_add_f32_dpp v14, v14, v14 row_half_mirror row_mask:0xf bank_mask:0xf
M 10,66,0
WL 7
LO 117,19,117
LU 19,117
M 10,67,1
WL 3
RA 236,1,24576
HS 29,19
LS 48,59
RA 252,7,768
v_cvt_f64_f32 v[14:15], v14
M 11,78,0
LL 17,74
LL 121,75
s_nop 2
v_mul_f32 v36, 0xbb2aaaab, v36
M 11,79,1
LA 17
DW 1024
v_mul_f32 v37, 0xbb2aaaab, v37
v_mul_f32 v38, 0xbb2aaaab, v38
M 6,8,2
LC_ 121
s_mov_b64 exec, s[54:55]
SP
s_mov_b64 exec, -1
v_mul_f32 v39, 0xbb2aaaab, v39
M 6,9,3
DW 2048
v_add_u32 v1, s37, v0
LK 40,240,48,0
LK 44,242,48,2
s_lshl_b32 s53, s52, 8
RA 240,7,1024
v_pk_add_f16 v26, v26, v29
LK 48,244,49,0
CF 40,40,41
LK 248,246,49,2
s_add_u32 s48, %[alo], s53
RA 244,7,1280
CF 42,42,43
M 7,20,2
WL 6
LO 120,16,120
LU 16,120
M 7,21,3
s_addc_u32 s49, %[ahi], 0
RA 216,1,0
HZ 24,16
CF 44,44,45
M 8,32,2
s_add_u32 s52, s52, %[d]
LL 18,76
LL 124,77
CF 46,46,47
AW 72,40
M 8,33,3
s_cmp_eq_u32 s52, %[wrap]
LA 18
v_and_b32 v22, 0xfffffcff, v1
AW 73,42
AW 74,44
M 9,44,2
s_cselect_b32 s52, %[rst], s52
LC_ 124
DQ 3072
AW 75,46
M 9,45,3
HX 26
LK 40,248,50,0
CF 48,48,49
LK 44,250,50,2
RA 248,7,1536
CF 50,50,51
GZ 48
CF 248,248,249
CF 250,250,251
M 10,68,2
WL 6
LO 121,17,121
LU 17,121
M 10,69,3
s_add_u32 s50, s50, %[d]
RA 220,1,1024
HS 24,17
AW 76,48
M 11,80,2
LL 19,78
LL 125,79
AW 77,50
AW 78,248
AW 79,250
M 11,81,3
s_cmp_eq_u32 s50, %[wrap]
LA 19
RV 252,22,5120
LK 48,252,51,0
CF 40,40,41
LK 248,254,51,2
s_cselect_b32 s50, %[rst], s50
RA 252,7,1792
CF 42,42,43
GS 49
CF 44,44,45
CF 46,46,47
M 6,10,4
LC_ 125
AW 80,40
M 6,11,5
AW 81,42
AW 82,44
M 7,22,4
WL 6
LO 124,18,124
LU 18,124
M 7,23,5
s_add_u32 s38, s38, 32768
RA 224,1,2048
HS 24,18
AW 83,46
LK 40,240,52,0
CF 48,48,49
LK 44,242,52,2
s_cmp_eq_u32 s38, s77
RA 240,7,2048
CF 50,50,51
GS 50
s_cselect_b32 s38, s76, s38
CF 248,248,249
CF 250,250,251
M 8,34,4
s_barrier
LL 16,80
LL 106,81
M 8,35,5
s_sub_u32 s39, 0, s39
LA 16
AW 84,48
M 9,46,4
LC_ 106
AW 85,50
M 9,47,5
AW 86,248
AW 87,250
LK 48,244,53,0
CF 40,40,41
LK 248,246,53,2
RA 244,7,2304
CF 42,42,43
GS 51
CF 44,44,45
CF 46,46,47
M 10,70,4
WL 5
LO 125,19,125
LU 19,125
M 10,71,5
RA 228,1,3072
HS 24,19
M 11,82,4
LL 17,82
LL 107,83
M 11,83,5
LA 17
)"
R"(HX 24
Z 0,0,0
LC_ 107
W1 24,0
AW 88,40
M 0,1,1
W1 26,2048
AW 89,42
AW 90,44
AW 91,46
LK 40,248,54,0
CF 48,48,49
LK 44,250,54,2
RA 248,7,2560
CF 50,50,51
GS 52
CF 248,248,249
CF 250,250,251
Z 1,12,0
WL 7
LO 16,16,106
LU 106,16
M 1,13,1
RA 232,1,4096
HZ 27,106
Z 2,24,0
LL 18,84
LL 110,85
M 2,25,1
LA 18
PK 106,16,104,33
AW 92,48
Z 3,36,0
LC_ 110
PK 104,16,104,32
AW 93,50
AW 94,248
AW 95,250
M 3,37,1
LK 48,252,55,0
CF 40,40,41
LK 248,254,55,2
RA 252,7,2816
CF 42,42,43
GS 53
CF 44,44,45
CF 46,46,47
Z 4,60,0
WL 7
LO 17,17,107
LU 107,17
M 4,61,1
RA 236,1,6144
HS 27,107
Z 5,72,0
LL 19,86
LL 111,87
AW 96,40
AW 97,42
M 5,73,1
s_add_u32 s42, s94, s60
LA 19
PK 107,17,105,33
AW 98,44
AW 99,46
LK 40,240,56,0
CF 48,48,49
LK 44,242,56,2
s_addc_u32 s43, s95, 0
CF 50,50,51
GS 54
CF 248,248,249
CF 250,250,251
M 0,2,2
LC_ 111
PK 105,17,105,32
M 0,3,3
SU 104
AW 100,48
M 1,14,2
WL 4
LO 18,18,110
LU 110,18
M 1,15,3
RA 216,1,7168
HS 27,110
AW 101,50
AW 102,248
M 2,26,2
LL 16,88
LL 114,89
AW 103,250
M 2,27,3
LA 16
PK 110,18,108,33
LK 48,244,57,0
CF 40,40,41
LK 248,246,57,2
CF 42,42,43
GS 55
CF 44,44,45
CF 46,46,47
M 3,38,2
LC_ 114
PK 108,18,108,32
M 3,39,3
PO 110
AW 104,40
M 4,62,2
WL 3
LO 19,19,111
LU 111,19
AW 105,42
AW 106,44
AW 107,46
M 4,63,3
RA 220,1,8192
HS 27,111
LK 40,248,58,0
CF 48,48,49
LK 44,250,58,2
CF 50,50,51
GS 56
CF 248,248,249
CF 250,250,251
M 5,74,2
LL 17,90
LL 115,91
M 5,75,3
s_mov_b32 m0, %[pm]
LA 17
PK 111,19,109,33
M 0,4,4
LC_ 115
PK 109,19,109,32
AW 108,48
AW 109,50
M 0,5,5
DF
PO 111
AW 110,248
AW 111,250
LK 48,252,59,0
CF 40,40,41
LK 248,254,59,2
s_add_u32 s42, s94, s61
CF 42,42,43
GS 57
s_addc_u32 s43, s95, 0
CF 44,44,45
CF 46,46,47
M 1,16,4
WL 3
LO 16,16,114
LU 114,16
M 1,17,5
RA 224,1,9216
HZ 29,114
M 2,28,4
LL 18,92
LL 118,93
AW 112,40
M 2,29,5
LA 18
PK 114,16,112,33
AW 113,42
AW 114,44
M 3,40,4
LC_ 118
PK 112,16,112,32
AW 115,46
RV 36,8,6912
M 3,41,5
SU 108
GS 58
CF 48,48,49
CF 50,50,51
M 4,64,4
WL 4
LO 17,17,115
LU 115,17
CF 248,248,249
CF 250,250,251
AW 116,48
M 4,65,5
RA 228,1,10240
HS 29,115
AW 117,50
AW 118,248
M 5,76,4
LL 19,94
LL 119,95
AW 119,250
M 5,77,5
s_add_u32 s42, s94, s62
LA 19
PK 115,17,113,33
GS 59
s_addc_u32 s43, s95, 0
s_nop 6
v_cndmask_b32 v52, v52, v53, s[78:79]
M 0,6,0
LC_ 119
PK 113,17,113,32
v_cndmask_b32 v54, v54, v55, s[78:79]
M 0,7,1
SU 112
v_cndmask_b32 v52, v52, v54, s[80:81]
M 1,18,0
WL 4
LO 18,18,118
LU 118,18
v_mul_f32 v52, 0x3b2aaaab, v52
M 1,19,1
WL 3
RA 232,1,11264
HS 29,118
v_med3_f32 v53, v52, v39, v37
M 2,30,0
LL 16,96
LL 122,97
v_cmp_eq_f32 vcc, v52, v53
M 2,31,1
LA 16
PK 118,18,116,33
C 52,52,52
M 3,42,0
LC_ 122
PK 116,18,116,32
v_lshlrev_b32 v52, 16, v52
M 3,43,1
RV 252,22,5392
PO 118
v_add_f32 v52, v36, v52
M 4,66,0
WL 4
LO 19,19,119
LU 119,19
C 52,52,52
M 4,67,1
RA 236,1,12288
HS 29,119
v_lshlrev_b32 v52, 16, v52
M 5,78,0
LL 17,98
LL 123,99
v_sqrt_f32 v52, v52
M 5,79,1
LA 17
PK 119,19,117,33
C 52,52,52
M 0,8,2
s_add_u32 s42, s94, s63
LC_ 123
PK 117,19,117,32
v_lshlrev_b32 v52, 16, v52
M 0,9,3
s_addc_u32 s43, s95, 0
PO 119
v_pk_add_f16 v27, v27, v29
v_rcp_f32 v52, v52
M 1,20,2
WL 4
LO 16,16,122
LU 122,16
v_mul_f32 v52, v38, v52
M 1,21,3
RA 216,1,13312
HZ 25,122
v_cndmask_b32 v52, 0, v52, vcc
M 2,32,2
LL 18,100
LL 126,101
CF 52,52,52
M 2,33,3
LA 18
PK 122,16,120,33
ds_bpermute_b32 v13, v28, v52
M 3,44,2
LC_ 126
PK 120,16,120,32
M 3,45,3
SU 116
HX 27
M 4,68,2
WL 4
LO 17,17,123
LU 123,17
M 4,69,3
RA 220,1,14336
HS 25,123
M 5,80,2
LL 19,102
LL 127,103
M 5,81,3
s_add_u32 s42, s44, s64
LA 19
PK 123,17,121,33
M 0,10,4
s_addc_u32 s43, s45, 0
LC_ 127
PK 121,17,121,32
M 0,11,5
ST 120
AR 248,72
M 1,22,4
WL 3
LO 18,18,126
LU 126,18
M 1,23,5
RA 224,1,15360
HS 25,126
M 2,34,4
LL 16,56
LL 104,57
AR 249,73
M 2,35,5
LA 16
PK 126,18,124,33
AR 250,74
M 3,46,4
LC_ 104
PK 124,18,124,32
AR 251,75
M 3,47,5
ds_read_b64 v[14:15], v5 offset:0
PO 126
AR 52,76
M 4,70,4
WL 4
LO 19,19,127
LU 127,19
AR 53,77
M 4,71,5
RA 228,1,16384
HS 25,127
AR 54,78
M 5,82,4
LL 17,58
LL 105,59
AR 55,79
M 5,83,5
LA 17
PK 127,19,125,33
XS 248
Z 6,0,0
s_add_u32 s42, s44, s65
LC_ 105
PK 125,19,125,32
XS 249
M 6,1,1
s_addc_u32 s43, s45, 0
PO 127
HX 25
XS 250
Z 7,12,0
WL 4
LO 104,16,104
LU 16,104
XS 251
AW 72,248
M 7,13,1
RA 232,1,17408
HZ 26,16
AW 73,249
Z 8,24,0
LL 18,60
LL 108,61
AW 74,250
M 8,25,1
LA 18
W1 25,1024
AW 75,251
Z 9,36,0
LC_ 108
W1 27,3072
XS 52
M 9,37,1
WL 8
ST 124
v_add_f32 v14, v14, v15
XS 53
Z 10,60,0
WL 5
LO 105,17,105
LU 17,105
XS 54
M 10,61,1
s_mul_i32 s53, s50, 25600
RA 236,1,18432
HS 26,17
XS 55
Z 11,72,0
LL 19,62
LL 109,63
AW 76,52
M 11,73,1
s_add_u32 s40, %[wlo], s53
LA 19
v_add_f32_dpp v14, v14, v14 quad_perm:[1,0,3,2] row_mask:0xf bank_mask:0xf
)"
R"(AW 77,53
M 6,2,2
s_addc_u32 s41, %[whi], 0
LC_ 109
v_add_f32_dpp v14, v14, v14 quad_perm:[2,3,0,1] row_mask:0xf bank_mask:0xf
AW 78,54
AW 79,55
M 6,3,3
s_mov_b32 m0, s38
s_nop 0
DW 0
v_add_f32_dpp v14, v14, v14 row_half_mirror row_mask:0xf bank_mask:0xf
v_min_u32 v35, v35, v13
M 7,14,2
WL 4
LO 108,18,108
LU 18,108
AR 248,80
M 7,15,3
s_add_u32 s37, s37, 32768
RA 216,1,19456
HS 26,18
AR 249,81
M 8,26,2
s_cmp_eq_u32 s37, s75
LL 16,64
LL 112,65
AR 250,82
M 8,27,3
s_cselect_b32 s37, s74, s37
LA 16
v_cvt_f64_f32 v[14:15], v14
AR 251,83
M 9,38,2
s_mul_i32 s53, %[d], 393216
LC_ 112
s_mov_b64 exec, s[54:55]
SP
s_mov_b64 exec, -1
XS 248
M 9,39,3
s_add_u32 s53, s53, s57
ds_read_b64 v[14:15], v5 offset:2048
v_add_u32 v4, s39, v4
XS 249
M 10,62,2
WL 4
LO 109,19,109
LU 19,109
XS 250
M 10,63,3
s_add_u32 s46, %[xlo], s53
RA 220,1,20480
HS 26,19
XS 251
M 11,74,2
LL 17,66
LL 113,67
AW 80,248
AW 81,249
M 11,75,3
s_addc_u32 s47, %[xhi], 0
LA 17
v_subrev_u32 v5, s39, v5
AW 82,250
M 6,4,4
s_bitcmp1_b64 s[92:93], 48
LC_ 113
AW 83,251
M 6,5,5
s_cselect_b32 s58, 16, 0
AR 52,84
AR 53,85
M 7,16,4
WL 4
LO 112,16,112
LU 16,112
M 7,17,5
s_lshr_b32 s58, 0xda43cb52, s58
RA 224,1,21504
HZ 29,16
AR 54,86
M 8,28,4
LL 18,68
LL 116,69
AR 55,87
M 8,29,5
WL 4
LA 18
v_add_f32 v14, v14, v15
XS 52
M 9,40,4
LC_ 116
v_add_f32_dpp v14, v14, v14 quad_perm:[1,0,3,2] row_mask:0xf bank_mask:0xf
v_lshrrev_b32 v3, 6, v0
v_and_b32 v3, 12, v3
v_bfe_u32 v3, s58, v3, 4
v_mad_u32_u24 v3, v3, s97, v21
XS 53
M 9,41,5
DW 1024
v_add_f32_dpp v14, v14, v14 quad_perm:[2,3,0,1] row_mask:0xf bank_mask:0xf
XS 54
M 10,64,4
WL 3
LO 113,17,113
LU 17,113
XS 55
AW 84,52
M 10,65,5
RA 228,1,22528
HS 29,17
AW 85,53
M 11,76,4
LL 19,70
LL 117,71
AW 86,54
M 11,77,5
LA 19
XL 60,0
AW 87,55
M 6,6,0
LC_ 117
DW 2048
AR 248,88
M 6,7,1
XL 61,64
v_add_f32_dpp v14, v14, v14 row_half_mirror row_mask:0xf bank_mask:0xf
AR 249,89
M 7,18,0
WL 3
LO 116,18,116
LU 18,116
AR 250,90
M 7,19,1
s_mul_i32 s53, s51, 49152
RA 232,1,23552
HS 29,18
AR 251,91
M 8,30,0
s_bitcmp1_b64 s[90:91], s51
LL 16,72
LL 120,73
XS 248
M 8,31,1
s_cselect_b64 s[44:45], s[84:85], s[82:83]
LA 16
v_cvt_f64_f32 v[14:15], v14
s_add_u32 s44, s44, s53
s_addc_u32 s45, s45, 0
s_bitcmp1_b64 s[92:93], s51
s_cselect_b64 s[94:95], s[88:89], s[86:87]
s_add_u32 s94, s94, s53
s_addc_u32 s95, s95, 0
s_add_u32 s51, s51, %[d]
s_cmp_eq_u32 s51, %[wrap]
s_cselect_b32 s51, %[rst], s51
XS 249
M 9,42,0
s_add_u32 s40, s40, 3072
LC_ 120
s_mov_b64 exec, s[54:55]
SP
s_mov_b64 exec, -1
s_addc_u32 s41, s41, 0
s_add_u32 m0, s38, 3072
XS 250
XS 251
M 9,43,1
XL 62,128
AW 88,248
M 10,66,0
WL 3
LO 117,19,117
LU 19,117
AW 89,249
M 10,67,1
RA 236,1,24576
HS 29,19
AW 90,250
M 11,78,0
LL 17,74
LL 121,75
AW 91,251
M 11,79,1
LA 17
DW 0
AR 52,92
M 6,8,2
LC_ 121
XL 63,192
AR 53,93
M 6,9,3
DW 1024
v_add_u32 v1, s37, v0
AR 54,94
M 7,20,2
WL 3
LO 120,16,120
LU 16,120
AR 55,95
M 7,21,3
RA 216,1,0
HZ 24,16
XS 52
M 8,32,2
LL 18,76
LL 124,77
XS 53
XS 54
M 8,33,3
LA 18
XL 64,256
XS 55
M 9,44,2
LC_ 124
DW 2048
AW 92,52
M 9,45,3
XL 65,320
v_and_b32 v22, 0xfffffcff, v1
AW 93,53
M 10,68,2
WL 3
LO 121,17,121
LU 17,121
AW 94,54
M 10,69,3
s_lshl_b32 s53, s52, 8
RA 220,1,1024
HS 24,17
AW 95,55
M 11,80,2
LL 19,78
LL 125,79
AR 248,96
M 11,81,3
s_add_u32 s48, %[alo], s53
LA 19
RV 252,22,5120
AR 249,97
M 6,10,4
s_addc_u32 s49, %[ahi], 0
LC_ 125
v_pk_add_f16 v26, v26, v29
AR 250,98
M 6,11,5
s_add_u32 s52, s52, %[d]
HX 26
AR 251,99
M 7,22,4
WL 4
LO 124,18,124
LU 18,124
XS 248
XS 249
M 7,23,5
s_cmp_eq_u32 s52, %[wrap]
RA 224,1,2048
HS 24,18
XS 250
M 8,34,4
s_cselect_b32 s52, %[rst], s52
LL 16,80
LL 106,81
XS 251
M 8,35,5
LA 16
DQ 3072
AW 96,248
M 9,46,4
LC_ 106
XL 66,384
AW 97,249
M 9,47,5
s_add_u32 s50, s50, %[d]
AW 98,250
AW 99,251
M 10,70,4
WL 3
LO 125,19,125
LU 19,125
M 10,71,5
RA 228,1,3072
HS 24,19
AR 52,100
M 11,82,4
LL 17,82
LL 107,83
AR 53,101
M 11,83,5
LA 17
HX 24
s_cmp_eq_u32 s50, %[wrap]
s_cselect_b32 s50, %[rst], s50
XL 67,448
AR 54,102
Z 0,0,0
WV 38
LC_ 107
XL 68,512
s_add_u32 s38, s38, 32768
s_cmp_eq_u32 s38, s77
s_cselect_b32 s38, s76, s38
s_barrier
s_sub_u32 s39, 0, s39
AR 55,103
XS 52
M 0,1,1
ds_read_b64 v[14:15], v5 offset:0
XL 69,576
XS 53
Z 1,12,0
WL 4
LO 16,16,106
LU 106,16
XS 54
M 1,13,1
RA 232,1,4096
HZ 27,106
XS 55
Z 2,24,0
LL 18,84
LL 110,85
AW 100,52
M 2,25,1
LA 18
PK 106,16,104,33
AW 101,53
Z 3,36,0
WL 5
LC_ 110
PK 104,16,104,32
XL 70,640
AW 102,54
M 3,37,1
RA 236,1,6144
W1 24,0
W1 26,2048
AW 103,55
M 0,2,2
WL 7
LO 17,17,107
LU 107,17
AR 248,104
M 0,3,3
XL 71,704
HS 27,107
AR 249,105
M 1,14,2
LL 19,86
LL 111,87
AR 250,106
AR 251,107
M 1,15,3
s_add_u32 s42, s94, s60
LA 19
PK 107,17,105,33
XS 248
M 2,26,2
s_addc_u32 s43, s95, 0
LC_ 111
PK 105,17,105,32
XS 249
M 2,27,3
WL 8
RA 216,1,7168
v_add_f32 v14, v14, v15
s_nop 1
v_add_f32_dpp v14, v14, v14 quad_perm:[1,0,3,2] row_mask:0xf bank_mask:0xf
XS 250
M 3,38,2
WL 5
LO 18,18,110
LU 110,18
v_add_f32_dpp v14, v14, v14 quad_perm:[2,3,0,1] row_mask:0xf bank_mask:0xf
XS 251
M 3,39,3
RA 220,1,8192
HS 27,110
AW 104,248
M 0,4,4
LL 16,88
LL 114,89
v_add_f32_dpp v14, v14, v14 row_half_mirror row_mask:0xf bank_mask:0xf
AW 105,249
M 0,5,5
LA 16
PK 110,18,108,33
AW 106,250
M 1,16,4
LC_ 114
PK 108,18,108,32
v_cvt_f64_f32 v[14:15], v14
AW 107,251
M 1,17,5
RA 224,1,9216
PO 110
AR 52,108
M 2,28,4
)"
R"(WL 5
LO 19,19,111
LU 111,19
s_mov_b64 exec, s[54:55]
SP
s_mov_b64 exec, -1
SU 104
AR 53,109
AR 54,110
M 2,29,5
ds_read_b64 v[14:15], v5 offset:2048
HS 27,111
AR 55,111
M 3,40,4
WL 4
LL 17,90
LL 115,91
XS 52
M 3,41,5
WV 19
LA 17
PK 111,19,109,33
XS 53
M 0,6,0
s_add_u32 s42, s94, s61
LC_ 115
PK 109,19,109,32
XS 54
M 0,7,1
s_addc_u32 s43, s95, 0
RA 228,1,10240
PO 111
XS 55
BW 60
M 1,18,0
WL 6
LO 16,16,114
LU 114,16
AW 108,52
M 1,19,1
RA 232,1,11264
HZ 29,114
RV 40,8,6144
AW 109,53
M 2,30,0
LL 18,92
LL 118,93
RV 44,8,6208
AW 110,54
M 2,31,1
LA 18
PK 114,16,112,33
v_subrev_u32 v5, s39, v5
AW 111,55
RV 48,8,6272
M 3,42,0
WL 6
LC_ 118
PK 112,16,112,32
AR 248,112
BR 60
AR 249,113
M 3,43,1
WV 17
RV 252,22,5392
v_add_f32 v14, v14, v15
BW 61
AR 250,114
M 0,8,2
LO 17,17,115
LU 115,17
BR 61
AR 251,115
M 0,9,3
WL 3
RA 236,1,12288
HS 29,115
v_add_f32_dpp v14, v14, v14 quad_perm:[1,0,3,2] row_mask:0xf bank_mask:0xf
SU 108
XS 248
LZ 40,60
WV 16
BW 62
M 1,20,2
LL 19,94
LL 119,95
XS 249
M 1,21,3
s_add_u32 s42, s94, s62
LA 19
PK 115,17,113,33
v_add_f32_dpp v14, v14, v14 quad_perm:[2,3,0,1] row_mask:0xf bank_mask:0xf
RV 40,8,6336
XS 250
M 2,32,2
s_addc_u32 s43, s95, 0
LC_ 119
PK 113,17,113,32
XS 251
BR 62
M 2,33,3
WL 6
RA 216,1,13312
v_add_f32_dpp v14, v14, v14 row_half_mirror row_mask:0xf bank_mask:0xf
AW 112,248
LS 44,61
WV 14
BW 63
M 3,44,2
WL 7
LO 18,18,118
LU 118,18
AW 113,249
M 3,45,3
RA 220,1,14336
HS 29,118
v_cvt_f64_f32 v[14:15], v14
AW 114,250
RV 44,8,6400
M 0,10,4
LL 16,96
LL 122,97
AW 115,251
BR 63
AR 52,116
M 0,11,5
WL 5
LA 16
PK 118,18,116,33
s_mov_b64 exec, s[54:55]
SP
s_mov_b64 exec, -1
AR 53,117
LS 48,62
WV 13
BW 64
M 1,22,4
LC_ 122
PK 116,18,116,32
AR 54,118
M 1,23,5
RA 224,1,15360
PO 118
AR 55,119
RV 48,8,6464
M 2,34,4
LO 19,19,119
LU 119,19
SU 112
XS 52
M 2,35,5
BR 64
HS 29,119
XS 53
M 3,46,4
WL 8
LL 17,98
LL 123,99
XS 54
M 3,47,5
WL 6
LA 17
PK 119,19,117,33
XS 55
LS 40,63
WV 12
BW 65
Z 6,0,0
s_add_u32 s42, s94, s63
LC_ 123
PK 117,19,117,32
RV 40,8,6528
AW 116,52
M 6,1,1
s_addc_u32 s43, s95, 0
RA 228,1,16384
PO 119
AW 117,53
BR 65
Z 7,12,0
WL 9
LO 16,16,122
LU 122,16
AW 118,54
AW 119,55
M 7,13,1
WL 6
RA 232,1,17408
HZ 25,122
LS 44,64
WV 10
BW 66
Z 8,24,0
LL 18,100
LL 126,101
RV 44,8,6592
M 8,25,1
LA 18
PK 122,16,120,33
v_pk_add_f16 v27, v27, v29
BR 66
Z 9,36,0
WL 6
LC_ 126
PK 120,16,120,32
HX 27
M 9,37,1
WL 6
RA 236,1,18432
LS 48,65
WV 9
BW 67
M 6,2,2
LO 17,17,123
LU 123,17
RV 48,8,6656
M 6,3,3
SU 116
HS 25,123
BR 67
M 7,14,2
s_mul_i32 s53, s50, 25600
LL 19,102
LL 127,103
s_add_u32 s40, %[wlo], s53
s_addc_u32 s41, %[whi], 0
s_mov_b32 m0, s38
M 7,15,3
WL 5
LA 19
PK 123,17,121,33
LS 40,66
WV 9
BW 68
M 8,26,2
LC_ 127
PK 121,17,121,32
RV 40,8,6720
M 8,27,3
WV 8
RA 216,1,19456
BR 68
DW 0
M 9,38,2
WL 6
LO 18,18,126
LU 126,18
M 9,39,3
s_add_u32 s37, s37, 32768
RA 220,1,20480
HS 25,126
LS 44,67
s_cmp_eq_u32 s37, s75
BW 69
M 6,4,4
s_cselect_b32 s37, s74, s37
LL 16,56
LL 104,57
M 6,5,5
LA 16
PK 126,18,124,33
DW 1024
RV 44,8,6784
M 7,16,4
s_add_u32 s42, s44, s64
LC_ 104
PK 124,18,124,32
s_addc_u32 s43, s45, 0
BR 69
M 7,17,5
WL 6
RA 224,1,21504
PO 126
LS 48,68
WV 9
BW 70
M 8,28,4
LO 19,19,127
LU 127,19
ST 120
M 8,29,5
DW 2048
HS 25,127
RV 48,8,6848
M 9,40,4
WL 8
LL 17,58
LL 105,59
RA 240,7,0
M 9,41,5
s_add_u32 s40, s40, 3072
LA 17
PK 127,19,125,33
BR 70
M 6,6,0
s_addc_u32 s41, s41, 0
LC_ 105
PK 125,19,125,32
s_add_u32 m0, s38, 3072
M 6,7,1
WL 7
RA 228,1,22528
PO 127
LS 40,69
WV 10
BW 71
M 7,18,0
LO 104,16,104
LU 16,104
RA 244,7,256
M 7,19,1
RA 232,1,23552
HZ 26,16
BR 71
M 8,30,0
WL 6
LL 18,60
LL 108,61
DW 0
M 8,31,1
s_add_u32 s42, s44, s65
LA 18
HX 25
LS 44,70
s_addc_u32 s43, s45, 0
RA 248,7,512
M 9,42,0
WL 2
LC_ 108
W1 25,1024
W1 27,3072
M 9,43,1
s_lshl_b32 s53, s52, 8
RA 236,1,24576
v_add_u32 v4, s39, v4
LS 48,71
s_add_u32 s48, %[alo], s53
RA 252,7,768
M 6,8,2
LO 105,17,105
LU 17,105
s_addc_u32 s49, %[ahi], 0
s_add_u32 s52, s52, %[d]
s_cmp_eq_u32 s52, %[wrap]
s_cselect_b32 s52, %[rst], s52
DW 1024
v_mul_f32 v36, 0xbb2aaaab, v36
M 6,9,3
ST 124
HS 26,17
v_mul_f32 v37, 0xbb2aaaab, v37
v_mul_f32 v38, 0xbb2aaaab, v38
M 7,20,2
s_mul_i32 s53, s51, 49152
LL 19,62
LL 109,63
s_bitcmp1_b64 s[92:93], s51
s_cselect_b64 s[94:95], s[88:89], s[86:87]
s_add_u32 s94, s94, s53
s_addc_u32 s95, s95, 0
s_add_u32 s51, s51, %[d]
s_cmp_eq_u32 s51, %[wrap]
s_cselect_b32 s51, %[rst], s51
v_mul_f32 v39, 0xbb2aaaab, v39
M 7,21,3
LA 19
DW 2048
LK 40,240,60,0
LK 44,242,60,2
RA 240,7,1024
v_add_u32 v1, s37, v0
LK 48,244,61,0
s_nop 0
CF 40,40,41
LK 248,246,61,2
RA 244,7,1280
CF 42,42,43
M 8,32,2
LC_ 109
RA 216,1,0
M 8,33,3
DQ 3072
v_and_b32 v22, 0xfffffcff, v1
CF 44,44,45
CF 46,46,47
M 9,44,2
WL 6
LO 108,18,108
LU 18,108
AW 120,40
AW 121,42
M 9,45,3
s_add_u32 s50, s50, %[d]
RA 220,1,1024
HS 26,18
AW 122,44
AW 123,46
LK 40,248,62,0
WL 6
CF 48,48,49
)"
R"(LK 44,250,62,2
s_cmp_eq_u32 s50, %[wrap]
RA 248,7,1536
CF 50,50,51
GZ 60
s_cselect_b32 s50, %[rst], s50
CF 248,248,249
CF 250,250,251
M 6,10,4
s_add_u32 s38, s38, 32768
LL 16,64
LL 112,65
M 6,11,5
s_cmp_eq_u32 s38, s77
LA 16
RV 252,22,5120
M 7,22,4
s_cselect_b32 s38, s76, s38
LC_ 112
AW 124,48
M 7,23,5
s_barrier
RA 224,1,2048
AW 125,50
AW 126,248
AW 127,250
LK 48,252,63,0
CF 40,40,41
LK 248,254,63,2
s_sub_u32 s39, 0, s39
RA 252,7,1792
CF 42,42,43
GS 61
CF 44,44,45
CF 46,46,47
M 8,34,4
WL 8
LO 109,19,109
LU 19,109
M 8,35,5
ds_read_b64 v[14:15], v5 offset:0
HS 26,19
M 9,46,4
WL 7
LL 17,66
LL 113,67
M 9,47,5
WL 4
LA 17
RA 228,1,3072
AW 128,40
AW 129,42
Z 0,0,0
LC_ 113
AW 130,44
AW 131,46
M 0,1,1
LK 40,240,64,0
CF 48,48,49
LK 44,242,64,2
RA 240,7,2048
CF 50,50,51
GS 62
CF 248,248,249
CF 250,250,251
Z 1,12,0
WL 7
LO 112,16,112
LU 16,112
M 1,13,1
RA 232,1,4096
HZ 29,16
AW 132,48
Z 2,24,0
LL 18,68
LL 116,69
AW 133,50
AW 134,248
M 2,25,1
LA 18
AW 135,250
LK 48,244,65,0
CF 40,40,41
LK 248,246,65,2
RA 244,7,2304
CF 42,42,43
GS 63
CF 44,44,45
CF 46,46,47
Z 10,36,0
WL 5
LC_ 116
AW 136,40
M 10,37,1
RA 236,1,6144
v_add_f32 v14, v14, v15
M 0,2,2
WL 6
LO 113,17,113
LU 17,113
AW 137,42
M 0,3,3
HS 29,17
v_add_f32_dpp v14, v14, v14 quad_perm:[1,0,3,2] row_mask:0xf bank_mask:0xf
AW 138,44
AW 139,46
LK 40,248,66,0
CF 48,48,49
LK 44,250,66,2
RA 248,7,2560
CF 50,50,51
GS 64
CF 248,248,249
CF 250,250,251
M 1,14,2
LL 19,70
LL 117,71
M 1,15,3
LA 19
RA 216,1,7168
M 2,26,2
LC_ 117
v_add_f32_dpp v14, v14, v14 quad_perm:[2,3,0,1] row_mask:0xf bank_mask:0xf
M 2,27,3
s_nop 0
v_add_f32_dpp v14, v14, v14 row_half_mirror row_mask:0xf bank_mask:0xf
AW 140,48
AW 141,50
AW 142,248
M 10,38,2
WL 4
LO 116,18,116
LU 18,116
AW 143,250
M 10,39,3
RA 220,1,8192
HS 29,18
LK 48,252,67,0
CF 40,40,41
LK 248,254,67,2
RA 252,7,2816
CF 42,42,43
GS 65
CF 44,44,45
CF 46,46,47
M 0,4,4
LL 16,80
LL 106,81
M 0,5,5
LA 16
v_cvt_f64_f32 v[14:15], v14
AW 144,40
AW 145,42
M 1,16,4
LC_ 106
s_mov_b64 exec, s[54:55]
SP
s_mov_b64 exec, -1
AW 146,44
AW 147,46
M 1,17,5
RA 224,1,9216
LK 40,240,68,0
CF 48,48,49
LK 44,242,68,2
CF 50,50,51
GS 66
CF 248,248,249
CF 250,250,251
M 2,28,4
WL 5
LO 117,19,117
LU 19,117
M 2,29,5
ds_read_b64 v[14:15], v5 offset:2048
HS 29,19
AW 148,48
M 10,40,4
WL 5
LL 17,82
LL 107,83
AW 149,50
AW 150,248
AW 151,250
M 10,41,5
LA 17
RA 228,1,10240
LK 48,244,69,0
CF 40,40,41
LK 248,246,69,2
CF 42,42,43
v_subrev_u32 v5, s39, v5
GS 67
CF 44,44,45
CF 46,46,47
M 0,6,0
LC_ 107
v_pk_add_f16 v26, v26, v29
M 0,7,1
HX 26
AW 152,40
M 1,18,0
WL 5
LO 16,16,106
LU 106,16
AW 153,42
AW 154,44
M 1,19,1
RA 232,1,11264
HZ 27,106
AW 155,46
LK 40,248,70,0
CF 48,48,49
LK 44,250,70,2
CF 50,50,51
GS 68
CF 248,248,249
CF 250,250,251
M 2,30,0
LL 18,84
LL 110,85
M 2,31,1
LA 18
PK 106,16,104,33
M 10,42,0
WL 3
LC_ 110
PK 104,16,104,32
AW 156,48
AW 157,50
AW 158,248
M 10,43,1
RV 252,22,5392
v_add_f32 v14, v14, v15
W1 26,2048
AW 159,250
LK 48,252,71,0
CF 40,40,41
LK 248,254,71,2
CF 42,42,43
v_add_f32_dpp v14, v14, v14 quad_perm:[1,0,3,2] row_mask:0xf bank_mask:0xf
GS 69
CF 44,44,45
CF 46,46,47
M 0,8,2
WL 5
LO 17,17,107
LU 107,17
M 0,9,3
RA 236,1,12288
HS 27,107
M 1,20,2
LL 19,86
LL 111,87
AW 160,40
AW 161,42
M 1,21,3
s_add_u32 s42, s94, s60
LA 19
PK 107,17,105,33
v_add_f32_dpp v14, v14, v14 quad_perm:[2,3,0,1] row_mask:0xf bank_mask:0xf
AW 162,44
AW 163,46
RV 36,8,6912
M 2,32,2
s_addc_u32 s43, s95, 0
LC_ 111
PK 105,17,105,32
M 2,33,3
RA 216,1,13312
v_add_f32_dpp v14, v14, v14 row_half_mirror row_mask:0xf bank_mask:0xf
GS 70
CF 48,48,49
CF 50,50,51
CF 248,248,249
CF 250,250,251
M 10,44,2
WL 4
LO 18,18,110
LU 110,18
AW 164,48
AW 165,50
AW 166,248
M 10,45,3
RA 220,1,14336
HS 27,110
v_cvt_f64_f32 v[14:15], v14
AW 167,250
s_nop 1
GS 71
s_mov_b64 exec, s[54:55]
SP
s_mov_b64 exec, -1
s_nop 4
v_cndmask_b32 v52, v52, v53, s[78:79]
M 0,10,4
LL 16,88
LL 114,89
v_cndmask_b32 v54, v54, v55, s[78:79]
M 0,11,5
LA 16
PK 110,18,108,33
v_cndmask_b32 v52, v52, v54, s[80:81]
M 1,22,4
LC_ 114
PK 108,18,108,32
v_mul_f32 v52, 0x3b2aaaab, v52
M 1,23,5
WL 5
RA 224,1,15360
PO 110
v_med3_f32 v53, v52, v39, v37
v_cmp_eq_f32 vcc, v52, v53
M 2,34,4
WL 5
LO 19,19,111
LU 111,19
SU 104
C 52,52,52
M 2,35,5
HS 27,111
v_lshlrev_b32 v52, 16, v52
M 10,46,4
WL 3
LL 17,90
LL 115,91
v_add_f32 v52, v36, v52
M 10,47,5
LA 17
PK 111,19,109,33
C 52,52,52
v_lshlrev_b32 v52, 16, v52
Z 6,0,0
s_add_u32 s42, s94, s61
LC_ 115
PK 109,19,109,32
v_sqrt_f32 v52, v52
M 6,1,1
s_addc_u32 s43, s95, 0
RA 228,1,16384
PO 111
C 52,52,52
Z 7,12,0
WL 4
LO 16,16,114
LU 114,16
v_lshlrev_b32 v52, 16, v52
M 7,13,1
RA 232,1,17408
HZ 29,114
v_rcp_f32 v52, v52
s_nop 0
v_mul_f32 v52, v38, v52
Z 8,24,0
LL 18,92
LL 118,93
v_cndmask_b32 v52, 0, v52, vcc
M 8,25,1
LA 18
PK 114,16,112,33
CF 52,52,52
Z 11,36,0
WL 2
LC_ 118
PK 112,16,112,32
ds_bpermute_b32 v13, v28, v52
M 11,37,1
RA 236,1,18432
AR 248,120
M 6,2,2
LO 17,17,115
LU 115,17
M 6,3,3
SU 108
HS 29,115
M 7,14,2
s_mul_i32 s53, s50, 25600
LL 19,94
LL 119,95
s_add_u32 s40, %[wlo], s53
s_addc_u32 s41, %[whi], 0
s_mov_b32 m0, s38
M 7,15,3
LA 19
PK 115,17,113,33
M 8,26,2
LC_ 119
PK 113,17,113,32
M 8,27,3
RA 216,1,19456
DW 0
M 11,38,2
WL 3
LO 18,18,118
LU 118,18
M 11,39,3
s_add_u32 s37, s37, 32768
RA 220,1,20480
HS 29,118
M 6,4,4
)"
R"(s_cmp_eq_u32 s37, s75
LL 16,56
LL 104,57
s_cselect_b32 s37, s74, s37
AR 249,121
M 6,5,5
LA 16
PK 118,18,116,33
DW 1024
AR 250,122
M 7,16,4
s_add_u32 s42, s94, s62
LC_ 104
PK 116,18,116,32
s_addc_u32 s43, s95, 0
AR 251,123
AR 52,124
M 7,17,5
RA 224,1,21504
PO 118
AR 53,125
M 8,28,4
WL 5
LO 19,19,119
LU 119,19
SU 112
AR 54,126
AR 55,127
M 8,29,5
DW 2048
HS 29,119
XS 248
M 11,40,4
WL 3
LL 17,58
LL 105,59
XS 249
M 11,41,5
s_add_u32 s40, s40, 3072
LA 17
PK 119,19,117,33
XS 250
XS 251
M 6,6,0
s_addc_u32 s41, s41, 0
LC_ 105
PK 117,19,117,32
s_add_u32 m0, s38, 3072
AW 120,248
M 6,7,1
RA 228,1,22528
PO 119
AW 121,249
AW 122,250
M 7,18,0
WL 4
LO 104,16,104
LU 16,104
AW 123,251
M 7,19,1
RA 232,1,23552
HZ 26,16
XS 52
XS 53
M 8,30,0
LL 18,60
LL 108,61
DW 0
XS 54
M 8,31,1
s_add_u32 s42, s94, s63
LA 18
v_pk_add_f16 v27, v27, v29
XS 55
M 11,42,0
WL 2
LC_ 108
HX 27
AW 124,52
AW 125,53
M 11,43,1
s_addc_u32 s43, s95, 0
RA 236,1,24576
W1 27,3072
AW 126,54
M 6,8,2
LO 105,17,105
LU 17,105
s_lshl_b32 s53, s52, 8
s_add_u32 s48, %[alo], s53
s_addc_u32 s49, %[ahi], 0
s_add_u32 s52, s52, %[d]
s_cmp_eq_u32 s52, %[wrap]
s_cselect_b32 s52, %[rst], s52
DW 1024
AW 127,55
v_min_u32 v35, v35, v13
M 6,9,3
SU 116
HS 26,17
AR 248,128
M 7,20,2
s_mul_i32 s53, s51, 49152
LL 19,62
LL 109,63
s_bitcmp1_b64 s[92:93], s51
s_cselect_b64 s[94:95], s[88:89], s[86:87]
s_add_u32 s94, s94, s53
s_addc_u32 s95, s95, 0
s_add_u32 s51, s51, %[d]
s_cmp_eq_u32 s51, %[wrap]
s_cselect_b32 s51, %[rst], s51
AR 249,129
AR 250,130
M 7,21,3
LA 19
DW 2048
AR 251,131
M 8,32,2
LC_ 109
DQ 3072
XS 248
M 8,33,3
s_add_u32 s50, s50, %[d]
v_add_u32 v1, s37, v0
v_add_u32 v4, s39, v4
XS 249
XS 250
M 11,44,2
WL 3
LO 108,18,108
LU 18,108
XS 251
M 11,45,3
s_cmp_eq_u32 s50, %[wrap]
RA 216,1,0
HS 26,18
AW 128,248
AW 129,249
M 6,10,4
s_cselect_b32 s50, %[rst], s50
LL 16,64
LL 112,65
AW 130,250
M 6,11,5
s_add_u32 s38, s38, 32768
LA 16
v_and_b32 v22, 0xfffffcff, v1
AW 131,251
AR 52,132
M 7,22,4
s_cmp_eq_u32 s38, s77
LC_ 112
RA 220,1,1024
AR 53,133
M 7,23,5
s_cselect_b32 s38, s76, s38
RV 252,22,5120
AR 54,134
M 8,34,4
WL 5
LO 109,19,109
LU 19,109
AR 55,135
XS 52
M 8,35,5
s_barrier
RA 224,1,2048
HS 26,19
XS 53
M 11,46,4
WL 1
LL 17,66
LL 113,67
XS 54
XS 55
M 11,47,5
s_sub_u32 s39, 0, s39
LA 17
RA 228,1,3072
AW 132,52
Z 0,0,0
LC_ 113
AW 133,53
AW 134,54
M 0,1,1
AW 135,55
AR 248,136
Z 1,12,0
LO 112,16,112
LU 16,112
M 1,13,1
RA 232,1,4096
HZ 29,16
AR 249,137
AR 250,138
Z 2,24,0
LL 18,96
LL 116,97
AR 251,139
M 2,25,1
LA 18
XS 248
XS 249
Z 3,36,0
LC_ 116
XS 250
M 3,37,1
XS 251
AW 136,248
Z 4,48,0
WL 3
LO 113,17,113
LU 17,113
AW 137,249
M 4,49,1
RA 236,1,6144
HS 29,17
AW 138,250
Z 5,60,0
LL 19,98
LL 117,99
AW 139,251
AR 52,140
M 5,61,1
LA 19
AR 53,141
M 0,2,2
LC_ 117
AR 54,142
AR 55,143
M 0,3,3
XS 52
XS 53
M 1,14,2
WL 3
LO 116,18,116
LU 18,116
XS 54
M 1,15,3
RA 216,1,7168
HS 29,18
XS 55
M 2,26,2
LL 16,80
LL 106,81
AW 140,52
M 2,27,3
LA 16
AW 141,53
AW 142,54
M 3,38,2
LC_ 106
AW 143,55
M 3,39,3
AR 248,144
AR 249,145
M 4,50,2
WL 3
LO 117,19,117
LU 19,117
AR 250,146
M 4,51,3
RA 220,1,8192
HS 29,19
AR 251,147
M 5,62,2
LL 17,82
LL 107,83
XS 248
XS 249
M 5,63,3
LA 17
v_pk_add_f16 v26, v26, v29
XS 250
M 0,4,4
LC_ 107
HX 26
XS 251
AW 144,248
M 0,5,5
W1 26,2048
AW 145,249
M 1,16,4
WL 4
LO 16,16,106
LU 106,16
AW 146,250
AW 147,251
M 1,17,5
RA 224,1,9216
HZ 27,106
AR 52,148
M 2,28,4
LL 18,84
LL 110,85
AR 53,149
M 2,29,5
LA 18
PK 106,16,104,33
AR 54,150
AR 55,151
M 3,40,4
LC_ 110
PK 104,16,104,32
XS 52
M 3,41,5
XS 53
XS 54
M 4,52,4
WL 4
LO 17,17,107
LU 107,17
XS 55
M 4,53,5
RA 228,1,10240
HS 27,107
AW 148,52
AW 149,53
M 5,64,4
LL 19,86
LL 111,87
AW 150,54
M 5,65,5
s_add_u32 s42, s94, s60
LA 19
PK 107,17,105,33
AW 151,55
M 0,6,0
s_addc_u32 s43, s95, 0
LC_ 111
PK 105,17,105,32
AR 248,152
AR 249,153
M 0,7,1
SU 104
AR 250,154
M 1,18,0
WL 3
LO 18,18,110
LU 110,18
AR 251,155
XS 248
M 1,19,1
RA 232,1,11264
HS 27,110
XS 249
M 2,30,0
LL 16,88
LL 114,89
XS 250
XS 251
M 2,31,1
LA 16
PK 110,18,108,33
AW 152,248
M 3,42,0
LC_ 114
PK 108,18,108,32
AW 153,249
M 3,43,1
RV 252,22,5392
PO 110
AW 154,250
AW 155,251
M 4,54,0
WL 4
LO 19,19,111
LU 111,19
AR 52,156
M 4,55,1
RA 236,1,12288
HS 27,111
AR 53,157
AR 54,158
M 5,66,0
LL 17,90
LL 115,91
AR 55,159
M 5,67,1
LA 17
PK 111,19,109,33
XS 52
XS 53
M 0,8,2
s_add_u32 s42, s94, s61
LC_ 115
PK 109,19,109,32
XS 54
M 0,9,3
s_addc_u32 s43, s95, 0
PO 111
XS 55
M 1,20,2
WL 4
LO 16,16,114
LU 114,16
AW 156,52
AW 157,53
M 1,21,3
)"
R"(RA 216,1,13312
HZ 29,114
AW 158,54
M 2,32,2
LL 18,100
LL 118,101
AW 159,55
AR 248,160
M 2,33,3
LA 18
PK 114,16,112,33
AR 249,161
M 3,44,2
LC_ 118
PK 112,16,112,32
AR 250,162
AR 251,163
M 3,45,3
SU 108
XS 248
M 4,56,2
WL 3
LO 17,17,115
LU 115,17
XS 249
M 4,57,3
RA 220,1,14336
HS 29,115
XS 250
XS 251
M 5,68,2
LL 19,102
LL 119,103
AW 160,248
M 5,69,3
s_add_u32 s42, s94, s62
LA 19
PK 115,17,113,33
AW 161,249
AW 162,250
M 0,10,4
s_addc_u32 s43, s95, 0
LC_ 119
PK 113,17,113,32
AW 163,251
M 0,11,5
SU 112
AR 52,164
AR 53,165
M 1,22,4
WL 3
LO 18,18,118
LU 118,18
AR 54,166
M 1,23,5
RA 224,1,15360
HS 29,118
AR 55,167
M 2,34,4
LL 16,56
LL 104,57
XS 52
XS 53
M 2,35,5
LA 16
PK 118,18,116,33
XS 54
M 3,46,4
LC_ 104
PK 116,18,116,32
XS 55
AW 164,52
M 3,47,5
ds_read_b64 v[14:15], v5 offset:2048
PO 118
AW 165,53
M 4,58,4
WL 4
LO 19,19,119
LU 119,19
AW 166,54
AW 167,55
M 4,59,5
RA 228,1,16384
HS 29,119
M 5,70,4
LL 17,58
LL 105,59
M 5,71,5
LA 17
PK 119,19,117,33
Z 6,0,0
s_add_u32 s42, s94, s63
LC_ 105
PK 117,19,117,32
M 6,1,1
s_addc_u32 s43, s95, 0
PO 119
v_subrev_u32 v5, s39, v5
Z 7,12,0
WL 4
LO 104,16,104
LU 16,104
M 7,13,1
RA 232,1,17408
HZ 26,16
Z 8,24,0
LL 18,60
LL 108,61
M 8,25,1
WL 4
LA 18
SU 116
Z 9,36,0
s_mul_i32 s53, s50, 25600
LC_ 108
v_add_f32 v14, v14, v15
M 9,37,1
s_add_u32 s40, %[wlo], s53
v_add_f32_dpp v14, v14, v14 quad_perm:[1,0,3,2] row_mask:0xf bank_mask:0xf
v_pk_add_f16 v27, v27, v29
Z 10,48,0
WL 3
LO 105,17,105
LU 17,105
M 10,49,1
s_addc_u32 s41, %[whi], 0
RA 236,1,18432
HS 26,17
Z 11,60,0
LL 19,62
LL 109,63
M 11,61,1
s_mov_b32 m0, s38
LA 19
DW 0
M 6,2,2
LC_ 109
v_add_f32_dpp v14, v14, v14 quad_perm:[2,3,0,1] row_mask:0xf bank_mask:0xf
M 6,3,3
s_nop 0
v_add_f32_dpp v14, v14, v14 row_half_mirror row_mask:0xf bank_mask:0xf
HX 27
M 7,14,2
WL 3
LO 108,18,108
LU 18,108
M 7,15,3
s_add_u32 s37, s37, 32768
RA 216,1,19456
HS 26,18
W1 27,3072
M 8,26,2
s_cmp_eq_u32 s37, s75
LL 16,64
LL 112,65
M 8,27,3
s_cselect_b32 s37, s74, s37
LA 16
DW 1024
M 9,38,2
LC_ 112
v_cvt_f64_f32 v[14:15], v14
M 9,39,3
s_mov_b64 exec, s[54:55]
SP
s_mov_b64 exec, -1
v_add_u32 v4, s39, v4
M 10,50,2
WL 4
LO 109,19,109
LU 19,109
M 10,51,3
RA 220,1,20480
HS 26,19
M 11,62,2
LL 17,66
LL 113,67
M 11,63,3
LA 17
DW 2048
M 6,4,4
LC_ 113
M 6,5,5
M 7,16,4
WL 3
LO 112,16,112
LU 16,112
M 7,17,5
s_mul_i32 s53, s51, 49152
RA 224,1,21504
HZ 29,16
M 8,28,4
s_bitcmp1_b64 s[92:93], s51
LL 18,68
LL 116,69
M 8,29,5
s_cselect_b64 s[94:95], s[88:89], s[86:87]
LA 18
M 9,40,4
s_add_u32 s94, s94, s53
LC_ 116
M 9,41,5
s_addc_u32 s95, s95, 0
M 10,52,4
WL 3
LO 113,17,113
LU 17,113
M 10,53,5
s_add_u32 s51, s51, %[d]
RA 228,1,22528
HS 29,17
M 11,64,4
LL 19,70
LL 117,71
M 11,65,5
s_cmp_eq_u32 s51, %[wrap]
LA 19
M 6,6,0
s_cselect_b32 s51, %[rst], s51
LC_ 117
M 6,7,1
s_mul_i32 s53, %[d], 32768
M 7,18,0
WL 3
LO 116,18,116
LU 18,116
M 7,19,1
s_add_u32 s64, s53, %[sb1]
RA 232,1,23552
HS 29,18
M 8,30,0
s_add_u32 s65, s64, 512
LL 16,72
LL 120,73
M 8,31,1
s_add_u32 s40, s40, 3072
LA 16
M 9,42,0
s_addc_u32 s41, s41, 0
LC_ 120
s_add_u32 m0, s38, 3072
M 9,43,1
DW 0
M 10,54,0
WL 3
LO 117,19,117
LU 19,117
M 10,55,1
RA 236,1,24576
HS 29,19
M 11,66,0
LL 17,74
LL 121,75
M 11,67,1
LA 17
DW 1024
M 6,8,2
LC_ 121
DW 2048
M 6,9,3
s_lshl_b32 s53, s52, 8
v_add_u32 v1, s37, v0
M 7,20,2
WL 3
LO 120,16,120
LU 16,120
M 7,21,3
s_add_u32 s48, %[alo], s53
RA 216,1,0
HZ 30,16
M 8,32,2
s_addc_u32 s49, %[ahi], 0
LL 18,76
LL 124,77
M 8,33,3
s_add_u32 s52, s52, %[d]
LA 18
v_and_b32 v22, 0xfffffcff, v1
M 9,44,2
s_cmp_eq_u32 s52, %[wrap]
LC_ 124
M 9,45,3
s_cselect_b32 s52, %[rst], s52
DQ 3072
M 10,56,2
WL 3
LO 121,17,121
LU 17,121
M 10,57,3
s_add_u32 s50, s50, %[d]
RA 220,1,1024
HS 30,17
M 11,68,2
LL 19,78
LL 125,79
M 11,69,3
s_cmp_eq_u32 s50, %[wrap]
LA 19
RV 252,22,5120
M 6,10,4
s_cselect_b32 s50, %[rst], s50
LC_ 125
M 6,11,5
WV 25
M 7,22,4
WL 4
LO 124,18,124
LU 18,124
M 7,23,5
s_add_u32 s38, s38, 32768
RA 224,1,2048
HS 30,18
M 8,34,4
s_cmp_eq_u32 s38, s77
LL 16,80
LL 106,81
M 8,35,5
s_cselect_b32 s38, s76, s38
LA 16
M 9,46,4
s_barrier
LC_ 106
M 9,47,5
s_sub_u32 s39, 0, s39
M 10,58,4
WL 3
LO 125,19,125
LU 19,125
M 10,59,5
RA 228,1,3072
HS 30,19
M 11,70,4
LL 17,82
LL 107,83
M 11,71,5
LA 17
v_pk_add_f16 v26, v26, v30
Z 0,0,0
LC_ 107
v_pk_add_f16 v26, v26, v29
M 0,1,1
HX 26
Z 1,12,0
WL 3
LO 16,16,106
LU 106,16
M 1,13,1
RA 232,1,4096
HZ 27,106
Z 2,24,0
LL 18,84
LL 110,85
M 2,25,1
LA 18
PK 106,16,104,33
Z 3,36,0
LC_ 110
PK 104,16,104,32
M 3,37,1
W1 26,2048
Z 4,48,0
WL 4
LO 17,17,107
LU 107,17
M 4,49,1
RA 236,1,6144
HS 27,107
Z 5,60,0
LL 19,86
LL 111,87
M 5,61,1
s_add_u32 s42, s94, s60
LA 19
PK 107,17,105,33
M 0,2,2
s_addc_u32 s43, s95, 0
LC_ 111
PK 105,17,105,32
M 0,3,3
SU 104
M 1,14,2
WL 4
LO 18,18,110
LU 110,18
M 1,15,3
RA 216,1,7168
HS 27,110
M 2,26,2
LL 16,88
LL 114,89
M 2,27,3
LA 16
PK 110,18,108,33
M 3,38,2
LC_ 114
PK 108,18,108,32
M 3,39,3
PO 110
M 4,50,2
)"
R"(WL 3
LO 19,19,111
LU 111,19
M 4,51,3
RA 220,1,8192
HS 27,111
M 5,62,2
LL 17,90
LL 115,91
M 5,63,3
PFN
LA 17
PK 111,19,109,33
M 0,4,4
s_mov_b32 m0, %[pm]
LC_ 115
PK 109,19,109,32
M 0,5,5
DF
PO 111
M 1,16,4
WL 3
LO 16,16,114
LU 114,16
M 1,17,5
s_add_u32 s42, s94, s61
RA 224,1,9216
HZ 29,114
M 2,28,4
s_addc_u32 s43, s95, 0
LL 18,92
LL 118,93
M 2,29,5
LA 18
PK 114,16,112,33
M 3,40,4
LC_ 118
PK 112,16,112,32
M 3,41,5
SU 108
M 4,52,4
WL 3
LO 17,17,115
LU 115,17
M 4,53,5
RA 228,1,10240
HS 29,115
M 5,64,4
LL 19,94
LL 119,95
M 5,65,5
s_add_u32 s42, s94, s62
LA 19
PK 115,17,113,33
M 0,6,0
s_addc_u32 s43, s95, 0
LC_ 119
PK 113,17,113,32
M 0,7,1
SU 112
M 1,18,0
WL 3
LO 18,18,118
LU 118,18
M 1,19,1
RA 232,1,11264
HS 29,118
M 2,30,0
LL 16,96
LL 122,97
M 2,31,1
LA 16
PK 118,18,116,33
M 3,42,0
LC_ 122
PK 116,18,116,32
M 3,43,1
RV 252,22,5392
PO 118
M 4,54,0
WL 4
LO 19,19,119
LU 119,19
M 4,55,1
RA 236,1,12288
HS 29,119
M 5,66,0
LL 17,98
LL 123,99
M 5,67,1
LA 17
PK 119,19,117,33
M 0,8,2
s_add_u32 s42, s94, s63
LC_ 123
PK 117,19,117,32
M 0,9,3
s_addc_u32 s43, s95, 0
PO 119
M 1,20,2
WL 4
LO 16,16,122
LU 122,16
M 1,21,3
RA 216,1,13312
HZ 30,122
M 2,32,2
LL 18,100
LL 126,101
M 2,33,3
LA 18
PK 122,16,120,33
M 3,44,2
LC_ 126
PK 120,16,120,32
M 3,45,3
SU 116
M 4,56,2
WL 3
LO 17,17,123
LU 123,17
M 4,57,3
RA 220,1,14336
HS 30,123
M 5,68,2
LL 19,102
LL 127,103
M 5,69,3
s_add_u32 s42, s94, s64
LA 19
PK 123,17,121,33
M 0,10,4
s_addc_u32 s43, s95, 0
LC_ 127
PK 121,17,121,32
M 0,11,5
SU 120
M 1,22,4
WL 3
LO 18,18,126
LU 126,18
M 1,23,5
RA 224,1,15360
HS 30,126
M 2,34,4
LL 16,56
LL 104,57
M 2,35,5
LA 16
PK 126,18,124,33
M 3,46,4
LC_ 104
PK 124,18,124,32
M 3,47,5
ds_read_b64 v[14:15], v5 offset:2048
PO 126
M 4,58,4
WL 4
LO 19,19,127
LU 127,19
M 4,59,5
RA 228,1,16384
HS 30,127
M 5,70,4
LL 17,58
LL 105,59
M 5,71,5
LA 17
PK 127,19,125,33
Z 6,0,0
s_add_u32 s42, s94, s65
LC_ 105
PK 125,19,125,32
M 6,1,1
s_addc_u32 s43, s95, 0
PO 127
v_subrev_u32 v5, s39, v5
Z 7,12,0
WL 4
LO 104,16,104
LU 16,104
M 7,13,1
RA 232,1,17408
HZ 26,16
Z 8,24,0
LL 18,60
LL 108,61
M 8,25,1
WL 4
LA 18
SU 124
Z 9,36,0
s_mul_i32 s53, s50, 25600
LC_ 108
v_add_f32 v14, v14, v15
M 9,37,1
s_add_u32 s40, %[wlo], s53
v_add_f32_dpp v14, v14, v14 quad_perm:[1,0,3,2] row_mask:0xf bank_mask:0xf
v_pk_add_f16 v27, v27, v30
Z 10,48,0
WL 3
LO 105,17,105
LU 17,105
M 10,49,1
s_addc_u32 s41, %[whi], 0
RA 236,1,18432
HS 26,17
Z 11,60,0
LL 19,62
LL 109,63
M 11,61,1
s_mov_b32 m0, s38
LA 19
DW 0
M 6,2,2
LC_ 109
v_add_f32_dpp v14, v14, v14 quad_perm:[2,3,0,1] row_mask:0xf bank_mask:0xf
M 6,3,3
s_nop 0
v_add_f32_dpp v14, v14, v14 row_half_mirror row_mask:0xf bank_mask:0xf
v_pk_add_f16 v27, v27, v29
M 7,14,2
WL 3
LO 108,18,108
LU 18,108
M 7,15,3
s_add_u32 s37, s37, 32768
RA 216,1,19456
HS 26,18
HX 27
M 8,26,2
s_cmp_eq_u32 s37, s75
LL 16,64
LL 112,65
W1 27,3072
M 8,27,3
s_cselect_b32 s37, s74, s37
LA 16
DW 1024
M 9,38,2
LC_ 112
v_cvt_f64_f32 v[14:15], v14
M 9,39,3
s_mov_b64 exec, s[54:55]
SP
s_mov_b64 exec, -1
v_add_u32 v4, s39, v4
s_mov_b64 exec, 1
AT
s_mov_b64 exec, -1
M 10,50,2
WL 4
LO 109,19,109
LU 19,109
M 10,51,3
RA 220,1,20480
HS 26,19
M 11,62,2
LL 17,66
LL 113,67
M 11,63,3
LA 17
DW 2048
M 6,4,4
LC_ 113
M 6,5,5
M 7,16,4
WL 3
LO 112,16,112
LU 16,112
M 7,17,5
s_mul_i32 s53, s51, 49152
RA 224,1,21504
HZ 29,16
M 8,28,4
s_bitcmp1_b64 s[92:93], s51
LL 18,68
LL 116,69
M 8,29,5
s_cselect_b64 s[94:95], s[88:89], s[86:87]
LA 18
M 9,40,4
s_add_u32 s94, s94, s53
LC_ 116
M 9,41,5
s_addc_u32 s95, s95, 0
M 10,52,4
WL 3
LO 113,17,113
LU 17,113
M 10,53,5
s_add_u32 s51, s51, %[d]
RA 228,1,22528
HS 29,17
M 11,64,4
LL 19,70
LL 117,71
M 11,65,5
s_cmp_eq_u32 s51, %[wrap]
LA 19
M 6,6,0
s_cselect_b32 s51, %[rst], s51
LC_ 117
M 6,7,1
s_add_u32 s40, s40, 3072
M 7,18,0
WL 3
LO 116,18,116
LU 18,116
M 7,19,1
s_addc_u32 s41, s41, 0
RA 232,1,23552
HS 29,18
M 8,30,0
s_add_u32 m0, s38, 3072
LL 16,72
LL 120,73
M 8,31,1
LA 16
DW 0
M 9,42,0
LC_ 120
DW 1024
M 9,43,1
M 10,54,0
WL 3
LO 117,19,117
LU 19,117
M 10,55,1
RA 236,1,24576
HS 29,19
M 11,66,0
LL 17,74
LL 121,75
M 11,67,1
LA 17
DW 2048
M 6,8,2
LC_ 121
M 6,9,3
s_lshl_b32 s53, s52, 8
v_add_u32 v1, s37, v0
M 7,20,2
WL 3
LO 120,16,120
LU 16,120
M 7,21,3
s_add_u32 s48, %[alo], s53
RA 216,1,0
HZ 30,16
M 8,32,2
s_addc_u32 s49, %[ahi], 0
LL 18,76
LL 124,77
M 8,33,3
s_add_u32 s52, s52, %[d]
LA 18
v_and_b32 v22, 0xfffffcff, v1
M 9,44,2
s_cmp_eq_u32 s52, %[wrap]
LC_ 124
M 9,45,3
s_cselect_b32 s52, %[rst], s52
DQ 3072
M 10,56,2
WL 3
LO 121,17,121
LU 17,121
M 10,57,3
s_add_u32 s50, s50, %[d]
RA 220,1,1024
HS 30,17
M 11,68,2
LL 19,78
LL 125,79
M 11,69,3
s_cmp_eq_u32 s50, %[wrap]
LA 19
RV 252,22,5120
M 6,10,4
s_cselect_b32 s50, %[rst], s50
LC_ 125
M 6,11,5
WV 28
M 7,22,4
WL 4
LO 124,18,124
LU 18,124
M 7,23,5
s_add_u32 s38, s38, 32768
RA 224,1,2048
HS 30,18
M 8,34,4
s_cmp_eq_u32 s38, s77
LL 16,80
LL 106,81
M 8,35,5
s_cselect_b32 s38, s76, s38
LA 16
M 9,46,4
)"
R"(s_barrier
LC_ 106
M 9,47,5
s_sub_u32 s39, 0, s39
M 10,58,4
WL 3
LO 125,19,125
LU 19,125
M 10,59,5
RA 228,1,3072
HS 30,19
M 11,70,4
LL 17,82
LL 107,83
M 11,71,5
LA 17
v_pk_add_f16 v26, v26, v30
Z 0,0,0
LC_ 107
v_pk_add_f16 v26, v26, v29
M 0,1,1
HX 26
Z 1,12,0
WL 3
LO 16,16,106
LU 106,16
M 1,13,1
RA 232,1,4096
HZ 27,106
Z 2,24,0
LL 18,84
LL 110,85
M 2,25,1
LA 18
PK 106,16,104,33
Z 3,36,0
LC_ 110
PK 104,16,104,32
M 3,37,1
W1 26,2048
Z 4,48,0
WL 4
LO 17,17,107
LU 107,17
M 4,49,1
RA 236,1,6144
HS 27,107
Z 5,60,0
LL 19,86
LL 111,87
M 5,61,1
s_add_u32 s42, s94, s60
LA 19
PK 107,17,105,33
M 0,2,2
s_addc_u32 s43, s95, 0
LC_ 111
PK 105,17,105,32
M 0,3,3
SU 104
M 1,14,2
WL 4
LO 18,18,110
LU 110,18
M 1,15,3
RA 216,1,7168
HS 27,110
M 2,26,2
LL 16,88
LL 114,89
M 2,27,3
LA 16
PK 110,18,108,33
M 3,38,2
LC_ 114
PK 108,18,108,32
M 3,39,3
PO 110
M 4,50,2
WL 3
LO 19,19,111
LU 111,19
M 4,51,3
RA 220,1,8192
HS 27,111
M 5,62,2
LL 17,90
LL 115,91
M 5,63,3
PFW
LA 17
PK 111,19,109,33
M 0,4,4
s_mov_b32 m0, %[pm]
LC_ 115
PK 109,19,109,32
M 0,5,5
DF
PO 111
M 1,16,4
WL 3
LO 16,16,114
LU 114,16
M 1,17,5
s_add_u32 s42, s94, s61
RA 224,1,9216
HZ 29,114
M 2,28,4
s_addc_u32 s43, s95, 0
LL 18,92
LL 118,93
M 2,29,5
LA 18
PK 114,16,112,33
M 3,40,4
LC_ 118
PK 112,16,112,32
M 3,41,5
SU 108
M 4,52,4
WL 3
LO 17,17,115
LU 115,17
M 4,53,5
RA 228,1,10240
HS 29,115
M 5,64,4
LL 19,94
LL 119,95
M 5,65,5
s_add_u32 s42, s94, s62
LA 19
PK 115,17,113,33
M 0,6,0
s_addc_u32 s43, s95, 0
LC_ 119
PK 113,17,113,32
M 0,7,1
SU 112
M 1,18,0
WL 3
LO 18,18,118
LU 118,18
M 1,19,1
RA 232,1,11264
HS 29,118
M 2,30,0
LL 16,96
LL 122,97
M 2,31,1
LA 16
PK 118,18,116,33
M 3,42,0
LC_ 122
PK 116,18,116,32
M 3,43,1
RV 252,22,5392
PO 118
M 4,54,0
WL 4
LO 19,19,119
LU 119,19
M 4,55,1
RA 236,1,12288
HS 29,119
M 5,66,0
LL 17,98
LL 123,99
M 5,67,1
LA 17
PK 119,19,117,33
M 0,8,2
s_add_u32 s42, s94, s63
LC_ 123
PK 117,19,117,32
M 0,9,3
s_addc_u32 s43, s95, 0
PO 119
M 1,20,2
WL 4
LO 16,16,122
LU 122,16
M 1,21,3
RA 216,1,13312
HZ 30,122
M 2,32,2
LL 18,100
LL 126,101
M 2,33,3
LA 18
PK 122,16,120,33
M 3,44,2
LC_ 126
PK 120,16,120,32
M 3,45,3
SU 116
M 4,56,2
WL 3
LO 17,17,123
LU 123,17
M 4,57,3
RA 220,1,14336
HS 30,123
M 5,68,2
LL 19,102
LL 127,103
M 5,69,3
s_add_u32 s42, s94, s64
LA 19
PK 123,17,121,33
M 0,10,4
s_addc_u32 s43, s95, 0
LC_ 127
PK 121,17,121,32
M 0,11,5
SU 120
M 1,22,4
WL 3
LO 18,18,126
LU 126,18
M 1,23,5
RA 224,1,15360
HS 30,126
M 2,34,4
LL 16,56
LL 104,57
M 2,35,5
LA 16
PK 126,18,124,33
M 3,46,4
LC_ 104
PK 124,18,124,32
M 3,47,5
ds_read_b64 v[14:15], v5 offset:2048
PO 126
M 4,58,4
WL 4
LO 19,19,127
LU 127,19
M 4,59,5
RA 228,1,16384
HS 30,127
M 5,70,4
LL 17,58
LL 105,59
M 5,71,5
LA 17
PK 127,19,125,33
Z 6,0,0
s_add_u32 s42, s94, s65
LC_ 105
PK 125,19,125,32
M 6,1,1
s_addc_u32 s43, s95, 0
PO 127
v_subrev_u32 v5, s39, v5
Z 7,12,0
WL 4
LO 104,16,104
LU 16,104
M 7,13,1
RA 232,1,17408
HZ 26,16
Z 8,24,0
LL 18,60
LL 108,61
M 8,25,1
WL 4
LA 18
SU 124
Z 9,36,0
s_mul_i32 s53, s50, 25600
LC_ 108
v_add_f32 v14, v14, v15
M 9,37,1
s_add_u32 s40, %[wlo], s53
v_add_f32_dpp v14, v14, v14 quad_perm:[1,0,3,2] row_mask:0xf bank_mask:0xf
v_pk_add_f16 v27, v27, v30
Z 10,48,0
WL 3
LO 105,17,105
LU 17,105
M 10,49,1
s_addc_u32 s41, %[whi], 0
RA 236,1,18432
HS 26,17
Z 11,60,0
LL 19,62
LL 109,63
M 11,61,1
s_mov_b32 m0, s38
LA 19
DW 0
M 6,2,2
LC_ 109
v_add_f32_dpp v14, v14, v14 quad_perm:[2,3,0,1] row_mask:0xf bank_mask:0xf
M 6,3,3
WV 13
v_add_f32_dpp v14, v14, v14 row_half_mirror row_mask:0xf bank_mask:0xf
v_pk_add_f16 v27, v27, v29
M 7,14,2
WL 3
LO 108,18,108
LU 18,108
M 7,15,3
s_add_u32 s37, s37, 32768
RA 216,1,19456
HS 26,18
HX 27
M 8,26,2
s_cmp_eq_u32 s37, s75
LL 16,64
LL 112,65
W1 27,3072
M 8,27,3
s_cselect_b32 s37, s74, s37
LA 16
DW 1024
v_readfirstlane_b32 s72, v11
M 9,38,2
LC_ 112
v_cvt_f64_f32 v[14:15], v14
v_mov_b32 v12, s72
M 9,39,3
W2 12,12,16
M 10,50,2
WL 5
LO 109,19,109
LU 19,109
M 10,51,3
RA 220,1,20480
HS 26,19
M 11,62,2
LL 17,66
LL 113,67
M 11,63,3
LA 17
s_mov_b64 exec, s[54:55]
SP
s_mov_b64 exec, -1
M 6,4,4
LC_ 113
DW 2048
M 6,5,5
v_add_u32 v4, s39, v4
s_mov_b64 exec, 1
AT
s_mov_b64 exec, -1
M 7,16,4
WL 4
LO 112,16,112
LU 16,112
M 7,17,5
s_mul_i32 s53, s51, 49152
RA 224,1,21504
HZ 29,16
M 8,28,4
s_bitcmp1_b64 s[92:93], s51
LL 18,68
LL 116,69
M 8,29,5
s_cselect_b64 s[94:95], s[88:89], s[86:87]
LA 18
M 9,40,4
s_add_u32 s94, s94, s53
LC_ 116
M 9,41,5
s_addc_u32 s95, s95, 0
M 10,52,4
WL 3
LO 113,17,113
LU 17,113
M 10,53,5
s_add_u32 s51, s51, %[d]
RA 228,1,22528
HS 29,17
M 11,64,4
LL 19,70
LL 117,71
M 11,65,5
s_cmp_eq_u32 s51, %[wrap]
LA 19
M 6,6,0
s_cselect_b32 s51, %[rst], s51
LC_ 117
M 6,7,1
s_add_u32 s40, s40, 3072
M 7,18,0
WL 3
LO 116,18,116
LU 18,116
M 7,19,1
s_addc_u32 s41, s41, 0
RA 232,1,23552
HS 29,18
M 8,30,0
s_add_u32 m0, s38, 3072
LL 16,72
LL 120,73
M 8,31,1
)"
R"(LA 16
DW 0
M 9,42,0
LC_ 120
DW 1024
M 9,43,1
M 10,54,0
WL 3
LO 117,19,117
LU 19,117
M 10,55,1
RA 236,1,24576
HS 29,19
M 11,66,0
LL 17,74
LL 121,75
M 11,67,1
LA 17
DW 2048
M 6,8,2
LC_ 121
M 6,9,3
s_lshl_b32 s53, s52, 8
v_add_u32 v1, s37, v0
M 7,20,2
WL 3
LO 120,16,120
LU 16,120
M 7,21,3
s_add_u32 s48, %[alo], s53
RA 216,1,0
HZ 30,16
M 8,32,2
s_addc_u32 s49, %[ahi], 0
LL 18,76
LL 124,77
M 8,33,3
s_add_u32 s52, s52, %[d]
LA 18
v_and_b32 v22, 0xfffffcff, v1
M 9,44,2
s_cmp_eq_u32 s52, %[wrap]
LC_ 124
M 9,45,3
s_cselect_b32 s52, %[rst], s52
DQ 3072
M 10,56,2
WL 3
LO 121,17,121
LU 17,121
M 10,57,3
s_add_u32 s50, s50, %[d]
RA 220,1,1024
HS 30,17
M 11,68,2
LL 19,78
LL 125,79
M 11,69,3
s_cmp_eq_u32 s50, %[wrap]
LA 19
RV 252,22,5120
M 6,10,4
s_cselect_b32 s50, %[rst], s50
LC_ 125
M 6,11,5
M 7,22,4
WL 4
LO 124,18,124
LU 18,124
M 7,23,5
s_add_u32 s38, s38, 32768
RA 224,1,2048
HS 30,18
M 8,34,4
s_cmp_eq_u32 s38, s77
LL 16,80
LL 106,81
M 8,35,5
s_cselect_b32 s38, s76, s38
LA 16
M 9,46,4
s_barrier
LC_ 106
M 9,47,5
s_sub_u32 s39, 0, s39
M 10,58,4
WL 3
LO 125,19,125
LU 19,125
M 10,59,5
RA 228,1,3072
HS 30,19
M 11,70,4
LL 17,82
LL 107,83
M 11,71,5
2:
LA 17
v_pk_add_f16 v26, v26, v30
Z 0,0,0
LC_ 107
ds_read_b32 v20, v5 offset:4096
M 0,1,1
v_pk_add_f16 v26, v26, v29
Z 1,12,0
WL 4
LO 16,16,106
LU 106,16
M 1,13,1
RA 232,1,4096
HZ 27,106
Z 2,24,0
LL 18,84
LL 110,85
M 2,25,1
LA 18
PK 106,16,104,33
Z 3,36,0
LC_ 110
PK 104,16,104,32
M 3,37,1
HX 26
Z 4,48,0
WL 4
LO 17,17,107
LU 107,17
M 4,49,1
RA 236,1,6144
HS 27,107
Z 5,60,0
LL 19,86
LL 111,87
M 5,61,1
s_add_u32 s42, s94, s60
LA 19
PK 107,17,105,33
M 0,2,2
s_addc_u32 s43, s95, 0
LC_ 111
PK 105,17,105,32
M 0,3,3
WL 6
W1 26,2048
v_readfirstlane_b32 s59, v20
M 1,14,2
WL 4
LO 18,18,110
LU 110,18
M 1,15,3
RA 216,1,7168
HS 27,110
M 2,26,2
LL 16,88
LL 114,89
M 2,27,3
LA 16
PK 110,18,108,33
M 3,38,2
LC_ 114
PK 108,18,108,32
M 3,39,3
SU 104
PO 110
M 4,50,2
WL 4
LO 19,19,111
LU 111,19
M 4,51,3
RA 220,1,8192
HS 27,111
M 5,62,2
LL 17,90
LL 115,91
M 5,63,3
LA 17
PK 111,19,109,33
M 0,4,4
s_add_u32 s42, s94, s61
LC_ 115
PK 109,19,109,32
M 0,5,5
s_addc_u32 s43, s95, 0
PO 111
M 1,16,4
WL 3
LO 16,16,114
LU 114,16
M 1,17,5
RA 224,1,9216
HZ 29,114
M 2,28,4
LL 18,92
LL 118,93
M 2,29,5
LA 18
PK 114,16,112,33
M 3,40,4
LC_ 118
PK 112,16,112,32
M 3,41,5
SU 108
M 4,52,4
WL 3
LO 17,17,115
LU 115,17
M 4,53,5
RA 228,1,10240
HS 29,115
M 5,64,4
LL 19,94
LL 119,95
M 5,65,5
s_add_u32 s42, s94, s62
LA 19
PK 115,17,113,33
M 0,6,0
s_addc_u32 s43, s95, 0
LC_ 119
PK 113,17,113,32
M 0,7,1
SU 112
M 1,18,0
WL 3
LO 18,18,118
LU 118,18
M 1,19,1
RA 232,1,11264
HS 29,118
M 2,30,0
LL 16,96
LL 122,97
M 2,31,1
LA 16
PK 118,18,116,33
M 3,42,0
LC_ 122
PK 116,18,116,32
M 3,43,1
RV 252,22,5392
PO 118
M 4,54,0
WL 4
LO 19,19,119
LU 119,19
M 4,55,1
RA 236,1,12288
HS 29,119
M 5,66,0
LL 17,98
LL 123,99
M 5,67,1
LA 17
PK 119,19,117,33
M 0,8,2
s_add_u32 s42, s94, s63
LC_ 123
PK 117,19,117,32
M 0,9,3
s_addc_u32 s43, s95, 0
PO 119
M 1,20,2
WL 4
LO 16,16,122
LU 122,16
M 1,21,3
RA 216,1,13312
HZ 30,122
M 2,32,2
LL 18,100
LL 126,101
M 2,33,3
LA 18
PK 122,16,120,33
M 3,44,2
LC_ 126
PK 120,16,120,32
M 3,45,3
SU 116
M 4,56,2
WL 3
LO 17,17,123
LU 123,17
M 4,57,3
RA 220,1,14336
HS 30,123
M 5,68,2
LL 19,102
LL 127,103
M 5,69,3
s_add_u32 s42, s94, s64
LA 19
PK 123,17,121,33
M 0,10,4
s_addc_u32 s43, s95, 0
LC_ 127
PK 121,17,121,32
M 0,11,5
SU 120
M 1,22,4
WL 3
LO 18,18,126
LU 126,18
M 1,23,5
RA 224,1,15360
HS 30,126
M 2,34,4
LL 16,56
LL 104,57
M 2,35,5
LA 16
PK 126,18,124,33
M 3,46,4
LC_ 104
PK 124,18,124,32
M 3,47,5
ds_read_b64 v[14:15], v5 offset:2048
PO 126
M 4,58,4
WL 4
LO 19,19,127
LU 127,19
M 4,59,5
RA 228,1,16384
HS 30,127
M 5,70,4
LL 17,58
LL 105,59
M 5,71,5
LA 17
PK 127,19,125,33
Z 6,0,0
s_add_u32 s42, s94, s65
LC_ 105
PK 125,19,125,32
M 6,1,1
s_addc_u32 s43, s95, 0
PO 127
v_subrev_u32 v5, s39, v5
Z 7,12,0
WL 4
LO 104,16,104
LU 16,104
M 7,13,1
RA 232,1,17408
HZ 26,16
Z 8,24,0
LL 18,60
LL 108,61
M 8,25,1
WL 4
LA 18
SU 124
Z 9,36,0
s_mul_i32 s53, s50, 25600
LC_ 108
v_add_f32 v14, v14, v15
M 9,37,1
s_add_u32 s40, %[wlo], s53
v_add_f32_dpp v14, v14, v14 quad_perm:[1,0,3,2] row_mask:0xf bank_mask:0xf
v_pk_add_f16 v27, v27, v30
Z 10,48,0
WL 3
LO 105,17,105
LU 17,105
M 10,49,1
s_addc_u32 s41, %[whi], 0
RA 236,1,18432
HS 26,17
Z 11,60,0
LL 19,62
LL 109,63
M 11,61,1
s_mov_b32 m0, s38
LA 19
DW 0
M 6,2,2
LC_ 109
v_add_f32_dpp v14, v14, v14 quad_perm:[2,3,0,1] row_mask:0xf bank_mask:0xf
M 6,3,3
WV 11
v_add_f32_dpp v14, v14, v14 row_half_mirror row_mask:0xf bank_mask:0xf
v_pk_add_f16 v27, v27, v29
M 7,14,2
WL 3
LO 108,18,108
LU 18,108
M 7,15,3
s_add_u32 s37, s37, 32768
RA 216,1,19456
HS 26,18
HX 27
M 8,26,2
s_cmp_eq_u32 s37, s75
LL 16,64
LL 112,65
W1 27,3072
M 8,27,3
s_cselect_b32 s37, s74, s37
LA 16
DW 1024
)"
R"(v_readfirstlane_b32 s72, v11
M 9,38,2
LC_ 112
v_cvt_f64_f32 v[14:15], v14
v_mov_b32 v12, s72
M 9,39,3
s_max_u32 s59, s59, %[nb]
W2 12,12,16
M 10,50,2
WL 5
LO 109,19,109
LU 19,109
M 10,51,3
s_add_u32 %[nb], %[nb], 1
RA 220,1,20480
HS 26,19
M 11,62,2
LL 17,66
LL 113,67
M 11,63,3
LA 17
s_mov_b64 exec, s[54:55]
SP
s_mov_b64 exec, -1
M 6,4,4
LC_ 113
DW 2048
M 6,5,5
v_add_u32 v4, s39, v4
s_mov_b64 exec, 1
AT
s_mov_b64 exec, -1
M 7,16,4
WL 4
LO 112,16,112
LU 16,112
M 7,17,5
s_mul_i32 s53, s51, 49152
RA 224,1,21504
HZ 29,16
M 8,28,4
s_bitcmp1_b64 s[92:93], s51
LL 18,68
LL 116,69
M 8,29,5
s_cselect_b64 s[94:95], s[88:89], s[86:87]
LA 18
M 9,40,4
s_add_u32 s94, s94, s53
LC_ 116
M 9,41,5
s_addc_u32 s95, s95, 0
M 10,52,4
WL 3
LO 113,17,113
LU 17,113
M 10,53,5
s_add_u32 s51, s51, %[d]
RA 228,1,22528
HS 29,17
M 11,64,4
LL 19,70
LL 117,71
M 11,65,5
s_cmp_eq_u32 s51, %[wrap]
LA 19
M 6,6,0
s_cselect_b32 s51, %[rst], s51
LC_ 117
M 6,7,1
s_add_u32 s40, s40, 3072
M 7,18,0
WL 3
LO 116,18,116
LU 18,116
M 7,19,1
s_addc_u32 s41, s41, 0
RA 232,1,23552
HS 29,18
M 8,30,0
s_add_u32 m0, s38, 3072
LL 16,72
LL 120,73
M 8,31,1
LA 16
DW 0
M 9,42,0
LC_ 120
DW 1024
M 9,43,1
M 10,54,0
WL 3
LO 117,19,117
LU 19,117
M 10,55,1
RA 236,1,24576
HS 29,19
M 11,66,0
LL 17,74
LL 121,75
M 11,67,1
LA 17
DW 2048
M 6,8,2
LC_ 121
M 6,9,3
s_lshl_b32 s53, s52, 8
v_add_u32 v1, s37, v0
M 7,20,2
WL 3
LO 120,16,120
LU 16,120
M 7,21,3
s_add_u32 s48, %[alo], s53
RA 216,1,0
HZ 30,16
M 8,32,2
s_addc_u32 s49, %[ahi], 0
LL 18,76
LL 124,77
M 8,33,3
s_add_u32 s52, s52, %[d]
LA 18
v_and_b32 v22, 0xfffffcff, v1
M 9,44,2
s_cmp_eq_u32 s52, %[wrap]
LC_ 124
M 9,45,3
s_cselect_b32 s52, %[rst], s52
DQ 3072
M 10,56,2
WL 3
LO 121,17,121
LU 17,121
M 10,57,3
s_add_u32 s50, s50, %[d]
RA 220,1,1024
HS 30,17
M 11,68,2
LL 19,78
LL 125,79
M 11,69,3
s_cmp_eq_u32 s50, %[wrap]
LA 19
RV 252,22,5120
M 6,10,4
s_cselect_b32 s50, %[rst], s50
LC_ 125
M 6,11,5
M 7,22,4
WL 4
LO 124,18,124
LU 18,124
M 7,23,5
s_add_u32 s38, s38, 32768
RA 224,1,2048
HS 30,18
M 8,34,4
s_cmp_eq_u32 s38, s77
LL 16,80
LL 106,81
M 8,35,5
s_cselect_b32 s38, s76, s38
LA 16
M 9,46,4
s_barrier
LC_ 106
M 9,47,5
s_sub_u32 s39, 0, s39
M 10,58,4
WL 3
LO 125,19,125
LU 19,125
M 10,59,5
RA 228,1,3072
HS 30,19
M 11,70,4
s_cmp_lt_u32 s59, 32
LL 17,82
LL 107,83
M 11,71,5
s_cbranch_scc1 2b
LA 17
v_pk_add_f16 v26, v26, v30
LC_ 107
ds_read_b64 v[14:15], v5 offset:2048
v_pk_add_f16 v26, v26, v29
HX 26
v_subrev_u32 v5, s39, v5
W1 26,2048
WL 0
LO 16,16,106
LU 106,16
HZ 27,106
LL 18,84
LL 110,85
LA 18
PK 106,16,104,33
v_add_f32 v14, v14, v15
s_nop 1
v_add_f32_dpp v14, v14, v14 quad_perm:[1,0,3,2] row_mask:0xf bank_mask:0xf
s_nop 1
v_add_f32_dpp v14, v14, v14 quad_perm:[2,3,0,1] row_mask:0xf bank_mask:0xf
s_nop 1
v_add_f32_dpp v14, v14, v14 row_half_mirror row_mask:0xf bank_mask:0xf
v_cvt_f64_f32 v[14:15], v14
s_mov_b64 exec, s[54:55]
SP
s_mov_b64 exec, -1
LC_ 110
PK 104,16,104,32
LO 17,17,107
LU 107,17
HS 27,107
LL 19,86
LL 111,87
LA 19
PK 107,17,105,33
s_add_u32 s42, s94, s60
s_addc_u32 s43, s95, 0
LC_ 111
PK 105,17,105,32
SU 104
WL 0
LO 18,18,110
LU 110,18
HS 27,110
LL 16,88
LL 114,89
LA 16
PK 110,18,108,33
LC_ 114
PK 108,18,108,32
PO 110
LO 19,19,111
LU 111,19
HS 27,111
LL 17,90
LL 115,91
LA 17
PK 111,19,109,33
LC_ 115
PK 109,19,109,32
s_add_u32 s42, s94, s61
s_addc_u32 s43, s95, 0
PO 111
WL 0
LO 16,16,114
LU 114,16
SU 108
HZ 29,114
LL 18,92
LL 118,93
LA 18
PK 114,16,112,33
LC_ 118
PK 112,16,112,32
LO 17,17,115
LU 115,17
HS 29,115
LL 19,94
LL 119,95
LA 19
PK 115,17,113,33
s_add_u32 s42, s94, s62
s_addc_u32 s43, s95, 0
LC_ 119
PK 113,17,113,32
SU 112
WL 0
LO 18,18,118
LU 118,18
HS 29,118
LL 16,96
LL 122,97
LA 16
PK 118,18,116,33
LC_ 122
PK 116,18,116,32
PO 118
LO 19,19,119
LU 119,19
HS 29,119
LL 17,98
LL 123,99
LA 17
PK 119,19,117,33
LC_ 123
PK 117,19,117,32
s_add_u32 s42, s94, s63
s_addc_u32 s43, s95, 0
PO 119
WL 0
LO 16,16,122
LU 122,16
SU 116
HZ 30,122
LL 18,100
LL 126,101
LA 18
PK 122,16,120,33
LC_ 126
PK 120,16,120,32
LO 17,17,123
LU 123,17
HS 30,123
LL 19,102
LL 127,103
LA 19
PK 123,17,121,33
s_add_u32 s42, s94, s64
s_addc_u32 s43, s95, 0
LC_ 127
PK 121,17,121,32
SU 120
WL 0
LO 18,18,126
LU 126,18
HS 30,126
PK 126,18,124,33
PK 124,18,124,32
PO 126
LO 19,19,127
LU 127,19
HS 30,127
v_pk_add_f16 v27, v27, v30
PK 127,19,125,33
v_pk_add_f16 v27, v27, v29
PK 125,19,125,32
HX 27
s_add_u32 s42, s94, s65
s_addc_u32 s43, s95, 0
PO 127
W1 27,3072
s_nop 1
v_add_u32 v4, s39, v4
SU 124
s_lshl_b32 s53, s52, 8
s_add_u32 s48, %[alo], s53
s_addc_u32 s49, %[ahi], 0
s_add_u32 s52, s52, %[d]
s_cmp_eq_u32 s52, %[wrap]
s_cselect_b32 s52, %[rst], s52
WL 0
WV 0
s_barrier
s_sub_u32 s39, 0, s39
ds_read_b64 v[14:15], v5 offset:2048
s_nop 1
v_subrev_u32 v5, s39, v5
WL 0
v_add_f32 v14, v14, v15
s_nop 1
v_add_f32_dpp v14, v14, v14 quad_perm:[1,0,3,2] row_mask:0xf bank_mask:0xf
s_nop 1
v_add_f32_dpp v14, v14, v14 quad_perm:[2,3,0,1] row_mask:0xf bank_mask:0xf
s_nop 1
v_add_f32_dpp v14, v14, v14 row_half_mirror row_mask:0xf bank_mask:0xf
v_cvt_f64_f32 v[14:15], v14
s_mov_b64 exec, s[54:55]
SP
s_mov_b64 exec, -1
GE
s_waitcnt vmcnt(0) lgkmcnt(0)
8:
.purgem M
.purgem Z
.purgem LZ
.purgem LS
.purgem LD
.purgem GZ
.purgem GS
.purgem CL
.purgem CH
.purgem XL
.purgem XN
.purgem F
.purgem U
.purgem E
.purgem A
.purgem R
.purgem C
.purgem S0
.purgem S
.purgem HC
.purgem HF
.purgem HM
.purgem HE
.purgem HG
.purgem HA
.purgem HR
.purgem HQ
.purgem HP
.purgem HY
.purgem HK
.purgem LQ
.purgem LL
.purgem LH
.purgem LA
.purgem LC_
)"
R"(.purgem LO
.purgem HZ
.purgem HS
.purgem HT
.purgem HX
.purgem UX
.purgem EX
.purgem AX
.purgem RX
.purgem TX
.purgem SX
.purgem AW
.purgem RA
.purgem RV
.purgem W2
.purgem W1
.purgem DW
.purgem DF
.purgem DX
.purgem ST
.purgem SU
.purgem SP
.purgem AT
.purgem PFA
.purgem PFN
.purgem PFW
.purgem PF0
.purgem WL
.purgem WV
.purgem LU
.purgem CF
.purgem XS
.purgem AR
.purgem DQ
.purgem GE
.purgem BW
.purgem BR
.purgem LK
.purgem PK
.purgem PO
)"
      : [nb] "=&s"(nb)
      : [xlo] "s"((unsigned)xp), [xhi] "s"((unsigned)(xp >> 32)), [hlo] "s"((unsigned)hp), [hhi] "s"((unsigned)(hp >> 32)),
        [glo] "s"((unsigned)gp), [ghi] "s"((unsigned)(gp >> 32)), [wlo] "s"((unsigned)wp), [whi] "s"((unsigned)(wp >> 32)),
        [w] "s"(w), [lds] "s"(lds0), [d] "s"(d), [wrap] "s"(wrap), [rst] "s"(rst), [pos0] "s"(pos0), [xo0] "s"(xo0), [xs1] "s"(xs1), [sb1] "s"(sb1), [r0o] "s"(r0o), [tsel] "s"(tsel), [s2] "s"(s2),
        [alo] "s"((unsigned)ap), [ahi] "s"((unsigned)(ap >> 32)), [ca0] "s"(ca0), [pm] "s"(pm), [pf] "s"(pf)
      : K1_CLOB);
  TS(6);
  MK_END(1);
}

typedef __bf16 bf16x8_t __attribute__((ext_vector_type(8)));
DI f32x4 mfma16(u32x4 a, u32x4 b, f32x4 c) {
  return __builtin_amdgcn_mfma_f32_16x16x32_bf16(__builtin_bit_cast(bf16x8_t, a), __builtin_bit_cast(bf16x8_t, b), c, 0, 0, 0);
}
DI float grn_gx(double s, float scale) { return round_bf16(__builtin_amdgcn_sqrtf(round_bf16(round_bf16((float)s * scale) + 1e-6f))); }
DI void slow_fc1(const u16* __restrict__ x, const u16* __restrict__ dw, char* __restrict__ hws, float* sacc, int w) {
  const int lane = __builtin_amdgcn_mbcnt_hi(~0u, __builtin_amdgcn_mbcnt_lo(~0u, 0u)), m = lane & 15, g = lane >> 4;
  for (int i = lane; i < HID; i += 64) sacc[i] = 0.f;
#pragma unroll 1
  for (int it = 0; it < 9; ++it) {
    const int sub = blockIdx.x * 36 + it * 4 + w;
    const unsigned px = sub_pixel(x, sub, m);
    u32x4 A[12];
    float sum = 0.f;
    const u16* dq = dw;
    asm volatile("" : "+s"(dq));
#pragma unroll
    for (int ks = 0; ks < 12; ++ks) {
      const u32x4 v = *(const u32x4*)(x + (size_t)px * CH + 32 * ks + 8 * g);
      const u32x4 d = *(const u32x4*)(dq + 32 * ks + 8 * g);
#pragma unroll
      for (int j = 0; j < 4; ++j) {
        A[ks][j] = pack_bf16(bf_lo(v[j]) * bf_lo(d[j]), bf_hi(v[j]) * bf_hi(d[j]));
        sum += bf_lo(A[ks][j]) + bf_hi(A[ks][j]);
      }
      asm volatile("" : "+v"(A[ks]));
    }
    sum += __shfl_xor(sum, 16); sum += __shfl_xor(sum, 32);
    const float mean = sum * (1.0f / CH);
    float var = 0.f;
#pragma unroll
    for (int ks = 0; ks < 12; ++ks) {
#pragma unroll
      for (int j = 0; j < 4; ++j) {
        const float a = bf_lo(A[ks][j]) - mean, b = bf_hi(A[ks][j]) - mean;
        var += a * a + b * b;
      }
      asm volatile("" : "+v"(A[ks]));
    }
    var += __shfl_xor(var, 16); var += __shfl_xor(var, 32);
    const float mb = round_bf16(mean);
    const float rd = 1.0f / round_bf16(sqrtf(round_bf16(round_bf16(var * (1.0f / CH)) + 1e-5f)));
#pragma unroll
    for (int ks = 0; ks < 12; ++ks)
#pragma unroll
      for (int j = 0; j < 4; ++j) A[ks][j] = pack_bf16((bf_lo(A[ks][j]) - mb) * rd, (bf_hi(A[ks][j]) - mb) * rd);
#pragma unroll 1
    for (int tc = 0; tc < 96; ++tc) {
      f32x4 acc = {0.f, 0.f, 0.f, 0.f};
#pragma unroll
      for (int ks = 0; ks < 12; ++ks) acc = mfma16(A[ks], *(const u32x4*)(g_buf + (size_t)(tc * 12 + ks) * 1024 + lane * 16), acc);
      float q = 0.f;
#pragma unroll
      for (int r = 0; r < 4; ++r) { acc[r] = gelu_tanh(acc[r]); q = __builtin_fmaf(acc[r], acc[r], q); }
      typedef unsigned u32x2_ __attribute__((ext_vector_type(2)));
      *(u32x2_*)(hws + h_piece(hws, sub, tc >> 1) + g * 256 + ((m + 4 * g) & 15) * 16 + (tc & 1) * 8) = (u32x2_){pack_bf16(acc[0], acc[1]), pack_bf16(acc[2], acc[3])};
      q += __shfl_xor(q, 16); q += __shfl_xor(q, 32);
      if (g == 0) sacc[tc * 16 + m] += q;
    }
  }
  __builtin_amdgcn_wave_barrier();
  __builtin_amdgcn_fence(__ATOMIC_ACQ_REL, "workgroup");
  for (int i = lane; i < HID; i += 64) atomicAdd((double*)(g_buf + G_ACC2) + i, (double)sacc[i]);
}
DI void slow_bar(unsigned* c, int w) {
  const int lane = __builtin_amdgcn_mbcnt_hi(~0u, __builtin_amdgcn_mbcnt_lo(~0u, 0u));
  asm volatile("s_waitcnt vmcnt(0)" ::: "memory");
  __syncthreads();
  if (w == 0 && lane == 0) {
    __threadfence();
    asm volatile("s_waitcnt vmcnt(0)" ::: "memory");
    __hip_atomic_fetch_add(c, 1u, __ATOMIC_RELAXED, __HIP_MEMORY_SCOPE_AGENT);
    for (int i = 0; i < (1 << 22) && __hip_atomic_load(c, __ATOMIC_RELAXED, __HIP_MEMORY_SCOPE_AGENT) < (unsigned)NWG; ++i) __builtin_amdgcn_s_sleep(32);
    __threadfence();
    asm volatile("s_waitcnt vmcnt(0)" ::: "memory");
  }
  __syncthreads();
}

#define K2_CLOB "memory", "vcc", "scc", "s48", "s49", "s50", "s51", "s52", "s53", "s54", "s55", "s56", "s57", "s58", "s59", "s60", "s61", "s62", "s63", "s64", "s65", "s66", "s67", \
  "s68", "s69", "s70", "s71", "s72", "s73", "s74", "s75", "s76", "s77", "s78", "s79", "s80", "s81", "s84", "s85", "s86", "s87", "s88", "s89", "s90", "s91", "s92", "s93", "s94", "s95", "s96", "s97", "s98", "s99", "s38", "s39", "s40", "s41", "s42", "s43", "s44", "s45", "s46", "s47", VA_CLOB
DI void slow_fc2(const u16* __restrict__ x, const char* __restrict__ hws, u16* __restrict__ out, const u16* __restrict__ grn_g, const u16* __restrict__ w2,
                 char* smem, int w) {
  float* sc = (float*)smem;
  float* red = sc + HID;
  u16* wf = (u16*)(smem + 8192);
  const int lane = __builtin_amdgcn_mbcnt_hi(~0u, __builtin_amdgcn_mbcnt_lo(~0u, 0u)), tid = w * 64 + lane, m = lane & 15, g = lane >> 4;
  __syncthreads();
  float part = 0.f;
  for (int c = tid; c < HID; c += 256) {
    const float gx = grn_gx(((const double*)(g_buf + G_ACC2))[c], 1.0f);
    sc[c] = gx; part += gx;
  }
#pragma unroll
  for (int o = 32; o > 0; o >>= 1) part += __shfl_xor(part, o);
  if (lane == 0) red[tid >> 6] = part;
  __syncthreads();
  const float gmean = round_bf16(round_bf16(((red[0] + red[1]) + (red[2] + red[3])) * (1.0f / HID)) + 1e-6f);
  for (int c = tid; c < HID; c += 256) sc[c] = (1.0f + ld_bf16(grn_g + c) * round_bf16(sc[c] / gmean)) ;
#pragma unroll 1
  for (int it = 0; it < 9; ++it) {
    const int sub = blockIdx.x * 36 + it * 4 + (tid >> 6);
    f32x4 acc[24];
#pragma unroll
    for (int i = 0; i < 24; ++i) acc[i] = (f32x4){0.f, 0.f, 0.f, 0.f};
#pragma unroll 1
    for (int t = 0; t < 48; ++t) {
      __syncthreads();
      for (int e = tid; e < 32 * CH; e += 256) {
        const int k = e / CH, n = e % CH;
        wf[((n >> 4) * 64 + (k >> 3) * 16 + (n & 15)) * 8 + (k & 7)] = (u16)pack_bf16(sc[32 * t + k] * ld_bf16(w2 + (size_t)(32 * t + k) * CH + n), 0.f);
      }
      __syncthreads();
      const char* hp = hws + h_piece(hws, sub, t) + (m >> 2) * 256 + (g >> 1) * 8 + 2 * (m & 3);
      u32x4 A;
#pragma unroll
      for (int j = 0; j < 4; ++j) {
        unsigned v[2];
#pragma unroll
        for (int k = 0; k < 2; ++k) {
          v[k] = *(const u16*)(hp + ((8 * (g & 1) + 2 * j + k + (m & 12)) & 15) * 16);
        }
        A[j] = v[0] | (v[1] << 16);
      }
#pragma unroll
      for (int i = 0; i < 24; ++i) acc[i] = mfma16(A, *(const u32x4*)(wf + (i * 64 + lane) * 8), acc[i]);
    }
#pragma unroll
    for (int i = 0; i < 24; ++i) {
      const int n = 16 * i + m;
      const float* bp = g_cs + 4 * 384 + cs_index(n);
      const float bias = (bp[0] + bp[384]) + (bp[768] + bp[1152]);
#pragma unroll
      for (int r = 0; r < 4; ++r) {
        const size_t o = (size_t)(sub_pixel(x, sub, 4 * g) + r) * CH + n;
        out[o] = (u16)pack_bf16(acc[i][r] + bias + ld_bf16(x + o), 0.f);
      }
    }
  }
}
__global__ __launch_bounds__(256, 1) void k2_fc2(const u16* __restrict__ x, char* __restrict__ hws, u16* __restrict__ out,
                                                 const u16* __restrict__ grn_g, const u16* __restrict__ w2, const u16* __restrict__ dw,
                                                 const u16* __restrict__ w1) {
  extern __shared__ __attribute__((aligned(16))) char smem[];
  const int w = __builtin_amdgcn_readfirstlane(threadIdx.x >> 6);
  const unsigned pg = w & 1, nh = w >> 1;
  const unsigned lds0 = lds_addr(smem);
  MK_START(4); TS(9);
  const uint64_t pp = uni64((uint64_t)g_prm + (blockIdx.x * 4 + w) * 256), ap = (uint64_t)g_buf + K1_G_ACC + w * 1024;
  const unsigned wm = lds0 + w * 6144, ab = lds0 + nh * 12288;
  const unsigned hb = lds0 + K2_HB + pg * 2 * K2_RSZ, hm = hb + nh * K2_RSZ;
  const unsigned bm = w * 384, bb = lds0 + K2_E_OFF + nh * 768;
  const unsigned pm = lds0 + K2_PF_OFF, sb = lds0 + K2_S16_OFF, sw = sb + w * 256;
  uint64_t flg;
#define K2_OPS [pp] "s"(pp), [alo] "s"((unsigned)ap), [ahi] "s"((unsigned)(ap >> 32)), [wm] "s"(wm), [hm] "s"(hm), [bm] "s"(bm), [ab] "s"(ab), [hb] "s"(hb), [bb] "s"(bb), \
         [sb] "s"(sb), [sw] "s"(sw), [pm] "s"(pm)
  asm volatile(
R"(.macro MA t,a,b
v_mfma_f32_16x16x32_f16 a[\t:\t+3], v[\b:\b+3], a[\a:\a+3], a[\t:\t+3]
.endm
.macro MV t,a,b
v_mfma_f32_16x16x32_f16 v[\t:\t+3], v[\b:\b+3], a[\a:\a+3], v[\t:\t+3]
.endm
.macro NA t,a,b
v_mfma_f32_16x16x32_f16 a[\t:\t+3], a[\b:\b+3], a[\a:\a+3], a[\t:\t+3]
.endm
.macro NV t,a,b
v_mfma_f32_16x16x32_f16 v[\t:\t+3], a[\b:\b+3], a[\a:\a+3], v[\t:\t+3]
.endm
.macro AN d,s
v_and_b32 v[\d], s93, v[\s]
.endm
.macro SH d
v_lshrrev_b32 v[\d], 4, v[\d]
.endm
.macro PA_ d,h,r
v_perm_b32 v[\d], v[\h], v[\r], s54
.endm
.macro PB_ d,h,r
v_perm_b32 v[\d], v[\h], v[\r], s55
.endm
.macro AW a,s
v_accvgpr_write_b32 a[\a], v[\s]
.endm
.macro AR d,a
v_accvgpr_read_b32 v[\d], a[\a]
.endm
.macro UL d,x
v_lshlrev_b32 v[\d], 16, v[\x]
.endm
.macro UH d,x
v_and_b32 v[\d], s38, v[\x]
.endm
.macro AD d,b
v_fmac_f32 v[\d], s53, v[\b]
.endm
.macro EF d,e
v_fma_f32 v[\d], v[\d], s39, v[\e]
.endm
.macro TY d,o
ds_read_b64_tr_b16 v[\d:\d+1], v2 offset:\o
.endm
.macro TX d,o
ds_read_b64_tr_b16 v[\d:\d+1], v51 offset:\o
.endm
.macro RA d,o
ds_read_b128 a[\d:\d+3], v1 offset:\o
.endm
.macro RC d,o
ds_read_b128 a[\d:\d+3], v5 offset:\o
.endm
.macro BV d,o
ds_read_b128 v[\d:\d+3], v4 offset:\o
.endm
.macro BA d,o
ds_read_b128 a[\d:\d+3], v4 offset:\o
.endm
.macro PA a
s_mov_b32 m0, %[pm]
s_getpc_b64 s[46:47]
s_andn2_b32 s46, s46, 0x7f
s_add_u32 s46, s46, s81
s_addc_u32 s47, s47, 0
s_add_u32 s46, s46, \a
s_addc_u32 s47, s47, 0
.endm
.macro DF
global_load_lds_dwordx4 v0, s[46:47]
.endm
.macro DW o
global_load_lds_dwordx4 v0, s[40:41] offset:\o sc1
.endm
.macro DR o
global_load_lds_dwordx4 v0, s[40:41] offset:\o
.endm
.macro DH o
global_load_lds_dwordx4 v0, s[40:41] offset:\o nt
.endm
.macro DG o
s_mov_b32 exec_hi, 0
global_load_lds_dwordx4 v0, s[40:41] offset:\o nt
s_mov_b32 exec_hi, -1
.endm
.macro XL d,o
global_load_dwordx2 v[\d:\d+1], v47, s[44:45] offset:\o
.endm
.macro ST d,o
global_store_dwordx2 v50, v[\d:\d+1], s[42:43] offset:\o nt sc1
.endm
.macro SA d,o
global_store_dwordx2 v50, a[\d:\d+1], s[42:43] offset:\o nt sc1
.endm
.macro CV d,a,b
v_cvt_pk_bf16_f32 v[\d], v[\a], v[\b]
.endm
.macro RS d,o
ds_read_b128 v[\d:\d+3], v46 offset:\o
.endm
.macro PM d,s
v_pk_mul_f16 v[\d], v[\d], v[\s]
.endm
.macro PF d,n
v_pk_fma_f16 v[\d], v[\d], v[14+\n], v[22+\n]
.endm
.macro FS
global_store_dword v0, v54, s[96:97] sc1
.endm
.macro FL
global_load_dword v54, v55, s[98:99] sc1
.endm
.macro FB
s_cbranch_vccnz 21b
22:
.endm
.macro FC
s_cbranch_vccnz 26b
27:
.endm
.macro FG
s_or_b64 s[54:55], s[44:45], vcc
s_cbranch_scc1 23b
24:
.endm
.macro FX
s_branch 25f
21:
s_sleep 4
global_load_dword v54, v55, s[98:99] sc1
s_waitcnt vmcnt(0)
v_cmp_eq_u32 vcc, 0, v54
s_sub_u32 s80, s80, 1
s_cbranch_scc1 22f
s_nop 2
s_cbranch_vccnz 21b
s_branch 22f
26:
s_sleep 4
global_load_dword v54, v55, s[98:99] sc1
s_waitcnt vmcnt(0)
v_cmp_ne_u32 vcc, 2, v54
s_sub_u32 s80, s80, 1
s_cbranch_scc1 27f
s_nop 2
s_cbranch_vccnz 26b
s_branch 27f
23:
v_mov_b32 v10, 1
s_mov_b64 exec, 1
global_store_dword v0, v10, s[78:79] offset:384 sc1
s_mov_b64 exec, -1
s_nop 4
s_branch 24f
25:
.endm
.macro WL n
s_waitcnt lgkmcnt(\n)
.endm
.macro WV n
s_waitcnt vmcnt(\n)
.endm
.macro M n
.if \n < 82
v_mfma_f32_16x16x32_f16 a[36+4*(\n/2):36+4*(\n/2)+3], v[56+8*((\n/2)%%6)+4*(\n&1):56+8*((\n/2)%%6)+4*(\n&1)+3], a[8*((\n/12)%%4)+4*(\n&1):8*((\n/12)%%4)+4*(\n&1)+3], a[36+4*(\n/2):36+4*(\n/2)+3]
.else
v_mfma_f32_16x16x32_f16 v[-32+4*(\n/2):-32+4*(\n/2)+3], v[56+8*((\n/2)%%6)+4*(\n&1):56+8*((\n/2)%%6)+4*(\n&1)+3], a[8*((\n/12)%%4)+4*(\n&1):8*((\n/12)%%4)+4*(\n&1)+3], v[-32+4*(\n/2):-32+4*(\n/2)+3]
.endif
.endm
.macro R1 n
.if \n < 64
RA 8*(((\n%%24)/2+2*((\n/24)&1))%%4)+4*(\n&1),1024*\n
.else
RC 8*(((\n%%24)/2+2*((\n/24)&1))%%4)+4*(\n&1),1024*\n-49152
.endif
.endm
.macro R n
R1 \n
R1 (\n+1)
.endm
.macro P n
.if \n & 2
PB_ 56+\n,104+8*(\n/16)+(\n&4)+2*((\n/8)&1)+(\n&1),56+(\n|2)
.else
PA_ 56+\n,104+8*(\n/16)+(\n&4)+2*((\n/8)&1)+(\n&1),56+(\n|2)
.endif
.endm
.macro Q n
P \n
P (\n+1)
.endm
.macro mw k
s_add_u32 m0, %[wm], 1024*\k
.endm
.macro mh k
s_add_u32 m0, %[hm], 512*\k
.endm
.macro ad r,k
s_add_u32 s40, s[\r], 512*\k
s_addc_u32 s41, s[\r+1], 0
.endm
.macro ae d,r,k
s_add_u32 s[\d], s[\r], 512*\k
s_addc_u32 s[\d+1], s[\r+1], 0
.endm
.macro bc m,b,d,r
s_bitcmp1_b32 s[\m], \b
s_cselect_b64 s[\d:\d+1], s[\r+2:\r+3], s[\r:\r+1]
.endm
.macro W k
DW 1024*\k
.endm
.macro H k
DH 1024*\k
.endm
k2_code:
s_load_dwordx16 s[48:63], %[pp], 0x0
s_load_dwordx16 s[64:79], %[pp], 0x40
s_load_dwordx16 s[84:99], %[pp], 0x80
v_mbcnt_lo_u32_b32 v7, -1, 0
v_mbcnt_hi_u32_b32 v7, -1, v7
v_lshlrev_b32 v0, 4, v7
v_add_u32 v88, 0x1000, v0
v_add_u32 v89, 0x2000, v0
s_mov_b32 s44, %[alo]
s_mov_b32 s45, %[ahi]
global_load_dwordx4 v[132:135], v0, s[44:45]
global_load_dwordx4 v[144:147], v88, s[44:45]
global_load_dwordx4 v[148:151], v89, s[44:45]
ae 44,44,24
global_load_dwordx4 v[168:171], v0, s[44:45]
global_load_dwordx4 v[172:175], v88, s[44:45]
global_load_dwordx4 v[192:195], v89, s[44:45]
s_waitcnt lgkmcnt(0)
bc 49,0,66,60
mh 0
ad 66,0
H 0
DG 1024
ae 44,44,24
global_load_dwordx4 v[196:199], v0, s[44:45]
global_load_dwordx4 v[216:219], v88, s[44:45]
global_load_dwordx4 v[220:223], v89, s[44:45]
ae 44,44,24
global_load_dwordx4 v[224:227], v0, s[44:45]
global_load_dwordx4 v[228:231], v88, s[44:45]
global_load_dwordx4 v[240:243], v89, s[44:45]
v_and_b32 v8, 15, v7
v_lshrrev_b32 v9, 4, v7
v_lshlrev_b32 v10, 2, v7
v_add_u32 v202, s65, v0
global_load_dword v244, v10, s[92:93] offset:0
global_load_dword v245, v10, s[92:93] offset:1024
global_load_dword v246, v10, s[92:93] offset:2048
global_load_dwordx4 v[152:155], v0, s[88:89]
s_mov_b64 exec, 0xff
global_load_dwordx4 v[156:159], v202, s[88:89]
s_mov_b64 exec, -1
FX
s_mov_b32 s92, s64
s_mov_b32 s93, 0x0f0f0f0f
v_add_u32 v1, %[ab], v0
v_add_u32 v5, 0xc000, v1
v_and_b32 v10, 3, v7
v_lshrrev_b32 v11, 2, v7
v_lshl_add_u32 v11, v10, 2, v11
v_and_b32 v11, 15, v11
mw 0
ad 86,0
DR 0
v_lshl_add_u32 v11, v10, 4, v11
v_lshl_add_u32 v2, v11, 4, %[hb]
v_lshl_add_u32 v51, v11, 3, %[hb]
v_lshl_add_u32 v4, v8, 4, %[bb]
s_mov_b32 s38, 0xffff0000
s_mov_b32 s80, 0x40000
s_mov_b32 s81, s52
s_mov_b32 s39, s54
v_lshl_add_u32 v46, v9, 4, %[sb]
v_add_u32 v46, 8192, v46
v_bfe_u32 v10, v7, 1, 2
DR 1024
v_bfe_u32 v11, v7, 3, 1
v_and_b32 v12, 1, v7
v_lshl_add_u32 v11, v11, 1, v12
v_lshl_add_u32 v10, v10, 2, v11
v_lshl_add_u32 v10, v9, 4, v10
v_lshl_add_u32 v90, v10, 2, %[sw]
DR 2048
v_lshl_add_u32 v91, v7, 2, %[sw]
s_and_b32 s42, s51, 0xffff
s_lshr_b32 s43, s51, 16
s_add_u32 s42, s42, %[sb]
s_add_u32 s43, s43, %[sb]
v_lshl_add_u32 v200, v9, 4, s42
v_mov_b32 v201, s43
v_mov_b32 v128, 0x35400000
v_mov_b32 v129, 0x35400000
v_mov_b32 v130, 0x35400000
v_mov_b32 v131, 0x35400000
mw 3
ad 86,6
DR 0
AW 32,128
AW 33,129
AW 34,130
AW 35,131
v_mov_b32 v95, 0x7f800000
s_mov_b32 s44, 0x44100000
WV 19
v_add_f64 v[132:133], v[132:133], v[168:169]
WV 18
v_add_f64 v[144:145], v[144:145], v[172:173]
DR 1024
WV 18
v_add_f64 v[148:149], v[148:149], v[192:193]
v_add_f64 v[134:135], v[134:135], v[170:171]
v_add_f64 v[146:147], v[146:147], v[174:175]
v_add_f64 v[150:151], v[150:151], v[194:195]
WV 15
v_add_f64 v[132:133], v[132:133], v[196:197]
WV 14
v_add_f64 v[144:145], v[144:145], v[216:217]
DR 2048
WV 14
v_add_f64 v[148:149], v[148:149], v[220:221]
v_add_f64 v[134:135], v[134:135], v[198:199]
v_add_f64 v[146:147], v[146:147], v[218:219]
v_add_f64 v[150:151], v[150:151], v[222:223]
WV 13
v_add_f64 v[132:133], v[132:133], v[224:225]
WV 12
v_add_f64 v[144:145], v[144:145], v[228:229]
bc 49,1,66,60
mh 9
ad 66,192
H 0
WV 12
v_add_f64 v[148:149], v[148:149], v[240:241]
v_add_f64 v[134:135], v[134:135], v[226:227]
v_add_f64 v[146:147], v[146:147], v[230:231]
v_add_f64 v[150:151], v[150:151], v[242:243]
v_cvt_f32_f64 v248, v[132:133]
v_cvt_f32_f64 v252, v[144:145]
DG 1024
v_cvt_f32_f64 v72, v[148:149]
v_cvt_f32_f64 v249, v[134:135]
v_cvt_f32_f64 v253, v[146:147]
v_cvt_f32_f64 v73, v[150:151]
v_mul_f32 v250, s55, v248
v_mul_f32 v254, s55, v252
bc 48,1,64,56
mh 12
ad 64,192
H 0
v_mul_f32 v74, s55, v72
v_mul_f32 v251, s55, v249
v_mul_f32 v255, s55, v253
v_mul_f32 v75, s55, v73
CV 251,250,251
CV 255,254,255
DG 1024
CV 75,74,75
UL 250,251
UL 254,255
UL 74,75
UH 251,251
UH 255,255
mw 24
ad 86,48
DR 0
UH 75,75
v_add_f32 v250, 0x358637bd, v250
v_add_f32 v254, 0x358637bd, v254
v_add_f32 v74, 0x358637bd, v74
v_add_f32 v251, 0x358637bd, v251
v_add_f32 v255, 0x358637bd, v255
DR 1024
v_add_f32 v75, 0x358637bd, v75
CV 251,250,251
CV 255,254,255
CV 75,74,75
UL 250,251
UL 254,255
DR 2048
UL 74,75
UH 251,251
UH 255,255
UH 75,75
v_sqrt_f32 v250, v250
v_sqrt_f32 v254, v254
mw 27
ad 86,54
DR 0
v_sqrt_f32 v74, v74
v_sqrt_f32 v251, v251
v_sqrt_f32 v255, v255
v_sqrt_f32 v75, v75
CV 251,250,251
)"
R"(CV 255,254,255
DR 1024
CV 75,74,75
UL 250,251
UL 254,255
UL 74,75
UH 251,251
UH 255,255
DR 2048
UH 75,75
v_cmp_le_f32 vcc, s44, v248
v_cmp_le_f32 s[54:55], s44, v249
s_nop 0
v_cndmask_b32 v248, v95, v250, vcc
v_cndmask_b32 v249, v95, v251, s[54:55]
v_add_f32 v248, v248, v249
v_cmp_le_f32 vcc, s44, v252
v_cmp_le_f32 s[54:55], s44, v253
s_nop 0
v_cndmask_b32 v252, v95, v254, vcc
v_cndmask_b32 v253, v95, v255, s[54:55]
v_add_f32 v252, v252, v253
v_cmp_le_f32 vcc, s44, v72
v_cmp_le_f32 s[54:55], s44, v73
s_nop 0
v_cndmask_b32 v72, v95, v74, vcc
v_cndmask_b32 v73, v95, v75, s[54:55]
bc 49,2,66,60
mh 18
ad 66,384
H 0
v_add_f32 v72, v72, v73
v_add_f32 v92, v248, v252
v_add_f32 v92, v92, v72
s_nop 1
v_add_f32_dpp v92, v92, v92 row_shr:1 row_mask:0xf bank_mask:0xf
s_nop 1
v_add_f32_dpp v92, v92, v92 row_shr:2 row_mask:0xf bank_mask:0xf
s_nop 1
v_add_f32_dpp v92, v92, v92 row_shr:4 row_mask:0xf bank_mask:0xf
s_nop 1
v_add_f32_dpp v92, v92, v92 row_shr:8 row_mask:0xf bank_mask:0xf
s_nop 1
v_add_f32_dpp v92, v92, v92 row_bcast:15 row_mask:0xa bank_mask:0xf
s_nop 1
v_add_f32_dpp v92, v92, v92 row_bcast:31 row_mask:0xc bank_mask:0xf
ds_write_b32 v91, v92 offset:7168
v_mov_b32 v10, %[sb]
WL 0
s_barrier
ds_read_b32 v96, v10 offset:7420
ds_read_b32 v97, v10 offset:7676
ds_read_b32 v98, v10 offset:7932
ds_read_b32 v99, v10 offset:8188
WV 21
UL 76,244
WV 20
UL 80,245
WV 19
UL 84,246
UH 77,244
DG 1024
UH 81,245
UH 85,246
v_mov_b32 v93, 0
WL 0
v_add_f32 v96, v96, v97
v_add_f32 v98, v98, v99
v_add_f32 v96, v96, v98
bc 48,2,64,56
mh 21
ad 64,384
H 0
v_mul_f32 v94, 0x3a2aaaab, v96
v_cmp_nlt_f32 s[44:45], v96, v95
CV 94,94,94
UL 94,94
v_add_f32 v94, 0x358637bd, v94
CV 94,94,94
UL 94,94
H 1
v_rcp_f32 v94, v94
s_nop 0
v_mul_f32 v250, v94, v250
v_mul_f32 v254, v94, v254
v_mul_f32 v74, v94, v74
v_mul_f32 v251, v94, v251
v_mul_f32 v255, v94, v255
v_mul_f32 v75, v94, v75
H 2
CV 251,250,251
CV 255,254,255
CV 75,74,75
UL 250,251
UL 254,255
UL 74,75
mw 48
ad 86,96
DR 0
UH 251,251
UH 255,255
UH 75,75
v_fma_f32 v250, v76, v250, 1.0
v_fma_f32 v254, v80, v254, 1.0
v_fma_f32 v74, v84, v74, 1.0
DR 1024
v_fma_f32 v251, v77, v251, 1.0
v_fma_f32 v255, v81, v255, 1.0
v_fma_f32 v75, v85, v75, 1.0
v_cvt_pk_f16_f32 v76, v250, v251
v_cvt_pk_f16_f32 v80, v254, v255
v_cvt_pk_f16_f32 v84, v74, v75
ds_write_b32 v90, v76 offset:8192
ds_write_b32 v90, v80 offset:1024
ds_write_b32 v90, v84 offset:2048
v_max3_f32 v93, |v250|, |v251|, v93
DR 2048
v_max3_f32 v93, |v254|, |v255|, v93
v_max3_f32 v93, |v74|, |v75|, v93
v_cmp_lt_f32 vcc, 0x43000000, v93
WL 0
WV 16
s_barrier
TY 106,520
TY 110,14344
TX 58,0
TX 62,13824
TY 66,512
TY 70,14336
RS 14,0
RS 18,64
ds_read_b128 v[176:179], v200
ds_read_b128 v[180:183], v201
R 0
R 2
FG
s_mov_b32 s54, 0x06020400
s_mov_b32 s55, 0x07030501
WL 13
AN 104,106
AN 105,107
SH 106
SH 107
mw 51
ad 86,102
DR 0
AN 106,106
AN 107,107
WL 12
AN 108,110
AN 109,111
SH 110
SH 111
DR 1024
AN 110,110
AN 111,111
WL 10
Q 0
Q 2
DR 2048
Q 4
Q 6
R 4
R 6
WL 7
v_pk_mul_f16 v22, v14, s92
v_pk_mul_f16 v23, v15, s92
v_pk_mul_f16 v24, v16, s92
v_pk_mul_f16 v25, v17, s92
v_pk_mul_f16 v26, v18, s92
v_pk_mul_f16 v27, v19, s92
v_pk_mul_f16 v28, v20, s92
v_pk_mul_f16 v29, v21, s92
PF 56,0
PF 57,1
PF 58,2
PF 59,3
PF 60,4
PF 61,5
PF 62,6
PF 63,7
Q 8
Q 10
Q 12
Q 14
PF 64,0
PF 65,1
PF 66,2
PF 67,3
PF 68,4
PF 69,5
PF 70,6
PF 71,7
PM 152,176
PM 153,177
PM 154,178
PM 155,179
PM 156,180
PM 157,181
PM 158,182
PM 159,183
s_mov_b32 s42, 0x1c001c00
v_pk_mul_f16 v152, v152, s42
v_pk_mul_f16 v153, v153, s42
v_pk_mul_f16 v154, v154, s42
v_pk_mul_f16 v155, v155, s42
v_pk_mul_f16 v156, v156, s42
v_pk_mul_f16 v157, v157, s42
v_pk_mul_f16 v158, v158, s42
v_pk_mul_f16 v159, v159, s42
global_store_dwordx4 v0, v[152:155], s[90:91] sc1
s_mov_b64 exec, 0xff
global_store_dwordx4 v202, v[156:159], s[90:91] sc1
s_mov_b64 exec, -1
s_mov_b32 s42, 0x3c003c00
v_dot2_f32_f16 v52, v152, s42, 0
v_dot2_f32_f16 v53, v156, s42, 0
s_nop 1
v_dot2_f32_f16 v52, v153, s42, v52
v_dot2_f32_f16 v53, v157, s42, v53
s_nop 1
v_dot2_f32_f16 v52, v154, s42, v52
v_dot2_f32_f16 v53, v158, s42, v53
s_nop 1
v_dot2_f32_f16 v52, v155, s42, v52
v_dot2_f32_f16 v53, v159, s42, v53
s_nop 1
WL 0
v_mfma_f32_16x16x32_f16 a[36:39], v[56:59], a[0:3], a[32:35]
mw 72
RS 14,128
RS 18,192
TY 106,5128
M 1
ad 86,144
DR 0
TY 110,18952
v_mfma_f32_16x16x32_f16 a[60:63], v[56:59], a[8:11], a[32:35]
DR 1024
TX 32,4608
TX 36,18432
M 13
DR 2048
TY 40,5120
TY 44,18944
v_mfma_f32_16x16x32_f16 a[40:43], v[64:67], a[0:3], a[32:35]
mw 75
M 3
R 8
v_mfma_f32_16x16x32_f16 a[64:67], v[64:67], a[8:11], a[32:35]
ad 86,150
DR 0
M 15
R 10
v_mfma_f32_16x16x32_f16 a[84:87], v[56:59], a[16:19], a[32:35]
WL 10
DR 1024
v_pk_mul_f16 v22, v14, s92
M 25
DR 2048
v_pk_mul_f16 v23, v15, s92
v_mfma_f32_16x16x32_f16 a[88:91], v[64:67], a[16:19], a[32:35]
bc 48,3,64,56
mh 3
M 27
ad 64,576
v_mfma_f32_16x16x32_f16 a[108:111], v[56:59], a[24:27], a[32:35]
WV 19
H 0
v_pk_mul_f16 v24, v16, s92
M 37
WL 0
H 1
H 2
v_mfma_f32_16x16x32_f16 a[112:115], v[64:67], a[24:27], a[32:35]
s_barrier
R 24
M 39
R 26
v_mfma_f32_16x16x32_f16 a[132:135], v[56:59], a[0:3], a[32:35]
mw 0
v_pk_mul_f16 v25, v17, s92
v_pk_mul_f16 v26, v18, s92
M 49
ad 86,192
DR 0
v_mfma_f32_16x16x32_f16 a[136:139], v[64:67], a[0:3], a[32:35]
DR 1024
v_pk_mul_f16 v27, v19, s92
M 51
R 28
v_mfma_f32_16x16x32_f16 a[156:159], v[56:59], a[8:11], a[32:35]
DR 2048
v_pk_mul_f16 v28, v20, s92
M 61
mw 3
v_pk_mul_f16 v29, v21, s92
AN 104,106
v_mfma_f32_16x16x32_f16 a[160:163], v[64:67], a[8:11], a[32:35]
ad 86,198
DR 0
M 63
WL 4
R 30
v_mfma_f32_16x16x32_f16 a[180:183], v[56:59], a[16:19], a[32:35]
DR 1024
AN 105,107
M 73
DR 2048
SH 106
v_mfma_f32_16x16x32_f16 a[184:187], v[64:67], a[16:19], a[32:35]
bc 49,3,66,60
SH 107
M 75
WL 4
R 32
v_mfma_f32_16x16x32_f16 v[136:139], v[56:59], a[24:27], v[128:131]
mh 0
AN 106,106
AN 107,107
M 85
ad 66,576
H 0
v_mfma_f32_16x16x32_f16 v[140:143], v[64:67], a[24:27], v[128:131]
DG 1024
M 87
WL 4
R 34
v_mfma_f32_16x16x32_f16 v[160:163], v[56:59], a[0:3], v[128:131]
AN 108,110
AN 109,111
M 97
SH 110
SH 111
v_mfma_f32_16x16x32_f16 v[164:167], v[64:67], a[0:3], v[128:131]
AN 110,110
AN 111,111
PA_ 30,104,32
M 99
WL 4
PA_ 31,105,33
PB_ 32,104,32
PB_ 33,105,33
v_mfma_f32_16x16x32_f16 v[184:187], v[56:59], a[8:11], v[128:131]
WV 19
PA_ 34,108,36
PA_ 35,109,37
PB_ 36,108,36
M 109
WL 0
PB_ 37,109,37
PF 30,0
PF 31,1
v_mfma_f32_16x16x32_f16 v[188:191], v[64:67], a[8:11], v[128:131]
s_barrier
R 48
PF 32,2
M 111
R 50
PF 33,3
v_mfma_f32_16x16x32_f16 v[208:211], v[56:59], a[16:19], v[128:131]
mw 24
PF 34,4
PF 35,5
PF 36,6
M 121
ad 86,240
DR 0
PF 37,7
v_mfma_f32_16x16x32_f16 v[232:235], v[56:59], a[24:27], v[128:131]
DR 1024
AW 200,30
AW 201,31
M 133
DR 2048
AW 202,32
AW 203,33
v_mfma_f32_16x16x32_f16 v[212:215], v[64:67], a[16:19], v[128:131]
mw 27
AW 204,34
AW 205,35
AW 206,36
M 123
R 52
AW 207,37
PA_ 38,106,40
PA_ 39,107,41
PB_ 40,106,40
v_mfma_f32_16x16x32_f16 v[236:239], v[64:67], a[24:27], v[128:131]
ad 86,246
DR 0
PB_ 41,107,41
PA_ 42,110,44
PA_ 43,111,45
PB_ 44,110,44
M 135
WL 2
R 54
PB_ 45,111,45
PF 38,0
)"
R"(PF 39,1
PF 40,2
NA 36,0,200
DR 1024
PF 41,3
PF 42,4
PF 43,5
PF 44,6
PF 45,7
NA 36,4,204
DR 2048
AW 208,38
AW 209,39
AW 210,40
AW 211,41
AW 212,42
NA 60,8,200
AW 213,43
AW 214,44
AW 215,45
NA 60,12,204
TY 114,6664
TY 118,20488
TX 32,6144
WL 2
AN 112,114
AN 113,115
SH 114
SH 115
AN 114,114
AN 115,115
WL 1
AN 116,118
AN 117,119
SH 118
SH 119
AN 118,118
AN 119,119
NA 40,0,208
TX 36,19968
TY 40,6656
TY 44,20480
WL 2
PA_ 30,112,32
PA_ 31,113,33
PB_ 32,112,32
PB_ 33,113,33
PA_ 34,116,36
PA_ 35,117,37
PB_ 36,116,36
NA 40,4,212
PB_ 37,117,37
PF 30,0
PF 31,1
PF 32,2
PF 33,3
PF 34,4
PF 35,5
PF 36,6
PF 37,7
AW 216,30
NA 64,8,208
AW 217,31
AW 218,32
AW 219,33
AW 220,34
AW 221,35
AW 222,36
AW 223,37
NA 64,12,212
WL 0
PA_ 38,114,40
PA_ 39,115,41
PB_ 40,114,40
PB_ 41,115,41
PA_ 42,118,44
PA_ 43,119,45
v_mfma_f32_16x16x32_f16 a[44:47], a[216:219], a[0:3], a[32:35]
PB_ 44,118,44
PB_ 45,119,45
PF 38,0
PF 39,1
PF 40,2
PF 41,3
NA 44,4,220
PF 42,4
PF 43,5
PF 44,6
PF 45,7
AW 224,38
AW 225,39
v_mfma_f32_16x16x32_f16 a[68:71], a[216:219], a[8:11], a[32:35]
AW 226,40
AW 227,41
AW 228,42
AW 229,43
AW 230,44
AW 231,45
NA 68,12,220
RS 14,256
RS 18,320
TY 106,9736
v_mfma_f32_16x16x32_f16 a[48:51], a[224:227], a[0:3], a[32:35]
TY 110,23560
TX 58,9216
TX 62,23040
NA 48,4,228
R 56
TY 66,9728
v_mfma_f32_16x16x32_f16 a[72:75], a[224:227], a[8:11], a[32:35]
TY 70,23552
NA 72,12,228
R 58
NA 84,16,200
NA 84,20,204
WL 10
v_pk_mul_f16 v22, v14, s92
v_pk_mul_f16 v23, v15, s92
NA 88,16,208
v_pk_mul_f16 v24, v16, s92
v_pk_mul_f16 v25, v17, s92
NA 88,20,212
v_pk_mul_f16 v26, v18, s92
v_pk_mul_f16 v27, v19, s92
v_mfma_f32_16x16x32_f16 a[92:95], a[216:219], a[16:19], a[32:35]
v_pk_mul_f16 v28, v20, s92
v_pk_mul_f16 v29, v21, s92
NA 92,20,220
WL 9
AN 104,106
AN 105,107
v_mfma_f32_16x16x32_f16 a[96:99], a[224:227], a[16:19], a[32:35]
SH 106
SH 107
NA 96,20,228
AN 106,106
AN 107,107
NA 108,24,200
WL 8
AN 108,110
AN 109,111
NA 108,28,204
SH 110
SH 111
NA 112,24,208
AN 110,110
AN 111,111
NA 112,28,212
WL 3
Q 0
v_mfma_f32_16x16x32_f16 a[116:119], a[216:219], a[24:27], a[32:35]
WV 17
Q 2
NA 116,28,220
WL 0
Q 4
v_mfma_f32_16x16x32_f16 a[120:123], a[224:227], a[24:27], a[32:35]
s_barrier
R 72
TY 114,11272
NA 120,28,228
R 74
TY 118,25096
NA 132,0,200
mw 48
v_mov_b32 v54, 1
s_mov_b64 exec, 1
FS
s_mov_b64 exec, -1
NA 132,4,204
ad 86,288
DR 0
TX 74,10752
NA 136,0,208
Q 6
TX 78,24576
NA 136,4,212
DR 1024
PF 56,0
TY 82,11264
v_mfma_f32_16x16x32_f16 a[140:143], a[216:219], a[0:3], a[32:35]
PF 57,1
PF 58,2
TY 86,25088
NA 140,4,220
DR 2048
PF 59,3
TY 122,12808
v_mfma_f32_16x16x32_f16 a[144:147], a[224:227], a[0:3], a[32:35]
PF 60,4
PF 61,5
TY 126,26632
NA 144,4,228
WL 6
R 76
TX 90,12288
NA 156,8,200
mw 51
PF 62,6
PF 63,7
TX 94,26112
NA 156,12,204
ad 86,294
DR 0
TY 98,12800
NA 160,8,208
Q 8
TY 102,26624
NA 160,12,212
DR 1024
P 10
v_mfma_f32_16x16x32_f16 a[164:167], a[216:219], a[8:11], a[32:35]
Q 11
NA 164,12,220
DR 2048
P 13
v_mfma_f32_16x16x32_f16 a[168:171], a[224:227], a[8:11], a[32:35]
Q 14
NA 168,12,228
R 78
NA 180,16,200
bc 49,4,66,60
PF 64,0
NA 180,20,204
mh 9
PF 65,1
PF 66,2
NA 184,16,208
ad 66,768
H 0
NA 184,20,212
PF 67,3
PF 68,4
v_mfma_f32_16x16x32_f16 a[188:191], a[216:219], a[16:19], a[32:35]
DG 1024
NA 188,20,220
PF 69,5
PF 70,6
v_mfma_f32_16x16x32_f16 a[192:195], a[224:227], a[16:19], a[32:35]
bc 48,4,64,56
PF 71,7
NA 192,20,228
WL 2
R 80
NV 136,24,200
mh 12
AN 112,114
AN 113,115
NV 136,28,204
ad 64,768
H 0
NV 140,24,208
SH 114
SH 115
NV 140,28,212
H 1
AN 114,114
v_mfma_f32_16x16x32_f16 v[144:147], a[216:219], a[24:27], v[128:131]
AN 115,115
AN 116,118
NV 144,28,220
H 2
AN 117,119
v_mfma_f32_16x16x32_f16 v[148:151], a[224:227], a[24:27], v[128:131]
SH 118
SH 119
NV 148,28,228
R 82
NV 160,0,200
AN 118,118
AN 119,119
NV 160,4,204
Q 16
NV 164,0,208
Q 18
NV 164,4,212
Q 20
v_mfma_f32_16x16x32_f16 v[168:171], a[216:219], a[0:3], v[128:131]
Q 22
NV 168,4,220
PF 72,0
PF 73,1
v_mfma_f32_16x16x32_f16 v[172:175], a[224:227], a[0:3], v[128:131]
PF 74,2
PF 75,3
NV 172,4,228
WL 2
PF 76,4
PF 77,5
NV 184,8,200
PF 78,6
PF 79,7
NV 184,12,204
Q 24
NV 188,8,208
Q 26
NV 188,12,212
Q 28
v_mfma_f32_16x16x32_f16 v[192:195], a[216:219], a[8:11], v[128:131]
WV 20
Q 30
NV 192,12,220
WL 0
PF 80,0
PF 81,1
v_mfma_f32_16x16x32_f16 v[196:199], a[224:227], a[8:11], v[128:131]
s_barrier
R 0
NV 196,12,228
R 2
NV 208,16,200
mw 72
PF 82,2
PF 83,3
NV 208,20,204
ad 86,336
DR 0
NV 232,24,200
PF 84,4
PF 85,5
NV 232,28,204
DR 1024
PF 86,6
NV 212,16,208
PF 87,7
AN 120,122
NV 212,20,212
DR 2048
AN 121,123
NV 236,24,208
SH 122
SH 123
NV 236,28,212
mw 75
AN 122,122
AN 123,123
v_mfma_f32_16x16x32_f16 v[216:219], a[216:219], a[16:19], v[128:131]
ad 86,342
DR 0
NV 216,20,220
AN 124,126
AN 125,127
v_mfma_f32_16x16x32_f16 v[240:243], a[216:219], a[24:27], v[128:131]
DR 1024
SH 126
NV 240,28,220
SH 127
AN 126,126
v_mfma_f32_16x16x32_f16 v[220:223], a[224:227], a[16:19], v[128:131]
DR 2048
AN 127,127
NV 220,20,228
R 4
v_mfma_f32_16x16x32_f16 v[244:247], a[224:227], a[24:27], v[128:131]
Q 32
NV 244,28,228
WL 2
R 6
M 0
Q 34
M 1
Q 36
M 12
Q 38
M 13
PF 88,0
PF 89,1
M 2
PF 90,2
PF 91,3
M 3
PF 92,4
PF 93,5
M 14
PF 94,6
PF 95,7
M 15
Q 40
M 4
Q 42
M 5
Q 44
M 16
Q 46
M 17
PF 96,0
PF 97,1
M 6
PF 98,2
PF 99,3
M 7
PF 100,4
PF 101,5
M 18
PF 102,6
PF 103,7
M 19
RS 14,384
RS 18,448
v_mfma_f32_16x16x32_f16 a[52:55], v[88:91], a[0:3], a[32:35]
)"
R"(v_lshrrev_b32 v9, 8, v0
v_bfe_u32 v8, v0, 4, 4
M 9
v_lshlrev_b32 v10, 2, v9
v_mfma_f32_16x16x32_f16 a[76:79], v[88:91], a[8:11], a[32:35]
s_mov_b32 s42, 0xcb527610
M 21
s_movk_i32 s43, 0xc00
v_lshlrev_b32 v11, 3, v8
v_bfe_u32 v3, s42, v10, 4
v_mfma_f32_16x16x32_f16 a[56:59], v[96:99], a[0:3], a[32:35]
v_add_u32 v10, 16, v10
v_mad_u32_u24 v3, v3, s43, v11
M 11
R 8
v_mfma_f32_16x16x32_f16 a[80:83], v[96:99], a[8:11], a[32:35]
WL 2
v_pk_mul_f16 v22, v14, s92
v_pk_mul_f16 v23, v15, s92
M 23
R 10
M 24
v_pk_mul_f16 v24, v16, s92
v_pk_mul_f16 v25, v17, s92
M 25
v_pk_mul_f16 v26, v18, s92
v_pk_mul_f16 v27, v19, s92
M 26
v_pk_mul_f16 v28, v20, s92
v_pk_mul_f16 v29, v21, s92
M 27
v_bfe_u32 v48, s42, v10, 4
v_add_u32 v10, 1, v9
M 28
v_mad_u32_u24 v48, v48, s43, v11
v_and_b32 v10, 2, v10
M 29
v_sub_u32 v10, 1, v10
v_mul_i32_i24 v49, s43, v10
M 30
M 31
v_mfma_f32_16x16x32_f16 a[100:103], v[88:91], a[16:19], a[32:35]
M 33
v_mfma_f32_16x16x32_f16 a[104:107], v[96:99], a[16:19], a[32:35]
M 35
M 36
M 37
M 38
M 39
M 40
M 41
M 42
M 43
WV 18
v_mfma_f32_16x16x32_f16 a[124:127], v[88:91], a[24:27], a[32:35]
WL 0
M 45
s_barrier
R 24
TY 106,520
v_mfma_f32_16x16x32_f16 a[128:131], v[96:99], a[24:27], a[32:35]
TY 110,14344
TX 32,0
TX 36,13824
M 47
R 26
TY 40,512
M 48
TY 44,14336
TY 114,2056
TY 118,15880
M 49
M 50
M 51
M 52
WL 9
AN 104,106
AN 105,107
M 53
bc 49,5,66,60
SH 106
M 54
mh 18
SH 107
AN 106,106
M 55
ad 66,960
H 0
v_mfma_f32_16x16x32_f16 a[148:151], v[88:91], a[0:3], a[32:35]
WL 8
AN 107,107
AN 108,110
M 57
AN 109,111
SH 110
v_mfma_f32_16x16x32_f16 a[152:155], v[96:99], a[0:3], a[32:35]
SH 111
AN 110,110
M 59
R 28
M 60
DG 1024
M 61
WL 5
AN 111,111
PA_ 30,104,32
M 62
PA_ 31,105,33
PB_ 32,104,32
M 63
PB_ 33,105,33
PA_ 34,108,36
M 64
PA_ 35,109,37
PB_ 36,108,36
M 65
PB_ 37,109,37
PF 30,0
M 66
v_lshlrev_b32 v55, 4, v0
PF 31,1
M 67
FL
PF 32,2
v_mfma_f32_16x16x32_f16 a[172:175], v[88:91], a[8:11], a[32:35]
PF 33,3
PF 34,4
M 69
PF 35,5
PF 36,6
v_mfma_f32_16x16x32_f16 a[176:179], v[96:99], a[8:11], a[32:35]
PF 37,7
AW 200,30
M 71
R 30
M 72
AW 201,31
AW 202,32
M 73
AW 203,33
AW 204,34
M 74
AW 205,35
AW 206,36
M 75
WL 2
AW 207,37
PA_ 38,106,40
M 76
PA_ 39,107,41
PB_ 40,106,40
TX 32,1536
M 77
PB_ 41,107,41
PA_ 42,110,44
TX 36,15360
M 78
PA_ 43,111,45
PB_ 44,110,44
M 79
PB_ 45,111,45
PF 38,0
v_mfma_f32_16x16x32_f16 a[196:199], v[88:91], a[16:19], a[32:35]
PF 39,1
PF 40,2
M 81
PF 41,3
PF 42,4
v_mfma_f32_16x16x32_f16 v[132:135], v[96:99], a[16:19], v[128:131]
PF 43,5
PF 44,6
M 83
R 32
M 84
PF 45,7
AW 208,38
M 85
bc 48,5,64,56
AW 209,39
M 86
mh 21
AW 210,40
AW 211,41
M 87
ad 64,960
H 0
M 88
AW 212,42
AW 213,43
M 89
AW 214,44
AW 215,45
M 90
TY 40,2048
TY 44,15872
TY 122,3592
M 91
H 1
AN 112,114
TY 126,17416
v_mfma_f32_16x16x32_f16 v[152:155], v[88:91], a[24:27], v[128:131]
AN 113,115
SH 114
M 93
SH 115
AN 114,114
v_mfma_f32_16x16x32_f16 v[156:159], v[96:99], a[24:27], v[128:131]
AN 115,115
AN 116,118
M 95
R 34
M 96
H 2
AN 117,119
M 97
SH 118
SH 119
M 98
AN 118,118
AN 119,119
M 99
WL 6
PA_ 30,112,32
PA_ 31,113,33
M 100
PB_ 32,112,32
PB_ 33,113,33
M 101
PA_ 34,116,36
PA_ 35,117,37
M 102
PB_ 36,116,36
PB_ 37,117,37
M 103
PF 30,0
PF 31,1
v_mfma_f32_16x16x32_f16 v[176:179], v[88:91], a[0:3], v[128:131]
PF 32,2
PF 33,3
M 105
PF 34,4
PF 35,5
v_mfma_f32_16x16x32_f16 v[180:183], v[96:99], a[0:3], v[128:131]
PF 36,6
PF 37,7
M 107
AW 216,30
AW 217,31
M 108
AW 218,32
AW 219,33
M 109
AW 220,34
AW 221,35
M 110
WV 3
v_cmp_eq_u32 vcc, 0, v54
AW 222,36
M 111
WL 0
AW 223,37
PA_ 38,114,40
M 112
PA_ 39,115,41
PB_ 40,114,40
TX 32,3072
M 113
FB
PB_ 41,115,41
PA_ 42,118,44
TX 36,16896
M 114
PA_ 43,119,45
PB_ 44,118,44
M 115
PB_ 45,119,45
PF 38,0
v_mfma_f32_16x16x32_f16 v[200:203], v[88:91], a[8:11], v[128:131]
PF 39,1
PF 40,2
M 117
s_barrier
R 48
v_mfma_f32_16x16x32_f16 v[204:207], v[96:99], a[8:11], v[128:131]
mw 0
PF 41,3
PF 42,4
M 119
R 50
M 120
ad 84,384
W 0
M 121
W 1
PF 43,5
M 132
W 2
PF 44,6
M 133
mw 3
PF 45,7
AW 224,38
M 122
ad 84,390
W 0
M 123
W 1
AW 225,39
M 134
W 2
AW 226,40
M 135
mw 24
AW 227,41
AW 228,42
M 124
ad 84,432
W 0
M 125
W 1
AW 229,43
M 136
W 2
AW 230,44
M 137
mw 27
AW 231,45
AN 120,122
M 126
ad 84,438
W 0
TY 40,3584
M 127
W 1
TY 44,17408
M 138
W 2
AN 121,123
M 139
SH 122
SH 123
v_mfma_f32_16x16x32_f16 v[224:227], v[88:91], a[16:19], v[128:131]
AN 122,122
AN 123,123
M 129
AN 124,126
AN 125,127
v_mfma_f32_16x16x32_f16 v[248:251], v[88:91], a[24:27], v[128:131]
SH 126
SH 127
M 141
AN 126,126
AN 127,127
v_mfma_f32_16x16x32_f16 v[228:231], v[96:99], a[16:19], v[128:131]
WL 2
PA_ 30,120,32
PA_ 31,121,33
M 131
R 52
v_mfma_f32_16x16x32_f16 v[252:255], v[96:99], a[24:27], v[128:131]
PB_ 32,120,32
PB_ 33,121,33
M 143
R 54
NA 36,0,200
PA_ 34,124,36
PA_ 35,125,37
NA 36,4,204
PB_ 36,124,36
PB_ 37,125,37
NA 60,8,200
PF 30,0
PF 31,1
NA 60,12,204
PF 32,2
PF 33,3
NA 40,0,208
PF 34,4
PF 35,5
NA 40,4,212
PF 36,6
PF 37,7
NA 64,8,208
AW 232,30
AW 233,31
NA 64,12,212
AW 234,32
AW 235,33
NA 44,0,216
AW 236,34
AW 237,35
NA 44,4,220
WL 4
AW 238,36
AW 239,37
PA_ 38,122,40
NA 68,8,216
PA_ 39,123,41
PB_ 40,122,40
PB_ 41,123,41
)"
R"(NA 68,12,220
PA_ 42,126,44
PA_ 43,127,45
PB_ 44,126,44
NA 48,0,224
PB_ 45,127,45
PF 38,0
PF 39,1
NA 48,4,228
PF 40,2
PF 41,3
PF 42,4
NA 72,8,224
PF 43,5
PF 44,6
PF 45,7
NA 72,12,228
AW 240,38
AW 241,39
AW 242,40
NA 52,0,232
AW 243,41
AW 244,42
AW 245,43
NA 52,4,236
AW 246,44
AW 247,45
NA 76,8,232
NA 76,12,236
NA 56,0,240
NA 56,4,244
R 56
NA 80,8,240
NA 80,12,244
WL 2
R 58
NA 84,16,200
NA 84,20,204
NA 88,16,208
NA 88,20,212
NA 92,16,216
NA 92,20,220
NA 96,16,224
NA 96,20,228
NA 100,16,232
NA 100,20,236
NA 104,16,240
NA 104,20,244
NA 108,24,200
NA 108,28,204
NA 112,24,208
NA 112,28,212
NA 116,24,216
NA 116,28,220
NA 120,24,224
NA 120,28,228
NA 124,24,232
WL 0
NA 124,28,236
s_barrier
R 72
TY 106,5128
NA 128,24,240
mw 48
TY 110,18952
TX 58,4608
TX 62,18432
NA 128,28,244
R 74
TY 66,5120
NA 132,0,200
ad 84,480
W 0
TY 70,18944
NA 132,4,204
v_lshrrev_b32 v8, 1, v0
s_mov_b64 exec, s[76:77]
global_load_dwordx2 v[6:7], v8, s[78:79] sc1
s_mov_b64 exec, -1
NA 136,0,208
TY 114,6664
TY 118,20488
TX 74,6144
NA 136,4,212
WL 7
TX 78,19968
TY 82,6656
TY 86,20480
NA 140,0,216
W 1
AN 104,106
TY 122,8200
NA 140,4,220
AN 105,107
SH 106
TY 126,22024
NA 144,0,224
SH 107
AN 106,106
TX 90,7680
NA 144,4,228
AN 107,107
AN 108,110
TX 94,21504
NA 148,0,232
W 2
AN 109,111
TY 98,8192
NA 148,4,236
WL 9
SH 110
SH 111
TY 102,22016
NA 152,0,240
AN 110,110
AN 111,111
NA 152,4,244
R 76
NA 156,8,200
mw 51
Q 0
NA 156,12,204
ad 84,486
W 0
NA 160,8,208
Q 2
NA 160,12,212
Q 4
NA 164,8,216
Q 6
NA 164,12,220
W 1
P 8
NA 168,8,224
Q 9
NA 168,12,228
Q 11
NA 172,8,232
Q 13
NA 172,12,236
W 2
P 15
NA 176,8,240
AN 112,114
AN 113,115
NA 176,12,244
R 78
NA 180,16,200
SH 114
SH 115
NA 180,20,204
bc 49,6,66,60
AN 114,114
NA 184,16,208
mh 0
AN 115,115
AN 116,118
NA 184,20,212
ad 66,1152
H 0
NA 188,16,216
AN 117,119
SH 118
NA 188,20,220
SH 119
AN 118,118
NA 192,16,224
WL 2
AN 119,119
P 16
NA 192,20,228
DG 1024
NA 196,16,232
Q 17
NA 196,20,236
Q 19
NV 132,16,240
Q 21
NV 132,20,244
R 80
NV 136,24,200
Q 23
NV 136,28,204
bc 48,6,64,56
P 25
NV 140,24,208
mh 3
Q 26
NV 140,28,212
ad 64,1152
H 0
NV 144,24,216
Q 28
NV 144,28,220
Q 30
NV 148,24,224
AN 120,122
AN 121,123
NV 148,28,228
H 1
SH 122
NV 152,24,232
SH 123
AN 122,122
NV 152,28,236
AN 123,123
AN 124,126
NV 156,24,240
AN 125,127
SH 126
NV 156,28,244
R 82
NV 160,0,200
H 2
SH 127
NV 160,4,204
AN 126,126
AN 127,127
NV 164,0,208
Q 32
NV 164,4,212
Q 34
NV 168,0,216
Q 36
NV 168,4,220
Q 38
NV 172,0,224
Q 40
NV 172,4,228
Q 42
NV 176,0,232
Q 44
NV 176,4,236
Q 46
NV 180,0,240
NV 180,4,244
WL 2
NV 184,8,200
NV 184,12,204
NV 188,8,208
NV 188,12,212
NV 192,8,216
NV 192,12,220
NV 196,8,224
NV 196,12,228
WV 18
NV 200,8,232
WL 0
NV 200,12,236
s_barrier
R 0
NV 204,8,240
mw 72
NV 204,12,244
R 2
NV 208,16,200
ad 84,528
W 0
NV 208,20,204
NV 232,24,200
NV 232,28,204
NV 212,16,208
W 1
NV 212,20,212
NV 236,24,208
NV 236,28,212
NV 216,16,216
W 2
NV 216,20,220
ae 44,70,0
NV 240,24,216
s_bitcmp1_b32 s50, 0
s_cselect_b32 s51, 24576, 0
v_add_u32 v47, s51, v3
NV 240,28,220
XL 14,0
NV 220,16,224
mw 75
NV 220,20,228
ad 84,534
W 0
NV 244,24,224
XL 16,768
NV 244,28,228
NV 224,16,232
NV 224,20,236
W 1
NV 248,24,232
NV 248,28,236
NV 228,16,240
XL 18,1536
NV 228,20,244
R 4
NV 252,24,240
W 2
NV 252,28,244
WL 2
R 6
M 0
M 1
M 12
XL 20,2304
M 13
M 2
M 3
WV 20
v_or_b32 v6, v6, v7
s_mov_b64 exec, s[76:77]
v_cmp_ne_u32 %[flg], 0, v6
s_mov_b64 exec, -1
M 14
M 15
M 4
M 5
M 16
M 17
M 6
M 7
M 18
M 19
M 8
M 9
M 20
M 21
M 10
M 11
R 8
M 22
M 23
WL 4
R 10
M 24
M 25
M 26
M 27
M 28
M 29
M 30
M 31
M 32
M 33
M 34
M 35
WL 4
M 36
M 37
M 38
M 39
M 40
M 41
M 42
M 43
M 44
WL 0
M 45
s_barrier
R 24
M 46
mw 0
M 47
R 26
M 48
ad 84,576
W 0
M 49
TY 106,9736
TY 110,23560
M 50
M 51
M 52
W 1
M 53
M 54
M 55
M 56
W 2
M 57
M 58
WL 1
AN 104,106
AN 105,107
M 59
R 28
M 60
mw 3
SH 106
SH 107
M 61
ad 84,582
W 0
M 62
AN 106,106
AN 107,107
M 63
WL 2
AN 108,110
AN 109,111
M 64
SH 110
SH 111
)"
R"(M 65
W 1
AN 110,110
M 66
TY 114,11272
AN 111,111
M 67
TY 118,25096
M 68
M 69
W 2
M 70
M 71
R 30
M 72
M 73
bc 49,7,66,60
M 74
mh 9
M 75
ad 66,1344
H 0
M 76
WL 3
AN 112,114
AN 113,115
M 77
SH 114
SH 115
M 78
AN 114,114
AN 115,115
M 79
DG 1024
M 80
WL 2
AN 116,118
AN 117,119
M 81
SH 118
SH 119
M 82
AN 118,118
AN 119,119
M 83
R 32
M 84
TY 122,12808
TY 126,26632
M 85
bc 48,7,64,56
M 86
mh 12
M 87
ad 64,1344
H 0
s_load_dwordx4 s[88:91], %[pp], 0xc0
s_load_dwordx2 s[86:87], %[pp], 0xd0
M 88
M 89
M 90
M 91
H 1
M 92
M 93
WL 1
AN 120,122
AN 121,123
M 94
SH 122
SH 123
M 95
R 34
M 96
H 2
AN 122,122
M 97
WL 2
AN 123,123
AN 124,126
M 98
AN 125,127
SH 126
M 99
SH 127
AN 126,126
M 100
AN 127,127
M 101
M 102
M 103
M 104
M 105
M 106
M 107
M 108
M 109
M 110
M 111
M 112
M 113
M 114
M 115
WV 26
M 116
s_waitcnt lgkmcnt(0)
M 117
s_barrier
R 48
M 118
mw 24
M 119
R 50
M 120
ad 84,624
W 0
M 121
M 132
M 133
TX 58,9216
TX 62,23040
M 122
W 1
M 123
M 134
M 135
TY 66,9728
TY 70,23552
M 124
W 2
M 125
M 136
ae 44,70,0
M 137
TX 74,10752
TX 78,24576
M 126
mw 27
WL 4
Q 0
M 127
s_bfe_u32 s51, s50, 0x10001
Q 2
M 138
Q 4
M 139
TY 82,11264
TY 86,25088
M 128
Q 6
M 129
WL 4
Q 8
M 140
Q 10
M 141
TX 90,12288
TX 94,26112
M 130
Q 12
M 131
R 52
M 142
Q 14
M 143
TY 98,12800
TY 102,26624
M 0
R 54
M 1
WL 10
Q 16
M 12
Q 18
M 13
Q 20
M 2
Q 22
M 3
WL 8
Q 24
M 14
Q 26
M 15
Q 28
M 4
Q 30
M 5
WL 6
Q 32
M 16
Q 34
M 17
Q 36
M 6
Q 38
M 7
WL 2
Q 40
M 18
Q 42
M 19
Q 44
M 8
Q 46
M 9
ad 84,630
M 20
W 0
v_mad_i32_i24 v47, v49, s51, v48
M 21
XL 22,0
M 10
XL 24,768
M 11
R 56
M 22
W 1
XL 26,1536
M 23
R 58
M 24
XL 28,2304
M 25
M 26
W 2
M 27
M 28
M 29
M 30
M 31
M 32
M 33
v_mov_b32 v54, v52
M 34
M 35
WL 4
M 36
v_permlane32_swap_b32 v52, v54
M 37
M 38
M 39
v_add_f32 v52, v52, v54
v_mov_b32 v54, v52
M 40
WV 27
UL 6,14
UH 7,14
M 41
UL 8,15
UH 9,15
M 42
v_permlane16_swap_b32 v52, v54
AR 10,36
M 43
WV 15
AR 11,60
AR 12,84
M 44
WL 0
AR 13,108
AD 10,6
M 45
s_barrier
R 72
M 46
v_add_f32 v52, v52, v54
AD 11,7
M 47
R 74
M 48
TY 106,520
TY 110,14344
M 49
v_permlane16_swap_b32 v52, v53
v_mov_b32 v54, s88
M 50
v_mov_b32 v53, s89
AD 12,8
M 51
AD 13,9
AW 36,10
M 52
AW 60,11
AW 84,12
M 53
v_permlane16_swap_b32 v54, v53
v_bfe_u32 v53, v0, 2, 6
M 54
AW 108,13
UL 6,16
M 55
UH 7,16
UL 8,17
M 56
WL 1
v_add_u32 v54, v53, v54
UH 9,17
M 57
mw 48
AN 104,106
AN 105,107
M 58
SH 106
SH 107
M 59
R 76
M 60
AN 106,106
AN 107,107
M 61
WL 2
AN 108,110
AN 109,111
M 62
SH 110
SH 111
M 63
AN 110,110
AN 111,111
M 64
TY 114,2056
TY 118,15880
M 65
ad 84,672
M 66
W 0
AR 10,37
M 67
AR 11,61
AR 12,85
M 68
AR 13,109
AD 10,6
M 69
W 1
AD 11,7
M 70
AD 12,8
AD 13,9
M 71
R 78
M 72
W 2
AW 37,10
M 73
WL 3
AN 112,114
AN 113,115
M 74
SH 114
SH 115
M 75
mw 51
AN 114,114
AN 115,115
M 76
WL 2
AN 116,118
AN 117,119
M 77
SH 118
SH 119
M 78
AN 118,118
AN 119,119
M 79
TY 122,3592
TY 126,17416
M 80
ad 84,678
W 0
M 81
AW 61,11
AW 85,12
M 82
AW 109,13
UL 6,18
M 83
R 80
M 84
W 1
UH 7,18
M 85
UL 8,19
UH 9,19
M 86
AR 10,38
AR 11,62
M 87
W 2
AR 12,86
M 88
WL 3
AN 120,122
AN 121,123
M 89
SH 122
SH 123
M 90
)"
R"(AN 122,122
AN 123,123
M 91
WL 2
AN 124,126
AN 125,127
M 92
SH 126
SH 127
M 93
AN 126,126
AN 127,127
M 94
bc 49,8,66,60
AR 13,110
M 95
R 82
M 96
mh 18
AD 10,6
AD 11,7
M 97
ad 66,1536
H 0
M 98
AD 12,8
AD 13,9
M 99
AW 38,10
AW 62,11
M 100
DG 1024
M 101
AW 86,12
AW 110,13
M 102
UL 6,20
UH 7,20
M 103
bc 48,8,64,56
UL 8,21
M 104
mh 21
UH 9,21
AR 10,39
M 105
ad 64,1536
H 0
M 106
AR 11,63
AR 12,87
M 107
R 0
M 108
H 1
AR 13,111
M 109
AD 10,6
AD 11,7
M 110
AD 12,8
AD 13,9
M 111
H 2
AW 39,10
M 112
AW 63,11
AW 87,12
M 113
AW 111,13
M 114
M 115
M 116
M 117
M 118
M 119
WL 2
R 2
M 120
s_bitcmp1_b32 s87, 0
s_cselect_b32 exec_lo, 0xffffff, 0
s_mov_b32 exec_hi, 0
global_atomic_add_f32 v54, v52, s[90:91]
s_mov_b64 exec, -1
M 121
M 132
M 133
TX 58,0
TX 62,13824
M 122
M 123
M 134
M 135
TY 66,512
TY 70,14336
M 124
M 125
M 136
M 137
TX 74,1536
TX 78,15360
M 126
WL 4
Q 0
M 127
Q 2
M 138
Q 4
M 139
TY 82,2048
TY 86,15872
M 128
Q 6
M 129
WL 4
Q 8
M 140
Q 10
M 141
TX 90,3072
TX 94,16896
M 130
Q 12
M 131
R 4
M 142
Q 14
M 143
TY 98,3584
TY 102,17408
M 0
R 6
M 1
WL 10
Q 16
M 12
Q 18
M 13
Q 20
M 2
Q 22
M 3
WL 8
Q 24
M 14
Q 26
M 15
Q 28
M 4
Q 30
M 5
WL 6
Q 32
M 16
Q 34
M 17
Q 36
M 6
Q 38
M 7
WL 2
Q 40
M 18
Q 42
M 19
Q 44
M 8
Q 46
M 9
ae 44,68,0
M 20
s_bitcmp1_b32 s50, 2
s_cselect_b32 s51, 24576, 0
v_add_u32 v47, s51, v3
M 21
XL 30,0
M 10
XL 32,768
M 11
R 8
M 22
XL 34,1536
M 23
R 10
M 24
XL 36,2304
M 25
M 26
M 27
M 28
M 29
M 30
M 31
M 32
M 33
M 34
M 35
WL 4
M 36
M 37
M 38
M 39
M 40
M 41
M 42
WV 20
UL 6,22
UH 7,22
M 43
WV 10
UL 8,23
UH 9,23
M 44
WL 0
AR 10,40
AR 11,64
M 45
s_barrier
R 24
M 46
mw 72
AR 12,88
AR 13,112
M 47
R 26
M 48
TY 106,5128
TY 110,18952
M 49
ad 84,720
W 0
M 50
s_bitcmp1_b32 s87, 1
s_cselect_b32 exec_lo, 0xffffff, 0
s_mov_b32 exec_hi, 0
global_atomic_add_f32 v54, v52, s[90:91]
s_mov_b64 exec, -1
M 51
AD 10,6
AD 11,7
M 52
W 1
AD 12,8
M 53
AD 13,9
AW 40,10
M 54
AW 64,11
AW 88,12
M 55
W 2
AW 112,13
M 56
UL 6,24
UH 7,24
M 57
WL 1
AN 104,106
AN 105,107
M 58
mw 75
SH 106
SH 107
M 59
R 28
M 60
AN 106,106
AN 107,107
M 61
WL 2
AN 108,110
AN 109,111
M 62
SH 110
SH 111
M 63
AN 110,110
AN 111,111
M 64
TY 114,6664
TY 118,20488
M 65
ad 84,726
M 66
W 0
UL 8,25
M 67
UH 9,25
AR 10,41
M 68
AR 11,65
AR 12,89
M 69
W 1
AR 13,113
M 70
AD 10,6
AD 11,7
M 71
R 30
M 72
W 2
AD 12,8
M 73
WL 3
AN 112,114
AN 113,115
M 74
SH 114
SH 115
M 75
mw 0
AN 114,114
AN 115,115
M 76
WL 2
AN 116,118
AN 117,119
M 77
SH 118
SH 119
M 78
AN 118,118
AN 119,119
M 79
TY 122,8200
TY 126,22024
M 80
ad 84,768
W 0
M 81
AD 13,9
AW 41,10
M 82
AW 65,11
AW 89,12
M 83
R 32
M 84
W 1
AW 113,13
M 85
UL 6,26
UH 7,26
M 86
UL 8,27
UH 9,27
M 87
W 2
AR 10,42
M 88
WL 3
AN 120,122
AN 121,123
M 89
SH 122
SH 123
M 90
mw 3
AN 122,122
AN 123,123
M 91
WL 2
AN 124,126
AN 125,127
M 92
SH 126
SH 127
M 93
AN 126,126
AN 127,127
M 94
ad 84,774
W 0
M 95
R 34
M 96
AR 11,66
AR 12,90
M 97
W 1
AR 13,114
M 98
AD 10,6
AD 11,7
M 99
AD 12,8
AD 13,9
M 100
W 2
AW 42,10
M 101
AW 66,11
AW 90,12
M 102
AW 114,13
UL 6,28
M 103
bc 49,9,66,60
UH 7,28
)"
R"(M 104
mh 0
UL 8,29
UH 9,29
M 105
ad 66,1728
H 0
M 106
AR 10,43
AR 11,67
M 107
R 48
M 108
DG 1024
M 109
AR 12,91
AR 13,115
M 110
AD 10,6
AD 11,7
M 111
bc 48,9,64,56
AD 12,8
M 112
mh 3
AD 13,9
AW 43,10
M 113
ad 64,1728
H 0
M 114
AW 67,11
AW 91,12
M 115
AW 115,13
M 116
H 1
M 117
M 118
M 119
WL 2
R 50
M 120
H 2
M 121
s_bitcmp1_b32 s87, 2
s_cselect_b32 exec_lo, 0xffffff, 0
s_mov_b32 exec_hi, 0
global_atomic_add_f32 v54, v52, s[90:91]
s_mov_b64 exec, -1
M 132
M 133
TX 58,4608
TX 62,18432
M 122
M 123
M 134
M 135
TY 66,5120
TY 70,18944
M 124
M 125
M 136
M 137
TX 74,6144
TX 78,19968
M 126
WL 4
Q 0
M 127
Q 2
M 138
Q 4
M 139
TY 82,6656
TY 86,20480
M 128
Q 6
M 129
WL 4
Q 8
M 140
Q 10
M 141
TX 90,7680
TX 94,21504
M 130
Q 12
M 131
R 52
M 142
Q 14
M 143
TY 98,8192
TY 102,22016
M 0
R 54
M 1
WL 10
Q 16
M 12
Q 18
M 13
Q 20
M 2
Q 22
M 3
WL 8
Q 24
M 14
Q 26
M 15
Q 28
M 4
Q 30
M 5
WL 6
Q 32
M 16
Q 34
M 17
Q 36
M 6
Q 38
M 7
WL 2
Q 40
M 18
Q 42
M 19
Q 44
M 8
Q 46
M 9
ae 44,68,0
M 20
s_bfe_u32 s51, s50, 0x10003
v_mad_i32_i24 v47, v49, s51, v48
XL 38,0
M 21
XL 40,768
M 10
XL 42,1536
M 11
R 56
M 22
XL 44,2304
M 23
R 58
M 24
M 25
M 26
M 27
M 28
M 29
M 30
M 31
M 32
M 33
M 34
M 35
WL 4
M 36
M 37
M 38
M 39
M 40
M 41
M 42
M 43
WL 0
M 44
WV 10
UL 6,30
UH 7,30
M 45
s_barrier
R 72
M 46
mw 24
UL 8,31
UH 9,31
M 47
R 74
M 48
TY 106,9736
TY 110,23560
M 49
ad 84,816
W 0
M 50
s_bitcmp1_b32 s87, 3
s_cselect_b32 exec_lo, 0xffffff, 0
s_mov_b32 exec_hi, 0
global_atomic_add_f32 v54, v52, s[90:91]
s_mov_b64 exec, -1
M 51
AR 10,44
AR 11,68
M 52
W 1
AR 12,92
M 53
AR 13,116
AD 10,6
M 54
AD 11,7
AD 12,8
M 55
W 2
AD 13,9
M 56
AW 44,10
AW 68,11
M 57
WL 1
AN 104,106
AN 105,107
M 58
mw 27
SH 106
SH 107
M 59
R 76
M 60
AN 106,106
AN 107,107
M 61
WL 2
AN 108,110
AN 109,111
M 62
SH 110
SH 111
M 63
AN 110,110
AN 111,111
M 64
TY 114,11272
TY 118,25096
M 65
ad 84,822
M 66
W 0
AW 92,12
M 67
AW 116,13
UL 6,32
M 68
UH 7,32
UL 8,33
M 69
W 1
UH 9,33
M 70
AR 10,45
AR 11,69
M 71
R 78
M 72
W 2
AR 12,93
M 73
WL 3
AN 112,114
AN 113,115
M 74
SH 114
SH 115
M 75
mw 48
AN 114,114
AN 115,115
M 76
WL 2
AN 116,118
AN 117,119
M 77
SH 118
SH 119
M 78
AN 118,118
AN 119,119
M 79
TY 122,12808
TY 126,26632
M 80
ad 84,864
W 0
M 81
AR 13,117
AD 10,6
M 82
AD 11,7
AD 12,8
M 83
R 80
M 84
W 1
AD 13,9
M 85
AW 45,10
AW 69,11
M 86
AW 93,12
AW 117,13
M 87
W 2
UL 6,34
M 88
WL 3
AN 120,122
AN 121,123
M 89
SH 122
SH 123
M 90
mw 51
AN 122,122
AN 123,123
M 91
WL 2
AN 124,126
AN 125,127
M 92
SH 126
SH 127
M 93
AN 126,126
AN 127,127
M 94
ad 84,870
W 0
M 95
R 82
M 96
UH 7,34
UL 8,35
M 97
W 1
UH 9,35
M 98
AR 10,46
AR 11,70
M 99
AR 12,94
AR 13,118
M 100
W 2
AD 10,6
M 101
AD 11,7
AD 12,8
M 102
AD 13,9
AW 46,10
M 103
bc 49,10,66,60
AW 70,11
M 104
mh 9
AW 94,12
AW 118,13
M 105
ad 66,1920
H 0
M 106
UL 6,36
UH 7,36
M 107
R 0
M 108
DG 1024
M 109
UL 8,37
UH 9,37
M 110
AR 10,47
AR 11,71
M 111
bc 48,10,64,56
AR 12,95
M 112
mh 12
AR 13,119
AD 10,6
M 113
ad 64,1920
H 0
M 114
AD 11,7
AD 12,8
M 115
AD 13,9
AW 47,10
M 116
H 1
AW 71,11
M 117
AW 95,12
)"
R"(AW 119,13
M 118
M 119
WL 2
R 2
M 120
H 2
M 121
s_bitcmp1_b32 s87, 4
s_cselect_b32 exec_lo, 0xffffff, 0
s_mov_b32 exec_hi, 0
global_atomic_add_f32 v54, v52, s[90:91]
s_mov_b64 exec, -1
M 132
M 133
TX 58,9216
TX 62,23040
M 122
M 123
M 134
M 135
TY 66,9728
TY 70,23552
M 124
M 125
M 136
M 137
TX 74,10752
TX 78,24576
M 126
WL 4
Q 0
M 127
Q 2
M 138
Q 4
M 139
TY 82,11264
TY 86,25088
M 128
Q 6
M 129
WL 4
Q 8
M 140
Q 10
M 141
TX 90,12288
TX 94,26112
M 130
Q 12
M 131
R 4
M 142
Q 14
M 143
TY 98,12800
TY 102,26624
M 0
R 6
M 1
WL 10
Q 16
M 12
Q 18
M 13
Q 20
M 2
Q 22
M 3
WL 8
Q 24
M 14
Q 26
M 15
Q 28
M 4
Q 30
M 5
WL 6
Q 32
M 16
Q 34
M 17
Q 36
M 6
Q 38
M 7
WL 2
Q 40
M 18
Q 42
M 19
Q 44
M 8
Q 46
M 9
ae 44,68,96
M 20
s_bitcmp1_b32 s50, 4
s_cselect_b32 s51, 24576, 0
v_add_u32 v47, s51, v3
M 21
XL 14,0
M 10
XL 16,768
M 11
R 8
M 22
XL 18,1536
M 23
R 10
M 24
XL 20,2304
M 25
M 26
M 27
M 28
M 29
M 30
M 31
M 32
M 33
M 34
M 35
WL 4
M 36
M 37
M 38
M 39
M 40
M 41
M 42
M 43
WV 10
M 44
WL 0
M 45
s_barrier
R 24
M 46
mw 72
UL 6,38
UH 7,38
M 47
R 26
M 48
TY 106,520
TY 110,14344
M 49
ad 84,912
W 0
M 50
s_bitcmp1_b32 s87, 5
s_cselect_b32 exec_lo, 0xffffff, 0
s_mov_b32 exec_hi, 0
global_atomic_add_f32 v54, v52, s[90:91]
s_mov_b64 exec, -1
M 51
UL 8,39
UH 9,39
M 52
W 1
AR 10,48
M 53
AR 11,72
AR 12,96
M 54
AR 13,120
AD 10,6
M 55
W 2
AD 11,7
M 56
AD 12,8
AD 13,9
M 57
WL 1
AN 104,106
AN 105,107
M 58
mw 75
SH 106
SH 107
M 59
R 28
M 60
AN 106,106
AN 107,107
M 61
WL 2
AN 108,110
AN 109,111
M 62
SH 110
SH 111
M 63
AN 110,110
AN 111,111
M 64
TY 114,2056
TY 118,15880
M 65
ad 84,918
M 66
W 0
AW 48,10
M 67
AW 72,11
AW 96,12
M 68
AW 120,13
UL 6,40
M 69
W 1
UH 7,40
M 70
UL 8,41
UH 9,41
M 71
R 30
M 72
W 2
AR 10,49
M 73
WL 3
AN 112,114
AN 113,115
M 74
SH 114
SH 115
M 75
mw 0
AN 114,114
AN 115,115
M 76
WL 2
AN 116,118
AN 117,119
M 77
SH 118
SH 119
M 78
AN 118,118
AN 119,119
M 79
TY 122,3592
TY 126,17416
M 80
ad 84,960
W 0
M 81
AR 11,73
AR 12,97
M 82
AR 13,121
AD 10,6
M 83
R 32
M 84
W 1
AD 11,7
M 85
AD 12,8
AD 13,9
M 86
AW 49,10
AW 73,11
M 87
W 2
AW 97,12
M 88
WL 3
AN 120,122
AN 121,123
M 89
SH 122
SH 123
M 90
mw 3
AN 122,122
AN 123,123
M 91
WL 2
AN 124,126
AN 125,127
M 92
SH 126
SH 127
M 93
AN 126,126
AN 127,127
M 94
ad 84,966
W 0
M 95
R 34
M 96
AW 121,13
UL 6,42
M 97
W 1
UH 7,42
M 98
UL 8,43
UH 9,43
M 99
AR 10,50
AR 11,74
M 100
W 2
AR 12,98
M 101
AR 13,122
AD 10,6
M 102
AD 11,7
AD 12,8
M 103
bc 49,11,66,60
AD 13,9
M 104
mh 18
AW 50,10
AW 74,11
M 105
ad 66,2112
H 0
M 106
AW 98,12
AW 122,13
M 107
R 48
M 108
DG 1024
M 109
UL 6,44
UH 7,44
M 110
UL 8,45
UH 9,45
M 111
bc 48,11,64,56
AR 10,51
M 112
mh 21
AR 11,75
AR 12,99
M 113
ad 64,2112
H 0
M 114
AR 13,123
AD 10,6
M 115
AD 11,7
AD 12,8
M 116
H 1
AD 13,9
M 117
AW 51,10
AW 75,11
M 118
AW 99,12
AW 123,13
M 119
WL 0
R 50
M 120
H 2
M 121
s_bitcmp1_b32 s87, 6
s_cselect_b32 exec_lo, 0xffffff, 0
s_mov_b32 exec_hi, 0
global_atomic_add_f32 v54, v52, s[90:91]
s_mov_b64 exec, -1
M 132
M 133
TX 58,0
TX 62,13824
M 122
M 123
M 134
M 135
TY 66,512
TY 70,14336
M 124
M 125
M 136
M 137
TX 74,1536
TX 78,15360
M 126
WL 4
Q 0
M 127
Q 2
M 138
Q 4
)"
R"(M 139
TY 82,2048
TY 86,15872
M 128
Q 6
M 129
WL 4
Q 8
M 140
Q 10
M 141
TX 90,3072
TX 94,16896
M 130
Q 12
M 131
R 52
M 142
Q 14
M 143
TY 98,3584
TY 102,17408
M 0
R 54
M 1
WL 10
Q 16
M 12
Q 18
M 13
Q 20
M 2
Q 22
M 3
WL 8
Q 24
M 14
Q 26
M 15
Q 28
M 4
Q 30
M 5
WL 6
Q 32
M 16
Q 34
M 17
Q 36
M 6
Q 38
M 7
WL 2
Q 40
M 18
Q 42
M 19
Q 44
M 8
Q 46
M 9
ae 44,68,96
M 20
s_bfe_u32 s51, s50, 0x10005
v_mad_i32_i24 v47, v49, s51, v48
XL 22,0
M 21
XL 24,768
M 10
XL 26,1536
M 11
R 56
M 22
XL 28,2304
M 23
R 58
M 24
M 25
M 26
M 27
M 28
M 29
M 30
M 31
M 32
M 33
M 34
M 35
WL 4
M 36
M 37
M 38
M 39
M 40
M 41
M 42
M 43
WV 10
M 44
WL 0
M 45
s_barrier
R 72
M 46
mw 24
M 47
R 74
M 48
TY 106,5128
TY 110,18952
M 49
ad 84,1008
W 0
M 50
s_bitcmp1_b32 s87, 7
s_cselect_b32 exec_lo, 0xffffff, 0
s_mov_b32 exec_hi, 0
global_atomic_add_f32 v54, v52, s[90:91]
s_mov_b64 exec, -1
M 51
UL 6,14
UH 7,14
M 52
W 1
UL 8,15
M 53
UH 9,15
AR 10,52
M 54
AR 11,76
AR 12,100
M 55
W 2
AR 13,124
M 56
AD 10,6
AD 11,7
M 57
WL 1
AN 104,106
AN 105,107
M 58
mw 27
SH 106
SH 107
M 59
R 76
M 60
AN 106,106
AN 107,107
M 61
WL 2
AN 108,110
AN 109,111
M 62
SH 110
SH 111
M 63
AN 110,110
AN 111,111
M 64
TY 114,6664
TY 118,20488
M 65
ad 84,1014
M 66
W 0
AD 12,8
M 67
AD 13,9
AW 52,10
M 68
AW 76,11
AW 100,12
M 69
W 1
AW 124,13
M 70
UL 6,16
UH 7,16
M 71
R 78
M 72
W 2
UL 8,17
M 73
WL 3
AN 112,114
AN 113,115
M 74
SH 114
SH 115
M 75
mw 48
AN 114,114
AN 115,115
M 76
WL 2
AN 116,118
AN 117,119
M 77
SH 118
SH 119
M 78
AN 118,118
AN 119,119
M 79
TY 122,8200
TY 126,22024
M 80
ad 84,1056
W 0
M 81
UH 9,17
AR 10,53
M 82
AR 11,77
AR 12,101
M 83
R 80
M 84
W 1
AR 13,125
M 85
AD 10,6
AD 11,7
M 86
AD 12,8
AD 13,9
M 87
W 2
AW 53,10
M 88
WL 3
AN 120,122
AN 121,123
M 89
SH 122
SH 123
M 90
mw 51
AN 122,122
AN 123,123
M 91
WL 2
AN 124,126
AN 125,127
M 92
SH 126
SH 127
M 93
AN 126,126
AN 127,127
M 94
ad 84,1062
W 0
M 95
R 82
M 96
AW 77,11
AW 101,12
M 97
W 1
AW 125,13
M 98
UL 6,18
UH 7,18
M 99
UL 8,19
UH 9,19
M 100
W 2
AR 10,54
M 101
AR 11,78
AR 12,102
M 102
AR 13,126
AD 10,6
M 103
bc 49,12,66,60
AD 11,7
M 104
mh 0
AD 12,8
AD 13,9
M 105
ad 66,2304
H 0
M 106
AW 54,10
AW 78,11
M 107
R 0
M 108
DG 1024
M 109
AW 102,12
AW 126,13
M 110
UL 6,20
UH 7,20
M 111
bc 48,12,64,56
UL 8,21
M 112
mh 3
UH 9,21
AR 10,55
M 113
ad 64,2304
H 0
M 114
AR 11,79
AR 12,103
M 115
AR 13,127
AD 10,6
M 116
H 1
AD 11,7
M 117
AD 12,8
AD 13,9
M 118
AW 55,10
AW 79,11
M 119
WL 0
R 2
M 120
H 2
AW 103,12
M 121
AW 127,13
M 132
M 133
TX 58,4608
TX 62,18432
M 122
M 123
M 134
M 135
TY 66,5120
TY 70,18944
M 124
M 125
M 136
M 137
TX 74,6144
TX 78,19968
M 126
WL 4
Q 0
M 127
Q 2
M 138
Q 4
M 139
TY 82,6656
TY 86,20480
M 128
Q 6
M 129
WL 4
Q 8
M 140
Q 10
M 141
TX 90,7680
TX 94,21504
M 130
Q 12
M 131
R 4
M 142
Q 14
M 143
TY 98,8192
TY 102,22016
M 0
R 6
M 1
WL 10
Q 16
M 12
Q 18
M 13
Q 20
M 2
Q 22
M 3
WL 8
Q 24
M 14
Q 26
M 15
Q 28
M 4
Q 30
)"
R"(M 5
WL 6
Q 32
M 16
Q 34
M 17
Q 36
M 6
Q 38
M 7
WL 2
Q 40
M 18
Q 42
M 19
Q 44
M 8
Q 46
M 9
M 20
M 21
M 10
M 11
R 8
M 22
M 23
R 10
M 24
M 25
M 26
M 27
M 28
M 29
M 30
M 31
M 32
ae 44,70,0
M 33
s_bitcmp1_b32 s50, 0
s_cselect_b32 s51, 24576, 0
v_add_u32 v47, s51, v3
M 34
XL 30,128
M 35
WL 4
M 36
M 37
M 38
XL 32,896
M 39
M 40
M 41
M 42
M 43
WV 7
M 44
WL 0
XL 34,1664
M 45
s_barrier
R 24
M 46
mw 72
M 47
R 26
M 48
TY 106,9736
TY 110,23560
M 49
ad 84,1104
W 0
M 50
XL 36,2432
UL 6,22
M 51
UH 7,22
UL 8,23
M 52
W 1
UH 9,23
M 53
AR 10,56
AR 11,80
M 54
AR 12,104
AR 13,128
M 55
W 2
AD 10,6
M 56
AD 11,7
AD 12,8
M 57
WL 1
AN 104,106
AN 105,107
M 58
mw 75
SH 106
SH 107
M 59
R 28
M 60
AN 106,106
AN 107,107
M 61
WL 2
AN 108,110
AN 109,111
M 62
SH 110
SH 111
M 63
AN 110,110
AN 111,111
M 64
TY 114,11272
TY 118,25096
M 65
ad 84,1110
M 66
W 0
AD 13,9
M 67
AW 56,10
AW 80,11
M 68
AW 104,12
AW 128,13
M 69
W 1
UL 6,24
M 70
UH 7,24
UL 8,25
M 71
R 30
M 72
W 2
UH 9,25
M 73
WL 3
AN 112,114
AN 113,115
M 74
SH 114
SH 115
M 75
mw 0
AN 114,114
AN 115,115
M 76
WL 2
AN 116,118
AN 117,119
M 77
SH 118
SH 119
M 78
AN 118,118
AN 119,119
M 79
TY 122,12808
TY 126,26632
M 80
ad 84,1152
W 0
M 81
AR 10,57
AR 11,81
M 82
AR 12,105
AR 13,129
M 83
R 32
M 84
W 1
AD 10,6
M 85
AD 11,7
AD 12,8
M 86
AD 13,9
AW 57,10
M 87
W 2
AW 81,11
M 88
WL 3
AN 120,122
AN 121,123
M 89
SH 122
SH 123
M 90
mw 3
AN 122,122
AN 123,123
M 91
WL 2
AN 124,126
AN 125,127
M 92
SH 126
SH 127
M 93
AN 126,126
AN 127,127
M 94
ad 84,1158
W 0
M 95
R 34
M 96
AW 105,12
AW 129,13
M 97
W 1
UL 6,26
M 98
UH 7,26
UL 8,27
M 99
UH 9,27
AR 10,58
M 100
W 2
AR 11,82
M 101
AR 12,106
AR 13,130
M 102
AD 10,6
AD 11,7
M 103
bc 49,13,66,60
AD 12,8
M 104
mh 9
AD 13,9
AW 58,10
M 105
ad 66,2496
H 0
M 106
AW 82,11
AW 106,12
M 107
R 48
M 108
DG 1024
M 109
AW 130,13
UL 6,28
M 110
UH 7,28
UL 8,29
M 111
bc 48,13,64,56
UH 9,29
M 112
mh 12
AR 10,59
AR 11,83
M 113
ad 64,2496
H 0
M 114
AR 12,107
AR 13,131
M 115
AD 10,6
AD 11,7
M 116
H 1
AD 12,8
M 117
AD 13,9
AW 59,10
M 118
AW 83,11
AW 107,12
M 119
WL 0
R 50
M 120
H 2
AW 131,13
M 121
M 132
M 133
TX 58,9216
TX 62,23040
M 122
M 123
M 134
M 135
TY 66,9728
TY 70,23552
M 124
M 125
M 136
M 137
TX 74,10752
TX 78,24576
M 126
WL 4
Q 0
M 127
Q 2
M 138
Q 4
M 139
TY 82,11264
TY 86,25088
M 128
Q 6
M 129
WL 4
Q 8
M 140
Q 10
M 141
TX 90,12288
TX 94,26112
M 130
Q 12
M 131
R 52
M 142
Q 14
M 143
TY 98,12800
TY 102,26624
M 0
R 54
M 1
WL 10
Q 16
M 12
Q 18
M 13
Q 20
M 2
Q 22
M 3
WL 8
Q 24
M 14
Q 26
M 15
Q 28
M 4
Q 30
M 5
WL 6
Q 32
M 16
Q 34
M 17
Q 36
M 6
Q 38
M 7
WL 2
Q 40
M 18
Q 42
M 19
Q 44
M 8
Q 46
M 9
M 20
M 21
M 10
M 11
R 56
M 22
M 23
R 58
M 24
M 25
M 26
M 27
M 28
M 29
M 30
M 31
M 32
M 33
M 34
ae 44,70,0
M 35
WL 4
M 36
s_bfe_u32 s51, s50, 0x10001
v_mad_i32_i24 v47, v49, s51, v48
XL 38,128
M 37
)"
R"(M 38
M 39
M 40
XL 40,896
M 41
M 42
M 43
WV 7
M 44
WL 0
M 45
s_barrier
R 72
M 46
mw 24
v_mov_b32 v54, 2
s_mov_b64 exec, 1
FS
s_mov_b64 exec, -1
M 47
R 74
M 48
TY 106,520
TY 110,14344
M 49
ad 84,1200
W 0
M 50
XL 42,1664
M 51
M 52
W 1
XL 44,2432
M 53
M 54
M 55
W 2
M 56
M 57
WL 1
AN 104,106
AN 105,107
M 58
mw 27
SH 106
SH 107
M 59
R 76
M 60
AN 106,106
AN 107,107
M 61
WL 2
AN 108,110
AN 109,111
M 62
SH 110
SH 111
M 63
AN 110,110
AN 111,111
M 64
TY 114,2056
TY 118,15880
M 65
ad 84,1206
M 66
W 0
M 67
M 68
M 69
W 1
M 70
M 71
R 78
M 72
W 2
M 73
WL 3
AN 112,114
AN 113,115
M 74
SH 114
SH 115
M 75
mw 48
AN 114,114
AN 115,115
M 76
WL 2
AN 116,118
AN 117,119
M 77
SH 118
SH 119
M 78
AN 118,118
AN 119,119
M 79
TY 122,3592
TY 126,17416
M 80
ad 84,1248
W 0
M 81
M 82
M 83
R 80
M 84
W 1
M 85
M 86
M 87
W 2
M 88
WL 3
AN 120,122
AN 121,123
M 89
SH 122
SH 123
M 90
mw 51
AN 122,122
AN 123,123
M 91
WL 2
AN 124,126
AN 125,127
M 92
SH 126
SH 127
M 93
AN 126,126
AN 127,127
M 94
ad 84,1254
W 0
M 95
R 82
M 96
UL 6,30
UH 7,30
M 97
W 1
UL 8,31
M 98
UH 9,31
AR 10,132
M 99
AR 11,156
AR 12,180
M 100
W 2
AD 10,6
M 101
AD 11,7
AD 12,8
M 102
AD 136,9
AW 132,10
M 103
bc 49,14,66,60
AW 156,11
M 104
mh 18
AW 180,12
UL 6,32
M 105
ad 66,2688
H 0
M 106
UH 7,32
UL 8,33
M 107
R 0
M 108
DG 1024
M 109
UH 9,33
AR 10,133
M 110
AR 11,157
AR 12,181
M 111
bc 48,14,64,56
AD 10,6
M 112
mh 21
AD 11,7
AD 12,8
M 113
ad 64,2688
H 0
M 114
AD 137,9
AW 133,10
M 115
AW 157,11
AW 181,12
M 116
H 1
UL 6,34
M 117
UH 7,34
UL 8,35
M 118
UH 9,35
AR 10,134
M 119
WL 0
R 2
M 120
H 2
AR 11,158
M 121
AR 12,182
AD 10,6
M 132
AD 11,7
AD 12,8
M 133
TX 58,0
TX 62,13824
M 122
AD 138,9
AW 134,10
M 123
AW 158,11
AW 182,12
M 134
UL 6,36
UH 7,36
M 135
TY 66,512
TY 70,14336
M 124
UL 8,37
UH 9,37
M 125
AR 10,135
AR 11,159
M 136
AR 12,183
AD 10,6
M 137
TX 74,1536
TX 78,15360
M 126
WL 4
Q 0
M 127
Q 2
M 138
Q 4
M 139
TY 82,2048
TY 86,15872
M 128
Q 6
M 129
WL 4
Q 8
M 140
Q 10
M 141
TX 90,3072
TX 94,16896
M 130
Q 12
M 131
R 4
M 142
Q 14
M 143
TY 98,3584
TY 102,17408
M 0
R 6
M 1
WL 10
Q 16
M 12
Q 18
M 13
Q 20
M 2
Q 22
M 3
WL 8
Q 24
M 14
Q 26
M 15
Q 28
M 4
Q 30
M 5
WL 6
Q 32
M 16
Q 34
M 17
Q 36
M 6
Q 38
M 7
WL 2
Q 40
M 18
Q 42
M 19
Q 44
M 8
Q 46
M 9
AD 11,7
AD 12,8
M 20
AD 139,9
AW 135,10
M 21
AW 159,11
AW 183,12
M 10
M 11
R 8
M 22
M 23
R 10
M 24
M 25
M 26
M 27
M 28
M 29
M 30
M 31
M 32
M 33
M 34
M 35
WL 4
M 36
ae 44,68,0
M 37
s_bitcmp1_b32 s50, 2
s_cselect_b32 s51, 24576, 0
v_add_u32 v47, s51, v3
M 38
XL 14,128
M 39
M 40
M 41
M 42
XL 16,896
M 43
WV 7
M 44
WL 0
M 45
s_barrier
R 24
M 46
mw 72
M 47
R 26
M 48
TY 106,5128
TY 110,18952
M 49
ad 84,1296
W 0
M 50
XL 18,1664
M 51
M 52
W 1
M 53
M 54
XL 20,2432
M 55
W 2
M 56
M 57
WL 1
AN 104,106
AN 105,107
M 58
mw 75
SH 106
SH 107
M 59
R 28
M 60
AN 106,106
AN 107,107
M 61
WL 2
AN 108,110
)"
R"(AN 109,111
M 62
SH 110
SH 111
M 63
AN 110,110
AN 111,111
M 64
TY 114,6664
TY 118,20488
M 65
ad 84,1302
M 66
W 0
M 67
M 68
M 69
W 1
M 70
M 71
R 30
M 72
W 2
M 73
WL 3
AN 112,114
AN 113,115
M 74
SH 114
SH 115
M 75
mw 0
AN 114,114
AN 115,115
M 76
WL 2
AN 116,118
AN 117,119
M 77
SH 118
SH 119
M 78
AN 118,118
AN 119,119
M 79
TY 122,8200
TY 126,22024
M 80
ad 84,1344
W 0
M 81
M 82
M 83
R 32
M 84
W 1
M 85
M 86
M 87
W 2
M 88
WL 3
AN 120,122
AN 121,123
M 89
SH 122
SH 123
M 90
mw 3
AN 122,122
AN 123,123
M 91
WL 2
AN 124,126
AN 125,127
M 92
SH 126
SH 127
M 93
AN 126,126
AN 127,127
M 94
ad 84,1350
W 0
M 95
R 34
M 96
UL 6,38
UH 7,38
M 97
W 1
UL 8,39
M 98
UH 9,39
AR 10,136
M 99
AR 11,160
AR 12,184
M 100
W 2
AD 10,6
M 101
AD 11,7
AD 12,8
M 102
AD 140,9
AW 136,10
M 103
bc 49,15,66,60
AW 160,11
M 104
mh 0
AW 184,12
UL 6,40
M 105
ad 66,2880
H 0
M 106
UH 7,40
UL 8,41
M 107
R 48
M 108
DG 1024
M 109
UH 9,41
AR 10,137
M 110
AR 11,161
AR 12,185
M 111
bc 48,15,64,56
AD 10,6
M 112
mh 3
AD 11,7
AD 12,8
M 113
ad 64,2880
H 0
M 114
AD 141,9
AW 137,10
M 115
AW 161,11
AW 185,12
M 116
H 1
UL 6,42
M 117
UH 7,42
UL 8,43
M 118
UH 9,43
AR 10,138
M 119
WL 0
R 50
M 120
H 2
AR 11,162
M 121
AR 12,186
AD 10,6
M 132
AD 11,7
AD 12,8
M 133
TX 58,4608
TX 62,18432
M 122
AD 142,9
AW 138,10
M 123
AW 162,11
AW 186,12
M 134
UL 6,44
UH 7,44
M 135
TY 66,5120
TY 70,18944
M 124
UL 8,45
UH 9,45
M 125
AR 10,139
AR 11,163
M 136
AR 12,187
AD 10,6
M 137
TX 74,6144
TX 78,19968
M 126
WL 4
Q 0
M 127
Q 2
M 138
Q 4
M 139
TY 82,6656
TY 86,20480
M 128
Q 6
M 129
WL 4
Q 8
M 140
Q 10
M 141
TX 90,7680
TX 94,21504
M 130
Q 12
M 131
R 52
M 142
Q 14
M 143
TY 98,8192
TY 102,22016
M 0
R 54
M 1
WL 10
Q 16
M 12
Q 18
M 13
Q 20
M 2
Q 22
M 3
WL 8
Q 24
M 14
Q 26
M 15
Q 28
M 4
Q 30
M 5
WL 6
Q 32
M 16
Q 34
M 17
Q 36
M 6
Q 38
M 7
WL 2
Q 40
M 18
Q 42
M 19
Q 44
M 8
Q 46
M 9
AD 11,7
AD 12,8
M 20
AD 143,9
AW 139,10
M 21
AW 163,11
AW 187,12
M 10
M 11
R 56
M 22
M 23
R 58
M 24
M 25
M 26
M 27
M 28
M 29
M 30
M 31
M 32
M 33
M 34
M 35
WL 4
M 36
M 37
M 38
ae 44,68,0
M 39
s_bfe_u32 s51, s50, 0x10003
v_mad_i32_i24 v47, v49, s51, v48
XL 22,128
M 40
M 41
M 42
M 43
WV 6
M 44
WL 0
XL 24,896
M 45
s_barrier
R 72
M 46
mw 24
M 47
R 74
M 48
TY 106,9736
TY 110,23560
M 49
ad 84,1392
W 0
M 50
XL 26,1664
M 51
M 52
W 1
M 53
M 54
M 55
W 2
M 56
XL 28,2432
M 57
WL 1
AN 104,106
AN 105,107
M 58
mw 27
SH 106
SH 107
M 59
R 76
M 60
AN 106,106
AN 107,107
M 61
WL 2
AN 108,110
AN 109,111
M 62
SH 110
SH 111
M 63
AN 110,110
AN 111,111
M 64
TY 114,11272
TY 118,25096
M 65
ad 84,1398
M 66
W 0
M 67
M 68
M 69
W 1
M 70
M 71
R 78
M 72
W 2
M 73
WL 3
AN 112,114
AN 113,115
M 74
SH 114
SH 115
M 75
mw 48
AN 114,114
AN 115,115
M 76
WL 2
AN 116,118
AN 117,119
M 77
SH 118
SH 119
M 78
AN 118,118
AN 119,119
M 79
TY 122,12808
TY 126,26632
M 80
ad 84,1440
W 0
M 81
M 82
M 83
R 80
M 84
)"
R"(W 1
M 85
M 86
M 87
W 2
M 88
WL 3
AN 120,122
AN 121,123
M 89
SH 122
SH 123
M 90
mw 51
AN 122,122
AN 123,123
M 91
WL 2
AN 124,126
AN 125,127
M 92
SH 126
SH 127
M 93
AN 126,126
AN 127,127
M 94
ad 84,1446
W 0
M 95
R 82
M 96
UL 6,14
UH 7,14
M 97
W 1
UL 8,15
M 98
UH 9,15
AR 10,140
M 99
AR 11,164
AR 12,188
M 100
W 2
AD 10,6
M 101
AD 11,7
AD 12,8
M 102
AD 144,9
AW 140,10
M 103
bc 49,16,66,60
AW 164,11
M 104
mh 9
AW 188,12
UL 6,16
M 105
ad 66,3072
H 0
M 106
UH 7,16
UL 8,17
M 107
R 0
M 108
DG 1024
M 109
UH 9,17
AR 10,141
M 110
AR 11,165
AR 12,189
M 111
bc 48,16,64,56
AD 10,6
M 112
mh 12
AD 11,7
AD 12,8
M 113
ad 64,3072
H 0
M 114
AD 145,9
AW 141,10
M 115
AW 165,11
AW 189,12
M 116
H 1
UL 6,18
M 117
UH 7,18
UL 8,19
M 118
UH 9,19
AR 10,142
M 119
WL 0
R 2
M 120
H 2
AR 11,166
M 121
AR 12,190
AD 10,6
M 132
AD 11,7
AD 12,8
M 133
TX 58,9216
TX 62,23040
M 122
AD 146,9
AW 142,10
M 123
AW 166,11
AW 190,12
M 134
UL 6,20
UH 7,20
M 135
TY 66,9728
TY 70,23552
M 124
UL 8,21
UH 9,21
M 125
AR 10,143
AR 11,167
M 136
AR 12,191
AD 10,6
M 137
TX 74,10752
TX 78,24576
M 126
WL 4
Q 0
M 127
Q 2
M 138
Q 4
M 139
TY 82,11264
TY 86,25088
M 128
Q 6
M 129
WL 4
Q 8
M 140
Q 10
M 141
TX 90,12288
TX 94,26112
M 130
Q 12
M 131
R 4
M 142
Q 14
M 143
TY 98,12800
TY 102,26624
M 0
R 6
M 1
WL 10
Q 16
M 12
Q 18
M 13
Q 20
M 2
Q 22
M 3
WL 8
Q 24
M 14
Q 26
M 15
Q 28
M 4
Q 30
M 5
WL 6
Q 32
M 16
Q 34
M 17
Q 36
M 6
Q 38
M 7
WL 2
Q 40
M 18
Q 42
M 19
Q 44
M 8
Q 46
M 9
AD 11,7
AD 12,8
M 20
AD 147,9
AW 143,10
M 21
AW 167,11
AW 191,12
M 10
M 11
R 8
M 22
M 23
R 10
M 24
M 25
M 26
M 27
M 28
M 29
M 30
M 31
M 32
M 33
M 34
M 35
WL 4
M 36
M 37
M 38
M 39
M 40
ae 44,68,96
M 41
s_bitcmp1_b32 s50, 4
s_cselect_b32 s51, 24576, 0
v_add_u32 v47, s51, v3
M 42
XL 30,128
M 43
WV 6
M 44
WL 0
M 45
s_barrier
R 24
M 46
mw 72
XL 32,896
M 47
R 26
M 48
TY 106,520
TY 110,14344
M 49
ad 84,1488
W 0
M 50
M 51
M 52
W 1
XL 34,1664
M 53
M 54
M 55
W 2
M 56
M 57
WL 1
AN 104,106
AN 105,107
M 58
mw 75
SH 106
SH 107
M 59
R 28
M 60
AN 106,106
AN 107,107
M 61
WL 2
AN 108,110
AN 109,111
M 62
SH 110
SH 111
M 63
AN 110,110
AN 111,111
M 64
TY 114,2056
TY 118,15880
M 65
ad 84,1494
M 66
W 0
XL 36,2432
M 67
M 68
M 69
W 1
M 70
M 71
R 30
M 72
W 2
v_lshlrev_b32 v55, 4, v0
M 73
WL 3
AN 112,114
AN 113,115
M 74
SH 114
SH 115
M 75
mw 0
AN 114,114
AN 115,115
M 76
WL 2
AN 116,118
AN 117,119
M 77
SH 118
SH 119
M 78
AN 118,118
AN 119,119
M 79
TY 122,3592
TY 126,17416
M 80
ad 84,1536
W 0
M 81
FL
M 82
M 83
R 32
M 84
W 1
M 85
M 86
M 87
W 2
M 88
WL 3
AN 120,122
AN 121,123
M 89
SH 122
SH 123
M 90
mw 3
AN 122,122
AN 123,123
M 91
WL 2
AN 124,126
AN 125,127
M 92
SH 126
SH 127
M 93
AN 126,126
AN 127,127
M 94
ad 84,1542
W 0
M 95
R 34
M 96
UL 6,22
UH 7,22
M 97
W 1
UL 8,23
M 98
UH 9,23
AR 10,144
M 99
AR 11,168
AR 12,192
M 100
W 2
AD 10,6
M 101
AD 11,7
AD 12,8
M 102
AD 148,9
AW 144,10
)"
R"(M 103
bc 49,17,66,60
AW 168,11
M 104
mh 18
AW 192,12
UL 6,24
M 105
ad 66,3264
H 0
M 106
UH 7,24
UL 8,25
M 107
R 48
M 108
DG 1024
M 109
UH 9,25
AR 10,145
M 110
AR 11,169
AR 12,193
M 111
bc 48,17,64,56
AD 10,6
M 112
mh 21
AD 11,7
AD 12,8
M 113
ad 64,3264
H 0
M 114
AD 149,9
AW 145,10
M 115
AW 169,11
AW 193,12
M 116
H 1
UL 6,26
M 117
UH 7,26
UL 8,27
M 118
UH 9,27
AR 10,146
M 119
WL 0
R 50
M 120
H 2
AR 11,170
M 121
AR 12,194
AD 10,6
M 132
AD 11,7
AD 12,8
M 133
TX 58,0
TX 62,13824
M 122
AD 150,9
AW 146,10
M 123
AW 170,11
AW 194,12
M 134
UL 6,28
UH 7,28
M 135
TY 66,512
TY 70,14336
M 124
UL 8,29
UH 9,29
M 125
AR 10,147
AR 11,171
M 136
AR 12,195
AD 10,6
M 137
TX 74,1536
TX 78,15360
M 126
WL 4
Q 0
M 127
Q 2
M 138
Q 4
M 139
TY 82,2048
TY 86,15872
M 128
Q 6
M 129
WL 4
Q 8
M 140
Q 10
M 141
TX 90,3072
TX 94,16896
M 130
Q 12
M 131
R 52
M 142
Q 14
M 143
TY 98,3584
TY 102,17408
M 0
R 54
M 1
WL 10
Q 16
M 12
Q 18
M 13
Q 20
M 2
Q 22
M 3
WL 8
Q 24
M 14
Q 26
M 15
Q 28
M 4
Q 30
M 5
WL 6
Q 32
M 16
Q 34
M 17
Q 36
M 6
Q 38
M 7
WL 2
Q 40
M 18
Q 42
M 19
Q 44
M 8
Q 46
M 9
AD 11,7
AD 12,8
M 20
AD 151,9
AW 147,10
M 21
AW 171,11
AW 195,12
M 10
M 11
R 56
M 22
M 23
R 58
M 24
M 25
M 26
M 27
M 28
M 29
M 30
M 31
M 32
M 33
M 34
M 35
WL 4
M 36
M 37
M 38
M 39
M 40
M 41
M 42
ae 44,68,96
M 43
WV 5
M 44
WL 0
M 45
s_barrier
R 72
M 46
mw 24
M 47
R 74
M 48
s_bfe_u32 s51, s50, 0x10005
TY 106,5128
TY 110,18952
M 49
ad 84,1584
W 0
M 50
v_mad_i32_i24 v47, v49, s51, v48
XL 38,128
M 51
XL 40,896
M 52
W 1
M 53
M 54
XL 42,1664
M 55
W 2
M 56
M 57
WL 1
AN 104,106
AN 105,107
M 58
mw 27
SH 106
SH 107
M 59
R 76
M 60
AN 106,106
AN 107,107
M 61
WL 2
AN 108,110
AN 109,111
M 62
SH 110
SH 111
M 63
AN 110,110
AN 111,111
M 64
TY 114,6664
TY 118,20488
M 65
ad 84,1590
M 66
W 0
XL 44,2432
M 67
M 68
M 69
W 1
M 70
M 71
R 78
M 72
W 2
M 73
WL 3
AN 112,114
AN 113,115
M 74
SH 114
SH 115
M 75
mw 48
AN 114,114
AN 115,115
M 76
WL 2
AN 116,118
AN 117,119
M 77
SH 118
SH 119
M 78
AN 118,118
AN 119,119
M 79
TY 122,8200
TY 126,22024
M 80
ad 84,1632
W 0
M 81
M 82
M 83
R 80
M 84
W 1
M 85
M 86
M 87
W 2
M 88
WL 3
AN 120,122
AN 121,123
M 89
SH 122
SH 123
M 90
mw 51
AN 122,122
AN 123,123
M 91
WL 2
AN 124,126
AN 125,127
M 92
SH 126
SH 127
M 93
AN 126,126
AN 127,127
M 94
ad 84,1638
W 0
M 95
R 82
M 96
UL 6,30
UH 7,30
M 97
W 1
UL 8,31
M 98
UH 9,31
AR 10,148
M 99
AR 11,172
AR 12,196
M 100
W 2
AD 10,6
M 101
AD 11,7
AD 12,8
M 102
AD 152,9
AW 148,10
M 103
bc 49,18,66,60
AW 172,11
M 104
mh 0
AW 196,12
UL 6,32
M 105
ad 66,3456
H 0
M 106
UH 7,32
UL 8,33
M 107
R 0
M 108
DG 1024
M 109
UH 9,33
AR 10,149
M 110
AR 11,173
AR 12,197
M 111
bc 48,18,64,56
AD 10,6
M 112
mh 3
AD 11,7
AD 12,8
M 113
ad 64,3456
H 0
M 114
AD 153,9
AW 149,10
M 115
AW 173,11
AW 197,12
M 116
H 1
UL 6,34
M 117
UH 7,34
UL 8,35
M 118
UH 9,35
AR 10,150
M 119
WL 0
R 2
M 120
H 2
AR 11,174
M 121
)"
R"(AR 12,198
AD 10,6
M 132
AD 11,7
AD 12,8
M 133
TX 58,4608
TX 62,18432
M 122
AD 154,9
AW 150,10
M 123
AW 174,11
AW 198,12
M 134
UL 6,36
UH 7,36
M 135
TY 66,5120
TY 70,18944
M 124
UL 8,37
UH 9,37
M 125
AR 10,151
AR 11,175
M 136
AR 12,199
AD 10,6
M 137
TX 74,6144
TX 78,19968
M 126
WL 4
Q 0
M 127
Q 2
M 138
Q 4
M 139
TY 82,6656
TY 86,20480
M 128
Q 6
M 129
WL 4
Q 8
M 140
Q 10
M 141
TX 90,7680
TX 94,21504
M 130
Q 12
M 131
R 4
M 142
Q 14
M 143
TY 98,8192
TY 102,22016
M 0
R 6
M 1
WL 10
Q 16
M 12
Q 18
M 13
Q 20
M 2
Q 22
M 3
WL 8
Q 24
M 14
Q 26
M 15
Q 28
M 4
Q 30
M 5
WL 6
Q 32
M 16
Q 34
M 17
Q 36
M 6
Q 38
M 7
WL 2
Q 40
M 18
Q 42
M 19
Q 44
M 8
Q 46
M 9
AD 11,7
AD 12,8
M 20
AD 155,9
AW 151,10
M 21
AW 175,11
AW 199,12
M 10
M 11
R 8
M 22
M 23
R 10
M 24
M 25
M 26
M 27
M 28
M 29
M 30
M 31
M 32
M 33
M 34
M 35
WL 4
M 36
M 37
M 38
v_cmp_ne_u32 vcc, 2, v54
M 39
M 40
M 41
FC
M 42
M 43
WV 5
M 44
WL 0
M 45
s_barrier
R 24
M 46
mw 72
M 47
R 26
M 48
TY 106,9736
TY 110,23560
M 49
ad 84,1680
W 0
M 50
M 51
M 52
W 1
M 53
M 54
M 55
W 2
M 56
M 57
WL 1
AN 104,106
AN 105,107
M 58
mw 75
SH 106
SH 107
M 59
R 28
M 60
AN 106,106
AN 107,107
M 61
WL 2
AN 108,110
AN 109,111
M 62
SH 110
SH 111
M 63
AN 110,110
AN 111,111
M 64
TY 114,11272
TY 118,25096
M 65
ad 84,1686
M 66
W 0
M 67
M 68
M 69
W 1
M 70
ae 44,70,0
M 71
R 30
M 72
W 2
M 73
WL 3
AN 112,114
AN 113,115
M 74
SH 114
SH 115
M 75
mw 0
AN 114,114
AN 115,115
M 76
WL 2
AN 116,118
AN 117,119
M 77
SH 118
SH 119
M 78
AN 118,118
AN 119,119
M 79
TY 122,12808
TY 126,26632
M 80
ad 84,1728
W 0
M 81
s_bitcmp1_b32 s50, 0
s_cselect_b32 s51, 24576, 0
M 82
v_add_u32 v47, s51, v3
XL 14,256
M 83
R 32
M 84
W 1
XL 16,1024
M 85
XL 18,1792
M 86
M 87
W 2
M 88
WL 3
AN 120,122
AN 121,123
M 89
SH 122
SH 123
M 90
mw 3
AN 122,122
AN 123,123
M 91
WL 2
AN 124,126
AN 125,127
M 92
SH 126
SH 127
M 93
AN 126,126
AN 127,127
M 94
ad 84,1734
W 0
M 95
R 34
M 96
XL 20,2560
M 97
W 1
M 98
UL 6,38
UH 7,38
M 99
UL 8,39
UH 9,39
M 100
W 2
AR 10,152
M 101
AR 11,176
AD 10,6
M 102
AD 11,7
AD 132,8
M 103
bc 49,19,66,60
AD 156,9
M 104
mh 9
AW 152,10
AW 176,11
M 105
ad 66,3648
H 0
M 106
UL 6,40
UH 7,40
M 107
R 48
M 108
DG 1024
M 109
UL 8,41
UH 9,41
M 110
AR 10,153
AR 11,177
M 111
bc 48,19,64,56
AD 10,6
M 112
mh 12
AD 11,7
AD 133,8
M 113
ad 64,3648
H 0
M 114
AD 157,9
AW 153,10
M 115
AW 177,11
UL 6,42
M 116
H 1
UH 7,42
M 117
UL 8,43
UH 9,43
M 118
AR 10,154
AR 11,178
M 119
WL 0
R 50
M 120
H 2
AD 10,6
M 121
AD 11,7
AD 134,8
M 132
AD 158,9
AW 154,10
M 133
TX 58,9216
TX 62,23040
v_min_u32 v52, 752, v0
v_lshrrev_b32 v52, 1, v52
v_add_u32 v53, 0x1800, v52
global_load_dwordx2 a[200:201], v52, s[94:95] offset:-2048 sc1
global_load_dwordx2 a[202:203], v52, s[94:95] offset:-512 sc1
global_load_dwordx2 a[204:205], v52, s[94:95] offset:1024 sc1
global_load_dwordx2 a[206:207], v52, s[94:95] offset:2560 sc1
global_load_dwordx2 a[208:209], v53, s[94:95] offset:-2048 sc1
global_load_dwordx2 a[210:211], v53, s[94:95] offset:-512 sc1
global_load_dwordx2 a[212:213], v53, s[94:95] offset:1024 sc1
global_load_dwordx2 a[214:215], v53, s[94:95] offset:2560 sc1
M 122
AW 178,11
UL 6,44
M 123
UH 7,44
UL 8,45
M 134
UH 9,45
AR 10,155
M 135
TY 66,9728
TY 70,23552
M 124
AR 11,179
AD 10,6
M 125
AD 11,7
AD 135,8
M 136
AD 159,9
AW 155,10
M 137
TX 74,10752
TX 78,24576
M 126
WL 4
Q 0
M 127
Q 2
M 138
Q 4
M 139
TY 82,11264
TY 86,25088
M 128
Q 6
)"
R"(M 129
WL 4
Q 8
M 140
Q 10
M 141
TX 90,12288
TX 94,26112
M 130
Q 12
M 131
R 52
M 142
Q 14
M 143
TY 98,12800
TY 102,26624
M 0
R 54
M 1
WL 10
Q 16
M 12
Q 18
M 13
Q 20
M 2
Q 22
M 3
WL 8
Q 24
M 14
Q 26
M 15
Q 28
M 4
Q 30
M 5
WL 6
Q 32
M 16
Q 34
M 17
Q 36
M 6
Q 38
M 7
WL 2
Q 40
M 18
Q 42
M 19
Q 44
M 8
Q 46
M 9
AW 179,11
M 20
M 21
M 10
M 11
R 56
M 22
M 23
R 58
M 24
M 25
M 26
M 27
M 28
M 29
M 30
M 31
M 32
M 33
M 34
M 35
WL 4
M 36
M 37
M 38
M 39
M 40
M 41
M 42
M 43
WV 13
M 44
WL 0
M 45
s_barrier
R 72
M 46
mw 24
M 47
R 74
M 48
TY 106,520
TY 110,14344
M 49
ad 84,1776
W 0
M 50
M 51
M 52
W 1
M 53
M 54
M 55
W 2
M 56
M 57
WL 1
AN 104,106
AN 105,107
M 58
mw 27
SH 106
SH 107
M 59
R 76
M 60
AN 106,106
AN 107,107
M 61
WL 2
AN 108,110
AN 109,111
M 62
SH 110
SH 111
M 63
AN 110,110
AN 111,111
M 64
TY 114,2056
TY 118,15880
M 65
ad 84,1782
M 66
W 0
M 67
M 68
M 69
W 1
M 70
M 71
R 78
M 72
W 2
M 73
WL 3
AN 112,114
AN 113,115
M 74
SH 114
SH 115
M 75
mw 48
AN 114,114
AN 115,115
M 76
WL 2
AN 116,118
AN 117,119
M 77
SH 118
SH 119
M 78
AN 118,118
AN 119,119
M 79
TY 122,3592
TY 126,17416
M 80
ad 84,1824
W 0
M 81
ae 44,70,0
M 82
s_bfe_u32 s51, s50, 0x10001
v_mad_i32_i24 v47, v49, s51, v48
XL 22,256
M 83
R 80
M 84
W 1
XL 24,1024
M 85
M 86
XL 26,1792
M 87
W 2
M 88
WL 3
AN 120,122
AN 121,123
M 89
SH 122
SH 123
M 90
mw 51
AN 122,122
AN 123,123
M 91
WL 2
AN 124,126
AN 125,127
M 92
SH 126
SH 127
M 93
AN 126,126
AN 127,127
M 94
ad 84,1830
W 0
M 95
R 82
M 96
XL 28,2560
M 97
W 1
M 98
M 99
M 100
W 2
M 101
M 102
M 103
bc 49,20,66,60
M 104
mh 18
M 105
ad 66,3840
H 0
M 106
M 107
R 0
M 108
DG 1024
M 109
M 110
M 111
bc 48,20,64,56
M 112
mh 21
M 113
ad 64,3840
H 0
M 114
M 115
M 116
H 1
M 117
M 118
M 119
WL 2
R 2
M 120
H 2
M 121
M 132
M 133
TX 58,0
TX 62,13824
M 122
M 123
M 134
UL 6,14
UH 7,14
M 135
TY 66,512
TY 70,14336
M 124
UL 8,15
UH 9,15
M 125
AD 160,6
AD 184,7
M 136
AD 208,8
AD 232,9
M 137
TX 74,1536
TX 78,15360
M 126
WL 4
Q 0
M 127
Q 2
M 138
Q 4
M 139
TY 82,2048
TY 86,15872
M 128
Q 6
M 129
WL 4
Q 8
M 140
Q 10
M 141
TX 90,3072
TX 94,16896
M 130
Q 12
M 131
R 4
M 142
Q 14
M 143
TY 98,3584
TY 102,17408
M 0
R 6
M 1
WL 10
Q 16
M 12
Q 18
M 13
Q 20
M 2
Q 22
M 3
WL 8
Q 24
M 14
Q 26
M 15
Q 28
M 4
Q 30
M 5
WL 6
Q 32
M 16
Q 34
M 17
Q 36
M 6
Q 38
M 7
WL 2
Q 40
M 18
Q 42
M 19
Q 44
M 8
Q 46
M 9
UL 6,16
UH 7,16
M 20
UL 8,17
UH 9,17
M 21
AD 161,6
AD 185,7
M 10
AD 209,8
AD 233,9
M 11
R 8
M 22
UL 6,18
UH 7,18
M 23
R 10
M 24
UL 8,19
UH 9,19
M 25
AD 162,6
AD 186,7
M 26
AD 210,8
AD 234,9
M 27
UL 6,20
UH 7,20
M 28
UL 8,21
UH 9,21
M 29
AD 163,6
AD 187,7
M 30
AD 211,8
AD 235,9
M 31
M 32
M 33
M 34
M 35
WL 4
M 36
)"
R"(M 37
M 38
M 39
M 40
M 41
M 42
M 43
WV 5
M 44
WL 0
M 45
s_barrier
R 24
M 46
mw 72
M 47
R 26
M 48
TY 106,5128
TY 110,18952
M 49
ad 84,1872
W 0
M 50
M 51
M 52
W 1
M 53
M 54
M 55
W 2
M 56
M 57
WL 1
AN 104,106
AN 105,107
M 58
mw 75
SH 106
SH 107
M 59
R 28
M 60
AN 106,106
AN 107,107
M 61
WL 2
AN 108,110
AN 109,111
M 62
SH 110
SH 111
M 63
AN 110,110
AN 111,111
M 64
TY 114,6664
TY 118,20488
M 65
ad 84,1878
M 66
W 0
M 67
M 68
M 69
W 1
M 70
M 71
R 30
M 72
W 2
M 73
WL 3
AN 112,114
AN 113,115
M 74
SH 114
SH 115
M 75
mw 0
AN 114,114
AN 115,115
M 76
WL 2
AN 116,118
AN 117,119
M 77
SH 118
SH 119
M 78
AN 118,118
AN 119,119
M 79
TY 122,8200
TY 126,22024
M 80
ad 84,1920
W 0
M 81
ae 44,68,0
M 82
s_bitcmp1_b32 s50, 2
s_cselect_b32 s51, 24576, 0
v_add_u32 v47, s51, v3
M 83
R 32
M 84
W 1
XL 30,256
M 85
XL 32,1024
M 86
M 87
W 2
M 88
WL 3
AN 120,122
AN 121,123
M 89
SH 122
SH 123
M 90
mw 3
AN 122,122
AN 123,123
M 91
WL 2
AN 124,126
AN 125,127
M 92
SH 126
SH 127
M 93
AN 126,126
AN 127,127
M 94
ad 84,1926
W 0
M 95
R 34
M 96
XL 34,1792
M 97
W 1
XL 36,2560
M 98
M 99
M 100
W 2
M 101
M 102
M 103
bc 49,21,66,60
M 104
mh 0
M 105
ad 66,4032
H 0
M 106
M 107
R 48
M 108
DG 1024
M 109
M 110
M 111
bc 48,21,64,56
M 112
mh 3
M 113
ad 64,4032
H 0
M 114
M 115
M 116
H 1
M 117
M 118
M 119
WL 2
R 50
M 120
H 2
M 121
v_mov_b32 v54, s39
v_mul_f32 v54, 0xb5400000, v54
M 132
v_add_u32 v52, %[bm], v52
v_bfe_u32 v53, v52, 6, 2
M 133
TX 58,4608
TX 62,18432
M 122
v_and_b32 v55, 63, v52
v_add_u32 v53, v53, v55
M 123
v_and_b32 v52, 0x700, v52
v_lshl_add_u32 v52, v53, 2, v52
M 134
v_add_u32 v52, %[sb], v52
AR 10,214
M 135
TY 66,5120
TY 70,18944
M 124
AR 11,215
AR 8,208
M 125
AR 9,209
v_add_f32 v10, v10, v8
M 136
v_add_f32 v11, v11, v9
AR 8,210
M 137
TX 74,6144
TX 78,19968
M 126
WL 4
Q 0
M 127
Q 2
M 138
Q 4
M 139
TY 82,6656
TY 86,20480
M 128
Q 6
M 129
WL 4
Q 8
M 140
Q 10
M 141
TX 90,7680
TX 94,21504
M 130
Q 12
M 131
R 52
M 142
Q 14
M 143
TY 98,8192
TY 102,22016
M 0
R 54
M 1
WL 10
Q 16
M 12
Q 18
M 13
Q 20
M 2
Q 22
M 3
WL 8
Q 24
M 14
Q 26
M 15
Q 28
M 4
Q 30
M 5
WL 6
Q 32
M 16
Q 34
M 17
Q 36
M 6
Q 38
M 7
WL 2
Q 40
M 18
Q 42
M 19
Q 44
M 8
Q 46
M 9
AR 9,211
v_add_f32 v10, v10, v8
M 20
v_add_f32 v11, v11, v9
AR 8,212
M 21
AR 9,213
v_add_f32 v10, v10, v8
M 10
v_add_f32 v11, v11, v9
AR 6,206
M 11
R 56
M 22
AR 7,207
v_add_f32 v10, v10, v54
M 23
R 58
M 24
v_add_f32 v11, v11, v54
v_fma_f32 v8, v6, s86, v10
M 25
ds_write_b32 v52, v8 offset:1024
v_fma_f32 v9, v7, s86, v11
M 26
ds_write_b32 v52, v9 offset:1040
AR 12,200
M 27
AR 13,201
v_add_f32 v6, v6, v12
M 28
v_add_f32 v7, v7, v13
v_fma_f32 v12, v6, s86, v10
M 29
ds_write_b32 v52, v12 offset:2560
v_fma_f32 v13, v7, s86, v11
M 30
ds_write_b32 v52, v13 offset:2576
AR 8,202
M 31
AR 9,203
v_add_f32 v6, v6, v8
M 32
v_add_f32 v7, v7, v9
v_fma_f32 v8, v6, s86, v10
M 33
ds_write_b32 v52, v8 offset:4096
v_fma_f32 v9, v7, s86, v11
M 34
ds_write_b32 v52, v9 offset:4112
AR 12,204
M 35
WL 8
AR 13,205
v_add_f32 v6, v6, v12
M 36
v_add_f32 v7, v7, v13
v_fma_f32 v12, v6, s86, v10
M 37
ds_write_b32 v52, v12 offset:5632
v_fma_f32 v13, v7, s86, v11
M 38
ds_write_b32 v52, v13 offset:5648
M 39
UL 6,22
UH 7,22
M 40
UL 8,23
UH 9,23
M 41
AD 164,6
AD 188,7
M 42
AD 212,8
AD 236,9
M 43
WV 5
UL 6,24
UH 7,24
M 44
WL 4
UL 8,25
UH 9,25
M 45
s_barrier
R 72
M 46
mw 24
AD 165,6
AD 189,7
M 47
R 74
M 48
TY 106,9736
TY 110,23560
M 49
ad 84,1968
W 0
M 50
AD 213,8
AD 237,9
M 51
UL 6,26
UH 7,26
M 52
W 1
UL 8,27
M 53
UH 9,27
AD 166,6
M 54
AD 190,7
AD 214,8
M 55
W 2
AD 238,9
)"
R"(M 56
UL 6,28
UH 7,28
M 57
WL 1
AN 104,106
AN 105,107
M 58
mw 27
SH 106
SH 107
M 59
R 76
M 60
AN 106,106
AN 107,107
M 61
WL 2
AN 108,110
AN 109,111
M 62
SH 110
SH 111
M 63
AN 110,110
AN 111,111
M 64
TY 114,11272
TY 118,25096
M 65
ad 84,1974
M 66
W 0
UL 8,29
M 67
UH 9,29
AD 167,6
M 68
AD 191,7
AD 215,8
M 69
W 1
AD 239,9
M 70
M 71
R 78
M 72
W 2
M 73
WL 3
AN 112,114
AN 113,115
M 74
SH 114
SH 115
M 75
mw 48
AN 114,114
AN 115,115
M 76
WL 2
AN 116,118
AN 117,119
M 77
SH 118
SH 119
M 78
AN 118,118
AN 119,119
M 79
TY 122,12808
TY 126,26632
M 80
ad 84,2016
W 0
M 81
M 82
ae 44,68,0
M 83
R 80
M 84
s_bfe_u32 s51, s50, 0x10003
W 1
v_mad_i32_i24 v47, v49, s51, v48
M 85
XL 38,256
M 86
M 87
W 2
M 88
WL 3
AN 120,122
AN 121,123
M 89
SH 122
SH 123
M 90
mw 51
AN 122,122
AN 123,123
M 91
WL 2
AN 124,126
AN 125,127
M 92
SH 126
SH 127
M 93
AN 126,126
AN 127,127
M 94
ad 84,2022
W 0
M 95
R 82
M 96
XL 40,1024
M 97
W 1
XL 42,1792
M 98
M 99
M 100
W 2
XL 44,2560
M 101
M 102
M 103
bc 49,22,66,60
M 104
mh 9
M 105
ad 66,4224
H 0
M 106
M 107
R 0
M 108
DG 1024
M 109
M 110
M 111
bc 48,22,64,56
M 112
mh 12
M 113
ad 64,4224
H 0
M 114
M 115
M 116
H 1
M 117
M 118
M 119
WL 2
R 2
M 120
H 2
M 121
M 132
M 133
TX 58,9216
TX 62,23040
M 122
M 123
M 134
M 135
TY 66,9728
TY 70,23552
M 124
M 125
M 136
M 137
TX 74,10752
TX 78,24576
M 126
WL 4
Q 0
M 127
Q 2
M 138
Q 4
M 139
TY 82,11264
TY 86,25088
M 128
Q 6
M 129
WL 4
Q 8
M 140
Q 10
M 141
TX 90,12288
TX 94,26112
M 130
Q 12
M 131
R 4
M 142
Q 14
M 143
TY 98,12800
TY 102,26624
M 0
R 6
M 1
WL 10
Q 16
M 12
Q 18
M 13
Q 20
M 2
Q 22
M 3
WL 8
Q 24
M 14
Q 26
M 15
Q 28
M 4
Q 30
M 5
WL 6
Q 32
M 16
Q 34
M 17
Q 36
M 6
Q 38
M 7
WL 2
Q 40
M 18
Q 42
M 19
Q 44
M 8
Q 46
M 9
UL 6,30
UH 7,30
M 20
UL 8,31
UH 9,31
M 21
AD 168,6
AD 192,7
M 10
AD 216,8
AD 240,9
M 11
R 8
M 22
UL 6,32
UH 7,32
M 23
R 10
M 24
UL 8,33
UH 9,33
M 25
AD 169,6
AD 193,7
M 26
AD 217,8
AD 241,9
M 27
UL 6,34
UH 7,34
M 28
UL 8,35
UH 9,35
M 29
AD 170,6
AD 194,7
M 30
AD 218,8
AD 242,9
M 31
UL 6,36
UH 7,36
M 32
UL 8,37
UH 9,37
M 33
AD 171,6
AD 195,7
M 34
AD 219,8
AD 243,9
M 35
WL 2
M 36
M 37
M 38
M 39
M 40
M 41
M 42
M 43
WV 6
M 44
WL 0
M 45
s_barrier
R 24
M 46
mw 72
M 47
R 26
M 48
TY 106,520
TY 110,14344
M 49
ad 84,2064
W 0
M 50
M 51
M 52
W 1
M 53
M 54
M 55
W 2
M 56
M 57
WL 1
AN 104,106
AN 105,107
M 58
mw 75
SH 106
SH 107
M 59
R 28
M 60
AN 106,106
AN 107,107
M 61
WL 2
AN 108,110
AN 109,111
M 62
SH 110
SH 111
M 63
AN 110,110
AN 111,111
M 64
TY 114,2056
TY 118,15880
M 65
ad 84,2070
M 66
W 0
M 67
M 68
M 69
W 1
M 70
M 71
R 30
M 72
W 2
M 73
WL 3
AN 112,114
AN 113,115
M 74
SH 114
SH 115
M 75
mw 0
AN 114,114
AN 115,115
M 76
WL 2
AN 116,118
AN 117,119
M 77
SH 118
SH 119
M 78
AN 118,118
AN 119,119
M 79
TY 122,3592
TY 126,17416
M 80
ad 84,2112
W 0
M 81
M 82
M 83
R 32
)"
R"(M 84
W 1
M 85
M 86
ae 44,68,96
M 87
W 2
M 88
WL 3
AN 120,122
AN 121,123
M 89
SH 122
SH 123
M 90
mw 3
AN 122,122
AN 123,123
M 91
WL 2
AN 124,126
AN 125,127
M 92
SH 126
SH 127
M 93
AN 126,126
AN 127,127
M 94
ad 84,2118
W 0
M 95
R 34
M 96
s_bitcmp1_b32 s50, 4
s_cselect_b32 s51, 24576, 0
M 97
W 1
v_add_u32 v47, s51, v3
M 98
XL 14,256
M 99
XL 16,1024
M 100
W 2
XL 18,1792
M 101
M 102
M 103
bc 49,23,66,60
M 104
mh 18
XL 20,2560
M 105
ad 66,4416
H 0
M 106
M 107
R 48
M 108
DG 1024
M 109
M 110
M 111
bc 48,23,64,56
M 112
mh 21
M 113
ad 64,4416
H 0
M 114
M 115
M 116
H 1
M 117
M 118
M 119
WL 2
R 50
M 120
H 2
M 121
M 132
M 133
TX 58,0
TX 62,13824
M 122
M 123
M 134
M 135
TY 66,512
TY 70,14336
M 124
M 125
M 136
M 137
TX 74,1536
TX 78,15360
M 126
WL 4
Q 0
M 127
Q 2
M 138
Q 4
M 139
TY 82,2048
TY 86,15872
M 128
Q 6
M 129
WL 4
Q 8
M 140
Q 10
M 141
TX 90,3072
TX 94,16896
M 130
Q 12
M 131
R 52
M 142
Q 14
M 143
TY 98,3584
TY 102,17408
M 0
R 54
M 1
WL 10
Q 16
M 12
Q 18
M 13
Q 20
M 2
Q 22
M 3
WL 8
Q 24
M 14
Q 26
M 15
Q 28
M 4
Q 30
M 5
WL 6
Q 32
M 16
Q 34
M 17
Q 36
M 6
Q 38
M 7
WL 2
Q 40
M 18
Q 42
M 19
Q 44
M 8
Q 46
M 9
UL 6,38
UH 7,38
M 20
UL 8,39
UH 9,39
M 21
AD 172,6
AD 196,7
M 10
AD 220,8
AD 244,9
M 11
R 56
M 22
UL 6,40
UH 7,40
M 23
R 58
M 24
UL 8,41
UH 9,41
M 25
AD 173,6
AD 197,7
M 26
AD 221,8
AD 245,9
M 27
UL 6,42
UH 7,42
M 28
UL 8,43
UH 9,43
M 29
AD 174,6
AD 198,7
M 30
AD 222,8
AD 246,9
M 31
WV 26
UL 6,44
UH 7,44
M 32
UL 8,45
UH 9,45
M 33
AD 175,6
AD 199,7
M 34
AD 223,8
AD 247,9
M 35
WL 2
M 36
M 37
M 38
M 39
M 40
M 41
M 42
M 43
WV 7
M 44
WL 0
M 45
s_barrier
R 72
M 46
mw 24
M 47
R 74
M 48
TY 106,5128
TY 110,18952
M 49
ad 84,2160
W 0
M 50
M 51
M 52
W 1
M 53
M 54
M 55
W 2
M 56
M 57
WL 1
AN 104,106
AN 105,107
M 58
mw 27
SH 106
SH 107
M 59
R 76
M 60
AN 106,106
AN 107,107
M 61
WL 2
AN 108,110
AN 109,111
M 62
SH 110
SH 111
M 63
AN 110,110
AN 111,111
M 64
TY 114,6664
TY 118,20488
M 65
ad 84,2166
M 66
W 0
M 67
M 68
M 69
W 1
M 70
M 71
R 78
M 72
W 2
M 73
WL 3
AN 112,114
AN 113,115
M 74
SH 114
SH 115
M 75
mw 48
AN 114,114
AN 115,115
M 76
WL 2
AN 116,118
AN 117,119
M 77
SH 118
SH 119
M 78
AN 118,118
AN 119,119
M 79
TY 122,8200
TY 126,22024
M 80
ad 84,2208
W 0
M 81
M 82
M 83
R 80
M 84
W 1
M 85
M 86
M 87
W 2
M 88
WL 3
AN 120,122
AN 121,123
M 89
SH 122
SH 123
M 90
mw 51
AN 122,122
AN 123,123
M 91
WL 2
AN 124,126
AN 125,127
M 92
SH 126
SH 127
M 93
AN 126,126
AN 127,127
M 94
ad 84,2214
W 0
M 95
R 82
M 96
ae 44,68,96
M 97
s_bfe_u32 s51, s50, 0x10005
W 1
v_mad_i32_i24 v47, v49, s51, v48
M 98
XL 22,256
M 99
XL 24,1024
M 100
W 2
M 101
M 102
XL 26,1792
M 103
bc 49,0,66,60
M 104
mh 0
M 105
ad 66,32
H 0
M 106
M 107
R 0
M 108
DG 1024
M 109
XL 28,2560
M 110
M 111
bc 48,0,64,56
M 112
mh 3
M 113
ad 64,0
H 0
M 114
M 115
M 116
H 1
M 117
M 118
M 119
WL 2
)"
R"(R 2
M 120
H 2
M 121
M 132
M 133
PA 12288
TX 58,4608
TX 62,18432
DF
M 122
M 123
M 134
M 135
TY 66,5120
TY 70,18944
M 124
M 125
M 136
M 137
TX 74,6144
TX 78,19968
M 126
WL 4
Q 0
M 127
Q 2
M 138
Q 4
M 139
TY 82,6656
TY 86,20480
M 128
Q 6
M 129
WL 4
Q 8
M 140
Q 10
M 141
TX 90,7680
TX 94,21504
M 130
Q 12
M 131
R 4
M 142
Q 14
M 143
TY 98,8192
TY 102,22016
M 0
R 6
M 1
WL 10
Q 16
M 12
Q 18
M 13
Q 20
M 2
Q 22
M 3
WL 8
Q 24
M 14
Q 26
M 15
Q 28
M 4
Q 30
M 5
WL 6
Q 32
M 16
Q 34
M 17
Q 36
M 6
Q 38
M 7
WL 2
Q 40
M 18
Q 42
M 19
Q 44
M 8
Q 46
M 9
UL 6,14
UH 7,14
M 20
UL 8,15
UH 9,15
M 21
AD 176,6
AD 200,7
M 10
AD 224,8
AD 248,9
M 11
R 8
M 22
UL 6,16
UH 7,16
M 23
R 10
M 24
UL 8,17
UH 9,17
M 25
AD 177,6
AD 201,7
M 26
AD 225,8
AD 249,9
M 27
WV 27
UL 6,18
UH 7,18
M 28
UL 8,19
UH 9,19
M 29
AD 178,6
AD 202,7
M 30
AD 226,8
AD 250,9
M 31
UL 6,20
UH 7,20
M 32
UL 8,21
UH 9,21
M 33
AD 179,6
AD 203,7
M 34
AD 227,8
AD 251,9
M 35
WL 2
M 36
M 37
M 38
M 39
M 40
M 41
M 42
M 43
WV 8
M 44
WL 0
M 45
s_barrier
R 24
M 46
mw 72
M 47
R 26
M 48
TY 106,9736
TY 110,23560
M 49
ad 84,2256
W 0
M 50
M 51
M 52
W 1
M 53
M 54
M 55
W 2
M 56
M 57
WL 1
AN 104,106
AN 105,107
M 58
mw 75
SH 106
SH 107
M 59
R 28
M 60
AN 106,106
AN 107,107
M 61
WL 2
AN 108,110
AN 109,111
M 62
SH 110
SH 111
M 63
AN 110,110
AN 111,111
M 64
TY 114,11272
TY 118,25096
M 65
ad 84,2262
M 66
W 0
M 67
M 68
M 69
W 1
M 70
M 71
R 30
M 72
W 2
M 73
WL 3
AN 112,114
AN 113,115
M 74
SH 114
SH 115
M 75
mw 0
AN 114,114
AN 115,115
M 76
WL 2
AN 116,118
AN 117,119
M 77
SH 118
SH 119
M 78
AN 118,118
AN 119,119
M 79
TY 122,12808
TY 126,26632
M 80
ad 84,0
W 0
M 81
M 82
M 83
R 32
M 84
W 1
M 85
M 86
M 87
W 2
M 88
WL 3
AN 120,122
AN 121,123
M 89
SH 122
SH 123
M 90
mw 3
AN 122,122
AN 123,123
M 91
WL 2
AN 124,126
AN 125,127
M 92
SH 126
SH 127
M 93
AN 126,126
AN 127,127
M 94
ad 84,6
W 0
M 95
R 34
M 96
M 97
W 1
M 98
M 99
M 100
W 2
M 101
M 102
M 103
bc 49,1,66,60
M 104
mh 9
M 105
ad 66,224
H 0
M 106
M 107
R 48
M 108
DG 1024
M 109
M 110
M 111
bc 48,1,64,56
M 112
mh 12
M 113
ad 64,224
H 0
M 114
M 115
M 116
DG 1024
M 117
M 118
M 119
WL 2
R 50
M 120
mh 15
M 121
ad 64,195
H 0
M 132
M 133
TX 58,9216
TX 62,23040
M 122
DG 1024
M 123
M 134
M 135
TY 66,9728
TY 70,23552
M 124
M 125
M 136
M 137
TX 74,10752
TX 78,24576
M 126
WL 4
Q 0
M 127
Q 2
M 138
Q 4
M 139
TY 82,11264
TY 86,25088
M 128
Q 6
M 129
WL 4
Q 8
M 140
Q 10
M 141
TX 90,12288
TX 94,26112
M 130
Q 12
M 131
R 52
M 142
Q 14
M 143
TY 98,12800
TY 102,26624
M 0
R 54
M 1
WL 10
Q 16
M 12
Q 18
M 13
Q 20
M 2
Q 22
M 3
WL 8
Q 24
M 14
Q 26
M 15
Q 28
M 4
Q 30
M 5
WL 6
Q 32
M 16
Q 34
M 17
Q 36
M 6
Q 38
M 7
WL 2
)"
R"(Q 40
M 18
Q 42
M 19
Q 44
M 8
Q 46
M 9
UL 6,22
UH 7,22
M 20
UL 8,23
UH 9,23
M 21
AD 180,6
AD 204,7
M 10
AD 228,8
AD 252,9
M 11
R 56
M 22
UL 6,24
UH 7,24
M 23
R 58
M 24
UL 8,25
UH 9,25
M 25
AD 181,6
AD 205,7
M 26
AD 229,8
AD 253,9
BV 52,0
M 27
WV 25
UL 6,26
UH 7,26
M 28
UL 8,27
UH 9,27
M 29
AD 182,6
AD 206,7
M 30
AD 230,8
AD 254,9
M 31
WV 22
UL 6,28
UH 7,28
M 32
UL 8,29
UH 9,29
M 33
AD 183,6
AD 207,7
M 34
AD 231,8
AD 255,9
M 35
WL 3
M 36
M 37
M 38
M 39
M 40
ds_read_b64 v[12:13], v4 offset:256
ds_read_b64 v[46:47], v4 offset:264
M 41
AR 6,36
AR 7,60
M 42
AR 8,84
AR 9,108
M 43
WL 2
EF 6,52
EF 7,53
M 44
WV 6
EF 8,54
EF 9,55
M 45
s_barrier
R 72
M 46
mw 24
CV 30,6,7
CV 31,8,9
M 47
R 74
M 48
TY 106,520
TY 110,14344
M 49
ad 84,48
W 0
M 50
AR 6,37
AR 7,61
M 51
AR 8,85
AR 9,109
M 52
W 1
EF 6,52
M 53
EF 7,53
EF 8,54
M 54
EF 9,55
CV 32,6,7
M 55
W 2
CV 33,8,9
M 56
AR 6,38
AR 7,62
M 57
WL 1
AN 104,106
AN 105,107
M 58
mw 27
SH 106
SH 107
M 59
R 76
M 60
AN 106,106
AN 107,107
M 61
WL 2
AN 108,110
AN 109,111
M 62
SH 110
SH 111
M 63
AN 110,110
AN 111,111
M 64
TY 114,2056
TY 118,15880
M 65
ad 84,54
M 66
W 0
AR 8,86
M 67
AR 9,110
EF 6,52
M 68
EF 7,53
EF 8,54
M 69
W 1
EF 9,55
M 70
CV 34,6,7
CV 35,8,9
M 71
R 78
M 72
W 2
AR 6,39
M 73
WL 3
AN 112,114
AN 113,115
M 74
SH 114
SH 115
M 75
mw 48
AN 114,114
AN 115,115
M 76
WL 2
AN 116,118
AN 117,119
M 77
SH 118
SH 119
M 78
AN 118,118
AN 119,119
M 79
TY 122,3592
TY 126,17416
M 80
ad 84,96
W 0
M 81
AR 7,63
AR 8,87
M 82
AR 9,111
EF 6,52
M 83
R 80
M 84
W 1
EF 7,53
M 85
EF 8,54
EF 9,55
M 86
CV 36,6,7
CV 37,8,9
M 87
W 2
ae 42,74,0
M 88
WL 3
AN 120,122
AN 121,123
M 89
SH 122
SH 123
M 90
mw 51
AN 122,122
AN 123,123
M 91
WL 2
AN 124,126
AN 125,127
M 92
SH 126
SH 127
M 93
AN 126,126
AN 127,127
M 94
ad 84,102
W 0
M 95
R 82
M 96
s_bitcmp1_b32 s50, 0
s_cselect_b32 s52, 24576, 0
v_add_u32 v50, s52, v3
M 97
W 1
ST 30,0
M 98
AR 6,40
AR 7,64
M 99
AR 8,88
AR 9,112
M 100
W 2
EF 6,52
M 101
EF 7,53
EF 8,54
M 102
EF 9,55
CV 38,6,7
M 103
bc 49,2,66,60
CV 39,8,9
M 104
mh 18
AR 6,41
AR 7,65
M 105
ad 66,416
H 0
M 106
AR 8,89
AR 9,113
M 107
R 0
M 108
DG 1024
M 109
ST 32,768
EF 6,52
M 110
EF 7,53
EF 8,54
M 111
bc 48,2,64,56
EF 9,55
M 112
mh 21
CV 40,6,7
CV 41,8,9
M 113
ad 64,416
H 0
M 114
AR 6,42
AR 7,66
M 115
AR 8,90
AR 9,114
M 116
H 1
EF 6,52
M 117
EF 7,53
EF 8,54
M 118
EF 9,55
CV 42,6,7
M 119
WL 0
R 2
M 120
H 2
CV 43,8,9
M 121
ST 34,1536
AR 6,43
M 132
AR 7,67
AR 8,91
M 133
TX 58,0
TX 62,13824
M 122
AR 9,115
EF 6,52
M 123
EF 7,53
EF 8,54
M 134
EF 9,55
CV 44,6,7
M 135
TY 66,512
TY 70,14336
M 124
CV 45,8,9
M 125
BV 52,512
AR 6,132
M 136
AR 7,156
AR 8,180
M 137
TX 74,1536
TX 78,15360
M 126
WL 5
Q 0
M 127
Q 2
ST 36,2304
M 138
Q 4
M 139
TY 82,2048
TY 86,15872
M 128
Q 6
M 129
WL 5
Q 8
M 140
Q 10
M 141
TX 90,3072
TX 94,16896
M 130
Q 12
M 131
R 4
M 142
Q 14
M 143
TY 98,3584
TY 102,17408
v_mfma_f32_16x16x32_f16 a[36:39], v[56:59], a[0:3], a[32:35]
R 6
M 1
WL 10
Q 16
v_mfma_f32_16x16x32_f16 a[60:63], v[56:59], a[8:11], a[32:35]
Q 18
M 13
Q 20
v_mfma_f32_16x16x32_f16 a[40:43], v[64:67], a[0:3], a[32:35]
Q 22
M 3
WL 8
Q 24
v_mfma_f32_16x16x32_f16 a[64:67], v[64:67], a[8:11], a[32:35]
Q 26
M 15
Q 28
M 4
Q 30
)"
R"(M 5
WL 6
Q 32
M 16
Q 34
M 17
Q 36
M 6
Q 38
M 7
WL 2
Q 40
M 18
Q 42
M 19
Q 44
M 8
Q 46
M 9
ae 42,74,0
M 20
s_bfe_u32 s52, s50, 0x10001
v_mad_i32_i24 v50, v49, s52, v48
ST 38,0
M 21
EF 6,12
EF 7,13
M 10
EF 8,46
EF 136,47
M 11
R 8
M 22
CV 14,6,7
CV 15,8,136
M 23
R 10
v_mfma_f32_16x16x32_f16 a[84:87], v[56:59], a[16:19], a[32:35]
AR 6,133
AR 7,157
M 25
AR 8,181
EF 6,12
v_mfma_f32_16x16x32_f16 a[88:91], v[64:67], a[16:19], a[32:35]
EF 7,13
EF 8,46
M 27
EF 137,47
CV 16,6,7
M 28
CV 17,8,137
AR 6,134
M 29
AR 7,158
AR 8,182
M 30
ST 40,768
EF 6,12
M 31
EF 7,13
EF 8,46
M 32
EF 138,47
CV 18,6,7
M 33
CV 19,8,138
AR 6,135
M 34
AR 7,159
AR 8,183
M 35
WL 2
EF 6,12
EF 7,13
v_mfma_f32_16x16x32_f16 a[108:111], v[56:59], a[24:27], a[32:35]
EF 8,46
EF 139,47
M 37
CV 20,6,7
CV 21,8,139
v_mfma_f32_16x16x32_f16 a[112:115], v[64:67], a[24:27], a[32:35]
AR 6,136
AR 7,160
AR 8,184
EF 6,12
M 39
EF 7,13
EF 8,46
EF 140,47
CV 22,6,7
M 40
CV 23,8,140
AR 6,137
AR 7,161
AR 8,185
M 41
EF 6,12
EF 7,13
EF 8,46
EF 141,47
M 42
ST 42,1536
CV 24,6,7
CV 25,8,141
AR 6,138
M 43
WV 11
AR 7,162
AR 8,186
EF 6,12
EF 7,13
M 44
WL 0
EF 8,46
EF 142,47
CV 26,6,7
CV 27,8,142
M 45
s_barrier
R 24
AR 6,139
AR 7,163
M 46
mw 72
AR 8,187
EF 6,12
EF 7,13
EF 8,46
M 47
R 26
EF 143,47
CV 28,6,7
v_mfma_f32_16x16x32_f16 a[132:135], v[56:59], a[0:3], a[32:35]
TY 106,5128
TY 110,18952
CV 29,8,143
M 49
ad 84,144
W 0
ds_read_b64 v[12:13], v4 offset:1536
v_mfma_f32_16x16x32_f16 a[136:139], v[64:67], a[0:3], a[32:35]
ds_read_b64 v[46:47], v4 offset:1544
EF 160,52
EF 184,53
M 51
EF 208,54
EF 232,55
CV 30,160,184
M 52
W 1
CV 31,208,232
EF 161,52
M 53
EF 185,53
EF 209,54
EF 233,55
M 54
ST 44,2304
CV 32,161,185
CV 33,209,233
M 55
W 2
EF 162,52
EF 186,53
M 56
EF 210,54
EF 234,55
CV 34,162,186
M 57
WL 3
AN 104,106
AN 105,107
CV 35,210,234
M 58
mw 75
SH 106
SH 107
EF 163,52
M 59
R 28
EF 187,53
v_mfma_f32_16x16x32_f16 a[156:159], v[56:59], a[8:11], a[32:35]
AN 106,106
AN 107,107
EF 211,54
M 61
WL 2
AN 108,110
AN 109,111
EF 235,55
v_mfma_f32_16x16x32_f16 a[160:163], v[64:67], a[8:11], a[32:35]
SH 110
SH 111
CV 36,163,187
M 63
AN 110,110
AN 111,111
CV 37,211,235
M 64
TY 114,6664
TY 118,20488
EF 164,52
M 65
ad 84,150
EF 188,53
M 66
W 0
EF 212,54
EF 236,55
M 67
ae 42,74,0
CV 38,164,188
CV 39,212,236
M 68
s_bitcmp1_b32 s50, 0
s_cselect_b32 s52, 24576, 0
v_add_u32 v50, s52, v3
ST 14,128
M 69
W 1
EF 165,52
EF 189,53
M 70
EF 213,54
EF 237,55
CV 40,165,189
M 71
R 30
CV 41,213,237
v_mfma_f32_16x16x32_f16 a[180:183], v[56:59], a[16:19], a[32:35]
W 2
EF 166,52
EF 190,53
M 73
WL 3
AN 112,114
AN 113,115
EF 214,54
v_mfma_f32_16x16x32_f16 a[184:187], v[64:67], a[16:19], a[32:35]
SH 114
SH 115
EF 238,55
M 75
mw 0
AN 114,114
AN 115,115
CV 42,166,190
M 76
WL 2
AN 116,118
AN 117,119
CV 43,214,238
M 77
SH 118
SH 119
EF 167,52
M 78
AN 118,118
AN 119,119
EF 191,53
M 79
TY 122,8200
TY 126,22024
EF 215,54
M 80
ad 84,192
W 0
ST 16,896
M 81
EF 239,55
CV 44,167,191
CV 45,215,239
M 82
BV 52,1792
AR 6,44
M 83
R 32
v_mfma_f32_16x16x32_f16 v[136:139], v[56:59], a[24:27], v[128:131]
W 1
AR 7,68
M 85
AR 8,92
AR 9,116
v_mfma_f32_16x16x32_f16 v[140:143], v[64:67], a[24:27], v[128:131]
EF 6,12
EF 7,13
M 87
W 2
EF 8,46
M 88
WL 4
AN 120,122
AN 121,123
M 89
SH 122
SH 123
M 90
mw 3
AN 122,122
AN 123,123
M 91
WL 3
AN 124,126
AN 125,127
M 92
SH 126
SH 127
ST 18,1664
M 93
AN 126,126
AN 127,127
M 94
ad 84,198
W 0
M 95
R 34
v_mfma_f32_16x16x32_f16 v[160:163], v[56:59], a[0:3], v[128:131]
EF 9,47
CV 10,6,7
M 97
W 1
CV 11,8,9
v_mfma_f32_16x16x32_f16 v[164:167], v[64:67], a[0:3], v[128:131]
AW 200,10
AW 201,11
M 99
AR 6,45
AR 7,69
M 100
W 2
AR 8,93
M 101
AR 9,117
EF 6,12
M 102
EF 7,13
EF 8,46
M 103
bc 49,3,66,60
EF 9,47
M 104
mh 0
ST 20,2432
CV 10,6,7
M 105
ad 66,608
H 0
M 106
CV 11,8,9
AW 202,10
M 107
R 48
v_mfma_f32_16x16x32_f16 v[184:187], v[56:59], a[8:11], v[128:131]
DG 1024
M 109
AW 203,11
AR 6,46
v_mfma_f32_16x16x32_f16 v[188:191], v[64:67], a[8:11], v[128:131]
AR 7,70
AR 8,94
M 111
bc 48,3,64,56
AR 9,118
M 112
mh 3
EF 6,12
EF 7,13
M 113
ad 64,608
H 0
M 114
EF 8,46
EF 9,47
M 115
CV 10,6,7
CV 11,8,9
M 116
H 1
AW 204,10
M 117
ae 42,74,0
AW 205,11
M 118
s_bfe_u32 s52, s50, 0x10001
v_mad_i32_i24 v50, v49, s52, v48
ST 22,128
M 119
WL 0
R 50
v_mfma_f32_16x16x32_f16 v[208:211], v[56:59], a[16:19], v[128:131]
H 2
AR 6,47
M 121
AR 7,71
AR 8,95
v_mfma_f32_16x16x32_f16 v[232:235], v[56:59], a[24:27], v[128:131]
AR 9,119
EF 6,12
M 133
PA 12288
)"
R"(TX 58,4608
TX 62,18432
DF
v_mfma_f32_16x16x32_f16 v[212:215], v[64:67], a[16:19], v[128:131]
EF 7,13
EF 8,46
M 123
EF 9,47
CV 10,6,7
v_mfma_f32_16x16x32_f16 v[236:239], v[64:67], a[24:27], v[128:131]
CV 11,8,9
AW 206,10
M 135
TY 66,5120
TY 70,18944
M 124
AW 207,11
M 125
AR 6,48
AR 7,72
AR 8,96
AR 9,120
EF 6,12
EF 7,13
M 136
ST 24,896
EF 8,46
EF 9,47
CV 14,6,7
CV 15,8,9
AR 6,49
M 137
TX 74,6144
TX 78,19968
AR 7,73
AR 8,97
AR 9,121
EF 6,12
M 126
WL 4
Q 0
EF 7,13
EF 8,46
EF 9,47
CV 16,6,7
M 127
Q 2
CV 17,8,9
AR 6,50
AR 7,74
AR 8,98
M 138
Q 4
AR 9,122
EF 6,12
EF 7,13
EF 8,46
M 139
TY 82,6656
TY 86,20480
EF 9,47
CV 18,6,7
CV 19,8,9
AR 6,51
M 128
Q 6
AR 7,75
AR 8,99
AR 9,123
EF 6,12
M 129
WL 2
Q 8
EF 7,13
EF 8,46
EF 9,47
CV 20,6,7
M 140
Q 10
CV 21,8,9
M 141
TX 90,7680
TX 94,21504
ds_read_b64 v[12:13], v4 offset:2048
M 130
Q 12
ds_read_b64 v[46:47], v4 offset:2056
M 131
R 52
M 142
Q 14
ST 26,1664
M 143
TY 98,8192
TY 102,22016
M 0
R 54
M 1
Q 16
M 12
Q 18
M 13
Q 20
M 2
Q 22
M 3
WL 10
Q 24
M 14
Q 26
M 15
Q 28
v_mfma_f32_16x16x32_f16 a[44:47], v[72:75], a[0:3], a[32:35]
Q 30
M 5
WL 6
Q 32
v_mfma_f32_16x16x32_f16 a[68:71], v[72:75], a[8:11], a[32:35]
Q 34
ST 28,2432
M 17
Q 36
v_mfma_f32_16x16x32_f16 a[48:51], v[80:83], a[0:3], a[32:35]
Q 38
M 7
WL 1
Q 40
v_mfma_f32_16x16x32_f16 a[72:75], v[80:83], a[8:11], a[32:35]
Q 42
M 19
Q 44
M 8
Q 46
M 9
AR 6,140
AR 7,164
M 20
AR 8,188
EF 6,52
M 21
EF 7,53
EF 8,54
M 10
EF 144,55
CV 10,6,7
M 11
R 56
M 22
ae 42,74,0
CV 11,8,144
M 23
R 58
M 24
s_bitcmp1_b32 s50, 0
s_cselect_b32 s52, 24576, 0
v_add_u32 v50, s52, v3
ST 30,256
M 25
AW 208,10
AW 209,11
M 26
AR 6,141
AR 7,165
M 27
AR 8,189
EF 6,52
v_mfma_f32_16x16x32_f16 a[92:95], v[72:75], a[16:19], a[32:35]
EF 7,53
EF 8,54
M 29
EF 145,55
CV 10,6,7
v_mfma_f32_16x16x32_f16 a[96:99], v[80:83], a[16:19], a[32:35]
CV 11,8,145
AW 210,10
M 31
AW 211,11
AR 6,142
M 32
AR 7,166
AR 8,190
M 33
EF 6,52
EF 7,53
M 34
EF 8,54
EF 146,55
M 35
WL 0
CV 10,6,7
CV 11,8,146
M 36
ST 32,1024
AW 212,10
M 37
AW 213,11
AR 6,143
M 38
AR 7,167
AR 8,191
M 39
EF 6,52
EF 7,53
v_mfma_f32_16x16x32_f16 a[116:119], v[72:75], a[24:27], a[32:35]
EF 8,54
EF 147,55
M 41
CV 10,6,7
CV 11,8,147
v_mfma_f32_16x16x32_f16 a[120:123], v[80:83], a[24:27], a[32:35]
AW 214,10
AW 215,11
M 43
AR 6,144
AR 7,168
AR 8,192
EF 6,52
EF 7,53
EF 8,54
M 44
WV 13
EF 148,55
CV 22,6,7
CV 23,8,148
AR 6,145
AR 7,169
AR 8,193
M 45
s_barrier
R 72
EF 6,52
EF 7,53
EF 8,54
EF 149,55
M 46
mw 24
CV 24,6,7
CV 25,8,149
AR 6,146
AR 7,170
AR 8,194
EF 6,52
M 47
R 74
EF 7,53
EF 8,54
EF 150,55
CV 26,6,7
M 48
TY 106,9736
TY 110,23560
ST 34,1792
CV 27,8,150
AR 6,147
AR 7,171
M 49
ad 84,240
W 0
AR 8,195
EF 6,52
EF 7,53
EF 8,54
M 50
EF 151,55
CV 28,6,7
CV 29,8,151
M 51
BV 52,3072
EF 168,12
EF 192,13
v_mfma_f32_16x16x32_f16 a[140:143], v[72:75], a[0:3], a[32:35]
W 1
EF 216,46
EF 240,47
M 53
CV 10,168,192
CV 11,216,240
AW 216,10
v_mfma_f32_16x16x32_f16 a[144:147], v[80:83], a[0:3], a[32:35]
AW 217,11
EF 169,12
EF 193,13
M 55
W 2
EF 217,46
EF 241,47
M 56
CV 10,169,193
CV 11,217,241
AW 218,10
M 57
WL 1
AN 104,106
AN 105,107
AW 219,11
M 58
mw 27
SH 106
SH 107
EF 170,12
M 59
R 76
EF 194,13
M 60
AN 106,106
AN 107,107
ST 36,2560
M 61
AN 108,110
AN 109,111
EF 218,46
M 62
SH 110
SH 111
EF 242,47
M 63
AN 110,110
AN 111,111
CV 10,170,194
v_mfma_f32_16x16x32_f16 a[164:167], v[72:75], a[8:11], a[32:35]
TY 114,11272
TY 118,25096
CV 11,218,242
M 65
ad 84,246
AW 220,10
v_mfma_f32_16x16x32_f16 a[168:171], v[80:83], a[8:11], a[32:35]
W 0
AW 221,11
EF 171,12
M 67
EF 195,13
EF 219,46
EF 243,47
M 68
CV 10,171,195
CV 11,219,243
AW 222,10
M 69
W 1
AW 223,11
M 70
EF 172,12
EF 196,13
EF 220,46
M 71
R 78
EF 244,47
M 72
WL 3
W 2
ae 42,74,0
M 73
s_bfe_u32 s52, s50, 0x10001
AN 112,114
AN 113,115
v_mad_i32_i24 v50, v49, s52, v48
M 74
SH 114
SH 115
ST 38,256
M 75
mw 48
AN 114,114
AN 115,115
CV 30,172,196
v_mfma_f32_16x16x32_f16 a[188:191], v[72:75], a[16:19], a[32:35]
WL 2
AN 116,118
AN 117,119
CV 31,220,244
M 77
SH 118
SH 119
EF 173,12
v_mfma_f32_16x16x32_f16 a[192:195], v[80:83], a[16:19], a[32:35]
AN 118,118
AN 119,119
EF 197,13
M 79
TY 122,12808
TY 126,26632
EF 221,46
M 80
ad 84,288
W 0
EF 245,47
M 81
CV 32,173,197
CV 33,221,245
EF 174,12
M 82
EF 198,13
EF 222,46
EF 246,47
M 83
R 80
CV 34,174,198
M 84
W 1
)"
R"(CV 35,222,246
EF 175,12
M 85
EF 199,13
EF 223,46
EF 247,47
M 86
ST 40,1024
CV 36,175,199
CV 37,223,247
M 87
W 2
ds_read_b64 v[12:13], v4 offset:3328
ds_read_b64 v[46:47], v4 offset:3336
v_mfma_f32_16x16x32_f16 v[144:147], v[72:75], a[24:27], v[128:131]
WL 5
AN 120,122
AN 121,123
AR 6,52
M 89
SH 122
SH 123
AR 7,76
v_mfma_f32_16x16x32_f16 v[148:151], v[80:83], a[24:27], v[128:131]
mw 51
AN 122,122
AN 123,123
AR 8,100
M 91
WL 4
AN 124,126
AN 125,127
AR 9,124
M 92
SH 126
SH 127
EF 6,52
M 93
AN 126,126
AN 127,127
EF 7,53
M 94
ad 84,294
W 0
EF 8,54
M 95
R 82
EF 9,55
M 96
CV 10,6,7
CV 11,8,9
AW 224,10
M 97
W 1
AW 225,11
AR 6,53
M 98
ST 42,1792
AR 7,77
AR 8,101
M 99
AR 9,125
EF 6,52
EF 7,53
v_mfma_f32_16x16x32_f16 v[168:171], v[72:75], a[0:3], v[128:131]
W 2
EF 8,54
EF 9,55
M 101
CV 10,6,7
CV 11,8,9
AW 226,10
v_mfma_f32_16x16x32_f16 v[172:175], v[80:83], a[0:3], v[128:131]
AW 227,11
AR 6,54
AR 7,78
M 103
bc 49,4,66,60
AR 8,102
AR 9,126
M 104
mh 9
EF 6,52
EF 7,53
EF 8,54
M 105
ad 66,800
H 0
EF 9,55
M 106
CV 10,6,7
CV 11,8,9
AW 228,10
M 107
R 0
AW 229,11
M 108
DG 1024
AR 6,55
M 109
AR 7,79
AR 8,103
AR 9,127
M 110
ST 44,2560
EF 6,52
EF 7,53
M 111
bc 48,4,64,56
EF 8,54
EF 9,55
v_mfma_f32_16x16x32_f16 v[192:195], v[72:75], a[8:11], v[128:131]
mh 12
CV 10,6,7
CV 11,8,9
AW 230,10
M 113
ad 64,800
H 0
AW 231,11
v_mfma_f32_16x16x32_f16 v[196:199], v[80:83], a[8:11], v[128:131]
AR 6,56
AR 7,80
AR 8,104
M 115
AR 9,128
EF 6,52
EF 7,53
M 116
H 1
EF 8,54
EF 9,55
M 117
CV 38,6,7
CV 39,8,9
AR 6,57
M 118
AR 7,81
AR 8,105
AR 9,129
M 119
WL 0
R 2
EF 6,52
M 120
H 2
EF 7,53
EF 8,54
M 121
EF 9,55
CV 40,6,7
CV 41,8,9
M 132
ae 42,72,0
AR 6,58
AR 7,82
M 133
s_bfe_u32 s52, s50, 0x10003
TX 58,9216
TX 62,23040
v_mad_i32_i24 v50, v49, s52, v48
M 122
ST 14,0
AR 8,106
AR 9,130
M 123
EF 6,52
EF 7,53
EF 8,54
M 134
EF 9,55
CV 42,6,7
CV 43,8,9
M 135
TY 66,9728
TY 70,23552
AR 6,59
v_mfma_f32_16x16x32_f16 v[216:219], v[72:75], a[16:19], v[128:131]
AR 7,83
AR 8,107
AR 9,131
M 125
EF 6,52
EF 7,53
EF 8,54
v_mfma_f32_16x16x32_f16 v[240:243], v[72:75], a[24:27], v[128:131]
EF 9,55
CV 44,6,7
CV 45,8,9
M 137
TX 74,10752
TX 78,24576
BV 52,3584
v_mfma_f32_16x16x32_f16 v[220:223], v[80:83], a[16:19], v[128:131]
WL 5
Q 0
M 127
Q 2
v_mfma_f32_16x16x32_f16 v[244:247], v[80:83], a[24:27], v[128:131]
Q 4
M 139
TY 82,11264
TY 86,25088
M 128
Q 6
ST 16,768
M 129
WL 5
Q 8
M 140
Q 10
M 141
TX 90,12288
TX 94,26112
M 130
Q 12
M 131
R 4
M 142
Q 14
M 143
TY 98,12800
TY 102,26624
M 0
R 6
M 1
WL 10
Q 16
M 12
Q 18
M 13
Q 20
M 2
Q 22
ST 18,1536
M 3
WL 8
Q 24
M 14
Q 26
M 15
Q 28
M 4
Q 30
M 5
WL 6
Q 32
M 16
Q 34
M 17
Q 36
M 6
Q 38
M 7
WL 1
Q 40
M 18
Q 42
M 19
Q 44
v_mfma_f32_16x16x32_f16 a[52:55], v[88:91], a[0:3], a[32:35]
Q 46
ST 20,2304
M 9
AR 6,148
AR 7,172
v_mfma_f32_16x16x32_f16 a[76:79], v[88:91], a[8:11], a[32:35]
AR 8,196
EF 6,12
M 21
EF 7,13
EF 8,46
v_mfma_f32_16x16x32_f16 a[56:59], v[96:99], a[0:3], a[32:35]
EF 152,47
CV 10,6,7
M 11
R 8
v_mfma_f32_16x16x32_f16 a[80:83], v[96:99], a[8:11], a[32:35]
CV 11,8,152
AW 232,10
M 23
R 10
M 24
AW 233,11
AR 6,149
M 25
AR 7,173
AR 8,197
M 26
EF 6,12
EF 7,13
M 27
EF 8,46
EF 153,47
M 28
ae 42,72,0
ST 22,128
M 29
CV 10,6,7
CV 11,8,153
M 30
AW 234,10
AW 235,11
M 31
AR 6,150
AR 7,174
v_mfma_f32_16x16x32_f16 a[100:103], v[88:91], a[16:19], a[32:35]
AR 8,198
EF 6,12
M 33
EF 7,13
EF 8,46
v_mfma_f32_16x16x32_f16 a[104:107], v[96:99], a[16:19], a[32:35]
EF 154,47
CV 10,6,7
M 35
WL 1
CV 11,8,154
AW 236,10
M 36
AW 237,11
AR 6,151
M 37
AR 7,175
AR 8,199
M 38
EF 6,12
EF 7,13
M 39
EF 8,46
EF 155,47
M 40
ST 24,896
CV 10,6,7
M 41
CV 11,8,155
AW 238,10
M 42
AW 239,11
M 43
WV 12
AR 6,152
AR 7,176
EF 6,12
EF 7,13
v_mfma_f32_16x16x32_f16 a[124:127], v[88:91], a[24:27], a[32:35]
WL 0
EF 132,46
EF 156,47
CV 14,6,7
CV 15,132,156
M 45
s_barrier
R 24
AR 6,153
AR 7,177
v_mfma_f32_16x16x32_f16 a[128:131], v[96:99], a[24:27], a[32:35]
mw 72
EF 6,12
EF 7,13
EF 133,46
EF 157,47
M 47
R 26
CV 16,6,7
CV 17,133,157
M 48
TY 106,520
TY 110,14344
AR 6,154
AR 7,178
M 49
ad 84,336
W 0
EF 6,12
EF 7,13
M 50
EF 134,46
EF 158,47
CV 18,6,7
CV 19,134,158
M 51
AR 6,155
AR 7,179
EF 6,12
EF 7,13
M 52
W 1
ST 26,1664
EF 135,46
EF 159,47
M 53
CV 20,6,7
CV 21,135,159
M 54
EF 176,52
EF 200,53
EF 224,54
M 55
W 2
)"
R"(EF 248,55
CV 10,176,200
v_mfma_f32_16x16x32_f16 a[148:151], v[88:91], a[0:3], a[32:35]
CV 11,224,248
AW 240,10
AW 241,11
M 57
WL 0
AN 104,106
AN 105,107
EF 177,52
v_mfma_f32_16x16x32_f16 a[152:155], v[96:99], a[0:3], a[32:35]
mw 75
SH 106
SH 107
EF 201,53
M 59
R 28
EF 225,54
M 60
AN 106,106
AN 107,107
EF 249,55
M 61
AN 108,110
AN 109,111
CV 10,177,201
M 62
SH 110
SH 111
CV 11,225,249
M 63
AN 110,110
AN 111,111
AW 242,10
M 64
TY 114,2056
TY 118,15880
ST 28,2432
M 65
ad 84,342
AW 243,11
M 66
W 0
EF 178,52
EF 202,53
M 67
EF 226,54
EF 250,55
CV 10,178,202
v_mfma_f32_16x16x32_f16 a[172:175], v[88:91], a[8:11], a[32:35]
CV 11,226,250
AW 244,10
AW 245,11
M 69
W 1
EF 179,52
EF 203,53
v_mfma_f32_16x16x32_f16 a[176:179], v[96:99], a[8:11], a[32:35]
EF 227,54
EF 251,55
CV 10,179,203
M 71
R 30
CV 11,227,251
M 72
W 2
AW 246,10
AW 247,11
M 73
WL 3
AN 112,114
AN 113,115
EF 180,52
M 74
SH 114
SH 115
EF 204,53
M 75
mw 0
AN 114,114
AN 115,115
EF 228,54
M 76
WL 2
AN 116,118
AN 117,119
EF 252,55
M 77
SH 118
SH 119
CV 10,180,204
M 78
AN 118,118
AN 119,119
CV 11,228,252
M 79
TY 122,3592
TY 126,17416
AW 248,10
v_mfma_f32_16x16x32_f16 a[196:199], v[88:91], a[16:19], a[32:35]
ad 84,384
W 0
AW 249,11
M 81
ae 42,72,0
ST 30,256
EF 181,52
v_mfma_f32_16x16x32_f16 v[132:135], v[96:99], a[16:19], v[128:131]
EF 205,53
EF 229,54
EF 253,55
M 83
R 32
CV 10,181,205
M 84
W 1
CV 11,229,253
AW 250,10
M 85
AW 251,11
EF 182,52
EF 206,53
M 86
EF 230,54
EF 254,55
CV 10,182,206
M 87
W 2
CV 11,230,254
AW 252,10
M 88
WL 3
AN 120,122
AN 121,123
AW 253,11
M 89
SH 122
SH 123
EF 183,52
M 90
mw 3
AN 122,122
AN 123,123
EF 207,53
M 91
WL 2
AN 124,126
AN 125,127
EF 231,54
v_mfma_f32_16x16x32_f16 v[152:155], v[88:91], a[24:27], v[128:131]
SH 126
SH 127
EF 255,55
M 93
AN 126,126
AN 127,127
ST 32,1024
v_mfma_f32_16x16x32_f16 v[156:159], v[96:99], a[24:27], v[128:131]
ad 84,390
W 0
CV 10,183,207
M 95
R 34
CV 11,231,255
M 96
AW 254,10
AW 255,11
M 97
W 1
M 98
M 99
M 100
W 2
M 101
M 102
M 103
bc 49,5,66,60
v_mfma_f32_16x16x32_f16 v[176:179], v[88:91], a[0:3], v[128:131]
mh 18
M 105
ad 66,992
H 0
ST 34,1792
v_mfma_f32_16x16x32_f16 v[180:183], v[96:99], a[0:3], v[128:131]
M 107
R 48
M 108
DG 1024
M 109
M 110
M 111
bc 48,5,64,56
M 112
mh 21
M 113
ad 64,992
H 0
M 114
M 115
v_mfma_f32_16x16x32_f16 v[200:203], v[88:91], a[8:11], v[128:131]
H 1
M 117
ST 36,2560
v_mfma_f32_16x16x32_f16 v[204:207], v[96:99], a[8:11], v[128:131]
M 119
WL 2
R 50
M 120
H 2
M 121
M 132
M 133
TX 58,0
TX 62,13824
M 122
M 123
M 134
M 135
TY 66,512
TY 70,14336
M 124
M 125
ae 42,72,96
M 136
s_bfe_u32 s52, s50, 0x10005
v_mad_i32_i24 v50, v49, s52, v48
ST 38,0
M 137
TX 74,1536
TX 78,15360
M 126
WL 4
Q 0
M 127
Q 2
M 138
Q 4
M 139
TY 82,2048
TY 86,15872
v_mfma_f32_16x16x32_f16 v[224:227], v[88:91], a[16:19], v[128:131]
Q 6
M 129
WL 4
Q 8
v_mfma_f32_16x16x32_f16 v[248:251], v[88:91], a[24:27], v[128:131]
Q 10
M 141
TX 90,3072
TX 94,16896
v_mfma_f32_16x16x32_f16 v[228:231], v[96:99], a[16:19], v[128:131]
Q 12
M 131
R 52
v_mfma_f32_16x16x32_f16 v[252:255], v[96:99], a[24:27], v[128:131]
Q 14
ST 40,768
M 143
TY 98,3584
TY 102,17408
M 0
R 54
M 1
WL 10
Q 16
M 12
Q 18
M 13
Q 20
M 2
Q 22
M 3
WL 8
Q 24
M 14
Q 26
M 15
Q 28
M 4
Q 30
M 5
WL 6
Q 32
M 16
Q 34
ST 42,1536
M 17
Q 36
M 6
Q 38
M 7
WL 1
Q 40
M 18
Q 42
M 19
Q 44
M 8
Q 46
M 9
M 20
M 21
M 10
M 11
R 56
M 22
ST 44,2304
M 23
R 58
M 24
M 25
M 26
M 27
M 28
M 29
M 30
M 31
M 32
M 33
M 34
ae 42,72,96
ST 14,128
M 35
WL 4
M 36
M 37
M 38
M 39
M 40
M 41
M 42
M 43
WV 12
M 44
WL 0
M 45
s_barrier
R 72
M 46
mw 24
ST 16,896
M 47
R 74
M 48
TY 106,5128
TY 110,18952
M 49
ad 84,432
W 0
M 50
M 51
M 52
W 1
M 53
M 54
M 55
W 2
M 56
M 57
WL 1
AN 104,106
AN 105,107
M 58
mw 27
SH 106
SH 107
ST 18,1664
M 59
R 76
M 60
AN 106,106
AN 107,107
M 61
WL 2
AN 108,110
AN 109,111
M 62
SH 110
SH 111
M 63
AN 110,110
AN 111,111
M 64
TY 114,6664
TY 118,20488
M 65
ad 84,438
M 66
W 0
M 67
M 68
M 69
W 1
M 70
ST 20,2432
M 71
R 78
M 72
W 2
)"
R"(M 73
WL 3
AN 112,114
AN 113,115
M 74
SH 114
SH 115
M 75
mw 48
AN 114,114
AN 115,115
M 76
WL 2
AN 116,118
AN 117,119
M 77
SH 118
SH 119
M 78
AN 118,118
AN 119,119
M 79
TY 122,8200
TY 126,22024
M 80
ad 84,480
W 0
M 81
M 82
M 83
R 80
M 84
W 1
M 85
M 86
M 87
W 2
M 88
WL 3
AN 120,122
AN 121,123
M 89
SH 122
SH 123
M 90
mw 51
AN 122,122
AN 123,123
M 91
WL 2
AN 124,126
AN 125,127
M 92
SH 126
SH 127
M 93
AN 126,126
AN 127,127
M 94
ad 84,486
W 0
M 95
R 82
M 96
M 97
W 1
M 98
M 99
M 100
W 2
M 101
M 102
M 103
bc 49,6,66,60
M 104
mh 0
M 105
ad 66,1184
H 0
M 106
M 107
R 0
M 108
DG 1024
M 109
M 110
M 111
bc 48,6,64,56
M 112
mh 3
M 113
ad 64,1184
H 0
M 114
M 115
M 116
H 1
M 117
M 118
M 119
WL 2
R 2
M 120
H 2
M 121
M 132
M 133
PA 12288
TX 58,4608
TX 62,18432
DF
M 122
M 123
M 134
M 135
TY 66,5120
TY 70,18944
M 124
ae 44,70,384
M 125
s_bitcmp1_b32 s50, 6
s_cselect_b32 s51, 24576, 0
v_add_u32 v47, s51, v3
M 136
XL 22,0
M 137
TX 74,6144
TX 78,19968
M 126
WL 4
Q 0
M 127
Q 2
M 138
Q 4
M 139
TY 82,6656
TY 86,20480
M 128
Q 6
M 129
WL 4
Q 8
M 140
Q 10
M 141
TX 90,7680
TX 94,21504
M 130
Q 12
M 131
R 4
M 142
Q 14
M 143
TY 98,8192
TY 102,22016
M 0
R 6
M 1
WL 10
Q 16
M 12
Q 18
M 13
Q 20
M 2
Q 22
M 3
WL 8
Q 24
M 14
Q 26
M 15
Q 28
M 4
Q 30
M 5
WL 6
Q 32
M 16
Q 34
M 17
Q 36
M 6
Q 38
M 7
WL 2
Q 40
M 18
Q 42
M 19
Q 44
M 8
Q 46
M 9
ae 42,72,0
M 20
s_bitcmp1_b32 s50, 2
s_cselect_b32 s52, 24576, 0
v_add_u32 v50, s52, v3
M 21
SA 200,0
XL 24,768
M 10
XL 26,1536
M 11
R 8
M 22
XL 28,2304
M 23
R 10
M 24
M 25
M 26
M 27
M 28
M 29
M 30
M 31
M 32
M 33
M 34
M 35
WL 4
M 36
M 37
M 38
M 39
M 40
M 41
M 42
M 43
WV 11
M 44
WL 0
M 45
s_barrier
R 24
M 46
mw 72
M 47
R 26
M 48
TY 106,9736
TY 110,23560
M 49
ad 84,528
W 0
M 50
M 51
M 52
W 1
M 53
M 54
M 55
W 2
M 56
M 57
WL 1
AN 104,106
AN 105,107
M 58
mw 75
SH 106
SH 107
M 59
R 28
M 60
AN 106,106
AN 107,107
M 61
WL 2
AN 108,110
AN 109,111
M 62
SH 110
SH 111
M 63
AN 110,110
AN 111,111
M 64
TY 114,11272
TY 118,25096
M 65
ad 84,534
M 66
W 0
M 67
M 68
M 69
W 1
M 70
M 71
R 30
M 72
W 2
M 73
WL 3
AN 112,114
AN 113,115
M 74
SH 114
SH 115
M 75
mw 0
AN 114,114
AN 115,115
M 76
WL 2
AN 116,118
AN 117,119
M 77
SH 118
SH 119
M 78
AN 118,118
AN 119,119
M 79
TY 122,12808
TY 126,26632
M 80
ad 84,576
W 0
M 81
M 82
M 83
R 32
M 84
W 1
SA 202,768
M 85
M 86
M 87
W 2
M 88
WL 3
AN 120,122
AN 121,123
M 89
SH 122
SH 123
M 90
mw 3
AN 122,122
AN 123,123
M 91
WL 2
AN 124,126
AN 125,127
M 92
SH 126
SH 127
M 93
AN 126,126
AN 127,127
M 94
ad 84,582
W 0
M 95
R 34
M 96
M 97
W 1
M 98
M 99
M 100
W 2
M 101
M 102
M 103
bc 49,7,66,60
M 104
mh 9
M 105
ad 66,1376
H 0
M 106
M 107
R 48
M 108
DG 1024
M 109
M 110
M 111
bc 48,7,64,56
M 112
mh 12
M 113
ad 64,1376
H 0
M 114
M 115
M 116
H 1
M 117
M 118
M 119
WL 2
R 50
)"
R"(M 120
H 2
M 121
M 132
M 133
TX 58,9216
TX 62,23040
M 122
M 123
M 134
M 135
TY 66,9728
TY 70,23552
M 124
M 125
M 136
ae 44,70,384
M 137
TX 74,10752
TX 78,24576
M 126
s_bfe_u32 s51, s50, 0x10007
WL 4
Q 0
M 127
Q 2
M 138
Q 4
M 139
TY 82,11264
TY 86,25088
M 128
Q 6
M 129
WL 4
Q 8
M 140
Q 10
M 141
TX 90,12288
TX 94,26112
M 130
Q 12
M 131
R 52
M 142
Q 14
M 143
TY 98,12800
TY 102,26624
M 0
R 54
M 1
WL 10
Q 16
M 12
Q 18
M 13
Q 20
M 2
Q 22
M 3
WL 8
Q 24
M 14
Q 26
M 15
Q 28
M 4
Q 30
M 5
WL 6
Q 32
M 16
Q 34
M 17
Q 36
M 6
Q 38
M 7
WL 2
Q 40
M 18
Q 42
M 19
Q 44
M 8
Q 46
M 9
SA 204,1536
v_mad_i32_i24 v47, v49, s51, v48
M 20
XL 30,0
M 21
XL 32,768
M 10
XL 34,1536
M 11
R 56
M 22
XL 36,2304
M 23
R 58
M 24
M 25
M 26
M 27
M 28
M 29
M 30
M 31
M 32
M 33
M 34
M 35
WL 4
M 36
M 37
M 38
M 39
M 40
WV 27
UL 6,22
UH 7,22
M 41
UL 8,23
UH 9,23
M 42
AR 10,36
AR 11,60
M 43
WV 10
AR 12,84
AR 13,108
M 44
WL 0
AD 10,6
AD 11,7
M 45
s_barrier
R 72
M 46
mw 24
AD 12,8
AD 13,9
M 47
R 74
M 48
TY 106,520
TY 110,14344
M 49
ad 84,624
W 0
M 50
AW 36,10
AW 60,11
M 51
AW 84,12
AW 108,13
M 52
W 1
UL 6,24
M 53
UH 7,24
UL 8,25
M 54
UH 9,25
AR 10,37
M 55
W 2
AR 11,61
M 56
AR 12,85
AR 13,109
M 57
WL 1
AN 104,106
AN 105,107
M 58
mw 27
SH 106
SH 107
M 59
R 76
M 60
AN 106,106
AN 107,107
M 61
WL 2
AN 108,110
AN 109,111
M 62
SH 110
SH 111
M 63
AN 110,110
AN 111,111
M 64
TY 114,2056
TY 118,15880
M 65
ad 84,630
M 66
W 0
AD 10,6
M 67
AD 11,7
AD 12,8
M 68
AD 13,9
AW 37,10
M 69
W 1
AW 61,11
M 70
AW 85,12
AW 109,13
M 71
R 78
M 72
W 2
UL 6,26
M 73
WL 3
AN 112,114
AN 113,115
M 74
SH 114
SH 115
M 75
mw 48
AN 114,114
AN 115,115
M 76
WL 2
AN 116,118
AN 117,119
M 77
SH 118
SH 119
M 78
AN 118,118
AN 119,119
M 79
TY 122,3592
TY 126,17416
M 80
ad 84,672
W 0
M 81
SA 206,2304
UH 7,26
M 82
UL 8,27
UH 9,27
M 83
R 80
M 84
W 1
AR 10,38
M 85
AR 11,62
AR 12,86
M 86
AR 13,110
AD 10,6
M 87
W 2
AD 11,7
M 88
WL 3
AN 120,122
AN 121,123
M 89
SH 122
SH 123
M 90
mw 51
AN 122,122
AN 123,123
M 91
WL 2
AN 124,126
AN 125,127
M 92
SH 126
SH 127
M 93
AN 126,126
AN 127,127
M 94
ad 84,678
W 0
M 95
R 82
M 96
AD 12,8
AD 13,9
M 97
W 1
AW 38,10
M 98
AW 62,11
AW 86,12
M 99
AW 110,13
UL 6,28
M 100
W 2
UH 7,28
M 101
UL 8,29
UH 9,29
M 102
AR 10,39
AR 11,63
M 103
bc 49,8,66,60
AR 12,87
M 104
mh 18
AR 13,111
AD 10,6
M 105
ad 66,1568
H 0
M 106
AD 11,7
AD 12,8
M 107
R 0
M 108
DG 1024
M 109
AD 13,9
AW 39,10
M 110
AW 63,11
AW 87,12
M 111
bc 48,8,64,56
AW 111,13
M 112
mh 21
M 113
ad 64,1568
H 0
M 114
M 115
M 116
H 1
M 117
M 118
M 119
WL 2
R 2
M 120
H 2
M 121
M 132
M 133
TX 58,0
TX 62,13824
M 122
M 123
M 134
M 135
TY 66,512
TY 70,14336
M 124
M 125
M 136
M 137
TX 74,1536
TX 78,15360
M 126
WL 4
Q 0
M 127
Q 2
M 138
Q 4
M 139
TY 82,2048
TY 86,15872
M 128
Q 6
M 129
WL 4
Q 8
M 140
Q 10
M 141
TX 90,3072
TX 94,16896
M 130
Q 12
M 131
R 4
M 142
)"
R"(Q 14
M 143
TY 98,3584
TY 102,17408
M 0
R 6
M 1
WL 10
Q 16
M 12
Q 18
M 13
Q 20
M 2
Q 22
M 3
WL 8
Q 24
M 14
Q 26
M 15
Q 28
M 4
Q 30
M 5
WL 6
Q 32
M 16
Q 34
M 17
Q 36
M 6
Q 38
M 7
WL 2
Q 40
M 18
Q 42
M 19
Q 44
M 8
Q 46
M 9
ae 42,72,0
M 20
SA 208,128
M 21
ae 44,68,384
M 10
s_bitcmp1_b32 s50, 8
s_cselect_b32 s51, 24576, 0
v_add_u32 v47, s51, v3
M 11
R 8
M 22
XL 38,0
M 23
R 10
M 24
XL 40,768
M 25
XL 42,1536
M 26
XL 44,2304
M 27
M 28
M 29
M 30
M 31
M 32
M 33
M 34
M 35
WL 4
M 36
M 37
M 38
M 39
M 40
M 41
M 42
WV 23
UL 6,30
UH 7,30
M 43
WV 10
UL 8,31
UH 9,31
M 44
WL 0
AR 10,40
AR 11,64
M 45
s_barrier
R 24
M 46
mw 72
AR 12,88
AR 13,112
M 47
R 26
M 48
TY 106,5128
TY 110,18952
M 49
ad 84,720
W 0
M 50
AD 10,6
AD 11,7
M 51
AD 12,8
AD 13,9
M 52
W 1
AW 40,10
M 53
AW 64,11
AW 88,12
M 54
AW 112,13
UL 6,32
M 55
W 2
UH 7,32
M 56
UL 8,33
UH 9,33
M 57
WL 1
AN 104,106
AN 105,107
M 58
mw 75
SH 106
SH 107
M 59
R 28
M 60
AN 106,106
AN 107,107
M 61
WL 2
AN 108,110
AN 109,111
M 62
SH 110
SH 111
M 63
AN 110,110
AN 111,111
M 64
TY 114,6664
TY 118,20488
M 65
ad 84,726
M 66
W 0
AR 10,41
M 67
AR 11,65
AR 12,89
M 68
AR 13,113
AD 10,6
M 69
W 1
AD 11,7
M 70
AD 12,8
AD 13,9
M 71
R 30
M 72
W 2
AW 41,10
M 73
WL 3
AN 112,114
AN 113,115
M 74
SH 114
SH 115
M 75
mw 0
AN 114,114
AN 115,115
M 76
WL 2
AN 116,118
AN 117,119
M 77
SH 118
SH 119
M 78
AN 118,118
AN 119,119
M 79
TY 122,8200
TY 126,22024
M 80
ad 84,768
W 0
M 81
AW 65,11
AW 89,12
M 82
SA 210,896
AW 113,13
M 83
R 32
M 84
W 1
UL 6,34
M 85
UH 7,34
UL 8,35
M 86
UH 9,35
AR 10,42
M 87
W 2
AR 11,66
M 88
WL 3
AN 120,122
AN 121,123
M 89
SH 122
SH 123
M 90
mw 3
AN 122,122
AN 123,123
M 91
WL 2
AN 124,126
AN 125,127
M 92
SH 126
SH 127
M 93
AN 126,126
AN 127,127
M 94
ad 84,774
W 0
M 95
R 34
M 96
AR 12,90
AR 13,114
M 97
W 1
AD 10,6
M 98
AD 11,7
AD 12,8
M 99
AD 13,9
AW 42,10
M 100
W 2
AW 66,11
M 101
AW 90,12
AW 114,13
M 102
UL 6,36
UH 7,36
M 103
bc 49,9,66,60
UL 8,37
M 104
mh 0
UH 9,37
AR 10,43
M 105
ad 66,1760
H 0
M 106
AR 11,67
AR 12,91
M 107
R 48
M 108
DG 1024
M 109
AR 13,115
AD 10,6
M 110
AD 11,7
AD 12,8
M 111
bc 48,9,64,56
AD 13,9
M 112
mh 3
AW 43,10
AW 67,11
M 113
ad 64,1760
H 0
M 114
AW 91,12
AW 115,13
M 115
M 116
H 1
M 117
M 118
M 119
WL 2
R 50
M 120
H 2
M 121
M 132
M 133
PA 12288
TX 58,4608
TX 62,18432
DF
M 122
M 123
M 134
M 135
TY 66,5120
TY 70,18944
M 124
M 125
M 136
M 137
TX 74,6144
TX 78,19968
M 126
WL 4
Q 0
M 127
Q 2
M 138
Q 4
M 139
TY 82,6656
TY 86,20480
M 128
Q 6
M 129
WL 4
Q 8
M 140
Q 10
M 141
TX 90,7680
TX 94,21504
M 130
Q 12
M 131
R 52
M 142
Q 14
M 143
TY 98,8192
TY 102,22016
M 0
R 54
M 1
WL 10
Q 16
M 12
Q 18
M 13
Q 20
M 2
Q 22
M 3
WL 8
Q 24
M 14
Q 26
M 15
Q 28
M 4
Q 30
M 5
WL 6
Q 32
M 16
Q 34
M 17
Q 36
M 6
Q 38
M 7
WL 2
Q 40
M 18
Q 42
M 19
Q 44
)"
R"(M 8
Q 46
M 9
SA 212,1664
M 20
ae 44,68,384
M 21
s_bfe_u32 s51, s50, 0x10009
v_mad_i32_i24 v47, v49, s51, v48
XL 14,0
M 10
XL 16,768
M 11
R 56
M 22
XL 18,1536
M 23
R 58
M 24
XL 20,2304
M 25
M 26
M 27
M 28
M 29
M 30
M 31
M 32
M 33
M 34
M 35
WL 4
M 36
M 37
M 38
M 39
M 40
M 41
M 42
M 43
WL 0
M 44
WV 11
UL 6,38
UH 7,38
M 45
s_barrier
R 72
M 46
mw 24
UL 8,39
UH 9,39
M 47
R 74
M 48
TY 106,9736
TY 110,23560
M 49
ad 84,816
W 0
M 50
AR 10,44
AR 11,68
M 51
AR 12,92
AR 13,116
M 52
W 1
AD 10,6
M 53
AD 11,7
AD 12,8
M 54
AD 13,9
AW 44,10
M 55
W 2
AW 68,11
M 56
AW 92,12
AW 116,13
M 57
WL 1
AN 104,106
AN 105,107
M 58
mw 27
SH 106
SH 107
M 59
R 76
M 60
AN 106,106
AN 107,107
M 61
WL 2
AN 108,110
AN 109,111
M 62
SH 110
SH 111
M 63
AN 110,110
AN 111,111
M 64
TY 114,11272
TY 118,25096
M 65
ad 84,822
M 66
W 0
UL 6,40
M 67
UH 7,40
UL 8,41
M 68
UH 9,41
AR 10,45
M 69
W 1
AR 11,69
M 70
AR 12,93
AR 13,117
M 71
R 78
M 72
W 2
AD 10,6
M 73
WL 3
AN 112,114
AN 113,115
M 74
SH 114
SH 115
M 75
mw 48
AN 114,114
AN 115,115
M 76
WL 2
AN 116,118
AN 117,119
M 77
SH 118
SH 119
M 78
AN 118,118
AN 119,119
M 79
TY 122,12808
TY 126,26632
M 80
ad 84,864
W 0
M 81
SA 214,2432
AD 11,7
M 82
AD 12,8
AD 13,9
M 83
R 80
M 84
W 1
AW 45,10
M 85
AW 69,11
AW 93,12
M 86
AW 117,13
UL 6,42
M 87
W 2
UH 7,42
M 88
WL 3
AN 120,122
AN 121,123
M 89
SH 122
SH 123
M 90
mw 51
AN 122,122
AN 123,123
M 91
WL 2
AN 124,126
AN 125,127
M 92
SH 126
SH 127
M 93
AN 126,126
AN 127,127
M 94
ad 84,870
W 0
M 95
R 82
M 96
UL 8,43
UH 9,43
M 97
W 1
AR 10,46
M 98
AR 11,70
AR 12,94
M 99
AR 13,118
AD 10,6
M 100
W 2
AD 11,7
M 101
AD 12,8
AD 13,9
M 102
AW 46,10
AW 70,11
M 103
bc 49,10,66,60
AW 94,12
M 104
mh 9
AW 118,13
UL 6,44
M 105
ad 66,1952
H 0
M 106
UH 7,44
UL 8,45
M 107
R 0
M 108
DG 1024
M 109
UH 9,45
AR 10,47
M 110
AR 11,71
AR 12,95
M 111
bc 48,10,64,56
AR 13,119
M 112
mh 12
AD 10,6
AD 11,7
M 113
ad 64,1952
H 0
M 114
AD 12,8
AD 13,9
M 115
AW 47,10
AW 71,11
M 116
H 1
AW 95,12
M 117
AW 119,13
M 118
M 119
WL 2
R 2
M 120
H 2
M 121
M 132
M 133
TX 58,9216
TX 62,23040
M 122
M 123
M 134
M 135
TY 66,9728
TY 70,23552
M 124
M 125
M 136
M 137
TX 74,10752
TX 78,24576
M 126
WL 4
Q 0
M 127
Q 2
M 138
Q 4
M 139
TY 82,11264
TY 86,25088
M 128
Q 6
M 129
WL 4
Q 8
M 140
Q 10
M 141
TX 90,12288
TX 94,26112
M 130
Q 12
M 131
R 4
M 142
Q 14
M 143
TY 98,12800
TY 102,26624
M 0
R 6
M 1
WL 10
Q 16
M 12
Q 18
M 13
Q 20
M 2
Q 22
M 3
WL 8
Q 24
M 14
Q 26
M 15
Q 28
M 4
Q 30
M 5
WL 6
Q 32
M 16
Q 34
M 17
Q 36
M 6
Q 38
M 7
WL 2
Q 40
M 18
Q 42
M 19
Q 44
M 8
Q 46
M 9
ae 42,72,0
M 20
SA 216,256
M 21
ae 44,68,480
M 10
s_bitcmp1_b32 s50, 10
s_cselect_b32 s51, 24576, 0
v_add_u32 v47, s51, v3
M 11
R 8
M 22
XL 22,0
M 23
R 10
M 24
XL 24,768
M 25
XL 26,1536
M 26
XL 28,2304
M 27
M 28
M 29
M 30
M 31
M 32
M 33
M 34
M 35
WL 4
M 36
M 37
M 38
M 39
M 40
M 41
M 42
M 43
WV 10
M 44
WL 0
)"
R"(M 45
s_barrier
R 24
M 46
mw 72
UL 6,14
UH 7,14
M 47
R 26
M 48
TY 106,520
TY 110,14344
M 49
ad 84,912
W 0
M 50
UL 8,15
UH 9,15
M 51
AR 10,48
AR 11,72
M 52
W 1
AR 12,96
M 53
AR 13,120
AD 10,6
M 54
AD 11,7
AD 12,8
M 55
W 2
AD 13,9
M 56
AW 48,10
AW 72,11
M 57
WL 1
AN 104,106
AN 105,107
M 58
mw 75
SH 106
SH 107
M 59
R 28
M 60
AN 106,106
AN 107,107
M 61
WL 2
AN 108,110
AN 109,111
M 62
SH 110
SH 111
M 63
AN 110,110
AN 111,111
M 64
TY 114,2056
TY 118,15880
M 65
ad 84,918
M 66
W 0
AW 96,12
M 67
AW 120,13
UL 6,16
M 68
UH 7,16
UL 8,17
M 69
W 1
UH 9,17
M 70
AR 10,49
AR 11,73
M 71
R 30
M 72
W 2
AR 12,97
M 73
WL 3
AN 112,114
AN 113,115
M 74
SH 114
SH 115
M 75
mw 0
AN 114,114
AN 115,115
M 76
WL 2
AN 116,118
AN 117,119
M 77
SH 118
SH 119
M 78
AN 118,118
AN 119,119
M 79
TY 122,3592
TY 126,17416
M 80
ad 84,960
W 0
M 81
AR 13,121
AD 10,6
M 82
SA 218,1024
AD 11,7
M 83
R 32
M 84
W 1
AD 12,8
M 85
AD 13,9
AW 49,10
M 86
AW 73,11
AW 97,12
M 87
W 2
AW 121,13
M 88
WL 3
AN 120,122
AN 121,123
M 89
SH 122
SH 123
M 90
mw 3
AN 122,122
AN 123,123
M 91
WL 2
AN 124,126
AN 125,127
M 92
SH 126
SH 127
M 93
AN 126,126
AN 127,127
M 94
ad 84,966
W 0
M 95
R 34
M 96
UL 6,18
UH 7,18
M 97
W 1
UL 8,19
M 98
UH 9,19
AR 10,50
M 99
AR 11,74
AR 12,98
M 100
W 2
AR 13,122
M 101
AD 10,6
AD 11,7
M 102
AD 12,8
AD 13,9
M 103
bc 49,11,66,60
AW 50,10
M 104
mh 18
AW 74,11
AW 98,12
M 105
ad 66,2144
H 0
M 106
AW 122,13
UL 6,20
M 107
R 48
M 108
DG 1024
M 109
UH 7,20
UL 8,21
M 110
UH 9,21
AR 10,51
M 111
bc 48,11,64,56
AR 11,75
M 112
mh 21
AR 12,99
AR 13,123
M 113
ad 64,2144
H 0
M 114
AD 10,6
AD 11,7
M 115
AD 12,8
AD 13,9
M 116
H 1
AW 51,10
M 117
AW 75,11
AW 99,12
M 118
AW 123,13
M 119
WL 1
R 50
M 120
H 2
M 121
M 132
M 133
TX 58,0
TX 62,13824
M 122
M 123
M 134
M 135
TY 66,512
TY 70,14336
M 124
M 125
M 136
M 137
TX 74,1536
TX 78,15360
M 126
WL 4
Q 0
M 127
Q 2
M 138
Q 4
M 139
TY 82,2048
TY 86,15872
M 128
Q 6
M 129
WL 4
Q 8
M 140
Q 10
M 141
TX 90,3072
TX 94,16896
M 130
Q 12
M 131
R 52
M 142
Q 14
M 143
TY 98,3584
TY 102,17408
M 0
R 54
M 1
WL 10
Q 16
M 12
Q 18
M 13
Q 20
M 2
Q 22
M 3
WL 8
Q 24
M 14
Q 26
M 15
Q 28
M 4
Q 30
M 5
WL 6
Q 32
M 16
Q 34
M 17
Q 36
M 6
Q 38
M 7
WL 2
Q 40
M 18
Q 42
M 19
Q 44
M 8
Q 46
M 9
SA 220,1792
M 20
ae 44,68,480
M 21
s_bfe_u32 s51, s50, 0x1000b
v_mad_i32_i24 v47, v49, s51, v48
XL 30,0
M 10
XL 32,768
M 11
R 56
M 22
XL 34,1536
M 23
R 58
M 24
XL 36,2304
M 25
M 26
M 27
M 28
M 29
M 30
M 31
M 32
M 33
M 34
M 35
WL 4
M 36
M 37
M 38
M 39
M 40
M 41
M 42
M 43
WV 10
M 44
WL 0
M 45
s_barrier
R 72
M 46
mw 24
M 47
R 74
M 48
TY 106,5128
TY 110,18952
M 49
ad 84,1008
W 0
M 50
UL 6,22
UH 7,22
M 51
UL 8,23
UH 9,23
M 52
W 1
AR 10,52
M 53
AR 11,76
AR 12,100
M 54
AR 13,124
AD 10,6
M 55
W 2
AD 11,7
M 56
AD 12,8
AD 13,9
M 57
WL 1
AN 104,106
AN 105,107
M 58
mw 27
SH 106
SH 107
M 59
R 76
M 60
)"
R"(AN 106,106
AN 107,107
M 61
WL 2
AN 108,110
AN 109,111
M 62
SH 110
SH 111
M 63
AN 110,110
AN 111,111
M 64
TY 114,6664
TY 118,20488
M 65
ad 84,1014
M 66
W 0
AW 52,10
M 67
AW 76,11
AW 100,12
M 68
AW 124,13
UL 6,24
M 69
W 1
UH 7,24
M 70
UL 8,25
UH 9,25
M 71
R 78
M 72
W 2
AR 10,53
M 73
WL 3
AN 112,114
AN 113,115
M 74
SH 114
SH 115
M 75
mw 48
AN 114,114
AN 115,115
M 76
WL 2
AN 116,118
AN 117,119
M 77
SH 118
SH 119
M 78
AN 118,118
AN 119,119
M 79
TY 122,8200
TY 126,22024
M 80
ad 84,1056
W 0
M 81
SA 222,2560
AR 11,77
M 82
AR 12,101
AR 13,125
M 83
R 80
M 84
W 1
AD 10,6
M 85
AD 11,7
AD 12,8
M 86
AD 13,9
AW 53,10
M 87
W 2
AW 77,11
M 88
WL 3
AN 120,122
AN 121,123
M 89
SH 122
SH 123
M 90
mw 51
AN 122,122
AN 123,123
M 91
WL 2
AN 124,126
AN 125,127
M 92
SH 126
SH 127
M 93
AN 126,126
AN 127,127
M 94
ad 84,1062
W 0
M 95
R 82
M 96
AW 101,12
AW 125,13
M 97
W 1
UL 6,26
M 98
UH 7,26
UL 8,27
M 99
UH 9,27
AR 10,54
M 100
W 2
AR 11,78
M 101
AR 12,102
AR 13,126
M 102
AD 10,6
AD 11,7
M 103
bc 49,12,66,60
AD 12,8
M 104
mh 0
AD 13,9
AW 54,10
M 105
ad 66,2336
H 0
M 106
AW 78,11
AW 102,12
M 107
R 0
M 108
DG 1024
M 109
AW 126,13
UL 6,28
M 110
UH 7,28
UL 8,29
M 111
bc 48,12,64,56
UH 9,29
M 112
mh 3
AR 10,55
AR 11,79
M 113
ad 64,2336
H 0
M 114
AR 12,103
AR 13,127
M 115
AD 10,6
AD 11,7
M 116
H 1
AD 12,8
M 117
AD 13,9
AW 55,10
M 118
AW 79,11
AW 103,12
M 119
WL 0
R 2
M 120
H 2
AW 127,13
M 121
M 132
M 133
PA 12288
TX 58,4608
TX 62,18432
DF
M 122
M 123
M 134
M 135
TY 66,5120
TY 70,18944
M 124
M 125
M 136
M 137
TX 74,6144
TX 78,19968
M 126
WL 4
Q 0
M 127
Q 2
M 138
Q 4
M 139
TY 82,6656
TY 86,20480
M 128
Q 6
M 129
WL 4
Q 8
M 140
Q 10
M 141
TX 90,7680
TX 94,21504
M 130
Q 12
M 131
R 4
M 142
Q 14
M 143
TY 98,8192
TY 102,22016
M 0
R 6
M 1
WL 10
Q 16
M 12
Q 18
M 13
Q 20
M 2
Q 22
M 3
WL 8
Q 24
M 14
Q 26
M 15
Q 28
M 4
Q 30
M 5
WL 6
Q 32
M 16
Q 34
M 17
Q 36
M 6
Q 38
M 7
WL 2
Q 40
M 18
Q 42
M 19
Q 44
M 8
Q 46
M 9
ae 42,72,96
M 20
s_bitcmp1_b32 s50, 4
s_cselect_b32 s52, 24576, 0
v_add_u32 v50, s52, v3
M 21
SA 224,0
M 10
M 11
R 8
M 22
M 23
R 10
M 24
M 25
M 26
M 27
M 28
M 29
M 30
M 31
M 32
ae 44,70,384
M 33
s_bitcmp1_b32 s50, 6
s_cselect_b32 s51, 24576, 0
v_add_u32 v47, s51, v3
M 34
XL 38,128
M 35
WL 4
M 36
M 37
M 38
XL 40,896
M 39
M 40
M 41
M 42
M 43
WV 9
M 44
WL 0
XL 42,1664
M 45
s_barrier
R 24
M 46
mw 72
M 47
R 26
M 48
TY 106,9736
TY 110,23560
M 49
ad 84,1104
W 0
M 50
XL 44,2432
UL 6,30
M 51
UH 7,30
UL 8,31
M 52
W 1
UH 9,31
M 53
AR 10,56
AR 11,80
M 54
AR 12,104
AR 13,128
M 55
W 2
AD 10,6
M 56
AD 11,7
AD 12,8
M 57
WL 1
AN 104,106
AN 105,107
M 58
mw 75
SH 106
SH 107
M 59
R 28
M 60
AN 106,106
AN 107,107
M 61
WL 2
AN 108,110
AN 109,111
M 62
SH 110
SH 111
M 63
AN 110,110
AN 111,111
M 64
TY 114,11272
TY 118,25096
M 65
ad 84,1110
M 66
W 0
AD 13,9
M 67
AW 56,10
AW 80,11
M 68
AW 104,12
AW 128,13
M 69
W 1
UL 6,32
M 70
UH 7,32
UL 8,33
M 71
R 30
M 72
W 2
UH 9,33
M 73
WL 3
)"
R"(AN 112,114
AN 113,115
M 74
SH 114
SH 115
M 75
mw 0
AN 114,114
AN 115,115
M 76
WL 2
AN 116,118
AN 117,119
M 77
SH 118
SH 119
M 78
AN 118,118
AN 119,119
M 79
TY 122,12808
TY 126,26632
M 80
ad 84,1152
W 0
M 81
AR 10,57
AR 11,81
M 82
AR 12,105
AR 13,129
M 83
R 32
M 84
W 1
SA 226,768
M 85
AD 10,6
AD 11,7
M 86
AD 12,8
AD 13,9
M 87
W 2
AW 57,10
M 88
WL 3
AN 120,122
AN 121,123
M 89
SH 122
SH 123
M 90
mw 3
AN 122,122
AN 123,123
M 91
WL 2
AN 124,126
AN 125,127
M 92
SH 126
SH 127
M 93
AN 126,126
AN 127,127
M 94
ad 84,1158
W 0
M 95
R 34
M 96
AW 81,11
AW 105,12
M 97
W 1
AW 129,13
M 98
UL 6,34
UH 7,34
M 99
UL 8,35
UH 9,35
M 100
W 2
AR 10,58
M 101
AR 11,82
AR 12,106
M 102
AR 13,130
AD 10,6
M 103
bc 49,13,66,60
AD 11,7
M 104
mh 9
AD 12,8
AD 13,9
M 105
ad 66,2528
H 0
M 106
AW 58,10
AW 82,11
M 107
R 48
M 108
DG 1024
M 109
AW 106,12
AW 130,13
M 110
UL 6,36
UH 7,36
M 111
bc 48,13,64,56
UL 8,37
M 112
mh 12
UH 9,37
AR 10,59
M 113
ad 64,2528
H 0
M 114
AR 11,83
AR 12,107
M 115
AR 13,131
AD 10,6
M 116
H 1
AD 11,7
M 117
AD 12,8
AD 13,9
M 118
AW 59,10
AW 83,11
M 119
WL 0
R 50
M 120
H 2
AW 107,12
M 121
AW 131,13
M 132
M 133
TX 58,9216
TX 62,23040
M 122
M 123
M 134
M 135
TY 66,9728
TY 70,23552
M 124
M 125
M 136
M 137
TX 74,10752
TX 78,24576
M 126
WL 4
Q 0
M 127
Q 2
M 138
Q 4
M 139
TY 82,11264
TY 86,25088
M 128
Q 6
M 129
WL 4
Q 8
M 140
Q 10
M 141
TX 90,12288
TX 94,26112
M 130
Q 12
M 131
R 52
M 142
Q 14
M 143
TY 98,12800
TY 102,26624
M 0
R 54
M 1
WL 10
Q 16
M 12
Q 18
M 13
Q 20
M 2
Q 22
M 3
WL 8
Q 24
M 14
Q 26
M 15
Q 28
M 4
Q 30
M 5
WL 6
Q 32
M 16
Q 34
M 17
Q 36
M 6
Q 38
M 7
WL 2
Q 40
M 18
Q 42
M 19
Q 44
M 8
Q 46
M 9
SA 228,1536
M 20
M 21
M 10
M 11
R 56
M 22
M 23
R 58
M 24
M 25
M 26
M 27
M 28
M 29
M 30
M 31
M 32
M 33
M 34
ae 44,70,384
M 35
WL 4
M 36
s_bfe_u32 s51, s50, 0x10007
v_mad_i32_i24 v47, v49, s51, v48
XL 14,128
M 37
M 38
M 39
M 40
XL 16,896
M 41
M 42
M 43
WV 8
M 44
WL 0
M 45
s_barrier
R 72
M 46
mw 24
XL 18,1664
M 47
R 74
M 48
TY 106,520
TY 110,14344
M 49
ad 84,1200
W 0
M 50
M 51
M 52
W 1
XL 20,2432
M 53
M 54
M 55
W 2
M 56
M 57
WL 1
AN 104,106
AN 105,107
M 58
mw 27
SH 106
SH 107
M 59
R 76
M 60
AN 106,106
AN 107,107
M 61
WL 2
AN 108,110
AN 109,111
M 62
SH 110
SH 111
M 63
AN 110,110
AN 111,111
M 64
TY 114,2056
TY 118,15880
M 65
ad 84,1206
M 66
W 0
M 67
M 68
M 69
W 1
M 70
M 71
R 78
M 72
W 2
M 73
WL 3
AN 112,114
AN 113,115
M 74
SH 114
SH 115
M 75
mw 48
AN 114,114
AN 115,115
M 76
WL 2
AN 116,118
AN 117,119
M 77
SH 118
SH 119
M 78
AN 118,118
AN 119,119
M 79
TY 122,3592
TY 126,17416
M 80
ad 84,1248
W 0
M 81
SA 230,2304
M 82
M 83
R 80
M 84
W 1
M 85
M 86
M 87
W 2
M 88
WL 3
AN 120,122
AN 121,123
M 89
SH 122
SH 123
M 90
mw 51
AN 122,122
AN 123,123
M 91
WL 2
AN 124,126
AN 125,127
M 92
SH 126
SH 127
M 93
AN 126,126
AN 127,127
M 94
ad 84,1254
W 0
M 95
R 82
M 96
UL 6,38
UH 7,38
)"
R"(M 97
W 1
UL 8,39
M 98
UH 9,39
AR 10,132
M 99
AR 11,156
AR 12,180
M 100
W 2
AD 10,6
M 101
AD 11,7
AD 12,8
M 102
AD 136,9
AW 132,10
M 103
bc 49,14,66,60
AW 156,11
M 104
mh 18
AW 180,12
UL 6,40
M 105
ad 66,2720
H 0
M 106
UH 7,40
UL 8,41
M 107
R 0
M 108
DG 1024
M 109
UH 9,41
AR 10,133
M 110
AR 11,157
AR 12,181
M 111
bc 48,14,64,56
AD 10,6
M 112
mh 21
AD 11,7
AD 12,8
M 113
ad 64,2720
H 0
M 114
AD 137,9
AW 133,10
M 115
AW 157,11
AW 181,12
M 116
H 1
UL 6,42
M 117
UH 7,42
UL 8,43
M 118
UH 9,43
AR 10,134
M 119
WL 0
R 2
M 120
H 2
AR 11,158
M 121
AR 12,182
AD 10,6
M 132
AD 11,7
AD 12,8
M 133
TX 58,0
TX 62,13824
M 122
AD 138,9
AW 134,10
M 123
AW 158,11
AW 182,12
M 134
UL 6,44
UH 7,44
M 135
TY 66,512
TY 70,14336
M 124
UL 8,45
UH 9,45
M 125
AR 10,135
AR 11,159
M 136
AR 12,183
AD 10,6
M 137
TX 74,1536
TX 78,15360
M 126
WL 4
Q 0
M 127
Q 2
M 138
Q 4
M 139
TY 82,2048
TY 86,15872
M 128
Q 6
M 129
WL 4
Q 8
M 140
Q 10
M 141
TX 90,3072
TX 94,16896
M 130
Q 12
M 131
R 4
M 142
Q 14
M 143
TY 98,3584
TY 102,17408
M 0
R 6
M 1
WL 10
Q 16
M 12
Q 18
M 13
Q 20
M 2
Q 22
M 3
WL 8
Q 24
M 14
Q 26
M 15
Q 28
M 4
Q 30
M 5
WL 6
Q 32
M 16
Q 34
M 17
Q 36
M 6
Q 38
M 7
WL 2
Q 40
M 18
Q 42
M 19
Q 44
M 8
Q 46
M 9
ae 42,72,96
M 20
SA 232,128
AD 11,7
M 21
AD 12,8
AD 139,9
M 10
AW 135,10
AW 159,11
M 11
R 8
M 22
AW 183,12
M 23
R 10
M 24
M 25
M 26
M 27
M 28
M 29
M 30
M 31
M 32
M 33
M 34
M 35
WL 4
M 36
ae 44,68,384
M 37
s_bitcmp1_b32 s50, 8
s_cselect_b32 s51, 24576, 0
v_add_u32 v47, s51, v3
M 38
XL 22,128
M 39
M 40
M 41
M 42
XL 24,896
M 43
WV 8
M 44
WL 0
M 45
s_barrier
R 24
M 46
mw 72
M 47
R 26
M 48
TY 106,5128
TY 110,18952
M 49
ad 84,1296
W 0
M 50
XL 26,1664
M 51
M 52
W 1
M 53
M 54
XL 28,2432
M 55
W 2
M 56
M 57
WL 1
AN 104,106
AN 105,107
M 58
mw 75
SH 106
SH 107
M 59
R 28
M 60
AN 106,106
AN 107,107
M 61
WL 2
AN 108,110
AN 109,111
M 62
SH 110
SH 111
M 63
AN 110,110
AN 111,111
M 64
TY 114,6664
TY 118,20488
M 65
ad 84,1302
M 66
W 0
M 67
M 68
M 69
W 1
M 70
M 71
R 30
M 72
W 2
M 73
WL 3
AN 112,114
AN 113,115
M 74
SH 114
SH 115
M 75
mw 0
AN 114,114
AN 115,115
M 76
WL 2
AN 116,118
AN 117,119
M 77
SH 118
SH 119
M 78
AN 118,118
AN 119,119
M 79
TY 122,8200
TY 126,22024
M 80
ad 84,1344
W 0
M 81
M 82
SA 234,896
M 83
R 32
M 84
W 1
M 85
M 86
M 87
W 2
M 88
WL 3
AN 120,122
AN 121,123
M 89
SH 122
SH 123
M 90
mw 3
AN 122,122
AN 123,123
M 91
WL 2
AN 124,126
AN 125,127
M 92
SH 126
SH 127
M 93
AN 126,126
AN 127,127
M 94
ad 84,1350
W 0
M 95
R 34
M 96
UL 6,14
UH 7,14
M 97
W 1
UL 8,15
M 98
UH 9,15
AR 10,136
M 99
AR 11,160
AR 12,184
M 100
W 2
AD 10,6
M 101
AD 11,7
AD 12,8
M 102
AD 140,9
AW 136,10
M 103
bc 49,15,66,60
AW 160,11
M 104
mh 0
AW 184,12
UL 6,16
M 105
ad 66,2912
H 0
M 106
UH 7,16
UL 8,17
M 107
R 48
M 108
DG 1024
M 109
UH 9,17
AR 10,137
M 110
AR 11,161
AR 12,185
M 111
bc 48,15,64,56
AD 10,6
M 112
mh 3
AD 11,7
AD 12,8
M 113
ad 64,2912
H 0
)"
R"(M 114
AD 141,9
AW 137,10
M 115
AW 161,11
AW 185,12
M 116
H 1
UL 6,18
M 117
UH 7,18
UL 8,19
M 118
UH 9,19
AR 10,138
M 119
WL 0
R 50
M 120
H 2
AR 11,162
M 121
AR 12,186
AD 10,6
M 132
AD 11,7
AD 12,8
M 133
PA 12288
TX 58,4608
TX 62,18432
DF
M 122
AD 142,9
AW 138,10
M 123
AW 162,11
AW 186,12
M 134
UL 6,20
UH 7,20
M 135
TY 66,5120
TY 70,18944
M 124
UL 8,21
UH 9,21
M 125
AR 10,139
AR 11,163
M 136
AR 12,187
AD 10,6
M 137
TX 74,6144
TX 78,19968
M 126
WL 4
Q 0
M 127
Q 2
M 138
Q 4
M 139
TY 82,6656
TY 86,20480
M 128
Q 6
M 129
WL 4
Q 8
M 140
Q 10
M 141
TX 90,7680
TX 94,21504
M 130
Q 12
M 131
R 52
M 142
Q 14
M 143
TY 98,8192
TY 102,22016
M 0
R 54
M 1
WL 10
Q 16
M 12
Q 18
M 13
Q 20
M 2
Q 22
M 3
WL 8
Q 24
M 14
Q 26
M 15
Q 28
M 4
Q 30
M 5
WL 6
Q 32
M 16
Q 34
M 17
Q 36
M 6
Q 38
M 7
WL 2
Q 40
M 18
Q 42
M 19
Q 44
M 8
Q 46
M 9
SA 236,1664
AD 11,7
M 20
AD 12,8
AD 143,9
M 21
AW 139,10
AW 163,11
M 10
AW 187,12
M 11
R 56
M 22
M 23
R 58
M 24
M 25
M 26
M 27
M 28
M 29
M 30
M 31
M 32
M 33
M 34
M 35
WL 4
M 36
M 37
M 38
ae 44,68,384
M 39
s_bfe_u32 s51, s50, 0x10009
v_mad_i32_i24 v47, v49, s51, v48
XL 30,128
M 40
M 41
M 42
M 43
WV 8
M 44
WL 0
XL 32,896
M 45
s_barrier
R 72
M 46
mw 24
M 47
R 74
M 48
TY 106,9736
TY 110,23560
M 49
ad 84,1392
W 0
M 50
XL 34,1664
M 51
M 52
W 1
M 53
M 54
M 55
W 2
M 56
XL 36,2432
M 57
WL 1
AN 104,106
AN 105,107
M 58
mw 27
SH 106
SH 107
M 59
R 76
M 60
AN 106,106
AN 107,107
M 61
WL 2
AN 108,110
AN 109,111
M 62
SH 110
SH 111
M 63
AN 110,110
AN 111,111
M 64
TY 114,11272
TY 118,25096
M 65
ad 84,1398
M 66
W 0
M 67
M 68
M 69
W 1
M 70
M 71
R 78
M 72
W 2
M 73
WL 3
AN 112,114
AN 113,115
M 74
SH 114
SH 115
M 75
mw 48
AN 114,114
AN 115,115
M 76
WL 2
AN 116,118
AN 117,119
M 77
SH 118
SH 119
M 78
AN 118,118
AN 119,119
M 79
TY 122,12808
TY 126,26632
M 80
ad 84,1440
W 0
M 81
SA 238,2432
M 82
M 83
R 80
M 84
W 1
M 85
M 86
M 87
W 2
M 88
WL 3
AN 120,122
AN 121,123
M 89
SH 122
SH 123
M 90
mw 51
AN 122,122
AN 123,123
M 91
WL 2
AN 124,126
AN 125,127
M 92
SH 126
SH 127
M 93
AN 126,126
AN 127,127
M 94
ad 84,1446
W 0
M 95
R 82
M 96
UL 6,22
UH 7,22
M 97
W 1
UL 8,23
M 98
UH 9,23
AR 10,140
M 99
AR 11,164
AR 12,188
M 100
W 2
AD 10,6
M 101
AD 11,7
AD 12,8
M 102
AD 144,9
AW 140,10
M 103
bc 49,16,66,60
AW 164,11
M 104
mh 9
AW 188,12
UL 6,24
M 105
ad 66,3104
H 0
M 106
UH 7,24
UL 8,25
M 107
R 0
M 108
DG 1024
M 109
UH 9,25
AR 10,141
M 110
AR 11,165
AR 12,189
M 111
bc 48,16,64,56
AD 10,6
M 112
mh 12
AD 11,7
AD 12,8
M 113
ad 64,3104
H 0
M 114
AD 145,9
AW 141,10
M 115
AW 165,11
AW 189,12
M 116
H 1
UL 6,26
M 117
UH 7,26
UL 8,27
M 118
UH 9,27
AR 10,142
M 119
WL 0
R 2
M 120
H 2
AR 11,166
M 121
AR 12,190
AD 10,6
M 132
AD 11,7
AD 12,8
M 133
TX 58,9216
TX 62,23040
M 122
AD 146,9
AW 142,10
M 123
AW 166,11
AW 190,12
M 134
UL 6,28
UH 7,28
M 135
TY 66,9728
TY 70,23552
M 124
UL 8,29
UH 9,29
M 125
AR 10,143
AR 11,167
M 136
AR 12,191
AD 10,6
)"
R"(M 137
TX 74,10752
TX 78,24576
M 126
WL 4
Q 0
M 127
Q 2
M 138
Q 4
M 139
TY 82,11264
TY 86,25088
M 128
Q 6
M 129
WL 4
Q 8
M 140
Q 10
M 141
TX 90,12288
TX 94,26112
M 130
Q 12
M 131
R 4
M 142
Q 14
M 143
TY 98,12800
TY 102,26624
M 0
R 6
M 1
WL 10
Q 16
M 12
Q 18
M 13
Q 20
M 2
Q 22
M 3
WL 8
Q 24
M 14
Q 26
M 15
Q 28
M 4
Q 30
M 5
WL 6
Q 32
M 16
Q 34
M 17
Q 36
M 6
Q 38
M 7
WL 2
Q 40
M 18
Q 42
M 19
Q 44
M 8
Q 46
M 9
ae 42,72,96
M 20
SA 240,256
AD 11,7
M 21
AD 12,8
AD 147,9
M 10
AW 143,10
AW 167,11
M 11
R 8
M 22
AW 191,12
M 23
R 10
M 24
M 25
M 26
M 27
M 28
M 29
M 30
M 31
M 32
M 33
M 34
M 35
WL 4
M 36
M 37
M 38
M 39
M 40
ae 44,68,480
M 41
s_bitcmp1_b32 s50, 10
s_cselect_b32 s51, 24576, 0
v_add_u32 v47, s51, v3
M 42
XL 38,128
M 43
WV 7
M 44
WL 0
M 45
s_barrier
R 24
M 46
mw 72
XL 40,896
M 47
R 26
M 48
TY 106,520
TY 110,14344
M 49
ad 84,1488
W 0
M 50
M 51
M 52
W 1
XL 42,1664
M 53
M 54
M 55
W 2
M 56
M 57
WL 1
AN 104,106
AN 105,107
M 58
mw 75
SH 106
SH 107
M 59
R 28
M 60
AN 106,106
AN 107,107
M 61
WL 2
AN 108,110
AN 109,111
M 62
SH 110
SH 111
M 63
AN 110,110
AN 111,111
M 64
TY 114,2056
TY 118,15880
M 65
ad 84,1494
M 66
W 0
XL 44,2432
M 67
M 68
M 69
W 1
M 70
M 71
R 30
M 72
W 2
M 73
WL 3
AN 112,114
AN 113,115
M 74
SH 114
SH 115
M 75
mw 0
AN 114,114
AN 115,115
M 76
WL 2
AN 116,118
AN 117,119
M 77
SH 118
SH 119
M 78
AN 118,118
AN 119,119
M 79
TY 122,3592
TY 126,17416
M 80
ad 84,1536
W 0
M 81
M 82
SA 242,1024
M 83
R 32
M 84
W 1
M 85
M 86
M 87
W 2
M 88
WL 3
AN 120,122
AN 121,123
M 89
SH 122
SH 123
M 90
mw 3
AN 122,122
AN 123,123
M 91
WL 2
AN 124,126
AN 125,127
M 92
SH 126
SH 127
M 93
AN 126,126
AN 127,127
M 94
ad 84,1542
W 0
M 95
R 34
M 96
UL 6,30
UH 7,30
M 97
W 1
UL 8,31
M 98
UH 9,31
AR 10,144
M 99
AR 11,168
AR 12,192
M 100
W 2
AD 10,6
M 101
AD 11,7
AD 12,8
M 102
AD 148,9
AW 144,10
M 103
bc 49,17,66,60
AW 168,11
M 104
mh 18
AW 192,12
UL 6,32
M 105
ad 66,3296
H 0
M 106
UH 7,32
UL 8,33
M 107
R 48
M 108
DG 1024
M 109
UH 9,33
AR 10,145
M 110
AR 11,169
AR 12,193
M 111
bc 48,17,64,56
AD 10,6
M 112
mh 21
AD 11,7
AD 12,8
M 113
ad 64,3296
H 0
M 114
AD 149,9
AW 145,10
M 115
AW 169,11
AW 193,12
M 116
H 1
UL 6,34
M 117
UH 7,34
UL 8,35
M 118
UH 9,35
AR 10,146
M 119
WL 0
R 50
M 120
H 2
AR 11,170
M 121
AR 12,194
AD 10,6
M 132
AD 11,7
AD 12,8
M 133
TX 58,0
TX 62,13824
M 122
AD 150,9
AW 146,10
M 123
AW 170,11
AW 194,12
M 134
UL 6,36
UH 7,36
M 135
TY 66,512
TY 70,14336
M 124
UL 8,37
UH 9,37
M 125
AR 10,147
AR 11,171
M 136
AR 12,195
AD 10,6
M 137
TX 74,1536
TX 78,15360
M 126
WL 4
Q 0
M 127
Q 2
M 138
Q 4
M 139
TY 82,2048
TY 86,15872
M 128
Q 6
M 129
WL 4
Q 8
M 140
Q 10
M 141
TX 90,3072
TX 94,16896
M 130
Q 12
M 131
R 52
M 142
Q 14
M 143
TY 98,3584
TY 102,17408
M 0
R 54
M 1
WL 10
Q 16
M 12
Q 18
M 13
Q 20
M 2
Q 22
M 3
WL 8
Q 24
M 14
Q 26
M 15
Q 28
M 4
)"
R"(Q 30
M 5
WL 6
Q 32
M 16
Q 34
M 17
Q 36
M 6
Q 38
M 7
WL 2
Q 40
M 18
Q 42
M 19
Q 44
M 8
Q 46
M 9
SA 244,1792
AD 11,7
M 20
AD 12,8
AD 151,9
M 21
AW 147,10
AW 171,11
M 10
AW 195,12
M 11
R 56
M 22
M 23
R 58
M 24
M 25
M 26
M 27
M 28
M 29
M 30
M 31
M 32
M 33
M 34
M 35
WL 4
M 36
M 37
M 38
M 39
M 40
M 41
M 42
ae 44,68,480
M 43
WV 6
M 44
WL 0
M 45
s_barrier
R 72
M 46
mw 24
M 47
R 74
M 48
s_bfe_u32 s51, s50, 0x1000b
TY 106,5128
TY 110,18952
M 49
ad 84,1584
W 0
M 50
v_mad_i32_i24 v47, v49, s51, v48
XL 14,128
M 51
XL 16,896
M 52
W 1
M 53
M 54
XL 18,1664
M 55
W 2
M 56
M 57
WL 1
AN 104,106
AN 105,107
M 58
mw 27
SH 106
SH 107
M 59
R 76
M 60
AN 106,106
AN 107,107
M 61
WL 2
AN 108,110
AN 109,111
M 62
SH 110
SH 111
M 63
AN 110,110
AN 111,111
M 64
TY 114,6664
TY 118,20488
M 65
ad 84,1590
M 66
W 0
XL 20,2432
M 67
M 68
M 69
W 1
M 70
M 71
R 78
M 72
W 2
M 73
WL 3
AN 112,114
AN 113,115
M 74
SH 114
SH 115
M 75
mw 48
AN 114,114
AN 115,115
M 76
WL 2
AN 116,118
AN 117,119
M 77
SH 118
SH 119
M 78
AN 118,118
AN 119,119
M 79
TY 122,8200
TY 126,22024
M 80
ad 84,1632
W 0
M 81
SA 246,2560
M 82
M 83
R 80
M 84
W 1
M 85
M 86
M 87
W 2
M 88
WL 3
AN 120,122
AN 121,123
M 89
SH 122
SH 123
M 90
mw 51
AN 122,122
AN 123,123
M 91
WL 2
AN 124,126
AN 125,127
M 92
SH 126
SH 127
M 93
AN 126,126
AN 127,127
M 94
ad 84,1638
W 0
M 95
R 82
M 96
UL 6,38
UH 7,38
M 97
W 1
UL 8,39
M 98
UH 9,39
AR 10,148
M 99
AR 11,172
AR 12,196
M 100
W 2
AD 10,6
M 101
AD 11,7
AD 12,8
M 102
AD 152,9
AW 148,10
M 103
bc 49,18,66,60
AW 172,11
M 104
mh 0
AW 196,12
UL 6,40
M 105
ad 66,3488
H 0
M 106
UH 7,40
UL 8,41
M 107
R 0
M 108
DG 1024
M 109
UH 9,41
AR 10,149
M 110
AR 11,173
AR 12,197
M 111
bc 48,18,64,56
AD 10,6
M 112
mh 3
AD 11,7
AD 12,8
M 113
ad 64,3488
H 0
M 114
AD 153,9
AW 149,10
M 115
AW 173,11
AW 197,12
M 116
H 1
UL 6,42
M 117
UH 7,42
UL 8,43
M 118
UH 9,43
AR 10,150
M 119
WL 0
R 2
M 120
H 2
AR 11,174
M 121
AR 12,198
AD 10,6
M 132
AD 11,7
AD 12,8
M 133
PA 12288
TX 58,4608
TX 62,18432
DF
M 122
AD 154,9
AW 150,10
M 123
AW 174,11
AW 198,12
M 134
UL 6,44
UH 7,44
M 135
TY 66,5120
TY 70,18944
M 124
UL 8,45
UH 9,45
M 125
AR 10,151
AR 11,175
M 136
AR 12,199
AD 10,6
M 137
TX 74,6144
TX 78,19968
M 126
WL 4
Q 0
M 127
Q 2
M 138
Q 4
M 139
TY 82,6656
TY 86,20480
M 128
Q 6
M 129
WL 4
Q 8
M 140
Q 10
M 141
TX 90,7680
TX 94,21504
M 130
Q 12
M 131
R 4
M 142
Q 14
M 143
TY 98,8192
TY 102,22016
M 0
R 6
M 1
WL 10
Q 16
M 12
Q 18
M 13
Q 20
M 2
Q 22
M 3
WL 8
Q 24
M 14
Q 26
M 15
Q 28
M 4
Q 30
M 5
WL 6
Q 32
M 16
Q 34
M 17
Q 36
M 6
Q 38
M 7
WL 2
Q 40
M 18
Q 42
M 19
Q 44
M 8
Q 46
M 9
ae 42,72,96
M 20
s_bfe_u32 s52, s50, 0x10005
v_mad_i32_i24 v50, v49, s52, v48
SA 248,256
M 21
AD 11,7
AD 12,8
M 10
AD 155,9
AW 151,10
M 11
R 8
M 22
AW 175,11
AW 199,12
M 23
R 10
M 24
M 25
M 26
M 27
M 28
M 29
M 30
M 31
M 32
M 33
M 34
M 35
WL 4
)"
R"(M 36
M 37
M 38
M 39
M 40
M 41
M 42
M 43
WV 7
M 44
WL 0
M 45
s_barrier
R 24
M 46
mw 72
M 47
R 26
M 48
TY 106,9736
TY 110,23560
M 49
ad 84,1680
W 0
M 50
M 51
M 52
W 1
M 53
M 54
M 55
W 2
M 56
M 57
WL 1
AN 104,106
AN 105,107
M 58
mw 75
SH 106
SH 107
M 59
R 28
M 60
AN 106,106
AN 107,107
M 61
WL 2
AN 108,110
AN 109,111
M 62
SH 110
SH 111
M 63
AN 110,110
AN 111,111
M 64
TY 114,11272
TY 118,25096
M 65
ad 84,1686
M 66
W 0
M 67
M 68
M 69
W 1
M 70
ae 44,70,384
M 71
R 30
M 72
W 2
M 73
WL 3
AN 112,114
AN 113,115
M 74
SH 114
SH 115
M 75
mw 0
AN 114,114
AN 115,115
M 76
WL 2
AN 116,118
AN 117,119
M 77
SH 118
SH 119
M 78
AN 118,118
AN 119,119
M 79
TY 122,12808
TY 126,26632
M 80
ad 84,1728
W 0
M 81
s_bitcmp1_b32 s50, 6
s_cselect_b32 s51, 24576, 0
M 82
SA 250,1024
v_add_u32 v47, s51, v3
M 83
R 32
M 84
W 1
XL 22,256
M 85
XL 24,1024
M 86
XL 26,1792
M 87
W 2
M 88
WL 3
AN 120,122
AN 121,123
M 89
SH 122
SH 123
M 90
mw 3
AN 122,122
AN 123,123
M 91
WL 2
AN 124,126
AN 125,127
M 92
SH 126
SH 127
M 93
AN 126,126
AN 127,127
M 94
ad 84,1734
W 0
M 95
R 34
M 96
XL 28,2560
M 97
W 1
M 98
UL 6,14
UH 7,14
M 99
UL 8,15
UH 9,15
M 100
W 2
AR 10,152
M 101
AR 11,176
AD 10,6
M 102
AD 11,7
AD 132,8
M 103
bc 49,19,66,60
AD 156,9
M 104
mh 9
AW 152,10
AW 176,11
M 105
ad 66,3680
H 0
M 106
UL 6,16
UH 7,16
M 107
R 48
M 108
DG 1024
M 109
UL 8,17
UH 9,17
M 110
AR 10,153
AR 11,177
M 111
bc 48,19,64,56
AD 10,6
M 112
mh 12
AD 11,7
AD 133,8
M 113
ad 64,3680
H 0
M 114
AD 157,9
AW 153,10
M 115
AW 177,11
UL 6,18
M 116
H 1
UH 7,18
M 117
UL 8,19
UH 9,19
M 118
AR 10,154
AR 11,178
M 119
WL 0
R 50
M 120
H 2
AD 10,6
M 121
AD 11,7
AD 134,8
M 132
AD 158,9
AW 154,10
M 133
TX 58,9216
TX 62,23040
M 122
AW 178,11
UL 6,20
M 123
UH 7,20
UL 8,21
M 134
UH 9,21
AR 10,155
M 135
TY 66,9728
TY 70,23552
M 124
AR 11,179
AD 10,6
M 125
AD 11,7
AD 135,8
M 136
AD 159,9
AW 155,10
M 137
TX 74,10752
TX 78,24576
M 126
WL 4
Q 0
M 127
Q 2
M 138
Q 4
M 139
TY 82,11264
TY 86,25088
M 128
Q 6
M 129
WL 4
Q 8
M 140
Q 10
M 141
TX 90,12288
TX 94,26112
M 130
Q 12
M 131
R 52
M 142
Q 14
M 143
TY 98,12800
TY 102,26624
M 0
R 54
M 1
WL 10
Q 16
M 12
Q 18
M 13
Q 20
M 2
Q 22
M 3
WL 8
Q 24
M 14
Q 26
M 15
Q 28
M 4
Q 30
M 5
WL 6
Q 32
M 16
Q 34
M 17
Q 36
M 6
Q 38
M 7
WL 2
Q 40
M 18
Q 42
M 19
Q 44
M 8
Q 46
M 9
SA 252,1792
AW 179,11
M 20
M 21
M 10
M 11
R 56
M 22
M 23
R 58
M 24
M 25
M 26
M 27
M 28
M 29
M 30
M 31
M 32
M 33
M 34
M 35
WL 4
M 36
M 37
M 38
M 39
M 40
M 41
M 42
M 43
WV 6
M 44
WL 0
M 45
s_barrier
R 72
M 46
mw 24
M 47
R 74
M 48
TY 106,520
TY 110,14344
M 49
ad 84,1776
W 0
M 50
M 51
M 52
W 1
M 53
M 54
M 55
W 2
M 56
M 57
WL 1
AN 104,106
AN 105,107
M 58
mw 27
SH 106
SH 107
M 59
R 76
M 60
AN 106,106
AN 107,107
M 61
WL 2
AN 108,110
AN 109,111
M 62
SH 110
SH 111
M 63
AN 110,110
AN 111,111
M 64
TY 114,2056
TY 118,15880
M 65
)"
R"(ad 84,1782
M 66
W 0
M 67
M 68
M 69
W 1
M 70
M 71
R 78
M 72
W 2
M 73
WL 3
AN 112,114
AN 113,115
M 74
SH 114
SH 115
M 75
mw 48
AN 114,114
AN 115,115
M 76
WL 2
AN 116,118
AN 117,119
M 77
SH 118
SH 119
M 78
AN 118,118
AN 119,119
M 79
TY 122,3592
TY 126,17416
M 80
ad 84,1824
W 0
M 81
SA 254,2560
M 82
ae 44,70,384
M 83
R 80
M 84
s_bfe_u32 s51, s50, 0x10007
W 1
v_mad_i32_i24 v47, v49, s51, v48
M 85
XL 30,256
M 86
XL 32,1024
M 87
W 2
XL 34,1792
M 88
WL 3
AN 120,122
AN 121,123
M 89
SH 122
SH 123
M 90
mw 51
AN 122,122
AN 123,123
M 91
WL 2
AN 124,126
AN 125,127
M 92
SH 126
SH 127
M 93
AN 126,126
AN 127,127
M 94
ad 84,1830
W 0
M 95
R 82
M 96
XL 36,2560
M 97
W 1
M 98
M 99
M 100
W 2
M 101
M 102
M 103
bc 49,20,66,60
M 104
mh 18
M 105
ad 66,3872
H 0
M 106
M 107
R 0
M 108
DG 1024
M 109
M 110
M 111
bc 48,20,64,56
M 112
mh 21
M 113
ad 64,3872
H 0
M 114
M 115
M 116
H 1
M 117
M 118
M 119
WL 2
R 2
M 120
H 2
M 121
M 132
M 133
TX 58,0
TX 62,13824
M 122
M 123
M 134
UL 6,22
UH 7,22
M 135
TY 66,512
TY 70,14336
M 124
UL 8,23
UH 9,23
M 125
AD 160,6
AD 184,7
M 136
AD 208,8
AD 232,9
M 137
TX 74,1536
TX 78,15360
M 126
WL 4
Q 0
M 127
Q 2
M 138
Q 4
M 139
TY 82,2048
TY 86,15872
M 128
Q 6
M 129
WL 4
Q 8
M 140
Q 10
M 141
TX 90,3072
TX 94,16896
M 130
Q 12
M 131
R 4
M 142
Q 14
M 143
TY 98,3584
TY 102,17408
M 0
R 6
M 1
WL 10
Q 16
M 12
Q 18
M 13
Q 20
M 2
Q 22
M 3
WL 8
Q 24
M 14
Q 26
M 15
Q 28
M 4
Q 30
M 5
WL 6
Q 32
M 16
Q 34
M 17
Q 36
M 6
Q 38
M 7
WL 2
Q 40
M 18
Q 42
M 19
Q 44
M 8
Q 46
M 9
UL 6,24
UH 7,24
M 20
UL 8,25
UH 9,25
M 21
AD 161,6
AD 185,7
M 10
AD 209,8
AD 233,9
M 11
R 8
M 22
UL 6,26
UH 7,26
M 23
R 10
M 24
UL 8,27
UH 9,27
M 25
AD 162,6
AD 186,7
M 26
AD 210,8
AD 234,9
M 27
UL 6,28
UH 7,28
M 28
UL 8,29
UH 9,29
M 29
AD 163,6
AD 187,7
M 30
AD 211,8
AD 235,9
M 31
M 32
M 33
M 34
M 35
WL 4
M 36
M 37
M 38
M 39
M 40
M 41
M 42
M 43
WV 5
M 44
WL 0
M 45
s_barrier
R 24
M 46
mw 72
M 47
R 26
M 48
TY 106,5128
TY 110,18952
M 49
ad 84,1872
W 0
M 50
M 51
M 52
W 1
M 53
M 54
M 55
W 2
M 56
M 57
WL 1
AN 104,106
AN 105,107
M 58
mw 75
SH 106
SH 107
M 59
R 28
M 60
AN 106,106
AN 107,107
M 61
WL 2
AN 108,110
AN 109,111
M 62
SH 110
SH 111
M 63
AN 110,110
AN 111,111
M 64
TY 114,6664
TY 118,20488
M 65
ad 84,1878
M 66
W 0
M 67
M 68
M 69
W 1
M 70
M 71
R 30
M 72
W 2
M 73
WL 3
AN 112,114
AN 113,115
M 74
SH 114
SH 115
M 75
mw 0
AN 114,114
AN 115,115
M 76
WL 2
AN 116,118
AN 117,119
M 77
SH 118
SH 119
M 78
AN 118,118
AN 119,119
M 79
TY 122,8200
TY 126,22024
M 80
ad 84,1920
W 0
M 81
ae 44,68,384
M 82
s_bitcmp1_b32 s50, 8
s_cselect_b32 s51, 24576, 0
v_add_u32 v47, s51, v3
M 83
R 32
M 84
W 1
XL 38,256
M 85
XL 40,1024
M 86
M 87
W 2
M 88
WL 3
AN 120,122
AN 121,123
M 89
SH 122
SH 123
M 90
mw 3
AN 122,122
AN 123,123
M 91
WL 2
AN 124,126
AN 125,127
M 92
SH 126
SH 127
M 93
AN 126,126
AN 127,127
M 94
ad 84,1926
)"
R"(W 0
M 95
R 34
M 96
XL 42,1792
M 97
W 1
XL 44,2560
M 98
M 99
M 100
W 2
M 101
M 102
M 103
bc 49,21,66,60
M 104
mh 0
M 105
ad 66,4064
H 0
M 106
M 107
R 48
M 108
DG 1024
M 109
M 110
M 111
bc 48,21,64,56
M 112
mh 3
M 113
ad 64,4064
H 0
M 114
M 115
M 116
H 1
M 117
M 118
M 119
WL 2
R 50
M 120
H 2
M 121
M 132
M 133
PA 12288
TX 58,4608
TX 62,18432
DF
M 122
M 123
M 134
M 135
TY 66,5120
TY 70,18944
M 124
M 125
M 136
UL 6,30
UH 7,30
M 137
TX 74,6144
TX 78,19968
M 126
WL 4
Q 0
M 127
Q 2
M 138
Q 4
M 139
TY 82,6656
TY 86,20480
M 128
Q 6
M 129
WL 4
Q 8
M 140
Q 10
M 141
TX 90,7680
TX 94,21504
M 130
Q 12
M 131
R 52
M 142
Q 14
M 143
TY 98,8192
TY 102,22016
M 0
R 54
M 1
WL 10
Q 16
M 12
Q 18
M 13
Q 20
M 2
Q 22
M 3
WL 8
Q 24
M 14
Q 26
M 15
Q 28
M 4
Q 30
M 5
WL 6
Q 32
M 16
Q 34
M 17
Q 36
M 6
Q 38
M 7
WL 2
Q 40
M 18
Q 42
M 19
Q 44
M 8
Q 46
M 9
UL 8,31
UH 9,31
M 20
AD 164,6
AD 188,7
M 21
AD 212,8
AD 236,9
M 10
UL 6,32
UH 7,32
M 11
R 56
M 22
UL 8,33
UH 9,33
M 23
R 58
M 24
AD 165,6
AD 189,7
M 25
AD 213,8
AD 237,9
M 26
UL 6,34
UH 7,34
M 27
UL 8,35
UH 9,35
M 28
AD 166,6
AD 190,7
M 29
AD 214,8
AD 238,9
M 30
UL 6,36
UH 7,36
M 31
UL 8,37
UH 9,37
M 32
AD 167,6
AD 191,7
M 33
AD 215,8
AD 239,9
M 34
M 35
WL 2
M 36
M 37
M 38
M 39
M 40
M 41
M 42
M 43
WV 6
M 44
WL 0
M 45
s_barrier
R 72
M 46
mw 24
M 47
R 74
M 48
TY 106,9736
TY 110,23560
M 49
ad 84,1968
W 0
M 50
M 51
M 52
W 1
M 53
M 54
M 55
W 2
M 56
M 57
WL 1
AN 104,106
AN 105,107
M 58
mw 27
SH 106
SH 107
M 59
R 76
M 60
AN 106,106
AN 107,107
M 61
WL 2
AN 108,110
AN 109,111
M 62
SH 110
SH 111
M 63
AN 110,110
AN 111,111
M 64
TY 114,11272
TY 118,25096
M 65
ad 84,1974
M 66
W 0
M 67
M 68
M 69
W 1
M 70
M 71
R 78
M 72
W 2
M 73
WL 3
AN 112,114
AN 113,115
M 74
SH 114
SH 115
M 75
mw 48
AN 114,114
AN 115,115
M 76
WL 2
AN 116,118
AN 117,119
M 77
SH 118
SH 119
M 78
AN 118,118
AN 119,119
M 79
TY 122,12808
TY 126,26632
M 80
ad 84,2016
W 0
M 81
M 82
ae 44,68,384
M 83
R 80
M 84
s_bfe_u32 s51, s50, 0x10009
W 1
v_mad_i32_i24 v47, v49, s51, v48
M 85
XL 14,256
M 86
M 87
W 2
M 88
WL 3
AN 120,122
AN 121,123
M 89
SH 122
SH 123
M 90
mw 51
AN 122,122
AN 123,123
M 91
WL 2
AN 124,126
AN 125,127
M 92
SH 126
SH 127
M 93
AN 126,126
AN 127,127
M 94
ad 84,2022
W 0
M 95
R 82
M 96
XL 16,1024
M 97
W 1
XL 18,1792
M 98
M 99
M 100
W 2
XL 20,2560
M 101
M 102
M 103
bc 49,22,66,60
M 104
mh 9
M 105
ad 66,4256
H 0
M 106
M 107
R 0
M 108
DG 1024
M 109
M 110
M 111
bc 48,22,64,56
M 112
mh 12
M 113
ad 64,4256
H 0
M 114
M 115
M 116
H 1
M 117
M 118
M 119
WL 2
R 2
M 120
H 2
M 121
M 132
M 133
TX 58,9216
TX 62,23040
M 122
M 123
M 134
M 135
TY 66,9728
TY 70,23552
M 124
M 125
M 136
M 137
TX 74,10752
TX 78,24576
M 126
WL 4
Q 0
M 127
Q 2
M 138
Q 4
M 139
TY 82,11264
TY 86,25088
M 128
Q 6
M 129
WL 4
)"
R"(Q 8
M 140
Q 10
M 141
TX 90,12288
TX 94,26112
M 130
Q 12
M 131
R 4
M 142
Q 14
M 143
TY 98,12800
TY 102,26624
M 0
R 6
M 1
WL 10
Q 16
M 12
Q 18
M 13
Q 20
M 2
Q 22
M 3
WL 8
Q 24
M 14
Q 26
M 15
Q 28
M 4
Q 30
M 5
WL 6
Q 32
M 16
Q 34
M 17
Q 36
M 6
Q 38
M 7
WL 2
Q 40
M 18
Q 42
M 19
Q 44
M 8
Q 46
M 9
UL 6,38
UH 7,38
M 20
UL 8,39
UH 9,39
M 21
AD 168,6
AD 192,7
M 10
AD 216,8
AD 240,9
M 11
R 8
M 22
UL 6,40
UH 7,40
M 23
R 10
M 24
UL 8,41
UH 9,41
M 25
AD 169,6
AD 193,7
M 26
AD 217,8
AD 241,9
M 27
UL 6,42
UH 7,42
M 28
UL 8,43
UH 9,43
M 29
AD 170,6
AD 194,7
M 30
AD 218,8
AD 242,9
M 31
UL 6,44
UH 7,44
M 32
UL 8,45
UH 9,45
M 33
AD 171,6
AD 195,7
M 34
AD 219,8
AD 243,9
M 35
WL 2
M 36
M 37
M 38
M 39
M 40
M 41
M 42
M 43
WV 6
M 44
WL 0
M 45
s_barrier
R 24
M 46
mw 72
M 47
R 26
M 48
TY 106,520
TY 110,14344
M 49
ad 84,2064
W 0
M 50
M 51
M 52
W 1
M 53
M 54
M 55
W 2
M 56
M 57
WL 1
AN 104,106
AN 105,107
M 58
mw 75
SH 106
SH 107
M 59
R 28
M 60
AN 106,106
AN 107,107
M 61
WL 2
AN 108,110
AN 109,111
M 62
SH 110
SH 111
M 63
AN 110,110
AN 111,111
M 64
TY 114,2056
TY 118,15880
M 65
ad 84,2070
M 66
W 0
M 67
M 68
M 69
W 1
M 70
M 71
R 30
M 72
W 2
M 73
WL 3
AN 112,114
AN 113,115
M 74
SH 114
SH 115
M 75
mw 0
AN 114,114
AN 115,115
M 76
WL 2
AN 116,118
AN 117,119
M 77
SH 118
SH 119
M 78
AN 118,118
AN 119,119
M 79
TY 122,3592
TY 126,17416
M 80
ad 84,2112
W 0
M 81
M 82
M 83
R 32
M 84
W 1
M 85
M 86
ae 44,68,480
M 87
W 2
M 88
WL 3
AN 120,122
AN 121,123
M 89
SH 122
SH 123
M 90
mw 3
AN 122,122
AN 123,123
M 91
WL 2
AN 124,126
AN 125,127
M 92
SH 126
SH 127
M 93
AN 126,126
AN 127,127
M 94
ad 84,2118
W 0
M 95
R 34
M 96
s_bitcmp1_b32 s50, 10
s_cselect_b32 s51, 24576, 0
M 97
W 1
v_add_u32 v47, s51, v3
M 98
XL 22,256
M 99
XL 24,1024
M 100
W 2
XL 26,1792
M 101
M 102
M 103
bc 49,23,66,60
M 104
mh 18
XL 28,2560
M 105
ad 66,4448
H 0
M 106
M 107
R 48
M 108
DG 1024
M 109
M 110
M 111
bc 48,23,64,56
M 112
mh 21
M 113
ad 64,4448
H 0
M 114
M 115
M 116
H 1
M 117
M 118
M 119
WL 2
R 50
M 120
H 2
M 121
M 132
M 133
TX 58,0
TX 62,13824
M 122
M 123
M 134
M 135
TY 66,512
TY 70,14336
M 124
M 125
M 136
M 137
TX 74,1536
TX 78,15360
M 126
WL 4
Q 0
M 127
Q 2
M 138
Q 4
M 139
TY 82,2048
TY 86,15872
M 128
Q 6
M 129
WL 4
Q 8
M 140
Q 10
M 141
TX 90,3072
TX 94,16896
M 130
Q 12
M 131
R 52
M 142
Q 14
M 143
TY 98,3584
TY 102,17408
M 0
R 54
M 1
WL 10
Q 16
M 12
Q 18
M 13
Q 20
M 2
Q 22
M 3
WL 8
Q 24
M 14
Q 26
M 15
Q 28
M 4
Q 30
M 5
WL 6
Q 32
M 16
Q 34
M 17
Q 36
M 6
Q 38
M 7
WL 2
Q 40
M 18
Q 42
M 19
Q 44
M 8
Q 46
M 9
UL 6,14
UH 7,14
M 20
UL 8,15
UH 9,15
M 21
AD 172,6
AD 196,7
M 10
AD 220,8
AD 244,9
M 11
R 56
M 22
UL 6,16
UH 7,16
M 23
R 58
M 24
UL 8,17
UH 9,17
M 25
AD 173,6
AD 197,7
)"
R"(M 26
AD 221,8
AD 245,9
M 27
UL 6,18
UH 7,18
M 28
UL 8,19
UH 9,19
M 29
AD 174,6
AD 198,7
M 30
AD 222,8
AD 246,9
M 31
WV 26
UL 6,20
UH 7,20
M 32
UL 8,21
UH 9,21
M 33
AD 175,6
AD 199,7
M 34
AD 223,8
AD 247,9
M 35
WL 2
M 36
M 37
M 38
M 39
M 40
M 41
M 42
M 43
WV 7
M 44
WL 0
M 45
s_barrier
R 72
M 46
mw 24
M 47
R 74
M 48
TY 106,5128
TY 110,18952
M 49
ad 84,2160
W 0
M 50
M 51
M 52
W 1
M 53
M 54
M 55
W 2
M 56
M 57
WL 1
AN 104,106
AN 105,107
M 58
mw 27
SH 106
SH 107
M 59
R 76
M 60
AN 106,106
AN 107,107
M 61
WL 2
AN 108,110
AN 109,111
M 62
SH 110
SH 111
M 63
AN 110,110
AN 111,111
M 64
TY 114,6664
TY 118,20488
M 65
ad 84,2166
M 66
W 0
M 67
M 68
M 69
W 1
M 70
M 71
R 78
M 72
W 2
M 73
WL 3
AN 112,114
AN 113,115
M 74
SH 114
SH 115
M 75
mw 48
AN 114,114
AN 115,115
M 76
WL 2
AN 116,118
AN 117,119
M 77
SH 118
SH 119
M 78
AN 118,118
AN 119,119
M 79
TY 122,8200
TY 126,22024
M 80
ad 84,2208
W 0
M 81
M 82
M 83
R 80
M 84
W 1
M 85
M 86
M 87
W 2
M 88
WL 3
AN 120,122
AN 121,123
M 89
SH 122
SH 123
M 90
mw 51
AN 122,122
AN 123,123
M 91
WL 2
AN 124,126
AN 125,127
M 92
SH 126
SH 127
M 93
AN 126,126
AN 127,127
M 94
ad 84,2214
W 0
M 95
R 82
M 96
ae 44,68,480
M 97
s_bfe_u32 s51, s50, 0x1000b
W 1
v_mad_i32_i24 v47, v49, s51, v48
M 98
XL 30,256
M 99
XL 32,1024
M 100
W 2
M 101
M 102
XL 34,1792
M 103
bc 49,0,66,60
M 104
mh 0
M 105
ad 66,64
H 0
M 106
M 107
R 0
M 108
DG 1024
M 109
XL 36,2560
M 110
M 111
bc 48,0,64,56
M 112
mh 3
M 113
ad 64,32
H 0
M 114
M 115
M 116
H 1
M 117
M 118
M 119
WL 2
R 2
M 120
H 2
M 121
M 132
M 133
PA 12288
TX 58,4608
TX 62,18432
DF
M 122
M 123
M 134
M 135
TY 66,5120
TY 70,18944
M 124
M 125
M 136
M 137
TX 74,6144
TX 78,19968
M 126
WL 4
Q 0
M 127
Q 2
M 138
Q 4
M 139
TY 82,6656
TY 86,20480
M 128
Q 6
M 129
WL 4
Q 8
M 140
Q 10
M 141
TX 90,7680
TX 94,21504
M 130
Q 12
M 131
R 4
M 142
Q 14
M 143
TY 98,8192
TY 102,22016
M 0
R 6
M 1
WL 10
Q 16
M 12
Q 18
M 13
Q 20
M 2
Q 22
M 3
WL 8
Q 24
M 14
Q 26
M 15
Q 28
M 4
Q 30
M 5
WL 6
Q 32
M 16
Q 34
M 17
Q 36
M 6
Q 38
M 7
WL 2
Q 40
M 18
Q 42
M 19
Q 44
M 8
Q 46
M 9
UL 6,22
UH 7,22
M 20
UL 8,23
UH 9,23
M 21
AD 176,6
AD 200,7
M 10
AD 224,8
AD 248,9
M 11
R 8
M 22
UL 6,24
UH 7,24
M 23
R 10
M 24
UL 8,25
UH 9,25
M 25
AD 177,6
AD 201,7
M 26
AD 225,8
AD 249,9
M 27
WV 27
UL 6,26
UH 7,26
M 28
UL 8,27
UH 9,27
M 29
AD 178,6
AD 202,7
M 30
AD 226,8
AD 250,9
M 31
UL 6,28
UH 7,28
M 32
UL 8,29
UH 9,29
M 33
AD 179,6
AD 203,7
M 34
AD 227,8
AD 251,9
M 35
WL 2
M 36
M 37
M 38
M 39
M 40
M 41
M 42
M 43
WV 8
M 44
WL 0
M 45
s_barrier
R 24
M 46
mw 72
M 47
R 26
M 48
TY 106,9736
TY 110,23560
M 49
ad 84,2256
W 0
M 50
M 51
M 52
W 1
M 53
M 54
M 55
W 2
M 56
M 57
WL 1
AN 104,106
AN 105,107
M 58
mw 75
SH 106
SH 107
M 59
R 28
M 60
AN 106,106
AN 107,107
)"
R"(M 61
WL 2
AN 108,110
AN 109,111
M 62
SH 110
SH 111
M 63
AN 110,110
AN 111,111
M 64
TY 114,11272
TY 118,25096
M 65
ad 84,2262
M 66
W 0
M 67
M 68
M 69
W 1
M 70
M 71
R 30
M 72
W 2
M 73
WL 3
AN 112,114
AN 113,115
M 74
SH 114
SH 115
M 75
mw 0
AN 114,114
AN 115,115
M 76
WL 2
AN 116,118
AN 117,119
M 77
SH 118
SH 119
M 78
AN 118,118
AN 119,119
M 79
TY 122,12808
TY 126,26632
M 80
ad 84,0
W 0
M 81
M 82
M 83
R 32
M 84
W 1
M 85
M 86
M 87
W 2
M 88
WL 3
AN 120,122
AN 121,123
M 89
SH 122
SH 123
M 90
mw 3
AN 122,122
AN 123,123
M 91
WL 2
AN 124,126
AN 125,127
M 92
SH 126
SH 127
M 93
AN 126,126
AN 127,127
M 94
ad 84,6
W 0
M 95
R 34
M 96
M 97
W 1
M 98
M 99
M 100
W 2
M 101
M 102
M 103
bc 49,1,66,60
M 104
mh 9
M 105
ad 66,256
H 0
M 106
M 107
R 48
M 108
DG 1024
M 109
M 110
M 111
bc 48,1,64,56
M 112
mh 12
M 113
ad 64,256
H 0
M 114
M 115
M 116
DG 1024
M 117
M 118
M 119
WL 2
R 50
M 120
mh 15
M 121
ad 64,227
H 0
M 132
M 133
TX 58,9216
TX 62,23040
M 122
DG 1024
M 123
M 134
M 135
TY 66,9728
TY 70,23552
M 124
M 125
M 136
M 137
TX 74,10752
TX 78,24576
M 126
WL 4
Q 0
M 127
Q 2
M 138
Q 4
M 139
TY 82,11264
TY 86,25088
M 128
Q 6
M 129
WL 4
Q 8
M 140
Q 10
M 141
TX 90,12288
TX 94,26112
M 130
Q 12
M 131
R 52
M 142
Q 14
M 143
TY 98,12800
TY 102,26624
M 0
R 54
M 1
WL 10
Q 16
M 12
Q 18
M 13
Q 20
M 2
Q 22
M 3
WL 8
Q 24
M 14
Q 26
M 15
Q 28
M 4
Q 30
M 5
WL 6
Q 32
M 16
Q 34
M 17
Q 36
M 6
Q 38
M 7
WL 2
Q 40
M 18
Q 42
M 19
Q 44
M 8
Q 46
M 9
UL 6,30
UH 7,30
M 20
UL 8,31
UH 9,31
M 21
AD 180,6
AD 204,7
M 10
AD 228,8
AD 252,9
M 11
R 56
M 22
UL 6,32
UH 7,32
M 23
R 58
M 24
UL 8,33
UH 9,33
M 25
AD 181,6
AD 205,7
M 26
AD 229,8
AD 253,9
BV 52,4608
M 27
WV 25
UL 6,34
UH 7,34
M 28
UL 8,35
UH 9,35
M 29
AD 182,6
AD 206,7
M 30
AD 230,8
AD 254,9
M 31
WV 22
UL 6,36
UH 7,36
M 32
UL 8,37
UH 9,37
M 33
AD 183,6
AD 207,7
M 34
AD 231,8
AD 255,9
M 35
WL 3
M 36
M 37
M 38
M 39
M 40
ds_read_b64 v[12:13], v4 offset:4864
ds_read_b64 v[46:47], v4 offset:4872
M 41
AR 6,36
AR 7,60
M 42
AR 8,84
AR 9,108
M 43
WL 2
EF 6,52
EF 7,53
M 44
WV 6
EF 8,54
EF 9,55
M 45
s_barrier
R 72
M 46
mw 24
CV 38,6,7
CV 39,8,9
M 47
R 74
M 48
TY 106,520
TY 110,14344
M 49
ad 84,48
W 0
M 50
AR 6,37
AR 7,61
M 51
AR 8,85
AR 9,109
M 52
W 1
EF 6,52
M 53
EF 7,53
EF 8,54
M 54
EF 9,55
CV 40,6,7
M 55
W 2
CV 41,8,9
M 56
AR 6,38
AR 7,62
M 57
WL 1
AN 104,106
AN 105,107
M 58
mw 27
SH 106
SH 107
M 59
R 76
M 60
AN 106,106
AN 107,107
M 61
WL 2
AN 108,110
AN 109,111
M 62
SH 110
SH 111
M 63
AN 110,110
AN 111,111
M 64
TY 114,2056
TY 118,15880
M 65
ad 84,54
M 66
W 0
AR 8,86
M 67
AR 9,110
EF 6,52
M 68
EF 7,53
EF 8,54
M 69
W 1
EF 9,55
M 70
CV 42,6,7
CV 43,8,9
M 71
R 78
M 72
W 2
AR 6,39
M 73
WL 3
AN 112,114
AN 113,115
M 74
SH 114
SH 115
M 75
mw 48
AN 114,114
AN 115,115
M 76
WL 2
AN 116,118
AN 117,119
M 77
SH 118
SH 119
M 78
AN 118,118
AN 119,119
)"
R"(M 79
TY 122,3592
TY 126,17416
M 80
ad 84,96
W 0
M 81
AR 7,63
AR 8,87
M 82
AR 9,111
EF 6,52
M 83
R 80
M 84
W 1
EF 7,53
M 85
EF 8,54
EF 9,55
M 86
CV 44,6,7
CV 45,8,9
M 87
W 2
ae 42,74,384
M 88
WL 3
AN 120,122
AN 121,123
M 89
SH 122
SH 123
M 90
mw 51
AN 122,122
AN 123,123
M 91
WL 2
AN 124,126
AN 125,127
M 92
SH 126
SH 127
M 93
AN 126,126
AN 127,127
M 94
ad 84,102
W 0
M 95
R 82
M 96
s_bitcmp1_b32 s50, 6
s_cselect_b32 s52, 24576, 0
v_add_u32 v50, s52, v3
M 97
W 1
ST 38,0
M 98
AR 6,40
AR 7,64
M 99
AR 8,88
AR 9,112
M 100
W 2
EF 6,52
M 101
EF 7,53
EF 8,54
M 102
EF 9,55
CV 14,6,7
M 103
bc 49,2,66,60
CV 15,8,9
M 104
mh 18
AR 6,41
AR 7,65
M 105
ad 66,448
H 0
M 106
AR 8,89
AR 9,113
M 107
R 0
M 108
DG 1024
M 109
ST 40,768
EF 6,52
M 110
EF 7,53
EF 8,54
M 111
bc 48,2,64,56
EF 9,55
M 112
mh 21
CV 16,6,7
CV 17,8,9
M 113
ad 64,448
H 0
M 114
AR 6,42
AR 7,66
M 115
AR 8,90
AR 9,114
M 116
H 1
EF 6,52
M 117
EF 7,53
EF 8,54
M 118
EF 9,55
CV 18,6,7
M 119
WL 0
R 2
M 120
H 2
CV 19,8,9
M 121
ST 42,1536
AR 6,43
M 132
AR 7,67
AR 8,91
M 133
TX 58,0
TX 62,13824
M 122
AR 9,115
EF 6,52
M 123
EF 7,53
EF 8,54
M 134
EF 9,55
CV 20,6,7
M 135
TY 66,512
TY 70,14336
M 124
CV 21,8,9
M 125
BV 52,5120
AR 6,132
M 136
AR 7,156
AR 8,180
M 137
TX 74,1536
TX 78,15360
M 126
WL 5
Q 0
M 127
Q 2
ST 44,2304
M 138
Q 4
M 139
TY 82,2048
TY 86,15872
M 128
Q 6
M 129
WL 5
Q 8
M 140
Q 10
M 141
TX 90,3072
TX 94,16896
M 130
Q 12
M 131
R 4
M 142
Q 14
M 143
TY 98,3584
TY 102,17408
v_mfma_f32_16x16x32_f16 a[36:39], v[56:59], a[0:3], a[32:35]
R 6
M 1
WL 10
Q 16
v_mfma_f32_16x16x32_f16 a[60:63], v[56:59], a[8:11], a[32:35]
Q 18
M 13
Q 20
v_mfma_f32_16x16x32_f16 a[40:43], v[64:67], a[0:3], a[32:35]
Q 22
M 3
WL 8
Q 24
v_mfma_f32_16x16x32_f16 a[64:67], v[64:67], a[8:11], a[32:35]
Q 26
M 15
Q 28
M 4
Q 30
M 5
WL 6
Q 32
M 16
Q 34
M 17
Q 36
M 6
Q 38
M 7
WL 2
Q 40
M 18
Q 42
M 19
Q 44
M 8
Q 46
M 9
ae 42,74,384
M 20
s_bfe_u32 s52, s50, 0x10007
v_mad_i32_i24 v50, v49, s52, v48
ST 14,0
M 21
EF 6,12
EF 7,13
M 10
EF 8,46
EF 136,47
M 11
R 8
M 22
CV 22,6,7
CV 23,8,136
M 23
R 10
v_mfma_f32_16x16x32_f16 a[84:87], v[56:59], a[16:19], a[32:35]
AR 6,133
AR 7,157
M 25
AR 8,181
EF 6,12
v_mfma_f32_16x16x32_f16 a[88:91], v[64:67], a[16:19], a[32:35]
EF 7,13
EF 8,46
M 27
EF 137,47
CV 24,6,7
M 28
CV 25,8,137
AR 6,134
M 29
AR 7,158
AR 8,182
M 30
ST 16,768
EF 6,12
M 31
EF 7,13
EF 8,46
M 32
EF 138,47
CV 26,6,7
M 33
CV 27,8,138
AR 6,135
M 34
AR 7,159
AR 8,183
M 35
WL 2
EF 6,12
EF 7,13
v_mfma_f32_16x16x32_f16 a[108:111], v[56:59], a[24:27], a[32:35]
EF 8,46
EF 139,47
M 37
CV 28,6,7
CV 29,8,139
v_mfma_f32_16x16x32_f16 a[112:115], v[64:67], a[24:27], a[32:35]
AR 6,136
AR 7,160
AR 8,184
EF 6,12
M 39
EF 7,13
EF 8,46
EF 140,47
CV 30,6,7
M 40
CV 31,8,140
AR 6,137
AR 7,161
AR 8,185
M 41
EF 6,12
EF 7,13
EF 8,46
EF 141,47
M 42
ST 18,1536
CV 32,6,7
CV 33,8,141
AR 6,138
M 43
WV 11
AR 7,162
AR 8,186
EF 6,12
EF 7,13
M 44
WL 0
EF 8,46
EF 142,47
CV 34,6,7
CV 35,8,142
M 45
s_barrier
R 24
AR 6,139
AR 7,163
M 46
mw 72
AR 8,187
EF 6,12
EF 7,13
EF 8,46
M 47
R 26
EF 143,47
CV 36,6,7
v_mfma_f32_16x16x32_f16 a[132:135], v[56:59], a[0:3], a[32:35]
TY 106,5128
TY 110,18952
CV 37,8,143
M 49
ad 84,144
W 0
ds_read_b64 v[12:13], v4 offset:4608
v_mfma_f32_16x16x32_f16 a[136:139], v[64:67], a[0:3], a[32:35]
ds_read_b64 v[46:47], v4 offset:4616
EF 160,52
EF 184,53
M 51
EF 208,54
EF 232,55
CV 38,160,184
M 52
W 1
CV 39,208,232
EF 161,52
M 53
EF 185,53
EF 209,54
EF 233,55
M 54
ST 20,2304
CV 40,161,185
CV 41,209,233
M 55
W 2
EF 162,52
EF 186,53
M 56
EF 210,54
EF 234,55
CV 42,162,186
M 57
WL 3
AN 104,106
AN 105,107
CV 43,210,234
M 58
mw 75
SH 106
SH 107
EF 163,52
M 59
R 28
EF 187,53
v_mfma_f32_16x16x32_f16 a[156:159], v[56:59], a[8:11], a[32:35]
AN 106,106
AN 107,107
EF 211,54
M 61
WL 2
AN 108,110
AN 109,111
EF 235,55
)"
R"(v_mfma_f32_16x16x32_f16 a[160:163], v[64:67], a[8:11], a[32:35]
SH 110
SH 111
CV 44,163,187
M 63
AN 110,110
AN 111,111
CV 45,211,235
M 64
TY 114,6664
TY 118,20488
EF 164,52
M 65
ad 84,150
EF 188,53
M 66
W 0
EF 212,54
EF 236,55
M 67
ae 42,74,384
CV 14,164,188
CV 15,212,236
M 68
s_bitcmp1_b32 s50, 6
s_cselect_b32 s52, 24576, 0
v_add_u32 v50, s52, v3
ST 22,128
M 69
W 1
EF 165,52
EF 189,53
M 70
EF 213,54
EF 237,55
CV 16,165,189
M 71
R 30
CV 17,213,237
v_mfma_f32_16x16x32_f16 a[180:183], v[56:59], a[16:19], a[32:35]
W 2
EF 166,52
EF 190,53
M 73
WL 3
AN 112,114
AN 113,115
EF 214,54
v_mfma_f32_16x16x32_f16 a[184:187], v[64:67], a[16:19], a[32:35]
SH 114
SH 115
EF 238,55
M 75
mw 0
AN 114,114
AN 115,115
CV 18,166,190
M 76
WL 2
AN 116,118
AN 117,119
CV 19,214,238
M 77
SH 118
SH 119
EF 167,52
M 78
AN 118,118
AN 119,119
EF 191,53
M 79
TY 122,8200
TY 126,22024
EF 215,54
M 80
ad 84,192
W 0
ST 24,896
M 81
EF 239,55
CV 20,167,191
CV 21,215,239
M 82
BV 52,4864
AR 6,44
M 83
R 32
v_mfma_f32_16x16x32_f16 v[136:139], v[56:59], a[24:27], v[128:131]
W 1
AR 7,68
M 85
AR 8,92
AR 9,116
v_mfma_f32_16x16x32_f16 v[140:143], v[64:67], a[24:27], v[128:131]
EF 6,12
EF 7,13
M 87
W 2
EF 8,46
M 88
WL 4
AN 120,122
AN 121,123
M 89
SH 122
SH 123
M 90
mw 3
AN 122,122
AN 123,123
M 91
WL 3
AN 124,126
AN 125,127
M 92
SH 126
SH 127
ST 26,1664
M 93
AN 126,126
AN 127,127
M 94
ad 84,198
W 0
M 95
R 34
v_mfma_f32_16x16x32_f16 v[160:163], v[56:59], a[0:3], v[128:131]
EF 9,47
CV 10,6,7
M 97
W 1
CV 11,8,9
v_mfma_f32_16x16x32_f16 v[164:167], v[64:67], a[0:3], v[128:131]
AW 200,10
AW 201,11
M 99
AR 6,45
AR 7,69
M 100
W 2
AR 8,93
M 101
AR 9,117
EF 6,12
M 102
EF 7,13
EF 8,46
M 103
bc 49,3,66,60
EF 9,47
M 104
mh 0
ST 28,2432
CV 10,6,7
M 105
ad 66,640
H 0
M 106
CV 11,8,9
AW 202,10
M 107
R 48
v_mfma_f32_16x16x32_f16 v[184:187], v[56:59], a[8:11], v[128:131]
DG 1024
M 109
AW 203,11
AR 6,46
v_mfma_f32_16x16x32_f16 v[188:191], v[64:67], a[8:11], v[128:131]
AR 7,70
AR 8,94
M 111
bc 48,3,64,56
AR 9,118
M 112
mh 3
EF 6,12
EF 7,13
M 113
ad 64,640
H 0
M 114
EF 8,46
EF 9,47
M 115
CV 10,6,7
CV 11,8,9
M 116
H 1
AW 204,10
M 117
ae 42,74,384
AW 205,11
M 118
s_bfe_u32 s52, s50, 0x10007
v_mad_i32_i24 v50, v49, s52, v48
ST 30,128
M 119
WL 0
R 50
v_mfma_f32_16x16x32_f16 v[208:211], v[56:59], a[16:19], v[128:131]
H 2
AR 6,47
M 121
AR 7,71
AR 8,95
v_mfma_f32_16x16x32_f16 v[232:235], v[56:59], a[24:27], v[128:131]
AR 9,119
EF 6,12
M 133
PA 12288
TX 58,4608
TX 62,18432
DF
v_mfma_f32_16x16x32_f16 v[212:215], v[64:67], a[16:19], v[128:131]
EF 7,13
EF 8,46
M 123
EF 9,47
CV 10,6,7
v_mfma_f32_16x16x32_f16 v[236:239], v[64:67], a[24:27], v[128:131]
CV 11,8,9
AW 206,10
M 135
TY 66,5120
TY 70,18944
M 124
AW 207,11
M 125
AR 6,48
AR 7,72
AR 8,96
AR 9,120
EF 6,12
EF 7,13
M 136
ST 32,896
EF 8,46
EF 9,47
CV 22,6,7
CV 23,8,9
AR 6,49
M 137
TX 74,6144
TX 78,19968
AR 7,73
AR 8,97
AR 9,121
EF 6,12
M 126
WL 4
Q 0
EF 7,13
EF 8,46
EF 9,47
CV 24,6,7
M 127
Q 2
CV 25,8,9
AR 6,50
AR 7,74
AR 8,98
M 138
Q 4
AR 9,122
EF 6,12
EF 7,13
EF 8,46
M 139
TY 82,6656
TY 86,20480
EF 9,47
CV 26,6,7
CV 27,8,9
AR 6,51
M 128
Q 6
AR 7,75
AR 8,99
AR 9,123
EF 6,12
M 129
WL 2
Q 8
EF 7,13
EF 8,46
EF 9,47
CV 28,6,7
M 140
Q 10
CV 29,8,9
M 141
TX 90,7680
TX 94,21504
ds_read_b64 v[12:13], v4 offset:5120
M 130
Q 12
ds_read_b64 v[46:47], v4 offset:5128
M 131
R 52
M 142
Q 14
ST 34,1664
M 143
TY 98,8192
TY 102,22016
M 0
R 54
M 1
Q 16
M 12
Q 18
M 13
Q 20
M 2
Q 22
M 3
WL 10
Q 24
M 14
Q 26
M 15
Q 28
v_mfma_f32_16x16x32_f16 a[44:47], v[72:75], a[0:3], a[32:35]
Q 30
M 5
WL 6
Q 32
v_mfma_f32_16x16x32_f16 a[68:71], v[72:75], a[8:11], a[32:35]
Q 34
ST 36,2432
M 17
Q 36
v_mfma_f32_16x16x32_f16 a[48:51], v[80:83], a[0:3], a[32:35]
Q 38
M 7
WL 1
Q 40
v_mfma_f32_16x16x32_f16 a[72:75], v[80:83], a[8:11], a[32:35]
Q 42
M 19
Q 44
M 8
Q 46
M 9
AR 6,140
AR 7,164
M 20
AR 8,188
EF 6,52
M 21
EF 7,53
EF 8,54
M 10
EF 144,55
CV 10,6,7
M 11
R 56
M 22
ae 42,74,384
CV 11,8,144
M 23
R 58
M 24
s_bitcmp1_b32 s50, 6
s_cselect_b32 s52, 24576, 0
v_add_u32 v50, s52, v3
ST 38,256
M 25
AW 208,10
AW 209,11
M 26
AR 6,141
AR 7,165
M 27
AR 8,189
EF 6,52
v_mfma_f32_16x16x32_f16 a[92:95], v[72:75], a[16:19], a[32:35]
EF 7,53
EF 8,54
M 29
EF 145,55
CV 10,6,7
v_mfma_f32_16x16x32_f16 a[96:99], v[80:83], a[16:19], a[32:35]
CV 11,8,145
AW 210,10
M 31
AW 211,11
AR 6,142
M 32
AR 7,166
AR 8,190
M 33
EF 6,52
EF 7,53
M 34
EF 8,54
EF 146,55
M 35
WL 0
CV 10,6,7
)"
R"(CV 11,8,146
M 36
ST 40,1024
AW 212,10
M 37
AW 213,11
AR 6,143
M 38
AR 7,167
AR 8,191
M 39
EF 6,52
EF 7,53
v_mfma_f32_16x16x32_f16 a[116:119], v[72:75], a[24:27], a[32:35]
EF 8,54
EF 147,55
M 41
CV 10,6,7
CV 11,8,147
v_mfma_f32_16x16x32_f16 a[120:123], v[80:83], a[24:27], a[32:35]
AW 214,10
AW 215,11
M 43
AR 6,144
AR 7,168
AR 8,192
EF 6,52
EF 7,53
EF 8,54
M 44
WV 13
EF 148,55
CV 30,6,7
CV 31,8,148
AR 6,145
AR 7,169
AR 8,193
M 45
s_barrier
R 72
EF 6,52
EF 7,53
EF 8,54
EF 149,55
M 46
mw 24
CV 32,6,7
CV 33,8,149
AR 6,146
AR 7,170
AR 8,194
EF 6,52
M 47
R 74
EF 7,53
EF 8,54
EF 150,55
CV 34,6,7
M 48
TY 106,9736
TY 110,23560
ST 42,1792
CV 35,8,150
AR 6,147
AR 7,171
M 49
ad 84,240
W 0
AR 8,195
EF 6,52
EF 7,53
EF 8,54
M 50
EF 151,55
CV 36,6,7
CV 37,8,151
M 51
BV 52,4608
EF 168,12
EF 192,13
v_mfma_f32_16x16x32_f16 a[140:143], v[72:75], a[0:3], a[32:35]
W 1
EF 216,46
EF 240,47
M 53
CV 10,168,192
CV 11,216,240
AW 216,10
v_mfma_f32_16x16x32_f16 a[144:147], v[80:83], a[0:3], a[32:35]
AW 217,11
EF 169,12
EF 193,13
M 55
W 2
EF 217,46
EF 241,47
M 56
CV 10,169,193
CV 11,217,241
AW 218,10
M 57
WL 1
AN 104,106
AN 105,107
AW 219,11
M 58
mw 27
SH 106
SH 107
EF 170,12
M 59
R 76
EF 194,13
M 60
AN 106,106
AN 107,107
ST 44,2560
M 61
AN 108,110
AN 109,111
EF 218,46
M 62
SH 110
SH 111
EF 242,47
M 63
AN 110,110
AN 111,111
CV 10,170,194
v_mfma_f32_16x16x32_f16 a[164:167], v[72:75], a[8:11], a[32:35]
TY 114,11272
TY 118,25096
CV 11,218,242
M 65
ad 84,246
AW 220,10
v_mfma_f32_16x16x32_f16 a[168:171], v[80:83], a[8:11], a[32:35]
W 0
AW 221,11
EF 171,12
M 67
EF 195,13
EF 219,46
EF 243,47
M 68
CV 10,171,195
CV 11,219,243
AW 222,10
M 69
W 1
AW 223,11
M 70
EF 172,12
EF 196,13
EF 220,46
M 71
R 78
EF 244,47
M 72
WL 3
W 2
ae 42,74,384
M 73
s_bfe_u32 s52, s50, 0x10007
AN 112,114
AN 113,115
v_mad_i32_i24 v50, v49, s52, v48
M 74
SH 114
SH 115
ST 14,256
M 75
mw 48
AN 114,114
AN 115,115
CV 38,172,196
v_mfma_f32_16x16x32_f16 a[188:191], v[72:75], a[16:19], a[32:35]
WL 2
AN 116,118
AN 117,119
CV 39,220,244
M 77
SH 118
SH 119
EF 173,12
v_mfma_f32_16x16x32_f16 a[192:195], v[80:83], a[16:19], a[32:35]
AN 118,118
AN 119,119
EF 197,13
M 79
TY 122,12808
TY 126,26632
EF 221,46
M 80
ad 84,288
W 0
EF 245,47
M 81
CV 40,173,197
CV 41,221,245
EF 174,12
M 82
EF 198,13
EF 222,46
EF 246,47
M 83
R 80
CV 42,174,198
M 84
W 1
CV 43,222,246
EF 175,12
M 85
EF 199,13
EF 223,46
EF 247,47
M 86
ST 16,1024
CV 44,175,199
CV 45,223,247
M 87
W 2
ds_read_b64 v[12:13], v4 offset:4864
ds_read_b64 v[46:47], v4 offset:4872
v_mfma_f32_16x16x32_f16 v[144:147], v[72:75], a[24:27], v[128:131]
WL 5
AN 120,122
AN 121,123
AR 6,52
M 89
SH 122
SH 123
AR 7,76
v_mfma_f32_16x16x32_f16 v[148:151], v[80:83], a[24:27], v[128:131]
mw 51
AN 122,122
AN 123,123
AR 8,100
M 91
WL 4
AN 124,126
AN 125,127
AR 9,124
M 92
SH 126
SH 127
EF 6,52
M 93
AN 126,126
AN 127,127
EF 7,53
M 94
ad 84,294
W 0
EF 8,54
M 95
R 82
EF 9,55
M 96
CV 10,6,7
CV 11,8,9
AW 224,10
M 97
W 1
AW 225,11
AR 6,53
M 98
ST 18,1792
AR 7,77
AR 8,101
M 99
AR 9,125
EF 6,52
EF 7,53
v_mfma_f32_16x16x32_f16 v[168:171], v[72:75], a[0:3], v[128:131]
W 2
EF 8,54
EF 9,55
M 101
CV 10,6,7
CV 11,8,9
AW 226,10
v_mfma_f32_16x16x32_f16 v[172:175], v[80:83], a[0:3], v[128:131]
AW 227,11
AR 6,54
AR 7,78
M 103
bc 49,4,66,60
AR 8,102
AR 9,126
M 104
mh 9
EF 6,52
EF 7,53
EF 8,54
M 105
ad 66,832
H 0
EF 9,55
M 106
CV 10,6,7
CV 11,8,9
AW 228,10
M 107
R 0
AW 229,11
M 108
DG 1024
AR 6,55
M 109
AR 7,79
AR 8,103
AR 9,127
M 110
ST 20,2560
EF 6,52
EF 7,53
M 111
bc 48,4,64,56
EF 8,54
EF 9,55
v_mfma_f32_16x16x32_f16 v[192:195], v[72:75], a[8:11], v[128:131]
mh 12
CV 10,6,7
CV 11,8,9
AW 230,10
M 113
ad 64,832
H 0
AW 231,11
v_mfma_f32_16x16x32_f16 v[196:199], v[80:83], a[8:11], v[128:131]
AR 6,56
AR 7,80
AR 8,104
M 115
AR 9,128
EF 6,52
EF 7,53
M 116
H 1
EF 8,54
EF 9,55
M 117
CV 14,6,7
CV 15,8,9
AR 6,57
M 118
AR 7,81
AR 8,105
AR 9,129
M 119
WL 0
R 2
EF 6,52
M 120
H 2
EF 7,53
EF 8,54
M 121
EF 9,55
CV 16,6,7
CV 17,8,9
M 132
ae 42,72,384
AR 6,58
AR 7,82
M 133
s_bfe_u32 s52, s50, 0x10009
TX 58,9216
TX 62,23040
v_mad_i32_i24 v50, v49, s52, v48
M 122
ST 22,0
AR 8,106
AR 9,130
M 123
EF 6,52
EF 7,53
EF 8,54
M 134
EF 9,55
CV 18,6,7
CV 19,8,9
M 135
TY 66,9728
TY 70,23552
AR 6,59
v_mfma_f32_16x16x32_f16 v[216:219], v[72:75], a[16:19], v[128:131]
AR 7,83
AR 8,107
AR 9,131
M 125
EF 6,52
EF 7,53
EF 8,54
v_mfma_f32_16x16x32_f16 v[240:243], v[72:75], a[24:27], v[128:131]
EF 9,55
CV 20,6,7
CV 21,8,9
M 137
)"
R"(TX 74,10752
TX 78,24576
BV 52,5120
v_mfma_f32_16x16x32_f16 v[220:223], v[80:83], a[16:19], v[128:131]
WL 5
Q 0
M 127
Q 2
v_mfma_f32_16x16x32_f16 v[244:247], v[80:83], a[24:27], v[128:131]
Q 4
M 139
TY 82,11264
TY 86,25088
M 128
Q 6
ST 24,768
M 129
WL 5
Q 8
M 140
Q 10
M 141
TX 90,12288
TX 94,26112
M 130
Q 12
M 131
R 4
M 142
Q 14
M 143
TY 98,12800
TY 102,26624
M 0
R 6
M 1
WL 10
Q 16
M 12
Q 18
M 13
Q 20
M 2
Q 22
ST 26,1536
M 3
WL 8
Q 24
M 14
Q 26
M 15
Q 28
M 4
Q 30
M 5
WL 6
Q 32
M 16
Q 34
M 17
Q 36
M 6
Q 38
M 7
WL 1
Q 40
M 18
Q 42
M 19
Q 44
v_mfma_f32_16x16x32_f16 a[52:55], v[88:91], a[0:3], a[32:35]
Q 46
ST 28,2304
M 9
AR 6,148
AR 7,172
v_mfma_f32_16x16x32_f16 a[76:79], v[88:91], a[8:11], a[32:35]
AR 8,196
EF 6,12
M 21
EF 7,13
EF 8,46
v_mfma_f32_16x16x32_f16 a[56:59], v[96:99], a[0:3], a[32:35]
EF 152,47
CV 10,6,7
M 11
R 8
v_mfma_f32_16x16x32_f16 a[80:83], v[96:99], a[8:11], a[32:35]
CV 11,8,152
AW 232,10
M 23
R 10
M 24
AW 233,11
AR 6,149
M 25
AR 7,173
AR 8,197
M 26
EF 6,12
EF 7,13
M 27
EF 8,46
EF 153,47
M 28
ae 42,72,384
ST 30,128
M 29
CV 10,6,7
CV 11,8,153
M 30
AW 234,10
AW 235,11
M 31
AR 6,150
AR 7,174
v_mfma_f32_16x16x32_f16 a[100:103], v[88:91], a[16:19], a[32:35]
AR 8,198
EF 6,12
M 33
EF 7,13
EF 8,46
v_mfma_f32_16x16x32_f16 a[104:107], v[96:99], a[16:19], a[32:35]
EF 154,47
CV 10,6,7
M 35
WL 1
CV 11,8,154
AW 236,10
M 36
AW 237,11
AR 6,151
M 37
AR 7,175
AR 8,199
M 38
EF 6,12
EF 7,13
M 39
EF 8,46
EF 155,47
M 40
ST 32,896
CV 10,6,7
M 41
CV 11,8,155
AW 238,10
M 42
AW 239,11
M 43
WV 12
AR 6,152
AR 7,176
EF 6,12
EF 7,13
v_mfma_f32_16x16x32_f16 a[124:127], v[88:91], a[24:27], a[32:35]
WL 0
EF 132,46
EF 156,47
CV 22,6,7
CV 23,132,156
M 45
s_barrier
R 24
AR 6,153
AR 7,177
v_mfma_f32_16x16x32_f16 a[128:131], v[96:99], a[24:27], a[32:35]
mw 72
EF 6,12
EF 7,13
EF 133,46
EF 157,47
M 47
R 26
CV 24,6,7
CV 25,133,157
M 48
TY 106,520
TY 110,14344
AR 6,154
AR 7,178
M 49
ad 84,336
W 0
EF 6,12
EF 7,13
M 50
EF 134,46
EF 158,47
CV 26,6,7
CV 27,134,158
M 51
AR 6,155
AR 7,179
EF 6,12
EF 7,13
M 52
W 1
ST 34,1664
EF 135,46
EF 159,47
M 53
CV 28,6,7
CV 29,135,159
M 54
EF 176,52
EF 200,53
EF 224,54
M 55
W 2
EF 248,55
CV 10,176,200
v_mfma_f32_16x16x32_f16 a[148:151], v[88:91], a[0:3], a[32:35]
CV 11,224,248
AW 240,10
AW 241,11
M 57
WL 0
AN 104,106
AN 105,107
EF 177,52
v_mfma_f32_16x16x32_f16 a[152:155], v[96:99], a[0:3], a[32:35]
mw 75
SH 106
SH 107
EF 201,53
M 59
R 28
EF 225,54
M 60
AN 106,106
AN 107,107
EF 249,55
M 61
AN 108,110
AN 109,111
CV 10,177,201
M 62
SH 110
SH 111
CV 11,225,249
M 63
AN 110,110
AN 111,111
AW 242,10
M 64
TY 114,2056
TY 118,15880
ST 36,2432
M 65
ad 84,342
AW 243,11
M 66
W 0
EF 178,52
EF 202,53
M 67
EF 226,54
EF 250,55
CV 10,178,202
v_mfma_f32_16x16x32_f16 a[172:175], v[88:91], a[8:11], a[32:35]
CV 11,226,250
AW 244,10
AW 245,11
M 69
W 1
EF 179,52
EF 203,53
v_mfma_f32_16x16x32_f16 a[176:179], v[96:99], a[8:11], a[32:35]
EF 227,54
EF 251,55
CV 10,179,203
M 71
R 30
CV 11,227,251
M 72
W 2
AW 246,10
AW 247,11
M 73
WL 3
AN 112,114
AN 113,115
EF 180,52
M 74
SH 114
SH 115
EF 204,53
M 75
mw 0
AN 114,114
AN 115,115
EF 228,54
M 76
WL 2
AN 116,118
AN 117,119
EF 252,55
M 77
SH 118
SH 119
CV 10,180,204
M 78
AN 118,118
AN 119,119
CV 11,228,252
M 79
TY 122,3592
TY 126,17416
AW 248,10
v_mfma_f32_16x16x32_f16 a[196:199], v[88:91], a[16:19], a[32:35]
ad 84,384
W 0
AW 249,11
M 81
ae 42,72,384
ST 38,256
EF 181,52
v_mfma_f32_16x16x32_f16 v[132:135], v[96:99], a[16:19], v[128:131]
EF 205,53
EF 229,54
EF 253,55
M 83
R 32
CV 10,181,205
M 84
W 1
CV 11,229,253
AW 250,10
M 85
AW 251,11
EF 182,52
EF 206,53
M 86
EF 230,54
EF 254,55
CV 10,182,206
M 87
W 2
CV 11,230,254
AW 252,10
M 88
WL 3
AN 120,122
AN 121,123
AW 253,11
M 89
SH 122
SH 123
EF 183,52
M 90
mw 3
AN 122,122
AN 123,123
EF 207,53
M 91
WL 2
AN 124,126
AN 125,127
EF 231,54
v_mfma_f32_16x16x32_f16 v[152:155], v[88:91], a[24:27], v[128:131]
SH 126
SH 127
EF 255,55
M 93
AN 126,126
AN 127,127
ST 40,1024
v_mfma_f32_16x16x32_f16 v[156:159], v[96:99], a[24:27], v[128:131]
ad 84,390
W 0
CV 10,183,207
M 95
R 34
CV 11,231,255
M 96
AW 254,10
AW 255,11
M 97
W 1
M 98
M 99
M 100
W 2
M 101
M 102
M 103
bc 49,5,66,60
v_mfma_f32_16x16x32_f16 v[176:179], v[88:91], a[0:3], v[128:131]
mh 18
M 105
ad 66,1024
H 0
ST 42,1792
v_mfma_f32_16x16x32_f16 v[180:183], v[96:99], a[0:3], v[128:131]
M 107
R 48
M 108
DG 1024
M 109
)"
R"(M 110
M 111
bc 48,5,64,56
M 112
mh 21
M 113
ad 64,1024
H 0
M 114
M 115
v_mfma_f32_16x16x32_f16 v[200:203], v[88:91], a[8:11], v[128:131]
H 1
M 117
ST 44,2560
v_mfma_f32_16x16x32_f16 v[204:207], v[96:99], a[8:11], v[128:131]
M 119
WL 2
R 50
M 120
H 2
M 121
M 132
M 133
TX 58,0
TX 62,13824
M 122
M 123
M 134
M 135
TY 66,512
TY 70,14336
M 124
M 125
ae 42,72,480
M 136
s_bfe_u32 s52, s50, 0x1000b
v_mad_i32_i24 v50, v49, s52, v48
ST 14,0
M 137
TX 74,1536
TX 78,15360
M 126
WL 4
Q 0
M 127
Q 2
M 138
Q 4
M 139
TY 82,2048
TY 86,15872
v_mfma_f32_16x16x32_f16 v[224:227], v[88:91], a[16:19], v[128:131]
Q 6
M 129
WL 4
Q 8
v_mfma_f32_16x16x32_f16 v[248:251], v[88:91], a[24:27], v[128:131]
Q 10
M 141
TX 90,3072
TX 94,16896
v_mfma_f32_16x16x32_f16 v[228:231], v[96:99], a[16:19], v[128:131]
Q 12
M 131
R 52
v_mfma_f32_16x16x32_f16 v[252:255], v[96:99], a[24:27], v[128:131]
Q 14
ST 16,768
M 143
TY 98,3584
TY 102,17408
M 0
R 54
M 1
WL 10
Q 16
M 12
Q 18
M 13
Q 20
M 2
Q 22
M 3
WL 8
Q 24
M 14
Q 26
M 15
Q 28
M 4
Q 30
M 5
WL 6
Q 32
M 16
Q 34
ST 18,1536
M 17
Q 36
M 6
Q 38
M 7
WL 1
Q 40
M 18
Q 42
M 19
Q 44
M 8
Q 46
M 9
M 20
M 21
M 10
M 11
R 56
M 22
ST 20,2304
M 23
R 58
M 24
M 25
M 26
M 27
M 28
M 29
M 30
M 31
M 32
M 33
M 34
ae 42,72,480
ST 22,128
M 35
WL 4
M 36
M 37
M 38
M 39
M 40
M 41
M 42
M 43
WV 12
M 44
WL 0
M 45
s_barrier
R 72
M 46
mw 24
ST 24,896
M 47
R 74
M 48
TY 106,5128
TY 110,18952
M 49
ad 84,432
W 0
M 50
M 51
M 52
W 1
M 53
M 54
M 55
W 2
M 56
M 57
WL 1
AN 104,106
AN 105,107
M 58
mw 27
SH 106
SH 107
ST 26,1664
M 59
R 76
M 60
AN 106,106
AN 107,107
M 61
WL 2
AN 108,110
AN 109,111
M 62
SH 110
SH 111
M 63
AN 110,110
AN 111,111
M 64
TY 114,6664
TY 118,20488
M 65
ad 84,438
M 66
W 0
M 67
M 68
M 69
W 1
M 70
ST 28,2432
M 71
R 78
M 72
W 2
M 73
WL 3
AN 112,114
AN 113,115
M 74
SH 114
SH 115
M 75
mw 48
AN 114,114
AN 115,115
M 76
WL 2
AN 116,118
AN 117,119
M 77
SH 118
SH 119
M 78
AN 118,118
AN 119,119
M 79
TY 122,8200
TY 126,22024
M 80
ad 84,480
W 0
M 81
M 82
M 83
R 80
M 84
W 1
M 85
M 86
M 87
W 2
M 88
WL 3
AN 120,122
AN 121,123
M 89
SH 122
SH 123
M 90
mw 51
AN 122,122
AN 123,123
M 91
WL 2
AN 124,126
AN 125,127
M 92
SH 126
SH 127
M 93
AN 126,126
AN 127,127
M 94
ad 84,486
W 0
M 95
R 82
M 96
M 97
W 1
M 98
M 99
M 100
W 2
M 101
M 102
M 103
bc 49,6,66,60
M 104
mh 0
M 105
ad 66,1216
H 0
M 106
M 107
R 0
M 108
DG 1024
M 109
M 110
M 111
bc 48,6,64,56
M 112
mh 3
M 113
ad 64,1216
H 0
M 114
M 115
M 116
H 1
M 117
M 118
M 119
WL 2
R 2
M 120
H 2
M 121
M 132
M 133
PA 12288
TX 58,4608
TX 62,18432
DF
M 122
M 123
M 134
M 135
TY 66,5120
TY 70,18944
M 124
ae 44,70,768
M 125
s_bitcmp1_b32 s50, 12
s_cselect_b32 s51, 24576, 0
v_add_u32 v47, s51, v3
M 136
XL 30,0
M 137
TX 74,6144
TX 78,19968
M 126
WL 4
Q 0
M 127
Q 2
M 138
Q 4
M 139
TY 82,6656
TY 86,20480
M 128
Q 6
M 129
WL 4
Q 8
M 140
Q 10
M 141
TX 90,7680
TX 94,21504
M 130
Q 12
M 131
R 4
M 142
Q 14
M 143
TY 98,8192
TY 102,22016
M 0
R 6
M 1
WL 10
Q 16
M 12
Q 18
M 13
Q 20
M 2
Q 22
M 3
WL 8
Q 24
M 14
Q 26
M 15
Q 28
M 4
Q 30
M 5
WL 6
Q 32
M 16
Q 34
M 17
Q 36
M 6
Q 38
M 7
WL 2
Q 40
M 18
)"
R"(Q 42
M 19
Q 44
M 8
Q 46
M 9
ae 42,72,384
M 20
s_bitcmp1_b32 s50, 8
s_cselect_b32 s52, 24576, 0
v_add_u32 v50, s52, v3
M 21
SA 200,0
XL 32,768
M 10
XL 34,1536
M 11
R 8
M 22
XL 36,2304
M 23
R 10
M 24
M 25
M 26
M 27
M 28
M 29
M 30
M 31
M 32
M 33
M 34
M 35
WL 4
M 36
M 37
M 38
M 39
M 40
M 41
M 42
M 43
WV 11
M 44
WL 0
M 45
s_barrier
R 24
M 46
mw 72
M 47
R 26
M 48
TY 106,9736
TY 110,23560
M 49
ad 84,528
W 0
M 50
M 51
M 52
W 1
M 53
M 54
M 55
W 2
M 56
M 57
WL 1
AN 104,106
AN 105,107
M 58
mw 75
SH 106
SH 107
M 59
R 28
M 60
AN 106,106
AN 107,107
M 61
WL 2
AN 108,110
AN 109,111
M 62
SH 110
SH 111
M 63
AN 110,110
AN 111,111
M 64
TY 114,11272
TY 118,25096
M 65
ad 84,534
M 66
W 0
M 67
M 68
M 69
W 1
M 70
M 71
R 30
M 72
W 2
M 73
WL 3
AN 112,114
AN 113,115
M 74
SH 114
SH 115
M 75
mw 0
AN 114,114
AN 115,115
M 76
WL 2
AN 116,118
AN 117,119
M 77
SH 118
SH 119
M 78
AN 118,118
AN 119,119
M 79
TY 122,12808
TY 126,26632
M 80
ad 84,576
W 0
M 81
M 82
M 83
R 32
M 84
W 1
SA 202,768
M 85
M 86
M 87
W 2
M 88
WL 3
AN 120,122
AN 121,123
M 89
SH 122
SH 123
M 90
mw 3
AN 122,122
AN 123,123
M 91
WL 2
AN 124,126
AN 125,127
M 92
SH 126
SH 127
M 93
AN 126,126
AN 127,127
M 94
ad 84,582
W 0
M 95
R 34
M 96
M 97
W 1
M 98
M 99
M 100
W 2
M 101
M 102
M 103
bc 49,7,66,60
M 104
mh 9
M 105
ad 66,1408
H 0
M 106
M 107
R 48
M 108
DG 1024
M 109
M 110
M 111
bc 48,7,64,56
M 112
mh 12
M 113
ad 64,1408
H 0
M 114
M 115
M 116
H 1
M 117
M 118
M 119
WL 2
R 50
M 120
H 2
M 121
M 132
M 133
TX 58,9216
TX 62,23040
M 122
M 123
M 134
M 135
TY 66,9728
TY 70,23552
M 124
M 125
M 136
ae 44,70,768
M 137
TX 74,10752
TX 78,24576
M 126
s_bfe_u32 s51, s50, 0x1000d
WL 4
Q 0
M 127
Q 2
M 138
Q 4
M 139
TY 82,11264
TY 86,25088
M 128
Q 6
M 129
WL 4
Q 8
M 140
Q 10
M 141
TX 90,12288
TX 94,26112
M 130
Q 12
M 131
R 52
M 142
Q 14
M 143
TY 98,12800
TY 102,26624
M 0
R 54
M 1
WL 10
Q 16
M 12
Q 18
M 13
Q 20
M 2
Q 22
M 3
WL 8
Q 24
M 14
Q 26
M 15
Q 28
M 4
Q 30
M 5
WL 6
Q 32
M 16
Q 34
M 17
Q 36
M 6
Q 38
M 7
WL 2
Q 40
M 18
Q 42
M 19
Q 44
M 8
Q 46
M 9
SA 204,1536
v_mad_i32_i24 v47, v49, s51, v48
M 20
XL 38,0
M 21
XL 40,768
M 10
XL 42,1536
M 11
R 56
M 22
XL 44,2304
M 23
R 58
M 24
M 25
M 26
M 27
M 28
M 29
M 30
M 31
M 32
M 33
M 34
M 35
WL 4
M 36
M 37
M 38
M 39
M 40
WV 27
UL 6,30
UH 7,30
M 41
UL 8,31
UH 9,31
M 42
AR 10,36
AR 11,60
M 43
WV 10
AR 12,84
AR 13,108
M 44
WL 0
AD 10,6
AD 11,7
M 45
s_barrier
R 72
M 46
mw 24
AD 12,8
AD 13,9
M 47
R 74
M 48
TY 106,520
TY 110,14344
M 49
ad 84,624
W 0
M 50
AW 36,10
AW 60,11
M 51
AW 84,12
AW 108,13
M 52
W 1
UL 6,32
M 53
UH 7,32
UL 8,33
M 54
UH 9,33
AR 10,37
M 55
W 2
AR 11,61
M 56
AR 12,85
AR 13,109
M 57
WL 1
AN 104,106
AN 105,107
M 58
mw 27
SH 106
SH 107
M 59
R 76
M 60
AN 106,106
AN 107,107
M 61
WL 2
AN 108,110
)"
R"(AN 109,111
M 62
SH 110
SH 111
M 63
AN 110,110
AN 111,111
M 64
TY 114,2056
TY 118,15880
M 65
ad 84,630
M 66
W 0
AD 10,6
M 67
AD 11,7
AD 12,8
M 68
AD 13,9
AW 37,10
M 69
W 1
AW 61,11
M 70
AW 85,12
AW 109,13
M 71
R 78
M 72
W 2
UL 6,34
M 73
WL 3
AN 112,114
AN 113,115
M 74
SH 114
SH 115
M 75
mw 48
AN 114,114
AN 115,115
M 76
WL 2
AN 116,118
AN 117,119
M 77
SH 118
SH 119
M 78
AN 118,118
AN 119,119
M 79
TY 122,3592
TY 126,17416
M 80
ad 84,672
W 0
M 81
SA 206,2304
UH 7,34
M 82
UL 8,35
UH 9,35
M 83
R 80
M 84
W 1
AR 10,38
M 85
AR 11,62
AR 12,86
M 86
AR 13,110
AD 10,6
M 87
W 2
AD 11,7
M 88
WL 3
AN 120,122
AN 121,123
M 89
SH 122
SH 123
M 90
mw 51
AN 122,122
AN 123,123
M 91
WL 2
AN 124,126
AN 125,127
M 92
SH 126
SH 127
M 93
AN 126,126
AN 127,127
M 94
ad 84,678
W 0
M 95
R 82
M 96
AD 12,8
AD 13,9
M 97
W 1
AW 38,10
M 98
AW 62,11
AW 86,12
M 99
AW 110,13
UL 6,36
M 100
W 2
UH 7,36
M 101
UL 8,37
UH 9,37
M 102
AR 10,39
AR 11,63
M 103
bc 49,8,66,60
AR 12,87
M 104
mh 18
AR 13,111
AD 10,6
M 105
ad 66,1600
H 0
M 106
AD 11,7
AD 12,8
M 107
R 0
M 108
DG 1024
M 109
AD 13,9
AW 39,10
M 110
AW 63,11
AW 87,12
M 111
bc 48,8,64,56
AW 111,13
M 112
mh 21
M 113
ad 64,1600
H 0
M 114
M 115
M 116
H 1
M 117
M 118
M 119
WL 2
R 2
M 120
H 2
M 121
M 132
M 133
TX 58,0
TX 62,13824
M 122
M 123
M 134
M 135
TY 66,512
TY 70,14336
M 124
M 125
M 136
M 137
TX 74,1536
TX 78,15360
M 126
WL 4
Q 0
M 127
Q 2
M 138
Q 4
M 139
TY 82,2048
TY 86,15872
M 128
Q 6
M 129
WL 4
Q 8
M 140
Q 10
M 141
TX 90,3072
TX 94,16896
M 130
Q 12
M 131
R 4
M 142
Q 14
M 143
TY 98,3584
TY 102,17408
M 0
R 6
M 1
WL 10
Q 16
M 12
Q 18
M 13
Q 20
M 2
Q 22
M 3
WL 8
Q 24
M 14
Q 26
M 15
Q 28
M 4
Q 30
M 5
WL 6
Q 32
M 16
Q 34
M 17
Q 36
M 6
Q 38
M 7
WL 2
Q 40
M 18
Q 42
M 19
Q 44
M 8
Q 46
M 9
ae 42,72,384
M 20
SA 208,128
M 21
ae 44,68,768
M 10
s_bitcmp1_b32 s50, 14
s_cselect_b32 s51, 24576, 0
v_add_u32 v47, s51, v3
M 11
R 8
M 22
XL 14,0
M 23
R 10
M 24
XL 16,768
M 25
XL 18,1536
M 26
XL 20,2304
M 27
M 28
M 29
M 30
M 31
M 32
M 33
M 34
M 35
WL 4
M 36
M 37
M 38
M 39
M 40
M 41
M 42
WV 23
UL 6,38
UH 7,38
M 43
WV 10
UL 8,39
UH 9,39
M 44
WL 0
AR 10,40
AR 11,64
M 45
s_barrier
R 24
M 46
mw 72
AR 12,88
AR 13,112
M 47
R 26
M 48
TY 106,5128
TY 110,18952
M 49
ad 84,720
W 0
M 50
AD 10,6
AD 11,7
M 51
AD 12,8
AD 13,9
M 52
W 1
AW 40,10
M 53
AW 64,11
AW 88,12
M 54
AW 112,13
UL 6,40
M 55
W 2
UH 7,40
M 56
UL 8,41
UH 9,41
M 57
WL 1
AN 104,106
AN 105,107
M 58
mw 75
SH 106
SH 107
M 59
R 28
M 60
AN 106,106
AN 107,107
M 61
WL 2
AN 108,110
AN 109,111
M 62
SH 110
SH 111
M 63
AN 110,110
AN 111,111
M 64
TY 114,6664
TY 118,20488
M 65
ad 84,726
M 66
W 0
AR 10,41
M 67
AR 11,65
AR 12,89
M 68
AR 13,113
AD 10,6
M 69
W 1
AD 11,7
M 70
AD 12,8
AD 13,9
M 71
R 30
M 72
W 2
AW 41,10
M 73
WL 3
AN 112,114
AN 113,115
M 74
SH 114
SH 115
M 75
mw 0
AN 114,114
AN 115,115
M 76
WL 2
AN 116,118
)"
R"(AN 117,119
M 77
SH 118
SH 119
M 78
AN 118,118
AN 119,119
M 79
TY 122,8200
TY 126,22024
M 80
ad 84,768
W 0
M 81
AW 65,11
AW 89,12
M 82
SA 210,896
AW 113,13
M 83
R 32
M 84
W 1
UL 6,42
M 85
UH 7,42
UL 8,43
M 86
UH 9,43
AR 10,42
M 87
W 2
AR 11,66
M 88
WL 3
AN 120,122
AN 121,123
M 89
SH 122
SH 123
M 90
mw 3
AN 122,122
AN 123,123
M 91
WL 2
AN 124,126
AN 125,127
M 92
SH 126
SH 127
M 93
AN 126,126
AN 127,127
M 94
ad 84,774
W 0
M 95
R 34
M 96
AR 12,90
AR 13,114
M 97
W 1
AD 10,6
M 98
AD 11,7
AD 12,8
M 99
AD 13,9
AW 42,10
M 100
W 2
AW 66,11
M 101
AW 90,12
AW 114,13
M 102
UL 6,44
UH 7,44
M 103
bc 49,9,66,60
UL 8,45
M 104
mh 0
UH 9,45
AR 10,43
M 105
ad 66,1792
H 0
M 106
AR 11,67
AR 12,91
M 107
R 48
M 108
DG 1024
M 109
AR 13,115
AD 10,6
M 110
AD 11,7
AD 12,8
M 111
bc 48,9,64,56
AD 13,9
M 112
mh 3
AW 43,10
AW 67,11
M 113
ad 64,1792
H 0
M 114
AW 91,12
AW 115,13
M 115
M 116
H 1
M 117
M 118
M 119
WL 2
R 50
M 120
H 2
M 121
M 132
M 133
TX 58,4608
TX 62,18432
M 122
M 123
M 134
M 135
TY 66,5120
TY 70,18944
M 124
M 125
M 136
M 137
TX 74,6144
TX 78,19968
M 126
WL 4
Q 0
M 127
Q 2
M 138
Q 4
M 139
TY 82,6656
TY 86,20480
M 128
Q 6
M 129
WL 4
Q 8
M 140
Q 10
M 141
TX 90,7680
TX 94,21504
M 130
Q 12
M 131
R 52
M 142
Q 14
M 143
TY 98,8192
TY 102,22016
M 0
R 54
M 1
WL 10
Q 16
M 12
Q 18
M 13
Q 20
M 2
Q 22
M 3
WL 8
Q 24
M 14
Q 26
M 15
Q 28
M 4
Q 30
M 5
WL 6
Q 32
M 16
Q 34
M 17
Q 36
M 6
Q 38
M 7
WL 2
Q 40
M 18
Q 42
M 19
Q 44
M 8
Q 46
M 9
SA 212,1664
M 20
ae 44,68,768
M 21
s_bfe_u32 s51, s50, 0x1000f
v_mad_i32_i24 v47, v49, s51, v48
XL 22,0
M 10
XL 24,768
M 11
R 56
M 22
XL 26,1536
M 23
R 58
M 24
XL 28,2304
M 25
M 26
M 27
M 28
M 29
M 30
M 31
M 32
M 33
M 34
M 35
WL 4
M 36
M 37
M 38
M 39
M 40
M 41
M 42
M 43
WL 0
M 44
WV 10
UL 6,14
UH 7,14
M 45
s_barrier
R 72
M 46
mw 24
UL 8,15
UH 9,15
M 47
R 74
M 48
TY 106,9736
TY 110,23560
M 49
ad 84,816
W 0
M 50
AR 10,44
AR 11,68
M 51
AR 12,92
AR 13,116
M 52
W 1
AD 10,6
M 53
AD 11,7
AD 12,8
M 54
AD 13,9
AW 44,10
M 55
W 2
AW 68,11
M 56
AW 92,12
AW 116,13
M 57
WL 1
AN 104,106
AN 105,107
M 58
mw 27
SH 106
SH 107
M 59
R 76
M 60
AN 106,106
AN 107,107
M 61
WL 2
AN 108,110
AN 109,111
M 62
SH 110
SH 111
M 63
AN 110,110
AN 111,111
M 64
TY 114,11272
TY 118,25096
M 65
ad 84,822
M 66
W 0
UL 6,16
M 67
UH 7,16
UL 8,17
M 68
UH 9,17
AR 10,45
M 69
W 1
AR 11,69
M 70
AR 12,93
AR 13,117
M 71
R 78
M 72
W 2
AD 10,6
M 73
WL 3
AN 112,114
AN 113,115
M 74
SH 114
SH 115
M 75
mw 48
AN 114,114
AN 115,115
M 76
WL 2
AN 116,118
AN 117,119
M 77
SH 118
SH 119
M 78
AN 118,118
AN 119,119
M 79
TY 122,12808
TY 126,26632
M 80
ad 84,864
W 0
M 81
SA 214,2432
AD 11,7
M 82
AD 12,8
AD 13,9
M 83
R 80
M 84
W 1
AW 45,10
M 85
AW 69,11
AW 93,12
M 86
AW 117,13
UL 6,18
M 87
W 2
UH 7,18
M 88
WL 3
AN 120,122
AN 121,123
M 89
SH 122
SH 123
M 90
mw 51
AN 122,122
AN 123,123
M 91
WL 2
AN 124,126
AN 125,127
M 92
)"
R"(SH 126
SH 127
M 93
AN 126,126
AN 127,127
M 94
ad 84,870
W 0
M 95
R 82
M 96
UL 8,19
UH 9,19
M 97
W 1
AR 10,46
M 98
AR 11,70
AR 12,94
M 99
AR 13,118
AD 10,6
M 100
W 2
AD 11,7
M 101
AD 12,8
AD 13,9
M 102
AW 46,10
AW 70,11
M 103
bc 49,10,66,60
AW 94,12
M 104
mh 9
AW 118,13
UL 6,20
M 105
ad 66,1984
H 0
M 106
UH 7,20
UL 8,21
M 107
R 0
M 108
DG 1024
M 109
UH 9,21
AR 10,47
M 110
AR 11,71
AR 12,95
M 111
bc 48,10,64,56
AR 13,119
M 112
mh 12
AD 10,6
AD 11,7
M 113
ad 64,1984
H 0
M 114
AD 12,8
AD 13,9
M 115
AW 47,10
AW 71,11
M 116
H 1
AW 95,12
M 117
AW 119,13
M 118
M 119
WL 2
R 2
M 120
H 2
M 121
M 132
M 133
TX 58,9216
TX 62,23040
M 122
M 123
M 134
M 135
TY 66,9728
TY 70,23552
M 124
M 125
M 136
M 137
TX 74,10752
TX 78,24576
M 126
WL 4
Q 0
M 127
Q 2
M 138
Q 4
M 139
TY 82,11264
TY 86,25088
M 128
Q 6
M 129
WL 4
Q 8
M 140
Q 10
M 141
TX 90,12288
TX 94,26112
M 130
Q 12
M 131
R 4
M 142
Q 14
M 143
TY 98,12800
TY 102,26624
M 0
R 6
M 1
WL 10
Q 16
M 12
Q 18
M 13
Q 20
M 2
Q 22
M 3
WL 8
Q 24
M 14
Q 26
M 15
Q 28
M 4
Q 30
M 5
WL 6
Q 32
M 16
Q 34
M 17
Q 36
M 6
Q 38
M 7
WL 2
Q 40
M 18
Q 42
M 19
Q 44
M 8
Q 46
M 9
ae 42,72,384
M 20
SA 216,256
M 21
ae 44,68,864
M 10
s_bitcmp1_b32 s50, 16
s_cselect_b32 s51, 24576, 0
v_add_u32 v47, s51, v3
M 11
R 8
M 22
XL 30,0
M 23
R 10
M 24
XL 32,768
M 25
XL 34,1536
M 26
XL 36,2304
M 27
M 28
M 29
M 30
M 31
M 32
M 33
M 34
M 35
WL 4
M 36
M 37
M 38
M 39
M 40
M 41
M 42
M 43
WV 10
M 44
WL 0
M 45
s_barrier
R 24
M 46
mw 72
UL 6,22
UH 7,22
M 47
R 26
M 48
TY 106,520
TY 110,14344
M 49
ad 84,912
W 0
M 50
UL 8,23
UH 9,23
M 51
AR 10,48
AR 11,72
M 52
W 1
AR 12,96
M 53
AR 13,120
AD 10,6
M 54
AD 11,7
AD 12,8
M 55
W 2
AD 13,9
M 56
AW 48,10
AW 72,11
M 57
WL 1
AN 104,106
AN 105,107
M 58
mw 75
SH 106
SH 107
M 59
R 28
M 60
AN 106,106
AN 107,107
M 61
WL 2
AN 108,110
AN 109,111
M 62
SH 110
SH 111
M 63
AN 110,110
AN 111,111
M 64
TY 114,2056
TY 118,15880
M 65
ad 84,918
M 66
W 0
AW 96,12
M 67
AW 120,13
UL 6,24
M 68
UH 7,24
UL 8,25
M 69
W 1
UH 9,25
M 70
AR 10,49
AR 11,73
M 71
R 30
M 72
W 2
AR 12,97
M 73
WL 3
AN 112,114
AN 113,115
M 74
SH 114
SH 115
M 75
mw 0
AN 114,114
AN 115,115
M 76
WL 2
AN 116,118
AN 117,119
M 77
SH 118
SH 119
M 78
AN 118,118
AN 119,119
M 79
TY 122,3592
TY 126,17416
M 80
ad 84,960
W 0
M 81
AR 13,121
AD 10,6
M 82
SA 218,1024
AD 11,7
M 83
R 32
M 84
W 1
AD 12,8
M 85
AD 13,9
AW 49,10
M 86
AW 73,11
AW 97,12
M 87
W 2
AW 121,13
M 88
WL 3
AN 120,122
AN 121,123
M 89
SH 122
SH 123
M 90
mw 3
AN 122,122
AN 123,123
M 91
WL 2
AN 124,126
AN 125,127
M 92
SH 126
SH 127
M 93
AN 126,126
AN 127,127
M 94
ad 84,966
W 0
M 95
R 34
M 96
UL 6,26
UH 7,26
M 97
W 1
UL 8,27
M 98
UH 9,27
AR 10,50
M 99
AR 11,74
AR 12,98
M 100
W 2
AR 13,122
M 101
AD 10,6
AD 11,7
M 102
AD 12,8
AD 13,9
M 103
bc 49,11,66,60
AW 50,10
M 104
mh 18
AW 74,11
AW 98,12
M 105
ad 66,2176
H 0
M 106
AW 122,13
UL 6,28
M 107
)"
R"(R 48
M 108
DG 1024
M 109
UH 7,28
UL 8,29
M 110
UH 9,29
AR 10,51
M 111
bc 48,11,64,56
AR 11,75
M 112
mh 21
AR 12,99
AR 13,123
M 113
ad 64,2176
H 0
M 114
AD 10,6
AD 11,7
M 115
AD 12,8
AD 13,9
M 116
H 1
AW 51,10
M 117
AW 75,11
AW 99,12
M 118
AW 123,13
M 119
WL 1
R 50
M 120
H 2
M 121
M 132
M 133
TX 58,0
TX 62,13824
M 122
M 123
M 134
M 135
TY 66,512
TY 70,14336
M 124
M 125
M 136
M 137
TX 74,1536
TX 78,15360
M 126
WL 4
Q 0
M 127
Q 2
M 138
Q 4
M 139
TY 82,2048
TY 86,15872
M 128
Q 6
M 129
WL 4
Q 8
M 140
Q 10
M 141
TX 90,3072
TX 94,16896
M 130
Q 12
M 131
R 52
M 142
Q 14
M 143
TY 98,3584
TY 102,17408
M 0
R 54
M 1
WL 10
Q 16
M 12
Q 18
M 13
Q 20
M 2
Q 22
M 3
WL 8
Q 24
M 14
Q 26
M 15
Q 28
M 4
Q 30
M 5
WL 6
Q 32
M 16
Q 34
M 17
Q 36
M 6
Q 38
M 7
WL 2
Q 40
M 18
Q 42
M 19
Q 44
M 8
Q 46
M 9
SA 220,1792
M 20
ae 44,68,864
M 21
s_bfe_u32 s51, s50, 0x10011
v_mad_i32_i24 v47, v49, s51, v48
XL 38,0
M 10
XL 40,768
M 11
R 56
M 22
XL 42,1536
M 23
R 58
M 24
XL 44,2304
M 25
M 26
M 27
M 28
M 29
M 30
M 31
M 32
M 33
M 34
M 35
WL 4
M 36
M 37
M 38
M 39
M 40
M 41
M 42
M 43
WV 10
M 44
WL 0
M 45
s_barrier
R 72
M 46
mw 24
M 47
R 74
M 48
TY 106,5128
TY 110,18952
M 49
ad 84,1008
W 0
M 50
UL 6,30
UH 7,30
M 51
UL 8,31
UH 9,31
M 52
W 1
AR 10,52
M 53
AR 11,76
AR 12,100
M 54
AR 13,124
AD 10,6
M 55
W 2
AD 11,7
M 56
AD 12,8
AD 13,9
M 57
WL 1
AN 104,106
AN 105,107
M 58
mw 27
SH 106
SH 107
M 59
R 76
M 60
AN 106,106
AN 107,107
M 61
WL 2
AN 108,110
AN 109,111
M 62
SH 110
SH 111
M 63
AN 110,110
AN 111,111
M 64
TY 114,6664
TY 118,20488
M 65
ad 84,1014
M 66
W 0
AW 52,10
M 67
AW 76,11
AW 100,12
M 68
AW 124,13
UL 6,32
M 69
W 1
UH 7,32
M 70
UL 8,33
UH 9,33
M 71
R 78
M 72
W 2
AR 10,53
M 73
WL 3
AN 112,114
AN 113,115
M 74
SH 114
SH 115
M 75
mw 48
AN 114,114
AN 115,115
M 76
WL 2
AN 116,118
AN 117,119
M 77
SH 118
SH 119
M 78
AN 118,118
AN 119,119
M 79
TY 122,8200
TY 126,22024
M 80
ad 84,1056
W 0
M 81
SA 222,2560
AR 11,77
M 82
AR 12,101
AR 13,125
M 83
R 80
M 84
W 1
AD 10,6
M 85
AD 11,7
AD 12,8
M 86
AD 13,9
AW 53,10
M 87
W 2
AW 77,11
M 88
WL 3
AN 120,122
AN 121,123
M 89
SH 122
SH 123
M 90
mw 51
AN 122,122
AN 123,123
M 91
WL 2
AN 124,126
AN 125,127
M 92
SH 126
SH 127
M 93
AN 126,126
AN 127,127
M 94
ad 84,1062
W 0
M 95
R 82
M 96
AW 101,12
AW 125,13
M 97
W 1
UL 6,34
M 98
UH 7,34
UL 8,35
M 99
UH 9,35
AR 10,54
M 100
W 2
AR 11,78
M 101
AR 12,102
AR 13,126
M 102
AD 10,6
AD 11,7
M 103
bc 49,12,66,60
AD 12,8
M 104
mh 0
AD 13,9
AW 54,10
M 105
ad 66,2368
H 0
M 106
AW 78,11
AW 102,12
M 107
R 0
M 108
DG 1024
M 109
AW 126,13
UL 6,36
M 110
UH 7,36
UL 8,37
M 111
bc 48,12,64,56
UH 9,37
M 112
mh 3
AR 10,55
AR 11,79
M 113
ad 64,2368
H 0
M 114
AR 12,103
AR 13,127
M 115
AD 10,6
AD 11,7
M 116
H 1
AD 12,8
M 117
AD 13,9
AW 55,10
M 118
AW 79,11
AW 103,12
M 119
WL 0
R 2
M 120
H 2
AW 127,13
M 121
M 132
M 133
TX 58,4608
TX 62,18432
M 122
M 123
)"
R"(M 134
M 135
TY 66,5120
TY 70,18944
M 124
M 125
M 136
M 137
TX 74,6144
TX 78,19968
M 126
WL 4
Q 0
M 127
Q 2
M 138
Q 4
M 139
TY 82,6656
TY 86,20480
M 128
Q 6
M 129
WL 4
Q 8
M 140
Q 10
M 141
TX 90,7680
TX 94,21504
M 130
Q 12
M 131
R 4
M 142
Q 14
M 143
TY 98,8192
TY 102,22016
M 0
R 6
M 1
WL 10
Q 16
M 12
Q 18
M 13
Q 20
M 2
Q 22
M 3
WL 8
Q 24
M 14
Q 26
M 15
Q 28
M 4
Q 30
M 5
WL 6
Q 32
M 16
Q 34
M 17
Q 36
M 6
Q 38
M 7
WL 2
Q 40
M 18
Q 42
M 19
Q 44
M 8
Q 46
M 9
ae 42,72,480
M 20
s_bitcmp1_b32 s50, 10
s_cselect_b32 s52, 24576, 0
v_add_u32 v50, s52, v3
M 21
SA 224,0
M 10
M 11
R 8
M 22
M 23
R 10
M 24
M 25
M 26
M 27
M 28
M 29
M 30
M 31
M 32
ae 44,70,768
M 33
s_bitcmp1_b32 s50, 12
s_cselect_b32 s51, 24576, 0
v_add_u32 v47, s51, v3
M 34
XL 14,128
M 35
WL 4
M 36
M 37
M 38
XL 16,896
M 39
M 40
M 41
M 42
M 43
WV 8
M 44
WL 0
XL 18,1664
M 45
s_barrier
R 24
M 46
mw 72
M 47
R 26
M 48
TY 106,9736
TY 110,23560
M 49
ad 84,1104
W 0
M 50
XL 20,2432
UL 6,38
M 51
UH 7,38
UL 8,39
M 52
W 1
UH 9,39
M 53
AR 10,56
AR 11,80
M 54
AR 12,104
AR 13,128
M 55
W 2
AD 10,6
M 56
AD 11,7
AD 12,8
M 57
WL 1
AN 104,106
AN 105,107
M 58
mw 75
SH 106
SH 107
M 59
R 28
M 60
AN 106,106
AN 107,107
M 61
WL 2
AN 108,110
AN 109,111
M 62
SH 110
SH 111
M 63
AN 110,110
AN 111,111
M 64
TY 114,11272
TY 118,25096
M 65
ad 84,1110
M 66
W 0
AD 13,9
M 67
AW 56,10
AW 80,11
M 68
AW 104,12
AW 128,13
M 69
W 1
UL 6,40
M 70
UH 7,40
UL 8,41
M 71
R 30
M 72
W 2
UH 9,41
M 73
WL 3
AN 112,114
AN 113,115
M 74
SH 114
SH 115
M 75
mw 0
AN 114,114
AN 115,115
M 76
WL 2
AN 116,118
AN 117,119
M 77
SH 118
SH 119
M 78
AN 118,118
AN 119,119
M 79
TY 122,12808
TY 126,26632
M 80
ad 84,1152
W 0
M 81
AR 10,57
AR 11,81
M 82
AR 12,105
AR 13,129
M 83
R 32
M 84
W 1
SA 226,768
M 85
AD 10,6
AD 11,7
M 86
AD 12,8
AD 13,9
M 87
W 2
AW 57,10
M 88
WL 3
AN 120,122
AN 121,123
M 89
SH 122
SH 123
M 90
mw 3
AN 122,122
AN 123,123
M 91
WL 2
AN 124,126
AN 125,127
M 92
SH 126
SH 127
M 93
AN 126,126
AN 127,127
M 94
ad 84,1158
W 0
M 95
R 34
M 96
AW 81,11
AW 105,12
M 97
W 1
AW 129,13
M 98
UL 6,42
UH 7,42
M 99
UL 8,43
UH 9,43
M 100
W 2
AR 10,58
M 101
AR 11,82
AR 12,106
M 102
AR 13,130
AD 10,6
M 103
bc 49,13,66,60
AD 11,7
M 104
mh 9
AD 12,8
AD 13,9
M 105
ad 66,2560
H 0
M 106
AW 58,10
AW 82,11
M 107
R 48
M 108
DG 1024
M 109
AW 106,12
AW 130,13
M 110
UL 6,44
UH 7,44
M 111
bc 48,13,64,56
UL 8,45
M 112
mh 12
UH 9,45
AR 10,59
M 113
ad 64,2560
H 0
M 114
AR 11,83
AR 12,107
M 115
AR 13,131
AD 10,6
M 116
H 1
AD 11,7
M 117
AD 12,8
AD 13,9
M 118
AW 59,10
AW 83,11
M 119
WL 0
R 50
M 120
H 2
AW 107,12
M 121
AW 131,13
M 132
M 133
TX 58,9216
TX 62,23040
M 122
M 123
M 134
M 135
TY 66,9728
TY 70,23552
M 124
M 125
M 136
M 137
TX 74,10752
TX 78,24576
M 126
WL 4
Q 0
M 127
Q 2
M 138
Q 4
M 139
TY 82,11264
TY 86,25088
M 128
Q 6
M 129
WL 4
Q 8
M 140
Q 10
M 141
TX 90,12288
TX 94,26112
M 130
Q 12
M 131
R 52
M 142
Q 14
M 143
TY 98,12800
TY 102,26624
M 0
)"
R"(R 54
M 1
WL 10
Q 16
M 12
Q 18
M 13
Q 20
M 2
Q 22
M 3
WL 8
Q 24
M 14
Q 26
M 15
Q 28
M 4
Q 30
M 5
WL 6
Q 32
M 16
Q 34
M 17
Q 36
M 6
Q 38
M 7
WL 2
Q 40
M 18
Q 42
M 19
Q 44
M 8
Q 46
M 9
SA 228,1536
M 20
M 21
M 10
M 11
R 56
M 22
M 23
R 58
M 24
M 25
M 26
M 27
M 28
M 29
M 30
M 31
M 32
M 33
M 34
ae 44,70,768
M 35
WL 4
M 36
s_bfe_u32 s51, s50, 0x1000d
v_mad_i32_i24 v47, v49, s51, v48
XL 22,128
M 37
M 38
M 39
M 40
XL 24,896
M 41
M 42
M 43
WV 8
M 44
WL 0
M 45
s_barrier
R 72
M 46
mw 24
XL 26,1664
M 47
R 74
M 48
TY 106,520
TY 110,14344
M 49
ad 84,1200
W 0
M 50
M 51
M 52
W 1
XL 28,2432
M 53
M 54
M 55
W 2
M 56
M 57
WL 1
AN 104,106
AN 105,107
M 58
mw 27
SH 106
SH 107
M 59
R 76
M 60
AN 106,106
AN 107,107
M 61
WL 2
AN 108,110
AN 109,111
M 62
SH 110
SH 111
M 63
AN 110,110
AN 111,111
M 64
TY 114,2056
TY 118,15880
M 65
ad 84,1206
M 66
W 0
M 67
M 68
M 69
W 1
M 70
M 71
R 78
M 72
W 2
M 73
WL 3
AN 112,114
AN 113,115
M 74
SH 114
SH 115
M 75
mw 48
AN 114,114
AN 115,115
M 76
WL 2
AN 116,118
AN 117,119
M 77
SH 118
SH 119
M 78
AN 118,118
AN 119,119
M 79
TY 122,3592
TY 126,17416
M 80
ad 84,1248
W 0
M 81
SA 230,2304
M 82
M 83
R 80
M 84
W 1
M 85
M 86
M 87
W 2
M 88
WL 3
AN 120,122
AN 121,123
M 89
SH 122
SH 123
M 90
mw 51
AN 122,122
AN 123,123
M 91
WL 2
AN 124,126
AN 125,127
M 92
SH 126
SH 127
M 93
AN 126,126
AN 127,127
M 94
ad 84,1254
W 0
M 95
R 82
M 96
UL 6,14
UH 7,14
M 97
W 1
UL 8,15
M 98
UH 9,15
AR 10,132
M 99
AR 11,156
AR 12,180
M 100
W 2
AD 10,6
M 101
AD 11,7
AD 12,8
M 102
AD 136,9
AW 132,10
M 103
bc 49,14,66,60
AW 156,11
M 104
mh 18
AW 180,12
UL 6,16
M 105
ad 66,2752
H 0
M 106
UH 7,16
UL 8,17
M 107
R 0
M 108
DG 1024
M 109
UH 9,17
AR 10,133
M 110
AR 11,157
AR 12,181
M 111
bc 48,14,64,56
AD 10,6
M 112
mh 21
AD 11,7
AD 12,8
M 113
ad 64,2752
H 0
M 114
AD 137,9
AW 133,10
M 115
AW 157,11
AW 181,12
M 116
H 1
UL 6,18
M 117
UH 7,18
UL 8,19
M 118
UH 9,19
AR 10,134
M 119
WL 0
R 2
M 120
H 2
AR 11,158
M 121
AR 12,182
AD 10,6
M 132
AD 11,7
AD 12,8
M 133
TX 58,0
TX 62,13824
M 122
AD 138,9
AW 134,10
M 123
AW 158,11
AW 182,12
M 134
UL 6,20
UH 7,20
M 135
TY 66,512
TY 70,14336
M 124
UL 8,21
UH 9,21
M 125
AR 10,135
AR 11,159
M 136
AR 12,183
AD 10,6
M 137
TX 74,1536
TX 78,15360
M 126
WL 4
Q 0
M 127
Q 2
M 138
Q 4
M 139
TY 82,2048
TY 86,15872
M 128
Q 6
M 129
WL 4
Q 8
M 140
Q 10
M 141
TX 90,3072
TX 94,16896
M 130
Q 12
M 131
R 4
M 142
Q 14
M 143
TY 98,3584
TY 102,17408
M 0
R 6
M 1
WL 10
Q 16
M 12
Q 18
M 13
Q 20
M 2
Q 22
M 3
WL 8
Q 24
M 14
Q 26
M 15
Q 28
M 4
Q 30
M 5
WL 6
Q 32
M 16
Q 34
M 17
Q 36
M 6
Q 38
M 7
WL 2
Q 40
M 18
Q 42
M 19
Q 44
M 8
Q 46
M 9
ae 42,72,480
M 20
SA 232,128
AD 11,7
M 21
AD 12,8
AD 139,9
M 10
AW 135,10
AW 159,11
M 11
R 8
M 22
AW 183,12
M 23
R 10
M 24
M 25
M 26
M 27
M 28
)"
R"(M 29
M 30
M 31
M 32
M 33
M 34
M 35
WL 4
M 36
ae 44,68,768
M 37
s_bitcmp1_b32 s50, 14
s_cselect_b32 s51, 24576, 0
v_add_u32 v47, s51, v3
M 38
XL 30,128
M 39
M 40
M 41
M 42
XL 32,896
M 43
WV 8
M 44
WL 0
M 45
s_barrier
R 24
M 46
mw 72
M 47
R 26
M 48
TY 106,5128
TY 110,18952
M 49
ad 84,1296
W 0
M 50
XL 34,1664
M 51
M 52
W 1
M 53
M 54
XL 36,2432
M 55
W 2
M 56
M 57
WL 1
AN 104,106
AN 105,107
M 58
mw 75
SH 106
SH 107
M 59
R 28
M 60
AN 106,106
AN 107,107
M 61
WL 2
AN 108,110
AN 109,111
M 62
SH 110
SH 111
M 63
AN 110,110
AN 111,111
M 64
TY 114,6664
TY 118,20488
M 65
ad 84,1302
M 66
W 0
M 67
M 68
M 69
W 1
M 70
M 71
R 30
M 72
W 2
M 73
WL 3
AN 112,114
AN 113,115
M 74
SH 114
SH 115
M 75
mw 0
AN 114,114
AN 115,115
M 76
WL 2
AN 116,118
AN 117,119
M 77
SH 118
SH 119
M 78
AN 118,118
AN 119,119
M 79
TY 122,8200
TY 126,22024
M 80
ad 84,1344
W 0
M 81
M 82
SA 234,896
M 83
R 32
M 84
W 1
M 85
M 86
M 87
W 2
M 88
WL 3
AN 120,122
AN 121,123
M 89
SH 122
SH 123
M 90
mw 3
AN 122,122
AN 123,123
M 91
WL 2
AN 124,126
AN 125,127
M 92
SH 126
SH 127
M 93
AN 126,126
AN 127,127
M 94
ad 84,1350
W 0
M 95
R 34
M 96
UL 6,22
UH 7,22
M 97
W 1
UL 8,23
M 98
UH 9,23
AR 10,136
M 99
AR 11,160
AR 12,184
M 100
W 2
AD 10,6
M 101
AD 11,7
AD 12,8
M 102
AD 140,9
AW 136,10
M 103
bc 49,15,66,60
AW 160,11
M 104
mh 0
AW 184,12
UL 6,24
M 105
ad 66,2944
H 0
M 106
UH 7,24
UL 8,25
M 107
R 48
M 108
DG 1024
M 109
UH 9,25
AR 10,137
M 110
AR 11,161
AR 12,185
M 111
bc 48,15,64,56
AD 10,6
M 112
mh 3
AD 11,7
AD 12,8
M 113
ad 64,2944
H 0
M 114
AD 141,9
AW 137,10
M 115
AW 161,11
AW 185,12
M 116
H 1
UL 6,26
M 117
UH 7,26
UL 8,27
M 118
UH 9,27
AR 10,138
M 119
WL 0
R 50
M 120
H 2
AR 11,162
M 121
AR 12,186
AD 10,6
M 132
AD 11,7
AD 12,8
M 133
TX 58,4608
TX 62,18432
M 122
AD 142,9
AW 138,10
M 123
AW 162,11
AW 186,12
M 134
UL 6,28
UH 7,28
M 135
TY 66,5120
TY 70,18944
M 124
UL 8,29
UH 9,29
M 125
AR 10,139
AR 11,163
M 136
AR 12,187
AD 10,6
M 137
TX 74,6144
TX 78,19968
M 126
WL 4
Q 0
M 127
Q 2
M 138
Q 4
M 139
TY 82,6656
TY 86,20480
M 128
Q 6
M 129
WL 4
Q 8
M 140
Q 10
M 141
TX 90,7680
TX 94,21504
M 130
Q 12
M 131
R 52
M 142
Q 14
M 143
TY 98,8192
TY 102,22016
M 0
R 54
M 1
WL 10
Q 16
M 12
Q 18
M 13
Q 20
M 2
Q 22
M 3
WL 8
Q 24
M 14
Q 26
M 15
Q 28
M 4
Q 30
M 5
WL 6
Q 32
M 16
Q 34
M 17
Q 36
M 6
Q 38
M 7
WL 2
Q 40
M 18
Q 42
M 19
Q 44
M 8
Q 46
M 9
SA 236,1664
AD 11,7
M 20
AD 12,8
AD 143,9
M 21
AW 139,10
AW 163,11
M 10
AW 187,12
M 11
R 56
M 22
M 23
R 58
M 24
M 25
M 26
M 27
M 28
M 29
M 30
M 31
M 32
M 33
M 34
M 35
WL 4
M 36
M 37
M 38
ae 44,68,768
M 39
s_bfe_u32 s51, s50, 0x1000f
v_mad_i32_i24 v47, v49, s51, v48
XL 38,128
M 40
M 41
M 42
M 43
WV 7
M 44
WL 0
XL 40,896
M 45
s_barrier
R 72
M 46
mw 24
M 47
R 74
M 48
TY 106,9736
TY 110,23560
M 49
ad 84,1392
W 0
M 50
XL 42,1664
M 51
M 52
W 1
M 53
M 54
M 55
W 2
M 56
XL 44,2432
M 57
WL 1
AN 104,106
AN 105,107
)"
R"(M 58
mw 27
SH 106
SH 107
M 59
R 76
M 60
AN 106,106
AN 107,107
M 61
WL 2
AN 108,110
AN 109,111
M 62
SH 110
SH 111
M 63
AN 110,110
AN 111,111
M 64
TY 114,11272
TY 118,25096
M 65
ad 84,1398
M 66
W 0
M 67
M 68
M 69
W 1
M 70
M 71
R 78
M 72
W 2
M 73
WL 3
AN 112,114
AN 113,115
M 74
SH 114
SH 115
M 75
mw 48
AN 114,114
AN 115,115
M 76
WL 2
AN 116,118
AN 117,119
M 77
SH 118
SH 119
M 78
AN 118,118
AN 119,119
M 79
TY 122,12808
TY 126,26632
M 80
ad 84,1440
W 0
M 81
SA 238,2432
M 82
M 83
R 80
M 84
W 1
M 85
M 86
M 87
W 2
M 88
WL 3
AN 120,122
AN 121,123
M 89
SH 122
SH 123
M 90
mw 51
AN 122,122
AN 123,123
M 91
WL 2
AN 124,126
AN 125,127
M 92
SH 126
SH 127
M 93
AN 126,126
AN 127,127
M 94
ad 84,1446
W 0
M 95
R 82
M 96
UL 6,30
UH 7,30
M 97
W 1
UL 8,31
M 98
UH 9,31
AR 10,140
M 99
AR 11,164
AR 12,188
M 100
W 2
AD 10,6
M 101
AD 11,7
AD 12,8
M 102
AD 144,9
AW 140,10
M 103
bc 49,16,66,60
AW 164,11
M 104
mh 9
AW 188,12
UL 6,32
M 105
ad 66,3136
H 0
M 106
UH 7,32
UL 8,33
M 107
R 0
M 108
DG 1024
M 109
UH 9,33
AR 10,141
M 110
AR 11,165
AR 12,189
M 111
bc 48,16,64,56
AD 10,6
M 112
mh 12
AD 11,7
AD 12,8
M 113
ad 64,3136
H 0
M 114
AD 145,9
AW 141,10
M 115
AW 165,11
AW 189,12
M 116
H 1
UL 6,34
M 117
UH 7,34
UL 8,35
M 118
UH 9,35
AR 10,142
M 119
WL 0
R 2
M 120
H 2
AR 11,166
M 121
AR 12,190
AD 10,6
M 132
AD 11,7
AD 12,8
M 133
TX 58,9216
TX 62,23040
M 122
AD 146,9
AW 142,10
M 123
AW 166,11
AW 190,12
M 134
UL 6,36
UH 7,36
M 135
TY 66,9728
TY 70,23552
M 124
UL 8,37
UH 9,37
M 125
AR 10,143
AR 11,167
M 136
AR 12,191
AD 10,6
M 137
TX 74,10752
TX 78,24576
M 126
WL 4
Q 0
M 127
Q 2
M 138
Q 4
M 139
TY 82,11264
TY 86,25088
M 128
Q 6
M 129
WL 4
Q 8
M 140
Q 10
M 141
TX 90,12288
TX 94,26112
M 130
Q 12
M 131
R 4
M 142
Q 14
M 143
TY 98,12800
TY 102,26624
M 0
R 6
M 1
WL 10
Q 16
M 12
Q 18
M 13
Q 20
M 2
Q 22
M 3
WL 8
Q 24
M 14
Q 26
M 15
Q 28
M 4
Q 30
M 5
WL 6
Q 32
M 16
Q 34
M 17
Q 36
M 6
Q 38
M 7
WL 2
Q 40
M 18
Q 42
M 19
Q 44
M 8
Q 46
M 9
ae 42,72,480
M 20
SA 240,256
AD 11,7
M 21
AD 12,8
AD 147,9
M 10
AW 143,10
AW 167,11
M 11
R 8
M 22
AW 191,12
M 23
R 10
M 24
M 25
M 26
M 27
M 28
M 29
M 30
M 31
M 32
M 33
M 34
M 35
WL 4
M 36
M 37
M 38
M 39
M 40
ae 44,68,864
M 41
s_bitcmp1_b32 s50, 16
s_cselect_b32 s51, 24576, 0
v_add_u32 v47, s51, v3
M 42
XL 14,128
M 43
WV 7
M 44
WL 0
M 45
s_barrier
R 24
M 46
mw 72
XL 16,896
M 47
R 26
M 48
TY 106,520
TY 110,14344
M 49
ad 84,1488
W 0
M 50
M 51
M 52
W 1
XL 18,1664
M 53
M 54
M 55
W 2
M 56
M 57
WL 1
AN 104,106
AN 105,107
M 58
mw 75
SH 106
SH 107
M 59
R 28
M 60
AN 106,106
AN 107,107
M 61
WL 2
AN 108,110
AN 109,111
M 62
SH 110
SH 111
M 63
AN 110,110
AN 111,111
M 64
TY 114,2056
TY 118,15880
M 65
ad 84,1494
M 66
W 0
XL 20,2432
M 67
M 68
M 69
W 1
M 70
M 71
R 30
M 72
W 2
M 73
WL 3
AN 112,114
AN 113,115
M 74
SH 114
SH 115
M 75
mw 0
AN 114,114
AN 115,115
M 76
WL 2
AN 116,118
AN 117,119
M 77
)"
R"(SH 118
SH 119
M 78
AN 118,118
AN 119,119
M 79
TY 122,3592
TY 126,17416
M 80
ad 84,1536
W 0
M 81
M 82
SA 242,1024
M 83
R 32
M 84
W 1
M 85
M 86
M 87
W 2
M 88
WL 3
AN 120,122
AN 121,123
M 89
SH 122
SH 123
M 90
mw 3
AN 122,122
AN 123,123
M 91
WL 2
AN 124,126
AN 125,127
M 92
SH 126
SH 127
M 93
AN 126,126
AN 127,127
M 94
ad 84,1542
W 0
M 95
R 34
M 96
UL 6,38
UH 7,38
M 97
W 1
UL 8,39
M 98
UH 9,39
AR 10,144
M 99
AR 11,168
AR 12,192
M 100
W 2
AD 10,6
M 101
AD 11,7
AD 12,8
M 102
AD 148,9
AW 144,10
M 103
bc 49,17,66,60
AW 168,11
M 104
mh 18
AW 192,12
UL 6,40
M 105
ad 66,3328
H 0
M 106
UH 7,40
UL 8,41
M 107
R 48
M 108
DG 1024
M 109
UH 9,41
AR 10,145
M 110
AR 11,169
AR 12,193
M 111
bc 48,17,64,56
AD 10,6
M 112
mh 21
AD 11,7
AD 12,8
M 113
ad 64,3328
H 0
M 114
AD 149,9
AW 145,10
M 115
AW 169,11
AW 193,12
M 116
H 1
UL 6,42
M 117
UH 7,42
UL 8,43
M 118
UH 9,43
AR 10,146
M 119
WL 0
R 50
M 120
H 2
AR 11,170
M 121
AR 12,194
AD 10,6
M 132
AD 11,7
AD 12,8
M 133
TX 58,0
TX 62,13824
M 122
AD 150,9
AW 146,10
M 123
AW 170,11
AW 194,12
M 134
UL 6,44
UH 7,44
M 135
TY 66,512
TY 70,14336
M 124
UL 8,45
UH 9,45
M 125
AR 10,147
AR 11,171
M 136
AR 12,195
AD 10,6
M 137
TX 74,1536
TX 78,15360
M 126
WL 4
Q 0
M 127
Q 2
M 138
Q 4
M 139
TY 82,2048
TY 86,15872
M 128
Q 6
M 129
WL 4
Q 8
M 140
Q 10
M 141
TX 90,3072
TX 94,16896
M 130
Q 12
M 131
R 52
M 142
Q 14
M 143
TY 98,3584
TY 102,17408
M 0
R 54
M 1
WL 10
Q 16
M 12
Q 18
M 13
Q 20
M 2
Q 22
M 3
WL 8
Q 24
M 14
Q 26
M 15
Q 28
M 4
Q 30
M 5
WL 6
Q 32
M 16
Q 34
M 17
Q 36
M 6
Q 38
M 7
WL 2
Q 40
M 18
Q 42
M 19
Q 44
M 8
Q 46
M 9
SA 244,1792
AD 11,7
M 20
AD 12,8
AD 151,9
M 21
AW 147,10
AW 171,11
M 10
AW 195,12
M 11
R 56
M 22
M 23
R 58
M 24
M 25
M 26
M 27
M 28
M 29
M 30
M 31
M 32
M 33
M 34
M 35
WL 4
M 36
M 37
M 38
M 39
M 40
M 41
M 42
ae 44,68,864
M 43
WV 6
M 44
WL 0
M 45
s_barrier
R 72
M 46
mw 24
M 47
R 74
M 48
s_bfe_u32 s51, s50, 0x10011
TY 106,5128
TY 110,18952
M 49
ad 84,1584
W 0
M 50
v_mad_i32_i24 v47, v49, s51, v48
XL 22,128
M 51
XL 24,896
M 52
W 1
M 53
M 54
XL 26,1664
M 55
W 2
M 56
M 57
WL 1
AN 104,106
AN 105,107
M 58
mw 27
SH 106
SH 107
M 59
R 76
M 60
AN 106,106
AN 107,107
M 61
WL 2
AN 108,110
AN 109,111
M 62
SH 110
SH 111
M 63
AN 110,110
AN 111,111
M 64
TY 114,6664
TY 118,20488
M 65
ad 84,1590
M 66
W 0
XL 28,2432
M 67
M 68
M 69
W 1
M 70
M 71
R 78
M 72
W 2
M 73
WL 3
AN 112,114
AN 113,115
M 74
SH 114
SH 115
M 75
mw 48
AN 114,114
AN 115,115
M 76
WL 2
AN 116,118
AN 117,119
M 77
SH 118
SH 119
M 78
AN 118,118
AN 119,119
M 79
TY 122,8200
TY 126,22024
M 80
ad 84,1632
W 0
M 81
SA 246,2560
M 82
M 83
R 80
M 84
W 1
M 85
M 86
M 87
W 2
M 88
WL 3
AN 120,122
AN 121,123
M 89
SH 122
SH 123
M 90
mw 51
AN 122,122
AN 123,123
M 91
WL 2
AN 124,126
AN 125,127
M 92
SH 126
SH 127
M 93
AN 126,126
AN 127,127
M 94
ad 84,1638
W 0
M 95
R 82
M 96
UL 6,14
UH 7,14
M 97
W 1
)"
R"(UL 8,15
M 98
UH 9,15
AR 10,148
M 99
AR 11,172
AR 12,196
M 100
W 2
AD 10,6
M 101
AD 11,7
AD 12,8
M 102
AD 152,9
AW 148,10
M 103
bc 49,18,66,60
AW 172,11
M 104
mh 0
AW 196,12
UL 6,16
M 105
ad 66,3520
H 0
M 106
UH 7,16
UL 8,17
M 107
R 0
M 108
DG 1024
M 109
UH 9,17
AR 10,149
M 110
AR 11,173
AR 12,197
M 111
bc 48,18,64,56
AD 10,6
M 112
mh 3
AD 11,7
AD 12,8
M 113
ad 64,3520
H 0
M 114
AD 153,9
AW 149,10
M 115
AW 173,11
AW 197,12
M 116
H 1
UL 6,18
M 117
UH 7,18
UL 8,19
M 118
UH 9,19
AR 10,150
M 119
WL 0
R 2
M 120
H 2
AR 11,174
M 121
AR 12,198
AD 10,6
M 132
AD 11,7
AD 12,8
M 133
TX 58,4608
TX 62,18432
M 122
AD 154,9
AW 150,10
M 123
AW 174,11
AW 198,12
M 134
UL 6,20
UH 7,20
M 135
TY 66,5120
TY 70,18944
M 124
UL 8,21
UH 9,21
M 125
AR 10,151
AR 11,175
M 136
AR 12,199
AD 10,6
M 137
TX 74,6144
TX 78,19968
M 126
WL 4
Q 0
M 127
Q 2
M 138
Q 4
M 139
TY 82,6656
TY 86,20480
M 128
Q 6
M 129
WL 4
Q 8
M 140
Q 10
M 141
TX 90,7680
TX 94,21504
M 130
Q 12
M 131
R 4
M 142
Q 14
M 143
TY 98,8192
TY 102,22016
M 0
R 6
M 1
WL 10
Q 16
M 12
Q 18
M 13
Q 20
M 2
Q 22
M 3
WL 8
Q 24
M 14
Q 26
M 15
Q 28
M 4
Q 30
M 5
WL 6
Q 32
M 16
Q 34
M 17
Q 36
M 6
Q 38
M 7
WL 2
Q 40
M 18
Q 42
M 19
Q 44
M 8
Q 46
M 9
ae 42,72,480
M 20
s_bfe_u32 s52, s50, 0x1000b
v_mad_i32_i24 v50, v49, s52, v48
SA 248,256
M 21
AD 11,7
AD 12,8
M 10
AD 155,9
AW 151,10
M 11
R 8
M 22
AW 175,11
AW 199,12
M 23
R 10
M 24
M 25
M 26
M 27
M 28
M 29
M 30
M 31
M 32
M 33
M 34
M 35
WL 4
M 36
M 37
M 38
M 39
M 40
M 41
M 42
M 43
WV 6
M 44
WL 0
M 45
s_barrier
R 24
M 46
mw 72
M 47
R 26
M 48
TY 106,9736
TY 110,23560
M 49
ad 84,1680
W 0
M 50
M 51
M 52
W 1
M 53
M 54
M 55
W 2
M 56
M 57
WL 1
AN 104,106
AN 105,107
M 58
mw 75
SH 106
SH 107
M 59
R 28
M 60
AN 106,106
AN 107,107
M 61
WL 2
AN 108,110
AN 109,111
M 62
SH 110
SH 111
M 63
AN 110,110
AN 111,111
M 64
TY 114,11272
TY 118,25096
M 65
ad 84,1686
M 66
W 0
M 67
M 68
M 69
W 1
M 70
ae 44,70,768
M 71
R 30
M 72
W 2
M 73
WL 3
AN 112,114
AN 113,115
M 74
SH 114
SH 115
M 75
mw 0
AN 114,114
AN 115,115
M 76
WL 2
AN 116,118
AN 117,119
M 77
SH 118
SH 119
M 78
AN 118,118
AN 119,119
M 79
TY 122,12808
TY 126,26632
M 80
ad 84,1728
W 0
M 81
s_bitcmp1_b32 s50, 12
s_cselect_b32 s51, 24576, 0
M 82
SA 250,1024
v_add_u32 v47, s51, v3
M 83
R 32
M 84
W 1
XL 30,256
M 85
XL 32,1024
M 86
XL 34,1792
M 87
W 2
M 88
WL 3
AN 120,122
AN 121,123
M 89
SH 122
SH 123
M 90
mw 3
AN 122,122
AN 123,123
M 91
WL 2
AN 124,126
AN 125,127
M 92
SH 126
SH 127
M 93
AN 126,126
AN 127,127
M 94
ad 84,1734
W 0
M 95
R 34
M 96
XL 36,2560
M 97
W 1
M 98
UL 6,22
UH 7,22
M 99
UL 8,23
UH 9,23
M 100
W 2
AR 10,152
M 101
AR 11,176
AD 10,6
M 102
AD 11,7
AD 132,8
M 103
bc 49,19,66,60
AD 156,9
M 104
mh 9
AW 152,10
AW 176,11
M 105
ad 66,3712
H 0
M 106
UL 6,24
UH 7,24
M 107
R 48
M 108
DG 1024
M 109
UL 8,25
UH 9,25
M 110
AR 10,153
AR 11,177
M 111
bc 48,19,64,56
AD 10,6
M 112
mh 12
AD 11,7
AD 133,8
M 113
ad 64,3712
H 0
M 114
AD 157,9
AW 153,10
)"
R"(M 115
AW 177,11
UL 6,26
M 116
H 1
UH 7,26
M 117
UL 8,27
UH 9,27
M 118
AR 10,154
AR 11,178
M 119
WL 0
R 50
M 120
H 2
AD 10,6
M 121
AD 11,7
AD 134,8
M 132
AD 158,9
AW 154,10
M 133
TX 58,9216
TX 62,23040
M 122
AW 178,11
UL 6,28
M 123
UH 7,28
UL 8,29
M 134
UH 9,29
AR 10,155
M 135
TY 66,9728
TY 70,23552
M 124
AR 11,179
AD 10,6
M 125
AD 11,7
AD 135,8
M 136
AD 159,9
AW 155,10
M 137
TX 74,10752
TX 78,24576
M 126
WL 4
Q 0
M 127
Q 2
M 138
Q 4
M 139
TY 82,11264
TY 86,25088
M 128
Q 6
M 129
WL 4
Q 8
M 140
Q 10
M 141
TX 90,12288
TX 94,26112
M 130
Q 12
M 131
R 52
M 142
Q 14
M 143
TY 98,12800
TY 102,26624
M 0
R 54
M 1
WL 10
Q 16
M 12
Q 18
M 13
Q 20
M 2
Q 22
M 3
WL 8
Q 24
M 14
Q 26
M 15
Q 28
M 4
Q 30
M 5
WL 6
Q 32
M 16
Q 34
M 17
Q 36
M 6
Q 38
M 7
WL 2
Q 40
M 18
Q 42
M 19
Q 44
M 8
Q 46
M 9
SA 252,1792
AW 179,11
M 20
M 21
M 10
M 11
R 56
M 22
M 23
R 58
M 24
M 25
M 26
M 27
M 28
M 29
M 30
M 31
M 32
M 33
M 34
M 35
WL 4
M 36
M 37
M 38
M 39
M 40
M 41
M 42
M 43
WV 6
M 44
WL 0
M 45
s_barrier
R 72
M 46
mw 24
M 47
R 74
M 48
TY 106,520
TY 110,14344
M 49
ad 84,1776
W 0
M 50
M 51
M 52
W 1
M 53
M 54
M 55
W 2
M 56
M 57
WL 1
AN 104,106
AN 105,107
M 58
mw 27
SH 106
SH 107
M 59
R 76
M 60
AN 106,106
AN 107,107
M 61
WL 2
AN 108,110
AN 109,111
M 62
SH 110
SH 111
M 63
AN 110,110
AN 111,111
M 64
TY 114,2056
TY 118,15880
M 65
ad 84,1782
M 66
W 0
M 67
M 68
M 69
W 1
M 70
M 71
R 78
M 72
W 2
M 73
WL 3
AN 112,114
AN 113,115
M 74
SH 114
SH 115
M 75
mw 48
AN 114,114
AN 115,115
M 76
WL 2
AN 116,118
AN 117,119
M 77
SH 118
SH 119
M 78
AN 118,118
AN 119,119
M 79
TY 122,3592
TY 126,17416
M 80
ad 84,1824
W 0
M 81
SA 254,2560
M 82
ae 44,70,768
M 83
R 80
M 84
s_bfe_u32 s51, s50, 0x1000d
W 1
v_mad_i32_i24 v47, v49, s51, v48
M 85
XL 38,256
M 86
XL 40,1024
M 87
W 2
XL 42,1792
M 88
WL 3
AN 120,122
AN 121,123
M 89
SH 122
SH 123
M 90
mw 51
AN 122,122
AN 123,123
M 91
WL 2
AN 124,126
AN 125,127
M 92
SH 126
SH 127
M 93
AN 126,126
AN 127,127
M 94
ad 84,1830
W 0
M 95
R 82
M 96
XL 44,2560
M 97
W 1
M 98
M 99
M 100
W 2
M 101
M 102
M 103
bc 49,20,66,60
M 104
mh 18
M 105
ad 66,3904
H 0
M 106
M 107
R 0
M 108
DG 1024
M 109
M 110
M 111
bc 48,20,64,56
M 112
mh 21
M 113
ad 64,3904
H 0
M 114
M 115
M 116
H 1
M 117
M 118
M 119
WL 2
R 2
M 120
H 2
M 121
M 132
M 133
TX 58,0
TX 62,13824
M 122
M 123
M 134
UL 6,30
UH 7,30
M 135
TY 66,512
TY 70,14336
M 124
UL 8,31
UH 9,31
M 125
AD 160,6
AD 184,7
M 136
AD 208,8
AD 232,9
M 137
TX 74,1536
TX 78,15360
M 126
WL 4
Q 0
M 127
Q 2
M 138
Q 4
M 139
TY 82,2048
TY 86,15872
M 128
Q 6
M 129
WL 4
Q 8
M 140
Q 10
M 141
TX 90,3072
TX 94,16896
M 130
Q 12
M 131
R 4
M 142
Q 14
M 143
TY 98,3584
TY 102,17408
M 0
R 6
M 1
WL 10
Q 16
M 12
Q 18
M 13
Q 20
M 2
Q 22
M 3
WL 8
Q 24
M 14
Q 26
M 15
Q 28
M 4
Q 30
)"
R"(M 5
WL 6
Q 32
M 16
Q 34
M 17
Q 36
M 6
Q 38
M 7
WL 2
Q 40
M 18
Q 42
M 19
Q 44
M 8
Q 46
M 9
UL 6,32
UH 7,32
M 20
UL 8,33
UH 9,33
M 21
AD 161,6
AD 185,7
M 10
AD 209,8
AD 233,9
M 11
R 8
M 22
UL 6,34
UH 7,34
M 23
R 10
M 24
UL 8,35
UH 9,35
M 25
AD 162,6
AD 186,7
M 26
AD 210,8
AD 234,9
M 27
UL 6,36
UH 7,36
M 28
UL 8,37
UH 9,37
M 29
AD 163,6
AD 187,7
M 30
AD 211,8
AD 235,9
M 31
M 32
M 33
M 34
M 35
WL 4
M 36
M 37
M 38
M 39
M 40
M 41
M 42
M 43
WV 5
M 44
WL 0
M 45
s_barrier
R 24
M 46
mw 72
M 47
R 26
M 48
TY 106,5128
TY 110,18952
M 49
ad 84,1872
W 0
M 50
M 51
M 52
W 1
M 53
M 54
M 55
W 2
M 56
M 57
WL 1
AN 104,106
AN 105,107
M 58
mw 75
SH 106
SH 107
M 59
R 28
M 60
AN 106,106
AN 107,107
M 61
WL 2
AN 108,110
AN 109,111
M 62
SH 110
SH 111
M 63
AN 110,110
AN 111,111
M 64
TY 114,6664
TY 118,20488
M 65
ad 84,1878
M 66
W 0
M 67
M 68
M 69
W 1
M 70
M 71
R 30
M 72
W 2
M 73
WL 3
AN 112,114
AN 113,115
M 74
SH 114
SH 115
M 75
mw 0
AN 114,114
AN 115,115
M 76
WL 2
AN 116,118
AN 117,119
M 77
SH 118
SH 119
M 78
AN 118,118
AN 119,119
M 79
TY 122,8200
TY 126,22024
M 80
ad 84,1920
W 0
M 81
ae 44,68,768
M 82
s_bitcmp1_b32 s50, 14
s_cselect_b32 s51, 24576, 0
v_add_u32 v47, s51, v3
M 83
R 32
M 84
W 1
XL 14,256
M 85
XL 16,1024
M 86
M 87
W 2
M 88
WL 3
AN 120,122
AN 121,123
M 89
SH 122
SH 123
M 90
mw 3
AN 122,122
AN 123,123
M 91
WL 2
AN 124,126
AN 125,127
M 92
SH 126
SH 127
M 93
AN 126,126
AN 127,127
M 94
ad 84,1926
W 0
M 95
R 34
M 96
XL 18,1792
M 97
W 1
XL 20,2560
M 98
M 99
M 100
W 2
M 101
M 102
M 103
bc 49,21,66,60
M 104
mh 0
M 105
ad 66,4096
H 0
M 106
M 107
R 48
M 108
DG 1024
M 109
M 110
M 111
bc 48,21,64,56
M 112
mh 3
M 113
ad 64,4096
H 0
M 114
M 115
M 116
H 1
M 117
M 118
M 119
WL 2
R 50
M 120
H 2
M 121
M 132
M 133
TX 58,4608
TX 62,18432
M 122
M 123
M 134
M 135
TY 66,5120
TY 70,18944
M 124
M 125
M 136
UL 6,38
UH 7,38
M 137
TX 74,6144
TX 78,19968
M 126
WL 4
Q 0
M 127
Q 2
M 138
Q 4
M 139
TY 82,6656
TY 86,20480
M 128
Q 6
M 129
WL 4
Q 8
M 140
Q 10
M 141
TX 90,7680
TX 94,21504
M 130
Q 12
M 131
R 52
M 142
Q 14
M 143
TY 98,8192
TY 102,22016
M 0
R 54
M 1
WL 10
Q 16
M 12
Q 18
M 13
Q 20
M 2
Q 22
M 3
WL 8
Q 24
M 14
Q 26
M 15
Q 28
M 4
Q 30
M 5
WL 6
Q 32
M 16
Q 34
M 17
Q 36
M 6
Q 38
M 7
WL 2
Q 40
M 18
Q 42
M 19
Q 44
M 8
Q 46
M 9
UL 8,39
UH 9,39
M 20
AD 164,6
AD 188,7
M 21
AD 212,8
AD 236,9
M 10
UL 6,40
UH 7,40
M 11
R 56
M 22
UL 8,41
UH 9,41
M 23
R 58
M 24
AD 165,6
AD 189,7
M 25
AD 213,8
AD 237,9
M 26
UL 6,42
UH 7,42
M 27
UL 8,43
UH 9,43
M 28
AD 166,6
AD 190,7
M 29
AD 214,8
AD 238,9
M 30
UL 6,44
UH 7,44
M 31
UL 8,45
UH 9,45
M 32
AD 167,6
AD 191,7
M 33
AD 215,8
AD 239,9
M 34
M 35
WL 2
M 36
M 37
M 38
M 39
M 40
M 41
M 42
M 43
WV 5
M 44
WL 0
M 45
s_barrier
R 72
)"
R"(M 46
mw 24
M 47
R 74
M 48
TY 106,9736
TY 110,23560
M 49
ad 84,1968
W 0
M 50
M 51
M 52
W 1
M 53
M 54
M 55
W 2
M 56
M 57
WL 1
AN 104,106
AN 105,107
M 58
mw 27
SH 106
SH 107
M 59
R 76
M 60
AN 106,106
AN 107,107
M 61
WL 2
AN 108,110
AN 109,111
M 62
SH 110
SH 111
M 63
AN 110,110
AN 111,111
M 64
TY 114,11272
TY 118,25096
M 65
ad 84,1974
M 66
W 0
M 67
M 68
M 69
W 1
M 70
M 71
R 78
M 72
W 2
M 73
WL 3
AN 112,114
AN 113,115
M 74
SH 114
SH 115
M 75
mw 48
AN 114,114
AN 115,115
M 76
WL 2
AN 116,118
AN 117,119
M 77
SH 118
SH 119
M 78
AN 118,118
AN 119,119
M 79
TY 122,12808
TY 126,26632
M 80
ad 84,2016
W 0
M 81
M 82
ae 44,68,768
M 83
R 80
M 84
s_bfe_u32 s51, s50, 0x1000f
W 1
v_mad_i32_i24 v47, v49, s51, v48
M 85
XL 22,256
M 86
M 87
W 2
M 88
WL 3
AN 120,122
AN 121,123
M 89
SH 122
SH 123
M 90
mw 51
AN 122,122
AN 123,123
M 91
WL 2
AN 124,126
AN 125,127
M 92
SH 126
SH 127
M 93
AN 126,126
AN 127,127
M 94
ad 84,2022
W 0
M 95
R 82
M 96
XL 24,1024
M 97
W 1
XL 26,1792
M 98
M 99
M 100
W 2
XL 28,2560
M 101
M 102
M 103
bc 49,22,66,60
M 104
mh 9
M 105
ad 66,4288
H 0
M 106
M 107
R 0
M 108
DG 1024
M 109
M 110
M 111
bc 48,22,64,56
M 112
mh 12
M 113
ad 64,4288
H 0
M 114
M 115
M 116
H 1
M 117
M 118
M 119
WL 2
R 2
M 120
H 2
M 121
M 132
M 133
TX 58,9216
TX 62,23040
M 122
M 123
M 134
M 135
TY 66,9728
TY 70,23552
M 124
M 125
M 136
M 137
TX 74,10752
TX 78,24576
M 126
WL 4
Q 0
M 127
Q 2
M 138
Q 4
M 139
TY 82,11264
TY 86,25088
M 128
Q 6
M 129
WL 4
Q 8
M 140
Q 10
M 141
TX 90,12288
TX 94,26112
M 130
Q 12
M 131
R 4
M 142
Q 14
M 143
TY 98,12800
TY 102,26624
M 0
R 6
M 1
WL 10
Q 16
M 12
Q 18
M 13
Q 20
M 2
Q 22
M 3
WL 8
Q 24
M 14
Q 26
M 15
Q 28
M 4
Q 30
M 5
WL 6
Q 32
M 16
Q 34
M 17
Q 36
M 6
Q 38
M 7
WL 2
Q 40
M 18
Q 42
M 19
Q 44
M 8
Q 46
M 9
UL 6,14
UH 7,14
M 20
UL 8,15
UH 9,15
M 21
AD 168,6
AD 192,7
M 10
AD 216,8
AD 240,9
M 11
R 8
M 22
UL 6,16
UH 7,16
M 23
R 10
M 24
UL 8,17
UH 9,17
M 25
AD 169,6
AD 193,7
M 26
AD 217,8
AD 241,9
M 27
UL 6,18
UH 7,18
M 28
UL 8,19
UH 9,19
M 29
AD 170,6
AD 194,7
M 30
AD 218,8
AD 242,9
M 31
UL 6,20
UH 7,20
M 32
UL 8,21
UH 9,21
M 33
AD 171,6
AD 195,7
M 34
AD 219,8
AD 243,9
M 35
WL 2
M 36
M 37
M 38
M 39
M 40
M 41
M 42
M 43
WV 6
M 44
WL 0
M 45
s_barrier
R 24
M 46
mw 72
M 47
R 26
M 48
TY 106,520
TY 110,14344
M 49
ad 84,2064
W 0
M 50
M 51
M 52
W 1
M 53
M 54
M 55
W 2
M 56
M 57
WL 1
AN 104,106
AN 105,107
M 58
mw 75
SH 106
SH 107
M 59
R 28
M 60
AN 106,106
AN 107,107
M 61
WL 2
AN 108,110
AN 109,111
M 62
SH 110
SH 111
M 63
AN 110,110
AN 111,111
M 64
TY 114,2056
TY 118,15880
M 65
ad 84,2070
M 66
W 0
M 67
M 68
M 69
W 1
M 70
M 71
R 30
M 72
W 2
M 73
WL 3
AN 112,114
AN 113,115
M 74
SH 114
SH 115
M 75
mw 0
AN 114,114
AN 115,115
M 76
WL 2
AN 116,118
AN 117,119
M 77
SH 118
SH 119
M 78
AN 118,118
AN 119,119
)"
R"(M 79
TY 122,3592
TY 126,17416
M 80
ad 84,2112
W 0
M 81
M 82
M 83
R 32
M 84
W 1
M 85
M 86
ae 44,68,864
M 87
W 2
M 88
WL 3
AN 120,122
AN 121,123
M 89
SH 122
SH 123
M 90
mw 3
AN 122,122
AN 123,123
M 91
WL 2
AN 124,126
AN 125,127
M 92
SH 126
SH 127
M 93
AN 126,126
AN 127,127
M 94
ad 84,2118
W 0
M 95
R 34
M 96
s_bitcmp1_b32 s50, 16
s_cselect_b32 s51, 24576, 0
M 97
W 1
v_add_u32 v47, s51, v3
M 98
XL 30,256
M 99
XL 32,1024
M 100
W 2
XL 34,1792
M 101
M 102
M 103
bc 49,23,66,60
M 104
mh 18
XL 36,2560
M 105
ad 66,4480
H 0
M 106
M 107
R 48
M 108
DG 1024
M 109
M 110
M 111
bc 48,23,64,56
M 112
mh 21
M 113
ad 64,4480
H 0
M 114
M 115
M 116
H 1
M 117
M 118
M 119
WL 2
R 50
M 120
H 2
M 121
M 132
M 133
TX 58,0
TX 62,13824
M 122
M 123
M 134
M 135
TY 66,512
TY 70,14336
M 124
M 125
M 136
M 137
TX 74,1536
TX 78,15360
M 126
WL 4
Q 0
M 127
Q 2
M 138
Q 4
M 139
TY 82,2048
TY 86,15872
M 128
Q 6
M 129
WL 4
Q 8
M 140
Q 10
M 141
TX 90,3072
TX 94,16896
M 130
Q 12
M 131
R 52
M 142
Q 14
M 143
TY 98,3584
TY 102,17408
M 0
R 54
M 1
WL 10
Q 16
M 12
Q 18
M 13
Q 20
M 2
Q 22
M 3
WL 8
Q 24
M 14
Q 26
M 15
Q 28
M 4
Q 30
M 5
WL 6
Q 32
M 16
Q 34
M 17
Q 36
M 6
Q 38
M 7
WL 2
Q 40
M 18
Q 42
M 19
Q 44
M 8
Q 46
M 9
UL 6,22
UH 7,22
M 20
UL 8,23
UH 9,23
M 21
AD 172,6
AD 196,7
M 10
AD 220,8
AD 244,9
M 11
R 56
M 22
UL 6,24
UH 7,24
M 23
R 58
M 24
UL 8,25
UH 9,25
M 25
AD 173,6
AD 197,7
M 26
AD 221,8
AD 245,9
M 27
UL 6,26
UH 7,26
M 28
UL 8,27
UH 9,27
M 29
AD 174,6
AD 198,7
M 30
AD 222,8
AD 246,9
M 31
WV 26
UL 6,28
UH 7,28
M 32
UL 8,29
UH 9,29
M 33
AD 175,6
AD 199,7
M 34
AD 223,8
AD 247,9
M 35
WL 2
M 36
M 37
M 38
M 39
M 40
M 41
M 42
M 43
WV 7
M 44
WL 0
M 45
s_barrier
R 72
M 46
mw 24
M 47
R 74
M 48
TY 106,5128
TY 110,18952
M 49
ad 84,2160
W 0
M 50
M 51
M 52
W 1
M 53
M 54
M 55
W 2
M 56
M 57
WL 1
AN 104,106
AN 105,107
M 58
mw 27
SH 106
SH 107
M 59
R 76
M 60
AN 106,106
AN 107,107
M 61
WL 2
AN 108,110
AN 109,111
M 62
SH 110
SH 111
M 63
AN 110,110
AN 111,111
M 64
TY 114,6664
TY 118,20488
M 65
ad 84,2166
M 66
W 0
M 67
M 68
M 69
W 1
M 70
M 71
R 78
M 72
W 2
M 73
WL 3
AN 112,114
AN 113,115
M 74
SH 114
SH 115
M 75
mw 48
AN 114,114
AN 115,115
M 76
WL 2
AN 116,118
AN 117,119
M 77
SH 118
SH 119
M 78
AN 118,118
AN 119,119
M 79
TY 122,8200
TY 126,22024
M 80
ad 84,2208
W 0
M 81
M 82
M 83
R 80
M 84
W 1
M 85
M 86
M 87
W 2
M 88
WL 3
AN 120,122
AN 121,123
M 89
SH 122
SH 123
M 90
mw 51
AN 122,122
AN 123,123
M 91
WL 2
AN 124,126
AN 125,127
M 92
SH 126
SH 127
M 93
AN 126,126
AN 127,127
M 94
ad 84,2214
W 0
M 95
R 82
M 96
ae 44,68,864
M 97
s_bfe_u32 s51, s50, 0x10011
W 1
v_mad_i32_i24 v47, v49, s51, v48
M 98
XL 38,256
M 99
XL 40,1024
M 100
W 2
M 101
M 102
XL 42,1792
M 103
bc 48,0,64,56
M 104
mh 3
M 105
ad 64,64
H 0
M 106
M 107
R 0
M 108
H 1
XL 44,2560
M 109
M 110
M 111
H 2
M 112
M 113
M 114
)"
R"(M 115
M 116
M 117
M 118
M 119
WL 2
R 2
M 120
M 121
M 132
M 133
TX 58,4608
TX 62,18432
M 122
M 123
M 134
M 135
TY 66,5120
TY 70,18944
M 124
M 125
M 136
M 137
TX 74,6144
TX 78,19968
M 126
WL 4
Q 0
M 127
Q 2
M 138
Q 4
M 139
TY 82,6656
TY 86,20480
M 128
Q 6
M 129
WL 4
Q 8
M 140
Q 10
M 141
TX 90,7680
TX 94,21504
M 130
Q 12
M 131
R 4
M 142
Q 14
M 143
TY 98,8192
TY 102,22016
M 0
R 6
M 1
WL 10
Q 16
M 12
Q 18
M 13
Q 20
M 2
Q 22
M 3
WL 8
Q 24
M 14
Q 26
M 15
Q 28
M 4
Q 30
M 5
WL 6
Q 32
M 16
Q 34
M 17
Q 36
M 6
Q 38
M 7
WL 2
Q 40
M 18
Q 42
M 19
Q 44
M 8
Q 46
M 9
UL 6,30
UH 7,30
M 20
UL 8,31
UH 9,31
M 21
AD 176,6
AD 200,7
M 10
AD 224,8
AD 248,9
M 11
R 8
M 22
UL 6,32
UH 7,32
M 23
R 10
M 24
UL 8,33
UH 9,33
M 25
AD 177,6
AD 201,7
M 26
AD 225,8
AD 249,9
M 27
WV 24
UL 6,34
UH 7,34
M 28
UL 8,35
UH 9,35
M 29
AD 178,6
AD 202,7
M 30
AD 226,8
AD 250,9
M 31
UL 6,36
UH 7,36
M 32
UL 8,37
UH 9,37
M 33
AD 179,6
AD 203,7
M 34
AD 227,8
AD 251,9
M 35
WL 2
M 36
M 37
M 38
M 39
M 40
M 41
M 42
M 43
WV 13
M 44
WL 0
M 45
s_barrier
R 24
M 46
mw 72
M 47
R 26
M 48
TY 106,9736
TY 110,23560
M 49
ad 84,2256
W 0
M 50
M 51
M 52
W 1
M 53
M 54
M 55
W 2
M 56
M 57
WL 1
AN 104,106
AN 105,107
M 58
mw 75
SH 106
SH 107
M 59
R 28
M 60
AN 106,106
AN 107,107
M 61
WL 2
AN 108,110
AN 109,111
M 62
SH 110
SH 111
M 63
AN 110,110
AN 111,111
M 64
TY 114,11272
TY 118,25096
M 65
ad 84,2262
M 66
W 0
M 67
M 68
M 69
W 1
M 70
M 71
R 30
M 72
W 2
M 73
WL 3
AN 112,114
AN 113,115
M 74
SH 114
SH 115
M 75
mw 0
AN 114,114
AN 115,115
M 76
WL 2
AN 116,118
AN 117,119
M 77
SH 118
SH 119
M 78
AN 118,118
AN 119,119
M 79
TY 122,12808
TY 126,26632
M 80
ad 84,0
W 0
M 81
M 82
M 83
R 32
M 84
W 1
M 85
M 86
M 87
W 2
M 88
WL 3
AN 120,122
AN 121,123
M 89
SH 122
SH 123
M 90
mw 3
AN 122,122
AN 123,123
M 91
WL 2
AN 124,126
AN 125,127
M 92
SH 126
SH 127
M 93
AN 126,126
AN 127,127
M 94
ad 84,6
W 0
M 95
R 34
M 96
M 97
W 1
M 98
M 99
M 100
W 2
M 101
M 102
M 103
bc 48,1,64,56
M 104
mh 15
M 105
ad 64,259
H 0
M 106
M 107
M 108
DG 1024
M 109
M 110
M 111
M 112
M 113
M 114
M 115
WV 19
M 116
WL 0
M 117
s_barrier
R 48
M 118
mw 24
M 119
R 50
M 120
ad 84,48
W 0
M 121
M 132
M 133
TX 58,9216
TX 62,23040
M 122
W 1
M 123
M 134
M 135
TY 66,9728
TY 70,23552
M 124
W 2
M 125
M 136
M 137
TX 74,10752
TX 78,24576
M 126
mw 27
WL 4
Q 0
M 127
Q 2
M 138
Q 4
M 139
TY 82,11264
TY 86,25088
M 128
Q 6
M 129
WL 4
Q 8
M 140
Q 10
M 141
TX 90,12288
TX 94,26112
M 130
Q 12
M 131
R 52
M 142
Q 14
M 143
TY 98,12800
TY 102,26624
M 0
R 54
M 1
WL 10
Q 16
M 12
Q 18
M 13
Q 20
M 2
Q 22
M 3
WL 8
Q 24
M 14
Q 26
M 15
Q 28
M 4
Q 30
M 5
WL 6
Q 32
M 16
Q 34
M 17
Q 36
M 6
Q 38
)"
R"(M 7
WL 2
Q 40
M 18
Q 42
M 19
Q 44
M 8
Q 46
M 9
ad 84,54
M 20
W 0
UL 6,38
M 21
UH 7,38
UL 8,39
M 10
UH 9,39
AD 180,6
M 11
R 56
M 22
W 1
AD 204,7
M 23
R 58
M 24
AD 228,8
AD 252,9
M 25
UL 6,40
UH 7,40
M 26
W 2
UL 8,41
BV 52,4608
M 27
UH 9,41
AD 181,6
M 28
AD 205,7
AD 229,8
M 29
WV 24
AD 253,9
UL 6,42
M 30
UH 7,42
UL 8,43
M 31
UH 9,43
AD 182,6
M 32
AD 206,7
AD 230,8
M 33
WV 21
AD 254,9
UL 6,44
M 34
UH 7,44
UL 8,45
M 35
WL 2
UH 9,45
AD 183,6
M 36
AD 207,7
AD 231,8
M 37
AD 255,9
M 38
M 39
M 40
ds_read_b64 v[12:13], v4 offset:4864
ds_read_b64 v[46:47], v4 offset:4872
M 41
AR 6,36
AR 7,60
M 42
AR 8,84
AR 9,108
M 43
WL 2
EF 6,52
EF 7,53
M 44
WV 14
EF 8,54
EF 9,55
M 45
s_barrier
R 72
M 46
mw 48
CV 14,6,7
CV 15,8,9
M 47
R 74
M 48
TY 114,2056
TY 118,15880
M 49
ad 84,96
W 0
M 50
AR 6,37
AR 7,61
M 51
AR 8,85
AR 9,109
M 52
EF 6,52
EF 7,53
M 53
W 1
EF 8,54
M 54
EF 9,55
CV 16,6,7
M 55
CV 17,8,9
AR 6,38
M 56
AR 7,62
AR 8,86
M 57
WL 1
AN 112,114
AN 113,115
M 58
SH 114
SH 115
M 59
R 76
M 60
AN 114,114
AN 115,115
M 61
WL 2
AN 116,118
AN 117,119
M 62
SH 118
SH 119
M 63
AN 118,118
AN 119,119
M 64
TY 122,3592
TY 126,17416
M 65
W 2
AR 9,110
M 66
EF 6,52
EF 7,53
M 67
EF 8,54
EF 9,55
M 68
CV 18,6,7
CV 19,8,9
M 69
mw 51
AR 6,39
AR 7,63
M 70
ad 84,102
W 0
M 71
R 78
M 72
AR 8,87
AR 9,111
M 73
WL 3
AN 120,122
AN 121,123
M 74
SH 122
SH 123
M 75
AN 122,122
AN 123,123
M 76
WL 2
AN 124,126
AN 125,127
M 77
SH 126
SH 127
M 78
AN 126,126
AN 127,127
M 79
W 1
EF 6,52
M 80
EF 7,53
EF 8,54
M 81
EF 9,55
CV 20,6,7
M 82
CV 21,8,9
M 83
R 80
M 84
W 2
AR 6,40
M 85
ae 42,74,768
AR 7,64
M 86
s_bitcmp1_b32 s50, 12
s_cselect_b32 s52, 24576, 0
v_add_u32 v50, s52, v3
M 87
ST 14,0
AR 8,88
M 88
AR 9,112
EF 6,52
M 89
EF 7,53
EF 8,54
M 90
EF 9,55
CV 22,6,7
M 91
ST 16,768
CV 23,8,9
M 92
AR 6,41
AR 7,65
M 93
AR 8,89
AR 9,113
M 94
EF 6,52
EF 7,53
M 95
R 82
M 96
ST 18,1536
EF 8,54
M 97
EF 9,55
CV 24,6,7
M 98
CV 25,8,9
AR 6,42
M 99
AR 7,66
AR 8,90
M 100
ST 20,2304
AR 9,114
M 101
EF 6,52
EF 7,53
M 102
EF 8,54
EF 9,55
M 103
CV 26,6,7
CV 27,8,9
M 104
AR 6,43
AR 7,67
M 105
AR 8,91
AR 9,115
M 106
EF 6,52
EF 7,53
M 107
WL 2
EF 8,54
EF 9,55
M 108
CV 28,6,7
CV 29,8,9
M 109
ae 42,74,768
BV 52,5120
M 110
s_bfe_u32 s52, s50, 0x1000d
v_mad_i32_i24 v50, v49, s52, v48
ST 22,0
M 111
AR 6,132
AR 7,156
M 112
AR 8,180
EF 6,12
M 113
EF 7,13
EF 8,46
M 114
ST 24,768
EF 136,47
M 115
WV 20
CV 30,6,7
CV 31,8,136
M 116
WL 1
AR 6,133
AR 7,157
M 117
s_barrier
R 0
M 118
mw 72
ST 26,1536
AR 8,181
M 119
R 2
M 120
ad 84,144
W 0
M 121
EF 6,12
EF 7,13
M 132
ST 28,2304
EF 8,46
M 133
EF 137,47
CV 32,6,7
M 122
W 1
CV 33,8,137
M 123
AR 6,134
AR 7,158
M 134
AR 8,182
EF 6,12
M 135
EF 7,13
EF 8,46
M 124
W 2
EF 138,47
M 125
CV 34,6,7
CV 35,8,138
M 136
AR 6,135
AR 7,159
M 137
TX 74,1536
TX 78,15360
M 126
mw 75
AR 8,183
EF 6,12
M 127
ad 84,150
W 0
M 138
EF 7,13
EF 8,46
M 139
TY 82,2048
TY 86,15872
M 128
EF 139,47
CV 36,6,7
M 129
W 1
CV 37,8,139
M 140
ae 42,74,768
M 141
WL 2
TX 90,3072
TX 94,16896
P 16
M 130
Q 17
P 19
M 131
R 4
P 20
M 142
Q 21
P 23
M 143
WL 4
TY 98,3584
TY 102,17408
P 24
M 4
R 6
P 25
)"
R"(M 5
Q 26
P 28
M 16
Q 29
P 31
M 17
WL 6
Q 32
M 6
Q 34
M 7
Q 36
M 18
Q 38
M 19
WL 2
Q 40
M 8
Q 42
M 9
Q 44
M 20
Q 46
M 21
W 2
AR 6,136
M 10
s_bitcmp1_b32 s50, 12
s_cselect_b32 s52, 24576, 0
M 11
R 8
M 22
v_add_u32 v50, s52, v3
ST 30,128
M 23
R 10
M 28
AR 7,160
AR 8,184
M 29
EF 6,12
EF 7,13
M 30
ST 32,896
EF 8,46
M 31
EF 140,47
CV 38,6,7
M 32
CV 39,8,140
AR 6,137
M 33
AR 7,161
AR 8,185
M 34
ST 34,1664
EF 6,12
M 35
WL 4
EF 7,13
EF 8,46
M 40
EF 141,47
CV 40,6,7
M 41
CV 41,8,141
AR 6,138
M 42
ST 36,2432
AR 7,162
M 43
AR 8,186
EF 6,12
M 44
WV 24
EF 7,13
EF 8,46
M 45
WL 0
EF 142,47
CV 42,6,7
M 46
s_barrier
R 24
M 47
R 26
M 52
TY 122,8200
TY 126,22024
M 53
CV 43,8,142
AR 6,139
M 54
AR 7,163
AR 8,187
M 55
EF 6,12
EF 7,13
M 56
EF 8,46
EF 143,47
M 57
CV 44,6,7
CV 45,8,143
M 58
ae 42,74,768
ds_read_b64 v[12:13], v4 offset:4608
ds_read_b64 v[46:47], v4 offset:4616
M 59
R 28
M 64
WL 5
EF 160,52
EF 184,53
M 65
s_bfe_u32 s52, s50, 0x1000d
AN 120,122
AN 121,123
M 66
SH 122
SH 123
M 67
AN 122,122
AN 123,123
M 68
WL 4
AN 124,126
AN 125,127
M 69
SH 126
SH 127
M 70
AN 126,126
AN 127,127
M 71
R 30
M 76
v_mad_i32_i24 v50, v49, s52, v48
ST 38,128
M 77
EF 208,54
EF 232,55
M 78
CV 14,160,184
CV 15,208,232
M 79
EF 161,52
EF 185,53
M 80
ST 40,896
EF 209,54
M 81
EF 233,55
CV 16,161,185
M 82
CV 17,209,233
EF 162,52
M 83
R 32
M 88
TX 58,7680
TX 62,21504
M 89
ST 42,1664
EF 186,53
M 90
EF 210,54
EF 234,55
M 91
CV 18,162,186
CV 19,210,234
M 92
TY 66,8192
TY 70,22016
M 93
ST 44,2432
EF 163,52
M 94
EF 187,53
EF 211,54
M 95
WL 6
R 34
M 100
EF 235,55
CV 20,163,187
M 101
WL 4
PA_ 56,120,58
PA_ 57,121,59
M 102
PB_ 58,120,58
PB_ 59,121,59
M 103
PA_ 60,124,62
PA_ 61,125,63
M 104
PB_ 62,124,62
PB_ 63,125,63
M 105
WL 2
PA_ 64,122,66
PA_ 65,123,67
M 106
PB_ 66,122,66
PB_ 67,123,67
M 107
PA_ 68,126,70
PA_ 69,127,71
M 112
PB_ 70,126,70
PB_ 71,127,71
M 113
CV 21,211,235
M 114
ae 42,74,768
EF 164,52
M 115
s_bitcmp1_b32 s50, 12
s_cselect_b32 s52, 24576, 0
v_add_u32 v50, s52, v3
M 116
WV 22
ST 14,256
EF 188,53
M 117
WL 0
EF 212,54
EF 236,55
M 118
s_barrier
R 48
M 119
R 50
M 124
ST 16,1024
CV 22,164,188
M 125
CV 23,212,236
EF 165,52
M 136
EF 189,53
EF 213,54
M 137
EF 237,55
CV 24,165,189
M 126
ST 18,1792
CV 25,213,237
M 127
EF 166,52
EF 190,53
M 138
EF 214,54
EF 238,55
M 139
CV 26,166,190
CV 27,214,238
M 128
ST 20,2560
EF 167,52
M 129
EF 191,53
EF 215,54
M 140
EF 239,55
CV 28,167,191
M 141
CV 29,215,239
M 130
ae 42,74,768
BV 52,4864
M 131
s_bfe_u32 s52, s50, 0x1000d
R 52
M 142
v_mad_i32_i24 v50, v49, s52, v48
ST 22,256
M 143
WL 3
R 54
MA 52,0,56
AR 6,44
AR 7,68
MA 52,4,60
AR 8,92
AR 9,116
MA 76,8,56
ST 24,1024
EF 6,12
MA 76,12,60
EF 7,13
EF 8,46
MA 56,0,64
EF 9,47
CV 30,6,7
MA 56,4,68
R 56
MA 80,8,64
ST 26,1792
CV 31,8,9
MA 80,12,68
WL 4
R 58
MA 100,16,56
AR 6,45
AR 7,69
MA 100,20,60
AR 8,93
AR 9,117
MA 104,16,64
ST 28,2560
EF 6,12
MA 104,20,68
WL 4
EF 7,13
EF 8,46
MA 124,24,56
WV 16
EF 9,47
CV 32,6,7
MA 124,28,60
WL 0
CV 33,8,9
AR 6,46
MA 128,24,64
s_barrier
R 72
MA 128,28,68
R 74
MA 148,0,56
AR 7,70
AR 8,94
MA 148,4,60
AR 9,118
EF 6,12
MA 152,0,64
EF 7,13
EF 8,46
MA 152,4,68
R 76
MA 172,8,56
EF 9,47
CV 34,6,7
MA 172,12,60
CV 35,8,9
AR 6,47
MA 176,8,64
AR 7,71
AR 8,95
MA 176,12,68
WL 4
R 78
MA 196,16,56
AR 9,119
EF 6,12
MA 196,20,60
EF 7,13
EF 8,46
MV 132,16,64
EF 9,47
CV 36,6,7
MV 132,20,68
WL 4
R 80
MV 152,24,56
CV 37,8,9
MV 152,28,60
ae 42,72,768
AR 6,48
AR 7,72
MV 156,24,64
s_bitcmp1_b32 s50, 14
s_cselect_b32 s52, 24576, 0
v_add_u32 v50, s52, v3
AR 8,96
MV 156,28,68
WL 4
R 82
AR 9,120
MV 176,0,56
ST 30,0
EF 6,12
EF 7,13
MV 176,4,60
EF 8,46
EF 9,47
CV 38,6,7
MV 180,0,64
CV 39,8,9
AR 6,49
AR 7,73
MV 180,4,68
WL 4
AR 8,97
AR 9,121
EF 6,12
MV 200,8,56
ST 32,768
)"
R"(EF 7,13
EF 8,46
MV 200,12,60
EF 9,47
CV 40,6,7
CV 41,8,9
MV 204,8,64
AR 6,50
AR 7,74
AR 8,98
MV 204,12,68
WL 0
AR 9,122
EF 6,12
EF 7,13
MV 224,16,56
ST 34,1536
EF 8,46
EF 9,47
MV 224,20,60
CV 42,6,7
CV 43,8,9
AR 6,51
MV 248,24,56
AR 7,75
AR 8,99
AR 9,123
MV 248,28,60
EF 6,12
EF 7,13
EF 8,46
MV 228,16,64
ST 36,2304
EF 9,47
CV 44,6,7
MV 228,20,68
CV 45,8,9
MV 252,24,64
WV 14
ds_read_b64 v[12:13], v4 offset:5120
ds_read_b64 v[46:47], v4 offset:5128
AR 6,140
AR 7,164
AR 8,188
EF 6,52
MV 252,28,68
ae 42,72,768
s_bfe_u32 s52, s50, 0x1000f
v_mad_i32_i24 v50, v49, s52, v48
ST 38,0
ST 40,768
ST 42,1536
ST 44,2304
EF 7,53
EF 8,54
EF 144,55
CV 14,6,7
CV 15,8,144
AR 6,141
AR 7,165
AR 8,189
EF 6,52
EF 7,53
EF 8,54
EF 145,55
CV 16,6,7
CV 17,8,145
AR 6,142
AR 7,166
AR 8,190
EF 6,52
EF 7,53
EF 8,54
EF 146,55
CV 18,6,7
CV 19,8,146
AR 6,143
AR 7,167
AR 8,191
EF 6,52
EF 7,53
EF 8,54
EF 147,55
CV 20,6,7
CV 21,8,147
ae 42,72,768
s_bitcmp1_b32 s50, 14
s_cselect_b32 s52, 24576, 0
v_add_u32 v50, s52, v3
ST 14,128
ST 16,896
ST 18,1664
ST 20,2432
AR 6,144
AR 7,168
AR 8,192
EF 6,52
EF 7,53
EF 8,54
EF 148,55
CV 22,6,7
CV 23,8,148
AR 6,145
AR 7,169
AR 8,193
EF 6,52
EF 7,53
EF 8,54
EF 149,55
CV 24,6,7
CV 25,8,149
AR 6,146
AR 7,170
AR 8,194
EF 6,52
EF 7,53
EF 8,54
EF 150,55
CV 26,6,7
CV 27,8,150
AR 6,147
AR 7,171
AR 8,195
EF 6,52
EF 7,53
EF 8,54
EF 151,55
CV 28,6,7
CV 29,8,151
ae 42,72,768
s_bfe_u32 s52, s50, 0x1000f
v_mad_i32_i24 v50, v49, s52, v48
ST 22,128
ST 24,896
ST 26,1664
ST 28,2432
BV 52,4608
WL 1
EF 168,12
EF 192,13
EF 216,46
EF 240,47
CV 30,168,192
CV 31,216,240
EF 169,12
EF 193,13
EF 217,46
EF 241,47
CV 32,169,193
CV 33,217,241
EF 170,12
EF 194,13
EF 218,46
EF 242,47
CV 34,170,194
CV 35,218,242
EF 171,12
EF 195,13
EF 219,46
EF 243,47
CV 36,171,195
CV 37,219,243
ae 42,72,768
s_bitcmp1_b32 s50, 14
s_cselect_b32 s52, 24576, 0
v_add_u32 v50, s52, v3
ST 30,256
ST 32,1024
ST 34,1792
ST 36,2560
EF 172,12
EF 196,13
EF 220,46
EF 244,47
CV 38,172,196
CV 39,220,244
EF 173,12
EF 197,13
EF 221,46
EF 245,47
CV 40,173,197
CV 41,221,245
EF 174,12
EF 198,13
EF 222,46
EF 246,47
CV 42,174,198
CV 43,222,246
EF 175,12
EF 199,13
EF 223,46
EF 247,47
CV 44,175,199
CV 45,223,247
ae 42,72,768
s_bfe_u32 s52, s50, 0x1000f
v_mad_i32_i24 v50, v49, s52, v48
ST 38,256
ST 40,1024
ST 42,1792
ST 44,2560
ds_read_b64 v[12:13], v4 offset:4864
ds_read_b64 v[46:47], v4 offset:4872
AR 6,52
AR 7,76
AR 8,100
AR 9,124
WL 2
EF 6,52
EF 7,53
EF 8,54
EF 9,55
CV 14,6,7
CV 15,8,9
AR 6,53
AR 7,77
AR 8,101
AR 9,125
EF 6,52
EF 7,53
EF 8,54
EF 9,55
CV 16,6,7
CV 17,8,9
AR 6,54
AR 7,78
AR 8,102
AR 9,126
EF 6,52
EF 7,53
EF 8,54
EF 9,55
CV 18,6,7
CV 19,8,9
AR 6,55
AR 7,79
AR 8,103
AR 9,127
EF 6,52
EF 7,53
EF 8,54
EF 9,55
CV 20,6,7
CV 21,8,9
ae 42,72,864
s_bitcmp1_b32 s50, 16
s_cselect_b32 s52, 24576, 0
v_add_u32 v50, s52, v3
ST 14,0
ST 16,768
ST 18,1536
ST 20,2304
AR 6,56
AR 7,80
AR 8,104
AR 9,128
EF 6,52
EF 7,53
EF 8,54
EF 9,55
CV 22,6,7
CV 23,8,9
AR 6,57
AR 7,81
AR 8,105
AR 9,129
EF 6,52
EF 7,53
EF 8,54
EF 9,55
CV 24,6,7
CV 25,8,9
AR 6,58
AR 7,82
AR 8,106
AR 9,130
EF 6,52
EF 7,53
EF 8,54
EF 9,55
CV 26,6,7
CV 27,8,9
AR 6,59
AR 7,83
AR 8,107
AR 9,131
EF 6,52
EF 7,53
EF 8,54
EF 9,55
CV 28,6,7
CV 29,8,9
ae 42,72,864
s_bfe_u32 s52, s50, 0x10011
v_mad_i32_i24 v50, v49, s52, v48
ST 22,0
ST 24,768
ST 26,1536
ST 28,2304
BV 52,5120
AR 6,148
AR 7,172
AR 8,196
WL 1
EF 6,12
EF 7,13
EF 8,46
EF 152,47
CV 30,6,7
CV 31,8,152
AR 6,149
AR 7,173
AR 8,197
EF 6,12
EF 7,13
EF 8,46
EF 153,47
CV 32,6,7
CV 33,8,153
AR 6,150
AR 7,174
AR 8,198
EF 6,12
EF 7,13
EF 8,46
EF 154,47
CV 34,6,7
CV 35,8,154
AR 6,151
AR 7,175
AR 8,199
EF 6,12
EF 7,13
EF 8,46
EF 155,47
CV 36,6,7
CV 37,8,155
ae 42,72,864
s_bitcmp1_b32 s50, 16
s_cselect_b32 s52, 24576, 0
v_add_u32 v50, s52, v3
ST 30,128
ST 32,896
ST 34,1664
ST 36,2432
AR 6,152
AR 7,176
EF 6,12
EF 7,13
EF 132,46
EF 156,47
CV 38,6,7
CV 39,132,156
AR 6,153
AR 7,177
EF 6,12
EF 7,13
EF 133,46
EF 157,47
CV 40,6,7
CV 41,133,157
AR 6,154
AR 7,178
EF 6,12
EF 7,13
EF 134,46
EF 158,47
CV 42,6,7
CV 43,134,158
AR 6,155
AR 7,179
EF 6,12
EF 7,13
EF 135,46
EF 159,47
CV 44,6,7
CV 45,135,159
ae 42,72,864
s_bfe_u32 s52, s50, 0x10011
v_mad_i32_i24 v50, v49, s52, v48
ST 38,128
ST 40,896
ST 42,1664
ST 44,2432
WL 0
EF 176,52
EF 200,53
EF 224,54
EF 248,55
CV 14,176,200
CV 15,224,248
EF 177,52
EF 201,53
EF 225,54
EF 249,55
CV 16,177,201
CV 17,225,249
EF 178,52
EF 202,53
EF 226,54
)"
R"(EF 250,55
CV 18,178,202
CV 19,226,250
EF 179,52
EF 203,53
EF 227,54
EF 251,55
CV 20,179,203
CV 21,227,251
ae 42,72,864
s_bitcmp1_b32 s50, 16
s_cselect_b32 s52, 24576, 0
v_add_u32 v50, s52, v3
ST 14,256
ST 16,1024
ST 18,1792
ST 20,2560
EF 180,52
EF 204,53
EF 228,54
EF 252,55
CV 22,180,204
CV 23,228,252
EF 181,52
EF 205,53
EF 229,54
EF 253,55
CV 24,181,205
CV 25,229,253
EF 182,52
EF 206,53
EF 230,54
EF 254,55
CV 26,182,206
CV 27,230,254
EF 183,52
EF 207,53
EF 231,54
EF 255,55
CV 28,183,207
CV 29,231,255
ae 42,72,864
s_bfe_u32 s52, s50, 0x10011
v_mad_i32_i24 v50, v49, s52, v48
ST 22,256
ST 24,1024
ST 26,1792
ST 28,2560
s_waitcnt vmcnt(0) lgkmcnt(0)
.purgem MA
.purgem MV
.purgem NA
.purgem NV
.purgem AN
.purgem SH
.purgem PA_
.purgem PB_
.purgem AW
.purgem AR
.purgem UL
.purgem UH
.purgem AD
.purgem EF
.purgem TY
.purgem TX
.purgem RA
.purgem RC
.purgem BV
.purgem BA
.purgem PA
.purgem DF
.purgem DW
.purgem DR
.purgem DH
.purgem DG
.purgem XL
.purgem ST
.purgem SA
.purgem CV
.purgem RS
.purgem PM
.purgem PF
.purgem FS
.purgem FL
.purgem FB
.purgem FC
.purgem FG
.purgem FX
.purgem WL
.purgem WV
.purgem M
.purgem R1
.purgem R
.purgem P
.purgem Q
.purgem mw
.purgem mh
.purgem ad
.purgem ae
.purgem bc
.purgem W
.purgem H
)"
      : [flg] "=&s"(flg) : K2_OPS : K2_CLOB);
  if (flg) {
    const unsigned lane = __builtin_amdgcn_mbcnt_hi(~0u, __builtin_amdgcn_mbcnt_lo(~0u, 0u));
    const u16* const volatile __attribute__((address_space(4)))* ka = (const u16* const volatile __attribute__((address_space(4)))*)__builtin_amdgcn_kernarg_segment_ptr();
    {
      const u16* w1p = ka[6];
#pragma unroll 1
      for (unsigned f = blockIdx.x * 4 + w; f < 1152; f += 1024) {
        const u16* p = w1p + (32 * (f % 12) + 8 * (lane >> 4)) * HID + 16 * (f / 12) + (lane & 15);
        u32x4 v;
#pragma unroll
        for (int j = 0; j < 4; ++j) v[j] = p[2 * j * HID] | (unsigned)p[(2 * j + 1) * HID] << 16;
        *(u32x4*)(g_buf + f * 1024 + lane * 16) = v;
      }
    }
    slow_bar((unsigned*)(g_buf + G_BAR), w);
    slow_fc1(ka[0], ka[5], (char*)ka[1], (float*)smem + w * HID, w);
    slow_bar((unsigned*)(g_buf + G_BAR + 64), w);
    slow_fc2(ka[0], (const char*)ka[1], (u16*)ka[2], ka[3], ka[4], smem, w);
  }
  TS(15);
  MK_END(4);
}

extern "C" void kernel_launch(void* const* d_in, const int* in_sizes, int n_in,
                              void* d_out, int out_size, void* d_ws, size_t ws_size,
                              hipStream_t stream) {
  const u16* x = (const u16*)d_in[0];
  const u16* dw = (const u16*)d_in[1];
  const u16* w1 = (const u16*)d_in[2];
  const u16* w2 = (const u16*)d_in[3];
  const u16* grn_g = (const u16*)d_in[4];
  const u16* grn_b = (const u16*)d_in[5];
  char* ws = (char*)d_ws;
  if (ws_size < H_BYTES) return;
  (void)hipFuncSetAttribute((const void*)k1_fc1, hipFuncAttributeMaxDynamicSharedMemorySize, K1_LDS);
  (void)hipFuncSetAttribute((const void*)k2_fc2, hipFuncAttributeMaxDynamicSharedMemorySize, K2_LDS);

  k0_prep<<<K0_NB + NPAGE + NPAGX, 256, 0, stream>>>(w1, dw, ws, (const char*)x, grn_b, w2);
  k1_fc1<<<NWG + 8, 256, K1_LDS, stream>>>(x, ws, (const u16*)d_out, grn_g);
  k2_fc2<<<NWG, 256, K2_LDS, stream>>>(x, ws, (u16*)d_out, grn_g, w2, dw, w1);
}
```

```cpp
#include <hip/hip_runtime.h>
#include <hip/hip_bf16.h>
#include <stdint.h>
#include <stdio.h>

typedef short bf16x8 __attribute__((ext_vector_type(8)));
typedef short s16x4 __attribute__((ext_vector_type(4)));
typedef short v4i16_t __attribute__((ext_vector_type(4)));
typedef float f32x16 __attribute__((ext_vector_type(16)));
typedef float f32x4 __attribute__((ext_vector_type(4)));
typedef float f32x2 __attribute__((ext_vector_type(2)));
typedef unsigned u32x4 __attribute__((ext_vector_type(4)));
typedef __bf16 bf16x2_t __attribute__((ext_vector_type(2)));
typedef unsigned short u16;

constexpr float TZ = 175.f, TH = 175.f, TW2 = 175.f;
#define DI __device__ __forceinline__
#define LAS __attribute__((address_space(3)))

constexpr int NPIX = 147456;
constexpr int CH = 384;
constexpr int HID = 1536;
constexpr int NTILE = NPIX / 32;
constexpr int NWG = 256;
constexpr int TPW = NTILE / NWG;

constexpr size_t H_BYTES = (size_t)NPIX * HID * 2;
constexpr unsigned K1_LDS = 163840, K1_PF_OFF = 130048, K1_SMAX = 12, K1_NREP = 4, K1_G_TAB = 1228800, K1_G_CNT = 1239040, K1_G_LOC = 1236992, K1_G_DUM = 1255424,
  K1_G_ACC = 1320960, K1_G_LUT = 1382400, K1_G_BYTES = 1398784;
constexpr bool K1_LUT = true;
constexpr float K1_ZS = 16.0f;
constexpr unsigned K1_GST = 25600, K1_WCH = 6400, K1_ZC = 1365;
constexpr unsigned K1_SB1 = 32768;
constexpr bool K1_F2 = true;
constexpr unsigned K2_SHT = 12483;
constexpr int K2_LDS = 162816, K2_HB = 98304, K2_RSZ = 13824, K2_E_OFF = 153600, K2_PF_OFF = 159744, K2_S16_OFF = 152576, K2_S16_D0 = 8192, K2_NAT = 8;

__device__ __attribute__((aligned(256))) char g_buf[K1_G_BYTES];
__device__ __attribute__((aligned(256))) float g_cs[8 * 384];
__device__ __attribute__((aligned(256))) char g_w2r[96 * 12 * 1024];
__device__ __attribute__((aligned(256))) char g_w2f[96 * 12 * 1024];
__device__ __attribute__((aligned(256))) char g_w2q[16 * 12 * 1024];
__device__ __attribute__((aligned(256))) float g_sc[64];
constexpr unsigned G_FLAG = K1_G_CNT + 14336;
constexpr unsigned G_PUB = K1_G_DUM;
__device__ __attribute__((aligned(256))) unsigned g_prm[1024 * 64];
constexpr unsigned G_STMP = K1_G_CNT + 12288;
constexpr unsigned G_BAR = K1_G_CNT + 13312;
constexpr unsigned H_TS = 49152, H_RS = 48 * H_TS, NPAGE = 217, NPAGX = 55;
#define TS(i) do { } while (0)
#define KST(i) do { } while (0)
#define MK_START(i) do { } while (0)
#define MK_END(i) do { } while (0)

DI unsigned pack_bf16(float a, float b) {
  f32x2 v = {a, b};
  bf16x2_t r = __builtin_convertvector(v, bf16x2_t);
  return __builtin_bit_cast(unsigned, r);
}
typedef _Float16 f16x2_t __attribute__((ext_vector_type(2)));
DI unsigned pack_f16(float a, float b) {
  f32x2 v = {a, b};
  f16x2_t r = __builtin_convertvector(v, f16x2_t);
  return __builtin_bit_cast(unsigned, r);
}
DI float bf_lo(unsigned u) { return __uint_as_float(u << 16); }
DI float bf_hi(unsigned u) { return __uint_as_float(u & 0xffff0000u); }
DI float round_bf16(float a) { return bf_lo(pack_bf16(a, 0.f)); }
DI float dot2acc(unsigned a, unsigned b, float c) {
  return __builtin_amdgcn_fdot2_f32_bf16(__builtin_bit_cast(bf16x2_t, a), __builtin_bit_cast(bf16x2_t, b), c, false);
}
DI float ld_bf16(const u16* p) { return __uint_as_float((unsigned)(*p) << 16); }

DI s16x4 tr_read(const char* p) {
  return __builtin_bit_cast(s16x4, __builtin_amdgcn_ds_read_tr16_b64_v4i16((LAS v4i16_t*)p));
}

#define MFMA32(a, b, c) __builtin_amdgcn_mfma_f32_32x32x16_bf16((a), (b), (c), 0, 0, 0)
#define BAR() __builtin_amdgcn_s_barrier()

DI void dma16(unsigned voff, const char* sbase, unsigned lds_dst) {
  unsigned keep;
  asm volatile("s_mov_b32 %0, m0\n\ts_mov_b32 m0, %3\n\ts_nop 0\n\tglobal_load_lds_dwordx4 %1, %2\n\ts_mov_b32 m0, %0"
               : "=&s"(keep) : "v"(voff), "s"(sbase), "s"(lds_dst) : "memory");
}
DI unsigned lds_addr(const char* p) { return (unsigned)(size_t)(const LAS char*)p; }

DI float gelu_tanh(float x) {
  const float c0 = -2.0f * 0.7978845608028654f * 1.4426950408889634f;
  const float c1 = c0 * 0.044715f;
  float x2 = x * x;
  float t = __builtin_fmaf(x2, c1, c0);
  float e = __builtin_amdgcn_exp2f(x * t);
  float r = __builtin_amdgcn_rcpf(1.0f + e);
  return x * r;
}

DI void gload_b128_asm(u32x4& dst, const char* sbase, unsigned voff) {
  asm volatile("global_load_dwordx4 %0, %1, %2" : "=v"(dst) : "v"(voff), "s"(sbase) : "memory");
}
DI void lds_rd128(u32x4& dst, unsigned addr, int off) {
  asm volatile("ds_read_b128 %0, %1 offset:%2" : "=v"(dst) : "v"(addr), "i"(off));
}
DI void lds_rd128_far(u32x4& dst, unsigned voff, unsigned sbase, int imm) {
  unsigned t;
  asm volatile("v_add_u32 %1, %3, %2\n\tv_add_u32 %1, %4, %1\n\tds_read_b128 %0, %1" : "=v"(dst), "=&v"(t) : "v"(voff), "s"(sbase), "i"(imm));
}
DI void gload16i(u32x4& dst, const char* sbase, unsigned voff, int imm) {
  unsigned t;
  asm volatile("v_add_u32 %1, %4, %2\n\tglobal_load_dwordx4 %0, %1, %3" : "=v"(dst), "=&v"(t) : "v"(voff), "s"(sbase), "i"(imm) : "memory");
}
DI void dma16i(const char* sbase, unsigned voff, int immv, unsigned lbase, int imml) {
  unsigned t, keep;
  asm volatile("s_mov_b32 %1, m0\n\ts_add_u32 m0, %4, %6\n\tv_add_u32 %0, %5, %2\n\tglobal_load_lds_dwordx4 %0, %3\n\ts_mov_b32 m0, %1"
               : "=&v"(t), "=&s"(keep) : "v"(voff), "s"(sbase), "s"(lbase), "i"(immv), "i"(imml) : "memory", "scc");
}

DI void st16_wt(void* p, u32x4 v) { asm volatile("global_store_dwordx4 %0, %1, off\n\ts_nop 1" :: "v"(p), "v"(v) : "memory"); }
template <int I> struct IC { static constexpr int value = I; };
template <int N, int I = 0, typename F> DI void static_for(F&& f) {
  if constexpr (I < N) { f(IC<I>{}); static_for<N, I + 1>(f); }
}
template <int C, int M> DI float dpp_add(float v) { return v + __int_as_float(__builtin_amdgcn_update_dpp(0, __float_as_int(v), C, M, 0xf, false)); }
DI float wave_sum63(float v) {
  v = dpp_add<0x111, 0xf>(v); v = dpp_add<0x112, 0xf>(v); v = dpp_add<0x114, 0xf>(v); v = dpp_add<0x118, 0xf>(v);
  v = dpp_add<0x142, 0xa>(v); return dpp_add<0x143, 0xc>(v);
}
DI unsigned cs_index(unsigned n) { return (n & ~63u) + (n & 3) * 16 + ((n >> 2) & 15); }
DI uint64_t uni64(uint64_t v) { return (uint64_t)__builtin_amdgcn_readfirstlane(v >> 32) << 32 | (unsigned)__builtin_amdgcn_readfirstlane(v); }
DI unsigned wg_side(unsigned b) { return (((const unsigned*)(g_buf + K1_G_LOC))[b & 3] ^ (b >> 2)) & 1; }
DI unsigned h_flip(const char* ws, unsigned region, unsigned t) { return __popc(((const unsigned*)(g_buf + K1_G_LOC + 64))[(((unsigned)(size_t)ws & 0x1fffffu) + region * H_RS + t * H_TS) >> 21]) > 2; }
DI unsigned x_flip(const u16* x, unsigned region) { return __popc(((const unsigned*)(g_buf + K1_G_LOC + 1024))[(((unsigned)(size_t)x & 0x1fffffu) + (12 * region + 6) * 49152u) >> 21]) > 2; }
DI unsigned sub_pixel(const u16* x, unsigned sub, unsigned m) {
  const unsigned blk = sub / 24, u = sub % 24;
  const unsigned region = blk < 256 ? (blk >> 3) * 4 + (blk & 3) : 128 + ((blk - 256) >> 1), side = blk < 256 ? wg_side(blk) : blk & 1;
  return (12 * region + (u >> 1)) * 64 + ((0xda43cb52fe987610ull >> (32 * (u & 1) + 16 * (side ^ x_flip(x, region)) + (m & 12))) & 15) * 4 + (m & 3);
}
DI unsigned h_piece(const char* ws, unsigned sub, unsigned t) {
  const unsigned blk = sub / 24, u = sub % 24;
  const unsigned region = blk < 256 ? (blk >> 3) * 4 + (blk & 3) : 128 + ((blk - 256) >> 1), side = blk < 256 ? wg_side(blk) : blk & 1;
  return region * H_RS + t * H_TS + (u >> 3) * 16384 + (u & 7) * 1024 + ((side ^ h_flip(ws, region, t)) << 13);
}
constexpr unsigned K0_NB = 145 + 24 + 96 + 16 + 48;
constexpr float U24 = 0x1p-24f, EPS = 1e-5f;
DI float wave_all(float v) { return __int_as_float(__builtin_amdgcn_readlane(__float_as_int(wave_sum63(v)), 63)); }
DI float pow2f(float v) { return __uint_as_float(__float_as_uint(v) & 0x7f800000u); }
DI u32x4 w_load(const u16* w) { return *(const u32x4*)(w + ((threadIdx.x >> 3) & 7) * 73408 + 36800 + (threadIdx.x & 7) * 8); }
DI float w_ms(u32x4 a) {
  float q = 0.f;
#pragma unroll
  for (int i = 0; i < 4; ++i) { const float lo = bf_lo(a[i]), hi = bf_hi(a[i]); q = __builtin_fmaf(lo, lo, __builtin_fmaf(hi, hi, q)); }
  return wave_all(q) * (1.0f / 512);
}
DI float w1_scale(float ms) { return pow2f(300.f * U24 * __builtin_amdgcn_rsqf(ms)); }
DI float w2_scale(float ms) { return TW2 * U24 * __builtin_amdgcn_rsqf(ms); }
struct Sc { float w1, zs, g, h, vlo, vhi, c; };
DI Sc scales(const u16* w1, const u16* x, const u16* dw) {
  const u32x4 a = w_load(w1);
  const unsigned l = threadIdx.x & 63;
  float s = 0.f, q = 0.f;
#pragma unroll
  for (int k = 0; k < 6; ++k) {
    const u32x4 v = *(const u32x4*)(x + (9001 + 18397 * (l >> 3)) * CH + 64 * k + 8 * (l & 7)), d = *(const u32x4*)(dw + 64 * k + 8 * (l & 7));
#pragma unroll
    for (int i = 0; i < 4; ++i) { const float lo = bf_lo(v[i]) * bf_lo(d[i]), hi = bf_hi(v[i]) * bf_hi(d[i]); s += lo + hi; q = __builtin_fmaf(lo, lo, __builtin_fmaf(hi, hi, q)); }
  }
  s = dpp_add<0x141, 0xf>(dpp_add<0x4e, 0xf>(dpp_add<0xb1, 0xf>(s))); q = dpp_add<0x141, 0xf>(dpp_add<0x4e, 0xf>(dpp_add<0xb1, 0xf>(q)));
  const float var = fmaxf(q * (1.0f / CH) - s * s * (1.0f / (CH * CH)), 0.f);
  const float r2 = wave_all(var * __builtin_amdgcn_rcpf(var + EPS)) * (1.0f / 64), rden = __builtin_amdgcn_exp2f(wave_all(__builtin_amdgcn_logf(var + EPS)) * (-1.0f / 128));
  const float s1 = w_ms(a), p2 = r2 * CH * s1;
  Sc c;
  c.w1 = w1_scale(s1);
  c.g = TZ * U24 * __builtin_amdgcn_rsqf(r2);
  c.h = TH * U24 * __builtin_amdgcn_rsqf(p2 * (0.25f + 0.5f * p2 * __builtin_amdgcn_rcpf(1.0f + 2.0f * p2)));
  c.c = fminf(__builtin_ceilf(0.1701f * 0x1p24f * c.h) + 1.0f, 1400.f);
  c.zs = fminf(pow2f(c.g * 2530.f), pow2f(c.g * 0x1p12f * rden));
  const float e = EPS * c.zs * c.zs, z2 = r2 * (660.f * 660.f / (TZ * TZ));
  c.vlo = 4.2f * c.g * c.g - e;
  c.vhi = fminf(0x1.ep29f * c.g * c.g - e, z2 < 1.0f ? e * z2 * __builtin_amdgcn_rcpf(1.0f - z2) : 0x1p120f);
  return c;
}
__global__ __launch_bounds__(256) void k0_prep(const u16* __restrict__ w1, const u16* __restrict__ dw, const char* __restrict__ ws, const char* __restrict__ x,
                                                const u16* __restrict__ grn_b, const u16* __restrict__ w2) {
  MK_START(0);
  uint64_t pc; int d1;
  asm volatile("s_getpc_b64 %0\n1:\n\ts_mov_b32 %1, k1_code-1b" : "=s"(pc), "=s"(d1));
  if (threadIdx.x >= 192) pc += (int64_t)d1 + ((int)(blockIdx.x >> 3) - 3) * 8192;
  unsigned pfd = 0;
  if (blockIdx.x < 64 && (threadIdx.x < 150 || (blockIdx.x < 48 && threadIdx.x >= 192))) pfd = *(const __attribute__((address_space(1))) unsigned*)((pc & ~127ull) + threadIdx.x * 128);
#define K0_END() do { asm volatile("" :: "v"(pfd)); MK_END(0); } while (0)
  unsigned xcc;
  asm volatile("s_getreg_b32 %0, hwreg(HW_REG_XCC_ID)" : "=s"(xcc));
  xcc = (xcc >> 2) & 1;
  if (blockIdx.x < 4 && threadIdx.x == 0) ((unsigned*)(g_buf + K1_G_LOC))[blockIdx.x] = 7 * xcc;
  constexpr unsigned NP = NPAGE + NPAGX;
  __shared__ Sc ssc;
  __shared__ float ss2;
  const bool sw = threadIdx.x >= 192;
  const unsigned bi = blockIdx.x;
  const unsigned bx = bi < NP ? bi + K0_NB : bi - NP;
  if (bx >= K0_NB) {
    __shared__ unsigned lat[8];
    const unsigned pg = bx - K0_NB, w = threadIdx.x >> 6, lane = threadIdx.x & 63;
    auto gran = [&](unsigned p) {
      const bool h = p < NPAGE;
      const char* buf = h ? ws : x;
      const int o0 = (int)((h ? p : p - NPAGE) << 21) - (int)((unsigned)(size_t)buf & 0x1fffffu);
      const unsigned al = h ? 49152 : 12 * 49152;
      const unsigned q = ((((unsigned)max(o0, 0) + al - 1) >> (h ? 14 : 16)) * (h ? 87382u : 29128u)) >> 18;
      return buf + min(q * al, (unsigned)(h ? H_BYTES : H_BYTES / 4) - 16384u);
    };
    if (lane < 5) {
      const char* a = gran(lane < 4 ? min(pg + NP - 1 - (lane >> 1), pg - 1 - (lane >> 1)) : pg) + (lane & 1) * 8192 + w * 2048 + (lane < 4 ? 1024 : 0);
      const char* b = a;
      const char* own = (const char*)uni64(__builtin_amdgcn_readlane((uint64_t)a >> 32, 4) * 4294967296ull + (unsigned)__builtin_amdgcn_readlane((unsigned)(uint64_t)a, 4));
      unsigned long long t0, t1, t2;
      int n = 2;
      asm volatile("" : "+s"(n));
#pragma unroll 1
      do {
        unsigned v;
        asm volatile("s_cmp_eq_u32 %6, 2\n\ts_cbranch_scc1 1f\n\ts_sleep 4\n1:\n\ts_memtime %0\n\ts_waitcnt lgkmcnt(0)\n\tglobal_load_dword %3, %4, off sc1\n\ts_waitcnt vmcnt(0)\n\ts_memtime %1\n\ts_waitcnt lgkmcnt(0)\n\t"
                     "global_load_dword %3, %5, off sc1\n\ts_waitcnt vmcnt(0)\n\ts_memtime %2\n\ts_waitcnt lgkmcnt(0)" : "=&s"(t0), "=&s"(t1), "=&s"(t2), "=&v"(v) : "v"(a), "v"(b), "s"(n) : "memory", "scc");
        a = own + 1024 + (w & 1) * 8192; b = a + 8192 - (w & 1) * 16384;
      } while (--n);
      lat[w + 4 * (w & 1)] = (unsigned)(t1 - t0); lat[w + 4 - 4 * (w & 1)] = (unsigned)(t2 - t1);
    }
    __syncthreads();
    if (threadIdx.x == 0) {
      auto fastest = [&](const unsigned* l) { unsigned m = 9999; for (int i = 0; i < 4; ++i) m = l[i] < 300 ? m : min(m, l[i]); return (int)m; };
      const int df = fastest(lat) - fastest(lat + 4);
      const unsigned v = xcc ^ (df > 0), sure = abs(df) > 80;
      char* e = g_buf + K1_G_LOC + 64 + 4 * (pg < NPAGE ? pg : pg + 240 - NPAGE);
      *(u16*)(e + 2) = v ? 3 + 4 * sure : 1 - sure;
      if (pg == 0 || pg == NPAGE) e[0] = v; else e[-3] = v ^ 1;
      if (pg == NPAGE - 1 || pg == NP - 1) e[1] = v; else e[4] = v ^ 1;
    }
    K0_END();
    return;
  }
  if (bx >= 145 + 24 + 96 + 16) {
    __shared__ __attribute__((aligned(16))) float red[2][16][32];
    const int t = bx - 281, c8 = (threadIdx.x & 3) * 8;
    u32x4 sm, v[6];
    if (sw) sm = w_load(w1);
#pragma unroll
    for (int k = 0; k < 6; ++k) v[k] = *(const u32x4*)(w1 + (size_t)(64 * k + (threadIdx.x >> 2)) * HID + 32 * t + c8);
    if (sw) ss2 = w1_scale(w_ms(sm));
    __syncthreads();
    const float s1 = ss2;
    float a[8], q[8];
#pragma unroll
    for (int i = 0; i < 8; ++i) {
      a[i] = q[i] = 0.f;
#pragma unroll
      for (int k = 0; k < 6; ++k) {
        const float f = (float)(_Float16)(s1 * (i & 1 ? bf_hi(v[k][i >> 1]) : bf_lo(v[k][i >> 1]))) * 0x1p24f;
        a[i] += f; q[i] = __builtin_fmaf(f, f, q[i]);
      }
      a[i] = dpp_add<0x118, 0xf>(dpp_add<0x114, 0xf>(a[i])); q[i] = dpp_add<0x118, 0xf>(dpp_add<0x114, 0xf>(q[i]));
    }
    if ((threadIdx.x & 12) == 12) {
#pragma unroll
      for (int i = 0; i < 2; ++i) {
        *(f32x4*)&red[0][threadIdx.x >> 4][c8 + 4 * i] = (f32x4){a[4 * i], a[4 * i + 1], a[4 * i + 2], a[4 * i + 3]};
        *(f32x4*)&red[1][threadIdx.x >> 4][c8 + 4 * i] = (f32x4){q[4 * i], q[4 * i + 1], q[4 * i + 2], q[4 * i + 3]};
      }
    }
    __syncthreads();
    if (threadIdx.x < 64) {
      const int n = threadIdx.x & 31;
      float s = 0.f;
#pragma unroll
      for (int i = 0; i < 16; ++i) s += red[threadIdx.x >> 5][i][n];
      const float c = (0.f - (float)K1_ZC * s) * 0x1p-48f;
      char* p = g_buf + t * K1_GST + 5120;
      if (threadIdx.x < 32) *(f32x4*)(p + 272 * (n >> 4) + (n & 15) * 16) = (f32x4){c, c, c, c};
      const bool any = __ballot(threadIdx.x >= 32 && !(s >= 384.f * 64 * 64 && s <= 384.f * 1024 * 1024)) != 0;
      if (threadIdx.x == 0) g_buf[G_FLAG + 4 * 98 + t] = any;
    }
    K0_END();
    return;
  }
  if (bx >= 145 + 24 + 96) {
    if (sw) ssc = scales(w1, (const u16*)x, dw);
    __syncthreads();
    const Sc sc = ssc;
    const unsigned e = (bx - 265) * 256 + threadIdx.x;
    const float u = 1.0f / (sc.g * sc.w1);
    float r[2];
#pragma unroll
    for (int i = 0; i < 2; ++i) {
      const unsigned c = 2 * e + i;
      r[i] = __builtin_fmaf(sc.h, gelu_tanh(u * __uint_as_float((c >> 12) << 31 | ((c & 4095) + 79 * 128) << 16 | 0x8000u)), sc.c * U24);
      if (!(r[i] >= 0.f && r[i] < 8189.f * U24)) r[i] = __builtin_inff();
    }
    *(unsigned*)(g_buf + K1_G_LUT + 4 * e) = pack_f16(r[0], r[1]);
    K0_END();
    return;
  }
  if (bx >= 145 + 24) {
    __shared__ __attribute__((aligned(16))) u16 tw[32][200];
    const int kappa = (bx - 169) >> 1, nh = (bx - 169) & 1;
    u32x4 sm, v[3];
    if (sw) sm = w_load(w2);
#pragma unroll
    for (int i = 0; i < 3; ++i) { const int e = threadIdx.x + 256 * i; v[i] = *(const u32x4*)(w2 + (size_t)(32 * kappa + e / 24) * CH + 192 * nh + (e % 24) * 8); }
    if (sw) ss2 = w2_scale(w_ms(sm));
#pragma unroll
    for (int i = 0; i < 3; ++i) { const int e = threadIdx.x + 256 * i; *(u32x4*)&tw[e / 24][(e % 24) * 8] = v[i]; }
    __syncthreads();
    const int lane = threadIdx.x & 63, g = lane >> 4;
    const float s2 = 256.f * ss2;
    bool bad = false;
#pragma unroll
    for (int i = 0; i < 3; ++i) {
      const int fi = (threadIdx.x >> 6) + 4 * i, n = 64 * (fi >> 2) + 4 * (lane & 15) + (fi & 3);
      float wv[8];
      unsigned bits = 0;
#pragma unroll
      for (int j = 0; j < 8; ++j) {
        wv[j] = ld_bf16(&tw[16 * (j >> 2) + 4 * g + (j & 3)][n]);
        bits |= wv[j] != 0.f ? 256u << j : 0u;
        wv[j] *= s2;
        bits |= fabsf(wv[j]) >= 48 * 256 * U24 ? 1u << j : 0u;
        bad |= !(fabsf(wv[j]) <= 500.f);
      }
      const unsigned o = (((kappa & ~1) + fi / 6) * 24 + nh * 12 + (fi % 6) * 2 + (kappa & 1)) * 1024 + lane * 16;
      st16_wt(g_w2r + o, (u32x4){pack_f16(wv[0], wv[1]), pack_f16(wv[2], wv[3]), pack_f16(wv[4], wv[5]), pack_f16(wv[6], wv[7])});
      constexpr float q = 1.0f / 256;
      if (kappa < 8) st16_wt(g_w2q + o, (u32x4){pack_f16(q * wv[0], q * wv[1]), pack_f16(q * wv[2], q * wv[3]), pack_f16(q * wv[4], q * wv[5]), pack_f16(q * wv[6], q * wv[7])});
      static_for<4>([&](auto ic) { bits |= __builtin_amdgcn_update_dpp(0u, bits, 0x120 + (1 << decltype(ic)::value), 0xf, 0xf, false); });
      bad |= ((bits >> 8) & ~bits & 255u) != 0;
    }
    const bool any = __ballot(bad) != 0;
    if (lane == 0) ((char*)(g_buf + G_FLAG))[4 * (bx - 169) + (threadIdx.x >> 6)] = any;
    K0_END();
    return;
  }
  if (bx >= 145) {
    __shared__ __attribute__((aligned(16))) float red[16][64];
    const int c0 = 64 * ((bx - 145) >> 2), rg = (bx - 145) & 3, c8 = (threadIdx.x & 7) * 8;
    float a[8];
#pragma unroll
    for (int i = 0; i < 8; ++i) a[i] = 0.f;
#pragma unroll
    for (int k = 0; k < 12; ++k) {
      const int ch = 384 * rg + 32 * k + (threadIdx.x >> 3);
      const float b = ld_bf16(grn_b + ch);
      const u32x4 v = *(const u32x4*)(w2 + (size_t)ch * CH + c0 + c8);
#pragma unroll
      for (int i = 0; i < 4; ++i) { a[2 * i] = __builtin_fmaf(b, bf_lo(v[i]), a[2 * i]); a[2 * i + 1] = __builtin_fmaf(b, bf_hi(v[i]), a[2 * i + 1]); }
    }
#pragma unroll
    for (int i = 0; i < 8; ++i) a[i] = dpp_add<0x118, 0xf>(a[i]);
    if (threadIdx.x & 8) {
      *(f32x4*)&red[threadIdx.x >> 4][c8] = (f32x4){a[0], a[1], a[2], a[3]};
      *(f32x4*)&red[threadIdx.x >> 4][c8 + 4] = (f32x4){a[4], a[5], a[6], a[7]};
    }
    __syncthreads();
    if (threadIdx.x < 64) {
      float s = 0.f;
#pragma unroll
      for (int i = 0; i < 16; ++i) s += red[i][threadIdx.x];
      g_cs[(4 + rg) * 384 + cs_index(c0 + threadIdx.x)] = s;
      g_cs[64 * (bx - 145) + threadIdx.x] = 0.f;
    }
    K0_END();
    return;
  }
  const unsigned bid = bx;
  if (bid == 0) {
    if (sw) ssc = scales(w1, (const u16*)x, dw);
    if ((threadIdx.x >> 6) == 2) ss2 = w2_scale(w_ms(w_load(w2)));
#pragma unroll
    for (int i = 0; i < 4; ++i) if (threadIdx.x + 256 * i < 896) *(u32x4*)(g_buf + K1_G_CNT + (threadIdx.x + 256 * i) * 16) = (u32x4){0u, 0u, 0u, 0u};
    *(unsigned*)(g_buf + G_PUB + threadIdx.x * 256) = 0u;
    const int e = threadIdx.x, e2 = e < 192 ? 32 * (e >> 4) + 8 * ((e & 15) >> 2) : 8 * min(e - 192, 47);
    const u32x4 d = *(const u32x4*)(dw + e2);
    __syncthreads();
    const Sc sc = ssc;
    const float s2 = ss2;
    u32x4 v;
#pragma unroll
    for (int k = 0; k < 4; ++k) v[k] = pack_bf16(sc.zs * bf_lo(d[k]), sc.zs * bf_hi(d[k]));
    if (e < 192) {
#pragma unroll
      for (int k = 0; k < 4; ++k) v[k] &= (k & 1) != ((e >> 1) & 1) ? 0u : e & 1 ? 0xffff0000u : 0xffffu;
    }
    if (e >= 240) v = (u32x4){__float_as_uint(EPS * sc.zs * sc.zs), __float_as_uint(sc.vhi), __float_as_uint(sc.g), __float_as_uint(sc.vlo)};
    *(u32x4*)(g_buf + K1_G_TAB + (e < 192 ? e : e + 192) * 16) = v;
    auto sane = [](float v) { return v >= 0x1p-45f && v <= 0x1p20f; };
    if (threadIdx.x < 2) ((unsigned*)(g_buf + G_FLAG))[96 + threadIdx.x] = !(sane(sc.w1) && sane(sc.zs) && sane(sc.g) && sane(sc.h) && sane(s2));
    if (threadIdx.x == 0) {
      const float ik = 1.0f / (sc.h * s2);
      *(f32x4*)g_sc = (f32x4){sc.h * s2, ik, 0x1p-28f / (sc.h * sc.h), -sc.c * U24 * ik};
      g_sc[4] = __uint_as_float(pack_f16(-sc.c * U24, -sc.c * U24));
      *(unsigned*)(g_buf + K1_G_CNT + 16320) = pack_f16(-sc.c * 0x1p-10f, -sc.c * 0x1p-10f);
    }
    if (threadIdx.x < 48) *(u32x4*)(g_buf + threadIdx.x * K1_GST + 5376) = (u32x4){pack_f16(sc.c * U24, 0.f), 0u, 0u, 0u};
    K0_END();
    return;
  }
  __shared__ __attribute__((aligned(16))) u16 tl[32][136];
  const int e = (bid - 1) * 256 + threadIdx.x, ks = (bid - 1) % 12, cb = (bid - 1) / 12;
  u32x4 sm, v[2];
  if (sw) sm = w_load(w1);
#pragma unroll
  for (int i = 0; i < 2; ++i) v[i] = *(const u32x4*)(w1 + (size_t)(32 * ks + (threadIdx.x >> 4) + 16 * i) * HID + 128 * cb + (threadIdx.x & 15) * 8);
  if (sw) ss2 = w1_scale(w_ms(sm));
#pragma unroll
  for (int i = 0; i < 2; ++i) *(u32x4*)&tl[(threadIdx.x >> 4) + 16 * i][(threadIdx.x & 15) * 8] = v[i];
  if (e < (int)(K1_NREP + 1) * 768) *(u32x4*)(g_buf + K1_G_ACC + e * 16) = (u32x4){0u, 0u, 0u, 0u};
  __syncthreads();
  const float s1 = ss2;
#pragma unroll
  for (int i = 0; i < 2; ++i) {
    const int lane = threadIdx.x & 63, f = (threadIdx.x >> 6) + 4 * i, n = 16 * f + (lane & 15), k0 = 8 * (lane >> 4);
    u32x4 P;
#pragma unroll
    for (int jj = 0; jj < 4; ++jj) P[jj] = pack_f16(s1 * ld_bf16(&tl[k0 + 2 * jj][n]), s1 * ld_bf16(&tl[k0 + 2 * jj + 1][n]));
    const int fs = (f & 1) * 12 + ks;
    st16_wt(g_buf + (4 * cb + (f >> 1)) * K1_GST + (fs + (fs >= 5)) * 1024 + lane * 16, P);
  }
  K0_END();
}

constexpr unsigned G_ACC2 = K1_G_ACC + K1_NREP * 12288;

#define VA_CLOB \
  "v0", "v1", "v2", "v3", "v4", "v5", "v6", "v7", "v8", "v9", "v10", "v11", "v12", "v13", "v14", "v15", "v16", "v17", "v18", "v19", "v20", "v21", "v22", "v23", "v24", "v25", "v26", "v27", "v28", "v29", "v30", "v31", \
  "v32", "v33", "v34", "v35", "v36", "v37", "v38", "v39", "v40", "v41", "v42", "v43", "v44", "v45", "v46", "v47", "v48", "v49", "v50", "v51", "v52", "v53", "v54", "v55", "v56", "v57", "v58", "v59", "v60", "v61", "v62", "v63", \
  "v64", "v65", "v66", "v67", "v68", "v69", "v70", "v71", "v72", "v73", "v74", "v75", "v76", "v77", "v78", "v79", "v80", "v81", "v82", "v83", "v84", "v85", "v86", "v87", "v88", "v89", "v90", "v91", "v92", "v93", "v94", "v95", \
  "v96", "v97", "v98", "v99", "v100", "v101", "v102", "v103", "v104", "v105", "v106", "v107", "v108", "v109", "v110", "v111", "v112", "v113", "v114", "v115", "v116", "v117", "v118", "v119", "v120", "v121", "v122", "v123", "v124", "v125", "v126", "v127", \
  "v128", "v129", "v130", "v131", "v132", "v133", "v134", "v135", "v136", "v137", "v138", "v139", "v140", "v141", "v142", "v143", "v144", "v145", "v146", "v147", "v148", "v149", "v150", "v151", "v152", "v153", "v154", "v155", "v156", "v157", "v158", "v159", \
  "v160", "v161", "v162", "v163", "v164", "v165", "v166", "v167", "v168", "v169", "v170", "v171", "v172", "v173", "v174", "v175", "v176", "v177", "v178", "v179", "v180", "v181", "v182", "v183", "v184", "v185", "v186", "v187", "v188", "v189", "v190", "v191", \
  "v192", "v193", "v194", "v195", "v196", "v197", "v198", "v199", "v200", "v201", "v202", "v203", "v204", "v205", "v206", "v207", "v208", "v209", "v210", "v211", "v212", "v213", "v214", "v215", "v216", "v217", "v218", "v219", "v220", "v221", "v222", "v223", \
  "v224", "v225", "v226", "v227", "v228", "v229", "v230", "v231", "v232", "v233", "v234", "v235", "v236", "v237", "v238", "v239", "v240", "v241", "v242", "v243", "v244", "v245", "v246", "v247", "v248", "v249", "v250", "v251", "v252", "v253", "v254", "v255", \
  "a0", "a1", "a2", "a3", "a4", "a5", "a6", "a7", "a8", "a9", "a10", "a11", "a12", "a13", "a14", "a15", "a16", "a17", "a18", "a19", "a20", "a21", "a22", "a23", "a24", "a25", "a26", "a27", "a28", "a29", "a30", "a31", \
  "a32", "a33", "a34", "a35", "a36", "a37", "a38", "a39", "a40", "a41", "a42", "a43", "a44", "a45", "a46", "a47", "a48", "a49", "a50", "a51", "a52", "a53", "a54", "a55", "a56", "a57", "a58", "a59", "a60", "a61", "a62", "a63", \
  "a64", "a65", "a66", "a67", "a68", "a69", "a70", "a71", "a72", "a73", "a74", "a75", "a76", "a77", "a78", "a79", "a80", "a81", "a82", "a83", "a84", "a85", "a86", "a87", "a88", "a89", "a90", "a91", "a92", "a93", "a94", "a95", \
  "a96", "a97", "a98", "a99", "a100", "a101", "a102", "a103", "a104", "a105", "a106", "a107", "a108", "a109", "a110", "a111", "a112", "a113", "a114", "a115", "a116", "a117", "a118", "a119", "a120", "a121", "a122", "a123", "a124", "a125", "a126", "a127", \
  "a128", "a129", "a130", "a131", "a132", "a133", "a134", "a135", "a136", "a137", "a138", "a139", "a140", "a141", "a142", "a143", "a144", "a145", "a146", "a147", "a148", "a149", "a150", "a151", "a152", "a153", "a154", "a155", "a156", "a157", "a158", "a159", \
  "a160", "a161", "a162", "a163", "a164", "a165", "a166", "a167", "a168", "a169", "a170", "a171", "a172", "a173", "a174", "a175", "a176", "a177", "a178", "a179", "a180", "a181", "a182", "a183", "a184", "a185", "a186", "a187", "a188", "a189", "a190", "a191", \
  "a192", "a193", "a194", "a195", "a196", "a197", "a198", "a199", "a200", "a201", "a202", "a203", "a204", "a205", "a206", "a207", "a208", "a209", "a210", "a211", "a212", "a213", "a214", "a215", "a216", "a217", "a218", "a219", "a220", "a221", "a222", "a223", \
  "a224", "a225", "a226", "a227", "a228", "a229", "a230", "a231", "a232", "a233", "a234", "a235", "a236", "a237", "a238", "a239", "a240", "a241", "a242", "a243", "a244", "a245", "a246", "a247", "a248", "a249", "a250", "a251", "a252", "a253", "a254", "a255"
#define K1_CLOB "memory", "vcc", "scc", "s36", "s37", "s38", "s39", "s40", "s41", "s42", "s43", "s44", "s45", "s46", "s47", "s48", "s49", "s50", "s51", "s52", "s53", "s54", "s55", "s56", "s57", "s58", "s59", "s60", "s61", "s62", "s63", "s64", "s65", "s66", "s67", "s68", "s69", "s70", "s71", "s72", "s73", "s74", "s75", "s76", "s77", "s78", "s79", "s80", "s81", "s82", "s83", "s84", "s85", "s86", "s87", "s88", "s89", "s90", "s91", "s92", "s93", "s94", "s95", "s96", "s97", VA_CLOB
DI void k2_params(const u16* x, const char* hws, const u16* out, const u16* grn_g, unsigned tid) {
  const unsigned e = (tid >> 2) * 32 + blockIdx.x * 4 + (tid & 3);
  const unsigned b = e >> 2, w = e & 3, pg = w & 1, nh = w >> 1, q = (b >> 3) * 4 + (b & 3), rs = 128 + (q >> 1), hf = q & 1, side = wg_side(b);
  auto fmask = [&](unsigned region) {
    const unsigned o = ((unsigned)(size_t)hws & 0x1fffffu) + region * H_RS + nh * H_TS;
    const unsigned* p = (const unsigned*)(g_buf + K1_G_LOC + 64) + (o >> 21);
    const unsigned r = 0x200000u + 2 * H_TS - 1 - (o & 0x1fffffu), d1 = ((r >> 15) * 171) >> 9, d2 = (((r + 0x200000u) >> 15) * 171) >> 9;
    const unsigned a1 = ~0u << d1, a2 = d2 < 32 ? ~0u << d2 : 0u;
    return ((__popc(p[0]) > 2 ? ~a1 : 0u) | (__popc(p[1]) > 2 ? a1 & ~a2 : 0u) | (__popc(p[2]) > 2 ? a2 : 0u)) ^ (0u - side);
  };
  const unsigned f0 = fmask(q), f1 = fmask(rs);
  const unsigned fx = ((side ^ x_flip(x, q)) ? ~K2_SHT : 0u) | ((side ^ x_flip(x, rs)) ? K2_SHT : 0u);
  const uint64_t h0 = (uint64_t)hws + q * H_RS + nh * H_TS + pg * 3072, h1 = (uint64_t)hws + rs * H_RS + nh * H_TS + hf * 3072 + pg * 1536;
  const unsigned p0 = (12 * q + 2 * pg) * 49152 + nh * 384, p1 = (12 * rs + 2 * hf + pg) * 49152 + nh * 384;
  const unsigned e1 = 1024 + (e >> 3);
  auto kap = [](unsigned f) { return f / 48 * 2 + (f & 1); };
  auto sco = [&](unsigned f) { return kap(f) * 64 + (kap(f) < 16 ? K2_S16_D0 : 0); };
  auto cso = [&](unsigned f) { const unsigned r = f % 48; return ((f < 192 ? min(f / 48, 2u) : 3) * 384 + (r / 12 % 2 * 12 + r / 24 * 6 + r % 12 / 2) * 16) * 4; };
  typedef uint64_t u64x2 __attribute__((ext_vector_type(2)));
  u64x2* P = (u64x2*)(g_prm + 64 * e);
  P[0] = (u64x2){f0 | (uint64_t)f1 << 32, fx | (uint64_t)(sco(e) | (sco(e1) + (e & 6) * 8) << 16) << 32};
  const u32x4 sc = *(const u32x4*)g_sc;
  P[1] = (u64x2){(((b >> 3) * 4 + w) & 7) * 1024 | (uint64_t)sc[0] << 32, sc[1] | (uint64_t)sc[2] << 32};
  P[2] = (u64x2){h0, h0 + 8192};
  P[3] = (u64x2){h1, h1 + 8192};
  P[4] = (u64x2){__float_as_uint(g_sc[4]) | (uint64_t)((e1 - e) * 1024 + (e & 7) * 128) << 32, 0};
  P[5] = (u64x2){(uint64_t)x + p0, (uint64_t)x + p1};
  P[6] = (u64x2){(uint64_t)out + p0, (uint64_t)out + p1};
  P[7] = (u64x2){0x7fffffffffffffull, (uint64_t)g_buf + G_FLAG};
  P[8] = (u64x2){(uint64_t)g_w2f + w * 6144, (uint64_t)g_w2q + w * 6144};
  P[9] = (u64x2){(uint64_t)g_w2r + e * 1024, (uint64_t)g_w2f + e * 1024};
  P[10] = (u64x2){(uint64_t)grn_g + w * 256, (uint64_t)g_cs + w * 384 + 2048};
  P[11] = (u64x2){(uint64_t)g_buf + G_PUB + b * 256, (uint64_t)g_buf + G_PUB + w * 16384};
  P[12] = (u64x2){cso(e) | (uint64_t)(cso(e1) + (e & 1) * 32) << 32, (uint64_t)g_cs};
  P[13] = (u64x2){sc[3] | (uint64_t)1 << (32 + (b >> 3) % K2_NAT), 0};
}
__global__ __launch_bounds__(256, 1) void k1_fc1(const u16* __restrict__ x, char* __restrict__ hws, const u16* __restrict__ out, const u16* __restrict__ grn_g) {
  extern __shared__ __attribute__((aligned(16))) char smem[];
  const unsigned w = __builtin_amdgcn_readfirstlane(threadIdx.x >> 6);
  if (blockIdx.x < 8) {
    if (w < 2) k2_params(x, hws, out, grn_g, w * 64 + __builtin_amdgcn_mbcnt_hi(~0u, __builtin_amdgcn_mbcnt_lo(~0u, 0u)));
    return;
  }
  const unsigned lds0 = lds_addr(smem);
  const unsigned b = blockIdx.x - 8;
  const unsigned dir = (b >> 3) & 1;
  TS(0); MK_START(1);
  const uint64_t xp = (uint64_t)x, hp = (uint64_t)hws, gp = (uint64_t)g_buf, wp = gp + w * K1_WCH;
  const unsigned d = dir ? ~0u : 1u, wrap = dir ? ~0u : 48u, rst = dir ? 47u : 0u, pos0 = dir ? K1_SMAX - 1 : 0u;
  const unsigned xo0 = (12 * ((b >> 3) * 4 + (b & 3)) + w) * 49152, xs1 = (1536 + w + (dir ? 8 : 0)) * 49152, sb1 = 1536 * w + (dir ? K1_SB1 : 0);
  const unsigned r0o = ((b >> 3) * 4 + (b & 3)) * H_RS, tsel = 4 * (b & 3), s2 = (b >> 2) & 1;
  const uint64_t ap = gp + K1_G_ACC + (b % K1_NREP) * 12288;
  const unsigned ca0 = w == 0 ? dir * 64 : K1_G_DUM - K1_G_CNT + (4 * b + w) * 64;
  const unsigned pm = lds0 + K1_PF_OFF, pf = ((b >> 3) * 4 + w) * 1024;
  unsigned nb;
  asm volatile(
R"(.macro M t,f,w
.if \f < 30
v_mfma_f32_16x16x32_f16 v[56+4*\t:56+4*\t+3], v[128+4*\f:128+4*\f+3], a[216+4*\w:216+4*\w+3], v[56+4*\t:56+4*\t+3]
.else
v_mfma_f32_16x16x32_f16 v[56+4*\t:56+4*\t+3], a[4*\f-4*30:4*\f-4*30+3], a[216+4*\w:216+4*\w+3], v[56+4*\t:56+4*\t+3]
.endif
.endm
.macro Z t,f,w
.if \f < 30
v_mfma_f32_16x16x32_f16 v[56+4*\t:56+4*\t+3], v[128+4*\f:128+4*\f+3], a[216+4*\w:216+4*\w+3], v[252:255]
.else
v_mfma_f32_16x16x32_f16 v[56+4*\t:56+4*\t+3], a[4*\f-4*30:4*\f-4*30+3], a[216+4*\w:216+4*\w+3], v[252:255]
.endif
.endm
.macro LZ a,f
.if \f < 30
v_mfma_f32_16x16x32_bf16 v[36:36+3], v[\a:\a+3], v[128+4*\f:128+4*\f+3], 0
.else
v_mfma_f32_16x16x32_bf16 v[36:36+3], v[\a:\a+3], a[4*\f-4*30:4*\f-4*30+3], 0
.endif
.endm
.macro LS a,f
.if \f < 30
v_mfma_f32_16x16x32_bf16 v[36:36+3], v[\a:\a+3], v[128+4*\f:128+4*\f+3], v[36:36+3]
.else
v_mfma_f32_16x16x32_bf16 v[36:36+3], v[\a:\a+3], a[4*\f-4*30:4*\f-4*30+3], v[36:36+3]
.endif
.endm
.macro LD d,a,f
.if \f < 30
v_mfma_f32_16x16x32_bf16 v[\d:\d+3], a[\a:\a+3], v[128+4*\f:128+4*\f+3], v[36:36+3]
.else
v_mfma_f32_16x16x32_bf16 v[\d:\d+3], a[\a:\a+3], a[4*\f-4*30:4*\f-4*30+3], v[36:36+3]
.endif
.endm
.macro GZ f
.if \f < 30
v_mfma_f32_16x16x32_f16 v[52:55], v[128+4*\f:128+4*\f+3], v[128+4*\f:128+4*\f+3], 0
.else
v_mfma_f32_16x16x32_f16 v[52:55], a[4*\f-4*30:4*\f-4*30+3], a[4*\f-4*30:4*\f-4*30+3], 0
.endif
.endm
.macro GS f
.if \f < 30
v_mfma_f32_16x16x32_f16 v[52:55], v[128+4*\f:128+4*\f+3], v[128+4*\f:128+4*\f+3], v[52:55]
.else
v_mfma_f32_16x16x32_f16 v[52:55], a[4*\f-4*30:4*\f-4*30+3], a[4*\f-4*30:4*\f-4*30+3], v[52:55]
.endif
.endm
.macro CL p,z,s
v_fma_mixlo_f16 v[\p], v[\z], v[\s], 0 op_sel_hi:[0,1,0]
.endm
.macro CH p,z,s
v_fma_mixhi_f16 v[\p], v[\z], v[\s], 0 op_sel:[0,1,0] op_sel_hi:[0,1,0]
.endm
.macro XL f,o
.if \f < 30
global_load_dwordx4 v[128+4*\f:128+4*\f+3], v3, s[46:47] offset:\o
.else
global_load_dwordx4 a[4*\f-4*30:4*\f-4*30+3], v3, s[46:47] offset:\o
.endif
.endm
.macro XN f,o
.if \f < 30
global_load_dwordx4 v[128+4*\f:128+4*\f+3], v3, s[46:47] offset:\o nt
.else
global_load_dwordx4 a[4*\f-4*30:4*\f-4*30+3], v3, s[46:47] offset:\o nt
.endif
.endm
.macro F t,z
v_fma_f32 v[\t], -v[\z], v[\z], s36
.endm
.macro U t,z
v_mul_f32 v[\t], v[\z], v[\t]
.endm
.macro E t
v_exp_f32 v[\t], v[\t]
.endm
.macro A t
v_add_f32 v[\t], 1.0, v[\t]
.endm
.macro R t
v_rcp_f32 v[\t], v[\t]
.endm
.macro C d,a,b
v_cvt_pk_bf16_f32 v[\d], v[\a], v[\b]
.endm
.macro S0 q,t
v_mul_f32 v[\q], v[\t], v[\t]
.endm
.macro S q,t
v_fmac_f32 v[\q], v[\t], v[\t]
.endm
.macro HC d,z
v_cvt_pk_f16_f32 v[\d], v[\z], v[\z+1]
.endm
.macro HF t,p
v_pk_fma_f16 v[\t], v[\p], v[\p], s36 neg_lo:[1,0,0] neg_hi:[1,0,0]
.endm
.macro HM t,p
v_pk_mul_f16 v[\t], v[\p], v[\t]
.endm
.macro HE d,s
v_exp_f16 v[\d], v[\s]
.endm
.macro HG d,s
v_exp_f16_sdwa v[\d], v[\s] dst_sel:WORD_1 dst_unused:UNUSED_PRESERVE src0_sel:WORD_1
.endm
.macro HA t
v_pk_add_f16 v[\t], v[\t], 1.0 op_sel_hi:[1,0]
.endm
.macro HR d,s
v_rcp_f16 v[\d], v[\s]
.endm
.macro HQ d,s
v_rcp_f16_sdwa v[\d], v[\s] dst_sel:WORD_1 dst_unused:UNUSED_PRESERVE src0_sel:WORD_1
.endm
.macro HP d,s
v_rcp_f16_sdwa v[\d], v[\s] dst_sel:DWORD dst_unused:UNUSED_PAD src0_sel:WORD_1
.endm
.macro HY t,z
v_fma_mix_f32 v[\t], v[\z], v[\t], 0 op_sel_hi:[0,1,0]
.endm
.macro HK p
v_and_b32 v[\p], s56, v[\p]
.endm
.macro LQ p
v_lshrrev_b32 v[\p], 2, v[\p]
.endm
.macro LL t,z
v_lshlrev_b32_sdwa v[\t], 1, v[\z] dst_sel:DWORD dst_unused:UNUSED_PAD src0_sel:DWORD src1_sel:WORD_1
.endm
.macro LH t,p
v_and_b32_sdwa v[\t], s56, v[\p] dst_sel:DWORD dst_unused:UNUSED_PAD src0_sel:DWORD src1_sel:WORD_1
.endm
.macro LA t
ds_read_u16 v[\t], v[\t] offset:5376
.endm
.macro LC_ t
ds_read_u16_d16_hi v[\t], v[\t] offset:5376
.endm
.macro LO p,a,b
v_bfi_b32 v[\p], s36, v[\a], v[\b]
.endm
.macro HZ q,p
v_pk_mul_f16 v[\q], v[\p], v[\p]
.endm
.macro HS q,p
v_pk_fma_f16 v[\q], v[\p], v[\p], v[\q]
.endm
.macro HT q,p
v_fma_mix_f32 v[\q], v[\p], v[\p], v[\q] op_sel:[1,1,0] op_sel_hi:[1,1,0]
.endm
.macro HX q
v_fma_mix_f32 v[\q], v[\q], 1.0, v[\q] op_sel:[0,0,1] op_sel_hi:[1,0,1]
.endm
.macro UX t,z
v_mul_f32_e64 v[\t], v[\z], v[\t]
.endm
.macro EX t
v_exp_f32_e64 v[\t], v[\t]
.endm
.macro AX t
v_add_f32_e64 v[\t], 1.0, v[\t]
.endm
.macro RX t
v_rcp_f32_e64 v[\t], v[\t]
.endm
.macro TX q,t
v_mul_f32_e64 v[\q], v[\t], v[\t]
.endm
.macro SX q,t
v_fmac_f32_e64 v[\q], v[\t], v[\t]
.endm
.macro AW a,s
v_accvgpr_write_b32 a[\a], v[\s]
.endm
.macro RA d,v,o
ds_read_b128 a[\d:\d+3], v[\v] offset:\o
.endm
.macro RV d,v,o
ds_read_b128 v[\d:\d+3], v[\v] offset:\o
.endm
.macro W2 a,b,o
ds_write2st64_b32 v4, v[\a], v[\b] offset0:\o offset1:\o+4
.endm
.macro W1 a,o
ds_write_b32 v4, v[\a] offset:\o
.endm
.macro DW o
global_load_lds_dwordx4 v0, s[40:41] offset:\o
.endm
.macro DF
global_load_lds_dwordx4 v0, s[70:71]
.endm
.macro DX
global_load_lds_dword v3, s[46:47]
.endm
.macro ST d
.if ((\d - 104) / 4) & 1
global_store_dwordx4 v2, v[\d:\d+3], s[42:43] nt sc1
.else
global_store_dwordx2 v31, v[\d:\d+1], s[42:43] nt sc1
.endif
.endm
.macro SU d
.if ((\d - 104) / 4) & 1
global_store_dwordx4 v2, v[\d:\d+3], s[42:43]  sc1
.else
global_store_dwordx2 v31, v[\d:\d+1], s[42:43]  sc1
.endif
.endm
.macro SP
global_atomic_add_f64 v6, v[14:15], s[48:49]
.endm
.macro AT
global_atomic_add v11, v9, v10, s[68:69] sc0
.endm
.macro PFA
s_getpc_b64 s[70:71]
s_add_u32 s70, s70, 7f-.
s_addc_u32 s71, s71, 0
s_andn2_b32 s70, s70, 0x7f
s_min_u32 s53, %[pf], 8f-7f-1152
s_andn2_b32 s53, s53, 0x7f
s_add_u32 s70, s70, s53
s_addc_u32 s71, s71, 0
.endm
.macro PFN
s_getpc_b64 s[70:71]
s_add_u32 s70, s70, k2_code@rel32@lo-1020
s_addc_u32 s71, s71, k2_code@rel32@hi-1012
s_add_u32 s70, s70, %[pf]
s_addc_u32 s71, s71, 0
s_andn2_b32 s70, s70, 0x7f
.endm
.macro PFW
s_getpc_b64 s[70:71]
s_add_u32 s70, s70, g_w2q@rel32@lo+4
s_addc_u32 s71, s71, g_w2q@rel32@hi+12
s_add_u32 s70, s70, %[pf]
s_addc_u32 s71, s71, 0
.endm
.macro PF0
s_getpc_b64 s[70:71]
s_andn2_b32 s70, s70, 0x7f
s_min_u32 s53, %[pf], 7f-.-1152
s_andn2_b32 s53, s53, 0x7f
s_add_u32 s70, s70, s53
s_addc_u32 s71, s71, 0
.endm
.macro WL n
s_waitcnt lgkmcnt(\n)
.endm
.macro WV n
s_waitcnt vmcnt(\n)
.endm
.macro LU t,p
v_pk_fma_f16 v[\t], v[\p], v34, s67
.endm
.macro CF d,a,b
v_cvt_pk_f16_f32 v[\d], v[\a], v[\b]
.endm
.macro XS d
v_pk_fma_f16 v[\d], v[\d], v13, s56
.endm
.macro AR d,a
v_accvgpr_read_b32 v[\d], a[\a]
.endm
.macro DQ o
global_load_lds_dword v23, s[40:41] offset:\o
.endm
.macro GE
v_cmp_eq_u32 vcc, 0, v35
s_nop 4
s_cbranch_vccz 9f
v_mov_b32 v35, 14720
s_mov_b64 exec, 1
global_store_dword v35, v10, s[68:69] sc1
s_mov_b64 exec, -1
9:
.endm
.macro BW f
.if \f < 30
ds_write_b128 v12, v[128+4*\f:128+4*\f+3]
.else
ds_write_b128 v12, a[4*\f-120:4*\f-120+3]
.endif
.endm
.macro BR f
.if \f < 30
ds_read_b128 v[128+4*\f:128+4*\f+3], v20
.else
ds_read_b128 a[4*\f-120:4*\f-120+3], v20
.endif
.endm
.macro LK d,a,f,h
.if \f < 30
v_mfma_f32_4x4x4_16b_bf16 v[\d:\d+3], a[\a:\a+1], v[128+4*\f+\h:128+4*\f+\h+1], v[36:39]
.else
v_mfma_f32_4x4x4_16b_bf16 v[\d:\d+3], a[\a:\a+1], a[4*\f-120+\h:4*\f-120+\h+1], v[36:39]
.endif
.endm
.macro PK d,a,b,s
v_perm_b32 v[\d], v[\a], v[\b], v[\s]
.endm
.macro PO d
v_lshl_or_b32 v[\d], v[\d], 4, v[\d-4]
.endm
.macro LG a,b
v_cmp_eq_f32 vcc, v[\a], v[\b]
v_mov_b32 v[\b], 0x7f800000
s_nop 0
v_cndmask_b32 v[\a], v[\b], v[\a], vcc
.endm
.macro FP
ds_write_b32 v6, v19 offset:61440
.endm
.macro FR
ds_read_b32 v15, v31 offset:61440
.endm
k1_code:
v_mbcnt_lo_u32_b32 v16, -1, 0
v_mbcnt_hi_u32_b32 v16, -1, v16
v_lshlrev_b32 v0, 4, v16
v_and_b32 v17, 15, v16
v_lshrrev_b32 v18, 4, v16
s_add_u32 s68, %[glo], 1239040
s_addc_u32 s69, %[ghi], 0
s_mov_b32 s53, %[r0o]
s_load_dword s67, s[68:69], 16320
s_add_u32 s82, %[hlo], s53
s_addc_u32 s83, %[hhi], 0
s_add_u32 s84, s82, 8192
s_addc_u32 s85, s83, 0
s_and_b32 s90, %[hlo], 0x1fffff
s_add_u32 s90, s90, s53
s_mov_b32 s91, 3072
v_mov_b32 v11, s90
v_mad_u32_u24 v11, v0, s91, v11
v_lshrrev_b32 v11, 21, v11
v_lshl_add_u32 v11, v11, 2, 64
v_writelane_b32 v11, %[tsel], 63
s_sub_u32 s53, s82, %[hlo]
s_lshr_b32 s53, s53, 2
s_and_b32 s90, %[xlo], 0x1fffff
s_add_u32 s53, s53, s90
s_add_u32 s53, s53, 0x20048000
s_lshr_b32 s53, s53, 21
s_lshl_b32 s90, s53, 2
v_writelane_b32 v11, s90, 48
global_load_dword v11, v11, s[68:69] offset:-2048
v_lshlrev_b32 v19, 1, v18
s_movk_i32 s53, 0x78
v_bfe_u32 v21, v16, 2, 2
v_lshrrev_b32 v19, v19, s53
v_mul_u32_u24 v21, 0x300, v21
v_xor_b32 v19, v19, v16
v_and_b32 v19, 3, v19
v_lshl_add_u32 v21, v19, 4, v21
s_mov_b32 s97, 3072
s_mov_b32 s50, %[pos0]
s_mov_b32 s51, %[pos0]
s_mov_b32 s52, %[pos0]
s_add_u32 s74, %[lds], 0
s_add_u32 s75, s74, 163840
s_mov_b32 s37, s74
s_mul_i32 s53, %[w], 6400
s_add_u32 s76, s74, s53
s_add_u32 s77, s76, 163840
s_mov_b32 s38, s76
s_mul_i32 s60, %[w], 1536
s_add_u32 s61, s60, 512
s_add_u32 s62, s60, 16384
s_add_u32 s63, s60, 16896
s_add_u32 s64, s60, 32768
s_add_u32 s65, s60, 33280
v_lshlrev_b32 v23, 2, v16
s_lshl_b32 s53, %[w], 11
s_min_u32 s53, s53, 5120
s_add_u32 s72, s53, 1228800
s_add_u32 s40, %[glo], s72
s_addc_u32 s41, %[ghi], 0
s_add_u32 s53, s53, %[lds]
s_add_u32 m0, s53, 58368
s_nop 0
DW 0
DW 1024
s_nop 0 ;PS
s_mul_i32 s53, s50, 25600
s_add_u32 s40, %[wlo], s53
s_addc_u32 s41, %[whi], 0
s_mov_b32 m0, s38
s_nop 0
DW 0
DW 1024
DW 2048
s_add_u32 s40, s40, 3072
s_addc_u32 s41, s41, 0
s_add_u32 m0, s38, 3072
)"
R"(s_nop 0
DW 0
DW 1024
DW 2048
DQ 3072
s_add_u32 s50, s50, %[d]
s_cmp_eq_u32 s50, %[wrap]
s_cselect_b32 s50, %[rst], s50
s_add_u32 s38, s38, 32768
s_cmp_eq_u32 s38, s77
s_cselect_b32 s38, s76, s38
v_add_u32 v1, s37, v0
v_lshl_add_u32 v19, v18, 2, v17
v_and_b32 v19, 15, v19
v_lshlrev_b32 v19, 4, v19
v_lshl_add_u32 v2, v18, 8, v19
s_lshl_b32 s53, %[w], 2
s_add_u32 s53, s53, %[lds]
s_add_u32 s53, s53, 123904
s_add_u32 s53, %[lds], 123904
v_bfe_u32 v4, v17, 1, 2
v_and_b32 v5, 8, v17
v_add3_u32 v4, v4, v5, %[w]
v_lshl_add_u32 v4, v18, 2, v4
v_and_b32 v4, 15, v4
v_lshl_add_u32 v4, v17, 4, v4
v_lshl_add_u32 v4, v4, 2, s53
s_lshl_b32 s53, %[w], 9
s_add_u32 s53, s53, %[lds]
s_add_u32 s53, s53, 123904
v_lshlrev_b32 v5, 3, v16
v_add_u32 v5, s53, v5
s_lshl_b32 s53, %[w], 6
v_and_b32 v6, 0x38, v16
v_add_u32 v6, s53, v6
s_add_u32 s53, %[lds], 58368
v_and_b32 v19, 3, v16
v_lshl_add_u32 v19, v18, 2, v19
v_lshl_add_u32 v7, v19, 4, s53
v_lshlrev_b32 v8, 4, v18
v_add_u32 v8, s53, v8
v_mov_b32 v10, 1
s_mov_b32 s36, 0xffff
s_mov_b32 s56, 0x5550555
s_mov_b32 s39, 32768
s_mov_b32 s54, 0x01010101
s_mov_b32 s55, 0x01010101
s_mov_b32 %[nb], 0
v_and_b32 v28, 12, v17
v_lshl_add_u32 v28, v28, 2, v17
v_lshlrev_b32 v28, 2, v28
v_mov_b32 v34, 0x74007400
v_mov_b32 v35, -1
v_and_b32 v22, 0xfffffcff, v1
v_lshrrev_b32 v31, 1, v2
v_mov_b32 v32, 0x06020400
v_mov_b32 v33, 0x07030501
s_waitcnt lgkmcnt(0)
s_mov_b32 s78, 0xaaaaaaaa
s_mov_b32 s79, 0xaaaaaaaa
s_mov_b32 s80, 0xcccccccc
s_mov_b32 s81, 0xcccccccc
s_and_b32 s53, %[w], 1
s_lshl_b32 s53, s53, 10
s_lshr_b32 s72, %[w], 1
s_mul_i32 s72, s72, 32768
s_add_u32 s72, s72, s53
s_add_u32 s72, s72, 128000
s_movk_i32 s53, 0x78
v_lshrrev_b32 v12, 5, v0
v_and_b32 v12, 6, v12
v_bfe_u32 v12, s53, v12, 2
v_lshrrev_b32 v20, 8, v0
v_xor_b32 v20, v12, v20
v_lshl_add_u32 v20, v20, 4, s72
v_and_b32 v12, 0xf0, v0
v_lshl_add_u32 v20, v12, 2, v20
v_add_u32 v12, s72, v0
WV 9
v_bcnt_u32_b32 v11, v11, 0
v_cmp_lt_u32 s[90:91], 2, v11
s_nop 0
s_lshr_b32 s96, s91, 31
s_xor_b32 s96, s96, %[s2]
s_sub_u32 s53, 0, s96
s_xor_b32 s90, s90, s53
s_xor_b32 s91, s91, s53
s_add_u32 s53, %[xo0], 0
s_add_u32 s46, %[xlo], s53
s_addc_u32 s47, %[xhi], 0
s_bitcmp1_b64 s[90:91], 48
s_cselect_b32 s58, 16, 0
s_lshr_b32 s58, 0xfe987610, s58
v_lshrrev_b32 v3, 6, v0
v_and_b32 v3, 12, v3
v_bfe_u32 v3, s58, v3, 4
v_mad_u32_u24 v3, v3, s97, v21
XL 12,0
XL 13,64
XL 14,128
XL 15,192
XL 16,256
XL 17,320
XL 18,384
XL 19,448
XL 20,512
XL 21,576
XL 22,640
XL 23,704
s_mul_i32 s53, s96, 8320
s_cmp_lt_u32 %[ca0], 128
s_cselect_b32 s53, s53, 0
s_add_u32 s73, s53, %[ca0]
v_mov_b32 v9, s73
WV 19
s_barrier
v_lshrrev_b32 v19, 8, v31
v_sub_u32 v19, 0, v19
ds_write_b32 v31, v19 offset:61440
s_nop 1
s_nop 0
v_mov_b32 v19, 0
WV 11
BW 12
RV 40,8,6144
RV 44,8,6208
RV 48,8,6272
RV 248,8,6336
BR 12
WV 10
BW 13
s_nop 1
BR 13
WL 2
LZ 40,12
WV 9
BW 14
RV 40,8,6400
s_nop 0
BR 14
s_add_u32 s53, %[xo0], 0
s_add_u32 s46, %[xlo], s53
s_addc_u32 s47, %[xhi], 0
s_bitcmp1_b64 s[90:91], 48
s_cselect_b32 s58, 16, 0
s_lshr_b32 s58, 0xda43cb52, s58
v_lshrrev_b32 v3, 6, v0
v_and_b32 v3, 12, v3
v_bfe_u32 v3, s58, v3, 4
v_mad_u32_u24 v3, v3, s97, v21
WL 3
LS 44,13
WV 8
BW 15
RV 44,8,6464
s_nop 0
BR 15
XL 24,0
WL 3
LS 48,14
WV 8
BW 16
RV 48,8,6528
s_nop 0
BR 16
WL 3
LS 248,15
WV 7
BW 17
RV 248,8,6592
s_nop 0
BR 17
XL 25,64
WL 3
LS 40,16
WV 7
BW 18
RV 40,8,6656
s_nop 0
BR 18
XL 26,128
WL 3
LS 44,17
WV 7
BW 19
RV 44,8,6720
s_nop 0
BR 19
WL 3
LS 48,18
WV 6
BW 20
RV 48,8,6784
s_nop 0
BR 20
XL 27,192
WL 3
LS 248,19
WV 6
BW 21
RV 248,8,6848
s_nop 0
BR 21
XL 28,256
WL 3
LS 40,20
WV 6
BW 22
RA 240,7,0
s_nop 0
BR 22
WL 3
LS 44,21
WV 5
BW 23
RA 244,7,256
s_nop 0
BR 23
XL 29,320
WL 3
LS 48,22
RA 248,7,512
XL 30,384
WL 1
LS 248,23
RA 252,7,768
s_nop 6
v_mul_f32 v36, 0xbb2aaaab, v36
v_mul_f32 v37, 0xbb2aaaab, v37
v_mul_f32 v38, 0xbb2aaaab, v38
v_mul_f32 v39, 0xbb2aaaab, v39
s_nop 1
LK 40,240,12,0
LK 44,242,12,2
RA 240,7,1024
XL 31,448
LK 48,244,13,0
s_nop 0
CF 176,40,41
LK 248,246,13,2
WL 2
RA 244,7,1280
CF 177,42,43
CF 178,44,45
CF 179,46,47
XL 32,512
LK 40,248,14,0
CF 180,48,49
LK 44,250,14,2
RA 248,7,1536
CF 181,50,51
GZ 12
WL 3
XL 33,576
CF 182,248,249
CF 183,250,251
LK 48,252,15,0
CF 184,40,41
LK 248,254,15,2
RA 252,7,1792
CF 185,42,43
XL 34,640
GS 13
WL 3
CF 186,44,45
CF 187,46,47
LK 40,240,16,0
CF 188,48,49
LK 44,242,16,2
WL 2
RA 240,7,2048
CF 189,50,51
XL 35,704
GS 14
s_mul_i32 s53, s50, 25600
s_add_u32 s40, %[wlo], s53
s_addc_u32 s41, %[whi], 0
s_mov_b32 m0, s38
CF 190,248,249
CF 191,250,251
LK 48,244,17,0
CF 192,40,41
LK 248,246,17,2
RA 244,7,2304
CF 193,42,43
GS 15
WL 3
DW 0
CF 194,44,45
CF 195,46,47
LK 40,248,18,0
CF 196,48,49
LK 44,250,18,2
RA 248,7,2560
CF 197,50,51
DW 1024
GS 16
WL 3
CF 198,248,249
CF 199,250,251
LK 48,252,19,0
CF 200,40,41
LK 248,254,19,2
WL 2
RA 252,7,2816
CF 201,42,43
DW 2048
GS 17
s_add_u32 s40, s40, 3072
s_addc_u32 s41, s41, 0
s_add_u32 m0, s38, 3072
CF 202,44,45
CF 203,46,47
LK 40,240,20,0
CF 204,48,49
LK 44,242,20,2
CF 205,50,51
GS 18
WL 2
DW 0
CF 206,248,249
CF 207,250,251
LK 48,244,21,0
CF 208,40,41
LK 248,246,21,2
CF 209,42,43
DW 1024
GS 19
WL 1
CF 210,44,45
CF 211,46,47
LK 40,248,22,0
CF 212,48,49
LK 44,250,22,2
CF 213,50,51
DW 2048
GS 20
WL 0
DQ 3072
CF 214,248,249
CF 215,250,251
LK 48,252,23,0
CF 216,40,41
LK 248,254,23,2
CF 217,42,43
GS 21
s_add_u32 s50, s50, %[d]
s_cmp_eq_u32 s50, %[wrap]
s_cselect_b32 s50, %[rst], s50
CF 218,44,45
CF 219,46,47
RV 36,8,6912
s_nop 0
GS 22
s_add_u32 s38, s38, 32768
s_cmp_eq_u32 s38, s77
s_cselect_b32 s38, s76, s38
CF 220,48,49
CF 221,50,51
CF 222,248,249
CF 223,250,251
s_nop 1
GS 23
s_nop 7
v_cndmask_b32 v52, v52, v53, s[78:79]
v_cndmask_b32 v54, v54, v55, s[78:79]
v_cndmask_b32 v52, v52, v54, s[80:81]
v_mul_f32 v52, 0x3b2aaaab, v52
WL 0
v_med3_f32 v53, v52, v39, v37
LG 52,53
s_nop 0
C 52,52,52
v_lshlrev_b32 v52, 16, v52
v_add_f32 v52, v36, v52
C 52,52,52
v_lshlrev_b32 v52, 16, v52
v_sqrt_f32 v52, v52
s_nop 0
C 52,52,52
v_lshlrev_b32 v52, 16, v52
v_rcp_f32 v52, v52
s_nop 0
v_mul_f32 v52, v38, v52
CF 52,52,52
ds_bpermute_b32 v13, v28, v52
WV 18
BW 24
RV 40,8,6144
RV 44,8,6208
RV 48,8,6272
BR 24
WV 17
BW 25
s_nop 1
BR 25
WL 7
XS 176
WL 2
LZ 40,24
)"
R"(s_and_b32 s53, %[w], 1
s_mul_i32 s53, s53, 3072
s_lshr_b32 s72, %[w], 1
s_lshl_b32 s40, s72, 16
s_add_u32 s40, s40, s53
s_add_u32 m0, s40, 25600
s_lshl_b32 s72, s72, 13
s_add_u32 s53, s53, s72
s_add_u32 s53, s53, 1382400
s_add_u32 s40, %[glo], s53
s_addc_u32 s41, %[ghi], 0
XS 177
WV 16
BW 26
XS 178
RV 40,8,6336
XS 179
BR 26
XS 180
WL 3
LS 44,25
WV 15
DW 0
XS 181
BW 27
XS 182
RV 44,8,6400
XS 183
BR 27
v_min_u32 v35, v35, v13
WL 3
LS 48,26
WV 15
DW 1024
XS 184
BW 28
XS 185
RV 48,8,6464
XS 186
BR 28
XS 187
WL 3
LS 40,27
WV 15
XS 188
BW 29
XS 189
RV 40,8,6528
XS 190
XS 191
BR 29
XS 192
WL 3
LS 44,28
WV 14
DW 2048
XS 193
BW 30
XS 194
RV 44,8,6592
XS 195
BR 30
XS 196
WL 3
LS 48,29
WV 14
DW 3072
XS 197
BW 31
XS 198
RV 48,8,6656
XS 199
BR 31
XS 200
WL 3
LS 40,30
WV 13
XS 201
BW 32
XS 202
RV 40,8,6720
XS 203
BR 32
XS 204
WL 3
LS 44,31
PF0
s_mov_b32 m0, %[pm]
s_nop 0
DF
PFA
XS 205
BW 33
XS 206
XS 207
RV 44,8,6784
XS 208
BR 33
XS 209
WL 3
LS 48,32
s_mul_i32 s53, s50, 25600
s_add_u32 s40, %[wlo], s53
s_addc_u32 s41, %[whi], 0
s_mov_b32 m0, s38
XS 210
WV 13
BW 34
XS 211
RV 48,8,6848
XS 212
RA 240,7,0
XS 213
BR 34
XS 214
WL 4
LS 40,33
WV 12
XS 215
BW 35
XS 216
RA 244,7,256
XS 217
BR 35
XS 218
WL 3
LS 44,34
DW 0
XS 219
RA 248,7,512
XS 220
WL 1
LS 48,35
DW 1024
XS 221
RA 252,7,768
XS 222
XS 223
s_nop 2
v_mul_f32 v36, 0xbb2aaaab, v36
v_mul_f32 v37, 0xbb2aaaab, v37
v_mul_f32 v38, 0xbb2aaaab, v38
v_mul_f32 v39, 0xbb2aaaab, v39
s_nop 1
LK 40,240,24,0
LK 44,242,24,2
RA 240,7,1024
LK 48,244,25,0
s_nop 1
CF 224,40,41
LK 248,246,25,2
WL 2
RA 244,7,1280
CF 225,42,43
CF 226,44,45
CF 227,46,47
DW 2048
LK 40,248,26,0
CF 228,48,49
LK 44,250,26,2
RA 248,7,1536
CF 229,50,51
s_add_u32 s40, s40, 3072
s_addc_u32 s41, s41, 0
s_add_u32 m0, s38, 3072
GZ 24
WL 3
CF 230,248,249
CF 231,250,251
LK 48,252,27,0
CF 232,40,41
LK 248,254,27,2
RA 252,7,1792
CF 233,42,43
DW 0
GS 25
WL 3
DW 1024
CF 234,44,45
CF 235,46,47
LK 40,240,28,0
CF 236,48,49
LK 44,242,28,2
RA 240,7,2048
CF 237,50,51
GS 26
WL 3
DW 2048
CF 238,248,249
CF 239,250,251
LK 48,244,29,0
CF 240,40,41
LK 248,246,29,2
RA 244,7,2304
CF 241,42,43
DQ 3072
GS 27
WL 3
CF 242,44,45
CF 243,46,47
LK 40,248,30,0
CF 244,48,49
LK 44,250,30,2
WL 2
RA 248,7,2560
CF 245,50,51
s_add_u32 s50, s50, %[d]
s_cmp_eq_u32 s50, %[wrap]
s_cselect_b32 s50, %[rst], s50
GS 28
s_add_u32 s38, s38, 32768
s_cmp_eq_u32 s38, s77
s_cselect_b32 s38, s76, s38
CF 246,248,249
CF 247,250,251
LK 48,252,31,0
CF 152,40,41
LK 248,254,31,2
WL 2
RA 252,7,2816
CF 153,42,43
GS 29
s_add_u32 s53, %[xo0], 196608
s_add_u32 s46, %[xlo], s53
s_addc_u32 s47, %[xhi], 0
s_bitcmp1_b64 s[90:91], 48
s_cselect_b32 s58, 16, 0
s_lshr_b32 s58, 0xfe987610, s58
v_lshrrev_b32 v3, 6, v0
v_and_b32 v3, 12, v3
v_bfe_u32 v3, s58, v3, 4
v_mad_u32_u24 v3, v3, s97, v21
CF 154,44,45
CF 155,46,47
LK 40,240,32,0
CF 156,48,49
LK 44,242,32,2
CF 157,50,51
XL 36,0
GS 6
WL 2
CF 158,248,249
CF 159,250,251
LK 48,244,33,0
CF 160,40,41
LK 248,246,33,2
CF 161,42,43
XL 37,64
GS 7
WL 1
XL 38,128
CF 162,44,45
CF 163,46,47
LK 40,248,34,0
CF 164,48,49
LK 44,250,34,2
CF 165,50,51
GS 8
WL 0
XL 39,192
CF 166,248,249
CF 167,250,251
LK 48,252,35,0
CF 168,40,41
LK 248,254,35,2
CF 169,42,43
XL 40,256
GS 9
CF 170,44,45
CF 171,46,47
RV 36,8,6912
s_nop 0
GS 10
XL 41,320
CF 172,48,49
CF 173,50,51
CF 174,248,249
CF 175,250,251
s_nop 1
GS 11
XL 42,384
WV 15
s_barrier
RA 216,1,0
RA 220,1,1024
RA 224,1,2048
RA 228,1,3072
RV 252,22,5120
v_cndmask_b32 v52, v52, v53, s[78:79]
v_cndmask_b32 v54, v54, v55, s[78:79]
v_cndmask_b32 v52, v52, v54, s[80:81]
v_mul_f32 v52, 0x3b2aaaab, v52
WL 5
v_med3_f32 v53, v52, v39, v37
LG 52,53
s_nop 0
C 52,52,52
v_lshlrev_b32 v52, 16, v52
v_add_f32 v52, v36, v52
C 52,52,52
XL 43,448
v_lshlrev_b32 v52, 16, v52
v_sqrt_f32 v52, v52
s_nop 0
C 52,52,52
v_lshlrev_b32 v52, 16, v52
v_rcp_f32 v52, v52
XL 44,512
v_mul_f32 v52, v38, v52
CF 52,52,52
ds_bpermute_b32 v13, v28, v52
WL 0
XS 224
XS 225
XS 226
XS 227
XS 228
XS 229
XS 230
XL 45,576
XS 231
v_min_u32 v35, v35, v13
XS 232
XS 233
XS 234
XL 46,640
XS 235
XS 236
XS 237
XS 238
XS 239
XL 47,704
Z 0,12,0
XS 240
XS 241
XS 242
XS 243
XS 244
XS 245
XS 246
XS 247
XS 152
XS 153
XS 154
XS 155
M 0,13,1
s_mul_i32 s53, s50, 25600
RA 232,1,4096
AW 0,152
AW 1,153
Z 1,24,0
s_add_u32 s40, %[wlo], s53
AW 2,154
AW 3,155
M 1,25,1
s_addc_u32 s41, %[whi], 0
RA 236,1,6144
XS 156
XS 157
M 0,14,2
s_mov_b32 m0, s38
s_nop 0
DW 0
XS 158
XS 159
AW 4,156
M 0,15,3
RA 216,1,7168
AW 5,157
AW 6,158
M 1,26,2
DW 1024
AW 7,159
XS 160
M 1,27,3
WL 2
RA 220,1,8192
XS 161
XS 162
M 0,16,4
WL 2
XS 163
AW 8,160
M 0,17,5
RA 224,1,9216
AW 9,161
AW 10,162
AW 11,163
M 1,28,4
WL 1
DW 2048
XS 164
XS 165
M 1,29,5
s_add_u32 s40, s40, 3072
RA 228,1,10240
XS 166
XS 167
M 0,18,0
s_addc_u32 s41, s41, 0
)"
R"(WV 14
BW 36
AW 12,164
AW 13,165
M 0,19,1
s_add_u32 m0, s38, 3072
RA 232,1,11264
RV 40,8,6144
AW 14,166
AW 15,167
M 1,30,0
RV 252,22,5392
RV 44,8,6208
XS 168
XS 169
M 1,31,1
WL 6
RA 236,1,12288
RV 48,8,6272
XS 170
XS 171
AW 16,168
M 0,20,2
WL 7
DW 0
BR 36
AW 17,169
AW 18,170
M 0,21,3
RA 216,1,13312
AW 19,171
XS 172
M 1,32,2
DW 1024
XS 173
XS 174
M 1,33,3
WL 7
RA 220,1,14336
XS 175
WV 15
BW 37
s_nop 1
BR 37
AW 20,172
M 0,22,4
WL 6
DW 2048
AW 21,173
AW 22,174
AW 23,175
M 0,23,5
WL 4
RA 224,1,15360
LZ 40,36
WV 15
BW 38
RV 40,8,6336
s_nop 0
BR 38
M 1,34,4
DQ 3072
M 1,35,5
WL 4
RA 228,1,16384
LS 44,37
WV 15
BW 39
RV 44,8,6400
Z 6,12,0
s_add_u32 s50, s50, %[d]
BR 39
LL 16,56
LL 104,57
M 6,13,1
s_cmp_eq_u32 s50, %[wrap]
LA 16
RA 232,1,17408
WL 6
LS 48,38
s_cselect_b32 s50, %[rst], s50
WV 14
BW 40
RV 48,8,6464
Z 7,24,0
s_add_u32 s38, s38, 32768
LC_ 104
BR 40
M 7,25,1
s_cmp_eq_u32 s38, s77
RA 236,1,18432
WL 7
LS 40,39
s_cselect_b32 s38, s76, s38
WV 13
BW 41
RV 40,8,6528
M 6,14,2
s_add_u32 s37, s37, 32768
BR 41
M 6,15,3
s_cmp_eq_u32 s37, s75
RA 216,1,19456
WL 5
LS 44,40
s_cselect_b32 s37, s74, s37
WV 12
BW 42
M 7,26,2
s_add_u32 s53, %[xo0], 196608
RV 44,8,6592
BR 42
LL 17,58
LL 105,59
M 7,27,3
s_add_u32 s46, %[xlo], s53
LA 17
RA 220,1,20480
s_addc_u32 s47, %[xhi], 0
s_bitcmp1_b64 s[90:91], 48
s_cselect_b32 s58, 16, 0
s_lshr_b32 s58, 0xda43cb52, s58
v_lshrrev_b32 v3, 6, v0
v_and_b32 v3, 12, v3
v_bfe_u32 v3, s58, v3, 4
v_mad_u32_u24 v3, v3, s97, v21
WL 6
LS 48,41
WV 10
BW 43
M 6,16,4
WL 3
LC_ 105
XL 48,0
RV 48,8,6656
BR 43
M 6,17,5
s_mul_i32 s53, s51, 49152
RA 224,1,21504
LS 40,42
s_bitcmp1_b64 s[90:91], s51
BW 44
M 7,28,4
WV 10
RV 40,8,6720
LO 104,16,104
LU 16,104
M 7,29,5
s_cselect_b64 s[44:45], s[84:85], s[82:83]
RA 228,1,22528
BR 44
HZ 24,16
WL 5
LS 44,43
s_add_u32 s44, s44, s53
BW 45
M 6,18,0
s_addc_u32 s45, s45, 0
LL 18,60
LL 108,61
s_add_u32 s51, s51, %[d]
s_cmp_eq_u32 s51, %[wrap]
s_cselect_b32 s51, %[rst], s51
XL 49,64
RV 44,8,6784
M 6,19,1
WL 2
LA 18
RA 232,1,23552
BR 45
LS 48,44
WV 10
BW 46
M 7,30,0
LC_ 108
XL 50,128
M 7,31,1
RA 236,1,24576
RV 48,8,6848
RA 240,7,0
M 6,20,2
BR 46
LO 105,17,105
LU 17,105
M 6,21,3
WL 6
XL 51,192
HS 24,17
LS 40,45
WV 11
BW 47
RA 244,7,256
M 7,32,2
LL 16,62
LL 109,63
v_add_u32 v1, s37, v0
BR 47
M 7,33,3
WL 3
LA 16
RA 216,1,0
XL 52,256
LS 44,46
RA 248,7,512
v_and_b32 v22, 0xfffffcff, v1
M 6,22,4
LC_ 109
RA 220,1,1024
M 6,23,5
WL 5
RV 252,22,5120
LS 48,47
RA 252,7,768
s_nop 6
v_mul_f32 v36, 0xbb2aaaab, v36
v_mul_f32 v37, 0xbb2aaaab, v37
v_mul_f32 v38, 0xbb2aaaab, v38
M 7,34,4
XL 53,320
LO 108,18,108
LU 18,108
v_mul_f32 v39, 0xbb2aaaab, v39
M 7,35,5
s_barrier
RA 224,1,2048
HS 24,18
WL 2
Z 0,12,0
LL 17,80
LL 106,81
M 0,13,1
s_mul_i32 s53, s50, 25600
LA 17
RA 228,1,3072
LK 40,240,36,0
LK 44,242,36,2
s_add_u32 s40, %[wlo], s53
RA 240,7,1024
LK 48,244,37,0
s_nop 0
CF 128,40,41
LK 248,246,37,2
s_addc_u32 s41, %[whi], 0
RA 244,7,1280
CF 129,42,43
CF 130,44,45
CF 131,46,47
LK 40,248,38,0
CF 132,48,49
LK 44,250,38,2
s_mov_b32 m0, s38
RA 248,7,1536
CF 133,50,51
GZ 0
WL 6
CF 134,248,249
CF 135,250,251
LK 48,252,39,0
CF 136,40,41
LK 248,254,39,2
RA 252,7,1792
CF 137,42,43
GS 1
CF 138,44,45
CF 139,46,47
Z 1,24,0
LC_ 106
RA 232,1,4096
M 1,25,1
WL 8
RA 236,1,6144
M 0,14,2
WL 7
LO 109,16,109
LU 16,109
M 0,15,3
WL 6
RA 216,1,7168
HS 24,16
LK 40,240,40,0
CF 140,48,49
LK 44,242,40,2
RA 240,7,2048
CF 141,50,51
GS 2
WL 7
CF 142,248,249
CF 143,250,251
LK 48,244,41,0
CF 144,40,41
LK 248,246,41,2
RA 244,7,2304
CF 145,42,43
GS 3
WL 7
CF 146,44,45
CF 147,46,47
LK 40,248,42,0
CF 148,48,49
LK 44,250,42,2
RA 248,7,2560
CF 149,50,51
GS 4
CF 150,248,249
CF 151,250,251
M 1,26,2
DW 0
LL 18,82
LL 107,83
M 1,27,3
WL 7
LA 18
RA 220,1,8192
LK 48,252,43,0
CF 152,40,41
LK 248,254,43,2
RA 252,7,2816
CF 153,42,43
GS 5
WL 8
CF 154,44,45
CF 155,46,47
M 0,16,4
WL 7
LC_ 107
DW 1024
M 0,17,5
WL 6
RA 224,1,9216
HX 24
LK 40,240,44,0
CF 156,48,49
LK 44,242,44,2
CF 157,50,51
GS 6
CF 158,248,249
CF 159,250,251
M 1,28,4
LO 17,17,106
LU 106,17
W1 24,0
M 1,29,5
WL 7
RA 228,1,10240
DW 2048
HZ 25,106
LK 48,244,45,0
CF 160,40,41
LK 248,246,45,2
CF 161,42,43
GS 7
WL 5
CF 162,44,45
CF 163,46,47
M 0,18,0
s_add_u32 s40, s40, 3072
PK 106,17,104,33
PK 104,17,104,32
LL 16,84
LL 110,85
M 0,19,1
s_addc_u32 s41, s41, 0
LA 16
RA 232,1,11264
LK 40,248,46,0
CF 164,48,49
LK 44,250,46,2
s_add_u32 m0, s38, 3072
CF 165,50,51
GS 8
CF 166,248,249
CF 167,250,251
M 1,30,0
LC_ 110
RV 252,22,5392
M 1,31,1
WL 6
RA 236,1,12288
M 0,20,2
WL 5
LO 18,18,107
LU 107,18
M 0,21,3
RA 216,1,13312
DW 0
HS 25,107
LK 48,252,47,0
CF 168,40,41
LK 248,254,47,2
CF 169,42,43
GS 9
CF 170,44,45
CF 171,46,47
M 1,32,2
PK 107,18,105,33
PK 105,18,105,32
LL 17,86
LL 111,87
M 1,33,3
WL 1
)"
R"(LA 17
RA 220,1,14336
DW 1024
M 0,22,4
s_add_u32 s42, s44, s60
LC_ 111
RV 36,8,6912
s_addc_u32 s43, s45, 0
M 0,23,5
RA 224,1,15360
GS 10
CF 172,48,49
CF 173,50,51
M 1,34,4
LO 16,16,110
LU 110,16
ST 104
DW 2048
M 1,35,5
WL 5
RA 228,1,16384
HS 25,110
Z 6,12,0
WL 4
PK 110,16,108,33
PK 108,16,108,32
LL 18,56
LL 104,57
M 6,13,1
WL 1
LA 18
DQ 3072
PO 110
CF 174,248,249
CF 175,250,251
s_nop 1
GS 11
s_add_u32 s50, s50, %[d]
RA 232,1,17408
s_nop 5
v_cndmask_b32 v52, v52, v53, s[78:79]
Z 7,24,0
s_cmp_eq_u32 s50, %[wrap]
LC_ 104
v_cndmask_b32 v54, v54, v55, s[78:79]
M 7,25,1
s_cselect_b32 s50, %[rst], s50
RA 236,1,18432
v_cndmask_b32 v52, v52, v54, s[80:81]
v_mul_f32 v52, 0x3b2aaaab, v52
M 6,14,2
WL 2
LO 17,17,111
LU 111,17
v_med3_f32 v53, v52, v39, v37
LG 52,53
M 6,15,3
s_add_u32 s38, s38, 32768
RA 216,1,19456
HS 25,111
C 52,52,52
v_lshlrev_b32 v52, 16, v52
M 7,26,2
s_cmp_eq_u32 s38, s77
PK 111,17,109,33
PK 109,17,109,32
LL 16,58
LL 105,59
s_cselect_b32 s38, s76, s38
v_add_f32 v52, v36, v52
C 52,52,52
M 7,27,3
s_add_u32 s37, s37, 32768
LA 16
PO 111
s_cmp_eq_u32 s37, s75
s_cselect_b32 s37, s74, s37
v_lshlrev_b32 v52, 16, v52
v_sqrt_f32 v52, v52
M 6,16,4
WL 2
LC_ 105
RA 220,1,20480
XL 54,384
C 52,52,52
v_lshlrev_b32 v52, 16, v52
M 6,17,5
s_add_u32 s42, s44, s61
RA 224,1,21504
HX 25
s_addc_u32 s43, s45, 0
v_rcp_f32 v52, v52
s_nop 0
v_mul_f32 v52, v38, v52
M 7,28,4
LO 104,18,104
LU 18,104
W1 25,1024
CF 52,52,52
M 7,29,5
WL 2
RA 228,1,22528
HZ 24,18
ST 108
XL 55,448
ds_bpermute_b32 v13, v28, v52
M 6,18,0
s_mul_i32 s53, s51, 49152
LL 17,60
LL 108,61
s_bitcmp1_b64 s[90:91], s51
s_cselect_b64 s[44:45], s[84:85], s[82:83]
s_add_u32 s44, s44, s53
s_addc_u32 s45, s45, 0
s_add_u32 s51, s51, %[d]
s_cmp_eq_u32 s51, %[wrap]
s_cselect_b32 s51, %[rst], s51
M 6,19,1
LA 17
RA 232,1,23552
M 7,30,0
WL 5
LC_ 108
XL 56,512
v_add_u32 v4, s39, v4
M 7,31,1
WV 17
RA 236,1,24576
s_nop 1
v_add_u32 v1, s37, v0
BW 48
M 6,20,2
WL 6
XL 57,576
RV 40,8,6144
RV 44,8,6208
LO 105,16,105
LU 16,105
M 6,21,3
RA 216,1,0
RV 48,8,6272
HS 24,16
M 7,32,2
WL 5
BR 48
LL 18,62
LL 109,63
M 7,33,3
WV 16
LA 18
XL 58,640
BW 49
s_nop 1
BR 49
M 6,22,4
s_lshl_b32 s53, s52, 8
LC_ 109
v_and_b32 v22, 0xfffffcff, v1
XS 128
M 6,23,5
s_add_u32 s48, %[alo], s53
RA 220,1,1024
XS 129
XS 130
XS 131
WL 5
LZ 40,48
s_addc_u32 s49, %[ahi], 0
BW 50
M 7,34,4
WV 16
LO 108,17,108
LU 17,108
s_add_u32 s52, s52, %[d]
s_cmp_eq_u32 s52, %[wrap]
s_cselect_b32 s52, %[rst], s52
XL 59,704
RV 40,8,6336
BR 50
AW 24,128
AW 25,129
M 7,35,5
s_barrier
RV 252,22,5120
HS 24,17
AW 26,130
AW 27,131
WL 0
Z 0,12,0
s_sub_u32 s39, 0, s39
RA 224,1,2048
LL 16,80
LL 106,81
XS 132
XS 133
M 0,13,1
s_mul_i32 s53, s50, 25600
LA 16
RA 228,1,3072
XS 134
XS 135
AW 28,132
LS 44,49
s_add_u32 s40, %[wlo], s53
BW 51
RV 44,8,6400
s_nop 0
BR 51
Z 1,24,0
s_addc_u32 s41, %[whi], 0
LC_ 106
RA 232,1,4096
AW 29,133
AW 30,134
M 1,25,1
s_mov_b32 m0, s38
RA 236,1,6144
ds_read_b64 v[14:15], v5 offset:0
AW 31,135
v_min_u32 v35, v35, v13
LS 48,50
WV 16
BW 52
RV 48,8,6464
WL 11
M 0,14,2
WL 9
BR 52
LO 109,18,109
LU 18,109
XS 136
XS 137
M 0,15,3
WL 7
RA 216,1,7168
HS 24,18
XS 138
XS 139
AW 32,136
LS 40,51
WV 15
BW 53
M 1,26,2
DW 0
RV 40,8,6528
LL 17,82
LL 107,83
AW 33,137
AW 34,138
M 1,27,3
WL 8
LA 17
RA 220,1,8192
v_subrev_u32 v5, s39, v5
BR 53
AW 35,139
XS 140
M 0,16,4
WL 10
LC_ 107
DW 1024
XS 141
XS 142
M 0,17,5
WL 7
RA 224,1,9216
v_add_f32 v14, v14, v15
s_nop 1
v_add_f32_dpp v14, v14, v14 quad_perm:[1,0,3,2] row_mask:0xf bank_mask:0xf
s_nop 1
v_add_f32_dpp v14, v14, v14 quad_perm:[2,3,0,1] row_mask:0xf bank_mask:0xf
s_nop 1
v_add_f32_dpp v14, v14, v14 row_half_mirror row_mask:0xf bank_mask:0xf
HX 24
XS 143
AW 36,140
AW 37,141
LS 44,52
WV 8
BW 54
RV 44,8,6592
M 1,28,4
LO 16,16,106
LU 106,16
v_cvt_f64_f32 v[14:15], v14
W1 24,0
BR 54
AW 38,142
AW 39,143
M 1,29,5
WL 6
RA 228,1,10240
HZ 25,106
s_mov_b64 exec, s[54:55]
SP
s_mov_b64 exec, -1
DW 2048
XS 144
XS 145
LS 48,53
s_add_u32 s40, s40, 3072
WV 8
BW 55
M 0,18,0
s_addc_u32 s41, s41, 0
RV 48,8,6656
PK 106,16,104,33
PK 104,16,104,32
LL 18,84
LL 110,85
XS 146
XS 147
M 0,19,1
s_add_u32 m0, s38, 3072
LA 18
RA 232,1,11264
BR 55
AW 40,144
AW 41,145
AW 42,146
M 1,30,0
LC_ 110
RV 252,22,5392
AW 43,147
XS 148
M 1,31,1
WL 8
RA 236,1,12288
XS 149
XS 150
LS 40,54
WV 7
BW 56
RV 40,8,6720
M 0,20,2
WL 10
DW 0
BR 56
LO 17,17,107
LU 107,17
XS 151
AW 44,148
M 0,21,3
WL 6
RA 216,1,13312
HS 25,107
AW 45,149
AW 46,150
AW 47,151
LS 44,55
WV 7
BW 57
M 1,32,2
WL 5
DW 1024
RV 44,8,6784
PK 107,17,105,33
PK 105,17,105,32
LL 16,86
LL 111,87
XS 152
XS 153
M 1,33,3
s_add_u32 s42, s44, s60
LA 16
RA 220,1,14336
BR 57
XS 154
XS 155
M 0,22,4
s_addc_u32 s43, s45, 0
LC_ 111
DW 2048
AW 48,152
AW 49,153
M 0,23,5
WL 7
RA 224,1,15360
AW 50,154
AW 51,155
XS 156
LS 48,56
WV 8
BW 58
RV 48,8,6848
M 1,34,4
LO 18,18,110
LU 110,18
ST 104
RA 240,7,0
XS 157
XS 158
M 1,35,5
WL 5
RA 228,1,16384
DQ 3072
BR 58
HS 25,110
XS 159
AW 52,156
)"
R"(LS 40,57
WV 9
BW 59
Z 6,12,0
s_add_u32 s50, s50, %[d]
PK 110,18,108,33
PK 108,18,108,32
LL 17,56
LL 104,57
AW 53,157
AW 54,158
M 6,13,1
s_cmp_eq_u32 s50, %[wrap]
LA 17
RA 244,7,256
PO 110
AW 55,159
XS 160
Z 7,24,0
s_cselect_b32 s50, %[rst], s50
LC_ 104
RA 232,1,17408
BR 59
XS 161
XS 162
XS 163
M 7,25,1
s_add_u32 s38, s38, 32768
RA 236,1,18432
AW 56,160
AW 57,161
WL 7
LS 44,58
s_cmp_eq_u32 s38, s77
RA 248,7,512
AW 58,162
M 6,14,2
WL 2
LO 16,16,111
LU 111,16
s_cselect_b32 s38, s76, s38
AW 59,163
M 6,15,3
s_add_u32 s37, s37, 32768
RA 216,1,19456
HS 25,111
XS 164
XS 165
LS 48,59
s_cmp_eq_u32 s37, s75
RA 252,7,768
XS 166
M 7,26,2
s_cselect_b32 s37, s74, s37
PK 111,16,109,33
PK 109,16,109,32
LL 18,58
LL 105,59
XS 167
AW 60,164
M 7,27,3
s_add_u32 s53, %[xo0], 393216
LA 18
PO 111
s_add_u32 s46, %[xlo], s53
s_addc_u32 s47, %[xhi], 0
s_bitcmp1_b64 s[90:91], 48
s_cselect_b32 s58, 16, 0
s_lshr_b32 s58, 0xfe987610, s58
v_lshrrev_b32 v3, 6, v0
v_and_b32 v3, 12, v3
v_bfe_u32 v3, s58, v3, 4
v_mad_u32_u24 v3, v3, s97, v21
AW 61,165
AW 62,166
M 6,16,4
s_add_u32 s42, s44, s61
LC_ 105
RA 220,1,20480
AW 63,167
XS 168
WL 6
M 6,17,5
s_addc_u32 s43, s45, 0
RA 224,1,21504
XL 60,0
HX 25
XS 169
XS 170
M 7,28,4
LO 104,17,104
LU 17,104
W1 25,1024
XS 171
AW 64,168
AW 65,169
M 7,29,5
WL 2
RA 228,1,22528
HZ 24,17
ST 108
AW 66,170
AW 67,171
M 6,18,0
s_mul_i32 s53, s51, 49152
LL 16,60
LL 108,61
s_bitcmp1_b64 s[90:91], s51
s_cselect_b64 s[44:45], s[84:85], s[82:83]
s_add_u32 s44, s44, s53
s_addc_u32 s45, s45, 0
s_add_u32 s51, s51, %[d]
s_cmp_eq_u32 s51, %[wrap]
s_cselect_b32 s51, %[rst], s51
XL 61,64
XS 172
XS 173
M 6,19,1
LA 16
RA 232,1,23552
XS 174
XS 175
M 7,30,0
LC_ 108
XL 62,128
v_add_u32 v4, s39, v4
AW 68,172
AW 69,173
AW 70,174
M 7,31,1
RA 236,1,24576
s_nop 1
v_add_u32 v1, s37, v0
AW 71,175
v_mul_f32 v36, 0xbb2aaaab, v36
v_mul_f32 v37, 0xbb2aaaab, v37
v_mul_f32 v38, 0xbb2aaaab, v38
v_mul_f32 v39, 0xbb2aaaab, v39
s_nop 1
LK 40,240,48,0
LK 44,242,48,2
RA 240,7,1024
LK 48,244,49,0
s_nop 1
CF 128,40,41
LK 248,246,49,2
RA 244,7,1280
CF 129,42,43
CF 130,44,45
CF 131,46,47
LK 40,248,50,0
CF 132,48,49
LK 44,250,50,2
RA 248,7,1536
CF 133,50,51
GZ 0
CF 134,248,249
CF 135,250,251
LK 48,252,51,0
CF 136,40,41
LK 248,254,51,2
RA 252,7,1792
CF 137,42,43
GS 1
WL 8
CF 138,44,45
CF 139,46,47
M 6,20,2
LO 105,18,105
LU 18,105
M 6,21,3
WL 3
RA 216,1,0
XL 63,192
HS 24,18
LK 40,240,52,0
CF 140,48,49
LK 44,242,52,2
RA 240,7,2048
CF 141,50,51
GS 2
WL 4
CF 142,248,249
CF 143,250,251
LK 48,244,53,0
CF 144,40,41
LK 248,246,53,2
RA 244,7,2304
CF 145,42,43
GS 3
WL 4
CF 146,44,45
CF 147,46,47
LK 40,248,54,0
CF 148,48,49
LK 44,250,54,2
RA 248,7,2560
CF 149,50,51
GS 4
CF 150,248,249
CF 151,250,251
M 7,32,2
LL 17,62
LL 109,63
M 7,33,3
WL 4
LA 17
XL 64,256
LK 48,252,55,0
CF 152,40,41
LK 248,254,55,2
s_lshl_b32 s53, s52, 8
RA 252,7,2816
CF 153,42,43
GS 5
s_add_u32 s48, %[alo], s53
CF 154,44,45
CF 155,46,47
M 6,22,4
s_addc_u32 s49, %[ahi], 0
LC_ 109
v_and_b32 v22, 0xfffffcff, v1
M 6,23,5
s_add_u32 s52, s52, %[d]
RA 220,1,1024
M 7,34,4
LO 108,16,108
LU 16,108
s_cmp_eq_u32 s52, %[wrap]
s_cselect_b32 s52, %[rst], s52
XL 65,320
M 7,35,5
s_barrier
RV 252,22,5120
HS 24,16
WL 0
Z 0,12,0
s_sub_u32 s39, 0, s39
RA 224,1,2048
LL 18,80
LL 106,81
M 0,13,1
s_mul_i32 s53, s50, 25600
LA 18
RA 228,1,3072
LK 40,240,56,0
CF 156,48,49
LK 44,242,56,2
s_add_u32 s40, %[wlo], s53
CF 157,50,51
GS 6
s_addc_u32 s41, %[whi], 0
CF 158,248,249
CF 159,250,251
LK 48,244,57,0
CF 160,40,41
LK 248,246,57,2
s_mov_b32 m0, s38
CF 161,42,43
GS 7
CF 162,44,45
CF 163,46,47
Z 1,24,0
LC_ 106
RA 232,1,4096
M 1,25,1
RA 236,1,6144
ds_read_b64 v[14:15], v5 offset:0
LK 40,248,58,0
CF 164,48,49
LK 44,250,58,2
CF 165,50,51
GS 8
WL 6
CF 166,248,249
CF 167,250,251
M 0,14,2
WL 4
LO 109,17,109
LU 17,109
M 0,15,3
RA 216,1,7168
HS 24,17
LK 48,252,59,0
CF 168,40,41
LK 248,254,59,2
CF 169,42,43
v_subrev_u32 v5, s39, v5
GS 9
CF 170,44,45
CF 171,46,47
M 1,26,2
DW 0
LL 16,82
LL 107,83
M 1,27,3
WL 3
LA 16
RA 220,1,8192
M 0,16,4
WL 4
LC_ 107
DW 1024
M 0,17,5
RA 224,1,9216
RV 36,8,6912
HX 24
GS 10
CF 172,48,49
CF 173,50,51
M 1,28,4
LO 18,18,106
LU 106,18
W1 24,0
M 1,29,5
WL 4
RA 228,1,10240
HZ 25,106
v_add_f32 v14, v14, v15
s_nop 1
v_add_f32_dpp v14, v14, v14 quad_perm:[1,0,3,2] row_mask:0xf bank_mask:0xf
s_nop 1
v_add_f32_dpp v14, v14, v14 quad_perm:[2,3,0,1] row_mask:0xf bank_mask:0xf
s_nop 1
v_add_f32_dpp v14, v14, v14 row_half_mirror row_mask:0xf bank_mask:0xf
v_cvt_f64_f32 v[14:15], v14
s_mov_b64 exec, s[54:55]
SP
s_mov_b64 exec, -1
DW 2048
M 0,18,0
s_add_u32 s40, s40, 3072
PK 106,18,104,33
PK 104,18,104,32
LL 17,84
LL 110,85
M 0,19,1
s_addc_u32 s41, s41, 0
LA 17
RA 232,1,11264
M 1,30,0
s_add_u32 m0, s38, 3072
LC_ 110
RV 252,22,5392
M 1,31,1
WL 7
RA 236,1,12288
CF 174,248,249
CF 175,250,251
M 0,20,2
WL 5
LO 16,16,107
LU 107,16
M 0,21,3
RA 216,1,13312
DW 0
HS 25,107
GS 11
WL 4
DW 1024
s_nop 5
v_cndmask_b32 v52, v52, v53, s[78:79]
v_cndmask_b32 v54, v54, v55, s[78:79]
v_cndmask_b32 v52, v52, v54, s[80:81]
M 1,32,2
s_add_u32 s42, s44, s60
PK 107,16,105,33
PK 105,16,105,32
LL 18,86
LL 111,87
v_mul_f32 v52, 0x3b2aaaab, v52
M 1,33,3
s_addc_u32 s43, s45, 0
LA 18
RA 220,1,14336
v_med3_f32 v53, v52, v39, v37
M 0,22,4
WL 3
LC_ 111
ST 104
LG 52,53
s_nop 0
C 52,52,52
M 0,23,5
RA 224,1,15360
v_lshlrev_b32 v52, 16, v52
M 1,34,4
DW 2048
LO 17,17,110
LU 110,17
v_add_f32 v52, v36, v52
M 1,35,5
WL 4
RA 228,1,16384
HS 25,110
C 52,52,52
v_lshlrev_b32 v52, 16, v52
Z 6,12,0
WL 3
PK 110,17,108,33
)"
R"(PK 108,17,108,32
LL 16,56
LL 104,57
v_sqrt_f32 v52, v52
M 6,13,1
WL 1
LA 16
DQ 3072
PO 110
C 52,52,52
Z 7,24,0
s_add_u32 s50, s50, %[d]
LC_ 104
RA 232,1,17408
v_lshlrev_b32 v52, 16, v52
v_rcp_f32 v52, v52
M 7,25,1
s_cmp_eq_u32 s50, %[wrap]
RA 236,1,18432
s_cselect_b32 s50, %[rst], s50
v_mul_f32 v52, v38, v52
M 6,14,2
WL 4
LO 18,18,111
LU 111,18
CF 52,52,52
M 6,15,3
s_add_u32 s38, s38, 32768
RA 216,1,19456
HS 25,111
ds_bpermute_b32 v13, v28, v52
M 7,26,2
s_cmp_eq_u32 s38, s77
PK 111,18,109,33
PK 109,18,109,32
LL 17,58
LL 105,59
s_cselect_b32 s38, s76, s38
M 7,27,3
s_add_u32 s37, s37, 32768
LA 17
PO 111
s_cmp_eq_u32 s37, s75
s_cselect_b32 s37, s74, s37
WL 4
M 6,16,4
WL 3
LC_ 105
RA 220,1,20480
XL 66,384
M 6,17,5
s_add_u32 s42, s44, s61
RA 224,1,21504
HX 25
s_addc_u32 s43, s45, 0
M 7,28,4
LO 104,16,104
LU 16,104
W1 25,1024
M 7,29,5
WL 2
RA 228,1,22528
HZ 24,16
ST 108
XL 67,448
M 6,18,0
s_mul_i32 s53, s51, 49152
LL 18,60
LL 108,61
s_bitcmp1_b64 s[90:91], s51
s_cselect_b64 s[44:45], s[84:85], s[82:83]
s_add_u32 s44, s44, s53
s_addc_u32 s45, s45, 0
s_add_u32 s51, s51, %[d]
s_cmp_eq_u32 s51, %[wrap]
s_cselect_b32 s51, %[rst], s51
M 6,19,1
LA 18
RA 232,1,23552
M 7,30,0
LC_ 108
XL 68,512
v_add_u32 v4, s39, v4
XS 128
M 7,31,1
WL 5
RA 236,1,24576
s_nop 1
v_add_u32 v1, s37, v0
XS 129
XS 130
M 6,20,2
WL 4
XL 69,576
LO 105,17,105
LU 17,105
XS 131
M 6,21,3
RA 216,1,0
HS 24,17
AW 72,128
AW 73,129
M 7,32,2
LL 16,62
LL 109,63
AW 74,130
AW 75,131
M 7,33,3
WL 1
LA 16
XL 70,640
XS 132
XS 133
M 6,22,4
s_lshl_b32 s53, s52, 8
LC_ 109
v_and_b32 v22, 0xfffffcff, v1
XS 134
M 6,23,5
s_add_u32 s48, %[alo], s53
RA 220,1,1024
XS 135
AW 76,132
M 7,34,4
WV 22
LO 108,18,108
LU 18,108
s_addc_u32 s49, %[ahi], 0
s_add_u32 s52, s52, %[d]
s_cmp_eq_u32 s52, %[wrap]
s_cselect_b32 s52, %[rst], s52
XL 71,704
AW 77,133
AW 78,134
M 7,35,5
s_barrier
RV 252,22,5120
HS 24,18
AW 79,135
v_min_u32 v35, v35, v13
WL 0
Z 0,12,0
s_sub_u32 s39, 0, s39
LL 17,80
LL 106,81
XS 136
XS 137
M 0,13,1
s_mul_i32 s53, s50, 25600
LA 17
RA 224,1,2048
XS 138
Z 1,24,0
s_add_u32 s40, %[wlo], s53
LC_ 106
RA 228,1,3072
XS 139
AW 80,136
M 1,25,1
s_addc_u32 s41, %[whi], 0
RA 232,1,4096
AW 81,137
AW 82,138
Z 2,36,0
LO 109,16,109
LU 16,109
AW 83,139
XS 140
M 2,37,1
s_mov_b32 m0, s38
ds_read_b64 v[14:15], v5 offset:0
HS 24,16
XS 141
Z 3,48,0
WL 2
LL 18,82
LL 107,83
XS 142
XS 143
M 3,49,1
LA 18
RA 236,1,6144
AW 84,140
AW 85,141
M 0,14,2
LC_ 107
DW 0
AW 86,142
AW 87,143
M 0,15,3
v_subrev_u32 v5, s39, v5
HX 24
XS 144
XS 145
M 1,26,2
LO 17,17,106
LU 106,17
XS 146
M 1,27,3
RA 216,1,7168
HZ 25,106
XS 147
AW 88,144
M 2,38,2
PK 106,17,104,33
PK 104,17,104,32
LL 16,84
LL 110,85
AW 89,145
AW 90,146
M 2,39,3
LA 16
W1 24,0
AW 91,147
XS 148
M 3,50,2
WL 4
LC_ 110
DW 1024
XS 149
M 3,51,3
RA 220,1,8192
v_add_f32 v14, v14, v15
XS 150
XS 151
M 0,16,4
WL 5
LO 18,18,107
LU 107,18
AW 92,148
AW 93,149
M 0,17,5
DW 2048
HS 25,107
v_add_f32_dpp v14, v14, v14 quad_perm:[1,0,3,2] row_mask:0xf bank_mask:0xf
AW 94,150
AW 95,151
M 1,28,4
s_add_u32 s42, s44, s60
PK 107,18,105,33
PK 105,18,105,32
LL 17,86
LL 111,87
XS 152
M 1,29,5
s_addc_u32 s43, s45, 0
LA 17
RA 224,1,9216
v_add_f32_dpp v14, v14, v14 quad_perm:[2,3,0,1] row_mask:0xf bank_mask:0xf
XS 153
XS 154
M 2,40,4
LC_ 111
ST 104
XS 155
AW 96,152
M 2,41,5
WV 26
BW 60
v_add_f32_dpp v14, v14, v14 row_half_mirror row_mask:0xf bank_mask:0xf
AW 97,153
AW 98,154
M 3,52,4
WL 4
LO 16,16,110
LU 110,16
AW 99,155
XS 156
M 3,53,5
s_add_u32 s40, s40, 3072
RA 228,1,10240
HS 25,110
v_cvt_f64_f32 v[14:15], v14
XS 157
M 0,18,0
s_addc_u32 s41, s41, 0
PK 110,16,108,33
PK 108,16,108,32
XS 158
XS 159
M 0,19,1
s_add_u32 m0, s38, 3072
s_mov_b64 exec, s[54:55]
SP
s_mov_b64 exec, -1
PO 110
AW 100,156
AW 101,157
M 1,30,0
RV 40,8,6144
RV 44,8,6208
AW 102,158
AW 103,159
M 1,31,1
RA 232,1,11264
RV 48,8,6272
XS 160
M 2,42,0
WL 6
LO 17,17,111
LU 111,17
XS 161
XS 162
M 2,43,1
RV 252,22,5392
HS 25,111
XS 163
AW 104,160
M 3,54,0
WL 5
PK 111,17,109,33
PK 109,17,109,32
AW 105,161
AW 106,162
M 3,55,1
RA 236,1,12288
PO 111
AW 107,163
M 0,20,2
WV 25
DW 0
BR 60
XS 164
XS 165
M 0,21,3
s_add_u32 s42, s44, s61
BW 61
XS 166
XS 167
M 1,32,2
s_addc_u32 s43, s45, 0
ST 108
BR 61
AW 108,164
AW 109,165
M 1,33,3
RA 216,1,13312
HX 25
AW 110,166
AW 111,167
M 2,44,2
W1 25,1024
XS 168
M 2,45,3
DW 1024
v_add_u32 v4, s39, v4
XS 169
XS 170
M 3,56,2
WL 5
XS 171
AW 112,168
M 3,57,3
WL 4
RA 220,1,14336
AW 113,169
AW 114,170
LZ 40,60
WV 27
BW 62
M 0,22,4
DW 2048
RV 40,8,6336
AW 115,171
M 0,23,5
WL 5
BR 62
XS 172
XS 173
LS 44,61
WV 27
BW 63
M 1,34,4
RV 44,8,6400
BR 63
XS 174
XS 175
M 1,35,5
RA 224,1,15360
AW 116,172
AW 117,173
M 2,46,4
LL 18,56
LL 104,57
AW 118,174
AW 119,175
M 2,47,5
LA 18
DQ 3072
M 3,58,4
WL 8
LC_ 104
M 3,59,5
WV 27
RA 228,1,16384
Z 6,12,0
WL 7
M 6,13,1
s_add_u32 s50, s50, %[d]
LS 48,62
)"
R"(s_cmp_eq_u32 s50, %[wrap]
BW 64
Z 7,24,0
s_cselect_b32 s50, %[rst], s50
RV 48,8,6464
LL 16,58
LL 105,59
M 7,25,1
WL 6
LA 16
RA 232,1,17408
BR 64
LS 40,63
WV 26
BW 65
Z 8,36,0
LC_ 105
RV 40,8,6528
M 8,37,1
WV 16
BR 65
Z 9,48,0
WL 9
LO 104,18,104
LU 18,104
M 9,49,1
s_add_u32 s38, s38, 32768
RA 236,1,18432
HZ 24,18
M 6,14,2
s_cmp_eq_u32 s38, s77
LL 17,60
LL 108,61
M 6,15,3
s_cselect_b32 s38, s76, s38
LA 17
WL 6
LS 44,64
s_add_u32 s37, s37, 32768
BW 66
M 7,26,2
s_cmp_eq_u32 s37, s75
LC_ 108
RV 44,8,6592
M 7,27,3
s_cselect_b32 s37, s74, s37
RA 216,1,19456
BR 66
M 8,38,2
WL 7
LO 105,16,105
LU 16,105
M 8,39,3
s_add_u32 s53, %[xo0], 393216
HS 24,16
LS 48,65
s_add_u32 s46, %[xlo], s53
WV 14
BW 67
M 9,50,2
WL 7
LL 18,62
LL 109,63
M 9,51,3
s_addc_u32 s47, %[xhi], 0
LA 18
RA 220,1,20480
RV 48,8,6656
M 6,16,4
s_bitcmp1_b64 s[90:91], 48
LC_ 109
BR 67
M 6,17,5
s_cselect_b32 s58, 16, 0
M 7,28,4
WL 9
LO 108,17,108
LU 17,108
s_lshr_b32 s58, 0xda43cb52, s58
v_lshrrev_b32 v3, 6, v0
v_and_b32 v3, 12, v3
v_bfe_u32 v3, s58, v3, 4
v_mad_u32_u24 v3, v3, s97, v21
M 7,29,5
WL 6
RA 224,1,21504
HS 24,17
LS 40,66
WV 13
BW 68
M 8,40,4
RV 40,8,6720
LL 16,64
LL 112,65
M 8,41,5
WV 12
LA 16
XL 72,0
BR 68
M 9,52,4
WL 5
LC_ 112
M 9,53,5
s_mul_i32 s53, s51, 49152
RA 228,1,22528
LS 44,67
s_bitcmp1_b64 s[90:91], s51
BW 69
M 6,18,0
RV 44,8,6784
LO 109,18,109
LU 18,109
M 6,19,1
s_cselect_b64 s[44:45], s[84:85], s[82:83]
BR 69
HS 24,18
s_add_u32 s44, s44, s53
s_addc_u32 s45, s45, 0
s_add_u32 s51, s51, %[d]
s_cmp_eq_u32 s51, %[wrap]
s_cselect_b32 s51, %[rst], s51
M 7,30,0
LL 17,66
LL 113,67
M 7,31,1
WL 5
LA 17
RA 232,1,23552
LS 48,68
WV 12
BW 70
M 8,42,0
LC_ 113
XL 73,64
M 8,43,1
s_lshl_b32 s53, s52, 8
RV 48,8,6848
RA 240,7,0
M 9,54,0
WL 9
LO 112,16,112
LU 16,112
M 9,55,1
s_add_u32 s48, %[alo], s53
RA 236,1,24576
BR 70
HS 24,16
M 6,20,2
s_addc_u32 s49, %[ahi], 0
LL 18,68
LL 116,69
s_add_u32 s52, s52, %[d]
s_cmp_eq_u32 s52, %[wrap]
s_cselect_b32 s52, %[rst], s52
M 6,21,3
WL 8
LA 18
XL 74,128
LS 40,69
WV 13
BW 71
M 7,32,2
LC_ 116
XL 75,192
RA 244,7,256
M 7,33,3
BR 71
v_add_u32 v1, s37, v0
M 8,44,2
WL 9
LO 113,17,113
LU 17,113
M 8,45,3
WL 5
RA 216,1,0
HS 24,17
LS 44,70
RA 248,7,512
v_and_b32 v22, 0xfffffcff, v1
M 9,56,2
LL 16,70
LL 117,71
M 9,57,3
WL 2
LA 16
RA 220,1,1024
LS 48,71
RA 252,7,768
M 6,22,4
LC_ 117
RV 252,22,5120
M 6,23,5
XL 76,256
s_nop 1
v_mul_f32 v36, 0xbb2aaaab, v36
v_mul_f32 v37, 0xbb2aaaab, v37
M 7,34,4
LO 116,18,116
LU 18,116
v_mul_f32 v38, 0xbb2aaaab, v38
M 7,35,5
s_barrier
RA 224,1,2048
HS 24,18
v_mul_f32 v39, 0xbb2aaaab, v39
s_nop 1
LK 40,240,60,0
LK 44,242,60,2
s_sub_u32 s39, 0, s39
RA 240,7,1024
LK 48,244,61,0
s_nop 0
CF 128,40,41
LK 248,246,61,2
RA 244,7,1280
CF 129,42,43
M 8,46,4
LL 17,80
LL 106,81
M 8,47,5
LA 17
ds_read_b64 v[14:15], v5 offset:0
M 9,58,4
WL 8
LC_ 106
XL 77,320
M 9,59,5
WL 6
RA 228,1,3072
v_subrev_u32 v5, s39, v5
Z 0,12,0
LO 117,16,117
LU 16,117
CF 130,44,45
M 0,13,1
s_mul_i32 s53, s50, 25600
HS 24,16
CF 131,46,47
LK 40,248,62,0
CF 132,48,49
LK 44,250,62,2
s_add_u32 s40, %[wlo], s53
RA 248,7,1536
CF 133,50,51
GZ 0
s_addc_u32 s41, %[whi], 0
CF 134,248,249
CF 135,250,251
LK 48,252,63,0
CF 136,40,41
LK 248,254,63,2
s_mov_b32 m0, s38
RA 252,7,1792
CF 137,42,43
GS 1
WL 7
CF 138,44,45
CF 139,46,47
LK 40,240,64,0
CF 140,48,49
LK 44,242,64,2
RA 240,7,2048
CF 141,50,51
GS 2
CF 142,248,249
CF 143,250,251
Z 1,24,0
LL 18,82
LL 107,83
M 1,25,1
LA 18
RA 232,1,4096
Z 2,36,0
LC_ 107
HX 24
M 2,37,1
WL 10
W1 24,0
LK 48,244,65,0
CF 144,40,41
LK 248,246,65,2
RA 244,7,2304
CF 145,42,43
GS 3
CF 146,44,45
CF 147,46,47
Z 3,48,0
WL 8
LO 17,17,106
LU 106,17
M 3,49,1
RA 236,1,6144
HZ 25,106
M 0,14,2
LL 16,84
LL 110,85
M 0,15,3
WL 8
LA 16
PK 106,17,104,33
LK 40,248,66,0
CF 148,48,49
LK 44,250,66,2
RA 248,7,2560
CF 149,50,51
GS 4
CF 150,248,249
CF 151,250,251
M 1,26,2
LC_ 110
PK 104,17,104,32
M 1,27,3
RA 216,1,7168
v_add_f32 v14, v14, v15
M 2,38,2
WL 7
LO 18,18,107
LU 107,18
v_add_f32_dpp v14, v14, v14 quad_perm:[1,0,3,2] row_mask:0xf bank_mask:0xf
M 2,39,3
DW 0
HS 25,107
LK 48,252,67,0
CF 152,40,41
LK 248,254,67,2
RA 252,7,2816
CF 153,42,43
GS 5
CF 154,44,45
CF 155,46,47
M 3,50,2
WL 5
LL 17,86
LL 111,87
v_add_f32_dpp v14, v14, v14 quad_perm:[2,3,0,1] row_mask:0xf bank_mask:0xf
M 3,51,3
LA 17
PK 107,18,105,33
M 0,16,4
LC_ 111
PK 105,18,105,32
v_add_f32_dpp v14, v14, v14 row_half_mirror row_mask:0xf bank_mask:0xf
M 0,17,5
RA 220,1,8192
v_cvt_f64_f32 v[14:15], v14
M 1,28,4
WL 5
LO 16,16,110
LU 110,16
M 1,29,5
RA 224,1,9216
DW 1024
HS 25,110
LK 40,240,68,0
CF 156,48,49
LK 44,242,68,2
s_add_u32 s42, s44, s60
CF 157,50,51
GS 6
s_addc_u32 s43, s45, 0
CF 158,248,249
CF 159,250,251
M 2,40,4
LL 18,88
LL 114,89
s_mov_b64 exec, s[54:55]
SP
s_mov_b64 exec, -1
M 2,41,5
LA 18
PK 110,16,108,33
M 3,52,4
WL 2
LC_ 114
PK 108,16,108,32
M 3,53,5
RA 228,1,10240
PO 110
LK 48,244,69,0
CF 160,40,41
LK 248,246,69,2
CF 161,42,43
GS 7
CF 162,44,45
CF 163,46,47
M 0,18,0
LO 17,17,111
LU 111,17
ST 104
DW 2048
M 0,19,1
s_add_u32 s40, s40, 3072
HS 25,111
M 1,30,0
s_addc_u32 s41, s41, 0
LL 16,90
LL 115,91
M 1,31,1
s_add_u32 m0, s38, 3072
LA 16
)"
R"(PK 111,17,109,33
LK 40,248,70,0
CF 164,48,49
LK 44,250,70,2
CF 165,50,51
GS 8
CF 166,248,249
CF 167,250,251
M 2,42,0
s_add_u32 s42, s44, s61
LC_ 115
PK 109,17,109,32
M 2,43,1
s_addc_u32 s43, s45, 0
RA 232,1,11264
PO 111
M 3,54,0
WL 3
LO 18,18,114
LU 114,18
M 3,55,1
RV 252,22,5392
HS 25,114
LK 48,252,71,0
CF 168,40,41
LK 248,254,71,2
CF 169,42,43
GS 9
CF 170,44,45
CF 171,46,47
M 0,20,2
LL 17,92
LL 118,93
M 0,21,3
LA 17
PK 114,18,112,33
M 1,32,2
LC_ 118
PK 112,18,112,32
M 1,33,3
RA 236,1,12288
RV 36,8,6912
GS 10
RA 216,1,13312
M 2,44,2
WL 7
DW 0
LO 16,16,115
LU 115,16
M 2,45,3
ST 108
HS 25,115
M 3,56,2
WL 2
LL 18,94
LL 119,95
M 3,57,3
LA 18
PK 115,16,113,33
M 0,22,4
LC_ 119
PK 113,16,113,32
M 0,23,5
RA 220,1,14336
DW 1024
M 1,34,4
LO 17,17,118
LU 118,17
M 1,35,5
s_add_u32 s42, s44, s62
RA 224,1,15360
HS 25,118
M 2,46,4
s_addc_u32 s43, s45, 0
LL 16,56
LL 104,57
M 2,47,5
LA 16
PK 118,17,116,33
M 3,58,4
WL 2
LC_ 104
PK 116,17,116,32
M 3,59,5
RA 228,1,16384
DW 2048
PO 118
CF 172,48,49
Z 6,12,0
LO 18,18,119
LU 119,18
ST 112
CF 173,50,51
M 6,13,1
DQ 3072
HS 25,119
CF 174,248,249
CF 175,250,251
Z 7,24,0
LL 17,58
LL 105,59
M 7,25,1
LA 17
PK 119,18,117,33
GS 11
RA 232,1,17408
HX 25
s_nop 5
v_cndmask_b32 v52, v52, v53, s[78:79]
v_cndmask_b32 v54, v54, v55, s[78:79]
Z 8,36,0
s_add_u32 s50, s50, %[d]
LC_ 105
PK 117,18,117,32
M 8,37,1
s_cmp_eq_u32 s50, %[wrap]
W1 25,1024
PO 119
v_cndmask_b32 v52, v52, v54, s[80:81]
Z 9,48,0
WL 4
LO 104,16,104
LU 16,104
v_mul_f32 v52, 0x3b2aaaab, v52
M 9,49,1
s_cselect_b32 s50, %[rst], s50
RA 236,1,18432
HZ 24,16
v_med3_f32 v53, v52, v39, v37
M 6,14,2
s_add_u32 s42, s44, s63
LL 18,60
LL 108,61
LG 52,53
M 6,15,3
s_addc_u32 s43, s45, 0
LA 18
ST 116
C 52,52,52
M 7,26,2
s_add_u32 s37, s37, 32768
LC_ 108
v_add_u32 v4, s39, v4
v_lshlrev_b32 v52, 16, v52
M 7,27,3
s_cmp_eq_u32 s37, s75
RA 216,1,19456
v_add_f32 v52, v36, v52
M 8,38,2
WL 5
LO 105,17,105
LU 17,105
C 52,52,52
M 8,39,3
s_cselect_b32 s37, s74, s37
HS 24,17
v_lshlrev_b32 v52, 16, v52
M 9,50,2
WL 3
LL 16,62
LL 109,63
v_sqrt_f32 v52, v52
M 9,51,3
s_add_u32 s38, s38, 32768
LA 16
RA 220,1,20480
C 52,52,52
M 6,16,4
s_cmp_eq_u32 s38, s77
LC_ 109
s_cselect_b32 s38, s76, s38
v_lshlrev_b32 v52, 16, v52
M 6,17,5
XL 78,384
v_rcp_f32 v52, v52
M 7,28,4
WL 4
LO 108,18,108
LU 18,108
v_mul_f32 v52, v38, v52
M 7,29,5
s_mul_i32 s53, s51, 49152
RA 224,1,21504
HS 24,18
CF 52,52,52
M 8,40,4
s_bitcmp1_b64 s[90:91], s51
LL 17,64
LL 112,65
M 8,41,5
s_cselect_b64 s[44:45], s[84:85], s[82:83]
LA 17
ds_bpermute_b32 v13, v28, v52
M 9,52,4
WL 4
LC_ 112
M 9,53,5
s_add_u32 s44, s44, s53
RA 228,1,22528
M 6,18,0
WL 5
LO 109,16,109
LU 16,109
M 6,19,1
s_addc_u32 s45, s45, 0
HS 24,16
s_add_u32 s51, s51, %[d]
s_cmp_eq_u32 s51, %[wrap]
s_cselect_b32 s51, %[rst], s51
M 7,30,0
LL 18,66
LL 113,67
M 7,31,1
WV 18
LA 18
RA 232,1,23552
XL 79,448
M 8,42,0
s_lshl_b32 s53, s52, 8
LC_ 113
M 8,43,1
s_add_u32 s48, %[alo], s53
BW 72
M 9,54,0
WL 4
LO 112,17,112
LU 17,112
M 9,55,1
s_addc_u32 s49, %[ahi], 0
RA 236,1,24576
RV 40,8,6144
HS 24,17
XS 128
M 6,20,2
s_add_u32 s52, s52, %[d]
LL 16,68
LL 116,69
s_cmp_eq_u32 s52, %[wrap]
s_cselect_b32 s52, %[rst], s52
RV 44,8,6208
XS 129
M 6,21,3
LA 16
XL 80,512
XS 130
M 7,32,2
LC_ 116
XL 81,576
RV 48,8,6272
XS 131
M 7,33,3
BR 72
v_add_u32 v1, s37, v0
AW 120,128
M 8,44,2
WL 8
LO 113,18,113
LU 18,113
AW 121,129
M 8,45,3
WV 20
RA 216,1,0
HS 24,18
AW 122,130
BW 73
M 9,56,2
WL 8
BR 73
LL 17,70
LL 117,71
AW 123,131
M 9,57,3
WL 3
LA 17
XL 82,640
XS 132
XS 133
M 6,22,4
s_barrier
LC_ 117
v_and_b32 v22, 0xfffffcff, v1
XS 134
M 6,23,5
s_sub_u32 s39, 0, s39
RA 220,1,1024
XS 135
LZ 40,72
WV 20
BW 74
M 7,34,4
LO 116,16,116
LU 16,116
AW 124,132
M 7,35,5
RV 252,22,5120
RV 40,8,6336
HS 24,16
AW 125,133
M 8,46,4
BR 74
LL 18,80
LL 106,81
AW 126,134
M 8,47,5
WL 7
LA 18
RA 224,1,2048
AW 127,135
LS 44,73
WV 19
BW 75
M 9,58,4
WL 7
LC_ 106
ds_read_b64 v[14:15], v5 offset:0
v_min_u32 v35, v35, v13
M 9,59,5
WL 7
RA 228,1,3072
RV 44,8,6400
XS 136
Z 0,12,0
WV 18
LO 117,17,117
LU 17,117
XS 137
M 0,13,1
WL 7
XL 83,704
HS 24,17
XS 138
Z 1,24,0
s_mul_i32 s53, s50, 25600
BR 75
LL 16,82
LL 107,83
XS 139
M 1,25,1
s_add_u32 s40, %[wlo], s53
LA 16
RA 232,1,4096
AW 128,136
LS 48,74
s_addc_u32 s41, %[whi], 0
BW 76
Z 2,36,0
s_mov_b32 m0, s38
LC_ 107
RV 48,8,6464
AW 129,137
M 2,37,1
BR 76
v_subrev_u32 v5, s39, v5
AW 130,138
Z 3,48,0
WL 8
LO 18,18,106
LU 106,18
AW 131,139
M 3,49,1
RA 236,1,6144
HZ 25,106
XS 140
M 0,14,2
LL 17,84
LL 110,85
XS 141
M 0,15,3
WL 7
LA 17
PK 106,18,104,33
HX 24
XS 142
LS 40,75
WV 18
BW 77
M 1,26,2
LC_ 110
PK 104,18,104,32
W1 24,0
XS 143
M 1,27,3
RA 216,1,7168
RV 40,8,6528
AW 132,140
M 2,38,2
WL 9
DW 0
LO 16,16,107
LU 107,16
AW 133,141
M 2,39,3
DW 1024
HS 25,107
AW 134,142
M 3,50,2
WL 6
BR 77
LL 18,86
LL 111,87
)"
R"(AW 135,143
M 3,51,3
LA 18
PK 107,16,105,33
v_add_f32 v14, v14, v15
s_nop 1
v_add_f32_dpp v14, v14, v14 quad_perm:[1,0,3,2] row_mask:0xf bank_mask:0xf
s_nop 1
v_add_f32_dpp v14, v14, v14 quad_perm:[2,3,0,1] row_mask:0xf bank_mask:0xf
XS 144
LS 44,76
WV 7
BW 78
M 0,16,4
LC_ 111
PK 105,16,105,32
v_add_f32_dpp v14, v14, v14 row_half_mirror row_mask:0xf bank_mask:0xf
XS 145
M 0,17,5
RA 220,1,8192
RV 44,8,6592
XS 146
M 1,28,4
WL 9
LO 17,17,110
LU 110,17
v_cvt_f64_f32 v[14:15], v14
XS 147
M 1,29,5
RA 224,1,9216
HS 25,110
AW 136,144
M 2,40,4
WL 6
LL 16,88
LL 114,89
s_mov_b64 exec, s[54:55]
SP
s_mov_b64 exec, -1
DW 2048
BR 78
AW 137,145
M 2,41,5
s_add_u32 s42, s44, s60
LA 16
PK 110,17,108,33
AW 138,146
LS 48,77
s_addc_u32 s43, s45, 0
WV 8
BW 79
M 3,52,4
WL 5
LC_ 114
PK 108,17,108,32
AW 139,147
M 3,53,5
RA 228,1,10240
PO 110
XS 148
M 0,18,0
LO 18,18,111
LU 111,18
ST 104
RV 48,8,6656
XS 149
M 0,19,1
s_add_u32 s40, s40, 3072
BR 79
HS 25,111
s_addc_u32 s41, s41, 0
s_add_u32 m0, s38, 3072
XS 150
M 1,30,0
LL 17,90
LL 115,91
XS 151
M 1,31,1
WL 6
LA 17
PK 111,18,109,33
AW 140,148
LS 40,78
WV 8
BW 80
M 2,42,0
LC_ 115
PK 109,18,109,32
AW 141,149
M 2,43,1
RA 232,1,11264
DW 0
PO 111
AW 142,150
M 3,54,0
WL 6
RV 40,8,6720
LO 16,16,114
LU 114,16
AW 143,151
M 3,55,1
RV 252,22,5392
HS 25,114
XS 152
M 0,20,2
WV 8
BR 80
LL 18,92
LL 118,93
XS 153
M 0,21,3
WL 7
LA 18
DW 1024
PK 114,16,112,33
XS 154
LS 44,79
s_add_u32 s42, s44, s61
BW 81
M 1,32,2
s_addc_u32 s43, s45, 0
LC_ 118
PK 112,16,112,32
XS 155
M 1,33,3
RA 236,1,12288
RV 44,8,6784
AW 144,152
M 2,44,2
WL 9
LO 17,17,115
LU 115,17
AW 145,153
M 2,45,3
RA 216,1,13312
HS 25,115
ST 108
BR 81
AW 146,154
M 3,56,2
WL 3
DW 2048
LL 16,94
LL 119,95
AW 147,155
M 3,57,3
LA 16
PK 115,17,113,33
XS 156
LS 48,80
WV 10
BW 82
M 0,22,4
LC_ 119
PK 113,17,113,32
XS 157
M 0,23,5
WV 9
RA 220,1,14336
RV 48,8,6848
XS 158
M 1,34,4
WL 5
DQ 3072
LO 18,18,118
LU 118,18
XS 159
M 1,35,5
s_add_u32 s42, s44, s62
RA 224,1,15360
RA 240,7,0
HS 25,118
AW 148,156
M 2,46,4
s_addc_u32 s43, s45, 0
LL 17,56
LL 104,57
AW 149,157
M 2,47,5
s_add_u32 s50, s50, %[d]
LA 17
BR 82
PK 118,18,116,33
AW 150,158
LS 40,81
s_cmp_eq_u32 s50, %[wrap]
BW 83
M 3,58,4
WL 6
LC_ 104
PK 116,18,116,32
s_cselect_b32 s50, %[rst], s50
AW 151,159
M 3,59,5
RA 228,1,16384
PO 118
XS 160
Z 6,12,0
WL 3
LO 16,16,119
LU 119,16
ST 112
XS 161
M 6,13,1
s_add_u32 s38, s38, 32768
RA 244,7,256
HS 25,119
XS 162
Z 7,24,0
s_cmp_eq_u32 s38, s77
LL 18,58
LL 105,59
s_cselect_b32 s38, s76, s38
XS 163
M 7,25,1
s_mul_i32 s53, s50, 25600
LA 18
BR 83
PK 119,16,117,33
AW 152,160
LS 44,82
s_add_u32 s40, %[wlo], s53
RA 248,7,512
AW 153,161
Z 8,36,0
s_addc_u32 s41, %[whi], 0
LC_ 105
PK 117,16,117,32
M 8,37,1
s_mov_b32 m0, s38
RA 232,1,17408
PO 119
AW 154,162
Z 9,48,0
WL 3
LO 104,17,104
LU 17,104
AW 155,163
M 9,49,1
s_add_u32 s42, s44, s63
RA 236,1,18432
HZ 24,17
XS 164
LS 48,83
s_addc_u32 s43, s45, 0
RA 252,7,768
XS 165
M 6,14,2
LL 16,60
LL 108,61
M 6,15,3
LA 16
DW 0
HX 25
XS 166
M 7,26,2
LC_ 108
W1 25,1024
XS 167
M 7,27,3
RA 216,1,19456
v_add_u32 v4, s39, v4
AW 156,164
M 8,38,2
WL 7
LO 105,18,105
LU 18,105
AW 157,165
M 8,39,3
ST 116
HS 24,18
AW 158,166
M 9,50,2
WL 5
LL 17,62
LL 109,63
AW 159,167
M 9,51,3
LA 17
RA 220,1,20480
DW 1024
XS 168
XS 169
M 6,16,4
s_add_u32 s37, s37, 32768
LC_ 109
s_cmp_eq_u32 s37, s75
s_cselect_b32 s37, s74, s37
XS 170
M 6,17,5
DW 2048
XS 171
M 7,28,4
WL 5
LO 108,16,108
LU 16,108
AW 160,168
M 7,29,5
s_add_u32 s40, s40, 3072
RA 224,1,21504
HS 24,16
AW 161,169
M 8,40,4
s_addc_u32 s41, s41, 0
LL 18,64
LL 112,65
AW 162,170
M 8,41,5
s_add_u32 m0, s38, 3072
LA 18
AW 163,171
M 9,52,4
WL 3
LC_ 112
XS 172
M 9,53,5
s_mul_i32 s53, s51, 49152
RA 228,1,22528
XS 173
M 6,18,0
WL 4
LO 109,17,109
LU 17,109
XS 174
M 6,19,1
s_bitcmp1_b64 s[90:91], s51
HS 24,17
s_cselect_b64 s[44:45], s[84:85], s[82:83]
s_add_u32 s44, s44, s53
s_addc_u32 s45, s45, 0
s_add_u32 s51, s51, %[d]
s_cmp_eq_u32 s51, %[wrap]
s_cselect_b32 s51, %[rst], s51
XS 175
M 7,30,0
LL 16,66
LL 113,67
AW 164,172
M 7,31,1
LA 16
RA 232,1,23552
AW 165,173
M 8,42,0
LC_ 113
DW 0
AW 166,174
M 8,43,1
s_lshl_b32 s53, s52, 8
AW 167,175
v_mul_f32 v36, 0xbb2aaaab, v36
v_mul_f32 v37, 0xbb2aaaab, v37
v_mul_f32 v38, 0xbb2aaaab, v38
v_mul_f32 v39, 0xbb2aaaab, v39
s_nop 1
LK 40,240,72,0
LK 44,242,72,2
s_add_u32 s48, %[alo], s53
RA 240,7,1024
LK 48,244,73,0
s_nop 0
CF 128,40,41
LK 248,246,73,2
s_addc_u32 s49, %[ahi], 0
RA 244,7,1280
CF 129,42,43
CF 130,44,45
CF 131,46,47
LK 40,248,74,0
CF 132,48,49
LK 44,250,74,2
s_add_u32 s52, s52, %[d]
RA 248,7,1536
CF 133,50,51
GZ 0
s_cmp_eq_u32 s52, %[wrap]
CF 134,248,249
CF 135,250,251
LK 48,252,75,0
CF 136,40,41
LK 248,254,75,2
s_cselect_b32 s52, %[rst], s52
RA 252,7,1792
CF 137,42,43
GS 1
CF 138,44,45
CF 139,46,47
M 9,54,0
WL 7
LO 112,18,112
LU 18,112
M 9,55,1
WL 3
RA 236,1,24576
HS 24,18
LK 40,240,76,0
CF 140,48,49
LK 44,242,76,2
RA 240,7,2048
CF 141,50,51
GS 2
)"
R"(WL 4
CF 142,248,249
CF 143,250,251
LK 48,244,77,0
CF 144,40,41
LK 248,246,77,2
RA 244,7,2304
CF 145,42,43
GS 3
WL 4
CF 146,44,45
CF 147,46,47
LK 40,248,78,0
CF 148,48,49
LK 44,250,78,2
RA 248,7,2560
CF 149,50,51
GS 4
CF 150,248,249
CF 151,250,251
M 6,20,2
DW 1024
LL 17,68
LL 116,69
M 6,21,3
LA 17
DW 2048
M 7,32,2
LC_ 116
v_add_u32 v1, s37, v0
M 7,33,3
RA 216,1,0
v_and_b32 v22, 0xfffffcff, v1
M 8,44,2
LO 113,16,113
LU 16,113
M 8,45,3
WL 7
DQ 3072
HS 24,16
LK 48,252,79,0
CF 152,40,41
LK 248,254,79,2
RA 252,7,2816
CF 153,42,43
GS 5
CF 154,44,45
CF 155,46,47
M 9,56,2
WL 7
LL 18,70
LL 117,71
M 9,57,3
s_add_u32 s50, s50, %[d]
LA 18
RA 220,1,1024
M 6,22,4
s_cmp_eq_u32 s50, %[wrap]
LC_ 117
RV 252,22,5120
M 6,23,5
s_cselect_b32 s50, %[rst], s50
M 7,34,4
WL 6
LO 116,17,116
LU 17,116
M 7,35,5
s_barrier
RA 224,1,2048
HS 24,17
LK 40,240,80,0
CF 156,48,49
LK 44,242,80,2
s_sub_u32 s39, 0, s39
CF 157,50,51
GS 6
s_add_u32 s38, s38, 32768
CF 158,248,249
CF 159,250,251
M 8,46,4
s_cmp_eq_u32 s38, s77
LL 16,80
LL 106,81
M 8,47,5
s_cselect_b32 s38, s76, s38
LA 16
ds_read_b64 v[14:15], v5 offset:0
M 9,58,4
WL 5
LC_ 106
v_subrev_u32 v5, s39, v5
M 9,59,5
WL 4
RA 228,1,3072
Z 0,12,0
LO 117,18,117
LU 18,117
M 0,13,1
s_mul_i32 s53, s50, 25600
HS 24,18
LK 48,244,81,0
CF 160,40,41
LK 248,246,81,2
s_add_u32 s40, %[wlo], s53
CF 161,42,43
GS 7
s_addc_u32 s41, %[whi], 0
CF 162,44,45
CF 163,46,47
Z 1,24,0
s_mov_b32 m0, s38
LL 17,82
LL 107,83
M 1,25,1
LA 17
RA 232,1,4096
Z 2,36,0
LC_ 107
HX 24
M 2,37,1
W1 24,0
LK 40,248,82,0
CF 164,48,49
LK 44,250,82,2
CF 165,50,51
GS 8
CF 166,248,249
CF 167,250,251
Z 3,48,0
WL 4
LO 16,16,106
LU 106,16
M 3,49,1
RA 236,1,6144
HZ 25,106
M 0,14,2
LL 18,84
LL 110,85
M 0,15,3
LA 18
PK 106,16,104,33
M 1,26,2
LC_ 110
PK 104,16,104,32
M 1,27,3
RA 216,1,7168
v_add_f32 v14, v14, v15
LK 48,252,83,0
CF 168,40,41
LK 248,254,83,2
CF 169,42,43
v_add_f32_dpp v14, v14, v14 quad_perm:[1,0,3,2] row_mask:0xf bank_mask:0xf
GS 9
CF 170,44,45
CF 171,46,47
M 2,38,2
WL 5
LO 17,17,107
LU 107,17
M 2,39,3
DW 0
HS 25,107
M 3,50,2
WL 3
LL 16,86
LL 111,87
v_add_f32_dpp v14, v14, v14 quad_perm:[2,3,0,1] row_mask:0xf bank_mask:0xf
M 3,51,3
s_add_u32 s42, s44, s60
LA 16
PK 107,17,105,33
M 0,16,4
s_addc_u32 s43, s45, 0
LC_ 111
PK 105,17,105,32
v_add_f32_dpp v14, v14, v14 row_half_mirror row_mask:0xf bank_mask:0xf
M 0,17,5
RA 220,1,8192
RV 36,8,6912
GS 10
CF 172,48,49
CF 173,50,51
M 1,28,4
WL 5
LO 18,18,110
LU 110,18
v_cvt_f64_f32 v[14:15], v14
M 1,29,5
RA 224,1,9216
HS 25,110
M 2,40,4
LL 17,88
LL 114,89
s_mov_b64 exec, s[54:55]
SP
s_mov_b64 exec, -1
M 2,41,5
LA 17
PK 110,18,108,33
M 3,52,4
WL 3
LC_ 114
PK 108,18,108,32
M 3,53,5
RA 228,1,10240
PO 110
M 0,18,0
LO 16,16,111
LU 111,16
ST 104
M 0,19,1
DW 1024
HS 25,111
M 1,30,0
LL 18,90
LL 115,91
M 1,31,1
LA 18
PK 111,16,109,33
M 2,42,0
s_add_u32 s42, s44, s61
LC_ 115
PK 109,16,109,32
M 2,43,1
s_addc_u32 s43, s45, 0
RA 232,1,11264
PO 111
CF 174,248,249
M 3,54,0
WL 3
LO 17,17,114
LU 114,17
CF 175,250,251
M 3,55,1
RV 252,22,5392
HS 25,114
GS 11
RA 236,1,12288
s_nop 6
v_cndmask_b32 v52, v52, v53, s[78:79]
M 0,20,2
LL 16,92
LL 118,93
M 0,21,3
LA 16
PK 114,17,112,33
v_cndmask_b32 v54, v54, v55, s[78:79]
M 1,32,2
LC_ 118
PK 112,17,112,32
v_cndmask_b32 v52, v52, v54, s[80:81]
M 1,33,3
RA 216,1,13312
DW 2048
M 2,44,2
WL 6
LO 18,18,115
LU 115,18
v_mul_f32 v52, 0x3b2aaaab, v52
M 2,45,3
ST 108
HS 25,115
v_med3_f32 v53, v52, v39, v37
M 3,56,2
WL 3
LL 17,94
LL 119,95
M 3,57,3
s_add_u32 s40, s40, 3072
LA 17
PK 115,18,113,33
LG 52,53
M 0,22,4
s_addc_u32 s41, s41, 0
LC_ 119
PK 113,18,113,32
C 52,52,52
M 0,23,5
s_add_u32 m0, s38, 3072
RA 220,1,14336
v_lshlrev_b32 v52, 16, v52
M 1,34,4
WL 4
LO 16,16,118
LU 118,16
M 1,35,5
s_add_u32 s42, s44, s62
RA 224,1,15360
HS 25,118
v_add_f32 v52, v36, v52
M 2,46,4
s_addc_u32 s43, s45, 0
LL 18,56
LL 104,57
M 2,47,5
LA 18
PK 118,16,116,33
C 52,52,52
M 3,58,4
WL 2
LC_ 104
PK 116,16,116,32
v_lshlrev_b32 v52, 16, v52
M 3,59,5
RA 228,1,16384
PO 118
Z 6,12,0
LO 17,17,119
LU 119,17
ST 112
v_sqrt_f32 v52, v52
M 6,13,1
DW 0
HS 25,119
C 52,52,52
Z 7,24,0
LL 16,58
LL 105,59
M 7,25,1
LA 16
PK 119,17,117,33
v_lshlrev_b32 v52, 16, v52
Z 8,36,0
s_add_u32 s42, s44, s63
LC_ 105
PK 117,17,117,32
v_rcp_f32 v52, v52
M 8,37,1
s_addc_u32 s43, s45, 0
RA 232,1,17408
PO 119
Z 9,48,0
WL 3
LO 104,18,104
LU 18,104
v_mul_f32 v52, v38, v52
M 9,49,1
RA 236,1,18432
HZ 24,18
CF 52,52,52
M 6,14,2
LL 17,60
LL 108,61
M 6,15,3
LA 17
ST 116
HX 25
ds_bpermute_b32 v13, v28, v52
M 7,26,2
LC_ 108
W1 25,1024
M 7,27,3
RA 216,1,19456
v_add_u32 v4, s39, v4
M 8,38,2
WL 7
LO 105,16,105
LU 16,105
M 8,39,3
DW 1024
HS 24,16
M 9,50,2
WL 5
LL 18,62
LL 109,63
M 9,51,3
s_add_u32 s37, s37, 32768
LA 18
RA 220,1,20480
M 6,16,4
s_cmp_eq_u32 s37, s75
LC_ 109
s_cselect_b32 s37, s74, s37
M 6,17,5
DW 2048
M 7,28,4
WL 5
LO 108,17,108
LU 17,108
M 7,29,5
s_mul_i32 s53, s51, 49152
RA 224,1,21504
HS 24,17
XS 128
M 8,40,4
s_bitcmp1_b64 s[90:91], s51
LL 16,64
LL 112,65
XS 129
M 8,41,5
s_cselect_b64 s[44:45], s[84:85], s[82:83]
LA 16
XS 130
M 9,52,4
WL 3
LC_ 112
XS 131
M 9,53,5
s_add_u32 s44, s44, s53
RA 228,1,22528
)"
R"(AW 168,128
M 6,18,0
WL 4
LO 109,18,109
LU 18,109
AW 169,129
M 6,19,1
s_addc_u32 s45, s45, 0
HS 24,18
s_add_u32 s51, s51, %[d]
s_cmp_eq_u32 s51, %[wrap]
s_cselect_b32 s51, %[rst], s51
AW 170,130
M 7,30,0
s_lshl_b32 s53, s52, 8
LL 17,66
LL 113,67
AW 171,131
M 7,31,1
s_add_u32 s48, %[alo], s53
LA 17
RA 232,1,23552
XS 132
M 8,42,0
s_addc_u32 s49, %[ahi], 0
LC_ 113
XS 133
M 8,43,1
s_add_u32 s52, s52, %[d]
XS 134
XS 135
M 9,54,0
WL 3
LO 112,16,112
LU 16,112
M 9,55,1
s_cmp_eq_u32 s52, %[wrap]
RA 236,1,24576
HS 24,16
AW 172,132
M 6,20,2
s_cselect_b32 s52, %[rst], s52
DQ 3072
LL 18,68
LL 116,69
AW 173,133
M 6,21,3
LA 18
v_add_u32 v1, s37, v0
AW 174,134
M 7,32,2
s_add_u32 s50, s50, %[d]
LC_ 116
AW 175,135
M 7,33,3
s_cmp_eq_u32 s50, %[wrap]
RA 216,1,0
v_and_b32 v22, 0xfffffcff, v1
v_min_u32 v35, v35, v13
M 8,44,2
WL 4
LO 113,17,113
LU 17,113
XS 136
M 8,45,3
s_cselect_b32 s50, %[rst], s50
HS 24,17
XS 137
M 9,56,2
WL 3
LL 16,70
LL 117,71
XS 138
M 9,57,3
WV 21
LA 16
RA 220,1,1024
XS 139
M 6,22,4
s_barrier
LC_ 117
RV 252,22,5120
AW 176,136
M 6,23,5
s_sub_u32 s39, 0, s39
AW 177,137
AW 178,138
M 7,34,4
WL 5
LO 116,18,116
LU 18,116
M 7,35,5
s_add_u32 s38, s38, 32768
RA 224,1,2048
HS 24,18
AW 179,139
M 8,46,4
s_cmp_eq_u32 s38, s77
LL 17,80
LL 106,81
XS 140
M 8,47,5
s_cselect_b32 s38, s76, s38
LA 17
XS 141
M 9,58,4
WL 4
LC_ 106
XS 142
M 9,59,5
WL 3
RA 228,1,3072
XS 143
Z 0,12,0
LO 117,16,117
LU 16,117
M 0,13,1
HS 24,16
AW 180,140
Z 1,24,0
LL 18,82
LL 107,83
AW 181,141
M 1,25,1
LA 18
RA 232,1,4096
AW 182,142
Z 2,36,0
LC_ 107
HX 24
AW 183,143
M 2,37,1
W1 24,0
XS 144
Z 3,48,0
WL 5
LO 17,17,106
LU 106,17
XS 145
M 3,49,1
HZ 25,106
XS 146
Z 4,60,0
LL 16,84
LL 110,85
XS 147
M 4,61,1
LA 16
PK 106,17,104,33
AW 184,144
Z 5,72,0
WL 5
LC_ 110
PK 104,17,104,32
AW 185,145
M 5,73,1
RA 236,1,6144
AW 186,146
M 0,14,2
WL 4
LO 18,18,107
LU 107,18
AW 187,147
M 0,15,3
HS 25,107
XS 148
M 1,26,2
LL 17,86
LL 111,87
XS 149
M 1,27,3
s_add_u32 s42, s44, s60
LA 17
PK 107,18,105,33
XS 150
M 2,38,2
s_addc_u32 s43, s45, 0
LC_ 111
PK 105,18,105,32
XS 151
M 2,39,3
RA 216,1,7168
AW 188,148
M 3,50,2
WL 4
LO 16,16,110
LU 110,16
AW 189,149
M 3,51,3
ST 104
HS 25,110
AW 190,150
M 4,62,2
LL 18,88
LL 114,89
AW 191,151
M 4,63,3
LA 18
PK 110,16,108,33
XS 152
M 5,74,2
WL 4
LC_ 114
PK 108,16,108,32
XS 153
M 5,75,3
s_mov_b32 m0, %[pm]
RA 220,1,8192
PO 110
XS 154
M 0,16,4
WL 4
LO 17,17,111
LU 111,17
XS 155
M 0,17,5
DF
HS 25,111
AW 192,152
M 1,28,4
LL 16,90
LL 115,91
AW 193,153
M 1,29,5
LA 16
PK 111,17,109,33
AW 194,154
M 2,40,4
s_add_u32 s42, s44, s61
LC_ 115
PK 109,17,109,32
AW 195,155
M 2,41,5
s_addc_u32 s43, s45, 0
RA 224,1,9216
PO 111
XS 156
M 3,52,4
WL 4
LO 18,18,114
LU 114,18
XS 157
M 3,53,5
ST 108
HS 25,114
XS 158
M 4,64,4
LL 17,92
LL 118,93
XS 159
M 4,65,5
LA 17
PK 114,18,112,33
AW 196,156
M 5,76,4
WL 4
LC_ 118
PK 112,18,112,32
AW 197,157
M 5,77,5
RA 228,1,10240
AW 198,158
M 0,18,0
WL 4
LO 16,16,115
LU 115,16
AW 199,159
M 0,19,1
HS 25,115
XS 160
M 1,30,0
LL 18,94
LL 119,95
XS 161
M 1,31,1
s_add_u32 s42, s44, s62
LA 18
PK 115,16,113,33
XS 162
M 2,42,0
s_addc_u32 s43, s45, 0
LC_ 119
PK 113,16,113,32
XS 163
M 2,43,1
RA 232,1,11264
AW 200,160
M 3,54,0
WL 4
LO 17,17,118
LU 118,17
AW 201,161
M 3,55,1
RV 252,22,5392
HS 25,118
AW 202,162
M 4,66,0
ST 112
AW 203,163
M 4,67,1
RA 236,1,12288
PK 118,17,116,33
XS 164
M 5,78,0
WL 5
PK 116,17,116,32
XS 165
M 5,79,1
PO 118
XS 166
M 0,20,2
WL 3
LO 18,18,119
LU 119,18
XS 167
M 0,21,3
HS 25,119
AW 204,164
M 1,32,2
HX 25
AW 205,165
M 1,33,3
RA 216,1,13312
PK 119,18,117,33
AW 206,166
M 2,44,2
s_add_u32 s42, s44, s63
W1 25,1024
PK 117,18,117,32
AW 207,167
M 2,45,3
s_addc_u32 s43, s45, 0
PO 119
v_add_u32 v4, s39, v4
XS 168
M 3,56,2
ST 116
XS 169
M 3,57,3
XS 170
XS 171
M 4,68,2
AW 208,168
AW 209,169
M 4,69,3
RA 220,1,14336
AW 210,170
M 5,80,2
WL 3
AW 211,171
XS 172
M 5,81,3
XS 173
XS 174
M 0,22,4
XS 175
AW 212,172
M 0,23,5
AW 213,173
AW 214,174
M 1,34,4
AW 215,175
M 1,35,5
RA 224,1,15360
M 2,46,4
s_nop 0
LL 16,56
LL 104,57
M 2,47,5
LA 16
ds_read_b64 v[14:15], v5 offset:0
M 3,58,4
LC_ 104
v_subrev_u32 v5, s39, v5
M 3,59,5
M 4,70,4
M 4,71,5
RA 228,1,16384
M 5,82,4
WL 5
LL 17,58
LL 105,59
M 5,83,5
LA 17
Z 6,12,0
LC_ 105
M 6,13,1
Z 7,24,0
WL 3
LO 104,16,104
LU 16,104
M 7,25,1
RA 232,1,17408
HZ 24,16
Z 8,36,0
LL 18,60
LL 108,61
M 8,37,1
s_mul_i32 s53, s50, 25600
LA 18
)"
R"(v_add_f32 v14, v14, v15
Z 9,48,0
s_add_u32 s40, %[wlo], s53
LC_ 108
v_add_f32_dpp v14, v14, v14 quad_perm:[1,0,3,2] row_mask:0xf bank_mask:0xf
M 9,49,1
s_addc_u32 s41, %[whi], 0
v_add_f32_dpp v14, v14, v14 quad_perm:[2,3,0,1] row_mask:0xf bank_mask:0xf
Z 10,60,0
WL 3
LO 105,17,105
LU 17,105
M 10,61,1
s_mov_b32 m0, s38
RA 236,1,18432
HS 24,17
Z 11,72,0
LL 16,62
LL 109,63
M 11,73,1
LA 16
DW 0
M 6,14,2
LC_ 109
v_add_f32_dpp v14, v14, v14 row_half_mirror row_mask:0xf bank_mask:0xf
M 6,15,3
v_cvt_f64_f32 v[14:15], v14
M 7,26,2
WL 3
LO 108,18,108
LU 18,108
M 7,27,3
s_add_u32 s37, s37, 32768
RA 216,1,19456
HS 24,18
M 8,38,2
s_cmp_eq_u32 s37, s75
LL 17,64
LL 112,65
M 8,39,3
s_cselect_b32 s37, s74, s37
LA 17
s_mov_b64 exec, s[54:55]
SP
s_mov_b64 exec, -1
M 9,50,2
LC_ 112
DW 1024
M 9,51,3
M 10,62,2
WL 3
LO 109,16,109
LU 16,109
M 10,63,3
RA 220,1,20480
HS 24,16
M 11,74,2
LL 18,66
LL 113,67
M 11,75,3
LA 18
DW 2048
M 6,16,4
LC_ 113
M 6,17,5
M 7,28,4
WL 3
LO 112,17,112
LU 17,112
M 7,29,5
s_mul_i32 s53, s51, 49152
RA 224,1,21504
HS 24,17
M 8,40,4
s_bitcmp1_b64 s[90:91], s51
LL 16,68
LL 116,69
M 8,41,5
s_cselect_b64 s[44:45], s[84:85], s[82:83]
LA 16
M 9,52,4
s_add_u32 s44, s44, s53
LC_ 116
M 9,53,5
s_addc_u32 s45, s45, 0
M 10,64,4
WL 3
LO 113,18,113
LU 18,113
M 10,65,5
s_add_u32 s51, s51, %[d]
RA 228,1,22528
HS 24,18
M 11,76,4
LL 17,70
LL 117,71
M 11,77,5
s_cmp_eq_u32 s51, %[wrap]
LA 17
M 6,18,0
s_cselect_b32 s51, %[rst], s51
LC_ 117
M 6,19,1
s_add_u32 s40, s40, 3072
M 7,30,0
WL 3
LO 116,16,116
LU 16,116
M 7,31,1
s_addc_u32 s41, s41, 0
RA 232,1,23552
HS 24,16
M 8,42,0
s_add_u32 m0, s38, 3072
LL 18,72
LL 120,73
M 8,43,1
LA 18
DW 0
M 9,54,0
LC_ 120
DW 1024
M 9,55,1
M 10,66,0
WL 3
LO 117,17,117
LU 17,117
M 10,67,1
RA 236,1,24576
HS 24,17
M 11,78,0
LL 16,74
LL 121,75
M 11,79,1
LA 16
DW 2048
M 6,20,2
LC_ 121
M 6,21,3
s_lshl_b32 s53, s52, 8
v_add_u32 v1, s37, v0
M 7,32,2
WL 3
LO 120,18,120
LU 18,120
M 7,33,3
s_add_u32 s48, %[alo], s53
RA 216,1,0
HS 24,18
M 8,44,2
s_addc_u32 s49, %[ahi], 0
LL 17,76
LL 124,77
M 8,45,3
s_add_u32 s52, s52, %[d]
LA 17
v_and_b32 v22, 0xfffffcff, v1
M 9,56,2
s_cmp_eq_u32 s52, %[wrap]
LC_ 124
M 9,57,3
s_cselect_b32 s52, %[rst], s52
DQ 3072
M 10,68,2
WL 3
LO 121,16,121
LU 16,121
M 10,69,3
s_add_u32 s50, s50, %[d]
RA 220,1,1024
HS 24,16
M 11,80,2
LL 18,78
LL 125,79
M 11,81,3
s_cmp_eq_u32 s50, %[wrap]
LA 18
RV 252,22,5120
M 6,22,4
s_cselect_b32 s50, %[rst], s50
LC_ 125
M 6,23,5
WV 25
M 7,34,4
WL 4
LO 124,17,124
LU 17,124
M 7,35,5
s_add_u32 s38, s38, 32768
RA 224,1,2048
HS 24,17
M 8,46,4
s_cmp_eq_u32 s38, s77
LL 16,80
LL 106,81
M 8,47,5
s_cselect_b32 s38, s76, s38
LA 16
M 9,58,4
s_barrier
LC_ 106
M 9,59,5
s_sub_u32 s39, 0, s39
M 10,70,4
WL 3
LO 125,18,125
LU 18,125
M 10,71,5
RA 228,1,3072
HS 24,18
M 11,82,4
LL 17,82
LL 107,83
M 11,83,5
LA 17
HX 24
Z 0,12,0
LC_ 107
W1 24,0
M 0,13,1
Z 1,24,0
WL 4
LO 16,16,106
LU 106,16
M 1,25,1
RA 232,1,4096
HZ 25,106
Z 2,36,0
LL 18,84
LL 110,85
M 2,37,1
LA 18
PK 106,16,104,33
Z 3,48,0
LC_ 110
PK 104,16,104,32
M 3,49,1
Z 4,60,0
WL 4
LO 17,17,107
LU 107,17
M 4,61,1
RA 236,1,6144
HS 25,107
Z 5,72,0
LL 16,86
LL 111,87
M 5,73,1
s_add_u32 s42, s44, s60
LA 16
PK 107,17,105,33
M 0,14,2
s_addc_u32 s43, s45, 0
LC_ 111
PK 105,17,105,32
M 0,15,3
ST 104
M 1,26,2
WL 3
LO 18,18,110
LU 110,18
M 1,27,3
RA 216,1,7168
HS 25,110
M 2,38,2
LL 17,88
LL 114,89
M 2,39,3
LA 17
PK 110,18,108,33
M 3,50,2
LC_ 114
PK 108,18,108,32
M 3,51,3
PO 110
M 4,62,2
WL 3
LO 16,16,111
LU 111,16
M 4,63,3
RA 220,1,8192
HS 25,111
M 5,74,2
LL 18,90
LL 115,91
M 5,75,3
s_mov_b32 m0, %[pm]
LA 18
PK 111,16,109,33
M 0,16,4
LC_ 115
PK 109,16,109,32
M 0,17,5
DF
PO 111
M 1,28,4
WL 3
LO 17,17,114
LU 114,17
M 1,29,5
s_add_u32 s42, s44, s61
RA 224,1,9216
HS 25,114
M 2,40,4
s_addc_u32 s43, s45, 0
LL 16,92
LL 118,93
M 2,41,5
LA 16
PK 114,17,112,33
M 3,52,4
LC_ 118
PK 112,17,112,32
M 3,53,5
ST 108
M 4,64,4
WL 3
LO 18,18,115
LU 115,18
M 4,65,5
RA 228,1,10240
HS 25,115
M 5,76,4
LL 17,94
LL 119,95
M 5,77,5
s_add_u32 s42, s44, s62
LA 17
PK 115,18,113,33
M 0,18,0
s_addc_u32 s43, s45, 0
LC_ 119
PK 113,18,113,32
M 0,19,1
ST 112
M 1,30,0
WL 3
LO 16,16,118
LU 118,16
M 1,31,1
RA 232,1,11264
HS 25,118
M 2,42,0
LL 18,96
LL 122,97
M 2,43,1
LA 18
PK 118,16,116,33
M 3,54,0
LC_ 122
PK 116,16,116,32
M 3,55,1
RV 252,22,5392
PO 118
M 4,66,0
WL 4
LO 17,17,119
LU 119,17
M 4,67,1
RA 236,1,12288
HS 25,119
M 5,78,0
LL 16,98
LL 123,99
M 5,79,1
LA 16
PK 119,17,117,33
M 0,20,2
s_add_u32 s42, s44, s63
LC_ 123
PK 117,17,117,32
M 0,21,3
s_addc_u32 s43, s45, 0
PO 119
M 1,32,2
WL 4
LO 18,18,122
LU 122,18
M 1,33,3
RA 216,1,13312
HS 25,122
M 2,44,2
LL 17,100
LL 126,101
M 2,45,3
LA 17
PK 122,18,120,33
M 3,56,2
LC_ 126
PK 120,18,120,32
M 3,57,3
ST 116
M 4,68,2
WL 3
LO 16,16,123
LU 123,16
M 4,69,3
RA 220,1,14336
HS 25,123
M 5,80,2
)"
R"(LL 18,102
LL 127,103
M 5,81,3
s_add_u32 s42, s44, s64
LA 18
PK 123,16,121,33
M 0,22,4
s_addc_u32 s43, s45, 0
LC_ 127
PK 121,16,121,32
M 0,23,5
ST 120
M 1,34,4
WL 3
LO 17,17,126
LU 126,17
M 1,35,5
RA 224,1,15360
HS 25,126
M 2,46,4
LL 16,56
LL 104,57
M 2,47,5
LA 16
PK 126,17,124,33
M 3,58,4
LC_ 104
PK 124,17,124,32
M 3,59,5
ds_read_b64 v[14:15], v5 offset:0
PO 126
M 4,70,4
WL 4
LO 18,18,127
LU 127,18
M 4,71,5
RA 228,1,16384
HS 25,127
M 5,82,4
LL 17,58
LL 105,59
M 5,83,5
LA 17
PK 127,18,125,33
Z 6,12,0
s_add_u32 s42, s44, s65
LC_ 105
PK 125,18,125,32
M 6,13,1
s_addc_u32 s43, s45, 0
PO 127
v_subrev_u32 v5, s39, v5
Z 7,24,0
WL 4
LO 104,16,104
LU 16,104
M 7,25,1
RA 232,1,17408
HZ 24,16
Z 8,36,0
LL 18,60
LL 108,61
M 8,37,1
WL 4
LA 18
ST 124
Z 9,48,0
s_mul_i32 s53, s50, 25600
LC_ 108
v_add_f32 v14, v14, v15
M 9,49,1
s_add_u32 s40, %[wlo], s53
v_add_f32_dpp v14, v14, v14 quad_perm:[1,0,3,2] row_mask:0xf bank_mask:0xf
HX 25
Z 10,60,0
WL 3
LO 105,17,105
LU 17,105
M 10,61,1
s_addc_u32 s41, %[whi], 0
RA 236,1,18432
HS 24,17
Z 11,72,0
LL 16,62
LL 109,63
M 11,73,1
s_mov_b32 m0, s38
LA 16
W1 25,1024
M 6,14,2
LC_ 109
DW 0
M 6,15,3
v_add_f32_dpp v14, v14, v14 quad_perm:[2,3,0,1] row_mask:0xf bank_mask:0xf
v_add_u32 v4, s39, v4
M 7,26,2
WL 4
LO 108,18,108
LU 18,108
M 7,27,3
s_add_u32 s37, s37, 32768
RA 216,1,19456
HS 24,18
M 8,38,2
s_cmp_eq_u32 s37, s75
LL 17,64
LL 112,65
M 8,39,3
s_cselect_b32 s37, s74, s37
LA 17
DW 1024
M 9,50,2
LC_ 112
v_add_f32_dpp v14, v14, v14 row_half_mirror row_mask:0xf bank_mask:0xf
M 9,51,3
v_cvt_f64_f32 v[14:15], v14
M 10,62,2
WL 3
LO 109,16,109
LU 16,109
M 10,63,3
RA 220,1,20480
HS 24,16
M 11,74,2
LL 18,66
LL 113,67
M 11,75,3
LA 18
s_mov_b64 exec, s[54:55]
SP
s_mov_b64 exec, -1
M 6,16,4
LC_ 113
DW 2048
M 6,17,5
M 7,28,4
WL 3
LO 112,17,112
LU 17,112
M 7,29,5
s_mul_i32 s53, s51, 49152
RA 224,1,21504
HS 24,17
M 8,40,4
s_bitcmp1_b64 s[90:91], s51
LL 16,68
LL 116,69
M 8,41,5
s_cselect_b64 s[44:45], s[84:85], s[82:83]
LA 16
M 9,52,4
s_add_u32 s44, s44, s53
LC_ 116
M 9,53,5
s_addc_u32 s45, s45, 0
M 10,64,4
WL 3
LO 113,18,113
LU 18,113
M 10,65,5
s_add_u32 s51, s51, %[d]
RA 228,1,22528
HS 24,18
M 11,76,4
LL 17,70
LL 117,71
M 11,77,5
s_cmp_eq_u32 s51, %[wrap]
LA 17
M 6,18,0
s_cselect_b32 s51, %[rst], s51
LC_ 117
M 6,19,1
s_add_u32 s40, s40, 3072
M 7,30,0
WL 3
LO 116,16,116
LU 16,116
M 7,31,1
s_addc_u32 s41, s41, 0
RA 232,1,23552
HS 24,16
M 8,42,0
s_add_u32 m0, s38, 3072
LL 18,72
LL 120,73
M 8,43,1
LA 18
DW 0
M 9,54,0
LC_ 120
DW 1024
M 9,55,1
M 10,66,0
WL 3
LO 117,17,117
LU 17,117
M 10,67,1
RA 236,1,24576
HS 24,17
M 11,78,0
LL 16,74
LL 121,75
M 11,79,1
LA 16
DW 2048
M 6,20,2
LC_ 121
M 6,21,3
s_lshl_b32 s53, s52, 8
v_add_u32 v1, s37, v0
M 7,32,2
WL 3
LO 120,18,120
LU 18,120
M 7,33,3
s_add_u32 s48, %[alo], s53
RA 216,1,0
HS 24,18
M 8,44,2
s_addc_u32 s49, %[ahi], 0
LL 17,76
LL 124,77
M 8,45,3
s_add_u32 s52, s52, %[d]
LA 17
v_and_b32 v22, 0xfffffcff, v1
M 9,56,2
s_cmp_eq_u32 s52, %[wrap]
LC_ 124
M 9,57,3
s_cselect_b32 s52, %[rst], s52
DQ 3072
M 10,68,2
WL 3
LO 121,16,121
LU 16,121
M 10,69,3
s_add_u32 s50, s50, %[d]
RA 220,1,1024
HS 24,16
M 11,80,2
LL 18,78
LL 125,79
M 11,81,3
s_cmp_eq_u32 s50, %[wrap]
LA 18
RV 252,22,5120
M 6,22,4
s_cselect_b32 s50, %[rst], s50
LC_ 125
M 6,23,5
WV 28
M 7,34,4
WL 4
LO 124,17,124
LU 17,124
M 7,35,5
s_add_u32 s38, s38, 32768
RA 224,1,2048
HS 24,17
M 8,46,4
s_cmp_eq_u32 s38, s77
LL 16,80
LL 106,81
M 8,47,5
s_cselect_b32 s38, s76, s38
LA 16
M 9,58,4
s_barrier
LC_ 106
M 9,59,5
s_sub_u32 s39, 0, s39
M 10,70,4
WL 3
LO 125,18,125
LU 18,125
M 10,71,5
RA 228,1,3072
HS 24,18
M 11,82,4
LL 17,82
LL 107,83
M 11,83,5
s_mov_b32 s66, 30
1:
LA 17
HX 24
Z 0,12,0
LC_ 107
W1 24,0
M 0,13,1
Z 1,24,0
WL 4
LO 16,16,106
LU 106,16
M 1,25,1
RA 232,1,4096
HZ 25,106
Z 2,36,0
LL 18,84
LL 110,85
M 2,37,1
LA 18
PK 106,16,104,33
Z 3,48,0
LC_ 110
PK 104,16,104,32
M 3,49,1
Z 4,60,0
WL 4
LO 17,17,107
LU 107,17
M 4,61,1
RA 236,1,6144
HS 25,107
Z 5,72,0
LL 16,86
LL 111,87
M 5,73,1
s_add_u32 s42, s44, s60
LA 16
PK 107,17,105,33
M 0,14,2
s_addc_u32 s43, s45, 0
LC_ 111
PK 105,17,105,32
M 0,15,3
ST 104
M 1,26,2
WL 3
LO 18,18,110
LU 110,18
M 1,27,3
RA 216,1,7168
HS 25,110
M 2,38,2
LL 17,88
LL 114,89
M 2,39,3
LA 17
PK 110,18,108,33
M 3,50,2
LC_ 114
PK 108,18,108,32
M 3,51,3
PO 110
M 4,62,2
WL 3
LO 16,16,111
LU 111,16
M 4,63,3
RA 220,1,8192
HS 25,111
M 5,74,2
LL 18,90
LL 115,91
M 5,75,3
s_mov_b32 m0, %[pm]
LA 18
PK 111,16,109,33
M 0,16,4
LC_ 115
PK 109,16,109,32
M 0,17,5
DF
PO 111
M 1,28,4
WL 3
LO 17,17,114
LU 114,17
M 1,29,5
s_add_u32 s42, s44, s61
RA 224,1,9216
HS 25,114
M 2,40,4
s_addc_u32 s43, s45, 0
LL 16,92
LL 118,93
M 2,41,5
LA 16
PK 114,17,112,33
M 3,52,4
LC_ 118
PK 112,17,112,32
M 3,53,5
ST 108
M 4,64,4
WL 3
LO 18,18,115
LU 115,18
M 4,65,5
RA 228,1,10240
HS 25,115
M 5,76,4
LL 17,94
LL 119,95
M 5,77,5
s_add_u32 s42, s44, s62
LA 17
)"
R"(PK 115,18,113,33
M 0,18,0
s_addc_u32 s43, s45, 0
LC_ 119
PK 113,18,113,32
M 0,19,1
ST 112
M 1,30,0
WL 3
LO 16,16,118
LU 118,16
M 1,31,1
RA 232,1,11264
HS 25,118
M 2,42,0
LL 18,96
LL 122,97
M 2,43,1
LA 18
PK 118,16,116,33
M 3,54,0
LC_ 122
PK 116,16,116,32
M 3,55,1
RV 252,22,5392
PO 118
M 4,66,0
WL 4
LO 17,17,119
LU 119,17
M 4,67,1
RA 236,1,12288
HS 25,119
M 5,78,0
LL 16,98
LL 123,99
M 5,79,1
LA 16
PK 119,17,117,33
M 0,20,2
s_add_u32 s42, s44, s63
LC_ 123
PK 117,17,117,32
M 0,21,3
s_addc_u32 s43, s45, 0
PO 119
M 1,32,2
WL 4
LO 18,18,122
LU 122,18
M 1,33,3
RA 216,1,13312
HS 25,122
M 2,44,2
LL 17,100
LL 126,101
M 2,45,3
LA 17
PK 122,18,120,33
M 3,56,2
LC_ 126
PK 120,18,120,32
M 3,57,3
ST 116
M 4,68,2
WL 3
LO 16,16,123
LU 123,16
M 4,69,3
RA 220,1,14336
HS 25,123
M 5,80,2
LL 18,102
LL 127,103
M 5,81,3
s_add_u32 s42, s44, s64
LA 18
PK 123,16,121,33
M 0,22,4
s_addc_u32 s43, s45, 0
LC_ 127
PK 121,16,121,32
M 0,23,5
ST 120
M 1,34,4
WL 3
LO 17,17,126
LU 126,17
M 1,35,5
RA 224,1,15360
HS 25,126
M 2,46,4
LL 16,56
LL 104,57
M 2,47,5
LA 16
PK 126,17,124,33
M 3,58,4
LC_ 104
PK 124,17,124,32
M 3,59,5
ds_read_b64 v[14:15], v5 offset:0
PO 126
M 4,70,4
WL 4
LO 18,18,127
LU 127,18
M 4,71,5
RA 228,1,16384
HS 25,127
M 5,82,4
LL 17,58
LL 105,59
M 5,83,5
LA 17
PK 127,18,125,33
Z 6,12,0
s_add_u32 s42, s44, s65
LC_ 105
PK 125,18,125,32
M 6,13,1
s_addc_u32 s43, s45, 0
PO 127
v_subrev_u32 v5, s39, v5
Z 7,24,0
WL 4
LO 104,16,104
LU 16,104
M 7,25,1
RA 232,1,17408
HZ 24,16
Z 8,36,0
LL 18,60
LL 108,61
M 8,37,1
WL 4
LA 18
ST 124
Z 9,48,0
s_mul_i32 s53, s50, 25600
LC_ 108
v_add_f32 v14, v14, v15
M 9,49,1
s_add_u32 s40, %[wlo], s53
v_add_f32_dpp v14, v14, v14 quad_perm:[1,0,3,2] row_mask:0xf bank_mask:0xf
HX 25
Z 10,60,0
WL 3
LO 105,17,105
LU 17,105
M 10,61,1
s_addc_u32 s41, %[whi], 0
RA 236,1,18432
HS 24,17
Z 11,72,0
LL 16,62
LL 109,63
M 11,73,1
s_mov_b32 m0, s38
LA 16
W1 25,1024
M 6,14,2
LC_ 109
DW 0
M 6,15,3
v_add_f32_dpp v14, v14, v14 quad_perm:[2,3,0,1] row_mask:0xf bank_mask:0xf
v_add_u32 v4, s39, v4
M 7,26,2
WL 4
LO 108,18,108
LU 18,108
M 7,27,3
s_add_u32 s37, s37, 32768
RA 216,1,19456
HS 24,18
M 8,38,2
s_cmp_eq_u32 s37, s75
LL 17,64
LL 112,65
M 8,39,3
s_cselect_b32 s37, s74, s37
LA 17
DW 1024
M 9,50,2
LC_ 112
v_add_f32_dpp v14, v14, v14 row_half_mirror row_mask:0xf bank_mask:0xf
M 9,51,3
v_cvt_f64_f32 v[14:15], v14
M 10,62,2
WL 3
LO 109,16,109
LU 16,109
M 10,63,3
RA 220,1,20480
HS 24,16
M 11,74,2
LL 18,66
LL 113,67
M 11,75,3
LA 18
s_mov_b64 exec, s[54:55]
SP
s_mov_b64 exec, -1
M 6,16,4
LC_ 113
DW 2048
M 6,17,5
M 7,28,4
WL 3
LO 112,17,112
LU 17,112
M 7,29,5
s_mul_i32 s53, s51, 49152
RA 224,1,21504
HS 24,17
M 8,40,4
s_bitcmp1_b64 s[90:91], s51
LL 16,68
LL 116,69
M 8,41,5
s_cselect_b64 s[44:45], s[84:85], s[82:83]
LA 16
M 9,52,4
s_add_u32 s44, s44, s53
LC_ 116
M 9,53,5
s_addc_u32 s45, s45, 0
M 10,64,4
WL 3
LO 113,18,113
LU 18,113
M 10,65,5
s_add_u32 s51, s51, %[d]
RA 228,1,22528
HS 24,18
M 11,76,4
LL 17,70
LL 117,71
M 11,77,5
s_cmp_eq_u32 s51, %[wrap]
LA 17
M 6,18,0
s_cselect_b32 s51, %[rst], s51
LC_ 117
M 6,19,1
s_add_u32 s40, s40, 3072
M 7,30,0
WL 3
LO 116,16,116
LU 16,116
M 7,31,1
s_addc_u32 s41, s41, 0
RA 232,1,23552
HS 24,16
M 8,42,0
s_add_u32 m0, s38, 3072
LL 18,72
LL 120,73
M 8,43,1
LA 18
DW 0
M 9,54,0
LC_ 120
DW 1024
M 9,55,1
M 10,66,0
WL 3
LO 117,17,117
LU 17,117
M 10,67,1
RA 236,1,24576
HS 24,17
M 11,78,0
LL 16,74
LL 121,75
M 11,79,1
LA 16
DW 2048
M 6,20,2
LC_ 121
M 6,21,3
s_lshl_b32 s53, s52, 8
v_add_u32 v1, s37, v0
M 7,32,2
WL 3
LO 120,18,120
LU 18,120
M 7,33,3
s_add_u32 s48, %[alo], s53
RA 216,1,0
HS 24,18
M 8,44,2
s_addc_u32 s49, %[ahi], 0
LL 17,76
LL 124,77
M 8,45,3
s_add_u32 s52, s52, %[d]
LA 17
v_and_b32 v22, 0xfffffcff, v1
M 9,56,2
s_cmp_eq_u32 s52, %[wrap]
LC_ 124
M 9,57,3
s_cselect_b32 s52, %[rst], s52
DQ 3072
M 10,68,2
WL 3
LO 121,16,121
LU 16,121
M 10,69,3
s_add_u32 s50, s50, %[d]
RA 220,1,1024
HS 24,16
M 11,80,2
LL 18,78
LL 125,79
M 11,81,3
s_cmp_eq_u32 s50, %[wrap]
LA 18
RV 252,22,5120
M 6,22,4
s_cselect_b32 s50, %[rst], s50
LC_ 125
M 6,23,5
WV 30
M 7,34,4
WL 4
LO 124,17,124
LU 17,124
M 7,35,5
s_add_u32 s38, s38, 32768
RA 224,1,2048
HS 24,17
M 8,46,4
s_cmp_eq_u32 s38, s77
LL 16,80
LL 106,81
M 8,47,5
s_cselect_b32 s38, s76, s38
LA 16
M 9,58,4
s_barrier
LC_ 106
M 9,59,5
s_sub_u32 s39, 0, s39
M 10,70,4
WL 3
LO 125,18,125
LU 18,125
M 10,71,5
RA 228,1,3072
HS 24,18
M 11,82,4
s_sub_u32 s66, s66, 1
LL 17,82
LL 107,83
M 11,83,5
s_cbranch_scc0 1b
7:
LA 17
HX 24
Z 0,12,0
LC_ 107
W1 24,0
M 0,13,1
Z 1,24,0
WL 4
LO 16,16,106
LU 106,16
M 1,25,1
RA 232,1,4096
HZ 25,106
Z 2,36,0
LL 18,84
LL 110,85
M 2,37,1
LA 18
PK 106,16,104,33
Z 3,48,0
LC_ 110
PK 104,16,104,32
M 3,49,1
Z 4,60,0
WL 4
LO 17,17,107
LU 107,17
M 4,61,1
RA 236,1,6144
HS 25,107
Z 5,72,0
LL 16,86
LL 111,87
M 5,73,1
s_add_u32 s42, s44, s60
LA 16
PK 107,17,105,33
M 0,14,2
s_addc_u32 s43, s45, 0
LC_ 111
PK 105,17,105,32
)"
R"(M 0,15,3
ST 104
M 1,26,2
WL 3
LO 18,18,110
LU 110,18
M 1,27,3
RA 216,1,7168
HS 25,110
M 2,38,2
LL 17,88
LL 114,89
M 2,39,3
LA 17
PK 110,18,108,33
M 3,50,2
LC_ 114
PK 108,18,108,32
M 3,51,3
PO 110
M 4,62,2
WL 3
LO 16,16,111
LU 111,16
M 4,63,3
RA 220,1,8192
HS 25,111
M 5,74,2
LL 18,90
LL 115,91
M 5,75,3
s_mov_b32 m0, %[pm]
LA 18
PK 111,16,109,33
M 0,16,4
LC_ 115
PK 109,16,109,32
M 0,17,5
DF
PO 111
M 1,28,4
WL 3
LO 17,17,114
LU 114,17
M 1,29,5
s_add_u32 s42, s44, s61
RA 224,1,9216
HS 25,114
M 2,40,4
s_addc_u32 s43, s45, 0
LL 16,92
LL 118,93
M 2,41,5
LA 16
PK 114,17,112,33
M 3,52,4
LC_ 118
PK 112,17,112,32
M 3,53,5
ST 108
M 4,64,4
WL 3
LO 18,18,115
LU 115,18
M 4,65,5
RA 228,1,10240
HS 25,115
M 5,76,4
LL 17,94
LL 119,95
M 5,77,5
s_add_u32 s42, s44, s62
LA 17
PK 115,18,113,33
M 0,18,0
s_addc_u32 s43, s45, 0
LC_ 119
PK 113,18,113,32
M 0,19,1
ST 112
M 1,30,0
WL 3
LO 16,16,118
LU 118,16
M 1,31,1
RA 232,1,11264
HS 25,118
M 2,42,0
LL 18,96
LL 122,97
M 2,43,1
LA 18
PK 118,16,116,33
M 3,54,0
LC_ 122
PK 116,16,116,32
M 3,55,1
RV 252,22,5392
PO 118
M 4,66,0
WL 4
LO 17,17,119
LU 119,17
M 4,67,1
RA 236,1,12288
HS 25,119
M 5,78,0
LL 16,98
LL 123,99
M 5,79,1
LA 16
PK 119,17,117,33
M 0,20,2
s_add_u32 s42, s44, s63
LC_ 123
PK 117,17,117,32
M 0,21,3
s_addc_u32 s43, s45, 0
PO 119
M 1,32,2
WL 4
LO 18,18,122
LU 122,18
M 1,33,3
RA 216,1,13312
HS 25,122
M 2,44,2
LL 17,100
LL 126,101
M 2,45,3
LA 17
PK 122,18,120,33
M 3,56,2
LC_ 126
PK 120,18,120,32
M 3,57,3
ST 116
M 4,68,2
WL 3
LO 16,16,123
LU 123,16
M 4,69,3
RA 220,1,14336
HS 25,123
M 5,80,2
LL 18,102
LL 127,103
M 5,81,3
s_add_u32 s42, s44, s64
LA 18
PK 123,16,121,33
M 0,22,4
s_addc_u32 s43, s45, 0
LC_ 127
PK 121,16,121,32
M 0,23,5
ST 120
M 1,34,4
WL 3
LO 17,17,126
LU 126,17
M 1,35,5
RA 224,1,15360
HS 25,126
M 2,46,4
LL 16,56
LL 104,57
M 2,47,5
LA 16
PK 126,17,124,33
M 3,58,4
LC_ 104
PK 124,17,124,32
M 3,59,5
ds_read_b64 v[14:15], v5 offset:0
PO 126
M 4,70,4
WL 4
LO 18,18,127
LU 127,18
M 4,71,5
RA 228,1,16384
HS 25,127
M 5,82,4
LL 17,58
LL 105,59
M 5,83,5
LA 17
PK 127,18,125,33
Z 6,12,0
s_add_u32 s42, s44, s65
LC_ 105
PK 125,18,125,32
M 6,13,1
s_addc_u32 s43, s45, 0
PO 127
v_subrev_u32 v5, s39, v5
Z 7,24,0
WL 4
LO 104,16,104
LU 16,104
M 7,25,1
RA 232,1,17408
HZ 24,16
Z 8,36,0
LL 18,60
LL 108,61
M 8,37,1
WL 4
LA 18
ST 124
Z 9,48,0
s_mul_i32 s53, s50, 25600
LC_ 108
v_add_f32 v14, v14, v15
M 9,49,1
s_add_u32 s40, %[wlo], s53
v_add_f32_dpp v14, v14, v14 quad_perm:[1,0,3,2] row_mask:0xf bank_mask:0xf
HX 25
Z 10,60,0
WL 3
LO 105,17,105
LU 17,105
M 10,61,1
s_addc_u32 s41, %[whi], 0
RA 236,1,18432
HS 24,17
Z 11,72,0
LL 16,62
LL 109,63
M 11,73,1
s_mov_b32 m0, s38
LA 16
W1 25,1024
M 6,14,2
LC_ 109
DW 0
M 6,15,3
v_add_f32_dpp v14, v14, v14 quad_perm:[2,3,0,1] row_mask:0xf bank_mask:0xf
v_add_u32 v4, s39, v4
M 7,26,2
WL 4
LO 108,18,108
LU 18,108
M 7,27,3
s_add_u32 s37, s37, 32768
RA 216,1,19456
HS 24,18
M 8,38,2
s_cmp_eq_u32 s37, s75
LL 17,64
LL 112,65
s_mov_b64 exec, 1
AT
s_mov_b64 exec, -1
M 8,39,3
s_cselect_b32 s37, s74, s37
LA 17
DW 1024
M 9,50,2
LC_ 112
v_add_f32_dpp v14, v14, v14 row_half_mirror row_mask:0xf bank_mask:0xf
M 9,51,3
v_cvt_f64_f32 v[14:15], v14
M 10,62,2
WL 3
LO 109,16,109
LU 16,109
M 10,63,3
RA 220,1,20480
HS 24,16
M 11,74,2
LL 18,66
LL 113,67
M 11,75,3
LA 18
s_mov_b64 exec, s[54:55]
SP
s_mov_b64 exec, -1
M 6,16,4
LC_ 113
DW 2048
M 6,17,5
M 7,28,4
WL 3
LO 112,17,112
LU 17,112
M 7,29,5
s_mul_i32 s53, s51, 49152
RA 224,1,21504
HS 24,17
M 8,40,4
s_bitcmp1_b64 s[90:91], s51
LL 16,68
LL 116,69
M 8,41,5
s_cselect_b64 s[44:45], s[84:85], s[82:83]
LA 16
M 9,52,4
s_add_u32 s44, s44, s53
LC_ 116
M 9,53,5
s_addc_u32 s45, s45, 0
M 10,64,4
WL 3
LO 113,18,113
LU 18,113
M 10,65,5
s_add_u32 s51, s51, %[d]
RA 228,1,22528
HS 24,18
M 11,76,4
LL 17,70
LL 117,71
M 11,77,5
s_cmp_eq_u32 s51, %[wrap]
LA 17
M 6,18,0
s_cselect_b32 s51, %[rst], s51
LC_ 117
M 6,19,1
s_add_u32 s40, s40, 3072
M 7,30,0
WL 3
LO 116,16,116
LU 16,116
M 7,31,1
s_addc_u32 s41, s41, 0
RA 232,1,23552
HS 24,16
M 8,42,0
s_add_u32 m0, s38, 3072
LL 18,72
LL 120,73
M 8,43,1
LA 18
DW 0
M 9,54,0
LC_ 120
DW 1024
M 9,55,1
M 10,66,0
WL 3
LO 117,17,117
LU 17,117
M 10,67,1
RA 236,1,24576
HS 24,17
M 11,78,0
LL 16,74
LL 121,75
M 11,79,1
LA 16
DW 2048
M 6,20,2
LC_ 121
M 6,21,3
s_lshl_b32 s53, s52, 8
v_add_u32 v1, s37, v0
M 7,32,2
WL 3
LO 120,18,120
LU 18,120
M 7,33,3
s_add_u32 s48, %[alo], s53
RA 216,1,0
HS 24,18
M 8,44,2
s_addc_u32 s49, %[ahi], 0
LL 17,76
LL 124,77
M 8,45,3
s_add_u32 s52, s52, %[d]
LA 17
v_and_b32 v22, 0xfffffcff, v1
M 9,56,2
s_cmp_eq_u32 s52, %[wrap]
LC_ 124
M 9,57,3
s_cselect_b32 s52, %[rst], s52
DQ 3072
M 10,68,2
WL 3
LO 121,16,121
LU 16,121
M 10,69,3
s_add_u32 s50, s50, %[d]
RA 220,1,1024
HS 24,16
M 11,80,2
LL 18,78
LL 125,79
M 11,81,3
s_cmp_eq_u32 s50, %[wrap]
LA 18
RV 252,22,5120
M 6,22,4
s_cselect_b32 s50, %[rst], s50
LC_ 125
M 6,23,5
)"
R"(WV 31
v_add_u32 v19, 1, v19
M 7,34,4
WL 4
LO 124,17,124
LU 17,124
M 7,35,5
s_add_u32 s38, s38, 32768
RA 224,1,2048
HS 24,17
M 8,46,4
s_cmp_eq_u32 s38, s77
LL 16,80
LL 106,81
M 8,47,5
s_cselect_b32 s38, s76, s38
LA 16
FP
M 9,58,4
s_sub_u32 s39, 0, s39
LC_ 106
M 9,59,5
M 10,70,4
WL 4
LO 125,18,125
LU 18,125
M 10,71,5
RA 228,1,3072
HS 24,18
M 11,82,4
LL 17,82
LL 107,83
M 11,83,5
LA 17
HX 24
Z 0,12,0
LC_ 107
M 0,13,1
Z 1,24,0
WL 3
LO 16,16,106
LU 106,16
M 1,25,1
RA 232,1,4096
HZ 25,106
Z 2,36,0
LL 18,84
LL 110,85
M 2,37,1
LA 18
PK 106,16,104,33
Z 3,48,0
LC_ 110
PK 104,16,104,32
M 3,49,1
Z 4,60,0
WL 3
LO 17,17,107
LU 107,17
M 4,61,1
RA 236,1,6144
HS 25,107
Z 5,72,0
LL 16,86
LL 111,87
M 5,73,1
s_add_u32 s42, s44, s60
LA 16
PK 107,17,105,33
M 0,14,2
s_addc_u32 s43, s45, 0
LC_ 111
PK 105,17,105,32
M 0,15,3
FR
M 1,26,2
WL 4
LO 18,18,110
LU 110,18
M 1,27,3
RA 216,1,7168
HS 25,110
M 2,38,2
LL 17,88
LL 114,89
M 2,39,3
LA 17
PK 110,18,108,33
M 3,50,2
LC_ 114
PK 108,18,108,32
M 3,51,3
ST 104
PO 110
M 4,62,2
WL 4
LO 16,16,111
LU 111,16
M 4,63,3
RA 220,1,8192
HS 25,111
M 5,74,2
LL 18,90
LL 115,91
M 5,75,3
s_mov_b32 m0, %[pm]
LA 18
PK 111,16,109,33
M 0,16,4
LC_ 115
PK 109,16,109,32
M 0,17,5
DF
PO 111
M 1,28,4
WL 3
LO 17,17,114
LU 114,17
M 1,29,5
s_add_u32 s42, s44, s61
RA 224,1,9216
HS 25,114
M 2,40,4
s_addc_u32 s43, s45, 0
LL 16,92
LL 118,93
M 2,41,5
LA 16
PK 114,17,112,33
M 3,52,4
LC_ 118
PK 112,17,112,32
M 3,53,5
ST 108
v_cmp_gt_u32 vcc, v19, v15
M 4,64,4
WL 3
LO 18,18,115
LU 115,18
M 4,65,5
RA 228,1,10240
HS 25,115
M 5,76,4
LL 17,94
LL 119,95
M 5,77,5
s_cbranch_vccnz .Lk1s0
.Lk1r0:
LA 17
PK 115,18,113,33
M 0,18,0
s_add_u32 s42, s44, s62
LC_ 119
PK 113,18,113,32
M 0,19,1
s_addc_u32 s43, s45, 0
W1 24,0
M 1,30,0
WL 4
LO 16,16,118
LU 118,16
M 1,31,1
RA 232,1,11264
HS 25,118
M 2,42,0
LL 18,96
LL 122,97
M 2,43,1
LA 18
PK 118,16,116,33
M 3,54,0
LC_ 122
PK 116,16,116,32
M 3,55,1
RV 252,22,5392
PO 118
M 4,66,0
WL 5
LO 17,17,119
LU 119,17
M 4,67,1
RA 236,1,12288
HS 25,119
M 5,78,0
LL 16,98
LL 123,99
M 5,79,1
LA 16
PK 119,17,117,33
M 0,20,2
LC_ 123
PK 117,17,117,32
M 0,21,3
ST 112
PO 119
M 1,32,2
WL 4
LO 18,18,122
LU 122,18
M 1,33,3
s_add_u32 s42, s44, s63
RA 216,1,13312
HS 25,122
M 2,44,2
s_addc_u32 s43, s45, 0
LL 17,100
LL 126,101
M 2,45,3
LA 17
PK 122,18,120,33
M 3,56,2
LC_ 126
PK 120,18,120,32
M 3,57,3
ST 116
M 4,68,2
WL 3
LO 16,16,123
LU 123,16
M 4,69,3
RA 220,1,14336
HS 25,123
M 5,80,2
LL 18,102
LL 127,103
M 5,81,3
s_add_u32 s42, s44, s64
LA 18
PK 123,16,121,33
M 0,22,4
s_addc_u32 s43, s45, 0
LC_ 127
PK 121,16,121,32
M 0,23,5
ST 120
M 1,34,4
WL 3
LO 17,17,126
LU 126,17
M 1,35,5
RA 224,1,15360
HS 25,126
M 2,46,4
LL 16,56
LL 104,57
M 2,47,5
LA 16
PK 126,17,124,33
M 3,58,4
LC_ 104
PK 124,17,124,32
M 3,59,5
ds_read_b64 v[14:15], v5 offset:0
PO 126
M 4,70,4
WL 4
LO 18,18,127
LU 127,18
M 4,71,5
RA 228,1,16384
HS 25,127
M 5,82,4
LL 17,58
LL 105,59
M 5,83,5
LA 17
PK 127,18,125,33
Z 6,12,0
s_add_u32 s42, s44, s65
LC_ 105
PK 125,18,125,32
M 6,13,1
s_addc_u32 s43, s45, 0
PO 127
v_subrev_u32 v5, s39, v5
Z 7,24,0
WL 4
LO 104,16,104
LU 16,104
M 7,25,1
RA 232,1,17408
HZ 24,16
Z 8,36,0
LL 18,60
LL 108,61
M 8,37,1
WL 4
LA 18
ST 124
Z 9,48,0
s_mul_i32 s53, s50, 25600
LC_ 108
v_add_f32 v14, v14, v15
M 9,49,1
s_add_u32 s40, %[wlo], s53
v_add_f32_dpp v14, v14, v14 quad_perm:[1,0,3,2] row_mask:0xf bank_mask:0xf
HX 25
Z 10,60,0
WL 3
LO 105,17,105
LU 17,105
M 10,61,1
s_addc_u32 s41, %[whi], 0
RA 236,1,18432
HS 24,17
Z 11,72,0
LL 16,62
LL 109,63
M 11,73,1
s_mov_b32 m0, s38
LA 16
W1 25,1024
M 6,14,2
LC_ 109
DW 0
M 6,15,3
WV 15
v_add_f32_dpp v14, v14, v14 quad_perm:[2,3,0,1] row_mask:0xf bank_mask:0xf
v_readfirstlane_b32 s72, v11
M 7,26,2
WL 4
LO 108,18,108
LU 18,108
M 7,27,3
s_add_u32 s37, s37, 32768
RA 216,1,19456
HS 24,18
M 8,38,2
s_cmp_eq_u32 s37, s75
LL 17,64
LL 112,65
v_mov_b32 v12, s72
M 8,39,3
s_cselect_b32 s37, s74, s37
LA 17
W2 12,12,16
M 9,50,2
LC_ 112
DW 1024
M 9,51,3
v_add_f32_dpp v14, v14, v14 row_half_mirror row_mask:0xf bank_mask:0xf
v_add_u32 v4, s39, v4
M 10,62,2
WL 4
LO 109,16,109
LU 16,109
M 10,63,3
RA 220,1,20480
HS 24,16
M 11,74,2
LL 18,66
LL 113,67
M 11,75,3
LA 18
DW 2048
M 6,16,4
LC_ 113
v_cvt_f64_f32 v[14:15], v14
M 6,17,5
s_mov_b64 exec, s[54:55]
SP
s_mov_b64 exec, -1
M 7,28,4
WL 3
LO 112,17,112
LU 17,112
M 7,29,5
s_mul_i32 s53, s51, 49152
RA 224,1,21504
HS 24,17
M 8,40,4
s_bitcmp1_b64 s[90:91], s51
LL 16,68
LL 116,69
M 8,41,5
s_cselect_b64 s[44:45], s[84:85], s[82:83]
LA 16
M 9,52,4
s_add_u32 s44, s44, s53
LC_ 116
M 9,53,5
s_addc_u32 s45, s45, 0
M 10,64,4
WL 3
LO 113,18,113
LU 18,113
M 10,65,5
s_add_u32 s51, s51, %[d]
RA 228,1,22528
HS 24,18
M 11,76,4
LL 17,70
LL 117,71
M 11,77,5
s_cmp_eq_u32 s51, %[wrap]
LA 17
M 6,18,0
s_cselect_b32 s51, %[rst], s51
LC_ 117
M 6,19,1
)"
R"(s_add_u32 s40, s40, 3072
M 7,30,0
WL 3
LO 116,16,116
LU 16,116
M 7,31,1
s_addc_u32 s41, s41, 0
RA 232,1,23552
HS 24,16
M 8,42,0
s_add_u32 m0, s38, 3072
LL 18,72
LL 120,73
M 8,43,1
LA 18
DW 0
M 9,54,0
LC_ 120
DW 1024
M 9,55,1
M 10,66,0
WL 3
LO 117,17,117
LU 17,117
M 10,67,1
RA 236,1,24576
HS 24,17
M 11,78,0
LL 16,74
LL 121,75
M 11,79,1
LA 16
DW 2048
M 6,20,2
LC_ 121
M 6,21,3
s_lshl_b32 s53, s52, 8
v_add_u32 v1, s37, v0
M 7,32,2
WL 3
LO 120,18,120
LU 18,120
M 7,33,3
s_add_u32 s48, %[alo], s53
RA 216,1,0
HS 24,18
M 8,44,2
s_addc_u32 s49, %[ahi], 0
LL 17,76
LL 124,77
M 8,45,3
s_add_u32 s52, s52, %[d]
LA 17
v_and_b32 v22, 0xfffffcff, v1
M 9,56,2
s_cmp_eq_u32 s52, %[wrap]
LC_ 124
M 9,57,3
s_cselect_b32 s52, %[rst], s52
DQ 3072
M 10,68,2
WL 3
LO 121,16,121
LU 16,121
M 10,69,3
s_add_u32 s50, s50, %[d]
RA 220,1,1024
HS 24,16
M 11,80,2
LL 18,78
LL 125,79
M 11,81,3
s_cmp_eq_u32 s50, %[wrap]
LA 18
RV 252,22,5120
M 6,22,4
s_cselect_b32 s50, %[rst], s50
LC_ 125
M 6,23,5
v_add_u32 v19, 1, v19
M 7,34,4
WL 4
LO 124,17,124
LU 17,124
M 7,35,5
s_add_u32 s38, s38, 32768
RA 224,1,2048
HS 24,17
M 8,46,4
s_cmp_eq_u32 s38, s77
LL 16,80
LL 106,81
M 8,47,5
s_cselect_b32 s38, s76, s38
LA 16
FP
M 9,58,4
s_sub_u32 s39, 0, s39
LC_ 106
M 9,59,5
M 10,70,4
WL 4
LO 125,18,125
LU 18,125
M 10,71,5
RA 228,1,3072
HS 24,18
M 11,82,4
LL 17,82
LL 107,83
M 11,83,5
LA 17
HX 24
Z 0,12,0
LC_ 107
M 0,13,1
Z 1,24,0
WL 3
LO 16,16,106
LU 106,16
M 1,25,1
RA 232,1,4096
HZ 25,106
Z 2,36,0
LL 18,84
LL 110,85
M 2,37,1
LA 18
PK 106,16,104,33
Z 3,48,0
LC_ 110
PK 104,16,104,32
M 3,49,1
Z 4,60,0
WL 3
LO 17,17,107
LU 107,17
M 4,61,1
RA 236,1,6144
HS 25,107
Z 5,72,0
LL 16,86
LL 111,87
M 5,73,1
s_add_u32 s42, s44, s60
LA 16
PK 107,17,105,33
M 0,14,2
s_addc_u32 s43, s45, 0
LC_ 111
PK 105,17,105,32
M 0,15,3
FR
M 1,26,2
WL 4
LO 18,18,110
LU 110,18
M 1,27,3
RA 216,1,7168
HS 25,110
M 2,38,2
LL 17,88
LL 114,89
M 2,39,3
LA 17
PK 110,18,108,33
M 3,50,2
LC_ 114
PK 108,18,108,32
M 3,51,3
ST 104
PO 110
M 4,62,2
WL 4
LO 16,16,111
LU 111,16
M 4,63,3
RA 220,1,8192
HS 25,111
M 5,74,2
LL 18,90
LL 115,91
M 5,75,3
s_mov_b32 m0, %[pm]
LA 18
PK 111,16,109,33
M 0,16,4
LC_ 115
PK 109,16,109,32
M 0,17,5
DF
PO 111
M 1,28,4
WL 3
LO 17,17,114
LU 114,17
M 1,29,5
s_add_u32 s42, s44, s61
RA 224,1,9216
HS 25,114
M 2,40,4
s_addc_u32 s43, s45, 0
LL 16,92
LL 118,93
M 2,41,5
LA 16
PK 114,17,112,33
M 3,52,4
LC_ 118
PK 112,17,112,32
M 3,53,5
ST 108
v_cmp_gt_u32 vcc, v19, v15
M 4,64,4
WL 3
LO 18,18,115
LU 115,18
M 4,65,5
RA 228,1,10240
HS 25,115
M 5,76,4
LL 17,94
LL 119,95
M 5,77,5
s_cbranch_vccnz .Lk1s1
.Lk1r1:
LA 17
PK 115,18,113,33
M 0,18,0
s_add_u32 s42, s44, s62
LC_ 119
PK 113,18,113,32
M 0,19,1
s_addc_u32 s43, s45, 0
ds_read_b32 v20, v5 offset:4096
M 1,30,0
WL 4
LO 16,16,118
LU 118,16
M 1,31,1
RA 232,1,11264
HS 25,118
M 2,42,0
LL 18,96
LL 122,97
M 2,43,1
LA 18
PK 118,16,116,33
M 3,54,0
LC_ 122
PK 116,16,116,32
M 3,55,1
RV 252,22,5392
PO 118
M 4,66,0
WL 5
LO 17,17,119
LU 119,17
M 4,67,1
RA 236,1,12288
HS 25,119
M 5,78,0
LL 16,98
LL 123,99
M 5,79,1
LA 16
PK 119,17,117,33
M 0,20,2
LC_ 123
PK 117,17,117,32
M 0,21,3
W1 24,0
PO 119
M 1,32,2
WL 5
LO 18,18,122
LU 122,18
M 1,33,3
RA 216,1,13312
HS 25,122
M 2,44,2
LL 17,100
LL 126,101
ST 112
M 2,45,3
LA 17
PK 122,18,120,33
M 3,56,2
LC_ 126
PK 120,18,120,32
M 3,57,3
v_readfirstlane_b32 s72, v20
M 4,68,2
WL 4
LO 16,16,123
LU 123,16
M 4,69,3
s_sub_u32 s53, 63, s72
RA 220,1,14336
HS 25,123
M 5,80,2
LL 18,102
LL 127,103
M 5,81,3
s_cmp_eq_u32 %[d], 1
LA 18
PK 123,16,121,33
M 0,22,4
s_cselect_b32 s72, s72, s53
LC_ 127
PK 121,16,121,32
M 0,23,5
s_and_b32 s72, s72, 63
M 1,34,4
WL 3
LO 17,17,126
LU 126,17
M 1,35,5
s_add_u32 s42, s44, s63
RA 224,1,15360
HS 25,126
s_addc_u32 s43, s45, 0
M 2,46,4
s_add_u32 s53, s72, 128
LL 16,56
LL 104,57
M 2,47,5
s_mul_i32 s53, s53, 2359296
LA 16
PK 126,17,124,33
M 3,58,4
s_add_u32 s86, %[hlo], s53
LC_ 104
PK 124,17,124,32
M 3,59,5
s_addc_u32 s87, %[hhi], 0
ds_read_b64 v[14:15], v5 offset:0
PO 126
M 4,70,4
WL 4
LO 18,18,127
LU 127,18
M 4,71,5
s_add_u32 s88, s86, 8192
RA 228,1,16384
HS 25,127
M 5,82,4
LL 17,58
LL 105,59
M 5,83,5
s_addc_u32 s89, s87, 0
LA 17
PK 127,18,125,33
s_and_b32 s92, %[hlo], 0x1fffff
s_add_u32 s92, s92, s53
s_mov_b32 s93, 3072
ST 116
s_mul_i32 s57, s72, 589824
s_add_u32 s57, s57, %[xs1]
s_lshl_b32 s72, s72, 1
s_add_u32 s72, s72, s96
s_lshl_b32 s53, s72, 6
s_add_u32 s53, s53, 128
s_cmp_lt_u32 %[ca0], 128
s_cselect_b32 s73, s53, %[ca0]
Z 6,12,0
LC_ 105
PK 125,18,125,32
v_mov_b32 v9, s73
M 6,13,1
s_add_u32 s42, s44, s64
PO 127
v_subrev_u32 v5, s39, v5
Z 7,24,0
WL 4
LO 104,16,104
LU 16,104
s_addc_u32 s43, s45, 0
M 7,25,1
RA 232,1,17408
HZ 24,16
v_mov_b32 v11, s92
Z 8,36,0
LL 18,60
LL 108,61
ST 120
M 8,37,1
WL 4
LA 18
v_add_f32 v14, v14, v15
Z 9,48,0
LC_ 108
v_add_f32_dpp v14, v14, v14 quad_perm:[1,0,3,2] row_mask:0xf bank_mask:0xf
v_mad_u32_u24 v11, v0, s93, v11
M 9,49,1
v_add_f32_dpp v14, v14, v14 quad_perm:[2,3,0,1] row_mask:0xf bank_mask:0xf
HX 25
Z 10,60,0
WL 3
LO 105,17,105
)"
R"(LU 17,105
M 10,61,1
RA 236,1,18432
HS 24,17
v_lshrrev_b32 v11, 21, v11
Z 11,72,0
LL 16,62
LL 109,63
M 11,73,1
LA 16
W1 25,1024
M 6,14,2
LC_ 109
v_add_f32_dpp v14, v14, v14 row_half_mirror row_mask:0xf bank_mask:0xf
v_lshl_add_u32 v11, v11, 2, 64
M 6,15,3
s_add_u32 s42, s44, s65
v_cvt_f64_f32 v[14:15], v14
v_add_u32 v4, s39, v4
M 7,26,2
WL 4
LO 108,18,108
LU 18,108
M 7,27,3
s_addc_u32 s43, s45, 0
RA 216,1,19456
HS 24,18
v_writelane_b32 v11, %[tsel], 63
M 8,38,2
LL 17,64
LL 112,65
M 8,39,3
LA 17
ST 124
M 9,50,2
s_sub_u32 s53, s86, %[hlo]
LC_ 112
s_mov_b64 exec, s[54:55]
SP
s_mov_b64 exec, -1
s_lshr_b32 s53, s53, 2
s_and_b32 s92, %[xlo], 0x1fffff
s_add_u32 s53, s53, s92
s_add_u32 s53, s53, 0x20048000
s_lshr_b32 s53, s53, 21
s_lshl_b32 s92, s53, 2
M 9,51,3
s_mul_i32 s53, s50, 25600
M 10,62,2
WL 3
LO 109,16,109
LU 16,109
s_add_u32 s40, %[wlo], s53
s_addc_u32 s41, %[whi], 0
s_mov_b32 m0, s38
M 10,63,3
RA 220,1,20480
HS 24,16
v_writelane_b32 v11, s92, 48
M 11,74,2
LL 18,66
LL 113,67
M 11,75,3
LA 18
global_load_dword v11, v11, s[68:69] offset:-2048
M 6,16,4
LC_ 113
DW 0
M 6,17,5
s_add_u32 s37, s37, 32768
M 7,28,4
WL 3
LO 112,17,112
LU 17,112
M 7,29,5
s_cmp_eq_u32 s37, s75
RA 224,1,21504
HS 24,17
M 8,40,4
s_cselect_b32 s37, s74, s37
LL 16,68
LL 116,69
M 8,41,5
LA 16
DW 1024
M 9,52,4
LC_ 116
DW 2048
M 9,53,5
s_mul_i32 s53, s51, 49152
M 10,64,4
WL 3
LO 113,18,113
LU 18,113
M 10,65,5
s_bitcmp1_b64 s[90:91], s51
RA 228,1,22528
HS 24,18
M 11,76,4
LL 17,70
LL 117,71
M 11,77,5
s_cselect_b64 s[44:45], s[84:85], s[82:83]
LA 17
M 6,18,0
s_add_u32 s44, s44, s53
LC_ 117
M 6,19,1
s_addc_u32 s45, s45, 0
M 7,30,0
WL 3
LO 116,16,116
LU 16,116
M 7,31,1
s_add_u32 s51, s51, %[d]
RA 232,1,23552
HS 24,16
M 8,42,0
s_cmp_eq_u32 s51, %[wrap]
LL 18,72
LL 120,73
M 8,43,1
s_cselect_b32 s51, %[rst], s51
LA 18
M 9,54,0
s_add_u32 s40, s40, 3072
LC_ 120
s_addc_u32 s41, s41, 0
s_add_u32 m0, s38, 3072
M 9,55,1
DW 0
M 10,66,0
WL 3
LO 117,17,117
LU 17,117
M 10,67,1
RA 236,1,24576
HS 24,17
M 11,78,0
LL 16,74
LL 121,75
M 11,79,1
LA 16
DW 1024
M 6,20,2
LC_ 121
DW 2048
M 6,21,3
s_lshl_b32 s53, s52, 8
v_add_u32 v1, s37, v0
M 7,32,2
WL 3
LO 120,18,120
LU 18,120
M 7,33,3
s_add_u32 s48, %[alo], s53
RA 216,1,0
HS 24,18
M 8,44,2
s_addc_u32 s49, %[ahi], 0
LL 17,76
LL 124,77
M 8,45,3
s_add_u32 s52, s52, %[d]
LA 17
v_and_b32 v22, 0xfffffcff, v1
M 9,56,2
s_cmp_eq_u32 s52, %[wrap]
LC_ 124
M 9,57,3
s_cselect_b32 s52, %[rst], s52
DQ 3072
M 10,68,2
WL 3
LO 121,16,121
LU 16,121
M 10,69,3
s_add_u32 s50, s50, %[d]
RA 220,1,1024
HS 24,16
M 11,80,2
LL 18,78
LL 125,79
M 11,81,3
s_cmp_eq_u32 s50, %[wrap]
LA 18
RV 252,22,5120
M 6,22,4
s_cselect_b32 s50, %[rst], s50
LC_ 125
M 6,23,5
WV 31
v_add_u32 v19, 1, v19
M 7,34,4
WL 4
LO 124,17,124
LU 17,124
M 7,35,5
s_add_u32 s38, s38, 32768
RA 224,1,2048
HS 24,17
M 8,46,4
s_cmp_eq_u32 s38, s77
LL 16,80
LL 106,81
M 8,47,5
s_cselect_b32 s38, s76, s38
LA 16
FP
M 9,58,4
s_sub_u32 s39, 0, s39
LC_ 106
M 9,59,5
M 10,70,4
WL 4
LO 125,18,125
LU 18,125
M 10,71,5
RA 228,1,3072
HS 24,18
M 11,82,4
LL 17,82
LL 107,83
M 11,83,5
LA 17
HX 24
Z 0,12,0
LC_ 107
M 0,13,1
Z 1,24,0
WL 3
LO 16,16,106
LU 106,16
M 1,25,1
RA 232,1,4096
HZ 25,106
Z 2,36,0
LL 18,84
LL 110,85
M 2,37,1
LA 18
PK 106,16,104,33
Z 3,48,0
LC_ 110
PK 104,16,104,32
M 3,49,1
Z 4,60,0
WL 3
LO 17,17,107
LU 107,17
M 4,61,1
RA 236,1,6144
HS 25,107
Z 5,72,0
LL 16,86
LL 111,87
M 5,73,1
s_add_u32 s42, s44, s60
LA 16
PK 107,17,105,33
M 0,14,2
s_addc_u32 s43, s45, 0
LC_ 111
PK 105,17,105,32
M 0,15,3
FR
M 1,26,2
WL 4
LO 18,18,110
LU 110,18
M 1,27,3
RA 216,1,7168
HS 25,110
M 2,38,2
LL 17,88
LL 114,89
M 2,39,3
LA 17
PK 110,18,108,33
M 3,50,2
LC_ 114
PK 108,18,108,32
M 3,51,3
ST 104
PO 110
M 4,62,2
WL 4
LO 16,16,111
LU 111,16
M 4,63,3
RA 220,1,8192
HS 25,111
M 5,74,2
LL 18,90
LL 115,91
M 5,75,3
s_mov_b32 m0, %[pm]
LA 18
PK 111,16,109,33
M 0,16,4
LC_ 115
PK 109,16,109,32
M 0,17,5
DF
PO 111
M 1,28,4
WL 3
LO 17,17,114
LU 114,17
M 1,29,5
s_add_u32 s42, s44, s61
RA 224,1,9216
HS 25,114
M 2,40,4
s_addc_u32 s43, s45, 0
LL 16,92
LL 118,93
M 2,41,5
LA 16
PK 114,17,112,33
M 3,52,4
LC_ 118
PK 112,17,112,32
M 3,53,5
ST 108
v_cmp_gt_u32 vcc, v19, v15
M 4,64,4
WL 3
LO 18,18,115
LU 115,18
M 4,65,5
RA 228,1,10240
HS 25,115
M 5,76,4
LL 17,94
LL 119,95
M 5,77,5
s_cbranch_vccnz .Lk1s2
.Lk1r2:
LA 17
PK 115,18,113,33
M 0,18,0
s_add_u32 s42, s44, s62
LC_ 119
PK 113,18,113,32
M 0,19,1
s_addc_u32 s43, s45, 0
W1 24,0
M 1,30,0
WL 4
LO 16,16,118
LU 118,16
M 1,31,1
RA 232,1,11264
HS 25,118
M 2,42,0
LL 18,96
LL 122,97
M 2,43,1
LA 18
PK 118,16,116,33
M 3,54,0
LC_ 122
PK 116,16,116,32
M 3,55,1
RV 252,22,5392
PO 118
M 4,66,0
WL 5
LO 17,17,119
LU 119,17
M 4,67,1
RA 236,1,12288
HS 25,119
M 5,78,0
LL 16,98
LL 123,99
M 5,79,1
LA 16
PK 119,17,117,33
M 0,20,2
LC_ 123
PK 117,17,117,32
M 0,21,3
ST 112
PO 119
M 1,32,2
WL 4
LO 18,18,122
LU 122,18
M 1,33,3
s_add_u32 s42, s44, s63
RA 216,1,13312
HS 25,122
M 2,44,2
s_addc_u32 s43, s45, 0
LL 17,100
LL 126,101
M 2,45,3
LA 17
)"
R"(PK 122,18,120,33
M 3,56,2
LC_ 126
PK 120,18,120,32
M 3,57,3
ST 116
M 4,68,2
WL 3
LO 16,16,123
LU 123,16
M 4,69,3
RA 220,1,14336
HS 25,123
M 5,80,2
LL 18,102
LL 127,103
M 5,81,3
s_add_u32 s42, s44, s64
LA 18
PK 123,16,121,33
M 0,22,4
s_addc_u32 s43, s45, 0
LC_ 127
PK 121,16,121,32
M 0,23,5
ST 120
M 1,34,4
WL 3
LO 17,17,126
LU 126,17
M 1,35,5
RA 224,1,15360
HS 25,126
M 2,46,4
LL 16,56
LL 104,57
M 2,47,5
LA 16
PK 126,17,124,33
M 3,58,4
LC_ 104
PK 124,17,124,32
M 3,59,5
ds_read_b64 v[14:15], v5 offset:0
PO 126
M 4,70,4
WL 4
LO 18,18,127
LU 127,18
M 4,71,5
RA 228,1,16384
HS 25,127
M 5,82,4
LL 17,58
LL 105,59
M 5,83,5
LA 17
PK 127,18,125,33
Z 6,12,0
s_add_u32 s42, s44, s65
LC_ 105
PK 125,18,125,32
M 6,13,1
s_addc_u32 s43, s45, 0
PO 127
v_subrev_u32 v5, s39, v5
Z 7,24,0
WL 4
LO 104,16,104
LU 16,104
M 7,25,1
RA 232,1,17408
HZ 24,16
Z 8,36,0
LL 18,60
LL 108,61
M 8,37,1
WL 4
LA 18
ST 124
Z 9,48,0
s_mul_i32 s53, s50, 25600
LC_ 108
v_add_f32 v14, v14, v15
M 9,49,1
s_add_u32 s40, %[wlo], s53
v_add_f32_dpp v14, v14, v14 quad_perm:[1,0,3,2] row_mask:0xf bank_mask:0xf
HX 25
Z 10,60,0
WL 3
LO 105,17,105
LU 17,105
M 10,61,1
s_addc_u32 s41, %[whi], 0
RA 236,1,18432
HS 24,17
Z 11,72,0
LL 16,62
LL 109,63
M 11,73,1
s_mov_b32 m0, s38
LA 16
W1 25,1024
M 6,14,2
LC_ 109
DW 0
M 6,15,3
v_add_f32_dpp v14, v14, v14 quad_perm:[2,3,0,1] row_mask:0xf bank_mask:0xf
v_add_u32 v4, s39, v4
M 7,26,2
WL 4
LO 108,18,108
LU 18,108
M 7,27,3
s_add_u32 s37, s37, 32768
RA 216,1,19456
HS 24,18
M 8,38,2
s_cmp_eq_u32 s37, s75
LL 17,64
LL 112,65
M 8,39,3
s_cselect_b32 s37, s74, s37
LA 17
DW 1024
M 9,50,2
s_and_b32 s53, %[w], 1
LC_ 112
v_add_f32_dpp v14, v14, v14 row_half_mirror row_mask:0xf bank_mask:0xf
M 9,51,3
s_lshl_b32 s53, s53, 10
v_cvt_f64_f32 v[14:15], v14
M 10,62,2
WL 3
LO 109,16,109
LU 16,109
M 10,63,3
s_lshr_b32 s72, %[w], 1
RA 220,1,20480
HS 24,16
M 11,74,2
LL 18,66
LL 113,67
M 11,75,3
s_mul_i32 s72, s72, 32768
LA 18
s_mov_b64 exec, s[54:55]
SP
s_mov_b64 exec, -1
M 6,16,4
s_add_u32 s72, s72, s53
LC_ 113
M 6,17,5
s_add_u32 s72, s72, 128000
M 7,28,4
WL 3
LO 112,17,112
LU 17,112
M 7,29,5
s_movk_i32 s53, 0x78
RA 224,1,21504
HS 24,17
M 8,40,4
LL 16,68
LL 116,69
M 8,41,5
LA 16
v_lshrrev_b32 v12, 5, v0
M 9,52,4
LC_ 116
v_and_b32 v12, 6, v12
M 9,53,5
v_bfe_u32 v12, s53, v12, 2
M 10,64,4
WL 3
LO 113,18,113
LU 18,113
M 10,65,5
RA 228,1,22528
HS 24,18
v_lshrrev_b32 v20, 8, v0
v_xor_b32 v20, v12, v20
v_lshl_add_u32 v20, v20, 4, s72
v_and_b32 v12, 0xf0, v0
v_lshl_add_u32 v20, v12, 2, v20
v_add_u32 v12, s72, v0
M 11,76,4
LL 17,70
LL 117,71
M 11,77,5
LA 17
DW 2048
M 6,18,0
s_mul_i32 s53, s51, 49152
LC_ 117
M 6,19,1
s_bitcmp1_b64 s[90:91], s51
M 7,30,0
WL 3
LO 116,16,116
LU 16,116
M 7,31,1
s_cselect_b64 s[44:45], s[84:85], s[82:83]
RA 232,1,23552
HS 24,16
M 8,42,0
s_add_u32 s44, s44, s53
LL 18,72
LL 120,73
M 8,43,1
s_addc_u32 s45, s45, 0
LA 18
s_add_u32 s51, s51, %[d]
s_cmp_eq_u32 s51, %[wrap]
s_cselect_b32 s51, %[rst], s51
M 9,54,0
s_add_u32 s40, s40, 3072
LC_ 120
s_addc_u32 s41, s41, 0
s_add_u32 m0, s38, 3072
M 9,55,1
DW 0
M 10,66,0
WL 3
LO 117,17,117
LU 17,117
M 10,67,1
RA 236,1,24576
HS 24,17
M 11,78,0
LL 16,74
LL 121,75
M 11,79,1
LA 16
DW 1024
M 6,20,2
LC_ 121
DW 2048
M 6,21,3
s_lshl_b32 s53, s52, 8
v_add_u32 v1, s37, v0
M 7,32,2
WL 3
LO 120,18,120
LU 18,120
M 7,33,3
s_add_u32 s48, %[alo], s53
RA 216,1,0
HS 24,18
M 8,44,2
s_addc_u32 s49, %[ahi], 0
LL 17,76
LL 124,77
M 8,45,3
s_add_u32 s52, s52, %[d]
LA 17
v_and_b32 v22, 0xfffffcff, v1
M 9,56,2
s_cmp_eq_u32 s52, %[wrap]
LC_ 124
M 9,57,3
s_cselect_b32 s52, %[rst], s52
DQ 3072
M 10,68,2
WL 3
LO 121,16,121
LU 16,121
M 10,69,3
s_add_u32 s50, s50, %[d]
RA 220,1,1024
HS 24,16
M 11,80,2
LL 18,78
LL 125,79
M 11,81,3
s_cmp_eq_u32 s50, %[wrap]
LA 18
RV 252,22,5120
M 6,22,4
s_cselect_b32 s50, %[rst], s50
LC_ 125
M 6,23,5
WV 31
v_add_u32 v19, 1, v19
M 7,34,4
WL 4
LO 124,17,124
LU 17,124
M 7,35,5
s_add_u32 s38, s38, 32768
RA 224,1,2048
HS 24,17
M 8,46,4
s_cmp_eq_u32 s38, s77
LL 16,80
LL 106,81
M 8,47,5
s_cselect_b32 s38, s76, s38
LA 16
FP
M 9,58,4
s_sub_u32 s39, 0, s39
LC_ 106
M 9,59,5
M 10,70,4
WL 4
LO 125,18,125
LU 18,125
M 10,71,5
RA 228,1,3072
HS 24,18
M 11,82,4
LL 17,82
LL 107,83
M 11,83,5
LA 17
HX 24
Z 0,12,0
WV 22
LC_ 107
v_bcnt_u32_b32 v11, v11, 0
M 0,13,1
v_cmp_lt_u32 s[92:93], 2, v11
Z 1,24,0
WL 3
LO 16,16,106
LU 106,16
M 1,25,1
s_nop 0
RA 232,1,4096
HZ 25,106
Z 2,36,0
s_sub_u32 s53, 0, s96
LL 18,84
LL 110,85
M 2,37,1
s_xor_b32 s92, s92, s53
LA 18
PK 106,16,104,33
Z 3,48,0
s_xor_b32 s93, s93, s53
LC_ 110
PK 104,16,104,32
M 3,49,1
s_mul_i32 s53, %[d], 0
Z 4,60,0
WL 3
LO 17,17,107
LU 107,17
M 4,61,1
s_add_u32 s53, s53, s57
RA 236,1,6144
HS 25,107
Z 5,72,0
LL 16,86
LL 111,87
M 5,73,1
s_add_u32 s46, %[xlo], s53
LA 16
PK 107,17,105,33
M 0,14,2
s_addc_u32 s47, %[xhi], 0
LC_ 111
PK 105,17,105,32
M 0,15,3
s_bitcmp1_b64 s[92:93], 48
FR
M 1,26,2
WL 4
LO 18,18,110
LU 110,18
M 1,27,3
s_cselect_b32 s58, 16, 0
RA 216,1,7168
HS 25,110
M 2,38,2
s_lshr_b32 s58, 0xfe987610, s58
LL 17,88
LL 114,89
M 2,39,3
LA 17
PK 110,18,108,33
M 3,50,2
LC_ 114
PK 108,18,108,32
M 3,51,3
PO 110
v_lshrrev_b32 v3, 6, v0
M 4,62,2
WL 4
LO 16,16,111
LU 111,16
M 4,63,3
RA 220,1,8192
HS 25,111
v_and_b32 v3, 12, v3
v_bfe_u32 v3, s58, v3, 4
v_mad_u32_u24 v3, v3, s97, v21
M 5,74,2
LL 18,90
)"
R"(LL 115,91
M 5,75,3
s_add_u32 s42, s44, s60
LA 18
PK 111,16,109,33
M 0,16,4
s_addc_u32 s43, s45, 0
LC_ 115
PK 109,16,109,32
M 0,17,5
ST 104
PO 111
M 1,28,4
WL 3
LO 17,17,114
LU 114,17
M 1,29,5
RA 224,1,9216
HS 25,114
M 2,40,4
LL 16,92
LL 118,93
M 2,41,5
LA 16
XL 0,0
PK 114,17,112,33
M 3,52,4
s_mov_b32 m0, %[pm]
LC_ 118
PK 112,17,112,32
M 3,53,5
DF
v_cmp_gt_u32 vcc, v19, v15
M 4,64,4
WL 3
LO 18,18,115
LU 115,18
M 4,65,5
RA 228,1,10240
HS 25,115
M 5,76,4
LL 17,94
LL 119,95
M 5,77,5
s_cbranch_vccnz .Lk1s3
.Lk1r3:
LA 17
PK 115,18,113,33
M 0,18,0
LC_ 119
PK 113,18,113,32
M 0,19,1
XL 1,64
M 1,30,0
WL 3
LO 16,16,118
LU 118,16
M 1,31,1
s_add_u32 s42, s44, s61
RA 232,1,11264
HS 25,118
s_addc_u32 s43, s45, 0
M 2,42,0
LL 18,96
LL 122,97
M 2,43,1
LA 18
PK 118,16,116,33
ST 108
M 3,54,0
LC_ 122
PK 116,16,116,32
M 3,55,1
RV 252,22,5392
PO 118
M 4,66,0
WL 4
LO 17,17,119
LU 119,17
M 4,67,1
RA 236,1,12288
XL 2,128
HS 25,119
M 5,78,0
LL 16,98
LL 123,99
M 5,79,1
s_add_u32 s42, s44, s62
LA 16
PK 119,17,117,33
M 0,20,2
s_addc_u32 s43, s45, 0
LC_ 123
PK 117,17,117,32
M 0,21,3
W1 24,0
PO 119
M 1,32,2
WL 5
LO 18,18,122
LU 122,18
M 1,33,3
RA 216,1,13312
HS 25,122
M 2,44,2
LL 17,100
LL 126,101
ST 112
M 2,45,3
LA 17
XL 3,192
PK 122,18,120,33
M 3,56,2
LC_ 126
PK 120,18,120,32
M 3,57,3
XL 4,256
M 4,68,2
WL 4
LO 16,16,123
LU 123,16
M 4,69,3
s_add_u32 s42, s44, s63
RA 220,1,14336
HS 25,123
M 5,80,2
LL 18,102
LL 127,103
M 5,81,3
s_addc_u32 s43, s45, 0
LA 18
PK 123,16,121,33
M 0,22,4
LC_ 127
PK 121,16,121,32
M 0,23,5
ST 116
M 1,34,4
WL 3
LO 17,17,126
LU 126,17
M 1,35,5
RA 224,1,15360
HS 25,126
M 2,46,4
LL 16,56
LL 104,57
M 2,47,5
LA 16
PK 126,17,124,33
M 3,58,4
LC_ 104
PK 124,17,124,32
M 3,59,5
XL 5,320
PO 126
M 4,70,4
WL 3
LO 18,18,127
LU 127,18
M 4,71,5
s_add_u32 s42, s44, s64
RA 228,1,16384
HS 25,127
M 5,82,4
LL 17,58
LL 105,59
M 5,83,5
s_addc_u32 s43, s45, 0
LA 17
PK 127,18,125,33
Z 6,12,0
LC_ 105
PK 125,18,125,32
M 6,13,1
ST 120
PO 127
Z 7,24,0
WL 3
LO 104,16,104
LU 16,104
M 7,25,1
RA 232,1,17408
HZ 24,16
Z 8,36,0
LL 18,60
LL 108,61
M 8,37,1
LA 18
XL 6,384
Z 9,48,0
LC_ 108
ds_read_b64 v[14:15], v5 offset:0
M 9,49,1
XL 7,448
v_subrev_u32 v5, s39, v5
Z 10,60,0
WL 4
LO 105,17,105
LU 17,105
M 10,61,1
s_add_u32 s42, s44, s65
RA 236,1,18432
HS 24,17
Z 11,72,0
LL 16,62
LL 109,63
M 11,73,1
s_addc_u32 s43, s45, 0
LA 16
ST 124
M 6,14,2
s_mul_i32 s53, s50, 25600
LC_ 109
HX 25
M 6,15,3
s_add_u32 s40, %[wlo], s53
W1 25,1024
M 7,26,2
WL 5
LO 108,18,108
LU 18,108
M 7,27,3
s_addc_u32 s41, %[whi], 0
RA 216,1,19456
HS 24,18
M 8,38,2
s_mov_b32 m0, s38
LL 17,64
LL 112,65
M 8,39,3
LA 17
XL 8,512
M 9,50,2
WL 6
LC_ 112
DW 0
M 9,51,3
s_add_u32 s37, s37, 32768
v_add_f32 v14, v14, v15
s_nop 1
v_add_f32_dpp v14, v14, v14 quad_perm:[1,0,3,2] row_mask:0xf bank_mask:0xf
v_add_u32 v4, s39, v4
M 10,62,2
WL 4
LO 109,16,109
LU 16,109
M 10,63,3
s_cmp_eq_u32 s37, s75
RA 220,1,20480
HS 24,16
v_add_f32_dpp v14, v14, v14 quad_perm:[2,3,0,1] row_mask:0xf bank_mask:0xf
M 11,74,2
LL 18,66
LL 113,67
M 11,75,3
s_cselect_b32 s37, s74, s37
LA 18
XL 9,576
M 6,16,4
LC_ 113
DW 1024
v_add_f32_dpp v14, v14, v14 row_half_mirror row_mask:0xf bank_mask:0xf
M 6,17,5
XL 10,640
v_cvt_f64_f32 v[14:15], v14
M 7,28,4
WL 3
LO 112,17,112
LU 17,112
M 7,29,5
RA 224,1,21504
HS 24,17
M 8,40,4
LL 16,68
LL 116,69
M 8,41,5
LA 16
s_mov_b64 exec, s[54:55]
SP
s_mov_b64 exec, -1
M 9,52,4
LC_ 116
DW 2048
M 9,53,5
s_mul_i32 s53, s51, 49152
M 10,64,4
WL 3
LO 113,18,113
LU 18,113
M 10,65,5
s_bitcmp1_b64 s[90:91], s51
RA 228,1,22528
HS 24,18
M 11,76,4
LL 17,70
LL 117,71
M 11,77,5
s_cselect_b64 s[44:45], s[84:85], s[82:83]
LA 17
M 6,18,0
s_add_u32 s44, s44, s53
LC_ 117
M 6,19,1
s_addc_u32 s45, s45, 0
M 7,30,0
WL 3
LO 116,16,116
LU 16,116
M 7,31,1
s_add_u32 s51, s51, %[d]
RA 232,1,23552
HS 24,16
M 8,42,0
s_cmp_eq_u32 s51, %[wrap]
LL 18,72
LL 120,73
M 8,43,1
s_cselect_b32 s51, %[rst], s51
LA 18
M 9,54,0
s_add_u32 s40, s40, 3072
LC_ 120
s_addc_u32 s41, s41, 0
s_add_u32 m0, s38, 3072
M 9,55,1
XL 11,704
M 10,66,0
WL 3
LO 117,17,117
LU 17,117
M 10,67,1
RA 236,1,24576
HS 24,17
M 11,78,0
LL 16,74
LL 121,75
M 11,79,1
LA 16
DW 0
M 6,20,2
LC_ 121
DW 1024
M 6,21,3
DW 2048
v_add_u32 v1, s37, v0
M 7,32,2
WL 3
LO 120,18,120
LU 18,120
M 7,33,3
s_lshl_b32 s53, s52, 8
RA 216,1,0
HS 24,18
M 8,44,2
s_add_u32 s48, %[alo], s53
LL 17,76
LL 124,77
M 8,45,3
s_addc_u32 s49, %[ahi], 0
LA 17
v_and_b32 v22, 0xfffffcff, v1
M 9,56,2
s_add_u32 s52, s52, %[d]
LC_ 124
M 9,57,3
s_cmp_eq_u32 s52, %[wrap]
M 10,68,2
WL 3
LO 121,16,121
LU 16,121
M 10,69,3
s_cselect_b32 s52, %[rst], s52
RA 220,1,1024
HS 24,16
M 11,80,2
LL 18,78
LL 125,79
M 11,81,3
LA 18
RV 252,22,5120
M 6,22,4
LC_ 125
DQ 3072
M 6,23,5
s_add_u32 s50, s50, %[d]
M 7,34,4
WL 4
LO 124,17,124
LU 17,124
M 7,35,5
s_cmp_eq_u32 s50, %[wrap]
RA 224,1,2048
HS 24,17
M 8,46,4
s_cselect_b32 s50, %[rst], s50
LL 16,80
LL 106,81
M 8,47,5
LA 16
)"
R"(v_add_u32 v19, 1, v19
M 9,58,4
s_add_u32 s38, s38, 32768
LC_ 106
M 9,59,5
M 10,70,4
WL 3
LO 125,18,125
LU 18,125
M 10,71,5
RA 228,1,3072
HS 24,18
M 11,82,4
LL 17,82
LL 107,83
M 11,83,5
LA 17
HX 24
Z 0,12,0
s_cmp_eq_u32 s38, s77
LC_ 107
M 0,13,1
s_cselect_b32 s38, s76, s38
WV 42
FP
Z 1,24,0
WL 4
LO 16,16,106
LU 106,16
M 1,25,1
s_sub_u32 s39, 0, s39
RA 232,1,4096
HZ 25,106
Z 2,36,0
LL 18,84
LL 110,85
M 2,37,1
LA 18
PK 106,16,104,33
Z 3,48,0
LC_ 110
PK 104,16,104,32
M 3,49,1
Z 4,60,0
WL 4
LO 17,17,107
LU 107,17
M 4,61,1
RA 236,1,6144
HS 25,107
Z 5,72,0
LL 16,86
LL 111,87
M 5,73,1
s_add_u32 s42, s44, s60
LA 16
PK 107,17,105,33
M 0,14,2
s_addc_u32 s43, s45, 0
LC_ 111
PK 105,17,105,32
M 0,15,3
FR
M 1,26,2
WL 4
LO 18,18,110
LU 110,18
M 1,27,3
RA 216,1,7168
HS 25,110
M 2,38,2
LL 17,88
LL 114,89
M 2,39,3
LA 17
PK 110,18,108,33
M 3,50,2
LC_ 114
PK 108,18,108,32
M 3,51,3
ST 104
PO 110
M 4,62,2
WL 4
LO 16,16,111
LU 111,16
M 4,63,3
RA 220,1,8192
HS 25,111
M 5,74,2
LL 18,90
LL 115,91
M 5,75,3
s_mov_b32 m0, %[pm]
LA 18
PK 111,16,109,33
M 0,16,4
LC_ 115
PK 109,16,109,32
M 0,17,5
DF
PO 111
M 1,28,4
WL 3
LO 17,17,114
LU 114,17
M 1,29,5
s_add_u32 s42, s44, s61
RA 224,1,9216
HS 25,114
M 2,40,4
s_addc_u32 s43, s45, 0
LL 16,92
LL 118,93
M 2,41,5
LA 16
PK 114,17,112,33
M 3,52,4
LC_ 118
PK 112,17,112,32
M 3,53,5
ST 108
v_cmp_gt_u32 vcc, v19, v15
M 4,64,4
WL 3
LO 18,18,115
LU 115,18
M 4,65,5
RA 228,1,10240
HS 25,115
M 5,76,4
LL 17,94
LL 119,95
M 5,77,5
s_cbranch_vccnz .Lk1s4
.Lk1r4:
LA 17
PK 115,18,113,33
M 0,18,0
s_add_u32 s42, s44, s62
LC_ 119
PK 113,18,113,32
M 0,19,1
s_addc_u32 s43, s45, 0
W1 24,0
M 1,30,0
WL 4
LO 16,16,118
LU 118,16
M 1,31,1
RA 232,1,11264
HS 25,118
M 2,42,0
LL 18,96
LL 122,97
M 2,43,1
LA 18
PK 118,16,116,33
M 3,54,0
LC_ 122
PK 116,16,116,32
M 3,55,1
RV 252,22,5392
PO 118
M 4,66,0
WL 5
LO 17,17,119
LU 119,17
M 4,67,1
RA 236,1,12288
HS 25,119
M 5,78,0
LL 16,98
LL 123,99
M 5,79,1
LA 16
PK 119,17,117,33
M 0,20,2
WV 28
LC_ 123
PK 117,17,117,32
BW 0
M 0,21,3
ST 112
PO 119
M 1,32,2
WL 5
RV 40,8,6144
LO 18,18,122
LU 122,18
M 1,33,3
s_add_u32 s42, s44, s63
RA 216,1,13312
RV 44,8,6208
HS 25,122
M 2,44,2
s_addc_u32 s43, s45, 0
LL 17,100
LL 126,101
M 2,45,3
LA 17
RV 48,8,6272
PK 122,18,120,33
M 3,56,2
LC_ 126
BR 0
PK 120,18,120,32
M 3,57,3
WV 27
ST 116
BW 1
M 4,68,2
WL 9
LO 16,16,123
LU 123,16
M 4,69,3
WV 26
RA 220,1,14336
BR 1
HS 25,123
M 5,80,2
WL 3
LL 18,102
LL 127,103
M 5,81,3
s_add_u32 s42, s44, s64
LA 18
PK 123,16,121,33
LZ 40,0
s_addc_u32 s43, s45, 0
BW 2
M 0,22,4
LC_ 127
PK 121,16,121,32
M 0,23,5
ST 120
RV 40,8,6336
M 1,34,4
BR 2
LO 17,17,126
LU 126,17
M 1,35,5
WL 5
RA 224,1,15360
HS 25,126
LS 44,1
WV 25
BW 3
M 2,46,4
LL 16,56
LL 104,57
M 2,47,5
LA 16
PK 126,17,124,33
M 3,58,4
LC_ 104
RV 44,8,6400
PK 124,17,124,32
M 3,59,5
ds_read_b64 v[14:15], v5 offset:0
BR 3
PO 126
M 4,70,4
WL 9
LO 18,18,127
LU 127,18
M 4,71,5
WL 7
RA 228,1,16384
HS 25,127
LS 48,2
WV 24
BW 4
M 5,82,4
LL 17,58
LL 105,59
M 5,83,5
LA 17
RV 48,8,6464
PK 127,18,125,33
Z 6,12,0
s_add_u32 s42, s44, s65
LC_ 105
PK 125,18,125,32
M 6,13,1
s_addc_u32 s43, s45, 0
BR 4
PO 127
Z 7,24,0
WL 9
LO 104,16,104
LU 16,104
M 7,25,1
WL 6
RA 232,1,17408
HZ 24,16
LS 40,3
WV 22
BW 5
Z 8,36,0
WV 20
LL 18,60
LL 108,61
M 8,37,1
WL 2
LA 18
ST 124
RV 40,8,6528
Z 9,48,0
s_mul_i32 s53, s50, 25600
LC_ 108
BR 5
M 9,49,1
s_add_u32 s40, %[wlo], s53
v_subrev_u32 v5, s39, v5
HX 25
LS 44,4
s_addc_u32 s41, %[whi], 0
BW 6
Z 10,60,0
LO 105,17,105
LU 17,105
M 10,61,1
s_mov_b32 m0, s38
RA 236,1,18432
HS 24,17
Z 11,72,0
RV 44,8,6592
LL 16,62
LL 109,63
M 11,73,1
LA 16
W1 25,1024
BR 6
M 6,14,2
LC_ 109
DW 0
M 6,15,3
WL 7
v_add_f32 v14, v14, v15
v_add_u32 v4, s39, v4
LS 48,5
WV 21
BW 7
M 7,26,2
LO 108,18,108
LU 18,108
M 7,27,3
s_add_u32 s37, s37, 32768
RA 216,1,19456
HS 24,18
M 8,38,2
s_cmp_eq_u32 s37, s75
RV 48,8,6656
LL 17,64
LL 112,65
M 8,39,3
s_cselect_b32 s37, s74, s37
LA 17
DW 1024
BR 7
M 9,50,2
LC_ 112
v_add_f32_dpp v14, v14, v14 quad_perm:[1,0,3,2] row_mask:0xf bank_mask:0xf
M 9,51,3
WL 7
v_add_f32_dpp v14, v14, v14 quad_perm:[2,3,0,1] row_mask:0xf bank_mask:0xf
LS 40,6
WV 20
BW 8
M 10,62,2
WL 7
LO 109,16,109
LU 16,109
M 10,63,3
RA 220,1,20480
RV 40,8,6720
HS 24,16
M 11,74,2
BR 8
LL 18,66
LL 113,67
M 11,75,3
LA 18
DW 2048
M 6,16,4
LC_ 113
v_add_f32_dpp v14, v14, v14 row_half_mirror row_mask:0xf bank_mask:0xf
M 6,17,5
WL 7
v_cvt_f64_f32 v[14:15], v14
LS 44,7
WV 17
BW 9
M 7,28,4
WL 3
RV 44,8,6784
LO 112,17,112
LU 17,112
M 7,29,5
)"
R"(s_mul_i32 s53, s51, 49152
RA 224,1,21504
HS 24,17
M 8,40,4
s_bitcmp1_b64 s[90:91], s51
BR 9
LL 16,68
LL 116,69
M 8,41,5
s_cselect_b64 s[44:45], s[84:85], s[82:83]
LA 16
s_mov_b64 exec, s[54:55]
SP
s_mov_b64 exec, -1
LS 48,8
s_add_u32 s44, s44, s53
BW 10
M 9,52,4
s_addc_u32 s45, s45, 0
LC_ 116
RV 48,8,6848
M 9,53,5
s_add_u32 s51, s51, %[d]
RA 240,7,0
BR 10
M 10,64,4
WL 10
LO 113,18,113
LU 18,113
M 10,65,5
s_cmp_eq_u32 s51, %[wrap]
RA 228,1,22528
HS 24,18
M 11,76,4
WL 7
LL 17,70
LL 117,71
M 11,77,5
s_cselect_b32 s51, %[rst], s51
LA 17
M 6,18,0
s_add_u32 s40, s40, 3072
LC_ 117
M 6,19,1
s_addc_u32 s41, s41, 0
LS 40,9
s_add_u32 m0, s38, 3072
WV 15
BW 11
M 7,30,0
WL 7
RA 244,7,256
LO 116,16,116
LU 16,116
M 7,31,1
RA 232,1,23552
HS 24,16
M 8,42,0
BR 11
LL 18,72
LL 120,73
M 8,43,1
WL 7
LA 18
DW 0
LS 44,10
RA 248,7,512
M 9,54,0
LC_ 120
DW 1024
M 9,55,1
M 10,66,0
WL 7
LO 117,17,117
LU 17,117
M 10,67,1
WL 3
RA 236,1,24576
HS 24,17
LS 48,11
RA 252,7,768
M 11,78,0
LL 16,74
LL 121,75
s_nop 3
v_mul_f32 v36, 0xbb2aaaab, v36
M 11,79,1
LA 16
DW 2048
v_mul_f32 v37, 0xbb2aaaab, v37
M 6,20,2
LC_ 121
v_mul_f32 v38, 0xbb2aaaab, v38
v_mul_f32 v39, 0xbb2aaaab, v39
M 6,21,3
s_lshl_b32 s53, s52, 8
v_add_u32 v1, s37, v0
LK 40,240,0,0
LK 44,242,0,2
s_add_u32 s48, %[alo], s53
RA 240,7,1024
LK 48,244,1,0
s_nop 0
CF 128,40,41
LK 248,246,1,2
s_addc_u32 s49, %[ahi], 0
RA 244,7,1280
CF 129,42,43
M 7,32,2
WL 6
LO 120,18,120
LU 18,120
M 7,33,3
s_add_u32 s52, s52, %[d]
RA 216,1,0
HS 24,18
M 8,44,2
s_cmp_eq_u32 s52, %[wrap]
LL 17,76
LL 124,77
M 8,45,3
s_cselect_b32 s52, %[rst], s52
LA 17
DQ 3072
M 9,56,2
LC_ 124
v_and_b32 v22, 0xfffffcff, v1
CF 130,44,45
M 9,57,3
CF 131,46,47
LK 40,248,2,0
CF 132,48,49
LK 44,250,2,2
RA 248,7,1536
CF 133,50,51
GZ 0
WL 8
CF 134,248,249
CF 135,250,251
LK 48,252,3,0
CF 136,40,41
LK 248,254,3,2
RA 252,7,1792
CF 137,42,43
GS 1
WL 6
CF 138,44,45
CF 139,46,47
LK 40,240,4,0
CF 140,48,49
LK 44,242,4,2
RA 240,7,2048
CF 141,50,51
GS 2
WL 6
CF 142,248,249
CF 143,250,251
LK 48,244,5,0
CF 144,40,41
LK 248,246,5,2
RA 244,7,2304
CF 145,42,43
GS 3
CF 146,44,45
CF 147,46,47
M 10,68,2
WL 3
LO 121,16,121
LU 16,121
M 10,69,3
s_add_u32 s50, s50, %[d]
RA 220,1,1024
HS 24,16
LK 40,248,6,0
WL 3
CF 148,48,49
LK 44,250,6,2
s_cmp_eq_u32 s50, %[wrap]
RA 248,7,2560
CF 149,50,51
GS 4
s_cselect_b32 s50, %[rst], s50
CF 150,248,249
CF 151,250,251
LK 48,252,7,0
CF 152,40,41
LK 248,254,7,2
RA 252,7,2816
CF 153,42,43
GS 5
WL 4
CF 154,44,45
CF 155,46,47
LK 40,240,8,0
CF 156,48,49
LK 44,242,8,2
CF 157,50,51
GS 6
CF 158,248,249
CF 159,250,251
M 11,80,2
LL 18,78
LL 125,79
M 11,81,3
WL 3
LA 18
RV 252,22,5120
LK 48,244,9,0
CF 160,40,41
LK 248,246,9,2
CF 161,42,43
GS 7
CF 162,44,45
CF 163,46,47
M 6,22,4
LC_ 125
M 6,23,5
v_add_u32 v19, 1, v19
M 7,34,4
WL 4
LO 124,17,124
LU 17,124
M 7,35,5
s_add_u32 s38, s38, 32768
RA 224,1,2048
HS 24,17
M 8,46,4
s_cmp_eq_u32 s38, s77
LL 16,80
LL 106,81
M 8,47,5
s_cselect_b32 s38, s76, s38
LA 16
FP
LK 40,248,10,0
CF 164,48,49
LK 44,250,10,2
s_sub_u32 s39, 0, s39
CF 165,50,51
GS 8
CF 166,248,249
CF 167,250,251
M 9,58,4
LC_ 106
M 9,59,5
M 10,70,4
WL 4
LO 125,18,125
LU 18,125
M 10,71,5
RA 228,1,3072
HS 24,18
LK 48,252,11,0
CF 168,40,41
LK 248,254,11,2
CF 169,42,43
GS 9
CF 170,44,45
CF 171,46,47
M 11,82,4
LL 17,82
LL 107,83
M 11,83,5
LA 17
HX 24
Z 0,12,0
LC_ 107
RV 36,8,6912
M 0,13,1
GS 10
CF 172,48,49
Z 1,24,0
WL 4
LO 16,16,106
LU 106,16
M 1,25,1
RA 232,1,4096
HZ 25,106
Z 2,36,0
LL 18,84
LL 110,85
M 2,37,1
LA 18
PK 106,16,104,33
Z 3,48,0
LC_ 110
PK 104,16,104,32
M 3,49,1
CF 173,50,51
CF 174,248,249
Z 4,60,0
WL 4
LO 17,17,107
LU 107,17
M 4,61,1
RA 236,1,6144
HS 25,107
Z 5,72,0
LL 16,86
LL 111,87
M 5,73,1
s_add_u32 s42, s44, s60
LA 16
PK 107,17,105,33
M 0,14,2
s_addc_u32 s43, s45, 0
LC_ 111
PK 105,17,105,32
M 0,15,3
FR
CF 175,250,251
M 1,26,2
WL 4
LO 18,18,110
LU 110,18
M 1,27,3
RA 216,1,7168
HS 25,110
GS 11
ST 104
M 2,38,2
LL 17,88
LL 114,89
M 2,39,3
LA 17
PK 110,18,108,33
M 3,50,2
LC_ 114
PK 108,18,108,32
M 3,51,3
PO 110
v_cndmask_b32 v52, v52, v53, s[78:79]
M 4,62,2
WL 4
LO 16,16,111
LU 111,16
M 4,63,3
RA 220,1,8192
HS 25,111
M 5,74,2
LL 18,90
LL 115,91
M 5,75,3
s_mov_b32 m0, %[pm]
LA 18
PK 111,16,109,33
M 0,16,4
LC_ 115
PK 109,16,109,32
M 0,17,5
DF
PO 111
M 1,28,4
WL 3
LO 17,17,114
LU 114,17
M 1,29,5
s_add_u32 s42, s44, s61
RA 224,1,9216
HS 25,114
M 2,40,4
s_addc_u32 s43, s45, 0
LL 16,92
LL 118,93
M 2,41,5
LA 16
PK 114,17,112,33
M 3,52,4
LC_ 118
PK 112,17,112,32
M 3,53,5
ST 108
v_cmp_gt_u32 vcc, v19, v15
M 4,64,4
WL 3
LO 18,18,115
LU 115,18
M 4,65,5
RA 228,1,10240
HS 25,115
M 5,76,4
LL 17,94
LL 119,95
M 5,77,5
s_cbranch_vccnz .Lk1s5
.Lk1r5:
LA 17
PK 115,18,113,33
M 0,18,0
s_add_u32 s42, s44, s62
LC_ 119
PK 113,18,113,32
v_cndmask_b32 v54, v54, v55, s[78:79]
M 0,19,1
s_addc_u32 s43, s45, 0
W1 24,0
v_cndmask_b32 v52, v52, v54, s[80:81]
M 1,30,0
WL 4
LO 16,16,118
)"
R"(LU 118,16
v_mul_f32 v52, 0x3b2aaaab, v52
M 1,31,1
RA 232,1,11264
HS 25,118
v_med3_f32 v53, v52, v39, v37
M 2,42,0
LL 18,96
LL 122,97
LG 52,53
M 2,43,1
LA 18
PK 118,16,116,33
C 52,52,52
M 3,54,0
LC_ 122
PK 116,16,116,32
v_lshlrev_b32 v52, 16, v52
M 3,55,1
RV 252,22,5392
PO 118
v_add_f32 v52, v36, v52
M 4,66,0
WL 5
LO 17,17,119
LU 119,17
C 52,52,52
M 4,67,1
RA 236,1,12288
HS 25,119
M 5,78,0
LL 16,98
LL 123,99
v_lshlrev_b32 v52, 16, v52
M 5,79,1
LA 16
PK 119,17,117,33
v_sqrt_f32 v52, v52
M 0,20,2
LC_ 123
PK 117,17,117,32
C 52,52,52
M 0,21,3
ST 112
PO 119
v_lshlrev_b32 v52, 16, v52
M 1,32,2
WL 4
LO 18,18,122
LU 122,18
v_rcp_f32 v52, v52
M 1,33,3
s_add_u32 s42, s44, s63
RA 216,1,13312
HS 25,122
v_mul_f32 v52, v38, v52
M 2,44,2
s_addc_u32 s43, s45, 0
LL 17,100
LL 126,101
CF 52,52,52
M 2,45,3
LA 17
PK 122,18,120,33
ds_bpermute_b32 v13, v28, v52
M 3,56,2
LC_ 126
PK 120,18,120,32
M 3,57,3
ST 116
M 4,68,2
WL 4
LO 16,16,123
LU 123,16
M 4,69,3
RA 220,1,14336
HS 25,123
M 5,80,2
LL 18,102
LL 127,103
M 5,81,3
s_add_u32 s42, s44, s64
LA 18
PK 123,16,121,33
M 0,22,4
s_addc_u32 s43, s45, 0
LC_ 127
PK 121,16,121,32
M 0,23,5
ST 120
M 1,34,4
WL 3
LO 17,17,126
LU 126,17
M 1,35,5
RA 224,1,15360
HS 25,126
XS 128
M 2,46,4
LL 16,56
LL 104,57
M 2,47,5
LA 16
PK 126,17,124,33
M 3,58,4
LC_ 104
PK 124,17,124,32
XS 129
M 3,59,5
ds_read_b64 v[14:15], v5 offset:0
PO 126
M 4,70,4
WL 4
LO 18,18,127
LU 127,18
M 4,71,5
RA 228,1,16384
HS 25,127
XS 130
M 5,82,4
LL 17,58
LL 105,59
M 5,83,5
LA 17
PK 127,18,125,33
Z 6,12,0
s_add_u32 s42, s44, s65
LC_ 105
PK 125,18,125,32
XS 131
M 6,13,1
s_addc_u32 s43, s45, 0
PO 127
v_subrev_u32 v5, s39, v5
Z 7,24,0
WL 4
LO 104,16,104
LU 16,104
XS 132
M 7,25,1
RA 232,1,17408
HZ 24,16
Z 8,36,0
LL 18,60
LL 108,61
M 8,37,1
WL 4
LA 18
ST 124
XS 133
Z 9,48,0
s_mul_i32 s53, s50, 25600
LC_ 108
v_add_f32 v14, v14, v15
M 9,49,1
s_add_u32 s40, %[wlo], s53
v_add_f32_dpp v14, v14, v14 quad_perm:[1,0,3,2] row_mask:0xf bank_mask:0xf
HX 25
Z 10,60,0
WL 3
LO 105,17,105
LU 17,105
XS 134
M 10,61,1
s_addc_u32 s41, %[whi], 0
RA 236,1,18432
HS 24,17
Z 11,72,0
LL 16,62
LL 109,63
M 11,73,1
s_mov_b32 m0, s38
LA 16
W1 25,1024
XS 135
M 6,14,2
LC_ 109
DW 0
M 6,15,3
v_add_f32_dpp v14, v14, v14 quad_perm:[2,3,0,1] row_mask:0xf bank_mask:0xf
v_add_u32 v4, s39, v4
v_min_u32 v35, v35, v13
M 7,26,2
WL 4
LO 108,18,108
LU 18,108
M 7,27,3
s_add_u32 s37, s37, 32768
RA 216,1,19456
HS 24,18
M 8,38,2
s_cmp_eq_u32 s37, s75
LL 17,64
LL 112,65
XS 136
M 8,39,3
s_cselect_b32 s37, s74, s37
LA 17
v_add_f32_dpp v14, v14, v14 row_half_mirror row_mask:0xf bank_mask:0xf
M 9,50,2
s_mul_i32 s53, %[d], 0
LC_ 112
v_cvt_f64_f32 v[14:15], v14
M 9,51,3
s_add_u32 s53, s53, s57
s_mov_b64 exec, s[54:55]
SP
s_mov_b64 exec, -1
XS 137
M 10,62,2
WL 3
LO 109,16,109
LU 16,109
M 10,63,3
s_add_u32 s46, %[xlo], s53
RA 220,1,20480
HS 24,16
M 11,74,2
LL 18,66
LL 113,67
XS 138
M 11,75,3
s_addc_u32 s47, %[xhi], 0
LA 18
XS 139
M 6,16,4
s_bitcmp1_b64 s[92:93], 48
LC_ 113
XS 140
M 6,17,5
s_cselect_b32 s58, 16, 0
XS 141
XS 142
M 7,28,4
WL 3
LO 112,17,112
LU 17,112
M 7,29,5
s_lshr_b32 s58, 0xda43cb52, s58
RA 224,1,21504
HS 24,17
M 8,40,4
LL 16,68
LL 116,69
M 8,41,5
LA 16
v_lshrrev_b32 v3, 6, v0
M 9,52,4
LC_ 116
v_and_b32 v3, 12, v3
v_bfe_u32 v3, s58, v3, 4
v_mad_u32_u24 v3, v3, s97, v21
M 9,53,5
DW 1024
XS 143
M 10,64,4
WL 3
LO 113,18,113
LU 18,113
M 10,65,5
RA 228,1,22528
HS 24,18
M 11,76,4
LL 17,70
LL 117,71
M 11,77,5
LA 17
XL 12,0
M 6,18,0
LC_ 117
DW 2048
M 6,19,1
XL 13,64
XS 144
M 7,30,0
WL 3
LO 116,16,116
LU 16,116
M 7,31,1
s_mul_i32 s53, s51, 49152
RA 232,1,23552
HS 24,16
M 8,42,0
s_bitcmp1_b64 s[90:91], s51
LL 18,72
LL 120,73
M 8,43,1
s_cselect_b64 s[44:45], s[84:85], s[82:83]
LA 18
s_add_u32 s44, s44, s53
s_addc_u32 s45, s45, 0
s_add_u32 s51, s51, %[d]
s_cmp_eq_u32 s51, %[wrap]
s_cselect_b32 s51, %[rst], s51
XS 145
M 9,54,0
s_add_u32 s40, s40, 3072
LC_ 120
s_addc_u32 s41, s41, 0
s_add_u32 m0, s38, 3072
M 9,55,1
XL 14,128
XS 146
M 10,66,0
WL 3
LO 117,17,117
LU 17,117
M 10,67,1
RA 236,1,24576
HS 24,17
M 11,78,0
LL 16,74
LL 121,75
XS 147
M 11,79,1
LA 16
DW 0
M 6,20,2
LC_ 121
XL 15,192
M 6,21,3
DW 1024
v_add_u32 v1, s37, v0
XS 148
M 7,32,2
WL 3
LO 120,18,120
LU 18,120
M 7,33,3
RA 216,1,0
HS 24,18
M 8,44,2
LL 17,76
LL 124,77
XS 149
M 8,45,3
LA 17
XL 16,256
M 9,56,2
LC_ 124
DW 2048
XS 150
M 9,57,3
XL 17,320
v_and_b32 v22, 0xfffffcff, v1
M 10,68,2
WL 3
LO 121,16,121
LU 16,121
M 10,69,3
s_lshl_b32 s53, s52, 8
RA 220,1,1024
HS 24,16
XS 151
M 11,80,2
LL 18,78
LL 125,79
M 11,81,3
s_add_u32 s48, %[alo], s53
LA 18
RV 252,22,5120
M 6,22,4
s_addc_u32 s49, %[ahi], 0
LC_ 125
XS 152
M 6,23,5
s_add_u32 s52, s52, %[d]
XS 153
XS 154
M 7,34,4
WL 4
LO 124,17,124
LU 17,124
M 7,35,5
s_cmp_eq_u32 s52, %[wrap]
RA 224,1,2048
HS 24,17
M 8,46,4
s_cselect_b32 s52, %[rst], s52
LL 16,80
LL 106,81
M 8,47,5
LA 16
DQ 3072
M 9,58,4
LC_ 106
XL 18,384
M 9,59,5
s_add_u32 s50, s50, %[d]
XS 155
XS 156
M 10,70,4
WL 3
LO 125,18,125
LU 18,125
M 10,71,5
RA 228,1,3072
HS 24,18
M 11,82,4
LL 17,82
)"
R"(LL 107,83
M 11,83,5
LA 17
HX 24
s_cmp_eq_u32 s50, %[wrap]
s_cselect_b32 s50, %[rst], s50
XL 19,448
Z 2,36,0
WV 38
LC_ 107
XL 20,512
v_add_u32 v19, 1, v19
s_add_u32 s38, s38, 32768
s_cmp_eq_u32 s38, s77
s_cselect_b32 s38, s76, s38
FP
s_sub_u32 s39, 0, s39
M 2,37,1
XL 21,576
XS 157
Z 3,48,0
WL 4
LO 16,16,106
LU 106,16
M 3,49,1
RA 232,1,4096
HZ 25,106
Z 4,60,0
LL 18,84
LL 110,85
XS 158
M 4,61,1
LA 18
PK 106,16,104,33
Z 5,72,0
WL 5
LC_ 110
XL 22,640
PK 104,16,104,32
M 5,73,1
RA 236,1,6144
FR
XS 159
M 2,38,2
WL 6
LO 17,17,107
LU 107,17
M 2,39,3
XL 23,704
HS 25,107
M 3,50,2
LL 16,86
LL 111,87
XS 160
M 3,51,3
s_add_u32 s42, s44, s60
LA 16
PK 107,17,105,33
M 4,62,2
s_addc_u32 s43, s45, 0
LC_ 111
PK 105,17,105,32
XS 161
M 4,63,3
RA 216,1,7168
XS 162
M 5,74,2
WL 4
LO 18,18,110
LU 110,18
M 5,75,3
RA 220,1,8192
HS 25,110
M 2,40,4
LL 17,88
LL 114,89
M 2,41,5
LA 17
PK 110,18,108,33
M 3,52,4
LC_ 114
PK 108,18,108,32
XS 163
M 3,53,5
RA 224,1,9216
PO 110
M 4,64,4
WL 5
LO 16,16,111
LU 111,16
ST 104
XS 164
M 4,65,5
HS 25,111
v_cmp_gt_u32 vcc, v19, v15
M 5,76,4
WL 3
LL 18,90
LL 115,91
M 5,77,5
WV 17
LA 18
PK 111,16,109,33
XS 165
M 2,42,0
s_cbranch_vccnz .Lk1s6
.Lk1r6:
LC_ 115
PK 109,16,109,32
M 2,43,1
s_add_u32 s42, s44, s61
RA 228,1,10240
PO 111
BW 12
M 3,54,0
WL 5
LO 17,17,114
LU 114,17
XS 166
M 3,55,1
s_addc_u32 s43, s45, 0
RA 232,1,11264
RV 40,8,6144
HS 25,114
M 4,66,0
RV 44,8,6208
LL 16,92
LL 118,93
M 4,67,1
LA 16
RV 48,8,6272
PK 114,17,112,33
XS 167
M 5,78,0
WL 6
LC_ 118
BR 12
PK 112,17,112,32
M 5,79,1
WV 15
RV 252,22,5392
ds_read_b64 v[14:15], v5 offset:0
W1 24,0
BW 13
XS 168
M 2,44,2
BR 13
LO 18,18,115
LU 115,18
M 2,45,3
WL 5
RA 236,1,12288
HS 25,115
ST 108
LZ 40,12
WV 15
BW 14
M 3,56,2
LL 17,94
LL 119,95
XS 169
M 3,57,3
s_add_u32 s42, s44, s62
LA 17
RV 40,8,6336
PK 115,18,113,33
M 4,68,2
s_addc_u32 s43, s45, 0
LC_ 119
BR 14
PK 113,18,113,32
M 4,69,3
WL 6
RA 216,1,13312
v_subrev_u32 v5, s39, v5
XS 170
LS 44,13
WV 13
BW 15
M 5,80,2
WL 7
LO 16,16,118
LU 118,16
M 5,81,3
RA 220,1,14336
RV 44,8,6400
HS 25,118
M 2,46,4
BR 15
LL 18,96
LL 122,97
XS 171
M 2,47,5
WL 5
LA 18
PK 118,16,116,33
LS 48,14
WV 11
BW 16
M 3,58,4
LC_ 122
PK 116,16,116,32
XS 172
M 3,59,5
RA 224,1,15360
RV 48,8,6464
PO 118
M 4,70,4
LO 17,17,119
LU 119,17
ST 112
M 4,71,5
BR 16
HS 25,119
XS 173
M 5,82,4
WL 8
LL 16,98
LL 123,99
M 5,83,5
WL 6
LA 16
PK 119,17,117,33
LS 40,15
WV 10
BW 17
Z 8,36,0
s_add_u32 s42, s44, s63
LC_ 123
PK 117,17,117,32
v_add_f32 v14, v14, v15
s_nop 1
v_add_f32_dpp v14, v14, v14 quad_perm:[1,0,3,2] row_mask:0xf bank_mask:0xf
s_nop 1
v_add_f32_dpp v14, v14, v14 quad_perm:[2,3,0,1] row_mask:0xf bank_mask:0xf
s_nop 1
v_add_f32_dpp v14, v14, v14 row_half_mirror row_mask:0xf bank_mask:0xf
v_cvt_f64_f32 v[14:15], v14
s_mov_b64 exec, s[54:55]
SP
s_mov_b64 exec, -1
RV 40,8,6528
XS 174
M 8,37,1
s_addc_u32 s43, s45, 0
RA 228,1,16384
BR 17
PO 119
Z 9,48,0
WL 9
LO 18,18,122
LU 122,18
XS 175
M 9,49,1
WL 6
RA 232,1,17408
HS 25,122
LS 44,16
WV 9
BW 18
Z 10,60,0
RV 44,8,6592
LL 17,100
LL 126,101
M 10,61,1
LA 17
BR 18
PK 122,18,120,33
Z 0,72,0
WL 6
LC_ 126
PK 120,18,120,32
M 0,73,1
WL 6
RA 236,1,18432
LS 48,17
WV 8
BW 19
M 8,38,2
RV 48,8,6656
LO 16,16,123
LU 123,16
M 8,39,3
ST 116
HS 25,123
s_mul_i32 s53, %[d], 196608
s_add_u32 s53, s53, s57
s_add_u32 s46, %[xlo], s53
s_addc_u32 s47, %[xhi], 0
s_bitcmp1_b64 s[92:93], 48
s_cselect_b32 s58, 16, 0
s_lshr_b32 s58, 0xfe987610, s58
v_lshrrev_b32 v3, 6, v0
v_and_b32 v3, 12, v3
v_bfe_u32 v3, s58, v3, 4
BR 19
v_mad_u32_u24 v3, v3, s97, v21
M 9,50,2
s_mul_i32 s53, s50, 25600
LL 18,102
LL 127,103
s_add_u32 s40, %[wlo], s53
s_addc_u32 s41, %[whi], 0
s_mov_b32 m0, s38
M 9,51,3
WL 5
LA 18
PK 123,16,121,33
LS 40,18
WV 8
BW 20
M 10,62,2
LC_ 127
RV 40,8,6720
PK 121,16,121,32
M 10,63,3
WV 7
RA 216,1,19456
BR 20
DW 0
M 0,74,2
WL 6
LO 17,17,126
LU 126,17
M 0,75,3
s_add_u32 s37, s37, 32768
RA 220,1,20480
HS 25,126
LS 44,19
s_cmp_eq_u32 s37, s75
BW 21
M 8,40,4
s_cselect_b32 s37, s74, s37
LL 16,64
LL 112,65
M 8,41,5
LA 16
XL 24,0
DW 1024
RV 44,8,6784
PK 126,17,124,33
M 9,52,4
s_add_u32 s42, s44, s64
LC_ 112
PK 124,17,124,32
s_addc_u32 s43, s45, 0
BR 21
M 9,53,5
WL 6
RA 224,1,21504
PO 126
LS 48,20
WV 9
BW 22
M 10,64,4
LO 18,18,127
LU 127,18
ST 120
M 10,65,5
DW 2048
RV 48,8,6848
HS 25,127
M 0,76,4
WL 8
RA 240,7,0
LL 17,66
LL 113,67
M 0,77,5
s_add_u32 s40, s40, 3072
LA 17
BR 22
PK 127,18,125,33
M 8,42,0
s_addc_u32 s41, s41, 0
LC_ 113
PK 125,18,125,32
s_add_u32 m0, s38, 3072
M 8,43,1
WL 7
RA 228,1,22528
XL 25,64
PO 127
LS 40,21
WV 11
BW 23
M 9,54,0
RA 244,7,256
LO 112,16,112
LU 16,112
M 9,55,1
RA 232,1,23552
BR 23
HZ 24,16
M 10,66,0
WL 6
DW 0
LL 18,68
LL 116,69
M 10,67,1
s_add_u32 s42, s44, s65
)"
R"(LA 18
HX 25
LS 44,22
s_addc_u32 s43, s45, 0
RA 248,7,512
M 0,78,0
WL 2
LC_ 116
W1 25,1024
M 0,79,1
s_lshl_b32 s53, s52, 8
RA 236,1,24576
v_add_u32 v4, s39, v4
LS 48,23
s_add_u32 s48, %[alo], s53
RA 252,7,768
M 8,44,2
LO 113,17,113
LU 17,113
s_addc_u32 s49, %[ahi], 0
s_add_u32 s52, s52, %[d]
s_cmp_eq_u32 s52, %[wrap]
s_cselect_b32 s52, %[rst], s52
DW 1024
v_mul_f32 v36, 0xbb2aaaab, v36
M 8,45,3
ST 124
HS 24,17
v_mul_f32 v37, 0xbb2aaaab, v37
M 9,56,2
s_mul_i32 s53, s51, 49152
LL 16,70
LL 117,71
s_bitcmp1_b64 s[90:91], s51
s_cselect_b64 s[44:45], s[84:85], s[82:83]
s_add_u32 s44, s44, s53
s_addc_u32 s45, s45, 0
s_add_u32 s51, s51, %[d]
s_cmp_eq_u32 s51, %[wrap]
s_cselect_b32 s51, %[rst], s51
XL 26,128
v_mul_f32 v38, 0xbb2aaaab, v38
v_mul_f32 v39, 0xbb2aaaab, v39
M 9,57,3
LA 16
DW 2048
LK 40,240,12,0
LK 44,242,12,2
RA 240,7,1024
v_add_u32 v1, s37, v0
LK 48,244,13,0
s_nop 0
CF 176,40,41
LK 248,246,13,2
RA 244,7,1280
CF 177,42,43
M 10,68,2
LC_ 117
RA 216,1,0
M 10,69,3
DQ 3072
v_and_b32 v22, 0xfffffcff, v1
M 0,80,2
WL 6
LO 116,18,116
LU 18,116
M 0,81,3
RA 220,1,1024
HS 24,18
CF 178,44,45
M 8,46,4
LL 17,72
LL 120,73
CF 179,46,47
M 8,47,5
LA 17
RV 252,22,5120
LK 40,248,14,0
CF 180,48,49
LK 44,250,14,2
RA 248,7,1536
CF 181,50,51
GZ 12
WL 9
CF 182,248,249
CF 183,250,251
LK 48,252,15,0
CF 184,40,41
LK 248,254,15,2
RA 252,7,1792
CF 185,42,43
GS 13
WL 8
CF 186,44,45
CF 187,46,47
LK 40,240,16,0
CF 188,48,49
LK 44,242,16,2
RA 240,7,2048
CF 189,50,51
GS 14
WL 8
CF 190,248,249
CF 191,250,251
LK 48,244,17,0
CF 192,40,41
LK 248,246,17,2
RA 244,7,2304
CF 193,42,43
GS 15
CF 194,44,45
CF 195,46,47
M 9,58,4
WL 3
LC_ 120
XL 27,192
M 9,59,5
s_add_u32 s50, s50, %[d]
RA 224,1,2048
s_cmp_eq_u32 s50, %[wrap]
s_cselect_b32 s50, %[rst], s50
v_add_u32 v19, 1, v19
LK 40,248,18,0
WL 4
CF 196,48,49
LK 44,250,18,2
s_add_u32 s38, s38, 32768
RA 248,7,2560
CF 197,50,51
GS 16
s_cmp_eq_u32 s38, s77
CF 198,248,249
CF 199,250,251
LK 48,252,19,0
CF 200,40,41
LK 248,254,19,2
s_cselect_b32 s38, s76, s38
RA 252,7,2816
CF 201,42,43
GS 17
WL 5
CF 202,44,45
CF 203,46,47
LK 40,240,20,0
CF 204,48,49
LK 44,242,20,2
CF 205,50,51
GS 18
CF 206,248,249
CF 207,250,251
M 10,70,4
LO 117,16,117
LU 16,117
M 10,71,5
FP
HS 24,16
M 0,82,4
LL 18,74
LL 121,75
M 0,83,5
s_sub_u32 s39, 0, s39
LA 18
RA 228,1,3072
Z 2,36,0
LC_ 121
XL 28,256
M 2,37,1
WL 8
FR
LK 48,244,21,0
CF 208,40,41
LK 248,246,21,2
CF 209,42,43
GS 19
CF 210,44,45
CF 211,46,47
Z 3,48,0
WL 8
LO 120,17,120
LU 17,120
M 3,49,1
WL 6
RA 232,1,4096
HS 24,17
LK 40,248,22,0
CF 212,48,49
LK 44,250,22,2
CF 213,50,51
GS 20
CF 214,248,249
CF 215,250,251
Z 4,60,0
LL 16,76
LL 124,77
M 4,61,1
LA 16
XL 29,320
Z 6,72,0
WL 4
LC_ 124
M 6,73,1
RA 236,1,6144
LK 48,252,23,0
CF 216,40,41
LK 248,254,23,2
CF 217,42,43
GS 21
CF 218,44,45
CF 219,46,47
M 2,38,2
WL 5
LO 121,18,121
LU 18,121
M 2,39,3
RV 36,8,6912
HS 24,18
M 3,50,2
LL 17,78
LL 125,79
M 3,51,3
LA 17
RA 216,1,7168
GS 22
WL 7
v_cmp_gt_u32 vcc, v19, v15
M 4,62,2
LC_ 125
CF 220,48,49
M 4,63,3
XL 30,384
CF 221,50,51
M 6,74,2
WL 4
LO 124,16,124
LU 16,124
M 6,75,3
s_cbranch_vccnz .Lk1s7
.Lk1r7:
RA 220,1,8192
HS 24,16
M 2,40,4
LL 18,88
LL 114,89
M 2,41,5
LA 18
ds_read_b64 v[14:15], v5 offset:0
M 3,52,4
LC_ 114
v_subrev_u32 v5, s39, v5
M 3,53,5
RA 224,1,9216
CF 222,248,249
M 4,64,4
WL 5
LO 125,17,125
LU 17,125
M 4,65,5
XL 31,448
HS 24,17
M 6,76,4
WL 4
LL 16,90
LL 115,91
M 6,77,5
LA 16
RA 228,1,10240
M 2,42,0
LC_ 115
HX 24
M 2,43,1
W1 24,0
CF 223,250,251
M 3,54,0
WL 5
LO 18,18,114
LU 114,18
M 3,55,1
RA 232,1,11264
HZ 25,114
GS 23
v_add_f32 v14, v14, v15
M 4,66,0
LL 17,92
LL 118,93
M 4,67,1
LA 17
PK 114,18,112,33
M 6,78,0
WL 4
LC_ 118
PK 112,18,112,32
M 6,79,1
RV 252,22,5392
v_add_f32_dpp v14, v14, v14 quad_perm:[1,0,3,2] row_mask:0xf bank_mask:0xf
M 2,44,2
WL 5
LO 16,16,115
LU 115,16
M 2,45,3
RA 236,1,12288
HS 25,115
M 3,56,2
LL 18,94
LL 119,95
v_add_f32_dpp v14, v14, v14 quad_perm:[2,3,0,1] row_mask:0xf bank_mask:0xf
M 3,57,3
LA 18
PK 115,16,113,33
M 4,68,2
LC_ 119
XL 32,512
PK 113,16,113,32
v_add_f32_dpp v14, v14, v14 row_half_mirror row_mask:0xf bank_mask:0xf
M 4,69,3
s_add_u32 s42, s44, s62
RA 216,1,13312
v_cvt_f64_f32 v[14:15], v14
M 6,80,2
WL 3
LO 17,17,118
LU 118,17
M 6,81,3
s_addc_u32 s43, s45, 0
RA 220,1,14336
HS 25,118
v_cndmask_b32 v52, v52, v53, s[78:79]
M 2,46,4
LL 16,96
LL 122,97
s_mov_b64 exec, s[54:55]
SP
s_mov_b64 exec, -1
v_cndmask_b32 v54, v54, v55, s[78:79]
M 2,47,5
LA 16
PK 118,17,116,33
v_cndmask_b32 v52, v52, v54, s[80:81]
M 3,58,4
LC_ 122
PK 116,17,116,32
v_mul_f32 v52, 0x3b2aaaab, v52
M 3,59,5
RA 224,1,15360
PO 118
v_med3_f32 v53, v52, v39, v37
M 4,70,4
WL 5
LO 18,18,119
LU 119,18
ST 112
XL 33,576
LG 52,53
s_nop 0
C 52,52,52
M 4,71,5
HS 25,119
v_lshlrev_b32 v52, 16, v52
M 6,82,4
WL 3
LL 17,98
LL 123,99
v_add_f32 v52, v36, v52
M 6,83,5
LA 17
PK 119,18,117,33
C 52,52,52
Z 8,36,0
LC_ 123
PK 117,18,117,32
v_lshlrev_b32 v52, 16, v52
M 8,37,1
RA 228,1,16384
PO 119
v_sqrt_f32 v52, v52
s_nop 0
C 52,52,52
Z 9,48,0
WL 4
LO 16,16,122
LU 122,16
v_lshlrev_b32 v52, 16, v52
M 9,49,1
RA 232,1,17408
HS 25,122
v_rcp_f32 v52, v52
Z 10,60,0
XL 34,640
LL 18,56
LL 126,57
v_mul_f32 v52, v38, v52
M 10,61,1
s_add_u32 s42, s44, s63
LA 18
PK 122,16,120,33
CF 52,52,52
Z 11,72,0
WL 2
LC_ 126
PK 120,16,120,32
ds_bpermute_b32 v13, v28, v52
)"
R"(M 11,73,1
s_addc_u32 s43, s45, 0
RA 236,1,18432
M 8,38,2
LO 17,17,123
LU 123,17
M 8,39,3
ST 116
HS 25,123
M 9,50,2
s_mul_i32 s53, s50, 25600
LL 16,58
LL 127,59
s_add_u32 s40, %[wlo], s53
s_addc_u32 s41, %[whi], 0
s_mov_b32 m0, s38
M 9,51,3
LA 16
PK 123,17,121,33
M 10,62,2
LC_ 127
PK 121,17,121,32
M 10,63,3
RA 216,1,19456
XL 35,704
DW 0
M 11,74,2
WL 3
LO 18,18,126
LU 126,18
M 11,75,3
s_add_u32 s37, s37, 32768
RA 220,1,20480
HS 25,126
XS 176
M 8,40,4
s_cmp_eq_u32 s37, s75
LL 17,64
LL 112,65
s_cselect_b32 s37, s74, s37
M 8,41,5
LA 17
DW 1024
PK 126,18,124,33
XS 177
M 9,52,4
s_add_u32 s42, s44, s64
LC_ 112
PK 124,18,124,32
s_addc_u32 s43, s45, 0
M 9,53,5
RA 224,1,21504
PO 126
XS 178
M 10,64,4
WL 5
LO 16,16,127
LU 127,16
ST 120
M 10,65,5
DW 2048
HS 25,127
XS 179
M 11,76,4
WL 3
LL 18,66
LL 113,67
M 11,77,5
s_add_u32 s40, s40, 3072
LA 18
PK 127,16,125,33
XS 180
M 8,42,0
s_addc_u32 s41, s41, 0
LC_ 113
PK 125,16,125,32
s_add_u32 m0, s38, 3072
M 8,43,1
RA 228,1,22528
PO 127
XS 181
M 9,54,0
WL 4
LO 112,17,112
LU 17,112
M 9,55,1
RA 232,1,23552
HZ 24,17
XS 182
M 10,66,0
DW 0
LL 16,68
LL 116,69
M 10,67,1
s_add_u32 s42, s44, s65
LA 16
HX 25
XS 183
M 11,78,0
WL 2
LC_ 116
W1 25,1024
M 11,79,1
s_addc_u32 s43, s45, 0
RA 236,1,24576
v_add_u32 v4, s39, v4
v_min_u32 v35, v35, v13
M 8,44,2
LO 113,18,113
LU 18,113
s_lshl_b32 s53, s52, 8
s_add_u32 s48, %[alo], s53
s_addc_u32 s49, %[ahi], 0
s_add_u32 s52, s52, %[d]
s_cmp_eq_u32 s52, %[wrap]
s_cselect_b32 s52, %[rst], s52
DW 1024
M 8,45,3
ST 124
HS 24,18
XS 184
M 9,56,2
s_mul_i32 s53, s51, 49152
LL 17,70
LL 117,71
s_bitcmp1_b64 s[90:91], s51
s_cselect_b64 s[44:45], s[84:85], s[82:83]
s_add_u32 s44, s44, s53
s_addc_u32 s45, s45, 0
s_add_u32 s51, s51, %[d]
s_cmp_eq_u32 s51, %[wrap]
s_cselect_b32 s51, %[rst], s51
M 9,57,3
LA 17
DW 2048
XS 185
M 10,68,2
LC_ 117
DQ 3072
M 10,69,3
s_add_u32 s50, s50, %[d]
v_add_u32 v1, s37, v0
XS 186
M 11,80,2
WL 2
LO 116,16,116
LU 16,116
M 11,81,3
s_cmp_eq_u32 s50, %[wrap]
RA 216,1,0
HS 24,16
XS 187
M 8,46,4
s_cselect_b32 s50, %[rst], s50
LL 18,72
LL 120,73
M 8,47,5
LA 18
v_and_b32 v22, 0xfffffcff, v1
XS 188
M 9,58,4
LC_ 120
RA 220,1,1024
M 9,59,5
RV 252,22,5120
v_add_u32 v19, 1, v19
XS 189
M 10,70,4
WL 5
LO 117,17,117
LU 17,117
M 10,71,5
s_add_u32 s38, s38, 32768
RA 224,1,2048
HS 24,17
XS 190
M 11,82,4
WL 1
LL 16,74
LL 121,75
XS 191
M 11,83,5
s_cmp_eq_u32 s38, s77
LA 16
RA 228,1,3072
s_cselect_b32 s38, s76, s38
FP
s_sub_u32 s39, 0, s39
Z 0,0,0
LC_ 121
XS 192
M 0,1,1
XS 193
XS 194
Z 1,12,0
LO 120,18,120
LU 18,120
M 1,13,1
RA 232,1,4096
HS 24,18
Z 2,36,0
LL 17,80
LL 124,81
M 2,37,1
LA 17
XS 195
Z 3,48,0
LC_ 124
XS 196
M 3,49,1
XS 197
XS 198
Z 4,60,0
WL 3
LO 121,16,121
LU 16,121
M 4,61,1
RA 236,1,6144
HS 24,16
Z 5,72,0
LL 18,82
LL 125,83
M 5,73,1
LA 18
FR
M 0,2,2
LC_ 125
XS 199
M 0,3,3
XS 200
XS 201
M 1,14,2
WL 4
LO 124,17,124
LU 17,124
M 1,15,3
RA 216,1,7168
HS 24,17
M 2,38,2
LL 16,88
LL 114,89
M 2,39,3
LA 16
XS 202
M 3,50,2
WL 3
LC_ 114
v_cmp_gt_u32 vcc, v19, v15
M 3,51,3
WV 31
BW 24
M 4,62,2
WL 4
LO 125,18,125
LU 18,125
M 4,63,3
RA 220,1,8192
HS 24,18
M 5,74,2
LL 17,90
LL 115,91
XS 203
M 5,75,3
s_cbranch_vccnz .Lk1s8
.Lk1r8:
LA 17
RV 40,8,6144
M 0,4,4
s_mov_b32 m0, %[pm]
LC_ 115
DF
XS 204
M 0,5,5
RV 44,8,6208
RV 48,8,6272
M 1,16,4
WL 7
LO 16,16,114
LU 114,16
XS 205
M 1,17,5
RA 224,1,9216
HZ 25,114
M 2,40,4
LL 18,92
LL 118,93
XS 206
M 2,41,5
LA 18
PK 114,16,112,33
XS 207
M 3,52,4
LC_ 118
PK 112,16,112,32
M 3,53,5
BR 24
HX 24
XS 208
M 4,64,4
WL 6
LO 17,17,115
LU 115,17
M 4,65,5
RA 228,1,10240
HS 25,115
XS 209
M 5,76,4
LL 16,94
LL 119,95
M 5,77,5
s_add_u32 s42, s44, s62
LA 16
PK 115,17,113,33
XS 210
M 0,6,0
s_addc_u32 s43, s45, 0
LC_ 119
PK 113,17,113,32
M 0,7,1
W1 24,0
XS 211
M 1,18,0
WL 5
LO 18,18,118
LU 118,18
M 1,19,1
RA 232,1,11264
HS 25,118
XS 212
M 2,42,0
LL 17,96
LL 122,97
M 2,43,1
LA 17
PK 118,18,116,33
XS 213
M 3,54,0
LC_ 122
PK 116,18,116,32
M 3,55,1
RV 252,22,5392
PO 118
XS 214
M 4,66,0
WL 5
LO 16,16,119
LU 119,16
M 4,67,1
RA 236,1,12288
HS 25,119
XS 215
M 5,78,0
LL 18,98
LL 123,99
M 5,79,1
LA 18
PK 119,16,117,33
XS 216
M 0,8,2
LC_ 123
PK 117,16,117,32
M 0,9,3
ST 112
PO 119
XS 217
M 1,20,2
WL 4
LO 17,17,122
LU 122,17
M 1,21,3
s_add_u32 s42, s44, s63
RA 216,1,13312
HS 25,122
XS 218
M 2,44,2
s_addc_u32 s43, s45, 0
LL 16,100
LL 126,101
M 2,45,3
LA 16
PK 122,17,120,33
XS 219
M 3,56,2
LC_ 126
PK 120,17,120,32
M 3,57,3
WV 29
ST 116
XS 220
BW 25
M 4,68,2
WL 4
LO 18,18,123
LU 123,18
M 4,69,3
RA 220,1,14336
BR 25
HS 25,123
XS 221
LZ 40,24
)"
R"(WV 26
BW 26
M 5,80,2
LL 17,102
LL 127,103
M 5,81,3
s_add_u32 s42, s44, s64
LA 17
PK 123,18,121,33
XS 222
M 0,10,4
s_addc_u32 s43, s45, 0
LC_ 127
PK 121,18,121,32
XS 223
M 0,11,5
ST 120
RV 40,8,6336
M 1,22,4
WL 7
BR 26
LO 16,16,126
LU 126,16
M 1,23,5
WL 5
RA 224,1,15360
HS 25,126
LS 44,25
WV 24
BW 27
M 2,46,4
LL 18,56
LL 104,57
M 2,47,5
LA 18
PK 126,16,124,33
M 3,58,4
LC_ 104
RV 44,8,6400
PK 124,16,124,32
M 3,59,5
ds_read_b64 v[14:15], v5 offset:0
BR 27
PO 126
M 4,70,4
WL 9
LO 17,17,127
LU 127,17
M 4,71,5
WL 7
RA 228,1,16384
HS 25,127
LS 48,26
WV 23
BW 28
M 5,82,4
LL 16,58
LL 105,59
M 5,83,5
LA 16
RV 48,8,6464
PK 127,17,125,33
Z 6,0,0
s_add_u32 s42, s44, s65
LC_ 105
PK 125,17,125,32
M 6,1,1
s_addc_u32 s43, s45, 0
BR 28
PO 127
Z 7,12,0
WL 9
LO 104,18,104
LU 18,104
M 7,13,1
WL 6
RA 232,1,17408
HZ 26,18
LS 40,27
WV 22
BW 29
Z 8,36,0
WV 21
LL 17,60
LL 108,61
M 8,37,1
WL 2
LA 17
ST 124
RV 40,8,6528
Z 9,48,0
s_mul_i32 s53, s50, 25600
LC_ 108
BR 29
M 9,49,1
s_add_u32 s40, %[wlo], s53
v_subrev_u32 v5, s39, v5
HX 25
LS 44,28
s_addc_u32 s41, %[whi], 0
BW 30
Z 10,60,0
LO 105,16,105
LU 16,105
M 10,61,1
s_mov_b32 m0, s38
RA 236,1,18432
HS 26,16
Z 11,72,0
RV 44,8,6592
LL 18,62
LL 109,63
M 11,73,1
LA 18
W1 25,1024
BR 30
M 6,2,2
LC_ 109
DW 0
M 6,3,3
WL 7
v_add_f32 v14, v14, v15
v_add_u32 v4, s39, v4
LS 48,29
WV 22
BW 31
M 7,14,2
LO 108,17,108
LU 17,108
M 7,15,3
s_add_u32 s37, s37, 32768
RA 216,1,19456
HS 26,17
M 8,38,2
s_cmp_eq_u32 s37, s75
RV 48,8,6656
LL 16,64
LL 112,65
M 8,39,3
s_cselect_b32 s37, s74, s37
LA 16
DW 1024
BR 31
M 9,50,2
LC_ 112
v_add_f32_dpp v14, v14, v14 quad_perm:[1,0,3,2] row_mask:0xf bank_mask:0xf
M 9,51,3
WL 7
v_add_f32_dpp v14, v14, v14 quad_perm:[2,3,0,1] row_mask:0xf bank_mask:0xf
LS 40,30
WV 22
BW 32
M 10,62,2
WL 7
LO 109,18,109
LU 18,109
M 10,63,3
RA 220,1,20480
RV 40,8,6720
HS 26,18
M 11,74,2
BR 32
LL 17,66
LL 113,67
M 11,75,3
LA 17
DW 2048
M 6,4,4
LC_ 113
v_add_f32_dpp v14, v14, v14 row_half_mirror row_mask:0xf bank_mask:0xf
M 6,5,5
WL 7
v_cvt_f64_f32 v[14:15], v14
HX 26
LS 44,31
WV 19
BW 33
M 7,16,4
WL 3
RV 44,8,6784
LO 112,16,112
LU 16,112
M 7,17,5
s_mul_i32 s53, s51, 49152
RA 224,1,21504
HZ 24,16
M 8,40,4
s_bitcmp1_b64 s[90:91], s51
BR 33
LL 18,68
LL 116,69
M 8,41,5
s_cselect_b64 s[44:45], s[84:85], s[82:83]
LA 18
s_mov_b64 exec, s[54:55]
SP
s_mov_b64 exec, -1
LS 48,32
s_add_u32 s44, s44, s53
BW 34
M 9,52,4
s_addc_u32 s45, s45, 0
LC_ 116
RV 48,8,6848
M 9,53,5
s_bitcmp1_b64 s[92:93], s51
RA 240,7,0
BR 34
M 10,64,4
WL 10
LO 113,17,113
LU 17,113
M 10,65,5
s_cselect_b64 s[94:95], s[88:89], s[86:87]
RA 228,1,22528
HS 24,17
M 11,76,4
WL 7
LL 16,70
LL 117,71
M 11,77,5
s_add_u32 s94, s94, s53
LA 16
M 6,6,0
s_addc_u32 s95, s95, 0
LC_ 117
M 6,7,1
s_add_u32 s51, s51, %[d]
LS 40,33
s_cmp_eq_u32 s51, %[wrap]
WV 18
BW 35
M 7,18,0
WL 4
RA 244,7,256
LO 116,18,116
LU 18,116
M 7,19,1
s_cselect_b32 s51, %[rst], s51
RA 232,1,23552
HS 24,18
M 8,42,0
s_mul_i32 s53, %[d], 0
BR 35
LL 17,72
LL 120,73
M 8,43,1
s_add_u32 s60, s53, %[sb1]
LA 17
s_add_u32 s61, s60, 512
LS 44,34
s_add_u32 s40, s40, 3072
RA 248,7,512
M 9,54,0
s_addc_u32 s41, s41, 0
LC_ 120
s_add_u32 m0, s38, 3072
M 9,55,1
DW 0
M 10,66,0
WL 7
LO 117,16,117
LU 16,117
M 10,67,1
WL 3
RA 236,1,24576
HS 24,16
LS 48,35
RA 252,7,768
M 11,78,0
LL 18,74
LL 121,75
s_nop 3
v_mul_f32 v36, 0xbb2aaaab, v36
M 11,79,1
LA 18
DW 1024
v_mul_f32 v37, 0xbb2aaaab, v37
v_mul_f32 v38, 0xbb2aaaab, v38
M 6,8,2
LC_ 121
DW 2048
v_mul_f32 v39, 0xbb2aaaab, v39
M 6,9,3
s_lshl_b32 s53, s52, 8
v_add_u32 v1, s37, v0
LK 40,240,24,0
LK 44,242,24,2
s_add_u32 s48, %[alo], s53
RA 240,7,1024
LK 48,244,25,0
s_nop 0
CF 224,40,41
LK 248,246,25,2
s_addc_u32 s49, %[ahi], 0
RA 244,7,1280
CF 225,42,43
M 7,20,2
WL 6
LO 120,17,120
LU 17,120
M 7,21,3
s_add_u32 s52, s52, %[d]
RA 216,1,0
HS 24,17
M 8,44,2
s_cmp_eq_u32 s52, %[wrap]
LL 16,76
LL 124,77
CF 226,44,45
M 8,45,3
s_cselect_b32 s52, %[rst], s52
LA 16
DQ 3072
CF 227,46,47
LK 40,248,26,0
CF 228,48,49
LK 44,250,26,2
RA 248,7,1536
CF 229,50,51
GZ 24
WL 7
CF 230,248,249
CF 231,250,251
LK 48,252,27,0
CF 232,40,41
LK 248,254,27,2
RA 252,7,1792
CF 233,42,43
GS 25
WL 5
CF 234,44,45
CF 235,46,47
LK 40,240,28,0
CF 236,48,49
LK 44,242,28,2
RA 240,7,2048
CF 237,50,51
GS 26
WL 5
CF 238,248,249
CF 239,250,251
LK 48,244,29,0
CF 240,40,41
LK 248,246,29,2
RA 244,7,2304
CF 241,42,43
GS 27
CF 242,44,45
CF 243,46,47
M 9,56,2
LC_ 124
v_and_b32 v22, 0xfffffcff, v1
M 9,57,3
WL 4
LK 40,248,30,0
CF 244,48,49
LK 44,250,30,2
RA 248,7,2560
CF 245,50,51
GS 28
WL 4
CF 246,248,249
CF 247,250,251
LK 48,252,31,0
CF 40,40,41
LK 248,254,31,2
RA 252,7,2816
CF 42,42,43
GS 29
CF 44,44,45
CF 46,46,47
M 10,68,2
LO 121,18,121
LU 18,121
M 10,69,3
s_add_u32 s50, s50, %[d]
RA 220,1,1024
HS 24,18
M 11,80,2
LL 17,78
LL 125,79
M 11,81,3
s_cmp_eq_u32 s50, %[wrap]
LA 17
RV 252,22,5120
M 6,10,4
s_cselect_b32 s50, %[rst], s50
LC_ 125
AW 0,40
M 6,11,5
v_add_u32 v19, 1, v19
AW 1,42
M 7,22,4
WL 6
LO 124,16,124
LU 16,124
M 7,23,5
s_add_u32 s38, s38, 32768
RA 224,1,2048
)"
R"(HS 24,16
M 8,46,4
s_cmp_eq_u32 s38, s77
LL 18,80
LL 106,81
M 8,47,5
s_cselect_b32 s38, s76, s38
LA 18
FP
M 9,58,4
s_sub_u32 s39, 0, s39
LC_ 106
AW 2,44
M 9,59,5
AW 3,46
LK 40,240,32,0
CF 48,48,49
LK 44,242,32,2
CF 50,50,51
GS 30
CF 248,248,249
CF 250,250,251
M 10,70,4
WL 4
LO 125,17,125
LU 17,125
M 10,71,5
RA 228,1,3072
HS 24,17
M 11,82,4
LL 16,82
LL 107,83
M 11,83,5
LA 16
HX 24
Z 0,0,0
LC_ 107
AW 4,48
M 0,1,1
AW 5,50
AW 6,248
Z 1,12,0
WL 3
LO 18,18,106
LU 106,18
M 1,13,1
RA 232,1,4096
HZ 27,106
Z 2,36,0
LL 17,84
LL 110,85
M 2,37,1
LA 17
PK 106,18,104,33
Z 3,48,0
LC_ 110
PK 104,18,104,32
M 3,49,1
AW 7,250
LK 48,244,33,0
CF 40,40,41
LK 248,246,33,2
CF 42,42,43
GS 31
CF 44,44,45
CF 46,46,47
Z 4,60,0
WL 3
LO 16,16,107
LU 107,16
M 4,61,1
RA 236,1,6144
HS 27,107
Z 5,72,0
LL 18,86
LL 111,87
M 5,73,1
s_add_u32 s42, s94, s60
LA 18
PK 107,16,105,33
M 0,2,2
s_addc_u32 s43, s95, 0
LC_ 111
PK 105,16,105,32
M 0,3,3
FR
AW 8,40
M 1,14,2
WL 4
LO 17,17,110
LU 110,17
M 1,15,3
RA 216,1,7168
HS 27,110
M 2,38,2
LL 16,88
LL 114,89
M 2,39,3
LA 16
PK 110,17,108,33
AW 9,42
AW 10,44
M 3,50,2
LC_ 114
PK 108,17,108,32
AW 11,46
M 3,51,3
SU 104
PO 110
LK 40,248,34,0
CF 48,48,49
LK 44,250,34,2
CF 50,50,51
GS 32
CF 248,248,249
CF 250,250,251
M 4,62,2
WL 4
LO 18,18,111
LU 111,18
M 4,63,3
RA 220,1,8192
HS 27,111
M 5,74,2
LL 17,90
LL 115,91
AW 12,48
AW 13,50
M 5,75,3
s_mov_b32 m0, %[pm]
LA 17
PK 111,18,109,33
AW 14,248
AW 15,250
LK 48,252,35,0
CF 40,40,41
LK 248,254,35,2
WL 5
CF 42,42,43
v_cmp_gt_u32 vcc, v19, v15
GS 33
CF 44,44,45
CF 46,46,47
M 0,4,4
LC_ 115
PK 109,18,109,32
M 0,5,5
DF
PO 111
M 1,16,4
WL 3
LO 16,16,114
LU 114,16
M 1,17,5
s_cbranch_vccnz .Lk1s9
.Lk1r9:
RA 224,1,9216
HZ 25,114
AW 16,40
M 2,40,4
s_add_u32 s42, s94, s61
LL 18,92
LL 118,93
AW 17,42
AW 18,44
M 2,41,5
s_addc_u32 s43, s95, 0
LA 18
RV 36,8,6912
PK 114,16,112,33
AW 19,46
M 3,52,4
LC_ 118
PK 112,16,112,32
M 3,53,5
SU 108
HX 27
GS 34
CF 48,48,49
CF 50,50,51
CF 248,248,249
M 4,64,4
WL 4
LO 17,17,115
LU 115,17
CF 250,250,251
AW 20,48
M 4,65,5
RA 228,1,10240
HS 25,115
AW 21,50
AW 22,248
M 5,76,4
LL 16,94
LL 119,95
AW 23,250
M 5,77,5
s_add_u32 s42, s44, s62
LA 16
PK 115,17,113,33
GS 35
s_addc_u32 s43, s45, 0
W1 24,0
s_nop 5
v_cndmask_b32 v52, v52, v53, s[78:79]
M 0,6,0
LC_ 119
PK 113,17,113,32
v_cndmask_b32 v54, v54, v55, s[78:79]
M 0,7,1
W1 26,2048
v_cndmask_b32 v52, v52, v54, s[80:81]
M 1,18,0
WL 5
LO 18,18,118
LU 118,18
v_mul_f32 v52, 0x3b2aaaab, v52
M 1,19,1
RA 232,1,11264
HS 25,118
v_med3_f32 v53, v52, v39, v37
M 2,42,0
LL 17,96
LL 122,97
LG 52,53
M 2,43,1
LA 17
PK 118,18,116,33
C 52,52,52
M 3,54,0
LC_ 122
PK 116,18,116,32
v_lshlrev_b32 v52, 16, v52
M 3,55,1
RV 252,22,5392
PO 118
v_add_f32 v52, v36, v52
M 4,66,0
WL 5
LO 16,16,119
LU 119,16
C 52,52,52
M 4,67,1
RA 236,1,12288
HS 25,119
M 5,78,0
LL 18,98
LL 123,99
v_lshlrev_b32 v52, 16, v52
M 5,79,1
LA 18
PK 119,16,117,33
v_sqrt_f32 v52, v52
M 0,8,2
LC_ 123
PK 117,16,117,32
C 52,52,52
M 0,9,3
ST 112
PO 119
v_lshlrev_b32 v52, 16, v52
M 1,20,2
WL 4
LO 17,17,122
LU 122,17
v_rcp_f32 v52, v52
M 1,21,3
s_add_u32 s42, s44, s63
RA 216,1,13312
HS 25,122
v_mul_f32 v52, v38, v52
M 2,44,2
s_addc_u32 s43, s45, 0
LL 16,100
LL 126,101
CF 52,52,52
M 2,45,3
LA 16
PK 122,17,120,33
ds_bpermute_b32 v13, v28, v52
M 3,56,2
LC_ 126
PK 120,17,120,32
M 3,57,3
ST 116
AR 248,0
M 4,68,2
WL 4
LO 18,18,123
LU 123,18
M 4,69,3
RA 220,1,14336
HS 25,123
M 5,80,2
LL 17,102
LL 127,103
M 5,81,3
s_add_u32 s42, s44, s64
LA 17
PK 123,18,121,33
M 0,10,4
s_addc_u32 s43, s45, 0
LC_ 127
PK 121,18,121,32
M 0,11,5
ST 120
AR 249,1
M 1,22,4
WL 3
LO 16,16,126
LU 126,16
M 1,23,5
RA 224,1,15360
HS 25,126
M 2,46,4
LL 18,56
LL 104,57
M 2,47,5
LA 18
PK 126,16,124,33
M 3,58,4
LC_ 104
PK 124,16,124,32
AR 250,2
M 3,59,5
ds_read_b64 v[14:15], v5 offset:0
PO 126
M 4,70,4
WL 4
LO 17,17,127
LU 127,17
AR 251,3
M 4,71,5
RA 228,1,16384
HS 25,127
AR 52,4
M 5,82,4
LL 16,58
LL 105,59
AR 53,5
M 5,83,5
LA 16
PK 127,17,125,33
Z 6,0,0
s_add_u32 s42, s44, s65
LC_ 105
PK 125,17,125,32
AR 54,6
M 6,1,1
s_addc_u32 s43, s45, 0
PO 127
v_subrev_u32 v5, s39, v5
AR 55,7
Z 7,12,0
WL 4
LO 104,18,104
LU 18,104
XS 224
M 7,13,1
RA 232,1,17408
HZ 26,18
Z 8,36,0
LL 17,60
LL 108,61
XS 225
M 8,37,1
WL 4
LA 17
ST 124
XS 226
Z 9,48,0
s_mul_i32 s53, s50, 25600
LC_ 108
v_add_f32 v14, v14, v15
M 9,49,1
s_add_u32 s40, %[wlo], s53
v_add_f32_dpp v14, v14, v14 quad_perm:[1,0,3,2] row_mask:0xf bank_mask:0xf
HX 25
XS 227
Z 10,60,0
WL 3
LO 105,16,105
LU 16,105
XS 228
M 10,61,1
s_addc_u32 s41, %[whi], 0
RA 236,1,18432
HS 26,16
XS 229
Z 11,72,0
LL 18,62
LL 109,63
M 11,73,1
s_mov_b32 m0, s38
LA 18
W1 25,1024
XS 230
M 6,2,2
LC_ 109
W1 27,3072
)"
R"(XS 231
M 6,3,3
DW 0
v_add_f32_dpp v14, v14, v14 quad_perm:[2,3,0,1] row_mask:0xf bank_mask:0xf
v_min_u32 v35, v35, v13
M 7,14,2
WL 5
LO 108,17,108
LU 17,108
M 7,15,3
s_add_u32 s37, s37, 32768
RA 216,1,19456
HS 26,17
XS 232
M 8,38,2
s_cmp_eq_u32 s37, s75
LL 16,64
LL 112,65
XS 233
M 8,39,3
s_cselect_b32 s37, s74, s37
LA 16
v_add_f32_dpp v14, v14, v14 row_half_mirror row_mask:0xf bank_mask:0xf
XS 234
M 9,50,2
s_mul_i32 s53, %[d], 196608
LC_ 112
v_cvt_f64_f32 v[14:15], v14
M 9,51,3
s_add_u32 s53, s53, s57
s_mov_b64 exec, s[54:55]
SP
s_mov_b64 exec, -1
v_add_u32 v4, s39, v4
XS 235
M 10,62,2
WL 4
LO 109,18,109
LU 18,109
XS 236
M 10,63,3
s_add_u32 s46, %[xlo], s53
RA 220,1,20480
HS 26,18
M 11,74,2
LL 17,66
LL 113,67
XS 237
M 11,75,3
s_addc_u32 s47, %[xhi], 0
LA 17
HX 26
XS 238
M 6,4,4
s_bitcmp1_b64 s[92:93], 48
LC_ 113
XS 239
M 6,5,5
s_cselect_b32 s58, 16, 0
XS 240
XS 241
M 7,16,4
WL 3
LO 112,16,112
LU 16,112
M 7,17,5
s_lshr_b32 s58, 0xda43cb52, s58
RA 224,1,21504
HZ 24,16
M 8,40,4
LL 18,68
LL 116,69
XS 242
M 8,41,5
LA 18
v_lshrrev_b32 v3, 6, v0
M 9,52,4
LC_ 116
v_and_b32 v3, 12, v3
v_bfe_u32 v3, s58, v3, 4
v_mad_u32_u24 v3, v3, s97, v21
XS 243
M 9,53,5
DW 1024
XS 244
M 10,64,4
WL 3
LO 113,17,113
LU 17,113
XS 245
M 10,65,5
RA 228,1,22528
HS 24,17
M 11,76,4
LL 16,70
LL 117,71
XS 246
M 11,77,5
LA 16
XL 36,0
XS 247
M 6,6,0
LC_ 117
DW 2048
M 6,7,1
XL 37,64
XS 248
M 7,18,0
WL 3
LO 116,18,116
LU 18,116
XS 249
M 7,19,1
s_mul_i32 s53, s51, 49152
RA 232,1,23552
HS 24,18
XS 250
M 8,42,0
s_bitcmp1_b64 s[90:91], s51
LL 17,72
LL 120,73
M 8,43,1
s_cselect_b64 s[44:45], s[84:85], s[82:83]
LA 17
s_add_u32 s44, s44, s53
s_addc_u32 s45, s45, 0
s_bitcmp1_b64 s[92:93], s51
s_cselect_b64 s[94:95], s[88:89], s[86:87]
s_add_u32 s94, s94, s53
s_addc_u32 s95, s95, 0
s_add_u32 s51, s51, %[d]
s_cmp_eq_u32 s51, %[wrap]
s_cselect_b32 s51, %[rst], s51
XS 251
M 9,54,0
s_add_u32 s40, s40, 3072
LC_ 120
s_addc_u32 s41, s41, 0
s_add_u32 m0, s38, 3072
AW 0,248
M 9,55,1
XL 38,128
AW 1,249
M 10,66,0
WL 3
LO 117,16,117
LU 16,117
M 10,67,1
RA 236,1,24576
HS 24,16
AW 2,250
M 11,78,0
LL 18,74
LL 121,75
AW 3,251
M 11,79,1
LA 18
DW 0
XS 52
M 6,8,2
LC_ 121
XL 39,192
M 6,9,3
DW 1024
v_add_u32 v1, s37, v0
XS 53
M 7,20,2
WL 3
LO 120,17,120
LU 17,120
XS 54
M 7,21,3
RA 216,1,0
HS 24,17
M 8,44,2
LL 16,76
LL 124,77
XS 55
M 8,45,3
LA 16
XL 40,256
AW 4,52
M 9,56,2
LC_ 124
DW 2048
AW 5,53
M 9,57,3
XL 41,320
v_and_b32 v22, 0xfffffcff, v1
M 10,68,2
WL 3
LO 121,18,121
LU 18,121
AW 6,54
M 10,69,3
s_lshl_b32 s53, s52, 8
RA 220,1,1024
HS 24,18
AW 7,55
M 11,80,2
LL 17,78
LL 125,79
AR 248,8
M 11,81,3
s_add_u32 s48, %[alo], s53
LA 17
RV 252,22,5120
M 6,10,4
s_addc_u32 s49, %[ahi], 0
LC_ 125
AR 249,9
M 6,11,5
s_add_u32 s52, s52, %[d]
AR 250,10
AR 251,11
M 7,22,4
WL 4
LO 124,16,124
LU 16,124
M 7,23,5
s_cmp_eq_u32 s52, %[wrap]
RA 224,1,2048
HS 24,16
M 8,46,4
s_cselect_b32 s52, %[rst], s52
LL 18,80
LL 106,81
XS 248
M 8,47,5
LA 18
DQ 3072
XS 249
M 9,58,4
LC_ 106
XL 42,384
M 9,59,5
s_add_u32 s50, s50, %[d]
XS 250
XS 251
M 10,70,4
WL 3
LO 125,17,125
LU 17,125
M 10,71,5
RA 228,1,3072
HS 24,17
AW 8,248
M 11,82,4
LL 16,82
LL 107,83
M 11,83,5
LA 16
HX 24
s_cmp_eq_u32 s50, %[wrap]
s_cselect_b32 s50, %[rst], s50
XL 43,448
AW 9,249
Z 0,0,0
WV 36
LC_ 107
XL 44,512
v_add_u32 v19, 1, v19
s_add_u32 s38, s38, 32768
s_cmp_eq_u32 s38, s77
s_cselect_b32 s38, s76, s38
FP
s_sub_u32 s39, 0, s39
AW 10,250
M 0,1,1
XL 45,576
AW 11,251
Z 1,12,0
WL 4
LO 18,18,106
LU 106,18
M 1,13,1
RA 232,1,4096
HZ 27,106
AR 52,12
Z 4,60,0
LL 17,84
LL 110,85
AR 53,13
M 4,61,1
LA 17
PK 106,18,104,33
AR 54,14
Z 5,72,0
WL 5
LC_ 110
XL 46,640
PK 104,18,104,32
M 5,73,1
RA 236,1,6144
FR
AR 55,15
M 0,2,2
WL 6
LO 16,16,107
LU 107,16
XS 52
M 0,3,3
XL 47,704
HS 27,107
M 1,14,2
LL 18,86
LL 111,87
XS 53
M 1,15,3
s_add_u32 s42, s94, s60
LA 18
PK 107,16,105,33
XS 54
M 4,62,2
s_addc_u32 s43, s95, 0
LC_ 111
PK 105,16,105,32
XS 55
M 4,63,3
RA 216,1,7168
AW 12,52
M 5,74,2
WL 4
LO 17,17,110
LU 110,17
M 5,75,3
RA 220,1,8192
HS 27,110
AW 13,53
M 0,4,4
LL 16,88
LL 114,89
AW 14,54
M 0,5,5
LA 16
PK 110,17,108,33
M 1,16,4
LC_ 114
PK 108,17,108,32
AW 15,55
M 1,17,5
RA 224,1,9216
PO 110
AR 248,16
M 4,64,4
WL 5
LO 18,18,111
LU 111,18
SU 104
AR 249,17
M 4,65,5
HS 27,111
v_cmp_gt_u32 vcc, v19, v15
M 5,76,4
WL 3
LL 17,90
LL 115,91
AR 250,18
M 5,77,5
WV 17
LA 17
PK 111,18,109,33
AR 251,19
M 0,6,0
s_cbranch_vccnz .Lk1s10
.Lk1r10:
LC_ 115
PK 109,18,109,32
M 0,7,1
s_add_u32 s42, s94, s61
RA 228,1,10240
PO 111
XS 248
BW 36
M 1,18,0
WL 5
LO 16,16,114
LU 114,16
XS 249
M 1,19,1
s_addc_u32 s43, s95, 0
RA 232,1,11264
RV 40,8,6144
HZ 25,114
XS 250
M 4,66,0
RV 44,8,6208
LL 18,92
LL 118,93
M 4,67,1
LA 18
RV 48,8,6272
PK 114,16,112,33
XS 251
M 5,78,0
WL 6
LC_ 118
BR 36
PK 112,16,112,32
AW 16,248
)"
R"(M 5,79,1
WV 15
RV 252,22,5392
ds_read_b64 v[14:15], v5 offset:0
W1 24,0
W1 26,2048
HX 27
BW 37
AW 17,249
M 0,8,2
BR 37
LO 17,17,115
LU 115,17
M 0,9,3
WL 6
RA 236,1,12288
HS 25,115
SU 108
AW 18,250
LZ 40,36
WV 15
BW 38
M 1,20,2
LL 16,94
LL 119,95
AW 19,251
M 1,21,3
s_add_u32 s42, s44, s62
LA 16
RV 40,8,6336
PK 115,17,113,33
AR 52,20
M 4,68,2
s_addc_u32 s43, s45, 0
LC_ 119
BR 38
PK 113,17,113,32
M 4,69,3
WL 6
RA 216,1,13312
AR 53,21
LS 44,37
WV 13
BW 39
M 5,80,2
WL 7
LO 18,18,118
LU 118,18
AR 54,22
M 5,81,3
RA 220,1,14336
RV 44,8,6400
HS 25,118
M 0,10,4
BR 39
LL 17,96
LL 122,97
AR 55,23
M 0,11,5
WL 5
LA 17
PK 118,18,116,33
XS 52
LS 48,38
WV 11
BW 40
M 1,22,4
LC_ 122
PK 116,18,116,32
XS 53
M 1,23,5
RA 224,1,15360
RV 48,8,6464
PO 118
M 4,70,4
LO 16,16,119
LU 119,16
ST 112
XS 54
M 4,71,5
BR 40
HS 25,119
XS 55
M 5,82,4
WL 8
LL 18,98
LL 123,99
AW 20,52
M 5,83,5
WL 6
LA 18
PK 119,16,117,33
LS 40,39
WV 10
BW 41
Z 6,0,0
s_add_u32 s42, s44, s63
LC_ 123
PK 117,16,117,32
v_add_f32 v14, v14, v15
s_nop 1
v_add_f32_dpp v14, v14, v14 quad_perm:[1,0,3,2] row_mask:0xf bank_mask:0xf
s_nop 1
v_add_f32_dpp v14, v14, v14 quad_perm:[2,3,0,1] row_mask:0xf bank_mask:0xf
s_nop 1
v_add_f32_dpp v14, v14, v14 row_half_mirror row_mask:0xf bank_mask:0xf
v_cvt_f64_f32 v[14:15], v14
s_mov_b64 exec, s[54:55]
SP
s_mov_b64 exec, -1
s_nop 0
ds_read_b64 v[14:15], v5 offset:2048
RV 40,8,6528
AW 21,53
M 6,1,1
s_addc_u32 s43, s45, 0
RA 228,1,16384
BR 41
PO 119
AW 22,54
Z 7,12,0
WL 10
LO 17,17,122
LU 122,17
AW 23,55
M 7,13,1
WL 7
RA 232,1,17408
HS 25,122
LS 44,40
WV 9
BW 42
Z 10,60,0
RV 44,8,6592
LL 16,100
LL 126,101
M 10,61,1
LA 16
BR 42
PK 122,17,120,33
Z 2,72,0
WL 6
LC_ 126
PK 120,17,120,32
M 2,73,1
WL 6
RA 236,1,18432
v_subrev_u32 v5, s39, v5
LS 48,41
WV 8
BW 43
M 6,2,2
RV 48,8,6656
LO 18,18,123
LU 123,18
M 6,3,3
ST 116
HS 25,123
s_mul_i32 s53, %[d], 393216
s_add_u32 s53, s53, s57
s_add_u32 s46, %[xlo], s53
s_addc_u32 s47, %[xhi], 0
s_bitcmp1_b64 s[92:93], 48
s_cselect_b32 s58, 16, 0
s_lshr_b32 s58, 0xfe987610, s58
v_lshrrev_b32 v3, 6, v0
v_and_b32 v3, 12, v3
v_bfe_u32 v3, s58, v3, 4
BR 43
v_mad_u32_u24 v3, v3, s97, v21
M 7,14,2
s_mul_i32 s53, s50, 25600
LL 17,102
LL 127,103
s_add_u32 s40, %[wlo], s53
s_addc_u32 s41, %[whi], 0
s_mov_b32 m0, s38
M 7,15,3
WL 5
LA 17
PK 123,18,121,33
LS 40,42
WV 8
BW 44
M 10,62,2
LC_ 127
RV 40,8,6720
PK 121,18,121,32
M 10,63,3
WV 7
RA 216,1,19456
BR 44
DW 0
M 2,74,2
WL 6
LO 16,16,126
LU 126,16
M 2,75,3
s_add_u32 s37, s37, 32768
RA 220,1,20480
HS 25,126
LS 44,43
s_cmp_eq_u32 s37, s75
BW 45
M 6,4,4
s_cselect_b32 s37, s74, s37
LL 18,56
LL 104,57
M 6,5,5
LA 18
PK 126,16,124,33
v_add_f32 v14, v14, v15
s_nop 1
v_add_f32_dpp v14, v14, v14 quad_perm:[1,0,3,2] row_mask:0xf bank_mask:0xf
s_nop 1
v_add_f32_dpp v14, v14, v14 quad_perm:[2,3,0,1] row_mask:0xf bank_mask:0xf
s_nop 1
v_add_f32_dpp v14, v14, v14 row_half_mirror row_mask:0xf bank_mask:0xf
v_cvt_f64_f32 v[14:15], v14
s_mov_b64 exec, s[54:55]
SP
s_mov_b64 exec, -1
XL 48,0
DW 1024
RV 44,8,6784
M 7,16,4
s_add_u32 s42, s44, s64
LC_ 104
PK 124,16,124,32
s_addc_u32 s43, s45, 0
BR 45
M 7,17,5
WL 6
RA 224,1,21504
PO 126
LS 48,44
WV 10
BW 46
M 10,64,4
LO 17,17,127
LU 127,17
ST 120
M 10,65,5
DW 2048
RV 48,8,6848
HS 25,127
M 2,76,4
WL 8
RA 240,7,0
LL 16,58
LL 105,59
M 2,77,5
s_add_u32 s40, s40, 3072
LA 16
BR 46
PK 127,17,125,33
M 6,6,0
s_addc_u32 s41, s41, 0
LC_ 105
PK 125,17,125,32
s_add_u32 m0, s38, 3072
M 6,7,1
WL 7
RA 228,1,22528
XL 49,64
PO 127
LS 40,45
WV 12
BW 47
M 7,18,0
RA 244,7,256
LO 104,18,104
LU 18,104
M 7,19,1
RA 232,1,23552
BR 47
HZ 26,18
M 10,66,0
WL 6
DW 0
LL 17,60
LL 108,61
M 10,67,1
s_add_u32 s42, s44, s65
LA 17
HX 25
LS 44,46
s_addc_u32 s43, s45, 0
RA 248,7,512
M 2,78,0
WL 2
LC_ 108
W1 25,1024
W1 27,3072
M 2,79,1
s_lshl_b32 s53, s52, 8
RA 236,1,24576
v_add_u32 v4, s39, v4
LS 48,47
s_add_u32 s48, %[alo], s53
RA 252,7,768
M 6,8,2
LO 105,16,105
LU 16,105
s_addc_u32 s49, %[ahi], 0
s_add_u32 s52, s52, %[d]
s_cmp_eq_u32 s52, %[wrap]
s_cselect_b32 s52, %[rst], s52
DW 1024
v_mul_f32 v36, 0xbb2aaaab, v36
M 6,9,3
ST 124
HS 26,16
v_mul_f32 v37, 0xbb2aaaab, v37
v_mul_f32 v38, 0xbb2aaaab, v38
M 7,20,2
s_mul_i32 s53, s51, 49152
LL 18,62
LL 109,63
s_bitcmp1_b64 s[90:91], s51
s_cselect_b64 s[44:45], s[84:85], s[82:83]
s_add_u32 s44, s44, s53
s_addc_u32 s45, s45, 0
s_bitcmp1_b64 s[92:93], s51
s_cselect_b64 s[94:95], s[88:89], s[86:87]
s_add_u32 s94, s94, s53
s_addc_u32 s95, s95, 0
s_add_u32 s51, s51, %[d]
s_cmp_eq_u32 s51, %[wrap]
s_cselect_b32 s51, %[rst], s51
XL 50,128
v_mul_f32 v39, 0xbb2aaaab, v39
M 7,21,3
LA 18
DW 2048
LK 40,240,36,0
LK 44,242,36,2
RA 240,7,1024
v_add_u32 v1, s37, v0
LK 48,244,37,0
s_nop 0
CF 40,40,41
LK 248,246,37,2
RA 244,7,1280
CF 42,42,43
M 10,68,2
LC_ 109
RA 216,1,0
M 10,69,3
DQ 3072
v_and_b32 v22, 0xfffffcff, v1
CF 44,44,45
CF 46,46,47
M 2,80,2
WL 6
LO 108,17,108
LU 17,108
AW 24,40
AW 25,42
M 2,81,3
RA 220,1,1024
HS 26,17
AW 26,44
AW 27,46
LK 40,248,38,0
CF 48,48,49
LK 44,250,38,2
RA 248,7,1536
CF 50,50,51
GZ 36
CF 248,248,249
CF 250,250,251
M 6,10,4
LL 16,72
LL 120,73
M 6,11,5
LA 16
RV 252,22,5120
M 7,22,4
WL 9
LC_ 120
XL 51,192
AW 28,48
M 7,23,5
s_add_u32 s50, s50, %[d]
RA 224,1,2048
s_cmp_eq_u32 s50, %[wrap]
s_cselect_b32 s50, %[rst], s50
v_add_u32 v19, 1, v19
AW 29,50
AW 30,248
AW 31,250
LK 48,252,39,0
CF 40,40,41
LK 248,254,39,2
s_add_u32 s38, s38, 32768
RA 252,7,1792
CF 42,42,43
GS 37
s_cmp_eq_u32 s38, s77
)"
R"(CF 44,44,45
CF 46,46,47
M 10,70,4
WL 8
LO 109,18,109
LU 18,109
M 10,71,5
s_cselect_b32 s38, s76, s38
FP
HS 26,18
M 2,82,4
WL 4
LL 17,74
LL 121,75
M 2,83,5
s_sub_u32 s39, 0, s39
LA 17
RA 228,1,3072
AW 32,40
AW 33,42
Z 0,0,0
LC_ 121
XL 52,256
AW 34,44
AW 35,46
M 0,1,1
FR
HX 26
LK 40,240,40,0
CF 48,48,49
LK 44,242,40,2
RA 240,7,2048
CF 50,50,51
GS 38
CF 248,248,249
CF 250,250,251
Z 1,12,0
WL 8
LO 120,16,120
LU 16,120
M 1,13,1
RA 232,1,4096
HZ 24,16
AW 36,48
Z 4,60,0
LL 18,76
LL 124,77
AW 37,50
AW 38,248
M 4,61,1
LA 18
XL 53,320
AW 39,250
LK 48,244,41,0
CF 40,40,41
LK 248,246,41,2
RA 244,7,2304
CF 42,42,43
GS 39
CF 44,44,45
CF 46,46,47
Z 8,72,0
WL 6
LC_ 124
AW 40,40
M 8,73,1
RA 236,1,6144
AW 41,42
M 0,2,2
WL 7
LO 121,17,121
LU 17,121
M 0,3,3
WL 6
HS 24,17
v_cmp_gt_u32 vcc, v19, v15
AW 42,44
AW 43,46
LK 40,248,42,0
CF 48,48,49
LK 44,250,42,2
RA 248,7,2560
CF 50,50,51
GS 40
CF 248,248,249
CF 250,250,251
M 1,14,2
LL 16,78
LL 125,79
M 1,15,3
LA 16
RA 216,1,7168
M 4,62,2
s_cbranch_vccnz .Lk1s11
.Lk1r11:
LC_ 125
AW 44,48
M 4,63,3
XL 54,384
AW 45,50
AW 46,248
M 8,74,2
WL 4
LO 124,18,124
LU 18,124
AW 47,250
M 8,75,3
RA 220,1,8192
HS 24,18
LK 48,252,43,0
CF 40,40,41
LK 248,254,43,2
RA 252,7,2816
CF 42,42,43
GS 41
CF 44,44,45
CF 46,46,47
M 0,4,4
LL 17,80
LL 106,81
M 0,5,5
LA 17
ds_read_b64 v[14:15], v5 offset:0
AW 48,40
AW 49,42
M 1,16,4
LC_ 106
AW 50,44
AW 51,46
M 1,17,5
RA 224,1,9216
LK 40,240,44,0
CF 48,48,49
LK 44,242,44,2
CF 50,50,51
GS 42
CF 248,248,249
CF 250,250,251
M 4,64,4
WL 6
LO 125,16,125
LU 16,125
M 4,65,5
XL 55,448
HS 24,16
AW 52,48
M 8,76,4
WL 5
LL 18,82
LL 107,83
AW 53,50
AW 54,248
AW 55,250
M 8,77,5
LA 18
RA 228,1,10240
LK 48,244,45,0
CF 40,40,41
LK 248,246,45,2
CF 42,42,43
HX 24
GS 43
CF 44,44,45
CF 46,46,47
M 0,6,0
LC_ 107
W1 24,0
M 0,7,1
W1 26,2048
AW 56,40
M 1,18,0
WL 6
LO 17,17,106
LU 106,17
AW 57,42
AW 58,44
M 1,19,1
RA 232,1,11264
HZ 27,106
AW 59,46
LK 40,248,46,0
CF 48,48,49
LK 44,250,46,2
CF 50,50,51
v_add_f32 v14, v14, v15
GS 44
CF 248,248,249
CF 250,250,251
M 4,66,0
LL 16,84
LL 110,85
M 4,67,1
LA 16
PK 106,17,104,33
M 8,78,0
WL 5
LC_ 110
PK 104,17,104,32
AW 60,48
AW 61,50
AW 62,248
M 8,79,1
RV 252,22,5392
v_add_f32_dpp v14, v14, v14 quad_perm:[1,0,3,2] row_mask:0xf bank_mask:0xf
AW 63,250
LK 48,252,47,0
CF 40,40,41
LK 248,254,47,2
CF 42,42,43
v_add_f32_dpp v14, v14, v14 quad_perm:[2,3,0,1] row_mask:0xf bank_mask:0xf
GS 45
CF 44,44,45
CF 46,46,47
M 0,8,2
WL 6
LO 18,18,107
LU 107,18
M 0,9,3
RA 236,1,12288
HS 27,107
M 1,20,2
LL 17,86
LL 111,87
AW 64,40
AW 65,42
M 1,21,3
LA 17
RV 36,8,6912
PK 107,18,105,33
AW 66,44
AW 67,46
M 4,68,2
LC_ 111
XL 56,512
PK 105,18,105,32
v_add_f32_dpp v14, v14, v14 row_half_mirror row_mask:0xf bank_mask:0xf
M 4,69,3
s_add_u32 s42, s94, s60
RA 216,1,13312
v_cvt_f64_f32 v[14:15], v14
GS 46
s_addc_u32 s43, s95, 0
CF 48,48,49
CF 50,50,51
CF 248,248,249
CF 250,250,251
M 8,80,2
WL 4
LO 16,16,110
LU 110,16
AW 68,48
AW 69,50
AW 70,248
M 8,81,3
RA 220,1,14336
HS 27,110
AW 71,250
s_nop 1
GS 47
s_mov_b64 exec, s[54:55]
SP
s_mov_b64 exec, -1
s_nop 4
v_cndmask_b32 v52, v52, v53, s[78:79]
M 0,10,4
LL 18,96
LL 122,97
v_cndmask_b32 v54, v54, v55, s[78:79]
M 0,11,5
LA 18
ds_read_b64 v[14:15], v5 offset:2048
PK 110,16,108,33
v_cndmask_b32 v52, v52, v54, s[80:81]
M 1,22,4
LC_ 122
PK 108,16,108,32
v_mul_f32 v52, 0x3b2aaaab, v52
M 1,23,5
WL 6
RA 224,1,15360
PO 110
v_med3_f32 v53, v52, v39, v37
M 4,70,4
WL 6
LO 17,17,111
LU 111,17
SU 104
XL 57,576
LG 52,53
s_nop 0
C 52,52,52
M 4,71,5
HS 27,111
v_subrev_u32 v5, s39, v5
v_lshlrev_b32 v52, 16, v52
M 8,82,4
WL 4
LL 16,98
LL 123,99
v_add_f32 v52, v36, v52
M 8,83,5
LA 16
PK 111,17,109,33
C 52,52,52
Z 6,0,0
LC_ 123
PK 109,17,109,32
v_lshlrev_b32 v52, 16, v52
M 6,1,1
RA 228,1,16384
PO 111
v_sqrt_f32 v52, v52
s_nop 0
C 52,52,52
Z 7,12,0
WL 4
LO 18,18,122
LU 122,18
v_lshlrev_b32 v52, 16, v52
M 7,13,1
RA 232,1,17408
HZ 25,122
v_rcp_f32 v52, v52
Z 10,60,0
XL 58,640
LL 17,64
LL 126,65
v_mul_f32 v52, v38, v52
M 10,61,1
s_add_u32 s42, s94, s61
LA 17
PK 122,18,120,33
HX 27
CF 52,52,52
Z 11,72,0
WL 2
LC_ 126
PK 120,18,120,32
ds_bpermute_b32 v13, v28, v52
M 11,73,1
s_addc_u32 s43, s95, 0
RA 236,1,18432
v_add_f32 v14, v14, v15
s_nop 1
v_add_f32_dpp v14, v14, v14 quad_perm:[1,0,3,2] row_mask:0xf bank_mask:0xf
s_nop 1
v_add_f32_dpp v14, v14, v14 quad_perm:[2,3,0,1] row_mask:0xf bank_mask:0xf
M 6,2,2
LO 16,16,123
LU 123,16
M 6,3,3
SU 108
HS 25,123
v_add_f32_dpp v14, v14, v14 row_half_mirror row_mask:0xf bank_mask:0xf
M 7,14,2
s_mul_i32 s53, s50, 25600
LL 18,66
LL 127,67
s_add_u32 s40, %[wlo], s53
s_addc_u32 s41, %[whi], 0
s_mov_b32 m0, s38
M 7,15,3
LA 18
PK 123,16,121,33
v_cvt_f64_f32 v[14:15], v14
M 10,62,2
LC_ 127
PK 121,16,121,32
M 10,63,3
RA 216,1,19456
s_mov_b64 exec, s[54:55]
SP
s_mov_b64 exec, -1
XL 59,704
DW 0
M 11,74,2
WL 3
LO 17,17,126
LU 126,17
M 11,75,3
s_add_u32 s37, s37, 32768
RA 220,1,20480
HS 25,126
AR 248,24
M 6,4,4
s_cmp_eq_u32 s37, s75
LL 16,56
LL 104,57
s_cselect_b32 s37, s74, s37
AR 249,25
M 6,5,5
LA 16
DW 1024
PK 126,17,124,33
AR 250,26
M 7,16,4
s_add_u32 s42, s44, s64
LC_ 104
PK 124,17,124,32
s_addc_u32 s43, s45, 0
)"
R"(AR 251,27
AR 52,28
M 7,17,5
RA 224,1,21504
PO 126
AR 53,29
M 10,64,4
WL 5
LO 18,18,127
LU 127,18
ST 120
AR 54,30
AR 55,31
M 10,65,5
DW 2048
HS 25,127
XS 248
M 11,76,4
WL 3
LL 17,58
LL 105,59
XS 249
XS 250
M 11,77,5
s_add_u32 s40, s40, 3072
LA 17
PK 127,18,125,33
XS 251
M 6,6,0
s_addc_u32 s41, s41, 0
LC_ 105
PK 125,18,125,32
s_add_u32 m0, s38, 3072
AW 24,248
AW 25,249
M 6,7,1
RA 228,1,22528
PO 127
AW 26,250
M 7,18,0
WL 4
LO 104,16,104
LU 16,104
AW 27,251
XS 52
M 7,19,1
RA 232,1,23552
HZ 26,16
XS 53
M 10,66,0
DW 0
LL 18,60
LL 108,61
XS 54
XS 55
M 10,67,1
s_add_u32 s42, s44, s65
LA 18
HX 25
AW 28,52
M 11,78,0
WL 2
LC_ 108
W1 25,1024
W1 27,3072
AW 29,53
AW 30,54
M 11,79,1
s_addc_u32 s43, s45, 0
RA 236,1,24576
v_add_u32 v4, s39, v4
AW 31,55
M 6,8,2
LO 105,17,105
LU 17,105
s_lshl_b32 s53, s52, 8
s_add_u32 s48, %[alo], s53
s_addc_u32 s49, %[ahi], 0
s_add_u32 s52, s52, %[d]
s_cmp_eq_u32 s52, %[wrap]
s_cselect_b32 s52, %[rst], s52
DW 1024
v_min_u32 v35, v35, v13
AR 248,32
M 6,9,3
ST 124
HS 26,17
AR 249,33
M 7,20,2
s_mul_i32 s53, s51, 49152
LL 16,62
LL 109,63
s_bitcmp1_b64 s[90:91], s51
s_cselect_b64 s[44:45], s[84:85], s[82:83]
s_add_u32 s44, s44, s53
s_addc_u32 s45, s45, 0
s_bitcmp1_b64 s[92:93], s51
s_cselect_b64 s[94:95], s[88:89], s[86:87]
s_add_u32 s94, s94, s53
s_addc_u32 s95, s95, 0
s_add_u32 s51, s51, %[d]
s_cmp_eq_u32 s51, %[wrap]
s_cselect_b32 s51, %[rst], s51
AR 250,34
AR 251,35
M 7,21,3
LA 16
DW 2048
XS 248
M 10,68,2
LC_ 109
DQ 3072
XS 249
XS 250
M 10,69,3
s_add_u32 s50, s50, %[d]
v_add_u32 v1, s37, v0
XS 251
M 11,80,2
WL 2
LO 108,18,108
LU 18,108
AW 32,248
AW 33,249
M 11,81,3
s_cmp_eq_u32 s50, %[wrap]
RA 216,1,0
HS 26,18
AW 34,250
M 6,10,4
s_cselect_b32 s50, %[rst], s50
LL 17,72
LL 120,73
AW 35,251
AR 52,36
M 6,11,5
LA 17
v_and_b32 v22, 0xfffffcff, v1
AR 53,37
M 7,22,4
LC_ 120
RA 220,1,1024
AR 54,38
AR 55,39
M 7,23,5
RV 252,22,5120
v_add_u32 v19, 1, v19
XS 52
M 10,70,4
WL 5
LO 109,16,109
LU 16,109
XS 53
XS 54
M 10,71,5
s_add_u32 s38, s38, 32768
RA 224,1,2048
HS 26,16
XS 55
M 11,82,4
WL 1
LL 18,74
LL 121,75
AW 36,52
AW 37,53
M 11,83,5
s_cmp_eq_u32 s38, s77
LA 18
RA 228,1,3072
s_cselect_b32 s38, s76, s38
FP
s_sub_u32 s39, 0, s39
AW 38,54
Z 0,0,0
LC_ 121
HX 26
AW 39,55
M 0,1,1
AR 248,40
AR 249,41
Z 1,12,0
LO 120,17,120
LU 17,120
AR 250,42
M 1,13,1
RA 232,1,4096
HZ 24,17
AR 251,43
XS 248
Z 2,24,0
LL 16,88
LL 124,89
XS 249
M 2,25,1
LA 16
XS 250
XS 251
Z 3,36,0
LC_ 124
AW 40,248
M 3,37,1
AW 41,249
AW 42,250
Z 4,60,0
WL 3
LO 121,18,121
LU 18,121
AW 43,251
M 4,61,1
RA 236,1,6144
HS 24,18
AR 52,44
AR 53,45
Z 5,72,0
LL 17,90
LL 125,91
AR 54,46
M 5,73,1
LA 17
FR
AR 55,47
XS 52
M 0,2,2
LC_ 125
XS 53
M 0,3,3
XS 54
XS 55
M 1,14,2
WL 4
LO 124,16,124
LU 16,124
AW 44,52
M 1,15,3
RA 216,1,7168
HS 24,16
AW 45,53
AW 46,54
M 2,26,2
LL 18,80
LL 106,81
AW 47,55
M 2,27,3
LA 18
AR 248,48
AR 249,49
M 3,38,2
WL 3
LC_ 106
v_cmp_gt_u32 vcc, v19, v15
AR 250,50
M 3,39,3
WV 32
BW 48
AR 251,51
XS 248
M 4,62,2
WL 4
LO 125,17,125
LU 17,125
XS 249
M 4,63,3
RA 220,1,8192
HS 24,17
XS 250
XS 251
M 5,74,2
LL 16,82
LL 107,83
AW 48,248
M 5,75,3
s_cbranch_vccnz .Lk1s12
.Lk1r12:
LA 16
RV 40,8,6144
AW 49,249
AW 50,250
M 0,4,4
s_mov_b32 m0, %[pm]
LC_ 107
DF
AW 51,251
M 0,5,5
RV 44,8,6208
RV 48,8,6272
AR 52,52
AR 53,53
M 1,16,4
WL 7
LO 18,18,106
LU 106,18
AR 54,54
M 1,17,5
RA 224,1,9216
HZ 27,106
AR 55,55
XS 52
M 2,28,4
LL 17,84
LL 110,85
XS 53
M 2,29,5
LA 17
PK 106,18,104,33
XS 54
XS 55
M 3,40,4
LC_ 110
PK 104,18,104,32
AW 52,52
M 3,41,5
BR 48
HX 24
AW 53,53
AW 54,54
M 4,64,4
WL 6
LO 16,16,107
LU 107,16
AW 55,55
M 4,65,5
RA 228,1,10240
HS 27,107
AR 248,56
M 5,76,4
LL 18,86
LL 111,87
AR 249,57
AR 250,58
M 5,77,5
s_add_u32 s42, s94, s60
LA 18
PK 107,16,105,33
AR 251,59
M 0,6,0
s_addc_u32 s43, s95, 0
LC_ 111
PK 105,16,105,32
XS 248
XS 249
M 0,7,1
W1 24,0
XS 250
M 1,18,0
WL 5
LO 17,17,110
LU 110,17
W1 26,2048
XS 251
AW 56,248
M 1,19,1
RA 232,1,11264
HS 27,110
AW 57,249
M 2,30,0
LL 16,96
LL 122,97
AW 58,250
AW 59,251
M 2,31,1
LA 16
PK 110,17,108,33
AR 52,60
M 3,42,0
LC_ 122
PK 108,17,108,32
AR 53,61
AR 54,62
M 3,43,1
RV 252,22,5392
PO 110
AR 55,63
M 4,66,0
WL 6
LO 18,18,111
LU 111,18
XS 52
XS 53
M 4,67,1
RA 236,1,12288
HS 27,111
XS 54
M 5,78,0
LL 17,98
LL 123,99
XS 55
AW 60,52
M 5,79,1
LA 17
PK 111,18,109,33
AW 61,53
M 0,8,2
LC_ 123
PK 109,18,109,32
AW 62,54
AW 63,55
M 0,9,3
SU 104
PO 111
AR 248,64
)"
R"(M 1,20,2
WL 4
LO 16,16,122
LU 122,16
AR 249,65
AR 250,66
M 1,21,3
s_add_u32 s42, s94, s61
RA 216,1,13312
HZ 25,122
AR 251,67
M 2,32,2
s_addc_u32 s43, s95, 0
LL 18,100
LL 126,101
XS 248
XS 249
M 2,33,3
LA 18
PK 122,16,120,33
XS 250
M 3,44,2
LC_ 126
PK 120,16,120,32
XS 251
AW 64,248
M 3,45,3
WV 30
SU 108
HX 27
AW 65,249
BW 49
M 4,68,2
WL 4
LO 17,17,123
LU 123,17
AW 66,250
AW 67,251
M 4,69,3
RA 220,1,14336
BR 49
HS 25,123
AR 52,68
LZ 40,48
WV 27
BW 50
M 5,80,2
LL 16,102
LL 127,103
AR 53,69
AR 54,70
M 5,81,3
s_add_u32 s42, s44, s64
LA 16
PK 123,17,121,33
AR 55,71
M 0,10,4
s_addc_u32 s43, s45, 0
LC_ 127
PK 121,17,121,32
XS 52
XS 53
M 0,11,5
ST 120
RV 40,8,6336
XS 54
M 1,22,4
WL 7
BR 50
LO 18,18,126
LU 126,18
XS 55
AW 68,52
M 1,23,5
WL 5
RA 224,1,15360
HS 25,126
AW 69,53
LS 44,49
WV 25
BW 51
M 2,34,4
LL 17,56
LL 104,57
AW 70,54
AW 71,55
M 2,35,5
LA 17
PK 126,18,124,33
M 3,46,4
LC_ 104
RV 44,8,6400
PK 124,18,124,32
M 3,47,5
ds_read_b64 v[14:15], v5 offset:0
BR 51
PO 126
M 4,70,4
WL 9
LO 16,16,127
LU 127,16
M 4,71,5
WL 7
RA 228,1,16384
HS 25,127
LS 48,50
WV 24
BW 52
M 5,82,4
LL 18,58
LL 105,59
M 5,83,5
LA 18
RV 48,8,6464
PK 127,16,125,33
Z 6,0,0
s_add_u32 s42, s44, s65
LC_ 105
PK 125,16,125,32
M 6,1,1
s_addc_u32 s43, s45, 0
BR 52
PO 127
Z 7,12,0
WL 9
LO 104,17,104
LU 17,104
M 7,13,1
WL 6
RA 232,1,17408
HZ 26,17
LS 40,51
WV 23
BW 53
Z 8,24,0
WV 22
LL 16,60
LL 108,61
M 8,25,1
WL 2
LA 16
ST 124
RV 40,8,6528
Z 9,36,0
s_mul_i32 s53, s50, 25600
LC_ 108
BR 53
M 9,37,1
s_add_u32 s40, %[wlo], s53
v_add_f32 v14, v14, v15
HX 25
LS 44,52
s_addc_u32 s41, %[whi], 0
BW 54
Z 10,60,0
LO 105,18,105
LU 18,105
M 10,61,1
s_mov_b32 m0, s38
RA 236,1,18432
HS 26,18
Z 11,72,0
RV 44,8,6592
LL 17,62
LL 109,63
M 11,73,1
LA 17
W1 25,1024
BR 54
M 6,2,2
LC_ 109
W1 27,3072
M 6,3,3
WL 8
DW 0
v_add_f32_dpp v14, v14, v14 quad_perm:[1,0,3,2] row_mask:0xf bank_mask:0xf
LS 48,53
WV 23
BW 55
M 7,14,2
LO 108,16,108
LU 16,108
M 7,15,3
s_add_u32 s37, s37, 32768
RA 216,1,19456
HS 26,16
M 8,26,2
s_cmp_eq_u32 s37, s75
RV 48,8,6656
LL 18,64
LL 112,65
M 8,27,3
s_cselect_b32 s37, s74, s37
LA 18
DW 1024
BR 55
M 9,38,2
LC_ 112
v_add_f32_dpp v14, v14, v14 quad_perm:[2,3,0,1] row_mask:0xf bank_mask:0xf
M 9,39,3
WL 8
v_add_f32_dpp v14, v14, v14 row_half_mirror row_mask:0xf bank_mask:0xf
v_add_u32 v4, s39, v4
LS 40,54
WV 23
BW 56
M 10,62,2
WL 8
LO 109,17,109
LU 17,109
M 10,63,3
RA 220,1,20480
RV 40,8,6720
HS 26,17
M 11,74,2
BR 56
LL 16,66
LL 113,67
M 11,75,3
LA 16
DW 2048
M 6,4,4
WL 6
LC_ 113
v_cvt_f64_f32 v[14:15], v14
M 6,5,5
s_mov_b64 exec, s[54:55]
SP
s_mov_b64 exec, -1
LS 44,55
WV 21
BW 57
M 7,16,4
WL 3
RV 44,8,6784
LO 112,18,112
LU 18,112
M 7,17,5
s_mul_i32 s53, s51, 49152
RA 224,1,21504
HZ 29,18
M 8,28,4
s_bitcmp1_b64 s[90:91], s51
BR 57
LL 17,68
LL 116,69
M 8,29,5
s_cselect_b64 s[44:45], s[84:85], s[82:83]
LA 17
ds_read_b64 v[14:15], v5 offset:2048
LS 48,56
s_add_u32 s44, s44, s53
BW 58
M 9,40,4
s_addc_u32 s45, s45, 0
LC_ 116
RV 48,8,6848
M 9,41,5
s_bitcmp1_b64 s[92:93], s51
RA 240,7,0
BR 58
M 10,64,4
WL 11
LO 113,16,113
LU 16,113
M 10,65,5
s_cselect_b64 s[94:95], s[88:89], s[86:87]
RA 228,1,22528
HS 29,16
M 11,76,4
WL 8
LL 18,70
LL 117,71
M 11,77,5
s_add_u32 s94, s94, s53
LA 18
v_subrev_u32 v5, s39, v5
M 6,6,0
s_addc_u32 s95, s95, 0
LC_ 117
M 6,7,1
s_add_u32 s51, s51, %[d]
LS 40,57
s_cmp_eq_u32 s51, %[wrap]
WV 18
BW 59
M 7,18,0
WL 4
RA 244,7,256
LO 116,17,116
LU 17,116
M 7,19,1
s_cselect_b32 s51, %[rst], s51
RA 232,1,23552
HS 29,17
M 8,30,0
s_mul_i32 s53, %[d], 16384
BR 59
LL 16,72
LL 120,73
M 8,31,1
s_add_u32 s62, s53, %[sb1]
LA 16
v_add_f32 v14, v14, v15
s_add_u32 s63, s62, 512
LS 44,58
s_add_u32 s40, s40, 3072
RA 248,7,512
v_add_f32_dpp v14, v14, v14 quad_perm:[1,0,3,2] row_mask:0xf bank_mask:0xf
M 9,42,0
s_addc_u32 s41, s41, 0
LC_ 120
v_add_f32_dpp v14, v14, v14 quad_perm:[2,3,0,1] row_mask:0xf bank_mask:0xf
s_add_u32 m0, s38, 3072
M 9,43,1
DW 0
v_add_f32_dpp v14, v14, v14 row_half_mirror row_mask:0xf bank_mask:0xf
M 10,66,0
WL 7
LO 117,18,117
LU 18,117
M 10,67,1
WL 3
RA 236,1,24576
HS 29,18
LS 48,59
RA 252,7,768
v_cvt_f64_f32 v[14:15], v14
M 11,78,0
LL 17,74
LL 121,75
s_nop 2
v_mul_f32 v36, 0xbb2aaaab, v36
M 11,79,1
LA 17
DW 1024
v_mul_f32 v37, 0xbb2aaaab, v37
v_mul_f32 v38, 0xbb2aaaab, v38
M 6,8,2
LC_ 121
s_mov_b64 exec, s[54:55]
SP
s_mov_b64 exec, -1
v_mul_f32 v39, 0xbb2aaaab, v39
M 6,9,3
DW 2048
v_add_u32 v1, s37, v0
LK 40,240,48,0
LK 44,242,48,2
s_lshl_b32 s53, s52, 8
RA 240,7,1024
v_pk_add_f16 v26, v26, v29
LK 48,244,49,0
CF 40,40,41
LK 248,246,49,2
s_add_u32 s48, %[alo], s53
RA 244,7,1280
CF 42,42,43
M 7,20,2
WL 6
LO 120,16,120
LU 16,120
M 7,21,3
s_addc_u32 s49, %[ahi], 0
RA 216,1,0
HZ 24,16
CF 44,44,45
M 8,32,2
s_add_u32 s52, s52, %[d]
LL 18,76
LL 124,77
CF 46,46,47
AW 72,40
M 8,33,3
s_cmp_eq_u32 s52, %[wrap]
LA 18
v_and_b32 v22, 0xfffffcff, v1
AW 73,42
AW 74,44
M 9,44,2
s_cselect_b32 s52, %[rst], s52
LC_ 124
DQ 3072
AW 75,46
M 9,45,3
HX 26
LK 40,248,50,0
CF 48,48,49
LK 44,250,50,2
RA 248,7,1536
CF 50,50,51
GZ 48
CF 248,248,249
CF 250,250,251
M 10,68,2
WL 6
LO 121,17,121
LU 17,121
M 10,69,3
s_add_u32 s50, s50, %[d]
)"
R"(RA 220,1,1024
HS 24,17
AW 76,48
M 11,80,2
LL 16,78
LL 125,79
AW 77,50
AW 78,248
AW 79,250
M 11,81,3
s_cmp_eq_u32 s50, %[wrap]
LA 16
RV 252,22,5120
LK 48,252,51,0
CF 40,40,41
LK 248,254,51,2
s_cselect_b32 s50, %[rst], s50
RA 252,7,1792
CF 42,42,43
GS 49
CF 44,44,45
CF 46,46,47
M 6,10,4
LC_ 125
AW 80,40
M 6,11,5
v_add_u32 v19, 1, v19
AW 81,42
M 7,22,4
WL 6
LO 124,18,124
LU 18,124
M 7,23,5
s_add_u32 s38, s38, 32768
RA 224,1,2048
HS 24,18
AW 82,44
AW 83,46
LK 40,240,52,0
CF 48,48,49
LK 44,242,52,2
s_cmp_eq_u32 s38, s77
RA 240,7,2048
CF 50,50,51
GS 50
s_cselect_b32 s38, s76, s38
CF 248,248,249
CF 250,250,251
M 8,34,4
LL 17,80
LL 106,81
M 8,35,5
LA 17
FP
M 9,46,4
s_sub_u32 s39, 0, s39
LC_ 106
AW 84,48
M 9,47,5
AW 85,50
AW 86,248
M 10,70,4
WL 5
LO 125,16,125
LU 16,125
AW 87,250
M 10,71,5
RA 228,1,3072
HS 24,16
LK 48,244,53,0
CF 40,40,41
LK 248,246,53,2
RA 244,7,2304
CF 42,42,43
GS 51
CF 44,44,45
CF 46,46,47
M 11,82,4
LL 18,82
LL 107,83
M 11,83,5
LA 18
HX 24
Z 0,0,0
LC_ 107
AW 88,40
M 0,1,1
AW 89,42
AW 90,44
AW 91,46
LK 40,248,54,0
CF 48,48,49
LK 44,250,54,2
RA 248,7,2560
CF 50,50,51
GS 52
CF 248,248,249
CF 250,250,251
Z 1,12,0
WL 5
LO 17,17,106
LU 106,17
M 1,13,1
RA 232,1,4096
HZ 27,106
Z 2,24,0
LL 16,84
LL 110,85
M 2,25,1
LA 16
PK 106,17,104,33
AW 92,48
Z 3,36,0
LC_ 110
PK 104,17,104,32
AW 93,50
AW 94,248
AW 95,250
M 3,37,1
LK 48,252,55,0
CF 40,40,41
LK 248,254,55,2
RA 252,7,2816
CF 42,42,43
GS 53
CF 44,44,45
CF 46,46,47
Z 4,60,0
WL 5
LO 18,18,107
LU 107,18
M 4,61,1
RA 236,1,6144
HS 27,107
Z 5,72,0
LL 17,86
LL 111,87
AW 96,40
AW 97,42
M 5,73,1
s_add_u32 s42, s94, s60
LA 17
PK 107,18,105,33
AW 98,44
AW 99,46
LK 40,240,56,0
CF 48,48,49
LK 44,242,56,2
s_addc_u32 s43, s95, 0
CF 50,50,51
GS 54
CF 248,248,249
CF 250,250,251
M 0,2,2
LC_ 111
PK 105,18,105,32
M 0,3,3
FR
AW 100,48
M 1,14,2
WL 5
LO 16,16,110
LU 110,16
M 1,15,3
RA 216,1,7168
HS 27,110
AW 101,50
AW 102,248
M 2,26,2
LL 18,88
LL 114,89
AW 103,250
M 2,27,3
LA 18
PK 110,16,108,33
LK 48,244,57,0
CF 40,40,41
LK 248,246,57,2
CF 42,42,43
GS 55
CF 44,44,45
CF 46,46,47
M 3,38,2
LC_ 114
PK 108,16,108,32
M 3,39,3
SU 104
PO 110
AW 104,40
M 4,62,2
WL 4
LO 17,17,111
LU 111,17
AW 105,42
AW 106,44
AW 107,46
M 4,63,3
RA 220,1,8192
HS 27,111
LK 40,248,58,0
CF 48,48,49
LK 44,250,58,2
WL 4
CF 50,50,51
v_cmp_gt_u32 vcc, v19, v15
GS 56
CF 248,248,249
CF 250,250,251
M 5,74,2
LL 16,90
LL 115,91
M 5,75,3
s_mov_b32 m0, %[pm]
LA 16
PK 111,17,109,33
M 0,4,4
s_cbranch_vccnz .Lk1s13
.Lk1r13:
LC_ 115
PK 109,17,109,32
AW 108,48
AW 109,50
M 0,5,5
DF
PO 111
AW 110,248
AW 111,250
LK 48,252,59,0
CF 40,40,41
LK 248,254,59,2
s_add_u32 s42, s94, s61
CF 42,42,43
GS 57
s_addc_u32 s43, s95, 0
CF 44,44,45
CF 46,46,47
M 1,16,4
WL 3
LO 18,18,114
LU 114,18
M 1,17,5
RA 224,1,9216
HZ 29,114
M 2,28,4
LL 17,92
LL 118,93
AW 112,40
M 2,29,5
LA 17
PK 114,18,112,33
AW 113,42
AW 114,44
M 3,40,4
LC_ 118
RV 36,8,6912
PK 112,18,112,32
AW 115,46
M 3,41,5
W1 24,0
GS 58
CF 48,48,49
CF 50,50,51
M 4,64,4
WL 5
LO 16,16,115
LU 115,16
CF 248,248,249
CF 250,250,251
AW 116,48
M 4,65,5
RA 228,1,10240
HS 29,115
AW 117,50
AW 118,248
M 5,76,4
LL 18,94
LL 119,95
AW 119,250
M 5,77,5
LA 18
PK 115,16,113,33
GS 59
W1 26,2048
s_nop 6
v_cndmask_b32 v52, v52, v53, s[78:79]
M 0,6,0
LC_ 119
PK 113,16,113,32
v_cndmask_b32 v54, v54, v55, s[78:79]
M 0,7,1
SU 108
v_cndmask_b32 v52, v52, v54, s[80:81]
M 1,18,0
WL 5
LO 17,17,118
LU 118,17
v_mul_f32 v52, 0x3b2aaaab, v52
M 1,19,1
s_add_u32 s42, s94, s62
RA 232,1,11264
HS 29,118
v_med3_f32 v53, v52, v39, v37
M 2,30,0
s_addc_u32 s43, s95, 0
LL 16,96
LL 122,97
LG 52,53
M 2,31,1
LA 16
PK 118,17,116,33
C 52,52,52
M 3,42,0
LC_ 122
PK 116,17,116,32
v_lshlrev_b32 v52, 16, v52
M 3,43,1
RV 252,22,5392
PO 118
v_add_f32 v52, v36, v52
M 4,66,0
WL 4
LO 18,18,119
LU 119,18
C 52,52,52
M 4,67,1
RA 236,1,12288
HS 29,119
M 5,78,0
LL 17,98
LL 123,99
v_lshlrev_b32 v52, 16, v52
M 5,79,1
LA 17
PK 119,18,117,33
v_sqrt_f32 v52, v52
M 0,8,2
LC_ 123
PK 117,18,117,32
C 52,52,52
M 0,9,3
SU 112
PO 119
v_lshlrev_b32 v52, 16, v52
M 1,20,2
WL 4
LO 16,16,122
LU 122,16
v_rcp_f32 v52, v52
M 1,21,3
s_add_u32 s42, s94, s63
RA 216,1,13312
HZ 25,122
v_mul_f32 v52, v38, v52
M 2,32,2
s_addc_u32 s43, s95, 0
LL 18,100
LL 126,101
CF 52,52,52
M 2,33,3
LA 18
PK 122,16,120,33
ds_bpermute_b32 v13, v28, v52
M 3,44,2
LC_ 126
PK 120,16,120,32
M 3,45,3
SU 116
v_pk_add_f16 v27, v27, v29
M 4,68,2
WL 4
LO 17,17,123
LU 123,17
M 4,69,3
RA 220,1,14336
HS 25,123
M 5,80,2
LL 16,102
LL 127,103
M 5,81,3
s_add_u32 s42, s44, s64
LA 16
PK 123,17,121,33
M 0,10,4
s_addc_u32 s43, s45, 0
LC_ 127
PK 121,17,121,32
M 0,11,5
ST 120
HX 27
M 1,22,4
WL 3
LO 18,18,126
LU 126,18
M 1,23,5
RA 224,1,15360
HS 25,126
AR 248,72
M 2,34,4
LL 17,56
LL 104,57
)"
R"(AR 249,73
M 2,35,5
LA 17
PK 126,18,124,33
AR 250,74
M 3,46,4
LC_ 104
PK 124,18,124,32
AR 251,75
M 3,47,5
ds_read_b64 v[14:15], v5 offset:0
PO 126
AR 52,76
M 4,70,4
WL 4
LO 16,16,127
LU 127,16
AR 53,77
M 4,71,5
RA 228,1,16384
HS 25,127
AR 54,78
M 5,82,4
LL 18,58
LL 105,59
AR 55,79
M 5,83,5
LA 18
PK 127,16,125,33
XS 248
Z 6,0,0
s_add_u32 s42, s44, s65
LC_ 105
PK 125,16,125,32
XS 249
M 6,1,1
s_addc_u32 s43, s45, 0
PO 127
HX 25
XS 250
Z 7,12,0
WL 4
LO 104,17,104
LU 17,104
XS 251
AW 72,248
M 7,13,1
RA 232,1,17408
HZ 26,17
AW 73,249
Z 8,24,0
LL 16,60
LL 108,61
AW 74,250
M 8,25,1
LA 16
W1 25,1024
AW 75,251
Z 9,36,0
LC_ 108
W1 27,3072
XS 52
M 9,37,1
WL 8
ST 124
v_add_f32 v14, v14, v15
XS 53
Z 10,60,0
WL 5
LO 105,18,105
LU 18,105
XS 54
M 10,61,1
s_mul_i32 s53, s50, 25600
RA 236,1,18432
HS 26,18
XS 55
Z 11,72,0
LL 17,62
LL 109,63
AW 76,52
M 11,73,1
s_add_u32 s40, %[wlo], s53
LA 17
v_add_f32_dpp v14, v14, v14 quad_perm:[1,0,3,2] row_mask:0xf bank_mask:0xf
AW 77,53
M 6,2,2
s_addc_u32 s41, %[whi], 0
LC_ 109
v_add_f32_dpp v14, v14, v14 quad_perm:[2,3,0,1] row_mask:0xf bank_mask:0xf
AW 78,54
AW 79,55
M 6,3,3
s_mov_b32 m0, s38
s_nop 0
DW 0
v_add_f32_dpp v14, v14, v14 row_half_mirror row_mask:0xf bank_mask:0xf
v_min_u32 v35, v35, v13
M 7,14,2
WL 4
LO 108,16,108
LU 16,108
AR 248,80
M 7,15,3
s_add_u32 s37, s37, 32768
RA 216,1,19456
HS 26,16
AR 249,81
M 8,26,2
s_cmp_eq_u32 s37, s75
LL 18,64
LL 112,65
AR 250,82
M 8,27,3
s_cselect_b32 s37, s74, s37
LA 18
v_cvt_f64_f32 v[14:15], v14
AR 251,83
M 9,38,2
s_mul_i32 s53, %[d], 393216
LC_ 112
s_mov_b64 exec, s[54:55]
SP
s_mov_b64 exec, -1
XS 248
M 9,39,3
s_add_u32 s53, s53, s57
ds_read_b64 v[14:15], v5 offset:2048
v_add_u32 v4, s39, v4
XS 249
M 10,62,2
WL 4
LO 109,17,109
LU 17,109
XS 250
M 10,63,3
s_add_u32 s46, %[xlo], s53
RA 220,1,20480
HS 26,17
XS 251
M 11,74,2
LL 16,66
LL 113,67
AW 80,248
AW 81,249
M 11,75,3
s_addc_u32 s47, %[xhi], 0
LA 16
v_subrev_u32 v5, s39, v5
AW 82,250
M 6,4,4
s_bitcmp1_b64 s[92:93], 48
LC_ 113
AW 83,251
M 6,5,5
s_cselect_b32 s58, 16, 0
AR 52,84
AR 53,85
M 7,16,4
WL 4
LO 112,18,112
LU 18,112
M 7,17,5
s_lshr_b32 s58, 0xda43cb52, s58
RA 224,1,21504
HZ 29,18
AR 54,86
M 8,28,4
LL 17,68
LL 116,69
AR 55,87
M 8,29,5
WL 4
LA 17
v_add_f32 v14, v14, v15
XS 52
M 9,40,4
LC_ 116
v_add_f32_dpp v14, v14, v14 quad_perm:[1,0,3,2] row_mask:0xf bank_mask:0xf
v_lshrrev_b32 v3, 6, v0
v_and_b32 v3, 12, v3
v_bfe_u32 v3, s58, v3, 4
v_mad_u32_u24 v3, v3, s97, v21
XS 53
M 9,41,5
DW 1024
v_add_f32_dpp v14, v14, v14 quad_perm:[2,3,0,1] row_mask:0xf bank_mask:0xf
XS 54
M 10,64,4
WL 3
LO 113,16,113
LU 16,113
XS 55
AW 84,52
M 10,65,5
RA 228,1,22528
HS 29,16
AW 85,53
M 11,76,4
LL 18,70
LL 117,71
AW 86,54
M 11,77,5
LA 18
XL 60,0
AW 87,55
M 6,6,0
LC_ 117
DW 2048
AR 248,88
M 6,7,1
XL 61,64
v_add_f32_dpp v14, v14, v14 row_half_mirror row_mask:0xf bank_mask:0xf
AR 249,89
M 7,18,0
WL 3
LO 116,17,116
LU 17,116
AR 250,90
M 7,19,1
s_mul_i32 s53, s51, 49152
RA 232,1,23552
HS 29,17
AR 251,91
M 8,30,0
s_bitcmp1_b64 s[90:91], s51
LL 16,72
LL 120,73
XS 248
M 8,31,1
s_cselect_b64 s[44:45], s[84:85], s[82:83]
LA 16
v_cvt_f64_f32 v[14:15], v14
s_add_u32 s44, s44, s53
s_addc_u32 s45, s45, 0
s_bitcmp1_b64 s[92:93], s51
s_cselect_b64 s[94:95], s[88:89], s[86:87]
s_add_u32 s94, s94, s53
s_addc_u32 s95, s95, 0
s_add_u32 s51, s51, %[d]
s_cmp_eq_u32 s51, %[wrap]
s_cselect_b32 s51, %[rst], s51
XS 249
M 9,42,0
s_add_u32 s40, s40, 3072
LC_ 120
s_mov_b64 exec, s[54:55]
SP
s_mov_b64 exec, -1
s_addc_u32 s41, s41, 0
s_add_u32 m0, s38, 3072
XS 250
XS 251
M 9,43,1
XL 62,128
AW 88,248
M 10,66,0
WL 3
LO 117,18,117
LU 18,117
AW 89,249
M 10,67,1
RA 236,1,24576
HS 29,18
AW 90,250
M 11,78,0
LL 17,74
LL 121,75
AW 91,251
M 11,79,1
LA 17
DW 0
AR 52,92
M 6,8,2
LC_ 121
XL 63,192
AR 53,93
M 6,9,3
DW 1024
v_add_u32 v1, s37, v0
AR 54,94
M 7,20,2
WL 3
LO 120,16,120
LU 16,120
AR 55,95
M 7,21,3
RA 216,1,0
HZ 24,16
XS 52
M 8,32,2
LL 18,76
LL 124,77
XS 53
XS 54
M 8,33,3
LA 18
XL 64,256
XS 55
M 9,44,2
LC_ 124
DW 2048
AW 92,52
M 9,45,3
XL 65,320
v_and_b32 v22, 0xfffffcff, v1
AW 93,53
M 10,68,2
WL 3
LO 121,17,121
LU 17,121
AW 94,54
M 10,69,3
s_lshl_b32 s53, s52, 8
RA 220,1,1024
HS 24,17
AW 95,55
M 11,80,2
LL 16,78
LL 125,79
AR 248,96
M 11,81,3
s_add_u32 s48, %[alo], s53
LA 16
RV 252,22,5120
AR 249,97
M 6,10,4
s_addc_u32 s49, %[ahi], 0
LC_ 125
v_pk_add_f16 v26, v26, v29
AR 250,98
M 6,11,5
s_add_u32 s52, s52, %[d]
HX 26
AR 251,99
M 7,22,4
WL 4
LO 124,18,124
LU 18,124
XS 248
XS 249
M 7,23,5
s_cmp_eq_u32 s52, %[wrap]
RA 224,1,2048
HS 24,18
XS 250
M 8,34,4
s_cselect_b32 s52, %[rst], s52
LL 17,80
LL 106,81
XS 251
M 8,35,5
LA 17
DQ 3072
AW 96,248
M 9,46,4
LC_ 106
XL 66,384
AW 97,249
M 9,47,5
s_add_u32 s50, s50, %[d]
AW 98,250
AW 99,251
M 10,70,4
WL 3
LO 125,16,125
LU 16,125
M 10,71,5
RA 228,1,3072
HS 24,16
AR 52,100
M 11,82,4
LL 18,82
LL 107,83
AR 53,101
M 11,83,5
LA 18
HX 24
s_cmp_eq_u32 s50, %[wrap]
s_cselect_b32 s50, %[rst], s50
XL 67,448
AR 54,102
Z 0,0,0
WV 38
LC_ 107
XL 68,512
v_add_u32 v19, 1, v19
s_add_u32 s38, s38, 32768
s_cmp_eq_u32 s38, s77
s_cselect_b32 s38, s76, s38
FP
s_sub_u32 s39, 0, s39
AR 55,103
XS 52
M 0,1,1
XL 69,576
XS 53
Z 1,12,0
WL 4
LO 17,17,106
)"
R"(LU 106,17
XS 54
M 1,13,1
RA 232,1,4096
HZ 27,106
XS 55
Z 2,24,0
LL 16,84
LL 110,85
AW 100,52
M 2,25,1
LA 16
PK 106,17,104,33
AW 101,53
Z 3,36,0
WL 5
LC_ 110
XL 70,640
PK 104,17,104,32
AW 102,54
M 3,37,1
RA 236,1,6144
FR
AW 103,55
M 0,2,2
WL 6
LO 18,18,107
LU 107,18
AR 248,104
M 0,3,3
XL 71,704
HS 27,107
AR 249,105
M 1,14,2
LL 17,86
LL 111,87
AR 250,106
AR 251,107
M 1,15,3
s_add_u32 s42, s94, s60
LA 17
PK 107,18,105,33
XS 248
M 2,26,2
s_addc_u32 s43, s95, 0
LC_ 111
PK 105,18,105,32
XS 249
M 2,27,3
RA 216,1,7168
XS 250
M 3,38,2
WL 4
LO 16,16,110
LU 110,16
XS 251
M 3,39,3
RA 220,1,8192
HS 27,110
AW 104,248
M 0,4,4
LL 18,88
LL 114,89
AW 105,249
M 0,5,5
LA 18
PK 110,16,108,33
AW 106,250
M 1,16,4
LC_ 114
PK 108,16,108,32
AW 107,251
M 1,17,5
RA 224,1,9216
PO 110
AR 52,108
M 2,28,4
WL 5
LO 17,17,111
LU 111,17
SU 104
AR 53,109
AR 54,110
M 2,29,5
HS 27,111
v_cmp_gt_u32 vcc, v19, v15
AR 55,111
M 3,40,4
WL 3
LL 16,90
LL 115,91
XS 52
M 3,41,5
WV 18
LA 16
PK 111,17,109,33
XS 53
M 0,6,0
s_cbranch_vccnz .Lk1s14
.Lk1r14:
LC_ 115
PK 109,17,109,32
XS 54
M 0,7,1
s_add_u32 s42, s94, s61
RA 228,1,10240
PO 111
XS 55
BW 60
M 1,18,0
WL 5
LO 18,18,114
LU 114,18
AW 108,52
M 1,19,1
s_addc_u32 s43, s95, 0
RA 232,1,11264
RV 40,8,6144
HZ 29,114
AW 109,53
M 2,30,0
RV 44,8,6208
LL 17,92
LL 118,93
AW 110,54
M 2,31,1
LA 17
RV 48,8,6272
PK 114,18,112,33
AW 111,55
M 3,42,0
WL 6
LC_ 118
BR 60
PK 112,18,112,32
AR 248,112
AR 249,113
M 3,43,1
WV 16
RV 252,22,5392
ds_read_b64 v[14:15], v5 offset:0
W1 24,0
W1 26,2048
BW 61
AR 250,114
M 0,8,2
BR 61
LO 16,16,115
LU 115,16
AR 251,115
M 0,9,3
WL 6
RA 236,1,12288
HS 29,115
SU 108
XS 248
LZ 40,60
WV 15
BW 62
M 1,20,2
LL 18,94
LL 119,95
XS 249
M 1,21,3
s_add_u32 s42, s94, s62
LA 18
RV 40,8,6336
PK 115,16,113,33
XS 250
M 2,32,2
s_addc_u32 s43, s95, 0
LC_ 119
BR 62
PK 113,16,113,32
XS 251
M 2,33,3
WL 6
RA 216,1,13312
AW 112,248
LS 44,61
WV 13
BW 63
M 3,44,2
WL 7
LO 17,17,118
LU 118,17
AW 113,249
M 3,45,3
RA 220,1,14336
RV 44,8,6400
HS 29,118
AW 114,250
M 0,10,4
BR 63
LL 16,96
LL 122,97
AW 115,251
AR 52,116
M 0,11,5
WL 5
LA 16
PK 118,17,116,33
AR 53,117
LS 48,62
WV 11
BW 64
M 1,22,4
LC_ 122
PK 116,17,116,32
AR 54,118
M 1,23,5
RA 224,1,15360
RV 48,8,6464
PO 118
AR 55,119
M 2,34,4
LO 18,18,119
LU 119,18
SU 112
XS 52
M 2,35,5
BR 64
HS 29,119
XS 53
M 3,46,4
WL 8
LL 17,98
LL 123,99
XS 54
M 3,47,5
WL 6
LA 17
PK 119,18,117,33
XS 55
LS 40,63
WV 10
BW 65
Z 6,0,0
s_add_u32 s42, s94, s63
LC_ 123
PK 117,18,117,32
v_add_f32 v14, v14, v15
s_nop 1
v_add_f32_dpp v14, v14, v14 quad_perm:[1,0,3,2] row_mask:0xf bank_mask:0xf
s_nop 1
v_add_f32_dpp v14, v14, v14 quad_perm:[2,3,0,1] row_mask:0xf bank_mask:0xf
s_nop 1
v_add_f32_dpp v14, v14, v14 row_half_mirror row_mask:0xf bank_mask:0xf
v_cvt_f64_f32 v[14:15], v14
s_mov_b64 exec, s[54:55]
SP
s_mov_b64 exec, -1
s_nop 0
ds_read_b64 v[14:15], v5 offset:2048
RV 40,8,6528
AW 116,52
M 6,1,1
s_addc_u32 s43, s95, 0
RA 228,1,16384
BR 65
PO 119
AW 117,53
WL 7
LS 44,64
WV 9
BW 66
Z 7,12,0
LO 16,16,122
LU 122,16
AW 118,54
AW 119,55
M 7,13,1
RA 232,1,17408
HZ 25,122
Z 8,24,0
RV 44,8,6592
LL 18,100
LL 126,101
M 8,25,1
LA 18
BR 66
PK 122,16,120,33
v_pk_add_f16 v27, v27, v29
Z 9,36,0
WL 6
LC_ 126
PK 120,16,120,32
HX 27
M 9,37,1
WL 6
RA 236,1,18432
v_subrev_u32 v5, s39, v5
LS 48,65
WV 8
BW 67
M 6,2,2
RV 48,8,6656
LO 17,17,123
LU 123,17
M 6,3,3
SU 116
BR 67
HS 25,123
M 7,14,2
s_mul_i32 s53, s50, 25600
LL 16,102
LL 127,103
s_add_u32 s40, %[wlo], s53
s_addc_u32 s41, %[whi], 0
s_mov_b32 m0, s38
M 7,15,3
WL 5
LA 16
PK 123,17,121,33
LS 40,66
WV 8
BW 68
M 8,26,2
LC_ 127
RV 40,8,6720
PK 121,17,121,32
M 8,27,3
WV 7
RA 216,1,19456
BR 68
DW 0
M 9,38,2
WL 6
LO 18,18,126
LU 126,18
M 9,39,3
s_add_u32 s37, s37, 32768
RA 220,1,20480
HS 25,126
LS 44,67
s_cmp_eq_u32 s37, s75
BW 69
M 6,4,4
s_cselect_b32 s37, s74, s37
LL 17,56
LL 104,57
M 6,5,5
LA 17
PK 126,18,124,33
v_add_f32 v14, v14, v15
s_nop 1
v_add_f32_dpp v14, v14, v14 quad_perm:[1,0,3,2] row_mask:0xf bank_mask:0xf
s_nop 1
v_add_f32_dpp v14, v14, v14 quad_perm:[2,3,0,1] row_mask:0xf bank_mask:0xf
s_nop 1
v_add_f32_dpp v14, v14, v14 row_half_mirror row_mask:0xf bank_mask:0xf
v_cvt_f64_f32 v[14:15], v14
s_mov_b64 exec, s[54:55]
SP
s_mov_b64 exec, -1
DW 1024
RV 44,8,6784
M 7,16,4
s_add_u32 s42, s44, s64
LC_ 104
PK 124,18,124,32
s_addc_u32 s43, s45, 0
BR 69
M 7,17,5
WL 6
RA 224,1,21504
PO 126
LS 48,68
WV 9
BW 70
M 8,28,4
LO 16,16,127
LU 127,16
ST 120
M 8,29,5
DW 2048
RV 48,8,6848
HS 25,127
M 9,40,4
WL 8
RA 240,7,0
LL 18,58
LL 105,59
M 9,41,5
s_add_u32 s40, s40, 3072
LA 18
BR 70
PK 127,16,125,33
M 6,6,0
s_addc_u32 s41, s41, 0
LC_ 105
PK 125,16,125,32
s_add_u32 m0, s38, 3072
M 6,7,1
WL 7
RA 228,1,22528
PO 127
LS 40,69
WV 10
BW 71
)"
R"(M 7,18,0
RA 244,7,256
LO 104,17,104
LU 17,104
M 7,19,1
RA 232,1,23552
BR 71
HZ 26,17
M 8,30,0
WL 6
DW 0
LL 16,60
LL 108,61
M 8,31,1
s_add_u32 s42, s44, s65
LA 16
HX 25
LS 44,70
s_addc_u32 s43, s45, 0
RA 248,7,512
M 9,42,0
WL 2
LC_ 108
W1 25,1024
W1 27,3072
M 9,43,1
s_lshl_b32 s53, s52, 8
RA 236,1,24576
v_add_u32 v4, s39, v4
LS 48,71
s_add_u32 s48, %[alo], s53
RA 252,7,768
M 6,8,2
LO 105,18,105
LU 18,105
s_addc_u32 s49, %[ahi], 0
s_add_u32 s52, s52, %[d]
s_cmp_eq_u32 s52, %[wrap]
s_cselect_b32 s52, %[rst], s52
DW 1024
v_mul_f32 v36, 0xbb2aaaab, v36
M 6,9,3
ST 124
HS 26,18
v_mul_f32 v37, 0xbb2aaaab, v37
v_mul_f32 v38, 0xbb2aaaab, v38
M 7,20,2
s_mul_i32 s53, s51, 49152
LL 17,62
LL 109,63
s_bitcmp1_b64 s[92:93], s51
s_cselect_b64 s[94:95], s[88:89], s[86:87]
s_add_u32 s94, s94, s53
s_addc_u32 s95, s95, 0
s_add_u32 s51, s51, %[d]
s_cmp_eq_u32 s51, %[wrap]
s_cselect_b32 s51, %[rst], s51
v_mul_f32 v39, 0xbb2aaaab, v39
M 7,21,3
LA 17
DW 2048
LK 40,240,60,0
LK 44,242,60,2
RA 240,7,1024
v_add_u32 v1, s37, v0
LK 48,244,61,0
s_nop 0
CF 40,40,41
LK 248,246,61,2
RA 244,7,1280
CF 42,42,43
M 8,32,2
LC_ 109
RA 216,1,0
M 8,33,3
DQ 3072
v_and_b32 v22, 0xfffffcff, v1
CF 44,44,45
CF 46,46,47
M 9,44,2
WL 6
LO 108,16,108
LU 16,108
AW 120,40
AW 121,42
M 9,45,3
s_add_u32 s50, s50, %[d]
RA 220,1,1024
HS 26,16
AW 122,44
AW 123,46
LK 40,248,62,0
CF 48,48,49
LK 44,250,62,2
s_cmp_eq_u32 s50, %[wrap]
RA 248,7,1536
CF 50,50,51
GZ 60
s_cselect_b32 s50, %[rst], s50
CF 248,248,249
CF 250,250,251
M 6,10,4
LL 18,64
LL 112,65
M 6,11,5
LA 18
RV 252,22,5120
M 7,22,4
WL 9
LC_ 112
v_add_u32 v19, 1, v19
AW 124,48
M 7,23,5
s_add_u32 s38, s38, 32768
RA 224,1,2048
AW 125,50
AW 126,248
AW 127,250
LK 48,252,63,0
CF 40,40,41
LK 248,254,63,2
s_cmp_eq_u32 s38, s77
RA 252,7,1792
CF 42,42,43
GS 61
s_cselect_b32 s38, s76, s38
CF 44,44,45
CF 46,46,47
M 8,34,4
WL 8
LO 109,17,109
LU 17,109
M 8,35,5
FP
HS 26,17
M 9,46,4
WL 4
LL 16,66
LL 113,67
M 9,47,5
s_sub_u32 s39, 0, s39
LA 16
RA 228,1,3072
AW 128,40
AW 129,42
Z 0,0,0
LC_ 113
FR
AW 130,44
AW 131,46
M 0,1,1
LK 40,240,64,0
CF 48,48,49
LK 44,242,64,2
RA 240,7,2048
CF 50,50,51
GS 62
CF 248,248,249
CF 250,250,251
Z 1,12,0
WL 8
LO 112,18,112
LU 18,112
M 1,13,1
RA 232,1,4096
HZ 29,18
AW 132,48
Z 2,24,0
LL 17,68
LL 116,69
AW 133,50
AW 134,248
M 2,25,1
LA 17
AW 135,250
LK 48,244,65,0
CF 40,40,41
LK 248,246,65,2
RA 244,7,2304
CF 42,42,43
GS 63
CF 44,44,45
CF 46,46,47
Z 10,36,0
WL 6
LC_ 116
AW 136,40
M 10,37,1
RA 236,1,6144
AW 137,42
M 0,2,2
WL 7
LO 113,16,113
LU 16,113
M 0,3,3
WL 6
HS 29,16
v_cmp_gt_u32 vcc, v19, v15
AW 138,44
AW 139,46
LK 40,248,66,0
CF 48,48,49
LK 44,250,66,2
RA 248,7,2560
CF 50,50,51
GS 64
CF 248,248,249
CF 250,250,251
M 1,14,2
LL 18,70
LL 117,71
M 1,15,3
LA 18
RA 216,1,7168
M 2,26,2
s_cbranch_vccnz .Lk1s15
.Lk1r15:
LC_ 117
AW 140,48
M 2,27,3
ds_read_b64 v[14:15], v5 offset:0
AW 141,50
AW 142,248
M 10,38,2
WL 5
LO 116,17,116
LU 17,116
AW 143,250
M 10,39,3
RA 220,1,8192
HS 29,17
LK 48,252,67,0
CF 40,40,41
LK 248,254,67,2
RA 252,7,2816
CF 42,42,43
GS 65
CF 44,44,45
CF 46,46,47
M 0,4,4
LL 16,80
LL 106,81
M 0,5,5
LA 16
AW 144,40
AW 145,42
M 1,16,4
LC_ 106
AW 146,44
AW 147,46
M 1,17,5
RA 224,1,9216
LK 40,240,68,0
CF 48,48,49
LK 44,242,68,2
CF 50,50,51
GS 66
CF 248,248,249
CF 250,250,251
M 2,28,4
WL 6
LO 117,18,117
LU 18,117
M 2,29,5
HS 29,18
AW 148,48
M 10,40,4
WL 4
LL 17,82
LL 107,83
AW 149,50
AW 150,248
AW 151,250
M 10,41,5
LA 17
RA 228,1,10240
LK 48,244,69,0
CF 40,40,41
LK 248,246,69,2
CF 42,42,43
v_add_f32 v14, v14, v15
GS 67
CF 44,44,45
CF 46,46,47
M 0,6,0
LC_ 107
v_pk_add_f16 v26, v26, v29
M 0,7,1
v_add_f32_dpp v14, v14, v14 quad_perm:[1,0,3,2] row_mask:0xf bank_mask:0xf
HX 26
AW 152,40
M 1,18,0
WL 4
LO 16,16,106
LU 106,16
AW 153,42
AW 154,44
M 1,19,1
RA 232,1,11264
HZ 27,106
AW 155,46
LK 40,248,70,0
CF 48,48,49
LK 44,250,70,2
CF 50,50,51
v_add_f32_dpp v14, v14, v14 quad_perm:[2,3,0,1] row_mask:0xf bank_mask:0xf
GS 68
CF 248,248,249
CF 250,250,251
M 2,30,0
LL 18,84
LL 110,85
M 2,31,1
LA 18
PK 106,16,104,33
M 10,42,0
WL 3
LC_ 110
PK 104,16,104,32
AW 156,48
AW 157,50
AW 158,248
M 10,43,1
RV 252,22,5392
v_add_f32_dpp v14, v14, v14 row_half_mirror row_mask:0xf bank_mask:0xf
W1 26,2048
AW 159,250
LK 48,252,71,0
CF 40,40,41
LK 248,254,71,2
CF 42,42,43
v_cvt_f64_f32 v[14:15], v14
GS 69
CF 44,44,45
CF 46,46,47
M 0,8,2
WL 5
LO 17,17,107
LU 107,17
M 0,9,3
RA 236,1,12288
HS 27,107
M 1,20,2
LL 16,86
LL 111,87
AW 160,40
AW 161,42
M 1,21,3
s_add_u32 s42, s94, s60
LA 16
RV 36,8,6912
PK 107,17,105,33
AW 162,44
AW 163,46
M 2,32,2
s_addc_u32 s43, s95, 0
LC_ 111
PK 105,17,105,32
M 2,33,3
RA 216,1,13312
GS 70
CF 48,48,49
CF 50,50,51
CF 248,248,249
CF 250,250,251
M 10,44,2
WL 4
LO 18,18,110
LU 110,18
AW 164,48
AW 165,50
AW 166,248
M 10,45,3
RA 220,1,14336
HS 27,110
AW 167,250
s_nop 1
GS 71
s_mov_b64 exec, s[54:55]
SP
s_mov_b64 exec, -1
s_nop 4
v_cndmask_b32 v52, v52, v53, s[78:79]
M 0,10,4
LL 17,88
LL 114,89
v_cndmask_b32 v54, v54, v55, s[78:79]
M 0,11,5
LA 17
ds_read_b64 v[14:15], v5 offset:2048
PK 110,18,108,33
v_cndmask_b32 v52, v52, v54, s[80:81]
M 1,22,4
LC_ 114
PK 108,18,108,32
v_mul_f32 v52, 0x3b2aaaab, v52
M 1,23,5
WL 6
RA 224,1,15360
PO 110
)"
R"(v_med3_f32 v53, v52, v39, v37
M 2,34,4
WL 6
LO 16,16,111
LU 111,16
SU 104
LG 52,53
s_nop 0
C 52,52,52
M 2,35,5
HS 27,111
v_subrev_u32 v5, s39, v5
v_lshlrev_b32 v52, 16, v52
M 10,46,4
WL 4
LL 18,90
LL 115,91
v_add_f32 v52, v36, v52
M 10,47,5
LA 18
PK 111,16,109,33
C 52,52,52
Z 6,0,0
s_add_u32 s42, s94, s61
LC_ 115
PK 109,16,109,32
v_lshlrev_b32 v52, 16, v52
M 6,1,1
s_addc_u32 s43, s95, 0
RA 228,1,16384
PO 111
v_sqrt_f32 v52, v52
s_nop 0
C 52,52,52
Z 7,12,0
WL 4
LO 17,17,114
LU 114,17
v_lshlrev_b32 v52, 16, v52
M 7,13,1
RA 232,1,17408
HZ 29,114
v_rcp_f32 v52, v52
Z 8,24,0
LL 16,92
LL 118,93
v_mul_f32 v52, v38, v52
M 8,25,1
LA 16
PK 114,17,112,33
CF 52,52,52
Z 11,36,0
WL 2
LC_ 118
PK 112,17,112,32
ds_bpermute_b32 v13, v28, v52
M 11,37,1
RA 236,1,18432
v_add_f32 v14, v14, v15
s_nop 1
v_add_f32_dpp v14, v14, v14 quad_perm:[1,0,3,2] row_mask:0xf bank_mask:0xf
s_nop 1
v_add_f32_dpp v14, v14, v14 quad_perm:[2,3,0,1] row_mask:0xf bank_mask:0xf
M 6,2,2
LO 18,18,115
LU 115,18
M 6,3,3
SU 108
HS 29,115
v_add_f32_dpp v14, v14, v14 row_half_mirror row_mask:0xf bank_mask:0xf
M 7,14,2
s_mul_i32 s53, s50, 25600
LL 17,94
LL 119,95
s_add_u32 s40, %[wlo], s53
s_addc_u32 s41, %[whi], 0
s_mov_b32 m0, s38
M 7,15,3
LA 17
PK 115,18,113,33
v_cvt_f64_f32 v[14:15], v14
M 8,26,2
LC_ 119
PK 113,18,113,32
M 8,27,3
RA 216,1,19456
s_mov_b64 exec, s[54:55]
SP
s_mov_b64 exec, -1
DW 0
M 11,38,2
WL 3
LO 16,16,118
LU 118,16
M 11,39,3
s_add_u32 s37, s37, 32768
RA 220,1,20480
HS 29,118
AR 248,120
M 6,4,4
s_cmp_eq_u32 s37, s75
LL 18,56
LL 104,57
s_cselect_b32 s37, s74, s37
AR 249,121
M 6,5,5
LA 18
DW 1024
PK 118,16,116,33
AR 250,122
M 7,16,4
s_add_u32 s42, s94, s62
LC_ 104
PK 116,16,116,32
s_addc_u32 s43, s95, 0
AR 251,123
AR 52,124
M 7,17,5
RA 224,1,21504
PO 118
AR 53,125
M 8,28,4
WL 5
LO 17,17,119
LU 119,17
SU 112
AR 54,126
AR 55,127
M 8,29,5
DW 2048
HS 29,119
XS 248
M 11,40,4
WL 3
LL 16,58
LL 105,59
XS 249
M 11,41,5
s_add_u32 s40, s40, 3072
LA 16
PK 119,17,117,33
XS 250
XS 251
M 6,6,0
s_addc_u32 s41, s41, 0
LC_ 105
PK 117,17,117,32
s_add_u32 m0, s38, 3072
AW 120,248
M 6,7,1
RA 228,1,22528
PO 119
AW 121,249
AW 122,250
M 7,18,0
WL 4
LO 104,18,104
LU 18,104
AW 123,251
M 7,19,1
RA 232,1,23552
HZ 26,18
XS 52
XS 53
M 8,30,0
DW 0
LL 17,60
LL 108,61
XS 54
M 8,31,1
s_add_u32 s42, s94, s63
LA 17
v_pk_add_f16 v27, v27, v29
XS 55
M 11,42,0
WL 2
LC_ 108
HX 27
AW 124,52
AW 125,53
M 11,43,1
s_addc_u32 s43, s95, 0
RA 236,1,24576
W1 27,3072
AW 126,54
M 6,8,2
LO 105,16,105
LU 16,105
s_lshl_b32 s53, s52, 8
s_add_u32 s48, %[alo], s53
s_addc_u32 s49, %[ahi], 0
s_add_u32 s52, s52, %[d]
s_cmp_eq_u32 s52, %[wrap]
s_cselect_b32 s52, %[rst], s52
DW 1024
AW 127,55
v_min_u32 v35, v35, v13
M 6,9,3
SU 116
HS 26,16
AR 248,128
M 7,20,2
s_mul_i32 s53, s51, 49152
LL 18,62
LL 109,63
s_bitcmp1_b64 s[92:93], s51
s_cselect_b64 s[94:95], s[88:89], s[86:87]
s_add_u32 s94, s94, s53
s_addc_u32 s95, s95, 0
s_add_u32 s51, s51, %[d]
s_cmp_eq_u32 s51, %[wrap]
s_cselect_b32 s51, %[rst], s51
AR 249,129
AR 250,130
M 7,21,3
LA 18
DW 2048
AR 251,131
M 8,32,2
LC_ 109
DQ 3072
XS 248
M 8,33,3
s_add_u32 s50, s50, %[d]
v_add_u32 v1, s37, v0
v_add_u32 v4, s39, v4
XS 249
XS 250
M 11,44,2
WL 3
LO 108,17,108
LU 17,108
XS 251
M 11,45,3
s_cmp_eq_u32 s50, %[wrap]
RA 216,1,0
HS 26,17
AW 128,248
AW 129,249
M 6,10,4
s_cselect_b32 s50, %[rst], s50
LL 16,64
LL 112,65
AW 130,250
M 6,11,5
LA 16
v_and_b32 v22, 0xfffffcff, v1
AW 131,251
AR 52,132
M 7,22,4
LC_ 112
RA 220,1,1024
AR 53,133
M 7,23,5
RV 252,22,5120
v_add_u32 v19, 1, v19
AR 54,134
M 8,34,4
WL 5
LO 109,18,109
LU 18,109
AR 55,135
XS 52
M 8,35,5
s_add_u32 s38, s38, 32768
RA 224,1,2048
HS 26,18
XS 53
M 11,46,4
WL 1
LL 17,66
LL 113,67
XS 54
XS 55
M 11,47,5
s_cmp_eq_u32 s38, s77
LA 17
RA 228,1,3072
s_cselect_b32 s38, s76, s38
FP
s_sub_u32 s39, 0, s39
AW 132,52
Z 0,0,0
LC_ 113
AW 133,53
AW 134,54
M 0,1,1
AW 135,55
AR 248,136
Z 1,12,0
LO 112,16,112
LU 16,112
M 1,13,1
RA 232,1,4096
HZ 29,16
AR 249,137
AR 250,138
Z 2,24,0
LL 18,96
LL 116,97
AR 251,139
M 2,25,1
LA 18
XS 248
XS 249
Z 3,36,0
LC_ 116
XS 250
M 3,37,1
XS 251
AW 136,248
Z 4,48,0
WL 3
LO 113,17,113
LU 17,113
AW 137,249
M 4,49,1
RA 236,1,6144
HS 29,17
AW 138,250
Z 5,60,0
LL 16,98
LL 117,99
AW 139,251
AR 52,140
M 5,61,1
LA 16
FR
AR 53,141
M 0,2,2
LC_ 117
AR 54,142
AR 55,143
M 0,3,3
XS 52
XS 53
M 1,14,2
WL 4
LO 116,18,116
LU 18,116
XS 54
M 1,15,3
RA 216,1,7168
HS 29,18
XS 55
M 2,26,2
LL 17,80
LL 106,81
AW 140,52
M 2,27,3
LA 17
AW 141,53
AW 142,54
M 3,38,2
WL 3
LC_ 106
v_cmp_gt_u32 vcc, v19, v15
AW 143,55
M 3,39,3
AR 248,144
AR 249,145
M 4,50,2
WL 3
LO 117,16,117
LU 16,117
AR 250,146
M 4,51,3
RA 220,1,8192
HS 29,16
AR 251,147
M 5,62,2
LL 18,82
LL 107,83
XS 248
XS 249
M 5,63,3
s_cbranch_vccnz .Lk1s16
.Lk1r16:
LA 18
v_pk_add_f16 v26, v26, v29
XS 250
M 0,4,4
LC_ 107
HX 26
XS 251
AW 144,248
M 0,5,5
W1 26,2048
AW 145,249
M 1,16,4
WL 4
LO 17,17,106
LU 106,17
AW 146,250
AW 147,251
M 1,17,5
RA 224,1,9216
HZ 27,106
AR 52,148
M 2,28,4
)"
R"(LL 16,84
LL 110,85
AR 53,149
M 2,29,5
LA 16
PK 106,17,104,33
AR 54,150
AR 55,151
M 3,40,4
LC_ 110
PK 104,17,104,32
XS 52
M 3,41,5
XS 53
XS 54
M 4,52,4
WL 4
LO 18,18,107
LU 107,18
XS 55
M 4,53,5
RA 228,1,10240
HS 27,107
AW 148,52
AW 149,53
M 5,64,4
LL 17,86
LL 111,87
AW 150,54
M 5,65,5
s_add_u32 s42, s94, s60
LA 17
PK 107,18,105,33
AW 151,55
M 0,6,0
s_addc_u32 s43, s95, 0
LC_ 111
PK 105,18,105,32
AR 248,152
AR 249,153
M 0,7,1
SU 104
AR 250,154
M 1,18,0
WL 3
LO 16,16,110
LU 110,16
AR 251,155
XS 248
M 1,19,1
RA 232,1,11264
HS 27,110
XS 249
M 2,30,0
LL 18,88
LL 114,89
XS 250
XS 251
M 2,31,1
LA 18
PK 110,16,108,33
AW 152,248
M 3,42,0
LC_ 114
PK 108,16,108,32
AW 153,249
M 3,43,1
RV 252,22,5392
PO 110
AW 154,250
AW 155,251
M 4,54,0
WL 4
LO 17,17,111
LU 111,17
AR 52,156
M 4,55,1
RA 236,1,12288
HS 27,111
AR 53,157
AR 54,158
M 5,66,0
LL 16,90
LL 115,91
AR 55,159
M 5,67,1
LA 16
PK 111,17,109,33
XS 52
XS 53
M 0,8,2
s_add_u32 s42, s94, s61
LC_ 115
PK 109,17,109,32
XS 54
M 0,9,3
s_addc_u32 s43, s95, 0
PO 111
XS 55
M 1,20,2
WL 4
LO 18,18,114
LU 114,18
AW 156,52
AW 157,53
M 1,21,3
RA 216,1,13312
HZ 29,114
AW 158,54
M 2,32,2
LL 17,100
LL 118,101
AW 159,55
AR 248,160
M 2,33,3
LA 17
PK 114,18,112,33
AR 249,161
M 3,44,2
LC_ 118
PK 112,18,112,32
AR 250,162
AR 251,163
M 3,45,3
SU 108
XS 248
M 4,56,2
WL 3
LO 16,16,115
LU 115,16
XS 249
M 4,57,3
RA 220,1,14336
HS 29,115
XS 250
XS 251
M 5,68,2
LL 18,102
LL 119,103
AW 160,248
M 5,69,3
s_add_u32 s42, s94, s62
LA 18
PK 115,16,113,33
AW 161,249
AW 162,250
M 0,10,4
s_addc_u32 s43, s95, 0
LC_ 119
PK 113,16,113,32
AW 163,251
M 0,11,5
SU 112
AR 52,164
AR 53,165
M 1,22,4
WL 3
LO 17,17,118
LU 118,17
AR 54,166
M 1,23,5
RA 224,1,15360
HS 29,118
AR 55,167
M 2,34,4
LL 16,56
LL 104,57
XS 52
XS 53
M 2,35,5
LA 16
PK 118,17,116,33
XS 54
M 3,46,4
LC_ 104
PK 116,17,116,32
XS 55
AW 164,52
M 3,47,5
ds_read_b64 v[14:15], v5 offset:2048
PO 118
AW 165,53
M 4,58,4
WL 4
LO 18,18,119
LU 119,18
AW 166,54
AW 167,55
M 4,59,5
RA 228,1,16384
HS 29,119
M 5,70,4
LL 17,58
LL 105,59
M 5,71,5
LA 17
PK 119,18,117,33
Z 6,0,0
s_add_u32 s42, s94, s63
LC_ 105
PK 117,18,117,32
M 6,1,1
s_addc_u32 s43, s95, 0
PO 119
v_subrev_u32 v5, s39, v5
Z 7,12,0
WL 4
LO 104,16,104
LU 16,104
M 7,13,1
RA 232,1,17408
HZ 26,16
Z 8,24,0
LL 18,60
LL 108,61
M 8,25,1
WL 4
LA 18
SU 116
Z 9,36,0
s_mul_i32 s53, s50, 25600
LC_ 108
v_add_f32 v14, v14, v15
M 9,37,1
s_add_u32 s40, %[wlo], s53
v_add_f32_dpp v14, v14, v14 quad_perm:[1,0,3,2] row_mask:0xf bank_mask:0xf
v_pk_add_f16 v27, v27, v29
Z 10,48,0
WL 3
LO 105,17,105
LU 17,105
M 10,49,1
s_addc_u32 s41, %[whi], 0
RA 236,1,18432
HS 26,17
Z 11,60,0
LL 16,62
LL 109,63
M 11,61,1
s_mov_b32 m0, s38
LA 16
DW 0
M 6,2,2
LC_ 109
v_add_f32_dpp v14, v14, v14 quad_perm:[2,3,0,1] row_mask:0xf bank_mask:0xf
M 6,3,3
s_nop 0
v_add_f32_dpp v14, v14, v14 row_half_mirror row_mask:0xf bank_mask:0xf
HX 27
M 7,14,2
WL 3
LO 108,18,108
LU 18,108
M 7,15,3
s_add_u32 s37, s37, 32768
RA 216,1,19456
HS 26,18
W1 27,3072
M 8,26,2
s_cmp_eq_u32 s37, s75
LL 17,64
LL 112,65
M 8,27,3
s_cselect_b32 s37, s74, s37
LA 17
DW 1024
M 9,38,2
LC_ 112
v_cvt_f64_f32 v[14:15], v14
M 9,39,3
s_mov_b64 exec, s[54:55]
SP
s_mov_b64 exec, -1
v_add_u32 v4, s39, v4
M 10,50,2
WL 4
LO 109,16,109
LU 16,109
M 10,51,3
RA 220,1,20480
HS 26,16
M 11,62,2
LL 18,66
LL 113,67
M 11,63,3
LA 18
DW 2048
M 6,4,4
LC_ 113
M 6,5,5
M 7,16,4
WL 3
LO 112,17,112
LU 17,112
M 7,17,5
s_mul_i32 s53, s51, 49152
RA 224,1,21504
HZ 29,17
M 8,28,4
s_bitcmp1_b64 s[92:93], s51
LL 16,68
LL 116,69
M 8,29,5
s_cselect_b64 s[94:95], s[88:89], s[86:87]
LA 16
M 9,40,4
s_add_u32 s94, s94, s53
LC_ 116
M 9,41,5
s_addc_u32 s95, s95, 0
M 10,52,4
WL 3
LO 113,18,113
LU 18,113
M 10,53,5
s_add_u32 s51, s51, %[d]
RA 228,1,22528
HS 29,18
M 11,64,4
LL 17,70
LL 117,71
M 11,65,5
s_cmp_eq_u32 s51, %[wrap]
LA 17
M 6,6,0
s_cselect_b32 s51, %[rst], s51
LC_ 117
M 6,7,1
s_mul_i32 s53, %[d], 32768
M 7,18,0
WL 3
LO 116,16,116
LU 16,116
M 7,19,1
s_add_u32 s64, s53, %[sb1]
RA 232,1,23552
HS 29,16
M 8,30,0
s_add_u32 s65, s64, 512
LL 18,72
LL 120,73
M 8,31,1
s_add_u32 s40, s40, 3072
LA 18
M 9,42,0
s_addc_u32 s41, s41, 0
LC_ 120
s_add_u32 m0, s38, 3072
M 9,43,1
DW 0
M 10,54,0
WL 3
LO 117,17,117
LU 17,117
M 10,55,1
RA 236,1,24576
HS 29,17
M 11,66,0
LL 16,74
LL 121,75
M 11,67,1
LA 16
DW 1024
M 6,8,2
LC_ 121
DW 2048
M 6,9,3
s_lshl_b32 s53, s52, 8
v_add_u32 v1, s37, v0
M 7,20,2
WL 3
LO 120,18,120
LU 18,120
M 7,21,3
s_add_u32 s48, %[alo], s53
RA 216,1,0
HZ 30,18
M 8,32,2
s_addc_u32 s49, %[ahi], 0
LL 17,76
LL 124,77
M 8,33,3
s_add_u32 s52, s52, %[d]
LA 17
v_and_b32 v22, 0xfffffcff, v1
M 9,44,2
s_cmp_eq_u32 s52, %[wrap]
LC_ 124
M 9,45,3
s_cselect_b32 s52, %[rst], s52
DQ 3072
M 10,56,2
WL 3
LO 121,16,121
LU 16,121
M 10,57,3
s_add_u32 s50, s50, %[d]
RA 220,1,1024
HS 30,16
M 11,68,2
)"
R"(LL 18,78
LL 125,79
M 11,69,3
s_cmp_eq_u32 s50, %[wrap]
LA 18
RV 252,22,5120
M 6,10,4
s_cselect_b32 s50, %[rst], s50
LC_ 125
M 6,11,5
WV 25
v_add_u32 v19, 1, v19
M 7,22,4
WL 4
LO 124,17,124
LU 17,124
M 7,23,5
s_add_u32 s38, s38, 32768
RA 224,1,2048
HS 30,17
M 8,34,4
s_cmp_eq_u32 s38, s77
LL 16,80
LL 106,81
M 8,35,5
s_cselect_b32 s38, s76, s38
LA 16
FP
M 9,46,4
s_sub_u32 s39, 0, s39
LC_ 106
M 9,47,5
M 10,58,4
WL 4
LO 125,18,125
LU 18,125
M 10,59,5
RA 228,1,3072
HS 30,18
M 11,70,4
LL 17,82
LL 107,83
M 11,71,5
LA 17
v_pk_add_f16 v26, v26, v30
Z 0,0,0
LC_ 107
v_pk_add_f16 v26, v26, v29
M 0,1,1
HX 26
Z 1,12,0
WL 3
LO 16,16,106
LU 106,16
M 1,13,1
RA 232,1,4096
HZ 27,106
Z 2,24,0
LL 18,84
LL 110,85
M 2,25,1
LA 18
PK 106,16,104,33
Z 3,36,0
LC_ 110
PK 104,16,104,32
M 3,37,1
Z 4,48,0
WL 3
LO 17,17,107
LU 107,17
M 4,49,1
RA 236,1,6144
HS 27,107
Z 5,60,0
LL 16,86
LL 111,87
M 5,61,1
s_add_u32 s42, s94, s60
LA 16
PK 107,17,105,33
M 0,2,2
s_addc_u32 s43, s95, 0
LC_ 111
PK 105,17,105,32
M 0,3,3
FR
M 1,14,2
WL 4
LO 18,18,110
LU 110,18
M 1,15,3
RA 216,1,7168
HS 27,110
M 2,26,2
LL 17,88
LL 114,89
M 2,27,3
LA 17
PK 110,18,108,33
M 3,38,2
LC_ 114
PK 108,18,108,32
M 3,39,3
SU 104
PO 110
M 4,50,2
WL 4
LO 16,16,111
LU 111,16
M 4,51,3
RA 220,1,8192
HS 27,111
M 5,62,2
LL 18,90
LL 115,91
M 5,63,3
PFN
LA 18
PK 111,16,109,33
M 0,4,4
s_mov_b32 m0, %[pm]
LC_ 115
PK 109,16,109,32
M 0,5,5
DF
PO 111
M 1,16,4
WL 3
LO 17,17,114
LU 114,17
M 1,17,5
s_add_u32 s42, s94, s61
RA 224,1,9216
HZ 29,114
M 2,28,4
s_addc_u32 s43, s95, 0
LL 16,92
LL 118,93
M 2,29,5
LA 16
PK 114,17,112,33
M 3,40,4
LC_ 118
PK 112,17,112,32
M 3,41,5
SU 108
v_cmp_gt_u32 vcc, v19, v15
M 4,52,4
WL 3
LO 18,18,115
LU 115,18
M 4,53,5
RA 228,1,10240
HS 29,115
M 5,64,4
LL 17,94
LL 119,95
M 5,65,5
s_cbranch_vccnz .Lk1s17
.Lk1r17:
LA 17
PK 115,18,113,33
M 0,6,0
s_add_u32 s42, s94, s62
LC_ 119
PK 113,18,113,32
M 0,7,1
s_addc_u32 s43, s95, 0
W1 26,2048
M 1,18,0
WL 4
LO 16,16,118
LU 118,16
M 1,19,1
RA 232,1,11264
HS 29,118
M 2,30,0
LL 18,96
LL 122,97
M 2,31,1
LA 18
PK 118,16,116,33
M 3,42,0
LC_ 122
PK 116,16,116,32
M 3,43,1
RV 252,22,5392
PO 118
M 4,54,0
WL 5
LO 17,17,119
LU 119,17
M 4,55,1
RA 236,1,12288
HS 29,119
M 5,66,0
LL 16,98
LL 123,99
M 5,67,1
LA 16
PK 119,17,117,33
M 0,8,2
LC_ 123
PK 117,17,117,32
M 0,9,3
SU 112
PO 119
M 1,20,2
WL 4
LO 18,18,122
LU 122,18
M 1,21,3
s_add_u32 s42, s94, s63
RA 216,1,13312
HZ 30,122
M 2,32,2
s_addc_u32 s43, s95, 0
LL 17,100
LL 126,101
M 2,33,3
LA 17
PK 122,18,120,33
M 3,44,2
LC_ 126
PK 120,18,120,32
M 3,45,3
SU 116
M 4,56,2
WL 3
LO 16,16,123
LU 123,16
M 4,57,3
RA 220,1,14336
HS 30,123
M 5,68,2
LL 18,102
LL 127,103
M 5,69,3
s_add_u32 s42, s94, s64
LA 18
PK 123,16,121,33
M 0,10,4
s_addc_u32 s43, s95, 0
LC_ 127
PK 121,16,121,32
M 0,11,5
SU 120
M 1,22,4
WL 3
LO 17,17,126
LU 126,17
M 1,23,5
RA 224,1,15360
HS 30,126
M 2,34,4
LL 16,56
LL 104,57
M 2,35,5
LA 16
PK 126,17,124,33
M 3,46,4
LC_ 104
PK 124,17,124,32
M 3,47,5
ds_read_b64 v[14:15], v5 offset:2048
PO 126
M 4,58,4
WL 4
LO 18,18,127
LU 127,18
M 4,59,5
RA 228,1,16384
HS 30,127
M 5,70,4
LL 17,58
LL 105,59
M 5,71,5
LA 17
PK 127,18,125,33
Z 6,0,0
s_add_u32 s42, s94, s65
LC_ 105
PK 125,18,125,32
M 6,1,1
s_addc_u32 s43, s95, 0
PO 127
v_subrev_u32 v5, s39, v5
Z 7,12,0
WL 4
LO 104,16,104
LU 16,104
M 7,13,1
RA 232,1,17408
HZ 26,16
Z 8,24,0
LL 18,60
LL 108,61
M 8,25,1
WL 4
LA 18
SU 124
Z 9,36,0
s_mul_i32 s53, s50, 25600
LC_ 108
v_add_f32 v14, v14, v15
M 9,37,1
s_add_u32 s40, %[wlo], s53
v_add_f32_dpp v14, v14, v14 quad_perm:[1,0,3,2] row_mask:0xf bank_mask:0xf
v_pk_add_f16 v27, v27, v30
Z 10,48,0
WL 3
LO 105,17,105
LU 17,105
M 10,49,1
s_addc_u32 s41, %[whi], 0
RA 236,1,18432
HS 26,17
Z 11,60,0
LL 16,62
LL 109,63
M 11,61,1
s_mov_b32 m0, s38
LA 16
DW 0
M 6,2,2
LC_ 109
v_add_f32_dpp v14, v14, v14 quad_perm:[2,3,0,1] row_mask:0xf bank_mask:0xf
M 6,3,3
s_nop 0
v_add_f32_dpp v14, v14, v14 row_half_mirror row_mask:0xf bank_mask:0xf
v_pk_add_f16 v27, v27, v29
M 7,14,2
WL 3
LO 108,18,108
LU 18,108
M 7,15,3
s_add_u32 s37, s37, 32768
RA 216,1,19456
HS 26,18
HX 27
M 8,26,2
s_cmp_eq_u32 s37, s75
LL 17,64
LL 112,65
W1 27,3072
M 8,27,3
s_cselect_b32 s37, s74, s37
LA 17
DW 1024
M 9,38,2
LC_ 112
v_cvt_f64_f32 v[14:15], v14
M 9,39,3
s_mov_b64 exec, s[54:55]
SP
s_mov_b64 exec, -1
v_add_u32 v4, s39, v4
s_mov_b64 exec, 1
AT
s_mov_b64 exec, -1
M 10,50,2
WL 4
LO 109,16,109
LU 16,109
M 10,51,3
RA 220,1,20480
HS 26,16
M 11,62,2
LL 18,66
LL 113,67
M 11,63,3
LA 18
DW 2048
M 6,4,4
LC_ 113
M 6,5,5
M 7,16,4
WL 3
LO 112,17,112
LU 17,112
M 7,17,5
s_mul_i32 s53, s51, 49152
RA 224,1,21504
HZ 29,17
M 8,28,4
s_bitcmp1_b64 s[92:93], s51
LL 16,68
LL 116,69
M 8,29,5
s_cselect_b64 s[94:95], s[88:89], s[86:87]
LA 16
M 9,40,4
s_add_u32 s94, s94, s53
LC_ 116
M 9,41,5
s_addc_u32 s95, s95, 0
M 10,52,4
WL 3
LO 113,18,113
)"
R"(LU 18,113
M 10,53,5
s_add_u32 s51, s51, %[d]
RA 228,1,22528
HS 29,18
M 11,64,4
LL 17,70
LL 117,71
M 11,65,5
s_cmp_eq_u32 s51, %[wrap]
LA 17
M 6,6,0
s_cselect_b32 s51, %[rst], s51
LC_ 117
M 6,7,1
s_add_u32 s40, s40, 3072
M 7,18,0
WL 3
LO 116,16,116
LU 16,116
M 7,19,1
s_addc_u32 s41, s41, 0
RA 232,1,23552
HS 29,16
M 8,30,0
s_add_u32 m0, s38, 3072
LL 18,72
LL 120,73
M 8,31,1
LA 18
DW 0
M 9,42,0
LC_ 120
DW 1024
M 9,43,1
M 10,54,0
WL 3
LO 117,17,117
LU 17,117
M 10,55,1
RA 236,1,24576
HS 29,17
M 11,66,0
LL 16,74
LL 121,75
M 11,67,1
LA 16
DW 2048
M 6,8,2
LC_ 121
M 6,9,3
s_lshl_b32 s53, s52, 8
v_add_u32 v1, s37, v0
M 7,20,2
WL 3
LO 120,18,120
LU 18,120
M 7,21,3
s_add_u32 s48, %[alo], s53
RA 216,1,0
HZ 30,18
M 8,32,2
s_addc_u32 s49, %[ahi], 0
LL 17,76
LL 124,77
M 8,33,3
s_add_u32 s52, s52, %[d]
LA 17
v_and_b32 v22, 0xfffffcff, v1
M 9,44,2
s_cmp_eq_u32 s52, %[wrap]
LC_ 124
M 9,45,3
s_cselect_b32 s52, %[rst], s52
DQ 3072
M 10,56,2
WL 3
LO 121,16,121
LU 16,121
M 10,57,3
s_add_u32 s50, s50, %[d]
RA 220,1,1024
HS 30,16
M 11,68,2
LL 18,78
LL 125,79
M 11,69,3
s_cmp_eq_u32 s50, %[wrap]
LA 18
RV 252,22,5120
M 6,10,4
s_cselect_b32 s50, %[rst], s50
LC_ 125
M 6,11,5
WV 28
M 7,22,4
WL 4
LO 124,17,124
LU 17,124
M 7,23,5
s_add_u32 s38, s38, 32768
RA 224,1,2048
HS 30,17
M 8,34,4
s_cmp_eq_u32 s38, s77
LL 16,80
LL 106,81
M 8,35,5
s_cselect_b32 s38, s76, s38
LA 16
M 9,46,4
s_barrier
LC_ 106
M 9,47,5
s_sub_u32 s39, 0, s39
M 10,58,4
WL 3
LO 125,18,125
LU 18,125
M 10,59,5
RA 228,1,3072
HS 30,18
M 11,70,4
LL 17,82
LL 107,83
M 11,71,5
LA 17
v_pk_add_f16 v26, v26, v30
Z 0,0,0
LC_ 107
v_pk_add_f16 v26, v26, v29
M 0,1,1
HX 26
Z 1,12,0
WL 3
LO 16,16,106
LU 106,16
M 1,13,1
RA 232,1,4096
HZ 27,106
Z 2,24,0
LL 18,84
LL 110,85
M 2,25,1
LA 18
PK 106,16,104,33
Z 3,36,0
LC_ 110
PK 104,16,104,32
M 3,37,1
W1 26,2048
Z 4,48,0
WL 4
LO 17,17,107
LU 107,17
M 4,49,1
RA 236,1,6144
HS 27,107
Z 5,60,0
LL 16,86
LL 111,87
M 5,61,1
s_add_u32 s42, s94, s60
LA 16
PK 107,17,105,33
M 0,2,2
s_addc_u32 s43, s95, 0
LC_ 111
PK 105,17,105,32
M 0,3,3
SU 104
M 1,14,2
WL 4
LO 18,18,110
LU 110,18
M 1,15,3
RA 216,1,7168
HS 27,110
M 2,26,2
LL 17,88
LL 114,89
M 2,27,3
LA 17
PK 110,18,108,33
M 3,38,2
LC_ 114
PK 108,18,108,32
M 3,39,3
PO 110
M 4,50,2
WL 3
LO 16,16,111
LU 111,16
M 4,51,3
RA 220,1,8192
HS 27,111
M 5,62,2
LL 18,90
LL 115,91
M 5,63,3
PFW
LA 18
PK 111,16,109,33
M 0,4,4
s_mov_b32 m0, %[pm]
LC_ 115
PK 109,16,109,32
M 0,5,5
DF
PO 111
M 1,16,4
WL 3
LO 17,17,114
LU 114,17
M 1,17,5
s_add_u32 s42, s94, s61
RA 224,1,9216
HZ 29,114
M 2,28,4
s_addc_u32 s43, s95, 0
LL 16,92
LL 118,93
M 2,29,5
LA 16
PK 114,17,112,33
M 3,40,4
LC_ 118
PK 112,17,112,32
M 3,41,5
SU 108
M 4,52,4
WL 3
LO 18,18,115
LU 115,18
M 4,53,5
RA 228,1,10240
HS 29,115
M 5,64,4
LL 17,94
LL 119,95
M 5,65,5
s_add_u32 s42, s94, s62
LA 17
PK 115,18,113,33
M 0,6,0
s_addc_u32 s43, s95, 0
LC_ 119
PK 113,18,113,32
M 0,7,1
SU 112
M 1,18,0
WL 3
LO 16,16,118
LU 118,16
M 1,19,1
RA 232,1,11264
HS 29,118
M 2,30,0
LL 18,96
LL 122,97
M 2,31,1
LA 18
PK 118,16,116,33
M 3,42,0
LC_ 122
PK 116,16,116,32
M 3,43,1
RV 252,22,5392
PO 118
M 4,54,0
WL 4
LO 17,17,119
LU 119,17
M 4,55,1
RA 236,1,12288
HS 29,119
M 5,66,0
LL 16,98
LL 123,99
M 5,67,1
LA 16
PK 119,17,117,33
M 0,8,2
s_add_u32 s42, s94, s63
LC_ 123
PK 117,17,117,32
M 0,9,3
s_addc_u32 s43, s95, 0
PO 119
M 1,20,2
WL 4
LO 18,18,122
LU 122,18
M 1,21,3
RA 216,1,13312
HZ 30,122
M 2,32,2
LL 17,100
LL 126,101
M 2,33,3
LA 17
PK 122,18,120,33
M 3,44,2
LC_ 126
PK 120,18,120,32
M 3,45,3
SU 116
M 4,56,2
WL 3
LO 16,16,123
LU 123,16
M 4,57,3
RA 220,1,14336
HS 30,123
M 5,68,2
LL 18,102
LL 127,103
M 5,69,3
s_add_u32 s42, s94, s64
LA 18
PK 123,16,121,33
M 0,10,4
s_addc_u32 s43, s95, 0
LC_ 127
PK 121,16,121,32
M 0,11,5
SU 120
M 1,22,4
WL 3
LO 17,17,126
LU 126,17
M 1,23,5
RA 224,1,15360
HS 30,126
M 2,34,4
LL 16,56
LL 104,57
M 2,35,5
LA 16
PK 126,17,124,33
M 3,46,4
LC_ 104
PK 124,17,124,32
M 3,47,5
ds_read_b64 v[14:15], v5 offset:2048
PO 126
M 4,58,4
WL 4
LO 18,18,127
LU 127,18
M 4,59,5
RA 228,1,16384
HS 30,127
M 5,70,4
LL 17,58
LL 105,59
M 5,71,5
LA 17
PK 127,18,125,33
Z 6,0,0
s_add_u32 s42, s94, s65
LC_ 105
PK 125,18,125,32
M 6,1,1
s_addc_u32 s43, s95, 0
PO 127
v_subrev_u32 v5, s39, v5
Z 7,12,0
WL 4
LO 104,16,104
LU 16,104
M 7,13,1
RA 232,1,17408
HZ 26,16
Z 8,24,0
LL 18,60
LL 108,61
M 8,25,1
WL 4
LA 18
SU 124
Z 9,36,0
s_mul_i32 s53, s50, 25600
LC_ 108
v_add_f32 v14, v14, v15
M 9,37,1
s_add_u32 s40, %[wlo], s53
v_add_f32_dpp v14, v14, v14 quad_perm:[1,0,3,2] row_mask:0xf bank_mask:0xf
v_pk_add_f16 v27, v27, v30
Z 10,48,0
WL 3
LO 105,17,105
LU 17,105
M 10,49,1
s_addc_u32 s41, %[whi], 0
RA 236,1,18432
HS 26,17
Z 11,60,0
LL 16,62
LL 109,63
)"
R"(M 11,61,1
s_mov_b32 m0, s38
LA 16
DW 0
M 6,2,2
LC_ 109
v_add_f32_dpp v14, v14, v14 quad_perm:[2,3,0,1] row_mask:0xf bank_mask:0xf
M 6,3,3
WV 13
v_add_f32_dpp v14, v14, v14 row_half_mirror row_mask:0xf bank_mask:0xf
v_pk_add_f16 v27, v27, v29
M 7,14,2
WL 3
LO 108,18,108
LU 18,108
M 7,15,3
s_add_u32 s37, s37, 32768
RA 216,1,19456
HS 26,18
HX 27
M 8,26,2
s_cmp_eq_u32 s37, s75
LL 17,64
LL 112,65
W1 27,3072
M 8,27,3
s_cselect_b32 s37, s74, s37
LA 17
DW 1024
v_readfirstlane_b32 s72, v11
M 9,38,2
LC_ 112
v_cvt_f64_f32 v[14:15], v14
v_mov_b32 v12, s72
M 9,39,3
W2 12,12,16
M 10,50,2
WL 5
LO 109,16,109
LU 16,109
M 10,51,3
RA 220,1,20480
HS 26,16
M 11,62,2
LL 18,66
LL 113,67
M 11,63,3
LA 18
s_mov_b64 exec, s[54:55]
SP
s_mov_b64 exec, -1
M 6,4,4
LC_ 113
DW 2048
M 6,5,5
v_add_u32 v4, s39, v4
s_mov_b64 exec, 1
AT
s_mov_b64 exec, -1
M 7,16,4
WL 4
LO 112,17,112
LU 17,112
M 7,17,5
s_mul_i32 s53, s51, 49152
RA 224,1,21504
HZ 29,17
M 8,28,4
s_bitcmp1_b64 s[92:93], s51
LL 16,68
LL 116,69
M 8,29,5
s_cselect_b64 s[94:95], s[88:89], s[86:87]
LA 16
M 9,40,4
s_add_u32 s94, s94, s53
LC_ 116
M 9,41,5
s_addc_u32 s95, s95, 0
M 10,52,4
WL 3
LO 113,18,113
LU 18,113
M 10,53,5
s_add_u32 s51, s51, %[d]
RA 228,1,22528
HS 29,18
M 11,64,4
LL 17,70
LL 117,71
M 11,65,5
s_cmp_eq_u32 s51, %[wrap]
LA 17
M 6,6,0
s_cselect_b32 s51, %[rst], s51
LC_ 117
M 6,7,1
s_add_u32 s40, s40, 3072
M 7,18,0
WL 3
LO 116,16,116
LU 16,116
M 7,19,1
s_addc_u32 s41, s41, 0
RA 232,1,23552
HS 29,16
M 8,30,0
s_add_u32 m0, s38, 3072
LL 18,72
LL 120,73
M 8,31,1
LA 18
DW 0
M 9,42,0
LC_ 120
DW 1024
M 9,43,1
M 10,54,0
WL 3
LO 117,17,117
LU 17,117
M 10,55,1
RA 236,1,24576
HS 29,17
M 11,66,0
LL 16,74
LL 121,75
M 11,67,1
LA 16
DW 2048
M 6,8,2
LC_ 121
M 6,9,3
s_lshl_b32 s53, s52, 8
v_add_u32 v1, s37, v0
M 7,20,2
WL 3
LO 120,18,120
LU 18,120
M 7,21,3
s_add_u32 s48, %[alo], s53
RA 216,1,0
HZ 30,18
M 8,32,2
s_addc_u32 s49, %[ahi], 0
LL 17,76
LL 124,77
M 8,33,3
s_add_u32 s52, s52, %[d]
LA 17
v_and_b32 v22, 0xfffffcff, v1
M 9,44,2
s_cmp_eq_u32 s52, %[wrap]
LC_ 124
M 9,45,3
s_cselect_b32 s52, %[rst], s52
DQ 3072
M 10,56,2
WL 3
LO 121,16,121
LU 16,121
M 10,57,3
s_add_u32 s50, s50, %[d]
RA 220,1,1024
HS 30,16
M 11,68,2
LL 18,78
LL 125,79
M 11,69,3
s_cmp_eq_u32 s50, %[wrap]
LA 18
RV 252,22,5120
M 6,10,4
s_cselect_b32 s50, %[rst], s50
LC_ 125
M 6,11,5
M 7,22,4
WL 4
LO 124,17,124
LU 17,124
M 7,23,5
s_add_u32 s38, s38, 32768
RA 224,1,2048
HS 30,17
M 8,34,4
s_cmp_eq_u32 s38, s77
LL 16,80
LL 106,81
M 8,35,5
s_cselect_b32 s38, s76, s38
LA 16
M 9,46,4
s_barrier
LC_ 106
M 9,47,5
s_sub_u32 s39, 0, s39
M 10,58,4
WL 3
LO 125,18,125
LU 18,125
M 10,59,5
RA 228,1,3072
HS 30,18
M 11,70,4
LL 17,82
LL 107,83
M 11,71,5
2:
LA 17
v_pk_add_f16 v26, v26, v30
Z 0,0,0
LC_ 107
ds_read_b32 v20, v5 offset:4096
M 0,1,1
v_pk_add_f16 v26, v26, v29
Z 1,12,0
WL 4
LO 16,16,106
LU 106,16
M 1,13,1
RA 232,1,4096
HZ 27,106
Z 2,24,0
LL 18,84
LL 110,85
M 2,25,1
LA 18
PK 106,16,104,33
Z 3,36,0
LC_ 110
PK 104,16,104,32
M 3,37,1
HX 26
Z 4,48,0
WL 4
LO 17,17,107
LU 107,17
M 4,49,1
RA 236,1,6144
HS 27,107
Z 5,60,0
LL 16,86
LL 111,87
M 5,61,1
s_add_u32 s42, s94, s60
LA 16
PK 107,17,105,33
M 0,2,2
s_addc_u32 s43, s95, 0
LC_ 111
PK 105,17,105,32
M 0,3,3
WL 6
W1 26,2048
v_readfirstlane_b32 s59, v20
M 1,14,2
WL 4
LO 18,18,110
LU 110,18
M 1,15,3
RA 216,1,7168
HS 27,110
M 2,26,2
LL 17,88
LL 114,89
M 2,27,3
LA 17
PK 110,18,108,33
M 3,38,2
LC_ 114
PK 108,18,108,32
M 3,39,3
SU 104
PO 110
M 4,50,2
WL 4
LO 16,16,111
LU 111,16
M 4,51,3
RA 220,1,8192
HS 27,111
M 5,62,2
LL 18,90
LL 115,91
M 5,63,3
LA 18
PK 111,16,109,33
M 0,4,4
s_add_u32 s42, s94, s61
LC_ 115
PK 109,16,109,32
M 0,5,5
s_addc_u32 s43, s95, 0
PO 111
M 1,16,4
WL 3
LO 17,17,114
LU 114,17
M 1,17,5
RA 224,1,9216
HZ 29,114
M 2,28,4
LL 16,92
LL 118,93
M 2,29,5
LA 16
PK 114,17,112,33
M 3,40,4
LC_ 118
PK 112,17,112,32
M 3,41,5
SU 108
M 4,52,4
WL 3
LO 18,18,115
LU 115,18
M 4,53,5
RA 228,1,10240
HS 29,115
M 5,64,4
LL 17,94
LL 119,95
M 5,65,5
s_add_u32 s42, s94, s62
LA 17
PK 115,18,113,33
M 0,6,0
s_addc_u32 s43, s95, 0
LC_ 119
PK 113,18,113,32
M 0,7,1
SU 112
M 1,18,0
WL 3
LO 16,16,118
LU 118,16
M 1,19,1
RA 232,1,11264
HS 29,118
M 2,30,0
LL 18,96
LL 122,97
M 2,31,1
LA 18
PK 118,16,116,33
M 3,42,0
LC_ 122
PK 116,16,116,32
M 3,43,1
RV 252,22,5392
PO 118
M 4,54,0
WL 4
LO 17,17,119
LU 119,17
M 4,55,1
RA 236,1,12288
HS 29,119
M 5,66,0
LL 16,98
LL 123,99
M 5,67,1
LA 16
PK 119,17,117,33
M 0,8,2
s_add_u32 s42, s94, s63
LC_ 123
PK 117,17,117,32
M 0,9,3
s_addc_u32 s43, s95, 0
PO 119
M 1,20,2
WL 4
LO 18,18,122
LU 122,18
M 1,21,3
RA 216,1,13312
HZ 30,122
M 2,32,2
LL 17,100
LL 126,101
M 2,33,3
LA 17
PK 122,18,120,33
M 3,44,2
LC_ 126
PK 120,18,120,32
M 3,45,3
SU 116
M 4,56,2
WL 3
LO 16,16,123
LU 123,16
M 4,57,3
RA 220,1,14336
HS 30,123
M 5,68,2
LL 18,102
LL 127,103
M 5,69,3
)"
R"(s_add_u32 s42, s94, s64
LA 18
PK 123,16,121,33
M 0,10,4
s_addc_u32 s43, s95, 0
LC_ 127
PK 121,16,121,32
M 0,11,5
SU 120
M 1,22,4
WL 3
LO 17,17,126
LU 126,17
M 1,23,5
RA 224,1,15360
HS 30,126
M 2,34,4
LL 16,56
LL 104,57
M 2,35,5
LA 16
PK 126,17,124,33
M 3,46,4
LC_ 104
PK 124,17,124,32
M 3,47,5
ds_read_b64 v[14:15], v5 offset:2048
PO 126
M 4,58,4
WL 4
LO 18,18,127
LU 127,18
M 4,59,5
RA 228,1,16384
HS 30,127
M 5,70,4
LL 17,58
LL 105,59
M 5,71,5
LA 17
PK 127,18,125,33
Z 6,0,0
s_add_u32 s42, s94, s65
LC_ 105
PK 125,18,125,32
M 6,1,1
s_addc_u32 s43, s95, 0
PO 127
v_subrev_u32 v5, s39, v5
Z 7,12,0
WL 4
LO 104,16,104
LU 16,104
M 7,13,1
RA 232,1,17408
HZ 26,16
Z 8,24,0
LL 18,60
LL 108,61
M 8,25,1
WL 4
LA 18
SU 124
Z 9,36,0
s_mul_i32 s53, s50, 25600
LC_ 108
v_add_f32 v14, v14, v15
M 9,37,1
s_add_u32 s40, %[wlo], s53
v_add_f32_dpp v14, v14, v14 quad_perm:[1,0,3,2] row_mask:0xf bank_mask:0xf
v_pk_add_f16 v27, v27, v30
Z 10,48,0
WL 3
LO 105,17,105
LU 17,105
M 10,49,1
s_addc_u32 s41, %[whi], 0
RA 236,1,18432
HS 26,17
Z 11,60,0
LL 16,62
LL 109,63
M 11,61,1
s_mov_b32 m0, s38
LA 16
DW 0
M 6,2,2
LC_ 109
v_add_f32_dpp v14, v14, v14 quad_perm:[2,3,0,1] row_mask:0xf bank_mask:0xf
M 6,3,3
WV 11
v_add_f32_dpp v14, v14, v14 row_half_mirror row_mask:0xf bank_mask:0xf
v_pk_add_f16 v27, v27, v29
M 7,14,2
WL 3
LO 108,18,108
LU 18,108
M 7,15,3
s_add_u32 s37, s37, 32768
RA 216,1,19456
HS 26,18
HX 27
M 8,26,2
s_cmp_eq_u32 s37, s75
LL 17,64
LL 112,65
W1 27,3072
M 8,27,3
s_cselect_b32 s37, s74, s37
LA 17
DW 1024
v_readfirstlane_b32 s72, v11
M 9,38,2
LC_ 112
v_cvt_f64_f32 v[14:15], v14
v_mov_b32 v12, s72
M 9,39,3
s_max_u32 s59, s59, %[nb]
W2 12,12,16
M 10,50,2
WL 5
LO 109,16,109
LU 16,109
M 10,51,3
s_add_u32 %[nb], %[nb], 1
RA 220,1,20480
HS 26,16
M 11,62,2
LL 18,66
LL 113,67
M 11,63,3
LA 18
s_mov_b64 exec, s[54:55]
SP
s_mov_b64 exec, -1
M 6,4,4
LC_ 113
DW 2048
M 6,5,5
v_add_u32 v4, s39, v4
s_mov_b64 exec, 1
AT
s_mov_b64 exec, -1
M 7,16,4
WL 4
LO 112,17,112
LU 17,112
M 7,17,5
s_mul_i32 s53, s51, 49152
RA 224,1,21504
HZ 29,17
M 8,28,4
s_bitcmp1_b64 s[92:93], s51
LL 16,68
LL 116,69
M 8,29,5
s_cselect_b64 s[94:95], s[88:89], s[86:87]
LA 16
M 9,40,4
s_add_u32 s94, s94, s53
LC_ 116
M 9,41,5
s_addc_u32 s95, s95, 0
M 10,52,4
WL 3
LO 113,18,113
LU 18,113
M 10,53,5
s_add_u32 s51, s51, %[d]
RA 228,1,22528
HS 29,18
M 11,64,4
LL 17,70
LL 117,71
M 11,65,5
s_cmp_eq_u32 s51, %[wrap]
LA 17
M 6,6,0
s_cselect_b32 s51, %[rst], s51
LC_ 117
M 6,7,1
s_add_u32 s40, s40, 3072
M 7,18,0
WL 3
LO 116,16,116
LU 16,116
M 7,19,1
s_addc_u32 s41, s41, 0
RA 232,1,23552
HS 29,16
M 8,30,0
s_add_u32 m0, s38, 3072
LL 18,72
LL 120,73
M 8,31,1
LA 18
DW 0
M 9,42,0
LC_ 120
DW 1024
M 9,43,1
M 10,54,0
WL 3
LO 117,17,117
LU 17,117
M 10,55,1
RA 236,1,24576
HS 29,17
M 11,66,0
LL 16,74
LL 121,75
M 11,67,1
LA 16
DW 2048
M 6,8,2
LC_ 121
M 6,9,3
s_lshl_b32 s53, s52, 8
v_add_u32 v1, s37, v0
M 7,20,2
WL 3
LO 120,18,120
LU 18,120
M 7,21,3
s_add_u32 s48, %[alo], s53
RA 216,1,0
HZ 30,18
M 8,32,2
s_addc_u32 s49, %[ahi], 0
LL 17,76
LL 124,77
M 8,33,3
s_add_u32 s52, s52, %[d]
LA 17
v_and_b32 v22, 0xfffffcff, v1
M 9,44,2
s_cmp_eq_u32 s52, %[wrap]
LC_ 124
M 9,45,3
s_cselect_b32 s52, %[rst], s52
DQ 3072
M 10,56,2
WL 3
LO 121,16,121
LU 16,121
M 10,57,3
s_add_u32 s50, s50, %[d]
RA 220,1,1024
HS 30,16
M 11,68,2
LL 18,78
LL 125,79
M 11,69,3
s_cmp_eq_u32 s50, %[wrap]
LA 18
RV 252,22,5120
M 6,10,4
s_cselect_b32 s50, %[rst], s50
LC_ 125
M 6,11,5
M 7,22,4
WL 4
LO 124,17,124
LU 17,124
M 7,23,5
s_add_u32 s38, s38, 32768
RA 224,1,2048
HS 30,17
M 8,34,4
s_cmp_eq_u32 s38, s77
LL 16,80
LL 106,81
M 8,35,5
s_cselect_b32 s38, s76, s38
LA 16
M 9,46,4
s_barrier
LC_ 106
M 9,47,5
s_sub_u32 s39, 0, s39
M 10,58,4
WL 3
LO 125,18,125
LU 18,125
M 10,59,5
RA 228,1,3072
HS 30,18
M 11,70,4
s_cmp_lt_u32 s59, 32
LL 17,82
LL 107,83
M 11,71,5
s_cbranch_scc1 2b
LA 17
LC_ 107
ds_read_b64 v[14:15], v5 offset:2048
v_pk_add_f16 v26, v26, v30
v_pk_add_f16 v26, v26, v29
HX 26
v_subrev_u32 v5, s39, v5
W1 26,2048
WL 0
LO 16,16,106
LU 106,16
HZ 27,106
LL 18,84
LA 18
LL 110,85
PK 106,16,104,33
v_add_f32 v14, v14, v15
s_nop 1
v_add_f32_dpp v14, v14, v14 quad_perm:[1,0,3,2] row_mask:0xf bank_mask:0xf
s_nop 1
v_add_f32_dpp v14, v14, v14 quad_perm:[2,3,0,1] row_mask:0xf bank_mask:0xf
s_nop 1
v_add_f32_dpp v14, v14, v14 row_half_mirror row_mask:0xf bank_mask:0xf
v_cvt_f64_f32 v[14:15], v14
s_mov_b64 exec, s[54:55]
SP
s_mov_b64 exec, -1
LC_ 110
PK 104,16,104,32
LO 17,17,107
LU 107,17
HS 27,107
LL 16,86
LA 16
LL 111,87
PK 107,17,105,33
s_add_u32 s42, s94, s60
s_addc_u32 s43, s95, 0
LC_ 111
PK 105,17,105,32
SU 104
WL 0
LO 18,18,110
LU 110,18
HS 27,110
LL 17,88
LA 17
LL 114,89
LC_ 114
PK 110,18,108,33
PK 108,18,108,32
PO 110
LO 16,16,111
LU 111,16
HS 27,111
LL 18,90
LA 18
LL 115,91
LC_ 115
PK 111,16,109,33
PK 109,16,109,32
s_add_u32 s42, s94, s61
s_addc_u32 s43, s95, 0
PO 111
WL 0
LO 17,17,114
LU 114,17
SU 108
HZ 29,114
LL 16,92
LA 16
LL 118,93
LC_ 118
PK 114,17,112,33
PK 112,17,112,32
LO 18,18,115
LU 115,18
HS 29,115
LL 17,94
LA 17
LL 119,95
PK 115,18,113,33
s_add_u32 s42, s94, s62
s_addc_u32 s43, s95, 0
LC_ 119
PK 113,18,113,32
SU 112
WL 0
LO 16,16,118
LU 118,16
HS 29,118
LL 18,96
LA 18
LL 122,97
LC_ 122
PK 118,16,116,33
PK 116,16,116,32
PO 118
LO 17,17,119
LU 119,17
HS 29,119
LL 16,98
LA 16
LL 123,99
LC_ 123
PK 119,17,117,33
PK 117,17,117,32
)"
R"(s_add_u32 s42, s94, s63
s_addc_u32 s43, s95, 0
PO 119
WL 0
LO 18,18,122
LU 122,18
SU 116
HZ 30,122
LL 17,100
LA 17
LL 126,101
LC_ 126
PK 122,18,120,33
PK 120,18,120,32
LO 16,16,123
LU 123,16
HS 30,123
LL 18,102
LA 18
LL 127,103
PK 123,16,121,33
s_add_u32 s42, s94, s64
s_addc_u32 s43, s95, 0
LC_ 127
PK 121,16,121,32
SU 120
WL 0
LO 17,17,126
LU 126,17
HS 30,126
PK 126,17,124,33
PK 124,17,124,32
PO 126
LO 18,18,127
LU 127,18
HS 30,127
v_pk_add_f16 v27, v27, v30
PK 127,18,125,33
v_pk_add_f16 v27, v27, v29
PK 125,18,125,32
HX 27
s_add_u32 s42, s94, s65
s_addc_u32 s43, s95, 0
PO 127
W1 27,3072
s_nop 1
v_add_u32 v4, s39, v4
SU 124
s_lshl_b32 s53, s52, 8
s_add_u32 s48, %[alo], s53
s_addc_u32 s49, %[ahi], 0
s_add_u32 s52, s52, %[d]
s_cmp_eq_u32 s52, %[wrap]
s_cselect_b32 s52, %[rst], s52
WL 0
WV 0
s_barrier
s_sub_u32 s39, 0, s39
ds_read_b64 v[14:15], v5 offset:2048
s_nop 1
v_subrev_u32 v5, s39, v5
WL 0
v_add_f32 v14, v14, v15
s_nop 1
v_add_f32_dpp v14, v14, v14 quad_perm:[1,0,3,2] row_mask:0xf bank_mask:0xf
s_nop 1
v_add_f32_dpp v14, v14, v14 quad_perm:[2,3,0,1] row_mask:0xf bank_mask:0xf
s_nop 1
v_add_f32_dpp v14, v14, v14 row_half_mirror row_mask:0xf bank_mask:0xf
v_cvt_f64_f32 v[14:15], v14
s_mov_b64 exec, s[54:55]
SP
s_mov_b64 exec, -1
GE
s_waitcnt vmcnt(0) lgkmcnt(0)
8:
s_branch 6f
.Lk1s0:
FR
s_waitcnt lgkmcnt(0)
v_cmp_gt_u32 vcc, v19, v15
s_nop 4
s_cbranch_vccnz .Lk1s0
s_branch .Lk1r0
.Lk1s1:
FR
s_waitcnt lgkmcnt(0)
v_cmp_gt_u32 vcc, v19, v15
s_nop 4
s_cbranch_vccnz .Lk1s1
s_branch .Lk1r1
.Lk1s2:
FR
s_waitcnt lgkmcnt(0)
v_cmp_gt_u32 vcc, v19, v15
s_nop 4
s_cbranch_vccnz .Lk1s2
s_branch .Lk1r2
.Lk1s3:
FR
s_waitcnt lgkmcnt(0)
v_cmp_gt_u32 vcc, v19, v15
s_nop 4
s_cbranch_vccnz .Lk1s3
s_branch .Lk1r3
.Lk1s4:
FR
s_waitcnt lgkmcnt(0)
v_cmp_gt_u32 vcc, v19, v15
s_nop 4
s_cbranch_vccnz .Lk1s4
s_branch .Lk1r4
.Lk1s5:
FR
s_waitcnt lgkmcnt(0)
v_cmp_gt_u32 vcc, v19, v15
s_nop 4
s_cbranch_vccnz .Lk1s5
s_branch .Lk1r5
.Lk1s6:
FR
s_waitcnt lgkmcnt(0)
v_cmp_gt_u32 vcc, v19, v15
s_nop 4
s_cbranch_vccnz .Lk1s6
s_branch .Lk1r6
.Lk1s7:
FR
s_waitcnt lgkmcnt(0)
v_cmp_gt_u32 vcc, v19, v15
s_nop 4
s_cbranch_vccnz .Lk1s7
s_branch .Lk1r7
.Lk1s8:
FR
s_waitcnt lgkmcnt(0)
v_cmp_gt_u32 vcc, v19, v15
s_nop 4
s_cbranch_vccnz .Lk1s8
s_branch .Lk1r8
.Lk1s9:
FR
s_waitcnt lgkmcnt(0)
v_cmp_gt_u32 vcc, v19, v15
s_nop 4
s_cbranch_vccnz .Lk1s9
s_branch .Lk1r9
.Lk1s10:
FR
s_waitcnt lgkmcnt(0)
v_cmp_gt_u32 vcc, v19, v15
s_nop 4
s_cbranch_vccnz .Lk1s10
s_branch .Lk1r10
.Lk1s11:
FR
s_waitcnt lgkmcnt(0)
v_cmp_gt_u32 vcc, v19, v15
s_nop 4
s_cbranch_vccnz .Lk1s11
s_branch .Lk1r11
.Lk1s12:
FR
s_waitcnt lgkmcnt(0)
v_cmp_gt_u32 vcc, v19, v15
s_nop 4
s_cbranch_vccnz .Lk1s12
s_branch .Lk1r12
.Lk1s13:
FR
s_waitcnt lgkmcnt(0)
v_cmp_gt_u32 vcc, v19, v15
s_nop 4
s_cbranch_vccnz .Lk1s13
s_branch .Lk1r13
.Lk1s14:
FR
s_waitcnt lgkmcnt(0)
v_cmp_gt_u32 vcc, v19, v15
s_nop 4
s_cbranch_vccnz .Lk1s14
s_branch .Lk1r14
.Lk1s15:
FR
s_waitcnt lgkmcnt(0)
v_cmp_gt_u32 vcc, v19, v15
s_nop 4
s_cbranch_vccnz .Lk1s15
s_branch .Lk1r15
.Lk1s16:
FR
s_waitcnt lgkmcnt(0)
v_cmp_gt_u32 vcc, v19, v15
s_nop 4
s_cbranch_vccnz .Lk1s16
s_branch .Lk1r16
.Lk1s17:
FR
s_waitcnt lgkmcnt(0)
v_cmp_gt_u32 vcc, v19, v15
s_nop 4
s_cbranch_vccnz .Lk1s17
s_branch .Lk1r17
6:
.purgem M
.purgem Z
.purgem LZ
.purgem LS
.purgem LD
.purgem GZ
.purgem GS
.purgem CL
.purgem CH
.purgem XL
.purgem XN
.purgem F
.purgem U
.purgem E
.purgem A
.purgem R
.purgem C
.purgem S0
.purgem S
.purgem HC
.purgem HF
.purgem HM
.purgem HE
.purgem HG
.purgem HA
.purgem HR
.purgem HQ
.purgem HP
.purgem HY
.purgem HK
.purgem LQ
.purgem LL
.purgem LH
.purgem LA
.purgem LC_
.purgem LO
.purgem HZ
.purgem HS
.purgem HT
.purgem HX
.purgem UX
.purgem EX
.purgem AX
.purgem RX
.purgem TX
.purgem SX
.purgem AW
.purgem RA
.purgem RV
.purgem W2
.purgem W1
.purgem DW
.purgem DF
.purgem DX
.purgem ST
.purgem SU
.purgem SP
.purgem AT
.purgem PFA
.purgem PFN
.purgem PFW
.purgem PF0
.purgem WL
.purgem WV
.purgem LU
.purgem CF
.purgem XS
.purgem AR
.purgem DQ
.purgem GE
.purgem BW
.purgem BR
.purgem LK
.purgem PK
.purgem PO
.purgem LG
.purgem FP
.purgem FR
)"
      : [nb] "=&s"(nb)
      : [xlo] "s"((unsigned)xp), [xhi] "s"((unsigned)(xp >> 32)), [hlo] "s"((unsigned)hp), [hhi] "s"((unsigned)(hp >> 32)),
        [glo] "s"((unsigned)gp), [ghi] "s"((unsigned)(gp >> 32)), [wlo] "s"((unsigned)wp), [whi] "s"((unsigned)(wp >> 32)),
        [w] "s"(w), [lds] "s"(lds0), [d] "s"(d), [wrap] "s"(wrap), [rst] "s"(rst), [pos0] "s"(pos0), [xo0] "s"(xo0), [xs1] "s"(xs1), [sb1] "s"(sb1), [r0o] "s"(r0o), [tsel] "s"(tsel), [s2] "s"(s2),
        [alo] "s"((unsigned)ap), [ahi] "s"((unsigned)(ap >> 32)), [ca0] "s"(ca0), [pm] "s"(pm), [pf] "s"(pf)
      : K1_CLOB);
  TS(6);
  MK_END(1);
}

typedef __bf16 bf16x8_t __attribute__((ext_vector_type(8)));
DI f32x4 mfma16(u32x4 a, u32x4 b, f32x4 c) {
  return __builtin_amdgcn_mfma_f32_16x16x32_bf16(__builtin_bit_cast(bf16x8_t, a), __builtin_bit_cast(bf16x8_t, b), c, 0, 0, 0);
}
DI float grn_gx(double s, float scale) { return round_bf16(__builtin_amdgcn_sqrtf(round_bf16(round_bf16((float)s * scale) + 1e-6f))); }
DI void slow_fc1(const u16* __restrict__ x, const u16* __restrict__ dw, char* __restrict__ hws, float* sacc, int w) {
  const int lane = __builtin_amdgcn_mbcnt_hi(~0u, __builtin_amdgcn_mbcnt_lo(~0u, 0u)), m = lane & 15, g = lane >> 4;
  for (int i = lane; i < HID; i += 64) sacc[i] = 0.f;
#pragma unroll 1
  for (int it = 0; it < 9; ++it) {
    const int sub = blockIdx.x * 36 + it * 4 + w;
    const unsigned px = sub_pixel(x, sub, m);
    u32x4 A[12];
    float sum = 0.f;
    const u16* dq = dw;
    asm volatile("" : "+s"(dq));
#pragma unroll
    for (int ks = 0; ks < 12; ++ks) {
      const u32x4 v = *(const u32x4*)(x + (size_t)px * CH + 32 * ks + 8 * g);
      const u32x4 d = *(const u32x4*)(dq + 32 * ks + 8 * g);
#pragma unroll
      for (int j = 0; j < 4; ++j) {
        A[ks][j] = pack_bf16(bf_lo(v[j]) * bf_lo(d[j]), bf_hi(v[j]) * bf_hi(d[j]));
        sum += bf_lo(A[ks][j]) + bf_hi(A[ks][j]);
      }
      asm volatile("" : "+v"(A[ks]));
    }
    sum += __shfl_xor(sum, 16); sum += __shfl_xor(sum, 32);
    const float mean = sum * (1.0f / CH);
    float var = 0.f;
#pragma unroll
    for (int ks = 0; ks < 12; ++ks) {
#pragma unroll
      for (int j = 0; j < 4; ++j) {
        const float a = bf_lo(A[ks][j]) - mean, b = bf_hi(A[ks][j]) - mean;
        var += a * a + b * b;
      }
      asm volatile("" : "+v"(A[ks]));
    }
    var += __shfl_xor(var, 16); var += __shfl_xor(var, 32);
    const float mb = round_bf16(mean);
    const float rd = 1.0f / round_bf16(sqrtf(round_bf16(round_bf16(var * (1.0f / CH)) + 1e-5f)));
#pragma unroll
    for (int ks = 0; ks < 12; ++ks)
#pragma unroll
      for (int j = 0; j < 4; ++j) A[ks][j] = pack_bf16((bf_lo(A[ks][j]) - mb) * rd, (bf_hi(A[ks][j]) - mb) * rd);
#pragma unroll 1
    for (int tc = 0; tc < 96; ++tc) {
      f32x4 acc = {0.f, 0.f, 0.f, 0.f};
#pragma unroll
      for (int ks = 0; ks < 12; ++ks) acc = mfma16(A[ks], *(const u32x4*)(g_buf + (size_t)(tc * 12 + ks) * 1024 + lane * 16), acc);
      float q = 0.f;
#pragma unroll
      for (int r = 0; r < 4; ++r) { acc[r] = gelu_tanh(acc[r]); q = __builtin_fmaf(acc[r], acc[r], q); }
      typedef unsigned u32x2_ __attribute__((ext_vector_type(2)));
      *(u32x2_*)(hws + h_piece(hws, sub, tc >> 1) + g * 256 + ((m + 4 * g) & 15) * 16 + (tc & 1) * 8) = (u32x2_){pack_bf16(acc[0], acc[1]), pack_bf16(acc[2], acc[3])};
      q += __shfl_xor(q, 16); q += __shfl_xor(q, 32);
      if (g == 0) sacc[tc * 16 + m] += q;
    }
  }
  __builtin_amdgcn_wave_barrier();
  __builtin_amdgcn_fence(__ATOMIC_ACQ_REL, "workgroup");
  for (int i = lane; i < HID; i += 64) atomicAdd((double*)(g_buf + G_ACC2) + i, (double)sacc[i]);
}
DI void slow_bar(unsigned* c, int w) {
  const int lane = __builtin_amdgcn_mbcnt_hi(~0u, __builtin_amdgcn_mbcnt_lo(~0u, 0u));
  asm volatile("s_waitcnt vmcnt(0)" ::: "memory");
  __syncthreads();
  if (w == 0 && lane == 0) {
    __threadfence();
    asm volatile("s_waitcnt vmcnt(0)" ::: "memory");
    __hip_atomic_fetch_add(c, 1u, __ATOMIC_RELAXED, __HIP_MEMORY_SCOPE_AGENT);
    for (int i = 0; i < (1 << 22) && __hip_atomic_load(c, __ATOMIC_RELAXED, __HIP_MEMORY_SCOPE_AGENT) < (unsigned)NWG; ++i) __builtin_amdgcn_s_sleep(32);
    __threadfence();
    asm volatile("s_waitcnt vmcnt(0)" ::: "memory");
  }
  __syncthreads();
}

#define K2_CLOB "memory", "vcc", "scc", "s48", "s49", "s50", "s51", "s52", "s53", "s54", "s55", "s56", "s57", "s58", "s59", "s60", "s61", "s62", "s63", "s64", "s65", "s66", "s67", \
  "s68", "s69", "s70", "s71", "s72", "s73", "s74", "s75", "s76", "s77", "s78", "s79", "s80", "s81", "s84", "s85", "s86", "s87", "s88", "s89", "s90", "s91", "s92", "s93", "s94", "s95", "s96", "s97", "s98", "s99", "s38", "s39", "s40", "s41", "s42", "s43", "s44", "s45", "s46", "s47", VA_CLOB
DI void slow_fc2(const u16* __restrict__ x, const char* __restrict__ hws, u16* __restrict__ out, const u16* __restrict__ grn_g, const u16* __restrict__ w2,
                 char* smem, int w) {
  float* sc = (float*)smem;
  float* red = sc + HID;
  u16* wf = (u16*)(smem + 8192);
  const int lane = __builtin_amdgcn_mbcnt_hi(~0u, __builtin_amdgcn_mbcnt_lo(~0u, 0u)), tid = w * 64 + lane, m = lane & 15, g = lane >> 4;
  __syncthreads();
  float part = 0.f;
  for (int c = tid; c < HID; c += 256) {
    const float gx = grn_gx(((const double*)(g_buf + G_ACC2))[c], 1.0f);
    sc[c] = gx; part += gx;
  }
#pragma unroll
  for (int o = 32; o > 0; o >>= 1) part += __shfl_xor(part, o);
  if (lane == 0) red[tid >> 6] = part;
  __syncthreads();
  const float gmean = round_bf16(round_bf16(((red[0] + red[1]) + (red[2] + red[3])) * (1.0f / HID)) + 1e-6f);
  for (int c = tid; c < HID; c += 256) sc[c] = (1.0f + ld_bf16(grn_g + c) * round_bf16(sc[c] / gmean)) ;
#pragma unroll 1
  for (int it = 0; it < 9; ++it) {
    const int sub = blockIdx.x * 36 + it * 4 + (tid >> 6);
    f32x4 acc[24];
#pragma unroll
    for (int i = 0; i < 24; ++i) acc[i] = (f32x4){0.f, 0.f, 0.f, 0.f};
#pragma unroll 1
    for (int t = 0; t < 48; ++t) {
      __syncthreads();
      for (int e = tid; e < 32 * CH; e += 256) {
        const int k = e / CH, n = e % CH;
        wf[((n >> 4) * 64 + (k >> 3) * 16 + (n & 15)) * 8 + (k & 7)] = (u16)pack_bf16(sc[32 * t + k] * ld_bf16(w2 + (size_t)(32 * t + k) * CH + n), 0.f);
      }
      __syncthreads();
      const char* hp = hws + h_piece(hws, sub, t) + (m >> 2) * 256 + (g >> 1) * 8 + 2 * (m & 3);
      u32x4 A;
#pragma unroll
      for (int j = 0; j < 4; ++j) {
        unsigned v[2];
#pragma unroll
        for (int k = 0; k < 2; ++k) {
          v[k] = *(const u16*)(hp + ((8 * (g & 1) + 2 * j + k + (m & 12)) & 15) * 16);
        }
        A[j] = v[0] | (v[1] << 16);
      }
#pragma unroll
      for (int i = 0; i < 24; ++i) acc[i] = mfma16(A, *(const u32x4*)(wf + (i * 64 + lane) * 8), acc[i]);
    }
#pragma unroll
    for (int i = 0; i < 24; ++i) {
      const int n = 16 * i + m;
      const float* bp = g_cs + 4 * 384 + cs_index(n);
      const float bias = (bp[0] + bp[384]) + (bp[768] + bp[1152]);
#pragma unroll
      for (int r = 0; r < 4; ++r) {
        const size_t o = (size_t)(sub_pixel(x, sub, 4 * g) + r) * CH + n;
        out[o] = (u16)pack_bf16(acc[i][r] + bias + ld_bf16(x + o), 0.f);
      }
    }
  }
}
__global__ __launch_bounds__(256, 1) void k2_fc2(const u16* __restrict__ x, char* __restrict__ hws, u16* __restrict__ out,
                                                 const u16* __restrict__ grn_g, const u16* __restrict__ w2, const u16* __restrict__ dw,
                                                 const u16* __restrict__ w1) {
  extern __shared__ __attribute__((aligned(16))) char smem[];
  const int w = __builtin_amdgcn_readfirstlane(threadIdx.x >> 6);
  const unsigned pg = w & 1, nh = w >> 1;
  const unsigned lds0 = lds_addr(smem);
  MK_START(4); TS(9);
  const uint64_t pp = uni64((uint64_t)g_prm + (blockIdx.x * 4 + w) * 256), ap = (uint64_t)g_buf + K1_G_ACC + w * 1024;
  const unsigned wm = lds0 + w * 6144, ab = lds0 + nh * 12288;
  const unsigned hb = lds0 + K2_HB + pg * 2 * K2_RSZ, hm = hb + nh * K2_RSZ;
  const unsigned bm = w * 384, bb = lds0 + K2_E_OFF + nh * 768;
  const unsigned pm = lds0 + K2_PF_OFF, sb = lds0 + K2_S16_OFF, sw = sb + w * 256;
  uint64_t flg;
#define K2_OPS [pp] "s"(pp), [alo] "s"((unsigned)ap), [ahi] "s"((unsigned)(ap >> 32)), [wm] "s"(wm), [hm] "s"(hm), [bm] "s"(bm), [ab] "s"(ab), [hb] "s"(hb), [bb] "s"(bb), \
         [sb] "s"(sb), [sw] "s"(sw), [pm] "s"(pm)
  asm volatile(
R"(.macro MA t,a,b
v_mfma_f32_16x16x32_f16 a[\t:\t+3], v[\b:\b+3], a[\a:\a+3], a[\t:\t+3]
.endm
.macro MV t,a,b
v_mfma_f32_16x16x32_f16 v[\t:\t+3], v[\b:\b+3], a[\a:\a+3], v[\t:\t+3]
.endm
.macro NA t,a,b
v_mfma_f32_16x16x32_f16 a[\t:\t+3], a[\b:\b+3], a[\a:\a+3], a[\t:\t+3]
.endm
.macro NV t,a,b
v_mfma_f32_16x16x32_f16 v[\t:\t+3], a[\b:\b+3], a[\a:\a+3], v[\t:\t+3]
.endm
.macro AN d,s
v_and_b32 v[\d], s93, v[\s]
.endm
.macro SH d
v_lshrrev_b32 v[\d], 4, v[\d]
.endm
.macro PA_ d,h,r
v_perm_b32 v[\d], v[\h], v[\r], s54
.endm
.macro PB_ d,h,r
v_perm_b32 v[\d], v[\h], v[\r], s55
.endm
.macro AW a,s
v_accvgpr_write_b32 a[\a], v[\s]
.endm
.macro AR d,a
v_accvgpr_read_b32 v[\d], a[\a]
.endm
.macro UL d,x
v_lshlrev_b32 v[\d], 16, v[\x]
.endm
.macro UH d,x
v_and_b32 v[\d], s38, v[\x]
.endm
.macro AD d,b
v_fmac_f32 v[\d], s53, v[\b]
.endm
.macro EF d,e
v_fma_f32 v[\d], v[\d], s39, v[\e]
.endm
.macro TY d,o
ds_read_b64_tr_b16 v[\d:\d+1], v2 offset:\o
.endm
.macro TX d,o
ds_read_b64_tr_b16 v[\d:\d+1], v51 offset:\o
.endm
.macro RA d,o
ds_read_b128 a[\d:\d+3], v1 offset:\o
.endm
.macro RC d,o
ds_read_b128 a[\d:\d+3], v5 offset:\o
.endm
.macro BV d,o
ds_read_b128 v[\d:\d+3], v4 offset:\o
.endm
.macro BA d,o
ds_read_b128 a[\d:\d+3], v4 offset:\o
.endm
.macro PA a
s_mov_b32 m0, %[pm]
s_getpc_b64 s[46:47]
s_andn2_b32 s46, s46, 0x7f
s_add_u32 s46, s46, s81
s_addc_u32 s47, s47, 0
s_add_u32 s46, s46, \a
s_addc_u32 s47, s47, 0
.endm
.macro DF
global_load_lds_dwordx4 v0, s[46:47]
.endm
.macro DW o
global_load_lds_dwordx4 v0, s[40:41] offset:\o sc1
.endm
.macro DR o
global_load_lds_dwordx4 v0, s[40:41] offset:\o
.endm
.macro DH o
global_load_lds_dwordx4 v0, s[40:41] offset:\o nt
.endm
.macro DG o
s_mov_b32 exec_hi, 0
global_load_lds_dwordx4 v0, s[40:41] offset:\o nt
s_mov_b32 exec_hi, -1
.endm
.macro XL d,o
global_load_dwordx2 v[\d:\d+1], v47, s[44:45] offset:\o
.endm
.macro ST d,o
global_store_dwordx2 v50, v[\d:\d+1], s[42:43] offset:\o nt sc1
.endm
.macro SA d,o
global_store_dwordx2 v50, a[\d:\d+1], s[42:43] offset:\o nt sc1
.endm
.macro CV d,a,b
v_cvt_pk_bf16_f32 v[\d], v[\a], v[\b]
.endm
.macro RS d,o
ds_read_b128 v[\d:\d+3], v46 offset:\o
.endm
.macro PM d,s
v_pk_mul_f16 v[\d], v[\d], v[\s]
.endm
.macro PF d,n
v_pk_fma_f16 v[\d], v[\d], v[14+\n], v[22+\n]
.endm
.macro FS
global_store_dword v0, v54, s[96:97] sc1
.endm
.macro FL
global_load_dword v54, v55, s[98:99] sc1
.endm
.macro FB
s_cbranch_vccnz 21b
22:
.endm
.macro FC
s_cbranch_vccnz 26b
27:
.endm
.macro FG
s_or_b64 s[54:55], s[44:45], vcc
s_cbranch_scc1 23b
24:
.endm
.macro FX
s_branch 25f
21:
s_sleep 4
global_load_dword v54, v55, s[98:99] sc1
s_waitcnt vmcnt(0)
v_cmp_eq_u32 vcc, 0, v54
s_sub_u32 s80, s80, 1
s_cbranch_scc1 22f
s_nop 2
s_cbranch_vccnz 21b
s_branch 22f
26:
s_sleep 4
global_load_dword v54, v55, s[98:99] sc1
s_waitcnt vmcnt(0)
v_cmp_ne_u32 vcc, 2, v54
s_sub_u32 s80, s80, 1
s_cbranch_scc1 27f
s_nop 2
s_cbranch_vccnz 26b
s_branch 27f
23:
v_mov_b32 v10, 1
s_mov_b64 exec, 1
global_store_dword v0, v10, s[78:79] offset:384 sc1
s_mov_b64 exec, -1
s_nop 4
s_branch 24f
25:
.endm
.macro WL n
s_waitcnt lgkmcnt(\n)
.endm
.macro WV n
s_waitcnt vmcnt(\n)
.endm
.macro M n
.if \n < 82
v_mfma_f32_16x16x32_f16 a[36+4*(\n/2):36+4*(\n/2)+3], v[56+8*((\n/2)%%6)+4*(\n&1):56+8*((\n/2)%%6)+4*(\n&1)+3], a[8*((\n/12)%%4)+4*(\n&1):8*((\n/12)%%4)+4*(\n&1)+3], a[36+4*(\n/2):36+4*(\n/2)+3]
.else
v_mfma_f32_16x16x32_f16 v[-32+4*(\n/2):-32+4*(\n/2)+3], v[56+8*((\n/2)%%6)+4*(\n&1):56+8*((\n/2)%%6)+4*(\n&1)+3], a[8*((\n/12)%%4)+4*(\n&1):8*((\n/12)%%4)+4*(\n&1)+3], v[-32+4*(\n/2):-32+4*(\n/2)+3]
.endif
.endm
.macro R1 n
.if \n < 64
RA 8*(((\n%%24)/2+2*((\n/24)&1))%%4)+4*(\n&1),1024*\n
.else
RC 8*(((\n%%24)/2+2*((\n/24)&1))%%4)+4*(\n&1),1024*\n-49152
.endif
.endm
.macro R n
R1 \n
R1 (\n+1)
.endm
.macro P n
.if \n & 2
PB_ 56+\n,104+8*(\n/16)+(\n&4)+2*((\n/8)&1)+(\n&1),56+(\n|2)
.else
PA_ 56+\n,104+8*(\n/16)+(\n&4)+2*((\n/8)&1)+(\n&1),56+(\n|2)
.endif
.endm
.macro Q n
P \n
P (\n+1)
.endm
.macro mw k
s_add_u32 m0, %[wm], 1024*\k
.endm
.macro mh k
s_add_u32 m0, %[hm], 512*\k
.endm
.macro ad r,k
s_add_u32 s40, s[\r], 512*\k
s_addc_u32 s41, s[\r+1], 0
.endm
.macro ae d,r,k
s_add_u32 s[\d], s[\r], 512*\k
s_addc_u32 s[\d+1], s[\r+1], 0
.endm
.macro bc m,b,d,r
s_bitcmp1_b32 s[\m], \b
s_cselect_b64 s[\d:\d+1], s[\r+2:\r+3], s[\r:\r+1]
.endm
.macro W k
DW 1024*\k
.endm
.macro H k
DH 1024*\k
.endm
k2_code:
s_load_dwordx16 s[48:63], %[pp], 0x0
s_load_dwordx16 s[64:79], %[pp], 0x40
s_load_dwordx16 s[84:99], %[pp], 0x80
v_mbcnt_lo_u32_b32 v7, -1, 0
v_mbcnt_hi_u32_b32 v7, -1, v7
v_lshlrev_b32 v0, 4, v7
v_add_u32 v88, 0x1000, v0
v_add_u32 v89, 0x2000, v0
s_mov_b32 s44, %[alo]
s_mov_b32 s45, %[ahi]
global_load_dwordx4 v[132:135], v0, s[44:45]
global_load_dwordx4 v[144:147], v88, s[44:45]
global_load_dwordx4 v[148:151], v89, s[44:45]
ae 44,44,24
global_load_dwordx4 v[168:171], v0, s[44:45]
global_load_dwordx4 v[172:175], v88, s[44:45]
global_load_dwordx4 v[192:195], v89, s[44:45]
s_waitcnt lgkmcnt(0)
bc 49,0,66,60
mh 0
ad 66,0
H 0
DG 1024
ae 44,44,24
global_load_dwordx4 v[196:199], v0, s[44:45]
global_load_dwordx4 v[216:219], v88, s[44:45]
global_load_dwordx4 v[220:223], v89, s[44:45]
ae 44,44,24
global_load_dwordx4 v[224:227], v0, s[44:45]
global_load_dwordx4 v[228:231], v88, s[44:45]
global_load_dwordx4 v[240:243], v89, s[44:45]
v_and_b32 v8, 15, v7
v_lshrrev_b32 v9, 4, v7
v_lshlrev_b32 v10, 2, v7
v_add_u32 v202, s65, v0
global_load_dword v244, v10, s[92:93] offset:0
global_load_dword v245, v10, s[92:93] offset:1024
global_load_dword v246, v10, s[92:93] offset:2048
global_load_dwordx4 v[152:155], v0, s[88:89]
s_mov_b64 exec, 0xff
global_load_dwordx4 v[156:159], v202, s[88:89]
s_mov_b64 exec, -1
FX
s_mov_b32 s92, s64
s_mov_b32 s93, 0x0f0f0f0f
v_add_u32 v1, %[ab], v0
v_add_u32 v5, 0xc000, v1
v_and_b32 v10, 3, v7
v_lshrrev_b32 v11, 2, v7
v_lshl_add_u32 v11, v10, 2, v11
v_and_b32 v11, 15, v11
mw 0
ad 86,0
DR 0
v_lshl_add_u32 v11, v10, 4, v11
v_lshl_add_u32 v2, v11, 4, %[hb]
v_lshl_add_u32 v51, v11, 3, %[hb]
v_lshl_add_u32 v4, v8, 4, %[bb]
s_mov_b32 s38, 0xffff0000
s_mov_b32 s80, 0x40000
s_mov_b32 s81, s52
s_mov_b32 s39, s54
v_lshl_add_u32 v46, v9, 4, %[sb]
v_add_u32 v46, 8192, v46
v_bfe_u32 v10, v7, 1, 2
DR 1024
v_bfe_u32 v11, v7, 3, 1
v_and_b32 v12, 1, v7
v_lshl_add_u32 v11, v11, 1, v12
v_lshl_add_u32 v10, v10, 2, v11
v_lshl_add_u32 v10, v9, 4, v10
v_lshl_add_u32 v90, v10, 2, %[sw]
DR 2048
v_lshl_add_u32 v91, v7, 2, %[sw]
s_and_b32 s42, s51, 0xffff
s_lshr_b32 s43, s51, 16
s_add_u32 s42, s42, %[sb]
s_add_u32 s43, s43, %[sb]
v_lshl_add_u32 v200, v9, 4, s42
v_mov_b32 v201, s43
v_mov_b32 v128, 0x35400000
v_mov_b32 v129, 0x35400000
v_mov_b32 v130, 0x35400000
v_mov_b32 v131, 0x35400000
mw 3
ad 86,6
DR 0
AW 32,128
AW 33,129
AW 34,130
AW 35,131
v_mov_b32 v95, 0x7f800000
s_mov_b32 s44, 0x44100000
WV 19
v_add_f64 v[132:133], v[132:133], v[168:169]
WV 18
v_add_f64 v[144:145], v[144:145], v[172:173]
DR 1024
WV 18
v_add_f64 v[148:149], v[148:149], v[192:193]
v_add_f64 v[134:135], v[134:135], v[170:171]
v_add_f64 v[146:147], v[146:147], v[174:175]
v_add_f64 v[150:151], v[150:151], v[194:195]
WV 15
v_add_f64 v[132:133], v[132:133], v[196:197]
WV 14
v_add_f64 v[144:145], v[144:145], v[216:217]
DR 2048
WV 14
v_add_f64 v[148:149], v[148:149], v[220:221]
v_add_f64 v[134:135], v[134:135], v[198:199]
v_add_f64 v[146:147], v[146:147], v[218:219]
v_add_f64 v[150:151], v[150:151], v[222:223]
WV 13
v_add_f64 v[132:133], v[132:133], v[224:225]
WV 12
v_add_f64 v[144:145], v[144:145], v[228:229]
bc 49,1,66,60
mh 9
ad 66,192
H 0
WV 12
v_add_f64 v[148:149], v[148:149], v[240:241]
v_add_f64 v[134:135], v[134:135], v[226:227]
v_add_f64 v[146:147], v[146:147], v[230:231]
v_add_f64 v[150:151], v[150:151], v[242:243]
v_cvt_f32_f64 v248, v[132:133]
v_cvt_f32_f64 v252, v[144:145]
DG 1024
v_cvt_f32_f64 v72, v[148:149]
v_cvt_f32_f64 v249, v[134:135]
v_cvt_f32_f64 v253, v[146:147]
v_cvt_f32_f64 v73, v[150:151]
v_mul_f32 v250, s55, v248
v_mul_f32 v254, s55, v252
bc 48,1,64,56
mh 12
ad 64,192
H 0
v_mul_f32 v74, s55, v72
v_mul_f32 v251, s55, v249
v_mul_f32 v255, s55, v253
v_mul_f32 v75, s55, v73
CV 251,250,251
CV 255,254,255
DG 1024
CV 75,74,75
UL 250,251
UL 254,255
UL 74,75
UH 251,251
UH 255,255
mw 24
ad 86,48
DR 0
UH 75,75
v_add_f32 v250, 0x358637bd, v250
v_add_f32 v254, 0x358637bd, v254
v_add_f32 v74, 0x358637bd, v74
v_add_f32 v251, 0x358637bd, v251
v_add_f32 v255, 0x358637bd, v255
DR 1024
v_add_f32 v75, 0x358637bd, v75
CV 251,250,251
CV 255,254,255
CV 75,74,75
UL 250,251
UL 254,255
DR 2048
UL 74,75
UH 251,251
UH 255,255
UH 75,75
v_sqrt_f32 v250, v250
v_sqrt_f32 v254, v254
mw 27
ad 86,54
DR 0
v_sqrt_f32 v74, v74
v_sqrt_f32 v251, v251
v_sqrt_f32 v255, v255
v_sqrt_f32 v75, v75
CV 251,250,251
)"
R"(CV 255,254,255
DR 1024
CV 75,74,75
UL 250,251
UL 254,255
UL 74,75
UH 251,251
UH 255,255
DR 2048
UH 75,75
v_cmp_le_f32 vcc, s44, v248
v_cmp_le_f32 s[54:55], s44, v249
s_nop 0
v_cndmask_b32 v248, v95, v250, vcc
v_cndmask_b32 v249, v95, v251, s[54:55]
v_add_f32 v248, v248, v249
v_cmp_le_f32 vcc, s44, v252
v_cmp_le_f32 s[54:55], s44, v253
s_nop 0
v_cndmask_b32 v252, v95, v254, vcc
v_cndmask_b32 v253, v95, v255, s[54:55]
v_add_f32 v252, v252, v253
v_cmp_le_f32 vcc, s44, v72
v_cmp_le_f32 s[54:55], s44, v73
s_nop 0
v_cndmask_b32 v72, v95, v74, vcc
v_cndmask_b32 v73, v95, v75, s[54:55]
bc 49,2,66,60
mh 18
ad 66,384
H 0
v_add_f32 v72, v72, v73
v_add_f32 v92, v248, v252
v_add_f32 v92, v92, v72
s_nop 1
v_add_f32_dpp v92, v92, v92 row_shr:1 row_mask:0xf bank_mask:0xf
s_nop 1
v_add_f32_dpp v92, v92, v92 row_shr:2 row_mask:0xf bank_mask:0xf
s_nop 1
v_add_f32_dpp v92, v92, v92 row_shr:4 row_mask:0xf bank_mask:0xf
s_nop 1
v_add_f32_dpp v92, v92, v92 row_shr:8 row_mask:0xf bank_mask:0xf
s_nop 1
v_add_f32_dpp v92, v92, v92 row_bcast:15 row_mask:0xa bank_mask:0xf
s_nop 1
v_add_f32_dpp v92, v92, v92 row_bcast:31 row_mask:0xc bank_mask:0xf
ds_write_b32 v91, v92 offset:7168
v_mov_b32 v10, %[sb]
WL 0
s_barrier
ds_read_b32 v96, v10 offset:7420
ds_read_b32 v97, v10 offset:7676
ds_read_b32 v98, v10 offset:7932
ds_read_b32 v99, v10 offset:8188
WV 21
UL 76,244
WV 20
UL 80,245
WV 19
UL 84,246
UH 77,244
DG 1024
UH 81,245
UH 85,246
v_mov_b32 v93, 0
WL 0
v_add_f32 v96, v96, v97
v_add_f32 v98, v98, v99
v_add_f32 v96, v96, v98
bc 48,2,64,56
mh 21
ad 64,384
H 0
v_mul_f32 v94, 0x3a2aaaab, v96
v_cmp_nlt_f32 s[44:45], v96, v95
CV 94,94,94
UL 94,94
v_add_f32 v94, 0x358637bd, v94
CV 94,94,94
UL 94,94
H 1
v_rcp_f32 v94, v94
s_nop 0
v_mul_f32 v250, v94, v250
v_mul_f32 v254, v94, v254
v_mul_f32 v74, v94, v74
v_mul_f32 v251, v94, v251
v_mul_f32 v255, v94, v255
v_mul_f32 v75, v94, v75
H 2
CV 251,250,251
CV 255,254,255
CV 75,74,75
UL 250,251
UL 254,255
UL 74,75
mw 48
ad 86,96
DR 0
UH 251,251
UH 255,255
UH 75,75
v_fma_f32 v250, v76, v250, 1.0
v_fma_f32 v254, v80, v254, 1.0
v_fma_f32 v74, v84, v74, 1.0
DR 1024
v_fma_f32 v251, v77, v251, 1.0
v_fma_f32 v255, v81, v255, 1.0
v_fma_f32 v75, v85, v75, 1.0
v_cvt_pk_f16_f32 v76, v250, v251
v_cvt_pk_f16_f32 v80, v254, v255
v_cvt_pk_f16_f32 v84, v74, v75
ds_write_b32 v90, v76 offset:8192
ds_write_b32 v90, v80 offset:1024
ds_write_b32 v90, v84 offset:2048
v_max3_f32 v93, |v250|, |v251|, v93
DR 2048
v_max3_f32 v93, |v254|, |v255|, v93
v_max3_f32 v93, |v74|, |v75|, v93
v_cmp_lt_f32 vcc, 0x43000000, v93
WL 0
WV 16
s_barrier
TY 106,520
TY 110,14344
TX 58,0
TX 62,13824
TY 66,512
TY 70,14336
RS 14,0
RS 18,64
ds_read_b128 v[176:179], v200
ds_read_b128 v[180:183], v201
R 0
R 2
FG
s_mov_b32 s54, 0x06020400
s_mov_b32 s55, 0x07030501
WL 13
AN 104,106
AN 105,107
SH 106
SH 107
mw 51
ad 86,102
DR 0
AN 106,106
AN 107,107
WL 12
AN 108,110
AN 109,111
SH 110
SH 111
DR 1024
AN 110,110
AN 111,111
WL 10
Q 0
Q 2
DR 2048
Q 4
Q 6
R 4
R 6
WL 7
v_pk_mul_f16 v22, v14, s92
v_pk_mul_f16 v23, v15, s92
v_pk_mul_f16 v24, v16, s92
v_pk_mul_f16 v25, v17, s92
v_pk_mul_f16 v26, v18, s92
v_pk_mul_f16 v27, v19, s92
v_pk_mul_f16 v28, v20, s92
v_pk_mul_f16 v29, v21, s92
PF 56,0
PF 57,1
PF 58,2
PF 59,3
PF 60,4
PF 61,5
PF 62,6
PF 63,7
Q 8
Q 10
Q 12
Q 14
PF 64,0
PF 65,1
PF 66,2
PF 67,3
PF 68,4
PF 69,5
PF 70,6
PF 71,7
PM 152,176
PM 153,177
PM 154,178
PM 155,179
PM 156,180
PM 157,181
PM 158,182
PM 159,183
s_mov_b32 s42, 0x1c001c00
v_pk_mul_f16 v152, v152, s42
v_pk_mul_f16 v153, v153, s42
v_pk_mul_f16 v154, v154, s42
v_pk_mul_f16 v155, v155, s42
v_pk_mul_f16 v156, v156, s42
v_pk_mul_f16 v157, v157, s42
v_pk_mul_f16 v158, v158, s42
v_pk_mul_f16 v159, v159, s42
global_store_dwordx4 v0, v[152:155], s[90:91] sc1
s_mov_b64 exec, 0xff
global_store_dwordx4 v202, v[156:159], s[90:91] sc1
s_mov_b64 exec, -1
s_mov_b32 s42, 0x3c003c00
v_dot2_f32_f16 v52, v152, s42, 0
v_dot2_f32_f16 v53, v156, s42, 0
s_nop 1
v_dot2_f32_f16 v52, v153, s42, v52
v_dot2_f32_f16 v53, v157, s42, v53
s_nop 1
v_dot2_f32_f16 v52, v154, s42, v52
v_dot2_f32_f16 v53, v158, s42, v53
s_nop 1
v_dot2_f32_f16 v52, v155, s42, v52
v_dot2_f32_f16 v53, v159, s42, v53
s_nop 1
WL 0
v_mfma_f32_16x16x32_f16 a[36:39], v[56:59], a[0:3], a[32:35]
mw 72
RS 14,128
RS 18,192
TY 106,5128
M 1
ad 86,144
DR 0
TY 110,18952
v_mfma_f32_16x16x32_f16 a[60:63], v[56:59], a[8:11], a[32:35]
DR 1024
TX 32,4608
TX 36,18432
M 13
DR 2048
TY 40,5120
TY 44,18944
v_mfma_f32_16x16x32_f16 a[40:43], v[64:67], a[0:3], a[32:35]
mw 75
M 3
R 8
v_mfma_f32_16x16x32_f16 a[64:67], v[64:67], a[8:11], a[32:35]
ad 86,150
DR 0
M 15
R 10
v_mfma_f32_16x16x32_f16 a[84:87], v[56:59], a[16:19], a[32:35]
WL 10
DR 1024
v_pk_mul_f16 v22, v14, s92
M 25
DR 2048
v_pk_mul_f16 v23, v15, s92
v_mfma_f32_16x16x32_f16 a[88:91], v[64:67], a[16:19], a[32:35]
bc 48,3,64,56
mh 3
M 27
ad 64,576
v_mfma_f32_16x16x32_f16 a[108:111], v[56:59], a[24:27], a[32:35]
WV 19
H 0
v_pk_mul_f16 v24, v16, s92
M 37
WL 0
H 1
H 2
v_mfma_f32_16x16x32_f16 a[112:115], v[64:67], a[24:27], a[32:35]
s_barrier
R 24
M 39
R 26
v_mfma_f32_16x16x32_f16 a[132:135], v[56:59], a[0:3], a[32:35]
mw 0
v_pk_mul_f16 v25, v17, s92
v_pk_mul_f16 v26, v18, s92
M 49
ad 86,192
DR 0
v_mfma_f32_16x16x32_f16 a[136:139], v[64:67], a[0:3], a[32:35]
DR 1024
v_pk_mul_f16 v27, v19, s92
M 51
R 28
v_mfma_f32_16x16x32_f16 a[156:159], v[56:59], a[8:11], a[32:35]
DR 2048
v_pk_mul_f16 v28, v20, s92
M 61
mw 3
v_pk_mul_f16 v29, v21, s92
AN 104,106
v_mfma_f32_16x16x32_f16 a[160:163], v[64:67], a[8:11], a[32:35]
ad 86,198
DR 0
M 63
WL 4
R 30
v_mfma_f32_16x16x32_f16 a[180:183], v[56:59], a[16:19], a[32:35]
DR 1024
AN 105,107
M 73
DR 2048
SH 106
v_mfma_f32_16x16x32_f16 a[184:187], v[64:67], a[16:19], a[32:35]
bc 49,3,66,60
SH 107
M 75
WL 4
R 32
v_mfma_f32_16x16x32_f16 v[136:139], v[56:59], a[24:27], v[128:131]
mh 0
AN 106,106
AN 107,107
M 85
ad 66,576
H 0
v_mfma_f32_16x16x32_f16 v[140:143], v[64:67], a[24:27], v[128:131]
DG 1024
M 87
WL 4
R 34
v_mfma_f32_16x16x32_f16 v[160:163], v[56:59], a[0:3], v[128:131]
AN 108,110
AN 109,111
M 97
SH 110
SH 111
v_mfma_f32_16x16x32_f16 v[164:167], v[64:67], a[0:3], v[128:131]
AN 110,110
AN 111,111
PA_ 30,104,32
M 99
WL 4
PA_ 31,105,33
PB_ 32,104,32
PB_ 33,105,33
v_mfma_f32_16x16x32_f16 v[184:187], v[56:59], a[8:11], v[128:131]
WV 19
PA_ 34,108,36
PA_ 35,109,37
PB_ 36,108,36
M 109
WL 0
PB_ 37,109,37
PF 30,0
PF 31,1
v_mfma_f32_16x16x32_f16 v[188:191], v[64:67], a[8:11], v[128:131]
s_barrier
R 48
PF 32,2
M 111
R 50
PF 33,3
v_mfma_f32_16x16x32_f16 v[208:211], v[56:59], a[16:19], v[128:131]
mw 24
PF 34,4
PF 35,5
PF 36,6
M 121
ad 86,240
DR 0
PF 37,7
v_mfma_f32_16x16x32_f16 v[232:235], v[56:59], a[24:27], v[128:131]
DR 1024
AW 200,30
AW 201,31
M 133
DR 2048
AW 202,32
AW 203,33
v_mfma_f32_16x16x32_f16 v[212:215], v[64:67], a[16:19], v[128:131]
mw 27
AW 204,34
AW 205,35
AW 206,36
M 123
R 52
AW 207,37
PA_ 38,106,40
PA_ 39,107,41
PB_ 40,106,40
v_mfma_f32_16x16x32_f16 v[236:239], v[64:67], a[24:27], v[128:131]
ad 86,246
DR 0
PB_ 41,107,41
PA_ 42,110,44
PA_ 43,111,45
PB_ 44,110,44
M 135
WL 2
R 54
PB_ 45,111,45
PF 38,0
)"
R"(PF 39,1
PF 40,2
NA 36,0,200
DR 1024
PF 41,3
PF 42,4
PF 43,5
PF 44,6
PF 45,7
NA 36,4,204
DR 2048
AW 208,38
AW 209,39
AW 210,40
AW 211,41
AW 212,42
NA 60,8,200
AW 213,43
AW 214,44
AW 215,45
NA 60,12,204
TY 114,6664
TY 118,20488
TX 32,6144
WL 2
AN 112,114
AN 113,115
SH 114
SH 115
AN 114,114
AN 115,115
WL 1
AN 116,118
AN 117,119
SH 118
SH 119
AN 118,118
AN 119,119
NA 40,0,208
TX 36,19968
TY 40,6656
TY 44,20480
WL 2
PA_ 30,112,32
PA_ 31,113,33
PB_ 32,112,32
PB_ 33,113,33
PA_ 34,116,36
PA_ 35,117,37
PB_ 36,116,36
NA 40,4,212
PB_ 37,117,37
PF 30,0
PF 31,1
PF 32,2
PF 33,3
PF 34,4
PF 35,5
PF 36,6
PF 37,7
AW 216,30
NA 64,8,208
AW 217,31
AW 218,32
AW 219,33
AW 220,34
AW 221,35
AW 222,36
AW 223,37
NA 64,12,212
WL 0
PA_ 38,114,40
PA_ 39,115,41
PB_ 40,114,40
PB_ 41,115,41
PA_ 42,118,44
PA_ 43,119,45
v_mfma_f32_16x16x32_f16 a[44:47], a[216:219], a[0:3], a[32:35]
PB_ 44,118,44
PB_ 45,119,45
PF 38,0
PF 39,1
PF 40,2
PF 41,3
NA 44,4,220
PF 42,4
PF 43,5
PF 44,6
PF 45,7
AW 224,38
AW 225,39
v_mfma_f32_16x16x32_f16 a[68:71], a[216:219], a[8:11], a[32:35]
AW 226,40
AW 227,41
AW 228,42
AW 229,43
AW 230,44
AW 231,45
NA 68,12,220
RS 14,256
RS 18,320
TY 106,9736
v_mfma_f32_16x16x32_f16 a[48:51], a[224:227], a[0:3], a[32:35]
TY 110,23560
TX 58,9216
TX 62,23040
NA 48,4,228
R 56
TY 66,9728
v_mfma_f32_16x16x32_f16 a[72:75], a[224:227], a[8:11], a[32:35]
TY 70,23552
NA 72,12,228
R 58
NA 84,16,200
NA 84,20,204
WL 10
v_pk_mul_f16 v22, v14, s92
v_pk_mul_f16 v23, v15, s92
NA 88,16,208
v_pk_mul_f16 v24, v16, s92
v_pk_mul_f16 v25, v17, s92
NA 88,20,212
v_pk_mul_f16 v26, v18, s92
v_pk_mul_f16 v27, v19, s92
v_mfma_f32_16x16x32_f16 a[92:95], a[216:219], a[16:19], a[32:35]
v_pk_mul_f16 v28, v20, s92
v_pk_mul_f16 v29, v21, s92
NA 92,20,220
WL 9
AN 104,106
AN 105,107
v_mfma_f32_16x16x32_f16 a[96:99], a[224:227], a[16:19], a[32:35]
SH 106
SH 107
NA 96,20,228
AN 106,106
AN 107,107
NA 108,24,200
WL 8
AN 108,110
AN 109,111
NA 108,28,204
SH 110
SH 111
NA 112,24,208
AN 110,110
AN 111,111
NA 112,28,212
WL 3
Q 0
v_mfma_f32_16x16x32_f16 a[116:119], a[216:219], a[24:27], a[32:35]
WV 17
Q 2
NA 116,28,220
WL 0
Q 4
v_mfma_f32_16x16x32_f16 a[120:123], a[224:227], a[24:27], a[32:35]
s_barrier
R 72
TY 114,11272
NA 120,28,228
R 74
TY 118,25096
NA 132,0,200
mw 48
v_mov_b32 v54, 1
s_mov_b64 exec, 1
FS
s_mov_b64 exec, -1
NA 132,4,204
ad 86,288
DR 0
TX 74,10752
NA 136,0,208
Q 6
TX 78,24576
NA 136,4,212
DR 1024
PF 56,0
TY 82,11264
v_mfma_f32_16x16x32_f16 a[140:143], a[216:219], a[0:3], a[32:35]
PF 57,1
PF 58,2
TY 86,25088
NA 140,4,220
DR 2048
PF 59,3
TY 122,12808
v_mfma_f32_16x16x32_f16 a[144:147], a[224:227], a[0:3], a[32:35]
PF 60,4
PF 61,5
TY 126,26632
NA 144,4,228
WL 6
R 76
TX 90,12288
NA 156,8,200
mw 51
PF 62,6
PF 63,7
TX 94,26112
NA 156,12,204
ad 86,294
DR 0
TY 98,12800
NA 160,8,208
Q 8
TY 102,26624
NA 160,12,212
DR 1024
P 10
v_mfma_f32_16x16x32_f16 a[164:167], a[216:219], a[8:11], a[32:35]
Q 11
NA 164,12,220
DR 2048
P 13
v_mfma_f32_16x16x32_f16 a[168:171], a[224:227], a[8:11], a[32:35]
Q 14
NA 168,12,228
R 78
NA 180,16,200
bc 49,4,66,60
PF 64,0
NA 180,20,204
mh 9
PF 65,1
PF 66,2
NA 184,16,208
ad 66,768
H 0
NA 184,20,212
PF 67,3
PF 68,4
v_mfma_f32_16x16x32_f16 a[188:191], a[216:219], a[16:19], a[32:35]
DG 1024
NA 188,20,220
PF 69,5
PF 70,6
v_mfma_f32_16x16x32_f16 a[192:195], a[224:227], a[16:19], a[32:35]
bc 48,4,64,56
PF 71,7
NA 192,20,228
WL 2
R 80
NV 136,24,200
mh 12
AN 112,114
AN 113,115
NV 136,28,204
ad 64,768
H 0
NV 140,24,208
SH 114
SH 115
NV 140,28,212
H 1
AN 114,114
v_mfma_f32_16x16x32_f16 v[144:147], a[216:219], a[24:27], v[128:131]
AN 115,115
AN 116,118
NV 144,28,220
H 2
AN 117,119
v_mfma_f32_16x16x32_f16 v[148:151], a[224:227], a[24:27], v[128:131]
SH 118
SH 119
NV 148,28,228
R 82
NV 160,0,200
AN 118,118
AN 119,119
NV 160,4,204
Q 16
NV 164,0,208
Q 18
NV 164,4,212
Q 20
v_mfma_f32_16x16x32_f16 v[168:171], a[216:219], a[0:3], v[128:131]
Q 22
NV 168,4,220
PF 72,0
PF 73,1
v_mfma_f32_16x16x32_f16 v[172:175], a[224:227], a[0:3], v[128:131]
PF 74,2
PF 75,3
NV 172,4,228
WL 2
PF 76,4
PF 77,5
NV 184,8,200
PF 78,6
PF 79,7
NV 184,12,204
Q 24
NV 188,8,208
Q 26
NV 188,12,212
Q 28
v_mfma_f32_16x16x32_f16 v[192:195], a[216:219], a[8:11], v[128:131]
WV 20
Q 30
NV 192,12,220
WL 0
PF 80,0
PF 81,1
v_mfma_f32_16x16x32_f16 v[196:199], a[224:227], a[8:11], v[128:131]
s_barrier
R 0
NV 196,12,228
R 2
NV 208,16,200
mw 72
PF 82,2
PF 83,3
NV 208,20,204
ad 86,336
DR 0
NV 232,24,200
PF 84,4
PF 85,5
NV 232,28,204
DR 1024
PF 86,6
NV 212,16,208
PF 87,7
AN 120,122
NV 212,20,212
DR 2048
AN 121,123
NV 236,24,208
SH 122
SH 123
NV 236,28,212
mw 75
AN 122,122
AN 123,123
v_mfma_f32_16x16x32_f16 v[216:219], a[216:219], a[16:19], v[128:131]
ad 86,342
DR 0
NV 216,20,220
AN 124,126
AN 125,127
v_mfma_f32_16x16x32_f16 v[240:243], a[216:219], a[24:27], v[128:131]
DR 1024
SH 126
NV 240,28,220
SH 127
AN 126,126
v_mfma_f32_16x16x32_f16 v[220:223], a[224:227], a[16:19], v[128:131]
DR 2048
AN 127,127
NV 220,20,228
R 4
v_mfma_f32_16x16x32_f16 v[244:247], a[224:227], a[24:27], v[128:131]
Q 32
NV 244,28,228
WL 2
R 6
M 0
Q 34
M 1
Q 36
M 12
Q 38
M 13
PF 88,0
PF 89,1
M 2
PF 90,2
PF 91,3
M 3
PF 92,4
PF 93,5
M 14
PF 94,6
PF 95,7
M 15
Q 40
M 4
Q 42
M 5
Q 44
M 16
Q 46
M 17
PF 96,0
PF 97,1
M 6
PF 98,2
PF 99,3
M 7
PF 100,4
PF 101,5
M 18
PF 102,6
PF 103,7
M 19
RS 14,384
RS 18,448
v_mfma_f32_16x16x32_f16 a[52:55], v[88:91], a[0:3], a[32:35]
)"
R"(v_lshrrev_b32 v9, 8, v0
v_bfe_u32 v8, v0, 4, 4
M 9
v_lshlrev_b32 v10, 2, v9
v_mfma_f32_16x16x32_f16 a[76:79], v[88:91], a[8:11], a[32:35]
s_mov_b32 s42, 0xcb527610
M 21
s_movk_i32 s43, 0xc00
v_lshlrev_b32 v11, 3, v8
v_bfe_u32 v3, s42, v10, 4
v_mfma_f32_16x16x32_f16 a[56:59], v[96:99], a[0:3], a[32:35]
v_add_u32 v10, 16, v10
v_mad_u32_u24 v3, v3, s43, v11
M 11
R 8
v_mfma_f32_16x16x32_f16 a[80:83], v[96:99], a[8:11], a[32:35]
WL 2
v_pk_mul_f16 v22, v14, s92
v_pk_mul_f16 v23, v15, s92
M 23
R 10
M 24
v_pk_mul_f16 v24, v16, s92
v_pk_mul_f16 v25, v17, s92
M 25
v_pk_mul_f16 v26, v18, s92
v_pk_mul_f16 v27, v19, s92
M 26
v_pk_mul_f16 v28, v20, s92
v_pk_mul_f16 v29, v21, s92
M 27
v_bfe_u32 v48, s42, v10, 4
v_add_u32 v10, 1, v9
M 28
v_mad_u32_u24 v48, v48, s43, v11
v_and_b32 v10, 2, v10
M 29
v_sub_u32 v10, 1, v10
v_mul_i32_i24 v49, s43, v10
M 30
M 31
v_mfma_f32_16x16x32_f16 a[100:103], v[88:91], a[16:19], a[32:35]
M 33
v_mfma_f32_16x16x32_f16 a[104:107], v[96:99], a[16:19], a[32:35]
M 35
M 36
M 37
M 38
M 39
M 40
M 41
M 42
M 43
WV 18
v_mfma_f32_16x16x32_f16 a[124:127], v[88:91], a[24:27], a[32:35]
WL 0
M 45
s_barrier
R 24
TY 106,520
v_mfma_f32_16x16x32_f16 a[128:131], v[96:99], a[24:27], a[32:35]
TY 110,14344
TX 32,0
TX 36,13824
M 47
R 26
TY 40,512
M 48
TY 44,14336
TY 114,2056
TY 118,15880
M 49
M 50
M 51
M 52
WL 9
AN 104,106
AN 105,107
M 53
bc 49,5,66,60
SH 106
M 54
mh 18
SH 107
AN 106,106
M 55
ad 66,960
H 0
v_mfma_f32_16x16x32_f16 a[148:151], v[88:91], a[0:3], a[32:35]
WL 8
AN 107,107
AN 108,110
M 57
AN 109,111
SH 110
v_mfma_f32_16x16x32_f16 a[152:155], v[96:99], a[0:3], a[32:35]
SH 111
AN 110,110
M 59
R 28
M 60
DG 1024
M 61
WL 5
AN 111,111
PA_ 30,104,32
M 62
PA_ 31,105,33
PB_ 32,104,32
M 63
PB_ 33,105,33
PA_ 34,108,36
M 64
PA_ 35,109,37
PB_ 36,108,36
M 65
PB_ 37,109,37
PF 30,0
M 66
v_lshlrev_b32 v55, 4, v0
PF 31,1
M 67
FL
PF 32,2
v_mfma_f32_16x16x32_f16 a[172:175], v[88:91], a[8:11], a[32:35]
PF 33,3
PF 34,4
M 69
PF 35,5
PF 36,6
v_mfma_f32_16x16x32_f16 a[176:179], v[96:99], a[8:11], a[32:35]
PF 37,7
AW 200,30
M 71
R 30
M 72
AW 201,31
AW 202,32
M 73
AW 203,33
AW 204,34
M 74
AW 205,35
AW 206,36
M 75
WL 2
AW 207,37
PA_ 38,106,40
M 76
PA_ 39,107,41
PB_ 40,106,40
TX 32,1536
M 77
PB_ 41,107,41
PA_ 42,110,44
TX 36,15360
M 78
PA_ 43,111,45
PB_ 44,110,44
M 79
PB_ 45,111,45
PF 38,0
v_mfma_f32_16x16x32_f16 a[196:199], v[88:91], a[16:19], a[32:35]
PF 39,1
PF 40,2
M 81
PF 41,3
PF 42,4
v_mfma_f32_16x16x32_f16 v[132:135], v[96:99], a[16:19], v[128:131]
PF 43,5
PF 44,6
M 83
R 32
M 84
PF 45,7
AW 208,38
M 85
bc 48,5,64,56
AW 209,39
M 86
mh 21
AW 210,40
AW 211,41
M 87
ad 64,960
H 0
M 88
AW 212,42
AW 213,43
M 89
AW 214,44
AW 215,45
M 90
TY 40,2048
TY 44,15872
TY 122,3592
M 91
H 1
AN 112,114
TY 126,17416
v_mfma_f32_16x16x32_f16 v[152:155], v[88:91], a[24:27], v[128:131]
AN 113,115
SH 114
M 93
SH 115
AN 114,114
v_mfma_f32_16x16x32_f16 v[156:159], v[96:99], a[24:27], v[128:131]
AN 115,115
AN 116,118
M 95
R 34
M 96
H 2
AN 117,119
M 97
SH 118
SH 119
M 98
AN 118,118
AN 119,119
M 99
WL 6
PA_ 30,112,32
PA_ 31,113,33
M 100
PB_ 32,112,32
PB_ 33,113,33
M 101
PA_ 34,116,36
PA_ 35,117,37
M 102
PB_ 36,116,36
PB_ 37,117,37
M 103
PF 30,0
PF 31,1
v_mfma_f32_16x16x32_f16 v[176:179], v[88:91], a[0:3], v[128:131]
PF 32,2
PF 33,3
M 105
PF 34,4
PF 35,5
v_mfma_f32_16x16x32_f16 v[180:183], v[96:99], a[0:3], v[128:131]
PF 36,6
PF 37,7
M 107
AW 216,30
AW 217,31
M 108
AW 218,32
AW 219,33
M 109
AW 220,34
AW 221,35
M 110
WV 3
v_cmp_eq_u32 vcc, 0, v54
AW 222,36
M 111
WL 0
AW 223,37
PA_ 38,114,40
M 112
PA_ 39,115,41
PB_ 40,114,40
TX 32,3072
M 113
FB
PB_ 41,115,41
PA_ 42,118,44
TX 36,16896
M 114
PA_ 43,119,45
PB_ 44,118,44
M 115
PB_ 45,119,45
PF 38,0
v_mfma_f32_16x16x32_f16 v[200:203], v[88:91], a[8:11], v[128:131]
PF 39,1
PF 40,2
M 117
s_barrier
R 48
v_mfma_f32_16x16x32_f16 v[204:207], v[96:99], a[8:11], v[128:131]
mw 0
PF 41,3
PF 42,4
M 119
R 50
M 120
ad 84,384
W 0
M 121
W 1
PF 43,5
M 132
W 2
PF 44,6
M 133
mw 3
PF 45,7
AW 224,38
M 122
ad 84,390
W 0
M 123
W 1
AW 225,39
M 134
W 2
AW 226,40
M 135
mw 24
AW 227,41
AW 228,42
M 124
ad 84,432
W 0
M 125
W 1
AW 229,43
M 136
W 2
AW 230,44
M 137
mw 27
AW 231,45
AN 120,122
M 126
ad 84,438
W 0
TY 40,3584
M 127
W 1
TY 44,17408
M 138
W 2
AN 121,123
M 139
SH 122
SH 123
v_mfma_f32_16x16x32_f16 v[224:227], v[88:91], a[16:19], v[128:131]
AN 122,122
AN 123,123
M 129
AN 124,126
AN 125,127
v_mfma_f32_16x16x32_f16 v[248:251], v[88:91], a[24:27], v[128:131]
SH 126
SH 127
M 141
AN 126,126
AN 127,127
v_mfma_f32_16x16x32_f16 v[228:231], v[96:99], a[16:19], v[128:131]
WL 2
PA_ 30,120,32
PA_ 31,121,33
M 131
R 52
v_mfma_f32_16x16x32_f16 v[252:255], v[96:99], a[24:27], v[128:131]
PB_ 32,120,32
PB_ 33,121,33
M 143
R 54
NA 36,0,200
PA_ 34,124,36
PA_ 35,125,37
NA 36,4,204
PB_ 36,124,36
PB_ 37,125,37
NA 60,8,200
PF 30,0
PF 31,1
NA 60,12,204
PF 32,2
PF 33,3
NA 40,0,208
PF 34,4
PF 35,5
NA 40,4,212
PF 36,6
PF 37,7
NA 64,8,208
AW 232,30
AW 233,31
NA 64,12,212
AW 234,32
AW 235,33
NA 44,0,216
AW 236,34
AW 237,35
NA 44,4,220
WL 4
AW 238,36
AW 239,37
PA_ 38,122,40
NA 68,8,216
PA_ 39,123,41
PB_ 40,122,40
PB_ 41,123,41
)"
R"(NA 68,12,220
PA_ 42,126,44
PA_ 43,127,45
PB_ 44,126,44
NA 48,0,224
PB_ 45,127,45
PF 38,0
PF 39,1
NA 48,4,228
PF 40,2
PF 41,3
PF 42,4
NA 72,8,224
PF 43,5
PF 44,6
PF 45,7
NA 72,12,228
AW 240,38
AW 241,39
AW 242,40
NA 52,0,232
AW 243,41
AW 244,42
AW 245,43
NA 52,4,236
AW 246,44
AW 247,45
NA 76,8,232
NA 76,12,236
NA 56,0,240
NA 56,4,244
R 56
NA 80,8,240
NA 80,12,244
WL 2
R 58
NA 84,16,200
NA 84,20,204
NA 88,16,208
NA 88,20,212
NA 92,16,216
NA 92,20,220
NA 96,16,224
NA 96,20,228
NA 100,16,232
NA 100,20,236
NA 104,16,240
NA 104,20,244
NA 108,24,200
NA 108,28,204
NA 112,24,208
NA 112,28,212
NA 116,24,216
NA 116,28,220
NA 120,24,224
NA 120,28,228
NA 124,24,232
WL 0
NA 124,28,236
s_barrier
R 72
TY 106,5128
NA 128,24,240
mw 48
TY 110,18952
TX 58,4608
TX 62,18432
NA 128,28,244
R 74
TY 66,5120
NA 132,0,200
ad 84,480
W 0
TY 70,18944
NA 132,4,204
v_lshrrev_b32 v8, 1, v0
s_mov_b64 exec, s[76:77]
global_load_dwordx2 v[6:7], v8, s[78:79] sc1
s_mov_b64 exec, -1
NA 136,0,208
TY 114,6664
TY 118,20488
TX 74,6144
NA 136,4,212
WL 7
TX 78,19968
TY 82,6656
TY 86,20480
NA 140,0,216
W 1
AN 104,106
TY 122,8200
NA 140,4,220
AN 105,107
SH 106
TY 126,22024
NA 144,0,224
SH 107
AN 106,106
TX 90,7680
NA 144,4,228
AN 107,107
AN 108,110
TX 94,21504
NA 148,0,232
W 2
AN 109,111
TY 98,8192
NA 148,4,236
WL 9
SH 110
SH 111
TY 102,22016
NA 152,0,240
AN 110,110
AN 111,111
NA 152,4,244
R 76
NA 156,8,200
mw 51
Q 0
NA 156,12,204
ad 84,486
W 0
NA 160,8,208
Q 2
NA 160,12,212
Q 4
NA 164,8,216
Q 6
NA 164,12,220
W 1
P 8
NA 168,8,224
Q 9
NA 168,12,228
Q 11
NA 172,8,232
Q 13
NA 172,12,236
W 2
P 15
NA 176,8,240
AN 112,114
AN 113,115
NA 176,12,244
R 78
NA 180,16,200
SH 114
SH 115
NA 180,20,204
bc 49,6,66,60
AN 114,114
NA 184,16,208
mh 0
AN 115,115
AN 116,118
NA 184,20,212
ad 66,1152
H 0
NA 188,16,216
AN 117,119
SH 118
NA 188,20,220
SH 119
AN 118,118
NA 192,16,224
WL 2
AN 119,119
P 16
NA 192,20,228
DG 1024
NA 196,16,232
Q 17
NA 196,20,236
Q 19
NV 132,16,240
Q 21
NV 132,20,244
R 80
NV 136,24,200
Q 23
NV 136,28,204
bc 48,6,64,56
P 25
NV 140,24,208
mh 3
Q 26
NV 140,28,212
ad 64,1152
H 0
NV 144,24,216
Q 28
NV 144,28,220
Q 30
NV 148,24,224
AN 120,122
AN 121,123
NV 148,28,228
H 1
SH 122
NV 152,24,232
SH 123
AN 122,122
NV 152,28,236
AN 123,123
AN 124,126
NV 156,24,240
AN 125,127
SH 126
NV 156,28,244
R 82
NV 160,0,200
H 2
SH 127
NV 160,4,204
AN 126,126
AN 127,127
NV 164,0,208
Q 32
NV 164,4,212
Q 34
NV 168,0,216
Q 36
NV 168,4,220
Q 38
NV 172,0,224
Q 40
NV 172,4,228
Q 42
NV 176,0,232
Q 44
NV 176,4,236
Q 46
NV 180,0,240
NV 180,4,244
WL 2
NV 184,8,200
NV 184,12,204
NV 188,8,208
NV 188,12,212
NV 192,8,216
NV 192,12,220
NV 196,8,224
NV 196,12,228
WV 18
NV 200,8,232
WL 0
NV 200,12,236
s_barrier
R 0
NV 204,8,240
mw 72
NV 204,12,244
R 2
NV 208,16,200
ad 84,528
W 0
NV 208,20,204
NV 232,24,200
NV 232,28,204
NV 212,16,208
W 1
NV 212,20,212
NV 236,24,208
NV 236,28,212
NV 216,16,216
W 2
NV 216,20,220
ae 44,70,0
NV 240,24,216
s_bitcmp1_b32 s50, 0
s_cselect_b32 s51, 24576, 0
v_add_u32 v47, s51, v3
NV 240,28,220
XL 14,0
NV 220,16,224
mw 75
NV 220,20,228
ad 84,534
W 0
NV 244,24,224
XL 16,768
NV 244,28,228
NV 224,16,232
NV 224,20,236
W 1
NV 248,24,232
NV 248,28,236
NV 228,16,240
XL 18,1536
NV 228,20,244
R 4
NV 252,24,240
W 2
NV 252,28,244
WL 2
R 6
M 0
M 1
M 12
XL 20,2304
M 13
M 2
M 3
WV 20
v_or_b32 v6, v6, v7
s_mov_b64 exec, s[76:77]
v_cmp_ne_u32 %[flg], 0, v6
s_mov_b64 exec, -1
M 14
M 15
M 4
M 5
M 16
M 17
M 6
M 7
M 18
M 19
M 8
M 9
M 20
M 21
M 10
M 11
R 8
M 22
M 23
WL 4
R 10
M 24
M 25
M 26
M 27
M 28
M 29
M 30
M 31
M 32
M 33
M 34
M 35
WL 4
M 36
M 37
M 38
M 39
M 40
M 41
M 42
M 43
M 44
WL 0
M 45
s_barrier
R 24
M 46
mw 0
M 47
R 26
M 48
ad 84,576
W 0
M 49
TY 106,9736
TY 110,23560
M 50
M 51
M 52
W 1
M 53
M 54
M 55
M 56
W 2
M 57
M 58
WL 1
AN 104,106
AN 105,107
M 59
R 28
M 60
mw 3
SH 106
SH 107
M 61
ad 84,582
W 0
M 62
AN 106,106
AN 107,107
M 63
WL 2
AN 108,110
AN 109,111
M 64
SH 110
SH 111
)"
R"(M 65
W 1
AN 110,110
M 66
TY 114,11272
AN 111,111
M 67
TY 118,25096
M 68
M 69
W 2
M 70
M 71
R 30
M 72
M 73
bc 49,7,66,60
M 74
mh 9
M 75
ad 66,1344
H 0
M 76
WL 3
AN 112,114
AN 113,115
M 77
SH 114
SH 115
M 78
AN 114,114
AN 115,115
M 79
DG 1024
M 80
WL 2
AN 116,118
AN 117,119
M 81
SH 118
SH 119
M 82
AN 118,118
AN 119,119
M 83
R 32
M 84
TY 122,12808
TY 126,26632
M 85
bc 48,7,64,56
M 86
mh 12
M 87
ad 64,1344
H 0
s_load_dwordx4 s[88:91], %[pp], 0xc0
s_load_dwordx2 s[86:87], %[pp], 0xd0
M 88
M 89
M 90
M 91
H 1
M 92
M 93
WL 1
AN 120,122
AN 121,123
M 94
SH 122
SH 123
M 95
R 34
M 96
H 2
AN 122,122
M 97
WL 2
AN 123,123
AN 124,126
M 98
AN 125,127
SH 126
M 99
SH 127
AN 126,126
M 100
AN 127,127
M 101
M 102
M 103
M 104
M 105
M 106
M 107
M 108
M 109
M 110
M 111
M 112
M 113
M 114
M 115
WV 26
M 116
s_waitcnt lgkmcnt(0)
M 117
s_barrier
R 48
M 118
mw 24
M 119
R 50
M 120
ad 84,624
W 0
M 121
M 132
M 133
TX 58,9216
TX 62,23040
M 122
W 1
M 123
M 134
M 135
TY 66,9728
TY 70,23552
M 124
W 2
M 125
M 136
ae 44,70,0
M 137
TX 74,10752
TX 78,24576
M 126
mw 27
WL 4
Q 0
M 127
s_bfe_u32 s51, s50, 0x10001
Q 2
M 138
Q 4
M 139
TY 82,11264
TY 86,25088
M 128
Q 6
M 129
WL 4
Q 8
M 140
Q 10
M 141
TX 90,12288
TX 94,26112
M 130
Q 12
M 131
R 52
M 142
Q 14
M 143
TY 98,12800
TY 102,26624
M 0
R 54
M 1
WL 10
Q 16
M 12
Q 18
M 13
Q 20
M 2
Q 22
M 3
WL 8
Q 24
M 14
Q 26
M 15
Q 28
M 4
Q 30
M 5
WL 6
Q 32
M 16
Q 34
M 17
Q 36
M 6
Q 38
M 7
WL 2
Q 40
M 18
Q 42
M 19
Q 44
M 8
Q 46
M 9
ad 84,630
M 20
W 0
v_mad_i32_i24 v47, v49, s51, v48
M 21
XL 22,0
M 10
XL 24,768
M 11
R 56
M 22
W 1
XL 26,1536
M 23
R 58
M 24
XL 28,2304
M 25
M 26
W 2
M 27
M 28
M 29
M 30
M 31
M 32
M 33
v_mov_b32 v54, v52
M 34
M 35
WL 4
M 36
v_permlane32_swap_b32 v52, v54
M 37
M 38
M 39
v_add_f32 v52, v52, v54
v_mov_b32 v54, v52
M 40
WV 27
UL 6,14
UH 7,14
M 41
UL 8,15
UH 9,15
M 42
v_permlane16_swap_b32 v52, v54
AR 10,36
M 43
WV 15
AR 11,60
AR 12,84
M 44
WL 0
AR 13,108
AD 10,6
M 45
s_barrier
R 72
M 46
v_add_f32 v52, v52, v54
AD 11,7
M 47
R 74
M 48
TY 106,520
TY 110,14344
M 49
v_permlane16_swap_b32 v52, v53
v_mov_b32 v54, s88
M 50
v_mov_b32 v53, s89
AD 12,8
M 51
AD 13,9
AW 36,10
M 52
AW 60,11
AW 84,12
M 53
v_permlane16_swap_b32 v54, v53
v_bfe_u32 v53, v0, 2, 6
M 54
AW 108,13
UL 6,16
M 55
UH 7,16
UL 8,17
M 56
WL 1
v_add_u32 v54, v53, v54
UH 9,17
M 57
mw 48
AN 104,106
AN 105,107
M 58
SH 106
SH 107
M 59
R 76
M 60
AN 106,106
AN 107,107
M 61
WL 2
AN 108,110
AN 109,111
M 62
SH 110
SH 111
M 63
AN 110,110
AN 111,111
M 64
TY 114,2056
TY 118,15880
M 65
ad 84,672
M 66
W 0
AR 10,37
M 67
AR 11,61
AR 12,85
M 68
AR 13,109
AD 10,6
M 69
W 1
AD 11,7
M 70
AD 12,8
AD 13,9
M 71
R 78
M 72
W 2
AW 37,10
M 73
WL 3
AN 112,114
AN 113,115
M 74
SH 114
SH 115
M 75
mw 51
AN 114,114
AN 115,115
M 76
WL 2
AN 116,118
AN 117,119
M 77
SH 118
SH 119
M 78
AN 118,118
AN 119,119
M 79
TY 122,3592
TY 126,17416
M 80
ad 84,678
W 0
M 81
AW 61,11
AW 85,12
M 82
AW 109,13
UL 6,18
M 83
R 80
M 84
W 1
UH 7,18
M 85
UL 8,19
UH 9,19
M 86
AR 10,38
AR 11,62
M 87
W 2
AR 12,86
M 88
WL 3
AN 120,122
AN 121,123
M 89
SH 122
SH 123
M 90
)"
R"(AN 122,122
AN 123,123
M 91
WL 2
AN 124,126
AN 125,127
M 92
SH 126
SH 127
M 93
AN 126,126
AN 127,127
M 94
bc 49,8,66,60
AR 13,110
M 95
R 82
M 96
mh 18
AD 10,6
AD 11,7
M 97
ad 66,1536
H 0
M 98
AD 12,8
AD 13,9
M 99
AW 38,10
AW 62,11
M 100
DG 1024
M 101
AW 86,12
AW 110,13
M 102
UL 6,20
UH 7,20
M 103
bc 48,8,64,56
UL 8,21
M 104
mh 21
UH 9,21
AR 10,39
M 105
ad 64,1536
H 0
M 106
AR 11,63
AR 12,87
M 107
R 0
M 108
H 1
AR 13,111
M 109
AD 10,6
AD 11,7
M 110
AD 12,8
AD 13,9
M 111
H 2
AW 39,10
M 112
AW 63,11
AW 87,12
M 113
AW 111,13
M 114
M 115
M 116
M 117
M 118
M 119
WL 2
R 2
M 120
s_bitcmp1_b32 s87, 0
s_cselect_b32 exec_lo, 0xffffff, 0
s_mov_b32 exec_hi, 0
global_atomic_add_f32 v54, v52, s[90:91]
s_mov_b64 exec, -1
M 121
M 132
M 133
TX 58,0
TX 62,13824
M 122
M 123
M 134
M 135
TY 66,512
TY 70,14336
M 124
M 125
M 136
M 137
TX 74,1536
TX 78,15360
M 126
WL 4
Q 0
M 127
Q 2
M 138
Q 4
M 139
TY 82,2048
TY 86,15872
M 128
Q 6
M 129
WL 4
Q 8
M 140
Q 10
M 141
TX 90,3072
TX 94,16896
M 130
Q 12
M 131
R 4
M 142
Q 14
M 143
TY 98,3584
TY 102,17408
M 0
R 6
M 1
WL 10
Q 16
M 12
Q 18
M 13
Q 20
M 2
Q 22
M 3
WL 8
Q 24
M 14
Q 26
M 15
Q 28
M 4
Q 30
M 5
WL 6
Q 32
M 16
Q 34
M 17
Q 36
M 6
Q 38
M 7
WL 2
Q 40
M 18
Q 42
M 19
Q 44
M 8
Q 46
M 9
ae 44,68,0
M 20
s_bitcmp1_b32 s50, 2
s_cselect_b32 s51, 24576, 0
v_add_u32 v47, s51, v3
M 21
XL 30,0
M 10
XL 32,768
M 11
R 8
M 22
XL 34,1536
M 23
R 10
M 24
XL 36,2304
M 25
M 26
M 27
M 28
M 29
M 30
M 31
M 32
M 33
M 34
M 35
WL 4
M 36
M 37
M 38
M 39
M 40
M 41
M 42
WV 20
UL 6,22
UH 7,22
M 43
WV 10
UL 8,23
UH 9,23
M 44
WL 0
AR 10,40
AR 11,64
M 45
s_barrier
R 24
M 46
mw 72
AR 12,88
AR 13,112
M 47
R 26
M 48
TY 106,5128
TY 110,18952
M 49
ad 84,720
W 0
M 50
s_bitcmp1_b32 s87, 1
s_cselect_b32 exec_lo, 0xffffff, 0
s_mov_b32 exec_hi, 0
global_atomic_add_f32 v54, v52, s[90:91]
s_mov_b64 exec, -1
M 51
AD 10,6
AD 11,7
M 52
W 1
AD 12,8
M 53
AD 13,9
AW 40,10
M 54
AW 64,11
AW 88,12
M 55
W 2
AW 112,13
M 56
UL 6,24
UH 7,24
M 57
WL 1
AN 104,106
AN 105,107
M 58
mw 75
SH 106
SH 107
M 59
R 28
M 60
AN 106,106
AN 107,107
M 61
WL 2
AN 108,110
AN 109,111
M 62
SH 110
SH 111
M 63
AN 110,110
AN 111,111
M 64
TY 114,6664
TY 118,20488
M 65
ad 84,726
M 66
W 0
UL 8,25
M 67
UH 9,25
AR 10,41
M 68
AR 11,65
AR 12,89
M 69
W 1
AR 13,113
M 70
AD 10,6
AD 11,7
M 71
R 30
M 72
W 2
AD 12,8
M 73
WL 3
AN 112,114
AN 113,115
M 74
SH 114
SH 115
M 75
mw 0
AN 114,114
AN 115,115
M 76
WL 2
AN 116,118
AN 117,119
M 77
SH 118
SH 119
M 78
AN 118,118
AN 119,119
M 79
TY 122,8200
TY 126,22024
M 80
ad 84,768
W 0
M 81
AD 13,9
AW 41,10
M 82
AW 65,11
AW 89,12
M 83
R 32
M 84
W 1
AW 113,13
M 85
UL 6,26
UH 7,26
M 86
UL 8,27
UH 9,27
M 87
W 2
AR 10,42
M 88
WL 3
AN 120,122
AN 121,123
M 89
SH 122
SH 123
M 90
mw 3
AN 122,122
AN 123,123
M 91
WL 2
AN 124,126
AN 125,127
M 92
SH 126
SH 127
M 93
AN 126,126
AN 127,127
M 94
ad 84,774
W 0
M 95
R 34
M 96
AR 11,66
AR 12,90
M 97
W 1
AR 13,114
M 98
AD 10,6
AD 11,7
M 99
AD 12,8
AD 13,9
M 100
W 2
AW 42,10
M 101
AW 66,11
AW 90,12
M 102
AW 114,13
UL 6,28
M 103
bc 49,9,66,60
UH 7,28
)"
R"(M 104
mh 0
UL 8,29
UH 9,29
M 105
ad 66,1728
H 0
M 106
AR 10,43
AR 11,67
M 107
R 48
M 108
DG 1024
M 109
AR 12,91
AR 13,115
M 110
AD 10,6
AD 11,7
M 111
bc 48,9,64,56
AD 12,8
M 112
mh 3
AD 13,9
AW 43,10
M 113
ad 64,1728
H 0
M 114
AW 67,11
AW 91,12
M 115
AW 115,13
M 116
H 1
M 117
M 118
M 119
WL 2
R 50
M 120
H 2
M 121
s_bitcmp1_b32 s87, 2
s_cselect_b32 exec_lo, 0xffffff, 0
s_mov_b32 exec_hi, 0
global_atomic_add_f32 v54, v52, s[90:91]
s_mov_b64 exec, -1
M 132
M 133
TX 58,4608
TX 62,18432
M 122
M 123
M 134
M 135
TY 66,5120
TY 70,18944
M 124
M 125
M 136
M 137
TX 74,6144
TX 78,19968
M 126
WL 4
Q 0
M 127
Q 2
M 138
Q 4
M 139
TY 82,6656
TY 86,20480
M 128
Q 6
M 129
WL 4
Q 8
M 140
Q 10
M 141
TX 90,7680
TX 94,21504
M 130
Q 12
M 131
R 52
M 142
Q 14
M 143
TY 98,8192
TY 102,22016
M 0
R 54
M 1
WL 10
Q 16
M 12
Q 18
M 13
Q 20
M 2
Q 22
M 3
WL 8
Q 24
M 14
Q 26
M 15
Q 28
M 4
Q 30
M 5
WL 6
Q 32
M 16
Q 34
M 17
Q 36
M 6
Q 38
M 7
WL 2
Q 40
M 18
Q 42
M 19
Q 44
M 8
Q 46
M 9
ae 44,68,0
M 20
s_bfe_u32 s51, s50, 0x10003
v_mad_i32_i24 v47, v49, s51, v48
XL 38,0
M 21
XL 40,768
M 10
XL 42,1536
M 11
R 56
M 22
XL 44,2304
M 23
R 58
M 24
M 25
M 26
M 27
M 28
M 29
M 30
M 31
M 32
M 33
M 34
M 35
WL 4
M 36
M 37
M 38
M 39
M 40
M 41
M 42
M 43
WL 0
M 44
WV 10
UL 6,30
UH 7,30
M 45
s_barrier
R 72
M 46
mw 24
UL 8,31
UH 9,31
M 47
R 74
M 48
TY 106,9736
TY 110,23560
M 49
ad 84,816
W 0
M 50
s_bitcmp1_b32 s87, 3
s_cselect_b32 exec_lo, 0xffffff, 0
s_mov_b32 exec_hi, 0
global_atomic_add_f32 v54, v52, s[90:91]
s_mov_b64 exec, -1
M 51
AR 10,44
AR 11,68
M 52
W 1
AR 12,92
M 53
AR 13,116
AD 10,6
M 54
AD 11,7
AD 12,8
M 55
W 2
AD 13,9
M 56
AW 44,10
AW 68,11
M 57
WL 1
AN 104,106
AN 105,107
M 58
mw 27
SH 106
SH 107
M 59
R 76
M 60
AN 106,106
AN 107,107
M 61
WL 2
AN 108,110
AN 109,111
M 62
SH 110
SH 111
M 63
AN 110,110
AN 111,111
M 64
TY 114,11272
TY 118,25096
M 65
ad 84,822
M 66
W 0
AW 92,12
M 67
AW 116,13
UL 6,32
M 68
UH 7,32
UL 8,33
M 69
W 1
UH 9,33
M 70
AR 10,45
AR 11,69
M 71
R 78
M 72
W 2
AR 12,93
M 73
WL 3
AN 112,114
AN 113,115
M 74
SH 114
SH 115
M 75
mw 48
AN 114,114
AN 115,115
M 76
WL 2
AN 116,118
AN 117,119
M 77
SH 118
SH 119
M 78
AN 118,118
AN 119,119
M 79
TY 122,12808
TY 126,26632
M 80
ad 84,864
W 0
M 81
AR 13,117
AD 10,6
M 82
AD 11,7
AD 12,8
M 83
R 80
M 84
W 1
AD 13,9
M 85
AW 45,10
AW 69,11
M 86
AW 93,12
AW 117,13
M 87
W 2
UL 6,34
M 88
WL 3
AN 120,122
AN 121,123
M 89
SH 122
SH 123
M 90
mw 51
AN 122,122
AN 123,123
M 91
WL 2
AN 124,126
AN 125,127
M 92
SH 126
SH 127
M 93
AN 126,126
AN 127,127
M 94
ad 84,870
W 0
M 95
R 82
M 96
UH 7,34
UL 8,35
M 97
W 1
UH 9,35
M 98
AR 10,46
AR 11,70
M 99
AR 12,94
AR 13,118
M 100
W 2
AD 10,6
M 101
AD 11,7
AD 12,8
M 102
AD 13,9
AW 46,10
M 103
bc 49,10,66,60
AW 70,11
M 104
mh 9
AW 94,12
AW 118,13
M 105
ad 66,1920
H 0
M 106
UL 6,36
UH 7,36
M 107
R 0
M 108
DG 1024
M 109
UL 8,37
UH 9,37
M 110
AR 10,47
AR 11,71
M 111
bc 48,10,64,56
AR 12,95
M 112
mh 12
AR 13,119
AD 10,6
M 113
ad 64,1920
H 0
M 114
AD 11,7
AD 12,8
M 115
AD 13,9
AW 47,10
M 116
H 1
AW 71,11
M 117
AW 95,12
)"
R"(AW 119,13
M 118
M 119
WL 2
R 2
M 120
H 2
M 121
s_bitcmp1_b32 s87, 4
s_cselect_b32 exec_lo, 0xffffff, 0
s_mov_b32 exec_hi, 0
global_atomic_add_f32 v54, v52, s[90:91]
s_mov_b64 exec, -1
M 132
M 133
TX 58,9216
TX 62,23040
M 122
M 123
M 134
M 135
TY 66,9728
TY 70,23552
M 124
M 125
M 136
M 137
TX 74,10752
TX 78,24576
M 126
WL 4
Q 0
M 127
Q 2
M 138
Q 4
M 139
TY 82,11264
TY 86,25088
M 128
Q 6
M 129
WL 4
Q 8
M 140
Q 10
M 141
TX 90,12288
TX 94,26112
M 130
Q 12
M 131
R 4
M 142
Q 14
M 143
TY 98,12800
TY 102,26624
M 0
R 6
M 1
WL 10
Q 16
M 12
Q 18
M 13
Q 20
M 2
Q 22
M 3
WL 8
Q 24
M 14
Q 26
M 15
Q 28
M 4
Q 30
M 5
WL 6
Q 32
M 16
Q 34
M 17
Q 36
M 6
Q 38
M 7
WL 2
Q 40
M 18
Q 42
M 19
Q 44
M 8
Q 46
M 9
ae 44,68,96
M 20
s_bitcmp1_b32 s50, 4
s_cselect_b32 s51, 24576, 0
v_add_u32 v47, s51, v3
M 21
XL 14,0
M 10
XL 16,768
M 11
R 8
M 22
XL 18,1536
M 23
R 10
M 24
XL 20,2304
M 25
M 26
M 27
M 28
M 29
M 30
M 31
M 32
M 33
M 34
M 35
WL 4
M 36
M 37
M 38
M 39
M 40
M 41
M 42
M 43
WV 10
M 44
WL 0
M 45
s_barrier
R 24
M 46
mw 72
UL 6,38
UH 7,38
M 47
R 26
M 48
TY 106,520
TY 110,14344
M 49
ad 84,912
W 0
M 50
s_bitcmp1_b32 s87, 5
s_cselect_b32 exec_lo, 0xffffff, 0
s_mov_b32 exec_hi, 0
global_atomic_add_f32 v54, v52, s[90:91]
s_mov_b64 exec, -1
M 51
UL 8,39
UH 9,39
M 52
W 1
AR 10,48
M 53
AR 11,72
AR 12,96
M 54
AR 13,120
AD 10,6
M 55
W 2
AD 11,7
M 56
AD 12,8
AD 13,9
M 57
WL 1
AN 104,106
AN 105,107
M 58
mw 75
SH 106
SH 107
M 59
R 28
M 60
AN 106,106
AN 107,107
M 61
WL 2
AN 108,110
AN 109,111
M 62
SH 110
SH 111
M 63
AN 110,110
AN 111,111
M 64
TY 114,2056
TY 118,15880
M 65
ad 84,918
M 66
W 0
AW 48,10
M 67
AW 72,11
AW 96,12
M 68
AW 120,13
UL 6,40
M 69
W 1
UH 7,40
M 70
UL 8,41
UH 9,41
M 71
R 30
M 72
W 2
AR 10,49
M 73
WL 3
AN 112,114
AN 113,115
M 74
SH 114
SH 115
M 75
mw 0
AN 114,114
AN 115,115
M 76
WL 2
AN 116,118
AN 117,119
M 77
SH 118
SH 119
M 78
AN 118,118
AN 119,119
M 79
TY 122,3592
TY 126,17416
M 80
ad 84,960
W 0
M 81
AR 11,73
AR 12,97
M 82
AR 13,121
AD 10,6
M 83
R 32
M 84
W 1
AD 11,7
M 85
AD 12,8
AD 13,9
M 86
AW 49,10
AW 73,11
M 87
W 2
AW 97,12
M 88
WL 3
AN 120,122
AN 121,123
M 89
SH 122
SH 123
M 90
mw 3
AN 122,122
AN 123,123
M 91
WL 2
AN 124,126
AN 125,127
M 92
SH 126
SH 127
M 93
AN 126,126
AN 127,127
M 94
ad 84,966
W 0
M 95
R 34
M 96
AW 121,13
UL 6,42
M 97
W 1
UH 7,42
M 98
UL 8,43
UH 9,43
M 99
AR 10,50
AR 11,74
M 100
W 2
AR 12,98
M 101
AR 13,122
AD 10,6
M 102
AD 11,7
AD 12,8
M 103
bc 49,11,66,60
AD 13,9
M 104
mh 18
AW 50,10
AW 74,11
M 105
ad 66,2112
H 0
M 106
AW 98,12
AW 122,13
M 107
R 48
M 108
DG 1024
M 109
UL 6,44
UH 7,44
M 110
UL 8,45
UH 9,45
M 111
bc 48,11,64,56
AR 10,51
M 112
mh 21
AR 11,75
AR 12,99
M 113
ad 64,2112
H 0
M 114
AR 13,123
AD 10,6
M 115
AD 11,7
AD 12,8
M 116
H 1
AD 13,9
M 117
AW 51,10
AW 75,11
M 118
AW 99,12
AW 123,13
M 119
WL 0
R 50
M 120
H 2
M 121
s_bitcmp1_b32 s87, 6
s_cselect_b32 exec_lo, 0xffffff, 0
s_mov_b32 exec_hi, 0
global_atomic_add_f32 v54, v52, s[90:91]
s_mov_b64 exec, -1
M 132
M 133
PA 12288
TX 58,0
TX 62,13824
DF
M 122
M 123
M 134
M 135
TY 66,512
TY 70,14336
M 124
M 125
M 136
M 137
TX 74,1536
TX 78,15360
M 126
WL 4
Q 0
M 127
Q 2
)"
R"(M 138
Q 4
M 139
TY 82,2048
TY 86,15872
M 128
Q 6
M 129
WL 4
Q 8
M 140
Q 10
M 141
TX 90,3072
TX 94,16896
M 130
Q 12
M 131
R 52
M 142
Q 14
M 143
TY 98,3584
TY 102,17408
M 0
R 54
M 1
WL 10
Q 16
M 12
Q 18
M 13
Q 20
M 2
Q 22
M 3
WL 8
Q 24
M 14
Q 26
M 15
Q 28
M 4
Q 30
M 5
WL 6
Q 32
M 16
Q 34
M 17
Q 36
M 6
Q 38
M 7
WL 2
Q 40
M 18
Q 42
M 19
Q 44
M 8
Q 46
M 9
ae 44,68,96
M 20
s_bfe_u32 s51, s50, 0x10005
v_mad_i32_i24 v47, v49, s51, v48
XL 22,0
M 21
XL 24,768
M 10
XL 26,1536
M 11
R 56
M 22
XL 28,2304
M 23
R 58
M 24
M 25
M 26
M 27
M 28
M 29
M 30
M 31
M 32
M 33
M 34
M 35
WL 4
M 36
M 37
M 38
M 39
M 40
M 41
M 42
M 43
WV 11
M 44
WL 0
M 45
s_barrier
R 72
M 46
mw 24
M 47
R 74
M 48
TY 106,5128
TY 110,18952
M 49
ad 84,1008
W 0
M 50
s_bitcmp1_b32 s87, 7
s_cselect_b32 exec_lo, 0xffffff, 0
s_mov_b32 exec_hi, 0
global_atomic_add_f32 v54, v52, s[90:91]
s_mov_b64 exec, -1
M 51
UL 6,14
UH 7,14
M 52
W 1
UL 8,15
M 53
UH 9,15
AR 10,52
M 54
AR 11,76
AR 12,100
M 55
W 2
AR 13,124
M 56
AD 10,6
AD 11,7
M 57
WL 1
AN 104,106
AN 105,107
M 58
mw 27
SH 106
SH 107
M 59
R 76
M 60
AN 106,106
AN 107,107
M 61
WL 2
AN 108,110
AN 109,111
M 62
SH 110
SH 111
M 63
AN 110,110
AN 111,111
M 64
TY 114,6664
TY 118,20488
M 65
ad 84,1014
M 66
W 0
AD 12,8
M 67
AD 13,9
AW 52,10
M 68
AW 76,11
AW 100,12
M 69
W 1
AW 124,13
M 70
UL 6,16
UH 7,16
M 71
R 78
M 72
W 2
UL 8,17
M 73
WL 3
AN 112,114
AN 113,115
M 74
SH 114
SH 115
M 75
mw 48
AN 114,114
AN 115,115
M 76
WL 2
AN 116,118
AN 117,119
M 77
SH 118
SH 119
M 78
AN 118,118
AN 119,119
M 79
TY 122,8200
TY 126,22024
M 80
ad 84,1056
W 0
M 81
UH 9,17
AR 10,53
M 82
AR 11,77
AR 12,101
M 83
R 80
M 84
W 1
AR 13,125
M 85
AD 10,6
AD 11,7
M 86
AD 12,8
AD 13,9
M 87
W 2
AW 53,10
M 88
WL 3
AN 120,122
AN 121,123
M 89
SH 122
SH 123
M 90
mw 51
AN 122,122
AN 123,123
M 91
WL 2
AN 124,126
AN 125,127
M 92
SH 126
SH 127
M 93
AN 126,126
AN 127,127
M 94
ad 84,1062
W 0
M 95
R 82
M 96
AW 77,11
AW 101,12
M 97
W 1
AW 125,13
M 98
UL 6,18
UH 7,18
M 99
UL 8,19
UH 9,19
M 100
W 2
AR 10,54
M 101
AR 11,78
AR 12,102
M 102
AR 13,126
AD 10,6
M 103
bc 49,12,66,60
AD 11,7
M 104
mh 0
AD 12,8
AD 13,9
M 105
ad 66,2304
H 0
M 106
AW 54,10
AW 78,11
M 107
R 0
M 108
DG 1024
M 109
AW 102,12
AW 126,13
M 110
UL 6,20
UH 7,20
M 111
bc 48,12,64,56
UL 8,21
M 112
mh 3
UH 9,21
AR 10,55
M 113
ad 64,2304
H 0
M 114
AR 11,79
AR 12,103
M 115
AR 13,127
AD 10,6
M 116
H 1
AD 11,7
M 117
AD 12,8
AD 13,9
M 118
AW 55,10
AW 79,11
M 119
WL 0
R 2
M 120
H 2
AW 103,12
M 121
AW 127,13
M 132
M 133
TX 58,4608
TX 62,18432
M 122
M 123
M 134
M 135
TY 66,5120
TY 70,18944
M 124
M 125
M 136
M 137
TX 74,6144
TX 78,19968
M 126
WL 4
Q 0
M 127
Q 2
M 138
Q 4
M 139
TY 82,6656
TY 86,20480
M 128
Q 6
M 129
WL 4
Q 8
M 140
Q 10
M 141
TX 90,7680
TX 94,21504
M 130
Q 12
M 131
R 4
M 142
Q 14
M 143
TY 98,8192
TY 102,22016
M 0
R 6
M 1
WL 10
Q 16
M 12
Q 18
M 13
Q 20
M 2
Q 22
M 3
WL 8
Q 24
M 14
Q 26
M 15
Q 28
)"
R"(M 4
Q 30
M 5
WL 6
Q 32
M 16
Q 34
M 17
Q 36
M 6
Q 38
M 7
WL 2
Q 40
M 18
Q 42
M 19
Q 44
M 8
Q 46
M 9
M 20
M 21
M 10
M 11
R 8
M 22
M 23
R 10
M 24
M 25
M 26
M 27
M 28
M 29
M 30
M 31
M 32
ae 44,70,0
M 33
s_bitcmp1_b32 s50, 0
s_cselect_b32 s51, 24576, 0
v_add_u32 v47, s51, v3
M 34
XL 30,128
M 35
WL 4
M 36
M 37
M 38
XL 32,896
M 39
M 40
M 41
M 42
M 43
WV 7
M 44
WL 0
XL 34,1664
M 45
s_barrier
R 24
M 46
mw 72
M 47
R 26
M 48
TY 106,9736
TY 110,23560
M 49
ad 84,1104
W 0
M 50
XL 36,2432
UL 6,22
M 51
UH 7,22
UL 8,23
M 52
W 1
UH 9,23
M 53
AR 10,56
AR 11,80
M 54
AR 12,104
AR 13,128
M 55
W 2
AD 10,6
M 56
AD 11,7
AD 12,8
M 57
WL 1
AN 104,106
AN 105,107
M 58
mw 75
SH 106
SH 107
M 59
R 28
M 60
AN 106,106
AN 107,107
M 61
WL 2
AN 108,110
AN 109,111
M 62
SH 110
SH 111
M 63
AN 110,110
AN 111,111
M 64
TY 114,11272
TY 118,25096
M 65
ad 84,1110
M 66
W 0
AD 13,9
M 67
AW 56,10
AW 80,11
M 68
AW 104,12
AW 128,13
M 69
W 1
UL 6,24
M 70
UH 7,24
UL 8,25
M 71
R 30
M 72
W 2
UH 9,25
M 73
WL 3
AN 112,114
AN 113,115
M 74
SH 114
SH 115
M 75
mw 0
AN 114,114
AN 115,115
M 76
WL 2
AN 116,118
AN 117,119
M 77
SH 118
SH 119
M 78
AN 118,118
AN 119,119
M 79
TY 122,12808
TY 126,26632
M 80
ad 84,1152
W 0
M 81
AR 10,57
AR 11,81
M 82
AR 12,105
AR 13,129
M 83
R 32
M 84
W 1
AD 10,6
M 85
AD 11,7
AD 12,8
M 86
AD 13,9
AW 57,10
M 87
W 2
AW 81,11
M 88
WL 3
AN 120,122
AN 121,123
M 89
SH 122
SH 123
M 90
mw 3
AN 122,122
AN 123,123
M 91
WL 2
AN 124,126
AN 125,127
M 92
SH 126
SH 127
M 93
AN 126,126
AN 127,127
M 94
ad 84,1158
W 0
M 95
R 34
M 96
AW 105,12
AW 129,13
M 97
W 1
UL 6,26
M 98
UH 7,26
UL 8,27
M 99
UH 9,27
AR 10,58
M 100
W 2
AR 11,82
M 101
AR 12,106
AR 13,130
M 102
AD 10,6
AD 11,7
M 103
bc 49,13,66,60
AD 12,8
M 104
mh 9
AD 13,9
AW 58,10
M 105
ad 66,2496
H 0
M 106
AW 82,11
AW 106,12
M 107
R 48
M 108
DG 1024
M 109
AW 130,13
UL 6,28
M 110
UH 7,28
UL 8,29
M 111
bc 48,13,64,56
UH 9,29
M 112
mh 12
AR 10,59
AR 11,83
M 113
ad 64,2496
H 0
M 114
AR 12,107
AR 13,131
M 115
AD 10,6
AD 11,7
M 116
H 1
AD 12,8
M 117
AD 13,9
AW 59,10
M 118
AW 83,11
AW 107,12
M 119
WL 0
R 50
M 120
H 2
AW 131,13
M 121
M 132
M 133
PA 12288
TX 58,9216
TX 62,23040
DF
M 122
M 123
M 134
M 135
TY 66,9728
TY 70,23552
M 124
M 125
M 136
M 137
TX 74,10752
TX 78,24576
M 126
WL 4
Q 0
M 127
Q 2
M 138
Q 4
M 139
TY 82,11264
TY 86,25088
M 128
Q 6
M 129
WL 4
Q 8
M 140
Q 10
M 141
TX 90,12288
TX 94,26112
M 130
Q 12
M 131
R 52
M 142
Q 14
M 143
TY 98,12800
TY 102,26624
M 0
R 54
M 1
WL 10
Q 16
M 12
Q 18
M 13
Q 20
M 2
Q 22
M 3
WL 8
Q 24
M 14
Q 26
M 15
Q 28
M 4
Q 30
M 5
WL 6
Q 32
M 16
Q 34
M 17
Q 36
M 6
Q 38
M 7
WL 2
Q 40
M 18
Q 42
M 19
Q 44
M 8
Q 46
M 9
M 20
M 21
M 10
M 11
R 56
M 22
M 23
R 58
M 24
M 25
M 26
M 27
M 28
M 29
M 30
M 31
M 32
M 33
M 34
ae 44,70,0
M 35
WL 4
M 36
)"
R"(s_bfe_u32 s51, s50, 0x10001
v_mad_i32_i24 v47, v49, s51, v48
XL 38,128
M 37
M 38
M 39
M 40
XL 40,896
M 41
M 42
M 43
WV 8
M 44
WL 0
M 45
s_barrier
R 72
M 46
mw 24
v_mov_b32 v54, 2
s_mov_b64 exec, 1
FS
s_mov_b64 exec, -1
M 47
R 74
M 48
TY 106,520
TY 110,14344
M 49
ad 84,1200
W 0
M 50
XL 42,1664
M 51
M 52
W 1
XL 44,2432
M 53
M 54
M 55
W 2
M 56
M 57
WL 1
AN 104,106
AN 105,107
M 58
mw 27
SH 106
SH 107
M 59
R 76
M 60
AN 106,106
AN 107,107
M 61
WL 2
AN 108,110
AN 109,111
M 62
SH 110
SH 111
M 63
AN 110,110
AN 111,111
M 64
TY 114,2056
TY 118,15880
M 65
ad 84,1206
M 66
W 0
M 67
M 68
M 69
W 1
M 70
M 71
R 78
M 72
W 2
M 73
WL 3
AN 112,114
AN 113,115
M 74
SH 114
SH 115
M 75
mw 48
AN 114,114
AN 115,115
M 76
WL 2
AN 116,118
AN 117,119
M 77
SH 118
SH 119
M 78
AN 118,118
AN 119,119
M 79
TY 122,3592
TY 126,17416
M 80
ad 84,1248
W 0
M 81
M 82
M 83
R 80
M 84
W 1
M 85
M 86
M 87
W 2
M 88
WL 3
AN 120,122
AN 121,123
M 89
SH 122
SH 123
M 90
mw 51
AN 122,122
AN 123,123
M 91
WL 2
AN 124,126
AN 125,127
M 92
SH 126
SH 127
M 93
AN 126,126
AN 127,127
M 94
ad 84,1254
W 0
M 95
R 82
M 96
UL 6,30
UH 7,30
M 97
W 1
UL 8,31
M 98
UH 9,31
AR 10,132
M 99
AR 11,156
AR 12,180
M 100
W 2
AD 10,6
M 101
AD 11,7
AD 12,8
M 102
AD 136,9
AW 132,10
M 103
bc 49,14,66,60
AW 156,11
M 104
mh 18
AW 180,12
UL 6,32
M 105
ad 66,2688
H 0
M 106
UH 7,32
UL 8,33
M 107
R 0
M 108
DG 1024
M 109
UH 9,33
AR 10,133
M 110
AR 11,157
AR 12,181
M 111
bc 48,14,64,56
AD 10,6
M 112
mh 21
AD 11,7
AD 12,8
M 113
ad 64,2688
H 0
M 114
AD 137,9
AW 133,10
M 115
AW 157,11
AW 181,12
M 116
H 1
UL 6,34
M 117
UH 7,34
UL 8,35
M 118
UH 9,35
AR 10,134
M 119
WL 0
R 2
M 120
H 2
AR 11,158
M 121
AR 12,182
AD 10,6
M 132
AD 11,7
AD 12,8
M 133
TX 58,0
TX 62,13824
M 122
AD 138,9
AW 134,10
M 123
AW 158,11
AW 182,12
M 134
UL 6,36
UH 7,36
M 135
TY 66,512
TY 70,14336
M 124
UL 8,37
UH 9,37
M 125
AR 10,135
AR 11,159
M 136
AR 12,183
AD 10,6
M 137
TX 74,1536
TX 78,15360
M 126
WL 4
Q 0
M 127
Q 2
M 138
Q 4
M 139
TY 82,2048
TY 86,15872
M 128
Q 6
M 129
WL 4
Q 8
M 140
Q 10
M 141
TX 90,3072
TX 94,16896
M 130
Q 12
M 131
R 4
M 142
Q 14
M 143
TY 98,3584
TY 102,17408
M 0
R 6
M 1
WL 10
Q 16
M 12
Q 18
M 13
Q 20
M 2
Q 22
M 3
WL 8
Q 24
M 14
Q 26
M 15
Q 28
M 4
Q 30
M 5
WL 6
Q 32
M 16
Q 34
M 17
Q 36
M 6
Q 38
M 7
WL 2
Q 40
M 18
Q 42
M 19
Q 44
M 8
Q 46
M 9
AD 11,7
AD 12,8
M 20
AD 139,9
AW 135,10
M 21
AW 159,11
AW 183,12
M 10
M 11
R 8
M 22
M 23
R 10
M 24
M 25
M 26
M 27
M 28
M 29
M 30
M 31
M 32
M 33
M 34
M 35
WL 4
M 36
ae 44,68,0
M 37
s_bitcmp1_b32 s50, 2
s_cselect_b32 s51, 24576, 0
v_add_u32 v47, s51, v3
M 38
XL 14,128
M 39
M 40
M 41
M 42
XL 16,896
M 43
WV 7
M 44
WL 0
M 45
s_barrier
R 24
M 46
mw 72
M 47
R 26
M 48
TY 106,5128
TY 110,18952
M 49
ad 84,1296
W 0
M 50
XL 18,1664
M 51
M 52
W 1
M 53
M 54
XL 20,2432
M 55
W 2
M 56
M 57
WL 1
AN 104,106
AN 105,107
M 58
mw 75
SH 106
SH 107
M 59
R 28
M 60
AN 106,106
)"
R"(AN 107,107
M 61
WL 2
AN 108,110
AN 109,111
M 62
SH 110
SH 111
M 63
AN 110,110
AN 111,111
M 64
TY 114,6664
TY 118,20488
M 65
ad 84,1302
M 66
W 0
M 67
M 68
M 69
W 1
M 70
M 71
R 30
M 72
W 2
M 73
WL 3
AN 112,114
AN 113,115
M 74
SH 114
SH 115
M 75
mw 0
AN 114,114
AN 115,115
M 76
WL 2
AN 116,118
AN 117,119
M 77
SH 118
SH 119
M 78
AN 118,118
AN 119,119
M 79
TY 122,8200
TY 126,22024
M 80
ad 84,1344
W 0
M 81
M 82
M 83
R 32
M 84
W 1
M 85
M 86
M 87
W 2
M 88
WL 3
AN 120,122
AN 121,123
M 89
SH 122
SH 123
M 90
mw 3
AN 122,122
AN 123,123
M 91
WL 2
AN 124,126
AN 125,127
M 92
SH 126
SH 127
M 93
AN 126,126
AN 127,127
M 94
ad 84,1350
W 0
M 95
R 34
M 96
UL 6,38
UH 7,38
M 97
W 1
UL 8,39
M 98
UH 9,39
AR 10,136
M 99
AR 11,160
AR 12,184
M 100
W 2
AD 10,6
M 101
AD 11,7
AD 12,8
M 102
AD 140,9
AW 136,10
M 103
bc 49,15,66,60
AW 160,11
M 104
mh 0
AW 184,12
UL 6,40
M 105
ad 66,2880
H 0
M 106
UH 7,40
UL 8,41
M 107
R 48
M 108
DG 1024
M 109
UH 9,41
AR 10,137
M 110
AR 11,161
AR 12,185
M 111
bc 48,15,64,56
AD 10,6
M 112
mh 3
AD 11,7
AD 12,8
M 113
ad 64,2880
H 0
M 114
AD 141,9
AW 137,10
M 115
AW 161,11
AW 185,12
M 116
H 1
UL 6,42
M 117
UH 7,42
UL 8,43
M 118
UH 9,43
AR 10,138
M 119
WL 0
R 50
M 120
H 2
AR 11,162
M 121
AR 12,186
AD 10,6
M 132
AD 11,7
AD 12,8
M 133
PA 12288
TX 58,4608
TX 62,18432
DF
M 122
AD 142,9
AW 138,10
M 123
AW 162,11
AW 186,12
M 134
UL 6,44
UH 7,44
M 135
TY 66,5120
TY 70,18944
M 124
UL 8,45
UH 9,45
M 125
AR 10,139
AR 11,163
M 136
AR 12,187
AD 10,6
M 137
TX 74,6144
TX 78,19968
M 126
WL 4
Q 0
M 127
Q 2
M 138
Q 4
M 139
TY 82,6656
TY 86,20480
M 128
Q 6
M 129
WL 4
Q 8
M 140
Q 10
M 141
TX 90,7680
TX 94,21504
M 130
Q 12
M 131
R 52
M 142
Q 14
M 143
TY 98,8192
TY 102,22016
M 0
R 54
M 1
WL 10
Q 16
M 12
Q 18
M 13
Q 20
M 2
Q 22
M 3
WL 8
Q 24
M 14
Q 26
M 15
Q 28
M 4
Q 30
M 5
WL 6
Q 32
M 16
Q 34
M 17
Q 36
M 6
Q 38
M 7
WL 2
Q 40
M 18
Q 42
M 19
Q 44
M 8
Q 46
M 9
AD 11,7
AD 12,8
M 20
AD 143,9
AW 139,10
M 21
AW 163,11
AW 187,12
M 10
M 11
R 56
M 22
M 23
R 58
M 24
M 25
M 26
M 27
M 28
M 29
M 30
M 31
M 32
M 33
M 34
M 35
WL 4
M 36
M 37
M 38
ae 44,68,0
M 39
s_bfe_u32 s51, s50, 0x10003
v_mad_i32_i24 v47, v49, s51, v48
XL 22,128
M 40
M 41
M 42
M 43
WV 7
M 44
WL 0
XL 24,896
M 45
s_barrier
R 72
M 46
mw 24
M 47
R 74
M 48
TY 106,9736
TY 110,23560
M 49
ad 84,1392
W 0
M 50
XL 26,1664
M 51
M 52
W 1
M 53
M 54
M 55
W 2
M 56
XL 28,2432
M 57
WL 1
AN 104,106
AN 105,107
M 58
mw 27
SH 106
SH 107
M 59
R 76
M 60
AN 106,106
AN 107,107
M 61
WL 2
AN 108,110
AN 109,111
M 62
SH 110
SH 111
M 63
AN 110,110
AN 111,111
M 64
TY 114,11272
TY 118,25096
M 65
ad 84,1398
M 66
W 0
M 67
M 68
M 69
W 1
M 70
M 71
R 78
M 72
W 2
M 73
WL 3
AN 112,114
AN 113,115
M 74
SH 114
SH 115
M 75
mw 48
AN 114,114
AN 115,115
M 76
WL 2
AN 116,118
AN 117,119
M 77
SH 118
SH 119
M 78
AN 118,118
AN 119,119
M 79
TY 122,12808
TY 126,26632
M 80
ad 84,1440
)"
R"(W 0
M 81
M 82
M 83
R 80
M 84
W 1
M 85
M 86
M 87
W 2
M 88
WL 3
AN 120,122
AN 121,123
M 89
SH 122
SH 123
M 90
mw 51
AN 122,122
AN 123,123
M 91
WL 2
AN 124,126
AN 125,127
M 92
SH 126
SH 127
M 93
AN 126,126
AN 127,127
M 94
ad 84,1446
W 0
M 95
R 82
M 96
UL 6,14
UH 7,14
M 97
W 1
UL 8,15
M 98
UH 9,15
AR 10,140
M 99
AR 11,164
AR 12,188
M 100
W 2
AD 10,6
M 101
AD 11,7
AD 12,8
M 102
AD 144,9
AW 140,10
M 103
bc 49,16,66,60
AW 164,11
M 104
mh 9
AW 188,12
UL 6,16
M 105
ad 66,3072
H 0
M 106
UH 7,16
UL 8,17
M 107
R 0
M 108
DG 1024
M 109
UH 9,17
AR 10,141
M 110
AR 11,165
AR 12,189
M 111
bc 48,16,64,56
AD 10,6
M 112
mh 12
AD 11,7
AD 12,8
M 113
ad 64,3072
H 0
M 114
AD 145,9
AW 141,10
M 115
AW 165,11
AW 189,12
M 116
H 1
UL 6,18
M 117
UH 7,18
UL 8,19
M 118
UH 9,19
AR 10,142
M 119
WL 0
R 2
M 120
H 2
AR 11,166
M 121
AR 12,190
AD 10,6
M 132
AD 11,7
AD 12,8
M 133
TX 58,9216
TX 62,23040
M 122
AD 146,9
AW 142,10
M 123
AW 166,11
AW 190,12
M 134
UL 6,20
UH 7,20
M 135
TY 66,9728
TY 70,23552
M 124
UL 8,21
UH 9,21
M 125
AR 10,143
AR 11,167
M 136
AR 12,191
AD 10,6
M 137
TX 74,10752
TX 78,24576
M 126
WL 4
Q 0
M 127
Q 2
M 138
Q 4
M 139
TY 82,11264
TY 86,25088
M 128
Q 6
M 129
WL 4
Q 8
M 140
Q 10
M 141
TX 90,12288
TX 94,26112
M 130
Q 12
M 131
R 4
M 142
Q 14
M 143
TY 98,12800
TY 102,26624
M 0
R 6
M 1
WL 10
Q 16
M 12
Q 18
M 13
Q 20
M 2
Q 22
M 3
WL 8
Q 24
M 14
Q 26
M 15
Q 28
M 4
Q 30
M 5
WL 6
Q 32
M 16
Q 34
M 17
Q 36
M 6
Q 38
M 7
WL 2
Q 40
M 18
Q 42
M 19
Q 44
M 8
Q 46
M 9
AD 11,7
AD 12,8
M 20
AD 147,9
AW 143,10
M 21
AW 167,11
AW 191,12
M 10
M 11
R 8
M 22
M 23
R 10
M 24
M 25
M 26
M 27
M 28
M 29
M 30
M 31
M 32
M 33
M 34
M 35
WL 4
M 36
M 37
M 38
M 39
M 40
ae 44,68,96
M 41
s_bitcmp1_b32 s50, 4
s_cselect_b32 s51, 24576, 0
v_add_u32 v47, s51, v3
M 42
XL 30,128
M 43
WV 6
M 44
WL 0
M 45
s_barrier
R 24
M 46
mw 72
XL 32,896
M 47
R 26
M 48
TY 106,520
TY 110,14344
M 49
ad 84,1488
W 0
M 50
M 51
M 52
W 1
XL 34,1664
M 53
M 54
M 55
W 2
M 56
M 57
WL 1
AN 104,106
AN 105,107
M 58
mw 75
SH 106
SH 107
M 59
R 28
M 60
AN 106,106
AN 107,107
M 61
WL 2
AN 108,110
AN 109,111
M 62
SH 110
SH 111
M 63
AN 110,110
AN 111,111
M 64
TY 114,2056
TY 118,15880
M 65
ad 84,1494
M 66
W 0
XL 36,2432
M 67
M 68
M 69
W 1
M 70
M 71
R 30
M 72
W 2
v_lshlrev_b32 v55, 4, v0
M 73
WL 3
AN 112,114
AN 113,115
M 74
SH 114
SH 115
M 75
mw 0
AN 114,114
AN 115,115
M 76
WL 2
AN 116,118
AN 117,119
M 77
SH 118
SH 119
M 78
AN 118,118
AN 119,119
M 79
TY 122,3592
TY 126,17416
M 80
ad 84,1536
W 0
M 81
FL
M 82
M 83
R 32
M 84
W 1
M 85
M 86
M 87
W 2
M 88
WL 3
AN 120,122
AN 121,123
M 89
SH 122
SH 123
M 90
mw 3
AN 122,122
AN 123,123
M 91
WL 2
AN 124,126
AN 125,127
M 92
SH 126
SH 127
M 93
AN 126,126
AN 127,127
M 94
ad 84,1542
W 0
M 95
R 34
M 96
UL 6,22
UH 7,22
M 97
W 1
UL 8,23
M 98
UH 9,23
AR 10,144
M 99
AR 11,168
AR 12,192
M 100
W 2
AD 10,6
)"
R"(M 101
AD 11,7
AD 12,8
M 102
AD 148,9
AW 144,10
M 103
bc 49,17,66,60
AW 168,11
M 104
mh 18
AW 192,12
UL 6,24
M 105
ad 66,3264
H 0
M 106
UH 7,24
UL 8,25
M 107
R 48
M 108
DG 1024
M 109
UH 9,25
AR 10,145
M 110
AR 11,169
AR 12,193
M 111
bc 48,17,64,56
AD 10,6
M 112
mh 21
AD 11,7
AD 12,8
M 113
ad 64,3264
H 0
M 114
AD 149,9
AW 145,10
M 115
AW 169,11
AW 193,12
M 116
H 1
UL 6,26
M 117
UH 7,26
UL 8,27
M 118
UH 9,27
AR 10,146
M 119
WL 0
R 50
M 120
H 2
AR 11,170
M 121
AR 12,194
AD 10,6
M 132
AD 11,7
AD 12,8
M 133
PA 12288
TX 58,0
TX 62,13824
DF
M 122
AD 150,9
AW 146,10
M 123
AW 170,11
AW 194,12
M 134
UL 6,28
UH 7,28
M 135
TY 66,512
TY 70,14336
M 124
UL 8,29
UH 9,29
M 125
AR 10,147
AR 11,171
M 136
AR 12,195
AD 10,6
M 137
TX 74,1536
TX 78,15360
M 126
WL 4
Q 0
M 127
Q 2
M 138
Q 4
M 139
TY 82,2048
TY 86,15872
M 128
Q 6
M 129
WL 4
Q 8
M 140
Q 10
M 141
TX 90,3072
TX 94,16896
M 130
Q 12
M 131
R 52
M 142
Q 14
M 143
TY 98,3584
TY 102,17408
M 0
R 54
M 1
WL 10
Q 16
M 12
Q 18
M 13
Q 20
M 2
Q 22
M 3
WL 8
Q 24
M 14
Q 26
M 15
Q 28
M 4
Q 30
M 5
WL 6
Q 32
M 16
Q 34
M 17
Q 36
M 6
Q 38
M 7
WL 2
Q 40
M 18
Q 42
M 19
Q 44
M 8
Q 46
M 9
AD 11,7
AD 12,8
M 20
AD 151,9
AW 147,10
M 21
AW 171,11
AW 195,12
M 10
M 11
R 56
M 22
M 23
R 58
M 24
M 25
M 26
M 27
M 28
M 29
M 30
M 31
M 32
M 33
M 34
M 35
WL 4
M 36
M 37
M 38
M 39
M 40
M 41
M 42
ae 44,68,96
M 43
WV 6
M 44
WL 0
M 45
s_barrier
R 72
M 46
mw 24
M 47
R 74
M 48
s_bfe_u32 s51, s50, 0x10005
TY 106,5128
TY 110,18952
M 49
ad 84,1584
W 0
M 50
v_mad_i32_i24 v47, v49, s51, v48
XL 38,128
M 51
XL 40,896
M 52
W 1
M 53
M 54
XL 42,1664
M 55
W 2
M 56
M 57
WL 1
AN 104,106
AN 105,107
M 58
mw 27
SH 106
SH 107
M 59
R 76
M 60
AN 106,106
AN 107,107
M 61
WL 2
AN 108,110
AN 109,111
M 62
SH 110
SH 111
M 63
AN 110,110
AN 111,111
M 64
TY 114,6664
TY 118,20488
M 65
ad 84,1590
M 66
W 0
XL 44,2432
M 67
M 68
M 69
W 1
M 70
M 71
R 78
M 72
W 2
M 73
WL 3
AN 112,114
AN 113,115
M 74
SH 114
SH 115
M 75
mw 48
AN 114,114
AN 115,115
M 76
WL 2
AN 116,118
AN 117,119
M 77
SH 118
SH 119
M 78
AN 118,118
AN 119,119
M 79
TY 122,8200
TY 126,22024
M 80
ad 84,1632
W 0
M 81
M 82
M 83
R 80
M 84
W 1
M 85
M 86
M 87
W 2
M 88
WL 3
AN 120,122
AN 121,123
M 89
SH 122
SH 123
M 90
mw 51
AN 122,122
AN 123,123
M 91
WL 2
AN 124,126
AN 125,127
M 92
SH 126
SH 127
M 93
AN 126,126
AN 127,127
M 94
ad 84,1638
W 0
M 95
R 82
M 96
UL 6,30
UH 7,30
M 97
W 1
UL 8,31
M 98
UH 9,31
AR 10,148
M 99
AR 11,172
AR 12,196
M 100
W 2
AD 10,6
M 101
AD 11,7
AD 12,8
M 102
AD 152,9
AW 148,10
M 103
bc 49,18,66,60
AW 172,11
M 104
mh 0
AW 196,12
UL 6,32
M 105
ad 66,3456
H 0
M 106
UH 7,32
UL 8,33
M 107
R 0
M 108
DG 1024
M 109
UH 9,33
AR 10,149
M 110
AR 11,173
AR 12,197
M 111
bc 48,18,64,56
AD 10,6
M 112
mh 3
AD 11,7
AD 12,8
M 113
ad 64,3456
H 0
M 114
AD 153,9
AW 149,10
M 115
AW 173,11
AW 197,12
M 116
H 1
UL 6,34
M 117
UH 7,34
UL 8,35
M 118
UH 9,35
)"
R"(AR 10,150
M 119
WL 0
R 2
M 120
H 2
AR 11,174
M 121
AR 12,198
AD 10,6
M 132
AD 11,7
AD 12,8
M 133
TX 58,4608
TX 62,18432
M 122
AD 154,9
AW 150,10
M 123
AW 174,11
AW 198,12
M 134
UL 6,36
UH 7,36
M 135
TY 66,5120
TY 70,18944
M 124
UL 8,37
UH 9,37
M 125
AR 10,151
AR 11,175
M 136
AR 12,199
AD 10,6
M 137
TX 74,6144
TX 78,19968
M 126
WL 4
Q 0
M 127
Q 2
M 138
Q 4
M 139
TY 82,6656
TY 86,20480
M 128
Q 6
M 129
WL 4
Q 8
M 140
Q 10
M 141
TX 90,7680
TX 94,21504
M 130
Q 12
M 131
R 4
M 142
Q 14
M 143
TY 98,8192
TY 102,22016
M 0
R 6
M 1
WL 10
Q 16
M 12
Q 18
M 13
Q 20
M 2
Q 22
M 3
WL 8
Q 24
M 14
Q 26
M 15
Q 28
M 4
Q 30
M 5
WL 6
Q 32
M 16
Q 34
M 17
Q 36
M 6
Q 38
M 7
WL 2
Q 40
M 18
Q 42
M 19
Q 44
M 8
Q 46
M 9
AD 11,7
AD 12,8
M 20
AD 155,9
AW 151,10
M 21
AW 175,11
AW 199,12
M 10
M 11
R 8
M 22
M 23
R 10
M 24
M 25
M 26
M 27
M 28
M 29
M 30
M 31
M 32
M 33
M 34
M 35
WL 4
M 36
M 37
M 38
v_cmp_ne_u32 vcc, 2, v54
M 39
M 40
M 41
FC
M 42
M 43
WV 5
M 44
WL 0
M 45
s_barrier
R 24
M 46
mw 72
M 47
R 26
M 48
TY 106,9736
TY 110,23560
M 49
ad 84,1680
W 0
M 50
M 51
M 52
W 1
M 53
M 54
M 55
W 2
M 56
M 57
WL 1
AN 104,106
AN 105,107
M 58
mw 75
SH 106
SH 107
M 59
R 28
M 60
AN 106,106
AN 107,107
M 61
WL 2
AN 108,110
AN 109,111
M 62
SH 110
SH 111
M 63
AN 110,110
AN 111,111
M 64
TY 114,11272
TY 118,25096
M 65
ad 84,1686
M 66
W 0
M 67
M 68
M 69
W 1
M 70
ae 44,70,0
M 71
R 30
M 72
W 2
M 73
WL 3
AN 112,114
AN 113,115
M 74
SH 114
SH 115
M 75
mw 0
AN 114,114
AN 115,115
M 76
WL 2
AN 116,118
AN 117,119
M 77
SH 118
SH 119
M 78
AN 118,118
AN 119,119
M 79
TY 122,12808
TY 126,26632
M 80
ad 84,1728
W 0
M 81
s_bitcmp1_b32 s50, 0
s_cselect_b32 s51, 24576, 0
M 82
v_add_u32 v47, s51, v3
XL 14,256
M 83
R 32
M 84
W 1
XL 16,1024
M 85
XL 18,1792
M 86
M 87
W 2
M 88
WL 3
AN 120,122
AN 121,123
M 89
SH 122
SH 123
M 90
mw 3
AN 122,122
AN 123,123
M 91
WL 2
AN 124,126
AN 125,127
M 92
SH 126
SH 127
M 93
AN 126,126
AN 127,127
M 94
ad 84,1734
W 0
M 95
R 34
M 96
XL 20,2560
M 97
W 1
M 98
UL 6,38
UH 7,38
M 99
UL 8,39
UH 9,39
M 100
W 2
AR 10,152
M 101
AR 11,176
AD 10,6
M 102
AD 11,7
AD 132,8
M 103
bc 49,19,66,60
AD 156,9
M 104
mh 9
AW 152,10
AW 176,11
M 105
ad 66,3648
H 0
M 106
UL 6,40
UH 7,40
M 107
R 48
M 108
DG 1024
M 109
UL 8,41
UH 9,41
M 110
AR 10,153
AR 11,177
M 111
bc 48,19,64,56
AD 10,6
M 112
mh 12
AD 11,7
AD 133,8
M 113
ad 64,3648
H 0
M 114
AD 157,9
AW 153,10
M 115
AW 177,11
UL 6,42
M 116
H 1
UH 7,42
M 117
UL 8,43
UH 9,43
M 118
AR 10,154
AR 11,178
M 119
WL 0
R 50
M 120
H 2
AD 10,6
M 121
AD 11,7
AD 134,8
M 132
AD 158,9
AW 154,10
M 133
PA 12288
TX 58,9216
TX 62,23040
v_min_u32 v52, 752, v0
v_lshrrev_b32 v52, 1, v52
v_add_u32 v53, 0x1800, v52
global_load_dwordx2 a[200:201], v52, s[94:95] offset:-2048 sc1
global_load_dwordx2 a[202:203], v52, s[94:95] offset:-512 sc1
global_load_dwordx2 a[204:205], v52, s[94:95] offset:1024 sc1
global_load_dwordx2 a[206:207], v52, s[94:95] offset:2560 sc1
global_load_dwordx2 a[208:209], v53, s[94:95] offset:-2048 sc1
global_load_dwordx2 a[210:211], v53, s[94:95] offset:-512 sc1
global_load_dwordx2 a[212:213], v53, s[94:95] offset:1024 sc1
global_load_dwordx2 a[214:215], v53, s[94:95] offset:2560 sc1
DF
M 122
AW 178,11
UL 6,44
M 123
UH 7,44
UL 8,45
M 134
UH 9,45
AR 10,155
M 135
TY 66,9728
TY 70,23552
M 124
AR 11,179
AD 10,6
M 125
AD 11,7
AD 135,8
M 136
AD 159,9
AW 155,10
M 137
TX 74,10752
TX 78,24576
M 126
WL 4
)"
R"(Q 0
M 127
Q 2
M 138
Q 4
M 139
TY 82,11264
TY 86,25088
M 128
Q 6
M 129
WL 4
Q 8
M 140
Q 10
M 141
TX 90,12288
TX 94,26112
M 130
Q 12
M 131
R 52
M 142
Q 14
M 143
TY 98,12800
TY 102,26624
M 0
R 54
M 1
WL 10
Q 16
M 12
Q 18
M 13
Q 20
M 2
Q 22
M 3
WL 8
Q 24
M 14
Q 26
M 15
Q 28
M 4
Q 30
M 5
WL 6
Q 32
M 16
Q 34
M 17
Q 36
M 6
Q 38
M 7
WL 2
Q 40
M 18
Q 42
M 19
Q 44
M 8
Q 46
M 9
AW 179,11
M 20
M 21
M 10
M 11
R 56
M 22
M 23
R 58
M 24
M 25
M 26
M 27
M 28
M 29
M 30
M 31
M 32
M 33
M 34
M 35
WL 4
M 36
M 37
M 38
M 39
M 40
M 41
M 42
M 43
WV 14
M 44
WL 0
M 45
s_barrier
R 72
M 46
mw 24
M 47
R 74
M 48
TY 106,520
TY 110,14344
M 49
ad 84,1776
W 0
M 50
M 51
M 52
W 1
M 53
M 54
M 55
W 2
M 56
M 57
WL 1
AN 104,106
AN 105,107
M 58
mw 27
SH 106
SH 107
M 59
R 76
M 60
AN 106,106
AN 107,107
M 61
WL 2
AN 108,110
AN 109,111
M 62
SH 110
SH 111
M 63
AN 110,110
AN 111,111
M 64
TY 114,2056
TY 118,15880
M 65
ad 84,1782
M 66
W 0
M 67
M 68
M 69
W 1
M 70
M 71
R 78
M 72
W 2
M 73
WL 3
AN 112,114
AN 113,115
M 74
SH 114
SH 115
M 75
mw 48
AN 114,114
AN 115,115
M 76
WL 2
AN 116,118
AN 117,119
M 77
SH 118
SH 119
M 78
AN 118,118
AN 119,119
M 79
TY 122,3592
TY 126,17416
M 80
ad 84,1824
W 0
M 81
ae 44,70,0
M 82
s_bfe_u32 s51, s50, 0x10001
v_mad_i32_i24 v47, v49, s51, v48
XL 22,256
M 83
R 80
M 84
W 1
XL 24,1024
M 85
M 86
XL 26,1792
M 87
W 2
M 88
WL 3
AN 120,122
AN 121,123
M 89
SH 122
SH 123
M 90
mw 51
AN 122,122
AN 123,123
M 91
WL 2
AN 124,126
AN 125,127
M 92
SH 126
SH 127
M 93
AN 126,126
AN 127,127
M 94
ad 84,1830
W 0
M 95
R 82
M 96
XL 28,2560
M 97
W 1
M 98
M 99
M 100
W 2
M 101
M 102
M 103
bc 49,20,66,60
M 104
mh 18
M 105
ad 66,3840
H 0
M 106
M 107
R 0
M 108
DG 1024
M 109
M 110
M 111
bc 48,20,64,56
M 112
mh 21
M 113
ad 64,3840
H 0
M 114
M 115
M 116
H 1
M 117
M 118
M 119
WL 2
R 2
M 120
H 2
M 121
M 132
M 133
TX 58,0
TX 62,13824
M 122
M 123
M 134
UL 6,14
UH 7,14
M 135
TY 66,512
TY 70,14336
M 124
UL 8,15
UH 9,15
M 125
AD 160,6
AD 184,7
M 136
AD 208,8
AD 232,9
M 137
TX 74,1536
TX 78,15360
M 126
WL 4
Q 0
M 127
Q 2
M 138
Q 4
M 139
TY 82,2048
TY 86,15872
M 128
Q 6
M 129
WL 4
Q 8
M 140
Q 10
M 141
TX 90,3072
TX 94,16896
M 130
Q 12
M 131
R 4
M 142
Q 14
M 143
TY 98,3584
TY 102,17408
M 0
R 6
M 1
WL 10
Q 16
M 12
Q 18
M 13
Q 20
M 2
Q 22
M 3
WL 8
Q 24
M 14
Q 26
M 15
Q 28
M 4
Q 30
M 5
WL 6
Q 32
M 16
Q 34
M 17
Q 36
M 6
Q 38
M 7
WL 2
Q 40
M 18
Q 42
M 19
Q 44
M 8
Q 46
M 9
UL 6,16
UH 7,16
M 20
UL 8,17
UH 9,17
M 21
AD 161,6
AD 185,7
M 10
AD 209,8
AD 233,9
M 11
R 8
M 22
UL 6,18
UH 7,18
M 23
R 10
M 24
UL 8,19
UH 9,19
M 25
AD 162,6
AD 186,7
M 26
AD 210,8
AD 234,9
M 27
UL 6,20
UH 7,20
M 28
UL 8,21
UH 9,21
M 29
AD 163,6
AD 187,7
)"
R"(M 30
AD 211,8
AD 235,9
M 31
M 32
M 33
M 34
M 35
WL 4
M 36
M 37
M 38
M 39
M 40
M 41
M 42
M 43
WV 5
M 44
WL 0
M 45
s_barrier
R 24
M 46
mw 72
M 47
R 26
M 48
TY 106,5128
TY 110,18952
M 49
ad 84,1872
W 0
M 50
M 51
M 52
W 1
M 53
M 54
M 55
W 2
M 56
M 57
WL 1
AN 104,106
AN 105,107
M 58
mw 75
SH 106
SH 107
M 59
R 28
M 60
AN 106,106
AN 107,107
M 61
WL 2
AN 108,110
AN 109,111
M 62
SH 110
SH 111
M 63
AN 110,110
AN 111,111
M 64
TY 114,6664
TY 118,20488
M 65
ad 84,1878
M 66
W 0
M 67
M 68
M 69
W 1
M 70
M 71
R 30
M 72
W 2
M 73
WL 3
AN 112,114
AN 113,115
M 74
SH 114
SH 115
M 75
mw 0
AN 114,114
AN 115,115
M 76
WL 2
AN 116,118
AN 117,119
M 77
SH 118
SH 119
M 78
AN 118,118
AN 119,119
M 79
TY 122,8200
TY 126,22024
M 80
ad 84,1920
W 0
M 81
ae 44,68,0
M 82
s_bitcmp1_b32 s50, 2
s_cselect_b32 s51, 24576, 0
v_add_u32 v47, s51, v3
M 83
R 32
M 84
W 1
XL 30,256
M 85
XL 32,1024
M 86
M 87
W 2
M 88
WL 3
AN 120,122
AN 121,123
M 89
SH 122
SH 123
M 90
mw 3
AN 122,122
AN 123,123
M 91
WL 2
AN 124,126
AN 125,127
M 92
SH 126
SH 127
M 93
AN 126,126
AN 127,127
M 94
ad 84,1926
W 0
M 95
R 34
M 96
XL 34,1792
M 97
W 1
XL 36,2560
M 98
M 99
M 100
W 2
M 101
M 102
M 103
bc 49,21,66,60
M 104
mh 0
M 105
ad 66,4032
H 0
M 106
M 107
R 48
M 108
DG 1024
M 109
M 110
M 111
bc 48,21,64,56
M 112
mh 3
M 113
ad 64,4032
H 0
M 114
M 115
M 116
H 1
M 117
M 118
M 119
WL 2
R 50
M 120
H 2
M 121
v_mov_b32 v54, s39
v_mul_f32 v54, 0xb5400000, v54
M 132
v_add_u32 v52, %[bm], v52
v_bfe_u32 v53, v52, 6, 2
M 133
PA 12288
TX 58,4608
TX 62,18432
DF
M 122
v_and_b32 v55, 63, v52
v_add_u32 v53, v53, v55
M 123
v_and_b32 v52, 0x700, v52
v_lshl_add_u32 v52, v53, 2, v52
M 134
v_add_u32 v52, %[sb], v52
AR 10,214
M 135
TY 66,5120
TY 70,18944
M 124
AR 11,215
AR 8,208
M 125
AR 9,209
v_add_f32 v10, v10, v8
M 136
v_add_f32 v11, v11, v9
AR 8,210
M 137
TX 74,6144
TX 78,19968
M 126
WL 4
Q 0
M 127
Q 2
M 138
Q 4
M 139
TY 82,6656
TY 86,20480
M 128
Q 6
M 129
WL 4
Q 8
M 140
Q 10
M 141
TX 90,7680
TX 94,21504
M 130
Q 12
M 131
R 52
M 142
Q 14
M 143
TY 98,8192
TY 102,22016
M 0
R 54
M 1
WL 10
Q 16
M 12
Q 18
M 13
Q 20
M 2
Q 22
M 3
WL 8
Q 24
M 14
Q 26
M 15
Q 28
M 4
Q 30
M 5
WL 6
Q 32
M 16
Q 34
M 17
Q 36
M 6
Q 38
M 7
WL 2
Q 40
M 18
Q 42
M 19
Q 44
M 8
Q 46
M 9
AR 9,211
v_add_f32 v10, v10, v8
M 20
v_add_f32 v11, v11, v9
AR 8,212
M 21
AR 9,213
v_add_f32 v10, v10, v8
M 10
v_add_f32 v11, v11, v9
AR 6,206
M 11
R 56
M 22
AR 7,207
v_add_f32 v10, v10, v54
M 23
R 58
M 24
v_add_f32 v11, v11, v54
v_fma_f32 v8, v6, s86, v10
M 25
ds_write_b32 v52, v8 offset:1024
v_fma_f32 v9, v7, s86, v11
M 26
ds_write_b32 v52, v9 offset:1040
AR 12,200
M 27
AR 13,201
v_add_f32 v6, v6, v12
M 28
v_add_f32 v7, v7, v13
v_fma_f32 v12, v6, s86, v10
M 29
ds_write_b32 v52, v12 offset:2560
v_fma_f32 v13, v7, s86, v11
M 30
ds_write_b32 v52, v13 offset:2576
AR 8,202
M 31
AR 9,203
v_add_f32 v6, v6, v8
M 32
v_add_f32 v7, v7, v9
v_fma_f32 v8, v6, s86, v10
M 33
ds_write_b32 v52, v8 offset:4096
v_fma_f32 v9, v7, s86, v11
M 34
ds_write_b32 v52, v9 offset:4112
AR 12,204
M 35
WL 8
AR 13,205
v_add_f32 v6, v6, v12
M 36
v_add_f32 v7, v7, v13
v_fma_f32 v12, v6, s86, v10
M 37
ds_write_b32 v52, v12 offset:5632
v_fma_f32 v13, v7, s86, v11
M 38
ds_write_b32 v52, v13 offset:5648
M 39
UL 6,22
UH 7,22
M 40
UL 8,23
UH 9,23
M 41
AD 164,6
AD 188,7
M 42
AD 212,8
AD 236,9
M 43
WV 6
UL 6,24
UH 7,24
M 44
WL 4
UL 8,25
UH 9,25
M 45
s_barrier
R 72
M 46
mw 24
AD 165,6
AD 189,7
M 47
R 74
M 48
TY 106,9736
TY 110,23560
M 49
ad 84,1968
W 0
M 50
AD 213,8
AD 237,9
M 51
UL 6,26
UH 7,26
)"
R"(M 52
W 1
UL 8,27
M 53
UH 9,27
AD 166,6
M 54
AD 190,7
AD 214,8
M 55
W 2
AD 238,9
M 56
UL 6,28
UH 7,28
M 57
WL 1
AN 104,106
AN 105,107
M 58
mw 27
SH 106
SH 107
M 59
R 76
M 60
AN 106,106
AN 107,107
M 61
WL 2
AN 108,110
AN 109,111
M 62
SH 110
SH 111
M 63
AN 110,110
AN 111,111
M 64
TY 114,11272
TY 118,25096
M 65
ad 84,1974
M 66
W 0
UL 8,29
M 67
UH 9,29
AD 167,6
M 68
AD 191,7
AD 215,8
M 69
W 1
AD 239,9
M 70
M 71
R 78
M 72
W 2
M 73
WL 3
AN 112,114
AN 113,115
M 74
SH 114
SH 115
M 75
mw 48
AN 114,114
AN 115,115
M 76
WL 2
AN 116,118
AN 117,119
M 77
SH 118
SH 119
M 78
AN 118,118
AN 119,119
M 79
TY 122,12808
TY 126,26632
M 80
ad 84,2016
W 0
M 81
M 82
ae 44,68,0
M 83
R 80
M 84
s_bfe_u32 s51, s50, 0x10003
W 1
v_mad_i32_i24 v47, v49, s51, v48
M 85
XL 38,256
M 86
M 87
W 2
M 88
WL 3
AN 120,122
AN 121,123
M 89
SH 122
SH 123
M 90
mw 51
AN 122,122
AN 123,123
M 91
WL 2
AN 124,126
AN 125,127
M 92
SH 126
SH 127
M 93
AN 126,126
AN 127,127
M 94
ad 84,2022
W 0
M 95
R 82
M 96
XL 40,1024
M 97
W 1
XL 42,1792
M 98
M 99
M 100
W 2
XL 44,2560
M 101
M 102
M 103
bc 49,22,66,60
M 104
mh 9
M 105
ad 66,4224
H 0
M 106
M 107
R 0
M 108
DG 1024
M 109
M 110
M 111
bc 48,22,64,56
M 112
mh 12
M 113
ad 64,4224
H 0
M 114
M 115
M 116
H 1
M 117
M 118
M 119
WL 2
R 2
M 120
H 2
M 121
M 132
M 133
TX 58,9216
TX 62,23040
M 122
M 123
M 134
M 135
TY 66,9728
TY 70,23552
M 124
M 125
M 136
M 137
TX 74,10752
TX 78,24576
M 126
WL 4
Q 0
M 127
Q 2
M 138
Q 4
M 139
TY 82,11264
TY 86,25088
M 128
Q 6
M 129
WL 4
Q 8
M 140
Q 10
M 141
TX 90,12288
TX 94,26112
M 130
Q 12
M 131
R 4
M 142
Q 14
M 143
TY 98,12800
TY 102,26624
M 0
R 6
M 1
WL 10
Q 16
M 12
Q 18
M 13
Q 20
M 2
Q 22
M 3
WL 8
Q 24
M 14
Q 26
M 15
Q 28
M 4
Q 30
M 5
WL 6
Q 32
M 16
Q 34
M 17
Q 36
M 6
Q 38
M 7
WL 2
Q 40
M 18
Q 42
M 19
Q 44
M 8
Q 46
M 9
UL 6,30
UH 7,30
M 20
UL 8,31
UH 9,31
M 21
AD 168,6
AD 192,7
M 10
AD 216,8
AD 240,9
M 11
R 8
M 22
UL 6,32
UH 7,32
M 23
R 10
M 24
UL 8,33
UH 9,33
M 25
AD 169,6
AD 193,7
M 26
AD 217,8
AD 241,9
M 27
UL 6,34
UH 7,34
M 28
UL 8,35
UH 9,35
M 29
AD 170,6
AD 194,7
M 30
AD 218,8
AD 242,9
M 31
UL 6,36
UH 7,36
M 32
UL 8,37
UH 9,37
M 33
AD 171,6
AD 195,7
M 34
AD 219,8
AD 243,9
M 35
WL 2
M 36
M 37
M 38
M 39
M 40
M 41
M 42
M 43
WV 6
M 44
WL 0
M 45
s_barrier
R 24
M 46
mw 72
M 47
R 26
M 48
TY 106,520
TY 110,14344
M 49
ad 84,2064
W 0
M 50
M 51
M 52
W 1
M 53
M 54
M 55
W 2
M 56
M 57
WL 1
AN 104,106
AN 105,107
M 58
mw 75
SH 106
SH 107
M 59
R 28
M 60
AN 106,106
AN 107,107
M 61
WL 2
AN 108,110
AN 109,111
M 62
SH 110
SH 111
M 63
AN 110,110
AN 111,111
M 64
TY 114,2056
TY 118,15880
M 65
ad 84,2070
M 66
W 0
M 67
M 68
M 69
W 1
M 70
M 71
R 30
M 72
W 2
M 73
WL 3
AN 112,114
AN 113,115
M 74
SH 114
SH 115
M 75
mw 0
AN 114,114
AN 115,115
M 76
WL 2
AN 116,118
AN 117,119
M 77
SH 118
SH 119
M 78
)"
R"(AN 118,118
AN 119,119
M 79
TY 122,3592
TY 126,17416
M 80
ad 84,2112
W 0
M 81
M 82
M 83
R 32
M 84
W 1
M 85
M 86
ae 44,68,96
M 87
W 2
M 88
WL 3
AN 120,122
AN 121,123
M 89
SH 122
SH 123
M 90
mw 3
AN 122,122
AN 123,123
M 91
WL 2
AN 124,126
AN 125,127
M 92
SH 126
SH 127
M 93
AN 126,126
AN 127,127
M 94
ad 84,2118
W 0
M 95
R 34
M 96
s_bitcmp1_b32 s50, 4
s_cselect_b32 s51, 24576, 0
M 97
W 1
v_add_u32 v47, s51, v3
M 98
XL 14,256
M 99
XL 16,1024
M 100
W 2
XL 18,1792
M 101
M 102
M 103
bc 49,23,66,60
M 104
mh 18
XL 20,2560
M 105
ad 66,4416
H 0
M 106
M 107
R 48
M 108
DG 1024
M 109
M 110
M 111
bc 48,23,64,56
M 112
mh 21
M 113
ad 64,4416
H 0
M 114
M 115
M 116
H 1
M 117
M 118
M 119
WL 2
R 50
M 120
H 2
M 121
M 132
M 133
PA 12288
TX 58,0
TX 62,13824
DF
M 122
M 123
M 134
M 135
TY 66,512
TY 70,14336
M 124
M 125
M 136
M 137
TX 74,1536
TX 78,15360
M 126
WL 4
Q 0
M 127
Q 2
M 138
Q 4
M 139
TY 82,2048
TY 86,15872
M 128
Q 6
M 129
WL 4
Q 8
M 140
Q 10
M 141
TX 90,3072
TX 94,16896
M 130
Q 12
M 131
R 52
M 142
Q 14
M 143
TY 98,3584
TY 102,17408
M 0
R 54
M 1
WL 10
Q 16
M 12
Q 18
M 13
Q 20
M 2
Q 22
M 3
WL 8
Q 24
M 14
Q 26
M 15
Q 28
M 4
Q 30
M 5
WL 6
Q 32
M 16
Q 34
M 17
Q 36
M 6
Q 38
M 7
WL 2
Q 40
M 18
Q 42
M 19
Q 44
M 8
Q 46
M 9
UL 6,38
UH 7,38
M 20
UL 8,39
UH 9,39
M 21
AD 172,6
AD 196,7
M 10
AD 220,8
AD 244,9
M 11
R 56
M 22
UL 6,40
UH 7,40
M 23
R 58
M 24
UL 8,41
UH 9,41
M 25
AD 173,6
AD 197,7
M 26
AD 221,8
AD 245,9
M 27
UL 6,42
UH 7,42
M 28
UL 8,43
UH 9,43
M 29
AD 174,6
AD 198,7
M 30
AD 222,8
AD 246,9
M 31
WV 27
UL 6,44
UH 7,44
M 32
UL 8,45
UH 9,45
M 33
AD 175,6
AD 199,7
M 34
AD 223,8
AD 247,9
M 35
WL 2
M 36
M 37
M 38
M 39
M 40
M 41
M 42
M 43
WV 8
M 44
WL 0
M 45
s_barrier
R 72
M 46
mw 24
M 47
R 74
M 48
TY 106,5128
TY 110,18952
M 49
ad 84,2160
W 0
M 50
M 51
M 52
W 1
M 53
M 54
M 55
W 2
M 56
M 57
WL 1
AN 104,106
AN 105,107
M 58
mw 27
SH 106
SH 107
M 59
R 76
M 60
AN 106,106
AN 107,107
M 61
WL 2
AN 108,110
AN 109,111
M 62
SH 110
SH 111
M 63
AN 110,110
AN 111,111
M 64
TY 114,6664
TY 118,20488
M 65
ad 84,2166
M 66
W 0
M 67
M 68
M 69
W 1
M 70
M 71
R 78
M 72
W 2
M 73
WL 3
AN 112,114
AN 113,115
M 74
SH 114
SH 115
M 75
mw 48
AN 114,114
AN 115,115
M 76
WL 2
AN 116,118
AN 117,119
M 77
SH 118
SH 119
M 78
AN 118,118
AN 119,119
M 79
TY 122,8200
TY 126,22024
M 80
ad 84,2208
W 0
M 81
M 82
M 83
R 80
M 84
W 1
M 85
M 86
M 87
W 2
M 88
WL 3
AN 120,122
AN 121,123
M 89
SH 122
SH 123
M 90
mw 51
AN 122,122
AN 123,123
M 91
WL 2
AN 124,126
AN 125,127
M 92
SH 126
SH 127
M 93
AN 126,126
AN 127,127
M 94
ad 84,2214
W 0
M 95
R 82
M 96
ae 44,68,96
M 97
s_bfe_u32 s51, s50, 0x10005
W 1
v_mad_i32_i24 v47, v49, s51, v48
M 98
XL 22,256
M 99
XL 24,1024
M 100
W 2
M 101
M 102
XL 26,1792
M 103
bc 49,0,66,60
M 104
mh 0
M 105
ad 66,32
H 0
M 106
M 107
R 0
M 108
DG 1024
M 109
XL 28,2560
M 110
M 111
)"
R"(bc 48,0,64,56
M 112
mh 3
M 113
ad 64,0
H 0
M 114
M 115
M 116
H 1
M 117
M 118
M 119
WL 2
R 2
M 120
H 2
M 121
M 132
M 133
TX 58,4608
TX 62,18432
M 122
M 123
M 134
M 135
TY 66,5120
TY 70,18944
M 124
M 125
M 136
M 137
TX 74,6144
TX 78,19968
M 126
WL 4
Q 0
M 127
Q 2
M 138
Q 4
M 139
TY 82,6656
TY 86,20480
M 128
Q 6
M 129
WL 4
Q 8
M 140
Q 10
M 141
TX 90,7680
TX 94,21504
M 130
Q 12
M 131
R 4
M 142
Q 14
M 143
TY 98,8192
TY 102,22016
M 0
R 6
M 1
WL 10
Q 16
M 12
Q 18
M 13
Q 20
M 2
Q 22
M 3
WL 8
Q 24
M 14
Q 26
M 15
Q 28
M 4
Q 30
M 5
WL 6
Q 32
M 16
Q 34
M 17
Q 36
M 6
Q 38
M 7
WL 2
Q 40
M 18
Q 42
M 19
Q 44
M 8
Q 46
M 9
UL 6,14
UH 7,14
M 20
UL 8,15
UH 9,15
M 21
AD 176,6
AD 200,7
M 10
AD 224,8
AD 248,9
M 11
R 8
M 22
UL 6,16
UH 7,16
M 23
R 10
M 24
UL 8,17
UH 9,17
M 25
AD 177,6
AD 201,7
M 26
AD 225,8
AD 249,9
M 27
WV 27
UL 6,18
UH 7,18
M 28
UL 8,19
UH 9,19
M 29
AD 178,6
AD 202,7
M 30
AD 226,8
AD 250,9
M 31
UL 6,20
UH 7,20
M 32
UL 8,21
UH 9,21
M 33
AD 179,6
AD 203,7
M 34
AD 227,8
AD 251,9
M 35
WL 2
M 36
M 37
M 38
M 39
M 40
M 41
M 42
M 43
WV 7
M 44
WL 0
M 45
s_barrier
R 24
M 46
mw 72
M 47
R 26
M 48
TY 106,9736
TY 110,23560
M 49
ad 84,2256
W 0
M 50
M 51
M 52
W 1
M 53
M 54
M 55
W 2
M 56
M 57
WL 1
AN 104,106
AN 105,107
M 58
mw 75
SH 106
SH 107
M 59
R 28
M 60
AN 106,106
AN 107,107
M 61
WL 2
AN 108,110
AN 109,111
M 62
SH 110
SH 111
M 63
AN 110,110
AN 111,111
M 64
TY 114,11272
TY 118,25096
M 65
ad 84,2262
M 66
W 0
M 67
M 68
M 69
W 1
M 70
M 71
R 30
M 72
W 2
M 73
WL 3
AN 112,114
AN 113,115
M 74
SH 114
SH 115
M 75
mw 0
AN 114,114
AN 115,115
M 76
WL 2
AN 116,118
AN 117,119
M 77
SH 118
SH 119
M 78
AN 118,118
AN 119,119
M 79
TY 122,12808
TY 126,26632
M 80
ad 84,0
W 0
M 81
M 82
M 83
R 32
M 84
W 1
M 85
M 86
M 87
W 2
M 88
WL 3
AN 120,122
AN 121,123
M 89
SH 122
SH 123
M 90
mw 3
AN 122,122
AN 123,123
M 91
WL 2
AN 124,126
AN 125,127
M 92
SH 126
SH 127
M 93
AN 126,126
AN 127,127
M 94
ad 84,6
W 0
M 95
R 34
M 96
M 97
W 1
M 98
M 99
M 100
W 2
M 101
M 102
M 103
bc 49,1,66,60
M 104
mh 9
M 105
ad 66,224
H 0
M 106
M 107
R 48
M 108
DG 1024
M 109
M 110
M 111
bc 48,1,64,56
M 112
mh 12
M 113
ad 64,224
H 0
M 114
M 115
M 116
DG 1024
M 117
M 118
M 119
WL 2
R 50
M 120
mh 15
M 121
ad 64,195
H 0
M 132
M 133
TX 58,9216
TX 62,23040
M 122
DG 1024
M 123
M 134
M 135
PA 12288
TY 66,9728
TY 70,23552
DF
M 124
M 125
M 136
M 137
TX 74,10752
TX 78,24576
M 126
WL 4
Q 0
M 127
Q 2
M 138
Q 4
M 139
TY 82,11264
TY 86,25088
M 128
Q 6
M 129
WL 4
Q 8
M 140
Q 10
M 141
TX 90,12288
TX 94,26112
M 130
Q 12
M 131
R 52
M 142
Q 14
M 143
TY 98,12800
TY 102,26624
M 0
R 54
M 1
WL 10
Q 16
M 12
Q 18
M 13
Q 20
M 2
Q 22
M 3
WL 8
Q 24
M 14
Q 26
M 15
)"
R"(Q 28
M 4
Q 30
M 5
WL 6
Q 32
M 16
Q 34
M 17
Q 36
M 6
Q 38
M 7
WL 2
Q 40
M 18
Q 42
M 19
Q 44
M 8
Q 46
M 9
UL 6,22
UH 7,22
M 20
UL 8,23
UH 9,23
M 21
AD 180,6
AD 204,7
M 10
AD 228,8
AD 252,9
M 11
R 56
M 22
UL 6,24
UH 7,24
M 23
R 58
M 24
UL 8,25
UH 9,25
M 25
AD 181,6
AD 205,7
M 26
AD 229,8
AD 253,9
BV 52,0
M 27
WV 25
UL 6,26
UH 7,26
M 28
UL 8,27
UH 9,27
M 29
AD 182,6
AD 206,7
M 30
AD 230,8
AD 254,9
M 31
WV 22
UL 6,28
UH 7,28
M 32
UL 8,29
UH 9,29
M 33
AD 183,6
AD 207,7
M 34
AD 231,8
AD 255,9
M 35
WL 3
M 36
M 37
M 38
M 39
M 40
ds_read_b64 v[12:13], v4 offset:256
ds_read_b64 v[46:47], v4 offset:264
M 41
AR 6,36
AR 7,60
M 42
AR 8,84
AR 9,108
M 43
WL 2
EF 6,52
EF 7,53
M 44
WV 7
EF 8,54
EF 9,55
M 45
s_barrier
R 72
M 46
mw 24
CV 30,6,7
CV 31,8,9
M 47
R 74
M 48
TY 106,520
TY 110,14344
M 49
ad 84,48
W 0
M 50
AR 6,37
AR 7,61
M 51
AR 8,85
AR 9,109
M 52
W 1
EF 6,52
M 53
EF 7,53
EF 8,54
M 54
EF 9,55
CV 32,6,7
M 55
W 2
CV 33,8,9
M 56
AR 6,38
AR 7,62
M 57
WL 1
AN 104,106
AN 105,107
M 58
mw 27
SH 106
SH 107
M 59
R 76
M 60
AN 106,106
AN 107,107
M 61
WL 2
AN 108,110
AN 109,111
M 62
SH 110
SH 111
M 63
AN 110,110
AN 111,111
M 64
TY 114,2056
TY 118,15880
M 65
ad 84,54
M 66
W 0
AR 8,86
M 67
AR 9,110
EF 6,52
M 68
EF 7,53
EF 8,54
M 69
W 1
EF 9,55
M 70
CV 34,6,7
CV 35,8,9
M 71
R 78
M 72
W 2
AR 6,39
M 73
WL 3
AN 112,114
AN 113,115
M 74
SH 114
SH 115
M 75
mw 48
AN 114,114
AN 115,115
M 76
WL 2
AN 116,118
AN 117,119
M 77
SH 118
SH 119
M 78
AN 118,118
AN 119,119
M 79
TY 122,3592
TY 126,17416
M 80
ad 84,96
W 0
M 81
AR 7,63
AR 8,87
M 82
AR 9,111
EF 6,52
M 83
R 80
M 84
W 1
EF 7,53
M 85
EF 8,54
EF 9,55
M 86
CV 36,6,7
CV 37,8,9
M 87
W 2
ae 42,74,0
M 88
WL 3
AN 120,122
AN 121,123
M 89
SH 122
SH 123
M 90
mw 51
AN 122,122
AN 123,123
M 91
WL 2
AN 124,126
AN 125,127
M 92
SH 126
SH 127
M 93
AN 126,126
AN 127,127
M 94
ad 84,102
W 0
M 95
R 82
M 96
s_bitcmp1_b32 s50, 0
s_cselect_b32 s52, 24576, 0
v_add_u32 v50, s52, v3
M 97
W 1
ST 30,0
M 98
AR 6,40
AR 7,64
M 99
AR 8,88
AR 9,112
M 100
W 2
EF 6,52
M 101
EF 7,53
EF 8,54
M 102
EF 9,55
CV 38,6,7
M 103
bc 49,2,66,60
CV 39,8,9
M 104
mh 18
AR 6,41
AR 7,65
M 105
ad 66,416
H 0
M 106
AR 8,89
AR 9,113
M 107
R 0
M 108
DG 1024
M 109
ST 32,768
EF 6,52
M 110
EF 7,53
EF 8,54
M 111
bc 48,2,64,56
EF 9,55
M 112
mh 21
CV 40,6,7
CV 41,8,9
M 113
ad 64,416
H 0
M 114
AR 6,42
AR 7,66
M 115
AR 8,90
AR 9,114
M 116
H 1
EF 6,52
M 117
EF 7,53
EF 8,54
M 118
EF 9,55
CV 42,6,7
M 119
WL 0
R 2
M 120
H 2
CV 43,8,9
M 121
ST 34,1536
AR 6,43
M 132
AR 7,67
AR 8,91
M 133
TX 58,0
TX 62,13824
M 122
AR 9,115
EF 6,52
M 123
EF 7,53
EF 8,54
M 134
EF 9,55
CV 44,6,7
M 135
TY 66,512
TY 70,14336
M 124
CV 45,8,9
M 125
BV 52,512
AR 6,132
M 136
AR 7,156
AR 8,180
M 137
TX 74,1536
TX 78,15360
M 126
WL 5
Q 0
M 127
Q 2
ST 36,2304
M 138
Q 4
M 139
TY 82,2048
TY 86,15872
M 128
Q 6
M 129
WL 5
Q 8
M 140
Q 10
M 141
TX 90,3072
TX 94,16896
M 130
Q 12
M 131
R 4
M 142
Q 14
M 143
TY 98,3584
TY 102,17408
v_mfma_f32_16x16x32_f16 a[36:39], v[56:59], a[0:3], a[32:35]
R 6
M 1
WL 10
Q 16
v_mfma_f32_16x16x32_f16 a[60:63], v[56:59], a[8:11], a[32:35]
)"
R"(Q 18
M 13
Q 20
v_mfma_f32_16x16x32_f16 a[40:43], v[64:67], a[0:3], a[32:35]
Q 22
M 3
WL 8
Q 24
v_mfma_f32_16x16x32_f16 a[64:67], v[64:67], a[8:11], a[32:35]
Q 26
M 15
Q 28
M 4
Q 30
M 5
WL 6
Q 32
M 16
Q 34
M 17
Q 36
M 6
Q 38
M 7
WL 2
Q 40
M 18
Q 42
M 19
Q 44
M 8
Q 46
M 9
ae 42,74,0
M 20
s_bfe_u32 s52, s50, 0x10001
v_mad_i32_i24 v50, v49, s52, v48
ST 38,0
M 21
EF 6,12
EF 7,13
M 10
EF 8,46
EF 136,47
M 11
R 8
M 22
CV 14,6,7
CV 15,8,136
M 23
R 10
v_mfma_f32_16x16x32_f16 a[84:87], v[56:59], a[16:19], a[32:35]
AR 6,133
AR 7,157
M 25
AR 8,181
EF 6,12
v_mfma_f32_16x16x32_f16 a[88:91], v[64:67], a[16:19], a[32:35]
EF 7,13
EF 8,46
M 27
EF 137,47
CV 16,6,7
M 28
CV 17,8,137
AR 6,134
M 29
AR 7,158
AR 8,182
M 30
ST 40,768
EF 6,12
M 31
EF 7,13
EF 8,46
M 32
EF 138,47
CV 18,6,7
M 33
CV 19,8,138
AR 6,135
M 34
AR 7,159
AR 8,183
M 35
WL 2
EF 6,12
EF 7,13
v_mfma_f32_16x16x32_f16 a[108:111], v[56:59], a[24:27], a[32:35]
EF 8,46
EF 139,47
M 37
CV 20,6,7
CV 21,8,139
v_mfma_f32_16x16x32_f16 a[112:115], v[64:67], a[24:27], a[32:35]
AR 6,136
AR 7,160
AR 8,184
EF 6,12
M 39
EF 7,13
EF 8,46
EF 140,47
CV 22,6,7
M 40
CV 23,8,140
AR 6,137
AR 7,161
AR 8,185
M 41
EF 6,12
EF 7,13
EF 8,46
EF 141,47
M 42
ST 42,1536
CV 24,6,7
CV 25,8,141
AR 6,138
M 43
WV 11
AR 7,162
AR 8,186
EF 6,12
EF 7,13
M 44
WL 0
EF 8,46
EF 142,47
CV 26,6,7
CV 27,8,142
M 45
s_barrier
R 24
AR 6,139
AR 7,163
M 46
mw 72
AR 8,187
EF 6,12
EF 7,13
EF 8,46
M 47
R 26
EF 143,47
CV 28,6,7
v_mfma_f32_16x16x32_f16 a[132:135], v[56:59], a[0:3], a[32:35]
TY 106,5128
TY 110,18952
CV 29,8,143
M 49
ad 84,144
W 0
ds_read_b64 v[12:13], v4 offset:1536
v_mfma_f32_16x16x32_f16 a[136:139], v[64:67], a[0:3], a[32:35]
ds_read_b64 v[46:47], v4 offset:1544
EF 160,52
EF 184,53
M 51
EF 208,54
EF 232,55
CV 30,160,184
M 52
W 1
CV 31,208,232
EF 161,52
M 53
EF 185,53
EF 209,54
EF 233,55
M 54
ST 44,2304
CV 32,161,185
CV 33,209,233
M 55
W 2
EF 162,52
EF 186,53
M 56
EF 210,54
EF 234,55
CV 34,162,186
M 57
WL 3
AN 104,106
AN 105,107
CV 35,210,234
M 58
mw 75
SH 106
SH 107
EF 163,52
M 59
R 28
EF 187,53
v_mfma_f32_16x16x32_f16 a[156:159], v[56:59], a[8:11], a[32:35]
AN 106,106
AN 107,107
EF 211,54
M 61
WL 2
AN 108,110
AN 109,111
EF 235,55
v_mfma_f32_16x16x32_f16 a[160:163], v[64:67], a[8:11], a[32:35]
SH 110
SH 111
CV 36,163,187
M 63
AN 110,110
AN 111,111
CV 37,211,235
M 64
TY 114,6664
TY 118,20488
EF 164,52
M 65
ad 84,150
EF 188,53
M 66
W 0
EF 212,54
EF 236,55
M 67
ae 42,74,0
CV 38,164,188
CV 39,212,236
M 68
s_bitcmp1_b32 s50, 0
s_cselect_b32 s52, 24576, 0
v_add_u32 v50, s52, v3
ST 14,128
M 69
W 1
EF 165,52
EF 189,53
M 70
EF 213,54
EF 237,55
CV 40,165,189
M 71
R 30
CV 41,213,237
v_mfma_f32_16x16x32_f16 a[180:183], v[56:59], a[16:19], a[32:35]
W 2
EF 166,52
EF 190,53
M 73
WL 3
AN 112,114
AN 113,115
EF 214,54
v_mfma_f32_16x16x32_f16 a[184:187], v[64:67], a[16:19], a[32:35]
SH 114
SH 115
EF 238,55
M 75
mw 0
AN 114,114
AN 115,115
CV 42,166,190
M 76
WL 2
AN 116,118
AN 117,119
CV 43,214,238
M 77
SH 118
SH 119
EF 167,52
M 78
AN 118,118
AN 119,119
EF 191,53
M 79
TY 122,8200
TY 126,22024
EF 215,54
M 80
ad 84,192
W 0
ST 16,896
M 81
EF 239,55
CV 44,167,191
CV 45,215,239
M 82
BV 52,1792
AR 6,44
M 83
R 32
v_mfma_f32_16x16x32_f16 v[136:139], v[56:59], a[24:27], v[128:131]
W 1
AR 7,68
M 85
AR 8,92
AR 9,116
v_mfma_f32_16x16x32_f16 v[140:143], v[64:67], a[24:27], v[128:131]
EF 6,12
EF 7,13
M 87
W 2
EF 8,46
M 88
WL 4
AN 120,122
AN 121,123
M 89
SH 122
SH 123
M 90
mw 3
AN 122,122
AN 123,123
M 91
WL 3
AN 124,126
AN 125,127
M 92
SH 126
SH 127
ST 18,1664
M 93
AN 126,126
AN 127,127
M 94
ad 84,198
W 0
M 95
R 34
v_mfma_f32_16x16x32_f16 v[160:163], v[56:59], a[0:3], v[128:131]
EF 9,47
CV 10,6,7
M 97
W 1
CV 11,8,9
v_mfma_f32_16x16x32_f16 v[164:167], v[64:67], a[0:3], v[128:131]
AW 200,10
AW 201,11
M 99
AR 6,45
AR 7,69
M 100
W 2
AR 8,93
M 101
AR 9,117
EF 6,12
M 102
EF 7,13
EF 8,46
M 103
bc 49,3,66,60
EF 9,47
M 104
mh 0
ST 20,2432
CV 10,6,7
M 105
ad 66,608
H 0
M 106
CV 11,8,9
AW 202,10
M 107
R 48
v_mfma_f32_16x16x32_f16 v[184:187], v[56:59], a[8:11], v[128:131]
DG 1024
M 109
AW 203,11
AR 6,46
v_mfma_f32_16x16x32_f16 v[188:191], v[64:67], a[8:11], v[128:131]
AR 7,70
AR 8,94
M 111
bc 48,3,64,56
AR 9,118
M 112
mh 3
EF 6,12
EF 7,13
M 113
ad 64,608
H 0
M 114
EF 8,46
EF 9,47
M 115
CV 10,6,7
CV 11,8,9
M 116
H 1
AW 204,10
M 117
ae 42,74,0
AW 205,11
M 118
s_bfe_u32 s52, s50, 0x10001
v_mad_i32_i24 v50, v49, s52, v48
ST 22,128
)"
R"(M 119
WL 0
R 50
v_mfma_f32_16x16x32_f16 v[208:211], v[56:59], a[16:19], v[128:131]
H 2
AR 6,47
M 121
AR 7,71
AR 8,95
v_mfma_f32_16x16x32_f16 v[232:235], v[56:59], a[24:27], v[128:131]
AR 9,119
EF 6,12
M 133
PA 12288
TX 58,4608
TX 62,18432
DF
v_mfma_f32_16x16x32_f16 v[212:215], v[64:67], a[16:19], v[128:131]
EF 7,13
EF 8,46
M 123
EF 9,47
CV 10,6,7
v_mfma_f32_16x16x32_f16 v[236:239], v[64:67], a[24:27], v[128:131]
CV 11,8,9
AW 206,10
M 135
TY 66,5120
TY 70,18944
M 124
AW 207,11
M 125
AR 6,48
AR 7,72
AR 8,96
AR 9,120
EF 6,12
EF 7,13
M 136
ST 24,896
EF 8,46
EF 9,47
CV 14,6,7
CV 15,8,9
AR 6,49
M 137
TX 74,6144
TX 78,19968
AR 7,73
AR 8,97
AR 9,121
EF 6,12
M 126
WL 4
Q 0
EF 7,13
EF 8,46
EF 9,47
CV 16,6,7
M 127
Q 2
CV 17,8,9
AR 6,50
AR 7,74
AR 8,98
M 138
Q 4
AR 9,122
EF 6,12
EF 7,13
EF 8,46
M 139
TY 82,6656
TY 86,20480
EF 9,47
CV 18,6,7
CV 19,8,9
AR 6,51
M 128
Q 6
AR 7,75
AR 8,99
AR 9,123
EF 6,12
M 129
WL 2
Q 8
EF 7,13
EF 8,46
EF 9,47
CV 20,6,7
M 140
Q 10
CV 21,8,9
M 141
TX 90,7680
TX 94,21504
ds_read_b64 v[12:13], v4 offset:2048
M 130
Q 12
ds_read_b64 v[46:47], v4 offset:2056
M 131
R 52
M 142
Q 14
ST 26,1664
M 143
TY 98,8192
TY 102,22016
M 0
R 54
M 1
Q 16
M 12
Q 18
M 13
Q 20
M 2
Q 22
M 3
WL 10
Q 24
M 14
Q 26
M 15
Q 28
v_mfma_f32_16x16x32_f16 a[44:47], v[72:75], a[0:3], a[32:35]
Q 30
M 5
WL 6
Q 32
v_mfma_f32_16x16x32_f16 a[68:71], v[72:75], a[8:11], a[32:35]
Q 34
ST 28,2432
M 17
Q 36
v_mfma_f32_16x16x32_f16 a[48:51], v[80:83], a[0:3], a[32:35]
Q 38
M 7
WL 1
Q 40
v_mfma_f32_16x16x32_f16 a[72:75], v[80:83], a[8:11], a[32:35]
Q 42
M 19
Q 44
M 8
Q 46
M 9
AR 6,140
AR 7,164
M 20
AR 8,188
EF 6,52
M 21
EF 7,53
EF 8,54
M 10
EF 144,55
CV 10,6,7
M 11
R 56
M 22
ae 42,74,0
CV 11,8,144
M 23
R 58
M 24
s_bitcmp1_b32 s50, 0
s_cselect_b32 s52, 24576, 0
v_add_u32 v50, s52, v3
ST 30,256
M 25
AW 208,10
AW 209,11
M 26
AR 6,141
AR 7,165
M 27
AR 8,189
EF 6,52
v_mfma_f32_16x16x32_f16 a[92:95], v[72:75], a[16:19], a[32:35]
EF 7,53
EF 8,54
M 29
EF 145,55
CV 10,6,7
v_mfma_f32_16x16x32_f16 a[96:99], v[80:83], a[16:19], a[32:35]
CV 11,8,145
AW 210,10
M 31
AW 211,11
AR 6,142
M 32
AR 7,166
AR 8,190
M 33
EF 6,52
EF 7,53
M 34
EF 8,54
EF 146,55
M 35
WL 0
CV 10,6,7
CV 11,8,146
M 36
ST 32,1024
AW 212,10
M 37
AW 213,11
AR 6,143
M 38
AR 7,167
AR 8,191
M 39
EF 6,52
EF 7,53
v_mfma_f32_16x16x32_f16 a[116:119], v[72:75], a[24:27], a[32:35]
EF 8,54
EF 147,55
M 41
CV 10,6,7
CV 11,8,147
v_mfma_f32_16x16x32_f16 a[120:123], v[80:83], a[24:27], a[32:35]
AW 214,10
AW 215,11
M 43
AR 6,144
AR 7,168
AR 8,192
EF 6,52
EF 7,53
EF 8,54
M 44
WV 13
EF 148,55
CV 22,6,7
CV 23,8,148
AR 6,145
AR 7,169
AR 8,193
M 45
s_barrier
R 72
EF 6,52
EF 7,53
EF 8,54
EF 149,55
M 46
mw 24
CV 24,6,7
CV 25,8,149
AR 6,146
AR 7,170
AR 8,194
EF 6,52
M 47
R 74
EF 7,53
EF 8,54
EF 150,55
CV 26,6,7
M 48
TY 106,9736
TY 110,23560
ST 34,1792
CV 27,8,150
AR 6,147
AR 7,171
M 49
ad 84,240
W 0
AR 8,195
EF 6,52
EF 7,53
EF 8,54
M 50
EF 151,55
CV 28,6,7
CV 29,8,151
M 51
BV 52,3072
EF 168,12
EF 192,13
v_mfma_f32_16x16x32_f16 a[140:143], v[72:75], a[0:3], a[32:35]
W 1
EF 216,46
EF 240,47
M 53
CV 10,168,192
CV 11,216,240
AW 216,10
v_mfma_f32_16x16x32_f16 a[144:147], v[80:83], a[0:3], a[32:35]
AW 217,11
EF 169,12
EF 193,13
M 55
W 2
EF 217,46
EF 241,47
M 56
CV 10,169,193
CV 11,217,241
AW 218,10
M 57
WL 1
AN 104,106
AN 105,107
AW 219,11
M 58
mw 27
SH 106
SH 107
EF 170,12
M 59
R 76
EF 194,13
M 60
AN 106,106
AN 107,107
ST 36,2560
M 61
AN 108,110
AN 109,111
EF 218,46
M 62
SH 110
SH 111
EF 242,47
M 63
AN 110,110
AN 111,111
CV 10,170,194
v_mfma_f32_16x16x32_f16 a[164:167], v[72:75], a[8:11], a[32:35]
TY 114,11272
TY 118,25096
CV 11,218,242
M 65
ad 84,246
AW 220,10
v_mfma_f32_16x16x32_f16 a[168:171], v[80:83], a[8:11], a[32:35]
W 0
AW 221,11
EF 171,12
M 67
EF 195,13
EF 219,46
EF 243,47
M 68
CV 10,171,195
CV 11,219,243
AW 222,10
M 69
W 1
AW 223,11
M 70
EF 172,12
EF 196,13
EF 220,46
M 71
R 78
EF 244,47
M 72
WL 3
W 2
ae 42,74,0
M 73
s_bfe_u32 s52, s50, 0x10001
AN 112,114
AN 113,115
v_mad_i32_i24 v50, v49, s52, v48
M 74
SH 114
SH 115
ST 38,256
M 75
mw 48
AN 114,114
AN 115,115
CV 30,172,196
v_mfma_f32_16x16x32_f16 a[188:191], v[72:75], a[16:19], a[32:35]
WL 2
AN 116,118
AN 117,119
CV 31,220,244
M 77
SH 118
SH 119
EF 173,12
v_mfma_f32_16x16x32_f16 a[192:195], v[80:83], a[16:19], a[32:35]
AN 118,118
AN 119,119
EF 197,13
M 79
TY 122,12808
TY 126,26632
EF 221,46
M 80
ad 84,288
W 0
)"
R"(EF 245,47
M 81
CV 32,173,197
CV 33,221,245
EF 174,12
M 82
EF 198,13
EF 222,46
EF 246,47
M 83
R 80
CV 34,174,198
M 84
W 1
CV 35,222,246
EF 175,12
M 85
EF 199,13
EF 223,46
EF 247,47
M 86
ST 40,1024
CV 36,175,199
CV 37,223,247
M 87
W 2
ds_read_b64 v[12:13], v4 offset:3328
ds_read_b64 v[46:47], v4 offset:3336
v_mfma_f32_16x16x32_f16 v[144:147], v[72:75], a[24:27], v[128:131]
WL 5
AN 120,122
AN 121,123
AR 6,52
M 89
SH 122
SH 123
AR 7,76
v_mfma_f32_16x16x32_f16 v[148:151], v[80:83], a[24:27], v[128:131]
mw 51
AN 122,122
AN 123,123
AR 8,100
M 91
WL 4
AN 124,126
AN 125,127
AR 9,124
M 92
SH 126
SH 127
EF 6,52
M 93
AN 126,126
AN 127,127
EF 7,53
M 94
ad 84,294
W 0
EF 8,54
M 95
R 82
EF 9,55
M 96
CV 10,6,7
CV 11,8,9
AW 224,10
M 97
W 1
AW 225,11
AR 6,53
M 98
ST 42,1792
AR 7,77
AR 8,101
M 99
AR 9,125
EF 6,52
EF 7,53
v_mfma_f32_16x16x32_f16 v[168:171], v[72:75], a[0:3], v[128:131]
W 2
EF 8,54
EF 9,55
M 101
CV 10,6,7
CV 11,8,9
AW 226,10
v_mfma_f32_16x16x32_f16 v[172:175], v[80:83], a[0:3], v[128:131]
AW 227,11
AR 6,54
AR 7,78
M 103
bc 49,4,66,60
AR 8,102
AR 9,126
M 104
mh 9
EF 6,52
EF 7,53
EF 8,54
M 105
ad 66,800
H 0
EF 9,55
M 106
CV 10,6,7
CV 11,8,9
AW 228,10
M 107
R 0
AW 229,11
M 108
DG 1024
AR 6,55
M 109
AR 7,79
AR 8,103
AR 9,127
M 110
ST 44,2560
EF 6,52
EF 7,53
M 111
bc 48,4,64,56
EF 8,54
EF 9,55
v_mfma_f32_16x16x32_f16 v[192:195], v[72:75], a[8:11], v[128:131]
mh 12
CV 10,6,7
CV 11,8,9
AW 230,10
M 113
ad 64,800
H 0
AW 231,11
v_mfma_f32_16x16x32_f16 v[196:199], v[80:83], a[8:11], v[128:131]
AR 6,56
AR 7,80
AR 8,104
M 115
AR 9,128
EF 6,52
EF 7,53
M 116
H 1
EF 8,54
EF 9,55
M 117
CV 38,6,7
CV 39,8,9
AR 6,57
M 118
AR 7,81
AR 8,105
AR 9,129
M 119
WL 0
R 2
EF 6,52
M 120
H 2
EF 7,53
EF 8,54
M 121
EF 9,55
CV 40,6,7
CV 41,8,9
M 132
ae 42,72,0
AR 6,58
AR 7,82
M 133
s_bfe_u32 s52, s50, 0x10003
TX 58,9216
TX 62,23040
v_mad_i32_i24 v50, v49, s52, v48
M 122
ST 14,0
AR 8,106
AR 9,130
M 123
EF 6,52
EF 7,53
EF 8,54
M 134
EF 9,55
CV 42,6,7
CV 43,8,9
M 135
TY 66,9728
TY 70,23552
AR 6,59
v_mfma_f32_16x16x32_f16 v[216:219], v[72:75], a[16:19], v[128:131]
AR 7,83
AR 8,107
AR 9,131
M 125
EF 6,52
EF 7,53
EF 8,54
v_mfma_f32_16x16x32_f16 v[240:243], v[72:75], a[24:27], v[128:131]
EF 9,55
CV 44,6,7
CV 45,8,9
M 137
TX 74,10752
TX 78,24576
BV 52,3584
v_mfma_f32_16x16x32_f16 v[220:223], v[80:83], a[16:19], v[128:131]
WL 5
Q 0
M 127
Q 2
v_mfma_f32_16x16x32_f16 v[244:247], v[80:83], a[24:27], v[128:131]
Q 4
M 139
TY 82,11264
TY 86,25088
M 128
Q 6
ST 16,768
M 129
WL 5
Q 8
M 140
Q 10
M 141
TX 90,12288
TX 94,26112
M 130
Q 12
M 131
R 4
M 142
Q 14
M 143
TY 98,12800
TY 102,26624
M 0
R 6
M 1
WL 10
Q 16
M 12
Q 18
M 13
Q 20
M 2
Q 22
ST 18,1536
M 3
WL 8
Q 24
M 14
Q 26
M 15
Q 28
M 4
Q 30
M 5
WL 6
Q 32
M 16
Q 34
M 17
Q 36
M 6
Q 38
M 7
WL 1
Q 40
M 18
Q 42
M 19
Q 44
v_mfma_f32_16x16x32_f16 a[52:55], v[88:91], a[0:3], a[32:35]
Q 46
ST 20,2304
M 9
AR 6,148
AR 7,172
v_mfma_f32_16x16x32_f16 a[76:79], v[88:91], a[8:11], a[32:35]
AR 8,196
EF 6,12
M 21
EF 7,13
EF 8,46
v_mfma_f32_16x16x32_f16 a[56:59], v[96:99], a[0:3], a[32:35]
EF 152,47
CV 10,6,7
M 11
R 8
v_mfma_f32_16x16x32_f16 a[80:83], v[96:99], a[8:11], a[32:35]
CV 11,8,152
AW 232,10
M 23
R 10
M 24
AW 233,11
AR 6,149
M 25
AR 7,173
AR 8,197
M 26
EF 6,12
EF 7,13
M 27
EF 8,46
EF 153,47
M 28
ae 42,72,0
ST 22,128
M 29
CV 10,6,7
CV 11,8,153
M 30
AW 234,10
AW 235,11
M 31
AR 6,150
AR 7,174
v_mfma_f32_16x16x32_f16 a[100:103], v[88:91], a[16:19], a[32:35]
AR 8,198
EF 6,12
M 33
EF 7,13
EF 8,46
v_mfma_f32_16x16x32_f16 a[104:107], v[96:99], a[16:19], a[32:35]
EF 154,47
CV 10,6,7
M 35
WL 1
CV 11,8,154
AW 236,10
M 36
AW 237,11
AR 6,151
M 37
AR 7,175
AR 8,199
M 38
EF 6,12
EF 7,13
M 39
EF 8,46
EF 155,47
M 40
ST 24,896
CV 10,6,7
M 41
CV 11,8,155
AW 238,10
M 42
AW 239,11
M 43
WV 12
AR 6,152
AR 7,176
EF 6,12
EF 7,13
v_mfma_f32_16x16x32_f16 a[124:127], v[88:91], a[24:27], a[32:35]
WL 0
EF 132,46
EF 156,47
CV 14,6,7
CV 15,132,156
M 45
s_barrier
R 24
AR 6,153
AR 7,177
v_mfma_f32_16x16x32_f16 a[128:131], v[96:99], a[24:27], a[32:35]
mw 72
EF 6,12
EF 7,13
EF 133,46
EF 157,47
M 47
R 26
CV 16,6,7
CV 17,133,157
M 48
TY 106,520
TY 110,14344
AR 6,154
AR 7,178
M 49
ad 84,336
W 0
EF 6,12
EF 7,13
M 50
EF 134,46
EF 158,47
CV 18,6,7
CV 19,134,158
M 51
AR 6,155
AR 7,179
EF 6,12
EF 7,13
)"
R"(M 52
W 1
ST 26,1664
EF 135,46
EF 159,47
M 53
CV 20,6,7
CV 21,135,159
M 54
EF 176,52
EF 200,53
EF 224,54
M 55
W 2
EF 248,55
CV 10,176,200
v_mfma_f32_16x16x32_f16 a[148:151], v[88:91], a[0:3], a[32:35]
CV 11,224,248
AW 240,10
AW 241,11
M 57
WL 0
AN 104,106
AN 105,107
EF 177,52
v_mfma_f32_16x16x32_f16 a[152:155], v[96:99], a[0:3], a[32:35]
mw 75
SH 106
SH 107
EF 201,53
M 59
R 28
EF 225,54
M 60
AN 106,106
AN 107,107
EF 249,55
M 61
AN 108,110
AN 109,111
CV 10,177,201
M 62
SH 110
SH 111
CV 11,225,249
M 63
AN 110,110
AN 111,111
AW 242,10
M 64
TY 114,2056
TY 118,15880
ST 28,2432
M 65
ad 84,342
AW 243,11
M 66
W 0
EF 178,52
EF 202,53
M 67
EF 226,54
EF 250,55
CV 10,178,202
v_mfma_f32_16x16x32_f16 a[172:175], v[88:91], a[8:11], a[32:35]
CV 11,226,250
AW 244,10
AW 245,11
M 69
W 1
EF 179,52
EF 203,53
v_mfma_f32_16x16x32_f16 a[176:179], v[96:99], a[8:11], a[32:35]
EF 227,54
EF 251,55
CV 10,179,203
M 71
R 30
CV 11,227,251
M 72
W 2
AW 246,10
AW 247,11
M 73
WL 3
AN 112,114
AN 113,115
EF 180,52
M 74
SH 114
SH 115
EF 204,53
M 75
mw 0
AN 114,114
AN 115,115
EF 228,54
M 76
WL 2
AN 116,118
AN 117,119
EF 252,55
M 77
SH 118
SH 119
CV 10,180,204
M 78
AN 118,118
AN 119,119
CV 11,228,252
M 79
TY 122,3592
TY 126,17416
AW 248,10
v_mfma_f32_16x16x32_f16 a[196:199], v[88:91], a[16:19], a[32:35]
ad 84,384
W 0
AW 249,11
M 81
ae 42,72,0
ST 30,256
EF 181,52
v_mfma_f32_16x16x32_f16 v[132:135], v[96:99], a[16:19], v[128:131]
EF 205,53
EF 229,54
EF 253,55
M 83
R 32
CV 10,181,205
M 84
W 1
CV 11,229,253
AW 250,10
M 85
AW 251,11
EF 182,52
EF 206,53
M 86
EF 230,54
EF 254,55
CV 10,182,206
M 87
W 2
CV 11,230,254
AW 252,10
M 88
WL 3
AN 120,122
AN 121,123
AW 253,11
M 89
SH 122
SH 123
EF 183,52
M 90
mw 3
AN 122,122
AN 123,123
EF 207,53
M 91
WL 2
AN 124,126
AN 125,127
EF 231,54
v_mfma_f32_16x16x32_f16 v[152:155], v[88:91], a[24:27], v[128:131]
SH 126
SH 127
EF 255,55
M 93
AN 126,126
AN 127,127
ST 32,1024
v_mfma_f32_16x16x32_f16 v[156:159], v[96:99], a[24:27], v[128:131]
ad 84,390
W 0
CV 10,183,207
M 95
R 34
CV 11,231,255
M 96
AW 254,10
AW 255,11
M 97
W 1
M 98
M 99
M 100
W 2
M 101
M 102
M 103
bc 49,5,66,60
v_mfma_f32_16x16x32_f16 v[176:179], v[88:91], a[0:3], v[128:131]
mh 18
M 105
ad 66,992
H 0
ST 34,1792
v_mfma_f32_16x16x32_f16 v[180:183], v[96:99], a[0:3], v[128:131]
M 107
R 48
M 108
DG 1024
M 109
M 110
M 111
bc 48,5,64,56
M 112
mh 21
M 113
ad 64,992
H 0
M 114
M 115
v_mfma_f32_16x16x32_f16 v[200:203], v[88:91], a[8:11], v[128:131]
H 1
M 117
ST 36,2560
v_mfma_f32_16x16x32_f16 v[204:207], v[96:99], a[8:11], v[128:131]
M 119
WL 2
R 50
M 120
H 2
M 121
M 132
M 133
PA 12288
TX 58,0
TX 62,13824
DF
M 122
M 123
M 134
M 135
TY 66,512
TY 70,14336
M 124
M 125
ae 42,72,96
M 136
s_bfe_u32 s52, s50, 0x10005
v_mad_i32_i24 v50, v49, s52, v48
ST 38,0
M 137
TX 74,1536
TX 78,15360
M 126
WL 4
Q 0
M 127
Q 2
M 138
Q 4
M 139
TY 82,2048
TY 86,15872
v_mfma_f32_16x16x32_f16 v[224:227], v[88:91], a[16:19], v[128:131]
Q 6
M 129
WL 4
Q 8
v_mfma_f32_16x16x32_f16 v[248:251], v[88:91], a[24:27], v[128:131]
Q 10
M 141
TX 90,3072
TX 94,16896
v_mfma_f32_16x16x32_f16 v[228:231], v[96:99], a[16:19], v[128:131]
Q 12
M 131
R 52
v_mfma_f32_16x16x32_f16 v[252:255], v[96:99], a[24:27], v[128:131]
Q 14
ST 40,768
M 143
TY 98,3584
TY 102,17408
M 0
R 54
M 1
WL 10
Q 16
M 12
Q 18
M 13
Q 20
M 2
Q 22
M 3
WL 8
Q 24
M 14
Q 26
M 15
Q 28
M 4
Q 30
M 5
WL 6
Q 32
M 16
Q 34
ST 42,1536
M 17
Q 36
M 6
Q 38
M 7
WL 1
Q 40
M 18
Q 42
M 19
Q 44
M 8
Q 46
M 9
M 20
M 21
M 10
M 11
R 56
M 22
ST 44,2304
M 23
R 58
M 24
M 25
M 26
M 27
M 28
M 29
M 30
M 31
M 32
M 33
M 34
ae 42,72,96
ST 14,128
M 35
WL 4
M 36
M 37
M 38
M 39
M 40
M 41
M 42
M 43
WV 13
M 44
WL 0
M 45
s_barrier
R 72
M 46
mw 24
ST 16,896
M 47
R 74
M 48
TY 106,5128
TY 110,18952
M 49
ad 84,432
W 0
M 50
M 51
M 52
W 1
M 53
M 54
M 55
W 2
M 56
M 57
WL 1
AN 104,106
AN 105,107
M 58
mw 27
SH 106
SH 107
ST 18,1664
M 59
R 76
M 60
AN 106,106
AN 107,107
M 61
WL 2
AN 108,110
AN 109,111
M 62
SH 110
SH 111
M 63
AN 110,110
AN 111,111
M 64
)"
R"(TY 114,6664
TY 118,20488
M 65
ad 84,438
M 66
W 0
M 67
M 68
M 69
W 1
M 70
ST 20,2432
M 71
R 78
M 72
W 2
M 73
WL 3
AN 112,114
AN 113,115
M 74
SH 114
SH 115
M 75
mw 48
AN 114,114
AN 115,115
M 76
WL 2
AN 116,118
AN 117,119
M 77
SH 118
SH 119
M 78
AN 118,118
AN 119,119
M 79
TY 122,8200
TY 126,22024
M 80
ad 84,480
W 0
M 81
M 82
M 83
R 80
M 84
W 1
M 85
M 86
M 87
W 2
M 88
WL 3
AN 120,122
AN 121,123
M 89
SH 122
SH 123
M 90
mw 51
AN 122,122
AN 123,123
M 91
WL 2
AN 124,126
AN 125,127
M 92
SH 126
SH 127
M 93
AN 126,126
AN 127,127
M 94
ad 84,486
W 0
M 95
R 82
M 96
M 97
W 1
M 98
M 99
M 100
W 2
M 101
M 102
M 103
bc 49,6,66,60
M 104
mh 0
M 105
ad 66,1184
H 0
M 106
M 107
R 0
M 108
DG 1024
M 109
M 110
M 111
bc 48,6,64,56
M 112
mh 3
M 113
ad 64,1184
H 0
M 114
M 115
M 116
H 1
M 117
M 118
M 119
WL 2
R 2
M 120
H 2
M 121
M 132
M 133
TX 58,4608
TX 62,18432
M 122
M 123
M 134
M 135
TY 66,5120
TY 70,18944
M 124
ae 44,70,384
M 125
s_bitcmp1_b32 s50, 6
s_cselect_b32 s51, 24576, 0
v_add_u32 v47, s51, v3
M 136
XL 22,0
M 137
TX 74,6144
TX 78,19968
M 126
WL 4
Q 0
M 127
Q 2
M 138
Q 4
M 139
TY 82,6656
TY 86,20480
M 128
Q 6
M 129
WL 4
Q 8
M 140
Q 10
M 141
TX 90,7680
TX 94,21504
M 130
Q 12
M 131
R 4
M 142
Q 14
M 143
TY 98,8192
TY 102,22016
M 0
R 6
M 1
WL 10
Q 16
M 12
Q 18
M 13
Q 20
M 2
Q 22
M 3
WL 8
Q 24
M 14
Q 26
M 15
Q 28
M 4
Q 30
M 5
WL 6
Q 32
M 16
Q 34
M 17
Q 36
M 6
Q 38
M 7
WL 2
Q 40
M 18
Q 42
M 19
Q 44
M 8
Q 46
M 9
ae 42,72,0
M 20
s_bitcmp1_b32 s50, 2
s_cselect_b32 s52, 24576, 0
v_add_u32 v50, s52, v3
M 21
SA 200,0
XL 24,768
M 10
XL 26,1536
M 11
R 8
M 22
XL 28,2304
M 23
R 10
M 24
M 25
M 26
M 27
M 28
M 29
M 30
M 31
M 32
M 33
M 34
M 35
WL 4
M 36
M 37
M 38
M 39
M 40
M 41
M 42
M 43
WV 10
M 44
WL 0
M 45
s_barrier
R 24
M 46
mw 72
M 47
R 26
M 48
TY 106,9736
TY 110,23560
M 49
ad 84,528
W 0
M 50
M 51
M 52
W 1
M 53
M 54
M 55
W 2
M 56
M 57
WL 1
AN 104,106
AN 105,107
M 58
mw 75
SH 106
SH 107
M 59
R 28
M 60
AN 106,106
AN 107,107
M 61
WL 2
AN 108,110
AN 109,111
M 62
SH 110
SH 111
M 63
AN 110,110
AN 111,111
M 64
TY 114,11272
TY 118,25096
M 65
ad 84,534
M 66
W 0
M 67
M 68
M 69
W 1
M 70
M 71
R 30
M 72
W 2
M 73
WL 3
AN 112,114
AN 113,115
M 74
SH 114
SH 115
M 75
mw 0
AN 114,114
AN 115,115
M 76
WL 2
AN 116,118
AN 117,119
M 77
SH 118
SH 119
M 78
AN 118,118
AN 119,119
M 79
TY 122,12808
TY 126,26632
M 80
ad 84,576
W 0
M 81
M 82
M 83
R 32
M 84
W 1
SA 202,768
M 85
M 86
M 87
W 2
M 88
WL 3
AN 120,122
AN 121,123
M 89
SH 122
SH 123
M 90
mw 3
AN 122,122
AN 123,123
M 91
WL 2
AN 124,126
AN 125,127
M 92
SH 126
SH 127
M 93
AN 126,126
AN 127,127
M 94
ad 84,582
W 0
M 95
R 34
M 96
M 97
W 1
M 98
M 99
M 100
W 2
M 101
M 102
M 103
bc 49,7,66,60
M 104
mh 9
M 105
ad 66,1376
H 0
M 106
M 107
R 48
M 108
DG 1024
M 109
M 110
M 111
bc 48,7,64,56
)"
R"(M 112
mh 12
M 113
ad 64,1376
H 0
M 114
M 115
M 116
H 1
M 117
M 118
M 119
WL 2
R 50
M 120
H 2
M 121
M 132
M 133
PA 12288
TX 58,9216
TX 62,23040
DF
M 122
M 123
M 134
M 135
TY 66,9728
TY 70,23552
M 124
M 125
M 136
ae 44,70,384
M 137
TX 74,10752
TX 78,24576
M 126
s_bfe_u32 s51, s50, 0x10007
WL 4
Q 0
M 127
Q 2
M 138
Q 4
M 139
TY 82,11264
TY 86,25088
M 128
Q 6
M 129
WL 4
Q 8
M 140
Q 10
M 141
TX 90,12288
TX 94,26112
M 130
Q 12
M 131
R 52
M 142
Q 14
M 143
TY 98,12800
TY 102,26624
M 0
R 54
M 1
WL 10
Q 16
M 12
Q 18
M 13
Q 20
M 2
Q 22
M 3
WL 8
Q 24
M 14
Q 26
M 15
Q 28
M 4
Q 30
M 5
WL 6
Q 32
M 16
Q 34
M 17
Q 36
M 6
Q 38
M 7
WL 2
Q 40
M 18
Q 42
M 19
Q 44
M 8
Q 46
M 9
SA 204,1536
v_mad_i32_i24 v47, v49, s51, v48
M 20
XL 30,0
M 21
XL 32,768
M 10
XL 34,1536
M 11
R 56
M 22
XL 36,2304
M 23
R 58
M 24
M 25
M 26
M 27
M 28
M 29
M 30
M 31
M 32
M 33
M 34
M 35
WL 4
M 36
M 37
M 38
M 39
M 40
WV 28
UL 6,22
UH 7,22
M 41
UL 8,23
UH 9,23
M 42
AR 10,36
AR 11,60
M 43
WV 11
AR 12,84
AR 13,108
M 44
WL 0
AD 10,6
AD 11,7
M 45
s_barrier
R 72
M 46
mw 24
AD 12,8
AD 13,9
M 47
R 74
M 48
TY 106,520
TY 110,14344
M 49
ad 84,624
W 0
M 50
AW 36,10
AW 60,11
M 51
AW 84,12
AW 108,13
M 52
W 1
UL 6,24
M 53
UH 7,24
UL 8,25
M 54
UH 9,25
AR 10,37
M 55
W 2
AR 11,61
M 56
AR 12,85
AR 13,109
M 57
WL 1
AN 104,106
AN 105,107
M 58
mw 27
SH 106
SH 107
M 59
R 76
M 60
AN 106,106
AN 107,107
M 61
WL 2
AN 108,110
AN 109,111
M 62
SH 110
SH 111
M 63
AN 110,110
AN 111,111
M 64
TY 114,2056
TY 118,15880
M 65
ad 84,630
M 66
W 0
AD 10,6
M 67
AD 11,7
AD 12,8
M 68
AD 13,9
AW 37,10
M 69
W 1
AW 61,11
M 70
AW 85,12
AW 109,13
M 71
R 78
M 72
W 2
UL 6,26
M 73
WL 3
AN 112,114
AN 113,115
M 74
SH 114
SH 115
M 75
mw 48
AN 114,114
AN 115,115
M 76
WL 2
AN 116,118
AN 117,119
M 77
SH 118
SH 119
M 78
AN 118,118
AN 119,119
M 79
TY 122,3592
TY 126,17416
M 80
ad 84,672
W 0
M 81
SA 206,2304
UH 7,26
M 82
UL 8,27
UH 9,27
M 83
R 80
M 84
W 1
AR 10,38
M 85
AR 11,62
AR 12,86
M 86
AR 13,110
AD 10,6
M 87
W 2
AD 11,7
M 88
WL 3
AN 120,122
AN 121,123
M 89
SH 122
SH 123
M 90
mw 51
AN 122,122
AN 123,123
M 91
WL 2
AN 124,126
AN 125,127
M 92
SH 126
SH 127
M 93
AN 126,126
AN 127,127
M 94
ad 84,678
W 0
M 95
R 82
M 96
AD 12,8
AD 13,9
M 97
W 1
AW 38,10
M 98
AW 62,11
AW 86,12
M 99
AW 110,13
UL 6,28
M 100
W 2
UH 7,28
M 101
UL 8,29
UH 9,29
M 102
AR 10,39
AR 11,63
M 103
bc 49,8,66,60
AR 12,87
M 104
mh 18
AR 13,111
AD 10,6
M 105
ad 66,1568
H 0
M 106
AD 11,7
AD 12,8
M 107
R 0
M 108
DG 1024
M 109
AD 13,9
AW 39,10
M 110
AW 63,11
AW 87,12
M 111
bc 48,8,64,56
AW 111,13
M 112
mh 21
M 113
ad 64,1568
H 0
M 114
M 115
M 116
H 1
M 117
M 118
M 119
WL 2
R 2
M 120
H 2
M 121
M 132
M 133
TX 58,0
TX 62,13824
M 122
M 123
M 134
M 135
TY 66,512
TY 70,14336
M 124
M 125
M 136
M 137
TX 74,1536
TX 78,15360
M 126
WL 4
Q 0
M 127
Q 2
M 138
Q 4
M 139
TY 82,2048
)"
R"(TY 86,15872
M 128
Q 6
M 129
WL 4
Q 8
M 140
Q 10
M 141
TX 90,3072
TX 94,16896
M 130
Q 12
M 131
R 4
M 142
Q 14
M 143
TY 98,3584
TY 102,17408
M 0
R 6
M 1
WL 10
Q 16
M 12
Q 18
M 13
Q 20
M 2
Q 22
M 3
WL 8
Q 24
M 14
Q 26
M 15
Q 28
M 4
Q 30
M 5
WL 6
Q 32
M 16
Q 34
M 17
Q 36
M 6
Q 38
M 7
WL 2
Q 40
M 18
Q 42
M 19
Q 44
M 8
Q 46
M 9
ae 42,72,0
M 20
SA 208,128
M 21
ae 44,68,384
M 10
s_bitcmp1_b32 s50, 8
s_cselect_b32 s51, 24576, 0
v_add_u32 v47, s51, v3
M 11
R 8
M 22
XL 38,0
M 23
R 10
M 24
XL 40,768
M 25
XL 42,1536
M 26
XL 44,2304
M 27
M 28
M 29
M 30
M 31
M 32
M 33
M 34
M 35
WL 4
M 36
M 37
M 38
M 39
M 40
M 41
M 42
WV 23
UL 6,30
UH 7,30
M 43
WV 10
UL 8,31
UH 9,31
M 44
WL 0
AR 10,40
AR 11,64
M 45
s_barrier
R 24
M 46
mw 72
AR 12,88
AR 13,112
M 47
R 26
M 48
TY 106,5128
TY 110,18952
M 49
ad 84,720
W 0
M 50
AD 10,6
AD 11,7
M 51
AD 12,8
AD 13,9
M 52
W 1
AW 40,10
M 53
AW 64,11
AW 88,12
M 54
AW 112,13
UL 6,32
M 55
W 2
UH 7,32
M 56
UL 8,33
UH 9,33
M 57
WL 1
AN 104,106
AN 105,107
M 58
mw 75
SH 106
SH 107
M 59
R 28
M 60
AN 106,106
AN 107,107
M 61
WL 2
AN 108,110
AN 109,111
M 62
SH 110
SH 111
M 63
AN 110,110
AN 111,111
M 64
TY 114,6664
TY 118,20488
M 65
ad 84,726
M 66
W 0
AR 10,41
M 67
AR 11,65
AR 12,89
M 68
AR 13,113
AD 10,6
M 69
W 1
AD 11,7
M 70
AD 12,8
AD 13,9
M 71
R 30
M 72
W 2
AW 41,10
M 73
WL 3
AN 112,114
AN 113,115
M 74
SH 114
SH 115
M 75
mw 0
AN 114,114
AN 115,115
M 76
WL 2
AN 116,118
AN 117,119
M 77
SH 118
SH 119
M 78
AN 118,118
AN 119,119
M 79
TY 122,8200
TY 126,22024
M 80
ad 84,768
W 0
M 81
AW 65,11
AW 89,12
M 82
SA 210,896
AW 113,13
M 83
R 32
M 84
W 1
UL 6,34
M 85
UH 7,34
UL 8,35
M 86
UH 9,35
AR 10,42
M 87
W 2
AR 11,66
M 88
WL 3
AN 120,122
AN 121,123
M 89
SH 122
SH 123
M 90
mw 3
AN 122,122
AN 123,123
M 91
WL 2
AN 124,126
AN 125,127
M 92
SH 126
SH 127
M 93
AN 126,126
AN 127,127
M 94
ad 84,774
W 0
M 95
R 34
M 96
AR 12,90
AR 13,114
M 97
W 1
AD 10,6
M 98
AD 11,7
AD 12,8
M 99
AD 13,9
AW 42,10
M 100
W 2
AW 66,11
M 101
AW 90,12
AW 114,13
M 102
UL 6,36
UH 7,36
M 103
bc 49,9,66,60
UL 8,37
M 104
mh 0
UH 9,37
AR 10,43
M 105
ad 66,1760
H 0
M 106
AR 11,67
AR 12,91
M 107
R 48
M 108
DG 1024
M 109
AR 13,115
AD 10,6
M 110
AD 11,7
AD 12,8
M 111
bc 48,9,64,56
AD 13,9
M 112
mh 3
AW 43,10
AW 67,11
M 113
ad 64,1760
H 0
M 114
AW 91,12
AW 115,13
M 115
M 116
H 1
M 117
M 118
M 119
WL 2
R 50
M 120
H 2
M 121
M 132
M 133
PA 12288
TX 58,4608
TX 62,18432
DF
M 122
M 123
M 134
M 135
TY 66,5120
TY 70,18944
M 124
M 125
M 136
M 137
TX 74,6144
TX 78,19968
M 126
WL 4
Q 0
M 127
Q 2
M 138
Q 4
M 139
TY 82,6656
TY 86,20480
M 128
Q 6
M 129
WL 4
Q 8
M 140
Q 10
M 141
TX 90,7680
TX 94,21504
M 130
Q 12
M 131
R 52
M 142
Q 14
M 143
TY 98,8192
TY 102,22016
M 0
R 54
M 1
WL 10
Q 16
M 12
Q 18
M 13
Q 20
M 2
Q 22
M 3
WL 8
Q 24
M 14
Q 26
M 15
Q 28
M 4
Q 30
)"
R"(M 5
WL 6
Q 32
M 16
Q 34
M 17
Q 36
M 6
Q 38
M 7
WL 2
Q 40
M 18
Q 42
M 19
Q 44
M 8
Q 46
M 9
SA 212,1664
M 20
ae 44,68,384
M 21
s_bfe_u32 s51, s50, 0x10009
v_mad_i32_i24 v47, v49, s51, v48
XL 14,0
M 10
XL 16,768
M 11
R 56
M 22
XL 18,1536
M 23
R 58
M 24
XL 20,2304
M 25
M 26
M 27
M 28
M 29
M 30
M 31
M 32
M 33
M 34
M 35
WL 4
M 36
M 37
M 38
M 39
M 40
M 41
M 42
M 43
WL 0
M 44
WV 11
UL 6,38
UH 7,38
M 45
s_barrier
R 72
M 46
mw 24
UL 8,39
UH 9,39
M 47
R 74
M 48
TY 106,9736
TY 110,23560
M 49
ad 84,816
W 0
M 50
AR 10,44
AR 11,68
M 51
AR 12,92
AR 13,116
M 52
W 1
AD 10,6
M 53
AD 11,7
AD 12,8
M 54
AD 13,9
AW 44,10
M 55
W 2
AW 68,11
M 56
AW 92,12
AW 116,13
M 57
WL 1
AN 104,106
AN 105,107
M 58
mw 27
SH 106
SH 107
M 59
R 76
M 60
AN 106,106
AN 107,107
M 61
WL 2
AN 108,110
AN 109,111
M 62
SH 110
SH 111
M 63
AN 110,110
AN 111,111
M 64
TY 114,11272
TY 118,25096
M 65
ad 84,822
M 66
W 0
UL 6,40
M 67
UH 7,40
UL 8,41
M 68
UH 9,41
AR 10,45
M 69
W 1
AR 11,69
M 70
AR 12,93
AR 13,117
M 71
R 78
M 72
W 2
AD 10,6
M 73
WL 3
AN 112,114
AN 113,115
M 74
SH 114
SH 115
M 75
mw 48
AN 114,114
AN 115,115
M 76
WL 2
AN 116,118
AN 117,119
M 77
SH 118
SH 119
M 78
AN 118,118
AN 119,119
M 79
TY 122,12808
TY 126,26632
M 80
ad 84,864
W 0
M 81
SA 214,2432
AD 11,7
M 82
AD 12,8
AD 13,9
M 83
R 80
M 84
W 1
AW 45,10
M 85
AW 69,11
AW 93,12
M 86
AW 117,13
UL 6,42
M 87
W 2
UH 7,42
M 88
WL 3
AN 120,122
AN 121,123
M 89
SH 122
SH 123
M 90
mw 51
AN 122,122
AN 123,123
M 91
WL 2
AN 124,126
AN 125,127
M 92
SH 126
SH 127
M 93
AN 126,126
AN 127,127
M 94
ad 84,870
W 0
M 95
R 82
M 96
UL 8,43
UH 9,43
M 97
W 1
AR 10,46
M 98
AR 11,70
AR 12,94
M 99
AR 13,118
AD 10,6
M 100
W 2
AD 11,7
M 101
AD 12,8
AD 13,9
M 102
AW 46,10
AW 70,11
M 103
bc 49,10,66,60
AW 94,12
M 104
mh 9
AW 118,13
UL 6,44
M 105
ad 66,1952
H 0
M 106
UH 7,44
UL 8,45
M 107
R 0
M 108
DG 1024
M 109
UH 9,45
AR 10,47
M 110
AR 11,71
AR 12,95
M 111
bc 48,10,64,56
AR 13,119
M 112
mh 12
AD 10,6
AD 11,7
M 113
ad 64,1952
H 0
M 114
AD 12,8
AD 13,9
M 115
AW 47,10
AW 71,11
M 116
H 1
AW 95,12
M 117
AW 119,13
M 118
M 119
WL 2
R 2
M 120
H 2
M 121
M 132
M 133
TX 58,9216
TX 62,23040
M 122
M 123
M 134
M 135
TY 66,9728
TY 70,23552
M 124
M 125
M 136
M 137
TX 74,10752
TX 78,24576
M 126
WL 4
Q 0
M 127
Q 2
M 138
Q 4
M 139
TY 82,11264
TY 86,25088
M 128
Q 6
M 129
WL 4
Q 8
M 140
Q 10
M 141
TX 90,12288
TX 94,26112
M 130
Q 12
M 131
R 4
M 142
Q 14
M 143
TY 98,12800
TY 102,26624
M 0
R 6
M 1
WL 10
Q 16
M 12
Q 18
M 13
Q 20
M 2
Q 22
M 3
WL 8
Q 24
M 14
Q 26
M 15
Q 28
M 4
Q 30
M 5
WL 6
Q 32
M 16
Q 34
M 17
Q 36
M 6
Q 38
M 7
WL 2
Q 40
M 18
Q 42
M 19
Q 44
M 8
Q 46
M 9
ae 42,72,0
M 20
SA 216,256
M 21
ae 44,68,480
M 10
s_bitcmp1_b32 s50, 10
s_cselect_b32 s51, 24576, 0
v_add_u32 v47, s51, v3
M 11
R 8
M 22
XL 22,0
M 23
R 10
M 24
XL 24,768
M 25
XL 26,1536
M 26
XL 28,2304
M 27
M 28
M 29
M 30
M 31
)"
R"(M 32
M 33
M 34
M 35
WL 4
M 36
M 37
M 38
M 39
M 40
M 41
M 42
M 43
WV 10
M 44
WL 0
M 45
s_barrier
R 24
M 46
mw 72
UL 6,14
UH 7,14
M 47
R 26
M 48
TY 106,520
TY 110,14344
M 49
ad 84,912
W 0
M 50
UL 8,15
UH 9,15
M 51
AR 10,48
AR 11,72
M 52
W 1
AR 12,96
M 53
AR 13,120
AD 10,6
M 54
AD 11,7
AD 12,8
M 55
W 2
AD 13,9
M 56
AW 48,10
AW 72,11
M 57
WL 1
AN 104,106
AN 105,107
M 58
mw 75
SH 106
SH 107
M 59
R 28
M 60
AN 106,106
AN 107,107
M 61
WL 2
AN 108,110
AN 109,111
M 62
SH 110
SH 111
M 63
AN 110,110
AN 111,111
M 64
TY 114,2056
TY 118,15880
M 65
ad 84,918
M 66
W 0
AW 96,12
M 67
AW 120,13
UL 6,16
M 68
UH 7,16
UL 8,17
M 69
W 1
UH 9,17
M 70
AR 10,49
AR 11,73
M 71
R 30
M 72
W 2
AR 12,97
M 73
WL 3
AN 112,114
AN 113,115
M 74
SH 114
SH 115
M 75
mw 0
AN 114,114
AN 115,115
M 76
WL 2
AN 116,118
AN 117,119
M 77
SH 118
SH 119
M 78
AN 118,118
AN 119,119
M 79
TY 122,3592
TY 126,17416
M 80
ad 84,960
W 0
M 81
AR 13,121
AD 10,6
M 82
SA 218,1024
AD 11,7
M 83
R 32
M 84
W 1
AD 12,8
M 85
AD 13,9
AW 49,10
M 86
AW 73,11
AW 97,12
M 87
W 2
AW 121,13
M 88
WL 3
AN 120,122
AN 121,123
M 89
SH 122
SH 123
M 90
mw 3
AN 122,122
AN 123,123
M 91
WL 2
AN 124,126
AN 125,127
M 92
SH 126
SH 127
M 93
AN 126,126
AN 127,127
M 94
ad 84,966
W 0
M 95
R 34
M 96
UL 6,18
UH 7,18
M 97
W 1
UL 8,19
M 98
UH 9,19
AR 10,50
M 99
AR 11,74
AR 12,98
M 100
W 2
AR 13,122
M 101
AD 10,6
AD 11,7
M 102
AD 12,8
AD 13,9
M 103
bc 49,11,66,60
AW 50,10
M 104
mh 18
AW 74,11
AW 98,12
M 105
ad 66,2144
H 0
M 106
AW 122,13
UL 6,20
M 107
R 48
M 108
DG 1024
M 109
UH 7,20
UL 8,21
M 110
UH 9,21
AR 10,51
M 111
bc 48,11,64,56
AR 11,75
M 112
mh 21
AR 12,99
AR 13,123
M 113
ad 64,2144
H 0
M 114
AD 10,6
AD 11,7
M 115
AD 12,8
AD 13,9
M 116
H 1
AW 51,10
M 117
AW 75,11
AW 99,12
M 118
AW 123,13
M 119
WL 1
R 50
M 120
H 2
M 121
M 132
M 133
PA 12288
TX 58,0
TX 62,13824
DF
M 122
M 123
M 134
M 135
TY 66,512
TY 70,14336
M 124
M 125
M 136
M 137
TX 74,1536
TX 78,15360
M 126
WL 4
Q 0
M 127
Q 2
M 138
Q 4
M 139
TY 82,2048
TY 86,15872
M 128
Q 6
M 129
WL 4
Q 8
M 140
Q 10
M 141
TX 90,3072
TX 94,16896
M 130
Q 12
M 131
R 52
M 142
Q 14
M 143
TY 98,3584
TY 102,17408
M 0
R 54
M 1
WL 10
Q 16
M 12
Q 18
M 13
Q 20
M 2
Q 22
M 3
WL 8
Q 24
M 14
Q 26
M 15
Q 28
M 4
Q 30
M 5
WL 6
Q 32
M 16
Q 34
M 17
Q 36
M 6
Q 38
M 7
WL 2
Q 40
M 18
Q 42
M 19
Q 44
M 8
Q 46
M 9
SA 220,1792
M 20
ae 44,68,480
M 21
s_bfe_u32 s51, s50, 0x1000b
v_mad_i32_i24 v47, v49, s51, v48
XL 30,0
M 10
XL 32,768
M 11
R 56
M 22
XL 34,1536
M 23
R 58
M 24
XL 36,2304
M 25
M 26
M 27
M 28
M 29
M 30
M 31
M 32
M 33
M 34
M 35
WL 4
M 36
M 37
M 38
M 39
M 40
M 41
M 42
M 43
WV 11
M 44
WL 0
M 45
s_barrier
R 72
M 46
mw 24
M 47
R 74
M 48
TY 106,5128
TY 110,18952
M 49
ad 84,1008
W 0
M 50
UL 6,22
UH 7,22
M 51
UL 8,23
UH 9,23
M 52
W 1
AR 10,52
M 53
AR 11,76
AR 12,100
M 54
AR 13,124
)"
R"(AD 10,6
M 55
W 2
AD 11,7
M 56
AD 12,8
AD 13,9
M 57
WL 1
AN 104,106
AN 105,107
M 58
mw 27
SH 106
SH 107
M 59
R 76
M 60
AN 106,106
AN 107,107
M 61
WL 2
AN 108,110
AN 109,111
M 62
SH 110
SH 111
M 63
AN 110,110
AN 111,111
M 64
TY 114,6664
TY 118,20488
M 65
ad 84,1014
M 66
W 0
AW 52,10
M 67
AW 76,11
AW 100,12
M 68
AW 124,13
UL 6,24
M 69
W 1
UH 7,24
M 70
UL 8,25
UH 9,25
M 71
R 78
M 72
W 2
AR 10,53
M 73
WL 3
AN 112,114
AN 113,115
M 74
SH 114
SH 115
M 75
mw 48
AN 114,114
AN 115,115
M 76
WL 2
AN 116,118
AN 117,119
M 77
SH 118
SH 119
M 78
AN 118,118
AN 119,119
M 79
TY 122,8200
TY 126,22024
M 80
ad 84,1056
W 0
M 81
SA 222,2560
AR 11,77
M 82
AR 12,101
AR 13,125
M 83
R 80
M 84
W 1
AD 10,6
M 85
AD 11,7
AD 12,8
M 86
AD 13,9
AW 53,10
M 87
W 2
AW 77,11
M 88
WL 3
AN 120,122
AN 121,123
M 89
SH 122
SH 123
M 90
mw 51
AN 122,122
AN 123,123
M 91
WL 2
AN 124,126
AN 125,127
M 92
SH 126
SH 127
M 93
AN 126,126
AN 127,127
M 94
ad 84,1062
W 0
M 95
R 82
M 96
AW 101,12
AW 125,13
M 97
W 1
UL 6,26
M 98
UH 7,26
UL 8,27
M 99
UH 9,27
AR 10,54
M 100
W 2
AR 11,78
M 101
AR 12,102
AR 13,126
M 102
AD 10,6
AD 11,7
M 103
bc 49,12,66,60
AD 12,8
M 104
mh 0
AD 13,9
AW 54,10
M 105
ad 66,2336
H 0
M 106
AW 78,11
AW 102,12
M 107
R 0
M 108
DG 1024
M 109
AW 126,13
UL 6,28
M 110
UH 7,28
UL 8,29
M 111
bc 48,12,64,56
UH 9,29
M 112
mh 3
AR 10,55
AR 11,79
M 113
ad 64,2336
H 0
M 114
AR 12,103
AR 13,127
M 115
AD 10,6
AD 11,7
M 116
H 1
AD 12,8
M 117
AD 13,9
AW 55,10
M 118
AW 79,11
AW 103,12
M 119
WL 0
R 2
M 120
H 2
AW 127,13
M 121
M 132
M 133
TX 58,4608
TX 62,18432
M 122
M 123
M 134
M 135
TY 66,5120
TY 70,18944
M 124
M 125
M 136
M 137
TX 74,6144
TX 78,19968
M 126
WL 4
Q 0
M 127
Q 2
M 138
Q 4
M 139
TY 82,6656
TY 86,20480
M 128
Q 6
M 129
WL 4
Q 8
M 140
Q 10
M 141
TX 90,7680
TX 94,21504
M 130
Q 12
M 131
R 4
M 142
Q 14
M 143
TY 98,8192
TY 102,22016
M 0
R 6
M 1
WL 10
Q 16
M 12
Q 18
M 13
Q 20
M 2
Q 22
M 3
WL 8
Q 24
M 14
Q 26
M 15
Q 28
M 4
Q 30
M 5
WL 6
Q 32
M 16
Q 34
M 17
Q 36
M 6
Q 38
M 7
WL 2
Q 40
M 18
Q 42
M 19
Q 44
M 8
Q 46
M 9
ae 42,72,96
M 20
s_bitcmp1_b32 s50, 4
s_cselect_b32 s52, 24576, 0
v_add_u32 v50, s52, v3
M 21
SA 224,0
M 10
M 11
R 8
M 22
M 23
R 10
M 24
M 25
M 26
M 27
M 28
M 29
M 30
M 31
M 32
ae 44,70,384
M 33
s_bitcmp1_b32 s50, 6
s_cselect_b32 s51, 24576, 0
v_add_u32 v47, s51, v3
M 34
XL 38,128
M 35
WL 4
M 36
M 37
M 38
XL 40,896
M 39
M 40
M 41
M 42
M 43
WV 8
M 44
WL 0
XL 42,1664
M 45
s_barrier
R 24
M 46
mw 72
M 47
R 26
M 48
TY 106,9736
TY 110,23560
M 49
ad 84,1104
W 0
M 50
XL 44,2432
UL 6,30
M 51
UH 7,30
UL 8,31
M 52
W 1
UH 9,31
M 53
AR 10,56
AR 11,80
M 54
AR 12,104
AR 13,128
M 55
W 2
AD 10,6
M 56
AD 11,7
AD 12,8
M 57
WL 1
AN 104,106
AN 105,107
M 58
mw 75
SH 106
SH 107
M 59
R 28
M 60
AN 106,106
AN 107,107
M 61
WL 2
AN 108,110
AN 109,111
M 62
SH 110
SH 111
M 63
AN 110,110
AN 111,111
M 64
TY 114,11272
TY 118,25096
M 65
ad 84,1110
M 66
W 0
AD 13,9
M 67
AW 56,10
AW 80,11
)"
R"(M 68
AW 104,12
AW 128,13
M 69
W 1
UL 6,32
M 70
UH 7,32
UL 8,33
M 71
R 30
M 72
W 2
UH 9,33
M 73
WL 3
AN 112,114
AN 113,115
M 74
SH 114
SH 115
M 75
mw 0
AN 114,114
AN 115,115
M 76
WL 2
AN 116,118
AN 117,119
M 77
SH 118
SH 119
M 78
AN 118,118
AN 119,119
M 79
TY 122,12808
TY 126,26632
M 80
ad 84,1152
W 0
M 81
AR 10,57
AR 11,81
M 82
AR 12,105
AR 13,129
M 83
R 32
M 84
W 1
SA 226,768
M 85
AD 10,6
AD 11,7
M 86
AD 12,8
AD 13,9
M 87
W 2
AW 57,10
M 88
WL 3
AN 120,122
AN 121,123
M 89
SH 122
SH 123
M 90
mw 3
AN 122,122
AN 123,123
M 91
WL 2
AN 124,126
AN 125,127
M 92
SH 126
SH 127
M 93
AN 126,126
AN 127,127
M 94
ad 84,1158
W 0
M 95
R 34
M 96
AW 81,11
AW 105,12
M 97
W 1
AW 129,13
M 98
UL 6,34
UH 7,34
M 99
UL 8,35
UH 9,35
M 100
W 2
AR 10,58
M 101
AR 11,82
AR 12,106
M 102
AR 13,130
AD 10,6
M 103
bc 49,13,66,60
AD 11,7
M 104
mh 9
AD 12,8
AD 13,9
M 105
ad 66,2528
H 0
M 106
AW 58,10
AW 82,11
M 107
R 48
M 108
DG 1024
M 109
AW 106,12
AW 130,13
M 110
UL 6,36
UH 7,36
M 111
bc 48,13,64,56
UL 8,37
M 112
mh 12
UH 9,37
AR 10,59
M 113
ad 64,2528
H 0
M 114
AR 11,83
AR 12,107
M 115
AR 13,131
AD 10,6
M 116
H 1
AD 11,7
M 117
AD 12,8
AD 13,9
M 118
AW 59,10
AW 83,11
M 119
WL 0
R 50
M 120
H 2
AW 107,12
M 121
AW 131,13
M 132
M 133
PA 12288
TX 58,9216
TX 62,23040
DF
M 122
M 123
M 134
M 135
TY 66,9728
TY 70,23552
M 124
M 125
M 136
M 137
TX 74,10752
TX 78,24576
M 126
WL 4
Q 0
M 127
Q 2
M 138
Q 4
M 139
TY 82,11264
TY 86,25088
M 128
Q 6
M 129
WL 4
Q 8
M 140
Q 10
M 141
TX 90,12288
TX 94,26112
M 130
Q 12
M 131
R 52
M 142
Q 14
M 143
TY 98,12800
TY 102,26624
M 0
R 54
M 1
WL 10
Q 16
M 12
Q 18
M 13
Q 20
M 2
Q 22
M 3
WL 8
Q 24
M 14
Q 26
M 15
Q 28
M 4
Q 30
M 5
WL 6
Q 32
M 16
Q 34
M 17
Q 36
M 6
Q 38
M 7
WL 2
Q 40
M 18
Q 42
M 19
Q 44
M 8
Q 46
M 9
SA 228,1536
M 20
M 21
M 10
M 11
R 56
M 22
M 23
R 58
M 24
M 25
M 26
M 27
M 28
M 29
M 30
M 31
M 32
M 33
M 34
ae 44,70,384
M 35
WL 4
M 36
s_bfe_u32 s51, s50, 0x10007
v_mad_i32_i24 v47, v49, s51, v48
XL 14,128
M 37
M 38
M 39
M 40
XL 16,896
M 41
M 42
M 43
WV 9
M 44
WL 0
M 45
s_barrier
R 72
M 46
mw 24
XL 18,1664
M 47
R 74
M 48
TY 106,520
TY 110,14344
M 49
ad 84,1200
W 0
M 50
M 51
M 52
W 1
XL 20,2432
M 53
M 54
M 55
W 2
M 56
M 57
WL 1
AN 104,106
AN 105,107
M 58
mw 27
SH 106
SH 107
M 59
R 76
M 60
AN 106,106
AN 107,107
M 61
WL 2
AN 108,110
AN 109,111
M 62
SH 110
SH 111
M 63
AN 110,110
AN 111,111
M 64
TY 114,2056
TY 118,15880
M 65
ad 84,1206
M 66
W 0
M 67
M 68
M 69
W 1
M 70
M 71
R 78
M 72
W 2
M 73
WL 3
AN 112,114
AN 113,115
M 74
SH 114
SH 115
M 75
mw 48
AN 114,114
AN 115,115
M 76
WL 2
AN 116,118
AN 117,119
M 77
SH 118
SH 119
M 78
AN 118,118
AN 119,119
M 79
TY 122,3592
TY 126,17416
M 80
ad 84,1248
W 0
M 81
SA 230,2304
M 82
M 83
R 80
M 84
W 1
M 85
M 86
M 87
W 2
M 88
WL 3
AN 120,122
AN 121,123
M 89
SH 122
SH 123
M 90
mw 51
AN 122,122
AN 123,123
)"
R"(M 91
WL 2
AN 124,126
AN 125,127
M 92
SH 126
SH 127
M 93
AN 126,126
AN 127,127
M 94
ad 84,1254
W 0
M 95
R 82
M 96
UL 6,38
UH 7,38
M 97
W 1
UL 8,39
M 98
UH 9,39
AR 10,132
M 99
AR 11,156
AR 12,180
M 100
W 2
AD 10,6
M 101
AD 11,7
AD 12,8
M 102
AD 136,9
AW 132,10
M 103
bc 49,14,66,60
AW 156,11
M 104
mh 18
AW 180,12
UL 6,40
M 105
ad 66,2720
H 0
M 106
UH 7,40
UL 8,41
M 107
R 0
M 108
DG 1024
M 109
UH 9,41
AR 10,133
M 110
AR 11,157
AR 12,181
M 111
bc 48,14,64,56
AD 10,6
M 112
mh 21
AD 11,7
AD 12,8
M 113
ad 64,2720
H 0
M 114
AD 137,9
AW 133,10
M 115
AW 157,11
AW 181,12
M 116
H 1
UL 6,42
M 117
UH 7,42
UL 8,43
M 118
UH 9,43
AR 10,134
M 119
WL 0
R 2
M 120
H 2
AR 11,158
M 121
AR 12,182
AD 10,6
M 132
AD 11,7
AD 12,8
M 133
TX 58,0
TX 62,13824
M 122
AD 138,9
AW 134,10
M 123
AW 158,11
AW 182,12
M 134
UL 6,44
UH 7,44
M 135
TY 66,512
TY 70,14336
M 124
UL 8,45
UH 9,45
M 125
AR 10,135
AR 11,159
M 136
AR 12,183
AD 10,6
M 137
TX 74,1536
TX 78,15360
M 126
WL 4
Q 0
M 127
Q 2
M 138
Q 4
M 139
TY 82,2048
TY 86,15872
M 128
Q 6
M 129
WL 4
Q 8
M 140
Q 10
M 141
TX 90,3072
TX 94,16896
M 130
Q 12
M 131
R 4
M 142
Q 14
M 143
TY 98,3584
TY 102,17408
M 0
R 6
M 1
WL 10
Q 16
M 12
Q 18
M 13
Q 20
M 2
Q 22
M 3
WL 8
Q 24
M 14
Q 26
M 15
Q 28
M 4
Q 30
M 5
WL 6
Q 32
M 16
Q 34
M 17
Q 36
M 6
Q 38
M 7
WL 2
Q 40
M 18
Q 42
M 19
Q 44
M 8
Q 46
M 9
ae 42,72,96
M 20
SA 232,128
AD 11,7
M 21
AD 12,8
AD 139,9
M 10
AW 135,10
AW 159,11
M 11
R 8
M 22
AW 183,12
M 23
R 10
M 24
M 25
M 26
M 27
M 28
M 29
M 30
M 31
M 32
M 33
M 34
M 35
WL 4
M 36
ae 44,68,384
M 37
s_bitcmp1_b32 s50, 8
s_cselect_b32 s51, 24576, 0
v_add_u32 v47, s51, v3
M 38
XL 22,128
M 39
M 40
M 41
M 42
XL 24,896
M 43
WV 8
M 44
WL 0
M 45
s_barrier
R 24
M 46
mw 72
M 47
R 26
M 48
TY 106,5128
TY 110,18952
M 49
ad 84,1296
W 0
M 50
XL 26,1664
M 51
M 52
W 1
M 53
M 54
XL 28,2432
M 55
W 2
M 56
M 57
WL 1
AN 104,106
AN 105,107
M 58
mw 75
SH 106
SH 107
M 59
R 28
M 60
AN 106,106
AN 107,107
M 61
WL 2
AN 108,110
AN 109,111
M 62
SH 110
SH 111
M 63
AN 110,110
AN 111,111
M 64
TY 114,6664
TY 118,20488
M 65
ad 84,1302
M 66
W 0
M 67
M 68
M 69
W 1
M 70
M 71
R 30
M 72
W 2
M 73
WL 3
AN 112,114
AN 113,115
M 74
SH 114
SH 115
M 75
mw 0
AN 114,114
AN 115,115
M 76
WL 2
AN 116,118
AN 117,119
M 77
SH 118
SH 119
M 78
AN 118,118
AN 119,119
M 79
TY 122,8200
TY 126,22024
M 80
ad 84,1344
W 0
M 81
M 82
SA 234,896
M 83
R 32
M 84
W 1
M 85
M 86
M 87
W 2
M 88
WL 3
AN 120,122
AN 121,123
M 89
SH 122
SH 123
M 90
mw 3
AN 122,122
AN 123,123
M 91
WL 2
AN 124,126
AN 125,127
M 92
SH 126
SH 127
M 93
AN 126,126
AN 127,127
M 94
ad 84,1350
W 0
M 95
R 34
M 96
UL 6,14
UH 7,14
M 97
W 1
UL 8,15
M 98
UH 9,15
AR 10,136
M 99
AR 11,160
AR 12,184
M 100
W 2
AD 10,6
M 101
AD 11,7
AD 12,8
M 102
AD 140,9
AW 136,10
M 103
bc 49,15,66,60
AW 160,11
M 104
mh 0
AW 184,12
UL 6,16
M 105
ad 66,2912
H 0
M 106
UH 7,16
UL 8,17
M 107
R 48
)"
R"(M 108
DG 1024
M 109
UH 9,17
AR 10,137
M 110
AR 11,161
AR 12,185
M 111
bc 48,15,64,56
AD 10,6
M 112
mh 3
AD 11,7
AD 12,8
M 113
ad 64,2912
H 0
M 114
AD 141,9
AW 137,10
M 115
AW 161,11
AW 185,12
M 116
H 1
UL 6,18
M 117
UH 7,18
UL 8,19
M 118
UH 9,19
AR 10,138
M 119
WL 0
R 50
M 120
H 2
AR 11,162
M 121
AR 12,186
AD 10,6
M 132
AD 11,7
AD 12,8
M 133
PA 12288
TX 58,4608
TX 62,18432
DF
M 122
AD 142,9
AW 138,10
M 123
AW 162,11
AW 186,12
M 134
UL 6,20
UH 7,20
M 135
TY 66,5120
TY 70,18944
M 124
UL 8,21
UH 9,21
M 125
AR 10,139
AR 11,163
M 136
AR 12,187
AD 10,6
M 137
TX 74,6144
TX 78,19968
M 126
WL 4
Q 0
M 127
Q 2
M 138
Q 4
M 139
TY 82,6656
TY 86,20480
M 128
Q 6
M 129
WL 4
Q 8
M 140
Q 10
M 141
TX 90,7680
TX 94,21504
M 130
Q 12
M 131
R 52
M 142
Q 14
M 143
TY 98,8192
TY 102,22016
M 0
R 54
M 1
WL 10
Q 16
M 12
Q 18
M 13
Q 20
M 2
Q 22
M 3
WL 8
Q 24
M 14
Q 26
M 15
Q 28
M 4
Q 30
M 5
WL 6
Q 32
M 16
Q 34
M 17
Q 36
M 6
Q 38
M 7
WL 2
Q 40
M 18
Q 42
M 19
Q 44
M 8
Q 46
M 9
SA 236,1664
AD 11,7
M 20
AD 12,8
AD 143,9
M 21
AW 139,10
AW 163,11
M 10
AW 187,12
M 11
R 56
M 22
M 23
R 58
M 24
M 25
M 26
M 27
M 28
M 29
M 30
M 31
M 32
M 33
M 34
M 35
WL 4
M 36
M 37
M 38
ae 44,68,384
M 39
s_bfe_u32 s51, s50, 0x10009
v_mad_i32_i24 v47, v49, s51, v48
XL 30,128
M 40
M 41
M 42
M 43
WV 8
M 44
WL 0
XL 32,896
M 45
s_barrier
R 72
M 46
mw 24
M 47
R 74
M 48
TY 106,9736
TY 110,23560
M 49
ad 84,1392
W 0
M 50
XL 34,1664
M 51
M 52
W 1
M 53
M 54
M 55
W 2
M 56
XL 36,2432
M 57
WL 1
AN 104,106
AN 105,107
M 58
mw 27
SH 106
SH 107
M 59
R 76
M 60
AN 106,106
AN 107,107
M 61
WL 2
AN 108,110
AN 109,111
M 62
SH 110
SH 111
M 63
AN 110,110
AN 111,111
M 64
TY 114,11272
TY 118,25096
M 65
ad 84,1398
M 66
W 0
M 67
M 68
M 69
W 1
M 70
M 71
R 78
M 72
W 2
M 73
WL 3
AN 112,114
AN 113,115
M 74
SH 114
SH 115
M 75
mw 48
AN 114,114
AN 115,115
M 76
WL 2
AN 116,118
AN 117,119
M 77
SH 118
SH 119
M 78
AN 118,118
AN 119,119
M 79
TY 122,12808
TY 126,26632
M 80
ad 84,1440
W 0
M 81
SA 238,2432
M 82
M 83
R 80
M 84
W 1
M 85
M 86
M 87
W 2
M 88
WL 3
AN 120,122
AN 121,123
M 89
SH 122
SH 123
M 90
mw 51
AN 122,122
AN 123,123
M 91
WL 2
AN 124,126
AN 125,127
M 92
SH 126
SH 127
M 93
AN 126,126
AN 127,127
M 94
ad 84,1446
W 0
M 95
R 82
M 96
UL 6,22
UH 7,22
M 97
W 1
UL 8,23
M 98
UH 9,23
AR 10,140
M 99
AR 11,164
AR 12,188
M 100
W 2
AD 10,6
M 101
AD 11,7
AD 12,8
M 102
AD 144,9
AW 140,10
M 103
bc 49,16,66,60
AW 164,11
M 104
mh 9
AW 188,12
UL 6,24
M 105
ad 66,3104
H 0
M 106
UH 7,24
UL 8,25
M 107
R 0
M 108
DG 1024
M 109
UH 9,25
AR 10,141
M 110
AR 11,165
AR 12,189
M 111
bc 48,16,64,56
AD 10,6
M 112
mh 12
AD 11,7
AD 12,8
M 113
ad 64,3104
H 0
M 114
AD 145,9
AW 141,10
M 115
AW 165,11
AW 189,12
M 116
H 1
UL 6,26
M 117
UH 7,26
UL 8,27
M 118
UH 9,27
AR 10,142
M 119
WL 0
R 2
M 120
H 2
AR 11,166
M 121
AR 12,190
AD 10,6
M 132
AD 11,7
AD 12,8
M 133
TX 58,9216
TX 62,23040
M 122
AD 146,9
AW 142,10
)"
R"(M 123
AW 166,11
AW 190,12
M 134
UL 6,28
UH 7,28
M 135
TY 66,9728
TY 70,23552
M 124
UL 8,29
UH 9,29
M 125
AR 10,143
AR 11,167
M 136
AR 12,191
AD 10,6
M 137
TX 74,10752
TX 78,24576
M 126
WL 4
Q 0
M 127
Q 2
M 138
Q 4
M 139
TY 82,11264
TY 86,25088
M 128
Q 6
M 129
WL 4
Q 8
M 140
Q 10
M 141
TX 90,12288
TX 94,26112
M 130
Q 12
M 131
R 4
M 142
Q 14
M 143
TY 98,12800
TY 102,26624
M 0
R 6
M 1
WL 10
Q 16
M 12
Q 18
M 13
Q 20
M 2
Q 22
M 3
WL 8
Q 24
M 14
Q 26
M 15
Q 28
M 4
Q 30
M 5
WL 6
Q 32
M 16
Q 34
M 17
Q 36
M 6
Q 38
M 7
WL 2
Q 40
M 18
Q 42
M 19
Q 44
M 8
Q 46
M 9
ae 42,72,96
M 20
SA 240,256
AD 11,7
M 21
AD 12,8
AD 147,9
M 10
AW 143,10
AW 167,11
M 11
R 8
M 22
AW 191,12
M 23
R 10
M 24
M 25
M 26
M 27
M 28
M 29
M 30
M 31
M 32
M 33
M 34
M 35
WL 4
M 36
M 37
M 38
M 39
M 40
ae 44,68,480
M 41
s_bitcmp1_b32 s50, 10
s_cselect_b32 s51, 24576, 0
v_add_u32 v47, s51, v3
M 42
XL 38,128
M 43
WV 7
M 44
WL 0
M 45
s_barrier
R 24
M 46
mw 72
XL 40,896
M 47
R 26
M 48
TY 106,520
TY 110,14344
M 49
ad 84,1488
W 0
M 50
M 51
M 52
W 1
XL 42,1664
M 53
M 54
M 55
W 2
M 56
M 57
WL 1
AN 104,106
AN 105,107
M 58
mw 75
SH 106
SH 107
M 59
R 28
M 60
AN 106,106
AN 107,107
M 61
WL 2
AN 108,110
AN 109,111
M 62
SH 110
SH 111
M 63
AN 110,110
AN 111,111
M 64
TY 114,2056
TY 118,15880
M 65
ad 84,1494
M 66
W 0
XL 44,2432
M 67
M 68
M 69
W 1
M 70
M 71
R 30
M 72
W 2
M 73
WL 3
AN 112,114
AN 113,115
M 74
SH 114
SH 115
M 75
mw 0
AN 114,114
AN 115,115
M 76
WL 2
AN 116,118
AN 117,119
M 77
SH 118
SH 119
M 78
AN 118,118
AN 119,119
M 79
TY 122,3592
TY 126,17416
M 80
ad 84,1536
W 0
M 81
M 82
SA 242,1024
M 83
R 32
M 84
W 1
M 85
M 86
M 87
W 2
M 88
WL 3
AN 120,122
AN 121,123
M 89
SH 122
SH 123
M 90
mw 3
AN 122,122
AN 123,123
M 91
WL 2
AN 124,126
AN 125,127
M 92
SH 126
SH 127
M 93
AN 126,126
AN 127,127
M 94
ad 84,1542
W 0
M 95
R 34
M 96
UL 6,30
UH 7,30
M 97
W 1
UL 8,31
M 98
UH 9,31
AR 10,144
M 99
AR 11,168
AR 12,192
M 100
W 2
AD 10,6
M 101
AD 11,7
AD 12,8
M 102
AD 148,9
AW 144,10
M 103
bc 49,17,66,60
AW 168,11
M 104
mh 18
AW 192,12
UL 6,32
M 105
ad 66,3296
H 0
M 106
UH 7,32
UL 8,33
M 107
R 48
M 108
DG 1024
M 109
UH 9,33
AR 10,145
M 110
AR 11,169
AR 12,193
M 111
bc 48,17,64,56
AD 10,6
M 112
mh 21
AD 11,7
AD 12,8
M 113
ad 64,3296
H 0
M 114
AD 149,9
AW 145,10
M 115
AW 169,11
AW 193,12
M 116
H 1
UL 6,34
M 117
UH 7,34
UL 8,35
M 118
UH 9,35
AR 10,146
M 119
WL 0
R 50
M 120
H 2
AR 11,170
M 121
AR 12,194
AD 10,6
M 132
AD 11,7
AD 12,8
M 133
PA 12288
TX 58,0
TX 62,13824
DF
M 122
AD 150,9
AW 146,10
M 123
AW 170,11
AW 194,12
M 134
UL 6,36
UH 7,36
M 135
TY 66,512
TY 70,14336
M 124
UL 8,37
UH 9,37
M 125
AR 10,147
AR 11,171
M 136
AR 12,195
AD 10,6
M 137
TX 74,1536
TX 78,15360
M 126
WL 4
Q 0
M 127
Q 2
M 138
Q 4
M 139
TY 82,2048
TY 86,15872
M 128
Q 6
M 129
WL 4
Q 8
M 140
Q 10
M 141
TX 90,3072
TX 94,16896
M 130
Q 12
M 131
R 52
M 142
Q 14
M 143
TY 98,3584
)"
R"(TY 102,17408
M 0
R 54
M 1
WL 10
Q 16
M 12
Q 18
M 13
Q 20
M 2
Q 22
M 3
WL 8
Q 24
M 14
Q 26
M 15
Q 28
M 4
Q 30
M 5
WL 6
Q 32
M 16
Q 34
M 17
Q 36
M 6
Q 38
M 7
WL 2
Q 40
M 18
Q 42
M 19
Q 44
M 8
Q 46
M 9
SA 244,1792
AD 11,7
M 20
AD 12,8
AD 151,9
M 21
AW 147,10
AW 171,11
M 10
AW 195,12
M 11
R 56
M 22
M 23
R 58
M 24
M 25
M 26
M 27
M 28
M 29
M 30
M 31
M 32
M 33
M 34
M 35
WL 4
M 36
M 37
M 38
M 39
M 40
M 41
M 42
ae 44,68,480
M 43
WV 7
M 44
WL 0
M 45
s_barrier
R 72
M 46
mw 24
M 47
R 74
M 48
s_bfe_u32 s51, s50, 0x1000b
TY 106,5128
TY 110,18952
M 49
ad 84,1584
W 0
M 50
v_mad_i32_i24 v47, v49, s51, v48
XL 14,128
M 51
XL 16,896
M 52
W 1
M 53
M 54
XL 18,1664
M 55
W 2
M 56
M 57
WL 1
AN 104,106
AN 105,107
M 58
mw 27
SH 106
SH 107
M 59
R 76
M 60
AN 106,106
AN 107,107
M 61
WL 2
AN 108,110
AN 109,111
M 62
SH 110
SH 111
M 63
AN 110,110
AN 111,111
M 64
TY 114,6664
TY 118,20488
M 65
ad 84,1590
M 66
W 0
XL 20,2432
M 67
M 68
M 69
W 1
M 70
M 71
R 78
M 72
W 2
M 73
WL 3
AN 112,114
AN 113,115
M 74
SH 114
SH 115
M 75
mw 48
AN 114,114
AN 115,115
M 76
WL 2
AN 116,118
AN 117,119
M 77
SH 118
SH 119
M 78
AN 118,118
AN 119,119
M 79
TY 122,8200
TY 126,22024
M 80
ad 84,1632
W 0
M 81
SA 246,2560
M 82
M 83
R 80
M 84
W 1
M 85
M 86
M 87
W 2
M 88
WL 3
AN 120,122
AN 121,123
M 89
SH 122
SH 123
M 90
mw 51
AN 122,122
AN 123,123
M 91
WL 2
AN 124,126
AN 125,127
M 92
SH 126
SH 127
M 93
AN 126,126
AN 127,127
M 94
ad 84,1638
W 0
M 95
R 82
M 96
UL 6,38
UH 7,38
M 97
W 1
UL 8,39
M 98
UH 9,39
AR 10,148
M 99
AR 11,172
AR 12,196
M 100
W 2
AD 10,6
M 101
AD 11,7
AD 12,8
M 102
AD 152,9
AW 148,10
M 103
bc 49,18,66,60
AW 172,11
M 104
mh 0
AW 196,12
UL 6,40
M 105
ad 66,3488
H 0
M 106
UH 7,40
UL 8,41
M 107
R 0
M 108
DG 1024
M 109
UH 9,41
AR 10,149
M 110
AR 11,173
AR 12,197
M 111
bc 48,18,64,56
AD 10,6
M 112
mh 3
AD 11,7
AD 12,8
M 113
ad 64,3488
H 0
M 114
AD 153,9
AW 149,10
M 115
AW 173,11
AW 197,12
M 116
H 1
UL 6,42
M 117
UH 7,42
UL 8,43
M 118
UH 9,43
AR 10,150
M 119
WL 0
R 2
M 120
H 2
AR 11,174
M 121
AR 12,198
AD 10,6
M 132
AD 11,7
AD 12,8
M 133
TX 58,4608
TX 62,18432
M 122
AD 154,9
AW 150,10
M 123
AW 174,11
AW 198,12
M 134
UL 6,44
UH 7,44
M 135
TY 66,5120
TY 70,18944
M 124
UL 8,45
UH 9,45
M 125
AR 10,151
AR 11,175
M 136
AR 12,199
AD 10,6
M 137
TX 74,6144
TX 78,19968
M 126
WL 4
Q 0
M 127
Q 2
M 138
Q 4
M 139
TY 82,6656
TY 86,20480
M 128
Q 6
M 129
WL 4
Q 8
M 140
Q 10
M 141
TX 90,7680
TX 94,21504
M 130
Q 12
M 131
R 4
M 142
Q 14
M 143
TY 98,8192
TY 102,22016
M 0
R 6
M 1
WL 10
Q 16
M 12
Q 18
M 13
Q 20
M 2
Q 22
M 3
WL 8
Q 24
M 14
Q 26
M 15
Q 28
M 4
Q 30
M 5
WL 6
Q 32
M 16
Q 34
M 17
Q 36
M 6
Q 38
M 7
WL 2
Q 40
M 18
Q 42
M 19
Q 44
M 8
Q 46
M 9
ae 42,72,96
M 20
s_bfe_u32 s52, s50, 0x10005
v_mad_i32_i24 v50, v49, s52, v48
SA 248,256
M 21
AD 11,7
AD 12,8
M 10
AD 155,9
AW 151,10
M 11
R 8
)"
R"(M 22
AW 175,11
AW 199,12
M 23
R 10
M 24
M 25
M 26
M 27
M 28
M 29
M 30
M 31
M 32
M 33
M 34
M 35
WL 4
M 36
M 37
M 38
M 39
M 40
M 41
M 42
M 43
WV 6
M 44
WL 0
M 45
s_barrier
R 24
M 46
mw 72
M 47
R 26
M 48
TY 106,9736
TY 110,23560
M 49
ad 84,1680
W 0
M 50
M 51
M 52
W 1
M 53
M 54
M 55
W 2
M 56
M 57
WL 1
AN 104,106
AN 105,107
M 58
mw 75
SH 106
SH 107
M 59
R 28
M 60
AN 106,106
AN 107,107
M 61
WL 2
AN 108,110
AN 109,111
M 62
SH 110
SH 111
M 63
AN 110,110
AN 111,111
M 64
TY 114,11272
TY 118,25096
M 65
ad 84,1686
M 66
W 0
M 67
M 68
M 69
W 1
M 70
ae 44,70,384
M 71
R 30
M 72
W 2
M 73
WL 3
AN 112,114
AN 113,115
M 74
SH 114
SH 115
M 75
mw 0
AN 114,114
AN 115,115
M 76
WL 2
AN 116,118
AN 117,119
M 77
SH 118
SH 119
M 78
AN 118,118
AN 119,119
M 79
TY 122,12808
TY 126,26632
M 80
ad 84,1728
W 0
M 81
s_bitcmp1_b32 s50, 6
s_cselect_b32 s51, 24576, 0
M 82
SA 250,1024
v_add_u32 v47, s51, v3
M 83
R 32
M 84
W 1
XL 22,256
M 85
XL 24,1024
M 86
XL 26,1792
M 87
W 2
M 88
WL 3
AN 120,122
AN 121,123
M 89
SH 122
SH 123
M 90
mw 3
AN 122,122
AN 123,123
M 91
WL 2
AN 124,126
AN 125,127
M 92
SH 126
SH 127
M 93
AN 126,126
AN 127,127
M 94
ad 84,1734
W 0
M 95
R 34
M 96
XL 28,2560
M 97
W 1
M 98
UL 6,14
UH 7,14
M 99
UL 8,15
UH 9,15
M 100
W 2
AR 10,152
M 101
AR 11,176
AD 10,6
M 102
AD 11,7
AD 132,8
M 103
bc 49,19,66,60
AD 156,9
M 104
mh 9
AW 152,10
AW 176,11
M 105
ad 66,3680
H 0
M 106
UL 6,16
UH 7,16
M 107
R 48
M 108
DG 1024
M 109
UL 8,17
UH 9,17
M 110
AR 10,153
AR 11,177
M 111
bc 48,19,64,56
AD 10,6
M 112
mh 12
AD 11,7
AD 133,8
M 113
ad 64,3680
H 0
M 114
AD 157,9
AW 153,10
M 115
AW 177,11
UL 6,18
M 116
H 1
UH 7,18
M 117
UL 8,19
UH 9,19
M 118
AR 10,154
AR 11,178
M 119
WL 0
R 50
M 120
H 2
AD 10,6
M 121
AD 11,7
AD 134,8
M 132
AD 158,9
AW 154,10
M 133
PA 12288
TX 58,9216
TX 62,23040
DF
M 122
AW 178,11
UL 6,20
M 123
UH 7,20
UL 8,21
M 134
UH 9,21
AR 10,155
M 135
TY 66,9728
TY 70,23552
M 124
AR 11,179
AD 10,6
M 125
AD 11,7
AD 135,8
M 136
AD 159,9
AW 155,10
M 137
TX 74,10752
TX 78,24576
M 126
WL 4
Q 0
M 127
Q 2
M 138
Q 4
M 139
TY 82,11264
TY 86,25088
M 128
Q 6
M 129
WL 4
Q 8
M 140
Q 10
M 141
TX 90,12288
TX 94,26112
M 130
Q 12
M 131
R 52
M 142
Q 14
M 143
TY 98,12800
TY 102,26624
M 0
R 54
M 1
WL 10
Q 16
M 12
Q 18
M 13
Q 20
M 2
Q 22
M 3
WL 8
Q 24
M 14
Q 26
M 15
Q 28
M 4
Q 30
M 5
WL 6
Q 32
M 16
Q 34
M 17
Q 36
M 6
Q 38
M 7
WL 2
Q 40
M 18
Q 42
M 19
Q 44
M 8
Q 46
M 9
SA 252,1792
AW 179,11
M 20
M 21
M 10
M 11
R 56
M 22
M 23
R 58
M 24
M 25
M 26
M 27
M 28
M 29
M 30
M 31
M 32
M 33
M 34
M 35
WL 4
M 36
M 37
M 38
M 39
M 40
M 41
M 42
M 43
WV 7
M 44
WL 0
M 45
s_barrier
R 72
M 46
mw 24
M 47
R 74
M 48
TY 106,520
TY 110,14344
M 49
ad 84,1776
W 0
M 50
M 51
M 52
W 1
M 53
M 54
M 55
W 2
M 56
M 57
WL 1
AN 104,106
AN 105,107
M 58
mw 27
SH 106
)"
R"(SH 107
M 59
R 76
M 60
AN 106,106
AN 107,107
M 61
WL 2
AN 108,110
AN 109,111
M 62
SH 110
SH 111
M 63
AN 110,110
AN 111,111
M 64
TY 114,2056
TY 118,15880
M 65
ad 84,1782
M 66
W 0
M 67
M 68
M 69
W 1
M 70
M 71
R 78
M 72
W 2
M 73
WL 3
AN 112,114
AN 113,115
M 74
SH 114
SH 115
M 75
mw 48
AN 114,114
AN 115,115
M 76
WL 2
AN 116,118
AN 117,119
M 77
SH 118
SH 119
M 78
AN 118,118
AN 119,119
M 79
TY 122,3592
TY 126,17416
M 80
ad 84,1824
W 0
M 81
SA 254,2560
M 82
ae 44,70,384
M 83
R 80
M 84
s_bfe_u32 s51, s50, 0x10007
W 1
v_mad_i32_i24 v47, v49, s51, v48
M 85
XL 30,256
M 86
XL 32,1024
M 87
W 2
XL 34,1792
M 88
WL 3
AN 120,122
AN 121,123
M 89
SH 122
SH 123
M 90
mw 51
AN 122,122
AN 123,123
M 91
WL 2
AN 124,126
AN 125,127
M 92
SH 126
SH 127
M 93
AN 126,126
AN 127,127
M 94
ad 84,1830
W 0
M 95
R 82
M 96
XL 36,2560
M 97
W 1
M 98
M 99
M 100
W 2
M 101
M 102
M 103
bc 49,20,66,60
M 104
mh 18
M 105
ad 66,3872
H 0
M 106
M 107
R 0
M 108
DG 1024
M 109
M 110
M 111
bc 48,20,64,56
M 112
mh 21
M 113
ad 64,3872
H 0
M 114
M 115
M 116
H 1
M 117
M 118
M 119
WL 2
R 2
M 120
H 2
M 121
M 132
M 133
TX 58,0
TX 62,13824
M 122
M 123
M 134
UL 6,22
UH 7,22
M 135
TY 66,512
TY 70,14336
M 124
UL 8,23
UH 9,23
M 125
AD 160,6
AD 184,7
M 136
AD 208,8
AD 232,9
M 137
TX 74,1536
TX 78,15360
M 126
WL 4
Q 0
M 127
Q 2
M 138
Q 4
M 139
TY 82,2048
TY 86,15872
M 128
Q 6
M 129
WL 4
Q 8
M 140
Q 10
M 141
TX 90,3072
TX 94,16896
M 130
Q 12
M 131
R 4
M 142
Q 14
M 143
TY 98,3584
TY 102,17408
M 0
R 6
M 1
WL 10
Q 16
M 12
Q 18
M 13
Q 20
M 2
Q 22
M 3
WL 8
Q 24
M 14
Q 26
M 15
Q 28
M 4
Q 30
M 5
WL 6
Q 32
M 16
Q 34
M 17
Q 36
M 6
Q 38
M 7
WL 2
Q 40
M 18
Q 42
M 19
Q 44
M 8
Q 46
M 9
UL 6,24
UH 7,24
M 20
UL 8,25
UH 9,25
M 21
AD 161,6
AD 185,7
M 10
AD 209,8
AD 233,9
M 11
R 8
M 22
UL 6,26
UH 7,26
M 23
R 10
M 24
UL 8,27
UH 9,27
M 25
AD 162,6
AD 186,7
M 26
AD 210,8
AD 234,9
M 27
UL 6,28
UH 7,28
M 28
UL 8,29
UH 9,29
M 29
AD 163,6
AD 187,7
M 30
AD 211,8
AD 235,9
M 31
M 32
M 33
M 34
M 35
WL 4
M 36
M 37
M 38
M 39
M 40
M 41
M 42
M 43
WV 5
M 44
WL 0
M 45
s_barrier
R 24
M 46
mw 72
M 47
R 26
M 48
TY 106,5128
TY 110,18952
M 49
ad 84,1872
W 0
M 50
M 51
M 52
W 1
M 53
M 54
M 55
W 2
M 56
M 57
WL 1
AN 104,106
AN 105,107
M 58
mw 75
SH 106
SH 107
M 59
R 28
M 60
AN 106,106
AN 107,107
M 61
WL 2
AN 108,110
AN 109,111
M 62
SH 110
SH 111
M 63
AN 110,110
AN 111,111
M 64
TY 114,6664
TY 118,20488
M 65
ad 84,1878
M 66
W 0
M 67
M 68
M 69
W 1
M 70
M 71
R 30
M 72
W 2
M 73
WL 3
AN 112,114
AN 113,115
M 74
SH 114
SH 115
M 75
mw 0
AN 114,114
AN 115,115
M 76
WL 2
AN 116,118
AN 117,119
M 77
SH 118
SH 119
M 78
AN 118,118
AN 119,119
M 79
TY 122,8200
TY 126,22024
M 80
ad 84,1920
W 0
M 81
ae 44,68,384
M 82
s_bitcmp1_b32 s50, 8
s_cselect_b32 s51, 24576, 0
v_add_u32 v47, s51, v3
M 83
R 32
M 84
W 1
XL 38,256
M 85
XL 40,1024
M 86
M 87
W 2
M 88
WL 3
AN 120,122
)"
R"(AN 121,123
M 89
SH 122
SH 123
M 90
mw 3
AN 122,122
AN 123,123
M 91
WL 2
AN 124,126
AN 125,127
M 92
SH 126
SH 127
M 93
AN 126,126
AN 127,127
M 94
ad 84,1926
W 0
M 95
R 34
M 96
XL 42,1792
M 97
W 1
XL 44,2560
M 98
M 99
M 100
W 2
M 101
M 102
M 103
bc 49,21,66,60
M 104
mh 0
M 105
ad 66,4064
H 0
M 106
M 107
R 48
M 108
DG 1024
M 109
M 110
M 111
bc 48,21,64,56
M 112
mh 3
M 113
ad 64,4064
H 0
M 114
M 115
M 116
H 1
M 117
M 118
M 119
WL 2
R 50
M 120
H 2
M 121
M 132
M 133
PA 12288
TX 58,4608
TX 62,18432
DF
M 122
M 123
M 134
M 135
TY 66,5120
TY 70,18944
M 124
M 125
M 136
UL 6,30
UH 7,30
M 137
TX 74,6144
TX 78,19968
M 126
WL 4
Q 0
M 127
Q 2
M 138
Q 4
M 139
TY 82,6656
TY 86,20480
M 128
Q 6
M 129
WL 4
Q 8
M 140
Q 10
M 141
TX 90,7680
TX 94,21504
M 130
Q 12
M 131
R 52
M 142
Q 14
M 143
TY 98,8192
TY 102,22016
M 0
R 54
M 1
WL 10
Q 16
M 12
Q 18
M 13
Q 20
M 2
Q 22
M 3
WL 8
Q 24
M 14
Q 26
M 15
Q 28
M 4
Q 30
M 5
WL 6
Q 32
M 16
Q 34
M 17
Q 36
M 6
Q 38
M 7
WL 2
Q 40
M 18
Q 42
M 19
Q 44
M 8
Q 46
M 9
UL 8,31
UH 9,31
M 20
AD 164,6
AD 188,7
M 21
AD 212,8
AD 236,9
M 10
UL 6,32
UH 7,32
M 11
R 56
M 22
UL 8,33
UH 9,33
M 23
R 58
M 24
AD 165,6
AD 189,7
M 25
AD 213,8
AD 237,9
M 26
UL 6,34
UH 7,34
M 27
UL 8,35
UH 9,35
M 28
AD 166,6
AD 190,7
M 29
AD 214,8
AD 238,9
M 30
UL 6,36
UH 7,36
M 31
UL 8,37
UH 9,37
M 32
AD 167,6
AD 191,7
M 33
AD 215,8
AD 239,9
M 34
M 35
WL 2
M 36
M 37
M 38
M 39
M 40
M 41
M 42
M 43
WV 6
M 44
WL 0
M 45
s_barrier
R 72
M 46
mw 24
M 47
R 74
M 48
TY 106,9736
TY 110,23560
M 49
ad 84,1968
W 0
M 50
M 51
M 52
W 1
M 53
M 54
M 55
W 2
M 56
M 57
WL 1
AN 104,106
AN 105,107
M 58
mw 27
SH 106
SH 107
M 59
R 76
M 60
AN 106,106
AN 107,107
M 61
WL 2
AN 108,110
AN 109,111
M 62
SH 110
SH 111
M 63
AN 110,110
AN 111,111
M 64
TY 114,11272
TY 118,25096
M 65
ad 84,1974
M 66
W 0
M 67
M 68
M 69
W 1
M 70
M 71
R 78
M 72
W 2
M 73
WL 3
AN 112,114
AN 113,115
M 74
SH 114
SH 115
M 75
mw 48
AN 114,114
AN 115,115
M 76
WL 2
AN 116,118
AN 117,119
M 77
SH 118
SH 119
M 78
AN 118,118
AN 119,119
M 79
TY 122,12808
TY 126,26632
M 80
ad 84,2016
W 0
M 81
M 82
ae 44,68,384
M 83
R 80
M 84
s_bfe_u32 s51, s50, 0x10009
W 1
v_mad_i32_i24 v47, v49, s51, v48
M 85
XL 14,256
M 86
M 87
W 2
M 88
WL 3
AN 120,122
AN 121,123
M 89
SH 122
SH 123
M 90
mw 51
AN 122,122
AN 123,123
M 91
WL 2
AN 124,126
AN 125,127
M 92
SH 126
SH 127
M 93
AN 126,126
AN 127,127
M 94
ad 84,2022
W 0
M 95
R 82
M 96
XL 16,1024
M 97
W 1
XL 18,1792
M 98
M 99
M 100
W 2
XL 20,2560
M 101
M 102
M 103
bc 49,22,66,60
M 104
mh 9
M 105
ad 66,4256
H 0
M 106
M 107
R 0
M 108
DG 1024
M 109
M 110
M 111
bc 48,22,64,56
M 112
mh 12
M 113
ad 64,4256
H 0
M 114
M 115
M 116
H 1
M 117
M 118
M 119
WL 2
R 2
M 120
H 2
M 121
M 132
M 133
TX 58,9216
TX 62,23040
M 122
M 123
M 134
M 135
TY 66,9728
TY 70,23552
)"
R"(M 124
M 125
M 136
M 137
TX 74,10752
TX 78,24576
M 126
WL 4
Q 0
M 127
Q 2
M 138
Q 4
M 139
TY 82,11264
TY 86,25088
M 128
Q 6
M 129
WL 4
Q 8
M 140
Q 10
M 141
TX 90,12288
TX 94,26112
M 130
Q 12
M 131
R 4
M 142
Q 14
M 143
TY 98,12800
TY 102,26624
M 0
R 6
M 1
WL 10
Q 16
M 12
Q 18
M 13
Q 20
M 2
Q 22
M 3
WL 8
Q 24
M 14
Q 26
M 15
Q 28
M 4
Q 30
M 5
WL 6
Q 32
M 16
Q 34
M 17
Q 36
M 6
Q 38
M 7
WL 2
Q 40
M 18
Q 42
M 19
Q 44
M 8
Q 46
M 9
UL 6,38
UH 7,38
M 20
UL 8,39
UH 9,39
M 21
AD 168,6
AD 192,7
M 10
AD 216,8
AD 240,9
M 11
R 8
M 22
UL 6,40
UH 7,40
M 23
R 10
M 24
UL 8,41
UH 9,41
M 25
AD 169,6
AD 193,7
M 26
AD 217,8
AD 241,9
M 27
UL 6,42
UH 7,42
M 28
UL 8,43
UH 9,43
M 29
AD 170,6
AD 194,7
M 30
AD 218,8
AD 242,9
M 31
UL 6,44
UH 7,44
M 32
UL 8,45
UH 9,45
M 33
AD 171,6
AD 195,7
M 34
AD 219,8
AD 243,9
M 35
WL 2
M 36
M 37
M 38
M 39
M 40
M 41
M 42
M 43
WV 6
M 44
WL 0
M 45
s_barrier
R 24
M 46
mw 72
M 47
R 26
M 48
TY 106,520
TY 110,14344
M 49
ad 84,2064
W 0
M 50
M 51
M 52
W 1
M 53
M 54
M 55
W 2
M 56
M 57
WL 1
AN 104,106
AN 105,107
M 58
mw 75
SH 106
SH 107
M 59
R 28
M 60
AN 106,106
AN 107,107
M 61
WL 2
AN 108,110
AN 109,111
M 62
SH 110
SH 111
M 63
AN 110,110
AN 111,111
M 64
TY 114,2056
TY 118,15880
M 65
ad 84,2070
M 66
W 0
M 67
M 68
M 69
W 1
M 70
M 71
R 30
M 72
W 2
M 73
WL 3
AN 112,114
AN 113,115
M 74
SH 114
SH 115
M 75
mw 0
AN 114,114
AN 115,115
M 76
WL 2
AN 116,118
AN 117,119
M 77
SH 118
SH 119
M 78
AN 118,118
AN 119,119
M 79
TY 122,3592
TY 126,17416
M 80
ad 84,2112
W 0
M 81
M 82
M 83
R 32
M 84
W 1
M 85
M 86
ae 44,68,480
M 87
W 2
M 88
WL 3
AN 120,122
AN 121,123
M 89
SH 122
SH 123
M 90
mw 3
AN 122,122
AN 123,123
M 91
WL 2
AN 124,126
AN 125,127
M 92
SH 126
SH 127
M 93
AN 126,126
AN 127,127
M 94
ad 84,2118
W 0
M 95
R 34
M 96
s_bitcmp1_b32 s50, 10
s_cselect_b32 s51, 24576, 0
M 97
W 1
v_add_u32 v47, s51, v3
M 98
XL 22,256
M 99
XL 24,1024
M 100
W 2
XL 26,1792
M 101
M 102
M 103
bc 49,23,66,60
M 104
mh 18
XL 28,2560
M 105
ad 66,4448
H 0
M 106
M 107
R 48
M 108
DG 1024
M 109
M 110
M 111
bc 48,23,64,56
M 112
mh 21
M 113
ad 64,4448
H 0
M 114
M 115
M 116
H 1
M 117
M 118
M 119
WL 2
R 50
M 120
H 2
M 121
M 132
M 133
PA 12288
TX 58,0
TX 62,13824
DF
M 122
M 123
M 134
M 135
TY 66,512
TY 70,14336
M 124
M 125
M 136
M 137
TX 74,1536
TX 78,15360
M 126
WL 4
Q 0
M 127
Q 2
M 138
Q 4
M 139
TY 82,2048
TY 86,15872
M 128
Q 6
M 129
WL 4
Q 8
M 140
Q 10
M 141
TX 90,3072
TX 94,16896
M 130
Q 12
M 131
R 52
M 142
Q 14
M 143
TY 98,3584
TY 102,17408
M 0
R 54
M 1
WL 10
Q 16
M 12
Q 18
M 13
Q 20
M 2
Q 22
M 3
WL 8
Q 24
M 14
Q 26
M 15
Q 28
M 4
Q 30
M 5
WL 6
Q 32
M 16
Q 34
M 17
Q 36
M 6
Q 38
M 7
WL 2
Q 40
M 18
Q 42
M 19
Q 44
M 8
Q 46
M 9
UL 6,14
UH 7,14
)"
R"(M 20
UL 8,15
UH 9,15
M 21
AD 172,6
AD 196,7
M 10
AD 220,8
AD 244,9
M 11
R 56
M 22
UL 6,16
UH 7,16
M 23
R 58
M 24
UL 8,17
UH 9,17
M 25
AD 173,6
AD 197,7
M 26
AD 221,8
AD 245,9
M 27
UL 6,18
UH 7,18
M 28
UL 8,19
UH 9,19
M 29
AD 174,6
AD 198,7
M 30
AD 222,8
AD 246,9
M 31
WV 27
UL 6,20
UH 7,20
M 32
UL 8,21
UH 9,21
M 33
AD 175,6
AD 199,7
M 34
AD 223,8
AD 247,9
M 35
WL 2
M 36
M 37
M 38
M 39
M 40
M 41
M 42
M 43
WV 8
M 44
WL 0
M 45
s_barrier
R 72
M 46
mw 24
M 47
R 74
M 48
TY 106,5128
TY 110,18952
M 49
ad 84,2160
W 0
M 50
M 51
M 52
W 1
M 53
M 54
M 55
W 2
M 56
M 57
WL 1
AN 104,106
AN 105,107
M 58
mw 27
SH 106
SH 107
M 59
R 76
M 60
AN 106,106
AN 107,107
M 61
WL 2
AN 108,110
AN 109,111
M 62
SH 110
SH 111
M 63
AN 110,110
AN 111,111
M 64
TY 114,6664
TY 118,20488
M 65
ad 84,2166
M 66
W 0
M 67
M 68
M 69
W 1
M 70
M 71
R 78
M 72
W 2
M 73
WL 3
AN 112,114
AN 113,115
M 74
SH 114
SH 115
M 75
mw 48
AN 114,114
AN 115,115
M 76
WL 2
AN 116,118
AN 117,119
M 77
SH 118
SH 119
M 78
AN 118,118
AN 119,119
M 79
TY 122,8200
TY 126,22024
M 80
ad 84,2208
W 0
M 81
M 82
M 83
R 80
M 84
W 1
M 85
M 86
M 87
W 2
M 88
WL 3
AN 120,122
AN 121,123
M 89
SH 122
SH 123
M 90
mw 51
AN 122,122
AN 123,123
M 91
WL 2
AN 124,126
AN 125,127
M 92
SH 126
SH 127
M 93
AN 126,126
AN 127,127
M 94
ad 84,2214
W 0
M 95
R 82
M 96
ae 44,68,480
M 97
s_bfe_u32 s51, s50, 0x1000b
W 1
v_mad_i32_i24 v47, v49, s51, v48
M 98
XL 30,256
M 99
XL 32,1024
M 100
W 2
M 101
M 102
XL 34,1792
M 103
bc 49,0,66,60
M 104
mh 0
M 105
ad 66,64
H 0
M 106
M 107
R 0
M 108
DG 1024
M 109
XL 36,2560
M 110
M 111
bc 48,0,64,56
M 112
mh 3
M 113
ad 64,32
H 0
M 114
M 115
M 116
H 1
M 117
M 118
M 119
WL 2
R 2
M 120
H 2
M 121
M 132
M 133
TX 58,4608
TX 62,18432
M 122
M 123
M 134
M 135
TY 66,5120
TY 70,18944
M 124
M 125
M 136
M 137
TX 74,6144
TX 78,19968
M 126
WL 4
Q 0
M 127
Q 2
M 138
Q 4
M 139
TY 82,6656
TY 86,20480
M 128
Q 6
M 129
WL 4
Q 8
M 140
Q 10
M 141
TX 90,7680
TX 94,21504
M 130
Q 12
M 131
R 4
M 142
Q 14
M 143
TY 98,8192
TY 102,22016
M 0
R 6
M 1
WL 10
Q 16
M 12
Q 18
M 13
Q 20
M 2
Q 22
M 3
WL 8
Q 24
M 14
Q 26
M 15
Q 28
M 4
Q 30
M 5
WL 6
Q 32
M 16
Q 34
M 17
Q 36
M 6
Q 38
M 7
WL 2
Q 40
M 18
Q 42
M 19
Q 44
M 8
Q 46
M 9
UL 6,22
UH 7,22
M 20
UL 8,23
UH 9,23
M 21
AD 176,6
AD 200,7
M 10
AD 224,8
AD 248,9
M 11
R 8
M 22
UL 6,24
UH 7,24
M 23
R 10
M 24
UL 8,25
UH 9,25
M 25
AD 177,6
AD 201,7
M 26
AD 225,8
AD 249,9
M 27
WV 27
UL 6,26
UH 7,26
M 28
UL 8,27
UH 9,27
M 29
AD 178,6
AD 202,7
M 30
AD 226,8
AD 250,9
M 31
UL 6,28
UH 7,28
M 32
UL 8,29
UH 9,29
M 33
AD 179,6
AD 203,7
M 34
AD 227,8
AD 251,9
M 35
WL 2
M 36
M 37
M 38
M 39
M 40
M 41
M 42
M 43
WV 7
M 44
WL 0
M 45
s_barrier
R 24
M 46
mw 72
M 47
R 26
M 48
TY 106,9736
TY 110,23560
M 49
ad 84,2256
W 0
M 50
M 51
)"
R"(M 52
W 1
M 53
M 54
M 55
W 2
M 56
M 57
WL 1
AN 104,106
AN 105,107
M 58
mw 75
SH 106
SH 107
M 59
R 28
M 60
AN 106,106
AN 107,107
M 61
WL 2
AN 108,110
AN 109,111
M 62
SH 110
SH 111
M 63
AN 110,110
AN 111,111
M 64
TY 114,11272
TY 118,25096
M 65
ad 84,2262
M 66
W 0
M 67
M 68
M 69
W 1
M 70
M 71
R 30
M 72
W 2
M 73
WL 3
AN 112,114
AN 113,115
M 74
SH 114
SH 115
M 75
mw 0
AN 114,114
AN 115,115
M 76
WL 2
AN 116,118
AN 117,119
M 77
SH 118
SH 119
M 78
AN 118,118
AN 119,119
M 79
TY 122,12808
TY 126,26632
M 80
ad 84,0
W 0
M 81
M 82
M 83
R 32
M 84
W 1
M 85
M 86
M 87
W 2
M 88
WL 3
AN 120,122
AN 121,123
M 89
SH 122
SH 123
M 90
mw 3
AN 122,122
AN 123,123
M 91
WL 2
AN 124,126
AN 125,127
M 92
SH 126
SH 127
M 93
AN 126,126
AN 127,127
M 94
ad 84,6
W 0
M 95
R 34
M 96
M 97
W 1
M 98
M 99
M 100
W 2
M 101
M 102
M 103
bc 49,1,66,60
M 104
mh 9
M 105
ad 66,256
H 0
M 106
M 107
R 48
M 108
DG 1024
M 109
M 110
M 111
bc 48,1,64,56
M 112
mh 12
M 113
ad 64,256
H 0
M 114
M 115
M 116
DG 1024
M 117
M 118
M 119
WL 2
R 50
M 120
mh 15
M 121
ad 64,227
H 0
M 132
M 133
TX 58,9216
TX 62,23040
M 122
DG 1024
M 123
M 134
M 135
PA 12288
TY 66,9728
TY 70,23552
DF
M 124
M 125
M 136
M 137
TX 74,10752
TX 78,24576
M 126
WL 4
Q 0
M 127
Q 2
M 138
Q 4
M 139
TY 82,11264
TY 86,25088
M 128
Q 6
M 129
WL 4
Q 8
M 140
Q 10
M 141
TX 90,12288
TX 94,26112
M 130
Q 12
M 131
R 52
M 142
Q 14
M 143
TY 98,12800
TY 102,26624
M 0
R 54
M 1
WL 10
Q 16
M 12
Q 18
M 13
Q 20
M 2
Q 22
M 3
WL 8
Q 24
M 14
Q 26
M 15
Q 28
M 4
Q 30
M 5
WL 6
Q 32
M 16
Q 34
M 17
Q 36
M 6
Q 38
M 7
WL 2
Q 40
M 18
Q 42
M 19
Q 44
M 8
Q 46
M 9
UL 6,30
UH 7,30
M 20
UL 8,31
UH 9,31
M 21
AD 180,6
AD 204,7
M 10
AD 228,8
AD 252,9
M 11
R 56
M 22
UL 6,32
UH 7,32
M 23
R 58
M 24
UL 8,33
UH 9,33
M 25
AD 181,6
AD 205,7
M 26
AD 229,8
AD 253,9
BV 52,4608
M 27
WV 25
UL 6,34
UH 7,34
M 28
UL 8,35
UH 9,35
M 29
AD 182,6
AD 206,7
M 30
AD 230,8
AD 254,9
M 31
WV 22
UL 6,36
UH 7,36
M 32
UL 8,37
UH 9,37
M 33
AD 183,6
AD 207,7
M 34
AD 231,8
AD 255,9
M 35
WL 3
M 36
M 37
M 38
M 39
M 40
ds_read_b64 v[12:13], v4 offset:4864
ds_read_b64 v[46:47], v4 offset:4872
M 41
AR 6,36
AR 7,60
M 42
AR 8,84
AR 9,108
M 43
WL 2
EF 6,52
EF 7,53
M 44
WV 7
EF 8,54
EF 9,55
M 45
s_barrier
R 72
M 46
mw 24
CV 38,6,7
CV 39,8,9
M 47
R 74
M 48
TY 106,520
TY 110,14344
M 49
ad 84,48
W 0
M 50
AR 6,37
AR 7,61
M 51
AR 8,85
AR 9,109
M 52
W 1
EF 6,52
M 53
EF 7,53
EF 8,54
M 54
EF 9,55
CV 40,6,7
M 55
W 2
CV 41,8,9
M 56
AR 6,38
AR 7,62
M 57
WL 1
AN 104,106
AN 105,107
M 58
mw 27
SH 106
SH 107
M 59
R 76
M 60
AN 106,106
AN 107,107
M 61
WL 2
AN 108,110
AN 109,111
M 62
SH 110
SH 111
M 63
AN 110,110
AN 111,111
M 64
TY 114,2056
TY 118,15880
M 65
ad 84,54
M 66
W 0
AR 8,86
M 67
AR 9,110
EF 6,52
M 68
EF 7,53
EF 8,54
M 69
W 1
EF 9,55
M 70
CV 42,6,7
CV 43,8,9
M 71
R 78
M 72
W 2
)"
R"(AR 6,39
M 73
WL 3
AN 112,114
AN 113,115
M 74
SH 114
SH 115
M 75
mw 48
AN 114,114
AN 115,115
M 76
WL 2
AN 116,118
AN 117,119
M 77
SH 118
SH 119
M 78
AN 118,118
AN 119,119
M 79
TY 122,3592
TY 126,17416
M 80
ad 84,96
W 0
M 81
AR 7,63
AR 8,87
M 82
AR 9,111
EF 6,52
M 83
R 80
M 84
W 1
EF 7,53
M 85
EF 8,54
EF 9,55
M 86
CV 44,6,7
CV 45,8,9
M 87
W 2
ae 42,74,384
M 88
WL 3
AN 120,122
AN 121,123
M 89
SH 122
SH 123
M 90
mw 51
AN 122,122
AN 123,123
M 91
WL 2
AN 124,126
AN 125,127
M 92
SH 126
SH 127
M 93
AN 126,126
AN 127,127
M 94
ad 84,102
W 0
M 95
R 82
M 96
s_bitcmp1_b32 s50, 6
s_cselect_b32 s52, 24576, 0
v_add_u32 v50, s52, v3
M 97
W 1
ST 38,0
M 98
AR 6,40
AR 7,64
M 99
AR 8,88
AR 9,112
M 100
W 2
EF 6,52
M 101
EF 7,53
EF 8,54
M 102
EF 9,55
CV 14,6,7
M 103
bc 49,2,66,60
CV 15,8,9
M 104
mh 18
AR 6,41
AR 7,65
M 105
ad 66,448
H 0
M 106
AR 8,89
AR 9,113
M 107
R 0
M 108
DG 1024
M 109
ST 40,768
EF 6,52
M 110
EF 7,53
EF 8,54
M 111
bc 48,2,64,56
EF 9,55
M 112
mh 21
CV 16,6,7
CV 17,8,9
M 113
ad 64,448
H 0
M 114
AR 6,42
AR 7,66
M 115
AR 8,90
AR 9,114
M 116
H 1
EF 6,52
M 117
EF 7,53
EF 8,54
M 118
EF 9,55
CV 18,6,7
M 119
WL 0
R 2
M 120
H 2
CV 19,8,9
M 121
ST 42,1536
AR 6,43
M 132
AR 7,67
AR 8,91
M 133
TX 58,0
TX 62,13824
M 122
AR 9,115
EF 6,52
M 123
EF 7,53
EF 8,54
M 134
EF 9,55
CV 20,6,7
M 135
TY 66,512
TY 70,14336
M 124
CV 21,8,9
M 125
BV 52,5120
AR 6,132
M 136
AR 7,156
AR 8,180
M 137
TX 74,1536
TX 78,15360
M 126
WL 5
Q 0
M 127
Q 2
ST 44,2304
M 138
Q 4
M 139
TY 82,2048
TY 86,15872
M 128
Q 6
M 129
WL 5
Q 8
M 140
Q 10
M 141
TX 90,3072
TX 94,16896
M 130
Q 12
M 131
R 4
M 142
Q 14
M 143
TY 98,3584
TY 102,17408
v_mfma_f32_16x16x32_f16 a[36:39], v[56:59], a[0:3], a[32:35]
R 6
M 1
WL 10
Q 16
v_mfma_f32_16x16x32_f16 a[60:63], v[56:59], a[8:11], a[32:35]
Q 18
M 13
Q 20
v_mfma_f32_16x16x32_f16 a[40:43], v[64:67], a[0:3], a[32:35]
Q 22
M 3
WL 8
Q 24
v_mfma_f32_16x16x32_f16 a[64:67], v[64:67], a[8:11], a[32:35]
Q 26
M 15
Q 28
M 4
Q 30
M 5
WL 6
Q 32
M 16
Q 34
M 17
Q 36
M 6
Q 38
M 7
WL 2
Q 40
M 18
Q 42
M 19
Q 44
M 8
Q 46
M 9
ae 42,74,384
M 20
s_bfe_u32 s52, s50, 0x10007
v_mad_i32_i24 v50, v49, s52, v48
ST 14,0
M 21
EF 6,12
EF 7,13
M 10
EF 8,46
EF 136,47
M 11
R 8
M 22
CV 22,6,7
CV 23,8,136
M 23
R 10
v_mfma_f32_16x16x32_f16 a[84:87], v[56:59], a[16:19], a[32:35]
AR 6,133
AR 7,157
M 25
AR 8,181
EF 6,12
v_mfma_f32_16x16x32_f16 a[88:91], v[64:67], a[16:19], a[32:35]
EF 7,13
EF 8,46
M 27
EF 137,47
CV 24,6,7
M 28
CV 25,8,137
AR 6,134
M 29
AR 7,158
AR 8,182
M 30
ST 16,768
EF 6,12
M 31
EF 7,13
EF 8,46
M 32
EF 138,47
CV 26,6,7
M 33
CV 27,8,138
AR 6,135
M 34
AR 7,159
AR 8,183
M 35
WL 2
EF 6,12
EF 7,13
v_mfma_f32_16x16x32_f16 a[108:111], v[56:59], a[24:27], a[32:35]
EF 8,46
EF 139,47
M 37
CV 28,6,7
CV 29,8,139
v_mfma_f32_16x16x32_f16 a[112:115], v[64:67], a[24:27], a[32:35]
AR 6,136
AR 7,160
AR 8,184
EF 6,12
M 39
EF 7,13
EF 8,46
EF 140,47
CV 30,6,7
M 40
CV 31,8,140
AR 6,137
AR 7,161
AR 8,185
M 41
EF 6,12
EF 7,13
EF 8,46
EF 141,47
M 42
ST 18,1536
CV 32,6,7
CV 33,8,141
AR 6,138
M 43
WV 11
AR 7,162
AR 8,186
EF 6,12
EF 7,13
M 44
WL 0
EF 8,46
EF 142,47
CV 34,6,7
CV 35,8,142
M 45
s_barrier
R 24
AR 6,139
AR 7,163
M 46
mw 72
AR 8,187
EF 6,12
EF 7,13
EF 8,46
M 47
R 26
EF 143,47
CV 36,6,7
v_mfma_f32_16x16x32_f16 a[132:135], v[56:59], a[0:3], a[32:35]
TY 106,5128
TY 110,18952
CV 37,8,143
M 49
ad 84,144
W 0
ds_read_b64 v[12:13], v4 offset:4608
v_mfma_f32_16x16x32_f16 a[136:139], v[64:67], a[0:3], a[32:35]
ds_read_b64 v[46:47], v4 offset:4616
EF 160,52
EF 184,53
M 51
EF 208,54
EF 232,55
CV 38,160,184
M 52
W 1
CV 39,208,232
EF 161,52
M 53
EF 185,53
EF 209,54
EF 233,55
M 54
ST 20,2304
CV 40,161,185
CV 41,209,233
M 55
W 2
EF 162,52
EF 186,53
M 56
EF 210,54
EF 234,55
CV 42,162,186
)"
R"(M 57
WL 3
AN 104,106
AN 105,107
CV 43,210,234
M 58
mw 75
SH 106
SH 107
EF 163,52
M 59
R 28
EF 187,53
v_mfma_f32_16x16x32_f16 a[156:159], v[56:59], a[8:11], a[32:35]
AN 106,106
AN 107,107
EF 211,54
M 61
WL 2
AN 108,110
AN 109,111
EF 235,55
v_mfma_f32_16x16x32_f16 a[160:163], v[64:67], a[8:11], a[32:35]
SH 110
SH 111
CV 44,163,187
M 63
AN 110,110
AN 111,111
CV 45,211,235
M 64
TY 114,6664
TY 118,20488
EF 164,52
M 65
ad 84,150
EF 188,53
M 66
W 0
EF 212,54
EF 236,55
M 67
ae 42,74,384
CV 14,164,188
CV 15,212,236
M 68
s_bitcmp1_b32 s50, 6
s_cselect_b32 s52, 24576, 0
v_add_u32 v50, s52, v3
ST 22,128
M 69
W 1
EF 165,52
EF 189,53
M 70
EF 213,54
EF 237,55
CV 16,165,189
M 71
R 30
CV 17,213,237
v_mfma_f32_16x16x32_f16 a[180:183], v[56:59], a[16:19], a[32:35]
W 2
EF 166,52
EF 190,53
M 73
WL 3
AN 112,114
AN 113,115
EF 214,54
v_mfma_f32_16x16x32_f16 a[184:187], v[64:67], a[16:19], a[32:35]
SH 114
SH 115
EF 238,55
M 75
mw 0
AN 114,114
AN 115,115
CV 18,166,190
M 76
WL 2
AN 116,118
AN 117,119
CV 19,214,238
M 77
SH 118
SH 119
EF 167,52
M 78
AN 118,118
AN 119,119
EF 191,53
M 79
TY 122,8200
TY 126,22024
EF 215,54
M 80
ad 84,192
W 0
ST 24,896
M 81
EF 239,55
CV 20,167,191
CV 21,215,239
M 82
BV 52,4864
AR 6,44
M 83
R 32
v_mfma_f32_16x16x32_f16 v[136:139], v[56:59], a[24:27], v[128:131]
W 1
AR 7,68
M 85
AR 8,92
AR 9,116
v_mfma_f32_16x16x32_f16 v[140:143], v[64:67], a[24:27], v[128:131]
EF 6,12
EF 7,13
M 87
W 2
EF 8,46
M 88
WL 4
AN 120,122
AN 121,123
M 89
SH 122
SH 123
M 90
mw 3
AN 122,122
AN 123,123
M 91
WL 3
AN 124,126
AN 125,127
M 92
SH 126
SH 127
ST 26,1664
M 93
AN 126,126
AN 127,127
M 94
ad 84,198
W 0
M 95
R 34
v_mfma_f32_16x16x32_f16 v[160:163], v[56:59], a[0:3], v[128:131]
EF 9,47
CV 10,6,7
M 97
W 1
CV 11,8,9
v_mfma_f32_16x16x32_f16 v[164:167], v[64:67], a[0:3], v[128:131]
AW 200,10
AW 201,11
M 99
AR 6,45
AR 7,69
M 100
W 2
AR 8,93
M 101
AR 9,117
EF 6,12
M 102
EF 7,13
EF 8,46
M 103
bc 49,3,66,60
EF 9,47
M 104
mh 0
ST 28,2432
CV 10,6,7
M 105
ad 66,640
H 0
M 106
CV 11,8,9
AW 202,10
M 107
R 48
v_mfma_f32_16x16x32_f16 v[184:187], v[56:59], a[8:11], v[128:131]
DG 1024
M 109
AW 203,11
AR 6,46
v_mfma_f32_16x16x32_f16 v[188:191], v[64:67], a[8:11], v[128:131]
AR 7,70
AR 8,94
M 111
bc 48,3,64,56
AR 9,118
M 112
mh 3
EF 6,12
EF 7,13
M 113
ad 64,640
H 0
M 114
EF 8,46
EF 9,47
M 115
CV 10,6,7
CV 11,8,9
M 116
H 1
AW 204,10
M 117
ae 42,74,384
AW 205,11
M 118
s_bfe_u32 s52, s50, 0x10007
v_mad_i32_i24 v50, v49, s52, v48
ST 30,128
M 119
WL 0
R 50
v_mfma_f32_16x16x32_f16 v[208:211], v[56:59], a[16:19], v[128:131]
H 2
AR 6,47
M 121
AR 7,71
AR 8,95
v_mfma_f32_16x16x32_f16 v[232:235], v[56:59], a[24:27], v[128:131]
AR 9,119
EF 6,12
M 133
PA 12288
TX 58,4608
TX 62,18432
DF
v_mfma_f32_16x16x32_f16 v[212:215], v[64:67], a[16:19], v[128:131]
EF 7,13
EF 8,46
M 123
EF 9,47
CV 10,6,7
v_mfma_f32_16x16x32_f16 v[236:239], v[64:67], a[24:27], v[128:131]
CV 11,8,9
AW 206,10
M 135
TY 66,5120
TY 70,18944
M 124
AW 207,11
M 125
AR 6,48
AR 7,72
AR 8,96
AR 9,120
EF 6,12
EF 7,13
M 136
ST 32,896
EF 8,46
EF 9,47
CV 22,6,7
CV 23,8,9
AR 6,49
M 137
TX 74,6144
TX 78,19968
AR 7,73
AR 8,97
AR 9,121
EF 6,12
M 126
WL 4
Q 0
EF 7,13
EF 8,46
EF 9,47
CV 24,6,7
M 127
Q 2
CV 25,8,9
AR 6,50
AR 7,74
AR 8,98
M 138
Q 4
AR 9,122
EF 6,12
EF 7,13
EF 8,46
M 139
TY 82,6656
TY 86,20480
EF 9,47
CV 26,6,7
CV 27,8,9
AR 6,51
M 128
Q 6
AR 7,75
AR 8,99
AR 9,123
EF 6,12
M 129
WL 2
Q 8
EF 7,13
EF 8,46
EF 9,47
CV 28,6,7
M 140
Q 10
CV 29,8,9
M 141
TX 90,7680
TX 94,21504
ds_read_b64 v[12:13], v4 offset:5120
M 130
Q 12
ds_read_b64 v[46:47], v4 offset:5128
M 131
R 52
M 142
Q 14
ST 34,1664
M 143
TY 98,8192
TY 102,22016
M 0
R 54
M 1
Q 16
M 12
Q 18
M 13
Q 20
M 2
Q 22
M 3
WL 10
Q 24
M 14
Q 26
M 15
Q 28
v_mfma_f32_16x16x32_f16 a[44:47], v[72:75], a[0:3], a[32:35]
Q 30
M 5
WL 6
Q 32
v_mfma_f32_16x16x32_f16 a[68:71], v[72:75], a[8:11], a[32:35]
Q 34
ST 36,2432
M 17
Q 36
v_mfma_f32_16x16x32_f16 a[48:51], v[80:83], a[0:3], a[32:35]
Q 38
M 7
WL 1
Q 40
v_mfma_f32_16x16x32_f16 a[72:75], v[80:83], a[8:11], a[32:35]
Q 42
M 19
Q 44
M 8
Q 46
M 9
AR 6,140
AR 7,164
M 20
AR 8,188
EF 6,52
M 21
EF 7,53
EF 8,54
M 10
EF 144,55
CV 10,6,7
M 11
R 56
M 22
ae 42,74,384
CV 11,8,144
M 23
R 58
M 24
s_bitcmp1_b32 s50, 6
s_cselect_b32 s52, 24576, 0
v_add_u32 v50, s52, v3
ST 38,256
M 25
AW 208,10
AW 209,11
M 26
AR 6,141
AR 7,165
M 27
AR 8,189
EF 6,52
v_mfma_f32_16x16x32_f16 a[92:95], v[72:75], a[16:19], a[32:35]
EF 7,53
)"
R"(EF 8,54
M 29
EF 145,55
CV 10,6,7
v_mfma_f32_16x16x32_f16 a[96:99], v[80:83], a[16:19], a[32:35]
CV 11,8,145
AW 210,10
M 31
AW 211,11
AR 6,142
M 32
AR 7,166
AR 8,190
M 33
EF 6,52
EF 7,53
M 34
EF 8,54
EF 146,55
M 35
WL 0
CV 10,6,7
CV 11,8,146
M 36
ST 40,1024
AW 212,10
M 37
AW 213,11
AR 6,143
M 38
AR 7,167
AR 8,191
M 39
EF 6,52
EF 7,53
v_mfma_f32_16x16x32_f16 a[116:119], v[72:75], a[24:27], a[32:35]
EF 8,54
EF 147,55
M 41
CV 10,6,7
CV 11,8,147
v_mfma_f32_16x16x32_f16 a[120:123], v[80:83], a[24:27], a[32:35]
AW 214,10
AW 215,11
M 43
AR 6,144
AR 7,168
AR 8,192
EF 6,52
EF 7,53
EF 8,54
M 44
WV 13
EF 148,55
CV 30,6,7
CV 31,8,148
AR 6,145
AR 7,169
AR 8,193
M 45
s_barrier
R 72
EF 6,52
EF 7,53
EF 8,54
EF 149,55
M 46
mw 24
CV 32,6,7
CV 33,8,149
AR 6,146
AR 7,170
AR 8,194
EF 6,52
M 47
R 74
EF 7,53
EF 8,54
EF 150,55
CV 34,6,7
M 48
TY 106,9736
TY 110,23560
ST 42,1792
CV 35,8,150
AR 6,147
AR 7,171
M 49
ad 84,240
W 0
AR 8,195
EF 6,52
EF 7,53
EF 8,54
M 50
EF 151,55
CV 36,6,7
CV 37,8,151
M 51
BV 52,4608
EF 168,12
EF 192,13
v_mfma_f32_16x16x32_f16 a[140:143], v[72:75], a[0:3], a[32:35]
W 1
EF 216,46
EF 240,47
M 53
CV 10,168,192
CV 11,216,240
AW 216,10
v_mfma_f32_16x16x32_f16 a[144:147], v[80:83], a[0:3], a[32:35]
AW 217,11
EF 169,12
EF 193,13
M 55
W 2
EF 217,46
EF 241,47
M 56
CV 10,169,193
CV 11,217,241
AW 218,10
M 57
WL 1
AN 104,106
AN 105,107
AW 219,11
M 58
mw 27
SH 106
SH 107
EF 170,12
M 59
R 76
EF 194,13
M 60
AN 106,106
AN 107,107
ST 44,2560
M 61
AN 108,110
AN 109,111
EF 218,46
M 62
SH 110
SH 111
EF 242,47
M 63
AN 110,110
AN 111,111
CV 10,170,194
v_mfma_f32_16x16x32_f16 a[164:167], v[72:75], a[8:11], a[32:35]
TY 114,11272
TY 118,25096
CV 11,218,242
M 65
ad 84,246
AW 220,10
v_mfma_f32_16x16x32_f16 a[168:171], v[80:83], a[8:11], a[32:35]
W 0
AW 221,11
EF 171,12
M 67
EF 195,13
EF 219,46
EF 243,47
M 68
CV 10,171,195
CV 11,219,243
AW 222,10
M 69
W 1
AW 223,11
M 70
EF 172,12
EF 196,13
EF 220,46
M 71
R 78
EF 244,47
M 72
WL 3
W 2
ae 42,74,384
M 73
s_bfe_u32 s52, s50, 0x10007
AN 112,114
AN 113,115
v_mad_i32_i24 v50, v49, s52, v48
M 74
SH 114
SH 115
ST 14,256
M 75
mw 48
AN 114,114
AN 115,115
CV 38,172,196
v_mfma_f32_16x16x32_f16 a[188:191], v[72:75], a[16:19], a[32:35]
WL 2
AN 116,118
AN 117,119
CV 39,220,244
M 77
SH 118
SH 119
EF 173,12
v_mfma_f32_16x16x32_f16 a[192:195], v[80:83], a[16:19], a[32:35]
AN 118,118
AN 119,119
EF 197,13
M 79
TY 122,12808
TY 126,26632
EF 221,46
M 80
ad 84,288
W 0
EF 245,47
M 81
CV 40,173,197
CV 41,221,245
EF 174,12
M 82
EF 198,13
EF 222,46
EF 246,47
M 83
R 80
CV 42,174,198
M 84
W 1
CV 43,222,246
EF 175,12
M 85
EF 199,13
EF 223,46
EF 247,47
M 86
ST 16,1024
CV 44,175,199
CV 45,223,247
M 87
W 2
ds_read_b64 v[12:13], v4 offset:4864
ds_read_b64 v[46:47], v4 offset:4872
v_mfma_f32_16x16x32_f16 v[144:147], v[72:75], a[24:27], v[128:131]
WL 5
AN 120,122
AN 121,123
AR 6,52
M 89
SH 122
SH 123
AR 7,76
v_mfma_f32_16x16x32_f16 v[148:151], v[80:83], a[24:27], v[128:131]
mw 51
AN 122,122
AN 123,123
AR 8,100
M 91
WL 4
AN 124,126
AN 125,127
AR 9,124
M 92
SH 126
SH 127
EF 6,52
M 93
AN 126,126
AN 127,127
EF 7,53
M 94
ad 84,294
W 0
EF 8,54
M 95
R 82
EF 9,55
M 96
CV 10,6,7
CV 11,8,9
AW 224,10
M 97
W 1
AW 225,11
AR 6,53
M 98
ST 18,1792
AR 7,77
AR 8,101
M 99
AR 9,125
EF 6,52
EF 7,53
v_mfma_f32_16x16x32_f16 v[168:171], v[72:75], a[0:3], v[128:131]
W 2
EF 8,54
EF 9,55
M 101
CV 10,6,7
CV 11,8,9
AW 226,10
v_mfma_f32_16x16x32_f16 v[172:175], v[80:83], a[0:3], v[128:131]
AW 227,11
AR 6,54
AR 7,78
M 103
bc 49,4,66,60
AR 8,102
AR 9,126
M 104
mh 9
EF 6,52
EF 7,53
EF 8,54
M 105
ad 66,832
H 0
EF 9,55
M 106
CV 10,6,7
CV 11,8,9
AW 228,10
M 107
R 0
AW 229,11
M 108
DG 1024
AR 6,55
M 109
AR 7,79
AR 8,103
AR 9,127
M 110
ST 20,2560
EF 6,52
EF 7,53
M 111
bc 48,4,64,56
EF 8,54
EF 9,55
v_mfma_f32_16x16x32_f16 v[192:195], v[72:75], a[8:11], v[128:131]
mh 12
CV 10,6,7
CV 11,8,9
AW 230,10
M 113
ad 64,832
H 0
AW 231,11
v_mfma_f32_16x16x32_f16 v[196:199], v[80:83], a[8:11], v[128:131]
AR 6,56
AR 7,80
AR 8,104
M 115
AR 9,128
EF 6,52
EF 7,53
M 116
H 1
EF 8,54
EF 9,55
M 117
CV 14,6,7
CV 15,8,9
AR 6,57
M 118
AR 7,81
AR 8,105
AR 9,129
M 119
WL 0
R 2
EF 6,52
M 120
H 2
EF 7,53
EF 8,54
M 121
EF 9,55
CV 16,6,7
CV 17,8,9
M 132
ae 42,72,384
AR 6,58
AR 7,82
M 133
s_bfe_u32 s52, s50, 0x10009
TX 58,9216
TX 62,23040
v_mad_i32_i24 v50, v49, s52, v48
M 122
ST 22,0
AR 8,106
AR 9,130
M 123
EF 6,52
EF 7,53
)"
R"(EF 8,54
M 134
EF 9,55
CV 18,6,7
CV 19,8,9
M 135
TY 66,9728
TY 70,23552
AR 6,59
v_mfma_f32_16x16x32_f16 v[216:219], v[72:75], a[16:19], v[128:131]
AR 7,83
AR 8,107
AR 9,131
M 125
EF 6,52
EF 7,53
EF 8,54
v_mfma_f32_16x16x32_f16 v[240:243], v[72:75], a[24:27], v[128:131]
EF 9,55
CV 20,6,7
CV 21,8,9
M 137
TX 74,10752
TX 78,24576
BV 52,5120
v_mfma_f32_16x16x32_f16 v[220:223], v[80:83], a[16:19], v[128:131]
WL 5
Q 0
M 127
Q 2
v_mfma_f32_16x16x32_f16 v[244:247], v[80:83], a[24:27], v[128:131]
Q 4
M 139
TY 82,11264
TY 86,25088
M 128
Q 6
ST 24,768
M 129
WL 5
Q 8
M 140
Q 10
M 141
TX 90,12288
TX 94,26112
M 130
Q 12
M 131
R 4
M 142
Q 14
M 143
TY 98,12800
TY 102,26624
M 0
R 6
M 1
WL 10
Q 16
M 12
Q 18
M 13
Q 20
M 2
Q 22
ST 26,1536
M 3
WL 8
Q 24
M 14
Q 26
M 15
Q 28
M 4
Q 30
M 5
WL 6
Q 32
M 16
Q 34
M 17
Q 36
M 6
Q 38
M 7
WL 1
Q 40
M 18
Q 42
M 19
Q 44
v_mfma_f32_16x16x32_f16 a[52:55], v[88:91], a[0:3], a[32:35]
Q 46
ST 28,2304
M 9
AR 6,148
AR 7,172
v_mfma_f32_16x16x32_f16 a[76:79], v[88:91], a[8:11], a[32:35]
AR 8,196
EF 6,12
M 21
EF 7,13
EF 8,46
v_mfma_f32_16x16x32_f16 a[56:59], v[96:99], a[0:3], a[32:35]
EF 152,47
CV 10,6,7
M 11
R 8
v_mfma_f32_16x16x32_f16 a[80:83], v[96:99], a[8:11], a[32:35]
CV 11,8,152
AW 232,10
M 23
R 10
M 24
AW 233,11
AR 6,149
M 25
AR 7,173
AR 8,197
M 26
EF 6,12
EF 7,13
M 27
EF 8,46
EF 153,47
M 28
ae 42,72,384
ST 30,128
M 29
CV 10,6,7
CV 11,8,153
M 30
AW 234,10
AW 235,11
M 31
AR 6,150
AR 7,174
v_mfma_f32_16x16x32_f16 a[100:103], v[88:91], a[16:19], a[32:35]
AR 8,198
EF 6,12
M 33
EF 7,13
EF 8,46
v_mfma_f32_16x16x32_f16 a[104:107], v[96:99], a[16:19], a[32:35]
EF 154,47
CV 10,6,7
M 35
WL 1
CV 11,8,154
AW 236,10
M 36
AW 237,11
AR 6,151
M 37
AR 7,175
AR 8,199
M 38
EF 6,12
EF 7,13
M 39
EF 8,46
EF 155,47
M 40
ST 32,896
CV 10,6,7
M 41
CV 11,8,155
AW 238,10
M 42
AW 239,11
M 43
WV 12
AR 6,152
AR 7,176
EF 6,12
EF 7,13
v_mfma_f32_16x16x32_f16 a[124:127], v[88:91], a[24:27], a[32:35]
WL 0
EF 132,46
EF 156,47
CV 22,6,7
CV 23,132,156
M 45
s_barrier
R 24
AR 6,153
AR 7,177
v_mfma_f32_16x16x32_f16 a[128:131], v[96:99], a[24:27], a[32:35]
mw 72
EF 6,12
EF 7,13
EF 133,46
EF 157,47
M 47
R 26
CV 24,6,7
CV 25,133,157
M 48
TY 106,520
TY 110,14344
AR 6,154
AR 7,178
M 49
ad 84,336
W 0
EF 6,12
EF 7,13
M 50
EF 134,46
EF 158,47
CV 26,6,7
CV 27,134,158
M 51
AR 6,155
AR 7,179
EF 6,12
EF 7,13
M 52
W 1
ST 34,1664
EF 135,46
EF 159,47
M 53
CV 28,6,7
CV 29,135,159
M 54
EF 176,52
EF 200,53
EF 224,54
M 55
W 2
EF 248,55
CV 10,176,200
v_mfma_f32_16x16x32_f16 a[148:151], v[88:91], a[0:3], a[32:35]
CV 11,224,248
AW 240,10
AW 241,11
M 57
WL 0
AN 104,106
AN 105,107
EF 177,52
v_mfma_f32_16x16x32_f16 a[152:155], v[96:99], a[0:3], a[32:35]
mw 75
SH 106
SH 107
EF 201,53
M 59
R 28
EF 225,54
M 60
AN 106,106
AN 107,107
EF 249,55
M 61
AN 108,110
AN 109,111
CV 10,177,201
M 62
SH 110
SH 111
CV 11,225,249
M 63
AN 110,110
AN 111,111
AW 242,10
M 64
TY 114,2056
TY 118,15880
ST 36,2432
M 65
ad 84,342
AW 243,11
M 66
W 0
EF 178,52
EF 202,53
M 67
EF 226,54
EF 250,55
CV 10,178,202
v_mfma_f32_16x16x32_f16 a[172:175], v[88:91], a[8:11], a[32:35]
CV 11,226,250
AW 244,10
AW 245,11
M 69
W 1
EF 179,52
EF 203,53
v_mfma_f32_16x16x32_f16 a[176:179], v[96:99], a[8:11], a[32:35]
EF 227,54
EF 251,55
CV 10,179,203
M 71
R 30
CV 11,227,251
M 72
W 2
AW 246,10
AW 247,11
M 73
WL 3
AN 112,114
AN 113,115
EF 180,52
M 74
SH 114
SH 115
EF 204,53
M 75
mw 0
AN 114,114
AN 115,115
EF 228,54
M 76
WL 2
AN 116,118
AN 117,119
EF 252,55
M 77
SH 118
SH 119
CV 10,180,204
M 78
AN 118,118
AN 119,119
CV 11,228,252
M 79
TY 122,3592
TY 126,17416
AW 248,10
v_mfma_f32_16x16x32_f16 a[196:199], v[88:91], a[16:19], a[32:35]
ad 84,384
W 0
AW 249,11
M 81
ae 42,72,384
ST 38,256
EF 181,52
v_mfma_f32_16x16x32_f16 v[132:135], v[96:99], a[16:19], v[128:131]
EF 205,53
EF 229,54
EF 253,55
M 83
R 32
CV 10,181,205
M 84
W 1
CV 11,229,253
AW 250,10
M 85
AW 251,11
EF 182,52
EF 206,53
M 86
EF 230,54
EF 254,55
CV 10,182,206
M 87
W 2
CV 11,230,254
AW 252,10
M 88
WL 3
AN 120,122
AN 121,123
AW 253,11
M 89
SH 122
SH 123
EF 183,52
M 90
mw 3
AN 122,122
AN 123,123
EF 207,53
M 91
WL 2
AN 124,126
AN 125,127
EF 231,54
v_mfma_f32_16x16x32_f16 v[152:155], v[88:91], a[24:27], v[128:131]
SH 126
SH 127
EF 255,55
M 93
AN 126,126
AN 127,127
ST 40,1024
v_mfma_f32_16x16x32_f16 v[156:159], v[96:99], a[24:27], v[128:131]
ad 84,390
W 0
CV 10,183,207
M 95
R 34
CV 11,231,255
M 96
AW 254,10
AW 255,11
)"
R"(M 97
W 1
M 98
M 99
M 100
W 2
M 101
M 102
M 103
bc 49,5,66,60
v_mfma_f32_16x16x32_f16 v[176:179], v[88:91], a[0:3], v[128:131]
mh 18
M 105
ad 66,1024
H 0
ST 42,1792
v_mfma_f32_16x16x32_f16 v[180:183], v[96:99], a[0:3], v[128:131]
M 107
R 48
M 108
DG 1024
M 109
M 110
M 111
bc 48,5,64,56
M 112
mh 21
M 113
ad 64,1024
H 0
M 114
M 115
v_mfma_f32_16x16x32_f16 v[200:203], v[88:91], a[8:11], v[128:131]
H 1
M 117
ST 44,2560
v_mfma_f32_16x16x32_f16 v[204:207], v[96:99], a[8:11], v[128:131]
M 119
WL 2
R 50
M 120
H 2
M 121
M 132
M 133
PA 12288
TX 58,0
TX 62,13824
DF
M 122
M 123
M 134
M 135
TY 66,512
TY 70,14336
M 124
M 125
ae 42,72,480
M 136
s_bfe_u32 s52, s50, 0x1000b
v_mad_i32_i24 v50, v49, s52, v48
ST 14,0
M 137
TX 74,1536
TX 78,15360
M 126
WL 4
Q 0
M 127
Q 2
M 138
Q 4
M 139
TY 82,2048
TY 86,15872
v_mfma_f32_16x16x32_f16 v[224:227], v[88:91], a[16:19], v[128:131]
Q 6
M 129
WL 4
Q 8
v_mfma_f32_16x16x32_f16 v[248:251], v[88:91], a[24:27], v[128:131]
Q 10
M 141
TX 90,3072
TX 94,16896
v_mfma_f32_16x16x32_f16 v[228:231], v[96:99], a[16:19], v[128:131]
Q 12
M 131
R 52
v_mfma_f32_16x16x32_f16 v[252:255], v[96:99], a[24:27], v[128:131]
Q 14
ST 16,768
M 143
TY 98,3584
TY 102,17408
M 0
R 54
M 1
WL 10
Q 16
M 12
Q 18
M 13
Q 20
M 2
Q 22
M 3
WL 8
Q 24
M 14
Q 26
M 15
Q 28
M 4
Q 30
M 5
WL 6
Q 32
M 16
Q 34
ST 18,1536
M 17
Q 36
M 6
Q 38
M 7
WL 1
Q 40
M 18
Q 42
M 19
Q 44
M 8
Q 46
M 9
M 20
M 21
M 10
M 11
R 56
M 22
ST 20,2304
M 23
R 58
M 24
M 25
M 26
M 27
M 28
M 29
M 30
M 31
M 32
M 33
M 34
ae 42,72,480
ST 22,128
M 35
WL 4
M 36
M 37
M 38
M 39
M 40
M 41
M 42
M 43
WV 13
M 44
WL 0
M 45
s_barrier
R 72
M 46
mw 24
ST 24,896
M 47
R 74
M 48
TY 106,5128
TY 110,18952
M 49
ad 84,432
W 0
M 50
M 51
M 52
W 1
M 53
M 54
M 55
W 2
M 56
M 57
WL 1
AN 104,106
AN 105,107
M 58
mw 27
SH 106
SH 107
ST 26,1664
M 59
R 76
M 60
AN 106,106
AN 107,107
M 61
WL 2
AN 108,110
AN 109,111
M 62
SH 110
SH 111
M 63
AN 110,110
AN 111,111
M 64
TY 114,6664
TY 118,20488
M 65
ad 84,438
M 66
W 0
M 67
M 68
M 69
W 1
M 70
ST 28,2432
M 71
R 78
M 72
W 2
M 73
WL 3
AN 112,114
AN 113,115
M 74
SH 114
SH 115
M 75
mw 48
AN 114,114
AN 115,115
M 76
WL 2
AN 116,118
AN 117,119
M 77
SH 118
SH 119
M 78
AN 118,118
AN 119,119
M 79
TY 122,8200
TY 126,22024
M 80
ad 84,480
W 0
M 81
M 82
M 83
R 80
M 84
W 1
M 85
M 86
M 87
W 2
M 88
WL 3
AN 120,122
AN 121,123
M 89
SH 122
SH 123
M 90
mw 51
AN 122,122
AN 123,123
M 91
WL 2
AN 124,126
AN 125,127
M 92
SH 126
SH 127
M 93
AN 126,126
AN 127,127
M 94
ad 84,486
W 0
M 95
R 82
M 96
M 97
W 1
M 98
M 99
M 100
W 2
M 101
M 102
M 103
bc 49,6,66,60
M 104
mh 0
M 105
ad 66,1216
H 0
M 106
M 107
R 0
M 108
DG 1024
M 109
M 110
M 111
bc 48,6,64,56
M 112
mh 3
M 113
ad 64,1216
H 0
M 114
M 115
M 116
H 1
M 117
M 118
M 119
WL 2
R 2
M 120
H 2
M 121
M 132
M 133
TX 58,4608
TX 62,18432
M 122
M 123
M 134
M 135
TY 66,5120
TY 70,18944
M 124
ae 44,70,768
M 125
s_bitcmp1_b32 s50, 12
s_cselect_b32 s51, 24576, 0
v_add_u32 v47, s51, v3
M 136
XL 30,0
M 137
TX 74,6144
TX 78,19968
M 126
WL 4
Q 0
M 127
Q 2
M 138
Q 4
M 139
TY 82,6656
TY 86,20480
M 128
Q 6
M 129
WL 4
Q 8
M 140
Q 10
M 141
TX 90,7680
TX 94,21504
M 130
Q 12
M 131
R 4
M 142
Q 14
M 143
TY 98,8192
TY 102,22016
M 0
R 6
M 1
WL 10
Q 16
M 12
Q 18
M 13
Q 20
M 2
Q 22
)"
R"(M 3
WL 8
Q 24
M 14
Q 26
M 15
Q 28
M 4
Q 30
M 5
WL 6
Q 32
M 16
Q 34
M 17
Q 36
M 6
Q 38
M 7
WL 2
Q 40
M 18
Q 42
M 19
Q 44
M 8
Q 46
M 9
ae 42,72,384
M 20
s_bitcmp1_b32 s50, 8
s_cselect_b32 s52, 24576, 0
v_add_u32 v50, s52, v3
M 21
SA 200,0
XL 32,768
M 10
XL 34,1536
M 11
R 8
M 22
XL 36,2304
M 23
R 10
M 24
M 25
M 26
M 27
M 28
M 29
M 30
M 31
M 32
M 33
M 34
M 35
WL 4
M 36
M 37
M 38
M 39
M 40
M 41
M 42
M 43
WV 10
M 44
WL 0
M 45
s_barrier
R 24
M 46
mw 72
M 47
R 26
M 48
TY 106,9736
TY 110,23560
M 49
ad 84,528
W 0
M 50
M 51
M 52
W 1
M 53
M 54
M 55
W 2
M 56
M 57
WL 1
AN 104,106
AN 105,107
M 58
mw 75
SH 106
SH 107
M 59
R 28
M 60
AN 106,106
AN 107,107
M 61
WL 2
AN 108,110
AN 109,111
M 62
SH 110
SH 111
M 63
AN 110,110
AN 111,111
M 64
TY 114,11272
TY 118,25096
M 65
ad 84,534
M 66
W 0
M 67
M 68
M 69
W 1
M 70
M 71
R 30
M 72
W 2
M 73
WL 3
AN 112,114
AN 113,115
M 74
SH 114
SH 115
M 75
mw 0
AN 114,114
AN 115,115
M 76
WL 2
AN 116,118
AN 117,119
M 77
SH 118
SH 119
M 78
AN 118,118
AN 119,119
M 79
TY 122,12808
TY 126,26632
M 80
ad 84,576
W 0
M 81
M 82
M 83
R 32
M 84
W 1
SA 202,768
M 85
M 86
M 87
W 2
M 88
WL 3
AN 120,122
AN 121,123
M 89
SH 122
SH 123
M 90
mw 3
AN 122,122
AN 123,123
M 91
WL 2
AN 124,126
AN 125,127
M 92
SH 126
SH 127
M 93
AN 126,126
AN 127,127
M 94
ad 84,582
W 0
M 95
R 34
M 96
M 97
W 1
M 98
M 99
M 100
W 2
M 101
M 102
M 103
bc 49,7,66,60
M 104
mh 9
M 105
ad 66,1408
H 0
M 106
M 107
R 48
M 108
DG 1024
M 109
M 110
M 111
bc 48,7,64,56
M 112
mh 12
M 113
ad 64,1408
H 0
M 114
M 115
M 116
H 1
M 117
M 118
M 119
WL 2
R 50
M 120
H 2
M 121
M 132
M 133
PA 12288
TX 58,9216
TX 62,23040
DF
M 122
M 123
M 134
M 135
TY 66,9728
TY 70,23552
M 124
M 125
M 136
ae 44,70,768
M 137
TX 74,10752
TX 78,24576
M 126
s_bfe_u32 s51, s50, 0x1000d
WL 4
Q 0
M 127
Q 2
M 138
Q 4
M 139
TY 82,11264
TY 86,25088
M 128
Q 6
M 129
WL 4
Q 8
M 140
Q 10
M 141
TX 90,12288
TX 94,26112
M 130
Q 12
M 131
R 52
M 142
Q 14
M 143
TY 98,12800
TY 102,26624
M 0
R 54
M 1
WL 10
Q 16
M 12
Q 18
M 13
Q 20
M 2
Q 22
M 3
WL 8
Q 24
M 14
Q 26
M 15
Q 28
M 4
Q 30
M 5
WL 6
Q 32
M 16
Q 34
M 17
Q 36
M 6
Q 38
M 7
WL 2
Q 40
M 18
Q 42
M 19
Q 44
M 8
Q 46
M 9
SA 204,1536
v_mad_i32_i24 v47, v49, s51, v48
M 20
XL 38,0
M 21
XL 40,768
M 10
XL 42,1536
M 11
R 56
M 22
XL 44,2304
M 23
R 58
M 24
M 25
M 26
M 27
M 28
M 29
M 30
M 31
M 32
M 33
M 34
M 35
WL 4
M 36
M 37
M 38
M 39
M 40
WV 28
UL 6,30
UH 7,30
M 41
UL 8,31
UH 9,31
M 42
AR 10,36
AR 11,60
M 43
WV 11
AR 12,84
AR 13,108
M 44
WL 0
AD 10,6
AD 11,7
M 45
s_barrier
R 72
M 46
mw 24
AD 12,8
AD 13,9
M 47
R 74
M 48
TY 106,520
TY 110,14344
M 49
ad 84,624
W 0
M 50
AW 36,10
AW 60,11
M 51
AW 84,12
AW 108,13
M 52
W 1
UL 6,32
M 53
UH 7,32
UL 8,33
M 54
)"
R"(UH 9,33
AR 10,37
M 55
W 2
AR 11,61
M 56
AR 12,85
AR 13,109
M 57
WL 1
AN 104,106
AN 105,107
M 58
mw 27
SH 106
SH 107
M 59
R 76
M 60
AN 106,106
AN 107,107
M 61
WL 2
AN 108,110
AN 109,111
M 62
SH 110
SH 111
M 63
AN 110,110
AN 111,111
M 64
TY 114,2056
TY 118,15880
M 65
ad 84,630
M 66
W 0
AD 10,6
M 67
AD 11,7
AD 12,8
M 68
AD 13,9
AW 37,10
M 69
W 1
AW 61,11
M 70
AW 85,12
AW 109,13
M 71
R 78
M 72
W 2
UL 6,34
M 73
WL 3
AN 112,114
AN 113,115
M 74
SH 114
SH 115
M 75
mw 48
AN 114,114
AN 115,115
M 76
WL 2
AN 116,118
AN 117,119
M 77
SH 118
SH 119
M 78
AN 118,118
AN 119,119
M 79
TY 122,3592
TY 126,17416
M 80
ad 84,672
W 0
M 81
SA 206,2304
UH 7,34
M 82
UL 8,35
UH 9,35
M 83
R 80
M 84
W 1
AR 10,38
M 85
AR 11,62
AR 12,86
M 86
AR 13,110
AD 10,6
M 87
W 2
AD 11,7
M 88
WL 3
AN 120,122
AN 121,123
M 89
SH 122
SH 123
M 90
mw 51
AN 122,122
AN 123,123
M 91
WL 2
AN 124,126
AN 125,127
M 92
SH 126
SH 127
M 93
AN 126,126
AN 127,127
M 94
ad 84,678
W 0
M 95
R 82
M 96
AD 12,8
AD 13,9
M 97
W 1
AW 38,10
M 98
AW 62,11
AW 86,12
M 99
AW 110,13
UL 6,36
M 100
W 2
UH 7,36
M 101
UL 8,37
UH 9,37
M 102
AR 10,39
AR 11,63
M 103
bc 49,8,66,60
AR 12,87
M 104
mh 18
AR 13,111
AD 10,6
M 105
ad 66,1600
H 0
M 106
AD 11,7
AD 12,8
M 107
R 0
M 108
DG 1024
M 109
AD 13,9
AW 39,10
M 110
AW 63,11
AW 87,12
M 111
bc 48,8,64,56
AW 111,13
M 112
mh 21
M 113
ad 64,1600
H 0
M 114
M 115
M 116
H 1
M 117
M 118
M 119
WL 2
R 2
M 120
H 2
M 121
M 132
M 133
TX 58,0
TX 62,13824
M 122
M 123
M 134
M 135
TY 66,512
TY 70,14336
M 124
M 125
M 136
M 137
TX 74,1536
TX 78,15360
M 126
WL 4
Q 0
M 127
Q 2
M 138
Q 4
M 139
TY 82,2048
TY 86,15872
M 128
Q 6
M 129
WL 4
Q 8
M 140
Q 10
M 141
TX 90,3072
TX 94,16896
M 130
Q 12
M 131
R 4
M 142
Q 14
M 143
TY 98,3584
TY 102,17408
M 0
R 6
M 1
WL 10
Q 16
M 12
Q 18
M 13
Q 20
M 2
Q 22
M 3
WL 8
Q 24
M 14
Q 26
M 15
Q 28
M 4
Q 30
M 5
WL 6
Q 32
M 16
Q 34
M 17
Q 36
M 6
Q 38
M 7
WL 2
Q 40
M 18
Q 42
M 19
Q 44
M 8
Q 46
M 9
ae 42,72,384
M 20
SA 208,128
M 21
ae 44,68,768
M 10
s_bitcmp1_b32 s50, 14
s_cselect_b32 s51, 24576, 0
v_add_u32 v47, s51, v3
M 11
R 8
M 22
XL 14,0
M 23
R 10
M 24
XL 16,768
M 25
XL 18,1536
M 26
XL 20,2304
M 27
M 28
M 29
M 30
M 31
M 32
M 33
M 34
M 35
WL 4
M 36
M 37
M 38
M 39
M 40
M 41
M 42
WV 23
UL 6,38
UH 7,38
M 43
WV 10
UL 8,39
UH 9,39
M 44
WL 0
AR 10,40
AR 11,64
M 45
s_barrier
R 24
M 46
mw 72
AR 12,88
AR 13,112
M 47
R 26
M 48
TY 106,5128
TY 110,18952
M 49
ad 84,720
W 0
M 50
AD 10,6
AD 11,7
M 51
AD 12,8
AD 13,9
M 52
W 1
AW 40,10
M 53
AW 64,11
AW 88,12
M 54
AW 112,13
UL 6,40
M 55
W 2
UH 7,40
M 56
UL 8,41
UH 9,41
M 57
WL 1
AN 104,106
AN 105,107
M 58
mw 75
SH 106
SH 107
M 59
R 28
M 60
AN 106,106
AN 107,107
M 61
WL 2
AN 108,110
AN 109,111
M 62
SH 110
SH 111
M 63
AN 110,110
AN 111,111
M 64
TY 114,6664
TY 118,20488
M 65
ad 84,726
M 66
W 0
AR 10,41
M 67
AR 11,65
AR 12,89
M 68
AR 13,113
AD 10,6
M 69
)"
R"(W 1
AD 11,7
M 70
AD 12,8
AD 13,9
M 71
R 30
M 72
W 2
AW 41,10
M 73
WL 3
AN 112,114
AN 113,115
M 74
SH 114
SH 115
M 75
mw 0
AN 114,114
AN 115,115
M 76
WL 2
AN 116,118
AN 117,119
M 77
SH 118
SH 119
M 78
AN 118,118
AN 119,119
M 79
TY 122,8200
TY 126,22024
M 80
ad 84,768
W 0
M 81
AW 65,11
AW 89,12
M 82
SA 210,896
AW 113,13
M 83
R 32
M 84
W 1
UL 6,42
M 85
UH 7,42
UL 8,43
M 86
UH 9,43
AR 10,42
M 87
W 2
AR 11,66
M 88
WL 3
AN 120,122
AN 121,123
M 89
SH 122
SH 123
M 90
mw 3
AN 122,122
AN 123,123
M 91
WL 2
AN 124,126
AN 125,127
M 92
SH 126
SH 127
M 93
AN 126,126
AN 127,127
M 94
ad 84,774
W 0
M 95
R 34
M 96
AR 12,90
AR 13,114
M 97
W 1
AD 10,6
M 98
AD 11,7
AD 12,8
M 99
AD 13,9
AW 42,10
M 100
W 2
AW 66,11
M 101
AW 90,12
AW 114,13
M 102
UL 6,44
UH 7,44
M 103
bc 49,9,66,60
UL 8,45
M 104
mh 0
UH 9,45
AR 10,43
M 105
ad 66,1792
H 0
M 106
AR 11,67
AR 12,91
M 107
R 48
M 108
DG 1024
M 109
AR 13,115
AD 10,6
M 110
AD 11,7
AD 12,8
M 111
bc 48,9,64,56
AD 13,9
M 112
mh 3
AW 43,10
AW 67,11
M 113
ad 64,1792
H 0
M 114
AW 91,12
AW 115,13
M 115
M 116
H 1
M 117
M 118
M 119
WL 2
R 50
M 120
H 2
M 121
M 132
M 133
PA 12288
TX 58,4608
TX 62,18432
DF
M 122
M 123
M 134
M 135
TY 66,5120
TY 70,18944
M 124
M 125
M 136
M 137
TX 74,6144
TX 78,19968
M 126
WL 4
Q 0
M 127
Q 2
M 138
Q 4
M 139
TY 82,6656
TY 86,20480
M 128
Q 6
M 129
WL 4
Q 8
M 140
Q 10
M 141
TX 90,7680
TX 94,21504
M 130
Q 12
M 131
R 52
M 142
Q 14
M 143
TY 98,8192
TY 102,22016
M 0
R 54
M 1
WL 10
Q 16
M 12
Q 18
M 13
Q 20
M 2
Q 22
M 3
WL 8
Q 24
M 14
Q 26
M 15
Q 28
M 4
Q 30
M 5
WL 6
Q 32
M 16
Q 34
M 17
Q 36
M 6
Q 38
M 7
WL 2
Q 40
M 18
Q 42
M 19
Q 44
M 8
Q 46
M 9
SA 212,1664
M 20
ae 44,68,768
M 21
s_bfe_u32 s51, s50, 0x1000f
v_mad_i32_i24 v47, v49, s51, v48
XL 22,0
M 10
XL 24,768
M 11
R 56
M 22
XL 26,1536
M 23
R 58
M 24
XL 28,2304
M 25
M 26
M 27
M 28
M 29
M 30
M 31
M 32
M 33
M 34
M 35
WL 4
M 36
M 37
M 38
M 39
M 40
M 41
M 42
M 43
WL 0
M 44
WV 11
UL 6,14
UH 7,14
M 45
s_barrier
R 72
M 46
mw 24
UL 8,15
UH 9,15
M 47
R 74
M 48
TY 106,9736
TY 110,23560
M 49
ad 84,816
W 0
M 50
AR 10,44
AR 11,68
M 51
AR 12,92
AR 13,116
M 52
W 1
AD 10,6
M 53
AD 11,7
AD 12,8
M 54
AD 13,9
AW 44,10
M 55
W 2
AW 68,11
M 56
AW 92,12
AW 116,13
M 57
WL 1
AN 104,106
AN 105,107
M 58
mw 27
SH 106
SH 107
M 59
R 76
M 60
AN 106,106
AN 107,107
M 61
WL 2
AN 108,110
AN 109,111
M 62
SH 110
SH 111
M 63
AN 110,110
AN 111,111
M 64
TY 114,11272
TY 118,25096
M 65
ad 84,822
M 66
W 0
UL 6,16
M 67
UH 7,16
UL 8,17
M 68
UH 9,17
AR 10,45
M 69
W 1
AR 11,69
M 70
AR 12,93
AR 13,117
M 71
R 78
M 72
W 2
AD 10,6
M 73
WL 3
AN 112,114
AN 113,115
M 74
SH 114
SH 115
M 75
mw 48
AN 114,114
AN 115,115
M 76
WL 2
AN 116,118
AN 117,119
M 77
SH 118
SH 119
M 78
AN 118,118
AN 119,119
M 79
TY 122,12808
TY 126,26632
M 80
ad 84,864
W 0
M 81
SA 214,2432
AD 11,7
M 82
AD 12,8
AD 13,9
M 83
R 80
M 84
W 1
)"
R"(AW 45,10
M 85
AW 69,11
AW 93,12
M 86
AW 117,13
UL 6,18
M 87
W 2
UH 7,18
M 88
WL 3
AN 120,122
AN 121,123
M 89
SH 122
SH 123
M 90
mw 51
AN 122,122
AN 123,123
M 91
WL 2
AN 124,126
AN 125,127
M 92
SH 126
SH 127
M 93
AN 126,126
AN 127,127
M 94
ad 84,870
W 0
M 95
R 82
M 96
UL 8,19
UH 9,19
M 97
W 1
AR 10,46
M 98
AR 11,70
AR 12,94
M 99
AR 13,118
AD 10,6
M 100
W 2
AD 11,7
M 101
AD 12,8
AD 13,9
M 102
AW 46,10
AW 70,11
M 103
bc 49,10,66,60
AW 94,12
M 104
mh 9
AW 118,13
UL 6,20
M 105
ad 66,1984
H 0
M 106
UH 7,20
UL 8,21
M 107
R 0
M 108
DG 1024
M 109
UH 9,21
AR 10,47
M 110
AR 11,71
AR 12,95
M 111
bc 48,10,64,56
AR 13,119
M 112
mh 12
AD 10,6
AD 11,7
M 113
ad 64,1984
H 0
M 114
AD 12,8
AD 13,9
M 115
AW 47,10
AW 71,11
M 116
H 1
AW 95,12
M 117
AW 119,13
M 118
M 119
WL 2
R 2
M 120
H 2
M 121
M 132
M 133
TX 58,9216
TX 62,23040
M 122
M 123
M 134
M 135
TY 66,9728
TY 70,23552
M 124
M 125
M 136
M 137
TX 74,10752
TX 78,24576
M 126
WL 4
Q 0
M 127
Q 2
M 138
Q 4
M 139
TY 82,11264
TY 86,25088
M 128
Q 6
M 129
WL 4
Q 8
M 140
Q 10
M 141
TX 90,12288
TX 94,26112
M 130
Q 12
M 131
R 4
M 142
Q 14
M 143
TY 98,12800
TY 102,26624
M 0
R 6
M 1
WL 10
Q 16
M 12
Q 18
M 13
Q 20
M 2
Q 22
M 3
WL 8
Q 24
M 14
Q 26
M 15
Q 28
M 4
Q 30
M 5
WL 6
Q 32
M 16
Q 34
M 17
Q 36
M 6
Q 38
M 7
WL 2
Q 40
M 18
Q 42
M 19
Q 44
M 8
Q 46
M 9
ae 42,72,384
M 20
SA 216,256
M 21
ae 44,68,864
M 10
s_bitcmp1_b32 s50, 16
s_cselect_b32 s51, 24576, 0
v_add_u32 v47, s51, v3
M 11
R 8
M 22
XL 30,0
M 23
R 10
M 24
XL 32,768
M 25
XL 34,1536
M 26
XL 36,2304
M 27
M 28
M 29
M 30
M 31
M 32
M 33
M 34
M 35
WL 4
M 36
M 37
M 38
M 39
M 40
M 41
M 42
M 43
WV 10
M 44
WL 0
M 45
s_barrier
R 24
M 46
mw 72
UL 6,22
UH 7,22
M 47
R 26
M 48
TY 106,520
TY 110,14344
M 49
ad 84,912
W 0
M 50
UL 8,23
UH 9,23
M 51
AR 10,48
AR 11,72
M 52
W 1
AR 12,96
M 53
AR 13,120
AD 10,6
M 54
AD 11,7
AD 12,8
M 55
W 2
AD 13,9
M 56
AW 48,10
AW 72,11
M 57
WL 1
AN 104,106
AN 105,107
M 58
mw 75
SH 106
SH 107
M 59
R 28
M 60
AN 106,106
AN 107,107
M 61
WL 2
AN 108,110
AN 109,111
M 62
SH 110
SH 111
M 63
AN 110,110
AN 111,111
M 64
TY 114,2056
TY 118,15880
M 65
ad 84,918
M 66
W 0
AW 96,12
M 67
AW 120,13
UL 6,24
M 68
UH 7,24
UL 8,25
M 69
W 1
UH 9,25
M 70
AR 10,49
AR 11,73
M 71
R 30
M 72
W 2
AR 12,97
M 73
WL 3
AN 112,114
AN 113,115
M 74
SH 114
SH 115
M 75
mw 0
AN 114,114
AN 115,115
M 76
WL 2
AN 116,118
AN 117,119
M 77
SH 118
SH 119
M 78
AN 118,118
AN 119,119
M 79
TY 122,3592
TY 126,17416
M 80
ad 84,960
W 0
M 81
AR 13,121
AD 10,6
M 82
SA 218,1024
AD 11,7
M 83
R 32
M 84
W 1
AD 12,8
M 85
AD 13,9
AW 49,10
M 86
AW 73,11
AW 97,12
M 87
W 2
AW 121,13
M 88
WL 3
AN 120,122
AN 121,123
M 89
SH 122
SH 123
M 90
mw 3
AN 122,122
AN 123,123
M 91
WL 2
AN 124,126
AN 125,127
M 92
SH 126
SH 127
M 93
AN 126,126
AN 127,127
M 94
ad 84,966
W 0
M 95
R 34
M 96
UL 6,26
UH 7,26
M 97
W 1
UL 8,27
M 98
UH 9,27
AR 10,50
)"
R"(M 99
AR 11,74
AR 12,98
M 100
W 2
AR 13,122
M 101
AD 10,6
AD 11,7
M 102
AD 12,8
AD 13,9
M 103
bc 49,11,66,60
AW 50,10
M 104
mh 18
AW 74,11
AW 98,12
M 105
ad 66,2176
H 0
M 106
AW 122,13
UL 6,28
M 107
R 48
M 108
DG 1024
M 109
UH 7,28
UL 8,29
M 110
UH 9,29
AR 10,51
M 111
bc 48,11,64,56
AR 11,75
M 112
mh 21
AR 12,99
AR 13,123
M 113
ad 64,2176
H 0
M 114
AD 10,6
AD 11,7
M 115
AD 12,8
AD 13,9
M 116
H 1
AW 51,10
M 117
AW 75,11
AW 99,12
M 118
AW 123,13
M 119
WL 1
R 50
M 120
H 2
M 121
M 132
M 133
PA 12288
TX 58,0
TX 62,13824
DF
M 122
M 123
M 134
M 135
TY 66,512
TY 70,14336
M 124
M 125
M 136
M 137
TX 74,1536
TX 78,15360
M 126
WL 4
Q 0
M 127
Q 2
M 138
Q 4
M 139
TY 82,2048
TY 86,15872
M 128
Q 6
M 129
WL 4
Q 8
M 140
Q 10
M 141
TX 90,3072
TX 94,16896
M 130
Q 12
M 131
R 52
M 142
Q 14
M 143
TY 98,3584
TY 102,17408
M 0
R 54
M 1
WL 10
Q 16
M 12
Q 18
M 13
Q 20
M 2
Q 22
M 3
WL 8
Q 24
M 14
Q 26
M 15
Q 28
M 4
Q 30
M 5
WL 6
Q 32
M 16
Q 34
M 17
Q 36
M 6
Q 38
M 7
WL 2
Q 40
M 18
Q 42
M 19
Q 44
M 8
Q 46
M 9
SA 220,1792
M 20
ae 44,68,864
M 21
s_bfe_u32 s51, s50, 0x10011
v_mad_i32_i24 v47, v49, s51, v48
XL 38,0
M 10
XL 40,768
M 11
R 56
M 22
XL 42,1536
M 23
R 58
M 24
XL 44,2304
M 25
M 26
M 27
M 28
M 29
M 30
M 31
M 32
M 33
M 34
M 35
WL 4
M 36
M 37
M 38
M 39
M 40
M 41
M 42
M 43
WV 11
M 44
WL 0
M 45
s_barrier
R 72
M 46
mw 24
M 47
R 74
M 48
TY 106,5128
TY 110,18952
M 49
ad 84,1008
W 0
M 50
UL 6,30
UH 7,30
M 51
UL 8,31
UH 9,31
M 52
W 1
AR 10,52
M 53
AR 11,76
AR 12,100
M 54
AR 13,124
AD 10,6
M 55
W 2
AD 11,7
M 56
AD 12,8
AD 13,9
M 57
WL 1
AN 104,106
AN 105,107
M 58
mw 27
SH 106
SH 107
M 59
R 76
M 60
AN 106,106
AN 107,107
M 61
WL 2
AN 108,110
AN 109,111
M 62
SH 110
SH 111
M 63
AN 110,110
AN 111,111
M 64
TY 114,6664
TY 118,20488
M 65
ad 84,1014
M 66
W 0
AW 52,10
M 67
AW 76,11
AW 100,12
M 68
AW 124,13
UL 6,32
M 69
W 1
UH 7,32
M 70
UL 8,33
UH 9,33
M 71
R 78
M 72
W 2
AR 10,53
M 73
WL 3
AN 112,114
AN 113,115
M 74
SH 114
SH 115
M 75
mw 48
AN 114,114
AN 115,115
M 76
WL 2
AN 116,118
AN 117,119
M 77
SH 118
SH 119
M 78
AN 118,118
AN 119,119
M 79
TY 122,8200
TY 126,22024
M 80
ad 84,1056
W 0
M 81
SA 222,2560
AR 11,77
M 82
AR 12,101
AR 13,125
M 83
R 80
M 84
W 1
AD 10,6
M 85
AD 11,7
AD 12,8
M 86
AD 13,9
AW 53,10
M 87
W 2
AW 77,11
M 88
WL 3
AN 120,122
AN 121,123
M 89
SH 122
SH 123
M 90
mw 51
AN 122,122
AN 123,123
M 91
WL 2
AN 124,126
AN 125,127
M 92
SH 126
SH 127
M 93
AN 126,126
AN 127,127
M 94
ad 84,1062
W 0
M 95
R 82
M 96
AW 101,12
AW 125,13
M 97
W 1
UL 6,34
M 98
UH 7,34
UL 8,35
M 99
UH 9,35
AR 10,54
M 100
W 2
AR 11,78
M 101
AR 12,102
AR 13,126
M 102
AD 10,6
AD 11,7
M 103
bc 49,12,66,60
AD 12,8
M 104
mh 0
AD 13,9
AW 54,10
M 105
ad 66,2368
H 0
M 106
AW 78,11
AW 102,12
M 107
R 0
M 108
DG 1024
M 109
AW 126,13
UL 6,36
M 110
UH 7,36
UL 8,37
M 111
bc 48,12,64,56
UH 9,37
M 112
mh 3
AR 10,55
AR 11,79
M 113
ad 64,2368
H 0
)"
R"(M 114
AR 12,103
AR 13,127
M 115
AD 10,6
AD 11,7
M 116
H 1
AD 12,8
M 117
AD 13,9
AW 55,10
M 118
AW 79,11
AW 103,12
M 119
WL 0
R 2
M 120
H 2
AW 127,13
M 121
M 132
M 133
TX 58,4608
TX 62,18432
M 122
M 123
M 134
M 135
TY 66,5120
TY 70,18944
M 124
M 125
M 136
M 137
TX 74,6144
TX 78,19968
M 126
WL 4
Q 0
M 127
Q 2
M 138
Q 4
M 139
TY 82,6656
TY 86,20480
M 128
Q 6
M 129
WL 4
Q 8
M 140
Q 10
M 141
TX 90,7680
TX 94,21504
M 130
Q 12
M 131
R 4
M 142
Q 14
M 143
TY 98,8192
TY 102,22016
M 0
R 6
M 1
WL 10
Q 16
M 12
Q 18
M 13
Q 20
M 2
Q 22
M 3
WL 8
Q 24
M 14
Q 26
M 15
Q 28
M 4
Q 30
M 5
WL 6
Q 32
M 16
Q 34
M 17
Q 36
M 6
Q 38
M 7
WL 2
Q 40
M 18
Q 42
M 19
Q 44
M 8
Q 46
M 9
ae 42,72,480
M 20
s_bitcmp1_b32 s50, 10
s_cselect_b32 s52, 24576, 0
v_add_u32 v50, s52, v3
M 21
SA 224,0
M 10
M 11
R 8
M 22
M 23
R 10
M 24
M 25
M 26
M 27
M 28
M 29
M 30
M 31
M 32
ae 44,70,768
M 33
s_bitcmp1_b32 s50, 12
s_cselect_b32 s51, 24576, 0
v_add_u32 v47, s51, v3
M 34
XL 14,128
M 35
WL 4
M 36
M 37
M 38
XL 16,896
M 39
M 40
M 41
M 42
M 43
WV 8
M 44
WL 0
XL 18,1664
M 45
s_barrier
R 24
M 46
mw 72
M 47
R 26
M 48
TY 106,9736
TY 110,23560
M 49
ad 84,1104
W 0
M 50
XL 20,2432
UL 6,38
M 51
UH 7,38
UL 8,39
M 52
W 1
UH 9,39
M 53
AR 10,56
AR 11,80
M 54
AR 12,104
AR 13,128
M 55
W 2
AD 10,6
M 56
AD 11,7
AD 12,8
M 57
WL 1
AN 104,106
AN 105,107
M 58
mw 75
SH 106
SH 107
M 59
R 28
M 60
AN 106,106
AN 107,107
M 61
WL 2
AN 108,110
AN 109,111
M 62
SH 110
SH 111
M 63
AN 110,110
AN 111,111
M 64
TY 114,11272
TY 118,25096
M 65
ad 84,1110
M 66
W 0
AD 13,9
M 67
AW 56,10
AW 80,11
M 68
AW 104,12
AW 128,13
M 69
W 1
UL 6,40
M 70
UH 7,40
UL 8,41
M 71
R 30
M 72
W 2
UH 9,41
M 73
WL 3
AN 112,114
AN 113,115
M 74
SH 114
SH 115
M 75
mw 0
AN 114,114
AN 115,115
M 76
WL 2
AN 116,118
AN 117,119
M 77
SH 118
SH 119
M 78
AN 118,118
AN 119,119
M 79
TY 122,12808
TY 126,26632
M 80
ad 84,1152
W 0
M 81
AR 10,57
AR 11,81
M 82
AR 12,105
AR 13,129
M 83
R 32
M 84
W 1
SA 226,768
M 85
AD 10,6
AD 11,7
M 86
AD 12,8
AD 13,9
M 87
W 2
AW 57,10
M 88
WL 3
AN 120,122
AN 121,123
M 89
SH 122
SH 123
M 90
mw 3
AN 122,122
AN 123,123
M 91
WL 2
AN 124,126
AN 125,127
M 92
SH 126
SH 127
M 93
AN 126,126
AN 127,127
M 94
ad 84,1158
W 0
M 95
R 34
M 96
AW 81,11
AW 105,12
M 97
W 1
AW 129,13
M 98
UL 6,42
UH 7,42
M 99
UL 8,43
UH 9,43
M 100
W 2
AR 10,58
M 101
AR 11,82
AR 12,106
M 102
AR 13,130
AD 10,6
M 103
bc 49,13,66,60
AD 11,7
M 104
mh 9
AD 12,8
AD 13,9
M 105
ad 66,2560
H 0
M 106
AW 58,10
AW 82,11
M 107
R 48
M 108
DG 1024
M 109
AW 106,12
AW 130,13
M 110
UL 6,44
UH 7,44
M 111
bc 48,13,64,56
UL 8,45
M 112
mh 12
UH 9,45
AR 10,59
M 113
ad 64,2560
H 0
M 114
AR 11,83
AR 12,107
M 115
AR 13,131
AD 10,6
M 116
H 1
AD 11,7
M 117
AD 12,8
AD 13,9
M 118
AW 59,10
AW 83,11
M 119
WL 0
R 50
M 120
H 2
AW 107,12
M 121
AW 131,13
M 132
M 133
PA 12288
TX 58,9216
TX 62,23040
DF
M 122
M 123
M 134
M 135
TY 66,9728
TY 70,23552
M 124
M 125
M 136
M 137
TX 74,10752
TX 78,24576
)"
R"(M 126
WL 4
Q 0
M 127
Q 2
M 138
Q 4
M 139
TY 82,11264
TY 86,25088
M 128
Q 6
M 129
WL 4
Q 8
M 140
Q 10
M 141
TX 90,12288
TX 94,26112
M 130
Q 12
M 131
R 52
M 142
Q 14
M 143
TY 98,12800
TY 102,26624
M 0
R 54
M 1
WL 10
Q 16
M 12
Q 18
M 13
Q 20
M 2
Q 22
M 3
WL 8
Q 24
M 14
Q 26
M 15
Q 28
M 4
Q 30
M 5
WL 6
Q 32
M 16
Q 34
M 17
Q 36
M 6
Q 38
M 7
WL 2
Q 40
M 18
Q 42
M 19
Q 44
M 8
Q 46
M 9
SA 228,1536
M 20
M 21
M 10
M 11
R 56
M 22
M 23
R 58
M 24
M 25
M 26
M 27
M 28
M 29
M 30
M 31
M 32
M 33
M 34
ae 44,70,768
M 35
WL 4
M 36
s_bfe_u32 s51, s50, 0x1000d
v_mad_i32_i24 v47, v49, s51, v48
XL 22,128
M 37
M 38
M 39
M 40
XL 24,896
M 41
M 42
M 43
WV 9
M 44
WL 0
M 45
s_barrier
R 72
M 46
mw 24
XL 26,1664
M 47
R 74
M 48
TY 106,520
TY 110,14344
M 49
ad 84,1200
W 0
M 50
M 51
M 52
W 1
XL 28,2432
M 53
M 54
M 55
W 2
M 56
M 57
WL 1
AN 104,106
AN 105,107
M 58
mw 27
SH 106
SH 107
M 59
R 76
M 60
AN 106,106
AN 107,107
M 61
WL 2
AN 108,110
AN 109,111
M 62
SH 110
SH 111
M 63
AN 110,110
AN 111,111
M 64
TY 114,2056
TY 118,15880
M 65
ad 84,1206
M 66
W 0
M 67
M 68
M 69
W 1
M 70
M 71
R 78
M 72
W 2
M 73
WL 3
AN 112,114
AN 113,115
M 74
SH 114
SH 115
M 75
mw 48
AN 114,114
AN 115,115
M 76
WL 2
AN 116,118
AN 117,119
M 77
SH 118
SH 119
M 78
AN 118,118
AN 119,119
M 79
TY 122,3592
TY 126,17416
M 80
ad 84,1248
W 0
M 81
SA 230,2304
M 82
M 83
R 80
M 84
W 1
M 85
M 86
M 87
W 2
M 88
WL 3
AN 120,122
AN 121,123
M 89
SH 122
SH 123
M 90
mw 51
AN 122,122
AN 123,123
M 91
WL 2
AN 124,126
AN 125,127
M 92
SH 126
SH 127
M 93
AN 126,126
AN 127,127
M 94
ad 84,1254
W 0
M 95
R 82
M 96
UL 6,14
UH 7,14
M 97
W 1
UL 8,15
M 98
UH 9,15
AR 10,132
M 99
AR 11,156
AR 12,180
M 100
W 2
AD 10,6
M 101
AD 11,7
AD 12,8
M 102
AD 136,9
AW 132,10
M 103
bc 49,14,66,60
AW 156,11
M 104
mh 18
AW 180,12
UL 6,16
M 105
ad 66,2752
H 0
M 106
UH 7,16
UL 8,17
M 107
R 0
M 108
DG 1024
M 109
UH 9,17
AR 10,133
M 110
AR 11,157
AR 12,181
M 111
bc 48,14,64,56
AD 10,6
M 112
mh 21
AD 11,7
AD 12,8
M 113
ad 64,2752
H 0
M 114
AD 137,9
AW 133,10
M 115
AW 157,11
AW 181,12
M 116
H 1
UL 6,18
M 117
UH 7,18
UL 8,19
M 118
UH 9,19
AR 10,134
M 119
WL 0
R 2
M 120
H 2
AR 11,158
M 121
AR 12,182
AD 10,6
M 132
AD 11,7
AD 12,8
M 133
TX 58,0
TX 62,13824
M 122
AD 138,9
AW 134,10
M 123
AW 158,11
AW 182,12
M 134
UL 6,20
UH 7,20
M 135
TY 66,512
TY 70,14336
M 124
UL 8,21
UH 9,21
M 125
AR 10,135
AR 11,159
M 136
AR 12,183
AD 10,6
M 137
TX 74,1536
TX 78,15360
M 126
WL 4
Q 0
M 127
Q 2
M 138
Q 4
M 139
TY 82,2048
TY 86,15872
M 128
Q 6
M 129
WL 4
Q 8
M 140
Q 10
M 141
TX 90,3072
TX 94,16896
M 130
Q 12
M 131
R 4
M 142
Q 14
M 143
TY 98,3584
TY 102,17408
M 0
R 6
M 1
WL 10
Q 16
M 12
Q 18
M 13
Q 20
M 2
Q 22
M 3
WL 8
Q 24
M 14
Q 26
M 15
Q 28
M 4
Q 30
M 5
WL 6
Q 32
M 16
Q 34
M 17
Q 36
M 6
Q 38
M 7
)"
R"(WL 2
Q 40
M 18
Q 42
M 19
Q 44
M 8
Q 46
M 9
ae 42,72,480
M 20
SA 232,128
AD 11,7
M 21
AD 12,8
AD 139,9
M 10
AW 135,10
AW 159,11
M 11
R 8
M 22
AW 183,12
M 23
R 10
M 24
M 25
M 26
M 27
M 28
M 29
M 30
M 31
M 32
M 33
M 34
M 35
WL 4
M 36
ae 44,68,768
M 37
s_bitcmp1_b32 s50, 14
s_cselect_b32 s51, 24576, 0
v_add_u32 v47, s51, v3
M 38
XL 30,128
M 39
M 40
M 41
M 42
XL 32,896
M 43
WV 8
M 44
WL 0
M 45
s_barrier
R 24
M 46
mw 72
M 47
R 26
M 48
TY 106,5128
TY 110,18952
M 49
ad 84,1296
W 0
M 50
XL 34,1664
M 51
M 52
W 1
M 53
M 54
XL 36,2432
M 55
W 2
M 56
M 57
WL 1
AN 104,106
AN 105,107
M 58
mw 75
SH 106
SH 107
M 59
R 28
M 60
AN 106,106
AN 107,107
M 61
WL 2
AN 108,110
AN 109,111
M 62
SH 110
SH 111
M 63
AN 110,110
AN 111,111
M 64
TY 114,6664
TY 118,20488
M 65
ad 84,1302
M 66
W 0
M 67
M 68
M 69
W 1
M 70
M 71
R 30
M 72
W 2
M 73
WL 3
AN 112,114
AN 113,115
M 74
SH 114
SH 115
M 75
mw 0
AN 114,114
AN 115,115
M 76
WL 2
AN 116,118
AN 117,119
M 77
SH 118
SH 119
M 78
AN 118,118
AN 119,119
M 79
TY 122,8200
TY 126,22024
M 80
ad 84,1344
W 0
M 81
M 82
SA 234,896
M 83
R 32
M 84
W 1
M 85
M 86
M 87
W 2
M 88
WL 3
AN 120,122
AN 121,123
M 89
SH 122
SH 123
M 90
mw 3
AN 122,122
AN 123,123
M 91
WL 2
AN 124,126
AN 125,127
M 92
SH 126
SH 127
M 93
AN 126,126
AN 127,127
M 94
ad 84,1350
W 0
M 95
R 34
M 96
UL 6,22
UH 7,22
M 97
W 1
UL 8,23
M 98
UH 9,23
AR 10,136
M 99
AR 11,160
AR 12,184
M 100
W 2
AD 10,6
M 101
AD 11,7
AD 12,8
M 102
AD 140,9
AW 136,10
M 103
bc 49,15,66,60
AW 160,11
M 104
mh 0
AW 184,12
UL 6,24
M 105
ad 66,2944
H 0
M 106
UH 7,24
UL 8,25
M 107
R 48
M 108
DG 1024
M 109
UH 9,25
AR 10,137
M 110
AR 11,161
AR 12,185
M 111
bc 48,15,64,56
AD 10,6
M 112
mh 3
AD 11,7
AD 12,8
M 113
ad 64,2944
H 0
M 114
AD 141,9
AW 137,10
M 115
AW 161,11
AW 185,12
M 116
H 1
UL 6,26
M 117
UH 7,26
UL 8,27
M 118
UH 9,27
AR 10,138
M 119
WL 0
R 50
M 120
H 2
AR 11,162
M 121
AR 12,186
AD 10,6
M 132
AD 11,7
AD 12,8
M 133
PA 12288
TX 58,4608
TX 62,18432
DF
M 122
AD 142,9
AW 138,10
M 123
AW 162,11
AW 186,12
M 134
UL 6,28
UH 7,28
M 135
TY 66,5120
TY 70,18944
M 124
UL 8,29
UH 9,29
M 125
AR 10,139
AR 11,163
M 136
AR 12,187
AD 10,6
M 137
TX 74,6144
TX 78,19968
M 126
WL 4
Q 0
M 127
Q 2
M 138
Q 4
M 139
TY 82,6656
TY 86,20480
M 128
Q 6
M 129
WL 4
Q 8
M 140
Q 10
M 141
TX 90,7680
TX 94,21504
M 130
Q 12
M 131
R 52
M 142
Q 14
M 143
TY 98,8192
TY 102,22016
M 0
R 54
M 1
WL 10
Q 16
M 12
Q 18
M 13
Q 20
M 2
Q 22
M 3
WL 8
Q 24
M 14
Q 26
M 15
Q 28
M 4
Q 30
M 5
WL 6
Q 32
M 16
Q 34
M 17
Q 36
M 6
Q 38
M 7
WL 2
Q 40
M 18
Q 42
M 19
Q 44
M 8
Q 46
M 9
SA 236,1664
AD 11,7
M 20
AD 12,8
AD 143,9
M 21
AW 139,10
AW 163,11
M 10
AW 187,12
M 11
R 56
M 22
M 23
R 58
M 24
M 25
M 26
M 27
M 28
M 29
M 30
M 31
M 32
M 33
M 34
M 35
WL 4
M 36
M 37
M 38
ae 44,68,768
M 39
s_bfe_u32 s51, s50, 0x1000f
v_mad_i32_i24 v47, v49, s51, v48
XL 38,128
M 40
M 41
M 42
M 43
)"
R"(WV 8
M 44
WL 0
XL 40,896
M 45
s_barrier
R 72
M 46
mw 24
M 47
R 74
M 48
TY 106,9736
TY 110,23560
M 49
ad 84,1392
W 0
M 50
XL 42,1664
M 51
M 52
W 1
M 53
M 54
M 55
W 2
M 56
XL 44,2432
M 57
WL 1
AN 104,106
AN 105,107
M 58
mw 27
SH 106
SH 107
M 59
R 76
M 60
AN 106,106
AN 107,107
M 61
WL 2
AN 108,110
AN 109,111
M 62
SH 110
SH 111
M 63
AN 110,110
AN 111,111
M 64
TY 114,11272
TY 118,25096
M 65
ad 84,1398
M 66
W 0
M 67
M 68
M 69
W 1
M 70
M 71
R 78
M 72
W 2
M 73
WL 3
AN 112,114
AN 113,115
M 74
SH 114
SH 115
M 75
mw 48
AN 114,114
AN 115,115
M 76
WL 2
AN 116,118
AN 117,119
M 77
SH 118
SH 119
M 78
AN 118,118
AN 119,119
M 79
TY 122,12808
TY 126,26632
M 80
ad 84,1440
W 0
M 81
SA 238,2432
M 82
M 83
R 80
M 84
W 1
M 85
M 86
M 87
W 2
M 88
WL 3
AN 120,122
AN 121,123
M 89
SH 122
SH 123
M 90
mw 51
AN 122,122
AN 123,123
M 91
WL 2
AN 124,126
AN 125,127
M 92
SH 126
SH 127
M 93
AN 126,126
AN 127,127
M 94
ad 84,1446
W 0
M 95
R 82
M 96
UL 6,30
UH 7,30
M 97
W 1
UL 8,31
M 98
UH 9,31
AR 10,140
M 99
AR 11,164
AR 12,188
M 100
W 2
AD 10,6
M 101
AD 11,7
AD 12,8
M 102
AD 144,9
AW 140,10
M 103
bc 49,16,66,60
AW 164,11
M 104
mh 9
AW 188,12
UL 6,32
M 105
ad 66,3136
H 0
M 106
UH 7,32
UL 8,33
M 107
R 0
M 108
DG 1024
M 109
UH 9,33
AR 10,141
M 110
AR 11,165
AR 12,189
M 111
bc 48,16,64,56
AD 10,6
M 112
mh 12
AD 11,7
AD 12,8
M 113
ad 64,3136
H 0
M 114
AD 145,9
AW 141,10
M 115
AW 165,11
AW 189,12
M 116
H 1
UL 6,34
M 117
UH 7,34
UL 8,35
M 118
UH 9,35
AR 10,142
M 119
WL 0
R 2
M 120
H 2
AR 11,166
M 121
AR 12,190
AD 10,6
M 132
AD 11,7
AD 12,8
M 133
TX 58,9216
TX 62,23040
M 122
AD 146,9
AW 142,10
M 123
AW 166,11
AW 190,12
M 134
UL 6,36
UH 7,36
M 135
TY 66,9728
TY 70,23552
M 124
UL 8,37
UH 9,37
M 125
AR 10,143
AR 11,167
M 136
AR 12,191
AD 10,6
M 137
TX 74,10752
TX 78,24576
M 126
WL 4
Q 0
M 127
Q 2
M 138
Q 4
M 139
TY 82,11264
TY 86,25088
M 128
Q 6
M 129
WL 4
Q 8
M 140
Q 10
M 141
TX 90,12288
TX 94,26112
M 130
Q 12
M 131
R 4
M 142
Q 14
M 143
TY 98,12800
TY 102,26624
M 0
R 6
M 1
WL 10
Q 16
M 12
Q 18
M 13
Q 20
M 2
Q 22
M 3
WL 8
Q 24
M 14
Q 26
M 15
Q 28
M 4
Q 30
M 5
WL 6
Q 32
M 16
Q 34
M 17
Q 36
M 6
Q 38
M 7
WL 2
Q 40
M 18
Q 42
M 19
Q 44
M 8
Q 46
M 9
ae 42,72,480
M 20
SA 240,256
AD 11,7
M 21
AD 12,8
AD 147,9
M 10
AW 143,10
AW 167,11
M 11
R 8
M 22
AW 191,12
M 23
R 10
M 24
M 25
M 26
M 27
M 28
M 29
M 30
M 31
M 32
M 33
M 34
M 35
WL 4
M 36
M 37
M 38
M 39
M 40
ae 44,68,864
M 41
s_bitcmp1_b32 s50, 16
s_cselect_b32 s51, 24576, 0
v_add_u32 v47, s51, v3
M 42
XL 14,128
M 43
WV 7
M 44
WL 0
M 45
s_barrier
R 24
M 46
mw 72
XL 16,896
M 47
R 26
M 48
TY 106,520
TY 110,14344
M 49
ad 84,1488
W 0
M 50
M 51
M 52
W 1
XL 18,1664
M 53
M 54
M 55
W 2
M 56
M 57
WL 1
AN 104,106
AN 105,107
M 58
mw 75
SH 106
SH 107
M 59
R 28
M 60
AN 106,106
AN 107,107
M 61
WL 2
AN 108,110
AN 109,111
M 62
SH 110
SH 111
M 63
AN 110,110
AN 111,111
M 64
)"
R"(TY 114,2056
TY 118,15880
M 65
ad 84,1494
M 66
W 0
XL 20,2432
M 67
M 68
M 69
W 1
M 70
M 71
R 30
M 72
W 2
M 73
WL 3
AN 112,114
AN 113,115
M 74
SH 114
SH 115
M 75
mw 0
AN 114,114
AN 115,115
M 76
WL 2
AN 116,118
AN 117,119
M 77
SH 118
SH 119
M 78
AN 118,118
AN 119,119
M 79
TY 122,3592
TY 126,17416
M 80
ad 84,1536
W 0
M 81
M 82
SA 242,1024
M 83
R 32
M 84
W 1
M 85
M 86
M 87
W 2
M 88
WL 3
AN 120,122
AN 121,123
M 89
SH 122
SH 123
M 90
mw 3
AN 122,122
AN 123,123
M 91
WL 2
AN 124,126
AN 125,127
M 92
SH 126
SH 127
M 93
AN 126,126
AN 127,127
M 94
ad 84,1542
W 0
M 95
R 34
M 96
UL 6,38
UH 7,38
M 97
W 1
UL 8,39
M 98
UH 9,39
AR 10,144
M 99
AR 11,168
AR 12,192
M 100
W 2
AD 10,6
M 101
AD 11,7
AD 12,8
M 102
AD 148,9
AW 144,10
M 103
bc 49,17,66,60
AW 168,11
M 104
mh 18
AW 192,12
UL 6,40
M 105
ad 66,3328
H 0
M 106
UH 7,40
UL 8,41
M 107
R 48
M 108
DG 1024
M 109
UH 9,41
AR 10,145
M 110
AR 11,169
AR 12,193
M 111
bc 48,17,64,56
AD 10,6
M 112
mh 21
AD 11,7
AD 12,8
M 113
ad 64,3328
H 0
M 114
AD 149,9
AW 145,10
M 115
AW 169,11
AW 193,12
M 116
H 1
UL 6,42
M 117
UH 7,42
UL 8,43
M 118
UH 9,43
AR 10,146
M 119
WL 0
R 50
M 120
H 2
AR 11,170
M 121
AR 12,194
AD 10,6
M 132
AD 11,7
AD 12,8
M 133
PA 12288
TX 58,0
TX 62,13824
DF
M 122
AD 150,9
AW 146,10
M 123
AW 170,11
AW 194,12
M 134
UL 6,44
UH 7,44
M 135
TY 66,512
TY 70,14336
M 124
UL 8,45
UH 9,45
M 125
AR 10,147
AR 11,171
M 136
AR 12,195
AD 10,6
M 137
TX 74,1536
TX 78,15360
M 126
WL 4
Q 0
M 127
Q 2
M 138
Q 4
M 139
TY 82,2048
TY 86,15872
M 128
Q 6
M 129
WL 4
Q 8
M 140
Q 10
M 141
TX 90,3072
TX 94,16896
M 130
Q 12
M 131
R 52
M 142
Q 14
M 143
TY 98,3584
TY 102,17408
M 0
R 54
M 1
WL 10
Q 16
M 12
Q 18
M 13
Q 20
M 2
Q 22
M 3
WL 8
Q 24
M 14
Q 26
M 15
Q 28
M 4
Q 30
M 5
WL 6
Q 32
M 16
Q 34
M 17
Q 36
M 6
Q 38
M 7
WL 2
Q 40
M 18
Q 42
M 19
Q 44
M 8
Q 46
M 9
SA 244,1792
AD 11,7
M 20
AD 12,8
AD 151,9
M 21
AW 147,10
AW 171,11
M 10
AW 195,12
M 11
R 56
M 22
M 23
R 58
M 24
M 25
M 26
M 27
M 28
M 29
M 30
M 31
M 32
M 33
M 34
M 35
WL 4
M 36
M 37
M 38
M 39
M 40
M 41
M 42
ae 44,68,864
M 43
WV 7
M 44
WL 0
M 45
s_barrier
R 72
M 46
mw 24
M 47
R 74
M 48
s_bfe_u32 s51, s50, 0x10011
TY 106,5128
TY 110,18952
M 49
ad 84,1584
W 0
M 50
v_mad_i32_i24 v47, v49, s51, v48
XL 22,128
M 51
XL 24,896
M 52
W 1
M 53
M 54
XL 26,1664
M 55
W 2
M 56
M 57
WL 1
AN 104,106
AN 105,107
M 58
mw 27
SH 106
SH 107
M 59
R 76
M 60
AN 106,106
AN 107,107
M 61
WL 2
AN 108,110
AN 109,111
M 62
SH 110
SH 111
M 63
AN 110,110
AN 111,111
M 64
TY 114,6664
TY 118,20488
M 65
ad 84,1590
M 66
W 0
XL 28,2432
M 67
M 68
M 69
W 1
M 70
M 71
R 78
M 72
W 2
M 73
WL 3
AN 112,114
AN 113,115
M 74
SH 114
SH 115
M 75
mw 48
AN 114,114
AN 115,115
M 76
WL 2
AN 116,118
AN 117,119
M 77
SH 118
SH 119
M 78
AN 118,118
AN 119,119
M 79
TY 122,8200
TY 126,22024
M 80
ad 84,1632
W 0
M 81
SA 246,2560
M 82
M 83
R 80
M 84
W 1
M 85
)"
R"(M 86
M 87
W 2
M 88
WL 3
AN 120,122
AN 121,123
M 89
SH 122
SH 123
M 90
mw 51
AN 122,122
AN 123,123
M 91
WL 2
AN 124,126
AN 125,127
M 92
SH 126
SH 127
M 93
AN 126,126
AN 127,127
M 94
ad 84,1638
W 0
M 95
R 82
M 96
UL 6,14
UH 7,14
M 97
W 1
UL 8,15
M 98
UH 9,15
AR 10,148
M 99
AR 11,172
AR 12,196
M 100
W 2
AD 10,6
M 101
AD 11,7
AD 12,8
M 102
AD 152,9
AW 148,10
M 103
bc 49,18,66,60
AW 172,11
M 104
mh 0
AW 196,12
UL 6,16
M 105
ad 66,3520
H 0
M 106
UH 7,16
UL 8,17
M 107
R 0
M 108
DG 1024
M 109
UH 9,17
AR 10,149
M 110
AR 11,173
AR 12,197
M 111
bc 48,18,64,56
AD 10,6
M 112
mh 3
AD 11,7
AD 12,8
M 113
ad 64,3520
H 0
M 114
AD 153,9
AW 149,10
M 115
AW 173,11
AW 197,12
M 116
H 1
UL 6,18
M 117
UH 7,18
UL 8,19
M 118
UH 9,19
AR 10,150
M 119
WL 0
R 2
M 120
H 2
AR 11,174
M 121
AR 12,198
AD 10,6
M 132
AD 11,7
AD 12,8
M 133
TX 58,4608
TX 62,18432
M 122
AD 154,9
AW 150,10
M 123
AW 174,11
AW 198,12
M 134
UL 6,20
UH 7,20
M 135
TY 66,5120
TY 70,18944
M 124
UL 8,21
UH 9,21
M 125
AR 10,151
AR 11,175
M 136
AR 12,199
AD 10,6
M 137
TX 74,6144
TX 78,19968
M 126
WL 4
Q 0
M 127
Q 2
M 138
Q 4
M 139
TY 82,6656
TY 86,20480
M 128
Q 6
M 129
WL 4
Q 8
M 140
Q 10
M 141
TX 90,7680
TX 94,21504
M 130
Q 12
M 131
R 4
M 142
Q 14
M 143
TY 98,8192
TY 102,22016
M 0
R 6
M 1
WL 10
Q 16
M 12
Q 18
M 13
Q 20
M 2
Q 22
M 3
WL 8
Q 24
M 14
Q 26
M 15
Q 28
M 4
Q 30
M 5
WL 6
Q 32
M 16
Q 34
M 17
Q 36
M 6
Q 38
M 7
WL 2
Q 40
M 18
Q 42
M 19
Q 44
M 8
Q 46
M 9
ae 42,72,480
M 20
s_bfe_u32 s52, s50, 0x1000b
v_mad_i32_i24 v50, v49, s52, v48
SA 248,256
M 21
AD 11,7
AD 12,8
M 10
AD 155,9
AW 151,10
M 11
R 8
M 22
AW 175,11
AW 199,12
M 23
R 10
M 24
M 25
M 26
M 27
M 28
M 29
M 30
M 31
M 32
M 33
M 34
M 35
WL 4
M 36
M 37
M 38
M 39
M 40
M 41
M 42
M 43
WV 6
M 44
WL 0
M 45
s_barrier
R 24
M 46
mw 72
M 47
R 26
M 48
TY 106,9736
TY 110,23560
M 49
ad 84,1680
W 0
M 50
M 51
M 52
W 1
M 53
M 54
M 55
W 2
M 56
M 57
WL 1
AN 104,106
AN 105,107
M 58
mw 75
SH 106
SH 107
M 59
R 28
M 60
AN 106,106
AN 107,107
M 61
WL 2
AN 108,110
AN 109,111
M 62
SH 110
SH 111
M 63
AN 110,110
AN 111,111
M 64
TY 114,11272
TY 118,25096
M 65
ad 84,1686
M 66
W 0
M 67
M 68
M 69
W 1
M 70
ae 44,70,768
M 71
R 30
M 72
W 2
M 73
WL 3
AN 112,114
AN 113,115
M 74
SH 114
SH 115
M 75
mw 0
AN 114,114
AN 115,115
M 76
WL 2
AN 116,118
AN 117,119
M 77
SH 118
SH 119
M 78
AN 118,118
AN 119,119
M 79
TY 122,12808
TY 126,26632
M 80
ad 84,1728
W 0
M 81
s_bitcmp1_b32 s50, 12
s_cselect_b32 s51, 24576, 0
M 82
SA 250,1024
v_add_u32 v47, s51, v3
M 83
R 32
M 84
W 1
XL 30,256
M 85
XL 32,1024
M 86
XL 34,1792
M 87
W 2
M 88
WL 3
AN 120,122
AN 121,123
M 89
SH 122
SH 123
M 90
mw 3
AN 122,122
AN 123,123
M 91
WL 2
AN 124,126
AN 125,127
M 92
SH 126
SH 127
M 93
AN 126,126
AN 127,127
M 94
ad 84,1734
W 0
M 95
R 34
M 96
XL 36,2560
M 97
W 1
M 98
UL 6,22
UH 7,22
M 99
UL 8,23
UH 9,23
M 100
W 2
AR 10,152
M 101
AR 11,176
AD 10,6
M 102
AD 11,7
AD 132,8
M 103
bc 49,19,66,60
)"
R"(AD 156,9
M 104
mh 9
AW 152,10
AW 176,11
M 105
ad 66,3712
H 0
M 106
UL 6,24
UH 7,24
M 107
R 48
M 108
DG 1024
M 109
UL 8,25
UH 9,25
M 110
AR 10,153
AR 11,177
M 111
bc 48,19,64,56
AD 10,6
M 112
mh 12
AD 11,7
AD 133,8
M 113
ad 64,3712
H 0
M 114
AD 157,9
AW 153,10
M 115
AW 177,11
UL 6,26
M 116
H 1
UH 7,26
M 117
UL 8,27
UH 9,27
M 118
AR 10,154
AR 11,178
M 119
WL 0
R 50
M 120
H 2
AD 10,6
M 121
AD 11,7
AD 134,8
M 132
AD 158,9
AW 154,10
M 133
TX 58,9216
TX 62,23040
M 122
AW 178,11
UL 6,28
M 123
UH 7,28
UL 8,29
M 134
UH 9,29
AR 10,155
M 135
TY 66,9728
TY 70,23552
M 124
AR 11,179
AD 10,6
M 125
AD 11,7
AD 135,8
M 136
AD 159,9
AW 155,10
M 137
TX 74,10752
TX 78,24576
M 126
WL 4
Q 0
M 127
Q 2
M 138
Q 4
M 139
TY 82,11264
TY 86,25088
M 128
Q 6
M 129
WL 4
Q 8
M 140
Q 10
M 141
TX 90,12288
TX 94,26112
M 130
Q 12
M 131
R 52
M 142
Q 14
M 143
TY 98,12800
TY 102,26624
M 0
R 54
M 1
WL 10
Q 16
M 12
Q 18
M 13
Q 20
M 2
Q 22
M 3
WL 8
Q 24
M 14
Q 26
M 15
Q 28
M 4
Q 30
M 5
WL 6
Q 32
M 16
Q 34
M 17
Q 36
M 6
Q 38
M 7
WL 2
Q 40
M 18
Q 42
M 19
Q 44
M 8
Q 46
M 9
SA 252,1792
AW 179,11
M 20
M 21
M 10
M 11
R 56
M 22
M 23
R 58
M 24
M 25
M 26
M 27
M 28
M 29
M 30
M 31
M 32
M 33
M 34
M 35
WL 4
M 36
M 37
M 38
M 39
M 40
M 41
M 42
M 43
WV 6
M 44
WL 0
M 45
s_barrier
R 72
M 46
mw 24
M 47
R 74
M 48
TY 106,520
TY 110,14344
M 49
ad 84,1776
W 0
M 50
M 51
M 52
W 1
M 53
M 54
M 55
W 2
M 56
M 57
WL 1
AN 104,106
AN 105,107
M 58
mw 27
SH 106
SH 107
M 59
R 76
M 60
AN 106,106
AN 107,107
M 61
WL 2
AN 108,110
AN 109,111
M 62
SH 110
SH 111
M 63
AN 110,110
AN 111,111
M 64
TY 114,2056
TY 118,15880
M 65
ad 84,1782
M 66
W 0
M 67
M 68
M 69
W 1
M 70
M 71
R 78
M 72
W 2
M 73
WL 3
AN 112,114
AN 113,115
M 74
SH 114
SH 115
M 75
mw 48
AN 114,114
AN 115,115
M 76
WL 2
AN 116,118
AN 117,119
M 77
SH 118
SH 119
M 78
AN 118,118
AN 119,119
M 79
TY 122,3592
TY 126,17416
M 80
ad 84,1824
W 0
M 81
SA 254,2560
M 82
ae 44,70,768
M 83
R 80
M 84
s_bfe_u32 s51, s50, 0x1000d
W 1
v_mad_i32_i24 v47, v49, s51, v48
M 85
XL 38,256
M 86
XL 40,1024
M 87
W 2
XL 42,1792
M 88
WL 3
AN 120,122
AN 121,123
M 89
SH 122
SH 123
M 90
mw 51
AN 122,122
AN 123,123
M 91
WL 2
AN 124,126
AN 125,127
M 92
SH 126
SH 127
M 93
AN 126,126
AN 127,127
M 94
ad 84,1830
W 0
M 95
R 82
M 96
XL 44,2560
M 97
W 1
M 98
M 99
M 100
W 2
M 101
M 102
M 103
bc 49,20,66,60
M 104
mh 18
M 105
ad 66,3904
H 0
M 106
M 107
R 0
M 108
DG 1024
M 109
M 110
M 111
bc 48,20,64,56
M 112
mh 21
M 113
ad 64,3904
H 0
M 114
M 115
M 116
H 1
M 117
M 118
M 119
WL 2
R 2
M 120
H 2
M 121
M 132
M 133
TX 58,0
TX 62,13824
M 122
M 123
M 134
UL 6,30
UH 7,30
M 135
TY 66,512
TY 70,14336
M 124
UL 8,31
UH 9,31
M 125
AD 160,6
AD 184,7
M 136
AD 208,8
AD 232,9
M 137
TX 74,1536
TX 78,15360
M 126
WL 4
Q 0
M 127
Q 2
M 138
Q 4
M 139
TY 82,2048
TY 86,15872
M 128
Q 6
M 129
WL 4
Q 8
)"
R"(M 140
Q 10
M 141
TX 90,3072
TX 94,16896
M 130
Q 12
M 131
R 4
M 142
Q 14
M 143
TY 98,3584
TY 102,17408
M 0
R 6
M 1
WL 10
Q 16
M 12
Q 18
M 13
Q 20
M 2
Q 22
M 3
WL 8
Q 24
M 14
Q 26
M 15
Q 28
M 4
Q 30
M 5
WL 6
Q 32
M 16
Q 34
M 17
Q 36
M 6
Q 38
M 7
WL 2
Q 40
M 18
Q 42
M 19
Q 44
M 8
Q 46
M 9
UL 6,32
UH 7,32
M 20
UL 8,33
UH 9,33
M 21
AD 161,6
AD 185,7
M 10
AD 209,8
AD 233,9
M 11
R 8
M 22
UL 6,34
UH 7,34
M 23
R 10
M 24
UL 8,35
UH 9,35
M 25
AD 162,6
AD 186,7
M 26
AD 210,8
AD 234,9
M 27
UL 6,36
UH 7,36
M 28
UL 8,37
UH 9,37
M 29
AD 163,6
AD 187,7
M 30
AD 211,8
AD 235,9
M 31
M 32
M 33
M 34
M 35
WL 4
M 36
M 37
M 38
M 39
M 40
M 41
M 42
M 43
WV 5
M 44
WL 0
M 45
s_barrier
R 24
M 46
mw 72
M 47
R 26
M 48
TY 106,5128
TY 110,18952
M 49
ad 84,1872
W 0
M 50
M 51
M 52
W 1
M 53
M 54
M 55
W 2
M 56
M 57
WL 1
AN 104,106
AN 105,107
M 58
mw 75
SH 106
SH 107
M 59
R 28
M 60
AN 106,106
AN 107,107
M 61
WL 2
AN 108,110
AN 109,111
M 62
SH 110
SH 111
M 63
AN 110,110
AN 111,111
M 64
TY 114,6664
TY 118,20488
M 65
ad 84,1878
M 66
W 0
M 67
M 68
M 69
W 1
M 70
M 71
R 30
M 72
W 2
M 73
WL 3
AN 112,114
AN 113,115
M 74
SH 114
SH 115
M 75
mw 0
AN 114,114
AN 115,115
M 76
WL 2
AN 116,118
AN 117,119
M 77
SH 118
SH 119
M 78
AN 118,118
AN 119,119
M 79
TY 122,8200
TY 126,22024
M 80
ad 84,1920
W 0
M 81
ae 44,68,768
M 82
s_bitcmp1_b32 s50, 14
s_cselect_b32 s51, 24576, 0
v_add_u32 v47, s51, v3
M 83
R 32
M 84
W 1
XL 14,256
M 85
XL 16,1024
M 86
M 87
W 2
M 88
WL 3
AN 120,122
AN 121,123
M 89
SH 122
SH 123
M 90
mw 3
AN 122,122
AN 123,123
M 91
WL 2
AN 124,126
AN 125,127
M 92
SH 126
SH 127
M 93
AN 126,126
AN 127,127
M 94
ad 84,1926
W 0
M 95
R 34
M 96
XL 18,1792
M 97
W 1
XL 20,2560
M 98
M 99
M 100
W 2
M 101
M 102
M 103
bc 49,21,66,60
M 104
mh 0
M 105
ad 66,4096
H 0
M 106
M 107
R 48
M 108
DG 1024
M 109
M 110
M 111
bc 48,21,64,56
M 112
mh 3
M 113
ad 64,4096
H 0
M 114
M 115
M 116
H 1
M 117
M 118
M 119
WL 2
R 50
M 120
H 2
M 121
M 132
M 133
TX 58,4608
TX 62,18432
M 122
M 123
M 134
M 135
TY 66,5120
TY 70,18944
M 124
M 125
M 136
UL 6,38
UH 7,38
M 137
TX 74,6144
TX 78,19968
M 126
WL 4
Q 0
M 127
Q 2
M 138
Q 4
M 139
TY 82,6656
TY 86,20480
M 128
Q 6
M 129
WL 4
Q 8
M 140
Q 10
M 141
TX 90,7680
TX 94,21504
M 130
Q 12
M 131
R 52
M 142
Q 14
M 143
TY 98,8192
TY 102,22016
M 0
R 54
M 1
WL 10
Q 16
M 12
Q 18
M 13
Q 20
M 2
Q 22
M 3
WL 8
Q 24
M 14
Q 26
M 15
Q 28
M 4
Q 30
M 5
WL 6
Q 32
M 16
Q 34
M 17
Q 36
M 6
Q 38
M 7
WL 2
Q 40
M 18
Q 42
M 19
Q 44
M 8
Q 46
M 9
UL 8,39
UH 9,39
M 20
AD 164,6
AD 188,7
M 21
AD 212,8
AD 236,9
M 10
UL 6,40
UH 7,40
M 11
R 56
M 22
UL 8,41
UH 9,41
M 23
R 58
M 24
AD 165,6
AD 189,7
M 25
AD 213,8
AD 237,9
M 26
UL 6,42
UH 7,42
M 27
UL 8,43
UH 9,43
M 28
)"
R"(AD 166,6
AD 190,7
M 29
AD 214,8
AD 238,9
M 30
UL 6,44
UH 7,44
M 31
UL 8,45
UH 9,45
M 32
AD 167,6
AD 191,7
M 33
AD 215,8
AD 239,9
M 34
M 35
WL 2
M 36
M 37
M 38
M 39
M 40
M 41
M 42
M 43
WV 5
M 44
WL 0
M 45
s_barrier
R 72
M 46
mw 24
M 47
R 74
M 48
TY 106,9736
TY 110,23560
M 49
ad 84,1968
W 0
M 50
M 51
M 52
W 1
M 53
M 54
M 55
W 2
M 56
M 57
WL 1
AN 104,106
AN 105,107
M 58
mw 27
SH 106
SH 107
M 59
R 76
M 60
AN 106,106
AN 107,107
M 61
WL 2
AN 108,110
AN 109,111
M 62
SH 110
SH 111
M 63
AN 110,110
AN 111,111
M 64
TY 114,11272
TY 118,25096
M 65
ad 84,1974
M 66
W 0
M 67
M 68
M 69
W 1
M 70
M 71
R 78
M 72
W 2
M 73
WL 3
AN 112,114
AN 113,115
M 74
SH 114
SH 115
M 75
mw 48
AN 114,114
AN 115,115
M 76
WL 2
AN 116,118
AN 117,119
M 77
SH 118
SH 119
M 78
AN 118,118
AN 119,119
M 79
TY 122,12808
TY 126,26632
M 80
ad 84,2016
W 0
M 81
M 82
ae 44,68,768
M 83
R 80
M 84
s_bfe_u32 s51, s50, 0x1000f
W 1
v_mad_i32_i24 v47, v49, s51, v48
M 85
XL 22,256
M 86
M 87
W 2
M 88
WL 3
AN 120,122
AN 121,123
M 89
SH 122
SH 123
M 90
mw 51
AN 122,122
AN 123,123
M 91
WL 2
AN 124,126
AN 125,127
M 92
SH 126
SH 127
M 93
AN 126,126
AN 127,127
M 94
ad 84,2022
W 0
M 95
R 82
M 96
XL 24,1024
M 97
W 1
XL 26,1792
M 98
M 99
M 100
W 2
XL 28,2560
M 101
M 102
M 103
bc 49,22,66,60
M 104
mh 9
M 105
ad 66,4288
H 0
M 106
M 107
R 0
M 108
DG 1024
M 109
M 110
M 111
bc 48,22,64,56
M 112
mh 12
M 113
ad 64,4288
H 0
M 114
M 115
M 116
H 1
M 117
M 118
M 119
WL 2
R 2
M 120
H 2
M 121
M 132
M 133
TX 58,9216
TX 62,23040
M 122
M 123
M 134
M 135
TY 66,9728
TY 70,23552
M 124
M 125
M 136
M 137
TX 74,10752
TX 78,24576
M 126
WL 4
Q 0
M 127
Q 2
M 138
Q 4
M 139
TY 82,11264
TY 86,25088
M 128
Q 6
M 129
WL 4
Q 8
M 140
Q 10
M 141
TX 90,12288
TX 94,26112
M 130
Q 12
M 131
R 4
M 142
Q 14
M 143
TY 98,12800
TY 102,26624
M 0
R 6
M 1
WL 10
Q 16
M 12
Q 18
M 13
Q 20
M 2
Q 22
M 3
WL 8
Q 24
M 14
Q 26
M 15
Q 28
M 4
Q 30
M 5
WL 6
Q 32
M 16
Q 34
M 17
Q 36
M 6
Q 38
M 7
WL 2
Q 40
M 18
Q 42
M 19
Q 44
M 8
Q 46
M 9
UL 6,14
UH 7,14
M 20
UL 8,15
UH 9,15
M 21
AD 168,6
AD 192,7
M 10
AD 216,8
AD 240,9
M 11
R 8
M 22
UL 6,16
UH 7,16
M 23
R 10
M 24
UL 8,17
UH 9,17
M 25
AD 169,6
AD 193,7
M 26
AD 217,8
AD 241,9
M 27
UL 6,18
UH 7,18
M 28
UL 8,19
UH 9,19
M 29
AD 170,6
AD 194,7
M 30
AD 218,8
AD 242,9
M 31
UL 6,20
UH 7,20
M 32
UL 8,21
UH 9,21
M 33
AD 171,6
AD 195,7
M 34
AD 219,8
AD 243,9
M 35
WL 2
M 36
M 37
M 38
M 39
M 40
M 41
M 42
M 43
WV 6
M 44
WL 0
M 45
s_barrier
R 24
M 46
mw 72
M 47
R 26
M 48
TY 106,520
TY 110,14344
M 49
ad 84,2064
W 0
M 50
M 51
M 52
W 1
M 53
M 54
M 55
W 2
M 56
M 57
WL 1
AN 104,106
AN 105,107
M 58
mw 75
SH 106
SH 107
M 59
R 28
M 60
AN 106,106
AN 107,107
M 61
WL 2
AN 108,110
AN 109,111
M 62
SH 110
SH 111
M 63
AN 110,110
AN 111,111
M 64
TY 114,2056
TY 118,15880
)"
R"(M 65
ad 84,2070
M 66
W 0
M 67
M 68
M 69
W 1
M 70
M 71
R 30
M 72
W 2
M 73
WL 3
AN 112,114
AN 113,115
M 74
SH 114
SH 115
M 75
mw 0
AN 114,114
AN 115,115
M 76
WL 2
AN 116,118
AN 117,119
M 77
SH 118
SH 119
M 78
AN 118,118
AN 119,119
M 79
TY 122,3592
TY 126,17416
M 80
ad 84,2112
W 0
M 81
M 82
M 83
R 32
M 84
W 1
M 85
M 86
ae 44,68,864
M 87
W 2
M 88
WL 3
AN 120,122
AN 121,123
M 89
SH 122
SH 123
M 90
mw 3
AN 122,122
AN 123,123
M 91
WL 2
AN 124,126
AN 125,127
M 92
SH 126
SH 127
M 93
AN 126,126
AN 127,127
M 94
ad 84,2118
W 0
M 95
R 34
M 96
s_bitcmp1_b32 s50, 16
s_cselect_b32 s51, 24576, 0
M 97
W 1
v_add_u32 v47, s51, v3
M 98
XL 30,256
M 99
XL 32,1024
M 100
W 2
XL 34,1792
M 101
M 102
M 103
bc 49,23,66,60
M 104
mh 18
XL 36,2560
M 105
ad 66,4480
H 0
M 106
M 107
R 48
M 108
DG 1024
M 109
M 110
M 111
bc 48,23,64,56
M 112
mh 21
M 113
ad 64,4480
H 0
M 114
M 115
M 116
H 1
M 117
M 118
M 119
WL 2
R 50
M 120
H 2
M 121
M 132
M 133
TX 58,0
TX 62,13824
M 122
M 123
M 134
M 135
TY 66,512
TY 70,14336
M 124
M 125
M 136
M 137
TX 74,1536
TX 78,15360
M 126
WL 4
Q 0
M 127
Q 2
M 138
Q 4
M 139
TY 82,2048
TY 86,15872
M 128
Q 6
M 129
WL 4
Q 8
M 140
Q 10
M 141
TX 90,3072
TX 94,16896
M 130
Q 12
M 131
R 52
M 142
Q 14
M 143
TY 98,3584
TY 102,17408
M 0
R 54
M 1
WL 10
Q 16
M 12
Q 18
M 13
Q 20
M 2
Q 22
M 3
WL 8
Q 24
M 14
Q 26
M 15
Q 28
M 4
Q 30
M 5
WL 6
Q 32
M 16
Q 34
M 17
Q 36
M 6
Q 38
M 7
WL 2
Q 40
M 18
Q 42
M 19
Q 44
M 8
Q 46
M 9
UL 6,22
UH 7,22
M 20
UL 8,23
UH 9,23
M 21
AD 172,6
AD 196,7
M 10
AD 220,8
AD 244,9
M 11
R 56
M 22
UL 6,24
UH 7,24
M 23
R 58
M 24
UL 8,25
UH 9,25
M 25
AD 173,6
AD 197,7
M 26
AD 221,8
AD 245,9
M 27
UL 6,26
UH 7,26
M 28
UL 8,27
UH 9,27
M 29
AD 174,6
AD 198,7
M 30
AD 222,8
AD 246,9
M 31
WV 26
UL 6,28
UH 7,28
M 32
UL 8,29
UH 9,29
M 33
AD 175,6
AD 199,7
M 34
AD 223,8
AD 247,9
M 35
WL 2
M 36
M 37
M 38
M 39
M 40
M 41
M 42
M 43
WV 7
M 44
WL 0
M 45
s_barrier
R 72
M 46
mw 24
M 47
R 74
M 48
TY 106,5128
TY 110,18952
M 49
ad 84,2160
W 0
M 50
M 51
M 52
W 1
M 53
M 54
M 55
W 2
M 56
M 57
WL 1
AN 104,106
AN 105,107
M 58
mw 27
SH 106
SH 107
M 59
R 76
M 60
AN 106,106
AN 107,107
M 61
WL 2
AN 108,110
AN 109,111
M 62
SH 110
SH 111
M 63
AN 110,110
AN 111,111
M 64
TY 114,6664
TY 118,20488
M 65
ad 84,2166
M 66
W 0
M 67
M 68
M 69
W 1
M 70
M 71
R 78
M 72
W 2
M 73
WL 3
AN 112,114
AN 113,115
M 74
SH 114
SH 115
M 75
mw 48
AN 114,114
AN 115,115
M 76
WL 2
AN 116,118
AN 117,119
M 77
SH 118
SH 119
M 78
AN 118,118
AN 119,119
M 79
TY 122,8200
TY 126,22024
M 80
ad 84,2208
W 0
M 81
M 82
M 83
R 80
M 84
W 1
M 85
M 86
M 87
W 2
M 88
WL 3
AN 120,122
AN 121,123
M 89
SH 122
SH 123
M 90
mw 51
AN 122,122
AN 123,123
M 91
WL 2
AN 124,126
AN 125,127
M 92
SH 126
SH 127
M 93
AN 126,126
AN 127,127
M 94
ad 84,2214
W 0
M 95
R 82
M 96
)"
R"(ae 44,68,864
M 97
s_bfe_u32 s51, s50, 0x10011
W 1
v_mad_i32_i24 v47, v49, s51, v48
M 98
XL 38,256
M 99
XL 40,1024
M 100
W 2
M 101
M 102
XL 42,1792
M 103
bc 48,0,64,56
M 104
mh 3
M 105
ad 64,64
H 0
M 106
M 107
R 0
M 108
H 1
XL 44,2560
M 109
M 110
M 111
H 2
M 112
M 113
M 114
M 115
M 116
M 117
M 118
M 119
WL 2
R 2
M 120
M 121
M 132
M 133
TX 58,4608
TX 62,18432
M 122
M 123
M 134
M 135
TY 66,5120
TY 70,18944
M 124
M 125
M 136
M 137
TX 74,6144
TX 78,19968
M 126
WL 4
Q 0
M 127
Q 2
M 138
Q 4
M 139
TY 82,6656
TY 86,20480
M 128
Q 6
M 129
WL 4
Q 8
M 140
Q 10
M 141
TX 90,7680
TX 94,21504
M 130
Q 12
M 131
R 4
M 142
Q 14
M 143
TY 98,8192
TY 102,22016
M 0
R 6
M 1
WL 10
Q 16
M 12
Q 18
M 13
Q 20
M 2
Q 22
M 3
WL 8
Q 24
M 14
Q 26
M 15
Q 28
M 4
Q 30
M 5
WL 6
Q 32
M 16
Q 34
M 17
Q 36
M 6
Q 38
M 7
WL 2
Q 40
M 18
Q 42
M 19
Q 44
M 8
Q 46
M 9
UL 6,30
UH 7,30
M 20
UL 8,31
UH 9,31
M 21
AD 176,6
AD 200,7
M 10
AD 224,8
AD 248,9
M 11
R 8
M 22
UL 6,32
UH 7,32
M 23
R 10
M 24
UL 8,33
UH 9,33
M 25
AD 177,6
AD 201,7
M 26
AD 225,8
AD 249,9
M 27
WV 24
UL 6,34
UH 7,34
M 28
UL 8,35
UH 9,35
M 29
AD 178,6
AD 202,7
M 30
AD 226,8
AD 250,9
M 31
UL 6,36
UH 7,36
M 32
UL 8,37
UH 9,37
M 33
AD 179,6
AD 203,7
M 34
AD 227,8
AD 251,9
M 35
WL 2
M 36
M 37
M 38
M 39
M 40
M 41
M 42
M 43
WV 13
M 44
WL 0
M 45
s_barrier
R 24
M 46
mw 72
M 47
R 26
M 48
TY 106,9736
TY 110,23560
M 49
ad 84,2256
W 0
M 50
M 51
M 52
W 1
M 53
M 54
M 55
W 2
M 56
M 57
WL 1
AN 104,106
AN 105,107
M 58
mw 75
SH 106
SH 107
M 59
R 28
M 60
AN 106,106
AN 107,107
M 61
WL 2
AN 108,110
AN 109,111
M 62
SH 110
SH 111
M 63
AN 110,110
AN 111,111
M 64
TY 114,11272
TY 118,25096
M 65
ad 84,2262
M 66
W 0
M 67
M 68
M 69
W 1
M 70
M 71
R 30
M 72
W 2
M 73
WL 3
AN 112,114
AN 113,115
M 74
SH 114
SH 115
M 75
mw 0
AN 114,114
AN 115,115
M 76
WL 2
AN 116,118
AN 117,119
M 77
SH 118
SH 119
M 78
AN 118,118
AN 119,119
M 79
TY 122,12808
TY 126,26632
M 80
ad 84,0
W 0
M 81
M 82
M 83
R 32
M 84
W 1
M 85
M 86
M 87
W 2
M 88
WL 3
AN 120,122
AN 121,123
M 89
SH 122
SH 123
M 90
mw 3
AN 122,122
AN 123,123
M 91
WL 2
AN 124,126
AN 125,127
M 92
SH 126
SH 127
M 93
AN 126,126
AN 127,127
M 94
ad 84,6
W 0
M 95
R 34
M 96
M 97
W 1
M 98
M 99
M 100
W 2
M 101
M 102
M 103
bc 48,1,64,56
M 104
mh 15
M 105
ad 64,259
H 0
M 106
M 107
M 108
DG 1024
M 109
M 110
M 111
M 112
M 113
M 114
M 115
WV 19
M 116
WL 0
M 117
s_barrier
R 48
M 118
mw 24
M 119
R 50
M 120
ad 84,48
W 0
M 121
M 132
M 133
TX 58,9216
TX 62,23040
M 122
W 1
M 123
M 134
M 135
TY 66,9728
TY 70,23552
M 124
W 2
M 125
M 136
M 137
TX 74,10752
TX 78,24576
M 126
mw 27
WL 4
Q 0
M 127
Q 2
M 138
Q 4
M 139
TY 82,11264
TY 86,25088
M 128
Q 6
M 129
WL 4
Q 8
M 140
Q 10
M 141
TX 90,12288
TX 94,26112
M 130
Q 12
M 131
R 52
)"
R"(M 142
Q 14
M 143
TY 98,12800
TY 102,26624
M 0
R 54
M 1
WL 10
Q 16
M 12
Q 18
M 13
Q 20
M 2
Q 22
M 3
WL 8
Q 24
M 14
Q 26
M 15
Q 28
M 4
Q 30
M 5
WL 6
Q 32
M 16
Q 34
M 17
Q 36
M 6
Q 38
M 7
WL 2
Q 40
M 18
Q 42
M 19
Q 44
M 8
Q 46
M 9
ad 84,54
M 20
W 0
UL 6,38
M 21
UH 7,38
UL 8,39
M 10
UH 9,39
AD 180,6
M 11
R 56
M 22
W 1
AD 204,7
M 23
R 58
M 24
AD 228,8
AD 252,9
M 25
UL 6,40
UH 7,40
M 26
W 2
UL 8,41
BV 52,4608
M 27
UH 9,41
AD 181,6
M 28
AD 205,7
AD 229,8
M 29
WV 24
AD 253,9
UL 6,42
M 30
UH 7,42
UL 8,43
M 31
UH 9,43
AD 182,6
M 32
AD 206,7
AD 230,8
M 33
WV 21
AD 254,9
UL 6,44
M 34
UH 7,44
UL 8,45
M 35
WL 2
UH 9,45
AD 183,6
M 36
AD 207,7
AD 231,8
M 37
AD 255,9
M 38
M 39
M 40
ds_read_b64 v[12:13], v4 offset:4864
ds_read_b64 v[46:47], v4 offset:4872
M 41
AR 6,36
AR 7,60
M 42
AR 8,84
AR 9,108
M 43
WL 2
EF 6,52
EF 7,53
M 44
WV 14
EF 8,54
EF 9,55
M 45
s_barrier
R 72
M 46
mw 48
CV 14,6,7
CV 15,8,9
M 47
R 74
M 48
TY 114,2056
TY 118,15880
M 49
ad 84,96
W 0
M 50
AR 6,37
AR 7,61
M 51
AR 8,85
AR 9,109
M 52
EF 6,52
EF 7,53
M 53
W 1
EF 8,54
M 54
EF 9,55
CV 16,6,7
M 55
CV 17,8,9
AR 6,38
M 56
AR 7,62
AR 8,86
M 57
WL 1
AN 112,114
AN 113,115
M 58
SH 114
SH 115
M 59
R 76
M 60
AN 114,114
AN 115,115
M 61
WL 2
AN 116,118
AN 117,119
M 62
SH 118
SH 119
M 63
AN 118,118
AN 119,119
M 64
TY 122,3592
TY 126,17416
M 65
W 2
AR 9,110
M 66
EF 6,52
EF 7,53
M 67
EF 8,54
EF 9,55
M 68
CV 18,6,7
CV 19,8,9
M 69
mw 51
AR 6,39
AR 7,63
M 70
ad 84,102
W 0
M 71
R 78
M 72
AR 8,87
AR 9,111
M 73
WL 3
AN 120,122
AN 121,123
M 74
SH 122
SH 123
M 75
AN 122,122
AN 123,123
M 76
WL 2
AN 124,126
AN 125,127
M 77
SH 126
SH 127
M 78
AN 126,126
AN 127,127
M 79
W 1
EF 6,52
M 80
EF 7,53
EF 8,54
M 81
EF 9,55
CV 20,6,7
M 82
CV 21,8,9
M 83
R 80
M 84
W 2
AR 6,40
M 85
ae 42,74,768
AR 7,64
M 86
s_bitcmp1_b32 s50, 12
s_cselect_b32 s52, 24576, 0
v_add_u32 v50, s52, v3
M 87
ST 14,0
AR 8,88
M 88
AR 9,112
EF 6,52
M 89
EF 7,53
EF 8,54
M 90
EF 9,55
CV 22,6,7
M 91
ST 16,768
CV 23,8,9
M 92
AR 6,41
AR 7,65
M 93
AR 8,89
AR 9,113
M 94
EF 6,52
EF 7,53
M 95
R 82
M 96
ST 18,1536
EF 8,54
M 97
EF 9,55
CV 24,6,7
M 98
CV 25,8,9
AR 6,42
M 99
AR 7,66
AR 8,90
M 100
ST 20,2304
AR 9,114
M 101
EF 6,52
EF 7,53
M 102
EF 8,54
EF 9,55
M 103
CV 26,6,7
CV 27,8,9
M 104
AR 6,43
AR 7,67
M 105
AR 8,91
AR 9,115
M 106
EF 6,52
EF 7,53
M 107
WL 2
EF 8,54
EF 9,55
M 108
CV 28,6,7
CV 29,8,9
M 109
ae 42,74,768
BV 52,5120
M 110
s_bfe_u32 s52, s50, 0x1000d
v_mad_i32_i24 v50, v49, s52, v48
ST 22,0
M 111
AR 6,132
AR 7,156
M 112
AR 8,180
EF 6,12
M 113
EF 7,13
EF 8,46
M 114
ST 24,768
EF 136,47
M 115
WV 20
CV 30,6,7
CV 31,8,136
M 116
WL 1
AR 6,133
AR 7,157
M 117
s_barrier
R 0
M 118
mw 72
ST 26,1536
AR 8,181
M 119
R 2
M 120
ad 84,144
W 0
M 121
EF 6,12
EF 7,13
M 132
ST 28,2304
EF 8,46
M 133
EF 137,47
CV 32,6,7
M 122
W 1
CV 33,8,137
M 123
AR 6,134
AR 7,158
M 134
AR 8,182
EF 6,12
M 135
EF 7,13
EF 8,46
M 124
W 2
EF 138,47
M 125
CV 34,6,7
CV 35,8,138
M 136
AR 6,135
AR 7,159
M 137
TX 74,1536
TX 78,15360
M 126
mw 75
AR 8,183
EF 6,12
M 127
ad 84,150
W 0
M 138
EF 7,13
)"
R"(EF 8,46
M 139
TY 82,2048
TY 86,15872
M 128
EF 139,47
CV 36,6,7
M 129
W 1
CV 37,8,139
M 140
ae 42,74,768
M 141
WL 2
TX 90,3072
TX 94,16896
P 16
M 130
Q 17
P 19
M 131
R 4
P 20
M 142
Q 21
P 23
M 143
WL 4
TY 98,3584
TY 102,17408
P 24
M 4
R 6
P 25
M 5
Q 26
P 28
M 16
Q 29
P 31
M 17
WL 6
Q 32
M 6
Q 34
M 7
Q 36
M 18
Q 38
M 19
WL 2
Q 40
M 8
Q 42
M 9
Q 44
M 20
Q 46
M 21
W 2
AR 6,136
M 10
s_bitcmp1_b32 s50, 12
s_cselect_b32 s52, 24576, 0
M 11
R 8
M 22
v_add_u32 v50, s52, v3
ST 30,128
M 23
R 10
M 28
AR 7,160
AR 8,184
M 29
EF 6,12
EF 7,13
M 30
ST 32,896
EF 8,46
M 31
EF 140,47
CV 38,6,7
M 32
CV 39,8,140
AR 6,137
M 33
AR 7,161
AR 8,185
M 34
ST 34,1664
EF 6,12
M 35
WL 4
EF 7,13
EF 8,46
M 40
EF 141,47
CV 40,6,7
M 41
CV 41,8,141
AR 6,138
M 42
ST 36,2432
AR 7,162
M 43
AR 8,186
EF 6,12
M 44
WV 24
EF 7,13
EF 8,46
M 45
WL 0
EF 142,47
CV 42,6,7
M 46
s_barrier
R 24
M 47
R 26
M 52
TY 122,8200
TY 126,22024
M 53
CV 43,8,142
AR 6,139
M 54
AR 7,163
AR 8,187
M 55
EF 6,12
EF 7,13
M 56
EF 8,46
EF 143,47
M 57
CV 44,6,7
CV 45,8,143
M 58
ae 42,74,768
ds_read_b64 v[12:13], v4 offset:4608
ds_read_b64 v[46:47], v4 offset:4616
M 59
R 28
M 64
WL 5
EF 160,52
EF 184,53
M 65
s_bfe_u32 s52, s50, 0x1000d
AN 120,122
AN 121,123
M 66
SH 122
SH 123
M 67
AN 122,122
AN 123,123
M 68
WL 4
AN 124,126
AN 125,127
M 69
SH 126
SH 127
M 70
AN 126,126
AN 127,127
M 71
R 30
M 76
v_mad_i32_i24 v50, v49, s52, v48
ST 38,128
M 77
EF 208,54
EF 232,55
M 78
CV 14,160,184
CV 15,208,232
M 79
EF 161,52
EF 185,53
M 80
ST 40,896
EF 209,54
M 81
EF 233,55
CV 16,161,185
M 82
CV 17,209,233
EF 162,52
M 83
R 32
M 88
TX 58,7680
TX 62,21504
M 89
ST 42,1664
EF 186,53
M 90
EF 210,54
EF 234,55
M 91
CV 18,162,186
CV 19,210,234
M 92
TY 66,8192
TY 70,22016
M 93
ST 44,2432
EF 163,52
M 94
EF 187,53
EF 211,54
M 95
WL 6
R 34
M 100
EF 235,55
CV 20,163,187
M 101
WL 4
PA_ 56,120,58
PA_ 57,121,59
M 102
PB_ 58,120,58
PB_ 59,121,59
M 103
PA_ 60,124,62
PA_ 61,125,63
M 104
PB_ 62,124,62
PB_ 63,125,63
M 105
WL 2
PA_ 64,122,66
PA_ 65,123,67
M 106
PB_ 66,122,66
PB_ 67,123,67
M 107
PA_ 68,126,70
PA_ 69,127,71
M 112
PB_ 70,126,70
PB_ 71,127,71
M 113
CV 21,211,235
M 114
ae 42,74,768
EF 164,52
M 115
s_bitcmp1_b32 s50, 12
s_cselect_b32 s52, 24576, 0
v_add_u32 v50, s52, v3
M 116
WV 22
ST 14,256
EF 188,53
M 117
WL 0
EF 212,54
EF 236,55
M 118
s_barrier
R 48
M 119
R 50
M 124
ST 16,1024
CV 22,164,188
M 125
CV 23,212,236
EF 165,52
M 136
EF 189,53
EF 213,54
M 137
EF 237,55
CV 24,165,189
M 126
ST 18,1792
CV 25,213,237
M 127
EF 166,52
EF 190,53
M 138
EF 214,54
EF 238,55
M 139
CV 26,166,190
CV 27,214,238
M 128
ST 20,2560
EF 167,52
M 129
EF 191,53
EF 215,54
M 140
EF 239,55
CV 28,167,191
M 141
CV 29,215,239
M 130
ae 42,74,768
BV 52,4864
M 131
s_bfe_u32 s52, s50, 0x1000d
R 52
M 142
v_mad_i32_i24 v50, v49, s52, v48
ST 22,256
M 143
WL 3
R 54
MA 52,0,56
AR 6,44
AR 7,68
MA 52,4,60
AR 8,92
AR 9,116
MA 76,8,56
ST 24,1024
EF 6,12
MA 76,12,60
EF 7,13
EF 8,46
MA 56,0,64
EF 9,47
CV 30,6,7
MA 56,4,68
R 56
MA 80,8,64
ST 26,1792
CV 31,8,9
MA 80,12,68
WL 4
R 58
MA 100,16,56
AR 6,45
AR 7,69
MA 100,20,60
AR 8,93
AR 9,117
MA 104,16,64
ST 28,2560
EF 6,12
MA 104,20,68
WL 4
EF 7,13
EF 8,46
MA 124,24,56
WV 16
EF 9,47
CV 32,6,7
MA 124,28,60
WL 0
CV 33,8,9
AR 6,46
MA 128,24,64
s_barrier
R 72
MA 128,28,68
R 74
MA 148,0,56
AR 7,70
AR 8,94
MA 148,4,60
AR 9,118
EF 6,12
MA 152,0,64
EF 7,13
EF 8,46
MA 152,4,68
R 76
MA 172,8,56
EF 9,47
CV 34,6,7
MA 172,12,60
CV 35,8,9
AR 6,47
MA 176,8,64
AR 7,71
AR 8,95
MA 176,12,68
WL 4
R 78
MA 196,16,56
AR 9,119
EF 6,12
MA 196,20,60
EF 7,13
EF 8,46
MV 132,16,64
EF 9,47
CV 36,6,7
MV 132,20,68
WL 4
R 80
)"
R"(MV 152,24,56
CV 37,8,9
MV 152,28,60
ae 42,72,768
AR 6,48
AR 7,72
MV 156,24,64
s_bitcmp1_b32 s50, 14
s_cselect_b32 s52, 24576, 0
v_add_u32 v50, s52, v3
AR 8,96
MV 156,28,68
WL 4
R 82
AR 9,120
MV 176,0,56
ST 30,0
EF 6,12
EF 7,13
MV 176,4,60
EF 8,46
EF 9,47
CV 38,6,7
MV 180,0,64
CV 39,8,9
AR 6,49
AR 7,73
MV 180,4,68
WL 4
AR 8,97
AR 9,121
EF 6,12
MV 200,8,56
ST 32,768
EF 7,13
EF 8,46
MV 200,12,60
EF 9,47
CV 40,6,7
CV 41,8,9
MV 204,8,64
AR 6,50
AR 7,74
AR 8,98
MV 204,12,68
WL 0
AR 9,122
EF 6,12
EF 7,13
MV 224,16,56
ST 34,1536
EF 8,46
EF 9,47
MV 224,20,60
CV 42,6,7
CV 43,8,9
AR 6,51
MV 248,24,56
AR 7,75
AR 8,99
AR 9,123
MV 248,28,60
EF 6,12
EF 7,13
EF 8,46
MV 228,16,64
ST 36,2304
EF 9,47
CV 44,6,7
MV 228,20,68
CV 45,8,9
MV 252,24,64
WV 14
ds_read_b64 v[12:13], v4 offset:5120
ds_read_b64 v[46:47], v4 offset:5128
AR 6,140
AR 7,164
AR 8,188
EF 6,52
MV 252,28,68
ae 42,72,768
s_bfe_u32 s52, s50, 0x1000f
v_mad_i32_i24 v50, v49, s52, v48
ST 38,0
ST 40,768
ST 42,1536
ST 44,2304
EF 7,53
EF 8,54
EF 144,55
CV 14,6,7
CV 15,8,144
AR 6,141
AR 7,165
AR 8,189
EF 6,52
EF 7,53
EF 8,54
EF 145,55
CV 16,6,7
CV 17,8,145
AR 6,142
AR 7,166
AR 8,190
EF 6,52
EF 7,53
EF 8,54
EF 146,55
CV 18,6,7
CV 19,8,146
AR 6,143
AR 7,167
AR 8,191
EF 6,52
EF 7,53
EF 8,54
EF 147,55
CV 20,6,7
CV 21,8,147
ae 42,72,768
s_bitcmp1_b32 s50, 14
s_cselect_b32 s52, 24576, 0
v_add_u32 v50, s52, v3
ST 14,128
ST 16,896
ST 18,1664
ST 20,2432
AR 6,144
AR 7,168
AR 8,192
EF 6,52
EF 7,53
EF 8,54
EF 148,55
CV 22,6,7
CV 23,8,148
AR 6,145
AR 7,169
AR 8,193
EF 6,52
EF 7,53
EF 8,54
EF 149,55
CV 24,6,7
CV 25,8,149
AR 6,146
AR 7,170
AR 8,194
EF 6,52
EF 7,53
EF 8,54
EF 150,55
CV 26,6,7
CV 27,8,150
AR 6,147
AR 7,171
AR 8,195
EF 6,52
EF 7,53
EF 8,54
EF 151,55
CV 28,6,7
CV 29,8,151
ae 42,72,768
s_bfe_u32 s52, s50, 0x1000f
v_mad_i32_i24 v50, v49, s52, v48
ST 22,128
ST 24,896
ST 26,1664
ST 28,2432
BV 52,4608
WL 1
EF 168,12
EF 192,13
EF 216,46
EF 240,47
CV 30,168,192
CV 31,216,240
EF 169,12
EF 193,13
EF 217,46
EF 241,47
CV 32,169,193
CV 33,217,241
EF 170,12
EF 194,13
EF 218,46
EF 242,47
CV 34,170,194
CV 35,218,242
EF 171,12
EF 195,13
EF 219,46
EF 243,47
CV 36,171,195
CV 37,219,243
ae 42,72,768
s_bitcmp1_b32 s50, 14
s_cselect_b32 s52, 24576, 0
v_add_u32 v50, s52, v3
ST 30,256
ST 32,1024
ST 34,1792
ST 36,2560
EF 172,12
EF 196,13
EF 220,46
EF 244,47
CV 38,172,196
CV 39,220,244
EF 173,12
EF 197,13
EF 221,46
EF 245,47
CV 40,173,197
CV 41,221,245
EF 174,12
EF 198,13
EF 222,46
EF 246,47
CV 42,174,198
CV 43,222,246
EF 175,12
EF 199,13
EF 223,46
EF 247,47
CV 44,175,199
CV 45,223,247
ae 42,72,768
s_bfe_u32 s52, s50, 0x1000f
v_mad_i32_i24 v50, v49, s52, v48
ST 38,256
ST 40,1024
ST 42,1792
ST 44,2560
ds_read_b64 v[12:13], v4 offset:4864
ds_read_b64 v[46:47], v4 offset:4872
AR 6,52
AR 7,76
AR 8,100
AR 9,124
WL 2
EF 6,52
EF 7,53
EF 8,54
EF 9,55
CV 14,6,7
CV 15,8,9
AR 6,53
AR 7,77
AR 8,101
AR 9,125
EF 6,52
EF 7,53
EF 8,54
EF 9,55
CV 16,6,7
CV 17,8,9
AR 6,54
AR 7,78
AR 8,102
AR 9,126
EF 6,52
EF 7,53
EF 8,54
EF 9,55
CV 18,6,7
CV 19,8,9
AR 6,55
AR 7,79
AR 8,103
AR 9,127
EF 6,52
EF 7,53
EF 8,54
EF 9,55
CV 20,6,7
CV 21,8,9
ae 42,72,864
s_bitcmp1_b32 s50, 16
s_cselect_b32 s52, 24576, 0
v_add_u32 v50, s52, v3
ST 14,0
ST 16,768
ST 18,1536
ST 20,2304
AR 6,56
AR 7,80
AR 8,104
AR 9,128
EF 6,52
EF 7,53
EF 8,54
EF 9,55
CV 22,6,7
CV 23,8,9
AR 6,57
AR 7,81
AR 8,105
AR 9,129
EF 6,52
EF 7,53
EF 8,54
EF 9,55
CV 24,6,7
CV 25,8,9
AR 6,58
AR 7,82
AR 8,106
AR 9,130
EF 6,52
EF 7,53
EF 8,54
EF 9,55
CV 26,6,7
CV 27,8,9
AR 6,59
AR 7,83
AR 8,107
AR 9,131
EF 6,52
EF 7,53
EF 8,54
EF 9,55
CV 28,6,7
CV 29,8,9
ae 42,72,864
s_bfe_u32 s52, s50, 0x10011
v_mad_i32_i24 v50, v49, s52, v48
ST 22,0
ST 24,768
ST 26,1536
ST 28,2304
BV 52,5120
AR 6,148
AR 7,172
AR 8,196
WL 1
EF 6,12
EF 7,13
EF 8,46
EF 152,47
CV 30,6,7
CV 31,8,152
AR 6,149
AR 7,173
AR 8,197
EF 6,12
EF 7,13
EF 8,46
EF 153,47
CV 32,6,7
CV 33,8,153
AR 6,150
AR 7,174
AR 8,198
EF 6,12
EF 7,13
EF 8,46
EF 154,47
CV 34,6,7
CV 35,8,154
AR 6,151
AR 7,175
AR 8,199
EF 6,12
EF 7,13
EF 8,46
EF 155,47
CV 36,6,7
CV 37,8,155
ae 42,72,864
s_bitcmp1_b32 s50, 16
s_cselect_b32 s52, 24576, 0
v_add_u32 v50, s52, v3
ST 30,128
ST 32,896
ST 34,1664
ST 36,2432
AR 6,152
AR 7,176
EF 6,12
EF 7,13
EF 132,46
EF 156,47
CV 38,6,7
CV 39,132,156
AR 6,153
AR 7,177
EF 6,12
EF 7,13
EF 133,46
EF 157,47
CV 40,6,7
CV 41,133,157
AR 6,154
AR 7,178
EF 6,12
EF 7,13
EF 134,46
)"
R"(EF 158,47
CV 42,6,7
CV 43,134,158
AR 6,155
AR 7,179
EF 6,12
EF 7,13
EF 135,46
EF 159,47
CV 44,6,7
CV 45,135,159
ae 42,72,864
s_bfe_u32 s52, s50, 0x10011
v_mad_i32_i24 v50, v49, s52, v48
ST 38,128
ST 40,896
ST 42,1664
ST 44,2432
WL 0
EF 176,52
EF 200,53
EF 224,54
EF 248,55
CV 14,176,200
CV 15,224,248
EF 177,52
EF 201,53
EF 225,54
EF 249,55
CV 16,177,201
CV 17,225,249
EF 178,52
EF 202,53
EF 226,54
EF 250,55
CV 18,178,202
CV 19,226,250
EF 179,52
EF 203,53
EF 227,54
EF 251,55
CV 20,179,203
CV 21,227,251
ae 42,72,864
s_bitcmp1_b32 s50, 16
s_cselect_b32 s52, 24576, 0
v_add_u32 v50, s52, v3
ST 14,256
ST 16,1024
ST 18,1792
ST 20,2560
EF 180,52
EF 204,53
EF 228,54
EF 252,55
CV 22,180,204
CV 23,228,252
EF 181,52
EF 205,53
EF 229,54
EF 253,55
CV 24,181,205
CV 25,229,253
EF 182,52
EF 206,53
EF 230,54
EF 254,55
CV 26,182,206
CV 27,230,254
EF 183,52
EF 207,53
EF 231,54
EF 255,55
CV 28,183,207
CV 29,231,255
ae 42,72,864
s_bfe_u32 s52, s50, 0x10011
v_mad_i32_i24 v50, v49, s52, v48
ST 22,256
ST 24,1024
ST 26,1792
ST 28,2560
s_waitcnt vmcnt(0) lgkmcnt(0)
.purgem MA
.purgem MV
.purgem NA
.purgem NV
.purgem AN
.purgem SH
.purgem PA_
.purgem PB_
.purgem AW
.purgem AR
.purgem UL
.purgem UH
.purgem AD
.purgem EF
.purgem TY
.purgem TX
.purgem RA
.purgem RC
.purgem BV
.purgem BA
.purgem PA
.purgem DF
.purgem DW
.purgem DR
.purgem DH
.purgem DG
.purgem XL
.purgem ST
.purgem SA
.purgem CV
.purgem RS
.purgem PM
.purgem PF
.purgem FS
.purgem FL
.purgem FB
.purgem FC
.purgem FG
.purgem FX
.purgem WL
.purgem WV
.purgem M
.purgem R1
.purgem R
.purgem P
.purgem Q
.purgem mw
.purgem mh
.purgem ad
.purgem ae
.purgem bc
.purgem W
.purgem H
)"
      : [flg] "=&s"(flg) : K2_OPS : K2_CLOB);
  if (flg) {
    const unsigned lane = __builtin_amdgcn_mbcnt_hi(~0u, __builtin_amdgcn_mbcnt_lo(~0u, 0u));
    const u16* const volatile __attribute__((address_space(4)))* ka = (const u16* const volatile __attribute__((address_space(4)))*)__builtin_amdgcn_kernarg_segment_ptr();
    {
      const u16* w1p = ka[6];
#pragma unroll 1
      for (unsigned f = blockIdx.x * 4 + w; f < 1152; f += 1024) {
        const u16* p = w1p + (32 * (f % 12) + 8 * (lane >> 4)) * HID + 16 * (f / 12) + (lane & 15);
        u32x4 v;
#pragma unroll
        for (int j = 0; j < 4; ++j) v[j] = p[2 * j * HID] | (unsigned)p[(2 * j + 1) * HID] << 16;
        *(u32x4*)(g_buf + f * 1024 + lane * 16) = v;
      }
    }
    slow_bar((unsigned*)(g_buf + G_BAR), w);
    slow_fc1(ka[0], ka[5], (char*)ka[1], (float*)smem + w * HID, w);
    slow_bar((unsigned*)(g_buf + G_BAR + 64), w);
    slow_fc2(ka[0], (const char*)ka[1], (u16*)ka[2], ka[3], ka[4], smem, w);
  }
  TS(15);
  MK_END(4);
}

extern "C" void kernel_launch(void* const* d_in, const int* in_sizes, int n_in,
                              void* d_out, int out_size, void* d_ws, size_t ws_size,
                              hipStream_t stream) {
  const u16* x = (const u16*)d_in[0];
  const u16* dw = (const u16*)d_in[1];
  const u16* w1 = (const u16*)d_in[2];
  const u16* w2 = (const u16*)d_in[3];
  const u16* grn_g = (const u16*)d_in[4];
  const u16* grn_b = (const u16*)d_in[5];
  char* ws = (char*)d_ws;
  if (ws_size < H_BYTES) return;
  (void)hipFuncSetAttribute((const void*)k1_fc1, hipFuncAttributeMaxDynamicSharedMemorySize, K1_LDS);
  (void)hipFuncSetAttribute((const void*)k2_fc2, hipFuncAttributeMaxDynamicSharedMemorySize, K2_LDS);

  k0_prep<<<K0_NB + NPAGE + NPAGX, 256, 0, stream>>>(w1, dw, ws, (const char*)x, grn_b, w2);
  k1_fc1<<<NWG + 8, 256, K1_LDS, stream>>>(x, ws, (const u16*)d_out, grn_g);
  k2_fc2<<<NWG, 256, K2_LDS, stream>>>(x, ws, (u16*)d_out, grn_g, w2, dw, w1);
}
```

```cpp
#include <hip/hip_runtime.h>
#include <hip/hip_bf16.h>
#include <stdint.h>
#include <stdio.h>

typedef short bf16x8 __attribute__((ext_vector_type(8)));
typedef short s16x4 __attribute__((ext_vector_type(4)));
typedef short v4i16_t __attribute__((ext_vector_type(4)));
typedef float f32x16 __attribute__((ext_vector_type(16)));
typedef float f32x4 __attribute__((ext_vector_type(4)));
typedef float f32x2 __attribute__((ext_vector_type(2)));
typedef unsigned u32x4 __attribute__((ext_vector_type(4)));
typedef __bf16 bf16x2_t __attribute__((ext_vector_type(2)));
typedef unsigned short u16;

constexpr float TZ = 175.f, TH = 175.f, TW2 = 175.f;
#define DI __device__ __forceinline__
#define LAS __attribute__((address_space(3)))

constexpr int NPIX = 147456;
constexpr int CH = 384;
constexpr int HID = 1536;
constexpr int NTILE = NPIX / 32;
constexpr int NWG = 256;
constexpr int TPW = NTILE / NWG;

constexpr size_t H_BYTES = (size_t)NPIX * HID * 2;
constexpr unsigned K1_LDS = 163840, K1_PF_OFF = 130048, K1_SMAX = 12, K1_NREP = 4, K1_G_TAB = 1228800, K1_G_CNT = 1239040, K1_G_LOC = 1236992, K1_G_DUM = 1255424,
  K1_G_ACC = 1320960, K1_G_LUT = 1382400, K1_G_BYTES = 1398784;
constexpr bool K1_LUT = true;
constexpr float K1_ZS = 16.0f;
constexpr unsigned K1_GST = 25600, K1_WCH = 6400, K1_ZC = 1365;
constexpr unsigned K1_SB1 = 32768;
constexpr bool K1_F2 = true;
constexpr unsigned K2_SHT = 12483;
constexpr int K2_LDS = 163840, K2_HB = 98304, K2_RSZ = 13824, K2_E_OFF = 153600, K2_PF_OFF = 159744, K2_S16_OFF = 152576, K2_S16_D0 = 8192, K2_NAT = 8;

__device__ __attribute__((aligned(256))) char g_buf[K1_G_BYTES];
__device__ __attribute__((aligned(256))) float g_cs[8 * 384];
__device__ __attribute__((aligned(256))) char g_w2r[96 * 12 * 1024];
__device__ __attribute__((aligned(256))) char g_w2f[96 * 12 * 1024];
__device__ __attribute__((aligned(256))) char g_w2q[16 * 12 * 1024];
__device__ __attribute__((aligned(256))) float g_sc[64];
constexpr unsigned G_FLAG = K1_G_CNT + 14336;
constexpr unsigned G_PUB = K1_G_DUM;
__device__ __attribute__((aligned(256))) unsigned g_prm[1024 * 64];
constexpr unsigned G_STMP = K1_G_CNT + 12288;
constexpr unsigned G_BAR = K1_G_CNT + 13312;
constexpr unsigned H_TS = 49152, H_RS = 48 * H_TS, NPAGE = 217, NPAGX = 55;
#define TS(i) do { } while (0)
#define KST(i) do { } while (0)
#define MK_START(i) do { } while (0)
#define MK_END(i) do { } while (0)

DI unsigned pack_bf16(float a, float b) {
  f32x2 v = {a, b};
  bf16x2_t r = __builtin_convertvector(v, bf16x2_t);
  return __builtin_bit_cast(unsigned, r);
}
typedef _Float16 f16x2_t __attribute__((ext_vector_type(2)));
DI unsigned pack_f16(float a, float b) {
  f32x2 v = {a, b};
  f16x2_t r = __builtin_convertvector(v, f16x2_t);
  return __builtin_bit_cast(unsigned, r);
}
DI float bf_lo(unsigned u) { return __uint_as_float(u << 16); }
DI float bf_hi(unsigned u) { return __uint_as_float(u & 0xffff0000u); }
DI float round_bf16(float a) { return bf_lo(pack_bf16(a, 0.f)); }
DI float dot2acc(unsigned a, unsigned b, float c) {
  return __builtin_amdgcn_fdot2_f32_bf16(__builtin_bit_cast(bf16x2_t, a), __builtin_bit_cast(bf16x2_t, b), c, false);
}
DI float ld_bf16(const u16* p) { return __uint_as_float((unsigned)(*p) << 16); }

DI s16x4 tr_read(const char* p) {
  return __builtin_bit_cast(s16x4, __builtin_amdgcn_ds_read_tr16_b64_v4i16((LAS v4i16_t*)p));
}

#define MFMA32(a, b, c) __builtin_amdgcn_mfma_f32_32x32x16_bf16((a), (b), (c), 0, 0, 0)
#define BAR() __builtin_amdgcn_s_barrier()

DI void dma16(unsigned voff, const char* sbase, unsigned lds_dst) {
  unsigned keep;
  asm volatile("s_mov_b32 %0, m0\n\ts_mov_b32 m0, %3\n\ts_nop 0\n\tglobal_load_lds_dwordx4 %1, %2\n\ts_mov_b32 m0, %0"
               : "=&s"(keep) : "v"(voff), "s"(sbase), "s"(lds_dst) : "memory");
}
DI unsigned lds_addr(const char* p) { return (unsigned)(size_t)(const LAS char*)p; }

DI float gelu_tanh(float x) {
  const float c0 = -2.0f * 0.7978845608028654f * 1.4426950408889634f;
  const float c1 = c0 * 0.044715f;
  float x2 = x * x;
  float t = __builtin_fmaf(x2, c1, c0);
  float e = __builtin_amdgcn_exp2f(x * t);
  float r = __builtin_amdgcn_rcpf(1.0f + e);
  return x * r;
}

DI void gload_b128_asm(u32x4& dst, const char* sbase, unsigned voff) {
  asm volatile("global_load_dwordx4 %0, %1, %2" : "=v"(dst) : "v"(voff), "s"(sbase) : "memory");
}
DI void lds_rd128(u32x4& dst, unsigned addr, int off) {
  asm volatile("ds_read_b128 %0, %1 offset:%2" : "=v"(dst) : "v"(addr), "i"(off));
}
DI void lds_rd128_far(u32x4& dst, unsigned voff, unsigned sbase, int imm) {
  unsigned t;
  asm volatile("v_add_u32 %1, %3, %2\n\tv_add_u32 %1, %4, %1\n\tds_read_b128 %0, %1" : "=v"(dst), "=&v"(t) : "v"(voff), "s"(sbase), "i"(imm));
}
DI void gload16i(u32x4& dst, const char* sbase, unsigned voff, int imm) {
  unsigned t;
  asm volatile("v_add_u32 %1, %4, %2\n\tglobal_load_dwordx4 %0, %1, %3" : "=v"(dst), "=&v"(t) : "v"(voff), "s"(sbase), "i"(imm) : "memory");
}
DI void dma16i(const char* sbase, unsigned voff, int immv, unsigned lbase, int imml) {
  unsigned t, keep;
  asm volatile("s_mov_b32 %1, m0\n\ts_add_u32 m0, %4, %6\n\tv_add_u32 %0, %5, %2\n\tglobal_load_lds_dwordx4 %0, %3\n\ts_mov_b32 m0, %1"
               : "=&v"(t), "=&s"(keep) : "v"(voff), "s"(sbase), "s"(lbase), "i"(immv), "i"(imml) : "memory", "scc");
}

DI void st16_wt(void* p, u32x4 v) { asm volatile("global_store_dwordx4 %0, %1, off\n\ts_nop 1" :: "v"(p), "v"(v) : "memory"); }
template <int I> struct IC { static constexpr int value = I; };
template <int N, int I = 0, typename F> DI void static_for(F&& f) {
  if constexpr (I < N) { f(IC<I>{}); static_for<N, I + 1>(f); }
}
template <int C, int M> DI float dpp_add(float v) { return v + __int_as_float(__builtin_amdgcn_update_dpp(0, __float_as_int(v), C, M, 0xf, false)); }
DI float wave_sum63(float v) {
  v = dpp_add<0x111, 0xf>(v); v = dpp_add<0x112, 0xf>(v); v = dpp_add<0x114, 0xf>(v); v = dpp_add<0x118, 0xf>(v);
  v = dpp_add<0x142, 0xa>(v); return dpp_add<0x143, 0xc>(v);
}
DI unsigned cs_index(unsigned n) { return (n & ~63u) + (n & 3) * 16 + ((n >> 2) & 15); }
DI uint64_t uni64(uint64_t v) { return (uint64_t)__builtin_amdgcn_readfirstlane(v >> 32) << 32 | (unsigned)__builtin_amdgcn_readfirstlane(v); }
DI unsigned wg_side(unsigned b) { return (((const unsigned*)(g_buf + K1_G_LOC))[b & 3] ^ (b >> 2)) & 1; }
DI unsigned h_flip(const char* ws, unsigned region, unsigned t) { return __popc(((const unsigned*)(g_buf + K1_G_LOC + 64))[(((unsigned)(size_t)ws & 0x1fffffu) + region * H_RS + t * H_TS) >> 21]) > 2; }
DI unsigned x_flip(const u16* x, unsigned region) { return __popc(((const unsigned*)(g_buf + K1_G_LOC + 1024))[(((unsigned)(size_t)x & 0x1fffffu) + (12 * region + 6) * 49152u) >> 21]) > 2; }
DI unsigned sub_pixel(const u16* x, unsigned sub, unsigned m) {
  const unsigned blk = sub / 24, u = sub % 24;
  const unsigned region = blk < 256 ? (blk >> 3) * 4 + (blk & 3) : 128 + ((blk - 256) >> 1), side = blk < 256 ? wg_side(blk) : blk & 1;
  return (12 * region + (u >> 1)) * 64 + ((0xda43cb52fe987610ull >> (32 * (u & 1) + 16 * (side ^ x_flip(x, region)) + (m & 12))) & 15) * 4 + (m & 3);
}
DI unsigned h_piece(const char* ws, unsigned sub, unsigned t) {
  const unsigned blk = sub / 24, u = sub % 24;
  const unsigned region = blk < 256 ? (blk >> 3) * 4 + (blk & 3) : 128 + ((blk - 256) >> 1), side = blk < 256 ? wg_side(blk) : blk & 1;
  return region * H_RS + t * H_TS + (u >> 3) * 16384 + (u & 7) * 1024 + ((side ^ h_flip(ws, region, t)) << 13);
}
constexpr unsigned K0_NB = 145 + 24 + 96 + 16 + 48;
constexpr float U24 = 0x1p-24f, EPS = 1e-5f;
DI float wave_all(float v) { return __int_as_float(__builtin_amdgcn_readlane(__float_as_int(wave_sum63(v)), 63)); }
DI float pow2f(float v) { return __uint_as_float(__float_as_uint(v) & 0x7f800000u); }
DI u32x4 w_load(const u16* w) { return *(const u32x4*)(w + ((threadIdx.x >> 3) & 7) * 73408 + 36800 + (threadIdx.x & 7) * 8); }
DI float w_ms(u32x4 a) {
  float q = 0.f;
#pragma unroll
  for (int i = 0; i < 4; ++i) { const float lo = bf_lo(a[i]), hi = bf_hi(a[i]); q = __builtin_fmaf(lo, lo, __builtin_fmaf(hi, hi, q)); }
  return wave_all(q) * (1.0f / 512);
}
DI float w1_scale(float ms) { return pow2f(300.f * U24 * __builtin_amdgcn_rsqf(ms)); }
DI float w2_scale(float ms) { return TW2 * U24 * __builtin_amdgcn_rsqf(ms); }
struct Sc { float w1, zs, g, h, vlo, vhi, c; };
DI Sc scales(const u16* w1, const u16* x, const u16* dw) {
  const u32x4 a = w_load(w1);
  const unsigned l = threadIdx.x & 63;
  float s = 0.f, q = 0.f;
#pragma unroll
  for (int k = 0; k < 6; ++k) {
    const u32x4 v = *(const u32x4*)(x + (9001 + 18397 * (l >> 3)) * CH + 64 * k + 8 * (l & 7)), d = *(const u32x4*)(dw + 64 * k + 8 * (l & 7));
#pragma unroll
    for (int i = 0; i < 4; ++i) { const float lo = bf_lo(v[i]) * bf_lo(d[i]), hi = bf_hi(v[i]) * bf_hi(d[i]); s += lo + hi; q = __builtin_fmaf(lo, lo, __builtin_fmaf(hi, hi, q)); }
  }
  s = dpp_add<0x141, 0xf>(dpp_add<0x4e, 0xf>(dpp_add<0xb1, 0xf>(s))); q = dpp_add<0x141, 0xf>(dpp_add<0x4e, 0xf>(dpp_add<0xb1, 0xf>(q)));
  const float var = fmaxf(q * (1.0f / CH) - s * s * (1.0f / (CH * CH)), 0.f);
  const float r2 = wave_all(var * __builtin_amdgcn_rcpf(var + EPS)) * (1.0f / 64), rden = __builtin_amdgcn_exp2f(wave_all(__builtin_amdgcn_logf(var + EPS)) * (-1.0f / 128));
  const float s1 = w_ms(a), p2 = r2 * CH * s1;
  Sc c;
  c.w1 = w1_scale(s1);
  c.g = TZ * U24 * __builtin_amdgcn_rsqf(r2);
  c.h = TH * U24 * __builtin_amdgcn_rsqf(p2 * (0.25f + 0.5f * p2 * __builtin_amdgcn_rcpf(1.0f + 2.0f * p2)));
  c.c = fminf(__builtin_ceilf(0.1701f * 0x1p24f * c.h) + 1.0f, 1400.f);
  c.zs = fminf(pow2f(c.g * 2530.f), pow2f(c.g * 0x1p12f * rden));
  const float e = EPS * c.zs * c.zs, z2 = r2 * (660.f * 660.f / (TZ * TZ));
  c.vlo = 4.2f * c.g * c.g - e;
  c.vhi = fminf(0x1.ep29f * c.g * c.g - e, z2 < 1.0f ? e * z2 * __builtin_amdgcn_rcpf(1.0f - z2) : 0x1p120f);
  return c;
}
__global__ __launch_bounds__(256) void k0_prep(const u16* __restrict__ w1, const u16* __restrict__ dw, const char* __restrict__ ws, const char* __restrict__ x,
                                                const u16* __restrict__ grn_b, const u16* __restrict__ w2) {
  MK_START(0);
  uint64_t pc; int d1;
  asm volatile("s_getpc_b64 %0\n1:\n\ts_mov_b32 %1, k1_code-1b" : "=s"(pc), "=s"(d1));
  if (threadIdx.x >= 192) pc += (int64_t)d1 + ((int)(blockIdx.x >> 3) - 3) * 8192;
  unsigned pfd = 0;
  if (blockIdx.x < 64 && (threadIdx.x < 150 || (blockIdx.x < 48 && threadIdx.x >= 192))) pfd = *(const __attribute__((address_space(1))) unsigned*)((pc & ~127ull) + threadIdx.x * 128);
#define K0_END() do { asm volatile("" :: "v"(pfd)); MK_END(0); } while (0)
  unsigned xcc;
  asm volatile("s_getreg_b32 %0, hwreg(HW_REG_XCC_ID)" : "=s"(xcc));
  xcc = (xcc >> 2) & 1;
  if (blockIdx.x < 4 && threadIdx.x == 0) ((unsigned*)(g_buf + K1_G_LOC))[blockIdx.x] = 7 * xcc;
  constexpr unsigned NP = NPAGE + NPAGX;
  __shared__ Sc ssc;
  __shared__ float ss2;
  const bool sw = threadIdx.x >= 192;
  const unsigned bi = blockIdx.x;
  const unsigned bx = bi < NP ? bi + K0_NB : bi - NP;
  if (bx >= K0_NB) {
    __shared__ unsigned lat[8];
    const unsigned pg = bx - K0_NB, w = threadIdx.x >> 6, lane = threadIdx.x & 63;
    auto gran = [&](unsigned p) {
      const bool h = p < NPAGE;
      const char* buf = h ? ws : x;
      const int o0 = (int)((h ? p : p - NPAGE) << 21) - (int)((unsigned)(size_t)buf & 0x1fffffu);
      const unsigned al = h ? 49152 : 12 * 49152;
      const unsigned q = ((((unsigned)max(o0, 0) + al - 1) >> (h ? 14 : 16)) * (h ? 87382u : 29128u)) >> 18;
      return buf + min(q * al, (unsigned)(h ? H_BYTES : H_BYTES / 4) - 16384u);
    };
    if (lane < 5) {
      const char* a = gran(lane < 4 ? min(pg + NP - 1 - (lane >> 1), pg - 1 - (lane >> 1)) : pg) + (lane & 1) * 8192 + w * 2048 + (lane < 4 ? 1024 : 0);
      const char* b = a;
      const char* own = (const char*)uni64(__builtin_amdgcn_readlane((uint64_t)a >> 32, 4) * 4294967296ull + (unsigned)__builtin_amdgcn_readlane((unsigned)(uint64_t)a, 4));
      unsigned long long t0, t1, t2;
      int n = 2;
      asm volatile("" : "+s"(n));
#pragma unroll 1
      do {
        unsigned v;
        asm volatile("s_cmp_eq_u32 %6, 2\n\ts_cbranch_scc1 1f\n\ts_sleep 4\n1:\n\ts_memtime %0\n\ts_waitcnt lgkmcnt(0)\n\tglobal_load_dword %3, %4, off sc1\n\ts_waitcnt vmcnt(0)\n\ts_memtime %1\n\ts_waitcnt lgkmcnt(0)\n\t"
                     "global_load_dword %3, %5, off sc1\n\ts_waitcnt vmcnt(0)\n\ts_memtime %2\n\ts_waitcnt lgkmcnt(0)" : "=&s"(t0), "=&s"(t1), "=&s"(t2), "=&v"(v) : "v"(a), "v"(b), "s"(n) : "memory", "scc");
        a = own + 1024 + (w & 1) * 8192; b = a + 8192 - (w & 1) * 16384;
      } while (--n);
      lat[w + 4 * (w & 1)] = (unsigned)(t1 - t0); lat[w + 4 - 4 * (w & 1)] = (unsigned)(t2 - t1);
    }
    __syncthreads();
    if (threadIdx.x == 0) {
      auto fastest = [&](const unsigned* l) { unsigned m = 9999; for (int i = 0; i < 4; ++i) m = l[i] < 300 ? m : min(m, l[i]); return (int)m; };
      const int df = fastest(lat) - fastest(lat + 4);
      const unsigned v = xcc ^ (df > 0), sure = abs(df) > 80;
      char* e = g_buf + K1_G_LOC + 64 + 4 * (pg < NPAGE ? pg : pg + 240 - NPAGE);
      *(u16*)(e + 2) = v ? 3 + 4 * sure : 1 - sure;
      if (pg == 0 || pg == NPAGE) e[0] = v; else e[-3] = v ^ 1;
      if (pg == NPAGE - 1 || pg == NP - 1) e[1] = v; else e[4] = v ^ 1;
    }
    K0_END();
    return;
  }
  if (bx >= 145 + 24 + 96 + 16) {
    __shared__ __attribute__((aligned(16))) float red[2][16][32];
    const int t = bx - 281, c8 = (threadIdx.x & 3) * 8;
    u32x4 sm, v[6];
    if (sw) sm = w_load(w1);
#pragma unroll
    for (int k = 0; k < 6; ++k) v[k] = *(const u32x4*)(w1 + (size_t)(64 * k + (threadIdx.x >> 2)) * HID + 32 * t + c8);
    if (sw) ss2 = w1_scale(w_ms(sm));
    __syncthreads();
    const float s1 = ss2;
    float a[8], q[8];
#pragma unroll
    for (int i = 0; i < 8; ++i) {
      a[i] = q[i] = 0.f;
#pragma unroll
      for (int k = 0; k < 6; ++k) {
        const float f = (float)(_Float16)(s1 * (i & 1 ? bf_hi(v[k][i >> 1]) : bf_lo(v[k][i >> 1]))) * 0x1p24f;
        a[i] += f; q[i] = __builtin_fmaf(f, f, q[i]);
      }
      a[i] = dpp_add<0x118, 0xf>(dpp_add<0x114, 0xf>(a[i])); q[i] = dpp_add<0x118, 0xf>(dpp_add<0x114, 0xf>(q[i]));
    }
    if ((threadIdx.x & 12) == 12) {
#pragma unroll
      for (int i = 0; i < 2; ++i) {
        *(f32x4*)&red[0][threadIdx.x >> 4][c8 + 4 * i] = (f32x4){a[4 * i], a[4 * i + 1], a[4 * i + 2], a[4 * i + 3]};
        *(f32x4*)&red[1][threadIdx.x >> 4][c8 + 4 * i] = (f32x4){q[4 * i], q[4 * i + 1], q[4 * i + 2], q[4 * i + 3]};
      }
    }
    __syncthreads();
    if (threadIdx.x < 64) {
      const int n = threadIdx.x & 31;
      float s = 0.f;
#pragma unroll
      for (int i = 0; i < 16; ++i) s += red[threadIdx.x >> 5][i][n];
      const float c = (0.f - (float)K1_ZC * s) * 0x1p-48f;
      char* p = g_buf + t * K1_GST + 5120;
      if (threadIdx.x < 32) *(f32x4*)(p + 272 * (n >> 4) + (n & 15) * 16) = (f32x4){c, c, c, c};
      const bool any = __ballot(threadIdx.x >= 32 && !(s >= 384.f * 64 * 64 && s <= 384.f * 1024 * 1024)) != 0;
      if (threadIdx.x == 0) g_buf[G_FLAG + 4 * 98 + t] = any;
    }
    K0_END();
    return;
  }
  if (bx >= 145 + 24 + 96) {
    if (sw) ssc = scales(w1, (const u16*)x, dw);
    __syncthreads();
    const Sc sc = ssc;
    const unsigned e = (bx - 265) * 256 + threadIdx.x;
    const float u = 1.0f / (sc.g * sc.w1);
    float r[2];
#pragma unroll
    for (int i = 0; i < 2; ++i) {
      const unsigned c = 2 * e + i;
      r[i] = __builtin_fmaf(sc.h, gelu_tanh(u * __uint_as_float((c >> 12) << 31 | ((c & 4095) + 79 * 128) << 16 | 0x8000u)), sc.c * U24);
      if (!(r[i] >= 0.f && r[i] < 8189.f * U24)) r[i] = __builtin_inff();
    }
    *(unsigned*)(g_buf + K1_G_LUT + 4 * e) = pack_f16(r[0], r[1]);
    K0_END();
    return;
  }
  if (bx >= 145 + 24) {
    __shared__ __attribute__((aligned(16))) u16 tw[32][200];
    const int kappa = (bx - 169) >> 1, nh = (bx - 169) & 1;
    u32x4 sm, v[3];
    if (sw) sm = w_load(w2);
#pragma unroll
    for (int i = 0; i < 3; ++i) { const int e = threadIdx.x + 256 * i; v[i] = *(const u32x4*)(w2 + (size_t)(32 * kappa + e / 24) * CH + 192 * nh + (e % 24) * 8); }
    if (sw) ss2 = w2_scale(w_ms(sm));
#pragma unroll
    for (int i = 0; i < 3; ++i) { const int e = threadIdx.x + 256 * i; *(u32x4*)&tw[e / 24][(e % 24) * 8] = v[i]; }
    __syncthreads();
    const int lane = threadIdx.x & 63, g = lane >> 4;
    const float s2 = 256.f * ss2;
    bool bad = false;
#pragma unroll
    for (int i = 0; i < 3; ++i) {
      const int fi = (threadIdx.x >> 6) + 4 * i, n = 64 * (fi >> 2) + 4 * (lane & 15) + (fi & 3);
      float wv[8];
      unsigned bits = 0;
#pragma unroll
      for (int j = 0; j < 8; ++j) {
        wv[j] = ld_bf16(&tw[16 * (j >> 2) + 4 * g + (j & 3)][n]);
        bits |= wv[j] != 0.f ? 256u << j : 0u;
        wv[j] *= s2;
        bits |= fabsf(wv[j]) >= 48 * 256 * U24 ? 1u << j : 0u;
        bad |= !(fabsf(wv[j]) <= 500.f);
      }
      const unsigned o = (((kappa & ~1) + fi / 6) * 24 + nh * 12 + (fi % 6) * 2 + (kappa & 1)) * 1024 + lane * 16;
      st16_wt(g_w2r + o, (u32x4){pack_f16(wv[0], wv[1]), pack_f16(wv[2], wv[3]), pack_f16(wv[4], wv[5]), pack_f16(wv[6], wv[7])});
      constexpr float q = 1.0f / 256;
      if (kappa < 8) st16_wt(g_w2q + o, (u32x4){pack_f16(q * wv[0], q * wv[1]), pack_f16(q * wv[2], q * wv[3]), pack_f16(q * wv[4], q * wv[5]), pack_f16(q * wv[6], q * wv[7])});
      static_for<4>([&](auto ic) { bits |= __builtin_amdgcn_update_dpp(0u, bits, 0x120 + (1 << decltype(ic)::value), 0xf, 0xf, false); });
      bad |= ((bits >> 8) & ~bits & 255u) != 0;
    }
    const bool any = __ballot(bad) != 0;
    if (lane == 0) ((char*)(g_buf + G_FLAG))[4 * (bx - 169) + (threadIdx.x >> 6)] = any;
    K0_END();
    return;
  }
  if (bx >= 145) {
    __shared__ __attribute__((aligned(16))) float red[16][64];
    const int c0 = 64 * ((bx - 145) >> 2), rg = (bx - 145) & 3, c8 = (threadIdx.x & 7) * 8;
    float a[8];
#pragma unroll
    for (int i = 0; i < 8; ++i) a[i] = 0.f;
#pragma unroll
    for (int k = 0; k < 12; ++k) {
      const int ch = 384 * rg + 32 * k + (threadIdx.x >> 3);
      const float b = ld_bf16(grn_b + ch);
      const u32x4 v = *(const u32x4*)(w2 + (size_t)ch * CH + c0 + c8);
#pragma unroll
      for (int i = 0; i < 4; ++i) { a[2 * i] = __builtin_fmaf(b, bf_lo(v[i]), a[2 * i]); a[2 * i + 1] = __builtin_fmaf(b, bf_hi(v[i]), a[2 * i + 1]); }
    }
#pragma unroll
    for (int i = 0; i < 8; ++i) a[i] = dpp_add<0x118, 0xf>(a[i]);
    if (threadIdx.x & 8) {
      *(f32x4*)&red[threadIdx.x >> 4][c8] = (f32x4){a[0], a[1], a[2], a[3]};
      *(f32x4*)&red[threadIdx.x >> 4][c8 + 4] = (f32x4){a[4], a[5], a[6], a[7]};
    }
    __syncthreads();
    if (threadIdx.x < 64) {
      float s = 0.f;
#pragma unroll
      for (int i = 0; i < 16; ++i) s += red[i][threadIdx.x];
      g_cs[(4 + rg) * 384 + cs_index(c0 + threadIdx.x)] = s;
      g_cs[64 * (bx - 145) + threadIdx.x] = 0.f;
    }
    K0_END();
    return;
  }
  const unsigned bid = bx;
  if (bid == 0) {
    if (sw) ssc = scales(w1, (const u16*)x, dw);
    if ((threadIdx.x >> 6) == 2) ss2 = w2_scale(w_ms(w_load(w2)));
#pragma unroll
    for (int i = 0; i < 4; ++i) if (threadIdx.x + 256 * i < 896) *(u32x4*)(g_buf + K1_G_CNT + (threadIdx.x + 256 * i) * 16) = (u32x4){0u, 0u, 0u, 0u};
    *(unsigned*)(g_buf + G_PUB + threadIdx.x * 256) = 0u;
    const int e = threadIdx.x, e2 = e < 192 ? 32 * (e >> 4) + 8 * ((e & 15) >> 2) : 8 * min(e - 192, 47);
    const u32x4 d = *(const u32x4*)(dw + e2);
    __syncthreads();
    const Sc sc = ssc;
    const float s2 = ss2;
    u32x4 v;
#pragma unroll
    for (int k = 0; k < 4; ++k) v[k] = pack_bf16(sc.zs * bf_lo(d[k]), sc.zs * bf_hi(d[k]));
    if (e < 192) {
#pragma unroll
      for (int k = 0; k < 4; ++k) v[k] &= (k & 1) != ((e >> 1) & 1) ? 0u : e & 1 ? 0xffff0000u : 0xffffu;
    }
    if (e >= 240) v = (u32x4){__float_as_uint(EPS * sc.zs * sc.zs), __float_as_uint(sc.vhi), __float_as_uint(sc.g), __float_as_uint(sc.vlo)};
    *(u32x4*)(g_buf + K1_G_TAB + (e < 192 ? e : e + 192) * 16) = v;
    auto sane = [](float v) { return v >= 0x1p-45f && v <= 0x1p20f; };
    if (threadIdx.x < 2) ((unsigned*)(g_buf + G_FLAG))[96 + threadIdx.x] = !(sane(sc.w1) && sane(sc.zs) && sane(sc.g) && sane(sc.h) && sane(s2));
    if (threadIdx.x == 0) {
      const float ik = 1.0f / (sc.h * s2);
      *(f32x4*)g_sc = (f32x4){sc.h * s2, ik, 0x1p-28f / (sc.h * sc.h), -sc.c * U24 * ik};
      g_sc[4] = __uint_as_float(pack_f16(-sc.c * U24, -sc.c * U24));
      *(unsigned*)(g_buf + K1_G_CNT + 16320) = pack_f16(-sc.c * 0x1p-10f, -sc.c * 0x1p-10f);
    }
    if (threadIdx.x < 48) *(u32x4*)(g_buf + threadIdx.x * K1_GST + 5376) = (u32x4){pack_f16(sc.c * U24, 0.f), 0u, 0u, 0u};
    K0_END();
    return;
  }
  __shared__ __attribute__((aligned(16))) u16 tl[32][136];
  const int e = (bid - 1) * 256 + threadIdx.x, ks = (bid - 1) % 12, cb = (bid - 1) / 12;
  u32x4 sm, v[2];
  if (sw) sm = w_load(w1);
#pragma unroll
  for (int i = 0; i < 2; ++i) v[i] = *(const u32x4*)(w1 + (size_t)(32 * ks + (threadIdx.x >> 4) + 16 * i) * HID + 128 * cb + (threadIdx.x & 15) * 8);
  if (sw) ss2 = w1_scale(w_ms(sm));
#pragma unroll
  for (int i = 0; i < 2; ++i) *(u32x4*)&tl[(threadIdx.x >> 4) + 16 * i][(threadIdx.x & 15) * 8] = v[i];
  if (e < (int)(K1_NREP + 1) * 768) *(u32x4*)(g_buf + K1_G_ACC + e * 16) = (u32x4){0u, 0u, 0u, 0u};
  __syncthreads();
  const float s1 = ss2;
#pragma unroll
  for (int i = 0; i < 2; ++i) {
    const int lane = threadIdx.x & 63, f = (threadIdx.x >> 6) + 4 * i, n = 16 * f + (lane & 15), k0 = 8 * (lane >> 4);
    u32x4 P;
#pragma unroll
    for (int jj = 0; jj < 4; ++jj) P[jj] = pack_f16(s1 * ld_bf16(&tl[k0 + 2 * jj][n]), s1 * ld_bf16(&tl[k0 + 2 * jj + 1][n]));
    const int fs = (f & 1) * 12 + ks;
    st16_wt(g_buf + (4 * cb + (f >> 1)) * K1_GST + (fs + (fs >= 5)) * 1024 + lane * 16, P);
  }
  K0_END();
}

constexpr unsigned G_ACC2 = K1_G_ACC + K1_NREP * 12288;

#define VA_CLOB \
  "v0", "v1", "v2", "v3", "v4", "v5", "v6", "v7", "v8", "v9", "v10", "v11", "v12", "v13", "v14", "v15", "v16", "v17", "v18", "v19", "v20", "v21", "v22", "v23", "v24", "v25", "v26", "v27", "v28", "v29", "v30", "v31", \
  "v32", "v33", "v34", "v35", "v36", "v37", "v38", "v39", "v40", "v41", "v42", "v43", "v44", "v45", "v46", "v47", "v48", "v49", "v50", "v51", "v52", "v53", "v54", "v55", "v56", "v57", "v58", "v59", "v60", "v61", "v62", "v63", \
  "v64", "v65", "v66", "v67", "v68", "v69", "v70", "v71", "v72", "v73", "v74", "v75", "v76", "v77", "v78", "v79", "v80", "v81", "v82", "v83", "v84", "v85", "v86", "v87", "v88", "v89", "v90", "v91", "v92", "v93", "v94", "v95", \
  "v96", "v97", "v98", "v99", "v100", "v101", "v102", "v103", "v104", "v105", "v106", "v107", "v108", "v109", "v110", "v111", "v112", "v113", "v114", "v115", "v116", "v117", "v118", "v119", "v120", "v121", "v122", "v123", "v124", "v125", "v126", "v127", \
  "v128", "v129", "v130", "v131", "v132", "v133", "v134", "v135", "v136", "v137", "v138", "v139", "v140", "v141", "v142", "v143", "v144", "v145", "v146", "v147", "v148", "v149", "v150", "v151", "v152", "v153", "v154", "v155", "v156", "v157", "v158", "v159", \
  "v160", "v161", "v162", "v163", "v164", "v165", "v166", "v167", "v168", "v169", "v170", "v171", "v172", "v173", "v174", "v175", "v176", "v177", "v178", "v179", "v180", "v181", "v182", "v183", "v184", "v185", "v186", "v187", "v188", "v189", "v190", "v191", \
  "v192", "v193", "v194", "v195", "v196", "v197", "v198", "v199", "v200", "v201", "v202", "v203", "v204", "v205", "v206", "v207", "v208", "v209", "v210", "v211", "v212", "v213", "v214", "v215", "v216", "v217", "v218", "v219", "v220", "v221", "v222", "v223", \
  "v224", "v225", "v226", "v227", "v228", "v229", "v230", "v231", "v232", "v233", "v234", "v235", "v236", "v237", "v238", "v239", "v240", "v241", "v242", "v243", "v244", "v245", "v246", "v247", "v248", "v249", "v250", "v251", "v252", "v253", "v254", "v255", \
  "a0", "a1", "a2", "a3", "a4", "a5", "a6", "a7", "a8", "a9", "a10", "a11", "a12", "a13", "a14", "a15", "a16", "a17", "a18", "a19", "a20", "a21", "a22", "a23", "a24", "a25", "a26", "a27", "a28", "a29", "a30", "a31", \
  "a32", "a33", "a34", "a35", "a36", "a37", "a38", "a39", "a40", "a41", "a42", "a43", "a44", "a45", "a46", "a47", "a48", "a49", "a50", "a51", "a52", "a53", "a54", "a55", "a56", "a57", "a58", "a59", "a60", "a61", "a62", "a63", \
  "a64", "a65", "a66", "a67", "a68", "a69", "a70", "a71", "a72", "a73", "a74", "a75", "a76", "a77", "a78", "a79", "a80", "a81", "a82", "a83", "a84", "a85", "a86", "a87", "a88", "a89", "a90", "a91", "a92", "a93", "a94", "a95", \
  "a96", "a97", "a98", "a99", "a100", "a101", "a102", "a103", "a104", "a105", "a106", "a107", "a108", "a109", "a110", "a111", "a112", "a113", "a114", "a115", "a116", "a117", "a118", "a119", "a120", "a121", "a122", "a123", "a124", "a125", "a126", "a127", \
  "a128", "a129", "a130", "a131", "a132", "a133", "a134", "a135", "a136", "a137", "a138", "a139", "a140", "a141", "a142", "a143", "a144", "a145", "a146", "a147", "a148", "a149", "a150", "a151", "a152", "a153", "a154", "a155", "a156", "a157", "a158", "a159", \
  "a160", "a161", "a162", "a163", "a164", "a165", "a166", "a167", "a168", "a169", "a170", "a171", "a172", "a173", "a174", "a175", "a176", "a177", "a178", "a179", "a180", "a181", "a182", "a183", "a184", "a185", "a186", "a187", "a188", "a189", "a190", "a191", \
  "a192", "a193", "a194", "a195", "a196", "a197", "a198", "a199", "a200", "a201", "a202", "a203", "a204", "a205", "a206", "a207", "a208", "a209", "a210", "a211", "a212", "a213", "a214", "a215", "a216", "a217", "a218", "a219", "a220", "a221", "a222", "a223", \
  "a224", "a225", "a226", "a227", "a228", "a229", "a230", "a231", "a232", "a233", "a234", "a235", "a236", "a237", "a238", "a239", "a240", "a241", "a242", "a243", "a244", "a245", "a246", "a247", "a248", "a249", "a250", "a251", "a252", "a253", "a254", "a255"
#define K1_CLOB "memory", "vcc", "scc", "s36", "s37", "s38", "s39", "s40", "s41", "s42", "s43", "s44", "s45", "s46", "s47", "s48", "s49", "s50", "s51", "s52", "s53", "s54", "s55", "s56", "s57", "s58", "s59", "s60", "s61", "s62", "s63", "s64", "s65", "s66", "s67", "s68", "s69", "s70", "s71", "s72", "s73", "s74", "s75", "s76", "s77", "s78", "s79", "s80", "s81", "s82", "s83", "s84", "s85", "s86", "s87", "s88", "s89", "s90", "s91", "s92", "s93", "s94", "s95", "s96", "s97", VA_CLOB
DI void k2_params(const u16* x, const char* hws, const u16* out, const u16* grn_g, unsigned tid) {
  const unsigned e = (tid >> 2) * 32 + blockIdx.x * 4 + (tid & 3);
  const unsigned b = e >> 2, w = e & 3, pg = w & 1, nh = w >> 1, q = (b >> 3) * 4 + (b & 3), rs = 128 + (q >> 1), hf = q & 1, side = wg_side(b);
  auto fmask = [&](unsigned region) {
    const unsigned o = ((unsigned)(size_t)hws & 0x1fffffu) + region * H_RS + nh * H_TS;
    const unsigned* p = (const unsigned*)(g_buf + K1_G_LOC + 64) + (o >> 21);
    const unsigned r = 0x200000u + 2 * H_TS - 1 - (o & 0x1fffffu), d1 = ((r >> 15) * 171) >> 9, d2 = (((r + 0x200000u) >> 15) * 171) >> 9;
    const unsigned a1 = ~0u << d1, a2 = d2 < 32 ? ~0u << d2 : 0u;
    return ((__popc(p[0]) > 2 ? ~a1 : 0u) | (__popc(p[1]) > 2 ? a1 & ~a2 : 0u) | (__popc(p[2]) > 2 ? a2 : 0u)) ^ (0u - side);
  };
  const unsigned f0 = fmask(q), f1 = fmask(rs);
  const unsigned fx = ((side ^ x_flip(x, q)) ? ~K2_SHT : 0u) | ((side ^ x_flip(x, rs)) ? K2_SHT : 0u);
  const uint64_t h0 = (uint64_t)hws + q * H_RS + nh * H_TS + pg * 3072, h1 = (uint64_t)hws + rs * H_RS + nh * H_TS + hf * 3072 + pg * 1536;
  const unsigned p0 = (12 * q + 2 * pg) * 49152 + nh * 384, p1 = (12 * rs + 2 * hf + pg) * 49152 + nh * 384;
  const unsigned e1 = 1024 + (e >> 3);
  auto kap = [](unsigned f) { return f / 48 * 2 + (f & 1); };
  auto sco = [&](unsigned f) { return kap(f) * 64 + (kap(f) < 16 ? K2_S16_D0 : 0); };
  auto cso = [&](unsigned f) { const unsigned r = f % 48; return ((f < 192 ? min(f / 48, 2u) : 3) * 384 + (r / 12 % 2 * 12 + r / 24 * 6 + r % 12 / 2) * 16) * 4; };
  typedef uint64_t u64x2 __attribute__((ext_vector_type(2)));
  u64x2* P = (u64x2*)(g_prm + 64 * e);
  P[0] = (u64x2){f0 | (uint64_t)f1 << 32, fx | (uint64_t)(sco(e) | (sco(e1) + (e & 6) * 8) << 16) << 32};
  const u32x4 sc = *(const u32x4*)g_sc;
  P[1] = (u64x2){(((b >> 3) * 4 + w) & 7) * 1024 | (uint64_t)sc[0] << 32, sc[1] | (uint64_t)sc[2] << 32};
  P[2] = (u64x2){h0, h0 + 8192};
  P[3] = (u64x2){h1, h1 + 8192};
  P[4] = (u64x2){__float_as_uint(g_sc[4]) | (uint64_t)((e1 - e) * 1024 + (e & 7) * 128) << 32, 0};
  P[5] = (u64x2){(uint64_t)x + p0, (uint64_t)x + p1};
  P[6] = (u64x2){(uint64_t)out + p0, (uint64_t)out + p1};
  P[7] = (u64x2){0x7fffffffffffffull, (uint64_t)g_buf + G_FLAG};
  P[8] = (u64x2){(uint64_t)g_w2f + w * 6144, (uint64_t)g_w2q + w * 6144};
  P[9] = (u64x2){(uint64_t)g_w2r + e * 1024, (uint64_t)g_w2f + e * 1024};
  P[10] = (u64x2){(uint64_t)grn_g + w * 256, (uint64_t)g_cs + w * 384 + 2048};
  P[11] = (u64x2){(uint64_t)g_buf + G_PUB + b * 256, (uint64_t)g_buf + G_PUB + w * 16384};
  P[12] = (u64x2){cso(e) | (uint64_t)(cso(e1) + (e & 1) * 32) << 32, (uint64_t)g_cs};
  P[13] = (u64x2){sc[3] | (uint64_t)1 << (32 + (b >> 3) % K2_NAT), 0};
}
__global__ __launch_bounds__(256, 1) void k1_fc1(const u16* __restrict__ x, char* __restrict__ hws, const u16* __restrict__ out, const u16* __restrict__ grn_g) {
  extern __shared__ __attribute__((aligned(16))) char smem[];
  const unsigned w = __builtin_amdgcn_readfirstlane(threadIdx.x >> 6);
  if (blockIdx.x < 8) {
    if (w < 2) k2_params(x, hws, out, grn_g, w * 64 + __builtin_amdgcn_mbcnt_hi(~0u, __builtin_amdgcn_mbcnt_lo(~0u, 0u)));
    return;
  }
  const unsigned lds0 = lds_addr(smem);
  const unsigned b = blockIdx.x - 8;
  const unsigned dir = (b >> 3) & 1;
  TS(0); MK_START(1);
  const uint64_t xp = (uint64_t)x, hp = (uint64_t)hws, gp = (uint64_t)g_buf, wp = gp + w * K1_WCH;
  const unsigned d = dir ? ~0u : 1u, wrap = dir ? ~0u : 48u, rst = dir ? 47u : 0u, pos0 = dir ? K1_SMAX - 1 : 0u;
  const unsigned xo0 = (12 * ((b >> 3) * 4 + (b & 3)) + w) * 49152, xs1 = (1536 + w + (dir ? 8 : 0)) * 49152, sb1 = 1536 * w + (dir ? K1_SB1 : 0);
  const unsigned r0o = ((b >> 3) * 4 + (b & 3)) * H_RS, tsel = 4 * (b & 3), s2 = (b >> 2) & 1;
  const uint64_t ap = gp + K1_G_ACC + (b % K1_NREP) * 12288;
  const unsigned ca0 = w == 0 ? dir * 64 : K1_G_DUM - K1_G_CNT + (4 * b + w) * 64;
  const unsigned pm = lds0 + K1_PF_OFF, pf = ((b >> 3) * 4 + w) * 1024;
  unsigned nb;
  asm volatile(
R"(.macro M t,f,w
.if \f < 30
v_mfma_f32_16x16x32_f16 v[56+4*\t:56+4*\t+3], v[128+4*\f:128+4*\f+3], a[216+4*\w:216+4*\w+3], v[56+4*\t:56+4*\t+3]
.else
v_mfma_f32_16x16x32_f16 v[56+4*\t:56+4*\t+3], a[4*\f-4*30:4*\f-4*30+3], a[216+4*\w:216+4*\w+3], v[56+4*\t:56+4*\t+3]
.endif
.endm
.macro Z t,f,w
.if \f < 30
v_mfma_f32_16x16x32_f16 v[56+4*\t:56+4*\t+3], v[128+4*\f:128+4*\f+3], a[216+4*\w:216+4*\w+3], v[252:255]
.else
v_mfma_f32_16x16x32_f16 v[56+4*\t:56+4*\t+3], a[4*\f-4*30:4*\f-4*30+3], a[216+4*\w:216+4*\w+3], v[252:255]
.endif
.endm
.macro LZ a,f
.if \f < 30
v_mfma_f32_16x16x32_bf16 v[36:36+3], v[\a:\a+3], v[128+4*\f:128+4*\f+3], 0
.else
v_mfma_f32_16x16x32_bf16 v[36:36+3], v[\a:\a+3], a[4*\f-4*30:4*\f-4*30+3], 0
.endif
.endm
.macro LS a,f
.if \f < 30
v_mfma_f32_16x16x32_bf16 v[36:36+3], v[\a:\a+3], v[128+4*\f:128+4*\f+3], v[36:36+3]
.else
v_mfma_f32_16x16x32_bf16 v[36:36+3], v[\a:\a+3], a[4*\f-4*30:4*\f-4*30+3], v[36:36+3]
.endif
.endm
.macro LD d,a,f
.if \f < 30
v_mfma_f32_16x16x32_bf16 v[\d:\d+3], a[\a:\a+3], v[128+4*\f:128+4*\f+3], v[36:36+3]
.else
v_mfma_f32_16x16x32_bf16 v[\d:\d+3], a[\a:\a+3], a[4*\f-4*30:4*\f-4*30+3], v[36:36+3]
.endif
.endm
.macro GZ f
.if \f < 30
v_mfma_f32_16x16x32_f16 v[52:55], v[128+4*\f:128+4*\f+3], v[128+4*\f:128+4*\f+3], 0
.else
v_mfma_f32_16x16x32_f16 v[52:55], a[4*\f-4*30:4*\f-4*30+3], a[4*\f-4*30:4*\f-4*30+3], 0
.endif
.endm
.macro GS f
.if \f < 30
v_mfma_f32_16x16x32_f16 v[52:55], v[128+4*\f:128+4*\f+3], v[128+4*\f:128+4*\f+3], v[52:55]
.else
v_mfma_f32_16x16x32_f16 v[52:55], a[4*\f-4*30:4*\f-4*30+3], a[4*\f-4*30:4*\f-4*30+3], v[52:55]
.endif
.endm
.macro CL p,z,s
v_fma_mixlo_f16 v[\p], v[\z], v[\s], 0 op_sel_hi:[0,1,0]
.endm
.macro CH p,z,s
v_fma_mixhi_f16 v[\p], v[\z], v[\s], 0 op_sel:[0,1,0] op_sel_hi:[0,1,0]
.endm
.macro XL f,o
.if \f < 30
global_load_dwordx4 v[128+4*\f:128+4*\f+3], v3, s[46:47] offset:\o
.else
global_load_dwordx4 a[4*\f-4*30:4*\f-4*30+3], v3, s[46:47] offset:\o
.endif
.endm
.macro XN f,o
.if \f < 30
global_load_dwordx4 v[128+4*\f:128+4*\f+3], v3, s[46:47] offset:\o nt
.else
global_load_dwordx4 a[4*\f-4*30:4*\f-4*30+3], v3, s[46:47] offset:\o nt
.endif
.endm
.macro F t,z
v_fma_f32 v[\t], -v[\z], v[\z], s36
.endm
.macro U t,z
v_mul_f32 v[\t], v[\z], v[\t]
.endm
.macro E t
v_exp_f32 v[\t], v[\t]
.endm
.macro A t
v_add_f32 v[\t], 1.0, v[\t]
.endm
.macro R t
v_rcp_f32 v[\t], v[\t]
.endm
.macro C d,a,b
v_cvt_pk_bf16_f32 v[\d], v[\a], v[\b]
.endm
.macro S0 q,t
v_mul_f32 v[\q], v[\t], v[\t]
.endm
.macro S q,t
v_fmac_f32 v[\q], v[\t], v[\t]
.endm
.macro HC d,z
v_cvt_pk_f16_f32 v[\d], v[\z], v[\z+1]
.endm
.macro HF t,p
v_pk_fma_f16 v[\t], v[\p], v[\p], s36 neg_lo:[1,0,0] neg_hi:[1,0,0]
.endm
.macro HM t,p
v_pk_mul_f16 v[\t], v[\p], v[\t]
.endm
.macro HE d,s
v_exp_f16 v[\d], v[\s]
.endm
.macro HG d,s
v_exp_f16_sdwa v[\d], v[\s] dst_sel:WORD_1 dst_unused:UNUSED_PRESERVE src0_sel:WORD_1
.endm
.macro HA t
v_pk_add_f16 v[\t], v[\t], 1.0 op_sel_hi:[1,0]
.endm
.macro HR d,s
v_rcp_f16 v[\d], v[\s]
.endm
.macro HQ d,s
v_rcp_f16_sdwa v[\d], v[\s] dst_sel:WORD_1 dst_unused:UNUSED_PRESERVE src0_sel:WORD_1
.endm
.macro HP d,s
v_rcp_f16_sdwa v[\d], v[\s] dst_sel:DWORD dst_unused:UNUSED_PAD src0_sel:WORD_1
.endm
.macro HY t,z
v_fma_mix_f32 v[\t], v[\z], v[\t], 0 op_sel_hi:[0,1,0]
.endm
.macro HK p
v_and_b32 v[\p], s56, v[\p]
.endm
.macro LQ p
v_lshrrev_b32 v[\p], 2, v[\p]
.endm
.macro LL t,z
v_lshlrev_b32_sdwa v[\t], 1, v[\z] dst_sel:DWORD dst_unused:UNUSED_PAD src0_sel:DWORD src1_sel:WORD_1
.endm
.macro LH t,p
v_and_b32_sdwa v[\t], s56, v[\p] dst_sel:DWORD dst_unused:UNUSED_PAD src0_sel:DWORD src1_sel:WORD_1
.endm
.macro LA t
ds_read_u16 v[\t], v[\t] offset:5376
.endm
.macro LC_ t
ds_read_u16_d16_hi v[\t], v[\t] offset:5376
.endm
.macro LO p,a,b
v_bfi_b32 v[\p], s36, v[\a], v[\b]
.endm
.macro HZ q,p
v_pk_mul_f16 v[\q], v[\p], v[\p]
.endm
.macro HS q,p
v_pk_fma_f16 v[\q], v[\p], v[\p], v[\q]
.endm
.macro HT q,p
v_fma_mix_f32 v[\q], v[\p], v[\p], v[\q] op_sel:[1,1,0] op_sel_hi:[1,1,0]
.endm
.macro HX q
v_fma_mix_f32 v[\q], v[\q], 1.0, v[\q] op_sel:[0,0,1] op_sel_hi:[1,0,1]
.endm
.macro UX t,z
v_mul_f32_e64 v[\t], v[\z], v[\t]
.endm
.macro EX t
v_exp_f32_e64 v[\t], v[\t]
.endm
.macro AX t
v_add_f32_e64 v[\t], 1.0, v[\t]
.endm
.macro RX t
v_rcp_f32_e64 v[\t], v[\t]
.endm
.macro TX q,t
v_mul_f32_e64 v[\q], v[\t], v[\t]
.endm
.macro SX q,t
v_fmac_f32_e64 v[\q], v[\t], v[\t]
.endm
.macro AW a,s
v_accvgpr_write_b32 a[\a], v[\s]
.endm
.macro RA d,v,o
ds_read_b128 a[\d:\d+3], v[\v] offset:\o
.endm
.macro RV d,v,o
ds_read_b128 v[\d:\d+3], v[\v] offset:\o
.endm
.macro W2 a,b,o
ds_write2st64_b32 v4, v[\a], v[\b] offset0:\o offset1:\o+4
.endm
.macro W1 a,o
ds_write_b32 v4, v[\a] offset:\o
.endm
.macro DW o
global_load_lds_dwordx4 v0, s[40:41] offset:\o
.endm
.macro DF
global_load_lds_dwordx4 v0, s[70:71]
.endm
.macro DX
global_load_lds_dword v3, s[46:47]
.endm
.macro ST d
.if ((\d - 104) / 4) & 1
global_store_dwordx4 v2, v[\d:\d+3], s[42:43] nt sc1
.else
global_store_dwordx2 v31, v[\d:\d+1], s[42:43] nt sc1
.endif
.endm
.macro SU d
.if ((\d - 104) / 4) & 1
global_store_dwordx4 v2, v[\d:\d+3], s[42:43]  sc1
.else
global_store_dwordx2 v31, v[\d:\d+1], s[42:43]  sc1
.endif
.endm
.macro SP
global_atomic_add_f64 v6, v[14:15], s[48:49]
.endm
.macro AT
global_atomic_add v11, v9, v10, s[68:69] sc0
.endm
.macro PFA
s_getpc_b64 s[70:71]
s_add_u32 s70, s70, 7f-.
s_addc_u32 s71, s71, 0
s_andn2_b32 s70, s70, 0x7f
s_min_u32 s53, %[pf], 8f-7f-1152
s_andn2_b32 s53, s53, 0x7f
s_add_u32 s70, s70, s53
s_addc_u32 s71, s71, 0
.endm
.macro PFN
s_getpc_b64 s[70:71]
s_add_u32 s70, s70, k2_code@rel32@lo-1020
s_addc_u32 s71, s71, k2_code@rel32@hi-1012
s_add_u32 s70, s70, %[pf]
s_addc_u32 s71, s71, 0
s_andn2_b32 s70, s70, 0x7f
.endm
.macro PFW
s_getpc_b64 s[70:71]
s_add_u32 s70, s70, g_w2q@rel32@lo+4
s_addc_u32 s71, s71, g_w2q@rel32@hi+12
s_add_u32 s70, s70, %[pf]
s_addc_u32 s71, s71, 0
.endm
.macro PF0
s_getpc_b64 s[70:71]
s_andn2_b32 s70, s70, 0x7f
s_min_u32 s53, %[pf], 7f-.-1152
s_andn2_b32 s53, s53, 0x7f
s_add_u32 s70, s70, s53
s_addc_u32 s71, s71, 0
.endm
.macro WL n
s_waitcnt lgkmcnt(\n)
.endm
.macro WV n
s_waitcnt vmcnt(\n)
.endm
.macro LU t,p
v_pk_fma_f16 v[\t], v[\p], v34, s67
.endm
.macro CF d,a,b
v_cvt_pk_f16_f32 v[\d], v[\a], v[\b]
.endm
.macro XS d
v_pk_fma_f16 v[\d], v[\d], v13, s56
.endm
.macro AR d,a
v_accvgpr_read_b32 v[\d], a[\a]
.endm
.macro DQ o
global_load_lds_dword v23, s[40:41] offset:\o
.endm
.macro GE
v_cmp_eq_u32 vcc, 0, v35
s_nop 4
s_cbranch_vccz 9f
v_mov_b32 v35, 14720
s_mov_b64 exec, 1
global_store_dword v35, v10, s[68:69] sc1
s_mov_b64 exec, -1
9:
.endm
.macro BW f
.if \f < 30
ds_write_b128 v12, v[128+4*\f:128+4*\f+3]
.else
ds_write_b128 v12, a[4*\f-120:4*\f-120+3]
.endif
.endm
.macro BR f
.if \f < 30
ds_read_b128 v[128+4*\f:128+4*\f+3], v20
.else
ds_read_b128 a[4*\f-120:4*\f-120+3], v20
.endif
.endm
.macro LK d,a,f,h
.if \f < 30
v_mfma_f32_4x4x4_16b_bf16 v[\d:\d+3], a[\a:\a+1], v[128+4*\f+\h:128+4*\f+\h+1], v[36:39]
.else
v_mfma_f32_4x4x4_16b_bf16 v[\d:\d+3], a[\a:\a+1], a[4*\f-120+\h:4*\f-120+\h+1], v[36:39]
.endif
.endm
.macro PK d,a,b,s
v_perm_b32 v[\d], v[\a], v[\b], v[\s]
.endm
.macro PO d
v_lshl_or_b32 v[\d], v[\d], 4, v[\d-4]
.endm
.macro LG a,b
v_cmp_eq_f32 vcc, v[\a], v[\b]
v_mov_b32 v[\b], 0x7f800000
s_nop 0
v_cndmask_b32 v[\a], v[\b], v[\a], vcc
.endm
.macro DY
s_mov_b32 s72, m0
s_mov_b32 m0, %[pm]
s_nop 0
global_load_lds_dword v3, s[46:47]
s_mov_b32 m0, s72
s_nop 0
.endm
.macro FP
ds_write_b32 v6, v19 offset:61440
.endm
.macro FR
ds_read_b32 v15, v31 offset:61440
.endm
k1_code:
v_mbcnt_lo_u32_b32 v16, -1, 0
v_mbcnt_hi_u32_b32 v16, -1, v16
v_lshlrev_b32 v0, 4, v16
v_and_b32 v17, 15, v16
v_lshrrev_b32 v18, 4, v16
s_add_u32 s68, %[glo], 1239040
s_addc_u32 s69, %[ghi], 0
s_mov_b32 s53, %[r0o]
s_load_dword s67, s[68:69], 16320
s_add_u32 s82, %[hlo], s53
s_addc_u32 s83, %[hhi], 0
s_add_u32 s84, s82, 8192
s_addc_u32 s85, s83, 0
s_and_b32 s90, %[hlo], 0x1fffff
s_add_u32 s90, s90, s53
s_mov_b32 s91, 3072
v_mov_b32 v11, s90
v_mad_u32_u24 v11, v0, s91, v11
v_lshrrev_b32 v11, 21, v11
v_lshl_add_u32 v11, v11, 2, 64
v_writelane_b32 v11, %[tsel], 63
s_sub_u32 s53, s82, %[hlo]
s_lshr_b32 s53, s53, 2
s_and_b32 s90, %[xlo], 0x1fffff
s_add_u32 s53, s53, s90
s_add_u32 s53, s53, 0x20048000
s_lshr_b32 s53, s53, 21
s_lshl_b32 s90, s53, 2
v_writelane_b32 v11, s90, 48
global_load_dword v11, v11, s[68:69] offset:-2048
v_lshlrev_b32 v19, 1, v18
s_movk_i32 s53, 0x78
v_bfe_u32 v21, v16, 2, 2
v_lshrrev_b32 v19, v19, s53
v_mul_u32_u24 v21, 0x300, v21
v_xor_b32 v19, v19, v16
v_and_b32 v19, 3, v19
v_lshl_add_u32 v21, v19, 4, v21
s_mov_b32 s97, 3072
s_mov_b32 s50, %[pos0]
s_mov_b32 s51, %[pos0]
s_mov_b32 s52, %[pos0]
s_add_u32 s74, %[lds], 0
s_add_u32 s75, s74, 163840
s_mov_b32 s37, s74
s_mul_i32 s53, %[w], 6400
s_add_u32 s76, s74, s53
s_add_u32 s77, s76, 163840
s_mov_b32 s38, s76
s_mul_i32 s60, %[w], 1536
s_add_u32 s61, s60, 512
s_add_u32 s62, s60, 16384
s_add_u32 s63, s60, 16896
s_add_u32 s64, s60, 32768
s_add_u32 s65, s60, 33280
v_lshlrev_b32 v23, 2, v16
s_lshl_b32 s53, %[w], 11
s_min_u32 s53, s53, 5120
s_add_u32 s72, s53, 1228800
s_add_u32 s40, %[glo], s72
s_addc_u32 s41, %[ghi], 0
s_add_u32 s53, s53, %[lds]
s_add_u32 m0, s53, 58368
s_nop 0
DW 0
DW 1024
s_nop 0 ;PS
s_mul_i32 s53, s50, 25600
s_add_u32 s40, %[wlo], s53
s_addc_u32 s41, %[whi], 0
)"
R"(s_mov_b32 m0, s38
s_nop 0
DW 0
DW 1024
DW 2048
s_add_u32 s40, s40, 3072
s_addc_u32 s41, s41, 0
s_add_u32 m0, s38, 3072
s_nop 0
DW 0
DW 1024
DW 2048
DQ 3072
s_add_u32 s50, s50, %[d]
s_cmp_eq_u32 s50, %[wrap]
s_cselect_b32 s50, %[rst], s50
s_add_u32 s38, s38, 32768
s_cmp_eq_u32 s38, s77
s_cselect_b32 s38, s76, s38
v_add_u32 v1, s37, v0
v_lshl_add_u32 v19, v18, 2, v17
v_and_b32 v19, 15, v19
v_lshlrev_b32 v19, 4, v19
v_lshl_add_u32 v2, v18, 8, v19
s_lshl_b32 s53, %[w], 2
s_add_u32 s53, s53, %[lds]
s_add_u32 s53, s53, 123904
s_add_u32 s53, %[lds], 123904
v_bfe_u32 v4, v17, 1, 2
v_and_b32 v5, 8, v17
v_add3_u32 v4, v4, v5, %[w]
v_lshl_add_u32 v4, v18, 2, v4
v_and_b32 v4, 15, v4
v_lshl_add_u32 v4, v17, 4, v4
v_lshl_add_u32 v4, v4, 2, s53
s_lshl_b32 s53, %[w], 9
s_add_u32 s53, s53, %[lds]
s_add_u32 s53, s53, 123904
v_lshlrev_b32 v5, 3, v16
v_add_u32 v5, s53, v5
s_lshl_b32 s53, %[w], 6
v_and_b32 v6, 0x38, v16
v_add_u32 v6, s53, v6
s_add_u32 s53, %[lds], 58368
v_and_b32 v19, 3, v16
v_lshl_add_u32 v19, v18, 2, v19
v_lshl_add_u32 v7, v19, 4, s53
v_lshlrev_b32 v8, 4, v18
v_add_u32 v8, s53, v8
v_mov_b32 v10, 1
s_mov_b32 s36, 0xffff
s_mov_b32 s56, 0x5550555
s_mov_b32 s39, 32768
s_mov_b32 s54, 0x01010101
s_mov_b32 s55, 0x01010101
s_mov_b32 %[nb], 0
v_and_b32 v28, 12, v17
v_lshl_add_u32 v28, v28, 2, v17
v_lshlrev_b32 v28, 2, v28
v_mov_b32 v34, 0x74007400
v_mov_b32 v35, -1
v_and_b32 v22, 0xfffffcff, v1
v_lshrrev_b32 v31, 1, v2
v_mov_b32 v32, 0x06020400
v_mov_b32 v33, 0x07030501
s_waitcnt lgkmcnt(0)
s_mov_b32 s78, 0xaaaaaaaa
s_mov_b32 s79, 0xaaaaaaaa
s_mov_b32 s80, 0xcccccccc
s_mov_b32 s81, 0xcccccccc
s_and_b32 s53, %[w], 1
s_lshl_b32 s53, s53, 10
s_lshr_b32 s72, %[w], 1
s_mul_i32 s72, s72, 32768
s_add_u32 s72, s72, s53
s_add_u32 s72, s72, 128000
s_movk_i32 s53, 0x78
v_lshrrev_b32 v12, 5, v0
v_and_b32 v12, 6, v12
v_bfe_u32 v12, s53, v12, 2
v_lshrrev_b32 v20, 8, v0
v_xor_b32 v20, v12, v20
v_lshl_add_u32 v20, v20, 4, s72
v_and_b32 v12, 0xf0, v0
v_lshl_add_u32 v20, v12, 2, v20
v_add_u32 v12, s72, v0
WV 9
v_bcnt_u32_b32 v11, v11, 0
v_cmp_lt_u32 s[90:91], 2, v11
s_nop 0
s_lshr_b32 s96, s91, 31
s_xor_b32 s96, s96, %[s2]
s_sub_u32 s53, 0, s96
s_xor_b32 s90, s90, s53
s_xor_b32 s91, s91, s53
s_add_u32 s53, %[xo0], 0
s_add_u32 s46, %[xlo], s53
s_addc_u32 s47, %[xhi], 0
s_bitcmp1_b64 s[90:91], 48
s_cselect_b32 s58, 16, 0
s_lshr_b32 s58, 0xfe987610, s58
v_lshrrev_b32 v3, 6, v0
v_and_b32 v3, 12, v3
v_bfe_u32 v3, s58, v3, 4
v_mad_u32_u24 v3, v3, s97, v21
XL 12,0
XL 13,64
XL 14,128
XL 15,192
XL 16,256
XL 17,320
XL 18,384
XL 19,448
XL 20,512
XL 21,576
XL 22,640
XL 23,704
s_mul_i32 s53, s96, 8320
s_cmp_lt_u32 %[ca0], 128
s_cselect_b32 s53, s53, 0
s_add_u32 s73, s53, %[ca0]
v_mov_b32 v9, s73
WV 19
s_barrier
v_lshrrev_b32 v19, 8, v31
v_sub_u32 v19, 0, v19
ds_write_b32 v31, v19 offset:61440
s_nop 1
s_nop 0
v_mov_b32 v19, 0
WV 11
BW 12
RV 40,8,6144
RV 44,8,6208
RV 48,8,6272
RV 248,8,6336
BR 12
WV 10
BW 13
s_nop 1
BR 13
WL 2
LZ 40,12
WV 9
BW 14
RV 40,8,6400
s_nop 0
BR 14
s_add_u32 s53, %[xo0], 0
s_add_u32 s46, %[xlo], s53
s_addc_u32 s47, %[xhi], 0
s_bitcmp1_b64 s[90:91], 48
s_cselect_b32 s58, 16, 0
s_lshr_b32 s58, 0xda43cb52, s58
v_lshrrev_b32 v3, 6, v0
v_and_b32 v3, 12, v3
v_bfe_u32 v3, s58, v3, 4
v_mad_u32_u24 v3, v3, s97, v21
WL 3
LS 44,13
WV 8
BW 15
RV 44,8,6464
s_nop 0
BR 15
XL 24,0
WL 3
LS 48,14
WV 8
BW 16
RV 48,8,6528
s_nop 0
BR 16
WL 3
LS 248,15
WV 7
BW 17
RV 248,8,6592
s_nop 0
BR 17
XL 25,64
WL 3
LS 40,16
WV 7
BW 18
RV 40,8,6656
s_nop 0
BR 18
XL 26,128
WL 3
LS 44,17
WV 7
BW 19
RV 44,8,6720
s_nop 0
BR 19
WL 3
LS 48,18
WV 6
BW 20
RV 48,8,6784
s_nop 0
BR 20
XL 27,192
WL 3
LS 248,19
WV 6
BW 21
RV 248,8,6848
s_nop 0
BR 21
XL 28,256
WL 3
LS 40,20
WV 6
BW 22
RA 240,7,0
s_nop 0
BR 22
WL 3
LS 44,21
WV 5
BW 23
RA 244,7,256
s_nop 0
BR 23
XL 29,320
WL 3
LS 48,22
RA 248,7,512
XL 30,384
WL 1
LS 248,23
RA 252,7,768
s_nop 6
v_mul_f32 v36, 0xbb2aaaab, v36
v_mul_f32 v37, 0xbb2aaaab, v37
v_mul_f32 v38, 0xbb2aaaab, v38
v_mul_f32 v39, 0xbb2aaaab, v39
s_nop 1
LK 40,240,12,0
LK 44,242,12,2
RA 240,7,1024
XL 31,448
LK 48,244,13,0
s_nop 0
CF 176,40,41
LK 248,246,13,2
WL 2
RA 244,7,1280
CF 177,42,43
CF 178,44,45
CF 179,46,47
XL 32,512
LK 40,248,14,0
CF 180,48,49
LK 44,250,14,2
RA 248,7,1536
CF 181,50,51
GZ 12
WL 3
XL 33,576
CF 182,248,249
CF 183,250,251
LK 48,252,15,0
CF 184,40,41
LK 248,254,15,2
RA 252,7,1792
CF 185,42,43
XL 34,640
GS 13
WL 3
CF 186,44,45
CF 187,46,47
LK 40,240,16,0
CF 188,48,49
LK 44,242,16,2
WL 2
RA 240,7,2048
CF 189,50,51
XL 35,704
GS 14
s_mul_i32 s53, s50, 25600
s_add_u32 s40, %[wlo], s53
s_addc_u32 s41, %[whi], 0
s_mov_b32 m0, s38
CF 190,248,249
CF 191,250,251
LK 48,244,17,0
CF 192,40,41
LK 248,246,17,2
RA 244,7,2304
CF 193,42,43
GS 15
WL 3
DW 0
CF 194,44,45
CF 195,46,47
LK 40,248,18,0
CF 196,48,49
LK 44,250,18,2
RA 248,7,2560
CF 197,50,51
DW 1024
GS 16
WL 3
CF 198,248,249
CF 199,250,251
LK 48,252,19,0
CF 200,40,41
LK 248,254,19,2
WL 2
RA 252,7,2816
CF 201,42,43
DW 2048
GS 17
s_add_u32 s40, s40, 3072
s_addc_u32 s41, s41, 0
s_add_u32 m0, s38, 3072
CF 202,44,45
CF 203,46,47
LK 40,240,20,0
CF 204,48,49
LK 44,242,20,2
CF 205,50,51
GS 18
WL 2
DW 0
CF 206,248,249
CF 207,250,251
LK 48,244,21,0
CF 208,40,41
LK 248,246,21,2
CF 209,42,43
DW 1024
GS 19
WL 1
CF 210,44,45
CF 211,46,47
LK 40,248,22,0
CF 212,48,49
LK 44,250,22,2
CF 213,50,51
DW 2048
GS 20
WL 0
DQ 3072
CF 214,248,249
CF 215,250,251
LK 48,252,23,0
CF 216,40,41
LK 248,254,23,2
CF 217,42,43
GS 21
s_add_u32 s50, s50, %[d]
s_cmp_eq_u32 s50, %[wrap]
s_cselect_b32 s50, %[rst], s50
CF 218,44,45
CF 219,46,47
RV 36,8,6912
s_nop 0
GS 22
s_add_u32 s38, s38, 32768
s_cmp_eq_u32 s38, s77
s_cselect_b32 s38, s76, s38
CF 220,48,49
CF 221,50,51
CF 222,248,249
CF 223,250,251
s_nop 1
GS 23
s_nop 7
v_cndmask_b32 v52, v52, v53, s[78:79]
v_cndmask_b32 v54, v54, v55, s[78:79]
v_cndmask_b32 v52, v52, v54, s[80:81]
v_mul_f32 v52, 0x3b2aaaab, v52
WL 0
v_med3_f32 v53, v52, v39, v37
LG 52,53
s_nop 0
C 52,52,52
v_lshlrev_b32 v52, 16, v52
v_add_f32 v52, v36, v52
C 52,52,52
v_lshlrev_b32 v52, 16, v52
v_sqrt_f32 v52, v52
s_nop 0
C 52,52,52
v_lshlrev_b32 v52, 16, v52
v_rcp_f32 v52, v52
s_nop 0
v_mul_f32 v52, v38, v52
CF 52,52,52
ds_bpermute_b32 v13, v28, v52
WV 18
BW 24
RV 40,8,6144
RV 44,8,6208
RV 48,8,6272
BR 24
)"
R"(WV 17
BW 25
s_nop 1
BR 25
WL 7
XS 176
WL 2
LZ 40,24
s_and_b32 s53, %[w], 1
s_mul_i32 s53, s53, 3072
s_lshr_b32 s72, %[w], 1
s_lshl_b32 s40, s72, 16
s_add_u32 s40, s40, s53
s_add_u32 m0, s40, 25600
s_lshl_b32 s72, s72, 13
s_add_u32 s53, s53, s72
s_add_u32 s53, s53, 1382400
s_add_u32 s40, %[glo], s53
s_addc_u32 s41, %[ghi], 0
XS 177
WV 16
BW 26
XS 178
RV 40,8,6336
XS 179
BR 26
XS 180
WL 3
LS 44,25
WV 15
DW 0
XS 181
BW 27
XS 182
RV 44,8,6400
XS 183
BR 27
v_min_u32 v35, v35, v13
WL 3
LS 48,26
WV 15
DW 1024
XS 184
BW 28
XS 185
RV 48,8,6464
XS 186
BR 28
XS 187
WL 3
LS 40,27
WV 15
XS 188
BW 29
XS 189
RV 40,8,6528
XS 190
XS 191
BR 29
XS 192
WL 3
LS 44,28
WV 14
DW 2048
XS 193
BW 30
XS 194
RV 44,8,6592
XS 195
BR 30
XS 196
WL 3
LS 48,29
WV 14
DW 3072
XS 197
BW 31
XS 198
RV 48,8,6656
XS 199
BR 31
XS 200
WL 3
LS 40,30
WV 13
XS 201
BW 32
XS 202
RV 40,8,6720
XS 203
BR 32
XS 204
WL 3
LS 44,31
PF0
s_mov_b32 m0, %[pm]
s_nop 0
DF
PFA
XS 205
BW 33
XS 206
XS 207
RV 44,8,6784
XS 208
BR 33
XS 209
WL 3
LS 48,32
s_mul_i32 s53, s50, 25600
s_add_u32 s40, %[wlo], s53
s_addc_u32 s41, %[whi], 0
s_mov_b32 m0, s38
XS 210
WV 13
BW 34
XS 211
RV 48,8,6848
XS 212
RA 240,7,0
XS 213
BR 34
XS 214
WL 4
LS 40,33
WV 12
XS 215
BW 35
XS 216
RA 244,7,256
XS 217
BR 35
XS 218
WL 3
LS 44,34
DW 0
XS 219
RA 248,7,512
XS 220
WL 1
LS 48,35
DW 1024
XS 221
RA 252,7,768
XS 222
XS 223
s_nop 2
v_mul_f32 v36, 0xbb2aaaab, v36
v_mul_f32 v37, 0xbb2aaaab, v37
v_mul_f32 v38, 0xbb2aaaab, v38
v_mul_f32 v39, 0xbb2aaaab, v39
s_nop 1
LK 40,240,24,0
LK 44,242,24,2
RA 240,7,1024
LK 48,244,25,0
s_nop 1
CF 224,40,41
LK 248,246,25,2
WL 2
RA 244,7,1280
CF 225,42,43
CF 226,44,45
CF 227,46,47
DW 2048
LK 40,248,26,0
CF 228,48,49
LK 44,250,26,2
RA 248,7,1536
CF 229,50,51
s_add_u32 s40, s40, 3072
s_addc_u32 s41, s41, 0
s_add_u32 m0, s38, 3072
GZ 24
WL 3
CF 230,248,249
CF 231,250,251
LK 48,252,27,0
CF 232,40,41
LK 248,254,27,2
RA 252,7,1792
CF 233,42,43
DW 0
GS 25
WL 3
DW 1024
CF 234,44,45
CF 235,46,47
LK 40,240,28,0
CF 236,48,49
LK 44,242,28,2
RA 240,7,2048
CF 237,50,51
GS 26
WL 3
DW 2048
CF 238,248,249
CF 239,250,251
LK 48,244,29,0
CF 240,40,41
LK 248,246,29,2
RA 244,7,2304
CF 241,42,43
DQ 3072
GS 27
WL 3
CF 242,44,45
CF 243,46,47
LK 40,248,30,0
CF 244,48,49
LK 44,250,30,2
WL 2
RA 248,7,2560
CF 245,50,51
s_add_u32 s50, s50, %[d]
s_cmp_eq_u32 s50, %[wrap]
s_cselect_b32 s50, %[rst], s50
GS 28
s_add_u32 s38, s38, 32768
s_cmp_eq_u32 s38, s77
s_cselect_b32 s38, s76, s38
CF 246,248,249
CF 247,250,251
LK 48,252,31,0
CF 152,40,41
LK 248,254,31,2
WL 2
RA 252,7,2816
CF 153,42,43
GS 29
s_add_u32 s53, %[xo0], 196608
s_add_u32 s46, %[xlo], s53
s_addc_u32 s47, %[xhi], 0
s_bitcmp1_b64 s[90:91], 48
s_cselect_b32 s58, 16, 0
s_lshr_b32 s58, 0xfe987610, s58
v_lshrrev_b32 v3, 6, v0
v_and_b32 v3, 12, v3
v_bfe_u32 v3, s58, v3, 4
v_mad_u32_u24 v3, v3, s97, v21
CF 154,44,45
CF 155,46,47
LK 40,240,32,0
CF 156,48,49
LK 44,242,32,2
CF 157,50,51
XL 36,0
GS 6
WL 2
CF 158,248,249
CF 159,250,251
LK 48,244,33,0
CF 160,40,41
LK 248,246,33,2
CF 161,42,43
XL 37,64
GS 7
WL 1
XL 38,128
CF 162,44,45
CF 163,46,47
LK 40,248,34,0
CF 164,48,49
LK 44,250,34,2
CF 165,50,51
GS 8
WL 0
XL 39,192
CF 166,248,249
CF 167,250,251
LK 48,252,35,0
CF 168,40,41
LK 248,254,35,2
CF 169,42,43
XL 40,256
GS 9
CF 170,44,45
CF 171,46,47
RV 36,8,6912
s_nop 0
GS 10
XL 41,320
CF 172,48,49
CF 173,50,51
CF 174,248,249
CF 175,250,251
s_nop 1
GS 11
XL 42,384
WV 15
s_barrier
RA 216,1,0
RA 220,1,1024
RA 224,1,2048
RA 228,1,3072
RV 252,22,5120
v_cndmask_b32 v52, v52, v53, s[78:79]
v_cndmask_b32 v54, v54, v55, s[78:79]
v_cndmask_b32 v52, v52, v54, s[80:81]
v_mul_f32 v52, 0x3b2aaaab, v52
WL 5
v_med3_f32 v53, v52, v39, v37
LG 52,53
s_nop 0
C 52,52,52
v_lshlrev_b32 v52, 16, v52
v_add_f32 v52, v36, v52
C 52,52,52
XL 43,448
v_lshlrev_b32 v52, 16, v52
v_sqrt_f32 v52, v52
s_nop 0
C 52,52,52
v_lshlrev_b32 v52, 16, v52
v_rcp_f32 v52, v52
XL 44,512
v_mul_f32 v52, v38, v52
CF 52,52,52
ds_bpermute_b32 v13, v28, v52
WL 0
XS 224
XS 225
XS 226
XS 227
XS 228
XS 229
XS 230
XL 45,576
XS 231
v_min_u32 v35, v35, v13
XS 232
XS 233
XS 234
XL 46,640
XS 235
XS 236
XS 237
XS 238
XS 239
XL 47,704
Z 0,12,0
XS 240
XS 241
XS 242
XS 243
XS 244
XS 245
XS 246
XS 247
XS 152
XS 153
XS 154
XS 155
M 0,13,1
s_mul_i32 s53, s50, 25600
RA 232,1,4096
AW 0,152
AW 1,153
Z 1,24,0
s_add_u32 s40, %[wlo], s53
AW 2,154
AW 3,155
M 1,25,1
s_addc_u32 s41, %[whi], 0
RA 236,1,6144
XS 156
XS 157
M 0,14,2
s_mov_b32 m0, s38
s_nop 0
DW 0
XS 158
XS 159
AW 4,156
M 0,15,3
RA 216,1,7168
AW 5,157
AW 6,158
M 1,26,2
DW 1024
AW 7,159
XS 160
M 1,27,3
WL 2
RA 220,1,8192
XS 161
XS 162
M 0,16,4
WL 2
XS 163
AW 8,160
M 0,17,5
RA 224,1,9216
AW 9,161
AW 10,162
AW 11,163
M 1,28,4
WL 1
DW 2048
XS 164
)"
R"(XS 165
M 1,29,5
s_add_u32 s40, s40, 3072
RA 228,1,10240
XS 166
XS 167
M 0,18,0
s_addc_u32 s41, s41, 0
WV 14
BW 36
AW 12,164
AW 13,165
M 0,19,1
s_add_u32 m0, s38, 3072
RA 232,1,11264
RV 40,8,6144
AW 14,166
AW 15,167
M 1,30,0
RV 252,22,5392
RV 44,8,6208
XS 168
XS 169
M 1,31,1
WL 6
RA 236,1,12288
RV 48,8,6272
XS 170
XS 171
AW 16,168
M 0,20,2
WL 7
DW 0
BR 36
AW 17,169
AW 18,170
M 0,21,3
RA 216,1,13312
AW 19,171
XS 172
M 1,32,2
DW 1024
XS 173
XS 174
M 1,33,3
WL 7
RA 220,1,14336
XS 175
WV 15
BW 37
s_nop 1
BR 37
AW 20,172
M 0,22,4
WL 6
DW 2048
AW 21,173
AW 22,174
AW 23,175
M 0,23,5
WL 4
RA 224,1,15360
LZ 40,36
WV 15
BW 38
RV 40,8,6336
s_nop 0
BR 38
M 1,34,4
DQ 3072
M 1,35,5
WL 4
RA 228,1,16384
LS 44,37
WV 15
BW 39
RV 44,8,6400
Z 6,12,0
s_add_u32 s50, s50, %[d]
BR 39
LL 16,56
LL 104,57
M 6,13,1
s_cmp_eq_u32 s50, %[wrap]
LA 16
RA 232,1,17408
WL 6
LS 48,38
s_cselect_b32 s50, %[rst], s50
WV 14
BW 40
RV 48,8,6464
Z 7,24,0
s_add_u32 s38, s38, 32768
LC_ 104
BR 40
M 7,25,1
s_cmp_eq_u32 s38, s77
RA 236,1,18432
WL 7
LS 40,39
s_cselect_b32 s38, s76, s38
WV 13
BW 41
RV 40,8,6528
M 6,14,2
s_add_u32 s37, s37, 32768
BR 41
M 6,15,3
s_cmp_eq_u32 s37, s75
RA 216,1,19456
WL 5
LS 44,40
s_cselect_b32 s37, s74, s37
WV 12
BW 42
M 7,26,2
s_add_u32 s53, %[xo0], 196608
RV 44,8,6592
BR 42
LL 17,58
LL 105,59
M 7,27,3
s_add_u32 s46, %[xlo], s53
LA 17
RA 220,1,20480
s_addc_u32 s47, %[xhi], 0
s_bitcmp1_b64 s[90:91], 48
s_cselect_b32 s58, 16, 0
s_lshr_b32 s58, 0xda43cb52, s58
v_lshrrev_b32 v3, 6, v0
v_and_b32 v3, 12, v3
v_bfe_u32 v3, s58, v3, 4
v_mad_u32_u24 v3, v3, s97, v21
WL 6
LS 48,41
WV 10
BW 43
M 6,16,4
WL 3
LC_ 105
XL 48,0
RV 48,8,6656
BR 43
M 6,17,5
s_mul_i32 s53, s51, 49152
RA 224,1,21504
LS 40,42
s_bitcmp1_b64 s[90:91], s51
BW 44
M 7,28,4
WV 10
RV 40,8,6720
LO 104,16,104
LU 16,104
M 7,29,5
s_cselect_b64 s[44:45], s[84:85], s[82:83]
RA 228,1,22528
BR 44
HZ 24,16
WL 5
LS 44,43
s_add_u32 s44, s44, s53
BW 45
M 6,18,0
s_addc_u32 s45, s45, 0
LL 18,60
LL 108,61
s_add_u32 s51, s51, %[d]
s_cmp_eq_u32 s51, %[wrap]
s_cselect_b32 s51, %[rst], s51
XL 49,64
RV 44,8,6784
M 6,19,1
WL 2
LA 18
RA 232,1,23552
BR 45
LS 48,44
WV 10
BW 46
M 7,30,0
LC_ 108
XL 50,128
M 7,31,1
RA 236,1,24576
RV 48,8,6848
RA 240,7,0
M 6,20,2
BR 46
LO 105,17,105
LU 17,105
M 6,21,3
WL 6
XL 51,192
HS 24,17
LS 40,45
WV 11
BW 47
RA 244,7,256
M 7,32,2
LL 16,62
LL 109,63
v_add_u32 v1, s37, v0
BR 47
M 7,33,3
WL 3
LA 16
RA 216,1,0
XL 52,256
LS 44,46
RA 248,7,512
v_and_b32 v22, 0xfffffcff, v1
M 6,22,4
LC_ 109
RA 220,1,1024
M 6,23,5
WL 5
RV 252,22,5120
LS 48,47
RA 252,7,768
s_nop 6
v_mul_f32 v36, 0xbb2aaaab, v36
v_mul_f32 v37, 0xbb2aaaab, v37
v_mul_f32 v38, 0xbb2aaaab, v38
M 7,34,4
XL 53,320
LO 108,18,108
LU 18,108
v_mul_f32 v39, 0xbb2aaaab, v39
M 7,35,5
s_barrier
RA 224,1,2048
HS 24,18
WL 2
Z 0,12,0
LL 17,80
LL 106,81
M 0,13,1
s_mul_i32 s53, s50, 25600
LA 17
RA 228,1,3072
LK 40,240,36,0
LK 44,242,36,2
s_add_u32 s40, %[wlo], s53
RA 240,7,1024
LK 48,244,37,0
s_nop 0
CF 128,40,41
LK 248,246,37,2
s_addc_u32 s41, %[whi], 0
RA 244,7,1280
CF 129,42,43
CF 130,44,45
CF 131,46,47
LK 40,248,38,0
CF 132,48,49
LK 44,250,38,2
s_mov_b32 m0, s38
RA 248,7,1536
CF 133,50,51
GZ 0
WL 6
CF 134,248,249
CF 135,250,251
LK 48,252,39,0
CF 136,40,41
LK 248,254,39,2
RA 252,7,1792
CF 137,42,43
GS 1
CF 138,44,45
CF 139,46,47
Z 1,24,0
LC_ 106
RA 232,1,4096
M 1,25,1
WL 8
RA 236,1,6144
M 0,14,2
WL 7
LO 109,16,109
LU 16,109
M 0,15,3
WL 6
RA 216,1,7168
HS 24,16
LK 40,240,40,0
CF 140,48,49
LK 44,242,40,2
RA 240,7,2048
CF 141,50,51
GS 2
WL 7
CF 142,248,249
CF 143,250,251
LK 48,244,41,0
CF 144,40,41
LK 248,246,41,2
RA 244,7,2304
CF 145,42,43
GS 3
WL 7
CF 146,44,45
CF 147,46,47
LK 40,248,42,0
CF 148,48,49
LK 44,250,42,2
RA 248,7,2560
CF 149,50,51
GS 4
CF 150,248,249
CF 151,250,251
M 1,26,2
DW 0
LL 18,82
LL 107,83
M 1,27,3
WL 7
LA 18
RA 220,1,8192
LK 48,252,43,0
CF 152,40,41
LK 248,254,43,2
RA 252,7,2816
CF 153,42,43
GS 5
WL 8
CF 154,44,45
CF 155,46,47
M 0,16,4
WL 7
LC_ 107
DW 1024
M 0,17,5
WL 6
RA 224,1,9216
HX 24
LK 40,240,44,0
CF 156,48,49
LK 44,242,44,2
CF 157,50,51
GS 6
CF 158,248,249
CF 159,250,251
M 1,28,4
LO 17,17,106
LU 106,17
W1 24,0
M 1,29,5
WL 7
RA 228,1,10240
DW 2048
HZ 25,106
LK 48,244,45,0
CF 160,40,41
LK 248,246,45,2
CF 161,42,43
GS 7
WL 5
CF 162,44,45
CF 163,46,47
M 0,18,0
s_add_u32 s40, s40, 3072
PK 106,17,104,33
PK 104,17,104,32
LL 16,84
LL 110,85
M 0,19,1
s_addc_u32 s41, s41, 0
LA 16
RA 232,1,11264
LK 40,248,46,0
CF 164,48,49
LK 44,250,46,2
s_add_u32 m0, s38, 3072
CF 165,50,51
GS 8
CF 166,248,249
CF 167,250,251
M 1,30,0
LC_ 110
RV 252,22,5392
M 1,31,1
WL 6
RA 236,1,12288
M 0,20,2
WL 5
LO 18,18,107
LU 107,18
M 0,21,3
RA 216,1,13312
DW 0
HS 25,107
LK 48,252,47,0
CF 168,40,41
LK 248,254,47,2
CF 169,42,43
GS 9
CF 170,44,45
)"
R"(CF 171,46,47
M 1,32,2
PK 107,18,105,33
PK 105,18,105,32
LL 17,86
LL 111,87
M 1,33,3
WL 1
LA 17
RA 220,1,14336
DW 1024
M 0,22,4
s_add_u32 s42, s44, s60
LC_ 111
RV 36,8,6912
s_addc_u32 s43, s45, 0
M 0,23,5
RA 224,1,15360
GS 10
CF 172,48,49
CF 173,50,51
M 1,34,4
LO 16,16,110
LU 110,16
ST 104
DW 2048
M 1,35,5
WL 5
RA 228,1,16384
HS 25,110
Z 6,12,0
WL 4
PK 110,16,108,33
PK 108,16,108,32
LL 18,56
LL 104,57
M 6,13,1
WL 1
LA 18
DQ 3072
PO 110
CF 174,248,249
CF 175,250,251
s_nop 1
GS 11
s_add_u32 s50, s50, %[d]
RA 232,1,17408
s_nop 5
v_cndmask_b32 v52, v52, v53, s[78:79]
Z 7,24,0
s_cmp_eq_u32 s50, %[wrap]
LC_ 104
v_cndmask_b32 v54, v54, v55, s[78:79]
M 7,25,1
s_cselect_b32 s50, %[rst], s50
RA 236,1,18432
v_cndmask_b32 v52, v52, v54, s[80:81]
v_mul_f32 v52, 0x3b2aaaab, v52
M 6,14,2
WL 2
LO 17,17,111
LU 111,17
v_med3_f32 v53, v52, v39, v37
LG 52,53
M 6,15,3
s_add_u32 s38, s38, 32768
RA 216,1,19456
HS 25,111
C 52,52,52
v_lshlrev_b32 v52, 16, v52
M 7,26,2
s_cmp_eq_u32 s38, s77
PK 111,17,109,33
PK 109,17,109,32
LL 16,58
LL 105,59
s_cselect_b32 s38, s76, s38
v_add_f32 v52, v36, v52
C 52,52,52
M 7,27,3
s_add_u32 s37, s37, 32768
LA 16
PO 111
s_cmp_eq_u32 s37, s75
s_cselect_b32 s37, s74, s37
v_lshlrev_b32 v52, 16, v52
v_sqrt_f32 v52, v52
M 6,16,4
WL 2
LC_ 105
RA 220,1,20480
XL 54,384
C 52,52,52
v_lshlrev_b32 v52, 16, v52
M 6,17,5
s_add_u32 s42, s44, s61
RA 224,1,21504
HX 25
s_addc_u32 s43, s45, 0
v_rcp_f32 v52, v52
s_nop 0
v_mul_f32 v52, v38, v52
M 7,28,4
LO 104,18,104
LU 18,104
W1 25,1024
CF 52,52,52
M 7,29,5
WL 2
RA 228,1,22528
HZ 24,18
ST 108
XL 55,448
ds_bpermute_b32 v13, v28, v52
M 6,18,0
s_mul_i32 s53, s51, 49152
LL 17,60
LL 108,61
s_bitcmp1_b64 s[90:91], s51
s_cselect_b64 s[44:45], s[84:85], s[82:83]
s_add_u32 s44, s44, s53
s_addc_u32 s45, s45, 0
s_add_u32 s51, s51, %[d]
s_cmp_eq_u32 s51, %[wrap]
s_cselect_b32 s51, %[rst], s51
M 6,19,1
LA 17
RA 232,1,23552
M 7,30,0
WL 5
LC_ 108
XL 56,512
v_add_u32 v4, s39, v4
M 7,31,1
WV 17
RA 236,1,24576
s_nop 1
v_add_u32 v1, s37, v0
BW 48
M 6,20,2
WL 6
XL 57,576
RV 40,8,6144
RV 44,8,6208
LO 105,16,105
LU 16,105
M 6,21,3
RA 216,1,0
RV 48,8,6272
HS 24,16
M 7,32,2
WL 5
BR 48
LL 18,62
LL 109,63
M 7,33,3
WV 16
LA 18
XL 58,640
BW 49
s_nop 1
BR 49
M 6,22,4
s_lshl_b32 s53, s52, 8
LC_ 109
v_and_b32 v22, 0xfffffcff, v1
XS 128
M 6,23,5
s_add_u32 s48, %[alo], s53
RA 220,1,1024
XS 129
XS 130
XS 131
WL 5
LZ 40,48
s_addc_u32 s49, %[ahi], 0
BW 50
M 7,34,4
WV 16
LO 108,17,108
LU 17,108
s_add_u32 s52, s52, %[d]
s_cmp_eq_u32 s52, %[wrap]
s_cselect_b32 s52, %[rst], s52
XL 59,704
RV 40,8,6336
BR 50
AW 24,128
AW 25,129
M 7,35,5
s_barrier
RV 252,22,5120
HS 24,17
AW 26,130
AW 27,131
WL 0
Z 0,12,0
s_sub_u32 s39, 0, s39
RA 224,1,2048
LL 16,80
LL 106,81
XS 132
XS 133
M 0,13,1
s_mul_i32 s53, s50, 25600
LA 16
RA 228,1,3072
XS 134
XS 135
AW 28,132
LS 44,49
s_add_u32 s40, %[wlo], s53
BW 51
RV 44,8,6400
s_nop 0
BR 51
Z 1,24,0
s_addc_u32 s41, %[whi], 0
LC_ 106
RA 232,1,4096
AW 29,133
AW 30,134
M 1,25,1
s_mov_b32 m0, s38
RA 236,1,6144
ds_read_b64 v[14:15], v5 offset:0
AW 31,135
v_min_u32 v35, v35, v13
LS 48,50
WV 16
BW 52
RV 48,8,6464
WL 11
M 0,14,2
WL 9
BR 52
LO 109,18,109
LU 18,109
XS 136
XS 137
M 0,15,3
WL 7
RA 216,1,7168
HS 24,18
XS 138
XS 139
AW 32,136
LS 40,51
WV 15
BW 53
M 1,26,2
DW 0
RV 40,8,6528
LL 17,82
LL 107,83
AW 33,137
AW 34,138
M 1,27,3
WL 8
LA 17
RA 220,1,8192
v_subrev_u32 v5, s39, v5
BR 53
AW 35,139
XS 140
M 0,16,4
WL 10
LC_ 107
DW 1024
XS 141
XS 142
M 0,17,5
WL 7
RA 224,1,9216
v_add_f32 v14, v14, v15
s_nop 1
v_add_f32_dpp v14, v14, v14 quad_perm:[1,0,3,2] row_mask:0xf bank_mask:0xf
s_nop 1
v_add_f32_dpp v14, v14, v14 quad_perm:[2,3,0,1] row_mask:0xf bank_mask:0xf
s_nop 1
v_add_f32_dpp v14, v14, v14 row_half_mirror row_mask:0xf bank_mask:0xf
HX 24
XS 143
AW 36,140
AW 37,141
LS 44,52
WV 8
BW 54
RV 44,8,6592
M 1,28,4
LO 16,16,106
LU 106,16
v_cvt_f64_f32 v[14:15], v14
W1 24,0
BR 54
AW 38,142
AW 39,143
M 1,29,5
WL 6
RA 228,1,10240
HZ 25,106
s_mov_b64 exec, s[54:55]
SP
s_mov_b64 exec, -1
DW 2048
XS 144
XS 145
LS 48,53
s_add_u32 s40, s40, 3072
WV 8
BW 55
M 0,18,0
s_addc_u32 s41, s41, 0
RV 48,8,6656
PK 106,16,104,33
PK 104,16,104,32
LL 18,84
LL 110,85
XS 146
XS 147
M 0,19,1
s_add_u32 m0, s38, 3072
LA 18
RA 232,1,11264
BR 55
AW 40,144
AW 41,145
AW 42,146
M 1,30,0
LC_ 110
RV 252,22,5392
AW 43,147
XS 148
M 1,31,1
WL 8
RA 236,1,12288
XS 149
XS 150
LS 40,54
WV 7
BW 56
RV 40,8,6720
M 0,20,2
WL 10
DW 0
BR 56
LO 17,17,107
LU 107,17
XS 151
AW 44,148
M 0,21,3
WL 6
RA 216,1,13312
HS 25,107
AW 45,149
AW 46,150
AW 47,151
LS 44,55
WV 7
BW 57
M 1,32,2
WL 5
DW 1024
RV 44,8,6784
PK 107,17,105,33
PK 105,17,105,32
LL 16,86
LL 111,87
XS 152
XS 153
M 1,33,3
s_add_u32 s42, s44, s60
LA 16
RA 220,1,14336
BR 57
XS 154
XS 155
M 0,22,4
s_addc_u32 s43, s45, 0
LC_ 111
DW 2048
AW 48,152
AW 49,153
M 0,23,5
WL 7
RA 224,1,15360
AW 50,154
AW 51,155
XS 156
LS 48,56
WV 8
BW 58
RV 48,8,6848
M 1,34,4
LO 18,18,110
LU 110,18
ST 104
RA 240,7,0
XS 157
XS 158
)"
R"(M 1,35,5
WL 5
RA 228,1,16384
DQ 3072
BR 58
HS 25,110
XS 159
AW 52,156
LS 40,57
WV 9
BW 59
Z 6,12,0
s_add_u32 s50, s50, %[d]
PK 110,18,108,33
PK 108,18,108,32
LL 17,56
LL 104,57
AW 53,157
AW 54,158
M 6,13,1
s_cmp_eq_u32 s50, %[wrap]
LA 17
RA 244,7,256
PO 110
AW 55,159
XS 160
Z 7,24,0
s_cselect_b32 s50, %[rst], s50
LC_ 104
RA 232,1,17408
BR 59
XS 161
XS 162
XS 163
M 7,25,1
s_add_u32 s38, s38, 32768
RA 236,1,18432
AW 56,160
AW 57,161
WL 7
LS 44,58
s_cmp_eq_u32 s38, s77
RA 248,7,512
AW 58,162
M 6,14,2
WL 2
LO 16,16,111
LU 111,16
s_cselect_b32 s38, s76, s38
AW 59,163
M 6,15,3
s_add_u32 s37, s37, 32768
RA 216,1,19456
HS 25,111
XS 164
XS 165
LS 48,59
s_cmp_eq_u32 s37, s75
RA 252,7,768
XS 166
M 7,26,2
s_cselect_b32 s37, s74, s37
PK 111,16,109,33
PK 109,16,109,32
LL 18,58
LL 105,59
XS 167
AW 60,164
M 7,27,3
s_add_u32 s53, %[xo0], 393216
LA 18
PO 111
s_add_u32 s46, %[xlo], s53
s_addc_u32 s47, %[xhi], 0
s_bitcmp1_b64 s[90:91], 48
s_cselect_b32 s58, 16, 0
s_lshr_b32 s58, 0xfe987610, s58
v_lshrrev_b32 v3, 6, v0
v_and_b32 v3, 12, v3
v_bfe_u32 v3, s58, v3, 4
v_mad_u32_u24 v3, v3, s97, v21
AW 61,165
AW 62,166
M 6,16,4
s_add_u32 s42, s44, s61
LC_ 105
RA 220,1,20480
AW 63,167
XS 168
WL 6
M 6,17,5
s_addc_u32 s43, s45, 0
RA 224,1,21504
XL 60,0
HX 25
XS 169
XS 170
M 7,28,4
LO 104,17,104
LU 17,104
W1 25,1024
XS 171
AW 64,168
AW 65,169
M 7,29,5
WL 2
RA 228,1,22528
HZ 24,17
ST 108
AW 66,170
AW 67,171
M 6,18,0
s_mul_i32 s53, s51, 49152
LL 16,60
LL 108,61
s_bitcmp1_b64 s[90:91], s51
s_cselect_b64 s[44:45], s[84:85], s[82:83]
s_add_u32 s44, s44, s53
s_addc_u32 s45, s45, 0
s_add_u32 s51, s51, %[d]
s_cmp_eq_u32 s51, %[wrap]
s_cselect_b32 s51, %[rst], s51
XL 61,64
XS 172
XS 173
M 6,19,1
LA 16
RA 232,1,23552
XS 174
XS 175
M 7,30,0
LC_ 108
XL 62,128
v_add_u32 v4, s39, v4
AW 68,172
AW 69,173
AW 70,174
M 7,31,1
RA 236,1,24576
s_nop 1
v_add_u32 v1, s37, v0
AW 71,175
v_mul_f32 v36, 0xbb2aaaab, v36
v_mul_f32 v37, 0xbb2aaaab, v37
v_mul_f32 v38, 0xbb2aaaab, v38
v_mul_f32 v39, 0xbb2aaaab, v39
s_nop 1
LK 40,240,48,0
LK 44,242,48,2
RA 240,7,1024
LK 48,244,49,0
s_nop 1
CF 128,40,41
LK 248,246,49,2
RA 244,7,1280
CF 129,42,43
CF 130,44,45
CF 131,46,47
LK 40,248,50,0
CF 132,48,49
LK 44,250,50,2
RA 248,7,1536
CF 133,50,51
GZ 0
CF 134,248,249
CF 135,250,251
LK 48,252,51,0
CF 136,40,41
LK 248,254,51,2
RA 252,7,1792
CF 137,42,43
GS 1
WL 8
CF 138,44,45
CF 139,46,47
M 6,20,2
LO 105,18,105
LU 18,105
M 6,21,3
WL 3
RA 216,1,0
XL 63,192
HS 24,18
LK 40,240,52,0
CF 140,48,49
LK 44,242,52,2
RA 240,7,2048
CF 141,50,51
GS 2
WL 4
CF 142,248,249
CF 143,250,251
LK 48,244,53,0
CF 144,40,41
LK 248,246,53,2
RA 244,7,2304
CF 145,42,43
GS 3
WL 4
CF 146,44,45
CF 147,46,47
LK 40,248,54,0
CF 148,48,49
LK 44,250,54,2
RA 248,7,2560
CF 149,50,51
GS 4
CF 150,248,249
CF 151,250,251
M 7,32,2
LL 17,62
LL 109,63
M 7,33,3
WL 4
LA 17
XL 64,256
LK 48,252,55,0
CF 152,40,41
LK 248,254,55,2
s_lshl_b32 s53, s52, 8
RA 252,7,2816
CF 153,42,43
GS 5
s_add_u32 s48, %[alo], s53
CF 154,44,45
CF 155,46,47
M 6,22,4
s_addc_u32 s49, %[ahi], 0
LC_ 109
v_and_b32 v22, 0xfffffcff, v1
M 6,23,5
s_add_u32 s52, s52, %[d]
RA 220,1,1024
M 7,34,4
LO 108,16,108
LU 16,108
s_cmp_eq_u32 s52, %[wrap]
s_cselect_b32 s52, %[rst], s52
XL 65,320
M 7,35,5
s_barrier
RV 252,22,5120
HS 24,16
WL 0
Z 0,12,0
s_sub_u32 s39, 0, s39
RA 224,1,2048
LL 18,80
LL 106,81
M 0,13,1
s_mul_i32 s53, s50, 25600
LA 18
RA 228,1,3072
LK 40,240,56,0
CF 156,48,49
LK 44,242,56,2
s_add_u32 s40, %[wlo], s53
CF 157,50,51
GS 6
s_addc_u32 s41, %[whi], 0
CF 158,248,249
CF 159,250,251
LK 48,244,57,0
CF 160,40,41
LK 248,246,57,2
s_mov_b32 m0, s38
CF 161,42,43
GS 7
CF 162,44,45
CF 163,46,47
Z 1,24,0
LC_ 106
RA 232,1,4096
M 1,25,1
RA 236,1,6144
ds_read_b64 v[14:15], v5 offset:0
LK 40,248,58,0
CF 164,48,49
LK 44,250,58,2
CF 165,50,51
GS 8
WL 6
CF 166,248,249
CF 167,250,251
M 0,14,2
WL 4
LO 109,17,109
LU 17,109
M 0,15,3
RA 216,1,7168
HS 24,17
LK 48,252,59,0
CF 168,40,41
LK 248,254,59,2
CF 169,42,43
v_subrev_u32 v5, s39, v5
GS 9
CF 170,44,45
CF 171,46,47
M 1,26,2
DW 0
LL 16,82
LL 107,83
M 1,27,3
WL 3
LA 16
RA 220,1,8192
M 0,16,4
WL 4
LC_ 107
DW 1024
M 0,17,5
RA 224,1,9216
RV 36,8,6912
HX 24
GS 10
CF 172,48,49
CF 173,50,51
M 1,28,4
LO 18,18,106
LU 106,18
W1 24,0
M 1,29,5
WL 4
RA 228,1,10240
HZ 25,106
v_add_f32 v14, v14, v15
s_nop 1
v_add_f32_dpp v14, v14, v14 quad_perm:[1,0,3,2] row_mask:0xf bank_mask:0xf
s_nop 1
v_add_f32_dpp v14, v14, v14 quad_perm:[2,3,0,1] row_mask:0xf bank_mask:0xf
s_nop 1
v_add_f32_dpp v14, v14, v14 row_half_mirror row_mask:0xf bank_mask:0xf
v_cvt_f64_f32 v[14:15], v14
s_mov_b64 exec, s[54:55]
SP
s_mov_b64 exec, -1
DW 2048
M 0,18,0
s_add_u32 s40, s40, 3072
PK 106,18,104,33
PK 104,18,104,32
LL 17,84
LL 110,85
M 0,19,1
s_addc_u32 s41, s41, 0
LA 17
RA 232,1,11264
M 1,30,0
s_add_u32 m0, s38, 3072
LC_ 110
RV 252,22,5392
M 1,31,1
WL 7
RA 236,1,12288
CF 174,248,249
CF 175,250,251
M 0,20,2
WL 5
LO 16,16,107
LU 107,16
M 0,21,3
RA 216,1,13312
DW 0
HS 25,107
GS 11
WL 4
DW 1024
s_nop 5
v_cndmask_b32 v52, v52, v53, s[78:79]
v_cndmask_b32 v54, v54, v55, s[78:79]
v_cndmask_b32 v52, v52, v54, s[80:81]
M 1,32,2
s_add_u32 s42, s44, s60
PK 107,16,105,33
PK 105,16,105,32
LL 18,86
LL 111,87
v_mul_f32 v52, 0x3b2aaaab, v52
M 1,33,3
s_addc_u32 s43, s45, 0
LA 18
RA 220,1,14336
v_med3_f32 v53, v52, v39, v37
M 0,22,4
WL 3
LC_ 111
ST 104
LG 52,53
s_nop 0
C 52,52,52
M 0,23,5
RA 224,1,15360
v_lshlrev_b32 v52, 16, v52
M 1,34,4
DW 2048
LO 17,17,110
LU 110,17
v_add_f32 v52, v36, v52
M 1,35,5
)"
R"(WL 4
RA 228,1,16384
HS 25,110
C 52,52,52
v_lshlrev_b32 v52, 16, v52
Z 6,12,0
WL 3
PK 110,17,108,33
PK 108,17,108,32
LL 16,56
LL 104,57
v_sqrt_f32 v52, v52
M 6,13,1
WL 1
LA 16
DQ 3072
PO 110
C 52,52,52
Z 7,24,0
s_add_u32 s50, s50, %[d]
LC_ 104
RA 232,1,17408
v_lshlrev_b32 v52, 16, v52
v_rcp_f32 v52, v52
M 7,25,1
s_cmp_eq_u32 s50, %[wrap]
RA 236,1,18432
s_cselect_b32 s50, %[rst], s50
v_mul_f32 v52, v38, v52
M 6,14,2
WL 4
LO 18,18,111
LU 111,18
CF 52,52,52
M 6,15,3
s_add_u32 s38, s38, 32768
RA 216,1,19456
HS 25,111
ds_bpermute_b32 v13, v28, v52
M 7,26,2
s_cmp_eq_u32 s38, s77
PK 111,18,109,33
PK 109,18,109,32
LL 17,58
LL 105,59
s_cselect_b32 s38, s76, s38
M 7,27,3
s_add_u32 s37, s37, 32768
LA 17
PO 111
s_cmp_eq_u32 s37, s75
s_cselect_b32 s37, s74, s37
WL 4
M 6,16,4
WL 3
LC_ 105
RA 220,1,20480
XL 66,384
M 6,17,5
s_add_u32 s42, s44, s61
RA 224,1,21504
HX 25
s_addc_u32 s43, s45, 0
M 7,28,4
LO 104,16,104
LU 16,104
W1 25,1024
M 7,29,5
WL 2
RA 228,1,22528
HZ 24,16
ST 108
XL 67,448
M 6,18,0
s_mul_i32 s53, s51, 49152
LL 18,60
LL 108,61
s_bitcmp1_b64 s[90:91], s51
s_cselect_b64 s[44:45], s[84:85], s[82:83]
s_add_u32 s44, s44, s53
s_addc_u32 s45, s45, 0
s_add_u32 s51, s51, %[d]
s_cmp_eq_u32 s51, %[wrap]
s_cselect_b32 s51, %[rst], s51
M 6,19,1
LA 18
RA 232,1,23552
M 7,30,0
LC_ 108
XL 68,512
v_add_u32 v4, s39, v4
XS 128
M 7,31,1
WL 5
RA 236,1,24576
s_nop 1
v_add_u32 v1, s37, v0
XS 129
XS 130
M 6,20,2
WL 4
XL 69,576
LO 105,17,105
LU 17,105
XS 131
M 6,21,3
RA 216,1,0
HS 24,17
AW 72,128
AW 73,129
M 7,32,2
LL 16,62
LL 109,63
AW 74,130
AW 75,131
M 7,33,3
WL 1
LA 16
XL 70,640
XS 132
XS 133
M 6,22,4
s_lshl_b32 s53, s52, 8
LC_ 109
v_and_b32 v22, 0xfffffcff, v1
XS 134
M 6,23,5
s_add_u32 s48, %[alo], s53
RA 220,1,1024
XS 135
AW 76,132
M 7,34,4
WV 22
LO 108,18,108
LU 18,108
s_addc_u32 s49, %[ahi], 0
s_add_u32 s52, s52, %[d]
s_cmp_eq_u32 s52, %[wrap]
s_cselect_b32 s52, %[rst], s52
XL 71,704
AW 77,133
AW 78,134
M 7,35,5
s_barrier
RV 252,22,5120
HS 24,18
AW 79,135
v_min_u32 v35, v35, v13
WL 0
Z 0,12,0
s_sub_u32 s39, 0, s39
LL 17,80
LL 106,81
XS 136
XS 137
M 0,13,1
s_mul_i32 s53, s50, 25600
LA 17
RA 224,1,2048
XS 138
Z 1,24,0
s_add_u32 s40, %[wlo], s53
LC_ 106
RA 228,1,3072
XS 139
AW 80,136
M 1,25,1
s_addc_u32 s41, %[whi], 0
RA 232,1,4096
AW 81,137
AW 82,138
Z 2,36,0
LO 109,16,109
LU 16,109
AW 83,139
XS 140
M 2,37,1
s_mov_b32 m0, s38
ds_read_b64 v[14:15], v5 offset:0
HS 24,16
XS 141
Z 3,48,0
WL 2
LL 18,82
LL 107,83
XS 142
XS 143
M 3,49,1
LA 18
RA 236,1,6144
AW 84,140
AW 85,141
M 0,14,2
LC_ 107
DW 0
AW 86,142
AW 87,143
M 0,15,3
v_subrev_u32 v5, s39, v5
HX 24
XS 144
XS 145
M 1,26,2
LO 17,17,106
LU 106,17
XS 146
M 1,27,3
RA 216,1,7168
HZ 25,106
XS 147
AW 88,144
M 2,38,2
PK 106,17,104,33
PK 104,17,104,32
LL 16,84
LL 110,85
AW 89,145
AW 90,146
M 2,39,3
LA 16
W1 24,0
AW 91,147
XS 148
M 3,50,2
WL 4
LC_ 110
DW 1024
XS 149
M 3,51,3
RA 220,1,8192
v_add_f32 v14, v14, v15
XS 150
XS 151
M 0,16,4
WL 5
LO 18,18,107
LU 107,18
AW 92,148
AW 93,149
M 0,17,5
DW 2048
HS 25,107
v_add_f32_dpp v14, v14, v14 quad_perm:[1,0,3,2] row_mask:0xf bank_mask:0xf
AW 94,150
AW 95,151
M 1,28,4
s_add_u32 s42, s44, s60
PK 107,18,105,33
PK 105,18,105,32
LL 17,86
LL 111,87
XS 152
M 1,29,5
s_addc_u32 s43, s45, 0
LA 17
RA 224,1,9216
v_add_f32_dpp v14, v14, v14 quad_perm:[2,3,0,1] row_mask:0xf bank_mask:0xf
XS 153
XS 154
M 2,40,4
LC_ 111
ST 104
XS 155
AW 96,152
M 2,41,5
WV 26
BW 60
v_add_f32_dpp v14, v14, v14 row_half_mirror row_mask:0xf bank_mask:0xf
AW 97,153
AW 98,154
M 3,52,4
WL 4
LO 16,16,110
LU 110,16
AW 99,155
XS 156
M 3,53,5
s_add_u32 s40, s40, 3072
RA 228,1,10240
HS 25,110
v_cvt_f64_f32 v[14:15], v14
XS 157
M 0,18,0
s_addc_u32 s41, s41, 0
PK 110,16,108,33
PK 108,16,108,32
XS 158
XS 159
M 0,19,1
s_add_u32 m0, s38, 3072
s_mov_b64 exec, s[54:55]
SP
s_mov_b64 exec, -1
PO 110
AW 100,156
AW 101,157
M 1,30,0
RV 40,8,6144
RV 44,8,6208
AW 102,158
AW 103,159
M 1,31,1
RA 232,1,11264
RV 48,8,6272
XS 160
M 2,42,0
WL 6
LO 17,17,111
LU 111,17
XS 161
XS 162
M 2,43,1
RV 252,22,5392
HS 25,111
XS 163
AW 104,160
M 3,54,0
WL 5
PK 111,17,109,33
PK 109,17,109,32
AW 105,161
AW 106,162
M 3,55,1
RA 236,1,12288
PO 111
AW 107,163
M 0,20,2
WV 25
DW 0
BR 60
XS 164
XS 165
M 0,21,3
s_add_u32 s42, s44, s61
BW 61
XS 166
XS 167
M 1,32,2
s_addc_u32 s43, s45, 0
ST 108
BR 61
AW 108,164
AW 109,165
M 1,33,3
RA 216,1,13312
HX 25
AW 110,166
AW 111,167
M 2,44,2
W1 25,1024
XS 168
M 2,45,3
DW 1024
v_add_u32 v4, s39, v4
XS 169
XS 170
M 3,56,2
WL 5
XS 171
AW 112,168
M 3,57,3
WL 4
RA 220,1,14336
AW 113,169
AW 114,170
LZ 40,60
WV 27
BW 62
M 0,22,4
DW 2048
RV 40,8,6336
AW 115,171
M 0,23,5
WL 5
BR 62
XS 172
XS 173
LS 44,61
WV 27
BW 63
M 1,34,4
RV 44,8,6400
BR 63
XS 174
XS 175
M 1,35,5
RA 224,1,15360
AW 116,172
AW 117,173
M 2,46,4
LL 18,56
LL 104,57
AW 118,174
AW 119,175
M 2,47,5
LA 18
DQ 3072
M 3,58,4
WL 8
LC_ 104
)"
R"(M 3,59,5
WV 27
RA 228,1,16384
Z 6,12,0
WL 7
M 6,13,1
s_add_u32 s50, s50, %[d]
LS 48,62
s_cmp_eq_u32 s50, %[wrap]
BW 64
Z 7,24,0
s_cselect_b32 s50, %[rst], s50
RV 48,8,6464
LL 16,58
LL 105,59
M 7,25,1
WL 6
LA 16
RA 232,1,17408
BR 64
LS 40,63
WV 26
BW 65
Z 8,36,0
LC_ 105
RV 40,8,6528
M 8,37,1
WV 16
BR 65
Z 9,48,0
WL 9
LO 104,18,104
LU 18,104
M 9,49,1
s_add_u32 s38, s38, 32768
RA 236,1,18432
HZ 24,18
M 6,14,2
s_cmp_eq_u32 s38, s77
LL 17,60
LL 108,61
M 6,15,3
s_cselect_b32 s38, s76, s38
LA 17
WL 6
LS 44,64
s_add_u32 s37, s37, 32768
BW 66
M 7,26,2
s_cmp_eq_u32 s37, s75
LC_ 108
RV 44,8,6592
M 7,27,3
s_cselect_b32 s37, s74, s37
RA 216,1,19456
BR 66
M 8,38,2
WL 7
LO 105,16,105
LU 16,105
M 8,39,3
s_add_u32 s53, %[xo0], 393216
HS 24,16
LS 48,65
s_add_u32 s46, %[xlo], s53
WV 14
BW 67
M 9,50,2
WL 7
LL 18,62
LL 109,63
M 9,51,3
s_addc_u32 s47, %[xhi], 0
LA 18
RA 220,1,20480
RV 48,8,6656
M 6,16,4
s_bitcmp1_b64 s[90:91], 48
LC_ 109
BR 67
M 6,17,5
s_cselect_b32 s58, 16, 0
M 7,28,4
WL 9
LO 108,17,108
LU 17,108
s_lshr_b32 s58, 0xda43cb52, s58
v_lshrrev_b32 v3, 6, v0
v_and_b32 v3, 12, v3
v_bfe_u32 v3, s58, v3, 4
v_mad_u32_u24 v3, v3, s97, v21
M 7,29,5
WL 6
RA 224,1,21504
HS 24,17
LS 40,66
WV 13
BW 68
M 8,40,4
RV 40,8,6720
LL 16,64
LL 112,65
M 8,41,5
WV 12
LA 16
XL 72,0
BR 68
M 9,52,4
WL 5
LC_ 112
M 9,53,5
s_mul_i32 s53, s51, 49152
RA 228,1,22528
LS 44,67
s_bitcmp1_b64 s[90:91], s51
BW 69
M 6,18,0
RV 44,8,6784
LO 109,18,109
LU 18,109
M 6,19,1
s_cselect_b64 s[44:45], s[84:85], s[82:83]
BR 69
HS 24,18
s_add_u32 s44, s44, s53
s_addc_u32 s45, s45, 0
s_add_u32 s51, s51, %[d]
s_cmp_eq_u32 s51, %[wrap]
s_cselect_b32 s51, %[rst], s51
M 7,30,0
LL 17,66
LL 113,67
M 7,31,1
WL 5
LA 17
RA 232,1,23552
LS 48,68
WV 12
BW 70
M 8,42,0
LC_ 113
XL 73,64
M 8,43,1
s_lshl_b32 s53, s52, 8
RV 48,8,6848
RA 240,7,0
M 9,54,0
WL 9
LO 112,16,112
LU 16,112
M 9,55,1
s_add_u32 s48, %[alo], s53
RA 236,1,24576
BR 70
HS 24,16
M 6,20,2
s_addc_u32 s49, %[ahi], 0
LL 18,68
LL 116,69
s_add_u32 s52, s52, %[d]
s_cmp_eq_u32 s52, %[wrap]
s_cselect_b32 s52, %[rst], s52
M 6,21,3
WL 8
LA 18
XL 74,128
LS 40,69
WV 13
BW 71
M 7,32,2
LC_ 116
XL 75,192
RA 244,7,256
M 7,33,3
BR 71
v_add_u32 v1, s37, v0
M 8,44,2
WL 9
LO 113,17,113
LU 17,113
M 8,45,3
WL 5
RA 216,1,0
HS 24,17
LS 44,70
RA 248,7,512
v_and_b32 v22, 0xfffffcff, v1
M 9,56,2
LL 16,70
LL 117,71
M 9,57,3
WL 2
LA 16
RA 220,1,1024
LS 48,71
RA 252,7,768
M 6,22,4
LC_ 117
RV 252,22,5120
M 6,23,5
XL 76,256
s_nop 1
v_mul_f32 v36, 0xbb2aaaab, v36
v_mul_f32 v37, 0xbb2aaaab, v37
M 7,34,4
LO 116,18,116
LU 18,116
v_mul_f32 v38, 0xbb2aaaab, v38
M 7,35,5
s_barrier
RA 224,1,2048
HS 24,18
v_mul_f32 v39, 0xbb2aaaab, v39
s_nop 1
LK 40,240,60,0
LK 44,242,60,2
s_sub_u32 s39, 0, s39
RA 240,7,1024
LK 48,244,61,0
s_nop 0
CF 128,40,41
LK 248,246,61,2
RA 244,7,1280
CF 129,42,43
M 8,46,4
LL 17,80
LL 106,81
M 8,47,5
LA 17
ds_read_b64 v[14:15], v5 offset:0
M 9,58,4
WL 8
LC_ 106
XL 77,320
M 9,59,5
WL 6
RA 228,1,3072
v_subrev_u32 v5, s39, v5
Z 0,12,0
LO 117,16,117
LU 16,117
CF 130,44,45
M 0,13,1
s_mul_i32 s53, s50, 25600
HS 24,16
CF 131,46,47
LK 40,248,62,0
CF 132,48,49
LK 44,250,62,2
s_add_u32 s40, %[wlo], s53
RA 248,7,1536
CF 133,50,51
GZ 0
s_addc_u32 s41, %[whi], 0
CF 134,248,249
CF 135,250,251
LK 48,252,63,0
CF 136,40,41
LK 248,254,63,2
s_mov_b32 m0, s38
RA 252,7,1792
CF 137,42,43
GS 1
WL 7
CF 138,44,45
CF 139,46,47
LK 40,240,64,0
CF 140,48,49
LK 44,242,64,2
RA 240,7,2048
CF 141,50,51
GS 2
CF 142,248,249
CF 143,250,251
Z 1,24,0
LL 18,82
LL 107,83
M 1,25,1
LA 18
RA 232,1,4096
Z 2,36,0
LC_ 107
HX 24
M 2,37,1
WL 10
W1 24,0
LK 48,244,65,0
CF 144,40,41
LK 248,246,65,2
RA 244,7,2304
CF 145,42,43
GS 3
CF 146,44,45
CF 147,46,47
Z 3,48,0
WL 8
LO 17,17,106
LU 106,17
M 3,49,1
RA 236,1,6144
HZ 25,106
M 0,14,2
LL 16,84
LL 110,85
M 0,15,3
WL 8
LA 16
PK 106,17,104,33
LK 40,248,66,0
CF 148,48,49
LK 44,250,66,2
RA 248,7,2560
CF 149,50,51
GS 4
CF 150,248,249
CF 151,250,251
M 1,26,2
LC_ 110
PK 104,17,104,32
M 1,27,3
RA 216,1,7168
v_add_f32 v14, v14, v15
M 2,38,2
WL 7
LO 18,18,107
LU 107,18
v_add_f32_dpp v14, v14, v14 quad_perm:[1,0,3,2] row_mask:0xf bank_mask:0xf
M 2,39,3
DW 0
HS 25,107
LK 48,252,67,0
CF 152,40,41
LK 248,254,67,2
s_add_u32 s42, s44, s60
RA 252,7,2816
CF 153,42,43
GS 5
s_addc_u32 s43, s45, 0
CF 154,44,45
CF 155,46,47
M 3,50,2
WL 5
LL 17,86
LL 111,87
v_add_f32_dpp v14, v14, v14 quad_perm:[2,3,0,1] row_mask:0xf bank_mask:0xf
M 3,51,3
LA 17
PK 107,18,105,33
M 0,16,4
LC_ 111
PK 105,18,105,32
v_add_f32_dpp v14, v14, v14 row_half_mirror row_mask:0xf bank_mask:0xf
M 0,17,5
RA 220,1,8192
v_cvt_f64_f32 v[14:15], v14
M 1,28,4
WL 5
LO 16,16,110
LU 110,16
M 1,29,5
RA 224,1,9216
DW 1024
HS 25,110
LK 40,240,68,0
CF 156,48,49
LK 44,242,68,2
CF 157,50,51
GS 6
CF 158,248,249
CF 159,250,251
M 2,40,4
LL 18,88
LL 114,89
s_mov_b64 exec, s[54:55]
SP
s_mov_b64 exec, -1
M 2,41,5
LA 18
PK 110,16,108,33
M 3,52,4
WL 2
LC_ 114
PK 108,16,108,32
M 3,53,5
RA 228,1,10240
PO 110
LK 48,244,69,0
CF 160,40,41
LK 248,246,69,2
CF 161,42,43
GS 7
CF 162,44,45
CF 163,46,47
M 0,18,0
LO 17,17,111
LU 111,17
ST 104
DW 2048
M 0,19,1
s_add_u32 s40, s40, 3072
)"
R"(HS 25,111
M 1,30,0
s_addc_u32 s41, s41, 0
LL 16,90
LL 115,91
M 1,31,1
s_add_u32 m0, s38, 3072
LA 16
PK 111,17,109,33
LK 40,248,70,0
CF 164,48,49
LK 44,250,70,2
s_add_u32 s42, s44, s61
CF 165,50,51
GS 8
s_addc_u32 s43, s45, 0
CF 166,248,249
CF 167,250,251
M 2,42,0
LC_ 115
PK 109,17,109,32
M 2,43,1
RA 232,1,11264
PO 111
M 3,54,0
WL 3
LO 18,18,114
LU 114,18
M 3,55,1
RV 252,22,5392
HS 25,114
LK 48,252,71,0
CF 168,40,41
LK 248,254,71,2
CF 169,42,43
GS 9
CF 170,44,45
CF 171,46,47
M 0,20,2
LL 17,92
LL 118,93
M 0,21,3
LA 17
PK 114,18,112,33
M 1,32,2
LC_ 118
PK 112,18,112,32
M 1,33,3
RA 236,1,12288
RV 36,8,6912
GS 10
RA 216,1,13312
M 2,44,2
WL 7
DW 0
LO 16,16,115
LU 115,16
M 2,45,3
ST 108
HS 25,115
M 3,56,2
WL 2
LL 18,94
LL 119,95
M 3,57,3
LA 18
PK 115,16,113,33
M 0,22,4
LC_ 119
PK 113,16,113,32
M 0,23,5
RA 220,1,14336
DW 1024
M 1,34,4
LO 17,17,118
LU 118,17
M 1,35,5
s_add_u32 s42, s44, s62
RA 224,1,15360
HS 25,118
s_addc_u32 s43, s45, 0
M 2,46,4
LL 16,56
LL 104,57
M 2,47,5
LA 16
PK 118,17,116,33
M 3,58,4
WL 2
LC_ 104
PK 116,17,116,32
M 3,59,5
RA 228,1,16384
DW 2048
PO 118
CF 172,48,49
Z 6,12,0
LO 18,18,119
LU 119,18
ST 112
CF 173,50,51
M 6,13,1
DQ 3072
HS 25,119
CF 174,248,249
CF 175,250,251
Z 7,24,0
s_add_u32 s42, s44, s63
LL 17,58
LL 105,59
M 7,25,1
s_addc_u32 s43, s45, 0
LA 17
PK 119,18,117,33
GS 11
RA 232,1,17408
HX 25
s_nop 5
v_cndmask_b32 v52, v52, v53, s[78:79]
v_cndmask_b32 v54, v54, v55, s[78:79]
Z 8,36,0
s_add_u32 s50, s50, %[d]
LC_ 105
PK 117,18,117,32
M 8,37,1
s_cmp_eq_u32 s50, %[wrap]
W1 25,1024
PO 119
v_cndmask_b32 v52, v52, v54, s[80:81]
Z 9,48,0
WL 4
LO 104,16,104
LU 16,104
v_mul_f32 v52, 0x3b2aaaab, v52
M 9,49,1
s_cselect_b32 s50, %[rst], s50
RA 236,1,18432
HZ 24,16
v_med3_f32 v53, v52, v39, v37
M 6,14,2
LL 18,60
LL 108,61
LG 52,53
M 6,15,3
LA 18
ST 116
C 52,52,52
M 7,26,2
s_add_u32 s37, s37, 32768
LC_ 108
v_add_u32 v4, s39, v4
v_lshlrev_b32 v52, 16, v52
M 7,27,3
s_cmp_eq_u32 s37, s75
RA 216,1,19456
v_add_f32 v52, v36, v52
M 8,38,2
WL 5
LO 105,17,105
LU 17,105
C 52,52,52
M 8,39,3
s_cselect_b32 s37, s74, s37
HS 24,17
v_lshlrev_b32 v52, 16, v52
M 9,50,2
WL 3
LL 16,62
LL 109,63
v_sqrt_f32 v52, v52
M 9,51,3
s_add_u32 s38, s38, 32768
LA 16
RA 220,1,20480
C 52,52,52
M 6,16,4
s_cmp_eq_u32 s38, s77
LC_ 109
s_cselect_b32 s38, s76, s38
v_lshlrev_b32 v52, 16, v52
M 6,17,5
XL 78,384
v_rcp_f32 v52, v52
M 7,28,4
WL 4
LO 108,18,108
LU 18,108
v_mul_f32 v52, v38, v52
M 7,29,5
s_mul_i32 s53, s51, 49152
RA 224,1,21504
HS 24,18
CF 52,52,52
M 8,40,4
s_bitcmp1_b64 s[90:91], s51
LL 17,64
LL 112,65
M 8,41,5
s_cselect_b64 s[44:45], s[84:85], s[82:83]
LA 17
ds_bpermute_b32 v13, v28, v52
M 9,52,4
WL 4
LC_ 112
M 9,53,5
s_add_u32 s44, s44, s53
RA 228,1,22528
M 6,18,0
WL 5
LO 109,16,109
LU 16,109
M 6,19,1
s_addc_u32 s45, s45, 0
HS 24,16
s_add_u32 s51, s51, %[d]
s_cmp_eq_u32 s51, %[wrap]
s_cselect_b32 s51, %[rst], s51
M 7,30,0
LL 18,66
LL 113,67
M 7,31,1
WV 18
LA 18
RA 232,1,23552
XL 79,448
M 8,42,0
s_lshl_b32 s53, s52, 8
LC_ 113
M 8,43,1
s_add_u32 s48, %[alo], s53
BW 72
M 9,54,0
WL 4
LO 112,17,112
LU 17,112
M 9,55,1
s_addc_u32 s49, %[ahi], 0
RA 236,1,24576
RV 40,8,6144
HS 24,17
XS 128
M 6,20,2
s_add_u32 s52, s52, %[d]
LL 16,68
LL 116,69
s_cmp_eq_u32 s52, %[wrap]
s_cselect_b32 s52, %[rst], s52
RV 44,8,6208
XS 129
M 6,21,3
LA 16
XL 80,512
XS 130
M 7,32,2
LC_ 116
XL 81,576
RV 48,8,6272
XS 131
M 7,33,3
BR 72
v_add_u32 v1, s37, v0
AW 120,128
M 8,44,2
WL 8
LO 113,18,113
LU 18,113
AW 121,129
M 8,45,3
WV 20
RA 216,1,0
HS 24,18
AW 122,130
BW 73
M 9,56,2
WL 8
BR 73
LL 17,70
LL 117,71
AW 123,131
M 9,57,3
WL 3
LA 17
XL 82,640
XS 132
XS 133
M 6,22,4
s_barrier
LC_ 117
v_and_b32 v22, 0xfffffcff, v1
XS 134
M 6,23,5
s_sub_u32 s39, 0, s39
RA 220,1,1024
XS 135
LZ 40,72
WV 20
BW 74
M 7,34,4
LO 116,16,116
LU 16,116
AW 124,132
M 7,35,5
RV 252,22,5120
RV 40,8,6336
HS 24,16
AW 125,133
M 8,46,4
BR 74
LL 18,80
LL 106,81
AW 126,134
M 8,47,5
WL 7
LA 18
RA 224,1,2048
AW 127,135
LS 44,73
WV 19
BW 75
M 9,58,4
WL 7
LC_ 106
ds_read_b64 v[14:15], v5 offset:0
v_min_u32 v35, v35, v13
M 9,59,5
WL 7
RA 228,1,3072
RV 44,8,6400
XS 136
Z 0,12,0
WV 18
LO 117,17,117
LU 17,117
XS 137
M 0,13,1
WL 7
XL 83,704
HS 24,17
XS 138
Z 1,24,0
s_mul_i32 s53, s50, 25600
BR 75
LL 16,82
LL 107,83
XS 139
M 1,25,1
s_add_u32 s40, %[wlo], s53
LA 16
RA 232,1,4096
AW 128,136
LS 48,74
s_addc_u32 s41, %[whi], 0
BW 76
Z 2,36,0
s_mov_b32 m0, s38
LC_ 107
RV 48,8,6464
AW 129,137
M 2,37,1
BR 76
v_subrev_u32 v5, s39, v5
AW 130,138
Z 3,48,0
WL 8
LO 18,18,106
LU 106,18
AW 131,139
M 3,49,1
RA 236,1,6144
HZ 25,106
XS 140
M 0,14,2
LL 17,84
LL 110,85
XS 141
M 0,15,3
WL 7
LA 17
PK 106,18,104,33
HX 24
XS 142
LS 40,75
WV 18
BW 77
M 1,26,2
LC_ 110
PK 104,18,104,32
W1 24,0
XS 143
M 1,27,3
RA 216,1,7168
RV 40,8,6528
AW 132,140
M 2,38,2
WL 9
DW 0
LO 16,16,107
LU 107,16
AW 133,141
M 2,39,3
)"
R"(WV 6
DW 1024
HS 25,107
AW 134,142
M 3,50,2
WL 6
BR 77
LL 18,86
LL 111,87
AW 135,143
M 3,51,3
s_add_u32 s42, s44, s60
LA 18
PK 107,16,105,33
v_add_f32 v14, v14, v15
s_nop 1
v_add_f32_dpp v14, v14, v14 quad_perm:[1,0,3,2] row_mask:0xf bank_mask:0xf
s_nop 1
v_add_f32_dpp v14, v14, v14 quad_perm:[2,3,0,1] row_mask:0xf bank_mask:0xf
XS 144
LS 44,76
s_addc_u32 s43, s45, 0
BW 78
M 0,16,4
LC_ 111
PK 105,16,105,32
v_add_f32_dpp v14, v14, v14 row_half_mirror row_mask:0xf bank_mask:0xf
XS 145
M 0,17,5
RA 220,1,8192
RV 44,8,6592
XS 146
M 1,28,4
WL 9
LO 17,17,110
LU 110,17
v_cvt_f64_f32 v[14:15], v14
XS 147
M 1,29,5
RA 224,1,9216
HS 25,110
AW 136,144
M 2,40,4
LL 16,88
LL 114,89
s_mov_b64 exec, s[54:55]
SP
s_mov_b64 exec, -1
DW 2048
BR 78
AW 137,145
M 2,41,5
WL 7
LA 16
PK 110,17,108,33
AW 138,146
LS 48,77
WV 8
BW 79
M 3,52,4
WL 5
LC_ 114
PK 108,17,108,32
AW 139,147
M 3,53,5
RA 228,1,10240
PO 110
XS 148
M 0,18,0
LO 18,18,111
LU 111,18
ST 104
RV 48,8,6656
XS 149
M 0,19,1
s_add_u32 s40, s40, 3072
BR 79
HS 25,111
s_addc_u32 s41, s41, 0
s_add_u32 m0, s38, 3072
XS 150
M 1,30,0
LL 17,90
LL 115,91
XS 151
M 1,31,1
WL 6
LA 17
PK 111,18,109,33
AW 140,148
LS 40,78
WV 8
BW 80
M 2,42,0
LC_ 115
PK 109,18,109,32
AW 141,149
M 2,43,1
RA 232,1,11264
DW 0
PO 111
AW 142,150
M 3,54,0
WL 6
RV 40,8,6720
LO 16,16,114
LU 114,16
AW 143,151
M 3,55,1
s_add_u32 s42, s44, s61
RV 252,22,5392
HS 25,114
XS 152
M 0,20,2
s_addc_u32 s43, s45, 0
BR 80
LL 18,92
LL 118,93
XS 153
M 0,21,3
WL 7
LA 18
DW 1024
PK 114,16,112,33
XS 154
LS 44,79
WV 9
BW 81
M 1,32,2
LC_ 118
PK 112,16,112,32
XS 155
M 1,33,3
RA 236,1,12288
RV 44,8,6784
AW 144,152
M 2,44,2
WL 9
LO 17,17,115
LU 115,17
AW 145,153
M 2,45,3
RA 216,1,13312
HS 25,115
ST 108
BR 81
AW 146,154
M 3,56,2
WL 3
DW 2048
LL 16,94
LL 119,95
AW 147,155
M 3,57,3
LA 16
PK 115,17,113,33
XS 156
LS 48,80
WV 10
BW 82
M 0,22,4
LC_ 119
PK 113,17,113,32
XS 157
M 0,23,5
WV 9
RA 220,1,14336
RV 48,8,6848
XS 158
M 1,34,4
WL 5
DQ 3072
LO 18,18,118
LU 118,18
XS 159
M 1,35,5
s_add_u32 s42, s44, s62
RA 224,1,15360
HS 25,118
s_addc_u32 s43, s45, 0
RA 240,7,0
AW 148,156
M 2,46,4
s_add_u32 s50, s50, %[d]
LL 17,56
LL 104,57
AW 149,157
M 2,47,5
s_cmp_eq_u32 s50, %[wrap]
LA 17
BR 82
PK 118,18,116,33
AW 150,158
LS 40,81
s_cselect_b32 s50, %[rst], s50
BW 83
M 3,58,4
WL 6
LC_ 104
PK 116,18,116,32
AW 151,159
M 3,59,5
RA 228,1,16384
PO 118
XS 160
Z 6,12,0
WL 3
LO 16,16,119
LU 119,16
ST 112
XS 161
M 6,13,1
s_add_u32 s38, s38, 32768
RA 244,7,256
HS 25,119
XS 162
Z 7,24,0
s_cmp_eq_u32 s38, s77
LL 18,58
LL 105,59
s_cselect_b32 s38, s76, s38
XS 163
M 7,25,1
s_mul_i32 s53, s50, 25600
LA 18
BR 83
PK 119,16,117,33
AW 152,160
LS 44,82
s_add_u32 s40, %[wlo], s53
RA 248,7,512
AW 153,161
Z 8,36,0
s_addc_u32 s41, %[whi], 0
LC_ 105
PK 117,16,117,32
M 8,37,1
s_mov_b32 m0, s38
RA 232,1,17408
PO 119
AW 154,162
Z 9,48,0
WL 3
LO 104,17,104
LU 17,104
AW 155,163
M 9,49,1
s_add_u32 s42, s44, s63
RA 236,1,18432
HZ 24,17
XS 164
LS 48,83
s_addc_u32 s43, s45, 0
RA 252,7,768
XS 165
M 6,14,2
LL 16,60
LL 108,61
M 6,15,3
LA 16
DW 0
HX 25
XS 166
M 7,26,2
LC_ 108
W1 25,1024
XS 167
M 7,27,3
RA 216,1,19456
v_add_u32 v4, s39, v4
AW 156,164
M 8,38,2
WL 7
LO 105,18,105
LU 18,105
AW 157,165
M 8,39,3
ST 116
HS 24,18
AW 158,166
M 9,50,2
WL 5
LL 17,62
LL 109,63
AW 159,167
M 9,51,3
LA 17
RA 220,1,20480
DW 1024
XS 168
XS 169
M 6,16,4
s_add_u32 s37, s37, 32768
LC_ 109
s_cmp_eq_u32 s37, s75
s_cselect_b32 s37, s74, s37
XS 170
M 6,17,5
DW 2048
XS 171
M 7,28,4
WL 5
LO 108,16,108
LU 16,108
AW 160,168
M 7,29,5
s_add_u32 s40, s40, 3072
RA 224,1,21504
HS 24,16
AW 161,169
M 8,40,4
s_addc_u32 s41, s41, 0
LL 18,64
LL 112,65
AW 162,170
M 8,41,5
s_add_u32 m0, s38, 3072
LA 18
AW 163,171
M 9,52,4
WL 3
LC_ 112
XS 172
M 9,53,5
s_mul_i32 s53, s51, 49152
RA 228,1,22528
XS 173
M 6,18,0
WL 4
LO 109,17,109
LU 17,109
XS 174
M 6,19,1
s_bitcmp1_b64 s[90:91], s51
HS 24,17
s_cselect_b64 s[44:45], s[84:85], s[82:83]
s_add_u32 s44, s44, s53
s_addc_u32 s45, s45, 0
s_add_u32 s51, s51, %[d]
s_cmp_eq_u32 s51, %[wrap]
s_cselect_b32 s51, %[rst], s51
XS 175
M 7,30,0
LL 16,66
LL 113,67
AW 164,172
M 7,31,1
LA 16
RA 232,1,23552
AW 165,173
M 8,42,0
LC_ 113
DW 0
AW 166,174
M 8,43,1
s_lshl_b32 s53, s52, 8
AW 167,175
v_mul_f32 v36, 0xbb2aaaab, v36
v_mul_f32 v37, 0xbb2aaaab, v37
v_mul_f32 v38, 0xbb2aaaab, v38
v_mul_f32 v39, 0xbb2aaaab, v39
s_nop 1
LK 40,240,72,0
LK 44,242,72,2
s_add_u32 s48, %[alo], s53
RA 240,7,1024
LK 48,244,73,0
s_nop 0
CF 128,40,41
LK 248,246,73,2
s_addc_u32 s49, %[ahi], 0
RA 244,7,1280
CF 129,42,43
CF 130,44,45
CF 131,46,47
LK 40,248,74,0
CF 132,48,49
LK 44,250,74,2
s_add_u32 s52, s52, %[d]
RA 248,7,1536
CF 133,50,51
GZ 0
s_cmp_eq_u32 s52, %[wrap]
CF 134,248,249
CF 135,250,251
LK 48,252,75,0
CF 136,40,41
LK 248,254,75,2
s_cselect_b32 s52, %[rst], s52
RA 252,7,1792
CF 137,42,43
GS 1
CF 138,44,45
CF 139,46,47
M 9,54,0
WL 7
LO 112,18,112
LU 18,112
M 9,55,1
WL 3
)"
R"(RA 236,1,24576
HS 24,18
LK 40,240,76,0
CF 140,48,49
LK 44,242,76,2
RA 240,7,2048
CF 141,50,51
GS 2
WL 4
CF 142,248,249
CF 143,250,251
LK 48,244,77,0
CF 144,40,41
LK 248,246,77,2
RA 244,7,2304
CF 145,42,43
GS 3
WL 4
CF 146,44,45
CF 147,46,47
LK 40,248,78,0
CF 148,48,49
LK 44,250,78,2
RA 248,7,2560
CF 149,50,51
GS 4
CF 150,248,249
CF 151,250,251
M 6,20,2
DW 1024
LL 17,68
LL 116,69
M 6,21,3
LA 17
DW 2048
M 7,32,2
LC_ 116
v_add_u32 v1, s37, v0
M 7,33,3
RA 216,1,0
v_and_b32 v22, 0xfffffcff, v1
M 8,44,2
LO 113,16,113
LU 16,113
M 8,45,3
WL 7
DQ 3072
HS 24,16
LK 48,252,79,0
CF 152,40,41
LK 248,254,79,2
RA 252,7,2816
CF 153,42,43
GS 5
CF 154,44,45
CF 155,46,47
M 9,56,2
WL 7
LL 18,70
LL 117,71
M 9,57,3
s_add_u32 s50, s50, %[d]
LA 18
RA 220,1,1024
M 6,22,4
s_cmp_eq_u32 s50, %[wrap]
LC_ 117
RV 252,22,5120
M 6,23,5
s_cselect_b32 s50, %[rst], s50
M 7,34,4
WL 6
LO 116,17,116
LU 17,116
M 7,35,5
s_barrier
RA 224,1,2048
HS 24,17
LK 40,240,80,0
CF 156,48,49
LK 44,242,80,2
s_sub_u32 s39, 0, s39
CF 157,50,51
GS 6
s_add_u32 s38, s38, 32768
CF 158,248,249
CF 159,250,251
M 8,46,4
s_cmp_eq_u32 s38, s77
LL 16,80
LL 106,81
M 8,47,5
s_cselect_b32 s38, s76, s38
LA 16
ds_read_b64 v[14:15], v5 offset:0
M 9,58,4
WL 5
LC_ 106
v_subrev_u32 v5, s39, v5
M 9,59,5
WL 4
RA 228,1,3072
Z 0,12,0
LO 117,18,117
LU 18,117
M 0,13,1
s_mul_i32 s53, s50, 25600
HS 24,18
LK 48,244,81,0
CF 160,40,41
LK 248,246,81,2
s_add_u32 s40, %[wlo], s53
CF 161,42,43
GS 7
s_addc_u32 s41, %[whi], 0
CF 162,44,45
CF 163,46,47
Z 1,24,0
s_mov_b32 m0, s38
LL 17,82
LL 107,83
M 1,25,1
LA 17
RA 232,1,4096
Z 2,36,0
LC_ 107
HX 24
M 2,37,1
W1 24,0
LK 40,248,82,0
CF 164,48,49
LK 44,250,82,2
CF 165,50,51
GS 8
CF 166,248,249
CF 167,250,251
Z 3,48,0
WL 4
LO 16,16,106
LU 106,16
M 3,49,1
RA 236,1,6144
HZ 25,106
M 0,14,2
LL 18,84
LL 110,85
M 0,15,3
LA 18
PK 106,16,104,33
M 1,26,2
LC_ 110
PK 104,16,104,32
M 1,27,3
RA 216,1,7168
v_add_f32 v14, v14, v15
LK 48,252,83,0
CF 168,40,41
LK 248,254,83,2
CF 169,42,43
v_add_f32_dpp v14, v14, v14 quad_perm:[1,0,3,2] row_mask:0xf bank_mask:0xf
GS 9
CF 170,44,45
CF 171,46,47
M 2,38,2
WL 5
LO 17,17,107
LU 107,17
M 2,39,3
DW 0
HS 25,107
M 3,50,2
WL 3
LL 16,86
LL 111,87
v_add_f32_dpp v14, v14, v14 quad_perm:[2,3,0,1] row_mask:0xf bank_mask:0xf
M 3,51,3
s_add_u32 s42, s44, s60
LA 16
PK 107,17,105,33
M 0,16,4
s_addc_u32 s43, s45, 0
LC_ 111
PK 105,17,105,32
v_add_f32_dpp v14, v14, v14 row_half_mirror row_mask:0xf bank_mask:0xf
M 0,17,5
RA 220,1,8192
RV 36,8,6912
GS 10
CF 172,48,49
CF 173,50,51
M 1,28,4
WL 5
LO 18,18,110
LU 110,18
v_cvt_f64_f32 v[14:15], v14
M 1,29,5
RA 224,1,9216
HS 25,110
M 2,40,4
LL 17,88
LL 114,89
s_mov_b64 exec, s[54:55]
SP
s_mov_b64 exec, -1
M 2,41,5
LA 17
PK 110,18,108,33
M 3,52,4
WL 3
LC_ 114
PK 108,18,108,32
M 3,53,5
RA 228,1,10240
PO 110
M 0,18,0
LO 16,16,111
LU 111,16
ST 104
M 0,19,1
DW 1024
HS 25,111
M 1,30,0
s_add_u32 s42, s44, s61
LL 18,90
LL 115,91
M 1,31,1
s_addc_u32 s43, s45, 0
LA 18
PK 111,16,109,33
M 2,42,0
LC_ 115
PK 109,16,109,32
M 2,43,1
RA 232,1,11264
PO 111
CF 174,248,249
M 3,54,0
WL 3
LO 17,17,114
LU 114,17
CF 175,250,251
M 3,55,1
RV 252,22,5392
HS 25,114
GS 11
RA 236,1,12288
s_nop 6
v_cndmask_b32 v52, v52, v53, s[78:79]
M 0,20,2
LL 16,92
LL 118,93
M 0,21,3
LA 16
PK 114,17,112,33
v_cndmask_b32 v54, v54, v55, s[78:79]
M 1,32,2
LC_ 118
PK 112,17,112,32
v_cndmask_b32 v52, v52, v54, s[80:81]
M 1,33,3
RA 216,1,13312
DW 2048
M 2,44,2
WL 6
LO 18,18,115
LU 115,18
v_mul_f32 v52, 0x3b2aaaab, v52
M 2,45,3
ST 108
HS 25,115
v_med3_f32 v53, v52, v39, v37
M 3,56,2
WL 3
LL 17,94
LL 119,95
M 3,57,3
s_add_u32 s42, s44, s62
LA 17
PK 115,18,113,33
LG 52,53
M 0,22,4
s_addc_u32 s43, s45, 0
LC_ 119
PK 113,18,113,32
C 52,52,52
M 0,23,5
s_add_u32 s40, s40, 3072
RA 220,1,14336
v_lshlrev_b32 v52, 16, v52
M 1,34,4
WL 4
LO 16,16,118
LU 118,16
M 1,35,5
s_addc_u32 s41, s41, 0
RA 224,1,15360
HS 25,118
v_add_f32 v52, v36, v52
M 2,46,4
s_add_u32 m0, s38, 3072
LL 18,56
LL 104,57
M 2,47,5
LA 18
PK 118,16,116,33
C 52,52,52
M 3,58,4
WL 2
LC_ 104
PK 116,16,116,32
v_lshlrev_b32 v52, 16, v52
M 3,59,5
RA 228,1,16384
PO 118
Z 6,12,0
LO 17,17,119
LU 119,17
ST 112
v_sqrt_f32 v52, v52
M 6,13,1
DW 0
HS 25,119
C 52,52,52
Z 7,24,0
s_add_u32 s42, s44, s63
LL 16,58
LL 105,59
M 7,25,1
s_addc_u32 s43, s45, 0
LA 16
PK 119,17,117,33
v_lshlrev_b32 v52, 16, v52
Z 8,36,0
LC_ 105
PK 117,17,117,32
v_rcp_f32 v52, v52
M 8,37,1
RA 232,1,17408
PO 119
Z 9,48,0
WL 3
LO 104,18,104
LU 18,104
v_mul_f32 v52, v38, v52
M 9,49,1
RA 236,1,18432
HZ 24,18
CF 52,52,52
M 6,14,2
LL 17,60
LL 108,61
M 6,15,3
LA 17
ST 116
HX 25
ds_bpermute_b32 v13, v28, v52
M 7,26,2
LC_ 108
W1 25,1024
M 7,27,3
RA 216,1,19456
v_add_u32 v4, s39, v4
M 8,38,2
WL 7
LO 105,16,105
LU 16,105
M 8,39,3
DW 1024
HS 24,16
M 9,50,2
WL 5
LL 18,62
LL 109,63
M 9,51,3
s_add_u32 s37, s37, 32768
LA 18
RA 220,1,20480
M 6,16,4
s_cmp_eq_u32 s37, s75
LC_ 109
s_cselect_b32 s37, s74, s37
M 6,17,5
DW 2048
M 7,28,4
WL 5
LO 108,17,108
LU 17,108
M 7,29,5
s_mul_i32 s53, s51, 49152
RA 224,1,21504
HS 24,17
XS 128
M 8,40,4
s_bitcmp1_b64 s[90:91], s51
LL 16,64
LL 112,65
XS 129
M 8,41,5
s_cselect_b64 s[44:45], s[84:85], s[82:83]
LA 16
)"
R"(XS 130
M 9,52,4
WL 3
LC_ 112
XS 131
M 9,53,5
s_add_u32 s44, s44, s53
RA 228,1,22528
AW 168,128
M 6,18,0
WL 4
LO 109,18,109
LU 18,109
AW 169,129
M 6,19,1
s_addc_u32 s45, s45, 0
HS 24,18
s_add_u32 s51, s51, %[d]
s_cmp_eq_u32 s51, %[wrap]
s_cselect_b32 s51, %[rst], s51
AW 170,130
M 7,30,0
s_lshl_b32 s53, s52, 8
LL 17,66
LL 113,67
AW 171,131
M 7,31,1
s_add_u32 s48, %[alo], s53
LA 17
RA 232,1,23552
XS 132
M 8,42,0
s_addc_u32 s49, %[ahi], 0
LC_ 113
XS 133
M 8,43,1
s_add_u32 s52, s52, %[d]
XS 134
XS 135
M 9,54,0
WL 3
LO 112,16,112
LU 16,112
M 9,55,1
s_cmp_eq_u32 s52, %[wrap]
RA 236,1,24576
HS 24,16
AW 172,132
M 6,20,2
s_cselect_b32 s52, %[rst], s52
DQ 3072
LL 18,68
LL 116,69
AW 173,133
M 6,21,3
LA 18
v_add_u32 v1, s37, v0
AW 174,134
M 7,32,2
s_add_u32 s50, s50, %[d]
LC_ 116
AW 175,135
M 7,33,3
s_cmp_eq_u32 s50, %[wrap]
RA 216,1,0
v_and_b32 v22, 0xfffffcff, v1
v_min_u32 v35, v35, v13
M 8,44,2
WL 4
LO 113,17,113
LU 17,113
XS 136
M 8,45,3
s_cselect_b32 s50, %[rst], s50
HS 24,17
XS 137
M 9,56,2
WL 3
LL 16,70
LL 117,71
XS 138
M 9,57,3
WV 21
LA 16
RA 220,1,1024
XS 139
M 6,22,4
s_barrier
LC_ 117
RV 252,22,5120
AW 176,136
M 6,23,5
s_sub_u32 s39, 0, s39
AW 177,137
AW 178,138
M 7,34,4
WL 5
LO 116,18,116
LU 18,116
M 7,35,5
s_add_u32 s38, s38, 32768
RA 224,1,2048
HS 24,18
AW 179,139
M 8,46,4
s_cmp_eq_u32 s38, s77
LL 17,80
LL 106,81
XS 140
M 8,47,5
s_cselect_b32 s38, s76, s38
LA 17
XS 141
M 9,58,4
WL 4
LC_ 106
XS 142
M 9,59,5
WL 3
RA 228,1,3072
XS 143
Z 0,12,0
LO 117,16,117
LU 16,117
M 0,13,1
HS 24,16
AW 180,140
Z 1,24,0
LL 18,82
LL 107,83
AW 181,141
M 1,25,1
LA 18
RA 232,1,4096
AW 182,142
Z 2,36,0
LC_ 107
HX 24
AW 183,143
M 2,37,1
W1 24,0
XS 144
Z 3,48,0
WL 5
LO 17,17,106
LU 106,17
XS 145
M 3,49,1
HZ 25,106
XS 146
Z 4,60,0
LL 16,84
LL 110,85
XS 147
M 4,61,1
LA 16
PK 106,17,104,33
AW 184,144
Z 5,72,0
WL 5
LC_ 110
PK 104,17,104,32
AW 185,145
M 5,73,1
RA 236,1,6144
AW 186,146
M 0,14,2
WL 4
LO 18,18,107
LU 107,18
AW 187,147
M 0,15,3
s_add_u32 s42, s44, s60
HS 25,107
XS 148
M 1,26,2
s_addc_u32 s43, s45, 0
LL 17,86
LL 111,87
XS 149
M 1,27,3
LA 17
PK 107,18,105,33
XS 150
M 2,38,2
LC_ 111
PK 105,18,105,32
XS 151
M 2,39,3
RA 216,1,7168
AW 188,148
M 3,50,2
WL 4
LO 16,16,110
LU 110,16
AW 189,149
M 3,51,3
ST 104
HS 25,110
AW 190,150
M 4,62,2
LL 18,88
LL 114,89
AW 191,151
M 4,63,3
LA 18
PK 110,16,108,33
XS 152
M 5,74,2
WL 4
LC_ 114
PK 108,16,108,32
XS 153
M 5,75,3
s_mov_b32 m0, %[pm]
RA 220,1,8192
PO 110
XS 154
M 0,16,4
WL 4
LO 17,17,111
LU 111,17
XS 155
M 0,17,5
DF
HS 25,111
AW 192,152
M 1,28,4
s_add_u32 s42, s44, s61
LL 16,90
LL 115,91
AW 193,153
M 1,29,5
s_addc_u32 s43, s45, 0
LA 16
PK 111,17,109,33
AW 194,154
M 2,40,4
LC_ 115
PK 109,17,109,32
AW 195,155
M 2,41,5
RA 224,1,9216
PO 111
XS 156
M 3,52,4
WL 4
LO 18,18,114
LU 114,18
XS 157
M 3,53,5
ST 108
HS 25,114
XS 158
M 4,64,4
LL 17,92
LL 118,93
XS 159
M 4,65,5
LA 17
PK 114,18,112,33
AW 196,156
M 5,76,4
WL 4
LC_ 118
PK 112,18,112,32
AW 197,157
M 5,77,5
RA 228,1,10240
AW 198,158
M 0,18,0
WL 4
LO 16,16,115
LU 115,16
AW 199,159
M 0,19,1
s_add_u32 s42, s44, s62
HS 25,115
XS 160
M 1,30,0
s_addc_u32 s43, s45, 0
LL 18,94
LL 119,95
XS 161
M 1,31,1
LA 18
PK 115,16,113,33
XS 162
M 2,42,0
LC_ 119
PK 113,16,113,32
XS 163
M 2,43,1
RA 232,1,11264
AW 200,160
M 3,54,0
WL 4
LO 17,17,118
LU 118,17
AW 201,161
M 3,55,1
RV 252,22,5392
HS 25,118
AW 202,162
M 4,66,0
ST 112
AW 203,163
M 4,67,1
RA 236,1,12288
PK 118,17,116,33
XS 164
M 5,78,0
WL 5
PK 116,17,116,32
XS 165
M 5,79,1
PO 118
XS 166
M 0,20,2
WL 3
LO 18,18,119
LU 119,18
XS 167
M 0,21,3
s_add_u32 s42, s44, s63
HS 25,119
AW 204,164
M 1,32,2
s_addc_u32 s43, s45, 0
HX 25
AW 205,165
M 1,33,3
RA 216,1,13312
PK 119,18,117,33
AW 206,166
M 2,44,2
W1 25,1024
PK 117,18,117,32
AW 207,167
M 2,45,3
PO 119
v_add_u32 v4, s39, v4
XS 168
M 3,56,2
ST 116
XS 169
M 3,57,3
XS 170
XS 171
M 4,68,2
AW 208,168
AW 209,169
M 4,69,3
RA 220,1,14336
AW 210,170
M 5,80,2
WL 3
AW 211,171
XS 172
M 5,81,3
XS 173
XS 174
M 0,22,4
XS 175
AW 212,172
M 0,23,5
AW 213,173
AW 214,174
M 1,34,4
AW 215,175
M 1,35,5
RA 224,1,15360
M 2,46,4
s_nop 0
LL 16,56
LL 104,57
M 2,47,5
LA 16
ds_read_b64 v[14:15], v5 offset:0
M 3,58,4
LC_ 104
v_subrev_u32 v5, s39, v5
M 3,59,5
M 4,70,4
M 4,71,5
RA 228,1,16384
M 5,82,4
WL 5
LL 17,58
LL 105,59
M 5,83,5
LA 17
Z 6,12,0
LC_ 105
M 6,13,1
Z 7,24,0
WL 3
LO 104,16,104
LU 16,104
M 7,25,1
)"
R"(RA 232,1,17408
HZ 24,16
Z 8,36,0
LL 18,60
LL 108,61
M 8,37,1
s_mul_i32 s53, s50, 25600
LA 18
v_add_f32 v14, v14, v15
Z 9,48,0
s_add_u32 s40, %[wlo], s53
LC_ 108
v_add_f32_dpp v14, v14, v14 quad_perm:[1,0,3,2] row_mask:0xf bank_mask:0xf
M 9,49,1
s_addc_u32 s41, %[whi], 0
v_add_f32_dpp v14, v14, v14 quad_perm:[2,3,0,1] row_mask:0xf bank_mask:0xf
Z 10,60,0
WL 3
LO 105,17,105
LU 17,105
M 10,61,1
s_mov_b32 m0, s38
RA 236,1,18432
HS 24,17
Z 11,72,0
LL 16,62
LL 109,63
M 11,73,1
LA 16
DW 0
M 6,14,2
LC_ 109
v_add_f32_dpp v14, v14, v14 row_half_mirror row_mask:0xf bank_mask:0xf
M 6,15,3
v_cvt_f64_f32 v[14:15], v14
M 7,26,2
WL 3
LO 108,18,108
LU 18,108
M 7,27,3
s_add_u32 s37, s37, 32768
RA 216,1,19456
HS 24,18
M 8,38,2
s_cmp_eq_u32 s37, s75
LL 17,64
LL 112,65
M 8,39,3
s_cselect_b32 s37, s74, s37
LA 17
s_mov_b64 exec, s[54:55]
SP
s_mov_b64 exec, -1
M 9,50,2
LC_ 112
DW 1024
M 9,51,3
M 10,62,2
WL 3
LO 109,16,109
LU 16,109
M 10,63,3
RA 220,1,20480
HS 24,16
M 11,74,2
LL 18,66
LL 113,67
M 11,75,3
LA 18
DW 2048
M 6,16,4
LC_ 113
M 6,17,5
M 7,28,4
WL 3
LO 112,17,112
LU 17,112
M 7,29,5
s_mul_i32 s53, s51, 49152
RA 224,1,21504
HS 24,17
M 8,40,4
s_bitcmp1_b64 s[90:91], s51
LL 16,68
LL 116,69
M 8,41,5
s_cselect_b64 s[44:45], s[84:85], s[82:83]
LA 16
M 9,52,4
s_add_u32 s44, s44, s53
LC_ 116
M 9,53,5
s_addc_u32 s45, s45, 0
M 10,64,4
WL 3
LO 113,18,113
LU 18,113
M 10,65,5
s_add_u32 s51, s51, %[d]
RA 228,1,22528
HS 24,18
M 11,76,4
LL 17,70
LL 117,71
M 11,77,5
s_cmp_eq_u32 s51, %[wrap]
LA 17
M 6,18,0
s_cselect_b32 s51, %[rst], s51
LC_ 117
M 6,19,1
s_add_u32 s40, s40, 3072
M 7,30,0
WL 3
LO 116,16,116
LU 16,116
M 7,31,1
s_addc_u32 s41, s41, 0
RA 232,1,23552
HS 24,16
M 8,42,0
s_add_u32 m0, s38, 3072
LL 18,72
LL 120,73
M 8,43,1
LA 18
DW 0
M 9,54,0
LC_ 120
DW 1024
M 9,55,1
M 10,66,0
WL 3
LO 117,17,117
LU 17,117
M 10,67,1
RA 236,1,24576
HS 24,17
M 11,78,0
LL 16,74
LL 121,75
M 11,79,1
LA 16
DW 2048
M 6,20,2
LC_ 121
M 6,21,3
s_lshl_b32 s53, s52, 8
v_add_u32 v1, s37, v0
M 7,32,2
WL 3
LO 120,18,120
LU 18,120
M 7,33,3
s_add_u32 s48, %[alo], s53
RA 216,1,0
HS 24,18
M 8,44,2
s_addc_u32 s49, %[ahi], 0
LL 17,76
LL 124,77
M 8,45,3
s_add_u32 s52, s52, %[d]
LA 17
v_and_b32 v22, 0xfffffcff, v1
M 9,56,2
s_cmp_eq_u32 s52, %[wrap]
LC_ 124
M 9,57,3
s_cselect_b32 s52, %[rst], s52
DQ 3072
M 10,68,2
WL 3
LO 121,16,121
LU 16,121
M 10,69,3
s_add_u32 s50, s50, %[d]
RA 220,1,1024
HS 24,16
M 11,80,2
LL 18,78
LL 125,79
M 11,81,3
s_cmp_eq_u32 s50, %[wrap]
LA 18
RV 252,22,5120
M 6,22,4
s_cselect_b32 s50, %[rst], s50
LC_ 125
M 6,23,5
WV 25
M 7,34,4
WL 4
LO 124,17,124
LU 17,124
M 7,35,5
s_add_u32 s38, s38, 32768
RA 224,1,2048
HS 24,17
M 8,46,4
s_cmp_eq_u32 s38, s77
LL 16,80
LL 106,81
M 8,47,5
s_cselect_b32 s38, s76, s38
LA 16
M 9,58,4
s_barrier
LC_ 106
M 9,59,5
s_sub_u32 s39, 0, s39
M 10,70,4
WL 3
LO 125,18,125
LU 18,125
M 10,71,5
RA 228,1,3072
HS 24,18
M 11,82,4
LL 17,82
LL 107,83
M 11,83,5
LA 17
HX 24
Z 0,12,0
LC_ 107
W1 24,0
M 0,13,1
Z 1,24,0
WL 4
LO 16,16,106
LU 106,16
M 1,25,1
RA 232,1,4096
HZ 25,106
Z 2,36,0
LL 18,84
LL 110,85
M 2,37,1
LA 18
PK 106,16,104,33
Z 3,48,0
LC_ 110
PK 104,16,104,32
M 3,49,1
Z 4,60,0
WL 4
LO 17,17,107
LU 107,17
M 4,61,1
s_add_u32 s42, s44, s60
RA 236,1,6144
HS 25,107
Z 5,72,0
LL 16,86
LL 111,87
M 5,73,1
s_addc_u32 s43, s45, 0
LA 16
PK 107,17,105,33
M 0,14,2
LC_ 111
PK 105,17,105,32
M 0,15,3
ST 104
M 1,26,2
WL 3
LO 18,18,110
LU 110,18
M 1,27,3
RA 216,1,7168
HS 25,110
M 2,38,2
LL 17,88
LL 114,89
M 2,39,3
LA 17
PK 110,18,108,33
M 3,50,2
LC_ 114
PK 108,18,108,32
M 3,51,3
PO 110
M 4,62,2
WL 3
LO 16,16,111
LU 111,16
M 4,63,3
s_add_u32 s42, s44, s61
RA 220,1,8192
HS 25,111
M 5,74,2
LL 18,90
LL 115,91
M 5,75,3
s_addc_u32 s43, s45, 0
LA 18
PK 111,16,109,33
M 0,16,4
s_mov_b32 m0, %[pm]
LC_ 115
PK 109,16,109,32
M 0,17,5
DF
PO 111
M 1,28,4
WL 3
LO 17,17,114
LU 114,17
M 1,29,5
RA 224,1,9216
HS 25,114
M 2,40,4
LL 16,92
LL 118,93
M 2,41,5
LA 16
PK 114,17,112,33
M 3,52,4
LC_ 118
PK 112,17,112,32
M 3,53,5
ST 108
M 4,64,4
WL 3
LO 18,18,115
LU 115,18
M 4,65,5
s_add_u32 s42, s44, s62
RA 228,1,10240
HS 25,115
M 5,76,4
LL 17,94
LL 119,95
M 5,77,5
s_addc_u32 s43, s45, 0
LA 17
PK 115,18,113,33
M 0,18,0
LC_ 119
PK 113,18,113,32
M 0,19,1
ST 112
M 1,30,0
WL 3
LO 16,16,118
LU 118,16
M 1,31,1
RA 232,1,11264
HS 25,118
M 2,42,0
LL 18,96
LL 122,97
M 2,43,1
LA 18
PK 118,16,116,33
M 3,54,0
LC_ 122
PK 116,16,116,32
M 3,55,1
RV 252,22,5392
PO 118
M 4,66,0
WL 4
LO 17,17,119
LU 119,17
M 4,67,1
s_add_u32 s42, s44, s63
RA 236,1,12288
HS 25,119
M 5,78,0
LL 16,98
LL 123,99
M 5,79,1
s_addc_u32 s43, s45, 0
LA 16
PK 119,17,117,33
M 0,20,2
LC_ 123
PK 117,17,117,32
M 0,21,3
PO 119
M 1,32,2
WL 4
LO 18,18,122
LU 122,18
M 1,33,3
RA 216,1,13312
HS 25,122
M 2,44,2
LL 17,100
LL 126,101
M 2,45,3
LA 17
PK 122,18,120,33
M 3,56,2
LC_ 126
PK 120,18,120,32
M 3,57,3
ST 116
)"
R"(M 4,68,2
WL 3
LO 16,16,123
LU 123,16
M 4,69,3
s_add_u32 s42, s44, s64
RA 220,1,14336
HS 25,123
M 5,80,2
LL 18,102
LL 127,103
M 5,81,3
s_addc_u32 s43, s45, 0
LA 18
PK 123,16,121,33
M 0,22,4
LC_ 127
PK 121,16,121,32
M 0,23,5
ST 120
M 1,34,4
WL 3
LO 17,17,126
LU 126,17
M 1,35,5
RA 224,1,15360
HS 25,126
M 2,46,4
LL 16,56
LL 104,57
M 2,47,5
LA 16
PK 126,17,124,33
M 3,58,4
LC_ 104
PK 124,17,124,32
M 3,59,5
ds_read_b64 v[14:15], v5 offset:0
PO 126
M 4,70,4
WL 4
LO 18,18,127
LU 127,18
M 4,71,5
s_add_u32 s42, s44, s65
RA 228,1,16384
HS 25,127
M 5,82,4
LL 17,58
LL 105,59
M 5,83,5
s_addc_u32 s43, s45, 0
LA 17
PK 127,18,125,33
Z 6,12,0
LC_ 105
PK 125,18,125,32
M 6,13,1
PO 127
v_subrev_u32 v5, s39, v5
Z 7,24,0
WL 4
LO 104,16,104
LU 16,104
M 7,25,1
RA 232,1,17408
HZ 24,16
Z 8,36,0
WL 4
LL 18,60
LL 108,61
M 8,37,1
s_mul_i32 s53, s50, 25600
LA 18
v_add_f32 v14, v14, v15
Z 9,48,0
s_add_u32 s40, %[wlo], s53
LC_ 108
v_add_f32_dpp v14, v14, v14 quad_perm:[1,0,3,2] row_mask:0xf bank_mask:0xf
M 9,49,1
s_addc_u32 s41, %[whi], 0
v_add_f32_dpp v14, v14, v14 quad_perm:[2,3,0,1] row_mask:0xf bank_mask:0xf
HX 25
Z 10,60,0
WL 3
LO 105,17,105
LU 17,105
M 10,61,1
s_mov_b32 m0, s38
RA 236,1,18432
HS 24,17
Z 11,72,0
LL 16,62
LL 109,63
M 11,73,1
LA 16
W1 25,1024
M 6,14,2
LC_ 109
ST 124
M 6,15,3
DW 0
v_add_f32_dpp v14, v14, v14 row_half_mirror row_mask:0xf bank_mask:0xf
M 7,26,2
WL 4
LO 108,18,108
LU 18,108
M 7,27,3
s_add_u32 s37, s37, 32768
RA 216,1,19456
HS 24,18
M 8,38,2
s_cmp_eq_u32 s37, s75
LL 17,64
LL 112,65
M 8,39,3
s_cselect_b32 s37, s74, s37
LA 17
DW 1024
M 9,50,2
LC_ 112
v_cvt_f64_f32 v[14:15], v14
M 9,51,3
s_mov_b64 exec, s[54:55]
SP
s_mov_b64 exec, -1
v_add_u32 v4, s39, v4
M 10,62,2
WL 3
LO 109,16,109
LU 16,109
M 10,63,3
RA 220,1,20480
HS 24,16
M 11,74,2
LL 18,66
LL 113,67
M 11,75,3
LA 18
DW 2048
M 6,16,4
LC_ 113
M 6,17,5
M 7,28,4
WL 3
LO 112,17,112
LU 17,112
M 7,29,5
s_mul_i32 s53, s51, 49152
RA 224,1,21504
HS 24,17
M 8,40,4
s_bitcmp1_b64 s[90:91], s51
LL 16,68
LL 116,69
M 8,41,5
s_cselect_b64 s[44:45], s[84:85], s[82:83]
LA 16
M 9,52,4
s_add_u32 s44, s44, s53
LC_ 116
M 9,53,5
s_addc_u32 s45, s45, 0
M 10,64,4
WL 3
LO 113,18,113
LU 18,113
M 10,65,5
s_add_u32 s51, s51, %[d]
RA 228,1,22528
HS 24,18
M 11,76,4
LL 17,70
LL 117,71
M 11,77,5
s_cmp_eq_u32 s51, %[wrap]
LA 17
M 6,18,0
s_cselect_b32 s51, %[rst], s51
LC_ 117
M 6,19,1
s_add_u32 s40, s40, 3072
M 7,30,0
WL 3
LO 116,16,116
LU 16,116
M 7,31,1
s_addc_u32 s41, s41, 0
RA 232,1,23552
HS 24,16
M 8,42,0
s_add_u32 m0, s38, 3072
LL 18,72
LL 120,73
M 8,43,1
LA 18
DW 0
M 9,54,0
LC_ 120
DW 1024
M 9,55,1
M 10,66,0
WL 3
LO 117,17,117
LU 17,117
M 10,67,1
RA 236,1,24576
HS 24,17
M 11,78,0
LL 16,74
LL 121,75
M 11,79,1
LA 16
DW 2048
M 6,20,2
LC_ 121
M 6,21,3
s_lshl_b32 s53, s52, 8
v_add_u32 v1, s37, v0
M 7,32,2
WL 3
LO 120,18,120
LU 18,120
M 7,33,3
s_add_u32 s48, %[alo], s53
RA 216,1,0
HS 24,18
M 8,44,2
s_addc_u32 s49, %[ahi], 0
LL 17,76
LL 124,77
M 8,45,3
s_add_u32 s52, s52, %[d]
LA 17
v_and_b32 v22, 0xfffffcff, v1
M 9,56,2
s_cmp_eq_u32 s52, %[wrap]
LC_ 124
M 9,57,3
s_cselect_b32 s52, %[rst], s52
DQ 3072
M 10,68,2
WL 3
LO 121,16,121
LU 16,121
M 10,69,3
s_add_u32 s50, s50, %[d]
RA 220,1,1024
HS 24,16
M 11,80,2
LL 18,78
LL 125,79
M 11,81,3
s_cmp_eq_u32 s50, %[wrap]
LA 18
RV 252,22,5120
M 6,22,4
s_cselect_b32 s50, %[rst], s50
LC_ 125
M 6,23,5
WV 28
v_add_u32 v19, 1, v19
M 7,34,4
WL 4
LO 124,17,124
LU 17,124
M 7,35,5
s_add_u32 s38, s38, 32768
RA 224,1,2048
HS 24,17
M 8,46,4
s_cmp_eq_u32 s38, s77
LL 16,80
LL 106,81
M 8,47,5
s_cselect_b32 s38, s76, s38
LA 16
FP
M 9,58,4
s_sub_u32 s39, 0, s39
LC_ 106
M 9,59,5
M 10,70,4
WL 4
LO 125,18,125
LU 18,125
M 10,71,5
RA 228,1,3072
HS 24,18
M 11,82,4
LL 17,82
LL 107,83
M 11,83,5
s_mov_b32 s66, 30
1:
LA 17
HX 24
Z 0,12,0
LC_ 107
M 0,13,1
Z 1,24,0
WL 3
LO 16,16,106
LU 106,16
M 1,25,1
RA 232,1,4096
HZ 25,106
Z 2,36,0
LL 18,84
LL 110,85
M 2,37,1
LA 18
PK 106,16,104,33
Z 3,48,0
LC_ 110
PK 104,16,104,32
M 3,49,1
Z 4,60,0
WL 3
LO 17,17,107
LU 107,17
M 4,61,1
s_add_u32 s42, s44, s60
RA 236,1,6144
HS 25,107
Z 5,72,0
LL 16,86
LL 111,87
M 5,73,1
s_addc_u32 s43, s45, 0
LA 16
PK 107,17,105,33
M 0,14,2
LC_ 111
PK 105,17,105,32
M 0,15,3
ST 104
M 1,26,2
WL 3
LO 18,18,110
LU 110,18
M 1,27,3
RA 216,1,7168
HS 25,110
M 2,38,2
LL 17,88
LL 114,89
M 2,39,3
LA 17
PK 110,18,108,33
M 3,50,2
LC_ 114
PK 108,18,108,32
M 3,51,3
FR
PO 110
M 4,62,2
WL 4
LO 16,16,111
LU 111,16
M 4,63,3
s_add_u32 s42, s44, s61
RA 220,1,8192
HS 25,111
M 5,74,2
LL 18,90
LL 115,91
M 5,75,3
s_addc_u32 s43, s45, 0
LA 18
PK 111,16,109,33
M 0,16,4
s_mov_b32 m0, %[pm]
LC_ 115
PK 109,16,109,32
M 0,17,5
DF
PO 111
M 1,28,4
WL 4
LO 17,17,114
LU 114,17
M 1,29,5
RA 224,1,9216
HS 25,114
M 2,40,4
LL 16,92
LL 118,93
M 2,41,5
LA 16
PK 114,17,112,33
M 3,52,4
LC_ 118
PK 112,17,112,32
M 3,53,5
ST 108
M 4,64,4
WL 3
LO 18,18,115
LU 115,18
)"
R"(M 4,65,5
s_add_u32 s42, s44, s62
RA 228,1,10240
HS 25,115
M 5,76,4
LL 17,94
LL 119,95
M 5,77,5
s_addc_u32 s43, s45, 0
LA 17
PK 115,18,113,33
M 0,18,0
LC_ 119
PK 113,18,113,32
M 0,19,1
ST 112
M 1,30,0
WL 3
LO 16,16,118
LU 118,16
M 1,31,1
RA 232,1,11264
HS 25,118
M 2,42,0
LL 18,96
LL 122,97
M 2,43,1
LA 18
PK 118,16,116,33
v_cmp_gt_u32 vcc, v19, v15
M 3,54,0
LC_ 122
PK 116,16,116,32
M 3,55,1
RV 252,22,5392
PO 118
M 4,66,0
WL 4
LO 17,17,119
LU 119,17
M 4,67,1
s_cbranch_vccnz .Lk1s0
.Lk1r0:
RA 236,1,12288
HS 25,119
M 5,78,0
LL 16,98
LL 123,99
M 5,79,1
s_add_u32 s42, s44, s63
LA 16
PK 119,17,117,33
M 0,20,2
s_addc_u32 s43, s45, 0
LC_ 123
PK 117,17,117,32
M 0,21,3
W1 24,0
PO 119
M 1,32,2
WL 5
LO 18,18,122
LU 122,18
M 1,33,3
RA 216,1,13312
HS 25,122
M 2,44,2
LL 17,100
LL 126,101
M 2,45,3
LA 17
PK 122,18,120,33
M 3,56,2
LC_ 126
PK 120,18,120,32
M 3,57,3
ST 116
M 4,68,2
WL 4
LO 16,16,123
LU 123,16
M 4,69,3
s_add_u32 s42, s44, s64
RA 220,1,14336
HS 25,123
M 5,80,2
LL 18,102
LL 127,103
M 5,81,3
s_addc_u32 s43, s45, 0
LA 18
PK 123,16,121,33
M 0,22,4
LC_ 127
PK 121,16,121,32
M 0,23,5
ST 120
M 1,34,4
WL 3
LO 17,17,126
LU 126,17
M 1,35,5
RA 224,1,15360
HS 25,126
M 2,46,4
LL 16,56
LL 104,57
M 2,47,5
LA 16
PK 126,17,124,33
M 3,58,4
LC_ 104
PK 124,17,124,32
M 3,59,5
ds_read_b64 v[14:15], v5 offset:0
PO 126
M 4,70,4
WL 4
LO 18,18,127
LU 127,18
M 4,71,5
s_add_u32 s42, s44, s65
RA 228,1,16384
HS 25,127
M 5,82,4
LL 17,58
LL 105,59
M 5,83,5
s_addc_u32 s43, s45, 0
LA 17
PK 127,18,125,33
Z 6,12,0
LC_ 105
PK 125,18,125,32
M 6,13,1
PO 127
v_subrev_u32 v5, s39, v5
Z 7,24,0
WL 4
LO 104,16,104
LU 16,104
M 7,25,1
RA 232,1,17408
HZ 24,16
Z 8,36,0
WL 4
LL 18,60
LL 108,61
M 8,37,1
s_mul_i32 s53, s50, 25600
LA 18
v_add_f32 v14, v14, v15
Z 9,48,0
s_add_u32 s40, %[wlo], s53
LC_ 108
v_add_f32_dpp v14, v14, v14 quad_perm:[1,0,3,2] row_mask:0xf bank_mask:0xf
M 9,49,1
s_addc_u32 s41, %[whi], 0
Z 10,60,0
WL 3
LO 105,17,105
LU 17,105
M 10,61,1
s_mov_b32 m0, s38
RA 236,1,18432
HS 24,17
Z 11,72,0
LL 16,62
LL 109,63
M 11,73,1
LA 16
ST 124
M 6,14,2
LC_ 109
DW 0
M 6,15,3
v_add_f32_dpp v14, v14, v14 quad_perm:[2,3,0,1] row_mask:0xf bank_mask:0xf
HX 25
M 7,26,2
WL 3
LO 108,18,108
LU 18,108
W1 25,1024
M 7,27,3
s_add_u32 s37, s37, 32768
RA 216,1,19456
HS 24,18
M 8,38,2
s_cmp_eq_u32 s37, s75
LL 17,64
LL 112,65
M 8,39,3
s_cselect_b32 s37, s74, s37
LA 17
DW 1024
M 9,50,2
LC_ 112
v_add_f32_dpp v14, v14, v14 row_half_mirror row_mask:0xf bank_mask:0xf
M 9,51,3
v_cvt_f64_f32 v[14:15], v14
v_add_u32 v4, s39, v4
M 10,62,2
WL 4
LO 109,16,109
LU 16,109
M 10,63,3
RA 220,1,20480
HS 24,16
M 11,74,2
LL 18,66
LL 113,67
M 11,75,3
LA 18
s_mov_b64 exec, s[54:55]
SP
s_mov_b64 exec, -1
M 6,16,4
LC_ 113
DW 2048
M 6,17,5
M 7,28,4
WL 3
LO 112,17,112
LU 17,112
M 7,29,5
s_mul_i32 s53, s51, 49152
RA 224,1,21504
HS 24,17
M 8,40,4
s_bitcmp1_b64 s[90:91], s51
LL 16,68
LL 116,69
M 8,41,5
s_cselect_b64 s[44:45], s[84:85], s[82:83]
LA 16
M 9,52,4
s_add_u32 s44, s44, s53
LC_ 116
M 9,53,5
s_addc_u32 s45, s45, 0
M 10,64,4
WL 3
LO 113,18,113
LU 18,113
M 10,65,5
s_add_u32 s51, s51, %[d]
RA 228,1,22528
HS 24,18
M 11,76,4
LL 17,70
LL 117,71
M 11,77,5
s_cmp_eq_u32 s51, %[wrap]
LA 17
M 6,18,0
s_cselect_b32 s51, %[rst], s51
LC_ 117
M 6,19,1
s_add_u32 s40, s40, 3072
M 7,30,0
WL 3
LO 116,16,116
LU 16,116
M 7,31,1
s_addc_u32 s41, s41, 0
RA 232,1,23552
HS 24,16
M 8,42,0
s_add_u32 m0, s38, 3072
LL 18,72
LL 120,73
M 8,43,1
LA 18
DW 0
M 9,54,0
LC_ 120
DW 1024
M 9,55,1
M 10,66,0
WL 3
LO 117,17,117
LU 17,117
M 10,67,1
RA 236,1,24576
HS 24,17
M 11,78,0
LL 16,74
LL 121,75
M 11,79,1
LA 16
DW 2048
M 6,20,2
LC_ 121
M 6,21,3
s_lshl_b32 s53, s52, 8
v_add_u32 v1, s37, v0
M 7,32,2
WL 3
LO 120,18,120
LU 18,120
M 7,33,3
s_add_u32 s48, %[alo], s53
RA 216,1,0
HS 24,18
M 8,44,2
s_addc_u32 s49, %[ahi], 0
LL 17,76
LL 124,77
M 8,45,3
s_add_u32 s52, s52, %[d]
LA 17
v_and_b32 v22, 0xfffffcff, v1
M 9,56,2
s_cmp_eq_u32 s52, %[wrap]
LC_ 124
M 9,57,3
s_cselect_b32 s52, %[rst], s52
DQ 3072
M 10,68,2
WL 3
LO 121,16,121
LU 16,121
M 10,69,3
s_add_u32 s50, s50, %[d]
RA 220,1,1024
HS 24,16
M 11,80,2
LL 18,78
LL 125,79
M 11,81,3
s_cmp_eq_u32 s50, %[wrap]
LA 18
RV 252,22,5120
M 6,22,4
s_cselect_b32 s50, %[rst], s50
LC_ 125
M 6,23,5
WV 30
v_add_u32 v19, 1, v19
M 7,34,4
WL 4
LO 124,17,124
LU 17,124
M 7,35,5
s_add_u32 s38, s38, 32768
RA 224,1,2048
HS 24,17
M 8,46,4
s_cmp_eq_u32 s38, s77
LL 16,80
LL 106,81
M 8,47,5
s_cselect_b32 s38, s76, s38
LA 16
FP
M 9,58,4
s_sub_u32 s39, 0, s39
LC_ 106
M 9,59,5
M 10,70,4
WL 4
LO 125,18,125
LU 18,125
M 10,71,5
RA 228,1,3072
HS 24,18
M 11,82,4
s_sub_u32 s66, s66, 1
LL 17,82
LL 107,83
M 11,83,5
s_cbranch_scc0 1b
7:
LA 17
HX 24
Z 0,12,0
LC_ 107
M 0,13,1
Z 1,24,0
WL 3
LO 16,16,106
LU 106,16
M 1,25,1
RA 232,1,4096
HZ 25,106
Z 2,36,0
LL 18,84
LL 110,85
M 2,37,1
LA 18
PK 106,16,104,33
Z 3,48,0
LC_ 110
PK 104,16,104,32
M 3,49,1
Z 4,60,0
WL 3
LO 17,17,107
LU 107,17
M 4,61,1
)"
R"(s_add_u32 s42, s44, s60
RA 236,1,6144
HS 25,107
Z 5,72,0
LL 16,86
LL 111,87
M 5,73,1
s_addc_u32 s43, s45, 0
LA 16
PK 107,17,105,33
M 0,14,2
LC_ 111
PK 105,17,105,32
M 0,15,3
ST 104
M 1,26,2
WL 3
LO 18,18,110
LU 110,18
M 1,27,3
RA 216,1,7168
HS 25,110
M 2,38,2
LL 17,88
LL 114,89
M 2,39,3
LA 17
PK 110,18,108,33
M 3,50,2
LC_ 114
PK 108,18,108,32
M 3,51,3
FR
PO 110
M 4,62,2
WL 4
LO 16,16,111
LU 111,16
M 4,63,3
s_add_u32 s42, s44, s61
RA 220,1,8192
HS 25,111
M 5,74,2
LL 18,90
LL 115,91
M 5,75,3
s_addc_u32 s43, s45, 0
LA 18
PK 111,16,109,33
M 0,16,4
s_mov_b32 m0, %[pm]
LC_ 115
PK 109,16,109,32
M 0,17,5
DF
PO 111
M 1,28,4
WL 4
LO 17,17,114
LU 114,17
M 1,29,5
RA 224,1,9216
HS 25,114
M 2,40,4
LL 16,92
LL 118,93
M 2,41,5
LA 16
PK 114,17,112,33
M 3,52,4
LC_ 118
PK 112,17,112,32
M 3,53,5
ST 108
M 4,64,4
WL 3
LO 18,18,115
LU 115,18
M 4,65,5
s_add_u32 s42, s44, s62
RA 228,1,10240
HS 25,115
M 5,76,4
LL 17,94
LL 119,95
M 5,77,5
s_addc_u32 s43, s45, 0
LA 17
PK 115,18,113,33
M 0,18,0
LC_ 119
PK 113,18,113,32
M 0,19,1
ST 112
M 1,30,0
WL 3
LO 16,16,118
LU 118,16
M 1,31,1
RA 232,1,11264
HS 25,118
M 2,42,0
LL 18,96
LL 122,97
M 2,43,1
LA 18
PK 118,16,116,33
v_cmp_gt_u32 vcc, v19, v15
M 3,54,0
LC_ 122
PK 116,16,116,32
M 3,55,1
RV 252,22,5392
PO 118
M 4,66,0
WL 4
LO 17,17,119
LU 119,17
M 4,67,1
s_cbranch_vccnz .Lk1s1
.Lk1r1:
RA 236,1,12288
HS 25,119
M 5,78,0
LL 16,98
LL 123,99
M 5,79,1
s_add_u32 s42, s44, s63
LA 16
PK 119,17,117,33
M 0,20,2
s_addc_u32 s43, s45, 0
LC_ 123
PK 117,17,117,32
M 0,21,3
W1 24,0
PO 119
M 1,32,2
WL 5
LO 18,18,122
LU 122,18
M 1,33,3
RA 216,1,13312
HS 25,122
M 2,44,2
LL 17,100
LL 126,101
M 2,45,3
LA 17
PK 122,18,120,33
M 3,56,2
LC_ 126
PK 120,18,120,32
M 3,57,3
ST 116
M 4,68,2
WL 4
LO 16,16,123
LU 123,16
M 4,69,3
s_add_u32 s42, s44, s64
RA 220,1,14336
HS 25,123
M 5,80,2
LL 18,102
LL 127,103
M 5,81,3
s_addc_u32 s43, s45, 0
LA 18
PK 123,16,121,33
M 0,22,4
LC_ 127
PK 121,16,121,32
M 0,23,5
ST 120
M 1,34,4
WL 3
LO 17,17,126
LU 126,17
M 1,35,5
RA 224,1,15360
HS 25,126
M 2,46,4
LL 16,56
LL 104,57
M 2,47,5
LA 16
PK 126,17,124,33
M 3,58,4
LC_ 104
PK 124,17,124,32
M 3,59,5
ds_read_b64 v[14:15], v5 offset:0
PO 126
M 4,70,4
WL 4
LO 18,18,127
LU 127,18
M 4,71,5
s_add_u32 s42, s44, s65
RA 228,1,16384
HS 25,127
M 5,82,4
LL 17,58
LL 105,59
M 5,83,5
s_addc_u32 s43, s45, 0
LA 17
PK 127,18,125,33
Z 6,12,0
LC_ 105
PK 125,18,125,32
M 6,13,1
PO 127
v_subrev_u32 v5, s39, v5
Z 7,24,0
WL 4
LO 104,16,104
LU 16,104
M 7,25,1
RA 232,1,17408
HZ 24,16
Z 8,36,0
WL 4
LL 18,60
LL 108,61
M 8,37,1
s_mul_i32 s53, s50, 25600
LA 18
v_add_f32 v14, v14, v15
Z 9,48,0
s_add_u32 s40, %[wlo], s53
LC_ 108
v_add_f32_dpp v14, v14, v14 quad_perm:[1,0,3,2] row_mask:0xf bank_mask:0xf
M 9,49,1
s_addc_u32 s41, %[whi], 0
Z 10,60,0
WL 3
LO 105,17,105
LU 17,105
M 10,61,1
s_mov_b32 m0, s38
RA 236,1,18432
HS 24,17
Z 11,72,0
LL 16,62
LL 109,63
M 11,73,1
LA 16
ST 124
M 6,14,2
LC_ 109
DW 0
M 6,15,3
v_add_f32_dpp v14, v14, v14 quad_perm:[2,3,0,1] row_mask:0xf bank_mask:0xf
HX 25
M 7,26,2
WL 3
LO 108,18,108
LU 18,108
W1 25,1024
M 7,27,3
s_add_u32 s37, s37, 32768
RA 216,1,19456
HS 24,18
M 8,38,2
s_cmp_eq_u32 s37, s75
LL 17,64
LL 112,65
M 8,39,3
s_cselect_b32 s37, s74, s37
LA 17
DW 1024
M 9,50,2
LC_ 112
v_add_f32_dpp v14, v14, v14 row_half_mirror row_mask:0xf bank_mask:0xf
M 9,51,3
v_cvt_f64_f32 v[14:15], v14
v_add_u32 v4, s39, v4
s_mov_b64 exec, 1
AT
s_mov_b64 exec, -1
M 10,62,2
WL 4
LO 109,16,109
LU 16,109
M 10,63,3
RA 220,1,20480
HS 24,16
M 11,74,2
LL 18,66
LL 113,67
M 11,75,3
LA 18
s_mov_b64 exec, s[54:55]
SP
s_mov_b64 exec, -1
M 6,16,4
LC_ 113
DW 2048
M 6,17,5
M 7,28,4
WL 3
LO 112,17,112
LU 17,112
M 7,29,5
s_mul_i32 s53, s51, 49152
RA 224,1,21504
HS 24,17
M 8,40,4
s_bitcmp1_b64 s[90:91], s51
LL 16,68
LL 116,69
M 8,41,5
s_cselect_b64 s[44:45], s[84:85], s[82:83]
LA 16
M 9,52,4
s_add_u32 s44, s44, s53
LC_ 116
M 9,53,5
s_addc_u32 s45, s45, 0
M 10,64,4
WL 3
LO 113,18,113
LU 18,113
M 10,65,5
s_add_u32 s51, s51, %[d]
RA 228,1,22528
HS 24,18
M 11,76,4
LL 17,70
LL 117,71
M 11,77,5
s_cmp_eq_u32 s51, %[wrap]
LA 17
M 6,18,0
s_cselect_b32 s51, %[rst], s51
LC_ 117
M 6,19,1
s_add_u32 s40, s40, 3072
M 7,30,0
WL 3
LO 116,16,116
LU 16,116
M 7,31,1
s_addc_u32 s41, s41, 0
RA 232,1,23552
HS 24,16
M 8,42,0
s_add_u32 m0, s38, 3072
LL 18,72
LL 120,73
M 8,43,1
LA 18
DW 0
M 9,54,0
LC_ 120
DW 1024
M 9,55,1
M 10,66,0
WL 3
LO 117,17,117
LU 17,117
M 10,67,1
RA 236,1,24576
HS 24,17
M 11,78,0
LL 16,74
LL 121,75
M 11,79,1
LA 16
DW 2048
M 6,20,2
LC_ 121
M 6,21,3
s_lshl_b32 s53, s52, 8
v_add_u32 v1, s37, v0
M 7,32,2
WL 3
LO 120,18,120
LU 18,120
M 7,33,3
s_add_u32 s48, %[alo], s53
RA 216,1,0
HS 24,18
M 8,44,2
s_addc_u32 s49, %[ahi], 0
LL 17,76
LL 124,77
M 8,45,3
s_add_u32 s52, s52, %[d]
LA 17
v_and_b32 v22, 0xfffffcff, v1
M 9,56,2
s_cmp_eq_u32 s52, %[wrap]
LC_ 124
M 9,57,3
s_cselect_b32 s52, %[rst], s52
DQ 3072
M 10,68,2
)"
R"(WL 3
LO 121,16,121
LU 16,121
M 10,69,3
s_add_u32 s50, s50, %[d]
RA 220,1,1024
HS 24,16
M 11,80,2
LL 18,78
LL 125,79
M 11,81,3
s_cmp_eq_u32 s50, %[wrap]
LA 18
RV 252,22,5120
M 6,22,4
s_cselect_b32 s50, %[rst], s50
LC_ 125
M 6,23,5
WV 31
v_add_u32 v19, 1, v19
M 7,34,4
WL 4
LO 124,17,124
LU 17,124
M 7,35,5
s_add_u32 s38, s38, 32768
RA 224,1,2048
HS 24,17
M 8,46,4
s_cmp_eq_u32 s38, s77
LL 16,80
LL 106,81
M 8,47,5
s_cselect_b32 s38, s76, s38
LA 16
FP
M 9,58,4
s_sub_u32 s39, 0, s39
LC_ 106
M 9,59,5
M 10,70,4
WL 4
LO 125,18,125
LU 18,125
M 10,71,5
RA 228,1,3072
HS 24,18
M 11,82,4
LL 17,82
LL 107,83
M 11,83,5
LA 17
HX 24
Z 0,12,0
LC_ 107
M 0,13,1
Z 1,24,0
WL 3
LO 16,16,106
LU 106,16
M 1,25,1
RA 232,1,4096
HZ 25,106
Z 2,36,0
LL 18,84
LL 110,85
M 2,37,1
LA 18
PK 106,16,104,33
Z 3,48,0
LC_ 110
PK 104,16,104,32
M 3,49,1
Z 4,60,0
WL 3
LO 17,17,107
LU 107,17
M 4,61,1
s_add_u32 s42, s44, s60
RA 236,1,6144
HS 25,107
Z 5,72,0
LL 16,86
LL 111,87
M 5,73,1
s_addc_u32 s43, s45, 0
LA 16
PK 107,17,105,33
M 0,14,2
LC_ 111
PK 105,17,105,32
M 0,15,3
ST 104
M 1,26,2
WL 3
LO 18,18,110
LU 110,18
M 1,27,3
RA 216,1,7168
HS 25,110
M 2,38,2
LL 17,88
LL 114,89
M 2,39,3
LA 17
PK 110,18,108,33
M 3,50,2
LC_ 114
PK 108,18,108,32
M 3,51,3
FR
PO 110
M 4,62,2
WL 4
LO 16,16,111
LU 111,16
M 4,63,3
s_add_u32 s42, s44, s61
RA 220,1,8192
HS 25,111
M 5,74,2
LL 18,90
LL 115,91
M 5,75,3
s_addc_u32 s43, s45, 0
LA 18
PK 111,16,109,33
M 0,16,4
s_mov_b32 m0, %[pm]
LC_ 115
PK 109,16,109,32
M 0,17,5
DF
PO 111
M 1,28,4
WL 4
LO 17,17,114
LU 114,17
M 1,29,5
RA 224,1,9216
HS 25,114
M 2,40,4
LL 16,92
LL 118,93
M 2,41,5
LA 16
PK 114,17,112,33
M 3,52,4
LC_ 118
PK 112,17,112,32
M 3,53,5
ST 108
M 4,64,4
WL 3
LO 18,18,115
LU 115,18
M 4,65,5
s_add_u32 s42, s44, s62
RA 228,1,10240
HS 25,115
M 5,76,4
LL 17,94
LL 119,95
M 5,77,5
s_addc_u32 s43, s45, 0
LA 17
PK 115,18,113,33
M 0,18,0
LC_ 119
PK 113,18,113,32
M 0,19,1
ST 112
M 1,30,0
WL 3
LO 16,16,118
LU 118,16
M 1,31,1
RA 232,1,11264
HS 25,118
M 2,42,0
LL 18,96
LL 122,97
M 2,43,1
LA 18
PK 118,16,116,33
v_cmp_gt_u32 vcc, v19, v15
M 3,54,0
LC_ 122
PK 116,16,116,32
M 3,55,1
RV 252,22,5392
PO 118
M 4,66,0
WL 4
LO 17,17,119
LU 119,17
M 4,67,1
s_cbranch_vccnz .Lk1s2
.Lk1r2:
RA 236,1,12288
HS 25,119
M 5,78,0
LL 16,98
LL 123,99
M 5,79,1
s_add_u32 s42, s44, s63
LA 16
PK 119,17,117,33
M 0,20,2
s_addc_u32 s43, s45, 0
LC_ 123
PK 117,17,117,32
M 0,21,3
W1 24,0
PO 119
M 1,32,2
WL 5
LO 18,18,122
LU 122,18
M 1,33,3
RA 216,1,13312
HS 25,122
M 2,44,2
LL 17,100
LL 126,101
M 2,45,3
LA 17
PK 122,18,120,33
M 3,56,2
LC_ 126
PK 120,18,120,32
M 3,57,3
ST 116
M 4,68,2
WL 4
LO 16,16,123
LU 123,16
M 4,69,3
s_add_u32 s42, s44, s64
RA 220,1,14336
HS 25,123
M 5,80,2
LL 18,102
LL 127,103
M 5,81,3
s_addc_u32 s43, s45, 0
LA 18
PK 123,16,121,33
M 0,22,4
LC_ 127
PK 121,16,121,32
M 0,23,5
ST 120
M 1,34,4
WL 3
LO 17,17,126
LU 126,17
M 1,35,5
RA 224,1,15360
HS 25,126
M 2,46,4
LL 16,56
LL 104,57
M 2,47,5
LA 16
PK 126,17,124,33
M 3,58,4
LC_ 104
PK 124,17,124,32
M 3,59,5
ds_read_b64 v[14:15], v5 offset:0
PO 126
M 4,70,4
WL 4
LO 18,18,127
LU 127,18
M 4,71,5
s_add_u32 s42, s44, s65
RA 228,1,16384
HS 25,127
M 5,82,4
LL 17,58
LL 105,59
M 5,83,5
s_addc_u32 s43, s45, 0
LA 17
PK 127,18,125,33
Z 6,12,0
LC_ 105
PK 125,18,125,32
M 6,13,1
PO 127
v_subrev_u32 v5, s39, v5
Z 7,24,0
WL 4
LO 104,16,104
LU 16,104
M 7,25,1
RA 232,1,17408
HZ 24,16
Z 8,36,0
WL 4
LL 18,60
LL 108,61
M 8,37,1
s_mul_i32 s53, s50, 25600
LA 18
v_add_f32 v14, v14, v15
Z 9,48,0
s_add_u32 s40, %[wlo], s53
LC_ 108
v_add_f32_dpp v14, v14, v14 quad_perm:[1,0,3,2] row_mask:0xf bank_mask:0xf
M 9,49,1
s_addc_u32 s41, %[whi], 0
Z 10,60,0
WL 3
LO 105,17,105
LU 17,105
M 10,61,1
s_mov_b32 m0, s38
RA 236,1,18432
HS 24,17
Z 11,72,0
LL 16,62
LL 109,63
M 11,73,1
LA 16
ST 124
M 6,14,2
LC_ 109
DW 0
M 6,15,3
WV 14
v_add_f32_dpp v14, v14, v14 quad_perm:[2,3,0,1] row_mask:0xf bank_mask:0xf
HX 25
M 7,26,2
WL 3
LO 108,18,108
LU 18,108
W1 25,1024
M 7,27,3
s_add_u32 s37, s37, 32768
RA 216,1,19456
HS 24,18
v_readfirstlane_b32 s72, v11
M 8,38,2
s_cmp_eq_u32 s37, s75
LL 17,64
LL 112,65
v_mov_b32 v12, s72
M 8,39,3
s_cselect_b32 s37, s74, s37
LA 17
W2 12,12,16
M 9,50,2
LC_ 112
DW 1024
M 9,51,3
v_add_f32_dpp v14, v14, v14 row_half_mirror row_mask:0xf bank_mask:0xf
v_add_u32 v4, s39, v4
M 10,62,2
WL 5
LO 109,16,109
LU 16,109
M 10,63,3
RA 220,1,20480
HS 24,16
M 11,74,2
LL 18,66
LL 113,67
M 11,75,3
LA 18
DW 2048
M 6,16,4
LC_ 113
v_cvt_f64_f32 v[14:15], v14
M 6,17,5
s_mov_b64 exec, s[54:55]
SP
s_mov_b64 exec, -1
M 7,28,4
WL 3
LO 112,17,112
LU 17,112
M 7,29,5
s_mul_i32 s53, s51, 49152
RA 224,1,21504
HS 24,17
M 8,40,4
s_bitcmp1_b64 s[90:91], s51
LL 16,68
LL 116,69
M 8,41,5
s_cselect_b64 s[44:45], s[84:85], s[82:83]
LA 16
M 9,52,4
s_add_u32 s44, s44, s53
LC_ 116
M 9,53,5
s_addc_u32 s45, s45, 0
)"
R"(M 10,64,4
WL 3
LO 113,18,113
LU 18,113
M 10,65,5
s_add_u32 s51, s51, %[d]
RA 228,1,22528
HS 24,18
M 11,76,4
LL 17,70
LL 117,71
M 11,77,5
s_cmp_eq_u32 s51, %[wrap]
LA 17
M 6,18,0
s_cselect_b32 s51, %[rst], s51
LC_ 117
M 6,19,1
s_add_u32 s40, s40, 3072
M 7,30,0
WL 3
LO 116,16,116
LU 16,116
M 7,31,1
s_addc_u32 s41, s41, 0
RA 232,1,23552
HS 24,16
M 8,42,0
s_add_u32 m0, s38, 3072
LL 18,72
LL 120,73
M 8,43,1
LA 18
DW 0
M 9,54,0
LC_ 120
DW 1024
M 9,55,1
M 10,66,0
WL 3
LO 117,17,117
LU 17,117
M 10,67,1
RA 236,1,24576
HS 24,17
M 11,78,0
LL 16,74
LL 121,75
M 11,79,1
LA 16
DW 2048
M 6,20,2
LC_ 121
M 6,21,3
s_lshl_b32 s53, s52, 8
v_add_u32 v1, s37, v0
M 7,32,2
WL 3
LO 120,18,120
LU 18,120
M 7,33,3
s_add_u32 s48, %[alo], s53
RA 216,1,0
HS 24,18
M 8,44,2
s_addc_u32 s49, %[ahi], 0
LL 17,76
LL 124,77
M 8,45,3
s_add_u32 s52, s52, %[d]
LA 17
v_and_b32 v22, 0xfffffcff, v1
M 9,56,2
s_cmp_eq_u32 s52, %[wrap]
LC_ 124
M 9,57,3
s_cselect_b32 s52, %[rst], s52
DQ 3072
M 10,68,2
WL 3
LO 121,16,121
LU 16,121
M 10,69,3
s_add_u32 s50, s50, %[d]
RA 220,1,1024
HS 24,16
M 11,80,2
LL 18,78
LL 125,79
M 11,81,3
s_cmp_eq_u32 s50, %[wrap]
LA 18
RV 252,22,5120
M 6,22,4
s_cselect_b32 s50, %[rst], s50
LC_ 125
M 6,23,5
v_add_u32 v19, 1, v19
M 7,34,4
WL 4
LO 124,17,124
LU 17,124
M 7,35,5
s_add_u32 s38, s38, 32768
RA 224,1,2048
HS 24,17
M 8,46,4
s_cmp_eq_u32 s38, s77
LL 16,80
LL 106,81
M 8,47,5
s_cselect_b32 s38, s76, s38
LA 16
FP
M 9,58,4
s_sub_u32 s39, 0, s39
LC_ 106
M 9,59,5
M 10,70,4
WL 4
LO 125,18,125
LU 18,125
M 10,71,5
RA 228,1,3072
HS 24,18
M 11,82,4
LL 17,82
LL 107,83
M 11,83,5
LA 17
HX 24
Z 0,12,0
LC_ 107
M 0,13,1
Z 1,24,0
WL 3
LO 16,16,106
LU 106,16
M 1,25,1
RA 232,1,4096
HZ 25,106
Z 2,36,0
LL 18,84
LL 110,85
M 2,37,1
LA 18
PK 106,16,104,33
Z 3,48,0
LC_ 110
PK 104,16,104,32
M 3,49,1
Z 4,60,0
WL 3
LO 17,17,107
LU 107,17
M 4,61,1
s_add_u32 s42, s44, s60
RA 236,1,6144
HS 25,107
Z 5,72,0
LL 16,86
LL 111,87
M 5,73,1
s_addc_u32 s43, s45, 0
LA 16
PK 107,17,105,33
M 0,14,2
LC_ 111
PK 105,17,105,32
M 0,15,3
ST 104
M 1,26,2
WL 3
LO 18,18,110
LU 110,18
M 1,27,3
RA 216,1,7168
HS 25,110
M 2,38,2
LL 17,88
LL 114,89
M 2,39,3
LA 17
PK 110,18,108,33
M 3,50,2
LC_ 114
PK 108,18,108,32
M 3,51,3
FR
PO 110
M 4,62,2
WL 4
LO 16,16,111
LU 111,16
M 4,63,3
s_add_u32 s42, s44, s61
RA 220,1,8192
HS 25,111
M 5,74,2
LL 18,90
LL 115,91
M 5,75,3
s_addc_u32 s43, s45, 0
LA 18
PK 111,16,109,33
M 0,16,4
s_mov_b32 m0, %[pm]
LC_ 115
PK 109,16,109,32
M 0,17,5
DF
PO 111
M 1,28,4
WL 4
LO 17,17,114
LU 114,17
M 1,29,5
RA 224,1,9216
HS 25,114
M 2,40,4
LL 16,92
LL 118,93
M 2,41,5
LA 16
PK 114,17,112,33
M 3,52,4
LC_ 118
PK 112,17,112,32
M 3,53,5
ST 108
M 4,64,4
WL 3
LO 18,18,115
LU 115,18
M 4,65,5
s_add_u32 s42, s44, s62
RA 228,1,10240
HS 25,115
M 5,76,4
LL 17,94
LL 119,95
M 5,77,5
s_addc_u32 s43, s45, 0
LA 17
PK 115,18,113,33
M 0,18,0
LC_ 119
PK 113,18,113,32
M 0,19,1
ST 112
M 1,30,0
WL 3
LO 16,16,118
LU 118,16
M 1,31,1
RA 232,1,11264
HS 25,118
M 2,42,0
LL 18,96
LL 122,97
M 2,43,1
LA 18
PK 118,16,116,33
v_cmp_gt_u32 vcc, v19, v15
M 3,54,0
LC_ 122
PK 116,16,116,32
M 3,55,1
RV 252,22,5392
PO 118
M 4,66,0
WL 4
LO 17,17,119
LU 119,17
M 4,67,1
s_cbranch_vccnz .Lk1s3
.Lk1r3:
RA 236,1,12288
HS 25,119
M 5,78,0
LL 16,98
LL 123,99
M 5,79,1
s_add_u32 s42, s44, s63
LA 16
PK 119,17,117,33
M 0,20,2
s_addc_u32 s43, s45, 0
LC_ 123
PK 117,17,117,32
M 0,21,3
ds_read_b32 v20, v5 offset:4096
PO 119
M 1,32,2
WL 5
LO 18,18,122
LU 122,18
M 1,33,3
RA 216,1,13312
HS 25,122
M 2,44,2
LL 17,100
LL 126,101
M 2,45,3
LA 17
PK 122,18,120,33
M 3,56,2
LC_ 126
PK 120,18,120,32
M 3,57,3
M 4,68,2
WL 4
LO 16,16,123
LU 123,16
M 4,69,3
RA 220,1,14336
HS 25,123
M 5,80,2
LL 18,102
LL 127,103
M 5,81,3
LA 18
PK 123,16,121,33
M 0,22,4
LC_ 127
PK 121,16,121,32
W1 24,0
M 0,23,5
M 1,34,4
WL 4
LO 17,17,126
LU 126,17
M 1,35,5
RA 224,1,15360
HS 25,126
M 2,46,4
LL 16,56
LL 104,57
M 2,47,5
LA 16
PK 126,17,124,33
M 3,58,4
LC_ 104
PK 124,17,124,32
M 3,59,5
PO 126
v_readfirstlane_b32 s72, v20
M 4,70,4
WL 4
LO 18,18,127
LU 127,18
M 4,71,5
s_sub_u32 s53, 63, s72
RA 228,1,16384
HS 25,127
M 5,82,4
LL 17,58
LL 105,59
M 5,83,5
s_cmp_eq_u32 %[d], 1
LA 17
PK 127,18,125,33
Z 6,12,0
s_cselect_b32 s72, s72, s53
LC_ 105
PK 125,18,125,32
M 6,13,1
s_and_b32 s72, s72, 63
ds_read_b64 v[14:15], v5 offset:0
PO 127
Z 7,24,0
WL 4
LO 104,16,104
LU 16,104
M 7,25,1
s_add_u32 s53, s72, 128
RA 232,1,17408
HZ 24,16
Z 8,36,0
s_mul_i32 s53, s53, 2359296
LL 18,60
LL 108,61
M 8,37,1
s_add_u32 s86, %[hlo], s53
LA 18
v_subrev_u32 v5, s39, v5
Z 9,48,0
s_addc_u32 s87, %[hhi], 0
LC_ 108
HX 25
M 9,49,1
s_add_u32 s88, s86, 8192
Z 10,60,0
WL 4
LO 105,17,105
LU 17,105
M 10,61,1
s_addc_u32 s89, s87, 0
RA 236,1,18432
HS 24,17
)"
R"(Z 11,72,0
WL 4
LL 16,62
LL 109,63
M 11,73,1
s_and_b32 s92, %[hlo], 0x1fffff
LA 16
W1 25,1024
s_add_u32 s92, s92, s53
s_mov_b32 s93, 3072
ST 116
s_add_u32 s42, s44, s64
s_addc_u32 s43, s45, 0
s_mul_i32 s57, s72, 589824
s_add_u32 s57, s57, %[xs1]
s_lshl_b32 s72, s72, 1
s_add_u32 s72, s72, s96
s_lshl_b32 s53, s72, 6
s_add_u32 s53, s53, 128
s_cmp_lt_u32 %[ca0], 128
s_cselect_b32 s73, s53, %[ca0]
ST 120
v_mov_b32 v9, s73
v_mov_b32 v11, s92
M 6,14,2
s_add_u32 s42, s44, s65
LC_ 109
v_add_f32 v14, v14, v15
s_addc_u32 s43, s45, 0
v_mad_u32_u24 v11, v0, s93, v11
M 6,15,3
v_add_f32_dpp v14, v14, v14 quad_perm:[1,0,3,2] row_mask:0xf bank_mask:0xf
v_add_u32 v4, s39, v4
M 7,26,2
WL 4
LO 108,18,108
LU 18,108
M 7,27,3
RA 216,1,19456
HS 24,18
v_lshrrev_b32 v11, 21, v11
M 8,38,2
LL 17,64
LL 112,65
M 8,39,3
LA 17
v_add_f32_dpp v14, v14, v14 quad_perm:[2,3,0,1] row_mask:0xf bank_mask:0xf
M 9,50,2
LC_ 112
v_add_f32_dpp v14, v14, v14 row_half_mirror row_mask:0xf bank_mask:0xf
v_lshl_add_u32 v11, v11, 2, 64
M 9,51,3
s_mul_i32 s53, s50, 25600
v_cvt_f64_f32 v[14:15], v14
M 10,62,2
WL 3
LO 109,16,109
LU 16,109
s_add_u32 s40, %[wlo], s53
s_addc_u32 s41, %[whi], 0
s_mov_b32 m0, s38
M 10,63,3
RA 220,1,20480
HS 24,16
v_writelane_b32 v11, %[tsel], 63
M 11,74,2
LL 18,66
LL 113,67
ST 124
M 11,75,3
s_sub_u32 s53, s86, %[hlo]
LA 18
s_mov_b64 exec, s[54:55]
SP
s_mov_b64 exec, -1
M 6,16,4
s_lshr_b32 s53, s53, 2
LC_ 113
s_and_b32 s92, %[xlo], 0x1fffff
s_add_u32 s53, s53, s92
s_add_u32 s53, s53, 0x20048000
s_lshr_b32 s53, s53, 21
s_lshl_b32 s92, s53, 2
M 6,17,5
DW 0
v_writelane_b32 v11, s92, 48
M 7,28,4
WL 3
LO 112,17,112
LU 17,112
M 7,29,5
s_add_u32 s37, s37, 32768
RA 224,1,21504
HS 24,17
M 8,40,4
s_cmp_eq_u32 s37, s75
LL 16,68
LL 116,69
M 8,41,5
s_cselect_b32 s37, s74, s37
LA 16
global_load_dword v11, v11, s[68:69] offset:-2048
M 9,52,4
LC_ 116
DW 1024
M 9,53,5
DW 2048
M 10,64,4
WL 3
LO 113,18,113
LU 18,113
M 10,65,5
s_mul_i32 s53, s51, 49152
RA 228,1,22528
HS 24,18
M 11,76,4
LL 17,70
LL 117,71
M 11,77,5
s_bitcmp1_b64 s[90:91], s51
LA 17
M 6,18,0
s_cselect_b64 s[44:45], s[84:85], s[82:83]
LC_ 117
M 6,19,1
s_add_u32 s44, s44, s53
M 7,30,0
WL 3
LO 116,16,116
LU 16,116
M 7,31,1
s_addc_u32 s45, s45, 0
RA 232,1,23552
HS 24,16
M 8,42,0
s_add_u32 s51, s51, %[d]
LL 18,72
LL 120,73
M 8,43,1
s_cmp_eq_u32 s51, %[wrap]
LA 18
s_cselect_b32 s51, %[rst], s51
M 9,54,0
s_add_u32 s40, s40, 3072
LC_ 120
s_addc_u32 s41, s41, 0
s_add_u32 m0, s38, 3072
M 9,55,1
DW 0
M 10,66,0
WL 3
LO 117,17,117
LU 17,117
M 10,67,1
RA 236,1,24576
HS 24,17
M 11,78,0
LL 16,74
LL 121,75
M 11,79,1
LA 16
DW 1024
M 6,20,2
LC_ 121
DW 2048
M 6,21,3
s_lshl_b32 s53, s52, 8
v_add_u32 v1, s37, v0
M 7,32,2
WL 3
LO 120,18,120
LU 18,120
M 7,33,3
s_add_u32 s48, %[alo], s53
RA 216,1,0
HS 24,18
M 8,44,2
s_addc_u32 s49, %[ahi], 0
LL 17,76
LL 124,77
M 8,45,3
s_add_u32 s52, s52, %[d]
LA 17
v_and_b32 v22, 0xfffffcff, v1
M 9,56,2
s_cmp_eq_u32 s52, %[wrap]
LC_ 124
M 9,57,3
s_cselect_b32 s52, %[rst], s52
DQ 3072
M 10,68,2
WL 3
LO 121,16,121
LU 16,121
M 10,69,3
s_add_u32 s50, s50, %[d]
RA 220,1,1024
HS 24,16
M 11,80,2
LL 18,78
LL 125,79
M 11,81,3
s_cmp_eq_u32 s50, %[wrap]
LA 18
RV 252,22,5120
M 6,22,4
s_cselect_b32 s50, %[rst], s50
LC_ 125
M 6,23,5
WV 31
v_add_u32 v19, 1, v19
M 7,34,4
WL 4
LO 124,17,124
LU 17,124
M 7,35,5
s_add_u32 s38, s38, 32768
RA 224,1,2048
HS 24,17
M 8,46,4
s_cmp_eq_u32 s38, s77
LL 16,80
LL 106,81
M 8,47,5
s_cselect_b32 s38, s76, s38
LA 16
FP
M 9,58,4
s_sub_u32 s39, 0, s39
LC_ 106
M 9,59,5
M 10,70,4
WL 4
LO 125,18,125
LU 18,125
M 10,71,5
RA 228,1,3072
HS 24,18
M 11,82,4
LL 17,82
LL 107,83
M 11,83,5
LA 17
HX 24
Z 0,12,0
LC_ 107
M 0,13,1
Z 1,24,0
WL 3
LO 16,16,106
LU 106,16
M 1,25,1
RA 232,1,4096
HZ 25,106
Z 2,36,0
LL 18,84
LL 110,85
M 2,37,1
LA 18
PK 106,16,104,33
Z 3,48,0
LC_ 110
PK 104,16,104,32
M 3,49,1
Z 4,60,0
WL 3
LO 17,17,107
LU 107,17
M 4,61,1
s_add_u32 s42, s44, s60
RA 236,1,6144
HS 25,107
Z 5,72,0
LL 16,86
LL 111,87
M 5,73,1
s_addc_u32 s43, s45, 0
LA 16
PK 107,17,105,33
M 0,14,2
LC_ 111
PK 105,17,105,32
M 0,15,3
ST 104
M 1,26,2
WL 3
LO 18,18,110
LU 110,18
M 1,27,3
RA 216,1,7168
HS 25,110
M 2,38,2
LL 17,88
LL 114,89
M 2,39,3
LA 17
PK 110,18,108,33
M 3,50,2
LC_ 114
PK 108,18,108,32
M 3,51,3
FR
PO 110
M 4,62,2
WL 4
LO 16,16,111
LU 111,16
M 4,63,3
s_add_u32 s42, s44, s61
RA 220,1,8192
HS 25,111
M 5,74,2
LL 18,90
LL 115,91
M 5,75,3
s_addc_u32 s43, s45, 0
LA 18
PK 111,16,109,33
M 0,16,4
s_mov_b32 m0, %[pm]
LC_ 115
PK 109,16,109,32
M 0,17,5
DF
PO 111
M 1,28,4
WL 4
LO 17,17,114
LU 114,17
M 1,29,5
RA 224,1,9216
HS 25,114
M 2,40,4
LL 16,92
LL 118,93
M 2,41,5
LA 16
PK 114,17,112,33
M 3,52,4
LC_ 118
PK 112,17,112,32
M 3,53,5
ST 108
M 4,64,4
WL 3
LO 18,18,115
LU 115,18
M 4,65,5
s_add_u32 s42, s44, s62
RA 228,1,10240
HS 25,115
M 5,76,4
LL 17,94
LL 119,95
M 5,77,5
s_addc_u32 s43, s45, 0
LA 17
PK 115,18,113,33
M 0,18,0
LC_ 119
PK 113,18,113,32
M 0,19,1
ST 112
M 1,30,0
WL 3
LO 16,16,118
LU 118,16
M 1,31,1
RA 232,1,11264
HS 25,118
M 2,42,0
LL 18,96
LL 122,97
M 2,43,1
LA 18
PK 118,16,116,33
v_cmp_gt_u32 vcc, v19, v15
M 3,54,0
LC_ 122
PK 116,16,116,32
M 3,55,1
RV 252,22,5392
PO 118
M 4,66,0
WL 4
LO 17,17,119
LU 119,17
M 4,67,1
s_cbranch_vccnz .Lk1s4
.Lk1r4:
RA 236,1,12288
HS 25,119
M 5,78,0
LL 16,98
LL 123,99
M 5,79,1
s_add_u32 s42, s44, s63
LA 16
PK 119,17,117,33
M 0,20,2
)"
R"(s_addc_u32 s43, s45, 0
LC_ 123
PK 117,17,117,32
M 0,21,3
W1 24,0
PO 119
M 1,32,2
WL 5
LO 18,18,122
LU 122,18
M 1,33,3
RA 216,1,13312
HS 25,122
M 2,44,2
LL 17,100
LL 126,101
M 2,45,3
LA 17
PK 122,18,120,33
M 3,56,2
LC_ 126
PK 120,18,120,32
M 3,57,3
ST 116
M 4,68,2
WL 4
LO 16,16,123
LU 123,16
M 4,69,3
s_add_u32 s42, s44, s64
RA 220,1,14336
HS 25,123
M 5,80,2
LL 18,102
LL 127,103
M 5,81,3
s_addc_u32 s43, s45, 0
LA 18
PK 123,16,121,33
M 0,22,4
LC_ 127
PK 121,16,121,32
M 0,23,5
ST 120
M 1,34,4
WL 3
LO 17,17,126
LU 126,17
M 1,35,5
RA 224,1,15360
HS 25,126
M 2,46,4
LL 16,56
LL 104,57
M 2,47,5
LA 16
PK 126,17,124,33
M 3,58,4
LC_ 104
PK 124,17,124,32
M 3,59,5
ds_read_b64 v[14:15], v5 offset:0
PO 126
M 4,70,4
WL 4
LO 18,18,127
LU 127,18
M 4,71,5
s_add_u32 s42, s44, s65
RA 228,1,16384
HS 25,127
M 5,82,4
LL 17,58
LL 105,59
M 5,83,5
s_addc_u32 s43, s45, 0
LA 17
PK 127,18,125,33
Z 6,12,0
LC_ 105
PK 125,18,125,32
M 6,13,1
PO 127
v_subrev_u32 v5, s39, v5
Z 7,24,0
WL 4
LO 104,16,104
LU 16,104
M 7,25,1
RA 232,1,17408
HZ 24,16
Z 8,36,0
WL 4
LL 18,60
LL 108,61
M 8,37,1
s_mul_i32 s53, s50, 25600
LA 18
v_add_f32 v14, v14, v15
Z 9,48,0
s_add_u32 s40, %[wlo], s53
LC_ 108
v_add_f32_dpp v14, v14, v14 quad_perm:[1,0,3,2] row_mask:0xf bank_mask:0xf
M 9,49,1
s_addc_u32 s41, %[whi], 0
Z 10,60,0
WL 3
LO 105,17,105
LU 17,105
M 10,61,1
s_mov_b32 m0, s38
RA 236,1,18432
HS 24,17
Z 11,72,0
LL 16,62
LL 109,63
M 11,73,1
LA 16
ST 124
M 6,14,2
LC_ 109
DW 0
M 6,15,3
v_add_f32_dpp v14, v14, v14 quad_perm:[2,3,0,1] row_mask:0xf bank_mask:0xf
HX 25
M 7,26,2
WL 3
LO 108,18,108
LU 18,108
W1 25,1024
M 7,27,3
s_add_u32 s37, s37, 32768
RA 216,1,19456
HS 24,18
M 8,38,2
s_cmp_eq_u32 s37, s75
LL 17,64
LL 112,65
M 8,39,3
s_cselect_b32 s37, s74, s37
LA 17
DW 1024
M 9,50,2
LC_ 112
v_add_f32_dpp v14, v14, v14 row_half_mirror row_mask:0xf bank_mask:0xf
M 9,51,3
v_cvt_f64_f32 v[14:15], v14
v_add_u32 v4, s39, v4
M 10,62,2
WL 4
LO 109,16,109
LU 16,109
M 10,63,3
RA 220,1,20480
HS 24,16
M 11,74,2
LL 18,66
LL 113,67
M 11,75,3
LA 18
s_mov_b64 exec, s[54:55]
SP
s_mov_b64 exec, -1
M 6,16,4
LC_ 113
DW 2048
M 6,17,5
s_and_b32 s53, %[w], 1
M 7,28,4
WL 3
LO 112,17,112
LU 17,112
M 7,29,5
s_lshl_b32 s53, s53, 10
RA 224,1,21504
HS 24,17
M 8,40,4
s_lshr_b32 s72, %[w], 1
LL 16,68
LL 116,69
M 8,41,5
s_mul_i32 s72, s72, 32768
LA 16
M 9,52,4
s_add_u32 s72, s72, s53
LC_ 116
M 9,53,5
s_add_u32 s72, s72, 128000
M 10,64,4
WL 3
LO 113,18,113
LU 18,113
M 10,65,5
s_movk_i32 s53, 0x78
RA 228,1,22528
HS 24,18
M 11,76,4
LL 17,70
LL 117,71
M 11,77,5
LA 17
v_lshrrev_b32 v12, 5, v0
M 6,18,0
LC_ 117
v_and_b32 v12, 6, v12
M 6,19,1
v_bfe_u32 v12, s53, v12, 2
M 7,30,0
WL 3
LO 116,16,116
LU 16,116
M 7,31,1
RA 232,1,23552
HS 24,16
v_lshrrev_b32 v20, 8, v0
v_xor_b32 v20, v12, v20
v_lshl_add_u32 v20, v20, 4, s72
v_and_b32 v12, 0xf0, v0
v_lshl_add_u32 v20, v12, 2, v20
v_add_u32 v12, s72, v0
M 8,42,0
s_mul_i32 s53, s51, 49152
LL 18,72
LL 120,73
M 8,43,1
s_bitcmp1_b64 s[90:91], s51
LA 18
s_cselect_b64 s[44:45], s[84:85], s[82:83]
s_add_u32 s44, s44, s53
s_addc_u32 s45, s45, 0
s_add_u32 s51, s51, %[d]
s_cmp_eq_u32 s51, %[wrap]
s_cselect_b32 s51, %[rst], s51
M 9,54,0
s_add_u32 s40, s40, 3072
LC_ 120
s_addc_u32 s41, s41, 0
s_add_u32 m0, s38, 3072
M 9,55,1
DW 0
M 10,66,0
WL 3
LO 117,17,117
LU 17,117
M 10,67,1
RA 236,1,24576
HS 24,17
M 11,78,0
LL 16,74
LL 121,75
M 11,79,1
LA 16
DW 1024
M 6,20,2
LC_ 121
DW 2048
M 6,21,3
s_lshl_b32 s53, s52, 8
v_add_u32 v1, s37, v0
M 7,32,2
WL 3
LO 120,18,120
LU 18,120
M 7,33,3
s_add_u32 s48, %[alo], s53
RA 216,1,0
HS 24,18
M 8,44,2
s_addc_u32 s49, %[ahi], 0
LL 17,76
LL 124,77
M 8,45,3
s_add_u32 s52, s52, %[d]
LA 17
v_and_b32 v22, 0xfffffcff, v1
M 9,56,2
s_cmp_eq_u32 s52, %[wrap]
LC_ 124
M 9,57,3
s_cselect_b32 s52, %[rst], s52
DQ 3072
M 10,68,2
WL 3
LO 121,16,121
LU 16,121
M 10,69,3
s_add_u32 s50, s50, %[d]
RA 220,1,1024
HS 24,16
M 11,80,2
LL 18,78
LL 125,79
M 11,81,3
s_cmp_eq_u32 s50, %[wrap]
LA 18
RV 252,22,5120
M 6,22,4
s_cselect_b32 s50, %[rst], s50
LC_ 125
M 6,23,5
WV 31
v_add_u32 v19, 1, v19
M 7,34,4
WL 4
LO 124,17,124
LU 17,124
M 7,35,5
s_add_u32 s38, s38, 32768
RA 224,1,2048
HS 24,17
M 8,46,4
s_cmp_eq_u32 s38, s77
LL 16,80
LL 106,81
M 8,47,5
s_cselect_b32 s38, s76, s38
LA 16
FP
M 9,58,4
s_sub_u32 s39, 0, s39
LC_ 106
M 9,59,5
M 10,70,4
WL 4
LO 125,18,125
LU 18,125
M 10,71,5
RA 228,1,3072
HS 24,18
M 11,82,4
LL 17,82
LL 107,83
M 11,83,5
LA 17
HX 24
Z 0,12,0
LC_ 107
M 0,13,1
Z 1,24,0
WL 3
LO 16,16,106
LU 106,16
M 1,25,1
RA 232,1,4096
HZ 25,106
Z 2,36,0
LL 18,84
LL 110,85
M 2,37,1
LA 18
PK 106,16,104,33
Z 3,48,0
LC_ 110
PK 104,16,104,32
M 3,49,1
WV 21
v_bcnt_u32_b32 v11, v11, 0
Z 4,60,0
WL 3
LO 17,17,107
LU 107,17
M 4,61,1
RA 236,1,6144
HS 25,107
Z 5,72,0
LL 16,86
LL 111,87
M 5,73,1
LA 16
PK 107,17,105,33
M 0,14,2
LC_ 111
PK 105,17,105,32
M 0,15,3
v_cmp_lt_u32 s[92:93], 2, v11
M 1,26,2
WL 3
LO 18,18,110
LU 110,18
M 1,27,3
s_nop 0
RA 216,1,7168
HS 25,110
M 2,38,2
s_add_u32 s42, s44, s60
LL 17,88
LL 114,89
M 2,39,3
s_addc_u32 s43, s45, 0
LA 17
PK 110,18,108,33
M 3,50,2
s_sub_u32 s53, 0, s96
LC_ 114
PK 108,18,108,32
M 3,51,3
s_xor_b32 s92, s92, s53
FR
PO 110
M 4,62,2
)"
R"(WL 4
LO 16,16,111
LU 111,16
M 4,63,3
s_xor_b32 s93, s93, s53
RA 220,1,8192
HS 25,111
M 5,74,2
LL 18,90
LL 115,91
M 5,75,3
s_mul_i32 s53, %[d], 0
LA 18
PK 111,16,109,33
M 0,16,4
s_add_u32 s53, s53, s57
LC_ 115
PK 109,16,109,32
M 0,17,5
s_add_u32 s46, %[xlo], s53
PO 111
M 1,28,4
WL 4
LO 17,17,114
LU 114,17
M 1,29,5
s_addc_u32 s47, %[xhi], 0
RA 224,1,9216
HS 25,114
s_bitcmp1_b64 s[92:93], 48
s_cselect_b32 s58, 16, 0
s_lshr_b32 s58, 0xfe987610, s58
v_lshrrev_b32 v3, 6, v0
v_and_b32 v3, 12, v3
v_bfe_u32 v3, s58, v3, 4
v_mad_u32_u24 v3, v3, s97, v21
M 2,40,4
LL 16,92
LL 118,93
ST 104
M 2,41,5
LA 16
PK 114,17,112,33
M 3,52,4
LC_ 118
PK 112,17,112,32
M 3,53,5
WL 6
XL 0,0
v_cmp_gt_u32 vcc, v19, v15
M 4,64,4
WL 3
LO 18,18,115
LU 115,18
M 4,65,5
s_add_u32 s42, s44, s61
RA 228,1,10240
HS 25,115
M 5,76,4
LL 17,94
LL 119,95
M 5,77,5
s_addc_u32 s43, s45, 0
LA 17
PK 115,18,113,33
M 0,18,0
s_mov_b32 m0, %[pm]
LC_ 119
PK 113,18,113,32
M 0,19,1
s_cbranch_vccnz .Lk1s5
.Lk1r5:
DF
M 1,30,0
WL 3
LO 16,16,118
LU 118,16
M 1,31,1
RA 232,1,11264
HS 25,118
M 2,42,0
LL 18,96
LL 122,97
M 2,43,1
LA 18
PK 118,16,116,33
M 3,54,0
LC_ 122
XL 1,64
PK 116,16,116,32
M 3,55,1
RV 252,22,5392
PO 118
M 4,66,0
WL 4
LO 17,17,119
LU 119,17
M 4,67,1
RA 236,1,12288
HS 25,119
M 5,78,0
LL 16,98
LL 123,99
ST 108
M 5,79,1
s_add_u32 s42, s44, s62
LA 16
PK 119,17,117,33
s_addc_u32 s43, s45, 0
M 0,20,2
LC_ 123
PK 117,17,117,32
M 0,21,3
W1 24,0
XL 2,128
PO 119
M 1,32,2
WL 5
LO 18,18,122
LU 122,18
M 1,33,3
RA 216,1,13312
HS 25,122
M 2,44,2
LL 17,100
LL 126,101
ST 112
M 2,45,3
LA 17
PK 122,18,120,33
M 3,56,2
LC_ 126
PK 120,18,120,32
M 3,57,3
XL 3,192
M 4,68,2
WL 4
LO 16,16,123
LU 123,16
M 4,69,3
s_add_u32 s42, s44, s63
RA 220,1,14336
HS 25,123
M 5,80,2
LL 18,102
LL 127,103
M 5,81,3
s_addc_u32 s43, s45, 0
LA 18
PK 123,16,121,33
M 0,22,4
LC_ 127
PK 121,16,121,32
M 0,23,5
XL 4,256
M 1,34,4
WL 3
LO 17,17,126
LU 126,17
M 1,35,5
RA 224,1,15360
HS 25,126
M 2,46,4
LL 16,56
LL 104,57
M 2,47,5
LA 16
PK 126,17,124,33
M 3,58,4
LC_ 104
PK 124,17,124,32
M 3,59,5
ST 116
PO 126
M 4,70,4
WL 3
LO 18,18,127
LU 127,18
M 4,71,5
s_add_u32 s42, s44, s64
RA 228,1,16384
HS 25,127
M 5,82,4
LL 17,58
LL 105,59
M 5,83,5
s_addc_u32 s43, s45, 0
LA 17
PK 127,18,125,33
Z 6,12,0
LC_ 105
XL 5,320
PK 125,18,125,32
M 6,13,1
ST 120
PO 127
Z 7,24,0
WL 3
LO 104,16,104
LU 16,104
M 7,25,1
RA 232,1,17408
HZ 24,16
Z 8,36,0
LL 18,60
LL 108,61
M 8,37,1
LA 18
ds_read_b64 v[14:15], v5 offset:0
Z 9,48,0
LC_ 108
XL 6,384
M 9,49,1
s_add_u32 s42, s44, s65
v_subrev_u32 v5, s39, v5
HX 25
Z 10,60,0
WL 4
LO 105,17,105
LU 17,105
M 10,61,1
s_addc_u32 s43, s45, 0
RA 236,1,18432
HS 24,17
Z 11,72,0
LL 16,62
LL 109,63
M 11,73,1
LA 16
W1 25,1024
M 6,14,2
LC_ 109
XL 7,448
M 6,15,3
s_mul_i32 s53, s50, 25600
v_add_u32 v4, s39, v4
M 7,26,2
WL 4
LO 108,18,108
LU 18,108
M 7,27,3
s_add_u32 s40, %[wlo], s53
RA 216,1,19456
HS 24,18
M 8,38,2
s_addc_u32 s41, %[whi], 0
LL 17,64
LL 112,65
M 8,39,3
s_mov_b32 m0, s38
LA 17
ST 124
M 9,50,2
LC_ 112
XL 8,512
M 9,51,3
DW 0
v_add_f32 v14, v14, v15
s_nop 1
v_add_f32_dpp v14, v14, v14 quad_perm:[1,0,3,2] row_mask:0xf bank_mask:0xf
M 10,62,2
WL 3
LO 109,16,109
LU 16,109
M 10,63,3
s_add_u32 s37, s37, 32768
RA 220,1,20480
HS 24,16
v_add_f32_dpp v14, v14, v14 quad_perm:[2,3,0,1] row_mask:0xf bank_mask:0xf
M 11,74,2
LL 18,66
LL 113,67
M 11,75,3
s_cmp_eq_u32 s37, s75
LA 18
v_add_f32_dpp v14, v14, v14 row_half_mirror row_mask:0xf bank_mask:0xf
M 6,16,4
s_cselect_b32 s37, s74, s37
LC_ 113
XL 9,576
M 6,17,5
DW 1024
v_cvt_f64_f32 v[14:15], v14
M 7,28,4
WL 3
LO 112,17,112
LU 17,112
M 7,29,5
RA 224,1,21504
HS 24,17
M 8,40,4
LL 16,68
LL 116,69
M 8,41,5
LA 16
s_mov_b64 exec, s[54:55]
SP
s_mov_b64 exec, -1
M 9,52,4
LC_ 116
DW 2048
M 9,53,5
XL 10,640
M 10,64,4
WL 3
LO 113,18,113
LU 18,113
M 10,65,5
s_mul_i32 s53, s51, 49152
RA 228,1,22528
HS 24,18
M 11,76,4
LL 17,70
LL 117,71
M 11,77,5
s_bitcmp1_b64 s[90:91], s51
LA 17
M 6,18,0
s_cselect_b64 s[44:45], s[84:85], s[82:83]
LC_ 117
M 6,19,1
s_add_u32 s44, s44, s53
M 7,30,0
WL 3
LO 116,16,116
LU 16,116
M 7,31,1
s_addc_u32 s45, s45, 0
RA 232,1,23552
HS 24,16
M 8,42,0
s_add_u32 s51, s51, %[d]
LL 18,72
LL 120,73
M 8,43,1
s_cmp_eq_u32 s51, %[wrap]
LA 18
s_cselect_b32 s51, %[rst], s51
M 9,54,0
s_add_u32 s40, s40, 3072
LC_ 120
s_addc_u32 s41, s41, 0
s_add_u32 m0, s38, 3072
M 9,55,1
XL 11,704
M 10,66,0
WL 3
LO 117,17,117
LU 17,117
M 10,67,1
RA 236,1,24576
HS 24,17
M 11,78,0
LL 16,74
LL 121,75
M 11,79,1
LA 16
DW 0
M 6,20,2
LC_ 121
DW 1024
M 6,21,3
DW 2048
v_add_u32 v1, s37, v0
M 7,32,2
WL 3
LO 120,18,120
LU 18,120
M 7,33,3
s_lshl_b32 s53, s52, 8
RA 216,1,0
HS 24,18
M 8,44,2
s_add_u32 s48, %[alo], s53
LL 17,76
LL 124,77
M 8,45,3
s_addc_u32 s49, %[ahi], 0
LA 17
v_and_b32 v22, 0xfffffcff, v1
M 9,56,2
s_add_u32 s52, s52, %[d]
LC_ 124
M 9,57,3
s_cmp_eq_u32 s52, %[wrap]
M 10,68,2
WL 3
LO 121,16,121
LU 16,121
M 10,69,3
s_cselect_b32 s52, %[rst], s52
RA 220,1,1024
HS 24,16
M 11,80,2
LL 18,78
LL 125,79
M 11,81,3
LA 18
RV 252,22,5120
M 6,22,4
)"
R"(LC_ 125
DQ 3072
M 6,23,5
s_add_u32 s50, s50, %[d]
M 7,34,4
WL 4
LO 124,17,124
LU 17,124
M 7,35,5
s_cmp_eq_u32 s50, %[wrap]
RA 224,1,2048
HS 24,17
M 8,46,4
s_cselect_b32 s50, %[rst], s50
LL 16,80
LL 106,81
M 8,47,5
LA 16
v_add_u32 v19, 1, v19
M 9,58,4
s_add_u32 s38, s38, 32768
LC_ 106
M 9,59,5
M 10,70,4
WL 3
LO 125,18,125
LU 18,125
M 10,71,5
RA 228,1,3072
HS 24,18
M 11,82,4
LL 17,82
LL 107,83
M 11,83,5
LA 17
HX 24
Z 0,12,0
s_cmp_eq_u32 s38, s77
LC_ 107
M 0,13,1
s_cselect_b32 s38, s76, s38
WV 42
FP
Z 1,24,0
WL 4
LO 16,16,106
LU 106,16
M 1,25,1
s_sub_u32 s39, 0, s39
RA 232,1,4096
HZ 25,106
Z 2,36,0
LL 18,84
LL 110,85
M 2,37,1
LA 18
PK 106,16,104,33
Z 3,48,0
LC_ 110
PK 104,16,104,32
M 3,49,1
Z 4,60,0
WL 4
LO 17,17,107
LU 107,17
M 4,61,1
s_add_u32 s42, s44, s60
RA 236,1,6144
HS 25,107
Z 5,72,0
LL 16,86
LL 111,87
M 5,73,1
s_addc_u32 s43, s45, 0
LA 16
PK 107,17,105,33
M 0,14,2
LC_ 111
PK 105,17,105,32
M 0,15,3
ST 104
M 1,26,2
WL 3
LO 18,18,110
LU 110,18
M 1,27,3
RA 216,1,7168
HS 25,110
M 2,38,2
LL 17,88
LL 114,89
M 2,39,3
LA 17
PK 110,18,108,33
M 3,50,2
LC_ 114
PK 108,18,108,32
M 3,51,3
FR
PO 110
M 4,62,2
WL 4
LO 16,16,111
LU 111,16
M 4,63,3
s_add_u32 s42, s44, s61
RA 220,1,8192
HS 25,111
M 5,74,2
LL 18,90
LL 115,91
M 5,75,3
s_addc_u32 s43, s45, 0
LA 18
PK 111,16,109,33
M 0,16,4
s_mov_b32 m0, %[pm]
LC_ 115
PK 109,16,109,32
M 0,17,5
DF
PO 111
M 1,28,4
WL 4
LO 17,17,114
LU 114,17
M 1,29,5
RA 224,1,9216
HS 25,114
M 2,40,4
LL 16,92
LL 118,93
M 2,41,5
LA 16
PK 114,17,112,33
M 3,52,4
LC_ 118
PK 112,17,112,32
M 3,53,5
WL 6
ST 108
v_cmp_gt_u32 vcc, v19, v15
M 4,64,4
WL 3
LO 18,18,115
LU 115,18
M 4,65,5
s_add_u32 s42, s44, s62
RA 228,1,10240
HS 25,115
M 5,76,4
LL 17,94
LL 119,95
M 5,77,5
s_cbranch_vccnz .Lk1s6
.Lk1r6:
LA 17
PK 115,18,113,33
M 0,18,0
s_addc_u32 s43, s45, 0
LC_ 119
PK 113,18,113,32
M 0,19,1
W1 24,0
M 1,30,0
WL 4
LO 16,16,118
LU 118,16
M 1,31,1
RA 232,1,11264
HS 25,118
M 2,42,0
LL 18,96
LL 122,97
M 2,43,1
LA 18
PK 118,16,116,33
M 3,54,0
LC_ 122
PK 116,16,116,32
M 3,55,1
RV 252,22,5392
PO 118
M 4,66,0
WL 5
LO 17,17,119
LU 119,17
M 4,67,1
RA 236,1,12288
HS 25,119
M 5,78,0
LL 16,98
LL 123,99
M 5,79,1
LA 16
PK 119,17,117,33
M 0,20,2
WV 28
LC_ 123
PK 117,17,117,32
BW 0
M 0,21,3
ST 112
PO 119
M 1,32,2
WL 5
RV 40,8,6144
LO 18,18,122
LU 122,18
M 1,33,3
s_add_u32 s42, s44, s63
RA 216,1,13312
RV 44,8,6208
HS 25,122
M 2,44,2
s_addc_u32 s43, s45, 0
LL 17,100
LL 126,101
M 2,45,3
LA 17
RV 48,8,6272
PK 122,18,120,33
M 3,56,2
LC_ 126
BR 0
PK 120,18,120,32
M 3,57,3
WV 25
ST 116
BW 1
M 4,68,2
WL 9
LO 16,16,123
LU 123,16
M 4,69,3
s_add_u32 s42, s44, s64
RA 220,1,14336
BR 1
HS 25,123
M 5,80,2
WL 3
LL 18,102
LL 127,103
M 5,81,3
s_addc_u32 s43, s45, 0
LA 18
PK 123,16,121,33
LZ 40,0
s_bitcmp1_b64 s[92:93], 48
BW 2
M 0,22,4
s_cselect_b32 s58, 16, 0
LC_ 127
PK 121,16,121,32
M 0,23,5
s_lshr_b32 s58, 0xda43cb52, s58
RV 40,8,6336
BR 2
M 1,34,4
LO 17,17,126
LU 126,17
M 1,35,5
WL 5
RA 224,1,15360
HS 25,126
LS 44,1
WV 24
BW 3
M 2,46,4
LL 16,56
LL 104,57
M 2,47,5
LA 16
PK 126,17,124,33
M 3,58,4
LC_ 104
RV 44,8,6400
PK 124,17,124,32
M 3,59,5
ds_read_b64 v[14:15], v5 offset:0
BR 3
PO 126
M 4,70,4
WL 9
LO 18,18,127
LU 127,18
M 4,71,5
WL 7
RA 228,1,16384
HS 25,127
LS 48,2
WV 23
BW 4
M 5,82,4
LL 17,58
LL 105,59
M 5,83,5
LA 17
RV 48,8,6464
PK 127,18,125,33
Z 6,12,0
LC_ 105
PK 125,18,125,32
M 6,13,1
BR 4
PO 127
v_and_b32 v11, 0x1f0, v0
v_min_u32 v11, 368, v11
v_lshlrev_b32 v11, 3, v11
Z 7,24,0
WL 9
LO 104,16,104
LU 16,104
M 7,25,1
WL 6
RA 232,1,17408
HZ 24,16
LS 40,3
WV 21
BW 5
Z 8,36,0
LL 18,60
LL 108,61
ST 120
M 8,37,1
LA 18
RV 40,8,6528
Z 9,48,0
LC_ 108
BR 5
M 9,49,1
WL 6
v_subrev_u32 v5, s39, v5
HX 25
LS 44,4
WV 20
BW 6
Z 10,60,0
LO 105,17,105
LU 17,105
v_lshrrev_b32 v3, 9, v0
v_lshl_add_u32 v3, v3, 2, 0
v_bfe_u32 v3, s58, v3, 4
v_mad_u32_u24 v3, v3, s97, v11
M 10,61,1
s_add_u32 s42, s44, s65
RA 236,1,18432
HS 24,17
Z 11,72,0
WV 19
RV 44,8,6592
LL 16,62
LL 109,63
M 11,73,1
s_addc_u32 s43, s45, 0
LA 16
W1 25,1024
BR 6
M 6,14,2
WL 6
LC_ 109
DY
M 6,15,3
s_mul_i32 s53, s50, 25600
v_add_f32 v14, v14, v15
v_add_u32 v4, s39, v4
LS 48,5
s_add_u32 s40, %[wlo], s53
BW 7
M 7,26,2
LO 108,18,108
LU 18,108
M 7,27,3
s_addc_u32 s41, %[whi], 0
RA 216,1,19456
HS 24,18
M 8,38,2
s_mov_b32 m0, s38
RV 48,8,6656
LL 17,64
LL 112,65
M 8,39,3
LA 17
ST 124
BR 7
M 9,50,2
LC_ 112
v_add_f32_dpp v14, v14, v14 quad_perm:[1,0,3,2] row_mask:0xf bank_mask:0xf
M 9,51,3
WL 7
v_add_f32_dpp v14, v14, v14 quad_perm:[2,3,0,1] row_mask:0xf bank_mask:0xf
v_lshrrev_b32 v3, 9, v0
LS 40,6
WV 19
BW 8
M 10,62,2
WL 7
LO 109,16,109
LU 16,109
M 10,63,3
)"
R"(RA 220,1,20480
RV 40,8,6720
HS 24,16
M 11,74,2
BR 8
LL 18,66
LL 113,67
M 11,75,3
WV 17
LA 18
v_add_f32_dpp v14, v14, v14 row_half_mirror row_mask:0xf bank_mask:0xf
v_lshl_add_u32 v3, v3, 2, 8
v_bfe_u32 v3, s58, v3, 4
v_mad_u32_u24 v3, v3, s97, v11
M 6,16,4
WL 6
LC_ 113
DW 0
M 6,17,5
s_add_u32 s37, s37, 32768
v_cvt_f64_f32 v[14:15], v14
LS 44,7
s_cmp_eq_u32 s37, s75
BW 9
M 7,28,4
WL 7
RV 44,8,6784
LO 112,17,112
LU 17,112
M 7,29,5
s_cselect_b32 s37, s74, s37
RA 224,1,21504
HS 24,17
M 8,40,4
BR 9
LL 16,68
LL 116,69
M 8,41,5
WL 6
LA 16
s_mov_b64 exec, s[54:55]
SP
s_mov_b64 exec, -1
LS 48,8
WV 15
BW 10
M 9,52,4
LC_ 116
DY
M 9,53,5
DW 1024
RV 48,8,6848
M 10,64,4
WL 8
LO 113,18,113
LU 18,113
M 10,65,5
RA 228,1,22528
RA 240,7,0
HS 24,18
M 11,76,4
WV 16
BR 10
LL 17,70
LL 117,71
M 11,77,5
WL 7
LA 17
DW 2048
M 6,18,0
s_mul_i32 s53, s51, 49152
LC_ 117
M 6,19,1
s_bitcmp1_b64 s[90:91], s51
LS 40,9
s_cselect_b64 s[44:45], s[84:85], s[82:83]
BW 11
M 7,30,0
WL 3
RA 244,7,256
LO 116,16,116
LU 16,116
M 7,31,1
s_add_u32 s44, s44, s53
RA 232,1,23552
HS 24,16
M 8,42,0
s_addc_u32 s45, s45, 0
BR 11
LL 18,72
LL 120,73
M 8,43,1
s_add_u32 s51, s51, %[d]
LA 18
s_cmp_eq_u32 s51, %[wrap]
s_cselect_b32 s51, %[rst], s51
LS 44,10
s_add_u32 s40, s40, 3072
RA 248,7,512
M 9,54,0
s_addc_u32 s41, s41, 0
LC_ 120
s_add_u32 m0, s38, 3072
M 9,55,1
DW 0
M 10,66,0
WL 7
LO 117,17,117
LU 17,117
M 10,67,1
WL 3
RA 236,1,24576
HS 24,17
LS 48,11
RA 252,7,768
M 11,78,0
LL 16,74
LL 121,75
s_nop 3
v_mul_f32 v36, 0xbb2aaaab, v36
M 11,79,1
LA 16
DW 1024
v_mul_f32 v37, 0xbb2aaaab, v37
M 6,20,2
LC_ 121
DW 2048
v_mul_f32 v38, 0xbb2aaaab, v38
v_mul_f32 v39, 0xbb2aaaab, v39
M 6,21,3
s_lshl_b32 s53, s52, 8
v_add_u32 v1, s37, v0
LK 40,240,0,0
LK 44,242,0,2
s_add_u32 s48, %[alo], s53
RA 240,7,1024
LK 48,244,1,0
s_nop 0
CF 128,40,41
LK 248,246,1,2
s_addc_u32 s49, %[ahi], 0
RA 244,7,1280
CF 129,42,43
M 7,32,2
WL 6
LO 120,18,120
LU 18,120
M 7,33,3
s_add_u32 s52, s52, %[d]
RA 216,1,0
HS 24,18
M 8,44,2
s_cmp_eq_u32 s52, %[wrap]
LL 17,76
LL 124,77
M 8,45,3
s_cselect_b32 s52, %[rst], s52
LA 17
DQ 3072
M 9,56,2
LC_ 124
v_and_b32 v22, 0xfffffcff, v1
CF 130,44,45
M 9,57,3
CF 131,46,47
LK 40,248,2,0
CF 132,48,49
LK 44,250,2,2
RA 248,7,1536
CF 133,50,51
GZ 0
WL 8
CF 134,248,249
CF 135,250,251
LK 48,252,3,0
CF 136,40,41
LK 248,254,3,2
RA 252,7,1792
CF 137,42,43
GS 1
WL 6
CF 138,44,45
CF 139,46,47
LK 40,240,4,0
CF 140,48,49
LK 44,242,4,2
RA 240,7,2048
CF 141,50,51
GS 2
WL 6
CF 142,248,249
CF 143,250,251
LK 48,244,5,0
CF 144,40,41
LK 248,246,5,2
RA 244,7,2304
CF 145,42,43
GS 3
CF 146,44,45
CF 147,46,47
M 10,68,2
WL 3
LO 121,16,121
LU 16,121
M 10,69,3
s_add_u32 s50, s50, %[d]
RA 220,1,1024
HS 24,16
LK 40,248,6,0
WL 3
CF 148,48,49
LK 44,250,6,2
s_cmp_eq_u32 s50, %[wrap]
RA 248,7,2560
CF 149,50,51
GS 4
s_cselect_b32 s50, %[rst], s50
CF 150,248,249
CF 151,250,251
LK 48,252,7,0
CF 152,40,41
LK 248,254,7,2
RA 252,7,2816
CF 153,42,43
GS 5
WL 4
CF 154,44,45
CF 155,46,47
LK 40,240,8,0
CF 156,48,49
LK 44,242,8,2
CF 157,50,51
GS 6
CF 158,248,249
CF 159,250,251
M 11,80,2
LL 18,78
LL 125,79
M 11,81,3
WL 3
LA 18
RV 252,22,5120
LK 48,244,9,0
CF 160,40,41
LK 248,246,9,2
CF 161,42,43
GS 7
CF 162,44,45
CF 163,46,47
M 6,22,4
LC_ 125
M 6,23,5
v_add_u32 v19, 1, v19
M 7,34,4
WL 4
LO 124,17,124
LU 17,124
M 7,35,5
s_add_u32 s38, s38, 32768
RA 224,1,2048
HS 24,17
M 8,46,4
s_cmp_eq_u32 s38, s77
LL 16,80
LL 106,81
M 8,47,5
s_cselect_b32 s38, s76, s38
LA 16
FP
LK 40,248,10,0
CF 164,48,49
LK 44,250,10,2
s_sub_u32 s39, 0, s39
CF 165,50,51
GS 8
CF 166,248,249
CF 167,250,251
M 9,58,4
LC_ 106
M 9,59,5
M 10,70,4
WL 4
LO 125,18,125
LU 18,125
M 10,71,5
RA 228,1,3072
HS 24,18
LK 48,252,11,0
CF 168,40,41
LK 248,254,11,2
CF 169,42,43
GS 9
CF 170,44,45
CF 171,46,47
M 11,82,4
LL 17,82
LL 107,83
M 11,83,5
LA 17
HX 24
Z 0,12,0
LC_ 107
RV 36,8,6912
M 0,13,1
GS 10
CF 172,48,49
Z 1,24,0
WL 4
LO 16,16,106
LU 106,16
M 1,25,1
RA 232,1,4096
HZ 25,106
Z 2,36,0
LL 18,84
LL 110,85
M 2,37,1
LA 18
PK 106,16,104,33
Z 3,48,0
LC_ 110
PK 104,16,104,32
M 3,49,1
CF 173,50,51
CF 174,248,249
Z 4,60,0
WL 4
LO 17,17,107
LU 107,17
M 4,61,1
s_add_u32 s42, s44, s60
RA 236,1,6144
HS 25,107
Z 5,72,0
LL 16,86
LL 111,87
M 5,73,1
s_addc_u32 s43, s45, 0
LA 16
PK 107,17,105,33
M 0,14,2
LC_ 111
PK 105,17,105,32
M 0,15,3
ST 104
CF 175,250,251
M 1,26,2
WL 3
LO 18,18,110
LU 110,18
M 1,27,3
RA 216,1,7168
HS 25,110
GS 11
M 2,38,2
LL 17,88
LL 114,89
M 2,39,3
LA 17
PK 110,18,108,33
M 3,50,2
LC_ 114
PK 108,18,108,32
M 3,51,3
FR
PO 110
M 4,62,2
WL 4
LO 16,16,111
LU 111,16
M 4,63,3
s_add_u32 s42, s44, s61
RA 220,1,8192
HS 25,111
M 5,74,2
LL 18,90
LL 115,91
M 5,75,3
s_addc_u32 s43, s45, 0
LA 18
PK 111,16,109,33
M 0,16,4
s_mov_b32 m0, %[pm]
LC_ 115
PK 109,16,109,32
M 0,17,5
DF
PO 111
M 1,28,4
WL 4
LO 17,17,114
LU 114,17
M 1,29,5
RA 224,1,9216
HS 25,114
M 2,40,4
LL 16,92
LL 118,93
M 2,41,5
LA 16
)"
R"(PK 114,17,112,33
M 3,52,4
LC_ 118
PK 112,17,112,32
M 3,53,5
WL 6
ST 108
v_cmp_gt_u32 vcc, v19, v15
M 4,64,4
WL 3
LO 18,18,115
LU 115,18
M 4,65,5
s_add_u32 s42, s44, s62
RA 228,1,10240
HS 25,115
M 5,76,4
LL 17,94
LL 119,95
v_cndmask_b32 v52, v52, v53, s[78:79]
M 5,77,5
s_cbranch_vccnz .Lk1s7
.Lk1r7:
LA 17
PK 115,18,113,33
M 0,18,0
s_addc_u32 s43, s45, 0
LC_ 119
PK 113,18,113,32
v_cndmask_b32 v54, v54, v55, s[78:79]
M 0,19,1
W1 24,0
v_cndmask_b32 v52, v52, v54, s[80:81]
M 1,30,0
WL 4
LO 16,16,118
LU 118,16
v_mul_f32 v52, 0x3b2aaaab, v52
M 1,31,1
RA 232,1,11264
HS 25,118
v_med3_f32 v53, v52, v39, v37
M 2,42,0
LL 18,96
LL 122,97
LG 52,53
M 2,43,1
LA 18
PK 118,16,116,33
C 52,52,52
M 3,54,0
LC_ 122
PK 116,16,116,32
v_lshlrev_b32 v52, 16, v52
M 3,55,1
RV 252,22,5392
PO 118
v_add_f32 v52, v36, v52
M 4,66,0
WL 5
LO 17,17,119
LU 119,17
C 52,52,52
M 4,67,1
RA 236,1,12288
HS 25,119
M 5,78,0
LL 16,98
LL 123,99
v_lshlrev_b32 v52, 16, v52
M 5,79,1
LA 16
PK 119,17,117,33
v_sqrt_f32 v52, v52
M 0,20,2
LC_ 123
PK 117,17,117,32
C 52,52,52
M 0,21,3
ST 112
PO 119
v_lshlrev_b32 v52, 16, v52
M 1,32,2
WL 4
LO 18,18,122
LU 122,18
v_rcp_f32 v52, v52
M 1,33,3
s_add_u32 s42, s44, s63
RA 216,1,13312
HS 25,122
v_mul_f32 v52, v38, v52
M 2,44,2
s_addc_u32 s43, s45, 0
LL 17,100
LL 126,101
CF 52,52,52
M 2,45,3
LA 17
PK 122,18,120,33
ds_bpermute_b32 v13, v28, v52
M 3,56,2
LC_ 126
PK 120,18,120,32
M 3,57,3
ST 116
M 4,68,2
WL 4
LO 16,16,123
LU 123,16
M 4,69,3
s_add_u32 s42, s44, s64
RA 220,1,14336
HS 25,123
M 5,80,2
LL 18,102
LL 127,103
M 5,81,3
s_addc_u32 s43, s45, 0
LA 18
PK 123,16,121,33
M 0,22,4
LC_ 127
PK 121,16,121,32
M 0,23,5
ST 120
M 1,34,4
WL 3
LO 17,17,126
LU 126,17
M 1,35,5
RA 224,1,15360
HS 25,126
XS 128
M 2,46,4
LL 16,56
LL 104,57
M 2,47,5
LA 16
PK 126,17,124,33
M 3,58,4
LC_ 104
PK 124,17,124,32
XS 129
M 3,59,5
ds_read_b64 v[14:15], v5 offset:0
PO 126
M 4,70,4
WL 4
LO 18,18,127
LU 127,18
M 4,71,5
s_add_u32 s42, s44, s65
RA 228,1,16384
HS 25,127
XS 130
M 5,82,4
LL 17,58
LL 105,59
M 5,83,5
s_addc_u32 s43, s45, 0
LA 17
PK 127,18,125,33
Z 6,12,0
LC_ 105
PK 125,18,125,32
XS 131
M 6,13,1
PO 127
v_subrev_u32 v5, s39, v5
Z 7,24,0
WL 4
LO 104,16,104
LU 16,104
XS 132
M 7,25,1
RA 232,1,17408
HZ 24,16
Z 8,36,0
WL 4
LL 18,60
LL 108,61
M 8,37,1
s_mul_i32 s53, s50, 25600
LA 18
v_add_f32 v14, v14, v15
XS 133
Z 9,48,0
s_add_u32 s40, %[wlo], s53
LC_ 108
v_add_f32_dpp v14, v14, v14 quad_perm:[1,0,3,2] row_mask:0xf bank_mask:0xf
M 9,49,1
s_addc_u32 s41, %[whi], 0
v_add_f32_dpp v14, v14, v14 quad_perm:[2,3,0,1] row_mask:0xf bank_mask:0xf
HX 25
Z 10,60,0
WL 3
LO 105,17,105
LU 17,105
XS 134
M 10,61,1
s_mov_b32 m0, s38
RA 236,1,18432
HS 24,17
Z 11,72,0
LL 16,62
LL 109,63
M 11,73,1
LA 16
W1 25,1024
XS 135
M 6,14,2
LC_ 109
ST 124
M 6,15,3
DW 0
v_add_f32_dpp v14, v14, v14 row_half_mirror row_mask:0xf bank_mask:0xf
v_min_u32 v35, v35, v13
M 7,26,2
WL 4
LO 108,18,108
LU 18,108
M 7,27,3
s_add_u32 s37, s37, 32768
RA 216,1,19456
HS 24,18
M 8,38,2
s_cmp_eq_u32 s37, s75
LL 17,64
LL 112,65
XS 136
M 8,39,3
s_cselect_b32 s37, s74, s37
LA 17
v_cvt_f64_f32 v[14:15], v14
M 9,50,2
s_mul_i32 s53, %[d], 0
LC_ 112
s_mov_b64 exec, s[54:55]
SP
s_mov_b64 exec, -1
M 9,51,3
s_add_u32 s53, s53, s57
v_add_u32 v4, s39, v4
XS 137
M 10,62,2
WL 3
LO 109,16,109
LU 16,109
M 10,63,3
s_add_u32 s46, %[xlo], s53
RA 220,1,20480
HS 24,16
M 11,74,2
LL 18,66
LL 113,67
XS 138
M 11,75,3
s_addc_u32 s47, %[xhi], 0
LA 18
XS 139
M 6,16,4
s_bitcmp1_b64 s[92:93], 48
LC_ 113
XS 140
M 6,17,5
s_cselect_b32 s58, 16, 0
XS 141
XS 142
M 7,28,4
WL 3
LO 112,17,112
LU 17,112
M 7,29,5
s_lshr_b32 s58, 0xda43cb52, s58
RA 224,1,21504
HS 24,17
M 8,40,4
LL 16,68
LL 116,69
M 8,41,5
LA 16
v_lshrrev_b32 v3, 6, v0
M 9,52,4
LC_ 116
v_and_b32 v3, 12, v3
v_bfe_u32 v3, s58, v3, 4
v_mad_u32_u24 v3, v3, s97, v21
M 9,53,5
DW 1024
XS 143
M 10,64,4
WL 3
LO 113,18,113
LU 18,113
M 10,65,5
RA 228,1,22528
HS 24,18
M 11,76,4
LL 17,70
LL 117,71
M 11,77,5
LA 17
XL 12,0
M 6,18,0
LC_ 117
DW 2048
M 6,19,1
XL 13,64
XS 144
M 7,30,0
WL 3
LO 116,16,116
LU 16,116
M 7,31,1
s_mul_i32 s53, s51, 49152
RA 232,1,23552
HS 24,16
M 8,42,0
s_bitcmp1_b64 s[90:91], s51
LL 18,72
LL 120,73
M 8,43,1
s_cselect_b64 s[44:45], s[84:85], s[82:83]
LA 18
s_add_u32 s44, s44, s53
s_addc_u32 s45, s45, 0
s_add_u32 s51, s51, %[d]
s_cmp_eq_u32 s51, %[wrap]
s_cselect_b32 s51, %[rst], s51
XS 145
M 9,54,0
s_add_u32 s40, s40, 3072
LC_ 120
s_addc_u32 s41, s41, 0
s_add_u32 m0, s38, 3072
M 9,55,1
XL 14,128
XS 146
M 10,66,0
WL 3
LO 117,17,117
LU 17,117
M 10,67,1
RA 236,1,24576
HS 24,17
M 11,78,0
LL 16,74
LL 121,75
XS 147
M 11,79,1
LA 16
DW 0
M 6,20,2
LC_ 121
XL 15,192
M 6,21,3
DW 1024
v_add_u32 v1, s37, v0
XS 148
M 7,32,2
WL 3
LO 120,18,120
LU 18,120
M 7,33,3
RA 216,1,0
HS 24,18
M 8,44,2
LL 17,76
LL 124,77
XS 149
M 8,45,3
LA 17
XL 16,256
M 9,56,2
LC_ 124
DW 2048
XS 150
M 9,57,3
XL 17,320
v_and_b32 v22, 0xfffffcff, v1
M 10,68,2
WL 3
LO 121,16,121
LU 16,121
M 10,69,3
s_lshl_b32 s53, s52, 8
RA 220,1,1024
HS 24,16
XS 151
M 11,80,2
LL 18,78
LL 125,79
M 11,81,3
s_add_u32 s48, %[alo], s53
LA 18
RV 252,22,5120
M 6,22,4
s_addc_u32 s49, %[ahi], 0
LC_ 125
)"
R"(XS 152
M 6,23,5
s_add_u32 s52, s52, %[d]
XS 153
XS 154
M 7,34,4
WL 4
LO 124,17,124
LU 17,124
M 7,35,5
s_cmp_eq_u32 s52, %[wrap]
RA 224,1,2048
HS 24,17
M 8,46,4
s_cselect_b32 s52, %[rst], s52
LL 16,80
LL 106,81
M 8,47,5
LA 16
DQ 3072
M 9,58,4
LC_ 106
XL 18,384
M 9,59,5
s_add_u32 s50, s50, %[d]
XS 155
XS 156
M 10,70,4
WL 3
LO 125,18,125
LU 18,125
M 10,71,5
RA 228,1,3072
HS 24,18
M 11,82,4
LL 17,82
LL 107,83
M 11,83,5
LA 17
HX 24
s_cmp_eq_u32 s50, %[wrap]
s_cselect_b32 s50, %[rst], s50
XL 19,448
Z 2,36,0
WV 40
LC_ 107
XL 20,512
v_add_u32 v19, 1, v19
s_add_u32 s38, s38, 32768
s_cmp_eq_u32 s38, s77
s_cselect_b32 s38, s76, s38
FP
s_sub_u32 s39, 0, s39
M 2,37,1
XL 21,576
XS 157
Z 3,48,0
WL 4
LO 16,16,106
LU 106,16
M 3,49,1
RA 232,1,4096
HZ 25,106
Z 4,60,0
LL 18,84
LL 110,85
XS 158
M 4,61,1
LA 18
PK 106,16,104,33
Z 5,72,0
WL 5
LC_ 110
XL 22,640
PK 104,16,104,32
M 5,73,1
RA 236,1,6144
XS 159
M 2,38,2
WL 5
LO 17,17,107
LU 107,17
M 2,39,3
FR
HS 25,107
M 3,50,2
LL 16,86
LL 111,87
XS 160
M 3,51,3
LA 16
PK 107,17,105,33
M 4,62,2
LC_ 111
XL 23,704
PK 105,17,105,32
XS 161
M 4,63,3
s_add_u32 s42, s44, s60
RA 216,1,7168
XS 162
M 5,74,2
WL 4
LO 18,18,110
LU 110,18
M 5,75,3
s_addc_u32 s43, s45, 0
RA 220,1,8192
HS 25,110
M 2,40,4
LL 17,88
LL 114,89
M 2,41,5
LA 17
PK 110,18,108,33
M 3,52,4
LC_ 114
PK 108,18,108,32
XS 163
M 3,53,5
RA 224,1,9216
PO 110
M 4,64,4
WL 5
LO 16,16,111
LU 111,16
ST 104
XS 164
M 4,65,5
s_add_u32 s42, s44, s61
HS 25,111
v_cmp_gt_u32 vcc, v19, v15
M 5,76,4
WL 3
LL 18,90
LL 115,91
M 5,77,5
s_addc_u32 s43, s45, 0
LA 18
PK 111,16,109,33
XS 165
M 2,42,0
s_cbranch_vccnz .Lk1s8
.Lk1r8:
LC_ 115
PK 109,16,109,32
M 2,43,1
WV 17
RA 228,1,10240
PO 111
BW 12
M 3,54,0
WL 5
LO 17,17,114
LU 114,17
XS 166
M 3,55,1
RA 232,1,11264
RV 40,8,6144
HS 25,114
M 4,66,0
RV 44,8,6208
LL 16,92
LL 118,93
M 4,67,1
LA 16
RV 48,8,6272
PK 114,17,112,33
XS 167
M 5,78,0
WL 6
LC_ 118
BR 12
PK 112,17,112,32
M 5,79,1
WV 15
RV 252,22,5392
ds_read_b64 v[14:15], v5 offset:0
W1 24,0
BW 13
XS 168
M 2,44,2
BR 13
LO 18,18,115
LU 115,18
M 2,45,3
WL 5
RA 236,1,12288
HS 25,115
LZ 40,12
WV 14
BW 14
M 3,56,2
LL 17,94
LL 119,95
XS 169
M 3,57,3
LA 17
RV 40,8,6336
PK 115,18,113,33
M 4,68,2
LC_ 119
BR 14
PK 113,18,113,32
M 4,69,3
WL 6
RA 216,1,13312
v_subrev_u32 v5, s39, v5
XS 170
LS 44,13
WV 12
BW 15
M 5,80,2
WL 7
LO 16,16,118
LU 118,16
ST 108
M 5,81,3
s_add_u32 s42, s44, s62
RA 220,1,14336
HS 25,118
s_addc_u32 s43, s45, 0
RV 44,8,6400
M 2,46,4
BR 15
LL 18,96
LL 122,97
XS 171
M 2,47,5
WL 5
LA 18
PK 118,16,116,33
LS 48,14
WV 11
BW 16
M 3,58,4
LC_ 122
PK 116,16,116,32
XS 172
M 3,59,5
RA 224,1,15360
RV 48,8,6464
PO 118
M 4,70,4
LO 17,17,119
LU 119,17
ST 112
M 4,71,5
s_add_u32 s42, s44, s63
BR 16
HS 25,119
XS 173
M 5,82,4
WL 6
LL 16,98
LL 123,99
M 5,83,5
s_addc_u32 s43, s45, 0
LA 16
PK 119,17,117,33
LS 40,15
WV 10
BW 17
Z 8,36,0
LC_ 123
PK 117,17,117,32
v_add_f32 v14, v14, v15
s_nop 1
v_add_f32_dpp v14, v14, v14 quad_perm:[1,0,3,2] row_mask:0xf bank_mask:0xf
s_nop 1
v_add_f32_dpp v14, v14, v14 quad_perm:[2,3,0,1] row_mask:0xf bank_mask:0xf
s_nop 1
v_add_f32_dpp v14, v14, v14 row_half_mirror row_mask:0xf bank_mask:0xf
v_cvt_f64_f32 v[14:15], v14
s_mov_b64 exec, s[54:55]
SP
s_mov_b64 exec, -1
RV 40,8,6528
XS 174
M 8,37,1
WV 9
RA 228,1,16384
BR 17
PO 119
Z 9,48,0
WL 6
LO 18,18,122
LU 122,18
XS 175
M 9,49,1
s_mul_i32 s53, %[d], 196608
RA 232,1,17408
HS 25,122
LS 44,16
s_add_u32 s53, s53, s57
BW 18
Z 10,60,0
s_add_u32 s46, %[xlo], s53
RV 44,8,6592
LL 17,100
LL 126,101
M 10,61,1
s_addc_u32 s47, %[xhi], 0
LA 17
BR 18
PK 122,18,120,33
Z 0,72,0
WL 5
LC_ 126
PK 120,18,120,32
M 0,73,1
s_bitcmp1_b64 s[92:93], 48
RA 236,1,18432
LS 48,17
s_cselect_b32 s58, 16, 0
WV 8
BW 19
M 8,38,2
RV 48,8,6656
LO 16,16,123
LU 123,16
M 8,39,3
s_lshr_b32 s58, 0xfe987610, s58
BR 19
HS 25,123
v_lshrrev_b32 v3, 6, v0
v_and_b32 v3, 12, v3
v_bfe_u32 v3, s58, v3, 4
v_mad_u32_u24 v3, v3, s97, v21
M 9,50,2
s_mul_i32 s53, s50, 25600
LL 18,102
LL 127,103
s_add_u32 s40, %[wlo], s53
s_addc_u32 s41, %[whi], 0
s_mov_b32 m0, s38
M 9,51,3
WL 5
LA 18
PK 123,16,121,33
LS 40,18
WV 7
BW 20
M 10,62,2
LC_ 127
RV 40,8,6720
PK 121,16,121,32
M 10,63,3
WV 6
RA 216,1,19456
BR 20
DW 0
M 0,74,2
WL 6
LO 17,17,126
LU 126,17
ST 116
M 0,75,3
s_add_u32 s42, s44, s64
RA 220,1,20480
HS 25,126
s_addc_u32 s43, s45, 0
LS 44,19
s_add_u32 s37, s37, 32768
BW 21
M 8,40,4
s_cmp_eq_u32 s37, s75
LL 16,64
LL 112,65
s_cselect_b32 s37, s74, s37
M 8,41,5
LA 16
XL 24,0
DW 1024
RV 44,8,6784
PK 126,17,124,33
M 9,52,4
LC_ 112
BR 21
PK 124,17,124,32
M 9,53,5
WL 6
RA 224,1,21504
PO 126
LS 48,20
WV 9
BW 22
M 10,64,4
LO 18,18,127
LU 127,18
ST 120
M 10,65,5
WV 9
DW 2048
RV 48,8,6848
HS 25,127
M 0,76,4
WL 8
RA 240,7,0
LL 17,66
)"
R"(LL 113,67
M 0,77,5
s_add_u32 s40, s40, 3072
LA 17
BR 22
PK 127,18,125,33
M 8,42,0
s_addc_u32 s41, s41, 0
LC_ 113
PK 125,18,125,32
s_add_u32 m0, s38, 3072
M 8,43,1
WL 7
RA 228,1,22528
XL 25,64
PO 127
LS 40,21
s_add_u32 s42, s44, s65
BW 23
M 9,54,0
RA 244,7,256
LO 112,16,112
LU 16,112
M 9,55,1
s_addc_u32 s43, s45, 0
RA 232,1,23552
BR 23
HZ 24,16
M 10,66,0
WL 6
DW 0
LL 18,68
LL 116,69
M 10,67,1
s_lshl_b32 s53, s52, 8
LA 18
HX 25
LS 44,22
s_add_u32 s48, %[alo], s53
RA 248,7,512
M 0,78,0
WL 2
LC_ 116
W1 25,1024
M 0,79,1
s_addc_u32 s49, %[ahi], 0
RA 236,1,24576
v_add_u32 v4, s39, v4
LS 48,23
s_add_u32 s52, s52, %[d]
RA 252,7,768
M 8,44,2
LO 113,17,113
LU 17,113
s_cmp_eq_u32 s52, %[wrap]
s_cselect_b32 s52, %[rst], s52
DW 1024
v_mul_f32 v36, 0xbb2aaaab, v36
M 8,45,3
XL 26,128
HS 24,17
v_mul_f32 v37, 0xbb2aaaab, v37
M 9,56,2
LL 16,70
LL 117,71
v_mul_f32 v38, 0xbb2aaaab, v38
v_mul_f32 v39, 0xbb2aaaab, v39
M 9,57,3
LA 16
DW 2048
LK 40,240,12,0
LK 44,242,12,2
RA 240,7,1024
v_add_u32 v1, s37, v0
LK 48,244,13,0
s_nop 0
CF 176,40,41
LK 248,246,13,2
RA 244,7,1280
CF 177,42,43
M 10,68,2
LC_ 117
RA 216,1,0
M 10,69,3
ST 124
v_and_b32 v22, 0xfffffcff, v1
M 0,80,2
WL 6
LO 116,18,116
LU 18,116
M 0,81,3
s_mul_i32 s53, s51, 49152
RA 220,1,1024
HS 24,18
s_bitcmp1_b64 s[90:91], s51
s_cselect_b64 s[44:45], s[84:85], s[82:83]
s_add_u32 s44, s44, s53
s_addc_u32 s45, s45, 0
s_add_u32 s51, s51, %[d]
s_cmp_eq_u32 s51, %[wrap]
s_cselect_b32 s51, %[rst], s51
DQ 3072
CF 178,44,45
M 8,46,4
LL 17,72
LL 120,73
CF 179,46,47
M 8,47,5
LA 17
RV 252,22,5120
LK 40,248,14,0
CF 180,48,49
LK 44,250,14,2
RA 248,7,1536
CF 181,50,51
GZ 12
WL 9
CF 182,248,249
CF 183,250,251
LK 48,252,15,0
CF 184,40,41
LK 248,254,15,2
RA 252,7,1792
CF 185,42,43
GS 13
WL 8
CF 186,44,45
CF 187,46,47
LK 40,240,16,0
CF 188,48,49
LK 44,242,16,2
RA 240,7,2048
CF 189,50,51
GS 14
WL 8
CF 190,248,249
CF 191,250,251
LK 48,244,17,0
CF 192,40,41
LK 248,246,17,2
RA 244,7,2304
CF 193,42,43
GS 15
CF 194,44,45
CF 195,46,47
M 9,58,4
WL 3
LC_ 120
XL 27,192
M 9,59,5
s_add_u32 s50, s50, %[d]
RA 224,1,2048
s_cmp_eq_u32 s50, %[wrap]
s_cselect_b32 s50, %[rst], s50
v_add_u32 v19, 1, v19
LK 40,248,18,0
WL 4
CF 196,48,49
LK 44,250,18,2
s_add_u32 s38, s38, 32768
RA 248,7,2560
CF 197,50,51
GS 16
s_cmp_eq_u32 s38, s77
CF 198,248,249
CF 199,250,251
LK 48,252,19,0
CF 200,40,41
LK 248,254,19,2
s_cselect_b32 s38, s76, s38
RA 252,7,2816
CF 201,42,43
GS 17
WL 5
CF 202,44,45
CF 203,46,47
LK 40,240,20,0
CF 204,48,49
LK 44,242,20,2
CF 205,50,51
GS 18
CF 206,248,249
CF 207,250,251
M 10,70,4
LO 117,16,117
LU 16,117
M 10,71,5
FP
HS 24,16
M 0,82,4
LL 18,74
LL 121,75
M 0,83,5
s_sub_u32 s39, 0, s39
LA 18
RA 228,1,3072
Z 2,36,0
LC_ 121
XL 28,256
M 2,37,1
WL 8
LK 48,244,21,0
CF 208,40,41
LK 248,246,21,2
CF 209,42,43
GS 19
CF 210,44,45
CF 211,46,47
Z 3,48,0
WL 7
LO 120,17,120
LU 17,120
M 3,49,1
WL 5
RA 232,1,4096
HS 24,17
LK 40,248,22,0
CF 212,48,49
LK 44,250,22,2
CF 213,50,51
GS 20
CF 214,248,249
CF 215,250,251
Z 4,60,0
LL 16,76
LL 124,77
M 4,61,1
LA 16
FR
Z 6,72,0
WL 4
LC_ 124
XL 29,320
M 6,73,1
RA 236,1,6144
LK 48,252,23,0
CF 216,40,41
LK 248,254,23,2
CF 217,42,43
GS 21
CF 218,44,45
CF 219,46,47
M 2,38,2
WL 5
LO 121,18,121
LU 18,121
M 2,39,3
RV 36,8,6912
HS 24,18
M 3,50,2
LL 17,78
LL 125,79
M 3,51,3
LA 17
RA 216,1,7168
GS 22
M 4,62,2
WL 5
LC_ 125
v_cmp_gt_u32 vcc, v19, v15
M 4,63,3
XL 30,384
CF 220,48,49
M 6,74,2
WL 4
LO 124,16,124
LU 16,124
M 6,75,3
RA 220,1,8192
HS 24,16
M 2,40,4
s_cbranch_vccnz .Lk1s9
.Lk1r9:
LL 18,88
LL 114,89
M 2,41,5
LA 18
ds_read_b64 v[14:15], v5 offset:0
M 3,52,4
LC_ 114
v_subrev_u32 v5, s39, v5
M 3,53,5
RA 224,1,9216
CF 221,50,51
M 4,64,4
WL 5
LO 125,17,125
LU 17,125
M 4,65,5
XL 31,448
HS 24,17
M 6,76,4
WL 4
LL 16,90
LL 115,91
M 6,77,5
LA 16
RA 228,1,10240
M 2,42,0
LC_ 115
HX 24
M 2,43,1
W1 24,0
CF 222,248,249
M 3,54,0
WL 5
LO 18,18,114
LU 114,18
M 3,55,1
RA 232,1,11264
HZ 25,114
M 4,66,0
LL 17,92
LL 118,93
M 4,67,1
LA 17
PK 114,18,112,33
M 6,78,0
WL 4
LC_ 118
PK 112,18,112,32
M 6,79,1
RV 252,22,5392
v_add_f32 v14, v14, v15
M 2,44,2
WL 5
LO 16,16,115
LU 115,16
M 2,45,3
RA 236,1,12288
HS 25,115
M 3,56,2
LL 18,94
LL 119,95
M 3,57,3
LA 18
PK 115,16,113,33
M 4,68,2
LC_ 119
XL 32,512
PK 113,16,113,32
v_add_f32_dpp v14, v14, v14 quad_perm:[1,0,3,2] row_mask:0xf bank_mask:0xf
M 4,69,3
s_add_u32 s42, s44, s62
RA 216,1,13312
v_add_f32_dpp v14, v14, v14 quad_perm:[2,3,0,1] row_mask:0xf bank_mask:0xf
M 6,80,2
WL 3
LO 17,17,118
LU 118,17
CF 223,250,251
M 6,81,3
s_addc_u32 s43, s45, 0
RA 220,1,14336
HS 25,118
GS 23
ST 112
v_add_f32_dpp v14, v14, v14 row_half_mirror row_mask:0xf bank_mask:0xf
s_nop 5
v_cndmask_b32 v52, v52, v53, s[78:79]
M 2,46,4
LL 16,96
LL 122,97
v_cndmask_b32 v54, v54, v55, s[78:79]
M 2,47,5
LA 16
PK 118,17,116,33
v_cndmask_b32 v52, v52, v54, s[80:81]
M 3,58,4
LC_ 122
PK 116,17,116,32
v_cvt_f64_f32 v[14:15], v14
v_mul_f32 v52, 0x3b2aaaab, v52
M 3,59,5
RA 224,1,15360
PO 118
v_med3_f32 v53, v52, v39, v37
M 4,70,4
WL 5
LO 18,18,119
LU 119,18
s_mov_b64 exec, s[54:55]
SP
s_mov_b64 exec, -1
XL 33,576
LG 52,53
s_nop 0
C 52,52,52
M 4,71,5
s_add_u32 s42, s44, s63
HS 25,119
v_lshlrev_b32 v52, 16, v52
M 6,82,4
WL 3
LL 17,98
LL 123,99
v_add_f32 v52, v36, v52
M 6,83,5
s_addc_u32 s43, s45, 0
LA 17
)"
R"(PK 119,18,117,33
C 52,52,52
Z 8,36,0
LC_ 123
PK 117,18,117,32
v_lshlrev_b32 v52, 16, v52
M 8,37,1
RA 228,1,16384
PO 119
v_sqrt_f32 v52, v52
s_nop 0
C 52,52,52
Z 9,48,0
WL 4
LO 16,16,122
LU 122,16
v_lshlrev_b32 v52, 16, v52
M 9,49,1
RA 232,1,17408
HS 25,122
v_rcp_f32 v52, v52
Z 10,60,0
XL 34,640
LL 18,56
LL 126,57
v_mul_f32 v52, v38, v52
M 10,61,1
LA 18
PK 122,16,120,33
CF 52,52,52
Z 11,72,0
WL 2
LC_ 126
PK 120,16,120,32
ds_bpermute_b32 v13, v28, v52
M 11,73,1
RA 236,1,18432
M 8,38,2
LO 17,17,123
LU 123,17
M 8,39,3
ST 116
HS 25,123
M 9,50,2
s_mul_i32 s53, s50, 25600
LL 16,58
LL 127,59
s_add_u32 s40, %[wlo], s53
s_addc_u32 s41, %[whi], 0
s_mov_b32 m0, s38
M 9,51,3
LA 16
PK 123,17,121,33
M 10,62,2
LC_ 127
PK 121,17,121,32
M 10,63,3
RA 216,1,19456
XL 35,704
DW 0
M 11,74,2
WL 3
LO 18,18,126
LU 126,18
M 11,75,3
s_add_u32 s42, s44, s64
RA 220,1,20480
HS 25,126
s_addc_u32 s43, s45, 0
XS 176
M 8,40,4
s_add_u32 s37, s37, 32768
LL 17,64
LL 112,65
s_cmp_eq_u32 s37, s75
s_cselect_b32 s37, s74, s37
M 8,41,5
LA 17
DW 1024
PK 126,18,124,33
XS 177
M 9,52,4
LC_ 112
PK 124,18,124,32
M 9,53,5
RA 224,1,21504
PO 126
XS 178
M 10,64,4
WL 5
LO 16,16,127
LU 127,16
ST 120
M 10,65,5
DW 2048
HS 25,127
XS 179
M 11,76,4
WL 3
LL 18,66
LL 113,67
M 11,77,5
s_add_u32 s40, s40, 3072
LA 18
PK 127,16,125,33
XS 180
M 8,42,0
s_addc_u32 s41, s41, 0
LC_ 113
PK 125,16,125,32
s_add_u32 m0, s38, 3072
M 8,43,1
s_add_u32 s42, s44, s65
RA 228,1,22528
PO 127
XS 181
M 9,54,0
WL 4
LO 112,17,112
LU 17,112
M 9,55,1
s_addc_u32 s43, s45, 0
RA 232,1,23552
HZ 24,17
XS 182
M 10,66,0
DW 0
LL 16,68
LL 116,69
M 10,67,1
s_lshl_b32 s53, s52, 8
LA 16
HX 25
XS 183
M 11,78,0
WL 2
LC_ 116
W1 25,1024
M 11,79,1
s_add_u32 s48, %[alo], s53
RA 236,1,24576
v_add_u32 v4, s39, v4
v_min_u32 v35, v35, v13
M 8,44,2
LO 113,18,113
LU 18,113
s_addc_u32 s49, %[ahi], 0
s_add_u32 s52, s52, %[d]
s_cmp_eq_u32 s52, %[wrap]
s_cselect_b32 s52, %[rst], s52
DW 1024
M 8,45,3
ST 124
HS 24,18
XS 184
M 9,56,2
s_mul_i32 s53, s51, 49152
LL 17,70
LL 117,71
s_bitcmp1_b64 s[90:91], s51
s_cselect_b64 s[44:45], s[84:85], s[82:83]
s_add_u32 s44, s44, s53
s_addc_u32 s45, s45, 0
s_add_u32 s51, s51, %[d]
s_cmp_eq_u32 s51, %[wrap]
s_cselect_b32 s51, %[rst], s51
M 9,57,3
LA 17
DW 2048
XS 185
M 10,68,2
LC_ 117
DQ 3072
M 10,69,3
s_add_u32 s50, s50, %[d]
v_add_u32 v1, s37, v0
XS 186
M 11,80,2
WL 2
LO 116,16,116
LU 16,116
M 11,81,3
s_cmp_eq_u32 s50, %[wrap]
RA 216,1,0
HS 24,16
XS 187
M 8,46,4
s_cselect_b32 s50, %[rst], s50
LL 18,72
LL 120,73
M 8,47,5
LA 18
v_and_b32 v22, 0xfffffcff, v1
XS 188
M 9,58,4
LC_ 120
RA 220,1,1024
M 9,59,5
RV 252,22,5120
v_add_u32 v19, 1, v19
XS 189
M 10,70,4
WL 5
LO 117,17,117
LU 17,117
M 10,71,5
s_add_u32 s38, s38, 32768
RA 224,1,2048
HS 24,17
XS 190
M 11,82,4
WL 1
LL 16,74
LL 121,75
XS 191
M 11,83,5
s_cmp_eq_u32 s38, s77
LA 16
RA 228,1,3072
s_cselect_b32 s38, s76, s38
FP
s_sub_u32 s39, 0, s39
Z 0,0,0
LC_ 121
XS 192
M 0,1,1
XS 193
XS 194
Z 1,12,0
LO 120,18,120
LU 18,120
M 1,13,1
RA 232,1,4096
HS 24,18
Z 2,36,0
LL 17,80
LL 124,81
M 2,37,1
LA 17
XS 195
Z 3,48,0
LC_ 124
XS 196
M 3,49,1
XS 197
XS 198
Z 4,60,0
WL 3
LO 121,16,121
LU 16,121
M 4,61,1
RA 236,1,6144
HS 24,16
Z 5,72,0
LL 18,82
LL 125,83
M 5,73,1
LA 18
XS 199
M 0,2,2
LC_ 125
XS 200
M 0,3,3
XS 201
XS 202
M 1,14,2
WL 3
LO 124,17,124
LU 17,124
M 1,15,3
RA 216,1,7168
HS 24,17
M 2,38,2
LL 16,88
LL 114,89
M 2,39,3
LA 16
FR
M 3,50,2
LC_ 114
XS 203
M 3,51,3
WV 31
BW 24
M 4,62,2
WL 5
LO 125,18,125
LU 18,125
M 4,63,3
RA 220,1,8192
HS 24,18
M 5,74,2
LL 17,90
LL 115,91
M 5,75,3
s_mov_b32 m0, %[pm]
LA 17
DF
M 0,4,4
LC_ 115
RV 40,8,6144
XS 204
M 0,5,5
RV 44,8,6208
RV 48,8,6272
M 1,16,4
WL 7
LO 16,16,114
LU 114,16
XS 205
M 1,17,5
RA 224,1,9216
HZ 25,114
M 2,40,4
LL 18,92
LL 118,93
XS 206
M 2,41,5
LA 18
PK 114,16,112,33
XS 207
M 3,52,4
LC_ 118
PK 112,16,112,32
M 3,53,5
BR 24
v_cmp_gt_u32 vcc, v19, v15
XS 208
M 4,64,4
WL 7
LO 17,17,115
LU 115,17
M 4,65,5
s_add_u32 s42, s44, s62
RA 228,1,10240
HS 25,115
XS 209
M 5,76,4
LL 16,94
LL 119,95
M 5,77,5
s_cbranch_vccnz .Lk1s10
.Lk1r10:
LA 16
PK 115,17,113,33
XS 210
M 0,6,0
s_addc_u32 s43, s45, 0
LC_ 119
PK 113,17,113,32
M 0,7,1
ST 112
HX 24
XS 211
M 1,18,0
WL 4
LO 18,18,118
LU 118,18
M 1,19,1
RA 232,1,11264
HS 25,118
XS 212
M 2,42,0
LL 17,96
LL 122,97
M 2,43,1
LA 17
PK 118,18,116,33
XS 213
M 3,54,0
LC_ 122
PK 116,18,116,32
M 3,55,1
RV 252,22,5392
PO 118
XS 214
M 4,66,0
WL 4
LO 16,16,119
LU 119,16
M 4,67,1
s_add_u32 s42, s44, s63
RA 236,1,12288
HS 25,119
XS 215
M 5,78,0
LL 18,98
LL 123,99
M 5,79,1
s_addc_u32 s43, s45, 0
LA 18
PK 119,16,117,33
XS 216
M 0,8,2
LC_ 123
PK 117,16,117,32
M 0,9,3
W1 24,0
PO 119
XS 217
)"
R"(M 1,20,2
WL 5
LO 17,17,122
LU 122,17
M 1,21,3
RA 216,1,13312
HS 25,122
XS 218
M 2,44,2
LL 16,100
LL 126,101
M 2,45,3
LA 16
PK 122,17,120,33
XS 219
M 3,56,2
LC_ 126
PK 120,17,120,32
M 3,57,3
WV 26
ST 116
XS 220
BW 25
M 4,68,2
WL 5
LO 18,18,123
LU 123,18
M 4,69,3
s_add_u32 s42, s44, s64
RA 220,1,14336
BR 25
HS 25,123
XS 221
LZ 40,24
s_addc_u32 s43, s45, 0
BW 26
M 5,80,2
LL 17,102
LL 127,103
M 5,81,3
s_bitcmp1_b64 s[92:93], 48
LA 17
PK 123,18,121,33
XS 222
M 0,10,4
s_cselect_b32 s58, 16, 0
LC_ 127
PK 121,18,121,32
XS 223
M 0,11,5
s_lshr_b32 s58, 0xda43cb52, s58
RV 40,8,6336
BR 26
M 1,22,4
WL 8
LO 16,16,126
LU 126,16
M 1,23,5
WL 5
RA 224,1,15360
HS 25,126
LS 44,25
WV 23
BW 27
M 2,46,4
LL 18,56
LL 104,57
M 2,47,5
LA 18
PK 126,16,124,33
M 3,58,4
LC_ 104
RV 44,8,6400
PK 124,16,124,32
M 3,59,5
ds_read_b64 v[14:15], v5 offset:0
BR 27
PO 126
M 4,70,4
WL 9
LO 17,17,127
LU 127,17
M 4,71,5
WL 7
RA 228,1,16384
HS 25,127
LS 48,26
WV 22
BW 28
M 5,82,4
LL 16,58
LL 105,59
M 5,83,5
LA 16
RV 48,8,6464
PK 127,17,125,33
Z 6,0,0
LC_ 105
PK 125,17,125,32
M 6,1,1
BR 28
PO 127
v_and_b32 v11, 0x1f0, v0
v_min_u32 v11, 368, v11
v_lshlrev_b32 v11, 3, v11
Z 7,12,0
WL 9
LO 104,18,104
LU 18,104
M 7,13,1
WL 6
RA 232,1,17408
HZ 26,18
LS 40,27
WV 21
BW 29
Z 8,36,0
LL 17,60
LL 108,61
ST 120
M 8,37,1
LA 17
RV 40,8,6528
Z 9,48,0
LC_ 108
BR 29
M 9,49,1
WL 6
v_subrev_u32 v5, s39, v5
HX 25
LS 44,28
WV 21
BW 30
Z 10,60,0
LO 105,16,105
LU 16,105
v_lshrrev_b32 v3, 9, v0
v_lshl_add_u32 v3, v3, 2, 0
v_bfe_u32 v3, s58, v3, 4
v_mad_u32_u24 v3, v3, s97, v11
M 10,61,1
s_add_u32 s42, s44, s65
RA 236,1,18432
HS 26,16
Z 11,72,0
RV 44,8,6592
LL 18,62
LL 109,63
M 11,73,1
s_addc_u32 s43, s45, 0
LA 18
W1 25,1024
BR 30
M 6,2,2
WV 20
LC_ 109
DY
M 6,3,3
WL 7
ST 124
v_add_f32 v14, v14, v15
LS 48,29
s_mul_i32 s53, s50, 25600
BW 31
M 7,14,2
LO 108,17,108
LU 17,108
M 7,15,3
s_add_u32 s40, %[wlo], s53
RA 216,1,19456
HS 26,17
M 8,38,2
s_addc_u32 s41, %[whi], 0
RV 48,8,6656
LL 16,64
LL 112,65
M 8,39,3
s_mov_b32 m0, s38
LA 16
BR 31
M 9,50,2
LC_ 112
v_add_f32_dpp v14, v14, v14 quad_perm:[1,0,3,2] row_mask:0xf bank_mask:0xf
M 9,51,3
WL 7
v_add_f32_dpp v14, v14, v14 quad_perm:[2,3,0,1] row_mask:0xf bank_mask:0xf
v_add_u32 v4, s39, v4
LS 40,30
WV 21
BW 32
M 10,62,2
WL 7
LO 109,18,109
LU 18,109
M 10,63,3
RA 220,1,20480
RV 40,8,6720
HS 26,18
M 11,74,2
BR 32
LL 17,66
LL 113,67
M 11,75,3
WV 18
LA 17
v_add_f32_dpp v14, v14, v14 row_half_mirror row_mask:0xf bank_mask:0xf
v_lshrrev_b32 v3, 9, v0
v_lshl_add_u32 v3, v3, 2, 8
v_bfe_u32 v3, s58, v3, 4
v_mad_u32_u24 v3, v3, s97, v11
M 6,4,4
WL 6
LC_ 113
DW 0
M 6,5,5
s_add_u32 s37, s37, 32768
v_cvt_f64_f32 v[14:15], v14
HX 26
LS 44,31
s_cmp_eq_u32 s37, s75
BW 33
M 7,16,4
WL 7
RV 44,8,6784
LO 112,16,112
LU 16,112
M 7,17,5
s_cselect_b32 s37, s74, s37
RA 224,1,21504
HZ 24,16
M 8,40,4
BR 33
LL 18,68
LL 116,69
M 8,41,5
WL 6
LA 18
s_mov_b64 exec, s[54:55]
SP
s_mov_b64 exec, -1
LS 48,32
WV 19
BW 34
M 9,52,4
LC_ 116
DY
M 9,53,5
DW 1024
RV 48,8,6848
M 10,64,4
WL 8
LO 113,17,113
LU 17,113
M 10,65,5
RA 228,1,22528
RA 240,7,0
HS 24,17
M 11,76,4
WV 19
BR 34
LL 16,70
LL 117,71
M 11,77,5
WL 7
LA 16
DW 2048
M 6,6,0
s_mul_i32 s53, s51, 49152
LC_ 117
M 6,7,1
s_bitcmp1_b64 s[90:91], s51
LS 40,33
s_cselect_b64 s[44:45], s[84:85], s[82:83]
BW 35
M 7,18,0
WL 3
RA 244,7,256
LO 116,18,116
LU 18,116
M 7,19,1
s_add_u32 s44, s44, s53
RA 232,1,23552
HS 24,18
M 8,42,0
s_addc_u32 s45, s45, 0
BR 35
LL 17,72
LL 120,73
M 8,43,1
s_bitcmp1_b64 s[92:93], s51
LA 17
s_cselect_b64 s[94:95], s[88:89], s[86:87]
s_add_u32 s94, s94, s53
s_addc_u32 s95, s95, 0
s_add_u32 s51, s51, %[d]
s_cmp_eq_u32 s51, %[wrap]
s_cselect_b32 s51, %[rst], s51
s_mul_i32 s53, %[d], 0
s_add_u32 s60, s53, %[sb1]
s_add_u32 s61, s60, 512
LS 44,34
s_add_u32 s40, s40, 3072
RA 248,7,512
M 9,54,0
s_addc_u32 s41, s41, 0
LC_ 120
s_add_u32 m0, s38, 3072
M 9,55,1
DW 0
M 10,66,0
WL 7
LO 117,16,117
LU 16,117
M 10,67,1
WL 3
RA 236,1,24576
HS 24,16
LS 48,35
RA 252,7,768
M 11,78,0
LL 18,74
LL 121,75
s_nop 3
v_mul_f32 v36, 0xbb2aaaab, v36
M 11,79,1
LA 18
DW 1024
v_mul_f32 v37, 0xbb2aaaab, v37
v_mul_f32 v38, 0xbb2aaaab, v38
M 6,8,2
LC_ 121
DW 2048
v_mul_f32 v39, 0xbb2aaaab, v39
M 6,9,3
s_lshl_b32 s53, s52, 8
v_add_u32 v1, s37, v0
LK 40,240,24,0
LK 44,242,24,2
s_add_u32 s48, %[alo], s53
RA 240,7,1024
LK 48,244,25,0
s_nop 0
CF 224,40,41
LK 248,246,25,2
s_addc_u32 s49, %[ahi], 0
RA 244,7,1280
CF 225,42,43
M 7,20,2
WL 6
LO 120,17,120
LU 17,120
M 7,21,3
s_add_u32 s52, s52, %[d]
RA 216,1,0
HS 24,17
M 8,44,2
s_cmp_eq_u32 s52, %[wrap]
LL 16,76
LL 124,77
CF 226,44,45
M 8,45,3
s_cselect_b32 s52, %[rst], s52
LA 16
DQ 3072
CF 227,46,47
LK 40,248,26,0
CF 228,48,49
LK 44,250,26,2
RA 248,7,1536
CF 229,50,51
GZ 24
WL 7
CF 230,248,249
CF 231,250,251
LK 48,252,27,0
CF 232,40,41
LK 248,254,27,2
RA 252,7,1792
CF 233,42,43
GS 25
WL 5
CF 234,44,45
CF 235,46,47
LK 40,240,28,0
CF 236,48,49
LK 44,242,28,2
RA 240,7,2048
CF 237,50,51
GS 26
WL 5
CF 238,248,249
CF 239,250,251
LK 48,244,29,0
CF 240,40,41
LK 248,246,29,2
RA 244,7,2304
CF 241,42,43
GS 27
CF 242,44,45
CF 243,46,47
)"
R"(M 9,56,2
LC_ 124
v_and_b32 v22, 0xfffffcff, v1
M 9,57,3
WL 4
LK 40,248,30,0
CF 244,48,49
LK 44,250,30,2
RA 248,7,2560
CF 245,50,51
GS 28
WL 4
CF 246,248,249
CF 247,250,251
LK 48,252,31,0
CF 40,40,41
LK 248,254,31,2
RA 252,7,2816
CF 42,42,43
GS 29
CF 44,44,45
CF 46,46,47
M 10,68,2
LO 121,18,121
LU 18,121
M 10,69,3
s_add_u32 s50, s50, %[d]
RA 220,1,1024
HS 24,18
M 11,80,2
LL 17,78
LL 125,79
M 11,81,3
s_cmp_eq_u32 s50, %[wrap]
LA 17
RV 252,22,5120
M 6,10,4
s_cselect_b32 s50, %[rst], s50
LC_ 125
AW 0,40
M 6,11,5
v_add_u32 v19, 1, v19
AW 1,42
M 7,22,4
WL 6
LO 124,16,124
LU 16,124
M 7,23,5
s_add_u32 s38, s38, 32768
RA 224,1,2048
HS 24,16
M 8,46,4
s_cmp_eq_u32 s38, s77
LL 18,80
LL 106,81
M 8,47,5
s_cselect_b32 s38, s76, s38
LA 18
FP
M 9,58,4
s_sub_u32 s39, 0, s39
LC_ 106
AW 2,44
M 9,59,5
AW 3,46
LK 40,240,32,0
CF 48,48,49
LK 44,242,32,2
CF 50,50,51
GS 30
CF 248,248,249
CF 250,250,251
M 10,70,4
WL 4
LO 125,17,125
LU 17,125
M 10,71,5
RA 228,1,3072
HS 24,17
M 11,82,4
LL 16,82
LL 107,83
M 11,83,5
LA 16
HX 24
Z 0,0,0
LC_ 107
AW 4,48
M 0,1,1
AW 5,50
AW 6,248
Z 1,12,0
WL 3
LO 18,18,106
LU 106,18
M 1,13,1
RA 232,1,4096
HZ 27,106
Z 2,36,0
LL 17,84
LL 110,85
M 2,37,1
LA 17
PK 106,18,104,33
Z 3,48,0
LC_ 110
PK 104,18,104,32
M 3,49,1
AW 7,250
LK 48,244,33,0
CF 40,40,41
LK 248,246,33,2
CF 42,42,43
GS 31
CF 44,44,45
CF 46,46,47
Z 4,60,0
WL 3
LO 16,16,107
LU 107,16
M 4,61,1
s_add_u32 s42, s94, s60
RA 236,1,6144
HS 27,107
Z 5,72,0
LL 18,86
LL 111,87
M 5,73,1
s_addc_u32 s43, s95, 0
LA 18
PK 107,16,105,33
M 0,2,2
LC_ 111
PK 105,16,105,32
M 0,3,3
SU 104
AW 8,40
M 1,14,2
WL 3
LO 17,17,110
LU 110,17
M 1,15,3
RA 216,1,7168
HS 27,110
M 2,38,2
LL 16,88
LL 114,89
M 2,39,3
LA 16
PK 110,17,108,33
AW 9,42
AW 10,44
M 3,50,2
LC_ 114
PK 108,17,108,32
AW 11,46
M 3,51,3
FR
PO 110
LK 40,248,34,0
CF 48,48,49
LK 44,250,34,2
CF 50,50,51
GS 32
CF 248,248,249
CF 250,250,251
M 4,62,2
WL 4
LO 18,18,111
LU 111,18
M 4,63,3
s_add_u32 s42, s94, s61
RA 220,1,8192
HS 27,111
M 5,74,2
LL 17,90
LL 115,91
AW 12,48
AW 13,50
M 5,75,3
s_addc_u32 s43, s95, 0
LA 17
PK 111,18,109,33
AW 14,248
AW 15,250
LK 48,252,35,0
CF 40,40,41
LK 248,254,35,2
s_mov_b32 m0, %[pm]
CF 42,42,43
HX 27
GS 33
CF 44,44,45
CF 46,46,47
M 0,4,4
LC_ 115
PK 109,18,109,32
M 0,5,5
DF
PO 111
M 1,16,4
WL 4
LO 16,16,114
LU 114,16
M 1,17,5
RA 224,1,9216
HZ 25,114
AW 16,40
M 2,40,4
LL 18,92
LL 118,93
AW 17,42
AW 18,44
M 2,41,5
LA 18
RV 36,8,6912
PK 114,16,112,33
AW 19,46
M 3,52,4
LC_ 118
PK 112,16,112,32
M 3,53,5
WL 7
SU 108
v_cmp_gt_u32 vcc, v19, v15
GS 34
CF 48,48,49
CF 50,50,51
CF 248,248,249
M 4,64,4
WL 4
LO 17,17,115
LU 115,17
CF 250,250,251
AW 20,48
M 4,65,5
s_add_u32 s42, s44, s62
RA 228,1,10240
HS 25,115
AW 21,50
AW 22,248
M 5,76,4
LL 16,94
LL 119,95
AW 23,250
M 5,77,5
s_cbranch_vccnz .Lk1s11
.Lk1r11:
LA 16
PK 115,17,113,33
GS 35
s_addc_u32 s43, s45, 0
W1 24,0
s_nop 5
v_cndmask_b32 v52, v52, v53, s[78:79]
M 0,6,0
LC_ 119
PK 113,17,113,32
v_cndmask_b32 v54, v54, v55, s[78:79]
M 0,7,1
W1 26,2048
v_cndmask_b32 v52, v52, v54, s[80:81]
M 1,18,0
WL 5
LO 18,18,118
LU 118,18
v_mul_f32 v52, 0x3b2aaaab, v52
M 1,19,1
RA 232,1,11264
HS 25,118
v_med3_f32 v53, v52, v39, v37
M 2,42,0
LL 17,96
LL 122,97
LG 52,53
M 2,43,1
LA 17
PK 118,18,116,33
C 52,52,52
M 3,54,0
LC_ 122
PK 116,18,116,32
v_lshlrev_b32 v52, 16, v52
M 3,55,1
RV 252,22,5392
PO 118
v_add_f32 v52, v36, v52
M 4,66,0
WL 5
LO 16,16,119
LU 119,16
C 52,52,52
M 4,67,1
RA 236,1,12288
HS 25,119
M 5,78,0
LL 18,98
LL 123,99
v_lshlrev_b32 v52, 16, v52
M 5,79,1
LA 18
PK 119,16,117,33
v_sqrt_f32 v52, v52
M 0,8,2
LC_ 123
PK 117,16,117,32
C 52,52,52
M 0,9,3
ST 112
PO 119
v_lshlrev_b32 v52, 16, v52
M 1,20,2
WL 4
LO 17,17,122
LU 122,17
v_rcp_f32 v52, v52
M 1,21,3
s_add_u32 s42, s44, s63
RA 216,1,13312
HS 25,122
v_mul_f32 v52, v38, v52
M 2,44,2
s_addc_u32 s43, s45, 0
LL 16,100
LL 126,101
CF 52,52,52
M 2,45,3
LA 16
PK 122,17,120,33
ds_bpermute_b32 v13, v28, v52
M 3,56,2
LC_ 126
PK 120,17,120,32
M 3,57,3
ST 116
AR 248,0
M 4,68,2
WL 4
LO 18,18,123
LU 123,18
M 4,69,3
s_add_u32 s42, s44, s64
RA 220,1,14336
HS 25,123
M 5,80,2
LL 17,102
LL 127,103
M 5,81,3
s_addc_u32 s43, s45, 0
LA 17
PK 123,18,121,33
M 0,10,4
LC_ 127
PK 121,18,121,32
M 0,11,5
ST 120
AR 249,1
M 1,22,4
WL 3
LO 16,16,126
LU 126,16
M 1,23,5
RA 224,1,15360
HS 25,126
M 2,46,4
LL 18,56
LL 104,57
M 2,47,5
LA 18
PK 126,16,124,33
M 3,58,4
LC_ 104
PK 124,16,124,32
AR 250,2
M 3,59,5
ds_read_b64 v[14:15], v5 offset:0
PO 126
M 4,70,4
WL 4
LO 17,17,127
LU 127,17
AR 251,3
M 4,71,5
s_add_u32 s42, s44, s65
RA 228,1,16384
HS 25,127
AR 52,4
M 5,82,4
LL 16,58
LL 105,59
AR 53,5
M 5,83,5
s_addc_u32 s43, s45, 0
LA 16
PK 127,17,125,33
Z 6,0,0
LC_ 105
PK 125,17,125,32
AR 54,6
M 6,1,1
)"
R"(PO 127
v_subrev_u32 v5, s39, v5
AR 55,7
Z 7,12,0
WL 4
LO 104,18,104
LU 18,104
XS 224
M 7,13,1
RA 232,1,17408
HZ 26,18
Z 8,36,0
WL 4
LL 17,60
LL 108,61
XS 225
M 8,37,1
s_mul_i32 s53, s50, 25600
LA 17
v_add_f32 v14, v14, v15
XS 226
Z 9,48,0
s_add_u32 s40, %[wlo], s53
LC_ 108
v_add_f32_dpp v14, v14, v14 quad_perm:[1,0,3,2] row_mask:0xf bank_mask:0xf
M 9,49,1
s_addc_u32 s41, %[whi], 0
v_add_f32_dpp v14, v14, v14 quad_perm:[2,3,0,1] row_mask:0xf bank_mask:0xf
HX 25
XS 227
Z 10,60,0
WL 3
LO 105,16,105
LU 16,105
XS 228
M 10,61,1
s_mov_b32 m0, s38
RA 236,1,18432
HS 26,16
XS 229
Z 11,72,0
LL 18,62
LL 109,63
M 11,73,1
LA 18
W1 25,1024
XS 230
M 6,2,2
LC_ 109
W1 27,3072
XS 231
M 6,3,3
ST 124
v_add_f32_dpp v14, v14, v14 row_half_mirror row_mask:0xf bank_mask:0xf
v_min_u32 v35, v35, v13
M 7,14,2
WL 5
LO 108,17,108
LU 17,108
M 7,15,3
RA 216,1,19456
HS 26,17
XS 232
M 8,38,2
LL 16,64
LL 112,65
XS 233
M 8,39,3
LA 16
DW 0
XS 234
M 9,50,2
s_add_u32 s37, s37, 32768
LC_ 112
v_cvt_f64_f32 v[14:15], v14
M 9,51,3
s_cmp_eq_u32 s37, s75
s_mov_b64 exec, s[54:55]
SP
s_mov_b64 exec, -1
v_add_u32 v4, s39, v4
XS 235
M 10,62,2
WL 4
LO 109,18,109
LU 18,109
XS 236
M 10,63,3
s_cselect_b32 s37, s74, s37
RA 220,1,20480
HS 26,18
M 11,74,2
LL 17,66
LL 113,67
XS 237
M 11,75,3
s_mul_i32 s53, %[d], 196608
LA 17
HX 26
XS 238
M 6,4,4
s_add_u32 s53, s53, s57
LC_ 113
XS 239
M 6,5,5
s_add_u32 s46, %[xlo], s53
XS 240
XS 241
M 7,16,4
WL 3
LO 112,16,112
LU 16,112
M 7,17,5
s_addc_u32 s47, %[xhi], 0
RA 224,1,21504
HZ 24,16
M 8,40,4
s_bitcmp1_b64 s[92:93], 48
LL 18,68
LL 116,69
XS 242
M 8,41,5
s_cselect_b32 s58, 16, 0
LA 18
XS 243
M 9,52,4
s_lshr_b32 s58, 0xda43cb52, s58
LC_ 116
v_lshrrev_b32 v3, 6, v0
v_and_b32 v3, 12, v3
v_bfe_u32 v3, s58, v3, 4
v_mad_u32_u24 v3, v3, s97, v21
M 9,53,5
DW 1024
XS 244
M 10,64,4
WL 3
LO 113,17,113
LU 17,113
XS 245
M 10,65,5
RA 228,1,22528
HS 24,17
M 11,76,4
LL 16,70
LL 117,71
XS 246
M 11,77,5
LA 16
XL 36,0
XS 247
M 6,6,0
LC_ 117
DW 2048
M 6,7,1
XL 37,64
XS 248
M 7,18,0
WL 3
LO 116,18,116
LU 18,116
XS 249
M 7,19,1
s_mul_i32 s53, s51, 49152
RA 232,1,23552
HS 24,18
XS 250
M 8,42,0
s_bitcmp1_b64 s[90:91], s51
LL 17,72
LL 120,73
M 8,43,1
s_cselect_b64 s[44:45], s[84:85], s[82:83]
LA 17
s_add_u32 s44, s44, s53
s_addc_u32 s45, s45, 0
s_bitcmp1_b64 s[92:93], s51
s_cselect_b64 s[94:95], s[88:89], s[86:87]
s_add_u32 s94, s94, s53
s_addc_u32 s95, s95, 0
s_add_u32 s51, s51, %[d]
s_cmp_eq_u32 s51, %[wrap]
s_cselect_b32 s51, %[rst], s51
XS 251
M 9,54,0
s_add_u32 s40, s40, 3072
LC_ 120
s_addc_u32 s41, s41, 0
s_add_u32 m0, s38, 3072
AW 0,248
M 9,55,1
XL 38,128
AW 1,249
M 10,66,0
WL 3
LO 117,16,117
LU 16,117
M 10,67,1
RA 236,1,24576
HS 24,16
AW 2,250
M 11,78,0
LL 18,74
LL 121,75
AW 3,251
M 11,79,1
LA 18
DW 0
XS 52
M 6,8,2
LC_ 121
XL 39,192
M 6,9,3
DW 1024
v_add_u32 v1, s37, v0
XS 53
M 7,20,2
WL 3
LO 120,17,120
LU 17,120
XS 54
M 7,21,3
RA 216,1,0
HS 24,17
M 8,44,2
LL 16,76
LL 124,77
XS 55
M 8,45,3
LA 16
XL 40,256
AW 4,52
M 9,56,2
LC_ 124
DW 2048
AW 5,53
M 9,57,3
XL 41,320
v_and_b32 v22, 0xfffffcff, v1
M 10,68,2
WL 3
LO 121,18,121
LU 18,121
AW 6,54
M 10,69,3
s_lshl_b32 s53, s52, 8
RA 220,1,1024
HS 24,18
AW 7,55
M 11,80,2
LL 17,78
LL 125,79
AR 248,8
M 11,81,3
s_add_u32 s48, %[alo], s53
LA 17
RV 252,22,5120
M 6,10,4
s_addc_u32 s49, %[ahi], 0
LC_ 125
AR 249,9
M 6,11,5
s_add_u32 s52, s52, %[d]
AR 250,10
AR 251,11
M 7,22,4
WL 4
LO 124,16,124
LU 16,124
M 7,23,5
s_cmp_eq_u32 s52, %[wrap]
RA 224,1,2048
HS 24,16
M 8,46,4
s_cselect_b32 s52, %[rst], s52
LL 18,80
LL 106,81
XS 248
M 8,47,5
LA 18
DQ 3072
XS 249
M 9,58,4
LC_ 106
XL 42,384
M 9,59,5
s_add_u32 s50, s50, %[d]
XS 250
XS 251
M 10,70,4
WL 3
LO 125,17,125
LU 17,125
M 10,71,5
RA 228,1,3072
HS 24,17
AW 8,248
M 11,82,4
LL 16,82
LL 107,83
M 11,83,5
LA 16
HX 24
s_cmp_eq_u32 s50, %[wrap]
s_cselect_b32 s50, %[rst], s50
XL 43,448
AW 9,249
Z 0,0,0
WV 38
LC_ 107
XL 44,512
v_add_u32 v19, 1, v19
s_add_u32 s38, s38, 32768
s_cmp_eq_u32 s38, s77
s_cselect_b32 s38, s76, s38
FP
s_sub_u32 s39, 0, s39
AW 10,250
M 0,1,1
XL 45,576
AW 11,251
Z 1,12,0
WL 4
LO 18,18,106
LU 106,18
M 1,13,1
RA 232,1,4096
HZ 27,106
AR 52,12
Z 4,60,0
LL 17,84
LL 110,85
AR 53,13
M 4,61,1
LA 17
PK 106,18,104,33
AR 54,14
Z 5,72,0
WL 5
LC_ 110
XL 46,640
PK 104,18,104,32
M 5,73,1
RA 236,1,6144
AR 55,15
M 0,2,2
WL 5
LO 16,16,107
LU 107,16
XS 52
M 0,3,3
FR
HS 27,107
M 1,14,2
LL 18,86
LL 111,87
XS 53
M 1,15,3
LA 18
PK 107,16,105,33
XS 54
M 4,62,2
LC_ 111
XL 47,704
PK 105,16,105,32
XS 55
M 4,63,3
s_add_u32 s42, s94, s60
RA 216,1,7168
AW 12,52
M 5,74,2
WL 4
LO 17,17,110
LU 110,17
M 5,75,3
s_addc_u32 s43, s95, 0
RA 220,1,8192
HS 27,110
AW 13,53
M 0,4,4
LL 16,88
LL 114,89
AW 14,54
M 0,5,5
LA 16
PK 110,17,108,33
M 1,16,4
LC_ 114
PK 108,17,108,32
AW 15,55
M 1,17,5
RA 224,1,9216
PO 110
AR 248,16
M 4,64,4
WL 5
LO 18,18,111
LU 111,18
SU 104
AR 249,17
)"
R"(M 4,65,5
s_add_u32 s42, s94, s61
HS 27,111
v_cmp_gt_u32 vcc, v19, v15
M 5,76,4
WL 3
LL 17,90
LL 115,91
AR 250,18
M 5,77,5
s_addc_u32 s43, s95, 0
LA 17
PK 111,18,109,33
AR 251,19
M 0,6,0
s_cbranch_vccnz .Lk1s12
.Lk1r12:
LC_ 115
PK 109,18,109,32
M 0,7,1
WV 17
RA 228,1,10240
PO 111
XS 248
BW 36
M 1,18,0
WL 5
LO 16,16,114
LU 114,16
XS 249
M 1,19,1
RA 232,1,11264
RV 40,8,6144
HZ 25,114
XS 250
M 4,66,0
RV 44,8,6208
LL 18,92
LL 118,93
M 4,67,1
LA 18
RV 48,8,6272
PK 114,16,112,33
XS 251
M 5,78,0
WL 6
LC_ 118
BR 36
PK 112,16,112,32
AW 16,248
M 5,79,1
WV 15
RV 252,22,5392
ds_read_b64 v[14:15], v5 offset:0
W1 24,0
W1 26,2048
HX 27
BW 37
AW 17,249
M 0,8,2
BR 37
LO 17,17,115
LU 115,17
M 0,9,3
WL 6
RA 236,1,12288
HS 25,115
AW 18,250
LZ 40,36
WV 14
BW 38
M 1,20,2
LL 16,94
LL 119,95
AW 19,251
M 1,21,3
LA 16
RV 40,8,6336
PK 115,17,113,33
AR 52,20
M 4,68,2
LC_ 119
BR 38
PK 113,17,113,32
M 4,69,3
WL 6
RA 216,1,13312
AR 53,21
LS 44,37
WV 12
BW 39
M 5,80,2
WL 7
LO 18,18,118
LU 118,18
SU 108
AR 54,22
M 5,81,3
s_add_u32 s42, s44, s62
RA 220,1,14336
HS 25,118
s_addc_u32 s43, s45, 0
RV 44,8,6400
M 0,10,4
BR 39
LL 17,96
LL 122,97
AR 55,23
M 0,11,5
WL 5
LA 17
PK 118,18,116,33
XS 52
LS 48,38
WV 11
BW 40
M 1,22,4
LC_ 122
PK 116,18,116,32
XS 53
M 1,23,5
RA 224,1,15360
RV 48,8,6464
PO 118
M 4,70,4
LO 16,16,119
LU 119,16
ST 112
XS 54
M 4,71,5
s_add_u32 s42, s44, s63
BR 40
HS 25,119
XS 55
M 5,82,4
WL 6
LL 18,98
LL 123,99
AW 20,52
M 5,83,5
s_addc_u32 s43, s45, 0
LA 18
PK 119,16,117,33
LS 40,39
WV 10
BW 41
Z 6,0,0
LC_ 123
PK 117,16,117,32
v_add_f32 v14, v14, v15
s_nop 1
v_add_f32_dpp v14, v14, v14 quad_perm:[1,0,3,2] row_mask:0xf bank_mask:0xf
s_nop 1
v_add_f32_dpp v14, v14, v14 quad_perm:[2,3,0,1] row_mask:0xf bank_mask:0xf
s_nop 1
v_add_f32_dpp v14, v14, v14 row_half_mirror row_mask:0xf bank_mask:0xf
v_cvt_f64_f32 v[14:15], v14
s_mov_b64 exec, s[54:55]
SP
s_mov_b64 exec, -1
s_nop 0
ds_read_b64 v[14:15], v5 offset:2048
RV 40,8,6528
AW 21,53
M 6,1,1
WV 9
RA 228,1,16384
BR 41
PO 119
AW 22,54
Z 7,12,0
WL 7
LO 17,17,122
LU 122,17
AW 23,55
M 7,13,1
s_mul_i32 s53, %[d], 393216
RA 232,1,17408
HS 25,122
LS 44,40
s_add_u32 s53, s53, s57
BW 42
Z 10,60,0
s_add_u32 s46, %[xlo], s53
RV 44,8,6592
LL 16,100
LL 126,101
M 10,61,1
s_addc_u32 s47, %[xhi], 0
LA 16
BR 42
PK 122,17,120,33
Z 2,72,0
WL 5
LC_ 126
PK 120,17,120,32
M 2,73,1
s_bitcmp1_b64 s[92:93], 48
RA 236,1,18432
v_subrev_u32 v5, s39, v5
LS 48,41
s_cselect_b32 s58, 16, 0
WV 8
BW 43
M 6,2,2
RV 48,8,6656
LO 18,18,123
LU 123,18
M 6,3,3
s_lshr_b32 s58, 0xfe987610, s58
BR 43
HS 25,123
v_lshrrev_b32 v3, 6, v0
v_and_b32 v3, 12, v3
v_bfe_u32 v3, s58, v3, 4
v_mad_u32_u24 v3, v3, s97, v21
M 7,14,2
s_mul_i32 s53, s50, 25600
LL 17,102
LL 127,103
s_add_u32 s40, %[wlo], s53
s_addc_u32 s41, %[whi], 0
s_mov_b32 m0, s38
M 7,15,3
WL 5
LA 17
PK 123,18,121,33
LS 40,42
WV 7
BW 44
M 10,62,2
LC_ 127
RV 40,8,6720
PK 121,18,121,32
M 10,63,3
WV 6
RA 216,1,19456
BR 44
DW 0
M 2,74,2
WL 6
LO 16,16,126
LU 126,16
ST 116
M 2,75,3
s_add_u32 s42, s44, s64
RA 220,1,20480
HS 25,126
s_addc_u32 s43, s45, 0
LS 44,43
s_add_u32 s37, s37, 32768
BW 45
M 6,4,4
s_cmp_eq_u32 s37, s75
LL 18,56
LL 104,57
s_cselect_b32 s37, s74, s37
M 6,5,5
LA 18
PK 126,16,124,33
v_add_f32 v14, v14, v15
s_nop 1
v_add_f32_dpp v14, v14, v14 quad_perm:[1,0,3,2] row_mask:0xf bank_mask:0xf
s_nop 1
v_add_f32_dpp v14, v14, v14 quad_perm:[2,3,0,1] row_mask:0xf bank_mask:0xf
s_nop 1
v_add_f32_dpp v14, v14, v14 row_half_mirror row_mask:0xf bank_mask:0xf
v_cvt_f64_f32 v[14:15], v14
s_mov_b64 exec, s[54:55]
SP
s_mov_b64 exec, -1
XL 48,0
DW 1024
RV 44,8,6784
M 7,16,4
LC_ 104
BR 45
PK 124,16,124,32
M 7,17,5
WL 6
RA 224,1,21504
PO 126
LS 48,44
WV 10
BW 46
M 10,64,4
LO 17,17,127
LU 127,17
ST 120
M 10,65,5
WV 10
DW 2048
RV 48,8,6848
HS 25,127
M 2,76,4
WL 8
RA 240,7,0
LL 16,58
LL 105,59
M 2,77,5
s_add_u32 s40, s40, 3072
LA 16
BR 46
PK 127,17,125,33
M 6,6,0
s_addc_u32 s41, s41, 0
LC_ 105
PK 125,17,125,32
s_add_u32 m0, s38, 3072
M 6,7,1
WL 7
RA 228,1,22528
XL 49,64
PO 127
LS 40,45
s_add_u32 s42, s44, s65
BW 47
M 7,18,0
RA 244,7,256
LO 104,18,104
LU 18,104
M 7,19,1
s_addc_u32 s43, s45, 0
RA 232,1,23552
BR 47
HZ 26,18
M 10,66,0
WL 6
DW 0
LL 17,60
LL 108,61
M 10,67,1
s_lshl_b32 s53, s52, 8
LA 17
HX 25
LS 44,46
s_add_u32 s48, %[alo], s53
RA 248,7,512
M 2,78,0
WL 2
LC_ 108
W1 25,1024
W1 27,3072
M 2,79,1
s_addc_u32 s49, %[ahi], 0
RA 236,1,24576
v_add_u32 v4, s39, v4
LS 48,47
s_add_u32 s52, s52, %[d]
RA 252,7,768
M 6,8,2
LO 105,16,105
LU 16,105
s_cmp_eq_u32 s52, %[wrap]
s_cselect_b32 s52, %[rst], s52
DW 1024
v_mul_f32 v36, 0xbb2aaaab, v36
M 6,9,3
XL 50,128
HS 26,16
v_mul_f32 v37, 0xbb2aaaab, v37
v_mul_f32 v38, 0xbb2aaaab, v38
M 7,20,2
LL 18,62
LL 109,63
v_mul_f32 v39, 0xbb2aaaab, v39
M 7,21,3
LA 18
DW 2048
LK 40,240,36,0
LK 44,242,36,2
RA 240,7,1024
v_add_u32 v1, s37, v0
LK 48,244,37,0
s_nop 0
CF 40,40,41
LK 248,246,37,2
RA 244,7,1280
CF 42,42,43
M 10,68,2
LC_ 109
RA 216,1,0
M 10,69,3
ST 124
v_and_b32 v22, 0xfffffcff, v1
CF 44,44,45
CF 46,46,47
M 2,80,2
WL 6
LO 108,17,108
LU 17,108
AW 24,40
AW 25,42
M 2,81,3
s_mul_i32 s53, s51, 49152
RA 220,1,1024
HS 26,17
s_bitcmp1_b64 s[90:91], s51
s_cselect_b64 s[44:45], s[84:85], s[82:83]
s_add_u32 s44, s44, s53
)"
R"(s_addc_u32 s45, s45, 0
s_bitcmp1_b64 s[92:93], s51
s_cselect_b64 s[94:95], s[88:89], s[86:87]
s_add_u32 s94, s94, s53
s_addc_u32 s95, s95, 0
s_add_u32 s51, s51, %[d]
s_cmp_eq_u32 s51, %[wrap]
s_cselect_b32 s51, %[rst], s51
DQ 3072
AW 26,44
AW 27,46
LK 40,248,38,0
CF 48,48,49
LK 44,250,38,2
RA 248,7,1536
CF 50,50,51
GZ 36
CF 248,248,249
CF 250,250,251
M 6,10,4
LL 16,72
LL 120,73
M 6,11,5
LA 16
RV 252,22,5120
M 7,22,4
WL 6
LC_ 120
XL 51,192
AW 28,48
M 7,23,5
s_add_u32 s50, s50, %[d]
RA 224,1,2048
s_cmp_eq_u32 s50, %[wrap]
s_cselect_b32 s50, %[rst], s50
v_add_u32 v19, 1, v19
AW 29,50
AW 30,248
AW 31,250
LK 48,252,39,0
CF 40,40,41
LK 248,254,39,2
s_add_u32 s38, s38, 32768
RA 252,7,1792
CF 42,42,43
GS 37
s_cmp_eq_u32 s38, s77
CF 44,44,45
CF 46,46,47
M 10,70,4
WL 7
LO 109,18,109
LU 18,109
M 10,71,5
s_cselect_b32 s38, s76, s38
FP
HS 26,18
M 2,82,4
WL 4
LL 17,74
LL 121,75
M 2,83,5
s_sub_u32 s39, 0, s39
LA 17
RA 228,1,3072
AW 32,40
AW 33,42
Z 0,0,0
LC_ 121
XL 52,256
AW 34,44
AW 35,46
M 0,1,1
HX 26
LK 40,240,40,0
CF 48,48,49
LK 44,242,40,2
RA 240,7,2048
CF 50,50,51
GS 38
CF 248,248,249
CF 250,250,251
Z 1,12,0
WL 7
LO 120,16,120
LU 16,120
M 1,13,1
RA 232,1,4096
HZ 24,16
AW 36,48
Z 4,60,0
LL 18,76
LL 124,77
AW 37,50
AW 38,248
M 4,61,1
LA 18
FR
AW 39,250
LK 48,244,41,0
CF 40,40,41
LK 248,246,41,2
RA 244,7,2304
CF 42,42,43
GS 39
CF 44,44,45
CF 46,46,47
Z 8,72,0
WL 6
LC_ 124
XL 53,320
M 8,73,1
RA 236,1,6144
AW 40,40
M 0,2,2
WL 7
LO 121,17,121
LU 17,121
AW 41,42
M 0,3,3
HS 24,17
AW 42,44
AW 43,46
LK 40,248,42,0
CF 48,48,49
LK 44,250,42,2
RA 248,7,2560
CF 50,50,51
GS 40
CF 248,248,249
CF 250,250,251
M 1,14,2
LL 16,78
LL 125,79
M 1,15,3
LA 16
RA 216,1,7168
M 4,62,2
WL 6
LC_ 125
v_cmp_gt_u32 vcc, v19, v15
M 4,63,3
XL 54,384
AW 44,48
AW 45,50
AW 46,248
M 8,74,2
WL 4
LO 124,18,124
LU 18,124
AW 47,250
M 8,75,3
RA 220,1,8192
HS 24,18
LK 48,252,43,0
CF 40,40,41
LK 248,254,43,2
RA 252,7,2816
CF 42,42,43
GS 41
CF 44,44,45
CF 46,46,47
M 0,4,4
s_cbranch_vccnz .Lk1s13
.Lk1r13:
LL 17,80
LL 106,81
M 0,5,5
LA 17
ds_read_b64 v[14:15], v5 offset:0
AW 48,40
AW 49,42
M 1,16,4
LC_ 106
AW 50,44
AW 51,46
M 1,17,5
RA 224,1,9216
LK 40,240,44,0
CF 48,48,49
LK 44,242,44,2
CF 50,50,51
GS 42
CF 248,248,249
CF 250,250,251
M 4,64,4
WL 6
LO 125,16,125
LU 16,125
M 4,65,5
XL 55,448
HS 24,16
AW 52,48
M 8,76,4
WL 5
LL 18,82
LL 107,83
AW 53,50
AW 54,248
AW 55,250
M 8,77,5
LA 18
RA 228,1,10240
LK 48,244,45,0
CF 40,40,41
LK 248,246,45,2
CF 42,42,43
HX 24
GS 43
CF 44,44,45
CF 46,46,47
M 0,6,0
LC_ 107
W1 24,0
M 0,7,1
W1 26,2048
AW 56,40
M 1,18,0
WL 6
LO 17,17,106
LU 106,17
AW 57,42
AW 58,44
M 1,19,1
RA 232,1,11264
HZ 27,106
AW 59,46
LK 40,248,46,0
CF 48,48,49
LK 44,250,46,2
CF 50,50,51
v_add_f32 v14, v14, v15
GS 44
CF 248,248,249
CF 250,250,251
M 4,66,0
LL 16,84
LL 110,85
M 4,67,1
LA 16
PK 106,17,104,33
M 8,78,0
WL 5
LC_ 110
PK 104,17,104,32
AW 60,48
AW 61,50
AW 62,248
M 8,79,1
RV 252,22,5392
v_add_f32_dpp v14, v14, v14 quad_perm:[1,0,3,2] row_mask:0xf bank_mask:0xf
AW 63,250
LK 48,252,47,0
CF 40,40,41
LK 248,254,47,2
CF 42,42,43
v_add_f32_dpp v14, v14, v14 quad_perm:[2,3,0,1] row_mask:0xf bank_mask:0xf
GS 45
CF 44,44,45
CF 46,46,47
M 0,8,2
WL 6
LO 18,18,107
LU 107,18
M 0,9,3
RA 236,1,12288
HS 27,107
M 1,20,2
LL 17,86
LL 111,87
AW 64,40
AW 65,42
M 1,21,3
LA 17
RV 36,8,6912
PK 107,18,105,33
AW 66,44
AW 67,46
M 4,68,2
LC_ 111
XL 56,512
PK 105,18,105,32
M 4,69,3
s_add_u32 s42, s94, s60
RA 216,1,13312
v_add_f32_dpp v14, v14, v14 row_half_mirror row_mask:0xf bank_mask:0xf
GS 46
s_addc_u32 s43, s95, 0
CF 48,48,49
CF 50,50,51
CF 248,248,249
CF 250,250,251
M 8,80,2
WL 4
LO 16,16,110
LU 110,16
AW 68,48
AW 69,50
AW 70,248
M 8,81,3
RA 220,1,14336
HS 27,110
AW 71,250
s_nop 1
GS 47
SU 104
v_cvt_f64_f32 v[14:15], v14
s_nop 5
v_cndmask_b32 v52, v52, v53, s[78:79]
M 0,10,4
LL 18,96
LL 122,97
v_cndmask_b32 v54, v54, v55, s[78:79]
M 0,11,5
LA 18
PK 110,16,108,33
v_cndmask_b32 v52, v52, v54, s[80:81]
M 1,22,4
LC_ 122
PK 108,16,108,32
v_mul_f32 v52, 0x3b2aaaab, v52
M 1,23,5
WL 5
RA 224,1,15360
PO 110
v_med3_f32 v53, v52, v39, v37
M 4,70,4
WL 5
LO 17,17,111
LU 111,17
s_mov_b64 exec, s[54:55]
SP
s_mov_b64 exec, -1
XL 57,576
LG 52,53
s_nop 0
C 52,52,52
M 4,71,5
s_add_u32 s42, s94, s61
ds_read_b64 v[14:15], v5 offset:2048
HS 27,111
v_lshlrev_b32 v52, 16, v52
M 8,82,4
WL 4
LL 16,98
LL 123,99
v_add_f32 v52, v36, v52
M 8,83,5
s_addc_u32 s43, s95, 0
LA 16
PK 111,17,109,33
C 52,52,52
Z 6,0,0
LC_ 123
PK 109,17,109,32
v_lshlrev_b32 v52, 16, v52
M 6,1,1
RA 228,1,16384
PO 111
v_sqrt_f32 v52, v52
s_nop 0
C 52,52,52
Z 7,12,0
WL 5
LO 18,18,122
LU 122,18
v_lshlrev_b32 v52, 16, v52
M 7,13,1
RA 232,1,17408
HZ 25,122
v_rcp_f32 v52, v52
Z 10,60,0
XL 58,640
LL 17,64
LL 126,65
v_mul_f32 v52, v38, v52
M 10,61,1
LA 17
PK 122,18,120,33
v_subrev_u32 v5, s39, v5
HX 27
CF 52,52,52
Z 11,72,0
WL 2
LC_ 126
PK 120,18,120,32
ds_bpermute_b32 v13, v28, v52
M 11,73,1
RA 236,1,18432
v_add_f32 v14, v14, v15
M 6,2,2
LO 16,16,123
LU 123,16
M 6,3,3
SU 108
HS 25,123
v_add_f32_dpp v14, v14, v14 quad_perm:[1,0,3,2] row_mask:0xf bank_mask:0xf
M 7,14,2
s_mul_i32 s53, s50, 25600
LL 18,66
)"
R"(LL 127,67
s_add_u32 s40, %[wlo], s53
s_addc_u32 s41, %[whi], 0
s_mov_b32 m0, s38
M 7,15,3
LA 18
PK 123,16,121,33
v_add_f32_dpp v14, v14, v14 quad_perm:[2,3,0,1] row_mask:0xf bank_mask:0xf
M 10,62,2
LC_ 127
PK 121,16,121,32
M 10,63,3
RA 216,1,19456
XL 59,704
DW 0
v_add_f32_dpp v14, v14, v14 row_half_mirror row_mask:0xf bank_mask:0xf
M 11,74,2
WL 3
LO 17,17,126
LU 126,17
M 11,75,3
s_add_u32 s42, s44, s64
RA 220,1,20480
HS 25,126
v_cvt_f64_f32 v[14:15], v14
s_addc_u32 s43, s45, 0
AR 248,24
M 6,4,4
s_add_u32 s37, s37, 32768
LL 16,56
LL 104,57
s_cmp_eq_u32 s37, s75
s_cselect_b32 s37, s74, s37
AR 249,25
M 6,5,5
LA 16
PK 126,17,124,33
s_mov_b64 exec, s[54:55]
SP
s_mov_b64 exec, -1
DW 1024
AR 250,26
M 7,16,4
LC_ 104
PK 124,17,124,32
AR 251,27
AR 52,28
M 7,17,5
RA 224,1,21504
PO 126
AR 53,29
M 10,64,4
WL 5
LO 18,18,127
LU 127,18
ST 120
AR 54,30
AR 55,31
M 10,65,5
DW 2048
HS 25,127
XS 248
M 11,76,4
WL 3
LL 17,58
LL 105,59
XS 249
XS 250
M 11,77,5
s_add_u32 s40, s40, 3072
LA 17
PK 127,18,125,33
XS 251
M 6,6,0
s_addc_u32 s41, s41, 0
LC_ 105
PK 125,18,125,32
s_add_u32 m0, s38, 3072
AW 24,248
AW 25,249
M 6,7,1
s_add_u32 s42, s44, s65
RA 228,1,22528
PO 127
AW 26,250
M 7,18,0
WL 4
LO 104,16,104
LU 16,104
AW 27,251
XS 52
M 7,19,1
s_addc_u32 s43, s45, 0
RA 232,1,23552
HZ 26,16
XS 53
M 10,66,0
DW 0
LL 18,60
LL 108,61
XS 54
XS 55
M 10,67,1
s_lshl_b32 s53, s52, 8
LA 18
HX 25
AW 28,52
M 11,78,0
WL 2
LC_ 108
W1 25,1024
W1 27,3072
AW 29,53
AW 30,54
M 11,79,1
s_add_u32 s48, %[alo], s53
RA 236,1,24576
v_add_u32 v4, s39, v4
AW 31,55
M 6,8,2
LO 105,17,105
LU 17,105
s_addc_u32 s49, %[ahi], 0
s_add_u32 s52, s52, %[d]
s_cmp_eq_u32 s52, %[wrap]
s_cselect_b32 s52, %[rst], s52
DW 1024
v_min_u32 v35, v35, v13
AR 248,32
M 6,9,3
ST 124
HS 26,17
AR 249,33
M 7,20,2
s_mul_i32 s53, s51, 49152
LL 16,62
LL 109,63
s_bitcmp1_b64 s[90:91], s51
s_cselect_b64 s[44:45], s[84:85], s[82:83]
s_add_u32 s44, s44, s53
s_addc_u32 s45, s45, 0
s_bitcmp1_b64 s[92:93], s51
s_cselect_b64 s[94:95], s[88:89], s[86:87]
s_add_u32 s94, s94, s53
s_addc_u32 s95, s95, 0
s_add_u32 s51, s51, %[d]
s_cmp_eq_u32 s51, %[wrap]
s_cselect_b32 s51, %[rst], s51
AR 250,34
AR 251,35
M 7,21,3
LA 16
DW 2048
XS 248
M 10,68,2
LC_ 109
DQ 3072
XS 249
XS 250
M 10,69,3
s_add_u32 s50, s50, %[d]
v_add_u32 v1, s37, v0
XS 251
M 11,80,2
WL 2
LO 108,18,108
LU 18,108
AW 32,248
AW 33,249
M 11,81,3
s_cmp_eq_u32 s50, %[wrap]
RA 216,1,0
HS 26,18
AW 34,250
M 6,10,4
s_cselect_b32 s50, %[rst], s50
LL 17,72
LL 120,73
AW 35,251
AR 52,36
M 6,11,5
LA 17
v_and_b32 v22, 0xfffffcff, v1
AR 53,37
M 7,22,4
LC_ 120
RA 220,1,1024
AR 54,38
AR 55,39
M 7,23,5
RV 252,22,5120
v_add_u32 v19, 1, v19
XS 52
M 10,70,4
WL 5
LO 109,16,109
LU 16,109
XS 53
XS 54
M 10,71,5
s_add_u32 s38, s38, 32768
RA 224,1,2048
HS 26,16
XS 55
M 11,82,4
WL 1
LL 18,74
LL 121,75
AW 36,52
AW 37,53
M 11,83,5
s_cmp_eq_u32 s38, s77
LA 18
RA 228,1,3072
s_cselect_b32 s38, s76, s38
FP
s_sub_u32 s39, 0, s39
AW 38,54
Z 0,0,0
LC_ 121
HX 26
AW 39,55
M 0,1,1
AR 248,40
AR 249,41
Z 1,12,0
LO 120,17,120
LU 17,120
AR 250,42
M 1,13,1
RA 232,1,4096
HZ 24,17
AR 251,43
XS 248
Z 2,24,0
LL 16,88
LL 124,89
XS 249
M 2,25,1
LA 16
XS 250
XS 251
Z 3,36,0
LC_ 124
AW 40,248
M 3,37,1
AW 41,249
AW 42,250
Z 4,60,0
WL 3
LO 121,18,121
LU 18,121
AW 43,251
M 4,61,1
RA 236,1,6144
HS 24,18
AR 52,44
AR 53,45
Z 5,72,0
LL 17,90
LL 125,91
AR 54,46
M 5,73,1
LA 17
AR 55,47
XS 52
M 0,2,2
LC_ 125
XS 53
M 0,3,3
XS 54
XS 55
M 1,14,2
WL 3
LO 124,16,124
LU 16,124
AW 44,52
M 1,15,3
RA 216,1,7168
HS 24,16
AW 45,53
AW 46,54
M 2,26,2
LL 18,80
LL 106,81
AW 47,55
M 2,27,3
LA 18
FR
AR 248,48
AR 249,49
M 3,38,2
LC_ 106
AR 250,50
M 3,39,3
WV 32
BW 48
AR 251,51
XS 248
M 4,62,2
WL 5
LO 125,17,125
LU 17,125
XS 249
M 4,63,3
RA 220,1,8192
HS 24,17
XS 250
XS 251
M 5,74,2
LL 16,82
LL 107,83
AW 48,248
M 5,75,3
s_mov_b32 m0, %[pm]
LA 16
DF
AW 49,249
AW 50,250
M 0,4,4
LC_ 107
RV 40,8,6144
AW 51,251
M 0,5,5
RV 44,8,6208
RV 48,8,6272
AR 52,52
AR 53,53
M 1,16,4
WL 7
LO 18,18,106
LU 106,18
AR 54,54
M 1,17,5
RA 224,1,9216
HZ 27,106
AR 55,55
XS 52
M 2,28,4
LL 17,84
LL 110,85
XS 53
M 2,29,5
LA 17
PK 106,18,104,33
XS 54
XS 55
M 3,40,4
LC_ 110
PK 104,18,104,32
AW 52,52
M 3,41,5
BR 48
v_cmp_gt_u32 vcc, v19, v15
AW 53,53
AW 54,54
M 4,64,4
WL 7
LO 16,16,107
LU 107,16
AW 55,55
M 4,65,5
s_add_u32 s42, s94, s60
RA 228,1,10240
HS 27,107
AR 248,56
M 5,76,4
LL 18,86
LL 111,87
AR 249,57
AR 250,58
M 5,77,5
s_cbranch_vccnz .Lk1s14
.Lk1r14:
LA 18
PK 107,16,105,33
AR 251,59
M 0,6,0
s_addc_u32 s43, s95, 0
LC_ 111
PK 105,16,105,32
XS 248
XS 249
M 0,7,1
SU 104
HX 24
XS 250
M 1,18,0
WL 4
LO 17,17,110
LU 110,17
XS 251
AW 56,248
M 1,19,1
RA 232,1,11264
HS 27,110
AW 57,249
M 2,30,0
LL 16,96
LL 122,97
)"
R"(AW 58,250
AW 59,251
M 2,31,1
LA 16
PK 110,17,108,33
AR 52,60
M 3,42,0
LC_ 122
PK 108,17,108,32
AR 53,61
AR 54,62
M 3,43,1
RV 252,22,5392
PO 110
AR 55,63
M 4,66,0
WL 4
LO 18,18,111
LU 111,18
XS 52
XS 53
M 4,67,1
s_add_u32 s42, s94, s61
RA 236,1,12288
HS 27,111
XS 54
M 5,78,0
LL 17,98
LL 123,99
XS 55
AW 60,52
M 5,79,1
s_addc_u32 s43, s95, 0
LA 17
PK 111,18,109,33
AW 61,53
M 0,8,2
LC_ 123
PK 109,18,109,32
AW 62,54
AW 63,55
M 0,9,3
W1 24,0
PO 111
AR 248,64
M 1,20,2
WL 5
LO 16,16,122
LU 122,16
W1 26,2048
AR 249,65
AR 250,66
M 1,21,3
RA 216,1,13312
HZ 25,122
AR 251,67
M 2,32,2
LL 18,100
LL 126,101
XS 248
XS 249
M 2,33,3
LA 18
PK 122,16,120,33
XS 250
M 3,44,2
LC_ 126
PK 120,16,120,32
XS 251
AW 64,248
M 3,45,3
WV 27
SU 108
HX 27
AW 65,249
BW 49
M 4,68,2
WL 6
LO 17,17,123
LU 123,17
AW 66,250
AW 67,251
M 4,69,3
s_add_u32 s42, s44, s64
RA 220,1,14336
BR 49
HS 25,123
AR 52,68
LZ 40,48
s_addc_u32 s43, s45, 0
BW 50
M 5,80,2
LL 16,102
LL 127,103
AR 53,69
AR 54,70
M 5,81,3
s_bitcmp1_b64 s[92:93], 48
LA 16
PK 123,17,121,33
AR 55,71
M 0,10,4
s_cselect_b32 s58, 16, 0
LC_ 127
PK 121,17,121,32
XS 52
XS 53
M 0,11,5
s_lshr_b32 s58, 0xda43cb52, s58
RV 40,8,6336
BR 50
XS 54
M 1,22,4
WL 8
LO 18,18,126
LU 126,18
XS 55
AW 68,52
M 1,23,5
WL 5
RA 224,1,15360
HS 25,126
AW 69,53
LS 44,49
WV 24
BW 51
M 2,34,4
LL 17,56
LL 104,57
AW 70,54
AW 71,55
M 2,35,5
LA 17
PK 126,18,124,33
M 3,46,4
LC_ 104
RV 44,8,6400
PK 124,18,124,32
M 3,47,5
ds_read_b64 v[14:15], v5 offset:0
BR 51
PO 126
M 4,70,4
WL 9
LO 16,16,127
LU 127,16
M 4,71,5
WL 7
RA 228,1,16384
HS 25,127
LS 48,50
WV 23
BW 52
M 5,82,4
LL 18,58
LL 105,59
M 5,83,5
LA 18
RV 48,8,6464
PK 127,16,125,33
Z 6,0,0
LC_ 105
PK 125,16,125,32
M 6,1,1
BR 52
PO 127
v_and_b32 v11, 0x1f0, v0
v_min_u32 v11, 368, v11
v_lshlrev_b32 v11, 3, v11
Z 7,12,0
WL 9
LO 104,17,104
LU 17,104
M 7,13,1
WL 6
RA 232,1,17408
HZ 26,17
LS 40,51
WV 22
BW 53
Z 8,24,0
LL 16,60
LL 108,61
ST 120
M 8,25,1
LA 16
RV 40,8,6528
Z 9,36,0
LC_ 108
BR 53
M 9,37,1
WL 6
v_add_f32 v14, v14, v15
HX 25
LS 44,52
WV 22
BW 54
Z 10,60,0
LO 105,18,105
LU 18,105
v_lshrrev_b32 v3, 9, v0
v_lshl_add_u32 v3, v3, 2, 0
v_bfe_u32 v3, s58, v3, 4
v_mad_u32_u24 v3, v3, s97, v11
M 10,61,1
s_add_u32 s42, s44, s65
RA 236,1,18432
HS 26,18
Z 11,72,0
RV 44,8,6592
LL 17,62
LL 109,63
M 11,73,1
s_addc_u32 s43, s45, 0
LA 17
W1 25,1024
BR 54
M 6,2,2
LC_ 109
W1 27,3072
M 6,3,3
WL 8
DY
v_add_f32_dpp v14, v14, v14 quad_perm:[1,0,3,2] row_mask:0xf bank_mask:0xf
LS 48,53
WV 22
BW 55
M 7,14,2
LO 108,16,108
LU 16,108
M 7,15,3
RA 216,1,19456
HS 26,16
M 8,26,2
WV 21
RV 48,8,6656
LL 18,64
LL 112,65
M 8,27,3
WL 5
LA 18
ST 124
BR 55
M 9,38,2
s_mul_i32 s53, s50, 25600
LC_ 112
v_add_f32_dpp v14, v14, v14 quad_perm:[2,3,0,1] row_mask:0xf bank_mask:0xf
M 9,39,3
s_add_u32 s40, %[wlo], s53
v_add_f32_dpp v14, v14, v14 row_half_mirror row_mask:0xf bank_mask:0xf
v_add_u32 v4, s39, v4
LS 40,54
s_addc_u32 s41, %[whi], 0
BW 56
M 10,62,2
WL 8
LO 109,17,109
LU 17,109
s_mov_b32 m0, s38
M 10,63,3
RA 220,1,20480
RV 40,8,6720
HS 26,17
M 11,74,2
BR 56
LL 16,66
LL 113,67
M 11,75,3
WV 19
LA 16
v_cvt_f64_f32 v[14:15], v14
v_lshrrev_b32 v3, 9, v0
v_lshl_add_u32 v3, v3, 2, 8
v_bfe_u32 v3, s58, v3, 4
v_mad_u32_u24 v3, v3, s97, v11
M 6,4,4
WL 6
LC_ 113
DW 0
M 6,5,5
s_add_u32 s37, s37, 32768
s_mov_b64 exec, s[54:55]
SP
s_mov_b64 exec, -1
LS 44,55
s_cmp_eq_u32 s37, s75
BW 57
M 7,16,4
WL 7
RV 44,8,6784
LO 112,18,112
LU 18,112
M 7,17,5
s_cselect_b32 s37, s74, s37
RA 224,1,21504
HZ 29,18
M 8,28,4
BR 57
LL 17,68
LL 116,69
M 8,29,5
WL 6
LA 17
ds_read_b64 v[14:15], v5 offset:2048
LS 48,56
WV 20
BW 58
M 9,40,4
LC_ 116
DY
M 9,41,5
DW 1024
RV 48,8,6848
M 10,64,4
WL 9
LO 113,16,113
LU 16,113
M 10,65,5
RA 228,1,22528
RA 240,7,0
HS 29,16
M 11,76,4
WV 20
BR 58
LL 18,70
LL 117,71
M 11,77,5
WL 8
LA 18
DW 2048
M 6,6,0
s_mul_i32 s53, s51, 49152
LC_ 117
v_subrev_u32 v5, s39, v5
M 6,7,1
s_bitcmp1_b64 s[90:91], s51
LS 40,57
s_cselect_b64 s[44:45], s[84:85], s[82:83]
BW 59
M 7,18,0
WL 3
RA 244,7,256
LO 116,17,116
LU 17,116
M 7,19,1
s_add_u32 s44, s44, s53
RA 232,1,23552
HS 29,17
M 8,30,0
s_addc_u32 s45, s45, 0
BR 59
LL 16,72
LL 120,73
M 8,31,1
s_bitcmp1_b64 s[92:93], s51
LA 16
v_add_f32 v14, v14, v15
s_cselect_b64 s[94:95], s[88:89], s[86:87]
s_add_u32 s94, s94, s53
s_addc_u32 s95, s95, 0
s_add_u32 s51, s51, %[d]
s_cmp_eq_u32 s51, %[wrap]
s_cselect_b32 s51, %[rst], s51
s_mul_i32 s53, %[d], 16384
s_add_u32 s62, s53, %[sb1]
s_add_u32 s63, s62, 512
LS 44,58
s_add_u32 s40, s40, 3072
RA 248,7,512
v_add_f32_dpp v14, v14, v14 quad_perm:[1,0,3,2] row_mask:0xf bank_mask:0xf
M 9,42,0
s_addc_u32 s41, s41, 0
LC_ 120
v_add_f32_dpp v14, v14, v14 quad_perm:[2,3,0,1] row_mask:0xf bank_mask:0xf
s_add_u32 m0, s38, 3072
M 9,43,1
DW 0
v_add_f32_dpp v14, v14, v14 row_half_mirror row_mask:0xf bank_mask:0xf
M 10,66,0
WL 7
LO 117,18,117
LU 18,117
M 10,67,1
WL 3
RA 236,1,24576
HS 29,18
LS 48,59
RA 252,7,768
v_cvt_f64_f32 v[14:15], v14
M 11,78,0
LL 17,74
LL 121,75
s_nop 2
v_mul_f32 v36, 0xbb2aaaab, v36
M 11,79,1
LA 17
DW 1024
v_mul_f32 v37, 0xbb2aaaab, v37
v_mul_f32 v38, 0xbb2aaaab, v38
M 6,8,2
)"
R"(LC_ 121
s_mov_b64 exec, s[54:55]
SP
s_mov_b64 exec, -1
v_mul_f32 v39, 0xbb2aaaab, v39
M 6,9,3
DW 2048
v_add_u32 v1, s37, v0
LK 40,240,48,0
LK 44,242,48,2
s_lshl_b32 s53, s52, 8
RA 240,7,1024
v_pk_add_f16 v26, v26, v29
LK 48,244,49,0
CF 40,40,41
LK 248,246,49,2
s_add_u32 s48, %[alo], s53
RA 244,7,1280
CF 42,42,43
M 7,20,2
WL 6
LO 120,16,120
LU 16,120
M 7,21,3
s_addc_u32 s49, %[ahi], 0
RA 216,1,0
HZ 24,16
CF 44,44,45
M 8,32,2
s_add_u32 s52, s52, %[d]
LL 18,76
LL 124,77
CF 46,46,47
AW 72,40
M 8,33,3
s_cmp_eq_u32 s52, %[wrap]
LA 18
v_and_b32 v22, 0xfffffcff, v1
AW 73,42
AW 74,44
M 9,44,2
s_cselect_b32 s52, %[rst], s52
LC_ 124
DQ 3072
AW 75,46
M 9,45,3
HX 26
LK 40,248,50,0
CF 48,48,49
LK 44,250,50,2
RA 248,7,1536
CF 50,50,51
GZ 48
CF 248,248,249
CF 250,250,251
M 10,68,2
WL 6
LO 121,17,121
LU 17,121
M 10,69,3
s_add_u32 s50, s50, %[d]
RA 220,1,1024
HS 24,17
AW 76,48
M 11,80,2
LL 16,78
LL 125,79
AW 77,50
AW 78,248
AW 79,250
M 11,81,3
s_cmp_eq_u32 s50, %[wrap]
LA 16
RV 252,22,5120
LK 48,252,51,0
CF 40,40,41
LK 248,254,51,2
s_cselect_b32 s50, %[rst], s50
RA 252,7,1792
CF 42,42,43
GS 49
CF 44,44,45
CF 46,46,47
M 6,10,4
LC_ 125
AW 80,40
M 6,11,5
v_add_u32 v19, 1, v19
AW 81,42
M 7,22,4
WL 6
LO 124,18,124
LU 18,124
M 7,23,5
s_add_u32 s38, s38, 32768
RA 224,1,2048
HS 24,18
AW 82,44
AW 83,46
LK 40,240,52,0
CF 48,48,49
LK 44,242,52,2
s_cmp_eq_u32 s38, s77
RA 240,7,2048
CF 50,50,51
GS 50
s_cselect_b32 s38, s76, s38
CF 248,248,249
CF 250,250,251
M 8,34,4
LL 17,80
LL 106,81
M 8,35,5
LA 17
FP
M 9,46,4
s_sub_u32 s39, 0, s39
LC_ 106
AW 84,48
M 9,47,5
AW 85,50
AW 86,248
M 10,70,4
WL 5
LO 125,16,125
LU 16,125
AW 87,250
M 10,71,5
RA 228,1,3072
HS 24,16
LK 48,244,53,0
CF 40,40,41
LK 248,246,53,2
RA 244,7,2304
CF 42,42,43
GS 51
CF 44,44,45
CF 46,46,47
M 11,82,4
LL 18,82
LL 107,83
M 11,83,5
LA 18
HX 24
Z 0,0,0
LC_ 107
AW 88,40
M 0,1,1
AW 89,42
AW 90,44
AW 91,46
LK 40,248,54,0
CF 48,48,49
LK 44,250,54,2
RA 248,7,2560
CF 50,50,51
GS 52
CF 248,248,249
CF 250,250,251
Z 1,12,0
WL 5
LO 17,17,106
LU 106,17
M 1,13,1
RA 232,1,4096
HZ 27,106
Z 2,24,0
LL 16,84
LL 110,85
M 2,25,1
LA 16
PK 106,17,104,33
AW 92,48
Z 3,36,0
LC_ 110
PK 104,17,104,32
AW 93,50
AW 94,248
AW 95,250
M 3,37,1
LK 48,252,55,0
CF 40,40,41
LK 248,254,55,2
RA 252,7,2816
CF 42,42,43
GS 53
CF 44,44,45
CF 46,46,47
Z 4,60,0
WL 5
LO 18,18,107
LU 107,18
M 4,61,1
s_add_u32 s42, s94, s60
RA 236,1,6144
HS 27,107
Z 5,72,0
LL 17,86
LL 111,87
AW 96,40
AW 97,42
M 5,73,1
s_addc_u32 s43, s95, 0
LA 17
PK 107,18,105,33
AW 98,44
AW 99,46
LK 40,240,56,0
CF 48,48,49
LK 44,242,56,2
CF 50,50,51
GS 54
CF 248,248,249
CF 250,250,251
M 0,2,2
LC_ 111
PK 105,18,105,32
M 0,3,3
SU 104
AW 100,48
M 1,14,2
WL 4
LO 16,16,110
LU 110,16
M 1,15,3
RA 216,1,7168
HS 27,110
AW 101,50
AW 102,248
M 2,26,2
LL 18,88
LL 114,89
AW 103,250
M 2,27,3
LA 18
PK 110,16,108,33
LK 48,244,57,0
CF 40,40,41
LK 248,246,57,2
CF 42,42,43
GS 55
CF 44,44,45
CF 46,46,47
M 3,38,2
LC_ 114
PK 108,16,108,32
M 3,39,3
FR
PO 110
AW 104,40
M 4,62,2
WL 4
LO 17,17,111
LU 111,17
AW 105,42
AW 106,44
AW 107,46
M 4,63,3
s_add_u32 s42, s94, s61
RA 220,1,8192
HS 27,111
LK 40,248,58,0
CF 48,48,49
LK 44,250,58,2
s_addc_u32 s43, s95, 0
CF 50,50,51
GS 56
CF 248,248,249
CF 250,250,251
M 5,74,2
LL 16,90
LL 115,91
M 5,75,3
s_mov_b32 m0, %[pm]
LA 16
PK 111,17,109,33
M 0,4,4
LC_ 115
PK 109,17,109,32
AW 108,48
AW 109,50
M 0,5,5
DF
PO 111
AW 110,248
AW 111,250
LK 48,252,59,0
CF 40,40,41
LK 248,254,59,2
CF 42,42,43
GS 57
CF 44,44,45
CF 46,46,47
M 1,16,4
WL 4
LO 18,18,114
LU 114,18
M 1,17,5
RA 224,1,9216
HZ 29,114
M 2,28,4
LL 17,92
LL 118,93
AW 112,40
M 2,29,5
LA 17
PK 114,18,112,33
AW 113,42
AW 114,44
M 3,40,4
LC_ 118
RV 36,8,6912
PK 112,18,112,32
AW 115,46
M 3,41,5
WL 7
SU 108
v_cmp_gt_u32 vcc, v19, v15
GS 58
CF 48,48,49
CF 50,50,51
M 4,64,4
WL 4
LO 16,16,115
LU 115,16
CF 248,248,249
CF 250,250,251
AW 116,48
M 4,65,5
s_add_u32 s42, s94, s62
RA 228,1,10240
HS 29,115
AW 117,50
AW 118,248
M 5,76,4
LL 18,94
LL 119,95
AW 119,250
M 5,77,5
s_cbranch_vccnz .Lk1s15
.Lk1r15:
LA 18
PK 115,16,113,33
GS 59
s_addc_u32 s43, s95, 0
W1 24,0
s_nop 5
v_cndmask_b32 v52, v52, v53, s[78:79]
M 0,6,0
LC_ 119
PK 113,16,113,32
v_cndmask_b32 v54, v54, v55, s[78:79]
M 0,7,1
W1 26,2048
v_cndmask_b32 v52, v52, v54, s[80:81]
M 1,18,0
WL 6
LO 17,17,118
LU 118,17
v_mul_f32 v52, 0x3b2aaaab, v52
M 1,19,1
WL 5
RA 232,1,11264
HS 29,118
v_med3_f32 v53, v52, v39, v37
M 2,30,0
LL 16,96
LL 122,97
LG 52,53
M 2,31,1
LA 16
PK 118,17,116,33
C 52,52,52
M 3,42,0
LC_ 122
PK 116,17,116,32
v_lshlrev_b32 v52, 16, v52
M 3,43,1
RV 252,22,5392
PO 118
v_add_f32 v52, v36, v52
M 4,66,0
WL 5
LO 18,18,119
LU 119,18
C 52,52,52
M 4,67,1
RA 236,1,12288
HS 29,119
M 5,78,0
LL 17,98
LL 123,99
v_lshlrev_b32 v52, 16, v52
M 5,79,1
LA 17
PK 119,18,117,33
v_sqrt_f32 v52, v52
M 0,8,2
LC_ 123
PK 117,18,117,32
)"
R"(C 52,52,52
M 0,9,3
SU 112
PO 119
v_lshlrev_b32 v52, 16, v52
M 1,20,2
WL 4
LO 16,16,122
LU 122,16
v_rcp_f32 v52, v52
M 1,21,3
s_add_u32 s42, s94, s63
RA 216,1,13312
HZ 25,122
v_mul_f32 v52, v38, v52
M 2,32,2
s_addc_u32 s43, s95, 0
LL 18,100
LL 126,101
CF 52,52,52
M 2,33,3
LA 18
PK 122,16,120,33
ds_bpermute_b32 v13, v28, v52
M 3,44,2
LC_ 126
PK 120,16,120,32
M 3,45,3
SU 116
v_pk_add_f16 v27, v27, v29
M 4,68,2
WL 4
LO 17,17,123
LU 123,17
M 4,69,3
s_add_u32 s42, s44, s64
RA 220,1,14336
HS 25,123
M 5,80,2
LL 16,102
LL 127,103
M 5,81,3
s_addc_u32 s43, s45, 0
LA 16
PK 123,17,121,33
M 0,10,4
LC_ 127
PK 121,17,121,32
M 0,11,5
ST 120
HX 27
M 1,22,4
WL 3
LO 18,18,126
LU 126,18
M 1,23,5
RA 224,1,15360
HS 25,126
AR 248,72
M 2,34,4
LL 17,56
LL 104,57
AR 249,73
M 2,35,5
LA 17
PK 126,18,124,33
AR 250,74
M 3,46,4
LC_ 104
PK 124,18,124,32
AR 251,75
M 3,47,5
ds_read_b64 v[14:15], v5 offset:0
PO 126
AR 52,76
M 4,70,4
WL 4
LO 16,16,127
LU 127,16
AR 53,77
M 4,71,5
s_add_u32 s42, s44, s65
RA 228,1,16384
HS 25,127
AR 54,78
M 5,82,4
LL 18,58
LL 105,59
AR 55,79
M 5,83,5
s_addc_u32 s43, s45, 0
LA 18
PK 127,16,125,33
XS 248
Z 6,0,0
LC_ 105
PK 125,16,125,32
XS 249
M 6,1,1
PO 127
HX 25
XS 250
Z 7,12,0
WL 4
LO 104,17,104
LU 17,104
XS 251
AW 72,248
M 7,13,1
RA 232,1,17408
HZ 26,17
AW 73,249
Z 8,24,0
WL 4
LL 16,60
LL 108,61
AW 74,250
M 8,25,1
s_mul_i32 s53, s50, 25600
LA 16
W1 25,1024
AW 75,251
Z 9,36,0
s_add_u32 s40, %[wlo], s53
LC_ 108
W1 27,3072
XS 52
M 9,37,1
s_addc_u32 s41, %[whi], 0
v_add_f32 v14, v14, v15
v_add_u32 v4, s39, v4
XS 53
Z 10,60,0
WL 5
LO 105,18,105
LU 18,105
XS 54
M 10,61,1
s_mov_b32 m0, s38
RA 236,1,18432
HS 26,18
XS 55
Z 11,72,0
LL 17,62
LL 109,63
AW 76,52
M 11,73,1
LA 17
ST 124
AW 77,53
M 6,2,2
LC_ 109
DW 0
AW 78,54
AW 79,55
M 6,3,3
v_add_f32_dpp v14, v14, v14 quad_perm:[1,0,3,2] row_mask:0xf bank_mask:0xf
v_min_u32 v35, v35, v13
M 7,14,2
WL 4
LO 108,16,108
LU 16,108
AR 248,80
M 7,15,3
s_add_u32 s37, s37, 32768
RA 216,1,19456
HS 26,16
AR 249,81
M 8,26,2
s_cmp_eq_u32 s37, s75
LL 18,64
LL 112,65
AR 250,82
M 8,27,3
s_cselect_b32 s37, s74, s37
LA 18
v_add_f32_dpp v14, v14, v14 quad_perm:[2,3,0,1] row_mask:0xf bank_mask:0xf
AR 251,83
M 9,38,2
s_mul_i32 s53, %[d], 393216
LC_ 112
v_add_f32_dpp v14, v14, v14 row_half_mirror row_mask:0xf bank_mask:0xf
XS 248
M 9,39,3
s_add_u32 s53, s53, s57
v_cvt_f64_f32 v[14:15], v14
XS 249
M 10,62,2
WL 3
LO 109,17,109
LU 17,109
XS 250
M 10,63,3
s_add_u32 s46, %[xlo], s53
RA 220,1,20480
HS 26,17
XS 251
M 11,74,2
LL 16,66
LL 113,67
AW 80,248
AW 81,249
M 11,75,3
s_addc_u32 s47, %[xhi], 0
LA 16
s_mov_b64 exec, s[54:55]
SP
s_mov_b64 exec, -1
AW 82,250
M 6,4,4
s_bitcmp1_b64 s[92:93], 48
LC_ 113
ds_read_b64 v[14:15], v5 offset:2048
AW 83,251
M 6,5,5
s_cselect_b32 s58, 16, 0
v_subrev_u32 v5, s39, v5
AR 52,84
M 7,16,4
WL 4
LO 112,18,112
LU 18,112
AR 53,85
M 7,17,5
s_lshr_b32 s58, 0xda43cb52, s58
RA 224,1,21504
HZ 29,18
AR 54,86
M 8,28,4
LL 17,68
LL 116,69
AR 55,87
M 8,29,5
LA 17
v_lshrrev_b32 v3, 6, v0
XS 52
M 9,40,4
LC_ 116
v_and_b32 v3, 12, v3
v_bfe_u32 v3, s58, v3, 4
v_mad_u32_u24 v3, v3, s97, v21
XS 53
M 9,41,5
DW 1024
XS 54
M 10,64,4
WL 4
LO 113,16,113
LU 16,113
XS 55
AW 84,52
M 10,65,5
RA 228,1,22528
HS 29,16
AW 85,53
M 11,76,4
LL 18,70
LL 117,71
AW 86,54
M 11,77,5
LA 18
XL 60,0
AW 87,55
M 6,6,0
LC_ 117
DW 2048
AR 248,88
M 6,7,1
WL 6
XL 61,64
v_add_f32 v14, v14, v15
AR 249,89
M 7,18,0
WL 3
LO 116,17,116
LU 17,116
AR 250,90
M 7,19,1
s_mul_i32 s53, s51, 49152
RA 232,1,23552
HS 29,17
AR 251,91
M 8,30,0
s_bitcmp1_b64 s[90:91], s51
LL 16,72
LL 120,73
XS 248
M 8,31,1
s_cselect_b64 s[44:45], s[84:85], s[82:83]
LA 16
v_add_f32_dpp v14, v14, v14 quad_perm:[1,0,3,2] row_mask:0xf bank_mask:0xf
s_add_u32 s44, s44, s53
s_addc_u32 s45, s45, 0
s_bitcmp1_b64 s[92:93], s51
s_cselect_b64 s[94:95], s[88:89], s[86:87]
s_add_u32 s94, s94, s53
s_addc_u32 s95, s95, 0
s_add_u32 s51, s51, %[d]
s_cmp_eq_u32 s51, %[wrap]
s_cselect_b32 s51, %[rst], s51
XS 249
M 9,42,0
s_add_u32 s40, s40, 3072
LC_ 120
v_add_f32_dpp v14, v14, v14 quad_perm:[2,3,0,1] row_mask:0xf bank_mask:0xf
s_addc_u32 s41, s41, 0
s_add_u32 m0, s38, 3072
XS 250
XS 251
M 9,43,1
XL 62,128
v_add_f32_dpp v14, v14, v14 row_half_mirror row_mask:0xf bank_mask:0xf
AW 88,248
M 10,66,0
WL 3
LO 117,18,117
LU 18,117
AW 89,249
M 10,67,1
RA 236,1,24576
HS 29,18
AW 90,250
M 11,78,0
LL 17,74
LL 121,75
AW 91,251
M 11,79,1
LA 17
DW 0
AR 52,92
M 6,8,2
LC_ 121
XL 63,192
AR 53,93
M 6,9,3
DW 1024
v_add_u32 v1, s37, v0
AR 54,94
M 7,20,2
WL 3
LO 120,16,120
LU 16,120
AR 55,95
M 7,21,3
RA 216,1,0
HZ 24,16
XS 52
M 8,32,2
LL 18,76
LL 124,77
v_cvt_f64_f32 v[14:15], v14
XS 53
XS 54
M 8,33,3
LA 18
s_mov_b64 exec, s[54:55]
SP
s_mov_b64 exec, -1
XS 55
M 9,44,2
LC_ 124
XL 64,256
AW 92,52
M 9,45,3
DW 2048
v_and_b32 v22, 0xfffffcff, v1
AW 93,53
M 10,68,2
WL 3
LO 121,17,121
LU 17,121
AW 94,54
M 10,69,3
RA 220,1,1024
HS 24,17
AW 95,55
M 11,80,2
LL 16,78
LL 125,79
AR 248,96
M 11,81,3
LA 16
RV 252,22,5120
AR 249,97
M 6,10,4
LC_ 125
XL 65,320
AR 250,98
M 6,11,5
s_lshl_b32 s53, s52, 8
v_pk_add_f16 v26, v26, v29
AR 251,99
M 7,22,4
WL 4
LO 124,18,124
LU 18,124
HX 26
XS 248
XS 249
M 7,23,5
)"
R"(s_add_u32 s48, %[alo], s53
RA 224,1,2048
HS 24,18
XS 250
M 8,34,4
s_addc_u32 s49, %[ahi], 0
LL 17,80
LL 106,81
s_add_u32 s52, s52, %[d]
s_cmp_eq_u32 s52, %[wrap]
s_cselect_b32 s52, %[rst], s52
XS 251
M 8,35,5
LA 17
DQ 3072
AW 96,248
M 9,46,4
LC_ 106
XL 66,384
AW 97,249
M 9,47,5
s_add_u32 s50, s50, %[d]
AW 98,250
AW 99,251
M 10,70,4
WL 3
LO 125,16,125
LU 16,125
M 10,71,5
RA 228,1,3072
HS 24,16
AR 52,100
M 11,82,4
LL 18,82
LL 107,83
AR 53,101
M 11,83,5
LA 18
HX 24
s_cmp_eq_u32 s50, %[wrap]
s_cselect_b32 s50, %[rst], s50
XL 67,448
AR 54,102
Z 0,0,0
WV 40
LC_ 107
XL 68,512
v_add_u32 v19, 1, v19
s_add_u32 s38, s38, 32768
s_cmp_eq_u32 s38, s77
s_cselect_b32 s38, s76, s38
FP
s_sub_u32 s39, 0, s39
AR 55,103
XS 52
M 0,1,1
XL 69,576
XS 53
Z 1,12,0
WL 4
LO 17,17,106
LU 106,17
XS 54
M 1,13,1
RA 232,1,4096
HZ 27,106
XS 55
Z 2,24,0
LL 16,84
LL 110,85
AW 100,52
M 2,25,1
LA 16
PK 106,17,104,33
AW 101,53
Z 3,36,0
WL 5
LC_ 110
XL 70,640
PK 104,17,104,32
AW 102,54
M 3,37,1
RA 236,1,6144
AW 103,55
M 0,2,2
WL 5
LO 18,18,107
LU 107,18
AR 248,104
M 0,3,3
FR
HS 27,107
AR 249,105
M 1,14,2
LL 17,86
LL 111,87
AR 250,106
AR 251,107
M 1,15,3
LA 17
PK 107,18,105,33
XS 248
M 2,26,2
LC_ 111
XL 71,704
PK 105,18,105,32
XS 249
M 2,27,3
s_add_u32 s42, s94, s60
RA 216,1,7168
XS 250
M 3,38,2
WL 4
LO 16,16,110
LU 110,16
XS 251
M 3,39,3
s_addc_u32 s43, s95, 0
RA 220,1,8192
HS 27,110
AW 104,248
M 0,4,4
LL 18,88
LL 114,89
AW 105,249
M 0,5,5
LA 18
PK 110,16,108,33
AW 106,250
M 1,16,4
LC_ 114
PK 108,16,108,32
AW 107,251
M 1,17,5
RA 224,1,9216
PO 110
AR 52,108
M 2,28,4
WL 5
LO 17,17,111
LU 111,17
SU 104
AR 53,109
AR 54,110
M 2,29,5
s_add_u32 s42, s94, s61
HS 27,111
v_cmp_gt_u32 vcc, v19, v15
AR 55,111
M 3,40,4
WL 3
LL 16,90
LL 115,91
XS 52
M 3,41,5
s_addc_u32 s43, s95, 0
LA 16
PK 111,17,109,33
XS 53
M 0,6,0
s_cbranch_vccnz .Lk1s16
.Lk1r16:
LC_ 115
PK 109,17,109,32
XS 54
M 0,7,1
WV 18
RA 228,1,10240
PO 111
XS 55
BW 60
M 1,18,0
WL 5
LO 18,18,114
LU 114,18
AW 108,52
M 1,19,1
RA 232,1,11264
RV 40,8,6144
HZ 29,114
AW 109,53
M 2,30,0
RV 44,8,6208
LL 17,92
LL 118,93
AW 110,54
M 2,31,1
LA 17
RV 48,8,6272
PK 114,18,112,33
AW 111,55
M 3,42,0
WL 6
LC_ 118
BR 60
PK 112,18,112,32
AR 248,112
AR 249,113
M 3,43,1
WV 16
RV 252,22,5392
ds_read_b64 v[14:15], v5 offset:0
W1 24,0
W1 26,2048
BW 61
AR 250,114
M 0,8,2
BR 61
LO 16,16,115
LU 115,16
AR 251,115
M 0,9,3
WL 6
RA 236,1,12288
HS 29,115
XS 248
LZ 40,60
WV 15
BW 62
M 1,20,2
LL 18,94
LL 119,95
XS 249
M 1,21,3
LA 18
RV 40,8,6336
PK 115,16,113,33
XS 250
M 2,32,2
LC_ 119
BR 62
PK 113,16,113,32
XS 251
M 2,33,3
WL 6
RA 216,1,13312
AW 112,248
LS 44,61
WV 13
BW 63
M 3,44,2
WL 7
LO 17,17,118
LU 118,17
SU 108
AW 113,249
M 3,45,3
s_add_u32 s42, s94, s62
RA 220,1,14336
HS 29,118
s_addc_u32 s43, s95, 0
RV 44,8,6400
AW 114,250
M 0,10,4
BR 63
LL 16,96
LL 122,97
AW 115,251
AR 52,116
M 0,11,5
WL 5
LA 16
PK 118,17,116,33
AR 53,117
LS 48,62
WV 11
BW 64
M 1,22,4
LC_ 122
PK 116,17,116,32
AR 54,118
M 1,23,5
RA 224,1,15360
RV 48,8,6464
PO 118
AR 55,119
M 2,34,4
LO 18,18,119
LU 119,18
SU 112
XS 52
M 2,35,5
s_add_u32 s42, s94, s63
BR 64
HS 29,119
XS 53
M 3,46,4
WL 6
LL 17,98
LL 123,99
XS 54
M 3,47,5
s_addc_u32 s43, s95, 0
LA 17
PK 119,18,117,33
XS 55
LS 40,63
WV 10
BW 65
Z 6,0,0
LC_ 123
PK 117,18,117,32
v_add_f32 v14, v14, v15
s_nop 1
v_add_f32_dpp v14, v14, v14 quad_perm:[1,0,3,2] row_mask:0xf bank_mask:0xf
s_nop 1
v_add_f32_dpp v14, v14, v14 quad_perm:[2,3,0,1] row_mask:0xf bank_mask:0xf
s_nop 1
v_add_f32_dpp v14, v14, v14 row_half_mirror row_mask:0xf bank_mask:0xf
v_cvt_f64_f32 v[14:15], v14
s_mov_b64 exec, s[54:55]
SP
s_mov_b64 exec, -1
s_nop 0
ds_read_b64 v[14:15], v5 offset:2048
RV 40,8,6528
AW 116,52
M 6,1,1
WL 5
RA 228,1,16384
BR 65
PO 119
AW 117,53
LS 44,64
WV 9
BW 66
Z 7,12,0
LO 16,16,122
LU 122,16
AW 118,54
AW 119,55
M 7,13,1
s_mul_i32 s53, s50, 25600
RA 232,1,17408
HZ 25,122
Z 8,24,0
s_add_u32 s40, %[wlo], s53
RV 44,8,6592
LL 18,100
LL 126,101
M 8,25,1
s_addc_u32 s41, %[whi], 0
LA 18
BR 66
PK 122,16,120,33
v_pk_add_f16 v27, v27, v29
Z 9,36,0
WL 5
LC_ 126
PK 120,16,120,32
HX 27
M 9,37,1
s_mov_b32 m0, s38
RA 236,1,18432
v_subrev_u32 v5, s39, v5
LS 48,65
WV 8
BW 67
M 6,2,2
RV 48,8,6656
LO 17,17,123
LU 123,17
M 6,3,3
DW 0
BR 67
HS 25,123
M 7,14,2
LL 16,102
LL 127,103
M 7,15,3
WL 5
LA 16
PK 123,17,121,33
LS 40,66
WV 8
BW 68
M 8,26,2
LC_ 127
RV 40,8,6720
PK 121,17,121,32
M 8,27,3
WV 7
RA 216,1,19456
BR 68
M 9,38,2
WL 6
LO 18,18,126
LU 126,18
SU 116
M 9,39,3
s_add_u32 s42, s44, s64
RA 220,1,20480
HS 25,126
s_addc_u32 s43, s45, 0
LS 44,67
s_add_u32 s37, s37, 32768
BW 69
M 6,4,4
s_cmp_eq_u32 s37, s75
LL 17,56
)"
R"(LL 104,57
s_cselect_b32 s37, s74, s37
M 6,5,5
LA 17
PK 126,18,124,33
v_add_f32 v14, v14, v15
s_nop 1
v_add_f32_dpp v14, v14, v14 quad_perm:[1,0,3,2] row_mask:0xf bank_mask:0xf
s_nop 1
v_add_f32_dpp v14, v14, v14 quad_perm:[2,3,0,1] row_mask:0xf bank_mask:0xf
s_nop 1
v_add_f32_dpp v14, v14, v14 row_half_mirror row_mask:0xf bank_mask:0xf
v_cvt_f64_f32 v[14:15], v14
s_mov_b64 exec, s[54:55]
SP
s_mov_b64 exec, -1
DW 1024
RV 44,8,6784
M 7,16,4
LC_ 104
BR 69
PK 124,18,124,32
M 7,17,5
WL 6
RA 224,1,21504
PO 126
LS 48,68
WV 9
BW 70
M 8,28,4
LO 16,16,127
LU 127,16
ST 120
M 8,29,5
WV 9
DW 2048
RV 48,8,6848
HS 25,127
M 9,40,4
WL 3
RA 240,7,0
LL 18,58
LL 105,59
M 9,41,5
s_add_u32 s40, s40, 3072
LA 18
BR 70
PK 127,16,125,33
M 6,6,0
s_addc_u32 s41, s41, 0
LC_ 105
PK 125,16,125,32
s_add_u32 m0, s38, 3072
M 6,7,1
s_add_u32 s42, s44, s65
RA 228,1,22528
PO 127
LS 40,69
s_addc_u32 s43, s45, 0
BW 71
M 7,18,0
RA 244,7,256
LO 104,17,104
LU 17,104
M 7,19,1
RA 232,1,23552
BR 71
HZ 26,17
M 8,30,0
WL 6
DW 0
LL 16,60
LL 108,61
M 8,31,1
s_lshl_b32 s53, s52, 8
LA 16
HX 25
LS 44,70
s_add_u32 s48, %[alo], s53
RA 248,7,512
M 9,42,0
WL 2
LC_ 108
W1 25,1024
W1 27,3072
M 9,43,1
s_addc_u32 s49, %[ahi], 0
RA 236,1,24576
v_add_u32 v4, s39, v4
LS 48,71
s_add_u32 s52, s52, %[d]
RA 252,7,768
M 6,8,2
LO 105,18,105
LU 18,105
s_cmp_eq_u32 s52, %[wrap]
s_cselect_b32 s52, %[rst], s52
DW 1024
v_mul_f32 v36, 0xbb2aaaab, v36
M 6,9,3
DW 2048
HS 26,18
v_mul_f32 v37, 0xbb2aaaab, v37
v_mul_f32 v38, 0xbb2aaaab, v38
M 7,20,2
LL 17,62
LL 109,63
v_mul_f32 v39, 0xbb2aaaab, v39
M 7,21,3
LA 17
ST 124
LK 40,240,60,0
LK 44,242,60,2
s_mul_i32 s53, s51, 49152
RA 240,7,1024
v_add_u32 v1, s37, v0
LK 48,244,61,0
CF 40,40,41
LK 248,246,61,2
s_bitcmp1_b64 s[92:93], s51
RA 244,7,1280
CF 42,42,43
M 8,32,2
s_cselect_b64 s[94:95], s[88:89], s[86:87]
LC_ 109
RA 216,1,0
M 8,33,3
s_add_u32 s94, s94, s53
v_and_b32 v22, 0xfffffcff, v1
CF 44,44,45
CF 46,46,47
M 9,44,2
WL 6
LO 108,16,108
LU 16,108
AW 120,40
AW 121,42
M 9,45,3
s_addc_u32 s95, s95, 0
RA 220,1,1024
HS 26,16
s_add_u32 s51, s51, %[d]
s_cmp_eq_u32 s51, %[wrap]
s_cselect_b32 s51, %[rst], s51
DQ 3072
AW 122,44
AW 123,46
LK 40,248,62,0
CF 48,48,49
LK 44,250,62,2
s_add_u32 s50, s50, %[d]
RA 248,7,1536
CF 50,50,51
GZ 60
s_cmp_eq_u32 s50, %[wrap]
CF 248,248,249
CF 250,250,251
M 6,10,4
s_cselect_b32 s50, %[rst], s50
LL 18,64
LL 112,65
M 6,11,5
LA 18
RV 252,22,5120
M 7,22,4
WL 7
LC_ 112
v_add_u32 v19, 1, v19
AW 124,48
M 7,23,5
s_add_u32 s38, s38, 32768
RA 224,1,2048
AW 125,50
AW 126,248
AW 127,250
LK 48,252,63,0
CF 40,40,41
LK 248,254,63,2
s_cmp_eq_u32 s38, s77
RA 252,7,1792
CF 42,42,43
GS 61
s_cselect_b32 s38, s76, s38
CF 44,44,45
CF 46,46,47
M 8,34,4
WL 7
LO 109,17,109
LU 17,109
M 8,35,5
FP
HS 26,17
M 9,46,4
WL 4
LL 16,66
LL 113,67
M 9,47,5
s_sub_u32 s39, 0, s39
LA 16
RA 228,1,3072
AW 128,40
AW 129,42
Z 0,0,0
LC_ 113
AW 130,44
AW 131,46
M 0,1,1
LK 40,240,64,0
CF 48,48,49
LK 44,242,64,2
RA 240,7,2048
CF 50,50,51
GS 62
CF 248,248,249
CF 250,250,251
Z 1,12,0
WL 7
LO 112,18,112
LU 18,112
M 1,13,1
RA 232,1,4096
HZ 29,18
AW 132,48
Z 2,24,0
LL 17,68
LL 116,69
AW 133,50
AW 134,248
M 2,25,1
LA 17
FR
AW 135,250
LK 48,244,65,0
CF 40,40,41
LK 248,246,65,2
RA 244,7,2304
CF 42,42,43
GS 63
CF 44,44,45
CF 46,46,47
Z 10,36,0
WL 6
LC_ 116
AW 136,40
M 10,37,1
RA 236,1,6144
AW 137,42
M 0,2,2
WL 7
LO 113,16,113
LU 16,113
M 0,3,3
HS 29,16
AW 138,44
AW 139,46
LK 40,248,66,0
CF 48,48,49
LK 44,250,66,2
RA 248,7,2560
CF 50,50,51
GS 64
CF 248,248,249
CF 250,250,251
M 1,14,2
LL 18,70
LL 117,71
M 1,15,3
LA 18
RA 216,1,7168
M 2,26,2
WL 6
LC_ 117
v_cmp_gt_u32 vcc, v19, v15
M 2,27,3
AW 140,48
AW 141,50
AW 142,248
M 10,38,2
WL 4
LO 116,17,116
LU 17,116
AW 143,250
M 10,39,3
RA 220,1,8192
HS 29,17
LK 48,252,67,0
CF 40,40,41
LK 248,254,67,2
RA 252,7,2816
CF 42,42,43
GS 65
CF 44,44,45
CF 46,46,47
M 0,4,4
s_cbranch_vccnz .Lk1s17
.Lk1r17:
LL 16,80
LL 106,81
M 0,5,5
LA 16
ds_read_b64 v[14:15], v5 offset:0
AW 144,40
AW 145,42
M 1,16,4
LC_ 106
AW 146,44
AW 147,46
M 1,17,5
RA 224,1,9216
LK 40,240,68,0
CF 48,48,49
LK 44,242,68,2
CF 50,50,51
GS 66
CF 248,248,249
CF 250,250,251
M 2,28,4
WL 6
LO 117,18,117
LU 18,117
M 2,29,5
HS 29,18
AW 148,48
M 10,40,4
WL 5
LL 17,82
LL 107,83
AW 149,50
AW 150,248
AW 151,250
M 10,41,5
LA 17
RA 228,1,10240
LK 48,244,69,0
CF 40,40,41
LK 248,246,69,2
CF 42,42,43
v_pk_add_f16 v26, v26, v29
GS 67
CF 44,44,45
CF 46,46,47
M 0,6,0
LC_ 107
HX 26
M 0,7,1
W1 26,2048
AW 152,40
M 1,18,0
WL 5
LO 16,16,106
LU 106,16
AW 153,42
AW 154,44
M 1,19,1
RA 232,1,11264
HZ 27,106
AW 155,46
LK 40,248,70,0
CF 48,48,49
LK 44,250,70,2
CF 50,50,51
v_add_f32 v14, v14, v15
GS 68
CF 248,248,249
CF 250,250,251
M 2,30,0
LL 18,84
LL 110,85
M 2,31,1
LA 18
PK 106,16,104,33
M 10,42,0
WL 4
LC_ 110
PK 104,16,104,32
AW 156,48
AW 157,50
AW 158,248
M 10,43,1
RV 252,22,5392
v_add_f32_dpp v14, v14, v14 quad_perm:[1,0,3,2] row_mask:0xf bank_mask:0xf
AW 159,250
LK 48,252,71,0
CF 40,40,41
LK 248,254,71,2
CF 42,42,43
v_add_f32_dpp v14, v14, v14 quad_perm:[2,3,0,1] row_mask:0xf bank_mask:0xf
GS 69
CF 44,44,45
CF 46,46,47
M 0,8,2
WL 5
LO 17,17,107
LU 107,17
M 0,9,3
s_add_u32 s42, s94, s60
RA 236,1,12288
HS 27,107
M 1,20,2
s_addc_u32 s43, s95, 0
)"
R"(LL 16,86
LL 111,87
AW 160,40
AW 161,42
M 1,21,3
LA 16
RV 36,8,6912
PK 107,17,105,33
AW 162,44
AW 163,46
M 2,32,2
LC_ 111
PK 105,17,105,32
M 2,33,3
RA 216,1,13312
v_add_f32_dpp v14, v14, v14 row_half_mirror row_mask:0xf bank_mask:0xf
GS 70
CF 48,48,49
CF 50,50,51
CF 248,248,249
CF 250,250,251
M 10,44,2
WL 4
LO 18,18,110
LU 110,18
AW 164,48
AW 165,50
AW 166,248
M 10,45,3
RA 220,1,14336
HS 27,110
AW 167,250
s_nop 1
GS 71
SU 104
v_cvt_f64_f32 v[14:15], v14
s_nop 5
v_cndmask_b32 v52, v52, v53, s[78:79]
M 0,10,4
LL 17,88
LL 114,89
v_cndmask_b32 v54, v54, v55, s[78:79]
M 0,11,5
LA 17
PK 110,18,108,33
v_cndmask_b32 v52, v52, v54, s[80:81]
M 1,22,4
LC_ 114
PK 108,18,108,32
v_mul_f32 v52, 0x3b2aaaab, v52
M 1,23,5
WL 5
RA 224,1,15360
PO 110
v_med3_f32 v53, v52, v39, v37
M 2,34,4
WL 5
LO 16,16,111
LU 111,16
s_mov_b64 exec, s[54:55]
SP
s_mov_b64 exec, -1
LG 52,53
s_nop 0
C 52,52,52
M 2,35,5
s_add_u32 s42, s94, s61
ds_read_b64 v[14:15], v5 offset:2048
HS 27,111
v_lshlrev_b32 v52, 16, v52
M 10,46,4
WL 4
LL 18,90
LL 115,91
v_add_f32 v52, v36, v52
M 10,47,5
s_addc_u32 s43, s95, 0
LA 18
PK 111,16,109,33
C 52,52,52
Z 6,0,0
LC_ 115
PK 109,16,109,32
v_lshlrev_b32 v52, 16, v52
M 6,1,1
RA 228,1,16384
PO 111
v_sqrt_f32 v52, v52
s_nop 0
C 52,52,52
Z 7,12,0
WL 5
LO 17,17,114
LU 114,17
v_lshlrev_b32 v52, 16, v52
M 7,13,1
RA 232,1,17408
HZ 29,114
v_rcp_f32 v52, v52
Z 8,24,0
LL 16,92
LL 118,93
v_mul_f32 v52, v38, v52
M 8,25,1
LA 16
PK 114,17,112,33
v_subrev_u32 v5, s39, v5
CF 52,52,52
Z 11,36,0
WL 2
LC_ 118
PK 112,17,112,32
ds_bpermute_b32 v13, v28, v52
M 11,37,1
RA 236,1,18432
v_add_f32 v14, v14, v15
M 6,2,2
LO 18,18,115
LU 115,18
M 6,3,3
SU 108
HS 29,115
v_add_f32_dpp v14, v14, v14 quad_perm:[1,0,3,2] row_mask:0xf bank_mask:0xf
M 7,14,2
s_mul_i32 s53, s50, 25600
LL 17,94
LL 119,95
s_add_u32 s40, %[wlo], s53
s_addc_u32 s41, %[whi], 0
s_mov_b32 m0, s38
M 7,15,3
LA 17
PK 115,18,113,33
v_add_f32_dpp v14, v14, v14 quad_perm:[2,3,0,1] row_mask:0xf bank_mask:0xf
M 8,26,2
LC_ 119
PK 113,18,113,32
M 8,27,3
RA 216,1,19456
DW 0
v_add_f32_dpp v14, v14, v14 row_half_mirror row_mask:0xf bank_mask:0xf
M 11,38,2
WL 3
LO 16,16,118
LU 118,16
M 11,39,3
s_add_u32 s42, s94, s62
RA 220,1,20480
HS 29,118
v_cvt_f64_f32 v[14:15], v14
s_addc_u32 s43, s95, 0
AR 248,120
M 6,4,4
s_add_u32 s37, s37, 32768
LL 18,56
LL 104,57
s_cmp_eq_u32 s37, s75
s_cselect_b32 s37, s74, s37
AR 249,121
M 6,5,5
LA 18
PK 118,16,116,33
s_mov_b64 exec, s[54:55]
SP
s_mov_b64 exec, -1
DW 1024
AR 250,122
M 7,16,4
LC_ 104
PK 116,16,116,32
AR 251,123
AR 52,124
M 7,17,5
RA 224,1,21504
PO 118
AR 53,125
M 8,28,4
WL 5
LO 17,17,119
LU 119,17
SU 112
AR 54,126
AR 55,127
M 8,29,5
DW 2048
HS 29,119
XS 248
M 11,40,4
WL 3
LL 16,58
LL 105,59
XS 249
M 11,41,5
s_add_u32 s40, s40, 3072
LA 16
PK 119,17,117,33
XS 250
XS 251
M 6,6,0
s_addc_u32 s41, s41, 0
LC_ 105
PK 117,17,117,32
s_add_u32 m0, s38, 3072
AW 120,248
M 6,7,1
s_add_u32 s42, s94, s63
RA 228,1,22528
PO 119
AW 121,249
AW 122,250
M 7,18,0
WL 4
LO 104,18,104
LU 18,104
AW 123,251
M 7,19,1
s_addc_u32 s43, s95, 0
RA 232,1,23552
HZ 26,18
XS 52
XS 53
M 8,30,0
DW 0
LL 17,60
LL 108,61
XS 54
M 8,31,1
s_lshl_b32 s53, s52, 8
LA 17
v_pk_add_f16 v27, v27, v29
XS 55
M 11,42,0
WL 2
LC_ 108
HX 27
AW 124,52
AW 125,53
M 11,43,1
s_add_u32 s48, %[alo], s53
RA 236,1,24576
W1 27,3072
AW 126,54
M 6,8,2
LO 105,16,105
LU 16,105
s_addc_u32 s49, %[ahi], 0
s_add_u32 s52, s52, %[d]
s_cmp_eq_u32 s52, %[wrap]
s_cselect_b32 s52, %[rst], s52
DW 1024
AW 127,55
v_min_u32 v35, v35, v13
M 6,9,3
SU 116
HS 26,16
AR 248,128
M 7,20,2
s_mul_i32 s53, s51, 49152
LL 18,62
LL 109,63
s_bitcmp1_b64 s[92:93], s51
s_cselect_b64 s[94:95], s[88:89], s[86:87]
s_add_u32 s94, s94, s53
s_addc_u32 s95, s95, 0
s_add_u32 s51, s51, %[d]
s_cmp_eq_u32 s51, %[wrap]
s_cselect_b32 s51, %[rst], s51
AR 249,129
AR 250,130
M 7,21,3
LA 18
DW 2048
AR 251,131
M 8,32,2
LC_ 109
DQ 3072
XS 248
M 8,33,3
s_add_u32 s50, s50, %[d]
v_add_u32 v1, s37, v0
v_add_u32 v4, s39, v4
XS 249
XS 250
M 11,44,2
WL 3
LO 108,17,108
LU 17,108
XS 251
M 11,45,3
s_cmp_eq_u32 s50, %[wrap]
RA 216,1,0
HS 26,17
AW 128,248
AW 129,249
M 6,10,4
s_cselect_b32 s50, %[rst], s50
LL 16,64
LL 112,65
AW 130,250
M 6,11,5
LA 16
v_and_b32 v22, 0xfffffcff, v1
AW 131,251
AR 52,132
M 7,22,4
LC_ 112
RA 220,1,1024
AR 53,133
M 7,23,5
RV 252,22,5120
v_add_u32 v19, 1, v19
AR 54,134
M 8,34,4
WL 5
LO 109,18,109
LU 18,109
AR 55,135
XS 52
M 8,35,5
s_add_u32 s38, s38, 32768
RA 224,1,2048
HS 26,18
XS 53
M 11,46,4
WL 1
LL 17,66
LL 113,67
XS 54
XS 55
M 11,47,5
s_cmp_eq_u32 s38, s77
LA 17
RA 228,1,3072
s_cselect_b32 s38, s76, s38
FP
s_sub_u32 s39, 0, s39
AW 132,52
Z 0,0,0
LC_ 113
AW 133,53
AW 134,54
M 0,1,1
AW 135,55
AR 248,136
Z 1,12,0
LO 112,16,112
LU 16,112
M 1,13,1
RA 232,1,4096
HZ 29,16
AR 249,137
AR 250,138
Z 2,24,0
LL 18,96
LL 116,97
AR 251,139
M 2,25,1
LA 18
XS 248
XS 249
Z 3,36,0
LC_ 116
XS 250
M 3,37,1
XS 251
AW 136,248
Z 4,48,0
WL 3
LO 113,17,113
LU 17,113
AW 137,249
M 4,49,1
RA 236,1,6144
HS 29,17
AW 138,250
Z 5,60,0
LL 16,98
LL 117,99
AW 139,251
AR 52,140
M 5,61,1
LA 16
AR 53,141
M 0,2,2
LC_ 117
AR 54,142
AR 55,143
M 0,3,3
XS 52
XS 53
M 1,14,2
WL 3
LO 116,18,116
LU 18,116
XS 54
M 1,15,3
RA 216,1,7168
HS 29,18
XS 55
M 2,26,2
)"
R"(LL 17,80
LL 106,81
AW 140,52
M 2,27,3
LA 17
FR
AW 141,53
AW 142,54
M 3,38,2
LC_ 106
AW 143,55
M 3,39,3
AR 248,144
AR 249,145
M 4,50,2
WL 4
LO 117,16,117
LU 16,117
AR 250,146
M 4,51,3
RA 220,1,8192
HS 29,16
AR 251,147
M 5,62,2
LL 18,82
LL 107,83
XS 248
XS 249
M 5,63,3
LA 18
v_pk_add_f16 v26, v26, v29
XS 250
M 0,4,4
WL 3
LC_ 107
v_cmp_gt_u32 vcc, v19, v15
XS 251
AW 144,248
M 0,5,5
HX 26
AW 145,249
M 1,16,4
WL 3
LO 17,17,106
LU 106,17
AW 146,250
AW 147,251
M 1,17,5
RA 224,1,9216
HZ 27,106
AR 52,148
M 2,28,4
s_cbranch_vccnz .Lk1s18
.Lk1r18:
LL 16,84
LL 110,85
AR 53,149
M 2,29,5
LA 16
PK 106,17,104,33
AR 54,150
AR 55,151
M 3,40,4
LC_ 110
PK 104,17,104,32
XS 52
M 3,41,5
W1 26,2048
XS 53
XS 54
M 4,52,4
WL 4
LO 18,18,107
LU 107,18
XS 55
M 4,53,5
s_add_u32 s42, s94, s60
RA 228,1,10240
HS 27,107
AW 148,52
AW 149,53
M 5,64,4
LL 17,86
LL 111,87
AW 150,54
M 5,65,5
s_addc_u32 s43, s95, 0
LA 17
PK 107,18,105,33
AW 151,55
M 0,6,0
LC_ 111
PK 105,18,105,32
AR 248,152
AR 249,153
M 0,7,1
SU 104
AR 250,154
M 1,18,0
WL 4
LO 16,16,110
LU 110,16
AR 251,155
XS 248
M 1,19,1
RA 232,1,11264
HS 27,110
XS 249
M 2,30,0
LL 18,88
LL 114,89
XS 250
XS 251
M 2,31,1
LA 18
PK 110,16,108,33
AW 152,248
M 3,42,0
LC_ 114
PK 108,16,108,32
AW 153,249
M 3,43,1
RV 252,22,5392
PO 110
AW 154,250
AW 155,251
M 4,54,0
WL 4
LO 17,17,111
LU 111,17
AR 52,156
M 4,55,1
s_add_u32 s42, s94, s61
RA 236,1,12288
HS 27,111
AR 53,157
AR 54,158
M 5,66,0
LL 16,90
LL 115,91
AR 55,159
M 5,67,1
s_addc_u32 s43, s95, 0
LA 16
PK 111,17,109,33
XS 52
XS 53
M 0,8,2
LC_ 115
PK 109,17,109,32
XS 54
M 0,9,3
PO 111
XS 55
M 1,20,2
WL 4
LO 18,18,114
LU 114,18
AW 156,52
AW 157,53
M 1,21,3
RA 216,1,13312
HZ 29,114
AW 158,54
M 2,32,2
LL 17,100
LL 118,101
AW 159,55
AR 248,160
M 2,33,3
LA 17
PK 114,18,112,33
AR 249,161
M 3,44,2
LC_ 118
PK 112,18,112,32
AR 250,162
AR 251,163
M 3,45,3
SU 108
XS 248
M 4,56,2
WL 3
LO 16,16,115
LU 115,16
XS 249
M 4,57,3
s_add_u32 s42, s94, s62
RA 220,1,14336
HS 29,115
XS 250
XS 251
M 5,68,2
LL 18,102
LL 119,103
AW 160,248
M 5,69,3
s_addc_u32 s43, s95, 0
LA 18
PK 115,16,113,33
AW 161,249
AW 162,250
M 0,10,4
LC_ 119
PK 113,16,113,32
AW 163,251
M 0,11,5
SU 112
AR 52,164
AR 53,165
M 1,22,4
WL 3
LO 17,17,118
LU 118,17
AR 54,166
M 1,23,5
RA 224,1,15360
HS 29,118
AR 55,167
M 2,34,4
LL 16,56
LL 104,57
XS 52
XS 53
M 2,35,5
LA 16
PK 118,17,116,33
XS 54
M 3,46,4
LC_ 104
PK 116,17,116,32
XS 55
AW 164,52
M 3,47,5
ds_read_b64 v[14:15], v5 offset:2048
PO 118
AW 165,53
M 4,58,4
WL 4
LO 18,18,119
LU 119,18
AW 166,54
AW 167,55
M 4,59,5
s_add_u32 s42, s94, s63
RA 228,1,16384
HS 29,119
M 5,70,4
LL 17,58
LL 105,59
M 5,71,5
s_addc_u32 s43, s95, 0
LA 17
PK 119,18,117,33
Z 6,0,0
LC_ 105
PK 117,18,117,32
M 6,1,1
PO 119
v_subrev_u32 v5, s39, v5
Z 7,12,0
WL 4
LO 104,16,104
LU 16,104
M 7,13,1
RA 232,1,17408
HZ 26,16
Z 8,24,0
LL 18,60
LL 108,61
M 8,25,1
WL 4
LA 18
SU 116
Z 9,36,0
s_mul_i32 s53, s50, 25600
LC_ 108
v_add_f32 v14, v14, v15
M 9,37,1
s_add_u32 s40, %[wlo], s53
v_add_f32_dpp v14, v14, v14 quad_perm:[1,0,3,2] row_mask:0xf bank_mask:0xf
v_pk_add_f16 v27, v27, v29
Z 10,48,0
WL 3
LO 105,17,105
LU 17,105
M 10,49,1
s_addc_u32 s41, %[whi], 0
RA 236,1,18432
HS 26,17
Z 11,60,0
LL 16,62
LL 109,63
M 11,61,1
s_mov_b32 m0, s38
LA 16
DW 0
M 6,2,2
LC_ 109
v_add_f32_dpp v14, v14, v14 quad_perm:[2,3,0,1] row_mask:0xf bank_mask:0xf
M 6,3,3
s_nop 0
v_add_f32_dpp v14, v14, v14 row_half_mirror row_mask:0xf bank_mask:0xf
HX 27
M 7,14,2
WL 3
LO 108,18,108
LU 18,108
M 7,15,3
s_add_u32 s37, s37, 32768
RA 216,1,19456
HS 26,18
W1 27,3072
M 8,26,2
s_cmp_eq_u32 s37, s75
LL 17,64
LL 112,65
M 8,27,3
s_cselect_b32 s37, s74, s37
LA 17
DW 1024
M 9,38,2
LC_ 112
v_cvt_f64_f32 v[14:15], v14
M 9,39,3
s_mov_b64 exec, s[54:55]
SP
s_mov_b64 exec, -1
v_add_u32 v4, s39, v4
M 10,50,2
WL 4
LO 109,16,109
LU 16,109
M 10,51,3
RA 220,1,20480
HS 26,16
M 11,62,2
LL 18,66
LL 113,67
M 11,63,3
LA 18
DW 2048
M 6,4,4
LC_ 113
M 6,5,5
M 7,16,4
WL 3
LO 112,17,112
LU 17,112
M 7,17,5
s_mul_i32 s53, s51, 49152
RA 224,1,21504
HZ 29,17
M 8,28,4
s_bitcmp1_b64 s[92:93], s51
LL 16,68
LL 116,69
M 8,29,5
s_cselect_b64 s[94:95], s[88:89], s[86:87]
LA 16
M 9,40,4
s_add_u32 s94, s94, s53
LC_ 116
M 9,41,5
s_addc_u32 s95, s95, 0
M 10,52,4
WL 3
LO 113,18,113
LU 18,113
M 10,53,5
s_add_u32 s51, s51, %[d]
RA 228,1,22528
HS 29,18
M 11,64,4
LL 17,70
LL 117,71
M 11,65,5
s_cmp_eq_u32 s51, %[wrap]
LA 17
M 6,6,0
s_cselect_b32 s51, %[rst], s51
LC_ 117
M 6,7,1
s_mul_i32 s53, %[d], 32768
M 7,18,0
WL 3
LO 116,16,116
LU 16,116
M 7,19,1
s_add_u32 s64, s53, %[sb1]
RA 232,1,23552
HS 29,16
M 8,30,0
s_add_u32 s65, s64, 512
LL 18,72
LL 120,73
M 8,31,1
s_add_u32 s40, s40, 3072
LA 18
M 9,42,0
)"
R"(s_addc_u32 s41, s41, 0
LC_ 120
s_add_u32 m0, s38, 3072
M 9,43,1
DW 0
M 10,54,0
WL 3
LO 117,17,117
LU 17,117
M 10,55,1
RA 236,1,24576
HS 29,17
M 11,66,0
LL 16,74
LL 121,75
M 11,67,1
LA 16
DW 1024
M 6,8,2
LC_ 121
DW 2048
M 6,9,3
s_lshl_b32 s53, s52, 8
v_add_u32 v1, s37, v0
M 7,20,2
WL 3
LO 120,18,120
LU 18,120
M 7,21,3
s_add_u32 s48, %[alo], s53
RA 216,1,0
HZ 30,18
M 8,32,2
s_addc_u32 s49, %[ahi], 0
LL 17,76
LL 124,77
M 8,33,3
s_add_u32 s52, s52, %[d]
LA 17
v_and_b32 v22, 0xfffffcff, v1
M 9,44,2
s_cmp_eq_u32 s52, %[wrap]
LC_ 124
M 9,45,3
s_cselect_b32 s52, %[rst], s52
DQ 3072
M 10,56,2
WL 3
LO 121,16,121
LU 16,121
M 10,57,3
s_add_u32 s50, s50, %[d]
RA 220,1,1024
HS 30,16
M 11,68,2
LL 18,78
LL 125,79
M 11,69,3
s_cmp_eq_u32 s50, %[wrap]
LA 18
RV 252,22,5120
M 6,10,4
s_cselect_b32 s50, %[rst], s50
LC_ 125
M 6,11,5
WV 25
v_add_u32 v19, 1, v19
M 7,22,4
WL 4
LO 124,17,124
LU 17,124
M 7,23,5
s_add_u32 s38, s38, 32768
RA 224,1,2048
HS 30,17
M 8,34,4
s_cmp_eq_u32 s38, s77
LL 16,80
LL 106,81
M 8,35,5
s_cselect_b32 s38, s76, s38
LA 16
FP
M 9,46,4
s_sub_u32 s39, 0, s39
LC_ 106
M 9,47,5
M 10,58,4
WL 4
LO 125,18,125
LU 18,125
M 10,59,5
RA 228,1,3072
HS 30,18
M 11,70,4
LL 17,82
LL 107,83
M 11,71,5
LA 17
v_pk_add_f16 v26, v26, v30
Z 0,0,0
LC_ 107
v_pk_add_f16 v26, v26, v29
M 0,1,1
HX 26
Z 1,12,0
WL 3
LO 16,16,106
LU 106,16
M 1,13,1
RA 232,1,4096
HZ 27,106
Z 2,24,0
LL 18,84
LL 110,85
M 2,25,1
LA 18
PK 106,16,104,33
Z 3,36,0
LC_ 110
PK 104,16,104,32
M 3,37,1
Z 4,48,0
WL 3
LO 17,17,107
LU 107,17
M 4,49,1
s_add_u32 s42, s94, s60
RA 236,1,6144
HS 27,107
Z 5,60,0
LL 16,86
LL 111,87
M 5,61,1
s_addc_u32 s43, s95, 0
LA 16
PK 107,17,105,33
M 0,2,2
LC_ 111
PK 105,17,105,32
M 0,3,3
SU 104
M 1,14,2
WL 3
LO 18,18,110
LU 110,18
M 1,15,3
RA 216,1,7168
HS 27,110
M 2,26,2
LL 17,88
LL 114,89
M 2,27,3
LA 17
PK 110,18,108,33
M 3,38,2
LC_ 114
PK 108,18,108,32
M 3,39,3
FR
PO 110
M 4,50,2
WL 4
LO 16,16,111
LU 111,16
M 4,51,3
s_add_u32 s42, s94, s61
RA 220,1,8192
HS 27,111
M 5,62,2
LL 18,90
LL 115,91
M 5,63,3
s_addc_u32 s43, s95, 0
LA 18
PK 111,16,109,33
M 0,4,4
PFN
LC_ 115
PK 109,16,109,32
M 0,5,5
s_mov_b32 m0, %[pm]
s_nop 0
DF
PO 111
M 1,16,4
WL 4
LO 17,17,114
LU 114,17
M 1,17,5
RA 224,1,9216
HZ 29,114
M 2,28,4
LL 16,92
LL 118,93
M 2,29,5
LA 16
PK 114,17,112,33
M 3,40,4
LC_ 118
PK 112,17,112,32
M 3,41,5
WL 6
SU 108
v_cmp_gt_u32 vcc, v19, v15
M 4,52,4
WL 3
LO 18,18,115
LU 115,18
M 4,53,5
s_add_u32 s42, s94, s62
RA 228,1,10240
HS 29,115
M 5,64,4
LL 17,94
LL 119,95
M 5,65,5
s_cbranch_vccnz .Lk1s19
.Lk1r19:
LA 17
PK 115,18,113,33
M 0,6,0
s_addc_u32 s43, s95, 0
LC_ 119
PK 113,18,113,32
M 0,7,1
W1 26,2048
M 1,18,0
WL 4
LO 16,16,118
LU 118,16
M 1,19,1
RA 232,1,11264
HS 29,118
M 2,30,0
LL 18,96
LL 122,97
M 2,31,1
LA 18
PK 118,16,116,33
M 3,42,0
LC_ 122
PK 116,16,116,32
M 3,43,1
RV 252,22,5392
PO 118
M 4,54,0
WL 5
LO 17,17,119
LU 119,17
M 4,55,1
RA 236,1,12288
HS 29,119
M 5,66,0
LL 16,98
LL 123,99
M 5,67,1
LA 16
PK 119,17,117,33
M 0,8,2
LC_ 123
PK 117,17,117,32
M 0,9,3
SU 112
PO 119
M 1,20,2
WL 4
LO 18,18,122
LU 122,18
M 1,21,3
s_add_u32 s42, s94, s63
RA 216,1,13312
HZ 30,122
M 2,32,2
s_addc_u32 s43, s95, 0
LL 17,100
LL 126,101
M 2,33,3
LA 17
PK 122,18,120,33
M 3,44,2
LC_ 126
PK 120,18,120,32
M 3,45,3
SU 116
M 4,56,2
WL 3
LO 16,16,123
LU 123,16
M 4,57,3
s_add_u32 s42, s94, s64
RA 220,1,14336
HS 30,123
M 5,68,2
LL 18,102
LL 127,103
M 5,69,3
s_addc_u32 s43, s95, 0
LA 18
PK 123,16,121,33
M 0,10,4
LC_ 127
PK 121,16,121,32
M 0,11,5
SU 120
M 1,22,4
WL 3
LO 17,17,126
LU 126,17
M 1,23,5
RA 224,1,15360
HS 30,126
M 2,34,4
LL 16,56
LL 104,57
M 2,35,5
LA 16
PK 126,17,124,33
M 3,46,4
LC_ 104
PK 124,17,124,32
M 3,47,5
ds_read_b64 v[14:15], v5 offset:2048
PO 126
M 4,58,4
WL 4
LO 18,18,127
LU 127,18
M 4,59,5
s_add_u32 s42, s94, s65
RA 228,1,16384
HS 30,127
M 5,70,4
LL 17,58
LL 105,59
M 5,71,5
s_addc_u32 s43, s95, 0
LA 17
PK 127,18,125,33
Z 6,0,0
LC_ 105
PK 125,18,125,32
M 6,1,1
PO 127
v_subrev_u32 v5, s39, v5
Z 7,12,0
WL 4
LO 104,16,104
LU 16,104
M 7,13,1
RA 232,1,17408
HZ 26,16
Z 8,24,0
WL 4
LL 18,60
LL 108,61
M 8,25,1
s_mul_i32 s53, s50, 25600
LA 18
v_add_f32 v14, v14, v15
Z 9,36,0
s_add_u32 s40, %[wlo], s53
LC_ 108
v_add_f32_dpp v14, v14, v14 quad_perm:[1,0,3,2] row_mask:0xf bank_mask:0xf
M 9,37,1
s_addc_u32 s41, %[whi], 0
v_add_f32_dpp v14, v14, v14 quad_perm:[2,3,0,1] row_mask:0xf bank_mask:0xf
v_pk_add_f16 v27, v27, v30
Z 10,48,0
WL 3
LO 105,17,105
LU 17,105
M 10,49,1
s_mov_b32 m0, s38
RA 236,1,18432
HS 26,17
Z 11,60,0
LL 16,62
LL 109,63
M 11,61,1
LA 16
SU 124
M 6,2,2
LC_ 109
DW 0
M 6,3,3
v_add_f32_dpp v14, v14, v14 row_half_mirror row_mask:0xf bank_mask:0xf
v_pk_add_f16 v27, v27, v29
M 7,14,2
WL 3
LO 108,18,108
LU 18,108
M 7,15,3
s_add_u32 s37, s37, 32768
RA 216,1,19456
HS 26,18
HX 27
M 8,26,2
s_cmp_eq_u32 s37, s75
LL 17,64
)"
R"(LL 112,65
W1 27,3072
M 8,27,3
s_cselect_b32 s37, s74, s37
LA 17
DW 1024
M 9,38,2
LC_ 112
v_cvt_f64_f32 v[14:15], v14
M 9,39,3
s_mov_b64 exec, s[54:55]
SP
s_mov_b64 exec, -1
v_add_u32 v4, s39, v4
s_mov_b64 exec, 1
AT
s_mov_b64 exec, -1
M 10,50,2
WL 4
LO 109,16,109
LU 16,109
M 10,51,3
RA 220,1,20480
HS 26,16
M 11,62,2
LL 18,66
LL 113,67
M 11,63,3
LA 18
DW 2048
M 6,4,4
LC_ 113
M 6,5,5
M 7,16,4
WL 3
LO 112,17,112
LU 17,112
M 7,17,5
s_mul_i32 s53, s51, 49152
RA 224,1,21504
HZ 29,17
M 8,28,4
s_bitcmp1_b64 s[92:93], s51
LL 16,68
LL 116,69
M 8,29,5
s_cselect_b64 s[94:95], s[88:89], s[86:87]
LA 16
M 9,40,4
s_add_u32 s94, s94, s53
LC_ 116
M 9,41,5
s_addc_u32 s95, s95, 0
M 10,52,4
WL 3
LO 113,18,113
LU 18,113
M 10,53,5
s_add_u32 s51, s51, %[d]
RA 228,1,22528
HS 29,18
M 11,64,4
LL 17,70
LL 117,71
M 11,65,5
s_cmp_eq_u32 s51, %[wrap]
LA 17
M 6,6,0
s_cselect_b32 s51, %[rst], s51
LC_ 117
M 6,7,1
s_add_u32 s40, s40, 3072
M 7,18,0
WL 3
LO 116,16,116
LU 16,116
M 7,19,1
s_addc_u32 s41, s41, 0
RA 232,1,23552
HS 29,16
M 8,30,0
s_add_u32 m0, s38, 3072
LL 18,72
LL 120,73
M 8,31,1
LA 18
DW 0
M 9,42,0
LC_ 120
DW 1024
M 9,43,1
M 10,54,0
WL 3
LO 117,17,117
LU 17,117
M 10,55,1
RA 236,1,24576
HS 29,17
M 11,66,0
LL 16,74
LL 121,75
M 11,67,1
LA 16
DW 2048
M 6,8,2
LC_ 121
M 6,9,3
s_lshl_b32 s53, s52, 8
v_add_u32 v1, s37, v0
M 7,20,2
WL 3
LO 120,18,120
LU 18,120
M 7,21,3
s_add_u32 s48, %[alo], s53
RA 216,1,0
HZ 30,18
M 8,32,2
s_addc_u32 s49, %[ahi], 0
LL 17,76
LL 124,77
M 8,33,3
s_add_u32 s52, s52, %[d]
LA 17
v_and_b32 v22, 0xfffffcff, v1
M 9,44,2
s_cmp_eq_u32 s52, %[wrap]
LC_ 124
M 9,45,3
s_cselect_b32 s52, %[rst], s52
DQ 3072
M 10,56,2
WL 3
LO 121,16,121
LU 16,121
M 10,57,3
s_add_u32 s50, s50, %[d]
RA 220,1,1024
HS 30,16
M 11,68,2
LL 18,78
LL 125,79
M 11,69,3
s_cmp_eq_u32 s50, %[wrap]
LA 18
RV 252,22,5120
M 6,10,4
s_cselect_b32 s50, %[rst], s50
LC_ 125
M 6,11,5
WV 28
M 7,22,4
WL 4
LO 124,17,124
LU 17,124
M 7,23,5
s_add_u32 s38, s38, 32768
RA 224,1,2048
HS 30,17
M 8,34,4
s_cmp_eq_u32 s38, s77
LL 16,80
LL 106,81
M 8,35,5
s_cselect_b32 s38, s76, s38
LA 16
M 9,46,4
s_barrier
LC_ 106
M 9,47,5
s_sub_u32 s39, 0, s39
M 10,58,4
WL 3
LO 125,18,125
LU 18,125
M 10,59,5
RA 228,1,3072
HS 30,18
M 11,70,4
LL 17,82
LL 107,83
M 11,71,5
LA 17
v_pk_add_f16 v26, v26, v30
Z 0,0,0
LC_ 107
v_pk_add_f16 v26, v26, v29
M 0,1,1
HX 26
Z 1,12,0
WL 3
LO 16,16,106
LU 106,16
M 1,13,1
RA 232,1,4096
HZ 27,106
Z 2,24,0
LL 18,84
LL 110,85
M 2,25,1
LA 18
PK 106,16,104,33
Z 3,36,0
LC_ 110
PK 104,16,104,32
M 3,37,1
W1 26,2048
Z 4,48,0
WL 4
LO 17,17,107
LU 107,17
M 4,49,1
s_add_u32 s42, s94, s60
RA 236,1,6144
HS 27,107
Z 5,60,0
LL 16,86
LL 111,87
M 5,61,1
s_addc_u32 s43, s95, 0
LA 16
PK 107,17,105,33
M 0,2,2
LC_ 111
PK 105,17,105,32
M 0,3,3
SU 104
M 1,14,2
WL 4
LO 18,18,110
LU 110,18
M 1,15,3
RA 216,1,7168
HS 27,110
M 2,26,2
LL 17,88
LL 114,89
M 2,27,3
LA 17
PK 110,18,108,33
M 3,38,2
LC_ 114
PK 108,18,108,32
M 3,39,3
PO 110
M 4,50,2
WL 3
LO 16,16,111
LU 111,16
M 4,51,3
s_add_u32 s42, s94, s61
RA 220,1,8192
HS 27,111
M 5,62,2
LL 18,90
LL 115,91
M 5,63,3
s_addc_u32 s43, s95, 0
LA 18
PK 111,16,109,33
M 0,4,4
PFW
LC_ 115
PK 109,16,109,32
M 0,5,5
s_mov_b32 m0, %[pm]
s_nop 0
DF
PO 111
M 1,16,4
WL 3
LO 17,17,114
LU 114,17
M 1,17,5
RA 224,1,9216
HZ 29,114
M 2,28,4
LL 16,92
LL 118,93
M 2,29,5
LA 16
PK 114,17,112,33
M 3,40,4
LC_ 118
PK 112,17,112,32
M 3,41,5
SU 108
M 4,52,4
WL 3
LO 18,18,115
LU 115,18
M 4,53,5
s_add_u32 s42, s94, s62
RA 228,1,10240
HS 29,115
M 5,64,4
LL 17,94
LL 119,95
M 5,65,5
s_addc_u32 s43, s95, 0
LA 17
PK 115,18,113,33
M 0,6,0
LC_ 119
PK 113,18,113,32
M 0,7,1
SU 112
M 1,18,0
WL 3
LO 16,16,118
LU 118,16
M 1,19,1
RA 232,1,11264
HS 29,118
M 2,30,0
LL 18,96
LL 122,97
M 2,31,1
LA 18
PK 118,16,116,33
M 3,42,0
LC_ 122
PK 116,16,116,32
M 3,43,1
RV 252,22,5392
PO 118
M 4,54,0
WL 4
LO 17,17,119
LU 119,17
M 4,55,1
s_add_u32 s42, s94, s63
RA 236,1,12288
HS 29,119
M 5,66,0
LL 16,98
LL 123,99
M 5,67,1
s_addc_u32 s43, s95, 0
LA 16
PK 119,17,117,33
M 0,8,2
LC_ 123
PK 117,17,117,32
M 0,9,3
PO 119
M 1,20,2
WL 4
LO 18,18,122
LU 122,18
M 1,21,3
RA 216,1,13312
HZ 30,122
M 2,32,2
LL 17,100
LL 126,101
M 2,33,3
LA 17
PK 122,18,120,33
M 3,44,2
LC_ 126
PK 120,18,120,32
M 3,45,3
SU 116
M 4,56,2
WL 3
LO 16,16,123
LU 123,16
M 4,57,3
s_add_u32 s42, s94, s64
RA 220,1,14336
HS 30,123
M 5,68,2
LL 18,102
LL 127,103
M 5,69,3
s_addc_u32 s43, s95, 0
LA 18
PK 123,16,121,33
M 0,10,4
LC_ 127
PK 121,16,121,32
M 0,11,5
SU 120
M 1,22,4
WL 3
LO 17,17,126
LU 126,17
M 1,23,5
RA 224,1,15360
HS 30,126
M 2,34,4
LL 16,56
LL 104,57
M 2,35,5
LA 16
PK 126,17,124,33
M 3,46,4
LC_ 104
PK 124,17,124,32
)"
R"(M 3,47,5
ds_read_b64 v[14:15], v5 offset:2048
PO 126
M 4,58,4
WL 4
LO 18,18,127
LU 127,18
M 4,59,5
s_add_u32 s42, s94, s65
RA 228,1,16384
HS 30,127
M 5,70,4
LL 17,58
LL 105,59
M 5,71,5
s_addc_u32 s43, s95, 0
LA 17
PK 127,18,125,33
Z 6,0,0
LC_ 105
PK 125,18,125,32
M 6,1,1
PO 127
v_subrev_u32 v5, s39, v5
Z 7,12,0
WL 4
LO 104,16,104
LU 16,104
M 7,13,1
RA 232,1,17408
HZ 26,16
Z 8,24,0
WL 4
LL 18,60
LL 108,61
M 8,25,1
s_mul_i32 s53, s50, 25600
LA 18
v_add_f32 v14, v14, v15
Z 9,36,0
s_add_u32 s40, %[wlo], s53
LC_ 108
v_add_f32_dpp v14, v14, v14 quad_perm:[1,0,3,2] row_mask:0xf bank_mask:0xf
M 9,37,1
s_addc_u32 s41, %[whi], 0
Z 10,48,0
WL 3
LO 105,17,105
LU 17,105
M 10,49,1
s_mov_b32 m0, s38
RA 236,1,18432
HS 26,17
Z 11,60,0
LL 16,62
LL 109,63
M 11,61,1
LA 16
SU 124
M 6,2,2
LC_ 109
DW 0
M 6,3,3
WV 13
v_add_f32_dpp v14, v14, v14 quad_perm:[2,3,0,1] row_mask:0xf bank_mask:0xf
v_pk_add_f16 v27, v27, v30
M 7,14,2
WL 3
LO 108,18,108
LU 18,108
v_pk_add_f16 v27, v27, v29
M 7,15,3
s_add_u32 s37, s37, 32768
RA 216,1,19456
HS 26,18
HX 27
M 8,26,2
s_cmp_eq_u32 s37, s75
LL 17,64
LL 112,65
W1 27,3072
M 8,27,3
s_cselect_b32 s37, s74, s37
LA 17
DW 1024
v_readfirstlane_b32 s72, v11
M 9,38,2
LC_ 112
v_add_f32_dpp v14, v14, v14 row_half_mirror row_mask:0xf bank_mask:0xf
v_mov_b32 v12, s72
M 9,39,3
W2 12,12,16
v_cvt_f64_f32 v[14:15], v14
M 10,50,2
WL 5
LO 109,16,109
LU 16,109
M 10,51,3
RA 220,1,20480
HS 26,16
M 11,62,2
LL 18,66
LL 113,67
M 11,63,3
LA 18
s_mov_b64 exec, s[54:55]
SP
s_mov_b64 exec, -1
M 6,4,4
LC_ 113
DW 2048
M 6,5,5
v_add_u32 v4, s39, v4
s_mov_b64 exec, 1
AT
s_mov_b64 exec, -1
M 7,16,4
WL 4
LO 112,17,112
LU 17,112
M 7,17,5
s_mul_i32 s53, s51, 49152
RA 224,1,21504
HZ 29,17
M 8,28,4
s_bitcmp1_b64 s[92:93], s51
LL 16,68
LL 116,69
M 8,29,5
s_cselect_b64 s[94:95], s[88:89], s[86:87]
LA 16
M 9,40,4
s_add_u32 s94, s94, s53
LC_ 116
M 9,41,5
s_addc_u32 s95, s95, 0
M 10,52,4
WL 3
LO 113,18,113
LU 18,113
M 10,53,5
s_add_u32 s51, s51, %[d]
RA 228,1,22528
HS 29,18
M 11,64,4
LL 17,70
LL 117,71
M 11,65,5
s_cmp_eq_u32 s51, %[wrap]
LA 17
M 6,6,0
s_cselect_b32 s51, %[rst], s51
LC_ 117
M 6,7,1
s_add_u32 s40, s40, 3072
M 7,18,0
WL 3
LO 116,16,116
LU 16,116
M 7,19,1
s_addc_u32 s41, s41, 0
RA 232,1,23552
HS 29,16
M 8,30,0
s_add_u32 m0, s38, 3072
LL 18,72
LL 120,73
M 8,31,1
LA 18
DW 0
M 9,42,0
LC_ 120
DW 1024
M 9,43,1
M 10,54,0
WL 3
LO 117,17,117
LU 17,117
M 10,55,1
RA 236,1,24576
HS 29,17
M 11,66,0
LL 16,74
LL 121,75
M 11,67,1
LA 16
DW 2048
M 6,8,2
LC_ 121
M 6,9,3
s_lshl_b32 s53, s52, 8
v_add_u32 v1, s37, v0
M 7,20,2
WL 3
LO 120,18,120
LU 18,120
M 7,21,3
s_add_u32 s48, %[alo], s53
RA 216,1,0
HZ 30,18
M 8,32,2
s_addc_u32 s49, %[ahi], 0
LL 17,76
LL 124,77
M 8,33,3
s_add_u32 s52, s52, %[d]
LA 17
v_and_b32 v22, 0xfffffcff, v1
M 9,44,2
s_cmp_eq_u32 s52, %[wrap]
LC_ 124
M 9,45,3
s_cselect_b32 s52, %[rst], s52
DQ 3072
M 10,56,2
WL 3
LO 121,16,121
LU 16,121
M 10,57,3
s_add_u32 s50, s50, %[d]
RA 220,1,1024
HS 30,16
M 11,68,2
LL 18,78
LL 125,79
M 11,69,3
s_cmp_eq_u32 s50, %[wrap]
LA 18
RV 252,22,5120
M 6,10,4
s_cselect_b32 s50, %[rst], s50
LC_ 125
M 6,11,5
M 7,22,4
WL 4
LO 124,17,124
LU 17,124
M 7,23,5
s_add_u32 s38, s38, 32768
RA 224,1,2048
HS 30,17
M 8,34,4
s_cmp_eq_u32 s38, s77
LL 16,80
LL 106,81
M 8,35,5
s_cselect_b32 s38, s76, s38
LA 16
M 9,46,4
s_barrier
LC_ 106
M 9,47,5
s_sub_u32 s39, 0, s39
M 10,58,4
WL 3
LO 125,18,125
LU 18,125
M 10,59,5
RA 228,1,3072
HS 30,18
M 11,70,4
LL 17,82
LL 107,83
M 11,71,5
2:
LA 17
v_pk_add_f16 v26, v26, v30
Z 0,0,0
LC_ 107
ds_read_b32 v20, v5 offset:4096
M 0,1,1
v_pk_add_f16 v26, v26, v29
Z 1,12,0
WL 4
LO 16,16,106
LU 106,16
M 1,13,1
RA 232,1,4096
HZ 27,106
Z 2,24,0
LL 18,84
LL 110,85
M 2,25,1
LA 18
PK 106,16,104,33
Z 3,36,0
LC_ 110
PK 104,16,104,32
M 3,37,1
HX 26
Z 4,48,0
WL 4
LO 17,17,107
LU 107,17
M 4,49,1
s_add_u32 s42, s94, s60
RA 236,1,6144
HS 27,107
Z 5,60,0
LL 16,86
LL 111,87
M 5,61,1
s_addc_u32 s43, s95, 0
LA 16
PK 107,17,105,33
M 0,2,2
LC_ 111
PK 105,17,105,32
M 0,3,3
SU 104
M 1,14,2
WL 3
LO 18,18,110
LU 110,18
M 1,15,3
RA 216,1,7168
HS 27,110
M 2,26,2
LL 17,88
LL 114,89
W1 26,2048
M 2,27,3
LA 17
PK 110,18,108,33
M 3,38,2
LC_ 114
PK 108,18,108,32
M 3,39,3
PO 110
v_readfirstlane_b32 s59, v20
M 4,50,2
WL 4
LO 16,16,111
LU 111,16
M 4,51,3
s_add_u32 s42, s94, s61
RA 220,1,8192
HS 27,111
M 5,62,2
LL 18,90
LL 115,91
M 5,63,3
s_addc_u32 s43, s95, 0
LA 18
PK 111,16,109,33
M 0,4,4
LC_ 115
PK 109,16,109,32
M 0,5,5
PO 111
M 1,16,4
WL 3
LO 17,17,114
LU 114,17
M 1,17,5
RA 224,1,9216
HZ 29,114
M 2,28,4
LL 16,92
LL 118,93
M 2,29,5
LA 16
PK 114,17,112,33
M 3,40,4
LC_ 118
PK 112,17,112,32
M 3,41,5
SU 108
M 4,52,4
WL 3
LO 18,18,115
LU 115,18
M 4,53,5
s_add_u32 s42, s94, s62
RA 228,1,10240
HS 29,115
M 5,64,4
LL 17,94
LL 119,95
M 5,65,5
s_addc_u32 s43, s95, 0
LA 17
PK 115,18,113,33
M 0,6,0
LC_ 119
PK 113,18,113,32
M 0,7,1
SU 112
M 1,18,0
WL 3
LO 16,16,118
LU 118,16
M 1,19,1
RA 232,1,11264
HS 29,118
M 2,30,0
LL 18,96
LL 122,97
M 2,31,1
LA 18
)"
R"(PK 118,16,116,33
M 3,42,0
LC_ 122
PK 116,16,116,32
M 3,43,1
RV 252,22,5392
PO 118
M 4,54,0
WL 4
LO 17,17,119
LU 119,17
M 4,55,1
s_add_u32 s42, s94, s63
RA 236,1,12288
HS 29,119
M 5,66,0
LL 16,98
LL 123,99
M 5,67,1
s_addc_u32 s43, s95, 0
LA 16
PK 119,17,117,33
M 0,8,2
LC_ 123
PK 117,17,117,32
M 0,9,3
PO 119
M 1,20,2
WL 4
LO 18,18,122
LU 122,18
M 1,21,3
RA 216,1,13312
HZ 30,122
M 2,32,2
LL 17,100
LL 126,101
M 2,33,3
LA 17
PK 122,18,120,33
M 3,44,2
LC_ 126
PK 120,18,120,32
M 3,45,3
SU 116
M 4,56,2
WL 3
LO 16,16,123
LU 123,16
M 4,57,3
s_add_u32 s42, s94, s64
RA 220,1,14336
HS 30,123
M 5,68,2
LL 18,102
LL 127,103
M 5,69,3
s_addc_u32 s43, s95, 0
LA 18
PK 123,16,121,33
M 0,10,4
LC_ 127
PK 121,16,121,32
M 0,11,5
SU 120
M 1,22,4
WL 3
LO 17,17,126
LU 126,17
M 1,23,5
RA 224,1,15360
HS 30,126
M 2,34,4
LL 16,56
LL 104,57
M 2,35,5
LA 16
PK 126,17,124,33
M 3,46,4
LC_ 104
PK 124,17,124,32
M 3,47,5
ds_read_b64 v[14:15], v5 offset:2048
PO 126
M 4,58,4
WL 4
LO 18,18,127
LU 127,18
M 4,59,5
s_add_u32 s42, s94, s65
RA 228,1,16384
HS 30,127
M 5,70,4
LL 17,58
LL 105,59
M 5,71,5
s_addc_u32 s43, s95, 0
LA 17
PK 127,18,125,33
Z 6,0,0
LC_ 105
PK 125,18,125,32
M 6,1,1
PO 127
v_subrev_u32 v5, s39, v5
Z 7,12,0
WL 4
LO 104,16,104
LU 16,104
M 7,13,1
RA 232,1,17408
HZ 26,16
Z 8,24,0
WL 4
LL 18,60
LL 108,61
M 8,25,1
s_mul_i32 s53, s50, 25600
LA 18
v_add_f32 v14, v14, v15
Z 9,36,0
s_add_u32 s40, %[wlo], s53
LC_ 108
v_add_f32_dpp v14, v14, v14 quad_perm:[1,0,3,2] row_mask:0xf bank_mask:0xf
M 9,37,1
s_addc_u32 s41, %[whi], 0
Z 10,48,0
WL 3
LO 105,17,105
LU 17,105
M 10,49,1
s_mov_b32 m0, s38
RA 236,1,18432
HS 26,17
Z 11,60,0
LL 16,62
LL 109,63
M 11,61,1
LA 16
SU 124
M 6,2,2
LC_ 109
DW 0
M 6,3,3
WV 11
v_add_f32_dpp v14, v14, v14 quad_perm:[2,3,0,1] row_mask:0xf bank_mask:0xf
v_pk_add_f16 v27, v27, v30
M 7,14,2
WL 3
LO 108,18,108
LU 18,108
v_pk_add_f16 v27, v27, v29
M 7,15,3
s_add_u32 s37, s37, 32768
RA 216,1,19456
HS 26,18
HX 27
M 8,26,2
s_cmp_eq_u32 s37, s75
LL 17,64
LL 112,65
W1 27,3072
M 8,27,3
s_cselect_b32 s37, s74, s37
LA 17
DW 1024
v_readfirstlane_b32 s72, v11
M 9,38,2
LC_ 112
v_add_f32_dpp v14, v14, v14 row_half_mirror row_mask:0xf bank_mask:0xf
v_mov_b32 v12, s72
M 9,39,3
s_max_u32 s59, s59, %[nb]
W2 12,12,16
v_cvt_f64_f32 v[14:15], v14
M 10,50,2
WL 5
LO 109,16,109
LU 16,109
M 10,51,3
s_add_u32 %[nb], %[nb], 1
RA 220,1,20480
HS 26,16
M 11,62,2
LL 18,66
LL 113,67
M 11,63,3
LA 18
s_mov_b64 exec, s[54:55]
SP
s_mov_b64 exec, -1
M 6,4,4
LC_ 113
DW 2048
M 6,5,5
v_add_u32 v4, s39, v4
s_mov_b64 exec, 1
AT
s_mov_b64 exec, -1
M 7,16,4
WL 4
LO 112,17,112
LU 17,112
M 7,17,5
s_mul_i32 s53, s51, 49152
RA 224,1,21504
HZ 29,17
M 8,28,4
s_bitcmp1_b64 s[92:93], s51
LL 16,68
LL 116,69
M 8,29,5
s_cselect_b64 s[94:95], s[88:89], s[86:87]
LA 16
M 9,40,4
s_add_u32 s94, s94, s53
LC_ 116
M 9,41,5
s_addc_u32 s95, s95, 0
M 10,52,4
WL 3
LO 113,18,113
LU 18,113
M 10,53,5
s_add_u32 s51, s51, %[d]
RA 228,1,22528
HS 29,18
M 11,64,4
LL 17,70
LL 117,71
M 11,65,5
s_cmp_eq_u32 s51, %[wrap]
LA 17
M 6,6,0
s_cselect_b32 s51, %[rst], s51
LC_ 117
M 6,7,1
s_add_u32 s40, s40, 3072
M 7,18,0
WL 3
LO 116,16,116
LU 16,116
M 7,19,1
s_addc_u32 s41, s41, 0
RA 232,1,23552
HS 29,16
M 8,30,0
s_add_u32 m0, s38, 3072
LL 18,72
LL 120,73
M 8,31,1
LA 18
DW 0
M 9,42,0
LC_ 120
DW 1024
M 9,43,1
M 10,54,0
WL 3
LO 117,17,117
LU 17,117
M 10,55,1
RA 236,1,24576
HS 29,17
M 11,66,0
LL 16,74
LL 121,75
M 11,67,1
LA 16
DW 2048
M 6,8,2
LC_ 121
M 6,9,3
s_lshl_b32 s53, s52, 8
v_add_u32 v1, s37, v0
M 7,20,2
WL 3
LO 120,18,120
LU 18,120
M 7,21,3
s_add_u32 s48, %[alo], s53
RA 216,1,0
HZ 30,18
M 8,32,2
s_addc_u32 s49, %[ahi], 0
LL 17,76
LL 124,77
M 8,33,3
s_add_u32 s52, s52, %[d]
LA 17
v_and_b32 v22, 0xfffffcff, v1
M 9,44,2
s_cmp_eq_u32 s52, %[wrap]
LC_ 124
M 9,45,3
s_cselect_b32 s52, %[rst], s52
DQ 3072
M 10,56,2
WL 3
LO 121,16,121
LU 16,121
M 10,57,3
s_add_u32 s50, s50, %[d]
RA 220,1,1024
HS 30,16
M 11,68,2
LL 18,78
LL 125,79
M 11,69,3
s_cmp_eq_u32 s50, %[wrap]
LA 18
RV 252,22,5120
M 6,10,4
s_cselect_b32 s50, %[rst], s50
LC_ 125
M 6,11,5
M 7,22,4
WL 4
LO 124,17,124
LU 17,124
M 7,23,5
s_add_u32 s38, s38, 32768
RA 224,1,2048
HS 30,17
M 8,34,4
s_cmp_eq_u32 s38, s77
LL 16,80
LL 106,81
M 8,35,5
s_cselect_b32 s38, s76, s38
LA 16
M 9,46,4
s_barrier
LC_ 106
M 9,47,5
s_sub_u32 s39, 0, s39
M 10,58,4
WL 3
LO 125,18,125
LU 18,125
M 10,59,5
RA 228,1,3072
HS 30,18
M 11,70,4
s_cmp_lt_u32 s59, 32
LL 17,82
LL 107,83
M 11,71,5
s_cbranch_scc1 2b
LA 17
LC_ 107
ds_read_b64 v[14:15], v5 offset:2048
v_pk_add_f16 v26, v26, v30
v_pk_add_f16 v26, v26, v29
HX 26
v_subrev_u32 v5, s39, v5
W1 26,2048
WL 0
LO 16,16,106
LU 106,16
HZ 27,106
LL 18,84
LA 18
LL 110,85
PK 106,16,104,33
v_add_f32 v14, v14, v15
s_nop 1
v_add_f32_dpp v14, v14, v14 quad_perm:[1,0,3,2] row_mask:0xf bank_mask:0xf
s_nop 1
v_add_f32_dpp v14, v14, v14 quad_perm:[2,3,0,1] row_mask:0xf bank_mask:0xf
s_nop 1
v_add_f32_dpp v14, v14, v14 row_half_mirror row_mask:0xf bank_mask:0xf
v_cvt_f64_f32 v[14:15], v14
s_mov_b64 exec, s[54:55]
SP
s_mov_b64 exec, -1
LC_ 110
PK 104,16,104,32
LO 17,17,107
LU 107,17
s_add_u32 s42, s94, s60
s_addc_u32 s43, s95, 0
HS 27,107
LL 16,86
LA 16
LL 111,87
LC_ 111
PK 107,17,105,33
PK 105,17,105,32
SU 104
WL 0
LO 18,18,110
LU 110,18
HS 27,110
LL 17,88
LA 17
LL 114,89
LC_ 114
PK 110,18,108,33
PK 108,18,108,32
)"
R"(PO 110
LO 16,16,111
LU 111,16
HS 27,111
s_add_u32 s42, s94, s61
s_addc_u32 s43, s95, 0
LL 18,90
LA 18
LL 115,91
LC_ 115
PK 111,16,109,33
PK 109,16,109,32
PO 111
WL 0
LO 17,17,114
LU 114,17
HZ 29,114
LL 16,92
LA 16
LL 118,93
LC_ 118
PK 114,17,112,33
PK 112,17,112,32
SU 108
LO 18,18,115
LU 115,18
s_add_u32 s42, s94, s62
s_addc_u32 s43, s95, 0
HS 29,115
LL 17,94
LA 17
LL 119,95
LC_ 119
PK 115,18,113,33
PK 113,18,113,32
SU 112
WL 0
LO 16,16,118
LU 118,16
HS 29,118
LL 18,96
LA 18
LL 122,97
LC_ 122
PK 118,16,116,33
PK 116,16,116,32
PO 118
LO 17,17,119
LU 119,17
HS 29,119
s_add_u32 s42, s94, s63
s_addc_u32 s43, s95, 0
LL 16,98
LA 16
LL 123,99
LC_ 123
PK 119,17,117,33
PK 117,17,117,32
PO 119
WL 0
LO 18,18,122
LU 122,18
HZ 30,122
LL 17,100
LA 17
LL 126,101
LC_ 126
PK 122,18,120,33
PK 120,18,120,32
SU 116
LO 16,16,123
LU 123,16
s_add_u32 s42, s94, s64
s_addc_u32 s43, s95, 0
HS 30,123
LL 18,102
LA 18
LL 127,103
LC_ 127
PK 123,16,121,33
PK 121,16,121,32
SU 120
WL 0
LO 17,17,126
LU 126,17
HS 30,126
PK 126,17,124,33
PK 124,17,124,32
PO 126
LO 18,18,127
LU 127,18
HS 30,127
s_add_u32 s42, s94, s65
s_addc_u32 s43, s95, 0
v_pk_add_f16 v27, v27, v30
PK 127,18,125,33
v_pk_add_f16 v27, v27, v29
PK 125,18,125,32
HX 27
PO 127
W1 27,3072
s_nop 1
v_add_u32 v4, s39, v4
SU 124
s_lshl_b32 s53, s52, 8
s_add_u32 s48, %[alo], s53
s_addc_u32 s49, %[ahi], 0
s_add_u32 s52, s52, %[d]
s_cmp_eq_u32 s52, %[wrap]
s_cselect_b32 s52, %[rst], s52
WL 0
WV 0
s_barrier
s_sub_u32 s39, 0, s39
ds_read_b64 v[14:15], v5 offset:2048
s_nop 1
v_subrev_u32 v5, s39, v5
WL 0
v_add_f32 v14, v14, v15
s_nop 1
v_add_f32_dpp v14, v14, v14 quad_perm:[1,0,3,2] row_mask:0xf bank_mask:0xf
s_nop 1
v_add_f32_dpp v14, v14, v14 quad_perm:[2,3,0,1] row_mask:0xf bank_mask:0xf
s_nop 1
v_add_f32_dpp v14, v14, v14 row_half_mirror row_mask:0xf bank_mask:0xf
v_cvt_f64_f32 v[14:15], v14
s_mov_b64 exec, s[54:55]
SP
s_mov_b64 exec, -1
GE
s_waitcnt vmcnt(0) lgkmcnt(0)
8:
s_branch 6f
.Lk1s0:
FR
s_waitcnt lgkmcnt(0)
v_cmp_gt_u32 vcc, v19, v15
s_nop 4
s_cbranch_vccnz .Lk1s0
s_branch .Lk1r0
.Lk1s1:
FR
s_waitcnt lgkmcnt(0)
v_cmp_gt_u32 vcc, v19, v15
s_nop 4
s_cbranch_vccnz .Lk1s1
s_branch .Lk1r1
.Lk1s2:
FR
s_waitcnt lgkmcnt(0)
v_cmp_gt_u32 vcc, v19, v15
s_nop 4
s_cbranch_vccnz .Lk1s2
s_branch .Lk1r2
.Lk1s3:
FR
s_waitcnt lgkmcnt(0)
v_cmp_gt_u32 vcc, v19, v15
s_nop 4
s_cbranch_vccnz .Lk1s3
s_branch .Lk1r3
.Lk1s4:
FR
s_waitcnt lgkmcnt(0)
v_cmp_gt_u32 vcc, v19, v15
s_nop 4
s_cbranch_vccnz .Lk1s4
s_branch .Lk1r4
.Lk1s5:
FR
s_waitcnt lgkmcnt(0)
v_cmp_gt_u32 vcc, v19, v15
s_nop 4
s_cbranch_vccnz .Lk1s5
s_branch .Lk1r5
.Lk1s6:
FR
s_waitcnt lgkmcnt(0)
v_cmp_gt_u32 vcc, v19, v15
s_nop 4
s_cbranch_vccnz .Lk1s6
s_branch .Lk1r6
.Lk1s7:
FR
s_waitcnt lgkmcnt(0)
v_cmp_gt_u32 vcc, v19, v15
s_nop 4
s_cbranch_vccnz .Lk1s7
s_branch .Lk1r7
.Lk1s8:
FR
s_waitcnt lgkmcnt(0)
v_cmp_gt_u32 vcc, v19, v15
s_nop 4
s_cbranch_vccnz .Lk1s8
s_branch .Lk1r8
.Lk1s9:
FR
s_waitcnt lgkmcnt(0)
v_cmp_gt_u32 vcc, v19, v15
s_nop 4
s_cbranch_vccnz .Lk1s9
s_branch .Lk1r9
.Lk1s10:
FR
s_waitcnt lgkmcnt(0)
v_cmp_gt_u32 vcc, v19, v15
s_nop 4
s_cbranch_vccnz .Lk1s10
s_branch .Lk1r10
.Lk1s11:
FR
s_waitcnt lgkmcnt(0)
v_cmp_gt_u32 vcc, v19, v15
s_nop 4
s_cbranch_vccnz .Lk1s11
s_branch .Lk1r11
.Lk1s12:
FR
s_waitcnt lgkmcnt(0)
v_cmp_gt_u32 vcc, v19, v15
s_nop 4
s_cbranch_vccnz .Lk1s12
s_branch .Lk1r12
.Lk1s13:
FR
s_waitcnt lgkmcnt(0)
v_cmp_gt_u32 vcc, v19, v15
s_nop 4
s_cbranch_vccnz .Lk1s13
s_branch .Lk1r13
.Lk1s14:
FR
s_waitcnt lgkmcnt(0)
v_cmp_gt_u32 vcc, v19, v15
s_nop 4
s_cbranch_vccnz .Lk1s14
s_branch .Lk1r14
.Lk1s15:
FR
s_waitcnt lgkmcnt(0)
v_cmp_gt_u32 vcc, v19, v15
s_nop 4
s_cbranch_vccnz .Lk1s15
s_branch .Lk1r15
.Lk1s16:
FR
s_waitcnt lgkmcnt(0)
v_cmp_gt_u32 vcc, v19, v15
s_nop 4
s_cbranch_vccnz .Lk1s16
s_branch .Lk1r16
.Lk1s17:
FR
s_waitcnt lgkmcnt(0)
v_cmp_gt_u32 vcc, v19, v15
s_nop 4
s_cbranch_vccnz .Lk1s17
s_branch .Lk1r17
.Lk1s18:
FR
s_waitcnt lgkmcnt(0)
v_cmp_gt_u32 vcc, v19, v15
s_nop 4
s_cbranch_vccnz .Lk1s18
s_branch .Lk1r18
.Lk1s19:
FR
s_waitcnt lgkmcnt(0)
v_cmp_gt_u32 vcc, v19, v15
s_nop 4
s_cbranch_vccnz .Lk1s19
s_branch .Lk1r19
6:
.purgem M
.purgem Z
.purgem LZ
.purgem LS
.purgem LD
.purgem GZ
.purgem GS
.purgem CL
.purgem CH
.purgem XL
.purgem XN
.purgem F
.purgem U
.purgem E
.purgem A
.purgem R
.purgem C
.purgem S0
.purgem S
.purgem HC
.purgem HF
.purgem HM
.purgem HE
.purgem HG
.purgem HA
.purgem HR
.purgem HQ
.purgem HP
.purgem HY
.purgem HK
.purgem LQ
.purgem LL
.purgem LH
.purgem LA
.purgem LC_
.purgem LO
.purgem HZ
.purgem HS
.purgem HT
.purgem HX
.purgem UX
.purgem EX
.purgem AX
.purgem RX
.purgem TX
.purgem SX
.purgem AW
.purgem RA
.purgem RV
.purgem W2
.purgem W1
.purgem DW
.purgem DF
.purgem DX
.purgem ST
.purgem SU
.purgem SP
.purgem AT
.purgem PFA
.purgem PFN
.purgem PFW
.purgem PF0
.purgem WL
.purgem WV
.purgem LU
.purgem CF
.purgem XS
.purgem AR
.purgem DQ
.purgem GE
.purgem BW
.purgem BR
.purgem LK
.purgem PK
.purgem PO
.purgem LG
.purgem DY
.purgem FP
.purgem FR
)"
      : [nb] "=&s"(nb)
      : [xlo] "s"((unsigned)xp), [xhi] "s"((unsigned)(xp >> 32)), [hlo] "s"((unsigned)hp), [hhi] "s"((unsigned)(hp >> 32)),
        [glo] "s"((unsigned)gp), [ghi] "s"((unsigned)(gp >> 32)), [wlo] "s"((unsigned)wp), [whi] "s"((unsigned)(wp >> 32)),
        [w] "s"(w), [lds] "s"(lds0), [d] "s"(d), [wrap] "s"(wrap), [rst] "s"(rst), [pos0] "s"(pos0), [xo0] "s"(xo0), [xs1] "s"(xs1), [sb1] "s"(sb1), [r0o] "s"(r0o), [tsel] "s"(tsel), [s2] "s"(s2),
        [alo] "s"((unsigned)ap), [ahi] "s"((unsigned)(ap >> 32)), [ca0] "s"(ca0), [pm] "s"(pm), [pf] "s"(pf)
      : K1_CLOB);
  TS(6);
  MK_END(1);
}

typedef __bf16 bf16x8_t __attribute__((ext_vector_type(8)));
DI f32x4 mfma16(u32x4 a, u32x4 b, f32x4 c) {
  return __builtin_amdgcn_mfma_f32_16x16x32_bf16(__builtin_bit_cast(bf16x8_t, a), __builtin_bit_cast(bf16x8_t, b), c, 0, 0, 0);
}
DI float grn_gx(double s, float scale) { return round_bf16(__builtin_amdgcn_sqrtf(round_bf16(round_bf16((float)s * scale) + 1e-6f))); }
DI void slow_fc1(const u16* __restrict__ x, const u16* __restrict__ dw, char* __restrict__ hws, float* sacc, int w) {
  const int lane = __builtin_amdgcn_mbcnt_hi(~0u, __builtin_amdgcn_mbcnt_lo(~0u, 0u)), m = lane & 15, g = lane >> 4;
  for (int i = lane; i < HID; i += 64) sacc[i] = 0.f;
#pragma unroll 1
  for (int it = 0; it < 9; ++it) {
    const int sub = blockIdx.x * 36 + it * 4 + w;
    const unsigned px = sub_pixel(x, sub, m);
    u32x4 A[12];
    float sum = 0.f;
    const u16* dq = dw;
    asm volatile("" : "+s"(dq));
#pragma unroll
    for (int ks = 0; ks < 12; ++ks) {
      const u32x4 v = *(const u32x4*)(x + (size_t)px * CH + 32 * ks + 8 * g);
      const u32x4 d = *(const u32x4*)(dq + 32 * ks + 8 * g);
#pragma unroll
      for (int j = 0; j < 4; ++j) {
        A[ks][j] = pack_bf16(bf_lo(v[j]) * bf_lo(d[j]), bf_hi(v[j]) * bf_hi(d[j]));
        sum += bf_lo(A[ks][j]) + bf_hi(A[ks][j]);
      }
      asm volatile("" : "+v"(A[ks]));
    }
    sum += __shfl_xor(sum, 16); sum += __shfl_xor(sum, 32);
    const float mean = sum * (1.0f / CH);
    float var = 0.f;
#pragma unroll
    for (int ks = 0; ks < 12; ++ks) {
#pragma unroll
      for (int j = 0; j < 4; ++j) {
        const float a = bf_lo(A[ks][j]) - mean, b = bf_hi(A[ks][j]) - mean;
        var += a * a + b * b;
      }
      asm volatile("" : "+v"(A[ks]));
    }
    var += __shfl_xor(var, 16); var += __shfl_xor(var, 32);
    const float mb = round_bf16(mean);
    const float rd = 1.0f / round_bf16(sqrtf(round_bf16(round_bf16(var * (1.0f / CH)) + 1e-5f)));
#pragma unroll
    for (int ks = 0; ks < 12; ++ks)
#pragma unroll
      for (int j = 0; j < 4; ++j) A[ks][j] = pack_bf16((bf_lo(A[ks][j]) - mb) * rd, (bf_hi(A[ks][j]) - mb) * rd);
#pragma unroll 1
    for (int tc = 0; tc < 96; ++tc) {
      f32x4 acc = {0.f, 0.f, 0.f, 0.f};
#pragma unroll
      for (int ks = 0; ks < 12; ++ks) acc = mfma16(A[ks], *(const u32x4*)(g_buf + (size_t)(tc * 12 + ks) * 1024 + lane * 16), acc);
      float q = 0.f;
#pragma unroll
      for (int r = 0; r < 4; ++r) { acc[r] = gelu_tanh(acc[r]); q = __builtin_fmaf(acc[r], acc[r], q); }
      typedef unsigned u32x2_ __attribute__((ext_vector_type(2)));
      *(u32x2_*)(hws + h_piece(hws, sub, tc >> 1) + g * 256 + ((m + 4 * g) & 15) * 16 + (tc & 1) * 8) = (u32x2_){pack_bf16(acc[0], acc[1]), pack_bf16(acc[2], acc[3])};
      q += __shfl_xor(q, 16); q += __shfl_xor(q, 32);
      if (g == 0) sacc[tc * 16 + m] += q;
    }
  }
  __builtin_amdgcn_wave_barrier();
  __builtin_amdgcn_fence(__ATOMIC_ACQ_REL, "workgroup");
  for (int i = lane; i < HID; i += 64) atomicAdd((double*)(g_buf + G_ACC2) + i, (double)sacc[i]);
}
DI void slow_bar(unsigned* c, int w) {
  const int lane = __builtin_amdgcn_mbcnt_hi(~0u, __builtin_amdgcn_mbcnt_lo(~0u, 0u));
  asm volatile("s_waitcnt vmcnt(0)" ::: "memory");
  __syncthreads();
  if (w == 0 && lane == 0) {
    __threadfence();
    asm volatile("s_waitcnt vmcnt(0)" ::: "memory");
    __hip_atomic_fetch_add(c, 1u, __ATOMIC_RELAXED, __HIP_MEMORY_SCOPE_AGENT);
    for (int i = 0; i < (1 << 22) && __hip_atomic_load(c, __ATOMIC_RELAXED, __HIP_MEMORY_SCOPE_AGENT) < (unsigned)NWG; ++i) __builtin_amdgcn_s_sleep(32);
    __threadfence();
    asm volatile("s_waitcnt vmcnt(0)" ::: "memory");
  }
  __syncthreads();
}

#define K2_CLOB "memory", "vcc", "scc", "s48", "s49", "s50", "s51", "s52", "s53", "s54", "s55", "s56", "s57", "s58", "s59", "s60", "s61", "s62", "s63", "s64", "s65", "s66", "s67", \
  "s68", "s69", "s70", "s71", "s72", "s73", "s74", "s75", "s76", "s77", "s78", "s79", "s80", "s81", "s84", "s85", "s86", "s87", "s88", "s89", "s90", "s91", "s92", "s93", "s94", "s95", "s96", "s97", "s98", "s99", "s38", "s39", "s40", "s41", "s42", "s43", "s44", "s45", "s46", "s47", VA_CLOB
DI void slow_fc2(const u16* __restrict__ x, const char* __restrict__ hws, u16* __restrict__ out, const u16* __restrict__ grn_g, const u16* __restrict__ w2,
                 char* smem, int w) {
  float* sc = (float*)smem;
  float* red = sc + HID;
  u16* wf = (u16*)(smem + 8192);
  const int lane = __builtin_amdgcn_mbcnt_hi(~0u, __builtin_amdgcn_mbcnt_lo(~0u, 0u)), tid = w * 64 + lane, m = lane & 15, g = lane >> 4;
  __syncthreads();
  float part = 0.f;
  for (int c = tid; c < HID; c += 256) {
    const float gx = grn_gx(((const double*)(g_buf + G_ACC2))[c], 1.0f);
    sc[c] = gx; part += gx;
  }
#pragma unroll
  for (int o = 32; o > 0; o >>= 1) part += __shfl_xor(part, o);
  if (lane == 0) red[tid >> 6] = part;
  __syncthreads();
  const float gmean = round_bf16(round_bf16(((red[0] + red[1]) + (red[2] + red[3])) * (1.0f / HID)) + 1e-6f);
  for (int c = tid; c < HID; c += 256) sc[c] = (1.0f + ld_bf16(grn_g + c) * round_bf16(sc[c] / gmean)) ;
#pragma unroll 1
  for (int it = 0; it < 9; ++it) {
    const int sub = blockIdx.x * 36 + it * 4 + (tid >> 6);
    f32x4 acc[24];
#pragma unroll
    for (int i = 0; i < 24; ++i) acc[i] = (f32x4){0.f, 0.f, 0.f, 0.f};
#pragma unroll 1
    for (int t = 0; t < 48; ++t) {
      __syncthreads();
      for (int e = tid; e < 32 * CH; e += 256) {
        const int k = e / CH, n = e % CH;
        wf[((n >> 4) * 64 + (k >> 3) * 16 + (n & 15)) * 8 + (k & 7)] = (u16)pack_bf16(sc[32 * t + k] * ld_bf16(w2 + (size_t)(32 * t + k) * CH + n), 0.f);
      }
      __syncthreads();
      const char* hp = hws + h_piece(hws, sub, t) + (m >> 2) * 256 + (g >> 1) * 8 + 2 * (m & 3);
      u32x4 A;
#pragma unroll
      for (int j = 0; j < 4; ++j) {
        unsigned v[2];
#pragma unroll
        for (int k = 0; k < 2; ++k) {
          v[k] = *(const u16*)(hp + ((8 * (g & 1) + 2 * j + k + (m & 12)) & 15) * 16);
        }
        A[j] = v[0] | (v[1] << 16);
      }
#pragma unroll
      for (int i = 0; i < 24; ++i) acc[i] = mfma16(A, *(const u32x4*)(wf + (i * 64 + lane) * 8), acc[i]);
    }
#pragma unroll
    for (int i = 0; i < 24; ++i) {
      const int n = 16 * i + m;
      const float* bp = g_cs + 4 * 384 + cs_index(n);
      const float bias = (bp[0] + bp[384]) + (bp[768] + bp[1152]);
#pragma unroll
      for (int r = 0; r < 4; ++r) {
        const size_t o = (size_t)(sub_pixel(x, sub, 4 * g) + r) * CH + n;
        out[o] = (u16)pack_bf16(acc[i][r] + bias + ld_bf16(x + o), 0.f);
      }
    }
  }
}
__global__ __launch_bounds__(256, 1) void k2_fc2(const u16* __restrict__ x, char* __restrict__ hws, u16* __restrict__ out,
                                                 const u16* __restrict__ grn_g, const u16* __restrict__ w2, const u16* __restrict__ dw,
                                                 const u16* __restrict__ w1) {
  extern __shared__ __attribute__((aligned(16))) char smem[];
  const int w = __builtin_amdgcn_readfirstlane(threadIdx.x >> 6);
  const unsigned pg = w & 1, nh = w >> 1;
  const unsigned lds0 = lds_addr(smem) + 1024;
  MK_START(4); TS(9);
  const uint64_t pp = uni64((uint64_t)g_prm + (blockIdx.x * 4 + w) * 256), ap = (uint64_t)g_buf + K1_G_ACC + w * 1024;
  const unsigned wm = lds0 + w * 6144, ab = lds0 + nh * 12288;
  const unsigned hb = lds0 + K2_HB + pg * 2 * K2_RSZ, hm = hb + nh * K2_RSZ;
  const unsigned bm = w * 384, bb = lds0 + K2_E_OFF + nh * 768;
  const unsigned pm = lds0 + K2_PF_OFF, sb = lds0 + K2_S16_OFF, sw = sb + w * 256;
  uint64_t flg;
#define K2_OPS [pp] "s"(pp), [alo] "s"((unsigned)ap), [ahi] "s"((unsigned)(ap >> 32)), [wm] "s"(wm), [hm] "s"(hm), [bm] "s"(bm), [ab] "s"(ab), [hb] "s"(hb), [bb] "s"(bb), \
         [sb] "s"(sb), [sw] "s"(sw), [pm] "s"(pm)
  asm volatile(
R"(.macro MA t,a,b
v_mfma_f32_16x16x32_f16 a[\t:\t+3], v[\b:\b+3], a[\a:\a+3], a[\t:\t+3]
.endm
.macro MV t,a,b
v_mfma_f32_16x16x32_f16 v[\t:\t+3], v[\b:\b+3], a[\a:\a+3], v[\t:\t+3]
.endm
.macro NA t,a,b
v_mfma_f32_16x16x32_f16 a[\t:\t+3], a[\b:\b+3], a[\a:\a+3], a[\t:\t+3]
.endm
.macro NV t,a,b
v_mfma_f32_16x16x32_f16 v[\t:\t+3], a[\b:\b+3], a[\a:\a+3], v[\t:\t+3]
.endm
.macro AN d,s
v_and_b32 v[\d], s93, v[\s]
.endm
.macro SH d
v_lshrrev_b32 v[\d], 4, v[\d]
.endm
.macro PA_ d,h,r
v_perm_b32 v[\d], v[\h], v[\r], s54
.endm
.macro PB_ d,h,r
v_perm_b32 v[\d], v[\h], v[\r], s55
.endm
.macro AW a,s
v_accvgpr_write_b32 a[\a], v[\s]
.endm
.macro AR d,a
v_accvgpr_read_b32 v[\d], a[\a]
.endm
.macro UL d,x
v_lshlrev_b32 v[\d], 16, v[\x]
.endm
.macro UH d,x
v_and_b32 v[\d], s38, v[\x]
.endm
.macro AD d,b
v_fmac_f32 v[\d], s53, v[\b]
.endm
.macro EF d,e
v_fma_f32 v[\d], v[\d], s39, v[\e]
.endm
.macro TY d,o
ds_read_b64_tr_b16 v[\d:\d+1], v2 offset:\o
.endm
.macro TX d,o
ds_read_b64_tr_b16 v[\d:\d+1], v51 offset:\o
.endm
.macro RA d,o
ds_read_b128 a[\d:\d+3], v1 offset:\o
.endm
.macro RC d,o
ds_read_b128 a[\d:\d+3], v5 offset:\o
.endm
.macro BV d,o
ds_read_b128 v[\d:\d+3], v4 offset:\o
.endm
.macro BA d,o
ds_read_b128 a[\d:\d+3], v4 offset:\o
.endm
.macro PA a
s_mov_b32 m0, %[pm]
s_getpc_b64 s[46:47]
s_andn2_b32 s46, s46, 0x7f
s_add_u32 s46, s46, s81
s_addc_u32 s47, s47, 0
s_add_u32 s46, s46, \a
s_addc_u32 s47, s47, 0
.endm
.macro DF
global_load_lds_dwordx4 v0, s[46:47]
.endm
.macro DW o
global_load_lds_dwordx4 v0, s[40:41] offset:\o sc1
.endm
.macro DR o
global_load_lds_dwordx4 v0, s[40:41] offset:\o
.endm
.macro DH o
global_load_lds_dwordx4 v0, s[40:41] offset:\o nt
.endm
.macro DG o
s_mov_b32 exec_hi, 0
global_load_lds_dwordx4 v0, s[40:41] offset:\o nt
s_mov_b32 exec_hi, -1
.endm
.macro XL d,o
global_load_dwordx2 v[\d:\d+1], v47, s[44:45] offset:\o
.endm
.macro ST d,o
global_store_dwordx2 v50, v[\d:\d+1], s[42:43] offset:\o nt sc1
.endm
.macro SA d,o
global_store_dwordx2 v50, a[\d:\d+1], s[42:43] offset:\o nt sc1
.endm
.macro CV d,a,b
v_cvt_pk_bf16_f32 v[\d], v[\a], v[\b]
.endm
.macro RS d,o
ds_read_b128 v[\d:\d+3], v46 offset:\o
.endm
.macro PM d,s
v_pk_mul_f16 v[\d], v[\d], v[\s]
.endm
.macro PF d,n
v_pk_fma_f16 v[\d], v[\d], v[14+\n], v[22+\n]
.endm
.macro FS
global_store_dword v0, v54, s[96:97] sc1
.endm
.macro FL
global_load_dword v54, v55, s[98:99] sc1
.endm
.macro FB
s_cbranch_vccnz 21b
22:
.endm
.macro FC
s_cbranch_vccnz 26b
27:
.endm
.macro FG
s_or_b64 s[54:55], s[44:45], vcc
s_cbranch_scc1 23b
24:
.endm
.macro FX
s_branch 25f
21:
s_sleep 4
global_load_dword v54, v55, s[98:99] sc1
s_waitcnt vmcnt(0)
v_cmp_eq_u32 vcc, 0, v54
s_sub_u32 s80, s80, 1
s_cbranch_scc1 22f
s_nop 2
s_cbranch_vccnz 21b
s_branch 22f
26:
s_sleep 4
global_load_dword v54, v55, s[98:99] sc1
s_waitcnt vmcnt(0)
v_cmp_ne_u32 vcc, 2, v54
s_sub_u32 s80, s80, 1
s_cbranch_scc1 27f
s_nop 2
s_cbranch_vccnz 26b
s_branch 27f
23:
v_mov_b32 v10, 1
s_mov_b64 exec, 1
global_store_dword v0, v10, s[78:79] offset:384 sc1
s_mov_b64 exec, -1
s_nop 4
s_branch 24f
25:
.endm
.macro FP
ds_inc_u32 v0, v128
.endm
.macro FR
ds_read_b32 v10, v0
.endm
.macro FQ n
v_cmp_gt_u32 vcc, \n, v10
.endm
.macro FZ n
s_cbranch_vccnz .Lk2s\n
.Lk2r\n:
.endm
.macro FY n
.Lk2s\n:
FR
s_waitcnt lgkmcnt(0)
FQ \n
s_nop 4
s_cbranch_vccnz .Lk2s\n
s_branch .Lk2r\n
.endm
.macro WL n
s_waitcnt lgkmcnt(\n)
.endm
.macro WV n
s_waitcnt vmcnt(\n)
.endm
.macro M n
.if \n < 82
v_mfma_f32_16x16x32_f16 a[36+4*(\n/2):36+4*(\n/2)+3], v[56+8*((\n/2)%%6)+4*(\n&1):56+8*((\n/2)%%6)+4*(\n&1)+3], a[8*((\n/12)%%4)+4*(\n&1):8*((\n/12)%%4)+4*(\n&1)+3], a[36+4*(\n/2):36+4*(\n/2)+3]
.else
v_mfma_f32_16x16x32_f16 v[-32+4*(\n/2):-32+4*(\n/2)+3], v[56+8*((\n/2)%%6)+4*(\n&1):56+8*((\n/2)%%6)+4*(\n&1)+3], a[8*((\n/12)%%4)+4*(\n&1):8*((\n/12)%%4)+4*(\n&1)+3], v[-32+4*(\n/2):-32+4*(\n/2)+3]
.endif
.endm
.macro R1 n
.if \n < 64
RA 8*(((\n%%24)/2+2*((\n/24)&1))%%4)+4*(\n&1),1024*\n
.else
RC 8*(((\n%%24)/2+2*((\n/24)&1))%%4)+4*(\n&1),1024*\n-49152
.endif
.endm
.macro R n
R1 \n
R1 (\n+1)
.endm
.macro P n
.if \n & 2
PB_ 56+\n,104+8*(\n/16)+(\n&4)+2*((\n/8)&1)+(\n&1),56+(\n|2)
.else
PA_ 56+\n,104+8*(\n/16)+(\n&4)+2*((\n/8)&1)+(\n&1),56+(\n|2)
.endif
.endm
.macro Q n
P \n
P (\n+1)
.endm
.macro mw k
s_add_u32 m0, %[wm], 1024*\k
.endm
.macro mh k
s_add_u32 m0, %[hm], 512*\k
.endm
.macro ad r,k
s_add_u32 s40, s[\r], 512*\k
s_addc_u32 s41, s[\r+1], 0
.endm
.macro ae d,r,k
s_add_u32 s[\d], s[\r], 512*\k
s_addc_u32 s[\d+1], s[\r+1], 0
.endm
.macro bc m,b,d,r
s_bitcmp1_b32 s[\m], \b
s_cselect_b64 s[\d:\d+1], s[\r+2:\r+3], s[\r:\r+1]
.endm
.macro W k
DW 1024*\k
.endm
.macro H k
DH 1024*\k
.endm
k2_code:
s_load_dwordx16 s[48:63], %[pp], 0x0
s_load_dwordx16 s[64:79], %[pp], 0x40
s_load_dwordx16 s[84:99], %[pp], 0x80
v_mbcnt_lo_u32_b32 v7, -1, 0
v_mbcnt_hi_u32_b32 v7, -1, v7
v_lshlrev_b32 v0, 4, v7
v_add_u32 v88, 0x1000, v0
v_add_u32 v89, 0x2000, v0
s_mov_b32 s44, %[alo]
s_mov_b32 s45, %[ahi]
global_load_dwordx4 v[132:135], v0, s[44:45]
global_load_dwordx4 v[144:147], v88, s[44:45]
global_load_dwordx4 v[148:151], v89, s[44:45]
ae 44,44,24
global_load_dwordx4 v[168:171], v0, s[44:45]
global_load_dwordx4 v[172:175], v88, s[44:45]
global_load_dwordx4 v[192:195], v89, s[44:45]
s_waitcnt lgkmcnt(0)
bc 49,0,66,60
mh 0
ad 66,0
H 0
DG 1024
ae 44,44,24
global_load_dwordx4 v[196:199], v0, s[44:45]
global_load_dwordx4 v[216:219], v88, s[44:45]
global_load_dwordx4 v[220:223], v89, s[44:45]
ae 44,44,24
global_load_dwordx4 v[224:227], v0, s[44:45]
global_load_dwordx4 v[228:231], v88, s[44:45]
global_load_dwordx4 v[240:243], v89, s[44:45]
v_and_b32 v8, 15, v7
v_lshrrev_b32 v9, 4, v7
v_lshlrev_b32 v10, 2, v7
v_add_u32 v202, s65, v0
global_load_dword v244, v10, s[92:93] offset:0
global_load_dword v245, v10, s[92:93] offset:1024
global_load_dword v246, v10, s[92:93] offset:2048
global_load_dwordx4 v[152:155], v0, s[88:89]
s_mov_b64 exec, 0xff
global_load_dwordx4 v[156:159], v202, s[88:89]
s_mov_b64 exec, -1
s_branch 28f
FY 4
FY 8
FY 12
FY 16
FY 20
FY 24
FY 28
FY 32
FY 36
FY 40
FY 44
FY 48
FY 52
FY 56
FY 60
FY 64
FY 68
FY 72
FY 76
FY 80
FY 84
FY 88
FY 92
FY 96
FY 100
FY 104
FY 108
FY 112
FY 116
FY 120
FY 124
FY 128
28:
FX
s_mov_b32 s92, s64
s_mov_b32 s93, 0x0f0f0f0f
v_add_u32 v1, %[ab], v0
v_add_u32 v5, 0xc000, v1
v_and_b32 v10, 3, v7
v_lshrrev_b32 v11, 2, v7
v_lshl_add_u32 v11, v10, 2, v11
v_and_b32 v11, 15, v11
mw 0
ad 86,0
DR 0
v_lshl_add_u32 v11, v10, 4, v11
v_lshl_add_u32 v2, v11, 4, %[hb]
v_lshl_add_u32 v51, v11, 3, %[hb]
v_lshl_add_u32 v4, v8, 4, %[bb]
s_mov_b32 s38, 0xffff0000
s_mov_b32 s80, 0x40000
s_mov_b32 s81, s52
s_mov_b32 s39, s54
v_lshl_add_u32 v46, v9, 4, %[sb]
v_add_u32 v46, 8192, v46
v_bfe_u32 v10, v7, 1, 2
DR 1024
v_bfe_u32 v11, v7, 3, 1
v_and_b32 v12, 1, v7
v_lshl_add_u32 v11, v11, 1, v12
v_lshl_add_u32 v10, v10, 2, v11
v_lshl_add_u32 v10, v9, 4, v10
v_lshl_add_u32 v90, v10, 2, %[sw]
DR 2048
v_lshl_add_u32 v91, v7, 2, %[sw]
s_and_b32 s42, s51, 0xffff
s_lshr_b32 s43, s51, 16
s_add_u32 s42, s42, %[sb]
s_add_u32 s43, s43, %[sb]
v_lshl_add_u32 v200, v9, 4, s42
v_mov_b32 v201, s43
v_mov_b32 v128, 0x35400000
v_mov_b32 v129, 0x35400000
v_mov_b32 v130, 0x35400000
v_mov_b32 v131, 0x35400000
mw 3
ad 86,6
DR 0
AW 32,128
AW 33,129
AW 34,130
AW 35,131
v_mov_b32 v95, 0
ds_write_b32 v0, v95
s_nop 1
v_mov_b32 v95, 0x7f800000
s_mov_b32 s44, 0x44100000
WV 19
v_add_f64 v[132:133], v[132:133], v[168:169]
WV 18
v_add_f64 v[144:145], v[144:145], v[172:173]
DR 1024
WV 18
v_add_f64 v[148:149], v[148:149], v[192:193]
v_add_f64 v[134:135], v[134:135], v[170:171]
v_add_f64 v[146:147], v[146:147], v[174:175]
v_add_f64 v[150:151], v[150:151], v[194:195]
WV 15
v_add_f64 v[132:133], v[132:133], v[196:197]
WV 14
v_add_f64 v[144:145], v[144:145], v[216:217]
DR 2048
WV 14
v_add_f64 v[148:149], v[148:149], v[220:221]
v_add_f64 v[134:135], v[134:135], v[198:199]
v_add_f64 v[146:147], v[146:147], v[218:219]
v_add_f64 v[150:151], v[150:151], v[222:223]
WV 13
v_add_f64 v[132:133], v[132:133], v[224:225]
WV 12
v_add_f64 v[144:145], v[144:145], v[228:229]
bc 49,1,66,60
mh 9
ad 66,192
H 0
WV 12
v_add_f64 v[148:149], v[148:149], v[240:241]
v_add_f64 v[134:135], v[134:135], v[226:227]
)"
R"(v_add_f64 v[146:147], v[146:147], v[230:231]
v_add_f64 v[150:151], v[150:151], v[242:243]
v_cvt_f32_f64 v248, v[132:133]
v_cvt_f32_f64 v252, v[144:145]
DG 1024
v_cvt_f32_f64 v72, v[148:149]
v_cvt_f32_f64 v249, v[134:135]
v_cvt_f32_f64 v253, v[146:147]
v_cvt_f32_f64 v73, v[150:151]
v_mul_f32 v250, s55, v248
v_mul_f32 v254, s55, v252
bc 48,1,64,56
mh 12
ad 64,192
H 0
v_mul_f32 v74, s55, v72
v_mul_f32 v251, s55, v249
v_mul_f32 v255, s55, v253
v_mul_f32 v75, s55, v73
CV 251,250,251
CV 255,254,255
DG 1024
CV 75,74,75
UL 250,251
UL 254,255
UL 74,75
UH 251,251
UH 255,255
mw 24
ad 86,48
DR 0
UH 75,75
v_add_f32 v250, 0x358637bd, v250
v_add_f32 v254, 0x358637bd, v254
v_add_f32 v74, 0x358637bd, v74
v_add_f32 v251, 0x358637bd, v251
v_add_f32 v255, 0x358637bd, v255
DR 1024
v_add_f32 v75, 0x358637bd, v75
CV 251,250,251
CV 255,254,255
CV 75,74,75
UL 250,251
UL 254,255
DR 2048
UL 74,75
UH 251,251
UH 255,255
UH 75,75
v_sqrt_f32 v250, v250
v_sqrt_f32 v254, v254
mw 27
ad 86,54
DR 0
v_sqrt_f32 v74, v74
v_sqrt_f32 v251, v251
v_sqrt_f32 v255, v255
v_sqrt_f32 v75, v75
CV 251,250,251
CV 255,254,255
DR 1024
CV 75,74,75
UL 250,251
UL 254,255
UL 74,75
UH 251,251
UH 255,255
DR 2048
UH 75,75
v_cmp_le_f32 vcc, s44, v248
v_cmp_le_f32 s[54:55], s44, v249
s_nop 0
v_cndmask_b32 v248, v95, v250, vcc
v_cndmask_b32 v249, v95, v251, s[54:55]
v_add_f32 v248, v248, v249
v_cmp_le_f32 vcc, s44, v252
v_cmp_le_f32 s[54:55], s44, v253
s_nop 0
v_cndmask_b32 v252, v95, v254, vcc
v_cndmask_b32 v253, v95, v255, s[54:55]
v_add_f32 v252, v252, v253
v_cmp_le_f32 vcc, s44, v72
v_cmp_le_f32 s[54:55], s44, v73
s_nop 0
v_cndmask_b32 v72, v95, v74, vcc
v_cndmask_b32 v73, v95, v75, s[54:55]
bc 49,2,66,60
mh 18
ad 66,384
H 0
v_add_f32 v72, v72, v73
v_add_f32 v92, v248, v252
v_add_f32 v92, v92, v72
s_nop 1
v_add_f32_dpp v92, v92, v92 row_shr:1 row_mask:0xf bank_mask:0xf
s_nop 1
v_add_f32_dpp v92, v92, v92 row_shr:2 row_mask:0xf bank_mask:0xf
s_nop 1
v_add_f32_dpp v92, v92, v92 row_shr:4 row_mask:0xf bank_mask:0xf
s_nop 1
v_add_f32_dpp v92, v92, v92 row_shr:8 row_mask:0xf bank_mask:0xf
s_nop 1
v_add_f32_dpp v92, v92, v92 row_bcast:15 row_mask:0xa bank_mask:0xf
s_nop 1
v_add_f32_dpp v92, v92, v92 row_bcast:31 row_mask:0xc bank_mask:0xf
ds_write_b32 v91, v92 offset:7168
v_mov_b32 v10, %[sb]
WL 0
s_barrier
ds_read_b32 v96, v10 offset:7420
ds_read_b32 v97, v10 offset:7676
ds_read_b32 v98, v10 offset:7932
ds_read_b32 v99, v10 offset:8188
WV 21
UL 76,244
WV 20
UL 80,245
WV 19
UL 84,246
UH 77,244
DG 1024
UH 81,245
UH 85,246
v_mov_b32 v93, 0
WL 0
v_add_f32 v96, v96, v97
v_add_f32 v98, v98, v99
v_add_f32 v96, v96, v98
bc 48,2,64,56
mh 21
ad 64,384
H 0
v_mul_f32 v94, 0x3a2aaaab, v96
v_cmp_nlt_f32 s[44:45], v96, v95
CV 94,94,94
UL 94,94
v_add_f32 v94, 0x358637bd, v94
CV 94,94,94
UL 94,94
H 1
v_rcp_f32 v94, v94
s_nop 0
v_mul_f32 v250, v94, v250
v_mul_f32 v254, v94, v254
v_mul_f32 v74, v94, v74
v_mul_f32 v251, v94, v251
v_mul_f32 v255, v94, v255
v_mul_f32 v75, v94, v75
H 2
CV 251,250,251
CV 255,254,255
CV 75,74,75
UL 250,251
UL 254,255
UL 74,75
mw 48
ad 86,96
DR 0
UH 251,251
UH 255,255
UH 75,75
v_fma_f32 v250, v76, v250, 1.0
v_fma_f32 v254, v80, v254, 1.0
v_fma_f32 v74, v84, v74, 1.0
DR 1024
v_fma_f32 v251, v77, v251, 1.0
v_fma_f32 v255, v81, v255, 1.0
v_fma_f32 v75, v85, v75, 1.0
v_cvt_pk_f16_f32 v76, v250, v251
v_cvt_pk_f16_f32 v80, v254, v255
v_cvt_pk_f16_f32 v84, v74, v75
ds_write_b32 v90, v76 offset:8192
ds_write_b32 v90, v80 offset:1024
ds_write_b32 v90, v84 offset:2048
v_max3_f32 v93, |v250|, |v251|, v93
DR 2048
v_max3_f32 v93, |v254|, |v255|, v93
v_max3_f32 v93, |v74|, |v75|, v93
v_cmp_lt_f32 vcc, 0x43000000, v93
WL 0
WV 16
s_barrier
TY 106,520
TY 110,14344
TX 58,0
TX 62,13824
TY 66,512
TY 70,14336
RS 14,0
RS 18,64
ds_read_b128 v[176:179], v200
ds_read_b128 v[180:183], v201
R 0
R 2
FG
s_mov_b32 s54, 0x06020400
s_mov_b32 s55, 0x07030501
WL 13
AN 104,106
AN 105,107
SH 106
SH 107
mw 51
ad 86,102
DR 0
AN 106,106
AN 107,107
WL 12
AN 108,110
AN 109,111
SH 110
SH 111
DR 1024
AN 110,110
AN 111,111
WL 10
Q 0
Q 2
DR 2048
Q 4
Q 6
R 4
R 6
WL 7
v_pk_mul_f16 v22, v14, s92
v_pk_mul_f16 v23, v15, s92
v_pk_mul_f16 v24, v16, s92
v_pk_mul_f16 v25, v17, s92
v_pk_mul_f16 v26, v18, s92
v_pk_mul_f16 v27, v19, s92
v_pk_mul_f16 v28, v20, s92
v_pk_mul_f16 v29, v21, s92
PF 56,0
PF 57,1
PF 58,2
PF 59,3
PF 60,4
PF 61,5
PF 62,6
PF 63,7
Q 8
Q 10
Q 12
Q 14
PF 64,0
PF 65,1
PF 66,2
PF 67,3
PF 68,4
PF 69,5
PF 70,6
PF 71,7
PM 152,176
PM 153,177
PM 154,178
PM 155,179
PM 156,180
PM 157,181
PM 158,182
PM 159,183
s_mov_b32 s42, 0x1c001c00
v_pk_mul_f16 v152, v152, s42
v_pk_mul_f16 v153, v153, s42
v_pk_mul_f16 v154, v154, s42
v_pk_mul_f16 v155, v155, s42
v_pk_mul_f16 v156, v156, s42
v_pk_mul_f16 v157, v157, s42
v_pk_mul_f16 v158, v158, s42
v_pk_mul_f16 v159, v159, s42
global_store_dwordx4 v0, v[152:155], s[90:91] sc1
s_mov_b64 exec, 0xff
global_store_dwordx4 v202, v[156:159], s[90:91] sc1
s_mov_b64 exec, -1
s_mov_b32 s42, 0x3c003c00
v_dot2_f32_f16 v52, v152, s42, 0
v_dot2_f32_f16 v53, v156, s42, 0
s_nop 1
v_dot2_f32_f16 v52, v153, s42, v52
v_dot2_f32_f16 v53, v157, s42, v53
s_nop 1
v_dot2_f32_f16 v52, v154, s42, v52
v_dot2_f32_f16 v53, v158, s42, v53
s_nop 1
v_dot2_f32_f16 v52, v155, s42, v52
v_dot2_f32_f16 v53, v159, s42, v53
s_nop 1
WL 0
v_mfma_f32_16x16x32_f16 a[36:39], v[56:59], a[0:3], a[32:35]
mw 72
RS 14,128
RS 18,192
TY 106,5128
M 1
ad 86,144
DR 0
TY 110,18952
v_mfma_f32_16x16x32_f16 a[60:63], v[56:59], a[8:11], a[32:35]
DR 1024
TX 32,4608
TX 36,18432
M 13
DR 2048
TY 40,5120
TY 44,18944
v_mfma_f32_16x16x32_f16 a[40:43], v[64:67], a[0:3], a[32:35]
mw 75
M 3
R 8
v_mfma_f32_16x16x32_f16 a[64:67], v[64:67], a[8:11], a[32:35]
ad 86,150
DR 0
M 15
R 10
v_mfma_f32_16x16x32_f16 a[84:87], v[56:59], a[16:19], a[32:35]
WL 10
DR 1024
v_pk_mul_f16 v22, v14, s92
M 25
DR 2048
v_pk_mul_f16 v23, v15, s92
v_mfma_f32_16x16x32_f16 a[88:91], v[64:67], a[16:19], a[32:35]
bc 48,3,64,56
mh 3
M 27
ad 64,576
v_mfma_f32_16x16x32_f16 a[108:111], v[56:59], a[24:27], a[32:35]
WV 19
H 0
v_pk_mul_f16 v24, v16, s92
M 37
WL 0
H 1
H 2
v_mfma_f32_16x16x32_f16 a[112:115], v[64:67], a[24:27], a[32:35]
s_barrier
R 24
M 39
R 26
v_mfma_f32_16x16x32_f16 a[132:135], v[56:59], a[0:3], a[32:35]
mw 0
v_pk_mul_f16 v25, v17, s92
v_pk_mul_f16 v26, v18, s92
M 49
ad 86,192
DR 0
v_mfma_f32_16x16x32_f16 a[136:139], v[64:67], a[0:3], a[32:35]
DR 1024
v_pk_mul_f16 v27, v19, s92
M 51
R 28
v_mfma_f32_16x16x32_f16 a[156:159], v[56:59], a[8:11], a[32:35]
DR 2048
v_pk_mul_f16 v28, v20, s92
M 61
mw 3
v_pk_mul_f16 v29, v21, s92
AN 104,106
v_mfma_f32_16x16x32_f16 a[160:163], v[64:67], a[8:11], a[32:35]
ad 86,198
DR 0
M 63
WL 4
R 30
v_mfma_f32_16x16x32_f16 a[180:183], v[56:59], a[16:19], a[32:35]
DR 1024
AN 105,107
M 73
DR 2048
SH 106
v_mfma_f32_16x16x32_f16 a[184:187], v[64:67], a[16:19], a[32:35]
bc 49,3,66,60
SH 107
M 75
WL 4
R 32
v_mfma_f32_16x16x32_f16 v[136:139], v[56:59], a[24:27], v[128:131]
mh 0
AN 106,106
AN 107,107
M 85
ad 66,576
H 0
v_mfma_f32_16x16x32_f16 v[140:143], v[64:67], a[24:27], v[128:131]
DG 1024
M 87
WL 4
R 34
v_mfma_f32_16x16x32_f16 v[160:163], v[56:59], a[0:3], v[128:131]
AN 108,110
AN 109,111
M 97
SH 110
SH 111
v_mfma_f32_16x16x32_f16 v[164:167], v[64:67], a[0:3], v[128:131]
AN 110,110
AN 111,111
PA_ 30,104,32
M 99
WL 4
PA_ 31,105,33
)"
R"(PB_ 32,104,32
PB_ 33,105,33
v_mfma_f32_16x16x32_f16 v[184:187], v[56:59], a[8:11], v[128:131]
WV 19
PA_ 34,108,36
PA_ 35,109,37
PB_ 36,108,36
M 109
WL 0
PB_ 37,109,37
PF 30,0
PF 31,1
v_mfma_f32_16x16x32_f16 v[188:191], v[64:67], a[8:11], v[128:131]
s_barrier
R 48
PF 32,2
M 111
R 50
PF 33,3
v_mfma_f32_16x16x32_f16 v[208:211], v[56:59], a[16:19], v[128:131]
mw 24
PF 34,4
PF 35,5
PF 36,6
M 121
ad 86,240
DR 0
PF 37,7
v_mfma_f32_16x16x32_f16 v[232:235], v[56:59], a[24:27], v[128:131]
DR 1024
AW 200,30
AW 201,31
M 133
DR 2048
AW 202,32
AW 203,33
v_mfma_f32_16x16x32_f16 v[212:215], v[64:67], a[16:19], v[128:131]
mw 27
AW 204,34
AW 205,35
AW 206,36
M 123
R 52
AW 207,37
PA_ 38,106,40
PA_ 39,107,41
PB_ 40,106,40
v_mfma_f32_16x16x32_f16 v[236:239], v[64:67], a[24:27], v[128:131]
ad 86,246
DR 0
PB_ 41,107,41
PA_ 42,110,44
PA_ 43,111,45
PB_ 44,110,44
M 135
WL 2
R 54
PB_ 45,111,45
PF 38,0
PF 39,1
PF 40,2
NA 36,0,200
DR 1024
PF 41,3
PF 42,4
PF 43,5
PF 44,6
PF 45,7
NA 36,4,204
DR 2048
AW 208,38
AW 209,39
AW 210,40
AW 211,41
AW 212,42
NA 60,8,200
AW 213,43
AW 214,44
AW 215,45
NA 60,12,204
TY 114,6664
TY 118,20488
TX 32,6144
WL 2
AN 112,114
AN 113,115
SH 114
SH 115
AN 114,114
AN 115,115
WL 1
AN 116,118
AN 117,119
SH 118
SH 119
AN 118,118
AN 119,119
NA 40,0,208
TX 36,19968
TY 40,6656
TY 44,20480
WL 2
PA_ 30,112,32
PA_ 31,113,33
PB_ 32,112,32
PB_ 33,113,33
PA_ 34,116,36
PA_ 35,117,37
PB_ 36,116,36
NA 40,4,212
PB_ 37,117,37
PF 30,0
PF 31,1
PF 32,2
PF 33,3
PF 34,4
PF 35,5
PF 36,6
PF 37,7
AW 216,30
NA 64,8,208
AW 217,31
AW 218,32
AW 219,33
AW 220,34
AW 221,35
AW 222,36
AW 223,37
NA 64,12,212
WL 0
PA_ 38,114,40
PA_ 39,115,41
PB_ 40,114,40
PB_ 41,115,41
PA_ 42,118,44
PA_ 43,119,45
v_mfma_f32_16x16x32_f16 a[44:47], a[216:219], a[0:3], a[32:35]
PB_ 44,118,44
PB_ 45,119,45
PF 38,0
PF 39,1
PF 40,2
PF 41,3
NA 44,4,220
PF 42,4
PF 43,5
PF 44,6
PF 45,7
AW 224,38
AW 225,39
v_mfma_f32_16x16x32_f16 a[68:71], a[216:219], a[8:11], a[32:35]
AW 226,40
AW 227,41
AW 228,42
AW 229,43
AW 230,44
AW 231,45
NA 68,12,220
RS 14,256
RS 18,320
TY 106,9736
v_mfma_f32_16x16x32_f16 a[48:51], a[224:227], a[0:3], a[32:35]
TY 110,23560
TX 58,9216
TX 62,23040
NA 48,4,228
R 56
TY 66,9728
v_mfma_f32_16x16x32_f16 a[72:75], a[224:227], a[8:11], a[32:35]
TY 70,23552
NA 72,12,228
R 58
NA 84,16,200
NA 84,20,204
WL 10
v_pk_mul_f16 v22, v14, s92
v_pk_mul_f16 v23, v15, s92
NA 88,16,208
v_pk_mul_f16 v24, v16, s92
v_pk_mul_f16 v25, v17, s92
NA 88,20,212
v_pk_mul_f16 v26, v18, s92
v_pk_mul_f16 v27, v19, s92
v_mfma_f32_16x16x32_f16 a[92:95], a[216:219], a[16:19], a[32:35]
v_pk_mul_f16 v28, v20, s92
v_pk_mul_f16 v29, v21, s92
NA 92,20,220
WL 9
AN 104,106
AN 105,107
v_mfma_f32_16x16x32_f16 a[96:99], a[224:227], a[16:19], a[32:35]
SH 106
SH 107
NA 96,20,228
AN 106,106
AN 107,107
NA 108,24,200
WL 8
AN 108,110
AN 109,111
NA 108,28,204
SH 110
SH 111
NA 112,24,208
AN 110,110
AN 111,111
NA 112,28,212
WL 3
Q 0
v_mfma_f32_16x16x32_f16 a[116:119], a[216:219], a[24:27], a[32:35]
WV 17
Q 2
NA 116,28,220
WL 0
Q 4
v_mfma_f32_16x16x32_f16 a[120:123], a[224:227], a[24:27], a[32:35]
s_barrier
R 72
TY 114,11272
NA 120,28,228
R 74
TY 118,25096
NA 132,0,200
mw 48
v_mov_b32 v54, 1
s_mov_b64 exec, 1
FS
s_mov_b64 exec, -1
NA 132,4,204
ad 86,288
DR 0
TX 74,10752
NA 136,0,208
Q 6
TX 78,24576
NA 136,4,212
DR 1024
PF 56,0
TY 82,11264
v_mfma_f32_16x16x32_f16 a[140:143], a[216:219], a[0:3], a[32:35]
PF 57,1
PF 58,2
TY 86,25088
NA 140,4,220
DR 2048
PF 59,3
TY 122,12808
v_mfma_f32_16x16x32_f16 a[144:147], a[224:227], a[0:3], a[32:35]
PF 60,4
PF 61,5
TY 126,26632
NA 144,4,228
WL 6
R 76
TX 90,12288
NA 156,8,200
mw 51
PF 62,6
PF 63,7
TX 94,26112
NA 156,12,204
ad 86,294
DR 0
TY 98,12800
NA 160,8,208
Q 8
TY 102,26624
NA 160,12,212
DR 1024
P 10
v_mfma_f32_16x16x32_f16 a[164:167], a[216:219], a[8:11], a[32:35]
Q 11
NA 164,12,220
DR 2048
P 13
v_mfma_f32_16x16x32_f16 a[168:171], a[224:227], a[8:11], a[32:35]
Q 14
NA 168,12,228
R 78
NA 180,16,200
bc 49,4,66,60
PF 64,0
NA 180,20,204
mh 9
PF 65,1
PF 66,2
NA 184,16,208
ad 66,768
H 0
NA 184,20,212
PF 67,3
PF 68,4
v_mfma_f32_16x16x32_f16 a[188:191], a[216:219], a[16:19], a[32:35]
DG 1024
NA 188,20,220
PF 69,5
PF 70,6
v_mfma_f32_16x16x32_f16 a[192:195], a[224:227], a[16:19], a[32:35]
bc 48,4,64,56
PF 71,7
NA 192,20,228
WL 2
R 80
NV 136,24,200
mh 12
AN 112,114
AN 113,115
NV 136,28,204
ad 64,768
H 0
NV 140,24,208
SH 114
SH 115
NV 140,28,212
H 1
AN 114,114
v_mfma_f32_16x16x32_f16 v[144:147], a[216:219], a[24:27], v[128:131]
AN 115,115
AN 116,118
NV 144,28,220
H 2
AN 117,119
v_mfma_f32_16x16x32_f16 v[148:151], a[224:227], a[24:27], v[128:131]
SH 118
SH 119
NV 148,28,228
R 82
NV 160,0,200
AN 118,118
AN 119,119
NV 160,4,204
Q 16
NV 164,0,208
Q 18
NV 164,4,212
Q 20
v_mfma_f32_16x16x32_f16 v[168:171], a[216:219], a[0:3], v[128:131]
Q 22
NV 168,4,220
PF 72,0
PF 73,1
v_mfma_f32_16x16x32_f16 v[172:175], a[224:227], a[0:3], v[128:131]
PF 74,2
PF 75,3
NV 172,4,228
WL 2
PF 76,4
PF 77,5
NV 184,8,200
PF 78,6
PF 79,7
NV 184,12,204
Q 24
NV 188,8,208
Q 26
NV 188,12,212
Q 28
v_mfma_f32_16x16x32_f16 v[192:195], a[216:219], a[8:11], v[128:131]
WV 20
Q 30
NV 192,12,220
WL 0
PF 80,0
PF 81,1
v_mfma_f32_16x16x32_f16 v[196:199], a[224:227], a[8:11], v[128:131]
s_barrier
R 0
NV 196,12,228
R 2
NV 208,16,200
mw 72
PF 82,2
PF 83,3
NV 208,20,204
ad 86,336
DR 0
NV 232,24,200
PF 84,4
PF 85,5
NV 232,28,204
DR 1024
PF 86,6
NV 212,16,208
PF 87,7
AN 120,122
NV 212,20,212
DR 2048
AN 121,123
NV 236,24,208
SH 122
SH 123
NV 236,28,212
mw 75
AN 122,122
AN 123,123
v_mfma_f32_16x16x32_f16 v[216:219], a[216:219], a[16:19], v[128:131]
ad 86,342
DR 0
NV 216,20,220
AN 124,126
)"
R"(AN 125,127
v_mfma_f32_16x16x32_f16 v[240:243], a[216:219], a[24:27], v[128:131]
DR 1024
SH 126
NV 240,28,220
SH 127
AN 126,126
v_mfma_f32_16x16x32_f16 v[220:223], a[224:227], a[16:19], v[128:131]
DR 2048
AN 127,127
NV 220,20,228
R 4
v_mfma_f32_16x16x32_f16 v[244:247], a[224:227], a[24:27], v[128:131]
Q 32
NV 244,28,228
WL 2
R 6
M 0
Q 34
M 1
Q 36
M 12
Q 38
M 13
PF 88,0
PF 89,1
M 2
PF 90,2
PF 91,3
M 3
PF 92,4
PF 93,5
M 14
PF 94,6
PF 95,7
M 15
Q 40
M 4
Q 42
M 5
Q 44
M 16
Q 46
M 17
PF 96,0
PF 97,1
M 6
PF 98,2
PF 99,3
M 7
PF 100,4
PF 101,5
M 18
PF 102,6
PF 103,7
M 19
RS 14,384
RS 18,448
v_mfma_f32_16x16x32_f16 a[52:55], v[88:91], a[0:3], a[32:35]
v_lshrrev_b32 v9, 8, v0
v_bfe_u32 v8, v0, 4, 4
M 9
v_lshlrev_b32 v10, 2, v9
v_mfma_f32_16x16x32_f16 a[76:79], v[88:91], a[8:11], a[32:35]
s_mov_b32 s42, 0xcb527610
M 21
s_movk_i32 s43, 0xc00
v_lshlrev_b32 v11, 3, v8
v_bfe_u32 v3, s42, v10, 4
v_mfma_f32_16x16x32_f16 a[56:59], v[96:99], a[0:3], a[32:35]
v_add_u32 v10, 16, v10
v_mad_u32_u24 v3, v3, s43, v11
M 11
R 8
v_mfma_f32_16x16x32_f16 a[80:83], v[96:99], a[8:11], a[32:35]
WL 2
v_pk_mul_f16 v22, v14, s92
v_pk_mul_f16 v23, v15, s92
M 23
R 10
M 24
v_pk_mul_f16 v24, v16, s92
v_pk_mul_f16 v25, v17, s92
M 25
v_pk_mul_f16 v26, v18, s92
v_pk_mul_f16 v27, v19, s92
M 26
v_pk_mul_f16 v28, v20, s92
v_pk_mul_f16 v29, v21, s92
M 27
v_bfe_u32 v48, s42, v10, 4
v_add_u32 v10, 1, v9
M 28
v_mad_u32_u24 v48, v48, s43, v11
v_and_b32 v10, 2, v10
M 29
v_sub_u32 v10, 1, v10
v_mul_i32_i24 v49, s43, v10
M 30
M 31
v_mfma_f32_16x16x32_f16 a[100:103], v[88:91], a[16:19], a[32:35]
M 33
v_mfma_f32_16x16x32_f16 a[104:107], v[96:99], a[16:19], a[32:35]
M 35
M 36
M 37
M 38
M 39
M 40
M 41
M 42
M 43
WV 18
v_mfma_f32_16x16x32_f16 a[124:127], v[88:91], a[24:27], a[32:35]
WL 0
M 45
s_barrier
R 24
TY 106,520
v_mfma_f32_16x16x32_f16 a[128:131], v[96:99], a[24:27], a[32:35]
TY 110,14344
TX 32,0
TX 36,13824
M 47
R 26
TY 40,512
M 48
TY 44,14336
TY 114,2056
TY 118,15880
M 49
M 50
M 51
M 52
WL 9
AN 104,106
AN 105,107
M 53
bc 49,5,66,60
SH 106
M 54
mh 18
SH 107
AN 106,106
M 55
ad 66,960
H 0
v_mfma_f32_16x16x32_f16 a[148:151], v[88:91], a[0:3], a[32:35]
WL 8
AN 107,107
AN 108,110
M 57
AN 109,111
SH 110
v_mfma_f32_16x16x32_f16 a[152:155], v[96:99], a[0:3], a[32:35]
SH 111
AN 110,110
M 59
R 28
M 60
DG 1024
M 61
WL 5
AN 111,111
PA_ 30,104,32
M 62
PA_ 31,105,33
PB_ 32,104,32
M 63
PB_ 33,105,33
PA_ 34,108,36
M 64
PA_ 35,109,37
PB_ 36,108,36
M 65
PB_ 37,109,37
PF 30,0
M 66
v_lshlrev_b32 v55, 4, v0
PF 31,1
M 67
FL
PF 32,2
v_mfma_f32_16x16x32_f16 a[172:175], v[88:91], a[8:11], a[32:35]
PF 33,3
PF 34,4
M 69
PF 35,5
PF 36,6
v_mfma_f32_16x16x32_f16 a[176:179], v[96:99], a[8:11], a[32:35]
PF 37,7
AW 200,30
M 71
R 30
M 72
AW 201,31
AW 202,32
M 73
AW 203,33
AW 204,34
M 74
AW 205,35
AW 206,36
M 75
WL 2
AW 207,37
PA_ 38,106,40
M 76
PA_ 39,107,41
PB_ 40,106,40
TX 32,1536
M 77
PB_ 41,107,41
PA_ 42,110,44
TX 36,15360
M 78
PA_ 43,111,45
PB_ 44,110,44
M 79
PB_ 45,111,45
PF 38,0
v_mfma_f32_16x16x32_f16 a[196:199], v[88:91], a[16:19], a[32:35]
PF 39,1
PF 40,2
M 81
PF 41,3
PF 42,4
v_mfma_f32_16x16x32_f16 v[132:135], v[96:99], a[16:19], v[128:131]
PF 43,5
PF 44,6
M 83
R 32
M 84
PF 45,7
AW 208,38
M 85
bc 48,5,64,56
AW 209,39
M 86
mh 21
AW 210,40
AW 211,41
M 87
ad 64,960
H 0
M 88
AW 212,42
AW 213,43
M 89
AW 214,44
AW 215,45
M 90
TY 40,2048
TY 44,15872
TY 122,3592
M 91
H 1
AN 112,114
TY 126,17416
v_mfma_f32_16x16x32_f16 v[152:155], v[88:91], a[24:27], v[128:131]
AN 113,115
SH 114
M 93
SH 115
AN 114,114
v_mfma_f32_16x16x32_f16 v[156:159], v[96:99], a[24:27], v[128:131]
AN 115,115
AN 116,118
M 95
R 34
M 96
H 2
AN 117,119
M 97
SH 118
SH 119
M 98
AN 118,118
AN 119,119
M 99
WL 6
PA_ 30,112,32
PA_ 31,113,33
M 100
PB_ 32,112,32
PB_ 33,113,33
M 101
PA_ 34,116,36
PA_ 35,117,37
M 102
PB_ 36,116,36
PB_ 37,117,37
M 103
PF 30,0
PF 31,1
v_mfma_f32_16x16x32_f16 v[176:179], v[88:91], a[0:3], v[128:131]
PF 32,2
PF 33,3
M 105
PF 34,4
PF 35,5
v_mfma_f32_16x16x32_f16 v[180:183], v[96:99], a[0:3], v[128:131]
PF 36,6
PF 37,7
M 107
AW 216,30
AW 217,31
M 108
AW 218,32
AW 219,33
M 109
AW 220,34
AW 221,35
M 110
WV 3
v_cmp_eq_u32 vcc, 0, v54
AW 222,36
M 111
WL 0
AW 223,37
PA_ 38,114,40
M 112
PA_ 39,115,41
PB_ 40,114,40
TX 32,3072
M 113
FB
PB_ 41,115,41
PA_ 42,118,44
TX 36,16896
M 114
PA_ 43,119,45
PB_ 44,118,44
M 115
PB_ 45,119,45
PF 38,0
v_mfma_f32_16x16x32_f16 v[200:203], v[88:91], a[8:11], v[128:131]
PF 39,1
PF 40,2
M 117
s_barrier
R 48
v_mfma_f32_16x16x32_f16 v[204:207], v[96:99], a[8:11], v[128:131]
mw 0
PF 41,3
PF 42,4
M 119
R 50
M 120
ad 84,384
W 0
M 121
W 1
PF 43,5
M 132
W 2
PF 44,6
M 133
mw 3
PF 45,7
AW 224,38
M 122
ad 84,390
W 0
M 123
W 1
AW 225,39
M 134
W 2
AW 226,40
M 135
mw 24
AW 227,41
AW 228,42
M 124
ad 84,432
W 0
M 125
W 1
AW 229,43
M 136
W 2
AW 230,44
M 137
mw 27
AW 231,45
AN 120,122
M 126
ad 84,438
W 0
TY 40,3584
M 127
W 1
TY 44,17408
M 138
W 2
AN 121,123
M 139
SH 122
SH 123
)"
R"(v_mfma_f32_16x16x32_f16 v[224:227], v[88:91], a[16:19], v[128:131]
AN 122,122
AN 123,123
M 129
AN 124,126
AN 125,127
v_mfma_f32_16x16x32_f16 v[248:251], v[88:91], a[24:27], v[128:131]
SH 126
SH 127
M 141
AN 126,126
AN 127,127
v_mfma_f32_16x16x32_f16 v[228:231], v[96:99], a[16:19], v[128:131]
WL 2
PA_ 30,120,32
PA_ 31,121,33
M 131
R 52
v_mfma_f32_16x16x32_f16 v[252:255], v[96:99], a[24:27], v[128:131]
PB_ 32,120,32
PB_ 33,121,33
M 143
R 54
NA 36,0,200
PA_ 34,124,36
PA_ 35,125,37
NA 36,4,204
PB_ 36,124,36
PB_ 37,125,37
NA 60,8,200
PF 30,0
PF 31,1
NA 60,12,204
PF 32,2
PF 33,3
NA 40,0,208
PF 34,4
PF 35,5
NA 40,4,212
PF 36,6
PF 37,7
NA 64,8,208
AW 232,30
AW 233,31
NA 64,12,212
AW 234,32
AW 235,33
NA 44,0,216
AW 236,34
AW 237,35
NA 44,4,220
WL 4
AW 238,36
AW 239,37
PA_ 38,122,40
NA 68,8,216
PA_ 39,123,41
PB_ 40,122,40
PB_ 41,123,41
NA 68,12,220
PA_ 42,126,44
PA_ 43,127,45
PB_ 44,126,44
NA 48,0,224
PB_ 45,127,45
PF 38,0
PF 39,1
NA 48,4,228
PF 40,2
PF 41,3
PF 42,4
NA 72,8,224
PF 43,5
PF 44,6
PF 45,7
NA 72,12,228
AW 240,38
AW 241,39
AW 242,40
NA 52,0,232
AW 243,41
AW 244,42
AW 245,43
NA 52,4,236
AW 246,44
AW 247,45
NA 76,8,232
NA 76,12,236
NA 56,0,240
NA 56,4,244
R 56
NA 80,8,240
NA 80,12,244
WL 2
R 58
NA 84,16,200
NA 84,20,204
NA 88,16,208
NA 88,20,212
NA 92,16,216
NA 92,20,220
NA 96,16,224
NA 96,20,228
NA 100,16,232
NA 100,20,236
NA 104,16,240
NA 104,20,244
NA 108,24,200
NA 108,28,204
NA 112,24,208
NA 112,28,212
NA 116,24,216
NA 116,28,220
NA 120,24,224
NA 120,28,228
NA 124,24,232
WL 0
NA 124,28,236
s_barrier
R 72
TY 106,5128
NA 128,24,240
mw 48
TY 110,18952
TX 58,4608
TX 62,18432
NA 128,28,244
R 74
TY 66,5120
NA 132,0,200
ad 84,480
W 0
TY 70,18944
NA 132,4,204
v_lshrrev_b32 v8, 1, v0
s_mov_b64 exec, s[76:77]
global_load_dwordx2 v[6:7], v8, s[78:79] sc1
s_mov_b64 exec, -1
NA 136,0,208
TY 114,6664
TY 118,20488
TX 74,6144
NA 136,4,212
WL 7
TX 78,19968
TY 82,6656
TY 86,20480
NA 140,0,216
W 1
AN 104,106
TY 122,8200
NA 140,4,220
AN 105,107
SH 106
TY 126,22024
NA 144,0,224
SH 107
AN 106,106
TX 90,7680
NA 144,4,228
AN 107,107
AN 108,110
TX 94,21504
NA 148,0,232
W 2
AN 109,111
TY 98,8192
NA 148,4,236
WL 9
SH 110
SH 111
TY 102,22016
NA 152,0,240
AN 110,110
AN 111,111
NA 152,4,244
R 76
NA 156,8,200
mw 51
Q 0
NA 156,12,204
ad 84,486
W 0
NA 160,8,208
Q 2
NA 160,12,212
Q 4
NA 164,8,216
Q 6
NA 164,12,220
W 1
P 8
NA 168,8,224
Q 9
NA 168,12,228
Q 11
NA 172,8,232
Q 13
NA 172,12,236
W 2
P 15
NA 176,8,240
AN 112,114
AN 113,115
NA 176,12,244
R 78
NA 180,16,200
SH 114
SH 115
NA 180,20,204
bc 49,6,66,60
AN 114,114
NA 184,16,208
mh 0
AN 115,115
AN 116,118
NA 184,20,212
ad 66,1152
H 0
NA 188,16,216
AN 117,119
SH 118
NA 188,20,220
SH 119
AN 118,118
NA 192,16,224
WL 2
AN 119,119
P 16
NA 192,20,228
DG 1024
NA 196,16,232
Q 17
NA 196,20,236
Q 19
NV 132,16,240
Q 21
NV 132,20,244
R 80
NV 136,24,200
bc 48,6,64,56
P 23
NV 136,28,204
mh 3
Q 24
NV 140,24,208
ad 64,1152
H 0
NV 140,28,212
Q 26
NV 144,24,216
Q 28
NV 144,28,220
Q 30
NV 148,24,224
H 1
AN 120,122
NV 148,28,228
AN 121,123
SH 122
NV 152,24,232
SH 123
AN 122,122
NV 152,28,236
AN 123,123
AN 124,126
NV 156,24,240
H 2
AN 125,127
NV 156,28,244
R 82
NV 160,0,200
SH 126
SH 127
NV 160,4,204
AN 126,126
AN 127,127
NV 164,0,208
Q 32
NV 164,4,212
Q 34
NV 168,0,216
Q 36
NV 168,4,220
Q 38
NV 172,0,224
Q 40
NV 172,4,228
Q 42
NV 176,0,232
Q 44
NV 176,4,236
Q 46
NV 180,0,240
NV 180,4,244
WL 2
NV 184,8,200
NV 184,12,204
NV 188,8,208
NV 188,12,212
NV 192,8,216
NV 192,12,220
NV 196,8,224
NV 196,12,228
WV 18
NV 200,8,232
WL 0
NV 200,12,236
s_barrier
R 0
NV 204,8,240
mw 72
NV 204,12,244
R 2
NV 208,16,200
ad 84,528
W 0
NV 208,20,204
NV 232,24,200
NV 232,28,204
NV 212,16,208
W 1
NV 212,20,212
NV 236,24,208
NV 236,28,212
NV 216,16,216
W 2
NV 216,20,220
ae 44,70,0
NV 240,24,216
s_bitcmp1_b32 s50, 0
s_cselect_b32 s51, 24576, 0
v_add_u32 v47, s51, v3
NV 240,28,220
XL 14,0
NV 220,16,224
mw 75
NV 220,20,228
ad 84,534
W 0
NV 244,24,224
XL 16,768
NV 244,28,228
NV 224,16,232
NV 224,20,236
W 1
NV 248,24,232
NV 248,28,236
NV 228,16,240
XL 18,1536
NV 228,20,244
R 4
NV 252,24,240
W 2
NV 252,28,244
WL 2
R 6
M 0
M 1
M 12
XL 20,2304
M 13
M 2
M 3
WV 20
v_or_b32 v6, v6, v7
s_mov_b64 exec, s[76:77]
v_cmp_ne_u32 %[flg], 0, v6
s_mov_b64 exec, -1
M 14
M 15
M 4
M 5
M 16
M 17
M 6
M 7
M 18
M 19
M 8
M 9
M 20
M 21
M 10
M 11
R 8
M 22
M 23
WL 4
R 10
M 24
M 25
M 26
M 27
M 28
M 29
M 30
M 31
M 32
M 33
)"
R"(M 34
M 35
WL 4
M 36
M 37
M 38
M 39
M 40
M 41
M 42
M 43
WV 15
M 44
WL 0
M 45
s_barrier
R 24
M 46
mw 0
M 47
R 26
M 48
ad 84,576
W 0
M 49
TY 106,9736
TY 110,23560
M 50
M 51
W 1
M 52
M 53
M 54
W 2
M 55
M 56
M 57
mw 3
M 58
ad 84,582
W 0
M 59
R 28
M 60
WL 3
AN 104,106
AN 105,107
M 61
W 1
SH 106
M 62
SH 107
AN 106,106
M 63
WL 2
AN 107,107
AN 108,110
M 64
W 2
AN 109,111
M 65
SH 110
SH 111
M 66
AN 110,110
AN 111,111
M 67
bc 49,7,66,60
TY 114,11272
M 68
mh 9
TY 118,25096
M 69
ad 66,1344
H 0
M 70
M 71
R 30
M 72
DG 1024
M 73
M 74
M 75
bc 48,7,64,56
TY 122,12808
M 76
mh 12
TY 126,26632
M 77
ad 64,1344
H 0
M 78
WL 5
AN 112,114
AN 113,115
M 79
SH 114
SH 115
M 80
H 1
AN 114,114
M 81
WL 4
AN 115,115
AN 116,118
M 82
AN 117,119
SH 118
M 83
R 32
M 84
H 2
SH 119
M 85
AN 118,118
AN 119,119
M 86
WL 3
AN 120,122
AN 121,123
M 87
s_load_dwordx4 s[88:91], %[pp], 0xc0
s_load_dwordx2 s[86:87], %[pp], 0xd0
SH 122
M 88
SH 123
AN 122,122
M 89
WL 2
AN 123,123
AN 124,126
M 90
AN 125,127
SH 126
M 91
SH 127
AN 126,126
M 92
AN 127,127
M 93
M 94
M 95
R 34
M 96
M 97
M 98
M 99
M 100
M 101
M 102
M 103
M 104
M 105
M 106
M 107
R 48
M 108
M 109
M 110
M 111
M 112
M 113
M 114
M 115
M 116
s_waitcnt lgkmcnt(0)
M 117
M 118
M 119
R 50
M 120
v_mov_b32 v54, v52
M 121
M 132
M 133
TX 58,9216
TX 62,23040
M 122
v_permlane32_swap_b32 v52, v54
M 123
M 134
M 135
TY 66,9728
TY 70,23552
M 124
v_add_f32 v52, v52, v54
v_mov_b32 v54, v52
M 125
M 136
ae 44,70,0
M 137
TX 74,10752
TX 78,24576
M 126
s_bfe_u32 s51, s50, 0x10001
WL 4
Q 0
M 127
Q 2
M 138
Q 4
M 139
TY 82,11264
TY 86,25088
M 128
Q 6
M 129
WL 4
Q 8
M 140
Q 10
M 141
TX 90,12288
TX 94,26112
M 130
Q 12
M 131
R 52
M 142
Q 14
M 143
TY 98,12800
TY 102,26624
M 0
R 54
M 1
WL 10
Q 16
M 12
Q 18
M 13
Q 20
M 2
Q 22
M 3
WL 8
Q 24
M 14
Q 26
M 15
Q 28
M 4
Q 30
M 5
WL 6
Q 32
M 16
Q 34
M 17
Q 36
M 6
Q 38
M 7
WL 2
Q 40
M 18
Q 42
M 19
Q 44
M 8
Q 46
M 9
v_permlane16_swap_b32 v52, v54
v_mad_i32_i24 v47, v49, s51, v48
M 20
XL 22,0
M 21
XL 24,768
M 10
XL 26,1536
v_add_f32 v52, v52, v54
M 11
R 56
M 22
XL 28,2304
M 23
R 58
M 24
v_permlane16_swap_b32 v52, v53
v_mov_b32 v54, s88
M 25
v_mov_b32 v53, s89
M 26
M 27
M 28
v_permlane16_swap_b32 v54, v53
v_bfe_u32 v53, v0, 2, 6
M 29
M 30
M 31
v_add_u32 v54, v53, v54
M 32
M 33
WV 9
M 34
WL 0
FP
M 35
M 36
M 37
M 38
M 39
M 40
UL 6,14
UH 7,14
M 41
AR 8,36
AR 9,60
M 42
AD 8,6
AD 9,7
M 43
AW 36,8
AW 60,9
M 44
UL 6,15
UH 7,15
M 45
FR
AR 8,84
M 46
AR 9,108
AD 8,6
M 47
AD 9,7
AW 84,8
M 48
AW 108,9
UL 6,16
M 49
UH 7,16
AR 8,37
M 50
AR 9,61
AD 8,6
M 51
AD 9,7
AW 37,8
M 52
AW 61,9
UL 6,17
M 53
WL 0
FQ 4
UH 7,17
M 54
AR 8,85
AR 9,109
M 55
AD 8,6
AD 9,7
M 56
FZ 4
R 72
M 57
mw 24
R 74
M 58
TY 106,520
TY 110,14344
M 59
R 76
M 60
ad 84,624
W 0
M 61
AW 85,8
AW 109,9
M 62
UL 6,18
UH 7,18
M 63
W 1
AR 8,38
M 64
AR 9,62
AD 8,6
M 65
AD 9,7
AW 38,8
M 66
W 2
AW 62,9
M 67
WL 3
AN 104,106
AN 105,107
M 68
SH 106
SH 107
M 69
mw 27
AN 106,106
AN 107,107
M 70
WL 2
AN 108,110
AN 109,111
M 71
R 78
)"
R"(M 72
SH 110
SH 111
M 73
AN 110,110
AN 111,111
M 74
TY 114,2056
TY 118,15880
M 75
ad 84,630
W 0
M 76
UL 6,19
UH 7,19
M 77
AR 8,86
AR 9,110
M 78
W 1
AD 8,6
M 79
AD 9,7
AW 86,8
M 80
AW 110,9
UL 6,20
M 81
W 2
UH 7,20
M 82
AR 8,39
AR 9,63
M 83
WL 1
R 80
M 84
mw 48
AN 112,114
AN 113,115
M 85
SH 114
SH 115
M 86
AN 114,114
AN 115,115
M 87
WL 2
AN 116,118
AN 117,119
M 88
SH 118
SH 119
M 89
AN 118,118
AN 119,119
M 90
TY 122,3592
TY 126,17416
M 91
ad 84,672
M 92
W 0
AD 8,6
M 93
AD 9,7
AW 39,8
M 94
AW 63,9
UL 6,21
M 95
R 82
M 96
W 1
UH 7,21
M 97
AR 8,87
AR 9,111
M 98
AD 8,6
AD 9,7
M 99
WL 3
AN 120,122
AN 121,123
M 100
SH 122
SH 123
M 101
AN 122,122
AN 123,123
M 102
WL 2
AN 124,126
AN 125,127
M 103
SH 126
SH 127
M 104
AN 126,126
AN 127,127
M 105
W 2
AW 87,8
M 106
AW 111,9
M 107
R 0
M 108
mw 51
M 109
ad 84,678
W 0
M 110
M 111
M 112
W 1
M 113
M 114
M 115
W 2
M 116
M 117
M 118
bc 49,8,66,60
M 119
WL 2
R 2
M 120
mh 18
s_bitcmp1_b32 s87, 0
s_cselect_b32 exec_lo, 0xffffff, 0
s_mov_b32 exec_hi, 0
global_atomic_add_f32 v54, v52, s[90:91]
s_mov_b64 exec, -1
M 121
ad 66,1536
H 0
M 132
M 133
TX 58,0
TX 62,13824
M 122
DG 1024
M 123
M 134
M 135
TY 66,512
TY 70,14336
M 124
bc 48,8,64,56
M 125
mh 21
M 136
ad 64,1536
H 0
M 137
TX 74,1536
TX 78,15360
M 126
WL 4
Q 0
M 127
Q 2
M 138
Q 4
M 139
TY 82,2048
TY 86,15872
M 128
Q 6
M 129
WL 4
Q 8
M 140
Q 10
M 141
TX 90,3072
TX 94,16896
M 130
Q 12
M 131
R 4
M 142
Q 14
M 143
TY 98,3584
TY 102,17408
M 0
R 6
M 1
WL 10
Q 16
M 12
Q 18
M 13
Q 20
M 2
Q 22
M 3
WL 8
Q 24
M 14
Q 26
M 15
Q 28
M 4
Q 30
M 5
WL 6
Q 32
M 16
Q 34
M 17
Q 36
M 6
Q 38
M 7
WL 2
Q 40
M 18
Q 42
M 19
Q 44
M 8
Q 46
M 9
H 1
M 20
ae 44,68,0
M 21
s_bitcmp1_b32 s50, 2
s_cselect_b32 s51, 24576, 0
v_add_u32 v47, s51, v3
M 10
H 2
XL 30,0
M 11
R 8
M 22
XL 32,768
M 23
R 10
M 24
XL 34,1536
M 25
XL 36,2304
M 26
M 27
M 28
M 29
M 30
M 31
M 32
M 33
WV 10
M 34
WL 0
FP
M 35
M 36
M 37
s_bitcmp1_b32 s87, 1
s_cselect_b32 exec_lo, 0xffffff, 0
s_mov_b32 exec_hi, 0
global_atomic_add_f32 v54, v52, s[90:91]
s_mov_b64 exec, -1
M 38
M 39
M 40
M 41
M 42
UL 6,22
UH 7,22
M 43
AR 8,40
AR 9,64
M 44
AD 8,6
AD 9,7
M 45
FR
AW 40,8
M 46
AW 64,9
UL 6,23
M 47
UH 7,23
AR 8,88
M 48
AR 9,112
AD 8,6
M 49
AD 9,7
AW 88,8
M 50
AW 112,9
UL 6,24
M 51
UH 7,24
AR 8,41
M 52
AR 9,65
AD 8,6
M 53
WL 0
FQ 8
AD 9,7
M 54
AW 41,8
AW 65,9
M 55
UL 6,25
UH 7,25
M 56
FZ 8
R 24
M 57
mw 72
R 26
M 58
TY 106,5128
TY 110,18952
M 59
R 28
M 60
ad 84,720
W 0
M 61
AR 8,89
AR 9,113
M 62
AD 8,6
AD 9,7
M 63
W 1
AW 89,8
M 64
AW 113,9
UL 6,26
M 65
UH 7,26
AR 8,42
M 66
W 2
AR 9,66
M 67
WL 3
AN 104,106
AN 105,107
M 68
SH 106
SH 107
M 69
mw 75
AN 106,106
AN 107,107
M 70
WL 2
AN 108,110
AN 109,111
M 71
R 30
M 72
SH 110
SH 111
M 73
AN 110,110
AN 111,111
M 74
TY 114,6664
TY 118,20488
M 75
ad 84,726
W 0
M 76
AD 8,6
AD 9,7
M 77
AW 42,8
AW 66,9
M 78
W 1
UL 6,27
M 79
UH 7,27
AR 8,90
M 80
AR 9,114
AD 8,6
M 81
W 2
AD 9,7
M 82
AW 90,8
AW 114,9
M 83
WL 1
R 32
M 84
)"
R"(mw 0
AN 112,114
AN 113,115
M 85
SH 114
SH 115
M 86
AN 114,114
AN 115,115
M 87
WL 2
AN 116,118
AN 117,119
M 88
SH 118
SH 119
M 89
AN 118,118
AN 119,119
M 90
TY 122,8200
TY 126,22024
M 91
ad 84,768
M 92
W 0
UL 6,28
M 93
UH 7,28
AR 8,43
M 94
AR 9,67
AD 8,6
M 95
R 34
M 96
W 1
AD 9,7
M 97
AW 43,8
AW 67,9
M 98
UL 6,29
UH 7,29
M 99
WL 3
AN 120,122
AN 121,123
M 100
SH 122
SH 123
M 101
AN 122,122
AN 123,123
M 102
WL 2
AN 124,126
AN 125,127
M 103
SH 126
SH 127
M 104
AN 126,126
AN 127,127
M 105
W 2
AR 8,91
M 106
AR 9,115
AD 8,6
M 107
R 48
M 108
mw 3
AD 9,7
AW 91,8
M 109
ad 84,774
W 0
M 110
AW 115,9
M 111
M 112
W 1
M 113
M 114
M 115
W 2
M 116
M 117
M 118
bc 49,9,66,60
M 119
WL 2
R 50
M 120
mh 0
s_bitcmp1_b32 s87, 2
s_cselect_b32 exec_lo, 0xffffff, 0
s_mov_b32 exec_hi, 0
global_atomic_add_f32 v54, v52, s[90:91]
s_mov_b64 exec, -1
M 121
ad 66,1728
H 0
M 132
M 133
TX 58,4608
TX 62,18432
M 122
DG 1024
M 123
M 134
M 135
TY 66,5120
TY 70,18944
M 124
bc 48,9,64,56
M 125
mh 3
M 136
ad 64,1728
H 0
M 137
TX 74,6144
TX 78,19968
M 126
WL 4
Q 0
M 127
Q 2
M 138
Q 4
M 139
TY 82,6656
TY 86,20480
M 128
Q 6
M 129
WL 4
Q 8
M 140
Q 10
M 141
TX 90,7680
TX 94,21504
M 130
Q 12
M 131
R 52
M 142
Q 14
M 143
TY 98,8192
TY 102,22016
M 0
R 54
M 1
WL 10
Q 16
M 12
Q 18
M 13
Q 20
M 2
Q 22
M 3
WL 8
Q 24
M 14
Q 26
M 15
Q 28
M 4
Q 30
M 5
WL 6
Q 32
M 16
Q 34
M 17
Q 36
M 6
Q 38
M 7
WL 2
Q 40
M 18
Q 42
M 19
Q 44
M 8
Q 46
M 9
H 1
M 20
ae 44,68,0
M 21
s_bfe_u32 s51, s50, 0x10003
v_mad_i32_i24 v47, v49, s51, v48
XL 38,0
M 10
H 2
XL 40,768
M 11
R 56
M 22
XL 42,1536
M 23
R 58
M 24
XL 44,2304
M 25
M 26
M 27
M 28
M 29
M 30
M 31
M 32
M 33
WV 10
M 34
WL 0
FP
M 35
M 36
M 37
s_bitcmp1_b32 s87, 3
s_cselect_b32 exec_lo, 0xffffff, 0
s_mov_b32 exec_hi, 0
global_atomic_add_f32 v54, v52, s[90:91]
s_mov_b64 exec, -1
M 38
M 39
M 40
M 41
M 42
M 43
M 44
UL 6,30
UH 7,30
M 45
FR
AR 8,44
M 46
AR 9,68
AD 8,6
M 47
AD 9,7
AW 44,8
M 48
AW 68,9
UL 6,31
M 49
UH 7,31
AR 8,92
M 50
AR 9,116
AD 8,6
M 51
AD 9,7
AW 92,8
M 52
AW 116,9
UL 6,32
M 53
WL 0
FQ 12
UH 7,32
M 54
AR 8,45
AR 9,69
M 55
AD 8,6
AD 9,7
M 56
FZ 12
R 72
M 57
mw 24
R 74
M 58
TY 106,9736
TY 110,23560
M 59
R 76
M 60
ad 84,816
W 0
M 61
AW 45,8
AW 69,9
M 62
UL 6,33
UH 7,33
M 63
W 1
AR 8,93
M 64
AR 9,117
AD 8,6
M 65
AD 9,7
AW 93,8
M 66
W 2
AW 117,9
M 67
WL 3
AN 104,106
AN 105,107
M 68
SH 106
SH 107
M 69
mw 27
AN 106,106
AN 107,107
M 70
WL 2
AN 108,110
AN 109,111
M 71
R 78
M 72
SH 110
SH 111
M 73
AN 110,110
AN 111,111
M 74
TY 114,11272
TY 118,25096
M 75
ad 84,822
W 0
M 76
UL 6,34
UH 7,34
M 77
AR 8,46
AR 9,70
M 78
W 1
AD 8,6
M 79
AD 9,7
AW 46,8
M 80
AW 70,9
UL 6,35
M 81
W 2
UH 7,35
M 82
AR 8,94
AR 9,118
M 83
WL 1
R 80
M 84
mw 48
AN 112,114
AN 113,115
M 85
SH 114
SH 115
M 86
AN 114,114
AN 115,115
M 87
WL 2
AN 116,118
AN 117,119
M 88
SH 118
SH 119
M 89
AN 118,118
AN 119,119
M 90
TY 122,12808
TY 126,26632
M 91
ad 84,864
M 92
W 0
AD 8,6
M 93
AD 9,7
AW 94,8
M 94
AW 118,9
UL 6,36
M 95
R 82
M 96
W 1
UH 7,36
)"
R"(M 97
AR 8,47
AR 9,71
M 98
AD 8,6
AD 9,7
M 99
WL 3
AN 120,122
AN 121,123
M 100
SH 122
SH 123
M 101
AN 122,122
AN 123,123
M 102
WL 2
AN 124,126
AN 125,127
M 103
SH 126
SH 127
M 104
AN 126,126
AN 127,127
M 105
W 2
AW 47,8
M 106
AW 71,9
UL 6,37
M 107
R 0
M 108
mw 51
UH 7,37
AR 8,95
M 109
ad 84,870
W 0
M 110
AR 9,119
AD 8,6
M 111
AD 9,7
AW 95,8
M 112
W 1
AW 119,9
M 113
M 114
M 115
W 2
M 116
M 117
M 118
bc 49,10,66,60
M 119
WL 2
R 2
M 120
mh 9
s_bitcmp1_b32 s87, 4
s_cselect_b32 exec_lo, 0xffffff, 0
s_mov_b32 exec_hi, 0
global_atomic_add_f32 v54, v52, s[90:91]
s_mov_b64 exec, -1
M 121
ad 66,1920
H 0
M 132
M 133
TX 58,9216
TX 62,23040
M 122
DG 1024
M 123
M 134
M 135
TY 66,9728
TY 70,23552
M 124
bc 48,10,64,56
M 125
mh 12
M 136
ad 64,1920
H 0
M 137
TX 74,10752
TX 78,24576
M 126
WL 4
Q 0
M 127
Q 2
M 138
Q 4
M 139
TY 82,11264
TY 86,25088
M 128
Q 6
M 129
WL 4
Q 8
M 140
Q 10
M 141
TX 90,12288
TX 94,26112
M 130
Q 12
M 131
R 4
M 142
Q 14
M 143
TY 98,12800
TY 102,26624
M 0
R 6
M 1
WL 10
Q 16
M 12
Q 18
M 13
Q 20
M 2
Q 22
M 3
WL 8
Q 24
M 14
Q 26
M 15
Q 28
M 4
Q 30
M 5
WL 6
Q 32
M 16
Q 34
M 17
Q 36
M 6
Q 38
M 7
WL 2
Q 40
M 18
Q 42
M 19
Q 44
M 8
Q 46
M 9
H 1
M 20
ae 44,68,96
M 21
s_bitcmp1_b32 s50, 4
s_cselect_b32 s51, 24576, 0
v_add_u32 v47, s51, v3
M 10
H 2
XL 14,0
M 11
R 8
M 22
XL 16,768
M 23
R 10
PA 12288
DF
M 24
XL 18,1536
M 25
XL 20,2304
M 26
M 27
M 28
M 29
M 30
M 31
M 32
M 33
WV 11
M 34
WL 0
FP
M 35
M 36
M 37
s_bitcmp1_b32 s87, 5
s_cselect_b32 exec_lo, 0xffffff, 0
s_mov_b32 exec_hi, 0
global_atomic_add_f32 v54, v52, s[90:91]
s_mov_b64 exec, -1
M 38
M 39
M 40
M 41
M 42
M 43
M 44
M 45
FR
M 46
UL 6,38
UH 7,38
M 47
AR 8,48
AR 9,72
M 48
AD 8,6
AD 9,7
M 49
AW 48,8
AW 72,9
M 50
UL 6,39
UH 7,39
M 51
AR 8,96
AR 9,120
M 52
AD 8,6
AD 9,7
M 53
WL 0
FQ 16
AW 96,8
M 54
AW 120,9
UL 6,40
M 55
UH 7,40
AR 8,49
M 56
FZ 16
R 24
M 57
mw 72
R 26
M 58
TY 106,520
TY 110,14344
M 59
R 28
M 60
ad 84,912
W 0
M 61
AR 9,73
AD 8,6
M 62
AD 9,7
AW 49,8
M 63
W 1
AW 73,9
M 64
UL 6,41
UH 7,41
M 65
AR 8,97
AR 9,121
M 66
W 2
AD 8,6
M 67
WL 3
AN 104,106
AN 105,107
M 68
SH 106
SH 107
M 69
mw 75
AN 106,106
AN 107,107
M 70
WL 2
AN 108,110
AN 109,111
M 71
R 30
M 72
SH 110
SH 111
M 73
AN 110,110
AN 111,111
M 74
TY 114,2056
TY 118,15880
M 75
ad 84,918
W 0
M 76
AD 9,7
AW 97,8
M 77
AW 121,9
UL 6,42
M 78
W 1
UH 7,42
M 79
AR 8,50
AR 9,74
M 80
AD 8,6
AD 9,7
M 81
W 2
AW 50,8
M 82
AW 74,9
UL 6,43
M 83
WL 1
R 32
M 84
mw 0
AN 112,114
AN 113,115
M 85
SH 114
SH 115
M 86
AN 114,114
AN 115,115
M 87
WL 2
AN 116,118
AN 117,119
M 88
SH 118
SH 119
M 89
AN 118,118
AN 119,119
M 90
TY 122,3592
TY 126,17416
M 91
ad 84,960
M 92
W 0
UH 7,43
M 93
AR 8,98
AR 9,122
M 94
AD 8,6
AD 9,7
M 95
R 34
M 96
W 1
AW 98,8
M 97
AW 122,9
UL 6,44
M 98
UH 7,44
AR 8,51
M 99
WL 3
AN 120,122
AN 121,123
M 100
SH 122
SH 123
M 101
AN 122,122
AN 123,123
M 102
WL 2
AN 124,126
AN 125,127
M 103
SH 126
SH 127
M 104
AN 126,126
AN 127,127
M 105
W 2
AR 9,75
M 106
AD 8,6
AD 9,7
M 107
R 48
)"
R"(M 108
mw 3
AW 51,8
AW 75,9
M 109
ad 84,966
W 0
M 110
UL 6,45
UH 7,45
M 111
AR 8,99
AR 9,123
M 112
W 1
AD 8,6
M 113
AD 9,7
AW 99,8
M 114
AW 123,9
M 115
W 2
M 116
M 117
M 118
bc 49,11,66,60
M 119
WL 2
R 50
M 120
mh 18
s_bitcmp1_b32 s87, 6
s_cselect_b32 exec_lo, 0xffffff, 0
s_mov_b32 exec_hi, 0
global_atomic_add_f32 v54, v52, s[90:91]
s_mov_b64 exec, -1
M 121
ad 66,2112
H 0
M 132
M 133
TX 58,0
TX 62,13824
M 122
DG 1024
M 123
M 134
M 135
TY 66,512
TY 70,14336
M 124
bc 48,11,64,56
M 125
mh 21
M 136
ad 64,2112
H 0
M 137
TX 74,1536
TX 78,15360
M 126
WL 4
Q 0
M 127
Q 2
M 138
Q 4
M 139
TY 82,2048
TY 86,15872
M 128
Q 6
M 129
WL 4
Q 8
M 140
Q 10
M 141
TX 90,3072
TX 94,16896
M 130
Q 12
M 131
R 52
M 142
Q 14
M 143
TY 98,3584
TY 102,17408
M 0
R 54
M 1
WL 10
Q 16
M 12
Q 18
M 13
Q 20
M 2
Q 22
M 3
WL 8
Q 24
M 14
Q 26
M 15
Q 28
M 4
Q 30
M 5
WL 6
Q 32
M 16
Q 34
M 17
Q 36
M 6
Q 38
M 7
WL 2
Q 40
M 18
Q 42
M 19
Q 44
M 8
Q 46
M 9
H 1
M 20
ae 44,68,96
M 21
s_bfe_u32 s51, s50, 0x10005
v_mad_i32_i24 v47, v49, s51, v48
XL 22,0
M 10
H 2
XL 24,768
M 11
R 56
M 22
XL 26,1536
M 23
R 58
M 24
XL 28,2304
M 25
M 26
M 27
M 28
M 29
M 30
M 31
M 32
M 33
WV 10
M 34
WL 0
FP
M 35
M 36
M 37
s_bitcmp1_b32 s87, 7
s_cselect_b32 exec_lo, 0xffffff, 0
s_mov_b32 exec_hi, 0
global_atomic_add_f32 v54, v52, s[90:91]
s_mov_b64 exec, -1
M 38
M 39
M 40
M 41
M 42
M 43
M 44
M 45
FR
M 46
M 47
M 48
UL 6,14
UH 7,14
M 49
AR 8,52
AR 9,76
M 50
AD 8,6
AD 9,7
M 51
AW 52,8
AW 76,9
M 52
UL 6,15
UH 7,15
M 53
WL 0
FQ 20
AR 8,100
M 54
AR 9,124
AD 8,6
M 55
AD 9,7
AW 100,8
M 56
FZ 20
R 72
M 57
mw 24
R 74
M 58
TY 106,5128
TY 110,18952
M 59
R 76
M 60
ad 84,1008
W 0
M 61
AW 124,9
UL 6,16
M 62
UH 7,16
AR 8,53
M 63
W 1
AR 9,77
M 64
AD 8,6
AD 9,7
M 65
AW 53,8
AW 77,9
M 66
W 2
UL 6,17
M 67
WL 3
AN 104,106
AN 105,107
M 68
SH 106
SH 107
M 69
mw 27
AN 106,106
AN 107,107
M 70
WL 2
AN 108,110
AN 109,111
M 71
R 78
M 72
SH 110
SH 111
M 73
AN 110,110
AN 111,111
M 74
TY 114,6664
TY 118,20488
M 75
ad 84,1014
W 0
M 76
UH 7,17
AR 8,101
M 77
AR 9,125
AD 8,6
M 78
W 1
AD 9,7
M 79
AW 101,8
AW 125,9
M 80
UL 6,18
UH 7,18
M 81
W 2
AR 8,54
M 82
AR 9,78
AD 8,6
M 83
WL 1
R 80
M 84
mw 48
AN 112,114
AN 113,115
M 85
SH 114
SH 115
M 86
AN 114,114
AN 115,115
M 87
WL 2
AN 116,118
AN 117,119
M 88
SH 118
SH 119
M 89
AN 118,118
AN 119,119
M 90
TY 122,8200
TY 126,22024
M 91
ad 84,1056
M 92
W 0
AD 9,7
M 93
AW 54,8
AW 78,9
M 94
UL 6,19
UH 7,19
M 95
R 82
M 96
W 1
AR 8,102
M 97
AR 9,126
AD 8,6
M 98
AD 9,7
AW 102,8
M 99
WL 3
AN 120,122
AN 121,123
M 100
SH 122
SH 123
M 101
AN 122,122
AN 123,123
M 102
WL 2
AN 124,126
AN 125,127
M 103
SH 126
SH 127
M 104
AN 126,126
AN 127,127
M 105
W 2
AW 126,9
M 106
UL 6,20
UH 7,20
M 107
R 0
M 108
mw 51
AR 8,55
AR 9,79
M 109
ad 84,1062
W 0
M 110
AD 8,6
AD 9,7
M 111
AW 55,8
AW 79,9
M 112
W 1
UL 6,21
M 113
UH 7,21
AR 8,103
M 114
AR 9,127
AD 8,6
M 115
W 2
AD 9,7
M 116
AW 103,8
AW 127,9
M 117
M 118
bc 49,12,66,60
M 119
WL 2
R 2
M 120
mh 0
M 121
ad 66,2304
)"
R"(H 0
M 132
M 133
TX 58,4608
TX 62,18432
M 122
DG 1024
M 123
M 134
M 135
TY 66,5120
TY 70,18944
M 124
bc 48,12,64,56
M 125
mh 3
M 136
ad 64,2304
H 0
M 137
TX 74,6144
TX 78,19968
M 126
WL 4
Q 0
M 127
Q 2
M 138
Q 4
M 139
TY 82,6656
TY 86,20480
M 128
Q 6
M 129
WL 4
Q 8
M 140
Q 10
M 141
TX 90,7680
TX 94,21504
M 130
Q 12
M 131
R 4
M 142
Q 14
M 143
TY 98,8192
TY 102,22016
M 0
R 6
M 1
WL 10
Q 16
M 12
Q 18
M 13
Q 20
M 2
Q 22
M 3
WL 8
Q 24
M 14
Q 26
M 15
Q 28
M 4
Q 30
M 5
WL 6
Q 32
M 16
Q 34
M 17
Q 36
M 6
Q 38
M 7
WL 2
Q 40
M 18
Q 42
M 19
Q 44
M 8
Q 46
M 9
H 1
M 20
M 21
M 10
H 2
M 11
R 8
M 22
M 23
R 10
PA 12288
DF
M 24
M 25
M 26
M 27
M 28
M 29
M 30
M 31
M 32
ae 44,70,0
M 33
WV 6
M 34
WL 0
FP
M 35
M 36
s_bitcmp1_b32 s50, 0
s_cselect_b32 s51, 24576, 0
v_add_u32 v47, s51, v3
M 37
XL 30,128
M 38
XL 32,896
M 39
M 40
M 41
M 42
M 43
M 44
XL 34,1664
M 45
FR
M 46
M 47
M 48
M 49
M 50
XL 36,2432
UL 6,22
M 51
UH 7,22
AR 8,56
M 52
AR 9,80
AD 8,6
M 53
WL 0
FQ 24
AD 9,7
M 54
AW 56,8
AW 80,9
M 55
UL 6,23
UH 7,23
M 56
FZ 24
R 24
M 57
mw 72
R 26
M 58
TY 106,9736
TY 110,23560
M 59
R 28
M 60
ad 84,1104
W 0
M 61
AR 8,104
AR 9,128
M 62
AD 8,6
AD 9,7
M 63
W 1
AW 104,8
M 64
AW 128,9
UL 6,24
M 65
UH 7,24
AR 8,57
M 66
W 2
AR 9,81
M 67
WL 3
AN 104,106
AN 105,107
M 68
SH 106
SH 107
M 69
mw 75
AN 106,106
AN 107,107
M 70
WL 2
AN 108,110
AN 109,111
M 71
R 30
M 72
SH 110
SH 111
M 73
AN 110,110
AN 111,111
M 74
TY 114,11272
TY 118,25096
M 75
ad 84,1110
W 0
M 76
AD 8,6
AD 9,7
M 77
AW 57,8
AW 81,9
M 78
W 1
UL 6,25
M 79
UH 7,25
AR 8,105
M 80
AR 9,129
AD 8,6
M 81
W 2
AD 9,7
M 82
AW 105,8
AW 129,9
M 83
WL 1
R 32
M 84
mw 0
AN 112,114
AN 113,115
M 85
SH 114
SH 115
M 86
AN 114,114
AN 115,115
M 87
WL 2
AN 116,118
AN 117,119
M 88
SH 118
SH 119
M 89
AN 118,118
AN 119,119
M 90
TY 122,12808
TY 126,26632
M 91
ad 84,1152
M 92
W 0
UL 6,26
M 93
UH 7,26
AR 8,58
M 94
AR 9,82
AD 8,6
M 95
R 34
M 96
W 1
AD 9,7
M 97
AW 58,8
AW 82,9
M 98
UL 6,27
UH 7,27
M 99
WL 3
AN 120,122
AN 121,123
M 100
SH 122
SH 123
M 101
AN 122,122
AN 123,123
M 102
WL 2
AN 124,126
AN 125,127
M 103
SH 126
SH 127
M 104
AN 126,126
AN 127,127
M 105
W 2
AR 8,106
M 106
AR 9,130
AD 8,6
M 107
R 48
M 108
mw 3
AD 9,7
AW 106,8
M 109
ad 84,1158
W 0
M 110
AW 130,9
UL 6,28
M 111
UH 7,28
AR 8,59
M 112
W 1
AR 9,83
M 113
AD 8,6
AD 9,7
M 114
AW 59,8
AW 83,9
M 115
W 2
UL 6,29
M 116
UH 7,29
AR 8,107
M 117
AR 9,131
AD 8,6
M 118
bc 49,13,66,60
AD 9,7
M 119
WL 0
R 50
M 120
mh 9
AW 107,8
AW 131,9
M 121
ad 66,2496
H 0
M 132
M 133
TX 58,9216
TX 62,23040
M 122
DG 1024
M 123
M 134
M 135
TY 66,9728
TY 70,23552
M 124
bc 48,13,64,56
M 125
mh 12
M 136
ad 64,2496
H 0
M 137
TX 74,10752
TX 78,24576
M 126
WL 4
Q 0
M 127
Q 2
M 138
Q 4
M 139
TY 82,11264
TY 86,25088
M 128
Q 6
M 129
WL 4
Q 8
M 140
Q 10
M 141
TX 90,12288
)"
R"(TX 94,26112
M 130
Q 12
M 131
R 52
M 142
Q 14
M 143
TY 98,12800
TY 102,26624
M 0
R 54
M 1
WL 10
Q 16
M 12
Q 18
M 13
Q 20
M 2
Q 22
M 3
WL 8
Q 24
M 14
Q 26
M 15
Q 28
M 4
Q 30
M 5
WL 6
Q 32
M 16
Q 34
M 17
Q 36
M 6
Q 38
M 7
WL 2
Q 40
M 18
Q 42
M 19
Q 44
M 8
Q 46
M 9
H 1
M 20
M 21
M 10
H 2
M 11
R 56
M 22
M 23
R 58
M 24
M 25
M 26
M 27
M 28
M 29
M 30
M 31
M 32
M 33
WV 5
M 34
WL 0
FP
M 35
M 36
ae 44,70,0
M 37
s_bfe_u32 s51, s50, 0x10001
v_mad_i32_i24 v47, v49, s51, v48
XL 38,128
M 38
M 39
M 40
XL 40,896
M 41
M 42
M 43
M 44
M 45
FR
M 46
XL 42,1664
M 47
M 48
M 49
M 50
M 51
M 52
XL 44,2432
M 53
WL 0
FQ 28
M 54
M 55
M 56
FZ 28
R 72
M 57
R 74
M 58
TY 106,520
TY 110,14344
M 59
R 76
M 60
mw 24
v_mov_b32 v54, 2
s_mov_b64 exec, 1
FS
s_mov_b64 exec, -1
M 61
ad 84,1200
W 0
M 62
M 63
M 64
W 1
M 65
M 66
M 67
WL 3
AN 104,106
AN 105,107
M 68
SH 106
SH 107
M 69
AN 106,106
AN 107,107
M 70
WL 2
AN 108,110
AN 109,111
M 71
R 78
M 72
SH 110
SH 111
M 73
AN 110,110
AN 111,111
M 74
TY 114,2056
TY 118,15880
M 75
W 2
M 76
M 77
M 78
mw 27
M 79
ad 84,1206
W 0
M 80
M 81
M 82
W 1
M 83
R 80
M 84
WL 3
AN 112,114
AN 113,115
M 85
SH 114
SH 115
M 86
AN 114,114
AN 115,115
M 87
WL 2
AN 116,118
AN 117,119
M 88
SH 118
SH 119
M 89
AN 118,118
AN 119,119
M 90
TY 122,3592
TY 126,17416
M 91
W 2
UL 6,30
M 92
UH 7,30
AR 8,132
M 93
AR 9,156
AD 8,6
M 94
mw 48
AD 9,7
AW 132,8
M 95
R 82
M 96
ad 84,1248
W 0
M 97
AW 156,9
UL 6,31
M 98
UH 7,31
AR 8,180
M 99
WL 3
AN 120,122
AN 121,123
M 100
SH 122
SH 123
M 101
AN 122,122
AN 123,123
M 102
WL 2
AN 124,126
AN 125,127
M 103
SH 126
SH 127
M 104
AN 126,126
AN 127,127
M 105
W 1
AD 8,6
M 106
AD 136,7
AW 180,8
M 107
R 0
M 108
W 2
UL 6,32
M 109
UH 7,32
AR 8,133
M 110
AR 9,157
AD 8,6
M 111
mw 51
AD 9,7
AW 133,8
M 112
ad 84,1254
W 0
M 113
AW 157,9
UL 6,33
M 114
UH 7,33
AR 8,181
M 115
W 1
AD 8,6
M 116
AD 137,7
AW 181,8
M 117
UL 6,34
UH 7,34
M 118
W 2
AR 8,134
M 119
WL 0
R 2
M 120
AR 9,158
AD 8,6
M 121
bc 49,14,66,60
AD 9,7
M 132
mh 18
AW 134,8
AW 158,9
M 133
TX 58,0
TX 62,13824
M 122
ad 66,2688
H 0
M 123
UL 6,35
UH 7,35
M 134
AR 8,182
AD 8,6
M 135
TY 66,512
TY 70,14336
M 124
DG 1024
M 125
AD 138,7
AW 182,8
M 136
UL 6,36
UH 7,36
M 137
TX 74,1536
TX 78,15360
M 126
WL 4
Q 0
M 127
Q 2
M 138
Q 4
M 139
TY 82,2048
TY 86,15872
M 128
Q 6
M 129
WL 4
Q 8
M 140
Q 10
M 141
TX 90,3072
TX 94,16896
M 130
Q 12
M 131
R 4
M 142
Q 14
M 143
TY 98,3584
TY 102,17408
M 0
R 6
M 1
WL 10
Q 16
M 12
Q 18
M 13
Q 20
M 2
Q 22
M 3
WL 8
Q 24
M 14
Q 26
M 15
Q 28
M 4
Q 30
M 5
WL 6
Q 32
M 16
Q 34
M 17
Q 36
M 6
Q 38
M 7
WL 2
Q 40
M 18
Q 42
M 19
Q 44
M 8
Q 46
M 9
bc 48,14,64,56
M 20
mh 21
AR 8,135
AR 9,159
M 21
ad 64,2688
H 0
M 10
AD 8,6
AD 9,7
M 11
R 8
M 22
H 1
)"
R"(AW 135,8
M 23
R 10
M 24
AW 159,9
UL 6,37
M 25
H 2
UH 7,37
M 26
AR 8,183
AD 8,6
M 27
AD 139,7
AW 183,8
M 28
PA 12288
DF
M 29
M 30
M 31
M 32
M 33
WV 6
M 34
WL 0
FP
M 35
M 36
ae 44,68,0
M 37
s_bitcmp1_b32 s50, 2
s_cselect_b32 s51, 24576, 0
v_add_u32 v47, s51, v3
M 38
XL 14,128
M 39
M 40
M 41
M 42
XL 16,896
M 43
M 44
M 45
FR
M 46
M 47
M 48
XL 18,1664
M 49
M 50
M 51
M 52
M 53
WL 0
FQ 32
M 54
XL 20,2432
M 55
M 56
FZ 32
R 24
M 57
mw 72
R 26
M 58
TY 106,5128
TY 110,18952
M 59
R 28
M 60
ad 84,1296
W 0
M 61
M 62
M 63
W 1
M 64
M 65
M 66
W 2
M 67
WL 3
AN 104,106
AN 105,107
M 68
SH 106
SH 107
M 69
mw 75
AN 106,106
AN 107,107
M 70
WL 2
AN 108,110
AN 109,111
M 71
R 30
M 72
SH 110
SH 111
M 73
AN 110,110
AN 111,111
M 74
TY 114,6664
TY 118,20488
M 75
ad 84,1302
W 0
M 76
M 77
M 78
W 1
M 79
M 80
M 81
W 2
M 82
M 83
R 32
M 84
mw 0
WL 3
AN 112,114
AN 113,115
M 85
SH 114
SH 115
M 86
AN 114,114
AN 115,115
M 87
WL 2
AN 116,118
AN 117,119
M 88
SH 118
SH 119
M 89
AN 118,118
AN 119,119
M 90
TY 122,8200
TY 126,22024
M 91
ad 84,1344
M 92
W 0
UL 6,38
M 93
UH 7,38
AR 8,136
M 94
AR 9,160
AD 8,6
M 95
R 34
M 96
W 1
AD 9,7
M 97
AW 136,8
AW 160,9
M 98
UL 6,39
UH 7,39
M 99
WL 3
AN 120,122
AN 121,123
M 100
SH 122
SH 123
M 101
AN 122,122
AN 123,123
M 102
WL 2
AN 124,126
AN 125,127
M 103
SH 126
SH 127
M 104
AN 126,126
AN 127,127
M 105
W 2
AR 8,184
M 106
AD 8,6
AD 140,7
M 107
R 48
M 108
mw 3
AW 184,8
UL 6,40
M 109
ad 84,1350
W 0
M 110
UH 7,40
AR 8,137
M 111
AR 9,161
AD 8,6
M 112
W 1
AD 9,7
M 113
AW 137,8
AW 161,9
M 114
UL 6,41
UH 7,41
M 115
W 2
AR 8,185
M 116
AD 8,6
AD 141,7
M 117
AW 185,8
UL 6,42
M 118
bc 49,15,66,60
UH 7,42
M 119
WL 0
R 50
M 120
mh 0
AR 8,138
AR 9,162
M 121
ad 66,2880
H 0
M 132
AD 8,6
AD 9,7
M 133
TX 58,4608
TX 62,18432
M 122
DG 1024
M 123
AW 138,8
AW 162,9
M 134
UL 6,43
UH 7,43
M 135
TY 66,5120
TY 70,18944
M 124
bc 48,15,64,56
AR 8,186
M 125
mh 3
AD 8,6
AD 142,7
M 136
ad 64,2880
H 0
M 137
TX 74,6144
TX 78,19968
M 126
WL 4
Q 0
M 127
Q 2
M 138
Q 4
M 139
TY 82,6656
TY 86,20480
M 128
Q 6
M 129
WL 4
Q 8
M 140
Q 10
M 141
TX 90,7680
TX 94,21504
M 130
Q 12
M 131
R 52
M 142
Q 14
M 143
TY 98,8192
TY 102,22016
M 0
R 54
M 1
WL 10
Q 16
M 12
Q 18
M 13
Q 20
M 2
Q 22
M 3
WL 8
Q 24
M 14
Q 26
M 15
Q 28
M 4
Q 30
M 5
WL 6
Q 32
M 16
Q 34
M 17
Q 36
M 6
Q 38
M 7
WL 2
Q 40
M 18
Q 42
M 19
Q 44
M 8
Q 46
M 9
H 1
AW 186,8
M 20
UL 6,44
UH 7,44
M 21
AR 8,139
AR 9,163
M 10
H 2
AD 8,6
M 11
R 56
M 22
AD 9,7
AW 139,8
M 23
R 58
M 24
AW 163,9
UL 6,45
M 25
UH 7,45
AR 8,187
M 26
AD 8,6
AD 143,7
M 27
AW 187,8
M 28
M 29
M 30
M 31
M 32
M 33
WV 5
M 34
WL 0
FP
M 35
M 36
M 37
M 38
ae 44,68,0
M 39
s_bfe_u32 s51, s50, 0x10003
v_mad_i32_i24 v47, v49, s51, v48
XL 22,128
M 40
M 41
M 42
M 43
M 44
XL 24,896
M 45
FR
M 46
M 47
M 48
M 49
M 50
XL 26,1664
M 51
)"
R"(M 52
M 53
WL 0
FQ 36
M 54
M 55
M 56
FZ 36
R 72
M 57
mw 24
R 74
M 58
TY 106,9736
TY 110,23560
M 59
R 76
M 60
ad 84,1392
W 0
M 61
XL 28,2432
M 62
M 63
W 1
M 64
M 65
M 66
W 2
M 67
WL 3
AN 104,106
AN 105,107
M 68
SH 106
SH 107
M 69
mw 27
AN 106,106
AN 107,107
M 70
WL 2
AN 108,110
AN 109,111
M 71
R 78
M 72
SH 110
SH 111
M 73
AN 110,110
AN 111,111
M 74
TY 114,11272
TY 118,25096
M 75
ad 84,1398
W 0
M 76
M 77
M 78
W 1
M 79
M 80
M 81
W 2
M 82
M 83
R 80
M 84
mw 48
WL 3
AN 112,114
AN 113,115
M 85
SH 114
SH 115
M 86
AN 114,114
AN 115,115
M 87
WL 2
AN 116,118
AN 117,119
M 88
SH 118
SH 119
M 89
AN 118,118
AN 119,119
M 90
TY 122,12808
TY 126,26632
M 91
ad 84,1440
M 92
W 0
UL 6,14
M 93
UH 7,14
AR 8,140
M 94
AR 9,164
AD 8,6
M 95
R 82
M 96
W 1
AD 9,7
M 97
AW 140,8
AW 164,9
M 98
UL 6,15
UH 7,15
M 99
WL 3
AN 120,122
AN 121,123
M 100
SH 122
SH 123
M 101
AN 122,122
AN 123,123
M 102
WL 2
AN 124,126
AN 125,127
M 103
SH 126
SH 127
M 104
AN 126,126
AN 127,127
M 105
W 2
AR 8,188
M 106
AD 8,6
AD 144,7
M 107
R 0
M 108
mw 51
AW 188,8
UL 6,16
M 109
ad 84,1446
W 0
M 110
UH 7,16
AR 8,141
M 111
AR 9,165
AD 8,6
M 112
W 1
AD 9,7
M 113
AW 141,8
AW 165,9
M 114
UL 6,17
UH 7,17
M 115
W 2
AR 8,189
M 116
AD 8,6
AD 145,7
M 117
AW 189,8
UL 6,18
M 118
bc 49,16,66,60
UH 7,18
M 119
WL 0
R 2
M 120
mh 9
AR 8,142
AR 9,166
M 121
ad 66,3072
H 0
M 132
AD 8,6
AD 9,7
M 133
TX 58,9216
TX 62,23040
M 122
DG 1024
M 123
AW 142,8
AW 166,9
M 134
UL 6,19
UH 7,19
M 135
TY 66,9728
TY 70,23552
M 124
bc 48,16,64,56
AR 8,190
M 125
mh 12
AD 8,6
AD 146,7
M 136
ad 64,3072
H 0
M 137
TX 74,10752
TX 78,24576
M 126
WL 4
Q 0
M 127
Q 2
M 138
Q 4
M 139
TY 82,11264
TY 86,25088
M 128
Q 6
M 129
WL 4
Q 8
M 140
Q 10
M 141
TX 90,12288
TX 94,26112
M 130
Q 12
M 131
R 4
M 142
Q 14
M 143
TY 98,12800
TY 102,26624
M 0
R 6
M 1
WL 10
Q 16
M 12
Q 18
M 13
Q 20
M 2
Q 22
M 3
WL 8
Q 24
M 14
Q 26
M 15
Q 28
M 4
Q 30
M 5
WL 6
Q 32
M 16
Q 34
M 17
Q 36
M 6
Q 38
M 7
WL 2
Q 40
M 18
Q 42
M 19
Q 44
M 8
Q 46
M 9
H 1
AW 190,8
M 20
UL 6,20
UH 7,20
M 21
AR 8,143
AR 9,167
M 10
H 2
AD 8,6
M 11
R 8
M 22
AD 9,7
AW 143,8
M 23
R 10
PA 12288
DF
M 24
AW 167,9
UL 6,21
M 25
UH 7,21
AR 8,191
M 26
AD 8,6
AD 147,7
M 27
AW 191,8
M 28
M 29
M 30
M 31
M 32
M 33
WV 6
M 34
WL 0
FP
M 35
M 36
M 37
M 38
M 39
M 40
ae 44,68,96
M 41
s_bitcmp1_b32 s50, 4
s_cselect_b32 s51, 24576, 0
v_add_u32 v47, s51, v3
M 42
XL 30,128
M 43
M 44
M 45
FR
M 46
XL 32,896
M 47
M 48
M 49
M 50
M 51
M 52
XL 34,1664
M 53
WL 0
FQ 40
M 54
M 55
M 56
FZ 40
R 24
M 57
mw 72
R 26
M 58
TY 106,520
TY 110,14344
M 59
R 28
M 60
ad 84,1488
W 0
M 61
XL 36,2432
v_lshlrev_b32 v55, 4, v0
M 62
FL
M 63
W 1
M 64
M 65
M 66
W 2
M 67
WL 3
AN 104,106
AN 105,107
M 68
SH 106
SH 107
M 69
mw 75
AN 106,106
AN 107,107
M 70
WL 2
AN 108,110
AN 109,111
M 71
R 30
M 72
)"
R"(SH 110
SH 111
M 73
AN 110,110
AN 111,111
M 74
TY 114,2056
TY 118,15880
M 75
ad 84,1494
W 0
M 76
M 77
M 78
W 1
M 79
M 80
M 81
W 2
M 82
M 83
R 32
M 84
mw 0
WL 3
AN 112,114
AN 113,115
M 85
SH 114
SH 115
M 86
AN 114,114
AN 115,115
M 87
WL 2
AN 116,118
AN 117,119
M 88
SH 118
SH 119
M 89
AN 118,118
AN 119,119
M 90
TY 122,3592
TY 126,17416
M 91
ad 84,1536
M 92
W 0
M 93
M 94
UL 6,22
UH 7,22
M 95
R 34
M 96
W 1
AR 8,144
M 97
AR 9,168
AD 8,6
M 98
AD 9,7
AW 144,8
M 99
WL 3
AN 120,122
AN 121,123
M 100
SH 122
SH 123
M 101
AN 122,122
AN 123,123
M 102
WL 2
AN 124,126
AN 125,127
M 103
SH 126
SH 127
M 104
AN 126,126
AN 127,127
M 105
W 2
AW 168,9
M 106
UL 6,23
UH 7,23
M 107
R 48
M 108
mw 3
AR 8,192
AD 8,6
M 109
ad 84,1542
W 0
M 110
AD 148,7
AW 192,8
M 111
UL 6,24
UH 7,24
M 112
W 1
AR 8,145
M 113
AR 9,169
AD 8,6
M 114
AD 9,7
AW 145,8
M 115
W 2
AW 169,9
M 116
UL 6,25
UH 7,25
M 117
AR 8,193
AD 8,6
M 118
bc 49,17,66,60
AD 149,7
M 119
WL 0
R 50
M 120
mh 18
AW 193,8
UL 6,26
M 121
ad 66,3264
H 0
M 132
UH 7,26
AR 8,146
M 133
TX 58,0
TX 62,13824
M 122
DG 1024
M 123
AR 9,170
AD 8,6
M 134
AD 9,7
AW 146,8
M 135
TY 66,512
TY 70,14336
M 124
bc 48,17,64,56
AW 170,9
M 125
mh 21
UL 6,27
UH 7,27
M 136
ad 64,3264
H 0
M 137
TX 74,1536
TX 78,15360
M 126
WL 4
Q 0
M 127
Q 2
M 138
Q 4
M 139
TY 82,2048
TY 86,15872
M 128
Q 6
M 129
WL 4
Q 8
M 140
Q 10
M 141
TX 90,3072
TX 94,16896
M 130
Q 12
M 131
R 52
M 142
Q 14
M 143
TY 98,3584
TY 102,17408
M 0
R 54
M 1
WL 10
Q 16
M 12
Q 18
M 13
Q 20
M 2
Q 22
M 3
WL 8
Q 24
M 14
Q 26
M 15
Q 28
M 4
Q 30
M 5
WL 6
Q 32
M 16
Q 34
M 17
Q 36
M 6
Q 38
M 7
WL 2
Q 40
M 18
Q 42
M 19
Q 44
M 8
Q 46
M 9
H 1
AR 8,194
M 20
AD 8,6
AD 150,7
M 21
AW 194,8
UL 6,28
M 10
H 2
UH 7,28
M 11
R 56
M 22
AR 8,147
AR 9,171
M 23
R 58
M 24
AD 8,6
AD 9,7
M 25
AW 147,8
AW 171,9
M 26
UL 6,29
UH 7,29
M 27
AR 8,195
AD 8,6
M 28
AD 151,7
AW 195,8
M 29
M 30
M 31
M 32
M 33
WV 5
M 34
WL 0
FP
M 35
M 36
M 37
M 38
M 39
M 40
M 41
M 42
ae 44,68,96
M 43
s_bfe_u32 s51, s50, 0x10005
v_mad_i32_i24 v47, v49, s51, v48
XL 38,128
M 44
M 45
FR
M 46
M 47
M 48
XL 40,896
M 49
M 50
M 51
M 52
M 53
WL 0
FQ 44
M 54
XL 42,1664
M 55
M 56
FZ 44
R 72
M 57
mw 24
R 74
M 58
TY 106,5128
TY 110,18952
M 59
R 76
M 60
ad 84,1584
W 0
M 61
XL 44,2432
M 62
M 63
W 1
M 64
M 65
M 66
W 2
M 67
WL 3
AN 104,106
AN 105,107
M 68
SH 106
SH 107
M 69
mw 27
AN 106,106
AN 107,107
M 70
WL 2
AN 108,110
AN 109,111
M 71
R 78
M 72
SH 110
SH 111
M 73
AN 110,110
AN 111,111
M 74
TY 114,6664
TY 118,20488
M 75
ad 84,1590
W 0
M 76
M 77
M 78
W 1
M 79
M 80
M 81
W 2
M 82
M 83
R 80
M 84
mw 48
WL 3
AN 112,114
AN 113,115
M 85
SH 114
SH 115
M 86
AN 114,114
AN 115,115
M 87
WL 2
AN 116,118
AN 117,119
M 88
SH 118
SH 119
M 89
AN 118,118
AN 119,119
M 90
TY 122,8200
TY 126,22024
M 91
ad 84,1632
M 92
W 0
M 93
M 94
)"
R"(M 95
R 82
M 96
W 1
UL 6,30
M 97
UH 7,30
AR 8,148
M 98
AR 9,172
AD 8,6
M 99
WL 3
AN 120,122
AN 121,123
M 100
SH 122
SH 123
M 101
AN 122,122
AN 123,123
M 102
WL 2
AN 124,126
AN 125,127
M 103
SH 126
SH 127
M 104
AN 126,126
AN 127,127
M 105
W 2
AD 9,7
M 106
AW 148,8
AW 172,9
M 107
R 0
M 108
mw 51
UL 6,31
UH 7,31
M 109
ad 84,1638
W 0
M 110
AR 8,196
AD 8,6
M 111
AD 152,7
AW 196,8
M 112
W 1
UL 6,32
M 113
UH 7,32
AR 8,149
M 114
AR 9,173
AD 8,6
M 115
W 2
AD 9,7
M 116
AW 149,8
AW 173,9
M 117
UL 6,33
UH 7,33
M 118
bc 49,18,66,60
AR 8,197
M 119
WL 0
R 2
M 120
mh 0
AD 8,6
AD 153,7
M 121
ad 66,3456
H 0
M 132
AW 197,8
UL 6,34
M 133
TX 58,4608
TX 62,18432
M 122
DG 1024
M 123
UH 7,34
AR 8,150
M 134
AR 9,174
AD 8,6
M 135
TY 66,5120
TY 70,18944
M 124
bc 48,18,64,56
AD 9,7
M 125
mh 3
AW 150,8
AW 174,9
M 136
ad 64,3456
H 0
M 137
TX 74,6144
TX 78,19968
M 126
WL 4
Q 0
M 127
Q 2
M 138
Q 4
M 139
TY 82,6656
TY 86,20480
M 128
Q 6
M 129
WL 4
Q 8
M 140
Q 10
M 141
TX 90,7680
TX 94,21504
M 130
Q 12
M 131
R 4
M 142
Q 14
M 143
TY 98,8192
TY 102,22016
M 0
R 6
M 1
WL 10
Q 16
M 12
Q 18
M 13
Q 20
M 2
Q 22
M 3
WL 8
Q 24
M 14
Q 26
M 15
Q 28
M 4
Q 30
M 5
WL 6
Q 32
M 16
Q 34
M 17
Q 36
M 6
Q 38
M 7
WL 2
Q 40
M 18
Q 42
M 19
Q 44
M 8
Q 46
M 9
H 1
UL 6,35
M 20
UH 7,35
AR 8,198
M 21
AD 8,6
AD 154,7
M 10
H 2
AW 198,8
M 11
R 8
M 22
UL 6,36
UH 7,36
M 23
R 10
PA 12288
DF
M 24
AR 8,151
AR 9,175
M 25
AD 8,6
AD 9,7
M 26
AW 151,8
AW 175,9
M 27
v_cmp_ne_u32 vcc, 2, v54
UL 6,37
M 28
UH 7,37
AR 8,199
M 29
AD 8,6
AD 155,7
M 30
FC
AW 199,8
M 31
M 32
M 33
WV 6
M 34
WL 0
FP
M 35
M 36
M 37
M 38
M 39
M 40
M 41
M 42
M 43
M 44
M 45
FR
M 46
M 47
M 48
M 49
M 50
M 51
M 52
M 53
WL 0
FQ 48
M 54
M 55
M 56
FZ 48
R 24
M 57
mw 72
R 26
M 58
TY 106,9736
TY 110,23560
M 59
R 28
M 60
ad 84,1680
W 0
M 61
M 62
M 63
W 1
M 64
M 65
M 66
W 2
M 67
WL 3
AN 104,106
AN 105,107
M 68
SH 106
SH 107
M 69
mw 75
AN 106,106
AN 107,107
M 70
WL 2
AN 108,110
AN 109,111
M 71
R 30
M 72
SH 110
SH 111
M 73
AN 110,110
AN 111,111
M 74
TY 114,11272
TY 118,25096
M 75
ad 84,1686
W 0
M 76
ae 44,70,0
M 77
s_bitcmp1_b32 s50, 0
s_cselect_b32 s51, 24576, 0
v_add_u32 v47, s51, v3
M 78
W 1
XL 14,256
M 79
XL 16,1024
M 80
M 81
W 2
M 82
XL 18,1792
M 83
WL 1
R 32
M 84
mw 0
AN 112,114
AN 113,115
M 85
SH 114
SH 115
M 86
AN 114,114
AN 115,115
M 87
WL 2
AN 116,118
AN 117,119
M 88
SH 118
SH 119
M 89
AN 118,118
AN 119,119
M 90
TY 122,12808
TY 126,26632
M 91
ad 84,1728
M 92
W 0
XL 20,2560
M 93
M 94
M 95
R 34
M 96
W 1
M 97
M 98
UL 6,38
UH 7,38
M 99
WL 3
AN 120,122
AN 121,123
M 100
SH 122
SH 123
M 101
AN 122,122
AN 123,123
M 102
WL 2
AN 124,126
AN 125,127
M 103
SH 126
SH 127
M 104
AN 126,126
AN 127,127
M 105
W 2
AR 8,152
M 106
AR 9,176
AD 8,6
M 107
R 48
M 108
mw 3
AD 9,7
AW 152,8
M 109
ad 84,1734
W 0
M 110
AW 176,9
UL 6,39
M 111
)"
R"(UH 7,39
AD 132,6
M 112
W 1
AD 156,7
M 113
UL 6,40
UH 7,40
M 114
AR 8,153
AR 9,177
M 115
W 2
AD 8,6
M 116
AD 9,7
AW 153,8
M 117
AW 177,9
UL 6,41
M 118
bc 49,19,66,60
UH 7,41
M 119
WL 0
R 50
M 120
mh 9
AD 133,6
AD 157,7
M 121
ad 66,3648
H 0
M 132
UL 6,42
UH 7,42
M 133
TX 58,9216
TX 62,23040
M 122
DG 1024
M 123
AR 8,154
AR 9,178
M 134
AD 8,6
AD 9,7
M 135
TY 66,9728
TY 70,23552
M 124
bc 48,19,64,56
AW 154,8
M 125
mh 12
AW 178,9
UL 6,43
M 136
ad 64,3648
H 0
M 137
TX 74,10752
TX 78,24576
M 126
WL 4
Q 0
M 127
Q 2
M 138
Q 4
M 139
TY 82,11264
TY 86,25088
M 128
Q 6
M 129
WL 4
Q 8
M 140
Q 10
M 141
TX 90,12288
TX 94,26112
M 130
Q 12
M 131
R 52
M 142
Q 14
M 143
TY 98,12800
TY 102,26624
M 0
R 54
M 1
WL 10
Q 16
M 12
Q 18
M 13
Q 20
M 2
Q 22
M 3
WL 8
Q 24
M 14
Q 26
M 15
Q 28
M 4
Q 30
M 5
WL 6
Q 32
M 16
Q 34
M 17
Q 36
M 6
Q 38
M 7
WL 2
Q 40
M 18
Q 42
M 19
Q 44
M 8
Q 46
M 9
H 1
UH 7,43
M 20
AD 134,6
AD 158,7
M 21
UL 6,44
UH 7,44
M 10
H 2
AR 8,155
M 11
R 56
M 22
AR 9,179
AD 8,6
M 23
R 58
v_min_u32 v52, 752, v0
v_lshrrev_b32 v52, 1, v52
v_add_u32 v53, 0x1800, v52
global_load_dwordx2 a[200:201], v52, s[94:95] offset:-2048 sc1
global_load_dwordx2 a[202:203], v52, s[94:95] offset:-512 sc1
global_load_dwordx2 a[204:205], v52, s[94:95] offset:1024 sc1
global_load_dwordx2 a[206:207], v52, s[94:95] offset:2560 sc1
global_load_dwordx2 a[208:209], v53, s[94:95] offset:-2048 sc1
global_load_dwordx2 a[210:211], v53, s[94:95] offset:-512 sc1
global_load_dwordx2 a[212:213], v53, s[94:95] offset:1024 sc1
global_load_dwordx2 a[214:215], v53, s[94:95] offset:2560 sc1
M 24
AD 9,7
AW 155,8
M 25
AW 179,9
UL 6,45
M 26
UH 7,45
AD 135,6
M 27
AD 159,7
M 28
M 29
M 30
M 31
M 32
M 33
WV 13
M 34
WL 0
FP
M 35
M 36
M 37
M 38
M 39
M 40
M 41
M 42
M 43
M 44
M 45
FR
M 46
M 47
M 48
M 49
M 50
M 51
M 52
M 53
WL 0
FQ 52
M 54
M 55
M 56
FZ 52
R 72
M 57
mw 24
R 74
M 58
TY 106,520
TY 110,14344
M 59
R 76
M 60
ad 84,1776
W 0
M 61
M 62
M 63
W 1
M 64
M 65
M 66
W 2
M 67
WL 3
AN 104,106
AN 105,107
M 68
SH 106
SH 107
M 69
mw 27
AN 106,106
AN 107,107
M 70
WL 2
AN 108,110
AN 109,111
M 71
R 78
M 72
SH 110
SH 111
M 73
AN 110,110
AN 111,111
M 74
TY 114,2056
TY 118,15880
M 75
ad 84,1782
W 0
M 76
ae 44,70,0
M 77
s_bfe_u32 s51, s50, 0x10001
v_mad_i32_i24 v47, v49, s51, v48
XL 22,256
M 78
W 1
M 79
M 80
XL 24,1024
M 81
W 2
M 82
M 83
WL 1
R 80
M 84
mw 48
AN 112,114
AN 113,115
M 85
SH 114
SH 115
M 86
AN 114,114
AN 115,115
M 87
WL 2
AN 116,118
AN 117,119
M 88
SH 118
SH 119
M 89
AN 118,118
AN 119,119
M 90
TY 122,3592
TY 126,17416
M 91
ad 84,1824
M 92
W 0
XL 26,1792
M 93
XL 28,2560
M 94
M 95
R 82
M 96
W 1
M 97
M 98
M 99
WL 3
AN 120,122
AN 121,123
M 100
SH 122
SH 123
M 101
AN 122,122
AN 123,123
M 102
WL 2
AN 124,126
AN 125,127
M 103
SH 126
SH 127
M 104
AN 126,126
AN 127,127
M 105
W 2
M 106
M 107
R 0
M 108
mw 51
M 109
ad 84,1830
W 0
M 110
M 111
M 112
W 1
M 113
M 114
M 115
W 2
M 116
M 117
M 118
bc 49,20,66,60
M 119
WL 2
R 2
M 120
mh 18
M 121
ad 66,3840
H 0
M 132
M 133
TX 58,0
TX 62,13824
M 122
DG 1024
M 123
M 134
UL 6,14
UH 7,14
M 135
TY 66,512
TY 70,14336
M 124
bc 48,20,64,56
AD 160,6
M 125
mh 21
AD 184,7
UL 6,15
M 136
ad 64,3840
H 0
M 137
TX 74,1536
TX 78,15360
M 126
WL 4
Q 0
M 127
Q 2
M 138
Q 4
M 139
TY 82,2048
TY 86,15872
M 128
Q 6
M 129
WL 4
)"
R"(Q 8
M 140
Q 10
M 141
TX 90,3072
TX 94,16896
M 130
Q 12
M 131
R 4
M 142
Q 14
M 143
TY 98,3584
TY 102,17408
M 0
R 6
M 1
WL 10
Q 16
M 12
Q 18
M 13
Q 20
M 2
Q 22
M 3
WL 8
Q 24
M 14
Q 26
M 15
Q 28
M 4
Q 30
M 5
WL 6
Q 32
M 16
Q 34
M 17
Q 36
M 6
Q 38
M 7
WL 2
Q 40
M 18
Q 42
M 19
Q 44
M 8
Q 46
M 9
H 1
UH 7,15
M 20
AD 208,6
AD 232,7
M 21
UL 6,16
UH 7,16
M 10
H 2
AD 161,6
M 11
R 8
M 22
AD 185,7
UL 6,17
M 23
R 10
PA 12288
DF
M 24
UH 7,17
AD 209,6
M 25
AD 233,7
UL 6,18
M 26
UH 7,18
AD 162,6
M 27
AD 186,7
UL 6,19
M 28
UH 7,19
AD 210,6
M 29
AD 234,7
UL 6,20
M 30
UH 7,20
AD 163,6
M 31
AD 187,7
UL 6,21
M 32
UH 7,21
AD 211,6
M 33
WV 6
AD 235,7
M 34
WL 0
FP
M 35
M 36
M 37
M 38
M 39
M 40
M 41
M 42
M 43
M 44
M 45
FR
M 46
M 47
M 48
M 49
M 50
M 51
M 52
M 53
WL 0
FQ 56
M 54
M 55
M 56
FZ 56
R 24
M 57
mw 72
R 26
M 58
TY 106,5128
TY 110,18952
M 59
R 28
M 60
ad 84,1872
W 0
M 61
M 62
M 63
W 1
M 64
M 65
M 66
W 2
M 67
WL 3
AN 104,106
AN 105,107
M 68
SH 106
SH 107
M 69
mw 75
AN 106,106
AN 107,107
M 70
WL 2
AN 108,110
AN 109,111
M 71
R 30
M 72
SH 110
SH 111
M 73
AN 110,110
AN 111,111
M 74
TY 114,6664
TY 118,20488
M 75
ad 84,1878
W 0
M 76
M 77
M 78
W 1
M 79
ae 44,68,0
M 80
s_bitcmp1_b32 s50, 2
s_cselect_b32 s51, 24576, 0
v_add_u32 v47, s51, v3
M 81
W 2
XL 30,256
M 82
M 83
WL 1
R 32
M 84
mw 0
AN 112,114
AN 113,115
M 85
SH 114
SH 115
M 86
AN 114,114
AN 115,115
M 87
WL 2
AN 116,118
AN 117,119
M 88
SH 118
SH 119
M 89
AN 118,118
AN 119,119
M 90
TY 122,8200
TY 126,22024
M 91
ad 84,1920
M 92
W 0
XL 32,1024
M 93
XL 34,1792
M 94
M 95
R 34
M 96
W 1
XL 36,2560
M 97
M 98
M 99
WL 3
AN 120,122
AN 121,123
M 100
SH 122
SH 123
M 101
AN 122,122
AN 123,123
M 102
WL 2
AN 124,126
AN 125,127
M 103
SH 126
SH 127
M 104
AN 126,126
AN 127,127
M 105
W 2
M 106
M 107
R 48
M 108
mw 3
M 109
ad 84,1926
W 0
M 110
M 111
M 112
W 1
M 113
M 114
M 115
W 2
M 116
M 117
M 118
bc 49,21,66,60
M 119
WL 2
R 50
M 120
mh 0
M 121
ad 66,4032
H 0
M 132
v_mov_b32 v54, s39
v_mul_f32 v54, 0xb5400000, v54
M 133
TX 58,4608
TX 62,18432
M 122
DG 1024
M 123
v_add_u32 v52, %[bm], v52
v_bfe_u32 v53, v52, 6, 2
M 134
v_and_b32 v55, 63, v52
v_add_u32 v53, v53, v55
M 135
TY 66,5120
TY 70,18944
M 124
bc 48,21,64,56
v_and_b32 v52, 0x700, v52
M 125
mh 3
v_lshl_add_u32 v52, v53, 2, v52
v_add_u32 v52, %[sb], v52
M 136
ad 64,4032
H 0
M 137
TX 74,6144
TX 78,19968
M 126
WL 4
Q 0
M 127
Q 2
M 138
Q 4
M 139
TY 82,6656
TY 86,20480
M 128
Q 6
M 129
WL 4
Q 8
M 140
Q 10
M 141
TX 90,7680
TX 94,21504
M 130
Q 12
M 131
R 52
M 142
Q 14
M 143
TY 98,8192
TY 102,22016
M 0
R 54
M 1
WL 10
Q 16
M 12
Q 18
M 13
Q 20
M 2
Q 22
M 3
WL 8
Q 24
M 14
Q 26
M 15
Q 28
M 4
Q 30
M 5
WL 6
Q 32
M 16
Q 34
M 17
Q 36
M 6
Q 38
M 7
WL 2
Q 40
M 18
Q 42
M 19
Q 44
M 8
Q 46
M 9
H 1
AR 12,214
M 20
AR 13,215
AR 8,208
M 21
AR 9,209
v_add_f32 v12, v12, v8
M 10
H 2
v_add_f32 v13, v13, v9
M 11
R 56
M 22
AR 8,210
)"
R"(AR 9,211
M 23
R 58
M 24
v_add_f32 v12, v12, v8
v_add_f32 v13, v13, v9
M 25
AR 8,212
AR 9,213
M 26
v_add_f32 v12, v12, v8
v_add_f32 v13, v13, v9
M 27
AR 6,206
AR 7,207
M 28
v_add_f32 v12, v12, v54
v_add_f32 v13, v13, v54
M 29
v_fma_f32 v8, v6, s86, v12
v_fma_f32 v9, v7, s86, v13
M 30
ds_write_b32 v52, v8 offset:1024
ds_write_b32 v52, v9 offset:1040
M 31
AR 8,200
AR 9,201
M 32
v_add_f32 v6, v6, v8
v_add_f32 v7, v7, v9
M 33
WV 5
v_fma_f32 v8, v6, s86, v12
v_fma_f32 v9, v7, s86, v13
M 34
WL 2
FP
ds_write_b32 v52, v8 offset:2560
M 35
ds_write_b32 v52, v9 offset:2576
AR 8,202
M 36
AR 9,203
v_add_f32 v6, v6, v8
M 37
v_add_f32 v7, v7, v9
v_fma_f32 v8, v6, s86, v12
M 38
ds_write_b32 v52, v8 offset:4096
v_fma_f32 v9, v7, s86, v13
M 39
ds_write_b32 v52, v9 offset:4112
AR 8,204
M 40
AR 9,205
v_add_f32 v6, v6, v8
M 41
v_add_f32 v7, v7, v9
v_fma_f32 v8, v6, s86, v12
M 42
ds_write_b32 v52, v8 offset:5632
v_fma_f32 v9, v7, s86, v13
M 43
ds_write_b32 v52, v9 offset:5648
M 44
UL 6,22
UH 7,22
M 45
FR
AD 164,6
M 46
AD 188,7
UL 6,23
M 47
UH 7,23
AD 212,6
M 48
AD 236,7
UL 6,24
M 49
UH 7,24
AD 165,6
M 50
AD 189,7
UL 6,25
M 51
UH 7,25
AD 213,6
M 52
AD 237,7
UL 6,26
M 53
WL 0
FQ 60
UH 7,26
M 54
AD 166,6
AD 190,7
M 55
UL 6,27
UH 7,27
M 56
FZ 60
R 72
M 57
mw 24
R 74
M 58
TY 106,9736
TY 110,23560
M 59
R 76
M 60
ad 84,1968
W 0
M 61
AD 214,6
AD 238,7
M 62
UL 6,28
UH 7,28
M 63
W 1
AD 167,6
M 64
AD 191,7
UL 6,29
M 65
UH 7,29
AD 215,6
M 66
W 2
AD 239,7
M 67
WL 3
AN 104,106
AN 105,107
M 68
SH 106
SH 107
M 69
mw 27
AN 106,106
AN 107,107
M 70
WL 2
AN 108,110
AN 109,111
M 71
R 78
M 72
SH 110
SH 111
M 73
AN 110,110
AN 111,111
M 74
TY 114,11272
TY 118,25096
M 75
ad 84,1974
W 0
M 76
M 77
M 78
W 1
M 79
M 80
M 81
W 2
M 82
ae 44,68,0
M 83
WL 1
R 80
M 84
mw 48
AN 112,114
AN 113,115
M 85
s_bfe_u32 s51, s50, 0x10003
SH 114
SH 115
M 86
AN 114,114
AN 115,115
M 87
WL 2
AN 116,118
AN 117,119
M 88
SH 118
SH 119
M 89
AN 118,118
AN 119,119
M 90
TY 122,12808
TY 126,26632
M 91
ad 84,2016
M 92
W 0
v_mad_i32_i24 v47, v49, s51, v48
M 93
XL 38,256
M 94
XL 40,1024
M 95
R 82
M 96
W 1
XL 42,1792
M 97
M 98
M 99
WL 3
AN 120,122
AN 121,123
M 100
SH 122
SH 123
M 101
AN 122,122
AN 123,123
M 102
WL 2
AN 124,126
AN 125,127
M 103
SH 126
SH 127
M 104
AN 126,126
AN 127,127
M 105
W 2
XL 44,2560
M 106
M 107
R 0
M 108
mw 51
M 109
ad 84,2022
W 0
M 110
M 111
M 112
W 1
M 113
M 114
M 115
W 2
M 116
M 117
M 118
bc 49,22,66,60
M 119
WL 2
R 2
M 120
mh 9
M 121
ad 66,4224
H 0
M 132
M 133
TX 58,9216
TX 62,23040
M 122
DG 1024
M 123
M 134
M 135
TY 66,9728
TY 70,23552
M 124
bc 48,22,64,56
M 125
mh 12
M 136
ad 64,4224
H 0
M 137
TX 74,10752
TX 78,24576
M 126
WL 4
Q 0
M 127
Q 2
M 138
Q 4
M 139
TY 82,11264
TY 86,25088
M 128
Q 6
M 129
WL 4
Q 8
M 140
Q 10
M 141
TX 90,12288
TX 94,26112
M 130
Q 12
M 131
R 4
M 142
Q 14
M 143
TY 98,12800
TY 102,26624
M 0
R 6
M 1
WL 10
Q 16
M 12
Q 18
M 13
Q 20
M 2
Q 22
M 3
WL 8
Q 24
M 14
Q 26
M 15
Q 28
M 4
Q 30
M 5
WL 6
Q 32
M 16
Q 34
M 17
Q 36
M 6
Q 38
M 7
WL 2
Q 40
M 18
Q 42
M 19
Q 44
M 8
Q 46
M 9
H 1
UL 6,30
M 20
UH 7,30
AD 168,6
M 21
AD 192,7
UL 6,31
M 10
H 2
UH 7,31
M 11
R 8
M 22
AD 216,6
AD 240,7
M 23
R 10
PA 12288
DF
M 24
UL 6,32
UH 7,32
M 25
AD 169,6
AD 193,7
M 26
UL 6,33
UH 7,33
M 27
AD 217,6
AD 241,7
M 28
UL 6,34
UH 7,34
M 29
AD 170,6
AD 194,7
M 30
UL 6,35
UH 7,35
M 31
AD 218,6
AD 242,7
M 32
)"
R"(UL 6,36
UH 7,36
M 33
WV 6
AD 171,6
AD 195,7
M 34
WL 0
FP
UL 6,37
M 35
UH 7,37
AD 219,6
M 36
AD 243,7
M 37
M 38
M 39
M 40
M 41
M 42
M 43
M 44
M 45
FR
M 46
M 47
M 48
M 49
M 50
M 51
M 52
M 53
WL 0
FQ 64
M 54
M 55
M 56
FZ 64
R 24
M 57
mw 72
R 26
M 58
TY 106,520
TY 110,14344
M 59
R 28
M 60
ad 84,2064
W 0
M 61
M 62
M 63
W 1
M 64
M 65
M 66
W 2
M 67
WL 3
AN 104,106
AN 105,107
M 68
SH 106
SH 107
M 69
mw 75
AN 106,106
AN 107,107
M 70
WL 2
AN 108,110
AN 109,111
M 71
R 30
M 72
SH 110
SH 111
M 73
AN 110,110
AN 111,111
M 74
TY 114,2056
TY 118,15880
M 75
ad 84,2070
W 0
M 76
M 77
M 78
W 1
M 79
M 80
M 81
W 2
M 82
M 83
R 32
M 84
mw 0
WL 3
AN 112,114
AN 113,115
M 85
SH 114
SH 115
M 86
AN 114,114
AN 115,115
M 87
WL 2
AN 116,118
AN 117,119
M 88
SH 118
SH 119
M 89
AN 118,118
AN 119,119
M 90
TY 122,3592
TY 126,17416
M 91
ad 84,2112
M 92
W 0
M 93
ae 44,68,96
M 94
s_bitcmp1_b32 s50, 4
s_cselect_b32 s51, 24576, 0
v_add_u32 v47, s51, v3
M 95
R 34
M 96
W 1
XL 14,256
M 97
XL 16,1024
M 98
XL 18,1792
M 99
WL 3
AN 120,122
AN 121,123
M 100
SH 122
SH 123
M 101
AN 122,122
AN 123,123
M 102
WL 2
AN 124,126
AN 125,127
M 103
SH 126
SH 127
M 104
AN 126,126
AN 127,127
M 105
W 2
XL 20,2560
M 106
M 107
R 48
M 108
mw 3
M 109
ad 84,2118
W 0
M 110
M 111
M 112
W 1
M 113
M 114
M 115
W 2
M 116
M 117
M 118
bc 49,23,66,60
M 119
WL 2
R 50
M 120
mh 18
M 121
ad 66,4416
H 0
M 132
M 133
TX 58,0
TX 62,13824
M 122
DG 1024
M 123
M 134
M 135
TY 66,512
TY 70,14336
M 124
bc 48,23,64,56
M 125
mh 21
M 136
ad 64,4416
H 0
M 137
TX 74,1536
TX 78,15360
M 126
WL 4
Q 0
M 127
Q 2
M 138
Q 4
M 139
TY 82,2048
TY 86,15872
M 128
Q 6
M 129
WL 4
Q 8
M 140
Q 10
M 141
TX 90,3072
TX 94,16896
M 130
Q 12
M 131
R 52
M 142
Q 14
M 143
TY 98,3584
TY 102,17408
M 0
R 54
M 1
WL 10
Q 16
M 12
Q 18
M 13
Q 20
M 2
Q 22
M 3
WL 8
Q 24
M 14
Q 26
M 15
Q 28
M 4
Q 30
M 5
WL 6
Q 32
M 16
Q 34
M 17
Q 36
M 6
Q 38
M 7
WL 2
Q 40
M 18
Q 42
M 19
Q 44
M 8
Q 46
M 9
H 1
UL 6,38
M 20
UH 7,38
AD 172,6
M 21
AD 196,7
UL 6,39
M 10
H 2
UH 7,39
M 11
R 56
M 22
AD 220,6
AD 244,7
M 23
R 58
M 24
UL 6,40
UH 7,40
M 25
AD 173,6
AD 197,7
M 26
UL 6,41
UH 7,41
M 27
AD 221,6
AD 245,7
M 28
UL 6,42
UH 7,42
M 29
AD 174,6
AD 198,7
M 30
UL 6,43
UH 7,43
M 31
AD 222,6
AD 246,7
M 32
UL 6,44
UH 7,44
M 33
WV 5
AD 175,6
AD 199,7
M 34
WL 0
FP
UL 6,45
M 35
UH 7,45
AD 223,6
M 36
AD 247,7
M 37
M 38
M 39
M 40
M 41
M 42
M 43
M 44
M 45
FR
M 46
M 47
M 48
M 49
M 50
M 51
M 52
M 53
WL 0
FQ 68
M 54
M 55
M 56
FZ 68
R 72
M 57
mw 24
R 74
M 58
TY 106,5128
TY 110,18952
M 59
R 76
M 60
ad 84,2160
W 0
M 61
M 62
M 63
W 1
M 64
M 65
M 66
W 2
M 67
WL 3
AN 104,106
AN 105,107
M 68
SH 106
SH 107
M 69
mw 27
AN 106,106
AN 107,107
M 70
WL 2
AN 108,110
AN 109,111
M 71
)"
R"(R 78
M 72
SH 110
SH 111
M 73
AN 110,110
AN 111,111
M 74
TY 114,6664
TY 118,20488
M 75
ad 84,2166
W 0
M 76
M 77
M 78
W 1
M 79
M 80
M 81
W 2
M 82
M 83
R 80
M 84
mw 48
WL 3
AN 112,114
AN 113,115
M 85
SH 114
SH 115
M 86
AN 114,114
AN 115,115
M 87
WL 2
AN 116,118
AN 117,119
M 88
SH 118
SH 119
M 89
AN 118,118
AN 119,119
M 90
TY 122,8200
TY 126,22024
M 91
ad 84,2208
M 92
W 0
M 93
ae 44,68,96
M 94
s_bfe_u32 s51, s50, 0x10005
v_mad_i32_i24 v47, v49, s51, v48
XL 22,256
M 95
R 82
M 96
W 1
XL 24,1024
M 97
M 98
M 99
WL 3
AN 120,122
AN 121,123
M 100
SH 122
SH 123
M 101
AN 122,122
AN 123,123
M 102
WL 2
AN 124,126
AN 125,127
M 103
SH 126
SH 127
M 104
AN 126,126
AN 127,127
M 105
W 2
XL 26,1792
M 106
M 107
R 0
M 108
mw 51
XL 28,2560
M 109
ad 84,2214
W 0
M 110
M 111
M 112
W 1
M 113
M 114
M 115
W 2
M 116
M 117
M 118
bc 49,0,66,60
M 119
WL 2
R 2
M 120
mh 0
M 121
ad 66,32
H 0
M 132
M 133
TX 58,4608
TX 62,18432
M 122
DG 1024
M 123
M 134
M 135
TY 66,5120
TY 70,18944
M 124
bc 48,0,64,56
M 125
mh 3
M 136
ad 64,0
H 0
M 137
TX 74,6144
TX 78,19968
M 126
WL 4
Q 0
M 127
Q 2
M 138
Q 4
M 139
TY 82,6656
TY 86,20480
M 128
Q 6
M 129
WL 4
Q 8
M 140
Q 10
M 141
TX 90,7680
TX 94,21504
M 130
Q 12
M 131
R 4
M 142
Q 14
M 143
TY 98,8192
TY 102,22016
M 0
R 6
M 1
WL 10
Q 16
M 12
Q 18
M 13
Q 20
M 2
Q 22
M 3
WL 8
Q 24
M 14
Q 26
M 15
Q 28
M 4
Q 30
M 5
WL 6
Q 32
M 16
Q 34
M 17
Q 36
M 6
Q 38
M 7
WL 2
Q 40
M 18
Q 42
M 19
Q 44
M 8
Q 46
M 9
H 1
UL 6,14
M 20
UH 7,14
AD 176,6
M 21
AD 200,7
UL 6,15
M 10
H 2
UH 7,15
M 11
R 8
M 22
AD 224,6
AD 248,7
M 23
R 10
PA 12288
DF
M 24
UL 6,16
UH 7,16
M 25
AD 177,6
AD 201,7
M 26
UL 6,17
UH 7,17
M 27
AD 225,6
AD 249,7
M 28
UL 6,18
UH 7,18
M 29
AD 178,6
AD 202,7
M 30
UL 6,19
UH 7,19
M 31
AD 226,6
AD 250,7
M 32
UL 6,20
UH 7,20
M 33
WV 6
AD 179,6
AD 203,7
M 34
WL 0
FP
UL 6,21
M 35
UH 7,21
AD 227,6
M 36
AD 251,7
M 37
M 38
M 39
M 40
M 41
M 42
M 43
M 44
M 45
FR
M 46
M 47
M 48
M 49
M 50
M 51
M 52
M 53
WL 0
FQ 72
M 54
M 55
M 56
FZ 72
R 24
M 57
mw 72
R 26
M 58
TY 106,9736
TY 110,23560
M 59
R 28
M 60
ad 84,2256
W 0
M 61
M 62
M 63
W 1
M 64
M 65
M 66
W 2
M 67
WL 3
AN 104,106
AN 105,107
M 68
SH 106
SH 107
M 69
mw 75
AN 106,106
AN 107,107
M 70
WL 2
AN 108,110
AN 109,111
M 71
R 30
M 72
SH 110
SH 111
M 73
AN 110,110
AN 111,111
M 74
TY 114,11272
TY 118,25096
M 75
ad 84,2262
W 0
M 76
M 77
M 78
W 1
M 79
M 80
M 81
W 2
M 82
M 83
R 32
M 84
mw 0
WL 3
AN 112,114
AN 113,115
M 85
SH 114
SH 115
M 86
AN 114,114
AN 115,115
M 87
WL 2
AN 116,118
AN 117,119
M 88
SH 118
SH 119
M 89
AN 118,118
AN 119,119
M 90
TY 122,12808
TY 126,26632
M 91
ad 84,0
M 92
W 0
M 93
M 94
M 95
R 34
M 96
W 1
M 97
M 98
M 99
WL 3
AN 120,122
AN 121,123
M 100
SH 122
SH 123
M 101
AN 122,122
AN 123,123
M 102
WL 2
AN 124,126
AN 125,127
)"
R"(M 103
SH 126
SH 127
M 104
AN 126,126
AN 127,127
M 105
W 2
M 106
M 107
R 48
M 108
mw 3
M 109
ad 84,6
W 0
M 110
M 111
M 112
W 1
M 113
M 114
M 115
W 2
M 116
M 117
M 118
bc 49,1,66,60
M 119
WL 2
R 50
M 120
mh 9
M 121
ad 66,224
H 0
M 132
M 133
TX 58,9216
TX 62,23040
M 122
DG 1024
M 123
M 134
M 135
TY 66,9728
TY 70,23552
M 124
bc 48,1,64,56
M 125
mh 12
M 136
ad 64,224
H 0
M 137
TX 74,10752
TX 78,24576
M 126
WL 4
Q 0
M 127
Q 2
M 138
Q 4
M 139
TY 82,11264
TY 86,25088
M 128
Q 6
M 129
WL 4
Q 8
M 140
Q 10
M 141
TX 90,12288
TX 94,26112
M 130
Q 12
M 131
R 52
M 142
Q 14
M 143
TY 98,12800
TY 102,26624
M 0
R 54
M 1
WL 10
Q 16
M 12
Q 18
M 13
Q 20
M 2
Q 22
M 3
WL 8
Q 24
M 14
Q 26
M 15
Q 28
M 4
Q 30
M 5
WL 6
Q 32
M 16
Q 34
M 17
Q 36
M 6
Q 38
M 7
WL 2
Q 40
M 18
Q 42
M 19
Q 44
M 8
Q 46
M 9
DG 1024
M 20
UL 6,22
UH 7,22
M 21
AD 180,6
AD 204,7
M 10
mh 15
UL 6,23
UH 7,23
M 11
R 56
M 22
ad 64,195
H 0
M 23
R 58
M 24
AD 228,6
AD 252,7
M 25
DG 1024
M 26
UL 6,24
UH 7,24
BV 52,0
M 27
AD 181,6
AD 205,7
M 28
UL 6,25
UH 7,25
M 29
AD 229,6
AD 253,7
M 30
UL 6,26
UH 7,26
M 31
AD 182,6
AD 206,7
M 32
UL 6,27
UH 7,27
M 33
WV 6
AD 230,6
AD 254,7
M 34
WL 1
FP
UL 6,28
M 35
UH 7,28
AD 183,6
M 36
AD 207,7
UL 6,29
M 37
UH 7,29
AD 231,6
M 38
AD 255,7
M 39
M 40
ds_read_b64 v[12:13], v4 offset:256
ds_read_b64 v[46:47], v4 offset:264
M 41
AR 6,36
AR 7,60
M 42
AR 8,84
AR 9,108
M 43
WL 3
EF 6,52
EF 7,53
M 44
EF 8,54
EF 9,55
M 45
FR
CV 30,6,7
M 46
CV 31,8,9
AR 6,37
M 47
AR 7,61
AR 8,85
M 48
AR 9,109
EF 6,52
M 49
EF 7,53
EF 8,54
M 50
EF 9,55
CV 32,6,7
M 51
CV 33,8,9
AR 6,38
M 52
AR 7,62
AR 8,86
M 53
WL 0
FQ 76
AR 9,110
M 54
EF 6,52
EF 7,53
M 55
EF 8,54
EF 9,55
M 56
FZ 76
R 72
M 57
mw 24
R 74
M 58
TY 106,520
TY 110,14344
M 59
R 76
M 60
ad 84,48
W 0
M 61
CV 34,6,7
CV 35,8,9
M 62
AR 6,39
AR 7,63
M 63
W 1
AR 8,87
M 64
AR 9,111
EF 6,52
M 65
EF 7,53
EF 8,54
M 66
W 2
EF 9,55
M 67
WL 3
AN 104,106
AN 105,107
M 68
SH 106
SH 107
M 69
mw 27
AN 106,106
AN 107,107
M 70
WL 2
AN 108,110
AN 109,111
M 71
R 78
M 72
SH 110
SH 111
M 73
AN 110,110
AN 111,111
M 74
TY 114,2056
TY 118,15880
M 75
ad 84,54
W 0
M 76
CV 36,6,7
CV 37,8,9
M 77
ae 42,74,0
AR 6,40
M 78
W 1
AR 7,64
M 79
s_bitcmp1_b32 s50, 0
s_cselect_b32 s52, 24576, 0
v_add_u32 v50, s52, v3
ST 30,0
M 80
AR 8,88
AR 9,112
M 81
W 2
EF 6,52
M 82
EF 7,53
EF 8,54
M 83
WL 1
R 80
M 84
mw 48
AN 112,114
AN 113,115
M 85
SH 114
SH 115
M 86
AN 114,114
AN 115,115
M 87
WL 2
AN 116,118
AN 117,119
M 88
SH 118
SH 119
M 89
AN 118,118
AN 119,119
M 90
TY 122,3592
TY 126,17416
M 91
ad 84,96
ST 32,768
M 92
W 0
EF 9,55
M 93
CV 38,6,7
CV 39,8,9
M 94
AR 6,41
AR 7,65
M 95
R 82
M 96
W 1
AR 8,89
M 97
AR 9,113
EF 6,52
M 98
EF 7,53
EF 8,54
M 99
WL 3
AN 120,122
AN 121,123
M 100
SH 122
SH 123
M 101
AN 122,122
AN 123,123
M 102
WL 2
AN 124,126
AN 125,127
M 103
SH 126
SH 127
ST 34,1536
M 104
AN 126,126
AN 127,127
M 105
W 2
EF 9,55
M 106
CV 40,6,7
CV 41,8,9
)"
R"(M 107
R 0
M 108
mw 51
AR 6,42
AR 7,66
M 109
ad 84,102
W 0
M 110
AR 8,90
AR 9,114
M 111
EF 6,52
EF 7,53
M 112
W 1
EF 8,54
M 113
EF 9,55
CV 42,6,7
M 114
CV 43,8,9
AR 6,43
M 115
W 2
ST 36,2304
M 116
AR 7,67
AR 8,91
M 117
AR 9,115
EF 6,52
M 118
bc 49,2,66,60
EF 7,53
M 119
WL 0
R 2
M 120
mh 18
EF 8,54
EF 9,55
M 121
ad 66,416
H 0
M 132
CV 44,6,7
CV 45,8,9
M 133
TX 58,0
TX 62,13824
BV 52,512
M 122
DG 1024
M 123
AR 6,132
AR 7,156
M 134
AR 8,180
EF 6,12
M 135
TY 66,512
TY 70,14336
M 124
bc 48,2,64,56
ae 42,74,0
M 125
mh 21
EF 7,13
EF 8,46
M 136
ad 64,416
H 0
M 137
WL 3
TX 74,1536
TX 78,15360
M 126
s_bfe_u32 s52, s50, 0x10001
Q 0
v_mad_i32_i24 v50, v49, s52, v48
M 127
Q 2
ST 38,0
M 138
Q 4
M 139
TY 82,2048
TY 86,15872
M 128
Q 6
M 129
WL 4
Q 8
M 140
Q 10
M 141
TX 90,3072
TX 94,16896
M 130
Q 12
M 131
R 4
M 142
Q 14
M 143
TY 98,3584
TY 102,17408
v_mfma_f32_16x16x32_f16 a[36:39], v[56:59], a[0:3], a[32:35]
R 6
M 1
WL 10
Q 16
ST 40,768
v_mfma_f32_16x16x32_f16 a[60:63], v[56:59], a[8:11], a[32:35]
Q 18
M 13
Q 20
v_mfma_f32_16x16x32_f16 a[40:43], v[64:67], a[0:3], a[32:35]
Q 22
M 3
WL 8
Q 24
v_mfma_f32_16x16x32_f16 a[64:67], v[64:67], a[8:11], a[32:35]
Q 26
M 15
Q 28
M 4
Q 30
M 5
WL 6
Q 32
M 16
Q 34
M 17
Q 36
M 6
Q 38
M 7
WL 1
Q 40
ST 42,1536
M 18
Q 42
M 19
Q 44
M 8
Q 46
M 9
H 1
EF 136,47
M 20
CV 14,6,7
CV 15,8,136
M 21
AR 6,133
AR 7,157
M 10
H 2
AR 8,181
M 11
R 8
M 22
EF 6,12
EF 7,13
M 23
R 10
PA 12288
DF
v_mfma_f32_16x16x32_f16 a[84:87], v[56:59], a[16:19], a[32:35]
EF 8,46
EF 137,47
M 25
ST 44,2304
CV 16,6,7
v_mfma_f32_16x16x32_f16 a[88:91], v[64:67], a[16:19], a[32:35]
CV 17,8,137
AR 6,134
M 27
AR 7,158
AR 8,182
M 28
EF 6,12
EF 7,13
M 29
EF 8,46
EF 138,47
M 30
CV 18,6,7
CV 19,8,138
M 31
AR 6,135
AR 7,159
M 32
AR 8,183
EF 6,12
M 33
WV 11
EF 7,13
EF 8,46
M 34
WL 0
FP
EF 139,47
M 35
CV 20,6,7
CV 21,8,139
v_mfma_f32_16x16x32_f16 a[108:111], v[56:59], a[24:27], a[32:35]
AR 6,136
AR 7,160
AR 8,184
EF 6,12
M 37
ae 42,74,0
EF 7,13
EF 8,46
EF 140,47
v_mfma_f32_16x16x32_f16 a[112:115], v[64:67], a[24:27], a[32:35]
s_bitcmp1_b32 s50, 0
s_cselect_b32 s52, 24576, 0
v_add_u32 v50, s52, v3
ST 14,128
CV 22,6,7
M 39
CV 23,8,140
AR 6,137
AR 7,161
AR 8,185
M 40
EF 6,12
EF 7,13
EF 8,46
EF 141,47
M 41
CV 24,6,7
CV 25,8,141
AR 6,138
AR 7,162
M 42
AR 8,186
EF 6,12
EF 7,13
EF 8,46
M 43
EF 142,47
CV 26,6,7
CV 27,8,142
AR 6,139
M 44
AR 7,163
AR 8,187
EF 6,12
EF 7,13
M 45
FR
EF 8,46
EF 143,47
CV 28,6,7
M 46
CV 29,8,143
M 47
ds_read_b64 v[12:13], v4 offset:1536
ds_read_b64 v[46:47], v4 offset:1544
v_mfma_f32_16x16x32_f16 a[132:135], v[56:59], a[0:3], a[32:35]
EF 160,52
EF 184,53
M 49
EF 208,54
EF 232,55
v_mfma_f32_16x16x32_f16 a[136:139], v[64:67], a[0:3], a[32:35]
ST 16,896
CV 30,160,184
M 51
CV 31,208,232
EF 161,52
M 52
EF 185,53
EF 209,54
M 53
WL 2
FQ 80
EF 233,55
M 54
CV 32,161,185
CV 33,209,233
M 55
EF 162,52
EF 186,53
M 56
FZ 80
R 24
M 57
mw 72
R 26
M 58
TY 106,5128
TY 110,18952
M 59
R 28
v_mfma_f32_16x16x32_f16 a[156:159], v[56:59], a[8:11], a[32:35]
ad 84,144
W 0
M 61
EF 210,54
EF 234,55
v_mfma_f32_16x16x32_f16 a[160:163], v[64:67], a[8:11], a[32:35]
ST 18,1664
CV 34,162,186
M 63
W 1
CV 35,210,234
M 64
EF 163,52
EF 187,53
M 65
EF 211,54
EF 235,55
M 66
W 2
CV 36,163,187
M 67
WL 3
AN 104,106
AN 105,107
M 68
SH 106
SH 107
M 69
mw 75
AN 106,106
AN 107,107
M 70
WL 2
AN 108,110
AN 109,111
M 71
R 30
v_mfma_f32_16x16x32_f16 a[180:183], v[56:59], a[16:19], a[32:35]
SH 110
SH 111
M 73
AN 110,110
AN 111,111
v_mfma_f32_16x16x32_f16 a[184:187], v[64:67], a[16:19], a[32:35]
TY 114,6664
TY 118,20488
ST 20,2432
M 75
ad 84,150
W 0
M 76
CV 37,211,235
M 77
EF 164,52
EF 188,53
EF 212,54
EF 236,55
M 78
W 1
CV 38,164,188
CV 39,212,236
EF 165,52
M 79
EF 189,53
EF 213,54
EF 237,55
CV 40,165,189
M 80
CV 41,213,237
EF 166,52
EF 190,53
EF 214,54
M 81
W 2
EF 238,55
CV 42,166,190
CV 43,214,238
M 82
EF 167,52
EF 191,53
EF 215,54
EF 239,55
M 83
WL 0
R 32
CV 44,167,191
CV 45,215,239
v_mfma_f32_16x16x32_f16 v[136:139], v[56:59], a[24:27], v[128:131]
mw 0
AN 112,114
AN 113,115
BV 52,1792
M 85
SH 114
SH 115
AR 6,44
v_mfma_f32_16x16x32_f16 v[140:143], v[64:67], a[24:27], v[128:131]
AN 114,114
AN 115,115
)"
R"(AR 7,68
M 87
AN 116,118
AN 117,119
AR 8,92
M 88
SH 118
SH 119
AR 9,116
M 89
AN 118,118
AN 119,119
EF 6,12
M 90
TY 122,8200
TY 126,22024
EF 7,13
M 91
ad 84,192
EF 8,46
M 92
W 0
ae 42,74,0
M 93
s_bfe_u32 s52, s50, 0x10001
v_mad_i32_i24 v50, v49, s52, v48
ST 22,128
EF 9,47
M 94
CV 14,6,7
CV 15,8,9
AR 6,45
M 95
R 34
AR 7,69
v_mfma_f32_16x16x32_f16 v[160:163], v[56:59], a[0:3], v[128:131]
W 1
AR 8,93
AR 9,117
M 97
EF 6,12
EF 7,13
EF 8,46
v_mfma_f32_16x16x32_f16 v[164:167], v[64:67], a[0:3], v[128:131]
EF 9,47
CV 16,6,7
CV 17,8,9
M 99
WL 3
AN 120,122
AN 121,123
AR 6,46
M 100
SH 122
SH 123
AR 7,70
M 101
AN 122,122
AN 123,123
AR 8,94
M 102
WL 2
AN 124,126
AN 125,127
AR 9,118
M 103
SH 126
SH 127
EF 6,12
M 104
AN 126,126
AN 127,127
EF 7,13
M 105
W 2
ST 24,896
EF 8,46
M 106
EF 9,47
CV 18,6,7
CV 19,8,9
M 107
R 48
AR 6,47
v_mfma_f32_16x16x32_f16 v[184:187], v[56:59], a[8:11], v[128:131]
mw 3
AR 7,71
AR 8,95
AR 9,119
M 109
ad 84,198
W 0
EF 6,12
v_mfma_f32_16x16x32_f16 v[188:191], v[64:67], a[8:11], v[128:131]
EF 7,13
EF 8,46
EF 9,47
M 111
CV 20,6,7
CV 21,8,9
M 112
W 1
AR 6,48
AR 7,72
M 113
AR 8,96
AR 9,120
EF 6,12
M 114
EF 7,13
EF 8,46
EF 9,47
M 115
W 2
CV 6,6,7
CV 7,8,9
M 116
AW 200,6
AW 201,7
AR 6,49
M 117
ST 26,1664
AR 7,73
AR 8,97
M 118
bc 49,3,66,60
AR 9,121
EF 6,12
M 119
WL 0
R 50
EF 7,13
v_mfma_f32_16x16x32_f16 v[208:211], v[56:59], a[16:19], v[128:131]
mh 0
EF 8,46
EF 9,47
CV 6,6,7
M 121
ad 66,608
H 0
CV 7,8,9
v_mfma_f32_16x16x32_f16 v[232:235], v[56:59], a[24:27], v[128:131]
AW 202,6
AW 203,7
AR 6,50
M 133
TX 58,4608
TX 62,18432
AR 7,74
v_mfma_f32_16x16x32_f16 v[212:215], v[64:67], a[16:19], v[128:131]
DG 1024
AR 8,98
M 123
AR 9,122
EF 6,12
EF 7,13
v_mfma_f32_16x16x32_f16 v[236:239], v[64:67], a[24:27], v[128:131]
EF 8,46
EF 9,47
CV 6,6,7
M 135
TY 66,5120
TY 70,18944
CV 7,8,9
M 124
bc 48,3,64,56
AW 204,6
AW 205,7
M 125
mh 3
ST 28,2432
AR 6,51
AR 7,75
M 136
ad 64,608
H 0
AR 8,99
M 137
TX 74,6144
TX 78,19968
AR 9,123
M 126
WL 4
Q 0
EF 6,12
M 127
Q 2
EF 7,13
M 138
Q 4
EF 8,46
M 139
TY 82,6656
TY 86,20480
EF 9,47
M 128
Q 6
CV 6,6,7
M 129
WL 4
Q 8
CV 7,8,9
M 140
Q 10
AW 206,6
M 141
TX 90,7680
TX 94,21504
AW 207,7
M 130
Q 12
ds_read_b64 v[12:13], v4 offset:2048
M 131
R 52
ds_read_b64 v[46:47], v4 offset:2056
M 142
Q 14
M 143
TY 98,8192
TY 102,22016
M 0
R 54
M 1
WL 12
Q 16
M 12
Q 18
M 13
Q 20
M 2
Q 22
M 3
WL 10
Q 24
M 14
Q 26
M 15
Q 28
v_mfma_f32_16x16x32_f16 a[44:47], v[72:75], a[0:3], a[32:35]
Q 30
M 5
WL 7
Q 32
v_mfma_f32_16x16x32_f16 a[68:71], v[72:75], a[8:11], a[32:35]
Q 34
M 17
Q 36
v_mfma_f32_16x16x32_f16 a[48:51], v[80:83], a[0:3], a[32:35]
Q 38
M 7
WL 2
Q 40
v_mfma_f32_16x16x32_f16 a[72:75], v[80:83], a[8:11], a[32:35]
Q 42
M 19
Q 44
M 8
Q 46
M 9
H 1
ae 42,74,0
M 20
s_bitcmp1_b32 s50, 0
s_cselect_b32 s52, 24576, 0
v_add_u32 v50, s52, v3
ST 30,256
M 21
AR 6,140
AR 7,164
M 10
H 2
AR 8,188
M 11
R 56
M 22
EF 6,52
EF 7,53
M 23
R 58
M 24
EF 8,54
EF 144,55
M 25
CV 22,6,7
CV 23,8,144
M 26
AR 6,141
AR 7,165
M 27
AR 8,189
EF 6,52
v_mfma_f32_16x16x32_f16 a[92:95], v[72:75], a[16:19], a[32:35]
EF 7,53
EF 8,54
M 29
EF 145,55
CV 24,6,7
v_mfma_f32_16x16x32_f16 a[96:99], v[80:83], a[16:19], a[32:35]
ST 32,1024
CV 25,8,145
M 31
AR 6,142
AR 7,166
M 32
AR 8,190
EF 6,52
M 33
WV 9
EF 7,53
EF 8,54
M 34
WL 0
FP
EF 146,55
M 35
CV 26,6,7
CV 27,8,146
M 36
AR 6,143
AR 7,167
M 37
AR 8,191
EF 6,52
M 38
EF 7,53
EF 8,54
M 39
EF 147,55
CV 28,6,7
v_mfma_f32_16x16x32_f16 a[116:119], v[72:75], a[24:27], a[32:35]
CV 29,8,147
M 41
AR 6,144
AR 7,168
AR 8,192
EF 6,52
v_mfma_f32_16x16x32_f16 a[120:123], v[80:83], a[24:27], a[32:35]
ST 34,1792
EF 7,53
EF 8,54
EF 148,55
M 43
CV 6,6,7
CV 7,8,148
AW 208,6
AW 209,7
M 44
AR 6,145
AR 7,169
AR 8,193
EF 6,52
M 45
FR
EF 7,53
EF 8,54
EF 149,55
M 46
CV 6,6,7
CV 7,8,149
AW 210,6
AW 211,7
M 47
AR 6,146
AR 7,170
AR 8,194
EF 6,52
M 48
EF 7,53
EF 8,54
EF 150,55
CV 6,6,7
M 49
CV 7,8,150
AW 212,6
AW 213,7
AR 6,147
M 50
AR 7,171
AR 8,195
EF 6,52
EF 7,53
M 51
EF 8,54
EF 151,55
CV 6,6,7
CV 7,8,151
v_mfma_f32_16x16x32_f16 a[140:143], v[72:75], a[0:3], a[32:35]
AW 214,6
AW 215,7
M 53
WL 0
FQ 84
BV 52,3072
EF 168,12
v_mfma_f32_16x16x32_f16 a[144:147], v[80:83], a[0:3], a[32:35]
ST 36,2560
EF 192,13
EF 216,46
M 55
EF 240,47
)"
R"(CV 6,168,192
CV 7,216,240
M 56
FZ 84
R 72
AW 216,6
M 57
mw 24
R 74
AW 217,7
M 58
TY 106,9736
TY 110,23560
EF 169,12
M 59
R 76
EF 193,13
M 60
ad 84,240
W 0
EF 217,46
M 61
EF 241,47
CV 6,169,193
CV 7,217,241
M 62
AW 218,6
AW 219,7
EF 170,12
M 63
W 1
EF 194,13
EF 218,46
v_mfma_f32_16x16x32_f16 a[164:167], v[72:75], a[8:11], a[32:35]
EF 242,47
CV 6,170,194
CV 7,218,242
M 65
AW 220,6
AW 221,7
EF 171,12
v_mfma_f32_16x16x32_f16 a[168:171], v[80:83], a[8:11], a[32:35]
WL 3
W 2
ae 42,74,0
M 67
s_bfe_u32 s52, s50, 0x10001
AN 104,106
AN 105,107
v_mad_i32_i24 v50, v49, s52, v48
M 68
SH 106
SH 107
ST 38,256
M 69
mw 27
AN 106,106
AN 107,107
EF 195,13
M 70
WL 0
AN 108,110
AN 109,111
EF 219,46
M 71
R 78
EF 243,47
M 72
SH 110
SH 111
CV 6,171,195
M 73
AN 110,110
AN 111,111
CV 7,219,243
M 74
TY 114,11272
TY 118,25096
AW 222,6
M 75
ad 84,246
W 0
AW 223,7
v_mfma_f32_16x16x32_f16 a[188:191], v[72:75], a[16:19], a[32:35]
EF 172,12
EF 196,13
EF 220,46
M 77
EF 244,47
CV 30,172,196
CV 31,220,244
v_mfma_f32_16x16x32_f16 a[192:195], v[80:83], a[16:19], a[32:35]
W 1
EF 173,12
EF 197,13
M 79
EF 221,46
EF 245,47
CV 32,173,197
M 80
ST 40,1024
CV 33,221,245
EF 174,12
M 81
W 2
EF 198,13
EF 222,46
M 82
EF 246,47
CV 34,174,198
CV 35,222,246
M 83
WL 0
R 80
EF 175,12
M 84
mw 48
AN 112,114
AN 113,115
EF 199,13
M 85
SH 114
SH 115
EF 223,46
M 86
AN 114,114
AN 115,115
EF 247,47
M 87
AN 116,118
AN 117,119
CV 36,175,199
v_mfma_f32_16x16x32_f16 v[144:147], v[72:75], a[24:27], v[128:131]
SH 118
SH 119
CV 37,223,247
M 89
AN 118,118
AN 119,119
ds_read_b64 v[12:13], v4 offset:3328
v_mfma_f32_16x16x32_f16 v[148:151], v[80:83], a[24:27], v[128:131]
TY 122,12808
TY 126,26632
ds_read_b64 v[46:47], v4 offset:3336
M 91
ad 84,288
AR 6,52
M 92
W 0
ST 42,1792
AR 7,76
M 93
AR 8,100
AR 9,124
EF 6,52
M 94
EF 7,53
EF 8,54
EF 9,55
M 95
R 82
CV 6,6,7
M 96
W 1
CV 7,8,9
AW 224,6
M 97
AW 225,7
AR 6,53
AR 7,77
M 98
AR 8,101
AR 9,125
EF 6,52
M 99
WL 4
AN 120,122
AN 121,123
EF 7,53
v_mfma_f32_16x16x32_f16 v[168:171], v[72:75], a[0:3], v[128:131]
SH 122
SH 123
EF 8,54
M 101
AN 122,122
AN 123,123
EF 9,55
v_mfma_f32_16x16x32_f16 v[172:175], v[80:83], a[0:3], v[128:131]
WL 2
AN 124,126
AN 125,127
CV 6,6,7
M 103
SH 126
SH 127
CV 7,8,9
M 104
AN 126,126
AN 127,127
ST 44,2560
M 105
W 2
AW 226,6
AW 227,7
M 106
AR 6,54
AR 7,78
AR 8,102
M 107
R 0
AR 9,126
M 108
mw 51
EF 6,52
EF 7,53
EF 8,54
M 109
ad 84,294
W 0
EF 9,55
M 110
CV 6,6,7
CV 7,8,9
AW 228,6
M 111
AW 229,7
AR 6,55
AR 7,79
v_mfma_f32_16x16x32_f16 v[192:195], v[72:75], a[8:11], v[128:131]
W 1
AR 8,103
AR 9,127
M 113
EF 6,52
EF 7,53
EF 8,54
v_mfma_f32_16x16x32_f16 v[196:199], v[80:83], a[8:11], v[128:131]
EF 9,55
CV 6,6,7
CV 7,8,9
M 115
W 2
AW 230,6
AW 231,7
M 116
ae 42,72,0
AR 6,56
AR 7,80
M 117
s_bitcmp1_b32 s50, 2
s_cselect_b32 s52, 24576, 0
v_add_u32 v50, s52, v3
ST 14,0
M 118
bc 49,4,66,60
AR 8,104
AR 9,128
M 119
WL 0
R 2
EF 6,52
M 120
mh 9
EF 7,53
EF 8,54
EF 9,55
M 121
ad 66,800
H 0
CV 38,6,7
M 132
CV 39,8,9
AR 6,57
AR 7,81
M 133
TX 58,9216
TX 62,23040
AR 8,105
M 122
DG 1024
AR 9,129
M 123
EF 6,52
EF 7,53
EF 8,54
M 134
EF 9,55
CV 40,6,7
CV 41,8,9
M 135
TY 66,9728
TY 70,23552
AR 6,58
v_mfma_f32_16x16x32_f16 v[216:219], v[72:75], a[16:19], v[128:131]
bc 48,4,64,56
AR 7,82
AR 8,106
M 125
mh 12
ST 16,768
AR 9,130
EF 6,52
v_mfma_f32_16x16x32_f16 v[240:243], v[72:75], a[24:27], v[128:131]
ad 64,800
H 0
EF 7,53
M 137
TX 74,10752
TX 78,24576
EF 8,54
v_mfma_f32_16x16x32_f16 v[220:223], v[80:83], a[16:19], v[128:131]
WL 4
Q 0
EF 9,55
M 127
Q 2
CV 42,6,7
v_mfma_f32_16x16x32_f16 v[244:247], v[80:83], a[24:27], v[128:131]
Q 4
CV 43,8,9
M 139
TY 82,11264
TY 86,25088
AR 6,59
M 128
Q 6
AR 7,83
M 129
WL 4
Q 8
AR 8,107
M 140
Q 10
AR 9,131
M 141
TX 90,12288
TX 94,26112
EF 6,52
M 130
Q 12
EF 7,53
M 131
R 4
ST 18,1536
M 142
Q 14
EF 8,54
M 143
TY 98,12800
TY 102,26624
EF 9,55
M 0
R 6
CV 44,6,7
M 1
WL 8
Q 16
CV 45,8,9
M 12
Q 18
BV 52,3584
M 13
Q 20
M 2
Q 22
M 3
Q 24
M 14
Q 26
M 15
Q 28
M 4
Q 30
M 5
WL 5
Q 32
ST 20,2304
M 16
Q 34
M 17
Q 36
M 6
Q 38
M 7
WL 1
Q 40
M 18
Q 42
M 19
Q 44
v_mfma_f32_16x16x32_f16 a[52:55], v[88:91], a[0:3], a[32:35]
Q 46
M 9
H 1
AR 6,148
v_mfma_f32_16x16x32_f16 a[76:79], v[88:91], a[8:11], a[32:35]
AR 7,172
AR 8,196
M 21
EF 6,12
)"
R"(EF 7,13
v_mfma_f32_16x16x32_f16 a[56:59], v[96:99], a[0:3], a[32:35]
H 2
EF 8,46
M 11
R 8
v_mfma_f32_16x16x32_f16 a[80:83], v[96:99], a[8:11], a[32:35]
ae 42,72,0
ST 22,128
M 23
R 10
PA 12288
DF
M 24
EF 152,47
CV 6,6,7
M 25
CV 7,8,152
AW 232,6
M 26
AW 233,7
AR 6,149
M 27
AR 7,173
AR 8,197
M 28
EF 6,12
EF 7,13
M 29
EF 8,46
EF 153,47
M 30
CV 6,6,7
CV 7,8,153
M 31
AW 234,6
AW 235,7
v_mfma_f32_16x16x32_f16 a[100:103], v[88:91], a[16:19], a[32:35]
AR 6,150
AR 7,174
M 33
WV 11
AR 8,198
EF 6,12
v_mfma_f32_16x16x32_f16 a[104:107], v[96:99], a[16:19], a[32:35]
WL 0
FP
ST 24,896
M 35
EF 7,13
EF 8,46
M 36
EF 154,47
CV 6,6,7
M 37
CV 7,8,154
AW 236,6
M 38
AW 237,7
AR 6,151
M 39
AR 7,175
AR 8,199
M 40
EF 6,12
EF 7,13
M 41
EF 8,46
EF 155,47
M 42
CV 6,6,7
CV 7,8,155
M 43
AW 238,6
AW 239,7
v_mfma_f32_16x16x32_f16 a[124:127], v[88:91], a[24:27], a[32:35]
AR 6,152
AR 7,176
EF 6,12
M 45
FR
EF 7,13
EF 132,46
v_mfma_f32_16x16x32_f16 a[128:131], v[96:99], a[24:27], a[32:35]
ST 26,1664
EF 156,47
CV 14,6,7
M 47
CV 15,132,156
AR 6,153
AR 7,177
M 48
EF 6,12
EF 7,13
EF 133,46
M 49
EF 157,47
CV 16,6,7
CV 17,133,157
M 50
AR 6,154
AR 7,178
EF 6,12
M 51
EF 7,13
EF 134,46
EF 158,47
M 52
CV 18,6,7
CV 19,134,158
AR 6,155
M 53
WL 0
FQ 88
AR 7,179
EF 6,12
M 54
EF 7,13
EF 135,46
EF 159,47
M 55
CV 20,6,7
CV 21,135,159
v_mfma_f32_16x16x32_f16 a[148:151], v[88:91], a[0:3], a[32:35]
FZ 88
R 24
EF 176,52
M 57
mw 72
R 26
EF 200,53
v_mfma_f32_16x16x32_f16 a[152:155], v[96:99], a[0:3], a[32:35]
TY 106,520
TY 110,14344
ST 28,2432
M 59
R 28
EF 224,54
M 60
ad 84,336
W 0
EF 248,55
M 61
CV 6,176,200
CV 7,224,248
AW 240,6
M 62
AW 241,7
EF 177,52
EF 201,53
M 63
W 1
EF 225,54
EF 249,55
M 64
CV 6,177,201
CV 7,225,249
AW 242,6
M 65
AW 243,7
EF 178,52
EF 202,53
M 66
W 2
EF 226,54
EF 250,55
M 67
WL 3
AN 104,106
AN 105,107
CV 6,178,202
v_mfma_f32_16x16x32_f16 a[172:175], v[88:91], a[8:11], a[32:35]
SH 106
SH 107
CV 7,226,250
M 69
mw 75
AN 106,106
AN 107,107
AW 244,6
v_mfma_f32_16x16x32_f16 a[176:179], v[96:99], a[8:11], a[32:35]
WL 0
AN 108,110
AN 109,111
AW 245,7
M 71
R 30
EF 179,52
M 72
SH 110
SH 111
EF 203,53
M 73
AN 110,110
AN 111,111
EF 227,54
M 74
TY 114,2056
TY 118,15880
EF 251,55
M 75
ad 84,342
W 0
CV 6,179,203
M 76
ae 42,72,0
CV 7,227,251
AW 246,6
M 77
s_bfe_u32 s52, s50, 0x10003
v_mad_i32_i24 v50, v49, s52, v48
ST 30,256
AW 247,7
M 78
W 1
EF 180,52
EF 204,53
M 79
EF 228,54
EF 252,55
CV 6,180,204
v_mfma_f32_16x16x32_f16 a[196:199], v[88:91], a[16:19], a[32:35]
CV 7,228,252
AW 248,6
AW 249,7
M 81
W 2
EF 181,52
EF 205,53
v_mfma_f32_16x16x32_f16 v[132:135], v[96:99], a[16:19], v[128:131]
EF 229,54
EF 253,55
CV 6,181,205
M 83
WL 0
R 32
CV 7,229,253
M 84
mw 0
AN 112,114
AN 113,115
AW 250,6
M 85
SH 114
SH 115
AW 251,7
M 86
AN 114,114
AN 115,115
EF 182,52
M 87
AN 116,118
AN 117,119
EF 206,53
M 88
SH 118
SH 119
EF 230,54
M 89
AN 118,118
AN 119,119
ST 32,1024
M 90
TY 122,3592
TY 126,17416
EF 254,55
M 91
ad 84,384
CV 6,182,206
v_mfma_f32_16x16x32_f16 v[152:155], v[88:91], a[24:27], v[128:131]
W 0
CV 7,230,254
AW 252,6
M 93
AW 253,7
EF 183,52
EF 207,53
v_mfma_f32_16x16x32_f16 v[156:159], v[96:99], a[24:27], v[128:131]
EF 231,54
EF 255,55
CV 6,183,207
M 95
R 34
CV 7,231,255
M 96
W 1
AW 254,6
AW 255,7
M 97
M 98
M 99
WL 3
AN 120,122
AN 121,123
M 100
SH 122
SH 123
M 101
AN 122,122
AN 123,123
ST 34,1792
M 102
WL 2
AN 124,126
AN 125,127
M 103
SH 126
SH 127
v_mfma_f32_16x16x32_f16 v[176:179], v[88:91], a[0:3], v[128:131]
AN 126,126
AN 127,127
M 105
W 2
v_mfma_f32_16x16x32_f16 v[180:183], v[96:99], a[0:3], v[128:131]
M 107
R 48
M 108
mw 3
M 109
ad 84,390
W 0
M 110
M 111
M 112
W 1
M 113
ST 36,2560
M 114
M 115
W 2
v_mfma_f32_16x16x32_f16 v[200:203], v[88:91], a[8:11], v[128:131]
M 117
v_mfma_f32_16x16x32_f16 v[204:207], v[96:99], a[8:11], v[128:131]
bc 49,5,66,60
M 119
WL 2
R 50
M 120
mh 18
M 121
ad 66,992
H 0
M 132
M 133
TX 58,0
TX 62,13824
M 122
DG 1024
M 123
ae 42,72,96
M 134
s_bfe_u32 s52, s50, 0x10005
v_mad_i32_i24 v50, v49, s52, v48
ST 38,0
M 135
TY 66,512
TY 70,14336
M 124
bc 48,5,64,56
M 125
mh 21
M 136
ad 64,992
H 0
M 137
TX 74,1536
TX 78,15360
M 126
WL 4
Q 0
M 127
Q 2
M 138
Q 4
M 139
TY 82,2048
TY 86,15872
v_mfma_f32_16x16x32_f16 v[224:227], v[88:91], a[16:19], v[128:131]
Q 6
M 129
WL 4
Q 8
v_mfma_f32_16x16x32_f16 v[248:251], v[88:91], a[24:27], v[128:131]
Q 10
ST 40,768
M 141
TX 90,3072
TX 94,16896
v_mfma_f32_16x16x32_f16 v[228:231], v[96:99], a[16:19], v[128:131]
Q 12
M 131
R 52
v_mfma_f32_16x16x32_f16 v[252:255], v[96:99], a[24:27], v[128:131]
Q 14
M 143
TY 98,3584
TY 102,17408
M 0
R 54
M 1
)"
R"(WL 10
Q 16
M 12
Q 18
M 13
Q 20
M 2
Q 22
M 3
WL 8
Q 24
M 14
Q 26
ST 42,1536
M 15
Q 28
M 4
Q 30
M 5
WL 6
Q 32
M 16
Q 34
M 17
Q 36
M 6
Q 38
M 7
WL 1
Q 40
M 18
Q 42
M 19
Q 44
M 8
Q 46
M 9
H 1
M 20
ST 44,2304
M 21
M 10
H 2
M 11
R 56
M 22
M 23
R 58
M 24
M 25
M 26
M 27
M 28
M 29
M 30
ae 42,72,96
ST 14,128
M 31
M 32
M 33
WV 10
M 34
WL 0
FP
M 35
M 36
M 37
M 38
M 39
M 40
M 41
M 42
ST 16,896
M 43
M 44
M 45
FR
M 46
M 47
M 48
M 49
M 50
M 51
M 52
M 53
WL 0
FQ 92
M 54
ST 18,1664
M 55
M 56
FZ 92
R 72
M 57
mw 24
R 74
M 58
TY 106,5128
TY 110,18952
M 59
R 76
M 60
ad 84,432
W 0
M 61
M 62
M 63
W 1
M 64
M 65
M 66
W 2
ST 20,2432
M 67
WL 3
AN 104,106
AN 105,107
M 68
SH 106
SH 107
M 69
mw 27
AN 106,106
AN 107,107
M 70
WL 2
AN 108,110
AN 109,111
M 71
R 78
M 72
SH 110
SH 111
M 73
AN 110,110
AN 111,111
M 74
TY 114,6664
TY 118,20488
M 75
ad 84,438
W 0
M 76
M 77
M 78
W 1
M 79
M 80
M 81
W 2
M 82
M 83
R 80
M 84
mw 48
WL 3
AN 112,114
AN 113,115
M 85
SH 114
SH 115
M 86
AN 114,114
AN 115,115
M 87
WL 2
AN 116,118
AN 117,119
M 88
SH 118
SH 119
M 89
AN 118,118
AN 119,119
M 90
TY 122,8200
TY 126,22024
M 91
ad 84,480
M 92
W 0
M 93
M 94
M 95
R 82
M 96
W 1
M 97
M 98
M 99
WL 3
AN 120,122
AN 121,123
M 100
SH 122
SH 123
M 101
AN 122,122
AN 123,123
M 102
WL 2
AN 124,126
AN 125,127
M 103
SH 126
SH 127
M 104
AN 126,126
AN 127,127
M 105
W 2
M 106
M 107
R 0
M 108
mw 51
M 109
ad 84,486
W 0
M 110
M 111
M 112
W 1
M 113
M 114
M 115
W 2
M 116
M 117
M 118
bc 49,6,66,60
M 119
WL 2
R 2
M 120
mh 0
M 121
ad 66,1184
H 0
M 132
M 133
TX 58,4608
TX 62,18432
M 122
DG 1024
M 123
M 134
M 135
TY 66,5120
TY 70,18944
M 124
bc 48,6,64,56
M 125
mh 3
ae 44,70,384
M 136
ad 64,1184
H 0
M 137
TX 74,6144
TX 78,19968
M 126
WL 4
Q 0
M 127
Q 2
M 138
Q 4
M 139
TY 82,6656
TY 86,20480
M 128
Q 6
M 129
WL 4
Q 8
M 140
Q 10
M 141
TX 90,7680
TX 94,21504
M 130
Q 12
M 131
R 4
M 142
Q 14
M 143
TY 98,8192
TY 102,22016
M 0
R 6
M 1
WL 10
Q 16
M 12
Q 18
M 13
Q 20
M 2
Q 22
M 3
WL 8
Q 24
M 14
Q 26
M 15
Q 28
M 4
Q 30
M 5
WL 6
Q 32
M 16
Q 34
M 17
Q 36
M 6
Q 38
M 7
WL 2
Q 40
M 18
Q 42
M 19
Q 44
M 8
Q 46
M 9
H 1
M 20
ae 42,72,0
M 21
s_bfe_u32 s52, s50, 0x10003
v_mad_i32_i24 v50, v49, s52, v48
SA 200,0
M 10
H 2
M 11
R 8
M 22
s_bitcmp1_b32 s50, 6
s_cselect_b32 s51, 24576, 0
M 23
R 10
PA 12288
DF
M 24
v_add_u32 v47, s51, v3
XL 22,0
M 25
XL 24,768
M 26
XL 26,1536
M 27
XL 28,2304
M 28
M 29
M 30
M 31
M 32
M 33
WV 11
M 34
WL 0
FP
M 35
M 36
M 37
M 38
M 39
M 40
M 41
M 42
M 43
M 44
M 45
FR
M 46
M 47
M 48
M 49
M 50
M 51
M 52
M 53
WL 0
FQ 96
M 54
M 55
M 56
FZ 96
R 24
M 57
mw 72
R 26
M 58
TY 106,9736
TY 110,23560
M 59
)"
R"(R 28
M 60
ad 84,528
W 0
M 61
M 62
M 63
W 1
M 64
M 65
M 66
W 2
M 67
WL 3
AN 104,106
AN 105,107
M 68
SH 106
SH 107
M 69
mw 75
AN 106,106
AN 107,107
M 70
WL 2
AN 108,110
AN 109,111
M 71
R 30
M 72
SH 110
SH 111
M 73
AN 110,110
AN 111,111
M 74
TY 114,11272
TY 118,25096
M 75
ad 84,534
W 0
M 76
M 77
M 78
W 1
M 79
M 80
M 81
W 2
M 82
M 83
R 32
M 84
mw 0
WL 3
AN 112,114
AN 113,115
M 85
SH 114
SH 115
M 86
AN 114,114
AN 115,115
M 87
WL 2
AN 116,118
AN 117,119
M 88
SH 118
SH 119
M 89
AN 118,118
AN 119,119
M 90
TY 122,12808
TY 126,26632
M 91
ad 84,576
M 92
W 0
SA 202,768
M 93
M 94
M 95
R 34
M 96
W 1
M 97
M 98
M 99
WL 3
AN 120,122
AN 121,123
M 100
SH 122
SH 123
M 101
AN 122,122
AN 123,123
M 102
WL 2
AN 124,126
AN 125,127
M 103
SH 126
SH 127
M 104
AN 126,126
AN 127,127
M 105
W 2
M 106
M 107
R 48
M 108
mw 3
M 109
ad 84,582
W 0
M 110
M 111
M 112
W 1
M 113
M 114
M 115
W 2
M 116
M 117
M 118
bc 49,7,66,60
M 119
WL 2
R 50
M 120
mh 9
M 121
ad 66,1376
H 0
M 132
M 133
TX 58,9216
TX 62,23040
M 122
DG 1024
M 123
M 134
M 135
TY 66,9728
TY 70,23552
M 124
bc 48,7,64,56
M 125
mh 12
M 136
ad 64,1376
H 0
M 137
TX 74,10752
TX 78,24576
M 126
WL 4
Q 0
M 127
Q 2
M 138
Q 4
M 139
TY 82,11264
TY 86,25088
M 128
Q 6
M 129
WL 4
Q 8
M 140
Q 10
M 141
TX 90,12288
TX 94,26112
M 130
Q 12
M 131
R 52
M 142
Q 14
M 143
TY 98,12800
TY 102,26624
M 0
R 54
M 1
WL 10
Q 16
M 12
Q 18
M 13
Q 20
M 2
Q 22
M 3
WL 8
Q 24
M 14
Q 26
M 15
Q 28
M 4
Q 30
M 5
WL 6
Q 32
M 16
Q 34
M 17
Q 36
M 6
Q 38
M 7
WL 2
Q 40
M 18
Q 42
M 19
Q 44
M 8
Q 46
M 9
H 1
SA 204,1536
M 20
ae 44,70,384
M 21
s_bfe_u32 s51, s50, 0x10007
v_mad_i32_i24 v47, v49, s51, v48
XL 30,0
M 10
H 2
XL 32,768
M 11
R 56
M 22
XL 34,1536
M 23
R 58
M 24
XL 36,2304
M 25
M 26
M 27
M 28
M 29
M 30
M 31
M 32
M 33
WV 10
M 34
WL 0
FP
M 35
M 36
M 37
M 38
M 39
M 40
UL 6,22
UH 7,22
M 41
AR 8,36
AR 9,60
M 42
AD 8,6
AD 9,7
M 43
AW 36,8
AW 60,9
M 44
UL 6,23
UH 7,23
M 45
FR
AR 8,84
M 46
AR 9,108
AD 8,6
M 47
AD 9,7
AW 84,8
M 48
AW 108,9
UL 6,24
M 49
UH 7,24
AR 8,37
M 50
AR 9,61
AD 8,6
M 51
AD 9,7
AW 37,8
M 52
AW 61,9
UL 6,25
M 53
WL 0
FQ 100
UH 7,25
M 54
AR 8,85
AR 9,109
M 55
AD 8,6
AD 9,7
M 56
FZ 100
R 72
M 57
mw 24
R 74
M 58
TY 106,520
TY 110,14344
M 59
R 76
M 60
ad 84,624
W 0
M 61
AW 85,8
AW 109,9
M 62
UL 6,26
UH 7,26
M 63
W 1
AR 8,38
M 64
AR 9,62
AD 8,6
M 65
AD 9,7
AW 38,8
M 66
W 2
AW 62,9
M 67
WL 3
AN 104,106
AN 105,107
M 68
SH 106
SH 107
M 69
mw 27
AN 106,106
AN 107,107
M 70
WL 2
AN 108,110
AN 109,111
M 71
R 78
M 72
SH 110
SH 111
M 73
AN 110,110
AN 111,111
M 74
TY 114,2056
TY 118,15880
M 75
ad 84,630
W 0
M 76
UL 6,27
UH 7,27
M 77
AR 8,86
AR 9,110
M 78
W 1
AD 8,6
M 79
AD 9,7
AW 86,8
M 80
AW 110,9
UL 6,28
M 81
W 2
SA 206,2304
M 82
UH 7,28
AR 8,39
M 83
WL 1
R 80
M 84
)"
R"(mw 48
AN 112,114
AN 113,115
M 85
SH 114
SH 115
M 86
AN 114,114
AN 115,115
M 87
WL 2
AN 116,118
AN 117,119
M 88
SH 118
SH 119
M 89
AN 118,118
AN 119,119
M 90
TY 122,3592
TY 126,17416
M 91
ad 84,672
M 92
W 0
AR 9,63
M 93
AD 8,6
AD 9,7
M 94
AW 39,8
AW 63,9
M 95
R 82
M 96
W 1
UL 6,29
M 97
UH 7,29
AR 8,87
M 98
AR 9,111
AD 8,6
M 99
WL 3
AN 120,122
AN 121,123
M 100
SH 122
SH 123
M 101
AN 122,122
AN 123,123
M 102
WL 2
AN 124,126
AN 125,127
M 103
SH 126
SH 127
M 104
AN 126,126
AN 127,127
M 105
W 2
AD 9,7
M 106
AW 87,8
AW 111,9
M 107
R 0
M 108
mw 51
M 109
ad 84,678
W 0
M 110
M 111
M 112
W 1
M 113
M 114
M 115
W 2
M 116
M 117
M 118
bc 49,8,66,60
M 119
WL 2
R 2
M 120
mh 18
M 121
ad 66,1568
H 0
M 132
M 133
TX 58,0
TX 62,13824
M 122
DG 1024
M 123
M 134
M 135
TY 66,512
TY 70,14336
M 124
bc 48,8,64,56
M 125
mh 21
M 136
ad 64,1568
H 0
M 137
TX 74,1536
TX 78,15360
M 126
WL 4
Q 0
M 127
Q 2
M 138
Q 4
M 139
TY 82,2048
TY 86,15872
M 128
Q 6
M 129
WL 4
Q 8
M 140
Q 10
M 141
TX 90,3072
TX 94,16896
M 130
Q 12
M 131
R 4
M 142
Q 14
M 143
TY 98,3584
TY 102,17408
M 0
R 6
M 1
WL 10
Q 16
M 12
Q 18
M 13
Q 20
M 2
Q 22
M 3
WL 8
Q 24
M 14
Q 26
M 15
Q 28
M 4
Q 30
M 5
WL 6
Q 32
M 16
Q 34
M 17
Q 36
M 6
Q 38
M 7
WL 2
Q 40
M 18
Q 42
M 19
Q 44
M 8
Q 46
M 9
H 1
M 20
ae 42,72,0
M 21
SA 208,128
M 10
H 2
M 11
R 8
M 22
ae 44,68,384
M 23
R 10
PA 12288
DF
M 24
s_bitcmp1_b32 s50, 8
s_cselect_b32 s51, 24576, 0
v_add_u32 v47, s51, v3
M 25
XL 38,0
M 26
XL 40,768
M 27
XL 42,1536
M 28
XL 44,2304
M 29
M 30
M 31
M 32
M 33
WV 11
M 34
WL 0
FP
M 35
M 36
M 37
M 38
M 39
M 40
M 41
M 42
UL 6,30
UH 7,30
M 43
AR 8,40
AR 9,64
M 44
AD 8,6
AD 9,7
M 45
FR
AW 40,8
M 46
AW 64,9
UL 6,31
M 47
UH 7,31
AR 8,88
M 48
AR 9,112
AD 8,6
M 49
AD 9,7
AW 88,8
M 50
AW 112,9
UL 6,32
M 51
UH 7,32
AR 8,41
M 52
AR 9,65
AD 8,6
M 53
WL 0
FQ 104
AD 9,7
M 54
AW 41,8
AW 65,9
M 55
UL 6,33
UH 7,33
M 56
FZ 104
R 24
M 57
mw 72
R 26
M 58
TY 106,5128
TY 110,18952
M 59
R 28
M 60
ad 84,720
W 0
M 61
AR 8,89
AR 9,113
M 62
AD 8,6
AD 9,7
M 63
W 1
AW 89,8
M 64
AW 113,9
UL 6,34
M 65
UH 7,34
AR 8,42
M 66
W 2
AR 9,66
M 67
WL 3
AN 104,106
AN 105,107
M 68
SH 106
SH 107
M 69
mw 75
AN 106,106
AN 107,107
M 70
WL 2
AN 108,110
AN 109,111
M 71
R 30
M 72
SH 110
SH 111
M 73
AN 110,110
AN 111,111
M 74
TY 114,6664
TY 118,20488
M 75
ad 84,726
W 0
M 76
AD 8,6
AD 9,7
M 77
AW 42,8
AW 66,9
M 78
W 1
UL 6,35
M 79
UH 7,35
AR 8,90
M 80
AR 9,114
AD 8,6
M 81
W 2
AD 9,7
M 82
AW 90,8
AW 114,9
M 83
WL 1
R 32
M 84
mw 0
AN 112,114
AN 113,115
M 85
SH 114
SH 115
M 86
AN 114,114
AN 115,115
M 87
WL 2
AN 116,118
AN 117,119
M 88
SH 118
SH 119
M 89
AN 118,118
AN 119,119
M 90
TY 122,8200
TY 126,22024
M 91
ad 84,768
M 92
W 0
SA 210,896
M 93
UL 6,36
UH 7,36
M 94
AR 8,43
AR 9,67
M 95
R 34
M 96
W 1
AD 8,6
M 97
AD 9,7
AW 43,8
M 98
)"
R"(AW 67,9
UL 6,37
M 99
WL 3
AN 120,122
AN 121,123
M 100
SH 122
SH 123
M 101
AN 122,122
AN 123,123
M 102
WL 2
AN 124,126
AN 125,127
M 103
SH 126
SH 127
M 104
AN 126,126
AN 127,127
M 105
W 2
UH 7,37
M 106
AR 8,91
AR 9,115
M 107
R 48
M 108
mw 3
AD 8,6
AD 9,7
M 109
ad 84,774
W 0
M 110
AW 91,8
AW 115,9
M 111
M 112
W 1
M 113
M 114
M 115
W 2
M 116
M 117
M 118
bc 49,9,66,60
M 119
WL 2
R 50
M 120
mh 0
M 121
ad 66,1760
H 0
M 132
M 133
TX 58,4608
TX 62,18432
M 122
DG 1024
M 123
M 134
M 135
TY 66,5120
TY 70,18944
M 124
bc 48,9,64,56
M 125
mh 3
M 136
ad 64,1760
H 0
M 137
TX 74,6144
TX 78,19968
M 126
WL 4
Q 0
M 127
Q 2
M 138
Q 4
M 139
TY 82,6656
TY 86,20480
M 128
Q 6
M 129
WL 4
Q 8
M 140
Q 10
M 141
TX 90,7680
TX 94,21504
M 130
Q 12
M 131
R 52
M 142
Q 14
M 143
TY 98,8192
TY 102,22016
M 0
R 54
M 1
WL 10
Q 16
M 12
Q 18
M 13
Q 20
M 2
Q 22
M 3
WL 8
Q 24
M 14
Q 26
M 15
Q 28
M 4
Q 30
M 5
WL 6
Q 32
M 16
Q 34
M 17
Q 36
M 6
Q 38
M 7
WL 2
Q 40
M 18
Q 42
M 19
Q 44
M 8
Q 46
M 9
H 1
SA 212,1664
M 20
ae 44,68,384
M 21
s_bfe_u32 s51, s50, 0x10009
v_mad_i32_i24 v47, v49, s51, v48
XL 14,0
M 10
H 2
XL 16,768
M 11
R 56
M 22
XL 18,1536
M 23
R 58
M 24
XL 20,2304
M 25
M 26
M 27
M 28
M 29
M 30
M 31
M 32
M 33
WV 10
M 34
WL 0
FP
M 35
M 36
M 37
M 38
M 39
M 40
M 41
M 42
M 43
M 44
UL 6,38
UH 7,38
M 45
FR
AR 8,44
M 46
AR 9,68
AD 8,6
M 47
AD 9,7
AW 44,8
M 48
AW 68,9
UL 6,39
M 49
UH 7,39
AR 8,92
M 50
AR 9,116
AD 8,6
M 51
AD 9,7
AW 92,8
M 52
AW 116,9
UL 6,40
M 53
WL 0
FQ 108
UH 7,40
M 54
AR 8,45
AR 9,69
M 55
AD 8,6
AD 9,7
M 56
FZ 108
R 72
M 57
mw 24
R 74
M 58
TY 106,9736
TY 110,23560
M 59
R 76
M 60
ad 84,816
W 0
M 61
AW 45,8
AW 69,9
M 62
UL 6,41
UH 7,41
M 63
W 1
AR 8,93
M 64
AR 9,117
AD 8,6
M 65
AD 9,7
AW 93,8
M 66
W 2
AW 117,9
M 67
WL 3
AN 104,106
AN 105,107
M 68
SH 106
SH 107
M 69
mw 27
AN 106,106
AN 107,107
M 70
WL 2
AN 108,110
AN 109,111
M 71
R 78
M 72
SH 110
SH 111
M 73
AN 110,110
AN 111,111
M 74
TY 114,11272
TY 118,25096
M 75
ad 84,822
W 0
M 76
UL 6,42
UH 7,42
M 77
AR 8,46
AR 9,70
M 78
W 1
AD 8,6
M 79
AD 9,7
AW 46,8
M 80
AW 70,9
UL 6,43
M 81
W 2
SA 214,2432
M 82
UH 7,43
AR 8,94
M 83
WL 1
R 80
M 84
mw 48
AN 112,114
AN 113,115
M 85
SH 114
SH 115
M 86
AN 114,114
AN 115,115
M 87
WL 2
AN 116,118
AN 117,119
M 88
SH 118
SH 119
M 89
AN 118,118
AN 119,119
M 90
TY 122,12808
TY 126,26632
M 91
ad 84,864
M 92
W 0
AR 9,118
M 93
AD 8,6
AD 9,7
M 94
AW 94,8
AW 118,9
M 95
R 82
M 96
W 1
UL 6,44
M 97
UH 7,44
AR 8,47
M 98
AR 9,71
AD 8,6
M 99
WL 3
AN 120,122
AN 121,123
M 100
SH 122
SH 123
M 101
AN 122,122
AN 123,123
M 102
WL 2
AN 124,126
AN 125,127
M 103
SH 126
SH 127
M 104
AN 126,126
AN 127,127
M 105
W 2
AD 9,7
M 106
AW 47,8
AW 71,9
M 107
R 0
M 108
mw 51
UL 6,45
UH 7,45
M 109
ad 84,870
W 0
M 110
AR 8,95
AR 9,119
M 111
AD 8,6
AD 9,7
M 112
W 1
AW 95,8
)"
R"(M 113
AW 119,9
M 114
M 115
W 2
M 116
M 117
M 118
bc 49,10,66,60
M 119
WL 2
R 2
M 120
mh 9
M 121
ad 66,1952
H 0
M 132
M 133
TX 58,9216
TX 62,23040
M 122
DG 1024
M 123
M 134
M 135
TY 66,9728
TY 70,23552
M 124
bc 48,10,64,56
M 125
mh 12
M 136
ad 64,1952
H 0
M 137
TX 74,10752
TX 78,24576
M 126
WL 4
Q 0
M 127
Q 2
M 138
Q 4
M 139
TY 82,11264
TY 86,25088
M 128
Q 6
M 129
WL 4
Q 8
M 140
Q 10
M 141
TX 90,12288
TX 94,26112
M 130
Q 12
M 131
R 4
M 142
Q 14
M 143
TY 98,12800
TY 102,26624
M 0
R 6
M 1
WL 10
Q 16
M 12
Q 18
M 13
Q 20
M 2
Q 22
M 3
WL 8
Q 24
M 14
Q 26
M 15
Q 28
M 4
Q 30
M 5
WL 6
Q 32
M 16
Q 34
M 17
Q 36
M 6
Q 38
M 7
WL 2
Q 40
M 18
Q 42
M 19
Q 44
M 8
Q 46
M 9
H 1
M 20
ae 42,72,0
M 21
s_bitcmp1_b32 s50, 2
s_cselect_b32 s52, 24576, 0
v_add_u32 v50, s52, v3
M 10
H 2
SA 216,256
M 11
R 8
M 22
ae 44,68,480
M 23
R 10
PA 12288
DF
M 24
s_bitcmp1_b32 s50, 10
s_cselect_b32 s51, 24576, 0
v_add_u32 v47, s51, v3
M 25
XL 22,0
M 26
XL 24,768
M 27
XL 26,1536
M 28
XL 28,2304
M 29
M 30
M 31
M 32
M 33
WV 11
M 34
WL 0
FP
M 35
M 36
M 37
M 38
M 39
M 40
M 41
M 42
M 43
M 44
M 45
FR
M 46
UL 6,14
UH 7,14
M 47
AR 8,48
AR 9,72
M 48
AD 8,6
AD 9,7
M 49
AW 48,8
AW 72,9
M 50
UL 6,15
UH 7,15
M 51
AR 8,96
AR 9,120
M 52
AD 8,6
AD 9,7
M 53
WL 0
FQ 112
AW 96,8
M 54
AW 120,9
UL 6,16
M 55
UH 7,16
AR 8,49
M 56
FZ 112
R 24
M 57
mw 72
R 26
M 58
TY 106,520
TY 110,14344
M 59
R 28
M 60
ad 84,912
W 0
M 61
AR 9,73
AD 8,6
M 62
AD 9,7
AW 49,8
M 63
W 1
AW 73,9
M 64
UL 6,17
UH 7,17
M 65
AR 8,97
AR 9,121
M 66
W 2
AD 8,6
M 67
WL 3
AN 104,106
AN 105,107
M 68
SH 106
SH 107
M 69
mw 75
AN 106,106
AN 107,107
M 70
WL 2
AN 108,110
AN 109,111
M 71
R 30
M 72
SH 110
SH 111
M 73
AN 110,110
AN 111,111
M 74
TY 114,2056
TY 118,15880
M 75
ad 84,918
W 0
M 76
AD 9,7
AW 97,8
M 77
AW 121,9
UL 6,18
M 78
W 1
UH 7,18
M 79
AR 8,50
AR 9,74
M 80
AD 8,6
AD 9,7
M 81
W 2
AW 50,8
M 82
AW 74,9
UL 6,19
M 83
WL 1
R 32
M 84
mw 0
AN 112,114
AN 113,115
M 85
SH 114
SH 115
M 86
AN 114,114
AN 115,115
M 87
WL 2
AN 116,118
AN 117,119
M 88
SH 118
SH 119
M 89
AN 118,118
AN 119,119
M 90
TY 122,3592
TY 126,17416
M 91
ad 84,960
M 92
W 0
SA 218,1024
M 93
UH 7,19
AR 8,98
M 94
AR 9,122
AD 8,6
M 95
R 34
M 96
W 1
AD 9,7
M 97
AW 98,8
AW 122,9
M 98
UL 6,20
UH 7,20
M 99
WL 3
AN 120,122
AN 121,123
M 100
SH 122
SH 123
M 101
AN 122,122
AN 123,123
M 102
WL 2
AN 124,126
AN 125,127
M 103
SH 126
SH 127
M 104
AN 126,126
AN 127,127
M 105
W 2
AR 8,51
M 106
AR 9,75
AD 8,6
M 107
R 48
M 108
mw 3
AD 9,7
AW 51,8
M 109
ad 84,966
W 0
M 110
AW 75,9
UL 6,21
M 111
UH 7,21
AR 8,99
M 112
W 1
AR 9,123
M 113
AD 8,6
AD 9,7
M 114
AW 99,8
AW 123,9
M 115
W 2
M 116
M 117
M 118
bc 49,11,66,60
M 119
WL 2
R 50
M 120
mh 18
M 121
ad 66,2144
H 0
M 132
M 133
TX 58,0
TX 62,13824
M 122
DG 1024
M 123
M 134
M 135
TY 66,512
TY 70,14336
M 124
bc 48,11,64,56
M 125
mh 21
M 136
ad 64,2144
H 0
)"
R"(M 137
TX 74,1536
TX 78,15360
M 126
WL 4
Q 0
M 127
Q 2
M 138
Q 4
M 139
TY 82,2048
TY 86,15872
M 128
Q 6
M 129
WL 4
Q 8
M 140
Q 10
M 141
TX 90,3072
TX 94,16896
M 130
Q 12
M 131
R 52
M 142
Q 14
M 143
TY 98,3584
TY 102,17408
M 0
R 54
M 1
WL 10
Q 16
M 12
Q 18
M 13
Q 20
M 2
Q 22
M 3
WL 8
Q 24
M 14
Q 26
M 15
Q 28
M 4
Q 30
M 5
WL 6
Q 32
M 16
Q 34
M 17
Q 36
M 6
Q 38
M 7
WL 2
Q 40
M 18
Q 42
M 19
Q 44
M 8
Q 46
M 9
H 1
SA 220,1792
M 20
ae 44,68,480
M 21
s_bfe_u32 s51, s50, 0x1000b
v_mad_i32_i24 v47, v49, s51, v48
XL 30,0
M 10
H 2
XL 32,768
M 11
R 56
M 22
XL 34,1536
M 23
R 58
M 24
XL 36,2304
M 25
M 26
M 27
M 28
M 29
M 30
M 31
M 32
M 33
WV 10
M 34
WL 0
FP
M 35
M 36
M 37
M 38
M 39
M 40
M 41
M 42
M 43
M 44
M 45
FR
M 46
M 47
M 48
UL 6,22
UH 7,22
M 49
AR 8,52
AR 9,76
M 50
AD 8,6
AD 9,7
M 51
AW 52,8
AW 76,9
M 52
UL 6,23
UH 7,23
M 53
WL 0
FQ 116
AR 8,100
M 54
AR 9,124
AD 8,6
M 55
AD 9,7
AW 100,8
M 56
FZ 116
R 72
M 57
mw 24
R 74
M 58
TY 106,5128
TY 110,18952
M 59
R 76
M 60
ad 84,1008
W 0
M 61
AW 124,9
UL 6,24
M 62
UH 7,24
AR 8,53
M 63
W 1
AR 9,77
M 64
AD 8,6
AD 9,7
M 65
AW 53,8
AW 77,9
M 66
W 2
UL 6,25
M 67
WL 3
AN 104,106
AN 105,107
M 68
SH 106
SH 107
M 69
mw 27
AN 106,106
AN 107,107
M 70
WL 2
AN 108,110
AN 109,111
M 71
R 78
M 72
SH 110
SH 111
M 73
AN 110,110
AN 111,111
M 74
TY 114,6664
TY 118,20488
M 75
ad 84,1014
W 0
M 76
UH 7,25
AR 8,101
M 77
AR 9,125
AD 8,6
M 78
W 1
AD 9,7
M 79
AW 101,8
AW 125,9
M 80
UL 6,26
UH 7,26
M 81
W 2
SA 222,2560
M 82
AR 8,54
AR 9,78
M 83
WL 1
R 80
M 84
mw 48
AN 112,114
AN 113,115
M 85
SH 114
SH 115
M 86
AN 114,114
AN 115,115
M 87
WL 2
AN 116,118
AN 117,119
M 88
SH 118
SH 119
M 89
AN 118,118
AN 119,119
M 90
TY 122,8200
TY 126,22024
M 91
ad 84,1056
M 92
W 0
AD 8,6
M 93
AD 9,7
AW 54,8
M 94
AW 78,9
UL 6,27
M 95
R 82
M 96
W 1
UH 7,27
M 97
AR 8,102
AR 9,126
M 98
AD 8,6
AD 9,7
M 99
WL 3
AN 120,122
AN 121,123
M 100
SH 122
SH 123
M 101
AN 122,122
AN 123,123
M 102
WL 2
AN 124,126
AN 125,127
M 103
SH 126
SH 127
M 104
AN 126,126
AN 127,127
M 105
W 2
AW 102,8
M 106
AW 126,9
UL 6,28
M 107
R 0
M 108
mw 51
UH 7,28
AR 8,55
M 109
ad 84,1062
W 0
M 110
AR 9,79
AD 8,6
M 111
AD 9,7
AW 55,8
M 112
W 1
AW 79,9
M 113
UL 6,29
UH 7,29
M 114
AR 8,103
AR 9,127
M 115
W 2
AD 8,6
M 116
AD 9,7
AW 103,8
M 117
AW 127,9
M 118
bc 49,12,66,60
M 119
WL 1
R 2
M 120
mh 0
M 121
ad 66,2336
H 0
M 132
M 133
TX 58,4608
TX 62,18432
M 122
DG 1024
M 123
M 134
M 135
TY 66,5120
TY 70,18944
M 124
bc 48,12,64,56
M 125
mh 3
M 136
ad 64,2336
H 0
M 137
TX 74,6144
TX 78,19968
M 126
WL 4
Q 0
M 127
Q 2
M 138
Q 4
M 139
TY 82,6656
TY 86,20480
M 128
Q 6
M 129
WL 4
Q 8
M 140
Q 10
M 141
TX 90,7680
TX 94,21504
M 130
Q 12
M 131
R 4
M 142
Q 14
M 143
TY 98,8192
TY 102,22016
M 0
R 6
M 1
WL 10
Q 16
M 12
Q 18
M 13
Q 20
M 2
)"
R"(Q 22
M 3
WL 8
Q 24
M 14
Q 26
M 15
Q 28
M 4
Q 30
M 5
WL 6
Q 32
M 16
Q 34
M 17
Q 36
M 6
Q 38
M 7
WL 2
Q 40
M 18
Q 42
M 19
Q 44
M 8
Q 46
M 9
H 1
M 20
ae 42,72,96
M 21
s_bitcmp1_b32 s50, 4
s_cselect_b32 s52, 24576, 0
v_add_u32 v50, s52, v3
M 10
H 2
SA 224,0
M 11
R 8
M 22
M 23
R 10
PA 12288
DF
M 24
M 25
M 26
M 27
M 28
M 29
M 30
M 31
M 32
ae 44,70,384
M 33
WV 7
M 34
WL 0
FP
M 35
M 36
s_bitcmp1_b32 s50, 6
s_cselect_b32 s51, 24576, 0
v_add_u32 v47, s51, v3
M 37
XL 38,128
M 38
XL 40,896
M 39
M 40
M 41
M 42
M 43
M 44
XL 42,1664
M 45
FR
M 46
M 47
M 48
M 49
M 50
XL 44,2432
UL 6,30
M 51
UH 7,30
AR 8,56
M 52
AR 9,80
AD 8,6
M 53
WL 0
FQ 120
AD 9,7
M 54
AW 56,8
AW 80,9
M 55
UL 6,31
UH 7,31
M 56
FZ 120
R 24
M 57
mw 72
R 26
M 58
TY 106,9736
TY 110,23560
M 59
R 28
M 60
ad 84,1104
W 0
M 61
AR 8,104
AR 9,128
M 62
AD 8,6
AD 9,7
M 63
W 1
AW 104,8
M 64
AW 128,9
UL 6,32
M 65
UH 7,32
AR 8,57
M 66
W 2
AR 9,81
M 67
WL 3
AN 104,106
AN 105,107
M 68
SH 106
SH 107
M 69
mw 75
AN 106,106
AN 107,107
M 70
WL 2
AN 108,110
AN 109,111
M 71
R 30
M 72
SH 110
SH 111
M 73
AN 110,110
AN 111,111
M 74
TY 114,11272
TY 118,25096
M 75
ad 84,1110
W 0
M 76
AD 8,6
AD 9,7
M 77
AW 57,8
AW 81,9
M 78
W 1
UL 6,33
M 79
UH 7,33
AR 8,105
M 80
AR 9,129
AD 8,6
M 81
W 2
AD 9,7
M 82
AW 105,8
AW 129,9
M 83
WL 1
R 32
M 84
mw 0
AN 112,114
AN 113,115
M 85
SH 114
SH 115
M 86
AN 114,114
AN 115,115
M 87
WL 2
AN 116,118
AN 117,119
M 88
SH 118
SH 119
M 89
AN 118,118
AN 119,119
M 90
TY 122,12808
TY 126,26632
M 91
ad 84,1152
M 92
W 0
SA 226,768
M 93
UL 6,34
UH 7,34
M 94
AR 8,58
AR 9,82
M 95
R 34
M 96
W 1
AD 8,6
M 97
AD 9,7
AW 58,8
M 98
AW 82,9
UL 6,35
M 99
WL 3
AN 120,122
AN 121,123
M 100
SH 122
SH 123
M 101
AN 122,122
AN 123,123
M 102
WL 2
AN 124,126
AN 125,127
M 103
SH 126
SH 127
M 104
AN 126,126
AN 127,127
M 105
W 2
UH 7,35
M 106
AR 8,106
AR 9,130
M 107
R 48
M 108
mw 3
AD 8,6
AD 9,7
M 109
ad 84,1158
W 0
M 110
AW 106,8
AW 130,9
M 111
UL 6,36
UH 7,36
M 112
W 1
AR 8,59
M 113
AR 9,83
AD 8,6
M 114
AD 9,7
AW 59,8
M 115
W 2
AW 83,9
M 116
UL 6,37
UH 7,37
M 117
AR 8,107
AR 9,131
M 118
bc 49,13,66,60
AD 8,6
M 119
WL 0
R 50
M 120
mh 9
AD 9,7
AW 107,8
M 121
ad 66,2528
H 0
M 132
AW 131,9
M 133
TX 58,9216
TX 62,23040
M 122
DG 1024
M 123
M 134
M 135
TY 66,9728
TY 70,23552
M 124
bc 48,13,64,56
M 125
mh 12
M 136
ad 64,2528
H 0
M 137
TX 74,10752
TX 78,24576
M 126
WL 4
Q 0
M 127
Q 2
M 138
Q 4
M 139
TY 82,11264
TY 86,25088
M 128
Q 6
M 129
WL 4
Q 8
M 140
Q 10
M 141
TX 90,12288
TX 94,26112
M 130
Q 12
M 131
R 52
M 142
Q 14
M 143
TY 98,12800
TY 102,26624
M 0
R 54
M 1
WL 10
Q 16
M 12
Q 18
M 13
Q 20
M 2
Q 22
M 3
WL 8
Q 24
M 14
Q 26
M 15
Q 28
M 4
Q 30
M 5
WL 6
Q 32
M 16
Q 34
M 17
Q 36
M 6
Q 38
M 7
WL 2
Q 40
M 18
Q 42
M 19
Q 44
M 8
Q 46
M 9
H 1
SA 228,1536
M 20
M 21
M 10
H 2
)"
R"(M 11
R 56
M 22
M 23
R 58
M 24
M 25
M 26
M 27
M 28
M 29
M 30
M 31
M 32
M 33
WV 6
M 34
WL 0
FP
M 35
M 36
ae 44,70,384
M 37
s_bfe_u32 s51, s50, 0x10007
v_mad_i32_i24 v47, v49, s51, v48
XL 14,128
M 38
M 39
M 40
XL 16,896
M 41
M 42
M 43
M 44
M 45
FR
M 46
XL 18,1664
M 47
M 48
M 49
M 50
M 51
M 52
XL 20,2432
M 53
WL 0
FQ 124
M 54
M 55
M 56
FZ 124
R 72
M 57
mw 24
R 74
M 58
TY 106,520
TY 110,14344
M 59
R 76
M 60
ad 84,1200
W 0
M 61
M 62
M 63
W 1
M 64
M 65
M 66
W 2
M 67
WL 3
AN 104,106
AN 105,107
M 68
SH 106
SH 107
M 69
mw 27
AN 106,106
AN 107,107
M 70
WL 2
AN 108,110
AN 109,111
M 71
R 78
M 72
SH 110
SH 111
M 73
AN 110,110
AN 111,111
M 74
TY 114,2056
TY 118,15880
M 75
ad 84,1206
W 0
M 76
M 77
M 78
W 1
M 79
M 80
M 81
W 2
SA 230,2304
M 82
M 83
R 80
M 84
mw 48
WL 3
AN 112,114
AN 113,115
M 85
SH 114
SH 115
M 86
AN 114,114
AN 115,115
M 87
WL 2
AN 116,118
AN 117,119
M 88
SH 118
SH 119
M 89
AN 118,118
AN 119,119
M 90
TY 122,3592
TY 126,17416
M 91
ad 84,1248
M 92
W 0
UL 6,38
M 93
UH 7,38
AR 8,132
M 94
AR 9,156
AD 8,6
M 95
R 82
M 96
W 1
AD 9,7
M 97
AW 132,8
AW 156,9
M 98
UL 6,39
UH 7,39
M 99
WL 3
AN 120,122
AN 121,123
M 100
SH 122
SH 123
M 101
AN 122,122
AN 123,123
M 102
WL 2
AN 124,126
AN 125,127
M 103
SH 126
SH 127
M 104
AN 126,126
AN 127,127
M 105
W 2
AR 8,180
M 106
AD 8,6
AD 136,7
M 107
R 0
M 108
mw 51
AW 180,8
UL 6,40
M 109
ad 84,1254
W 0
M 110
UH 7,40
AR 8,133
M 111
AR 9,157
AD 8,6
M 112
W 1
AD 9,7
M 113
AW 133,8
AW 157,9
M 114
UL 6,41
UH 7,41
M 115
W 2
AR 8,181
M 116
AD 8,6
AD 137,7
M 117
AW 181,8
UL 6,42
M 118
bc 49,14,66,60
UH 7,42
M 119
WL 0
R 2
M 120
mh 18
AR 8,134
AR 9,158
M 121
ad 66,2720
H 0
M 132
AD 8,6
AD 9,7
M 133
TX 58,0
TX 62,13824
M 122
DG 1024
M 123
AW 134,8
AW 158,9
M 134
UL 6,43
UH 7,43
M 135
TY 66,512
TY 70,14336
M 124
bc 48,14,64,56
AR 8,182
M 125
mh 21
AD 8,6
AD 138,7
M 136
ad 64,2720
H 0
M 137
TX 74,1536
TX 78,15360
M 126
WL 4
Q 0
M 127
Q 2
M 138
Q 4
M 139
TY 82,2048
TY 86,15872
M 128
Q 6
M 129
WL 4
Q 8
M 140
Q 10
M 141
TX 90,3072
TX 94,16896
M 130
Q 12
M 131
R 4
M 142
Q 14
M 143
TY 98,3584
TY 102,17408
M 0
R 6
M 1
WL 10
Q 16
M 12
Q 18
M 13
Q 20
M 2
Q 22
M 3
WL 8
Q 24
M 14
Q 26
M 15
Q 28
M 4
Q 30
M 5
WL 6
Q 32
M 16
Q 34
M 17
Q 36
M 6
Q 38
M 7
WL 2
Q 40
M 18
Q 42
M 19
Q 44
M 8
Q 46
M 9
H 1
AW 182,8
M 20
ae 42,72,96
M 21
SA 232,128
UL 6,44
M 10
H 2
UH 7,44
M 11
R 8
M 22
AR 8,135
AR 9,159
M 23
R 10
PA 12288
DF
M 24
AD 8,6
AD 9,7
M 25
AW 135,8
AW 159,9
M 26
UL 6,45
UH 7,45
M 27
AR 8,183
AD 8,6
M 28
AD 139,7
AW 183,8
M 29
M 30
M 31
M 32
M 33
WV 7
M 34
WL 0
FP
M 35
M 36
ae 44,68,384
M 37
s_bitcmp1_b32 s50, 8
s_cselect_b32 s51, 24576, 0
v_add_u32 v47, s51, v3
M 38
XL 22,128
M 39
M 40
M 41
M 42
XL 24,896
M 43
M 44
M 45
FR
M 46
M 47
M 48
XL 26,1664
M 49
M 50
M 51
M 52
M 53
WL 0
)"
R"(FQ 128
M 54
XL 28,2432
M 55
M 56
FZ 128
R 24
M 57
mw 72
R 26
M 58
TY 106,5128
TY 110,18952
M 59
R 28
M 60
ad 84,1296
W 0
M 61
M 62
M 63
W 1
M 64
M 65
M 66
W 2
M 67
WL 3
AN 104,106
AN 105,107
M 68
SH 106
SH 107
M 69
mw 75
AN 106,106
AN 107,107
M 70
WL 2
AN 108,110
AN 109,111
M 71
R 30
M 72
SH 110
SH 111
M 73
AN 110,110
AN 111,111
M 74
TY 114,6664
TY 118,20488
M 75
ad 84,1302
W 0
M 76
M 77
M 78
W 1
M 79
M 80
M 81
W 2
M 82
M 83
R 32
M 84
mw 0
WL 3
AN 112,114
AN 113,115
M 85
SH 114
SH 115
M 86
AN 114,114
AN 115,115
M 87
WL 2
AN 116,118
AN 117,119
M 88
SH 118
SH 119
M 89
AN 118,118
AN 119,119
M 90
TY 122,8200
TY 126,22024
M 91
ad 84,1344
M 92
W 0
SA 234,896
M 93
UL 6,14
UH 7,14
M 94
AR 8,136
AR 9,160
M 95
R 34
M 96
W 1
AD 8,6
M 97
AD 9,7
AW 136,8
M 98
AW 160,9
UL 6,15
M 99
WL 3
AN 120,122
AN 121,123
M 100
SH 122
SH 123
M 101
AN 122,122
AN 123,123
M 102
WL 2
AN 124,126
AN 125,127
M 103
SH 126
SH 127
M 104
AN 126,126
AN 127,127
M 105
W 2
UH 7,15
M 106
AR 8,184
AD 8,6
M 107
R 48
M 108
mw 3
AD 140,7
AW 184,8
M 109
ad 84,1350
W 0
M 110
UL 6,16
UH 7,16
M 111
AR 8,137
AR 9,161
M 112
W 1
AD 8,6
M 113
AD 9,7
AW 137,8
M 114
AW 161,9
UL 6,17
M 115
W 2
UH 7,17
M 116
AR 8,185
AD 8,6
M 117
AD 141,7
AW 185,8
M 118
bc 49,15,66,60
UL 6,18
M 119
WL 0
R 50
M 120
mh 0
UH 7,18
AR 8,138
M 121
ad 66,2912
H 0
M 132
AR 9,162
AD 8,6
M 133
TX 58,4608
TX 62,18432
M 122
DG 1024
M 123
AD 9,7
AW 138,8
M 134
AW 162,9
UL 6,19
M 135
TY 66,5120
TY 70,18944
M 124
bc 48,15,64,56
UH 7,19
M 125
mh 3
AR 8,186
AD 8,6
M 136
ad 64,2912
H 0
M 137
TX 74,6144
TX 78,19968
M 126
WL 4
Q 0
M 127
Q 2
M 138
Q 4
M 139
TY 82,6656
TY 86,20480
M 128
Q 6
M 129
WL 4
Q 8
M 140
Q 10
M 141
TX 90,7680
TX 94,21504
M 130
Q 12
M 131
R 52
M 142
Q 14
M 143
TY 98,8192
TY 102,22016
M 0
R 54
M 1
WL 10
Q 16
M 12
Q 18
M 13
Q 20
M 2
Q 22
M 3
WL 8
Q 24
M 14
Q 26
M 15
Q 28
M 4
Q 30
M 5
WL 6
Q 32
M 16
Q 34
M 17
Q 36
M 6
Q 38
M 7
WL 2
Q 40
M 18
Q 42
M 19
Q 44
M 8
Q 46
M 9
H 1
SA 236,1664
M 20
AD 142,7
AW 186,8
M 21
UL 6,20
UH 7,20
M 10
H 2
AR 8,139
M 11
R 56
M 22
AR 9,163
AD 8,6
M 23
R 58
M 24
AD 9,7
AW 139,8
M 25
AW 163,9
UL 6,21
M 26
UH 7,21
AR 8,187
M 27
AD 8,6
AD 143,7
M 28
AW 187,8
M 29
M 30
M 31
M 32
M 33
WV 6
M 34
WL 0
FP
M 35
M 36
M 37
M 38
ae 44,68,384
M 39
s_bfe_u32 s51, s50, 0x10009
v_mad_i32_i24 v47, v49, s51, v48
XL 30,128
M 40
M 41
M 42
M 43
M 44
XL 32,896
M 45
FR
M 46
M 47
M 48
M 49
M 50
XL 34,1664
M 51
M 52
M 53
WL 0
FQ 132
M 54
M 55
M 56
FZ 132
R 72
M 57
mw 24
R 74
M 58
TY 106,9736
TY 110,23560
M 59
R 76
M 60
ad 84,1392
W 0
M 61
XL 36,2432
M 62
M 63
W 1
M 64
M 65
M 66
W 2
M 67
WL 3
AN 104,106
AN 105,107
M 68
SH 106
SH 107
M 69
mw 27
AN 106,106
AN 107,107
M 70
WL 2
AN 108,110
AN 109,111
M 71
R 78
M 72
SH 110
SH 111
M 73
AN 110,110
AN 111,111
M 74
)"
R"(TY 114,11272
TY 118,25096
M 75
ad 84,1398
W 0
M 76
M 77
M 78
W 1
M 79
M 80
M 81
W 2
SA 238,2432
M 82
M 83
R 80
M 84
mw 48
WL 3
AN 112,114
AN 113,115
M 85
SH 114
SH 115
M 86
AN 114,114
AN 115,115
M 87
WL 2
AN 116,118
AN 117,119
M 88
SH 118
SH 119
M 89
AN 118,118
AN 119,119
M 90
TY 122,12808
TY 126,26632
M 91
ad 84,1440
M 92
W 0
UL 6,22
M 93
UH 7,22
AR 8,140
M 94
AR 9,164
AD 8,6
M 95
R 82
M 96
W 1
AD 9,7
M 97
AW 140,8
AW 164,9
M 98
UL 6,23
UH 7,23
M 99
WL 3
AN 120,122
AN 121,123
M 100
SH 122
SH 123
M 101
AN 122,122
AN 123,123
M 102
WL 2
AN 124,126
AN 125,127
M 103
SH 126
SH 127
M 104
AN 126,126
AN 127,127
M 105
W 2
AR 8,188
M 106
AD 8,6
AD 144,7
M 107
R 0
M 108
mw 51
AW 188,8
UL 6,24
M 109
ad 84,1446
W 0
M 110
UH 7,24
AR 8,141
M 111
AR 9,165
AD 8,6
M 112
W 1
AD 9,7
M 113
AW 141,8
AW 165,9
M 114
UL 6,25
UH 7,25
M 115
W 2
AR 8,189
M 116
AD 8,6
AD 145,7
M 117
AW 189,8
UL 6,26
M 118
bc 49,16,66,60
UH 7,26
M 119
WL 0
R 2
M 120
mh 9
AR 8,142
AR 9,166
M 121
ad 66,3104
H 0
M 132
AD 8,6
AD 9,7
M 133
TX 58,9216
TX 62,23040
M 122
DG 1024
M 123
AW 142,8
AW 166,9
M 134
UL 6,27
UH 7,27
M 135
TY 66,9728
TY 70,23552
M 124
bc 48,16,64,56
AR 8,190
M 125
mh 12
AD 8,6
AD 146,7
M 136
ad 64,3104
H 0
M 137
TX 74,10752
TX 78,24576
M 126
WL 4
Q 0
M 127
Q 2
M 138
Q 4
M 139
TY 82,11264
TY 86,25088
M 128
Q 6
M 129
WL 4
Q 8
M 140
Q 10
M 141
TX 90,12288
TX 94,26112
M 130
Q 12
M 131
R 4
M 142
Q 14
M 143
TY 98,12800
TY 102,26624
M 0
R 6
M 1
WL 10
Q 16
M 12
Q 18
M 13
Q 20
M 2
Q 22
M 3
WL 8
Q 24
M 14
Q 26
M 15
Q 28
M 4
Q 30
M 5
WL 6
Q 32
M 16
Q 34
M 17
Q 36
M 6
Q 38
M 7
WL 2
Q 40
M 18
Q 42
M 19
Q 44
M 8
Q 46
M 9
H 1
AW 190,8
M 20
ae 42,72,96
M 21
SA 240,256
UL 6,28
M 10
H 2
UH 7,28
M 11
R 8
M 22
AR 8,143
AR 9,167
M 23
R 10
PA 12288
DF
M 24
AD 8,6
AD 9,7
M 25
AW 143,8
AW 167,9
M 26
UL 6,29
UH 7,29
M 27
AR 8,191
AD 8,6
M 28
AD 147,7
AW 191,8
M 29
M 30
M 31
M 32
M 33
WV 7
M 34
WL 0
FP
M 35
M 36
M 37
M 38
M 39
M 40
ae 44,68,480
M 41
s_bitcmp1_b32 s50, 10
s_cselect_b32 s51, 24576, 0
v_add_u32 v47, s51, v3
M 42
XL 38,128
M 43
M 44
M 45
FR
M 46
XL 40,896
M 47
M 48
M 49
M 50
M 51
M 52
XL 42,1664
M 53
WL 0
FQ 136
M 54
M 55
M 56
FZ 136
R 24
M 57
mw 72
R 26
M 58
TY 106,520
TY 110,14344
M 59
R 28
M 60
ad 84,1488
W 0
M 61
XL 44,2432
M 62
M 63
W 1
M 64
M 65
M 66
W 2
M 67
WL 3
AN 104,106
AN 105,107
M 68
SH 106
SH 107
M 69
mw 75
AN 106,106
AN 107,107
M 70
WL 2
AN 108,110
AN 109,111
M 71
R 30
M 72
SH 110
SH 111
M 73
AN 110,110
AN 111,111
M 74
TY 114,2056
TY 118,15880
M 75
ad 84,1494
W 0
M 76
M 77
M 78
W 1
M 79
M 80
M 81
W 2
M 82
M 83
R 32
M 84
mw 0
WL 3
AN 112,114
AN 113,115
M 85
SH 114
SH 115
M 86
AN 114,114
AN 115,115
M 87
WL 2
AN 116,118
AN 117,119
M 88
SH 118
SH 119
M 89
AN 118,118
AN 119,119
M 90
TY 122,3592
TY 126,17416
M 91
ad 84,1536
M 92
W 0
SA 242,1024
M 93
)"
R"(M 94
UL 6,30
UH 7,30
M 95
R 34
M 96
W 1
AR 8,144
M 97
AR 9,168
AD 8,6
M 98
AD 9,7
AW 144,8
M 99
WL 3
AN 120,122
AN 121,123
M 100
SH 122
SH 123
M 101
AN 122,122
AN 123,123
M 102
WL 2
AN 124,126
AN 125,127
M 103
SH 126
SH 127
M 104
AN 126,126
AN 127,127
M 105
W 2
AW 168,9
M 106
UL 6,31
UH 7,31
M 107
R 48
M 108
mw 3
AR 8,192
AD 8,6
M 109
ad 84,1542
W 0
M 110
AD 148,7
AW 192,8
M 111
UL 6,32
UH 7,32
M 112
W 1
AR 8,145
M 113
AR 9,169
AD 8,6
M 114
AD 9,7
AW 145,8
M 115
W 2
AW 169,9
M 116
UL 6,33
UH 7,33
M 117
AR 8,193
AD 8,6
M 118
bc 49,17,66,60
AD 149,7
M 119
WL 0
R 50
M 120
mh 18
AW 193,8
UL 6,34
M 121
ad 66,3296
H 0
M 132
UH 7,34
AR 8,146
M 133
TX 58,0
TX 62,13824
M 122
DG 1024
M 123
AR 9,170
AD 8,6
M 134
AD 9,7
AW 146,8
M 135
TY 66,512
TY 70,14336
M 124
bc 48,17,64,56
AW 170,9
M 125
mh 21
UL 6,35
UH 7,35
M 136
ad 64,3296
H 0
M 137
TX 74,1536
TX 78,15360
M 126
WL 4
Q 0
M 127
Q 2
M 138
Q 4
M 139
TY 82,2048
TY 86,15872
M 128
Q 6
M 129
WL 4
Q 8
M 140
Q 10
M 141
TX 90,3072
TX 94,16896
M 130
Q 12
M 131
R 52
M 142
Q 14
M 143
TY 98,3584
TY 102,17408
M 0
R 54
M 1
WL 10
Q 16
M 12
Q 18
M 13
Q 20
M 2
Q 22
M 3
WL 8
Q 24
M 14
Q 26
M 15
Q 28
M 4
Q 30
M 5
WL 6
Q 32
M 16
Q 34
M 17
Q 36
M 6
Q 38
M 7
WL 2
Q 40
M 18
Q 42
M 19
Q 44
M 8
Q 46
M 9
H 1
SA 244,1792
M 20
AR 8,194
AD 8,6
M 21
AD 150,7
AW 194,8
M 10
H 2
UL 6,36
M 11
R 56
M 22
UH 7,36
AR 8,147
M 23
R 58
M 24
AR 9,171
AD 8,6
M 25
AD 9,7
AW 147,8
M 26
AW 171,9
UL 6,37
M 27
UH 7,37
AR 8,195
M 28
AD 8,6
AD 151,7
M 29
AW 195,8
M 30
M 31
M 32
M 33
WV 6
M 34
WL 0
FP
M 35
M 36
M 37
M 38
M 39
M 40
M 41
M 42
ae 44,68,480
M 43
s_bfe_u32 s51, s50, 0x1000b
v_mad_i32_i24 v47, v49, s51, v48
XL 14,128
M 44
M 45
FR
M 46
M 47
M 48
XL 16,896
M 49
M 50
M 51
M 52
M 53
WL 0
FQ 140
M 54
XL 18,1664
M 55
M 56
FZ 140
R 72
M 57
mw 24
R 74
M 58
TY 106,5128
TY 110,18952
M 59
R 76
M 60
ad 84,1584
W 0
M 61
XL 20,2432
M 62
M 63
W 1
M 64
M 65
M 66
W 2
M 67
WL 3
AN 104,106
AN 105,107
M 68
SH 106
SH 107
M 69
mw 27
AN 106,106
AN 107,107
M 70
WL 2
AN 108,110
AN 109,111
M 71
R 78
M 72
SH 110
SH 111
M 73
AN 110,110
AN 111,111
M 74
TY 114,6664
TY 118,20488
M 75
ad 84,1590
W 0
M 76
M 77
M 78
W 1
M 79
M 80
M 81
W 2
SA 246,2560
M 82
M 83
R 80
M 84
mw 48
WL 3
AN 112,114
AN 113,115
M 85
SH 114
SH 115
M 86
AN 114,114
AN 115,115
M 87
WL 2
AN 116,118
AN 117,119
M 88
SH 118
SH 119
M 89
AN 118,118
AN 119,119
M 90
TY 122,8200
TY 126,22024
M 91
ad 84,1632
M 92
W 0
M 93
M 94
M 95
R 82
M 96
W 1
UL 6,38
M 97
UH 7,38
AR 8,148
M 98
AR 9,172
AD 8,6
M 99
WL 3
AN 120,122
AN 121,123
M 100
SH 122
SH 123
M 101
AN 122,122
AN 123,123
M 102
WL 2
AN 124,126
AN 125,127
M 103
SH 126
SH 127
M 104
AN 126,126
AN 127,127
M 105
W 2
AD 9,7
M 106
AW 148,8
AW 172,9
M 107
R 0
M 108
mw 51
UL 6,39
UH 7,39
M 109
ad 84,1638
W 0
M 110
AR 8,196
AD 8,6
)"
R"(M 111
AD 152,7
AW 196,8
M 112
W 1
UL 6,40
M 113
UH 7,40
AR 8,149
M 114
AR 9,173
AD 8,6
M 115
W 2
AD 9,7
M 116
AW 149,8
AW 173,9
M 117
UL 6,41
UH 7,41
M 118
bc 49,18,66,60
AR 8,197
M 119
WL 0
R 2
M 120
mh 0
AD 8,6
AD 153,7
M 121
ad 66,3488
H 0
M 132
AW 197,8
UL 6,42
M 133
TX 58,4608
TX 62,18432
M 122
DG 1024
M 123
UH 7,42
AR 8,150
M 134
AR 9,174
AD 8,6
M 135
TY 66,5120
TY 70,18944
M 124
bc 48,18,64,56
AD 9,7
M 125
mh 3
AW 150,8
AW 174,9
M 136
ad 64,3488
H 0
M 137
TX 74,6144
TX 78,19968
M 126
WL 4
Q 0
M 127
Q 2
M 138
Q 4
M 139
TY 82,6656
TY 86,20480
M 128
Q 6
M 129
WL 4
Q 8
M 140
Q 10
M 141
TX 90,7680
TX 94,21504
M 130
Q 12
M 131
R 4
M 142
Q 14
M 143
TY 98,8192
TY 102,22016
M 0
R 6
M 1
WL 10
Q 16
M 12
Q 18
M 13
Q 20
M 2
Q 22
M 3
WL 8
Q 24
M 14
Q 26
M 15
Q 28
M 4
Q 30
M 5
WL 6
Q 32
M 16
Q 34
M 17
Q 36
M 6
Q 38
M 7
WL 2
Q 40
M 18
Q 42
M 19
Q 44
M 8
Q 46
M 9
H 1
UL 6,43
M 20
ae 42,72,96
M 21
s_bfe_u32 s52, s50, 0x10005
v_mad_i32_i24 v50, v49, s52, v48
SA 248,256
M 10
H 2
UH 7,43
M 11
R 8
M 22
AR 8,198
AD 8,6
M 23
R 10
PA 12288
DF
M 24
AD 154,7
AW 198,8
M 25
UL 6,44
UH 7,44
M 26
AR 8,151
AR 9,175
M 27
AD 8,6
AD 9,7
M 28
AW 151,8
AW 175,9
M 29
UL 6,45
UH 7,45
M 30
AR 8,199
AD 8,6
M 31
AD 155,7
AW 199,8
M 32
M 33
WV 7
M 34
WL 0
FP
M 35
M 36
M 37
M 38
M 39
M 40
M 41
M 42
M 43
M 44
M 45
FR
M 46
M 47
M 48
M 49
M 50
M 51
M 52
M 53
WL 0
FQ 144
M 54
M 55
M 56
FZ 144
R 24
M 57
mw 72
R 26
M 58
TY 106,9736
TY 110,23560
M 59
R 28
M 60
ad 84,1680
W 0
M 61
M 62
M 63
W 1
M 64
M 65
M 66
W 2
M 67
WL 3
AN 104,106
AN 105,107
M 68
SH 106
SH 107
M 69
mw 75
AN 106,106
AN 107,107
M 70
WL 2
AN 108,110
AN 109,111
M 71
R 30
M 72
SH 110
SH 111
M 73
AN 110,110
AN 111,111
M 74
TY 114,11272
TY 118,25096
M 75
ad 84,1686
W 0
M 76
ae 44,70,384
M 77
s_bitcmp1_b32 s50, 6
s_cselect_b32 s51, 24576, 0
v_add_u32 v47, s51, v3
M 78
W 1
XL 22,256
M 79
XL 24,1024
M 80
M 81
W 2
M 82
XL 26,1792
M 83
WL 1
R 32
M 84
mw 0
AN 112,114
AN 113,115
M 85
SH 114
SH 115
M 86
AN 114,114
AN 115,115
M 87
WL 2
AN 116,118
AN 117,119
M 88
SH 118
SH 119
M 89
AN 118,118
AN 119,119
M 90
TY 122,12808
TY 126,26632
M 91
ad 84,1728
M 92
W 0
SA 250,1024
M 93
XL 28,2560
M 94
M 95
R 34
M 96
W 1
M 97
M 98
UL 6,14
UH 7,14
M 99
WL 3
AN 120,122
AN 121,123
M 100
SH 122
SH 123
M 101
AN 122,122
AN 123,123
M 102
WL 2
AN 124,126
AN 125,127
M 103
SH 126
SH 127
M 104
AN 126,126
AN 127,127
M 105
W 2
AR 8,152
M 106
AR 9,176
AD 8,6
M 107
R 48
M 108
mw 3
AD 9,7
AW 152,8
M 109
ad 84,1734
W 0
M 110
AW 176,9
UL 6,15
M 111
UH 7,15
AD 132,6
M 112
W 1
AD 156,7
M 113
UL 6,16
UH 7,16
M 114
AR 8,153
AR 9,177
M 115
W 2
AD 8,6
M 116
AD 9,7
AW 153,8
M 117
AW 177,9
UL 6,17
M 118
bc 49,19,66,60
UH 7,17
M 119
WL 0
R 50
M 120
mh 9
AD 133,6
AD 157,7
M 121
ad 66,3680
H 0
M 132
UL 6,18
UH 7,18
M 133
TX 58,9216
TX 62,23040
M 122
DG 1024
M 123
AR 8,154
AR 9,178
M 134
AD 8,6
)"
R"(AD 9,7
M 135
TY 66,9728
TY 70,23552
M 124
bc 48,19,64,56
AW 154,8
M 125
mh 12
AW 178,9
UL 6,19
M 136
ad 64,3680
H 0
M 137
TX 74,10752
TX 78,24576
M 126
WL 4
Q 0
M 127
Q 2
M 138
Q 4
M 139
TY 82,11264
TY 86,25088
M 128
Q 6
M 129
WL 4
Q 8
M 140
Q 10
M 141
TX 90,12288
TX 94,26112
M 130
Q 12
M 131
R 52
M 142
Q 14
M 143
TY 98,12800
TY 102,26624
M 0
R 54
M 1
WL 10
Q 16
M 12
Q 18
M 13
Q 20
M 2
Q 22
M 3
WL 8
Q 24
M 14
Q 26
M 15
Q 28
M 4
Q 30
M 5
WL 6
Q 32
M 16
Q 34
M 17
Q 36
M 6
Q 38
M 7
WL 2
Q 40
M 18
Q 42
M 19
Q 44
M 8
Q 46
M 9
H 1
SA 252,1792
M 20
UH 7,19
AD 134,6
M 21
AD 158,7
UL 6,20
M 10
H 2
UH 7,20
M 11
R 56
M 22
AR 8,155
AR 9,179
M 23
R 58
M 24
AD 8,6
AD 9,7
M 25
AW 155,8
AW 179,9
M 26
UL 6,21
UH 7,21
M 27
AD 135,6
AD 159,7
M 28
M 29
M 30
M 31
M 32
M 33
WV 6
M 34
WL 0
FP
M 35
M 36
M 37
M 38
M 39
M 40
M 41
M 42
M 43
M 44
M 45
FR
M 46
M 47
M 48
M 49
M 50
M 51
M 52
M 53
WL 0
FQ 148
M 54
M 55
M 56
FZ 148
R 72
M 57
mw 24
R 74
M 58
TY 106,520
TY 110,14344
M 59
R 76
M 60
ad 84,1776
W 0
M 61
M 62
M 63
W 1
M 64
M 65
M 66
W 2
M 67
WL 3
AN 104,106
AN 105,107
M 68
SH 106
SH 107
M 69
mw 27
AN 106,106
AN 107,107
M 70
WL 2
AN 108,110
AN 109,111
M 71
R 78
M 72
SH 110
SH 111
M 73
AN 110,110
AN 111,111
M 74
TY 114,2056
TY 118,15880
M 75
ad 84,1782
W 0
M 76
ae 44,70,384
M 77
s_bfe_u32 s51, s50, 0x10007
v_mad_i32_i24 v47, v49, s51, v48
XL 30,256
M 78
W 1
M 79
M 80
XL 32,1024
M 81
W 2
SA 254,2560
M 82
M 83
WL 1
R 80
M 84
mw 48
AN 112,114
AN 113,115
M 85
SH 114
SH 115
M 86
AN 114,114
AN 115,115
M 87
WL 2
AN 116,118
AN 117,119
M 88
SH 118
SH 119
M 89
AN 118,118
AN 119,119
M 90
TY 122,3592
TY 126,17416
M 91
ad 84,1824
M 92
W 0
XL 34,1792
M 93
XL 36,2560
M 94
M 95
R 82
M 96
W 1
M 97
M 98
M 99
WL 3
AN 120,122
AN 121,123
M 100
SH 122
SH 123
M 101
AN 122,122
AN 123,123
M 102
WL 2
AN 124,126
AN 125,127
M 103
SH 126
SH 127
M 104
AN 126,126
AN 127,127
M 105
W 2
M 106
M 107
R 0
M 108
mw 51
M 109
ad 84,1830
W 0
M 110
M 111
M 112
W 1
M 113
M 114
M 115
W 2
M 116
M 117
M 118
bc 49,20,66,60
M 119
WL 2
R 2
M 120
mh 18
M 121
ad 66,3872
H 0
M 132
M 133
TX 58,0
TX 62,13824
M 122
DG 1024
M 123
M 134
UL 6,22
UH 7,22
M 135
TY 66,512
TY 70,14336
M 124
bc 48,20,64,56
AD 160,6
M 125
mh 21
AD 184,7
UL 6,23
M 136
ad 64,3872
H 0
M 137
TX 74,1536
TX 78,15360
M 126
WL 4
Q 0
M 127
Q 2
M 138
Q 4
M 139
TY 82,2048
TY 86,15872
M 128
Q 6
M 129
WL 4
Q 8
M 140
Q 10
M 141
TX 90,3072
TX 94,16896
M 130
Q 12
M 131
R 4
M 142
Q 14
M 143
TY 98,3584
TY 102,17408
M 0
R 6
M 1
WL 10
Q 16
M 12
Q 18
M 13
Q 20
M 2
Q 22
M 3
WL 8
Q 24
M 14
Q 26
M 15
Q 28
M 4
Q 30
M 5
WL 6
Q 32
M 16
Q 34
M 17
Q 36
M 6
Q 38
M 7
WL 2
Q 40
M 18
Q 42
M 19
Q 44
M 8
Q 46
M 9
H 1
)"
R"(UH 7,23
M 20
AD 208,6
AD 232,7
M 21
UL 6,24
UH 7,24
M 10
H 2
AD 161,6
M 11
R 8
M 22
AD 185,7
UL 6,25
M 23
R 10
PA 12288
DF
M 24
UH 7,25
AD 209,6
M 25
AD 233,7
UL 6,26
M 26
UH 7,26
AD 162,6
M 27
AD 186,7
UL 6,27
M 28
UH 7,27
AD 210,6
M 29
AD 234,7
UL 6,28
M 30
UH 7,28
AD 163,6
M 31
AD 187,7
UL 6,29
M 32
UH 7,29
AD 211,6
M 33
WV 6
AD 235,7
M 34
WL 0
FP
M 35
M 36
M 37
M 38
M 39
M 40
M 41
M 42
M 43
M 44
M 45
FR
M 46
M 47
M 48
M 49
M 50
M 51
M 52
M 53
WL 0
FQ 152
M 54
M 55
M 56
FZ 152
R 24
M 57
mw 72
R 26
M 58
TY 106,5128
TY 110,18952
M 59
R 28
M 60
ad 84,1872
W 0
M 61
M 62
M 63
W 1
M 64
M 65
M 66
W 2
M 67
WL 3
AN 104,106
AN 105,107
M 68
SH 106
SH 107
M 69
mw 75
AN 106,106
AN 107,107
M 70
WL 2
AN 108,110
AN 109,111
M 71
R 30
M 72
SH 110
SH 111
M 73
AN 110,110
AN 111,111
M 74
TY 114,6664
TY 118,20488
M 75
ad 84,1878
W 0
M 76
M 77
M 78
W 1
M 79
ae 44,68,384
M 80
s_bitcmp1_b32 s50, 8
s_cselect_b32 s51, 24576, 0
v_add_u32 v47, s51, v3
M 81
W 2
XL 38,256
M 82
M 83
WL 1
R 32
M 84
mw 0
AN 112,114
AN 113,115
M 85
SH 114
SH 115
M 86
AN 114,114
AN 115,115
M 87
WL 2
AN 116,118
AN 117,119
M 88
SH 118
SH 119
M 89
AN 118,118
AN 119,119
M 90
TY 122,8200
TY 126,22024
M 91
ad 84,1920
M 92
W 0
XL 40,1024
M 93
XL 42,1792
M 94
M 95
R 34
M 96
W 1
XL 44,2560
M 97
M 98
M 99
WL 3
AN 120,122
AN 121,123
M 100
SH 122
SH 123
M 101
AN 122,122
AN 123,123
M 102
WL 2
AN 124,126
AN 125,127
M 103
SH 126
SH 127
M 104
AN 126,126
AN 127,127
M 105
W 2
M 106
M 107
R 48
M 108
mw 3
M 109
ad 84,1926
W 0
M 110
M 111
M 112
W 1
M 113
M 114
M 115
W 2
M 116
M 117
M 118
bc 49,21,66,60
M 119
WL 2
R 50
M 120
mh 0
M 121
ad 66,4064
H 0
M 132
M 133
TX 58,4608
TX 62,18432
M 122
DG 1024
M 123
M 134
M 135
TY 66,5120
TY 70,18944
M 124
bc 48,21,64,56
M 125
mh 3
M 136
ad 64,4064
H 0
M 137
TX 74,6144
TX 78,19968
M 126
WL 4
Q 0
M 127
Q 2
M 138
Q 4
M 139
TY 82,6656
TY 86,20480
M 128
Q 6
M 129
WL 4
Q 8
M 140
Q 10
M 141
TX 90,7680
TX 94,21504
M 130
Q 12
M 131
R 52
M 142
Q 14
M 143
TY 98,8192
TY 102,22016
M 0
R 54
M 1
WL 10
Q 16
M 12
Q 18
M 13
Q 20
M 2
Q 22
M 3
WL 8
Q 24
M 14
Q 26
M 15
Q 28
M 4
Q 30
M 5
WL 6
Q 32
M 16
Q 34
M 17
Q 36
M 6
Q 38
M 7
WL 2
Q 40
M 18
Q 42
M 19
Q 44
M 8
Q 46
M 9
H 1
UL 6,30
M 20
UH 7,30
AD 164,6
M 21
AD 188,7
UL 6,31
M 10
H 2
UH 7,31
M 11
R 56
M 22
AD 212,6
AD 236,7
M 23
R 58
M 24
UL 6,32
UH 7,32
M 25
AD 165,6
AD 189,7
M 26
UL 6,33
UH 7,33
M 27
AD 213,6
AD 237,7
M 28
UL 6,34
UH 7,34
M 29
AD 166,6
AD 190,7
M 30
UL 6,35
UH 7,35
M 31
AD 214,6
AD 238,7
M 32
UL 6,36
UH 7,36
M 33
WV 5
AD 167,6
AD 191,7
M 34
WL 0
FP
UL 6,37
M 35
UH 7,37
AD 215,6
M 36
AD 239,7
M 37
M 38
M 39
M 40
M 41
M 42
M 43
M 44
M 45
FR
M 46
M 47
M 48
M 49
M 50
M 51
M 52
M 53
WL 0
FQ 156
M 54
)"
R"(M 55
M 56
FZ 156
R 72
M 57
mw 24
R 74
M 58
TY 106,9736
TY 110,23560
M 59
R 76
M 60
ad 84,1968
W 0
M 61
M 62
M 63
W 1
M 64
M 65
M 66
W 2
M 67
WL 3
AN 104,106
AN 105,107
M 68
SH 106
SH 107
M 69
mw 27
AN 106,106
AN 107,107
M 70
WL 2
AN 108,110
AN 109,111
M 71
R 78
M 72
SH 110
SH 111
M 73
AN 110,110
AN 111,111
M 74
TY 114,11272
TY 118,25096
M 75
ad 84,1974
W 0
M 76
M 77
M 78
W 1
M 79
M 80
M 81
W 2
M 82
ae 44,68,384
M 83
WL 1
R 80
M 84
mw 48
AN 112,114
AN 113,115
M 85
s_bfe_u32 s51, s50, 0x10009
SH 114
SH 115
M 86
AN 114,114
AN 115,115
M 87
WL 2
AN 116,118
AN 117,119
M 88
SH 118
SH 119
M 89
AN 118,118
AN 119,119
M 90
TY 122,12808
TY 126,26632
M 91
ad 84,2016
M 92
W 0
v_mad_i32_i24 v47, v49, s51, v48
M 93
XL 14,256
M 94
XL 16,1024
M 95
R 82
M 96
W 1
XL 18,1792
M 97
M 98
M 99
WL 3
AN 120,122
AN 121,123
M 100
SH 122
SH 123
M 101
AN 122,122
AN 123,123
M 102
WL 2
AN 124,126
AN 125,127
M 103
SH 126
SH 127
M 104
AN 126,126
AN 127,127
M 105
W 2
XL 20,2560
M 106
M 107
R 0
M 108
mw 51
M 109
ad 84,2022
W 0
M 110
M 111
M 112
W 1
M 113
M 114
M 115
W 2
M 116
M 117
M 118
bc 49,22,66,60
M 119
WL 2
R 2
M 120
mh 9
M 121
ad 66,4256
H 0
M 132
M 133
TX 58,9216
TX 62,23040
M 122
DG 1024
M 123
M 134
M 135
TY 66,9728
TY 70,23552
M 124
bc 48,22,64,56
M 125
mh 12
M 136
ad 64,4256
H 0
M 137
TX 74,10752
TX 78,24576
M 126
WL 4
Q 0
M 127
Q 2
M 138
Q 4
M 139
TY 82,11264
TY 86,25088
M 128
Q 6
M 129
WL 4
Q 8
M 140
Q 10
M 141
TX 90,12288
TX 94,26112
M 130
Q 12
M 131
R 4
M 142
Q 14
M 143
TY 98,12800
TY 102,26624
M 0
R 6
M 1
WL 10
Q 16
M 12
Q 18
M 13
Q 20
M 2
Q 22
M 3
WL 8
Q 24
M 14
Q 26
M 15
Q 28
M 4
Q 30
M 5
WL 6
Q 32
M 16
Q 34
M 17
Q 36
M 6
Q 38
M 7
WL 2
Q 40
M 18
Q 42
M 19
Q 44
M 8
Q 46
M 9
H 1
UL 6,38
M 20
UH 7,38
AD 168,6
M 21
AD 192,7
UL 6,39
M 10
H 2
UH 7,39
M 11
R 8
M 22
AD 216,6
AD 240,7
M 23
R 10
PA 12288
DF
M 24
UL 6,40
UH 7,40
M 25
AD 169,6
AD 193,7
M 26
UL 6,41
UH 7,41
M 27
AD 217,6
AD 241,7
M 28
UL 6,42
UH 7,42
M 29
AD 170,6
AD 194,7
M 30
UL 6,43
UH 7,43
M 31
AD 218,6
AD 242,7
M 32
UL 6,44
UH 7,44
M 33
WV 6
AD 171,6
AD 195,7
M 34
WL 0
FP
UL 6,45
M 35
UH 7,45
AD 219,6
M 36
AD 243,7
M 37
M 38
M 39
M 40
M 41
M 42
M 43
M 44
M 45
FR
M 46
M 47
M 48
M 49
M 50
M 51
M 52
M 53
WL 0
FQ 160
M 54
M 55
M 56
FZ 160
R 24
M 57
mw 72
R 26
M 58
TY 106,520
TY 110,14344
M 59
R 28
M 60
ad 84,2064
W 0
M 61
M 62
M 63
W 1
M 64
M 65
M 66
W 2
M 67
WL 3
AN 104,106
AN 105,107
M 68
SH 106
SH 107
M 69
mw 75
AN 106,106
AN 107,107
M 70
WL 2
AN 108,110
AN 109,111
M 71
R 30
M 72
SH 110
SH 111
M 73
AN 110,110
AN 111,111
M 74
TY 114,2056
TY 118,15880
M 75
ad 84,2070
W 0
M 76
M 77
M 78
W 1
M 79
M 80
M 81
W 2
M 82
M 83
R 32
M 84
mw 0
WL 3
AN 112,114
AN 113,115
M 85
SH 114
SH 115
M 86
AN 114,114
AN 115,115
)"
R"(M 87
WL 2
AN 116,118
AN 117,119
M 88
SH 118
SH 119
M 89
AN 118,118
AN 119,119
M 90
TY 122,3592
TY 126,17416
M 91
ad 84,2112
M 92
W 0
M 93
ae 44,68,480
M 94
s_bitcmp1_b32 s50, 10
s_cselect_b32 s51, 24576, 0
v_add_u32 v47, s51, v3
M 95
R 34
M 96
W 1
XL 22,256
M 97
XL 24,1024
M 98
XL 26,1792
M 99
WL 3
AN 120,122
AN 121,123
M 100
SH 122
SH 123
M 101
AN 122,122
AN 123,123
M 102
WL 2
AN 124,126
AN 125,127
M 103
SH 126
SH 127
M 104
AN 126,126
AN 127,127
M 105
W 2
XL 28,2560
M 106
M 107
R 48
M 108
mw 3
M 109
ad 84,2118
W 0
M 110
M 111
M 112
W 1
M 113
M 114
M 115
W 2
M 116
M 117
M 118
bc 49,23,66,60
M 119
WL 2
R 50
M 120
mh 18
M 121
ad 66,4448
H 0
M 132
M 133
TX 58,0
TX 62,13824
M 122
DG 1024
M 123
M 134
M 135
TY 66,512
TY 70,14336
M 124
bc 48,23,64,56
M 125
mh 21
M 136
ad 64,4448
H 0
M 137
TX 74,1536
TX 78,15360
M 126
WL 4
Q 0
M 127
Q 2
M 138
Q 4
M 139
TY 82,2048
TY 86,15872
M 128
Q 6
M 129
WL 4
Q 8
M 140
Q 10
M 141
TX 90,3072
TX 94,16896
M 130
Q 12
M 131
R 52
M 142
Q 14
M 143
TY 98,3584
TY 102,17408
M 0
R 54
M 1
WL 10
Q 16
M 12
Q 18
M 13
Q 20
M 2
Q 22
M 3
WL 8
Q 24
M 14
Q 26
M 15
Q 28
M 4
Q 30
M 5
WL 6
Q 32
M 16
Q 34
M 17
Q 36
M 6
Q 38
M 7
WL 2
Q 40
M 18
Q 42
M 19
Q 44
M 8
Q 46
M 9
H 1
UL 6,14
M 20
UH 7,14
AD 172,6
M 21
AD 196,7
UL 6,15
M 10
H 2
UH 7,15
M 11
R 56
M 22
AD 220,6
AD 244,7
M 23
R 58
M 24
UL 6,16
UH 7,16
M 25
AD 173,6
AD 197,7
M 26
UL 6,17
UH 7,17
M 27
AD 221,6
AD 245,7
M 28
UL 6,18
UH 7,18
M 29
AD 174,6
AD 198,7
M 30
UL 6,19
UH 7,19
M 31
AD 222,6
AD 246,7
M 32
UL 6,20
UH 7,20
M 33
WV 5
AD 175,6
AD 199,7
M 34
WL 0
FP
UL 6,21
M 35
UH 7,21
AD 223,6
M 36
AD 247,7
M 37
M 38
M 39
M 40
M 41
M 42
M 43
M 44
M 45
FR
M 46
M 47
M 48
M 49
M 50
M 51
M 52
M 53
WL 0
FQ 164
M 54
M 55
M 56
FZ 164
R 72
M 57
mw 24
R 74
M 58
TY 106,5128
TY 110,18952
M 59
R 76
M 60
ad 84,2160
W 0
M 61
M 62
M 63
W 1
M 64
M 65
M 66
W 2
M 67
WL 3
AN 104,106
AN 105,107
M 68
SH 106
SH 107
M 69
mw 27
AN 106,106
AN 107,107
M 70
WL 2
AN 108,110
AN 109,111
M 71
R 78
M 72
SH 110
SH 111
M 73
AN 110,110
AN 111,111
M 74
TY 114,6664
TY 118,20488
M 75
ad 84,2166
W 0
M 76
M 77
M 78
W 1
M 79
M 80
M 81
W 2
M 82
M 83
R 80
M 84
mw 48
WL 3
AN 112,114
AN 113,115
M 85
SH 114
SH 115
M 86
AN 114,114
AN 115,115
M 87
WL 2
AN 116,118
AN 117,119
M 88
SH 118
SH 119
M 89
AN 118,118
AN 119,119
M 90
TY 122,8200
TY 126,22024
M 91
ad 84,2208
M 92
W 0
M 93
ae 44,68,480
M 94
s_bfe_u32 s51, s50, 0x1000b
v_mad_i32_i24 v47, v49, s51, v48
XL 30,256
M 95
R 82
M 96
W 1
XL 32,1024
M 97
M 98
M 99
WL 3
AN 120,122
AN 121,123
M 100
SH 122
SH 123
M 101
AN 122,122
AN 123,123
M 102
WL 2
AN 124,126
AN 125,127
M 103
SH 126
SH 127
M 104
AN 126,126
AN 127,127
M 105
W 2
XL 34,1792
M 106
M 107
R 0
M 108
mw 51
XL 36,2560
M 109
ad 84,2214
W 0
M 110
M 111
M 112
W 1
M 113
M 114
M 115
W 2
M 116
M 117
M 118
bc 49,0,66,60
M 119
)"
R"(WL 2
R 2
M 120
mh 0
M 121
ad 66,64
H 0
M 132
M 133
TX 58,4608
TX 62,18432
M 122
DG 1024
M 123
M 134
M 135
TY 66,5120
TY 70,18944
M 124
bc 48,0,64,56
M 125
mh 3
M 136
ad 64,32
H 0
M 137
TX 74,6144
TX 78,19968
M 126
WL 4
Q 0
M 127
Q 2
M 138
Q 4
M 139
TY 82,6656
TY 86,20480
M 128
Q 6
M 129
WL 4
Q 8
M 140
Q 10
M 141
TX 90,7680
TX 94,21504
M 130
Q 12
M 131
R 4
M 142
Q 14
M 143
TY 98,8192
TY 102,22016
M 0
R 6
M 1
WL 10
Q 16
M 12
Q 18
M 13
Q 20
M 2
Q 22
M 3
WL 8
Q 24
M 14
Q 26
M 15
Q 28
M 4
Q 30
M 5
WL 6
Q 32
M 16
Q 34
M 17
Q 36
M 6
Q 38
M 7
WL 2
Q 40
M 18
Q 42
M 19
Q 44
M 8
Q 46
M 9
H 1
UL 6,22
M 20
UH 7,22
AD 176,6
M 21
AD 200,7
UL 6,23
M 10
H 2
UH 7,23
M 11
R 8
M 22
AD 224,6
AD 248,7
M 23
R 10
PA 12288
DF
M 24
UL 6,24
UH 7,24
M 25
AD 177,6
AD 201,7
M 26
UL 6,25
UH 7,25
M 27
AD 225,6
AD 249,7
M 28
UL 6,26
UH 7,26
M 29
AD 178,6
AD 202,7
M 30
UL 6,27
UH 7,27
M 31
AD 226,6
AD 250,7
M 32
UL 6,28
UH 7,28
M 33
WV 6
AD 179,6
AD 203,7
M 34
WL 0
FP
UL 6,29
M 35
UH 7,29
AD 227,6
M 36
AD 251,7
M 37
M 38
M 39
M 40
M 41
M 42
M 43
M 44
M 45
FR
M 46
M 47
M 48
M 49
M 50
M 51
M 52
M 53
WL 0
FQ 168
M 54
M 55
M 56
FZ 168
R 24
M 57
mw 72
R 26
M 58
TY 106,9736
TY 110,23560
M 59
R 28
M 60
ad 84,2256
W 0
M 61
M 62
M 63
W 1
M 64
M 65
M 66
W 2
M 67
WL 3
AN 104,106
AN 105,107
M 68
SH 106
SH 107
M 69
mw 75
AN 106,106
AN 107,107
M 70
WL 2
AN 108,110
AN 109,111
M 71
R 30
M 72
SH 110
SH 111
M 73
AN 110,110
AN 111,111
M 74
TY 114,11272
TY 118,25096
M 75
ad 84,2262
W 0
M 76
M 77
M 78
W 1
M 79
M 80
M 81
W 2
M 82
M 83
R 32
M 84
mw 0
WL 3
AN 112,114
AN 113,115
M 85
SH 114
SH 115
M 86
AN 114,114
AN 115,115
M 87
WL 2
AN 116,118
AN 117,119
M 88
SH 118
SH 119
M 89
AN 118,118
AN 119,119
M 90
TY 122,12808
TY 126,26632
M 91
ad 84,0
M 92
W 0
M 93
M 94
M 95
R 34
M 96
W 1
M 97
M 98
M 99
WL 3
AN 120,122
AN 121,123
M 100
SH 122
SH 123
M 101
AN 122,122
AN 123,123
M 102
WL 2
AN 124,126
AN 125,127
M 103
SH 126
SH 127
M 104
AN 126,126
AN 127,127
M 105
W 2
M 106
M 107
R 48
M 108
mw 3
M 109
ad 84,6
W 0
M 110
M 111
M 112
W 1
M 113
M 114
M 115
W 2
M 116
M 117
M 118
bc 49,1,66,60
M 119
WL 2
R 50
M 120
mh 9
M 121
ad 66,256
H 0
M 132
M 133
TX 58,9216
TX 62,23040
M 122
DG 1024
M 123
M 134
M 135
TY 66,9728
TY 70,23552
M 124
bc 48,1,64,56
M 125
mh 12
M 136
ad 64,256
H 0
M 137
TX 74,10752
TX 78,24576
M 126
WL 4
Q 0
M 127
Q 2
M 138
Q 4
M 139
TY 82,11264
TY 86,25088
M 128
Q 6
M 129
WL 4
Q 8
M 140
Q 10
M 141
TX 90,12288
TX 94,26112
M 130
Q 12
M 131
R 52
M 142
Q 14
M 143
TY 98,12800
TY 102,26624
M 0
R 54
M 1
WL 10
Q 16
M 12
Q 18
M 13
Q 20
M 2
Q 22
M 3
WL 8
Q 24
M 14
Q 26
M 15
Q 28
M 4
Q 30
M 5
WL 6
Q 32
M 16
)"
R"(Q 34
M 17
Q 36
M 6
Q 38
M 7
WL 2
Q 40
M 18
Q 42
M 19
Q 44
M 8
Q 46
M 9
DG 1024
M 20
UL 6,30
UH 7,30
M 21
AD 180,6
AD 204,7
M 10
mh 15
UL 6,31
UH 7,31
M 11
R 56
M 22
ad 64,227
H 0
M 23
R 58
M 24
AD 228,6
AD 252,7
M 25
DG 1024
M 26
UL 6,32
UH 7,32
BV 52,4608
M 27
AD 181,6
AD 205,7
M 28
UL 6,33
UH 7,33
M 29
AD 229,6
AD 253,7
M 30
UL 6,34
UH 7,34
M 31
AD 182,6
AD 206,7
M 32
UL 6,35
UH 7,35
M 33
WV 6
AD 230,6
AD 254,7
M 34
WL 1
FP
UL 6,36
M 35
UH 7,36
AD 183,6
M 36
AD 207,7
UL 6,37
M 37
UH 7,37
AD 231,6
M 38
AD 255,7
M 39
M 40
ds_read_b64 v[12:13], v4 offset:4864
ds_read_b64 v[46:47], v4 offset:4872
M 41
AR 6,36
AR 7,60
M 42
AR 8,84
AR 9,108
M 43
WL 3
EF 6,52
EF 7,53
M 44
EF 8,54
EF 9,55
M 45
FR
CV 38,6,7
M 46
CV 39,8,9
AR 6,37
M 47
AR 7,61
AR 8,85
M 48
AR 9,109
EF 6,52
M 49
EF 7,53
EF 8,54
M 50
EF 9,55
CV 40,6,7
M 51
CV 41,8,9
AR 6,38
M 52
AR 7,62
AR 8,86
M 53
WL 0
FQ 172
AR 9,110
M 54
EF 6,52
EF 7,53
M 55
EF 8,54
EF 9,55
M 56
FZ 172
R 72
M 57
mw 24
R 74
M 58
TY 106,520
TY 110,14344
M 59
R 76
M 60
ad 84,48
W 0
M 61
CV 42,6,7
CV 43,8,9
M 62
AR 6,39
AR 7,63
M 63
W 1
AR 8,87
M 64
AR 9,111
EF 6,52
M 65
EF 7,53
EF 8,54
M 66
W 2
EF 9,55
M 67
WL 3
AN 104,106
AN 105,107
M 68
SH 106
SH 107
M 69
mw 27
AN 106,106
AN 107,107
M 70
WL 2
AN 108,110
AN 109,111
M 71
R 78
M 72
SH 110
SH 111
M 73
AN 110,110
AN 111,111
M 74
TY 114,2056
TY 118,15880
M 75
ad 84,54
W 0
M 76
CV 44,6,7
CV 45,8,9
M 77
ae 42,74,384
AR 6,40
M 78
W 1
AR 7,64
M 79
s_bitcmp1_b32 s50, 6
s_cselect_b32 s52, 24576, 0
v_add_u32 v50, s52, v3
ST 38,0
M 80
AR 8,88
AR 9,112
M 81
W 2
EF 6,52
M 82
EF 7,53
EF 8,54
M 83
WL 1
R 80
M 84
mw 48
AN 112,114
AN 113,115
M 85
SH 114
SH 115
M 86
AN 114,114
AN 115,115
M 87
WL 2
AN 116,118
AN 117,119
M 88
SH 118
SH 119
M 89
AN 118,118
AN 119,119
M 90
TY 122,3592
TY 126,17416
M 91
ad 84,96
ST 40,768
M 92
W 0
EF 9,55
M 93
CV 14,6,7
CV 15,8,9
M 94
AR 6,41
AR 7,65
M 95
R 82
M 96
W 1
AR 8,89
M 97
AR 9,113
EF 6,52
M 98
EF 7,53
EF 8,54
M 99
WL 3
AN 120,122
AN 121,123
M 100
SH 122
SH 123
M 101
AN 122,122
AN 123,123
M 102
WL 2
AN 124,126
AN 125,127
M 103
SH 126
SH 127
ST 42,1536
M 104
AN 126,126
AN 127,127
M 105
W 2
EF 9,55
M 106
CV 16,6,7
CV 17,8,9
M 107
R 0
M 108
mw 51
AR 6,42
AR 7,66
M 109
ad 84,102
W 0
M 110
AR 8,90
AR 9,114
M 111
EF 6,52
EF 7,53
M 112
W 1
EF 8,54
M 113
EF 9,55
CV 18,6,7
M 114
CV 19,8,9
AR 6,43
M 115
W 2
ST 44,2304
M 116
AR 7,67
AR 8,91
M 117
AR 9,115
EF 6,52
M 118
bc 49,2,66,60
EF 7,53
M 119
WL 0
R 2
M 120
mh 18
EF 8,54
EF 9,55
M 121
ad 66,448
H 0
M 132
CV 20,6,7
CV 21,8,9
M 133
TX 58,0
TX 62,13824
BV 52,5120
M 122
DG 1024
M 123
AR 6,132
AR 7,156
M 134
AR 8,180
EF 6,12
M 135
TY 66,512
TY 70,14336
M 124
bc 48,2,64,56
ae 42,74,384
M 125
mh 21
EF 7,13
EF 8,46
M 136
ad 64,448
H 0
M 137
WL 3
TX 74,1536
TX 78,15360
M 126
s_bfe_u32 s52, s50, 0x10007
Q 0
v_mad_i32_i24 v50, v49, s52, v48
M 127
Q 2
ST 14,0
M 138
Q 4
M 139
TY 82,2048
TY 86,15872
M 128
Q 6
M 129
WL 4
Q 8
M 140
Q 10
M 141
TX 90,3072
TX 94,16896
M 130
Q 12
M 131
R 4
M 142
Q 14
M 143
TY 98,3584
TY 102,17408
v_mfma_f32_16x16x32_f16 a[36:39], v[56:59], a[0:3], a[32:35]
)"
R"(R 6
M 1
WL 10
Q 16
ST 16,768
v_mfma_f32_16x16x32_f16 a[60:63], v[56:59], a[8:11], a[32:35]
Q 18
M 13
Q 20
v_mfma_f32_16x16x32_f16 a[40:43], v[64:67], a[0:3], a[32:35]
Q 22
M 3
WL 8
Q 24
v_mfma_f32_16x16x32_f16 a[64:67], v[64:67], a[8:11], a[32:35]
Q 26
M 15
Q 28
M 4
Q 30
M 5
WL 6
Q 32
M 16
Q 34
M 17
Q 36
M 6
Q 38
M 7
WL 1
Q 40
ST 18,1536
M 18
Q 42
M 19
Q 44
M 8
Q 46
M 9
H 1
EF 136,47
M 20
CV 22,6,7
CV 23,8,136
M 21
AR 6,133
AR 7,157
M 10
H 2
AR 8,181
M 11
R 8
M 22
EF 6,12
EF 7,13
M 23
R 10
PA 12288
DF
v_mfma_f32_16x16x32_f16 a[84:87], v[56:59], a[16:19], a[32:35]
EF 8,46
EF 137,47
M 25
ST 20,2304
CV 24,6,7
v_mfma_f32_16x16x32_f16 a[88:91], v[64:67], a[16:19], a[32:35]
CV 25,8,137
AR 6,134
M 27
AR 7,158
AR 8,182
M 28
EF 6,12
EF 7,13
M 29
EF 8,46
EF 138,47
M 30
CV 26,6,7
CV 27,8,138
M 31
AR 6,135
AR 7,159
M 32
AR 8,183
EF 6,12
M 33
WV 11
EF 7,13
EF 8,46
M 34
WL 0
FP
EF 139,47
M 35
CV 28,6,7
CV 29,8,139
v_mfma_f32_16x16x32_f16 a[108:111], v[56:59], a[24:27], a[32:35]
AR 6,136
AR 7,160
AR 8,184
EF 6,12
M 37
ae 42,74,384
EF 7,13
EF 8,46
EF 140,47
v_mfma_f32_16x16x32_f16 a[112:115], v[64:67], a[24:27], a[32:35]
s_bitcmp1_b32 s50, 6
s_cselect_b32 s52, 24576, 0
v_add_u32 v50, s52, v3
ST 22,128
CV 30,6,7
M 39
CV 31,8,140
AR 6,137
AR 7,161
AR 8,185
M 40
EF 6,12
EF 7,13
EF 8,46
EF 141,47
M 41
CV 32,6,7
CV 33,8,141
AR 6,138
AR 7,162
M 42
AR 8,186
EF 6,12
EF 7,13
EF 8,46
M 43
EF 142,47
CV 34,6,7
CV 35,8,142
AR 6,139
M 44
AR 7,163
AR 8,187
EF 6,12
EF 7,13
M 45
FR
EF 8,46
EF 143,47
CV 36,6,7
M 46
CV 37,8,143
M 47
ds_read_b64 v[12:13], v4 offset:4608
ds_read_b64 v[46:47], v4 offset:4616
v_mfma_f32_16x16x32_f16 a[132:135], v[56:59], a[0:3], a[32:35]
EF 160,52
EF 184,53
M 49
EF 208,54
EF 232,55
v_mfma_f32_16x16x32_f16 a[136:139], v[64:67], a[0:3], a[32:35]
ST 24,896
CV 38,160,184
M 51
CV 39,208,232
EF 161,52
M 52
EF 185,53
EF 209,54
M 53
WL 2
FQ 176
EF 233,55
M 54
CV 40,161,185
CV 41,209,233
M 55
EF 162,52
EF 186,53
M 56
FZ 176
R 24
M 57
mw 72
R 26
M 58
TY 106,5128
TY 110,18952
M 59
R 28
v_mfma_f32_16x16x32_f16 a[156:159], v[56:59], a[8:11], a[32:35]
ad 84,144
W 0
M 61
EF 210,54
EF 234,55
v_mfma_f32_16x16x32_f16 a[160:163], v[64:67], a[8:11], a[32:35]
ST 26,1664
CV 42,162,186
M 63
W 1
CV 43,210,234
M 64
EF 163,52
EF 187,53
M 65
EF 211,54
EF 235,55
M 66
W 2
CV 44,163,187
M 67
WL 3
AN 104,106
AN 105,107
M 68
SH 106
SH 107
M 69
mw 75
AN 106,106
AN 107,107
M 70
WL 2
AN 108,110
AN 109,111
M 71
R 30
v_mfma_f32_16x16x32_f16 a[180:183], v[56:59], a[16:19], a[32:35]
SH 110
SH 111
M 73
AN 110,110
AN 111,111
v_mfma_f32_16x16x32_f16 a[184:187], v[64:67], a[16:19], a[32:35]
TY 114,6664
TY 118,20488
ST 28,2432
M 75
ad 84,150
W 0
M 76
CV 45,211,235
M 77
EF 164,52
EF 188,53
EF 212,54
EF 236,55
M 78
W 1
CV 14,164,188
CV 15,212,236
EF 165,52
M 79
EF 189,53
EF 213,54
EF 237,55
CV 16,165,189
M 80
CV 17,213,237
EF 166,52
EF 190,53
EF 214,54
M 81
W 2
EF 238,55
CV 18,166,190
CV 19,214,238
M 82
EF 167,52
EF 191,53
EF 215,54
EF 239,55
M 83
WL 0
R 32
CV 20,167,191
CV 21,215,239
v_mfma_f32_16x16x32_f16 v[136:139], v[56:59], a[24:27], v[128:131]
mw 0
AN 112,114
AN 113,115
BV 52,4864
M 85
SH 114
SH 115
AR 6,44
v_mfma_f32_16x16x32_f16 v[140:143], v[64:67], a[24:27], v[128:131]
AN 114,114
AN 115,115
AR 7,68
M 87
AN 116,118
AN 117,119
AR 8,92
M 88
SH 118
SH 119
AR 9,116
M 89
AN 118,118
AN 119,119
EF 6,12
M 90
TY 122,8200
TY 126,22024
EF 7,13
M 91
ad 84,192
EF 8,46
M 92
W 0
ae 42,74,384
M 93
s_bfe_u32 s52, s50, 0x10007
v_mad_i32_i24 v50, v49, s52, v48
ST 30,128
EF 9,47
M 94
CV 22,6,7
CV 23,8,9
AR 6,45
M 95
R 34
AR 7,69
v_mfma_f32_16x16x32_f16 v[160:163], v[56:59], a[0:3], v[128:131]
W 1
AR 8,93
AR 9,117
M 97
EF 6,12
EF 7,13
EF 8,46
v_mfma_f32_16x16x32_f16 v[164:167], v[64:67], a[0:3], v[128:131]
EF 9,47
CV 24,6,7
CV 25,8,9
M 99
WL 3
AN 120,122
AN 121,123
AR 6,46
M 100
SH 122
SH 123
AR 7,70
M 101
AN 122,122
AN 123,123
AR 8,94
M 102
WL 2
AN 124,126
AN 125,127
AR 9,118
M 103
SH 126
SH 127
EF 6,12
M 104
AN 126,126
AN 127,127
EF 7,13
M 105
W 2
ST 32,896
EF 8,46
M 106
EF 9,47
CV 26,6,7
CV 27,8,9
M 107
R 48
AR 6,47
v_mfma_f32_16x16x32_f16 v[184:187], v[56:59], a[8:11], v[128:131]
mw 3
AR 7,71
AR 8,95
AR 9,119
M 109
ad 84,198
W 0
EF 6,12
v_mfma_f32_16x16x32_f16 v[188:191], v[64:67], a[8:11], v[128:131]
EF 7,13
EF 8,46
EF 9,47
M 111
CV 28,6,7
CV 29,8,9
M 112
W 1
AR 6,48
AR 7,72
M 113
AR 8,96
AR 9,120
EF 6,12
M 114
EF 7,13
)"
R"(EF 8,46
EF 9,47
M 115
W 2
CV 6,6,7
CV 7,8,9
M 116
AW 200,6
AW 201,7
AR 6,49
M 117
ST 34,1664
AR 7,73
AR 8,97
M 118
bc 49,3,66,60
AR 9,121
EF 6,12
M 119
WL 0
R 50
EF 7,13
v_mfma_f32_16x16x32_f16 v[208:211], v[56:59], a[16:19], v[128:131]
mh 0
EF 8,46
EF 9,47
CV 6,6,7
M 121
ad 66,640
H 0
CV 7,8,9
v_mfma_f32_16x16x32_f16 v[232:235], v[56:59], a[24:27], v[128:131]
AW 202,6
AW 203,7
AR 6,50
M 133
TX 58,4608
TX 62,18432
AR 7,74
v_mfma_f32_16x16x32_f16 v[212:215], v[64:67], a[16:19], v[128:131]
DG 1024
AR 8,98
M 123
AR 9,122
EF 6,12
EF 7,13
v_mfma_f32_16x16x32_f16 v[236:239], v[64:67], a[24:27], v[128:131]
EF 8,46
EF 9,47
CV 6,6,7
M 135
TY 66,5120
TY 70,18944
CV 7,8,9
M 124
bc 48,3,64,56
AW 204,6
AW 205,7
M 125
mh 3
ST 36,2432
AR 6,51
AR 7,75
M 136
ad 64,640
H 0
AR 8,99
M 137
TX 74,6144
TX 78,19968
AR 9,123
M 126
WL 4
Q 0
EF 6,12
M 127
Q 2
EF 7,13
M 138
Q 4
EF 8,46
M 139
TY 82,6656
TY 86,20480
EF 9,47
M 128
Q 6
CV 6,6,7
M 129
WL 4
Q 8
CV 7,8,9
M 140
Q 10
AW 206,6
M 141
TX 90,7680
TX 94,21504
AW 207,7
M 130
Q 12
ds_read_b64 v[12:13], v4 offset:5120
M 131
R 52
ds_read_b64 v[46:47], v4 offset:5128
M 142
Q 14
M 143
TY 98,8192
TY 102,22016
M 0
R 54
M 1
WL 12
Q 16
M 12
Q 18
M 13
Q 20
M 2
Q 22
M 3
WL 10
Q 24
M 14
Q 26
M 15
Q 28
v_mfma_f32_16x16x32_f16 a[44:47], v[72:75], a[0:3], a[32:35]
Q 30
M 5
WL 7
Q 32
v_mfma_f32_16x16x32_f16 a[68:71], v[72:75], a[8:11], a[32:35]
Q 34
M 17
Q 36
v_mfma_f32_16x16x32_f16 a[48:51], v[80:83], a[0:3], a[32:35]
Q 38
M 7
WL 2
Q 40
v_mfma_f32_16x16x32_f16 a[72:75], v[80:83], a[8:11], a[32:35]
Q 42
M 19
Q 44
M 8
Q 46
M 9
H 1
ae 42,74,384
M 20
s_bitcmp1_b32 s50, 6
s_cselect_b32 s52, 24576, 0
v_add_u32 v50, s52, v3
ST 38,256
M 21
AR 6,140
AR 7,164
M 10
H 2
AR 8,188
M 11
R 56
M 22
EF 6,52
EF 7,53
M 23
R 58
M 24
EF 8,54
EF 144,55
M 25
CV 30,6,7
CV 31,8,144
M 26
AR 6,141
AR 7,165
M 27
AR 8,189
EF 6,52
v_mfma_f32_16x16x32_f16 a[92:95], v[72:75], a[16:19], a[32:35]
EF 7,53
EF 8,54
M 29
EF 145,55
CV 32,6,7
v_mfma_f32_16x16x32_f16 a[96:99], v[80:83], a[16:19], a[32:35]
ST 40,1024
CV 33,8,145
M 31
AR 6,142
AR 7,166
M 32
AR 8,190
EF 6,52
M 33
WV 9
EF 7,53
EF 8,54
M 34
WL 0
FP
EF 146,55
M 35
CV 34,6,7
CV 35,8,146
M 36
AR 6,143
AR 7,167
M 37
AR 8,191
EF 6,52
M 38
EF 7,53
EF 8,54
M 39
EF 147,55
CV 36,6,7
v_mfma_f32_16x16x32_f16 a[116:119], v[72:75], a[24:27], a[32:35]
CV 37,8,147
M 41
AR 6,144
AR 7,168
AR 8,192
EF 6,52
v_mfma_f32_16x16x32_f16 a[120:123], v[80:83], a[24:27], a[32:35]
ST 42,1792
EF 7,53
EF 8,54
EF 148,55
M 43
CV 6,6,7
CV 7,8,148
AW 208,6
AW 209,7
M 44
AR 6,145
AR 7,169
AR 8,193
EF 6,52
M 45
FR
EF 7,53
EF 8,54
EF 149,55
M 46
CV 6,6,7
CV 7,8,149
AW 210,6
AW 211,7
M 47
AR 6,146
AR 7,170
AR 8,194
EF 6,52
M 48
EF 7,53
EF 8,54
EF 150,55
CV 6,6,7
M 49
CV 7,8,150
AW 212,6
AW 213,7
AR 6,147
M 50
AR 7,171
AR 8,195
EF 6,52
EF 7,53
M 51
EF 8,54
EF 151,55
CV 6,6,7
CV 7,8,151
v_mfma_f32_16x16x32_f16 a[140:143], v[72:75], a[0:3], a[32:35]
AW 214,6
AW 215,7
M 53
WL 0
FQ 180
BV 52,4608
EF 168,12
v_mfma_f32_16x16x32_f16 a[144:147], v[80:83], a[0:3], a[32:35]
ST 44,2560
EF 192,13
EF 216,46
M 55
EF 240,47
CV 6,168,192
CV 7,216,240
M 56
FZ 180
R 72
AW 216,6
M 57
mw 24
R 74
AW 217,7
M 58
TY 106,9736
TY 110,23560
EF 169,12
M 59
R 76
EF 193,13
M 60
ad 84,240
W 0
EF 217,46
M 61
EF 241,47
CV 6,169,193
CV 7,217,241
M 62
AW 218,6
AW 219,7
EF 170,12
M 63
W 1
EF 194,13
EF 218,46
v_mfma_f32_16x16x32_f16 a[164:167], v[72:75], a[8:11], a[32:35]
EF 242,47
CV 6,170,194
CV 7,218,242
M 65
AW 220,6
AW 221,7
EF 171,12
v_mfma_f32_16x16x32_f16 a[168:171], v[80:83], a[8:11], a[32:35]
WL 3
W 2
ae 42,74,384
M 67
s_bfe_u32 s52, s50, 0x10007
AN 104,106
AN 105,107
v_mad_i32_i24 v50, v49, s52, v48
M 68
SH 106
SH 107
ST 14,256
M 69
mw 27
AN 106,106
AN 107,107
EF 195,13
M 70
WL 0
AN 108,110
AN 109,111
EF 219,46
M 71
R 78
EF 243,47
M 72
SH 110
SH 111
CV 6,171,195
M 73
AN 110,110
AN 111,111
CV 7,219,243
M 74
TY 114,11272
TY 118,25096
AW 222,6
M 75
ad 84,246
W 0
AW 223,7
v_mfma_f32_16x16x32_f16 a[188:191], v[72:75], a[16:19], a[32:35]
EF 172,12
EF 196,13
EF 220,46
M 77
EF 244,47
CV 38,172,196
CV 39,220,244
v_mfma_f32_16x16x32_f16 a[192:195], v[80:83], a[16:19], a[32:35]
W 1
EF 173,12
EF 197,13
M 79
EF 221,46
EF 245,47
CV 40,173,197
M 80
ST 16,1024
CV 41,221,245
EF 174,12
M 81
W 2
EF 198,13
EF 222,46
M 82
EF 246,47
CV 42,174,198
)"
R"(CV 43,222,246
M 83
WL 0
R 80
EF 175,12
M 84
mw 48
AN 112,114
AN 113,115
EF 199,13
M 85
SH 114
SH 115
EF 223,46
M 86
AN 114,114
AN 115,115
EF 247,47
M 87
AN 116,118
AN 117,119
CV 44,175,199
v_mfma_f32_16x16x32_f16 v[144:147], v[72:75], a[24:27], v[128:131]
SH 118
SH 119
CV 45,223,247
M 89
AN 118,118
AN 119,119
ds_read_b64 v[12:13], v4 offset:4864
v_mfma_f32_16x16x32_f16 v[148:151], v[80:83], a[24:27], v[128:131]
TY 122,12808
TY 126,26632
ds_read_b64 v[46:47], v4 offset:4872
M 91
ad 84,288
AR 6,52
M 92
W 0
ST 18,1792
AR 7,76
M 93
AR 8,100
AR 9,124
EF 6,52
M 94
EF 7,53
EF 8,54
EF 9,55
M 95
R 82
CV 6,6,7
M 96
W 1
CV 7,8,9
AW 224,6
M 97
AW 225,7
AR 6,53
AR 7,77
M 98
AR 8,101
AR 9,125
EF 6,52
M 99
WL 4
AN 120,122
AN 121,123
EF 7,53
v_mfma_f32_16x16x32_f16 v[168:171], v[72:75], a[0:3], v[128:131]
SH 122
SH 123
EF 8,54
M 101
AN 122,122
AN 123,123
EF 9,55
v_mfma_f32_16x16x32_f16 v[172:175], v[80:83], a[0:3], v[128:131]
WL 2
AN 124,126
AN 125,127
CV 6,6,7
M 103
SH 126
SH 127
CV 7,8,9
M 104
AN 126,126
AN 127,127
ST 20,2560
M 105
W 2
AW 226,6
AW 227,7
M 106
AR 6,54
AR 7,78
AR 8,102
M 107
R 0
AR 9,126
M 108
mw 51
EF 6,52
EF 7,53
EF 8,54
M 109
ad 84,294
W 0
EF 9,55
M 110
CV 6,6,7
CV 7,8,9
AW 228,6
M 111
AW 229,7
AR 6,55
AR 7,79
v_mfma_f32_16x16x32_f16 v[192:195], v[72:75], a[8:11], v[128:131]
W 1
AR 8,103
AR 9,127
M 113
EF 6,52
EF 7,53
EF 8,54
v_mfma_f32_16x16x32_f16 v[196:199], v[80:83], a[8:11], v[128:131]
EF 9,55
CV 6,6,7
CV 7,8,9
M 115
W 2
AW 230,6
AW 231,7
M 116
ae 42,72,384
AR 6,56
AR 7,80
M 117
s_bitcmp1_b32 s50, 8
s_cselect_b32 s52, 24576, 0
v_add_u32 v50, s52, v3
ST 22,0
M 118
bc 49,4,66,60
AR 8,104
AR 9,128
M 119
WL 0
R 2
EF 6,52
M 120
mh 9
EF 7,53
EF 8,54
EF 9,55
M 121
ad 66,832
H 0
CV 14,6,7
M 132
CV 15,8,9
AR 6,57
AR 7,81
M 133
TX 58,9216
TX 62,23040
AR 8,105
M 122
DG 1024
AR 9,129
M 123
EF 6,52
EF 7,53
EF 8,54
M 134
EF 9,55
CV 16,6,7
CV 17,8,9
M 135
TY 66,9728
TY 70,23552
AR 6,58
v_mfma_f32_16x16x32_f16 v[216:219], v[72:75], a[16:19], v[128:131]
bc 48,4,64,56
AR 7,82
AR 8,106
M 125
mh 12
ST 24,768
AR 9,130
EF 6,52
v_mfma_f32_16x16x32_f16 v[240:243], v[72:75], a[24:27], v[128:131]
ad 64,832
H 0
EF 7,53
M 137
TX 74,10752
TX 78,24576
EF 8,54
v_mfma_f32_16x16x32_f16 v[220:223], v[80:83], a[16:19], v[128:131]
WL 4
Q 0
EF 9,55
M 127
Q 2
CV 18,6,7
v_mfma_f32_16x16x32_f16 v[244:247], v[80:83], a[24:27], v[128:131]
Q 4
CV 19,8,9
M 139
TY 82,11264
TY 86,25088
AR 6,59
M 128
Q 6
AR 7,83
M 129
WL 4
Q 8
AR 8,107
M 140
Q 10
AR 9,131
M 141
TX 90,12288
TX 94,26112
EF 6,52
M 130
Q 12
EF 7,53
M 131
R 4
ST 26,1536
M 142
Q 14
EF 8,54
M 143
TY 98,12800
TY 102,26624
EF 9,55
M 0
R 6
CV 20,6,7
M 1
WL 8
Q 16
CV 21,8,9
M 12
Q 18
BV 52,5120
M 13
Q 20
M 2
Q 22
M 3
Q 24
M 14
Q 26
M 15
Q 28
M 4
Q 30
M 5
WL 5
Q 32
ST 28,2304
M 16
Q 34
M 17
Q 36
M 6
Q 38
M 7
WL 1
Q 40
M 18
Q 42
M 19
Q 44
v_mfma_f32_16x16x32_f16 a[52:55], v[88:91], a[0:3], a[32:35]
Q 46
M 9
H 1
AR 6,148
v_mfma_f32_16x16x32_f16 a[76:79], v[88:91], a[8:11], a[32:35]
AR 7,172
AR 8,196
M 21
EF 6,12
EF 7,13
v_mfma_f32_16x16x32_f16 a[56:59], v[96:99], a[0:3], a[32:35]
H 2
EF 8,46
M 11
R 8
v_mfma_f32_16x16x32_f16 a[80:83], v[96:99], a[8:11], a[32:35]
ae 42,72,384
ST 30,128
M 23
R 10
PA 12288
DF
M 24
EF 152,47
CV 6,6,7
M 25
CV 7,8,152
AW 232,6
M 26
AW 233,7
AR 6,149
M 27
AR 7,173
AR 8,197
M 28
EF 6,12
EF 7,13
M 29
EF 8,46
EF 153,47
M 30
CV 6,6,7
CV 7,8,153
M 31
AW 234,6
AW 235,7
v_mfma_f32_16x16x32_f16 a[100:103], v[88:91], a[16:19], a[32:35]
AR 6,150
AR 7,174
M 33
WV 11
AR 8,198
EF 6,12
v_mfma_f32_16x16x32_f16 a[104:107], v[96:99], a[16:19], a[32:35]
WL 0
FP
ST 32,896
M 35
EF 7,13
EF 8,46
M 36
EF 154,47
CV 6,6,7
M 37
CV 7,8,154
AW 236,6
M 38
AW 237,7
AR 6,151
M 39
AR 7,175
AR 8,199
M 40
EF 6,12
EF 7,13
M 41
EF 8,46
EF 155,47
M 42
CV 6,6,7
CV 7,8,155
M 43
AW 238,6
AW 239,7
v_mfma_f32_16x16x32_f16 a[124:127], v[88:91], a[24:27], a[32:35]
AR 6,152
AR 7,176
EF 6,12
M 45
FR
EF 7,13
EF 132,46
v_mfma_f32_16x16x32_f16 a[128:131], v[96:99], a[24:27], a[32:35]
ST 34,1664
EF 156,47
CV 22,6,7
M 47
CV 23,132,156
AR 6,153
AR 7,177
M 48
EF 6,12
EF 7,13
EF 133,46
M 49
EF 157,47
CV 24,6,7
CV 25,133,157
M 50
AR 6,154
AR 7,178
EF 6,12
M 51
EF 7,13
EF 134,46
EF 158,47
M 52
CV 26,6,7
CV 27,134,158
)"
R"(AR 6,155
M 53
WL 0
FQ 184
AR 7,179
EF 6,12
M 54
EF 7,13
EF 135,46
EF 159,47
M 55
CV 28,6,7
CV 29,135,159
v_mfma_f32_16x16x32_f16 a[148:151], v[88:91], a[0:3], a[32:35]
FZ 184
R 24
EF 176,52
M 57
mw 72
R 26
EF 200,53
v_mfma_f32_16x16x32_f16 a[152:155], v[96:99], a[0:3], a[32:35]
TY 106,520
TY 110,14344
ST 36,2432
M 59
R 28
EF 224,54
M 60
ad 84,336
W 0
EF 248,55
M 61
CV 6,176,200
CV 7,224,248
AW 240,6
M 62
AW 241,7
EF 177,52
EF 201,53
M 63
W 1
EF 225,54
EF 249,55
M 64
CV 6,177,201
CV 7,225,249
AW 242,6
M 65
AW 243,7
EF 178,52
EF 202,53
M 66
W 2
EF 226,54
EF 250,55
M 67
WL 3
AN 104,106
AN 105,107
CV 6,178,202
v_mfma_f32_16x16x32_f16 a[172:175], v[88:91], a[8:11], a[32:35]
SH 106
SH 107
CV 7,226,250
M 69
mw 75
AN 106,106
AN 107,107
AW 244,6
v_mfma_f32_16x16x32_f16 a[176:179], v[96:99], a[8:11], a[32:35]
WL 0
AN 108,110
AN 109,111
AW 245,7
M 71
R 30
EF 179,52
M 72
SH 110
SH 111
EF 203,53
M 73
AN 110,110
AN 111,111
EF 227,54
M 74
TY 114,2056
TY 118,15880
EF 251,55
M 75
ad 84,342
W 0
CV 6,179,203
M 76
ae 42,72,384
CV 7,227,251
AW 246,6
M 77
s_bfe_u32 s52, s50, 0x10009
v_mad_i32_i24 v50, v49, s52, v48
ST 38,256
AW 247,7
M 78
W 1
EF 180,52
EF 204,53
M 79
EF 228,54
EF 252,55
CV 6,180,204
v_mfma_f32_16x16x32_f16 a[196:199], v[88:91], a[16:19], a[32:35]
CV 7,228,252
AW 248,6
AW 249,7
M 81
W 2
EF 181,52
EF 205,53
v_mfma_f32_16x16x32_f16 v[132:135], v[96:99], a[16:19], v[128:131]
EF 229,54
EF 253,55
CV 6,181,205
M 83
WL 0
R 32
CV 7,229,253
M 84
mw 0
AN 112,114
AN 113,115
AW 250,6
M 85
SH 114
SH 115
AW 251,7
M 86
AN 114,114
AN 115,115
EF 182,52
M 87
AN 116,118
AN 117,119
EF 206,53
M 88
SH 118
SH 119
EF 230,54
M 89
AN 118,118
AN 119,119
ST 40,1024
M 90
TY 122,3592
TY 126,17416
EF 254,55
M 91
ad 84,384
CV 6,182,206
v_mfma_f32_16x16x32_f16 v[152:155], v[88:91], a[24:27], v[128:131]
W 0
CV 7,230,254
AW 252,6
M 93
AW 253,7
EF 183,52
EF 207,53
v_mfma_f32_16x16x32_f16 v[156:159], v[96:99], a[24:27], v[128:131]
EF 231,54
EF 255,55
CV 6,183,207
M 95
R 34
CV 7,231,255
M 96
W 1
AW 254,6
AW 255,7
M 97
M 98
M 99
WL 3
AN 120,122
AN 121,123
M 100
SH 122
SH 123
M 101
AN 122,122
AN 123,123
ST 42,1792
M 102
WL 2
AN 124,126
AN 125,127
M 103
SH 126
SH 127
v_mfma_f32_16x16x32_f16 v[176:179], v[88:91], a[0:3], v[128:131]
AN 126,126
AN 127,127
M 105
W 2
v_mfma_f32_16x16x32_f16 v[180:183], v[96:99], a[0:3], v[128:131]
M 107
R 48
M 108
mw 3
M 109
ad 84,390
W 0
M 110
M 111
M 112
W 1
M 113
ST 44,2560
M 114
M 115
W 2
v_mfma_f32_16x16x32_f16 v[200:203], v[88:91], a[8:11], v[128:131]
M 117
v_mfma_f32_16x16x32_f16 v[204:207], v[96:99], a[8:11], v[128:131]
bc 49,5,66,60
M 119
WL 2
R 50
M 120
mh 18
M 121
ad 66,1024
H 0
M 132
M 133
TX 58,0
TX 62,13824
M 122
DG 1024
M 123
ae 42,72,480
M 134
s_bfe_u32 s52, s50, 0x1000b
v_mad_i32_i24 v50, v49, s52, v48
ST 14,0
M 135
TY 66,512
TY 70,14336
M 124
bc 48,5,64,56
M 125
mh 21
M 136
ad 64,1024
H 0
M 137
TX 74,1536
TX 78,15360
M 126
WL 4
Q 0
M 127
Q 2
M 138
Q 4
M 139
TY 82,2048
TY 86,15872
v_mfma_f32_16x16x32_f16 v[224:227], v[88:91], a[16:19], v[128:131]
Q 6
M 129
WL 4
Q 8
v_mfma_f32_16x16x32_f16 v[248:251], v[88:91], a[24:27], v[128:131]
Q 10
ST 16,768
M 141
TX 90,3072
TX 94,16896
v_mfma_f32_16x16x32_f16 v[228:231], v[96:99], a[16:19], v[128:131]
Q 12
M 131
R 52
v_mfma_f32_16x16x32_f16 v[252:255], v[96:99], a[24:27], v[128:131]
Q 14
M 143
TY 98,3584
TY 102,17408
M 0
R 54
M 1
WL 10
Q 16
M 12
Q 18
M 13
Q 20
M 2
Q 22
M 3
WL 8
Q 24
M 14
Q 26
ST 18,1536
M 15
Q 28
M 4
Q 30
M 5
WL 6
Q 32
M 16
Q 34
M 17
Q 36
M 6
Q 38
M 7
WL 1
Q 40
M 18
Q 42
M 19
Q 44
M 8
Q 46
M 9
H 1
M 20
ST 20,2304
M 21
M 10
H 2
M 11
R 56
M 22
M 23
R 58
M 24
M 25
M 26
M 27
M 28
M 29
M 30
ae 42,72,480
ST 22,128
M 31
M 32
M 33
WV 10
M 34
WL 0
FP
M 35
M 36
M 37
M 38
M 39
M 40
M 41
M 42
ST 24,896
M 43
M 44
M 45
FR
M 46
M 47
M 48
M 49
M 50
M 51
M 52
M 53
WL 0
FQ 188
M 54
ST 26,1664
M 55
M 56
FZ 188
R 72
M 57
mw 24
R 74
M 58
TY 106,5128
TY 110,18952
M 59
R 76
M 60
ad 84,432
W 0
M 61
M 62
M 63
W 1
M 64
M 65
)"
R"(M 66
W 2
ST 28,2432
M 67
WL 3
AN 104,106
AN 105,107
M 68
SH 106
SH 107
M 69
mw 27
AN 106,106
AN 107,107
M 70
WL 2
AN 108,110
AN 109,111
M 71
R 78
M 72
SH 110
SH 111
M 73
AN 110,110
AN 111,111
M 74
TY 114,6664
TY 118,20488
M 75
ad 84,438
W 0
M 76
M 77
M 78
W 1
M 79
M 80
M 81
W 2
M 82
M 83
R 80
M 84
mw 48
WL 3
AN 112,114
AN 113,115
M 85
SH 114
SH 115
M 86
AN 114,114
AN 115,115
M 87
WL 2
AN 116,118
AN 117,119
M 88
SH 118
SH 119
M 89
AN 118,118
AN 119,119
M 90
TY 122,8200
TY 126,22024
M 91
ad 84,480
M 92
W 0
M 93
M 94
M 95
R 82
M 96
W 1
M 97
M 98
M 99
WL 3
AN 120,122
AN 121,123
M 100
SH 122
SH 123
M 101
AN 122,122
AN 123,123
M 102
WL 2
AN 124,126
AN 125,127
M 103
SH 126
SH 127
M 104
AN 126,126
AN 127,127
M 105
W 2
M 106
M 107
R 0
M 108
mw 51
M 109
ad 84,486
W 0
M 110
M 111
M 112
W 1
M 113
M 114
M 115
W 2
M 116
M 117
M 118
bc 49,6,66,60
M 119
WL 2
R 2
M 120
mh 0
M 121
ad 66,1216
H 0
M 132
M 133
TX 58,4608
TX 62,18432
M 122
DG 1024
M 123
M 134
M 135
TY 66,5120
TY 70,18944
M 124
bc 48,6,64,56
M 125
mh 3
ae 44,70,768
M 136
ad 64,1216
H 0
M 137
TX 74,6144
TX 78,19968
M 126
WL 4
Q 0
M 127
Q 2
M 138
Q 4
M 139
TY 82,6656
TY 86,20480
M 128
Q 6
M 129
WL 4
Q 8
M 140
Q 10
M 141
TX 90,7680
TX 94,21504
M 130
Q 12
M 131
R 4
M 142
Q 14
M 143
TY 98,8192
TY 102,22016
M 0
R 6
M 1
WL 10
Q 16
M 12
Q 18
M 13
Q 20
M 2
Q 22
M 3
WL 8
Q 24
M 14
Q 26
M 15
Q 28
M 4
Q 30
M 5
WL 6
Q 32
M 16
Q 34
M 17
Q 36
M 6
Q 38
M 7
WL 2
Q 40
M 18
Q 42
M 19
Q 44
M 8
Q 46
M 9
H 1
M 20
ae 42,72,384
M 21
s_bfe_u32 s52, s50, 0x10009
v_mad_i32_i24 v50, v49, s52, v48
SA 200,0
M 10
H 2
M 11
R 8
M 22
s_bitcmp1_b32 s50, 12
s_cselect_b32 s51, 24576, 0
M 23
R 10
PA 12288
DF
M 24
v_add_u32 v47, s51, v3
XL 30,0
M 25
XL 32,768
M 26
XL 34,1536
M 27
XL 36,2304
M 28
M 29
M 30
M 31
M 32
M 33
WV 11
M 34
WL 0
FP
M 35
M 36
M 37
M 38
M 39
M 40
M 41
M 42
M 43
M 44
M 45
FR
M 46
M 47
M 48
M 49
M 50
M 51
M 52
M 53
WL 0
FQ 192
M 54
M 55
M 56
FZ 192
R 24
M 57
mw 72
R 26
M 58
TY 106,9736
TY 110,23560
M 59
R 28
M 60
ad 84,528
W 0
M 61
M 62
M 63
W 1
M 64
M 65
M 66
W 2
M 67
WL 3
AN 104,106
AN 105,107
M 68
SH 106
SH 107
M 69
mw 75
AN 106,106
AN 107,107
M 70
WL 2
AN 108,110
AN 109,111
M 71
R 30
M 72
SH 110
SH 111
M 73
AN 110,110
AN 111,111
M 74
TY 114,11272
TY 118,25096
M 75
ad 84,534
W 0
M 76
M 77
M 78
W 1
M 79
M 80
M 81
W 2
M 82
M 83
R 32
M 84
mw 0
WL 3
AN 112,114
AN 113,115
M 85
SH 114
SH 115
M 86
AN 114,114
AN 115,115
M 87
WL 2
AN 116,118
AN 117,119
M 88
SH 118
SH 119
M 89
AN 118,118
AN 119,119
M 90
TY 122,12808
TY 126,26632
M 91
ad 84,576
M 92
W 0
SA 202,768
M 93
M 94
M 95
R 34
M 96
W 1
M 97
M 98
M 99
WL 3
AN 120,122
AN 121,123
M 100
SH 122
SH 123
M 101
AN 122,122
AN 123,123
M 102
WL 2
AN 124,126
AN 125,127
M 103
SH 126
SH 127
M 104
AN 126,126
AN 127,127
M 105
)"
R"(W 2
M 106
M 107
R 48
M 108
mw 3
M 109
ad 84,582
W 0
M 110
M 111
M 112
W 1
M 113
M 114
M 115
W 2
M 116
M 117
M 118
bc 49,7,66,60
M 119
WL 2
R 50
M 120
mh 9
M 121
ad 66,1408
H 0
M 132
M 133
TX 58,9216
TX 62,23040
M 122
DG 1024
M 123
M 134
M 135
TY 66,9728
TY 70,23552
M 124
bc 48,7,64,56
M 125
mh 12
M 136
ad 64,1408
H 0
M 137
TX 74,10752
TX 78,24576
M 126
WL 4
Q 0
M 127
Q 2
M 138
Q 4
M 139
TY 82,11264
TY 86,25088
M 128
Q 6
M 129
WL 4
Q 8
M 140
Q 10
M 141
TX 90,12288
TX 94,26112
M 130
Q 12
M 131
R 52
M 142
Q 14
M 143
TY 98,12800
TY 102,26624
M 0
R 54
M 1
WL 10
Q 16
M 12
Q 18
M 13
Q 20
M 2
Q 22
M 3
WL 8
Q 24
M 14
Q 26
M 15
Q 28
M 4
Q 30
M 5
WL 6
Q 32
M 16
Q 34
M 17
Q 36
M 6
Q 38
M 7
WL 2
Q 40
M 18
Q 42
M 19
Q 44
M 8
Q 46
M 9
H 1
SA 204,1536
M 20
ae 44,70,768
M 21
s_bfe_u32 s51, s50, 0x1000d
v_mad_i32_i24 v47, v49, s51, v48
XL 38,0
M 10
H 2
XL 40,768
M 11
R 56
M 22
XL 42,1536
M 23
R 58
M 24
XL 44,2304
M 25
M 26
M 27
M 28
M 29
M 30
M 31
M 32
M 33
WV 10
M 34
WL 0
FP
M 35
M 36
M 37
M 38
M 39
M 40
UL 6,30
UH 7,30
M 41
AR 8,36
AR 9,60
M 42
AD 8,6
AD 9,7
M 43
AW 36,8
AW 60,9
M 44
UL 6,31
UH 7,31
M 45
FR
AR 8,84
M 46
AR 9,108
AD 8,6
M 47
AD 9,7
AW 84,8
M 48
AW 108,9
UL 6,32
M 49
UH 7,32
AR 8,37
M 50
AR 9,61
AD 8,6
M 51
AD 9,7
AW 37,8
M 52
AW 61,9
UL 6,33
M 53
WL 0
FQ 196
UH 7,33
M 54
AR 8,85
AR 9,109
M 55
AD 8,6
AD 9,7
M 56
FZ 196
R 72
M 57
mw 24
R 74
M 58
TY 106,520
TY 110,14344
M 59
R 76
M 60
ad 84,624
W 0
M 61
AW 85,8
AW 109,9
M 62
UL 6,34
UH 7,34
M 63
W 1
AR 8,38
M 64
AR 9,62
AD 8,6
M 65
AD 9,7
AW 38,8
M 66
W 2
AW 62,9
M 67
WL 3
AN 104,106
AN 105,107
M 68
SH 106
SH 107
M 69
mw 27
AN 106,106
AN 107,107
M 70
WL 2
AN 108,110
AN 109,111
M 71
R 78
M 72
SH 110
SH 111
M 73
AN 110,110
AN 111,111
M 74
TY 114,2056
TY 118,15880
M 75
ad 84,630
W 0
M 76
UL 6,35
UH 7,35
M 77
AR 8,86
AR 9,110
M 78
W 1
AD 8,6
M 79
AD 9,7
AW 86,8
M 80
AW 110,9
UL 6,36
M 81
W 2
SA 206,2304
M 82
UH 7,36
AR 8,39
M 83
WL 1
R 80
M 84
mw 48
AN 112,114
AN 113,115
M 85
SH 114
SH 115
M 86
AN 114,114
AN 115,115
M 87
WL 2
AN 116,118
AN 117,119
M 88
SH 118
SH 119
M 89
AN 118,118
AN 119,119
M 90
TY 122,3592
TY 126,17416
M 91
ad 84,672
M 92
W 0
AR 9,63
M 93
AD 8,6
AD 9,7
M 94
AW 39,8
AW 63,9
M 95
R 82
M 96
W 1
UL 6,37
M 97
UH 7,37
AR 8,87
M 98
AR 9,111
AD 8,6
M 99
WL 3
AN 120,122
AN 121,123
M 100
SH 122
SH 123
M 101
AN 122,122
AN 123,123
M 102
WL 2
AN 124,126
AN 125,127
M 103
SH 126
SH 127
M 104
AN 126,126
AN 127,127
M 105
W 2
AD 9,7
M 106
AW 87,8
AW 111,9
M 107
R 0
M 108
mw 51
M 109
ad 84,678
W 0
M 110
M 111
M 112
W 1
M 113
M 114
M 115
W 2
M 116
M 117
M 118
bc 49,8,66,60
M 119
WL 2
R 2
M 120
mh 18
M 121
ad 66,1600
H 0
M 132
M 133
TX 58,0
TX 62,13824
M 122
DG 1024
M 123
M 134
M 135
TY 66,512
TY 70,14336
M 124
bc 48,8,64,56
)"
R"(M 125
mh 21
M 136
ad 64,1600
H 0
M 137
TX 74,1536
TX 78,15360
M 126
WL 4
Q 0
M 127
Q 2
M 138
Q 4
M 139
TY 82,2048
TY 86,15872
M 128
Q 6
M 129
WL 4
Q 8
M 140
Q 10
M 141
TX 90,3072
TX 94,16896
M 130
Q 12
M 131
R 4
M 142
Q 14
M 143
TY 98,3584
TY 102,17408
M 0
R 6
M 1
WL 10
Q 16
M 12
Q 18
M 13
Q 20
M 2
Q 22
M 3
WL 8
Q 24
M 14
Q 26
M 15
Q 28
M 4
Q 30
M 5
WL 6
Q 32
M 16
Q 34
M 17
Q 36
M 6
Q 38
M 7
WL 2
Q 40
M 18
Q 42
M 19
Q 44
M 8
Q 46
M 9
H 1
M 20
ae 42,72,384
M 21
SA 208,128
M 10
H 2
M 11
R 8
M 22
ae 44,68,768
M 23
R 10
PA 12288
DF
M 24
s_bitcmp1_b32 s50, 14
s_cselect_b32 s51, 24576, 0
v_add_u32 v47, s51, v3
M 25
XL 14,0
M 26
XL 16,768
M 27
XL 18,1536
M 28
XL 20,2304
M 29
M 30
M 31
M 32
M 33
WV 11
M 34
WL 0
FP
M 35
M 36
M 37
M 38
M 39
M 40
M 41
M 42
UL 6,38
UH 7,38
M 43
AR 8,40
AR 9,64
M 44
AD 8,6
AD 9,7
M 45
FR
AW 40,8
M 46
AW 64,9
UL 6,39
M 47
UH 7,39
AR 8,88
M 48
AR 9,112
AD 8,6
M 49
AD 9,7
AW 88,8
M 50
AW 112,9
UL 6,40
M 51
UH 7,40
AR 8,41
M 52
AR 9,65
AD 8,6
M 53
WL 0
FQ 200
AD 9,7
M 54
AW 41,8
AW 65,9
M 55
UL 6,41
UH 7,41
M 56
FZ 200
R 24
M 57
mw 72
R 26
M 58
TY 106,5128
TY 110,18952
M 59
R 28
M 60
ad 84,720
W 0
M 61
AR 8,89
AR 9,113
M 62
AD 8,6
AD 9,7
M 63
W 1
AW 89,8
M 64
AW 113,9
UL 6,42
M 65
UH 7,42
AR 8,42
M 66
W 2
AR 9,66
M 67
WL 3
AN 104,106
AN 105,107
M 68
SH 106
SH 107
M 69
mw 75
AN 106,106
AN 107,107
M 70
WL 2
AN 108,110
AN 109,111
M 71
R 30
M 72
SH 110
SH 111
M 73
AN 110,110
AN 111,111
M 74
TY 114,6664
TY 118,20488
M 75
ad 84,726
W 0
M 76
AD 8,6
AD 9,7
M 77
AW 42,8
AW 66,9
M 78
W 1
UL 6,43
M 79
UH 7,43
AR 8,90
M 80
AR 9,114
AD 8,6
M 81
W 2
AD 9,7
M 82
AW 90,8
AW 114,9
M 83
WL 1
R 32
M 84
mw 0
AN 112,114
AN 113,115
M 85
SH 114
SH 115
M 86
AN 114,114
AN 115,115
M 87
WL 2
AN 116,118
AN 117,119
M 88
SH 118
SH 119
M 89
AN 118,118
AN 119,119
M 90
TY 122,8200
TY 126,22024
M 91
ad 84,768
M 92
W 0
SA 210,896
M 93
UL 6,44
UH 7,44
M 94
AR 8,43
AR 9,67
M 95
R 34
M 96
W 1
AD 8,6
M 97
AD 9,7
AW 43,8
M 98
AW 67,9
UL 6,45
M 99
WL 3
AN 120,122
AN 121,123
M 100
SH 122
SH 123
M 101
AN 122,122
AN 123,123
M 102
WL 2
AN 124,126
AN 125,127
M 103
SH 126
SH 127
M 104
AN 126,126
AN 127,127
M 105
W 2
UH 7,45
M 106
AR 8,91
AR 9,115
M 107
R 48
M 108
mw 3
AD 8,6
AD 9,7
M 109
ad 84,774
W 0
M 110
AW 91,8
AW 115,9
M 111
M 112
W 1
M 113
M 114
M 115
W 2
M 116
M 117
M 118
bc 49,9,66,60
M 119
WL 2
R 50
M 120
mh 0
M 121
ad 66,1792
H 0
M 132
M 133
TX 58,4608
TX 62,18432
M 122
DG 1024
M 123
M 134
M 135
TY 66,5120
TY 70,18944
M 124
bc 48,9,64,56
M 125
mh 3
M 136
ad 64,1792
H 0
M 137
TX 74,6144
TX 78,19968
M 126
WL 4
Q 0
M 127
Q 2
M 138
Q 4
M 139
TY 82,6656
TY 86,20480
M 128
Q 6
M 129
WL 4
Q 8
M 140
Q 10
M 141
TX 90,7680
TX 94,21504
M 130
Q 12
M 131
R 52
M 142
Q 14
M 143
TY 98,8192
TY 102,22016
M 0
)"
R"(R 54
M 1
WL 10
Q 16
M 12
Q 18
M 13
Q 20
M 2
Q 22
M 3
WL 8
Q 24
M 14
Q 26
M 15
Q 28
M 4
Q 30
M 5
WL 6
Q 32
M 16
Q 34
M 17
Q 36
M 6
Q 38
M 7
WL 2
Q 40
M 18
Q 42
M 19
Q 44
M 8
Q 46
M 9
H 1
SA 212,1664
M 20
ae 44,68,768
M 21
s_bfe_u32 s51, s50, 0x1000f
v_mad_i32_i24 v47, v49, s51, v48
XL 22,0
M 10
H 2
XL 24,768
M 11
R 56
M 22
XL 26,1536
M 23
R 58
M 24
XL 28,2304
M 25
M 26
M 27
M 28
M 29
M 30
M 31
M 32
M 33
WV 10
M 34
WL 0
FP
M 35
M 36
M 37
M 38
M 39
M 40
M 41
M 42
M 43
M 44
UL 6,14
UH 7,14
M 45
FR
AR 8,44
M 46
AR 9,68
AD 8,6
M 47
AD 9,7
AW 44,8
M 48
AW 68,9
UL 6,15
M 49
UH 7,15
AR 8,92
M 50
AR 9,116
AD 8,6
M 51
AD 9,7
AW 92,8
M 52
AW 116,9
UL 6,16
M 53
WL 0
FQ 204
UH 7,16
M 54
AR 8,45
AR 9,69
M 55
AD 8,6
AD 9,7
M 56
FZ 204
R 72
M 57
mw 24
R 74
M 58
TY 106,9736
TY 110,23560
M 59
R 76
M 60
ad 84,816
W 0
M 61
AW 45,8
AW 69,9
M 62
UL 6,17
UH 7,17
M 63
W 1
AR 8,93
M 64
AR 9,117
AD 8,6
M 65
AD 9,7
AW 93,8
M 66
W 2
AW 117,9
M 67
WL 3
AN 104,106
AN 105,107
M 68
SH 106
SH 107
M 69
mw 27
AN 106,106
AN 107,107
M 70
WL 2
AN 108,110
AN 109,111
M 71
R 78
M 72
SH 110
SH 111
M 73
AN 110,110
AN 111,111
M 74
TY 114,11272
TY 118,25096
M 75
ad 84,822
W 0
M 76
UL 6,18
UH 7,18
M 77
AR 8,46
AR 9,70
M 78
W 1
AD 8,6
M 79
AD 9,7
AW 46,8
M 80
AW 70,9
UL 6,19
M 81
W 2
SA 214,2432
M 82
UH 7,19
AR 8,94
M 83
WL 1
R 80
M 84
mw 48
AN 112,114
AN 113,115
M 85
SH 114
SH 115
M 86
AN 114,114
AN 115,115
M 87
WL 2
AN 116,118
AN 117,119
M 88
SH 118
SH 119
M 89
AN 118,118
AN 119,119
M 90
TY 122,12808
TY 126,26632
M 91
ad 84,864
M 92
W 0
AR 9,118
M 93
AD 8,6
AD 9,7
M 94
AW 94,8
AW 118,9
M 95
R 82
M 96
W 1
UL 6,20
M 97
UH 7,20
AR 8,47
M 98
AR 9,71
AD 8,6
M 99
WL 3
AN 120,122
AN 121,123
M 100
SH 122
SH 123
M 101
AN 122,122
AN 123,123
M 102
WL 2
AN 124,126
AN 125,127
M 103
SH 126
SH 127
M 104
AN 126,126
AN 127,127
M 105
W 2
AD 9,7
M 106
AW 47,8
AW 71,9
M 107
R 0
M 108
mw 51
UL 6,21
UH 7,21
M 109
ad 84,870
W 0
M 110
AR 8,95
AR 9,119
M 111
AD 8,6
AD 9,7
M 112
W 1
AW 95,8
M 113
AW 119,9
M 114
M 115
W 2
M 116
M 117
M 118
bc 49,10,66,60
M 119
WL 2
R 2
M 120
mh 9
M 121
ad 66,1984
H 0
M 132
M 133
TX 58,9216
TX 62,23040
M 122
DG 1024
M 123
M 134
M 135
TY 66,9728
TY 70,23552
M 124
bc 48,10,64,56
M 125
mh 12
M 136
ad 64,1984
H 0
M 137
TX 74,10752
TX 78,24576
M 126
WL 4
Q 0
M 127
Q 2
M 138
Q 4
M 139
TY 82,11264
TY 86,25088
M 128
Q 6
M 129
WL 4
Q 8
M 140
Q 10
M 141
TX 90,12288
TX 94,26112
M 130
Q 12
M 131
R 4
M 142
Q 14
M 143
TY 98,12800
TY 102,26624
M 0
R 6
M 1
WL 10
Q 16
M 12
Q 18
M 13
Q 20
M 2
Q 22
M 3
WL 8
Q 24
M 14
Q 26
M 15
Q 28
M 4
Q 30
M 5
WL 6
Q 32
M 16
Q 34
M 17
Q 36
M 6
Q 38
M 7
WL 2
Q 40
M 18
Q 42
M 19
Q 44
M 8
Q 46
M 9
H 1
M 20
ae 42,72,384
M 21
)"
R"(s_bitcmp1_b32 s50, 8
s_cselect_b32 s52, 24576, 0
v_add_u32 v50, s52, v3
M 10
H 2
SA 216,256
M 11
R 8
M 22
ae 44,68,864
M 23
R 10
PA 12288
DF
M 24
s_bitcmp1_b32 s50, 16
s_cselect_b32 s51, 24576, 0
v_add_u32 v47, s51, v3
M 25
XL 30,0
M 26
XL 32,768
M 27
XL 34,1536
M 28
XL 36,2304
M 29
M 30
M 31
M 32
M 33
WV 11
M 34
WL 0
FP
M 35
M 36
M 37
M 38
M 39
M 40
M 41
M 42
M 43
M 44
M 45
FR
M 46
UL 6,22
UH 7,22
M 47
AR 8,48
AR 9,72
M 48
AD 8,6
AD 9,7
M 49
AW 48,8
AW 72,9
M 50
UL 6,23
UH 7,23
M 51
AR 8,96
AR 9,120
M 52
AD 8,6
AD 9,7
M 53
WL 0
FQ 208
AW 96,8
M 54
AW 120,9
UL 6,24
M 55
UH 7,24
AR 8,49
M 56
FZ 208
R 24
M 57
mw 72
R 26
M 58
TY 106,520
TY 110,14344
M 59
R 28
M 60
ad 84,912
W 0
M 61
AR 9,73
AD 8,6
M 62
AD 9,7
AW 49,8
M 63
W 1
AW 73,9
M 64
UL 6,25
UH 7,25
M 65
AR 8,97
AR 9,121
M 66
W 2
AD 8,6
M 67
WL 3
AN 104,106
AN 105,107
M 68
SH 106
SH 107
M 69
mw 75
AN 106,106
AN 107,107
M 70
WL 2
AN 108,110
AN 109,111
M 71
R 30
M 72
SH 110
SH 111
M 73
AN 110,110
AN 111,111
M 74
TY 114,2056
TY 118,15880
M 75
ad 84,918
W 0
M 76
AD 9,7
AW 97,8
M 77
AW 121,9
UL 6,26
M 78
W 1
UH 7,26
M 79
AR 8,50
AR 9,74
M 80
AD 8,6
AD 9,7
M 81
W 2
AW 50,8
M 82
AW 74,9
UL 6,27
M 83
WL 1
R 32
M 84
mw 0
AN 112,114
AN 113,115
M 85
SH 114
SH 115
M 86
AN 114,114
AN 115,115
M 87
WL 2
AN 116,118
AN 117,119
M 88
SH 118
SH 119
M 89
AN 118,118
AN 119,119
M 90
TY 122,3592
TY 126,17416
M 91
ad 84,960
M 92
W 0
SA 218,1024
M 93
UH 7,27
AR 8,98
M 94
AR 9,122
AD 8,6
M 95
R 34
M 96
W 1
AD 9,7
M 97
AW 98,8
AW 122,9
M 98
UL 6,28
UH 7,28
M 99
WL 3
AN 120,122
AN 121,123
M 100
SH 122
SH 123
M 101
AN 122,122
AN 123,123
M 102
WL 2
AN 124,126
AN 125,127
M 103
SH 126
SH 127
M 104
AN 126,126
AN 127,127
M 105
W 2
AR 8,51
M 106
AR 9,75
AD 8,6
M 107
R 48
M 108
mw 3
AD 9,7
AW 51,8
M 109
ad 84,966
W 0
M 110
AW 75,9
UL 6,29
M 111
UH 7,29
AR 8,99
M 112
W 1
AR 9,123
M 113
AD 8,6
AD 9,7
M 114
AW 99,8
AW 123,9
M 115
W 2
M 116
M 117
M 118
bc 49,11,66,60
M 119
WL 2
R 50
M 120
mh 18
M 121
ad 66,2176
H 0
M 132
M 133
TX 58,0
TX 62,13824
M 122
DG 1024
M 123
M 134
M 135
TY 66,512
TY 70,14336
M 124
bc 48,11,64,56
M 125
mh 21
M 136
ad 64,2176
H 0
M 137
TX 74,1536
TX 78,15360
M 126
WL 4
Q 0
M 127
Q 2
M 138
Q 4
M 139
TY 82,2048
TY 86,15872
M 128
Q 6
M 129
WL 4
Q 8
M 140
Q 10
M 141
TX 90,3072
TX 94,16896
M 130
Q 12
M 131
R 52
M 142
Q 14
M 143
TY 98,3584
TY 102,17408
M 0
R 54
M 1
WL 10
Q 16
M 12
Q 18
M 13
Q 20
M 2
Q 22
M 3
WL 8
Q 24
M 14
Q 26
M 15
Q 28
M 4
Q 30
M 5
WL 6
Q 32
M 16
Q 34
M 17
Q 36
M 6
Q 38
M 7
WL 2
Q 40
M 18
Q 42
M 19
Q 44
M 8
Q 46
M 9
H 1
SA 220,1792
M 20
ae 44,68,864
M 21
s_bfe_u32 s51, s50, 0x10011
v_mad_i32_i24 v47, v49, s51, v48
XL 38,0
M 10
H 2
XL 40,768
M 11
R 56
M 22
XL 42,1536
M 23
R 58
M 24
XL 44,2304
M 25
M 26
M 27
M 28
M 29
M 30
M 31
M 32
M 33
WV 10
M 34
WL 0
FP
M 35
M 36
M 37
M 38
M 39
M 40
M 41
)"
R"(M 42
M 43
M 44
M 45
FR
M 46
M 47
M 48
UL 6,30
UH 7,30
M 49
AR 8,52
AR 9,76
M 50
AD 8,6
AD 9,7
M 51
AW 52,8
AW 76,9
M 52
UL 6,31
UH 7,31
M 53
WL 0
FQ 212
AR 8,100
M 54
AR 9,124
AD 8,6
M 55
AD 9,7
AW 100,8
M 56
FZ 212
R 72
M 57
mw 24
R 74
M 58
TY 106,5128
TY 110,18952
M 59
R 76
M 60
ad 84,1008
W 0
M 61
AW 124,9
UL 6,32
M 62
UH 7,32
AR 8,53
M 63
W 1
AR 9,77
M 64
AD 8,6
AD 9,7
M 65
AW 53,8
AW 77,9
M 66
W 2
UL 6,33
M 67
WL 3
AN 104,106
AN 105,107
M 68
SH 106
SH 107
M 69
mw 27
AN 106,106
AN 107,107
M 70
WL 2
AN 108,110
AN 109,111
M 71
R 78
M 72
SH 110
SH 111
M 73
AN 110,110
AN 111,111
M 74
TY 114,6664
TY 118,20488
M 75
ad 84,1014
W 0
M 76
UH 7,33
AR 8,101
M 77
AR 9,125
AD 8,6
M 78
W 1
AD 9,7
M 79
AW 101,8
AW 125,9
M 80
UL 6,34
UH 7,34
M 81
W 2
SA 222,2560
M 82
AR 8,54
AR 9,78
M 83
WL 1
R 80
M 84
mw 48
AN 112,114
AN 113,115
M 85
SH 114
SH 115
M 86
AN 114,114
AN 115,115
M 87
WL 2
AN 116,118
AN 117,119
M 88
SH 118
SH 119
M 89
AN 118,118
AN 119,119
M 90
TY 122,8200
TY 126,22024
M 91
ad 84,1056
M 92
W 0
AD 8,6
M 93
AD 9,7
AW 54,8
M 94
AW 78,9
UL 6,35
M 95
R 82
M 96
W 1
UH 7,35
M 97
AR 8,102
AR 9,126
M 98
AD 8,6
AD 9,7
M 99
WL 3
AN 120,122
AN 121,123
M 100
SH 122
SH 123
M 101
AN 122,122
AN 123,123
M 102
WL 2
AN 124,126
AN 125,127
M 103
SH 126
SH 127
M 104
AN 126,126
AN 127,127
M 105
W 2
AW 102,8
M 106
AW 126,9
UL 6,36
M 107
R 0
M 108
mw 51
UH 7,36
AR 8,55
M 109
ad 84,1062
W 0
M 110
AR 9,79
AD 8,6
M 111
AD 9,7
AW 55,8
M 112
W 1
AW 79,9
M 113
UL 6,37
UH 7,37
M 114
AR 8,103
AR 9,127
M 115
W 2
AD 8,6
M 116
AD 9,7
AW 103,8
M 117
AW 127,9
M 118
bc 49,12,66,60
M 119
WL 1
R 2
M 120
mh 0
M 121
ad 66,2368
H 0
M 132
M 133
TX 58,4608
TX 62,18432
M 122
DG 1024
M 123
M 134
M 135
TY 66,5120
TY 70,18944
M 124
bc 48,12,64,56
M 125
mh 3
M 136
ad 64,2368
H 0
M 137
TX 74,6144
TX 78,19968
M 126
WL 4
Q 0
M 127
Q 2
M 138
Q 4
M 139
TY 82,6656
TY 86,20480
M 128
Q 6
M 129
WL 4
Q 8
M 140
Q 10
M 141
TX 90,7680
TX 94,21504
M 130
Q 12
M 131
R 4
M 142
Q 14
M 143
TY 98,8192
TY 102,22016
M 0
R 6
M 1
WL 10
Q 16
M 12
Q 18
M 13
Q 20
M 2
Q 22
M 3
WL 8
Q 24
M 14
Q 26
M 15
Q 28
M 4
Q 30
M 5
WL 6
Q 32
M 16
Q 34
M 17
Q 36
M 6
Q 38
M 7
WL 2
Q 40
M 18
Q 42
M 19
Q 44
M 8
Q 46
M 9
H 1
M 20
ae 42,72,480
M 21
s_bitcmp1_b32 s50, 10
s_cselect_b32 s52, 24576, 0
v_add_u32 v50, s52, v3
M 10
H 2
SA 224,0
M 11
R 8
M 22
M 23
R 10
PA 12288
DF
M 24
M 25
M 26
M 27
M 28
M 29
M 30
M 31
M 32
ae 44,70,768
M 33
WV 7
M 34
WL 0
FP
M 35
M 36
s_bitcmp1_b32 s50, 12
s_cselect_b32 s51, 24576, 0
v_add_u32 v47, s51, v3
M 37
XL 14,128
M 38
XL 16,896
M 39
M 40
M 41
M 42
M 43
M 44
XL 18,1664
M 45
FR
M 46
M 47
M 48
M 49
M 50
XL 20,2432
UL 6,38
M 51
UH 7,38
AR 8,56
M 52
AR 9,80
AD 8,6
M 53
WL 0
FQ 216
AD 9,7
M 54
AW 56,8
AW 80,9
M 55
UL 6,39
UH 7,39
M 56
FZ 216
R 24
M 57
mw 72
R 26
M 58
TY 106,9736
)"
R"(TY 110,23560
M 59
R 28
M 60
ad 84,1104
W 0
M 61
AR 8,104
AR 9,128
M 62
AD 8,6
AD 9,7
M 63
W 1
AW 104,8
M 64
AW 128,9
UL 6,40
M 65
UH 7,40
AR 8,57
M 66
W 2
AR 9,81
M 67
WL 3
AN 104,106
AN 105,107
M 68
SH 106
SH 107
M 69
mw 75
AN 106,106
AN 107,107
M 70
WL 2
AN 108,110
AN 109,111
M 71
R 30
M 72
SH 110
SH 111
M 73
AN 110,110
AN 111,111
M 74
TY 114,11272
TY 118,25096
M 75
ad 84,1110
W 0
M 76
AD 8,6
AD 9,7
M 77
AW 57,8
AW 81,9
M 78
W 1
UL 6,41
M 79
UH 7,41
AR 8,105
M 80
AR 9,129
AD 8,6
M 81
W 2
AD 9,7
M 82
AW 105,8
AW 129,9
M 83
WL 1
R 32
M 84
mw 0
AN 112,114
AN 113,115
M 85
SH 114
SH 115
M 86
AN 114,114
AN 115,115
M 87
WL 2
AN 116,118
AN 117,119
M 88
SH 118
SH 119
M 89
AN 118,118
AN 119,119
M 90
TY 122,12808
TY 126,26632
M 91
ad 84,1152
M 92
W 0
SA 226,768
M 93
UL 6,42
UH 7,42
M 94
AR 8,58
AR 9,82
M 95
R 34
M 96
W 1
AD 8,6
M 97
AD 9,7
AW 58,8
M 98
AW 82,9
UL 6,43
M 99
WL 3
AN 120,122
AN 121,123
M 100
SH 122
SH 123
M 101
AN 122,122
AN 123,123
M 102
WL 2
AN 124,126
AN 125,127
M 103
SH 126
SH 127
M 104
AN 126,126
AN 127,127
M 105
W 2
UH 7,43
M 106
AR 8,106
AR 9,130
M 107
R 48
M 108
mw 3
AD 8,6
AD 9,7
M 109
ad 84,1158
W 0
M 110
AW 106,8
AW 130,9
M 111
UL 6,44
UH 7,44
M 112
W 1
AR 8,59
M 113
AR 9,83
AD 8,6
M 114
AD 9,7
AW 59,8
M 115
W 2
AW 83,9
M 116
UL 6,45
UH 7,45
M 117
AR 8,107
AR 9,131
M 118
bc 49,13,66,60
AD 8,6
M 119
WL 0
R 50
M 120
mh 9
AD 9,7
AW 107,8
M 121
ad 66,2560
H 0
M 132
AW 131,9
M 133
TX 58,9216
TX 62,23040
M 122
DG 1024
M 123
M 134
M 135
TY 66,9728
TY 70,23552
M 124
bc 48,13,64,56
M 125
mh 12
M 136
ad 64,2560
H 0
M 137
TX 74,10752
TX 78,24576
M 126
WL 4
Q 0
M 127
Q 2
M 138
Q 4
M 139
TY 82,11264
TY 86,25088
M 128
Q 6
M 129
WL 4
Q 8
M 140
Q 10
M 141
TX 90,12288
TX 94,26112
M 130
Q 12
M 131
R 52
M 142
Q 14
M 143
TY 98,12800
TY 102,26624
M 0
R 54
M 1
WL 10
Q 16
M 12
Q 18
M 13
Q 20
M 2
Q 22
M 3
WL 8
Q 24
M 14
Q 26
M 15
Q 28
M 4
Q 30
M 5
WL 6
Q 32
M 16
Q 34
M 17
Q 36
M 6
Q 38
M 7
WL 2
Q 40
M 18
Q 42
M 19
Q 44
M 8
Q 46
M 9
H 1
SA 228,1536
M 20
M 21
M 10
H 2
M 11
R 56
M 22
M 23
R 58
M 24
M 25
M 26
M 27
M 28
M 29
M 30
M 31
M 32
M 33
WV 6
M 34
WL 0
FP
M 35
M 36
ae 44,70,768
M 37
s_bfe_u32 s51, s50, 0x1000d
v_mad_i32_i24 v47, v49, s51, v48
XL 22,128
M 38
M 39
M 40
XL 24,896
M 41
M 42
M 43
M 44
M 45
FR
M 46
XL 26,1664
M 47
M 48
M 49
M 50
M 51
M 52
XL 28,2432
M 53
WL 0
FQ 220
M 54
M 55
M 56
FZ 220
R 72
M 57
mw 24
R 74
M 58
TY 106,520
TY 110,14344
M 59
R 76
M 60
ad 84,1200
W 0
M 61
M 62
M 63
W 1
M 64
M 65
M 66
W 2
M 67
WL 3
AN 104,106
AN 105,107
M 68
SH 106
SH 107
M 69
mw 27
AN 106,106
AN 107,107
M 70
WL 2
AN 108,110
AN 109,111
M 71
R 78
M 72
SH 110
SH 111
M 73
AN 110,110
AN 111,111
M 74
TY 114,2056
TY 118,15880
M 75
ad 84,1206
W 0
M 76
M 77
M 78
W 1
M 79
M 80
M 81
W 2
SA 230,2304
)"
R"(M 82
M 83
R 80
M 84
mw 48
WL 3
AN 112,114
AN 113,115
M 85
SH 114
SH 115
M 86
AN 114,114
AN 115,115
M 87
WL 2
AN 116,118
AN 117,119
M 88
SH 118
SH 119
M 89
AN 118,118
AN 119,119
M 90
TY 122,3592
TY 126,17416
M 91
ad 84,1248
M 92
W 0
UL 6,14
M 93
UH 7,14
AR 8,132
M 94
AR 9,156
AD 8,6
M 95
R 82
M 96
W 1
AD 9,7
M 97
AW 132,8
AW 156,9
M 98
UL 6,15
UH 7,15
M 99
WL 3
AN 120,122
AN 121,123
M 100
SH 122
SH 123
M 101
AN 122,122
AN 123,123
M 102
WL 2
AN 124,126
AN 125,127
M 103
SH 126
SH 127
M 104
AN 126,126
AN 127,127
M 105
W 2
AR 8,180
M 106
AD 8,6
AD 136,7
M 107
R 0
M 108
mw 51
AW 180,8
UL 6,16
M 109
ad 84,1254
W 0
M 110
UH 7,16
AR 8,133
M 111
AR 9,157
AD 8,6
M 112
W 1
AD 9,7
M 113
AW 133,8
AW 157,9
M 114
UL 6,17
UH 7,17
M 115
W 2
AR 8,181
M 116
AD 8,6
AD 137,7
M 117
AW 181,8
UL 6,18
M 118
bc 49,14,66,60
UH 7,18
M 119
WL 0
R 2
M 120
mh 18
AR 8,134
AR 9,158
M 121
ad 66,2752
H 0
M 132
AD 8,6
AD 9,7
M 133
TX 58,0
TX 62,13824
M 122
DG 1024
M 123
AW 134,8
AW 158,9
M 134
UL 6,19
UH 7,19
M 135
TY 66,512
TY 70,14336
M 124
bc 48,14,64,56
AR 8,182
M 125
mh 21
AD 8,6
AD 138,7
M 136
ad 64,2752
H 0
M 137
TX 74,1536
TX 78,15360
M 126
WL 4
Q 0
M 127
Q 2
M 138
Q 4
M 139
TY 82,2048
TY 86,15872
M 128
Q 6
M 129
WL 4
Q 8
M 140
Q 10
M 141
TX 90,3072
TX 94,16896
M 130
Q 12
M 131
R 4
M 142
Q 14
M 143
TY 98,3584
TY 102,17408
M 0
R 6
M 1
WL 10
Q 16
M 12
Q 18
M 13
Q 20
M 2
Q 22
M 3
WL 8
Q 24
M 14
Q 26
M 15
Q 28
M 4
Q 30
M 5
WL 6
Q 32
M 16
Q 34
M 17
Q 36
M 6
Q 38
M 7
WL 2
Q 40
M 18
Q 42
M 19
Q 44
M 8
Q 46
M 9
H 1
AW 182,8
M 20
ae 42,72,480
M 21
SA 232,128
UL 6,20
M 10
H 2
UH 7,20
M 11
R 8
M 22
AR 8,135
AR 9,159
M 23
R 10
PA 12288
DF
M 24
AD 8,6
AD 9,7
M 25
AW 135,8
AW 159,9
M 26
UL 6,21
UH 7,21
M 27
AR 8,183
AD 8,6
M 28
AD 139,7
AW 183,8
M 29
M 30
M 31
M 32
M 33
WV 7
M 34
WL 0
FP
M 35
M 36
ae 44,68,768
M 37
s_bitcmp1_b32 s50, 14
s_cselect_b32 s51, 24576, 0
v_add_u32 v47, s51, v3
M 38
XL 30,128
M 39
M 40
M 41
M 42
XL 32,896
M 43
M 44
M 45
FR
M 46
M 47
M 48
XL 34,1664
M 49
M 50
M 51
M 52
M 53
WL 0
FQ 224
M 54
XL 36,2432
M 55
M 56
FZ 224
R 24
M 57
mw 72
R 26
M 58
TY 106,5128
TY 110,18952
M 59
R 28
M 60
ad 84,1296
W 0
M 61
M 62
M 63
W 1
M 64
M 65
M 66
W 2
M 67
WL 3
AN 104,106
AN 105,107
M 68
SH 106
SH 107
M 69
mw 75
AN 106,106
AN 107,107
M 70
WL 2
AN 108,110
AN 109,111
M 71
R 30
M 72
SH 110
SH 111
M 73
AN 110,110
AN 111,111
M 74
TY 114,6664
TY 118,20488
M 75
ad 84,1302
W 0
M 76
M 77
M 78
W 1
M 79
M 80
M 81
W 2
M 82
M 83
R 32
M 84
mw 0
WL 3
AN 112,114
AN 113,115
M 85
SH 114
SH 115
M 86
AN 114,114
AN 115,115
M 87
WL 2
AN 116,118
AN 117,119
M 88
SH 118
SH 119
M 89
AN 118,118
AN 119,119
M 90
TY 122,8200
TY 126,22024
M 91
ad 84,1344
M 92
W 0
SA 234,896
M 93
UL 6,22
UH 7,22
M 94
AR 8,136
AR 9,160
M 95
R 34
M 96
W 1
AD 8,6
M 97
AD 9,7
AW 136,8
M 98
)"
R"(AW 160,9
UL 6,23
M 99
WL 3
AN 120,122
AN 121,123
M 100
SH 122
SH 123
M 101
AN 122,122
AN 123,123
M 102
WL 2
AN 124,126
AN 125,127
M 103
SH 126
SH 127
M 104
AN 126,126
AN 127,127
M 105
W 2
UH 7,23
M 106
AR 8,184
AD 8,6
M 107
R 48
M 108
mw 3
AD 140,7
AW 184,8
M 109
ad 84,1350
W 0
M 110
UL 6,24
UH 7,24
M 111
AR 8,137
AR 9,161
M 112
W 1
AD 8,6
M 113
AD 9,7
AW 137,8
M 114
AW 161,9
UL 6,25
M 115
W 2
UH 7,25
M 116
AR 8,185
AD 8,6
M 117
AD 141,7
AW 185,8
M 118
bc 49,15,66,60
UL 6,26
M 119
WL 0
R 50
M 120
mh 0
UH 7,26
AR 8,138
M 121
ad 66,2944
H 0
M 132
AR 9,162
AD 8,6
M 133
TX 58,4608
TX 62,18432
M 122
DG 1024
M 123
AD 9,7
AW 138,8
M 134
AW 162,9
UL 6,27
M 135
TY 66,5120
TY 70,18944
M 124
bc 48,15,64,56
UH 7,27
M 125
mh 3
AR 8,186
AD 8,6
M 136
ad 64,2944
H 0
M 137
TX 74,6144
TX 78,19968
M 126
WL 4
Q 0
M 127
Q 2
M 138
Q 4
M 139
TY 82,6656
TY 86,20480
M 128
Q 6
M 129
WL 4
Q 8
M 140
Q 10
M 141
TX 90,7680
TX 94,21504
M 130
Q 12
M 131
R 52
M 142
Q 14
M 143
TY 98,8192
TY 102,22016
M 0
R 54
M 1
WL 10
Q 16
M 12
Q 18
M 13
Q 20
M 2
Q 22
M 3
WL 8
Q 24
M 14
Q 26
M 15
Q 28
M 4
Q 30
M 5
WL 6
Q 32
M 16
Q 34
M 17
Q 36
M 6
Q 38
M 7
WL 2
Q 40
M 18
Q 42
M 19
Q 44
M 8
Q 46
M 9
H 1
SA 236,1664
M 20
AD 142,7
AW 186,8
M 21
UL 6,28
UH 7,28
M 10
H 2
AR 8,139
M 11
R 56
M 22
AR 9,163
AD 8,6
M 23
R 58
M 24
AD 9,7
AW 139,8
M 25
AW 163,9
UL 6,29
M 26
UH 7,29
AR 8,187
M 27
AD 8,6
AD 143,7
M 28
AW 187,8
M 29
M 30
M 31
M 32
M 33
WV 6
M 34
WL 0
FP
M 35
M 36
M 37
M 38
ae 44,68,768
M 39
s_bfe_u32 s51, s50, 0x1000f
v_mad_i32_i24 v47, v49, s51, v48
XL 38,128
M 40
M 41
M 42
M 43
M 44
XL 40,896
M 45
FR
M 46
M 47
M 48
M 49
M 50
XL 42,1664
M 51
M 52
M 53
WL 0
FQ 228
M 54
M 55
M 56
FZ 228
R 72
M 57
mw 24
R 74
M 58
TY 106,9736
TY 110,23560
M 59
R 76
M 60
ad 84,1392
W 0
M 61
XL 44,2432
M 62
M 63
W 1
M 64
M 65
M 66
W 2
M 67
WL 3
AN 104,106
AN 105,107
M 68
SH 106
SH 107
M 69
mw 27
AN 106,106
AN 107,107
M 70
WL 2
AN 108,110
AN 109,111
M 71
R 78
M 72
SH 110
SH 111
M 73
AN 110,110
AN 111,111
M 74
TY 114,11272
TY 118,25096
M 75
ad 84,1398
W 0
M 76
M 77
M 78
W 1
M 79
M 80
M 81
W 2
SA 238,2432
M 82
M 83
R 80
M 84
mw 48
WL 3
AN 112,114
AN 113,115
M 85
SH 114
SH 115
M 86
AN 114,114
AN 115,115
M 87
WL 2
AN 116,118
AN 117,119
M 88
SH 118
SH 119
M 89
AN 118,118
AN 119,119
M 90
TY 122,12808
TY 126,26632
M 91
ad 84,1440
M 92
W 0
UL 6,30
M 93
UH 7,30
AR 8,140
M 94
AR 9,164
AD 8,6
M 95
R 82
M 96
W 1
AD 9,7
M 97
AW 140,8
AW 164,9
M 98
UL 6,31
UH 7,31
M 99
WL 3
AN 120,122
AN 121,123
M 100
SH 122
SH 123
M 101
AN 122,122
AN 123,123
M 102
WL 2
AN 124,126
AN 125,127
M 103
SH 126
SH 127
M 104
AN 126,126
AN 127,127
M 105
W 2
AR 8,188
M 106
AD 8,6
AD 144,7
M 107
R 0
M 108
mw 51
AW 188,8
UL 6,32
M 109
ad 84,1446
W 0
M 110
UH 7,32
AR 8,141
M 111
AR 9,165
AD 8,6
M 112
W 1
AD 9,7
M 113
AW 141,8
AW 165,9
)"
R"(M 114
UL 6,33
UH 7,33
M 115
W 2
AR 8,189
M 116
AD 8,6
AD 145,7
M 117
AW 189,8
UL 6,34
M 118
bc 49,16,66,60
UH 7,34
M 119
WL 0
R 2
M 120
mh 9
AR 8,142
AR 9,166
M 121
ad 66,3136
H 0
M 132
AD 8,6
AD 9,7
M 133
TX 58,9216
TX 62,23040
M 122
DG 1024
M 123
AW 142,8
AW 166,9
M 134
UL 6,35
UH 7,35
M 135
TY 66,9728
TY 70,23552
M 124
bc 48,16,64,56
AR 8,190
M 125
mh 12
AD 8,6
AD 146,7
M 136
ad 64,3136
H 0
M 137
TX 74,10752
TX 78,24576
M 126
WL 4
Q 0
M 127
Q 2
M 138
Q 4
M 139
TY 82,11264
TY 86,25088
M 128
Q 6
M 129
WL 4
Q 8
M 140
Q 10
M 141
TX 90,12288
TX 94,26112
M 130
Q 12
M 131
R 4
M 142
Q 14
M 143
TY 98,12800
TY 102,26624
M 0
R 6
M 1
WL 10
Q 16
M 12
Q 18
M 13
Q 20
M 2
Q 22
M 3
WL 8
Q 24
M 14
Q 26
M 15
Q 28
M 4
Q 30
M 5
WL 6
Q 32
M 16
Q 34
M 17
Q 36
M 6
Q 38
M 7
WL 2
Q 40
M 18
Q 42
M 19
Q 44
M 8
Q 46
M 9
H 1
AW 190,8
M 20
ae 42,72,480
M 21
SA 240,256
UL 6,36
M 10
H 2
UH 7,36
M 11
R 8
M 22
AR 8,143
AR 9,167
M 23
R 10
PA 12288
DF
M 24
AD 8,6
AD 9,7
M 25
AW 143,8
AW 167,9
M 26
UL 6,37
UH 7,37
M 27
AR 8,191
AD 8,6
M 28
AD 147,7
AW 191,8
M 29
M 30
M 31
M 32
M 33
WV 7
M 34
WL 0
FP
M 35
M 36
M 37
M 38
M 39
M 40
ae 44,68,864
M 41
s_bitcmp1_b32 s50, 16
s_cselect_b32 s51, 24576, 0
v_add_u32 v47, s51, v3
M 42
XL 14,128
M 43
M 44
M 45
FR
M 46
XL 16,896
M 47
M 48
M 49
M 50
M 51
M 52
XL 18,1664
M 53
WL 0
FQ 232
M 54
M 55
M 56
FZ 232
R 24
M 57
mw 72
R 26
M 58
TY 106,520
TY 110,14344
M 59
R 28
M 60
ad 84,1488
W 0
M 61
XL 20,2432
M 62
M 63
W 1
M 64
M 65
M 66
W 2
M 67
WL 3
AN 104,106
AN 105,107
M 68
SH 106
SH 107
M 69
mw 75
AN 106,106
AN 107,107
M 70
WL 2
AN 108,110
AN 109,111
M 71
R 30
M 72
SH 110
SH 111
M 73
AN 110,110
AN 111,111
M 74
TY 114,2056
TY 118,15880
M 75
ad 84,1494
W 0
M 76
M 77
M 78
W 1
M 79
M 80
M 81
W 2
M 82
M 83
R 32
M 84
mw 0
WL 3
AN 112,114
AN 113,115
M 85
SH 114
SH 115
M 86
AN 114,114
AN 115,115
M 87
WL 2
AN 116,118
AN 117,119
M 88
SH 118
SH 119
M 89
AN 118,118
AN 119,119
M 90
TY 122,3592
TY 126,17416
M 91
ad 84,1536
M 92
W 0
SA 242,1024
M 93
M 94
UL 6,38
UH 7,38
M 95
R 34
M 96
W 1
AR 8,144
M 97
AR 9,168
AD 8,6
M 98
AD 9,7
AW 144,8
M 99
WL 3
AN 120,122
AN 121,123
M 100
SH 122
SH 123
M 101
AN 122,122
AN 123,123
M 102
WL 2
AN 124,126
AN 125,127
M 103
SH 126
SH 127
M 104
AN 126,126
AN 127,127
M 105
W 2
AW 168,9
M 106
UL 6,39
UH 7,39
M 107
R 48
M 108
mw 3
AR 8,192
AD 8,6
M 109
ad 84,1542
W 0
M 110
AD 148,7
AW 192,8
M 111
UL 6,40
UH 7,40
M 112
W 1
AR 8,145
M 113
AR 9,169
AD 8,6
M 114
AD 9,7
AW 145,8
M 115
W 2
AW 169,9
M 116
UL 6,41
UH 7,41
M 117
AR 8,193
AD 8,6
M 118
bc 49,17,66,60
AD 149,7
M 119
WL 0
R 50
M 120
mh 18
AW 193,8
UL 6,42
M 121
ad 66,3328
H 0
M 132
UH 7,42
AR 8,146
M 133
TX 58,0
TX 62,13824
M 122
DG 1024
M 123
AR 9,170
AD 8,6
M 134
AD 9,7
AW 146,8
M 135
TY 66,512
TY 70,14336
M 124
bc 48,17,64,56
AW 170,9
M 125
mh 21
UL 6,43
UH 7,43
)"
R"(M 136
ad 64,3328
H 0
M 137
TX 74,1536
TX 78,15360
M 126
WL 4
Q 0
M 127
Q 2
M 138
Q 4
M 139
TY 82,2048
TY 86,15872
M 128
Q 6
M 129
WL 4
Q 8
M 140
Q 10
M 141
TX 90,3072
TX 94,16896
M 130
Q 12
M 131
R 52
M 142
Q 14
M 143
TY 98,3584
TY 102,17408
M 0
R 54
M 1
WL 10
Q 16
M 12
Q 18
M 13
Q 20
M 2
Q 22
M 3
WL 8
Q 24
M 14
Q 26
M 15
Q 28
M 4
Q 30
M 5
WL 6
Q 32
M 16
Q 34
M 17
Q 36
M 6
Q 38
M 7
WL 2
Q 40
M 18
Q 42
M 19
Q 44
M 8
Q 46
M 9
H 1
SA 244,1792
M 20
AR 8,194
AD 8,6
M 21
AD 150,7
AW 194,8
M 10
H 2
UL 6,44
M 11
R 56
M 22
UH 7,44
AR 8,147
M 23
R 58
M 24
AR 9,171
AD 8,6
M 25
AD 9,7
AW 147,8
M 26
AW 171,9
UL 6,45
M 27
UH 7,45
AR 8,195
M 28
AD 8,6
AD 151,7
M 29
AW 195,8
M 30
M 31
M 32
M 33
WV 6
M 34
WL 0
FP
M 35
M 36
M 37
M 38
M 39
M 40
M 41
M 42
ae 44,68,864
M 43
s_bfe_u32 s51, s50, 0x10011
v_mad_i32_i24 v47, v49, s51, v48
XL 22,128
M 44
M 45
FR
M 46
M 47
M 48
XL 24,896
M 49
M 50
M 51
M 52
M 53
WL 0
FQ 236
M 54
XL 26,1664
M 55
M 56
FZ 236
R 72
M 57
mw 24
R 74
M 58
TY 106,5128
TY 110,18952
M 59
R 76
M 60
ad 84,1584
W 0
M 61
XL 28,2432
M 62
M 63
W 1
M 64
M 65
M 66
W 2
M 67
WL 3
AN 104,106
AN 105,107
M 68
SH 106
SH 107
M 69
mw 27
AN 106,106
AN 107,107
M 70
WL 2
AN 108,110
AN 109,111
M 71
R 78
M 72
SH 110
SH 111
M 73
AN 110,110
AN 111,111
M 74
TY 114,6664
TY 118,20488
M 75
ad 84,1590
W 0
M 76
M 77
M 78
W 1
M 79
M 80
M 81
W 2
SA 246,2560
M 82
M 83
R 80
M 84
mw 48
WL 3
AN 112,114
AN 113,115
M 85
SH 114
SH 115
M 86
AN 114,114
AN 115,115
M 87
WL 2
AN 116,118
AN 117,119
M 88
SH 118
SH 119
M 89
AN 118,118
AN 119,119
M 90
TY 122,8200
TY 126,22024
M 91
ad 84,1632
M 92
W 0
M 93
M 94
M 95
R 82
M 96
W 1
UL 6,14
M 97
UH 7,14
AR 8,148
M 98
AR 9,172
AD 8,6
M 99
WL 3
AN 120,122
AN 121,123
M 100
SH 122
SH 123
M 101
AN 122,122
AN 123,123
M 102
WL 2
AN 124,126
AN 125,127
M 103
SH 126
SH 127
M 104
AN 126,126
AN 127,127
M 105
W 2
AD 9,7
M 106
AW 148,8
AW 172,9
M 107
R 0
M 108
mw 51
UL 6,15
UH 7,15
M 109
ad 84,1638
W 0
M 110
AR 8,196
AD 8,6
M 111
AD 152,7
AW 196,8
M 112
W 1
UL 6,16
M 113
UH 7,16
AR 8,149
M 114
AR 9,173
AD 8,6
M 115
W 2
AD 9,7
M 116
AW 149,8
AW 173,9
M 117
UL 6,17
UH 7,17
M 118
bc 49,18,66,60
AR 8,197
M 119
WL 0
R 2
M 120
mh 0
AD 8,6
AD 153,7
M 121
ad 66,3520
H 0
M 132
AW 197,8
UL 6,18
M 133
TX 58,4608
TX 62,18432
M 122
DG 1024
M 123
UH 7,18
AR 8,150
M 134
AR 9,174
AD 8,6
M 135
TY 66,5120
TY 70,18944
M 124
bc 48,18,64,56
AD 9,7
M 125
mh 3
AW 150,8
AW 174,9
M 136
ad 64,3520
H 0
M 137
TX 74,6144
TX 78,19968
M 126
WL 4
Q 0
M 127
Q 2
M 138
Q 4
M 139
TY 82,6656
TY 86,20480
M 128
Q 6
M 129
WL 4
Q 8
M 140
Q 10
M 141
TX 90,7680
TX 94,21504
M 130
Q 12
M 131
R 4
M 142
Q 14
M 143
TY 98,8192
TY 102,22016
M 0
R 6
M 1
WL 10
Q 16
M 12
Q 18
M 13
Q 20
M 2
Q 22
M 3
WL 8
Q 24
M 14
Q 26
M 15
)"
R"(Q 28
M 4
Q 30
M 5
WL 6
Q 32
M 16
Q 34
M 17
Q 36
M 6
Q 38
M 7
WL 2
Q 40
M 18
Q 42
M 19
Q 44
M 8
Q 46
M 9
H 1
UL 6,19
M 20
ae 42,72,480
M 21
s_bfe_u32 s52, s50, 0x1000b
v_mad_i32_i24 v50, v49, s52, v48
SA 248,256
M 10
H 2
UH 7,19
M 11
R 8
M 22
AR 8,198
AD 8,6
M 23
R 10
PA 12288
DF
M 24
AD 154,7
AW 198,8
M 25
UL 6,20
UH 7,20
M 26
AR 8,151
AR 9,175
M 27
AD 8,6
AD 9,7
M 28
AW 151,8
AW 175,9
M 29
UL 6,21
UH 7,21
M 30
AR 8,199
AD 8,6
M 31
AD 155,7
AW 199,8
M 32
M 33
WV 7
M 34
WL 0
FP
M 35
M 36
M 37
M 38
M 39
M 40
M 41
M 42
M 43
M 44
M 45
FR
M 46
M 47
M 48
M 49
M 50
M 51
M 52
M 53
WL 0
FQ 240
M 54
M 55
M 56
FZ 240
R 24
M 57
mw 72
R 26
M 58
TY 106,9736
TY 110,23560
M 59
R 28
M 60
ad 84,1680
W 0
M 61
M 62
M 63
W 1
M 64
M 65
M 66
W 2
M 67
WL 3
AN 104,106
AN 105,107
M 68
SH 106
SH 107
M 69
mw 75
AN 106,106
AN 107,107
M 70
WL 2
AN 108,110
AN 109,111
M 71
R 30
M 72
SH 110
SH 111
M 73
AN 110,110
AN 111,111
M 74
TY 114,11272
TY 118,25096
M 75
ad 84,1686
W 0
M 76
ae 44,70,768
M 77
s_bitcmp1_b32 s50, 12
s_cselect_b32 s51, 24576, 0
v_add_u32 v47, s51, v3
M 78
W 1
XL 30,256
M 79
XL 32,1024
M 80
M 81
W 2
M 82
XL 34,1792
M 83
WL 1
R 32
M 84
mw 0
AN 112,114
AN 113,115
M 85
SH 114
SH 115
M 86
AN 114,114
AN 115,115
M 87
WL 2
AN 116,118
AN 117,119
M 88
SH 118
SH 119
M 89
AN 118,118
AN 119,119
M 90
TY 122,12808
TY 126,26632
M 91
ad 84,1728
M 92
W 0
SA 250,1024
M 93
XL 36,2560
M 94
M 95
R 34
M 96
W 1
M 97
M 98
UL 6,22
UH 7,22
M 99
WL 3
AN 120,122
AN 121,123
M 100
SH 122
SH 123
M 101
AN 122,122
AN 123,123
M 102
WL 2
AN 124,126
AN 125,127
M 103
SH 126
SH 127
M 104
AN 126,126
AN 127,127
M 105
W 2
AR 8,152
M 106
AR 9,176
AD 8,6
M 107
R 48
M 108
mw 3
AD 9,7
AW 152,8
M 109
ad 84,1734
W 0
M 110
AW 176,9
UL 6,23
M 111
UH 7,23
AD 132,6
M 112
W 1
AD 156,7
M 113
UL 6,24
UH 7,24
M 114
AR 8,153
AR 9,177
M 115
W 2
AD 8,6
M 116
AD 9,7
AW 153,8
M 117
AW 177,9
UL 6,25
M 118
bc 49,19,66,60
UH 7,25
M 119
WL 0
R 50
M 120
mh 9
AD 133,6
AD 157,7
M 121
ad 66,3712
H 0
M 132
UL 6,26
UH 7,26
M 133
TX 58,9216
TX 62,23040
M 122
DG 1024
M 123
AR 8,154
AR 9,178
M 134
AD 8,6
AD 9,7
M 135
TY 66,9728
TY 70,23552
M 124
bc 48,19,64,56
AW 154,8
M 125
mh 12
AW 178,9
UL 6,27
M 136
ad 64,3712
H 0
M 137
TX 74,10752
TX 78,24576
M 126
WL 4
Q 0
M 127
Q 2
M 138
Q 4
M 139
TY 82,11264
TY 86,25088
M 128
Q 6
M 129
WL 4
Q 8
M 140
Q 10
M 141
TX 90,12288
TX 94,26112
M 130
Q 12
M 131
R 52
M 142
Q 14
M 143
TY 98,12800
TY 102,26624
M 0
R 54
M 1
WL 10
Q 16
M 12
Q 18
M 13
Q 20
M 2
Q 22
M 3
WL 8
Q 24
M 14
Q 26
M 15
Q 28
M 4
Q 30
M 5
WL 6
Q 32
M 16
Q 34
M 17
Q 36
M 6
Q 38
M 7
WL 2
Q 40
M 18
Q 42
M 19
Q 44
M 8
Q 46
M 9
H 1
SA 252,1792
M 20
UH 7,27
AD 134,6
M 21
AD 158,7
UL 6,28
M 10
H 2
UH 7,28
M 11
R 56
M 22
AR 8,155
AR 9,179
M 23
R 58
M 24
AD 8,6
AD 9,7
M 25
AW 155,8
AW 179,9
M 26
)"
R"(UL 6,29
UH 7,29
M 27
AD 135,6
AD 159,7
M 28
M 29
M 30
M 31
M 32
M 33
WV 6
M 34
WL 0
FP
M 35
M 36
M 37
M 38
M 39
M 40
M 41
M 42
M 43
M 44
M 45
FR
M 46
M 47
M 48
M 49
M 50
M 51
M 52
M 53
WL 0
FQ 244
M 54
M 55
M 56
FZ 244
R 72
M 57
mw 24
R 74
M 58
TY 106,520
TY 110,14344
M 59
R 76
M 60
ad 84,1776
W 0
M 61
M 62
M 63
W 1
M 64
M 65
M 66
W 2
M 67
WL 3
AN 104,106
AN 105,107
M 68
SH 106
SH 107
M 69
mw 27
AN 106,106
AN 107,107
M 70
WL 2
AN 108,110
AN 109,111
M 71
R 78
M 72
SH 110
SH 111
M 73
AN 110,110
AN 111,111
M 74
TY 114,2056
TY 118,15880
M 75
ad 84,1782
W 0
M 76
ae 44,70,768
M 77
s_bfe_u32 s51, s50, 0x1000d
v_mad_i32_i24 v47, v49, s51, v48
XL 38,256
M 78
W 1
M 79
M 80
XL 40,1024
M 81
W 2
SA 254,2560
M 82
M 83
WL 1
R 80
M 84
mw 48
AN 112,114
AN 113,115
M 85
SH 114
SH 115
M 86
AN 114,114
AN 115,115
M 87
WL 2
AN 116,118
AN 117,119
M 88
SH 118
SH 119
M 89
AN 118,118
AN 119,119
M 90
TY 122,3592
TY 126,17416
M 91
ad 84,1824
M 92
W 0
XL 42,1792
M 93
XL 44,2560
M 94
M 95
R 82
M 96
W 1
M 97
M 98
M 99
WL 3
AN 120,122
AN 121,123
M 100
SH 122
SH 123
M 101
AN 122,122
AN 123,123
M 102
WL 2
AN 124,126
AN 125,127
M 103
SH 126
SH 127
M 104
AN 126,126
AN 127,127
M 105
W 2
M 106
M 107
R 0
M 108
mw 51
M 109
ad 84,1830
W 0
M 110
M 111
M 112
W 1
M 113
M 114
M 115
W 2
M 116
M 117
M 118
bc 49,20,66,60
M 119
WL 2
R 2
M 120
mh 18
M 121
ad 66,3904
H 0
M 132
M 133
TX 58,0
TX 62,13824
M 122
DG 1024
M 123
M 134
UL 6,30
UH 7,30
M 135
TY 66,512
TY 70,14336
M 124
bc 48,20,64,56
AD 160,6
M 125
mh 21
AD 184,7
UL 6,31
M 136
ad 64,3904
H 0
M 137
TX 74,1536
TX 78,15360
M 126
WL 4
Q 0
M 127
Q 2
M 138
Q 4
M 139
TY 82,2048
TY 86,15872
M 128
Q 6
M 129
WL 4
Q 8
M 140
Q 10
M 141
TX 90,3072
TX 94,16896
M 130
Q 12
M 131
R 4
M 142
Q 14
M 143
TY 98,3584
TY 102,17408
M 0
R 6
M 1
WL 10
Q 16
M 12
Q 18
M 13
Q 20
M 2
Q 22
M 3
WL 8
Q 24
M 14
Q 26
M 15
Q 28
M 4
Q 30
M 5
WL 6
Q 32
M 16
Q 34
M 17
Q 36
M 6
Q 38
M 7
WL 2
Q 40
M 18
Q 42
M 19
Q 44
M 8
Q 46
M 9
H 1
UH 7,31
M 20
AD 208,6
AD 232,7
M 21
UL 6,32
UH 7,32
M 10
H 2
AD 161,6
M 11
R 8
M 22
AD 185,7
UL 6,33
M 23
R 10
M 24
UH 7,33
AD 209,6
M 25
AD 233,7
UL 6,34
M 26
UH 7,34
AD 162,6
M 27
AD 186,7
UL 6,35
M 28
UH 7,35
AD 210,6
M 29
AD 234,7
UL 6,36
M 30
UH 7,36
AD 163,6
M 31
AD 187,7
UL 6,37
M 32
UH 7,37
AD 211,6
M 33
WV 5
AD 235,7
M 34
WL 0
FP
M 35
M 36
M 37
M 38
M 39
M 40
M 41
M 42
M 43
M 44
M 45
FR
M 46
M 47
M 48
M 49
M 50
M 51
M 52
M 53
WL 0
FQ 248
M 54
M 55
M 56
FZ 248
R 24
M 57
mw 72
R 26
M 58
TY 106,5128
TY 110,18952
M 59
R 28
M 60
ad 84,1872
W 0
M 61
M 62
M 63
W 1
M 64
M 65
M 66
W 2
M 67
WL 3
AN 104,106
AN 105,107
M 68
SH 106
SH 107
M 69
mw 75
AN 106,106
AN 107,107
M 70
WL 2
AN 108,110
)"
R"(AN 109,111
M 71
R 30
M 72
SH 110
SH 111
M 73
AN 110,110
AN 111,111
M 74
TY 114,6664
TY 118,20488
M 75
ad 84,1878
W 0
M 76
M 77
M 78
W 1
M 79
ae 44,68,768
M 80
s_bitcmp1_b32 s50, 14
s_cselect_b32 s51, 24576, 0
v_add_u32 v47, s51, v3
M 81
W 2
XL 14,256
M 82
M 83
WL 1
R 32
M 84
mw 0
AN 112,114
AN 113,115
M 85
SH 114
SH 115
M 86
AN 114,114
AN 115,115
M 87
WL 2
AN 116,118
AN 117,119
M 88
SH 118
SH 119
M 89
AN 118,118
AN 119,119
M 90
TY 122,8200
TY 126,22024
M 91
ad 84,1920
M 92
W 0
XL 16,1024
M 93
XL 18,1792
M 94
M 95
R 34
M 96
W 1
XL 20,2560
M 97
M 98
M 99
WL 3
AN 120,122
AN 121,123
M 100
SH 122
SH 123
M 101
AN 122,122
AN 123,123
M 102
WL 2
AN 124,126
AN 125,127
M 103
SH 126
SH 127
M 104
AN 126,126
AN 127,127
M 105
W 2
M 106
M 107
R 48
M 108
mw 3
M 109
ad 84,1926
W 0
M 110
M 111
M 112
W 1
M 113
M 114
M 115
W 2
M 116
M 117
M 118
bc 49,21,66,60
M 119
WL 2
R 50
M 120
mh 0
M 121
ad 66,4096
H 0
M 132
M 133
TX 58,4608
TX 62,18432
M 122
DG 1024
M 123
M 134
M 135
TY 66,5120
TY 70,18944
M 124
bc 48,21,64,56
M 125
mh 3
M 136
ad 64,4096
H 0
M 137
TX 74,6144
TX 78,19968
M 126
WL 4
Q 0
M 127
Q 2
M 138
Q 4
M 139
TY 82,6656
TY 86,20480
M 128
Q 6
M 129
WL 4
Q 8
M 140
Q 10
M 141
TX 90,7680
TX 94,21504
M 130
Q 12
M 131
R 52
M 142
Q 14
M 143
TY 98,8192
TY 102,22016
M 0
R 54
M 1
WL 10
Q 16
M 12
Q 18
M 13
Q 20
M 2
Q 22
M 3
WL 8
Q 24
M 14
Q 26
M 15
Q 28
M 4
Q 30
M 5
WL 6
Q 32
M 16
Q 34
M 17
Q 36
M 6
Q 38
M 7
WL 2
Q 40
M 18
Q 42
M 19
Q 44
M 8
Q 46
M 9
H 1
UL 6,38
M 20
UH 7,38
AD 164,6
M 21
AD 188,7
UL 6,39
M 10
H 2
UH 7,39
M 11
R 56
M 22
AD 212,6
AD 236,7
M 23
R 58
M 24
UL 6,40
UH 7,40
M 25
AD 165,6
AD 189,7
M 26
UL 6,41
UH 7,41
M 27
AD 213,6
AD 237,7
M 28
UL 6,42
UH 7,42
M 29
AD 166,6
AD 190,7
M 30
UL 6,43
UH 7,43
M 31
AD 214,6
AD 238,7
M 32
UL 6,44
UH 7,44
M 33
WV 5
AD 167,6
AD 191,7
M 34
WL 0
FP
UL 6,45
M 35
UH 7,45
AD 215,6
M 36
AD 239,7
M 37
M 38
M 39
M 40
M 41
M 42
M 43
M 44
M 45
FR
M 46
M 47
M 48
M 49
M 50
M 51
M 52
M 53
WL 0
FQ 252
M 54
M 55
M 56
FZ 252
R 72
M 57
mw 24
R 74
M 58
TY 106,9736
TY 110,23560
M 59
R 76
M 60
ad 84,1968
W 0
M 61
M 62
M 63
W 1
M 64
M 65
M 66
W 2
M 67
WL 3
AN 104,106
AN 105,107
M 68
SH 106
SH 107
M 69
mw 27
AN 106,106
AN 107,107
M 70
WL 2
AN 108,110
AN 109,111
M 71
R 78
M 72
SH 110
SH 111
M 73
AN 110,110
AN 111,111
M 74
TY 114,11272
TY 118,25096
M 75
ad 84,1974
W 0
M 76
M 77
M 78
W 1
M 79
M 80
M 81
W 2
M 82
ae 44,68,768
M 83
WL 1
R 80
M 84
mw 48
AN 112,114
AN 113,115
M 85
s_bfe_u32 s51, s50, 0x1000f
SH 114
SH 115
M 86
AN 114,114
AN 115,115
M 87
WL 2
AN 116,118
AN 117,119
M 88
SH 118
SH 119
M 89
AN 118,118
AN 119,119
M 90
TY 122,12808
TY 126,26632
M 91
ad 84,2016
M 92
W 0
v_mad_i32_i24 v47, v49, s51, v48
M 93
XL 22,256
M 94
XL 24,1024
M 95
R 82
M 96
W 1
XL 26,1792
M 97
M 98
M 99
WL 3
AN 120,122
AN 121,123
M 100
SH 122
SH 123
)"
R"(M 101
AN 122,122
AN 123,123
M 102
WL 2
AN 124,126
AN 125,127
M 103
SH 126
SH 127
M 104
AN 126,126
AN 127,127
M 105
W 2
XL 28,2560
M 106
M 107
R 0
M 108
mw 51
M 109
ad 84,2022
W 0
M 110
M 111
M 112
W 1
M 113
M 114
M 115
W 2
M 116
M 117
M 118
bc 49,22,66,60
M 119
WL 2
R 2
M 120
mh 9
M 121
ad 66,4288
H 0
M 132
M 133
TX 58,9216
TX 62,23040
M 122
DG 1024
M 123
M 134
M 135
TY 66,9728
TY 70,23552
M 124
bc 48,22,64,56
M 125
mh 12
M 136
ad 64,4288
H 0
M 137
TX 74,10752
TX 78,24576
M 126
WL 4
Q 0
M 127
Q 2
M 138
Q 4
M 139
TY 82,11264
TY 86,25088
M 128
Q 6
M 129
WL 4
Q 8
M 140
Q 10
M 141
TX 90,12288
TX 94,26112
M 130
Q 12
M 131
R 4
M 142
Q 14
M 143
TY 98,12800
TY 102,26624
M 0
R 6
M 1
WL 10
Q 16
M 12
Q 18
M 13
Q 20
M 2
Q 22
M 3
WL 8
Q 24
M 14
Q 26
M 15
Q 28
M 4
Q 30
M 5
WL 6
Q 32
M 16
Q 34
M 17
Q 36
M 6
Q 38
M 7
WL 2
Q 40
M 18
Q 42
M 19
Q 44
M 8
Q 46
M 9
H 1
UL 6,14
M 20
UH 7,14
AD 168,6
M 21
AD 192,7
UL 6,15
M 10
H 2
UH 7,15
M 11
R 8
M 22
AD 216,6
AD 240,7
M 23
R 10
M 24
UL 6,16
UH 7,16
M 25
AD 169,6
AD 193,7
M 26
UL 6,17
UH 7,17
M 27
AD 217,6
AD 241,7
M 28
UL 6,18
UH 7,18
M 29
AD 170,6
AD 194,7
M 30
UL 6,19
UH 7,19
M 31
AD 218,6
AD 242,7
M 32
UL 6,20
UH 7,20
M 33
WV 5
AD 171,6
AD 195,7
M 34
WL 0
FP
UL 6,21
M 35
UH 7,21
AD 219,6
M 36
AD 243,7
M 37
M 38
M 39
M 40
M 41
M 42
M 43
M 44
M 45
FR
M 46
M 47
M 48
M 49
M 50
M 51
M 52
M 53
WL 0
FQ 256
M 54
M 55
M 56
FZ 256
R 24
M 57
mw 72
R 26
M 58
TY 106,520
TY 110,14344
M 59
R 28
M 60
ad 84,2064
W 0
M 61
M 62
M 63
W 1
M 64
M 65
M 66
W 2
M 67
WL 3
AN 104,106
AN 105,107
M 68
SH 106
SH 107
M 69
mw 75
AN 106,106
AN 107,107
M 70
WL 2
AN 108,110
AN 109,111
M 71
R 30
M 72
SH 110
SH 111
M 73
AN 110,110
AN 111,111
M 74
TY 114,2056
TY 118,15880
M 75
ad 84,2070
W 0
M 76
M 77
M 78
W 1
M 79
M 80
M 81
W 2
M 82
M 83
R 32
M 84
mw 0
WL 3
AN 112,114
AN 113,115
M 85
SH 114
SH 115
M 86
AN 114,114
AN 115,115
M 87
WL 2
AN 116,118
AN 117,119
M 88
SH 118
SH 119
M 89
AN 118,118
AN 119,119
M 90
TY 122,3592
TY 126,17416
M 91
ad 84,2112
M 92
W 0
M 93
ae 44,68,864
M 94
s_bitcmp1_b32 s50, 16
s_cselect_b32 s51, 24576, 0
v_add_u32 v47, s51, v3
M 95
R 34
M 96
W 1
XL 30,256
M 97
XL 32,1024
M 98
XL 34,1792
M 99
WL 3
AN 120,122
AN 121,123
M 100
SH 122
SH 123
M 101
AN 122,122
AN 123,123
M 102
WL 2
AN 124,126
AN 125,127
M 103
SH 126
SH 127
M 104
AN 126,126
AN 127,127
M 105
W 2
XL 36,2560
M 106
M 107
R 48
M 108
mw 3
M 109
ad 84,2118
W 0
M 110
M 111
M 112
W 1
M 113
M 114
M 115
W 2
M 116
M 117
M 118
bc 49,23,66,60
M 119
WL 2
R 50
M 120
mh 18
M 121
ad 66,4480
H 0
M 132
M 133
TX 58,0
TX 62,13824
M 122
DG 1024
M 123
M 134
M 135
TY 66,512
TY 70,14336
M 124
bc 48,23,64,56
M 125
mh 21
M 136
ad 64,4480
H 0
M 137
TX 74,1536
TX 78,15360
M 126
WL 4
Q 0
M 127
Q 2
M 138
Q 4
M 139
TY 82,2048
TY 86,15872
)"
R"(M 128
Q 6
M 129
WL 4
Q 8
M 140
Q 10
M 141
TX 90,3072
TX 94,16896
M 130
Q 12
M 131
R 52
M 142
Q 14
M 143
TY 98,3584
TY 102,17408
M 0
R 54
M 1
WL 10
Q 16
M 12
Q 18
M 13
Q 20
M 2
Q 22
M 3
WL 8
Q 24
M 14
Q 26
M 15
Q 28
M 4
Q 30
M 5
WL 6
Q 32
M 16
Q 34
M 17
Q 36
M 6
Q 38
M 7
WL 2
Q 40
M 18
Q 42
M 19
Q 44
M 8
Q 46
M 9
H 1
UL 6,22
M 20
UH 7,22
AD 172,6
M 21
AD 196,7
UL 6,23
M 10
H 2
UH 7,23
M 11
R 56
M 22
AD 220,6
AD 244,7
M 23
R 58
M 24
UL 6,24
UH 7,24
M 25
AD 173,6
AD 197,7
M 26
UL 6,25
UH 7,25
M 27
AD 221,6
AD 245,7
M 28
UL 6,26
UH 7,26
M 29
AD 174,6
AD 198,7
M 30
UL 6,27
UH 7,27
M 31
AD 222,6
AD 246,7
M 32
UL 6,28
UH 7,28
M 33
WV 5
AD 175,6
AD 199,7
M 34
WL 0
FP
UL 6,29
M 35
UH 7,29
AD 223,6
M 36
AD 247,7
M 37
M 38
M 39
M 40
M 41
M 42
M 43
M 44
M 45
FR
M 46
M 47
M 48
M 49
M 50
M 51
M 52
M 53
WL 0
FQ 260
M 54
M 55
M 56
FZ 260
R 72
M 57
mw 24
R 74
M 58
TY 106,5128
TY 110,18952
M 59
R 76
M 60
ad 84,2160
W 0
M 61
M 62
M 63
W 1
M 64
M 65
M 66
W 2
M 67
WL 3
AN 104,106
AN 105,107
M 68
SH 106
SH 107
M 69
mw 27
AN 106,106
AN 107,107
M 70
WL 2
AN 108,110
AN 109,111
M 71
R 78
M 72
SH 110
SH 111
M 73
AN 110,110
AN 111,111
M 74
TY 114,6664
TY 118,20488
M 75
ad 84,2166
W 0
M 76
M 77
M 78
W 1
M 79
M 80
M 81
W 2
M 82
M 83
R 80
M 84
mw 48
WL 3
AN 112,114
AN 113,115
M 85
SH 114
SH 115
M 86
AN 114,114
AN 115,115
M 87
WL 2
AN 116,118
AN 117,119
M 88
SH 118
SH 119
M 89
AN 118,118
AN 119,119
M 90
TY 122,8200
TY 126,22024
M 91
ad 84,2208
M 92
W 0
M 93
ae 44,68,864
M 94
s_bfe_u32 s51, s50, 0x10011
v_mad_i32_i24 v47, v49, s51, v48
XL 38,256
M 95
R 82
M 96
W 1
XL 40,1024
M 97
M 98
M 99
WL 3
AN 120,122
AN 121,123
M 100
SH 122
SH 123
M 101
AN 122,122
AN 123,123
M 102
WL 2
AN 124,126
AN 125,127
M 103
SH 126
SH 127
M 104
AN 126,126
AN 127,127
M 105
W 2
XL 42,1792
M 106
M 107
R 0
M 108
mw 51
XL 44,2560
M 109
ad 84,2214
W 0
M 110
M 111
M 112
W 1
M 113
M 114
M 115
W 2
M 116
M 117
M 118
bc 48,0,64,56
M 119
WL 2
R 2
M 120
mh 3
M 121
ad 64,64
H 0
M 132
M 133
TX 58,4608
TX 62,18432
M 122
H 1
M 123
M 134
M 135
TY 66,5120
TY 70,18944
M 124
H 2
M 125
M 136
M 137
TX 74,6144
TX 78,19968
M 126
WL 4
Q 0
M 127
Q 2
M 138
Q 4
M 139
TY 82,6656
TY 86,20480
M 128
Q 6
M 129
WL 4
Q 8
M 140
Q 10
M 141
TX 90,7680
TX 94,21504
M 130
Q 12
M 131
R 4
M 142
Q 14
M 143
TY 98,8192
TY 102,22016
M 0
R 6
M 1
WL 10
Q 16
M 12
Q 18
M 13
Q 20
M 2
Q 22
M 3
WL 8
Q 24
M 14
Q 26
M 15
Q 28
M 4
Q 30
M 5
WL 6
Q 32
M 16
Q 34
M 17
Q 36
M 6
Q 38
M 7
WL 2
Q 40
M 18
Q 42
M 19
Q 44
M 8
Q 46
M 9
UL 6,30
UH 7,30
M 20
AD 176,6
AD 200,7
M 21
UL 6,31
UH 7,31
M 10
AD 224,6
AD 248,7
M 11
R 8
M 22
UL 6,32
UH 7,32
M 23
R 10
M 24
AD 177,6
AD 201,7
)"
R"(M 25
UL 6,33
UH 7,33
M 26
AD 225,6
AD 249,7
M 27
UL 6,34
UH 7,34
M 28
AD 178,6
AD 202,7
M 29
UL 6,35
UH 7,35
M 30
AD 226,6
AD 250,7
M 31
UL 6,36
UH 7,36
M 32
AD 179,6
AD 203,7
M 33
WV 3
UL 6,37
UH 7,37
M 34
WL 0
FP
AD 227,6
M 35
AD 251,7
M 36
M 37
M 38
M 39
M 40
M 41
M 42
M 43
M 44
M 45
FR
M 46
M 47
M 48
M 49
M 50
M 51
M 52
M 53
WL 0
FQ 264
M 54
M 55
M 56
FZ 264
R 24
M 57
mw 72
R 26
M 58
TY 106,9736
TY 110,23560
M 59
R 28
M 60
ad 84,2256
W 0
M 61
M 62
M 63
W 1
M 64
M 65
M 66
W 2
M 67
WL 3
AN 104,106
AN 105,107
M 68
SH 106
SH 107
M 69
mw 75
AN 106,106
AN 107,107
M 70
WL 2
AN 108,110
AN 109,111
M 71
R 30
M 72
SH 110
SH 111
M 73
AN 110,110
AN 111,111
M 74
TY 114,11272
TY 118,25096
M 75
ad 84,2262
W 0
M 76
M 77
M 78
W 1
M 79
M 80
M 81
W 2
M 82
M 83
R 32
M 84
mw 0
WL 3
AN 112,114
AN 113,115
M 85
SH 114
SH 115
M 86
AN 114,114
AN 115,115
M 87
WL 2
AN 116,118
AN 117,119
M 88
SH 118
SH 119
M 89
AN 118,118
AN 119,119
M 90
TY 122,12808
TY 126,26632
M 91
ad 84,0
M 92
W 0
M 93
M 94
M 95
R 34
M 96
W 1
M 97
M 98
M 99
WL 3
AN 120,122
AN 121,123
M 100
SH 122
SH 123
M 101
AN 122,122
AN 123,123
M 102
WL 2
AN 124,126
AN 125,127
M 103
SH 126
SH 127
M 104
AN 126,126
AN 127,127
M 105
W 2
M 106
M 107
R 48
M 108
mw 3
M 109
ad 84,6
W 0
M 110
M 111
M 112
W 1
M 113
M 114
M 115
W 2
M 116
M 117
M 118
bc 48,1,64,56
M 119
WL 2
R 50
M 120
mh 15
M 121
ad 64,259
H 0
M 132
M 133
TX 58,9216
TX 62,23040
M 122
DG 1024
M 123
M 134
M 135
TY 66,9728
TY 70,23552
M 124
M 125
M 136
M 137
TX 74,10752
TX 78,24576
M 126
WL 4
Q 0
M 127
Q 2
M 138
Q 4
M 139
TY 82,11264
TY 86,25088
M 128
Q 6
M 129
WL 4
Q 8
M 140
Q 10
M 141
TX 90,12288
TX 94,26112
M 130
Q 12
M 131
R 52
M 142
Q 14
M 143
TY 98,12800
TY 102,26624
M 0
R 54
M 1
WL 10
Q 16
M 12
Q 18
M 13
Q 20
M 2
Q 22
M 3
WL 8
Q 24
M 14
Q 26
M 15
Q 28
M 4
Q 30
M 5
WL 6
Q 32
M 16
Q 34
M 17
Q 36
M 6
Q 38
M 7
WL 2
Q 40
M 18
Q 42
M 19
Q 44
M 8
Q 46
M 9
UL 6,38
UH 7,38
M 20
AD 180,6
AD 204,7
M 21
UL 6,39
UH 7,39
M 10
AD 228,6
AD 252,7
M 11
R 56
M 22
UL 6,40
UH 7,40
M 23
R 58
M 24
AD 181,6
AD 205,7
M 25
UL 6,41
UH 7,41
M 26
AD 229,6
AD 253,7
BV 52,4608
M 27
UL 6,42
UH 7,42
M 28
AD 182,6
AD 206,7
M 29
UL 6,43
UH 7,43
M 30
AD 230,6
AD 254,7
M 31
UL 6,44
UH 7,44
M 32
AD 183,6
AD 207,7
M 33
WV 8
UL 6,45
UH 7,45
M 34
WL 1
FP
AD 231,6
M 35
AD 255,7
M 36
M 37
M 38
M 39
M 40
ds_read_b64 v[12:13], v4 offset:4864
ds_read_b64 v[46:47], v4 offset:4872
M 41
AR 6,36
AR 7,60
M 42
AR 8,84
AR 9,108
M 43
WL 3
EF 6,52
EF 7,53
M 44
EF 8,54
EF 9,55
M 45
FR
CV 14,6,7
M 46
CV 15,8,9
AR 6,37
M 47
AR 7,61
AR 8,85
M 48
AR 9,109
EF 6,52
M 49
EF 7,53
EF 8,54
M 50
EF 9,55
CV 16,6,7
M 51
CV 17,8,9
AR 6,38
M 52
AR 7,62
AR 8,86
M 53
WL 0
FQ 268
AR 9,110
M 54
EF 6,52
EF 7,53
M 55
EF 8,54
EF 9,55
)"
R"(M 56
FZ 268
R 72
M 57
mw 24
R 74
M 58
TY 114,2056
TY 118,15880
M 59
R 76
M 60
ad 84,48
W 0
M 61
CV 18,6,7
CV 19,8,9
M 62
AR 6,39
AR 7,63
M 63
W 1
AR 8,87
M 64
AR 9,111
EF 6,52
M 65
EF 7,53
EF 8,54
M 66
W 2
EF 9,55
M 67
WL 3
AN 112,114
AN 113,115
M 68
SH 114
SH 115
M 69
mw 27
AN 114,114
AN 115,115
M 70
WL 2
AN 116,118
AN 117,119
M 71
R 78
M 72
SH 118
SH 119
M 73
AN 118,118
AN 119,119
M 74
TY 122,3592
TY 126,17416
M 75
ad 84,54
W 0
M 76
CV 20,6,7
CV 21,8,9
M 77
ae 42,74,768
AR 6,40
M 78
W 1
AR 7,64
M 79
s_bitcmp1_b32 s50, 12
s_cselect_b32 s52, 24576, 0
v_add_u32 v50, s52, v3
M 80
ST 14,0
AR 8,88
M 81
W 2
AR 9,112
M 82
EF 6,52
EF 7,53
M 83
WL 1
R 80
M 84
mw 48
AN 120,122
AN 121,123
M 85
SH 122
SH 123
M 86
AN 122,122
AN 123,123
M 87
WL 2
AN 124,126
AN 125,127
M 88
SH 126
SH 127
M 89
AN 126,126
AN 127,127
M 90
ad 84,96
W 0
M 91
ST 16,768
EF 8,54
M 92
EF 9,55
CV 22,6,7
M 93
W 1
CV 23,8,9
M 94
AR 6,41
AR 7,65
M 95
R 82
M 96
W 2
ST 18,1536
M 97
AR 8,89
AR 9,113
M 98
EF 6,52
EF 7,53
M 99
mw 51
EF 8,54
EF 9,55
M 100
ad 84,102
W 0
M 101
ST 20,2304
CV 24,6,7
M 102
CV 25,8,9
AR 6,42
M 103
W 1
AR 7,66
M 104
AR 8,90
AR 9,114
M 105
EF 6,52
EF 7,53
M 106
W 2
EF 8,54
M 107
EF 9,55
CV 26,6,7
M 108
CV 27,8,9
AR 6,43
M 109
AR 7,67
AR 8,91
M 110
AR 9,115
EF 6,52
M 111
EF 7,53
EF 8,54
M 112
EF 9,55
CV 28,6,7
M 113
CV 29,8,9
M 114
ae 42,74,768
s_bfe_u32 s52, s50, 0x1000d
BV 52,5120
M 115
WV 18
v_mad_i32_i24 v50, v49, s52, v48
ST 22,0
M 116
WL 1
AR 6,132
AR 7,156
M 117
s_barrier
R 0
M 118
mw 72
AR 8,180
EF 6,12
M 119
R 2
M 120
ad 84,144
W 0
M 121
ST 24,768
EF 7,13
M 132
EF 8,46
EF 136,47
M 133
CV 30,6,7
CV 31,8,136
M 122
W 1
AR 6,133
M 123
ST 26,1536
AR 7,157
M 134
AR 8,181
EF 6,12
M 135
EF 7,13
EF 8,46
M 124
W 2
EF 137,47
M 125
ST 28,2304
CV 32,6,7
M 136
CV 33,8,137
AR 6,134
M 137
TX 74,1536
TX 78,15360
M 126
mw 75
AR 7,158
AR 8,182
M 127
ad 84,150
W 0
M 138
EF 6,12
EF 7,13
M 139
TY 82,2048
TY 86,15872
M 128
EF 8,46
EF 138,47
M 129
W 1
CV 34,6,7
M 140
CV 35,8,138
AR 6,135
M 141
WL 2
TX 90,3072
TX 94,16896
P 16
M 130
Q 17
P 19
M 131
R 4
P 20
M 142
Q 21
P 23
M 143
WL 4
TY 98,3584
TY 102,17408
P 24
M 4
R 6
P 25
M 5
Q 26
P 28
M 16
Q 29
P 31
M 17
WL 6
Q 32
M 6
Q 34
M 7
Q 36
M 18
Q 38
M 19
WL 2
Q 40
M 8
Q 42
M 9
Q 44
M 20
Q 46
M 21
W 2
AR 7,159
M 10
AR 8,183
EF 6,12
M 11
R 8
M 22
EF 7,13
EF 8,46
M 23
R 10
M 28
EF 139,47
CV 36,6,7
M 29
CV 37,8,139
M 30
ae 42,74,768
AR 6,136
M 31
s_bitcmp1_b32 s50, 12
s_cselect_b32 s52, 24576, 0
v_add_u32 v50, s52, v3
M 32
ST 30,128
AR 7,160
M 33
AR 8,184
EF 6,12
M 34
EF 7,13
EF 8,46
M 35
WL 4
EF 140,47
CV 38,6,7
M 40
ST 32,896
CV 39,8,140
M 41
AR 6,137
AR 7,161
M 42
AR 8,185
EF 6,12
M 43
EF 7,13
EF 8,46
M 44
WV 21
ST 34,1664
EF 141,47
M 45
WL 0
CV 40,6,7
CV 41,8,141
M 46
s_barrier
R 24
M 47
R 26
M 52
TY 122,8200
TY 126,22024
M 53
ST 36,2432
AR 6,138
M 54
AR 7,162
AR 8,186
M 55
EF 6,12
EF 7,13
M 56
EF 8,46
EF 142,47
M 57
CV 42,6,7
CV 43,8,142
M 58
AR 6,139
AR 7,163
M 59
R 28
M 64
AR 8,187
EF 6,12
M 65
WL 3
AN 120,122
AN 121,123
M 66
SH 122
SH 123
M 67
AN 122,122
)"
R"(AN 123,123
M 68
WL 2
AN 124,126
AN 125,127
M 69
SH 126
SH 127
M 70
AN 126,126
AN 127,127
M 71
R 30
M 76
EF 7,13
EF 8,46
M 77
EF 143,47
CV 44,6,7
M 78
CV 45,8,143
M 79
ae 42,74,768
ds_read_b64 v[12:13], v4 offset:4608
ds_read_b64 v[46:47], v4 offset:4616
M 80
s_bfe_u32 s52, s50, 0x1000d
v_mad_i32_i24 v50, v49, s52, v48
ST 38,128
M 81
EF 160,52
EF 184,53
M 82
EF 208,54
EF 232,55
M 83
R 32
M 88
TX 58,7680
TX 62,21504
M 89
ST 40,896
CV 14,160,184
M 90
CV 15,208,232
EF 161,52
M 91
EF 185,53
EF 209,54
M 92
TY 66,8192
TY 70,22016
M 93
ST 42,1664
EF 233,55
M 94
CV 16,161,185
CV 17,209,233
M 95
WL 7
R 34
M 100
EF 162,52
EF 186,53
M 101
WL 4
PA_ 56,120,58
PA_ 57,121,59
M 102
PB_ 58,120,58
PB_ 59,121,59
M 103
PA_ 60,124,62
PA_ 61,125,63
M 104
PB_ 62,124,62
PB_ 63,125,63
M 105
WL 2
PA_ 64,122,66
PA_ 65,123,67
M 106
PB_ 66,122,66
PB_ 67,123,67
M 107
PA_ 68,126,70
PA_ 69,127,71
M 112
PB_ 70,126,70
PB_ 71,127,71
M 113
ST 44,2432
EF 210,54
M 114
EF 234,55
CV 18,162,186
M 115
CV 19,210,234
EF 163,52
M 116
WV 18
EF 187,53
EF 211,54
M 117
WL 0
EF 235,55
CV 20,163,187
M 118
s_barrier
R 48
M 119
R 50
M 124
CV 21,211,235
M 125
ae 42,74,768
EF 164,52
M 136
s_bitcmp1_b32 s50, 12
s_cselect_b32 s52, 24576, 0
v_add_u32 v50, s52, v3
M 137
ST 14,256
EF 188,53
M 126
EF 212,54
EF 236,55
M 127
CV 22,164,188
CV 23,212,236
M 138
EF 165,52
EF 189,53
M 139
ST 16,1024
EF 213,54
M 128
EF 237,55
CV 24,165,189
M 129
CV 25,213,237
EF 166,52
M 140
EF 190,53
EF 214,54
M 141
ST 18,1792
EF 238,55
M 130
CV 26,166,190
CV 27,214,238
M 131
R 52
M 142
EF 167,52
EF 191,53
M 143
WL 2
R 54
MA 52,0,56
ST 20,2560
EF 215,54
MA 52,4,60
EF 239,55
CV 28,167,191
MA 76,8,56
CV 29,215,239
MA 76,12,60
BV 52,4864
AR 6,44
MA 56,0,64
ae 42,74,768
AR 7,68
MA 56,4,68
s_bfe_u32 s52, s50, 0x1000d
R 56
MA 80,8,64
v_mad_i32_i24 v50, v49, s52, v48
ST 22,256
MA 80,12,68
WL 5
R 58
MA 100,16,56
AR 8,92
AR 9,116
MA 100,20,60
EF 6,12
EF 7,13
MA 104,16,64
ST 24,1024
EF 8,46
MA 104,20,68
WL 5
EF 9,47
CV 30,6,7
MA 124,24,56
WV 14
CV 31,8,9
AR 6,45
MA 124,28,60
WL 0
AR 7,69
AR 8,93
MA 128,24,64
s_barrier
R 72
MA 128,28,68
R 74
MA 148,0,56
ST 26,1792
AR 9,117
MA 148,4,60
EF 6,12
EF 7,13
MA 152,0,64
EF 8,46
EF 9,47
MA 152,4,68
R 76
MA 172,8,56
ST 28,2560
CV 32,6,7
MA 172,12,60
CV 33,8,9
AR 6,46
MA 176,8,64
AR 7,70
AR 8,94
MA 176,12,68
WL 4
R 78
MA 196,16,56
AR 9,118
EF 6,12
MA 196,20,60
EF 7,13
EF 8,46
MV 132,16,64
EF 9,47
CV 34,6,7
MV 132,20,68
WL 4
R 80
MV 152,24,56
CV 35,8,9
AR 6,47
MV 152,28,60
AR 7,71
AR 8,95
MV 156,24,64
AR 9,119
EF 6,12
MV 156,28,68
WL 4
R 82
MV 176,0,56
EF 7,13
EF 8,46
MV 176,4,60
EF 9,47
CV 36,6,7
MV 180,0,64
CV 37,8,9
MV 180,4,68
WL 4
AR 6,48
AR 7,72
AR 8,96
AR 9,120
MV 200,8,56
ae 42,72,768
EF 6,12
EF 7,13
EF 8,46
MV 200,12,60
s_bitcmp1_b32 s50, 14
s_cselect_b32 s52, 24576, 0
v_add_u32 v50, s52, v3
EF 9,47
CV 38,6,7
MV 204,8,64
ST 30,0
CV 39,8,9
AR 6,49
AR 7,73
MV 204,12,68
WL 0
AR 8,97
AR 9,121
EF 6,12
EF 7,13
MV 224,16,56
EF 8,46
EF 9,47
CV 40,6,7
CV 41,8,9
MV 224,20,60
AR 6,50
AR 7,74
AR 8,98
AR 9,122
MV 248,24,56
ST 32,768
EF 6,12
EF 7,13
EF 8,46
MV 248,28,60
EF 9,47
CV 42,6,7
CV 43,8,9
AR 6,51
MV 228,16,64
AR 7,75
AR 8,99
AR 9,123
EF 6,12
MV 228,20,68
EF 7,13
EF 8,46
EF 9,47
CV 44,6,7
MV 252,24,64
ST 34,1536
CV 45,8,9
MV 252,28,68
WV 13
ST 36,2304
ae 42,72,768
s_bfe_u32 s52, s50, 0x1000f
v_mad_i32_i24 v50, v49, s52, v48
ST 38,0
ST 40,768
ST 42,1536
ST 44,2304
ds_read_b64 v[12:13], v4 offset:5120
ds_read_b64 v[46:47], v4 offset:5128
AR 6,140
AR 7,164
AR 8,188
EF 6,52
EF 7,53
EF 8,54
EF 144,55
CV 14,6,7
CV 15,8,144
AR 6,141
AR 7,165
AR 8,189
EF 6,52
EF 7,53
EF 8,54
EF 145,55
CV 16,6,7
CV 17,8,145
AR 6,142
AR 7,166
AR 8,190
EF 6,52
EF 7,53
EF 8,54
EF 146,55
CV 18,6,7
CV 19,8,146
AR 6,143
AR 7,167
AR 8,191
EF 6,52
EF 7,53
EF 8,54
EF 147,55
CV 20,6,7
CV 21,8,147
ae 42,72,768
s_bitcmp1_b32 s50, 14
s_cselect_b32 s52, 24576, 0
v_add_u32 v50, s52, v3
ST 14,128
ST 16,896
ST 18,1664
ST 20,2432
AR 6,144
AR 7,168
AR 8,192
EF 6,52
EF 7,53
EF 8,54
EF 148,55
CV 22,6,7
CV 23,8,148
AR 6,145
AR 7,169
AR 8,193
EF 6,52
EF 7,53
EF 8,54
EF 149,55
CV 24,6,7
CV 25,8,149
AR 6,146
AR 7,170
)"
R"(AR 8,194
EF 6,52
EF 7,53
EF 8,54
EF 150,55
CV 26,6,7
CV 27,8,150
AR 6,147
AR 7,171
AR 8,195
EF 6,52
EF 7,53
EF 8,54
EF 151,55
CV 28,6,7
CV 29,8,151
ae 42,72,768
s_bfe_u32 s52, s50, 0x1000f
v_mad_i32_i24 v50, v49, s52, v48
ST 22,128
ST 24,896
ST 26,1664
ST 28,2432
BV 52,4608
WL 1
EF 168,12
EF 192,13
EF 216,46
EF 240,47
CV 30,168,192
CV 31,216,240
EF 169,12
EF 193,13
EF 217,46
EF 241,47
CV 32,169,193
CV 33,217,241
EF 170,12
EF 194,13
EF 218,46
EF 242,47
CV 34,170,194
CV 35,218,242
EF 171,12
EF 195,13
EF 219,46
EF 243,47
CV 36,171,195
CV 37,219,243
ae 42,72,768
s_bitcmp1_b32 s50, 14
s_cselect_b32 s52, 24576, 0
v_add_u32 v50, s52, v3
ST 30,256
ST 32,1024
ST 34,1792
ST 36,2560
EF 172,12
EF 196,13
EF 220,46
EF 244,47
CV 38,172,196
CV 39,220,244
EF 173,12
EF 197,13
EF 221,46
EF 245,47
CV 40,173,197
CV 41,221,245
EF 174,12
EF 198,13
EF 222,46
EF 246,47
CV 42,174,198
CV 43,222,246
EF 175,12
EF 199,13
EF 223,46
EF 247,47
CV 44,175,199
CV 45,223,247
ae 42,72,768
s_bfe_u32 s52, s50, 0x1000f
v_mad_i32_i24 v50, v49, s52, v48
ST 38,256
ST 40,1024
ST 42,1792
ST 44,2560
ds_read_b64 v[12:13], v4 offset:4864
ds_read_b64 v[46:47], v4 offset:4872
AR 6,52
AR 7,76
AR 8,100
AR 9,124
WL 2
EF 6,52
EF 7,53
EF 8,54
EF 9,55
CV 14,6,7
CV 15,8,9
AR 6,53
AR 7,77
AR 8,101
AR 9,125
EF 6,52
EF 7,53
EF 8,54
EF 9,55
CV 16,6,7
CV 17,8,9
AR 6,54
AR 7,78
AR 8,102
AR 9,126
EF 6,52
EF 7,53
EF 8,54
EF 9,55
CV 18,6,7
CV 19,8,9
AR 6,55
AR 7,79
AR 8,103
AR 9,127
EF 6,52
EF 7,53
EF 8,54
EF 9,55
CV 20,6,7
CV 21,8,9
ae 42,72,864
s_bitcmp1_b32 s50, 16
s_cselect_b32 s52, 24576, 0
v_add_u32 v50, s52, v3
ST 14,0
ST 16,768
ST 18,1536
ST 20,2304
AR 6,56
AR 7,80
AR 8,104
AR 9,128
EF 6,52
EF 7,53
EF 8,54
EF 9,55
CV 22,6,7
CV 23,8,9
AR 6,57
AR 7,81
AR 8,105
AR 9,129
EF 6,52
EF 7,53
EF 8,54
EF 9,55
CV 24,6,7
CV 25,8,9
AR 6,58
AR 7,82
AR 8,106
AR 9,130
EF 6,52
EF 7,53
EF 8,54
EF 9,55
CV 26,6,7
CV 27,8,9
AR 6,59
AR 7,83
AR 8,107
AR 9,131
EF 6,52
EF 7,53
EF 8,54
EF 9,55
CV 28,6,7
CV 29,8,9
ae 42,72,864
s_bfe_u32 s52, s50, 0x10011
v_mad_i32_i24 v50, v49, s52, v48
ST 22,0
ST 24,768
ST 26,1536
ST 28,2304
BV 52,5120
AR 6,148
AR 7,172
AR 8,196
WL 1
EF 6,12
EF 7,13
EF 8,46
EF 152,47
CV 30,6,7
CV 31,8,152
AR 6,149
AR 7,173
AR 8,197
EF 6,12
EF 7,13
EF 8,46
EF 153,47
CV 32,6,7
CV 33,8,153
AR 6,150
AR 7,174
AR 8,198
EF 6,12
EF 7,13
EF 8,46
EF 154,47
CV 34,6,7
CV 35,8,154
AR 6,151
AR 7,175
AR 8,199
EF 6,12
EF 7,13
EF 8,46
EF 155,47
CV 36,6,7
CV 37,8,155
ae 42,72,864
s_bitcmp1_b32 s50, 16
s_cselect_b32 s52, 24576, 0
v_add_u32 v50, s52, v3
ST 30,128
ST 32,896
ST 34,1664
ST 36,2432
AR 6,152
AR 7,176
EF 6,12
EF 7,13
EF 132,46
EF 156,47
CV 38,6,7
CV 39,132,156
AR 6,153
AR 7,177
EF 6,12
EF 7,13
EF 133,46
EF 157,47
CV 40,6,7
CV 41,133,157
AR 6,154
AR 7,178
EF 6,12
EF 7,13
EF 134,46
EF 158,47
CV 42,6,7
CV 43,134,158
AR 6,155
AR 7,179
EF 6,12
EF 7,13
EF 135,46
EF 159,47
CV 44,6,7
CV 45,135,159
ae 42,72,864
s_bfe_u32 s52, s50, 0x10011
v_mad_i32_i24 v50, v49, s52, v48
ST 38,128
ST 40,896
ST 42,1664
ST 44,2432
WL 0
EF 176,52
EF 200,53
EF 224,54
EF 248,55
CV 14,176,200
CV 15,224,248
EF 177,52
EF 201,53
EF 225,54
EF 249,55
CV 16,177,201
CV 17,225,249
EF 178,52
EF 202,53
EF 226,54
EF 250,55
CV 18,178,202
CV 19,226,250
EF 179,52
EF 203,53
EF 227,54
EF 251,55
CV 20,179,203
CV 21,227,251
ae 42,72,864
s_bitcmp1_b32 s50, 16
s_cselect_b32 s52, 24576, 0
v_add_u32 v50, s52, v3
ST 14,256
ST 16,1024
ST 18,1792
ST 20,2560
EF 180,52
EF 204,53
EF 228,54
EF 252,55
CV 22,180,204
CV 23,228,252
EF 181,52
EF 205,53
EF 229,54
EF 253,55
CV 24,181,205
CV 25,229,253
EF 182,52
EF 206,53
EF 230,54
EF 254,55
CV 26,182,206
CV 27,230,254
EF 183,52
EF 207,53
EF 231,54
EF 255,55
CV 28,183,207
CV 29,231,255
ae 42,72,864
s_bfe_u32 s52, s50, 0x10011
v_mad_i32_i24 v50, v49, s52, v48
ST 22,256
ST 24,1024
ST 26,1792
ST 28,2560
s_waitcnt vmcnt(0) lgkmcnt(0)
s_branch 29f
FY 132
FY 136
FY 140
FY 144
FY 148
FY 152
FY 156
FY 160
FY 164
FY 168
FY 172
FY 176
FY 180
FY 184
FY 188
FY 192
FY 196
FY 200
FY 204
FY 208
FY 212
FY 216
FY 220
FY 224
FY 228
FY 232
FY 236
FY 240
FY 244
FY 248
FY 252
FY 256
FY 260
FY 264
FY 268
29:
.purgem MA
.purgem MV
.purgem NA
.purgem NV
.purgem AN
.purgem SH
.purgem PA_
.purgem PB_
.purgem AW
.purgem AR
.purgem UL
.purgem UH
.purgem AD
.purgem EF
.purgem TY
.purgem TX
.purgem RA
.purgem RC
.purgem BV
.purgem BA
.purgem PA
.purgem DF
.purgem DW
.purgem DR
.purgem DH
.purgem DG
.purgem XL
)"
R"(.purgem ST
.purgem SA
.purgem CV
.purgem RS
.purgem PM
.purgem PF
.purgem FS
.purgem FL
.purgem FB
.purgem FC
.purgem FG
.purgem FX
.purgem FP
.purgem FR
.purgem FQ
.purgem FZ
.purgem FY
.purgem WL
.purgem WV
.purgem M
.purgem R1
.purgem R
.purgem P
.purgem Q
.purgem mw
.purgem mh
.purgem ad
.purgem ae
.purgem bc
.purgem W
.purgem H
)"
      : [flg] "=&s"(flg) : K2_OPS : K2_CLOB);
  if (flg) {
    const unsigned lane = __builtin_amdgcn_mbcnt_hi(~0u, __builtin_amdgcn_mbcnt_lo(~0u, 0u));
    const u16* const volatile __attribute__((address_space(4)))* ka = (const u16* const volatile __attribute__((address_space(4)))*)__builtin_amdgcn_kernarg_segment_ptr();
    {
      const u16* w1p = ka[6];
#pragma unroll 1
      for (unsigned f = blockIdx.x * 4 + w; f < 1152; f += 1024) {
        const u16* p = w1p + (32 * (f % 12) + 8 * (lane >> 4)) * HID + 16 * (f / 12) + (lane & 15);
        u32x4 v;
#pragma unroll
        for (int j = 0; j < 4; ++j) v[j] = p[2 * j * HID] | (unsigned)p[(2 * j + 1) * HID] << 16;
        *(u32x4*)(g_buf + f * 1024 + lane * 16) = v;
      }
    }
    slow_bar((unsigned*)(g_buf + G_BAR), w);
    slow_fc1(ka[0], ka[5], (char*)ka[1], (float*)smem + w * HID, w);
    slow_bar((unsigned*)(g_buf + G_BAR + 64), w);
    slow_fc2(ka[0], (const char*)ka[1], (u16*)ka[2], ka[3], ka[4], smem, w);
  }
  TS(15);
  MK_END(4);
}

extern "C" void kernel_launch(void* const* d_in, const int* in_sizes, int n_in,
                              void* d_out, int out_size, void* d_ws, size_t ws_size,
                              hipStream_t stream) {
  const u16* x = (const u16*)d_in[0];
  const u16* dw = (const u16*)d_in[1];
  const u16* w1 = (const u16*)d_in[2];
  const u16* w2 = (const u16*)d_in[3];
  const u16* grn_g = (const u16*)d_in[4];
  const u16* grn_b = (const u16*)d_in[5];
  char* ws = (char*)d_ws;
  if (ws_size < H_BYTES) return;
  (void)hipFuncSetAttribute((const void*)k1_fc1, hipFuncAttributeMaxDynamicSharedMemorySize, K1_LDS);
  (void)hipFuncSetAttribute((const void*)k2_fc2, hipFuncAttributeMaxDynamicSharedMemorySize, K2_LDS);

  k0_prep<<<K0_NB + NPAGE + NPAGX, 256, 0, stream>>>(w1, dw, ws, (const char*)x, grn_b, w2);
  k1_fc1<<<NWG + 8, 256, K1_LDS, stream>>>(x, ws, (const u16*)d_out, grn_g);
  k2_fc2<<<NWG, 256, K2_LDS, stream>>>(x, ws, (u16*)d_out, grn_g, w2, dw, w1);
}
```

```cpp
#include <hip/hip_runtime.h>
#include <hip/hip_bf16.h>
#include <stdint.h>
#include <stdio.h>

typedef short bf16x8 __attribute__((ext_vector_type(8)));
typedef short s16x4 __attribute__((ext_vector_type(4)));
typedef short v4i16_t __attribute__((ext_vector_type(4)));
typedef float f32x16 __attribute__((ext_vector_type(16)));
typedef float f32x4 __attribute__((ext_vector_type(4)));
typedef float f32x2 __attribute__((ext_vector_type(2)));
typedef unsigned u32x4 __attribute__((ext_vector_type(4)));
typedef __bf16 bf16x2_t __attribute__((ext_vector_type(2)));
typedef unsigned short u16;

constexpr float TZ = 175.f, TH = 175.f, TW2 = 175.f;
#define DI __device__ __forceinline__
#define LAS __attribute__((address_space(3)))

constexpr int NPIX = 147456;
constexpr int CH = 384;
constexpr int HID = 1536;
constexpr int NTILE = NPIX / 32;
constexpr int NWG = 256;
constexpr int TPW = NTILE / NWG;

constexpr size_t H_BYTES = (size_t)NPIX * HID * 2;
constexpr unsigned K1_LDS = 163840, K1_PF_OFF = 130048, K1_SMAX = 12, K1_NREP = 4, K1_G_TAB = 1228800, K1_G_CNT = 1239040, K1_G_LOC = 1236992, K1_G_DUM = 1255424,
  K1_G_ACC = 1320960, K1_G_LUT = 1382400, K1_G_BYTES = 1398784;
constexpr bool K1_LUT = true;
constexpr float K1_ZS = 16.0f;
constexpr unsigned K1_GST = 25600, K1_WCH = 6400, K1_ZC = 1365;
constexpr unsigned K1_SB1 = 32768;
constexpr bool K1_F2 = true;
constexpr unsigned K2_SHT = 12483;
constexpr int K2_LDS = 163840, K2_HB = 98304, K2_RSZ = 13824, K2_E_OFF = 153600, K2_PF_OFF = 159744, K2_S16_OFF = 152576, K2_S16_D0 = 8192, K2_NAT = 8;

__device__ __attribute__((aligned(256))) char g_buf[K1_G_BYTES];
__device__ __attribute__((aligned(256))) float g_cs[8 * 384];
__device__ __attribute__((aligned(256))) char g_w2r[96 * 12 * 1024];
__device__ __attribute__((aligned(256))) char g_w2f[96 * 12 * 1024];
__device__ __attribute__((aligned(256))) char g_w2q[16 * 12 * 1024];
__device__ __attribute__((aligned(256))) float g_sc[64];
constexpr unsigned G_FLAG = K1_G_CNT + 14336;
constexpr unsigned G_PUB = K1_G_DUM;
__device__ __attribute__((aligned(256))) unsigned g_prm[1024 * 64];
constexpr unsigned G_STMP = K1_G_CNT + 12288;
constexpr unsigned G_BAR = K1_G_CNT + 13312;
constexpr unsigned H_TS = 49152, H_RS = 48 * H_TS, NPAGE = 217, NPAGX = 55;
#define TS(i) do { } while (0)
#define KST(i) do { } while (0)
#define MK_START(i) do { } while (0)
#define MK_END(i) do { } while (0)

DI unsigned pack_bf16(float a, float b) {
  f32x2 v = {a, b};
  bf16x2_t r = __builtin_convertvector(v, bf16x2_t);
  return __builtin_bit_cast(unsigned, r);
}
typedef _Float16 f16x2_t __attribute__((ext_vector_type(2)));
DI unsigned pack_f16(float a, float b) {
  f32x2 v = {a, b};
  f16x2_t r = __builtin_convertvector(v, f16x2_t);
  return __builtin_bit_cast(unsigned, r);
}
DI float bf_lo(unsigned u) { return __uint_as_float(u << 16); }
DI float bf_hi(unsigned u) { return __uint_as_float(u & 0xffff0000u); }
DI float round_bf16(float a) { return bf_lo(pack_bf16(a, 0.f)); }
DI float dot2acc(unsigned a, unsigned b, float c) {
  return __builtin_amdgcn_fdot2_f32_bf16(__builtin_bit_cast(bf16x2_t, a), __builtin_bit_cast(bf16x2_t, b), c, false);
}
DI float ld_bf16(const u16* p) { return __uint_as_float((unsigned)(*p) << 16); }

DI s16x4 tr_read(const char* p) {
  return __builtin_bit_cast(s16x4, __builtin_amdgcn_ds_read_tr16_b64_v4i16((LAS v4i16_t*)p));
}

#define MFMA32(a, b, c) __builtin_amdgcn_mfma_f32_32x32x16_bf16((a), (b), (c), 0, 0, 0)
#define BAR() __builtin_amdgcn_s_barrier()

DI void dma16(unsigned voff, const char* sbase, unsigned lds_dst) {
  unsigned keep;
  asm volatile("s_mov_b32 %0, m0\n\ts_mov_b32 m0, %3\n\ts_nop 0\n\tglobal_load_lds_dwordx4 %1, %2\n\ts_mov_b32 m0, %0"
               : "=&s"(keep) : "v"(voff), "s"(sbase), "s"(lds_dst) : "memory");
}
DI unsigned lds_addr(const char* p) { return (unsigned)(size_t)(const LAS char*)p; }

DI float gelu_tanh(float x) {
  const float c0 = -2.0f * 0.7978845608028654f * 1.4426950408889634f;
  const float c1 = c0 * 0.044715f;
  float x2 = x * x;
  float t = __builtin_fmaf(x2, c1, c0);
  float e = __builtin_amdgcn_exp2f(x * t);
  float r = __builtin_amdgcn_rcpf(1.0f + e);
  return x * r;
}

DI void gload_b128_asm(u32x4& dst, const char* sbase, unsigned voff) {
  asm volatile("global_load_dwordx4 %0, %1, %2" : "=v"(dst) : "v"(voff), "s"(sbase) : "memory");
}
DI void lds_rd128(u32x4& dst, unsigned addr, int off) {
  asm volatile("ds_read_b128 %0, %1 offset:%2" : "=v"(dst) : "v"(addr), "i"(off));
}
DI void lds_rd128_far(u32x4& dst, unsigned voff, unsigned sbase, int imm) {
  unsigned t;
  asm volatile("v_add_u32 %1, %3, %2\n\tv_add_u32 %1, %4, %1\n\tds_read_b128 %0, %1" : "=v"(dst), "=&v"(t) : "v"(voff), "s"(sbase), "i"(imm));
}
DI void gload16i(u32x4& dst, const char* sbase, unsigned voff, int imm) {
  unsigned t;
  asm volatile("v_add_u32 %1, %4, %2\n\tglobal_load_dwordx4 %0, %1, %3" : "=v"(dst), "=&v"(t) : "v"(voff), "s"(sbase), "i"(imm) : "memory");
}
DI void dma16i(const char* sbase, unsigned voff, int immv, unsigned lbase, int imml) {
  unsigned t, keep;
  asm volatile("s_mov_b32 %1, m0\n\ts_add_u32 m0, %4, %6\n\tv_add_u32 %0, %5, %2\n\tglobal_load_lds_dwordx4 %0, %3\n\ts_mov_b32 m0, %1"
               : "=&v"(t), "=&s"(keep) : "v"(voff), "s"(sbase), "s"(lbase), "i"(immv), "i"(imml) : "memory", "scc");
}

DI void st16_wt(void* p, u32x4 v) { asm volatile("global_store_dwordx4 %0, %1, off\n\ts_nop 1" :: "v"(p), "v"(v) : "memory"); }
template <int I> struct IC { static constexpr int value = I; };
template <int N, int I = 0, typename F> DI void static_for(F&& f) {
  if constexpr (I < N) { f(IC<I>{}); static_for<N, I + 1>(f); }
}
template <int C, int M> DI float dpp_add(float v) { return v + __int_as_float(__builtin_amdgcn_update_dpp(0, __float_as_int(v), C, M, 0xf, false)); }
DI float wave_sum63(float v) {
  v = dpp_add<0x111, 0xf>(v); v = dpp_add<0x112, 0xf>(v); v = dpp_add<0x114, 0xf>(v); v = dpp_add<0x118, 0xf>(v);
  v = dpp_add<0x142, 0xa>(v); return dpp_add<0x143, 0xc>(v);
}
DI unsigned cs_index(unsigned n) { return (n & ~63u) + (n & 3) * 16 + ((n >> 2) & 15); }
DI uint64_t uni64(uint64_t v) { return (uint64_t)__builtin_amdgcn_readfirstlane(v >> 32) << 32 | (unsigned)__builtin_amdgcn_readfirstlane(v); }
DI unsigned wg_side(unsigned b) { return (((const unsigned*)(g_buf + K1_G_LOC))[b & 3] ^ (b >> 2)) & 1; }
DI unsigned h_flip(const char* ws, unsigned region, unsigned t) { return __popc(((const unsigned*)(g_buf + K1_G_LOC + 64))[(((unsigned)(size_t)ws & 0x1fffffu) + region * H_RS + t * H_TS) >> 21]) > 2; }
DI unsigned x_flip(const u16* x, unsigned region) { return __popc(((const unsigned*)(g_buf + K1_G_LOC + 1024))[(((unsigned)(size_t)x & 0x1fffffu) + (12 * region + 6) * 49152u) >> 21]) > 2; }
DI unsigned sub_pixel(const u16* x, unsigned sub, unsigned m) {
  const unsigned blk = sub / 24, u = sub % 24;
  const unsigned region = blk < 256 ? (blk >> 3) * 4 + (blk & 3) : 128 + ((blk - 256) >> 1), side = blk < 256 ? wg_side(blk) : blk & 1;
  return (12 * region + (u >> 1)) * 64 + ((0xda43cb52fe987610ull >> (32 * (u & 1) + 16 * (side ^ x_flip(x, region)) + (m & 12))) & 15) * 4 + (m & 3);
}
DI unsigned h_piece(const char* ws, unsigned sub, unsigned t) {
  const unsigned blk = sub / 24, u = sub % 24;
  const unsigned region = blk < 256 ? (blk >> 3) * 4 + (blk & 3) : 128 + ((blk - 256) >> 1), side = blk < 256 ? wg_side(blk) : blk & 1;
  return region * H_RS + t * H_TS + (u >> 3) * 16384 + (u & 7) * 1024 + ((side ^ h_flip(ws, region, t)) << 13);
}
constexpr unsigned K0_NB = 145 + 24 + 96 + 16 + 48;
constexpr float U24 = 0x1p-24f, EPS = 1e-5f;
DI float wave_all(float v) { return __int_as_float(__builtin_amdgcn_readlane(__float_as_int(wave_sum63(v)), 63)); }
DI float pow2f(float v) { return __uint_as_float(__float_as_uint(v) & 0x7f800000u); }
DI u32x4 w_load(const u16* w) { return *(const u32x4*)(w + ((threadIdx.x >> 3) & 7) * 73408 + 36800 + (threadIdx.x & 7) * 8); }
DI float w_ms(u32x4 a) {
  float q = 0.f;
#pragma unroll
  for (int i = 0; i < 4; ++i) { const float lo = bf_lo(a[i]), hi = bf_hi(a[i]); q = __builtin_fmaf(lo, lo, __builtin_fmaf(hi, hi, q)); }
  return wave_all(q) * (1.0f / 512);
}
DI float w1_scale(float ms) { return pow2f(300.f * U24 * __builtin_amdgcn_rsqf(ms)); }
DI float w2_scale(float ms) { return TW2 * U24 * __builtin_amdgcn_rsqf(ms); }
struct Sc { float w1, zs, g, h, vlo, vhi, c; };
DI Sc scales(const u16* w1, const u16* x, const u16* dw) {
  const u32x4 a = w_load(w1);
  const unsigned l = threadIdx.x & 63;
  float s = 0.f, q = 0.f;
#pragma unroll
  for (int k = 0; k < 6; ++k) {
    const u32x4 v = *(const u32x4*)(x + (9001 + 18397 * (l >> 3)) * CH + 64 * k + 8 * (l & 7)), d = *(const u32x4*)(dw + 64 * k + 8 * (l & 7));
#pragma unroll
    for (int i = 0; i < 4; ++i) { const float lo = bf_lo(v[i]) * bf_lo(d[i]), hi = bf_hi(v[i]) * bf_hi(d[i]); s += lo + hi; q = __builtin_fmaf(lo, lo, __builtin_fmaf(hi, hi, q)); }
  }
  s = dpp_add<0x141, 0xf>(dpp_add<0x4e, 0xf>(dpp_add<0xb1, 0xf>(s))); q = dpp_add<0x141, 0xf>(dpp_add<0x4e, 0xf>(dpp_add<0xb1, 0xf>(q)));
  const float var = fmaxf(q * (1.0f / CH) - s * s * (1.0f / (CH * CH)), 0.f);
  const float r2 = wave_all(var * __builtin_amdgcn_rcpf(var + EPS)) * (1.0f / 64), rden = __builtin_amdgcn_exp2f(wave_all(__builtin_amdgcn_logf(var + EPS)) * (-1.0f / 128));
  const float s1 = w_ms(a), p2 = r2 * CH * s1;
  Sc c;
  c.w1 = w1_scale(s1);
  c.g = TZ * U24 * __builtin_amdgcn_rsqf(r2);
  c.h = TH * U24 * __builtin_amdgcn_rsqf(p2 * (0.25f + 0.5f * p2 * __builtin_amdgcn_rcpf(1.0f + 2.0f * p2)));
  c.c = fminf(__builtin_ceilf(0.1701f * 0x1p24f * c.h) + 1.0f, 1400.f);
  c.zs = fminf(pow2f(c.g * 2530.f), pow2f(c.g * 0x1p12f * rden));
  const float e = EPS * c.zs * c.zs, z2 = r2 * (660.f * 660.f / (TZ * TZ));
  c.vlo = 4.2f * c.g * c.g - e;
  c.vhi = fminf(0x1.ep29f * c.g * c.g - e, z2 < 1.0f ? e * z2 * __builtin_amdgcn_rcpf(1.0f - z2) : 0x1p120f);
  return c;
}
__global__ __launch_bounds__(256) void k0_prep(const u16* __restrict__ w1, const u16* __restrict__ dw, const char* __restrict__ ws, const char* __restrict__ x,
                                                const u16* __restrict__ grn_b, const u16* __restrict__ w2) {
  MK_START(0);
  uint64_t pc; int d1;
  asm volatile("s_getpc_b64 %0\n1:\n\ts_mov_b32 %1, k1_code-1b" : "=s"(pc), "=s"(d1));
  if (threadIdx.x >= 192) pc += (int64_t)d1 + ((int)(blockIdx.x >> 3) - 3) * 8192;
  unsigned pfd = 0;
  if (blockIdx.x < 64 && (threadIdx.x < 150 || (blockIdx.x < 48 && threadIdx.x >= 192))) pfd = *(const __attribute__((address_space(1))) unsigned*)((pc & ~127ull) + threadIdx.x * 128);
#define K0_END() do { asm volatile("" :: "v"(pfd)); MK_END(0); } while (0)
  unsigned xcc;
  asm volatile("s_getreg_b32 %0, hwreg(HW_REG_XCC_ID)" : "=s"(xcc));
  xcc = (xcc >> 2) & 1;
  if (blockIdx.x < 4 && threadIdx.x == 0) ((unsigned*)(g_buf + K1_G_LOC))[blockIdx.x] = 7 * xcc;
  constexpr unsigned NP = NPAGE + NPAGX;
  __shared__ Sc ssc;
  __shared__ float ss2;
  const bool sw = threadIdx.x >= 192;
  const unsigned bi = blockIdx.x;
  const unsigned bx = bi < NP ? bi + K0_NB : bi - NP;
  if (bx >= K0_NB) {
    __shared__ unsigned lat[8];
    const unsigned pg = bx - K0_NB, w = threadIdx.x >> 6, lane = threadIdx.x & 63;
    auto gran = [&](unsigned p) {
      const bool h = p < NPAGE;
      const char* buf = h ? ws : x;
      const int o0 = (int)((h ? p : p - NPAGE) << 21) - (int)((unsigned)(size_t)buf & 0x1fffffu);
      const unsigned al = h ? 49152 : 12 * 49152;
      const unsigned q = ((((unsigned)max(o0, 0) + al - 1) >> (h ? 14 : 16)) * (h ? 87382u : 29128u)) >> 18;
      return buf + min(q * al, (unsigned)(h ? H_BYTES : H_BYTES / 4) - 16384u);
    };
    if (lane < 5) {
      const char* a = gran(lane < 4 ? min(pg + NP - 1 - (lane >> 1), pg - 1 - (lane >> 1)) : pg) + (lane & 1) * 8192 + w * 2048 + (lane < 4 ? 1024 : 0);
      const char* b = a;
      const char* own = (const char*)uni64(__builtin_amdgcn_readlane((uint64_t)a >> 32, 4) * 4294967296ull + (unsigned)__builtin_amdgcn_readlane((unsigned)(uint64_t)a, 4));
      unsigned long long t0, t1, t2;
      int n = 2;
      asm volatile("" : "+s"(n));
#pragma unroll 1
      do {
        unsigned v;
        asm volatile("s_cmp_eq_u32 %6, 2\n\ts_cbranch_scc1 1f\n\ts_sleep 4\n1:\n\ts_memtime %0\n\ts_waitcnt lgkmcnt(0)\n\tglobal_load_dword %3, %4, off sc1\n\ts_waitcnt vmcnt(0)\n\ts_memtime %1\n\ts_waitcnt lgkmcnt(0)\n\t"
                     "global_load_dword %3, %5, off sc1\n\ts_waitcnt vmcnt(0)\n\ts_memtime %2\n\ts_waitcnt lgkmcnt(0)" : "=&s"(t0), "=&s"(t1), "=&s"(t2), "=&v"(v) : "v"(a), "v"(b), "s"(n) : "memory", "scc");
        a = own + 1024 + (w & 1) * 8192; b = a + 8192 - (w & 1) * 16384;
      } while (--n);
      lat[w + 4 * (w & 1)] = (unsigned)(t1 - t0); lat[w + 4 - 4 * (w & 1)] = (unsigned)(t2 - t1);
    }
    __syncthreads();
    if (threadIdx.x == 0) {
      auto fastest = [&](const unsigned* l) { unsigned m = 9999; for (int i = 0; i < 4; ++i) m = l[i] < 300 ? m : min(m, l[i]); return (int)m; };
      const int df = fastest(lat) - fastest(lat + 4);
      const unsigned v = xcc ^ (df > 0), sure = abs(df) > 80;
      char* e = g_buf + K1_G_LOC + 64 + 4 * (pg < NPAGE ? pg : pg + 240 - NPAGE);
      *(u16*)(e + 2) = v ? 3 + 4 * sure : 1 - sure;
      if (pg == 0 || pg == NPAGE) e[0] = v; else e[-3] = v ^ 1;
      if (pg == NPAGE - 1 || pg == NP - 1) e[1] = v; else e[4] = v ^ 1;
    }
    K0_END();
    return;
  }
  if (bx >= 145 + 24 + 96 + 16) {
    __shared__ __attribute__((aligned(16))) float red[2][16][32];
    const int t = bx - 281, c8 = (threadIdx.x & 3) * 8;
    u32x4 sm, v[6];
    if (sw) sm = w_load(w1);
#pragma unroll
    for (int k = 0; k < 6; ++k) v[k] = *(const u32x4*)(w1 + (size_t)(64 * k + (threadIdx.x >> 2)) * HID + 32 * t + c8);
    if (sw) ss2 = w1_scale(w_ms(sm));
    __syncthreads();
    const float s1 = ss2;
    float a[8], q[8];
#pragma unroll
    for (int i = 0; i < 8; ++i) {
      a[i] = q[i] = 0.f;
#pragma unroll
      for (int k = 0; k < 6; ++k) {
        const float f = (float)(_Float16)(s1 * (i & 1 ? bf_hi(v[k][i >> 1]) : bf_lo(v[k][i >> 1]))) * 0x1p24f;
        a[i] += f; q[i] = __builtin_fmaf(f, f, q[i]);
      }
      a[i] = dpp_add<0x118, 0xf>(dpp_add<0x114, 0xf>(a[i])); q[i] = dpp_add<0x118, 0xf>(dpp_add<0x114, 0xf>(q[i]));
    }
    if ((threadIdx.x & 12) == 12) {
#pragma unroll
      for (int i = 0; i < 2; ++i) {
        *(f32x4*)&red[0][threadIdx.x >> 4][c8 + 4 * i] = (f32x4){a[4 * i], a[4 * i + 1], a[4 * i + 2], a[4 * i + 3]};
        *(f32x4*)&red[1][threadIdx.x >> 4][c8 + 4 * i] = (f32x4){q[4 * i], q[4 * i + 1], q[4 * i + 2], q[4 * i + 3]};
      }
    }
    __syncthreads();
    if (threadIdx.x < 64) {
      const int n = threadIdx.x & 31;
      float s = 0.f;
#pragma unroll
      for (int i = 0; i < 16; ++i) s += red[threadIdx.x >> 5][i][n];
      const float c = (0.f - (float)K1_ZC * s) * 0x1p-48f;
      char* p = g_buf + t * K1_GST + 5120;
      if (threadIdx.x < 32) *(f32x4*)(p + 272 * (n >> 4) + (n & 15) * 16) = (f32x4){c, c, c, c};
      const bool any = __ballot(threadIdx.x >= 32 && !(s >= 384.f * 64 * 64 && s <= 384.f * 1024 * 1024)) != 0;
      if (threadIdx.x == 0) g_buf[G_FLAG + 4 * 98 + t] = any;
    }
    K0_END();
    return;
  }
  if (bx >= 145 + 24 + 96) {
    if (sw) ssc = scales(w1, (const u16*)x, dw);
    __syncthreads();
    const Sc sc = ssc;
    const unsigned e = (bx - 265) * 256 + threadIdx.x;
    const float u = 1.0f / (sc.g * sc.w1);
    float r[2];
#pragma unroll
    for (int i = 0; i < 2; ++i) {
      const unsigned c = 2 * e + i;
      r[i] = __builtin_fmaf(sc.h, gelu_tanh(u * __uint_as_float((c >> 12) << 31 | ((c & 4095) + 79 * 128) << 16 | 0x8000u)), sc.c * U24);
      if (!(r[i] >= 0.f && r[i] < 8189.f * U24)) r[i] = __builtin_inff();
    }
    *(unsigned*)(g_buf + K1_G_LUT + 4 * e) = pack_f16(r[0], r[1]);
    K0_END();
    return;
  }
  if (bx >= 145 + 24) {
    __shared__ __attribute__((aligned(16))) u16 tw[32][200];
    const int kappa = (bx - 169) >> 1, nh = (bx - 169) & 1;
    u32x4 sm, v[3];
    if (sw) sm = w_load(w2);
#pragma unroll
    for (int i = 0; i < 3; ++i) { const int e = threadIdx.x + 256 * i; v[i] = *(const u32x4*)(w2 + (size_t)(32 * kappa + e / 24) * CH + 192 * nh + (e % 24) * 8); }
    if (sw) ss2 = w2_scale(w_ms(sm));
#pragma unroll
    for (int i = 0; i < 3; ++i) { const int e = threadIdx.x + 256 * i; *(u32x4*)&tw[e / 24][(e % 24) * 8] = v[i]; }
    __syncthreads();
    const int lane = threadIdx.x & 63, g = lane >> 4;
    const float s2 = 256.f * ss2;
    bool bad = false;
#pragma unroll
    for (int i = 0; i < 3; ++i) {
      const int fi = (threadIdx.x >> 6) + 4 * i, n = 64 * (fi >> 2) + 4 * (lane & 15) + (fi & 3);
      float wv[8];
      unsigned bits = 0;
#pragma unroll
      for (int j = 0; j < 8; ++j) {
        wv[j] = ld_bf16(&tw[16 * (j >> 2) + 4 * g + (j & 3)][n]);
        bits |= wv[j] != 0.f ? 256u << j : 0u;
        wv[j] *= s2;
        bits |= fabsf(wv[j]) >= 48 * 256 * U24 ? 1u << j : 0u;
        bad |= !(fabsf(wv[j]) <= 500.f);
      }
      const unsigned o = (((kappa & ~1) + fi / 6) * 24 + nh * 12 + (fi % 6) * 2 + (kappa & 1)) * 1024 + lane * 16;
      st16_wt(g_w2r + o, (u32x4){pack_f16(wv[0], wv[1]), pack_f16(wv[2], wv[3]), pack_f16(wv[4], wv[5]), pack_f16(wv[6], wv[7])});
      constexpr float q = 1.0f / 256;
      if (kappa < 8) st16_wt(g_w2q + o, (u32x4){pack_f16(q * wv[0], q * wv[1]), pack_f16(q * wv[2], q * wv[3]), pack_f16(q * wv[4], q * wv[5]), pack_f16(q * wv[6], q * wv[7])});
      static_for<4>([&](auto ic) { bits |= __builtin_amdgcn_update_dpp(0u, bits, 0x120 + (1 << decltype(ic)::value), 0xf, 0xf, false); });
      bad |= ((bits >> 8) & ~bits & 255u) != 0;
    }
    const bool any = __ballot(bad) != 0;
    if (lane == 0) ((char*)(g_buf + G_FLAG))[4 * (bx - 169) + (threadIdx.x >> 6)] = any;
    K0_END();
    return;
  }
  if (bx >= 145) {
    __shared__ __attribute__((aligned(16))) float red[16][64];
    const int c0 = 64 * ((bx - 145) >> 2), rg = (bx - 145) & 3, c8 = (threadIdx.x & 7) * 8;
    float a[8];
#pragma unroll
    for (int i = 0; i < 8; ++i) a[i] = 0.f;
#pragma unroll
    for (int k = 0; k < 12; ++k) {
      const int ch = 384 * rg + 32 * k + (threadIdx.x >> 3);
      const float b = ld_bf16(grn_b + ch);
      const u32x4 v = *(const u32x4*)(w2 + (size_t)ch * CH + c0 + c8);
#pragma unroll
      for (int i = 0; i < 4; ++i) { a[2 * i] = __builtin_fmaf(b, bf_lo(v[i]), a[2 * i]); a[2 * i + 1] = __builtin_fmaf(b, bf_hi(v[i]), a[2 * i + 1]); }
    }
#pragma unroll
    for (int i = 0; i < 8; ++i) a[i] = dpp_add<0x118, 0xf>(a[i]);
    if (threadIdx.x & 8) {
      *(f32x4*)&red[threadIdx.x >> 4][c8] = (f32x4){a[0], a[1], a[2], a[3]};
      *(f32x4*)&red[threadIdx.x >> 4][c8 + 4] = (f32x4){a[4], a[5], a[6], a[7]};
    }
    __syncthreads();
    if (threadIdx.x < 64) {
      float s = 0.f;
#pragma unroll
      for (int i = 0; i < 16; ++i) s += red[i][threadIdx.x];
      g_cs[(4 + rg) * 384 + cs_index(c0 + threadIdx.x)] = s;
      g_cs[64 * (bx - 145) + threadIdx.x] = 0.f;
    }
    K0_END();
    return;
  }
  const unsigned bid = bx;
  if (bid == 0) {
    if (sw) ssc = scales(w1, (const u16*)x, dw);
    if ((threadIdx.x >> 6) == 2) ss2 = w2_scale(w_ms(w_load(w2)));
#pragma unroll
    for (int i = 0; i < 4; ++i) if (threadIdx.x + 256 * i < 896) *(u32x4*)(g_buf + K1_G_CNT + (threadIdx.x + 256 * i) * 16) = (u32x4){0u, 0u, 0u, 0u};
    *(unsigned*)(g_buf + G_PUB + threadIdx.x * 256) = 0u;
    const int e = threadIdx.x, e2 = e < 192 ? 32 * (e >> 4) + 8 * ((e & 15) >> 2) : 8 * min(e - 192, 47);
    const u32x4 d = *(const u32x4*)(dw + e2);
    __syncthreads();
    const Sc sc = ssc;
    const float s2 = ss2;
    u32x4 v;
#pragma unroll
    for (int k = 0; k < 4; ++k) v[k] = pack_bf16(sc.zs * bf_lo(d[k]), sc.zs * bf_hi(d[k]));
    if (e < 192) {
#pragma unroll
      for (int k = 0; k < 4; ++k) v[k] &= (k & 1) != ((e >> 1) & 1) ? 0u : e & 1 ? 0xffff0000u : 0xffffu;
    }
    if (e >= 240) v = (u32x4){__float_as_uint(EPS * sc.zs * sc.zs), __float_as_uint(sc.vhi), __float_as_uint(sc.g), __float_as_uint(sc.vlo)};
    *(u32x4*)(g_buf + K1_G_TAB + (e < 192 ? e : e + 192) * 16) = v;
    auto sane = [](float v) { return v >= 0x1p-45f && v <= 0x1p20f; };
    if (threadIdx.x < 2) ((unsigned*)(g_buf + G_FLAG))[96 + threadIdx.x] = !(sane(sc.w1) && sane(sc.zs) && sane(sc.g) && sane(sc.h) && sane(s2));
    if (threadIdx.x == 0) {
      const float ik = 1.0f / (sc.h * s2);
      *(f32x4*)g_sc = (f32x4){sc.h * s2, ik, 0x1p-28f / (sc.h * sc.h), -sc.c * U24 * ik};
      g_sc[4] = __uint_as_float(pack_f16(-sc.c * U24, -sc.c * U24));
      *(unsigned*)(g_buf + K1_G_CNT + 16320) = pack_f16(-sc.c * 0x1p-10f, -sc.c * 0x1p-10f);
    }
    if (threadIdx.x < 48) *(u32x4*)(g_buf + threadIdx.x * K1_GST + 5376) = (u32x4){pack_f16(sc.c * U24, 0.f), 0u, 0u, 0u};
    K0_END();
    return;
  }
  __shared__ __attribute__((aligned(16))) u16 tl[32][136];
  const int e = (bid - 1) * 256 + threadIdx.x, ks = (bid - 1) % 12, cb = (bid - 1) / 12;
  u32x4 sm, v[2];
  if (sw) sm = w_load(w1);
#pragma unroll
  for (int i = 0; i < 2; ++i) v[i] = *(const u32x4*)(w1 + (size_t)(32 * ks + (threadIdx.x >> 4) + 16 * i) * HID + 128 * cb + (threadIdx.x & 15) * 8);
  if (sw) ss2 = w1_scale(w_ms(sm));
#pragma unroll
  for (int i = 0; i < 2; ++i) *(u32x4*)&tl[(threadIdx.x >> 4) + 16 * i][(threadIdx.x & 15) * 8] = v[i];
  if (e < (int)(K1_NREP + 1) * 768) *(u32x4*)(g_buf + K1_G_ACC + e * 16) = (u32x4){0u, 0u, 0u, 0u};
  __syncthreads();
  const float s1 = ss2;
#pragma unroll
  for (int i = 0; i < 2; ++i) {
    const int lane = threadIdx.x & 63, f = (threadIdx.x >> 6) + 4 * i, n = 16 * f + (lane & 15), k0 = 8 * (lane >> 4);
    u32x4 P;
#pragma unroll
    for (int jj = 0; jj < 4; ++jj) P[jj] = pack_f16(s1 * ld_bf16(&tl[k0 + 2 * jj][n]), s1 * ld_bf16(&tl[k0 + 2 * jj + 1][n]));
    const int fs = (f & 1) * 12 + ks;
    st16_wt(g_buf + (4 * cb + (f >> 1)) * K1_GST + (fs + (fs >= 5)) * 1024 + lane * 16, P);
  }
  K0_END();
}

constexpr unsigned G_ACC2 = K1_G_ACC + K1_NREP * 12288;

#define VA_CLOB \
  "v0", "v1", "v2", "v3", "v4", "v5", "v6", "v7", "v8", "v9", "v10", "v11", "v12", "v13", "v14", "v15", "v16", "v17", "v18", "v19", "v20", "v21", "v22", "v23", "v24", "v25", "v26", "v27", "v28", "v29", "v30", "v31", \
  "v32", "v33", "v34", "v35", "v36", "v37", "v38", "v39", "v40", "v41", "v42", "v43", "v44", "v45", "v46", "v47", "v48", "v49", "v50", "v51", "v52", "v53", "v54", "v55", "v56", "v57", "v58", "v59", "v60", "v61", "v62", "v63", \
  "v64", "v65", "v66", "v67", "v68", "v69", "v70", "v71", "v72", "v73", "v74", "v75", "v76", "v77", "v78", "v79", "v80", "v81", "v82", "v83", "v84", "v85", "v86", "v87", "v88", "v89", "v90", "v91", "v92", "v93", "v94", "v95", \
  "v96", "v97", "v98", "v99", "v100", "v101", "v102", "v103", "v104", "v105", "v106", "v107", "v108", "v109", "v110", "v111", "v112", "v113", "v114", "v115", "v116", "v117", "v118", "v119", "v120", "v121", "v122", "v123", "v124", "v125", "v126", "v127", \
  "v128", "v129", "v130", "v131", "v132", "v133", "v134", "v135", "v136", "v137", "v138", "v139", "v140", "v141", "v142", "v143", "v144", "v145", "v146", "v147", "v148", "v149", "v150", "v151", "v152", "v153", "v154", "v155", "v156", "v157", "v158", "v159", \
  "v160", "v161", "v162", "v163", "v164", "v165", "v166", "v167", "v168", "v169", "v170", "v171", "v172", "v173", "v174", "v175", "v176", "v177", "v178", "v179", "v180", "v181", "v182", "v183", "v184", "v185", "v186", "v187", "v188", "v189", "v190", "v191", \
  "v192", "v193", "v194", "v195", "v196", "v197", "v198", "v199", "v200", "v201", "v202", "v203", "v204", "v205", "v206", "v207", "v208", "v209", "v210", "v211", "v212", "v213", "v214", "v215", "v216", "v217", "v218", "v219", "v220", "v221", "v222", "v223", \
  "v224", "v225", "v226", "v227", "v228", "v229", "v230", "v231", "v232", "v233", "v234", "v235", "v236", "v237", "v238", "v239", "v240", "v241", "v242", "v243", "v244", "v245", "v246", "v247", "v248", "v249", "v250", "v251", "v252", "v253", "v254", "v255", \
  "a0", "a1", "a2", "a3", "a4", "a5", "a6", "a7", "a8", "a9", "a10", "a11", "a12", "a13", "a14", "a15", "a16", "a17", "a18", "a19", "a20", "a21", "a22", "a23", "a24", "a25", "a26", "a27", "a28", "a29", "a30", "a31", \
  "a32", "a33", "a34", "a35", "a36", "a37", "a38", "a39", "a40", "a41", "a42", "a43", "a44", "a45", "a46", "a47", "a48", "a49", "a50", "a51", "a52", "a53", "a54", "a55", "a56", "a57", "a58", "a59", "a60", "a61", "a62", "a63", \
  "a64", "a65", "a66", "a67", "a68", "a69", "a70", "a71", "a72", "a73", "a74", "a75", "a76", "a77", "a78", "a79", "a80", "a81", "a82", "a83", "a84", "a85", "a86", "a87", "a88", "a89", "a90", "a91", "a92", "a93", "a94", "a95", \
  "a96", "a97", "a98", "a99", "a100", "a101", "a102", "a103", "a104", "a105", "a106", "a107", "a108", "a109", "a110", "a111", "a112", "a113", "a114", "a115", "a116", "a117", "a118", "a119", "a120", "a121", "a122", "a123", "a124", "a125", "a126", "a127", \
  "a128", "a129", "a130", "a131", "a132", "a133", "a134", "a135", "a136", "a137", "a138", "a139", "a140", "a141", "a142", "a143", "a144", "a145", "a146", "a147", "a148", "a149", "a150", "a151", "a152", "a153", "a154", "a155", "a156", "a157", "a158", "a159", \
  "a160", "a161", "a162", "a163", "a164", "a165", "a166", "a167", "a168", "a169", "a170", "a171", "a172", "a173", "a174", "a175", "a176", "a177", "a178", "a179", "a180", "a181", "a182", "a183", "a184", "a185", "a186", "a187", "a188", "a189", "a190", "a191", \
  "a192", "a193", "a194", "a195", "a196", "a197", "a198", "a199", "a200", "a201", "a202", "a203", "a204", "a205", "a206", "a207", "a208", "a209", "a210", "a211", "a212", "a213", "a214", "a215", "a216", "a217", "a218", "a219", "a220", "a221", "a222", "a223", \
  "a224", "a225", "a226", "a227", "a228", "a229", "a230", "a231", "a232", "a233", "a234", "a235", "a236", "a237", "a238", "a239", "a240", "a241", "a242", "a243", "a244", "a245", "a246", "a247", "a248", "a249", "a250", "a251", "a252", "a253", "a254", "a255"
#define K1_CLOB "memory", "vcc", "scc", "s36", "s37", "s38", "s39", "s40", "s41", "s42", "s43", "s44", "s45", "s46", "s47", "s48", "s49", "s50", "s51", "s52", "s53", "s54", "s55", "s56", "s57", "s58", "s59", "s60", "s61", "s62", "s63", "s64", "s65", "s66", "s67", "s68", "s69", "s70", "s71", "s72", "s73", "s74", "s75", "s76", "s77", "s78", "s79", "s80", "s81", "s82", "s83", "s84", "s85", "s86", "s87", "s88", "s89", "s90", "s91", "s92", "s93", "s94", "s95", "s96", "s97", VA_CLOB
DI void k2_params(const u16* x, const char* hws, const u16* out, const u16* grn_g, unsigned tid) {
  const unsigned e = (tid >> 2) * 32 + blockIdx.x * 4 + (tid & 3);
  const unsigned b = e >> 2, w = e & 3, pg = w & 1, nh = w >> 1, q = (b >> 3) * 4 + (b & 3), rs = 128 + (q >> 1), hf = q & 1, side = wg_side(b);
  auto fmask = [&](unsigned region) {
    const unsigned o = ((unsigned)(size_t)hws & 0x1fffffu) + region * H_RS + nh * H_TS;
    const unsigned* p = (const unsigned*)(g_buf + K1_G_LOC + 64) + (o >> 21);
    const unsigned r = 0x200000u + 2 * H_TS - 1 - (o & 0x1fffffu), d1 = ((r >> 15) * 171) >> 9, d2 = (((r + 0x200000u) >> 15) * 171) >> 9;
    const unsigned a1 = ~0u << d1, a2 = d2 < 32 ? ~0u << d2 : 0u;
    return ((__popc(p[0]) > 2 ? ~a1 : 0u) | (__popc(p[1]) > 2 ? a1 & ~a2 : 0u) | (__popc(p[2]) > 2 ? a2 : 0u)) ^ (0u - side);
  };
  const unsigned f0 = fmask(q), f1 = fmask(rs);
  const unsigned fx = ((side ^ x_flip(x, q)) ? ~K2_SHT : 0u) | ((side ^ x_flip(x, rs)) ? K2_SHT : 0u);
  const uint64_t h0 = (uint64_t)hws + q * H_RS + nh * (H_TS + 2048) + pg * 3072, h1 = (uint64_t)hws + rs * H_RS + nh * (H_TS + 2048) + hf * 3072 + pg * 1536;
  const unsigned p0 = (12 * q + 2 * pg) * 49152 + nh * 384, p1 = (12 * rs + 2 * hf + pg) * 49152 + nh * 384;
  const unsigned e1 = 1024 + (e >> 3);
  auto kap = [](unsigned f) { return f / 48 * 2 + (f & 1); };
  auto sco = [&](unsigned f) { return kap(f) * 64 + (kap(f) < 16 ? K2_S16_D0 : 0); };
  auto cso = [&](unsigned f) { const unsigned r = f % 48; return ((f < 192 ? min(f / 48, 2u) : 3) * 384 + (r / 12 % 2 * 12 + r / 24 * 6 + r % 12 / 2) * 16) * 4; };
  typedef uint64_t u64x2 __attribute__((ext_vector_type(2)));
  u64x2* P = (u64x2*)(g_prm + 64 * e);
  P[0] = (u64x2){f0 | (uint64_t)f1 << 32, fx | (uint64_t)(sco(e) | (sco(e1) + (e & 6) * 8) << 16) << 32};
  const u32x4 sc = *(const u32x4*)g_sc;
  P[1] = (u64x2){(((b >> 3) * 4 + w) & 7) * 1024 | (uint64_t)sc[0] << 32, sc[1] | (uint64_t)sc[2] << 32};
  P[2] = (u64x2){h0, h0 + 8192};
  P[3] = (u64x2){h1, h1 + 8192};
  P[4] = (u64x2){__float_as_uint(g_sc[4]) | (uint64_t)((e1 - e) * 1024 + (e & 7) * 128) << 32, 0};
  P[5] = (u64x2){(uint64_t)x + p0, (uint64_t)x + p1};
  P[6] = (u64x2){(uint64_t)out + p0, (uint64_t)out + p1};
  P[7] = (u64x2){0x7fffffffffffffull, (uint64_t)g_buf + G_FLAG};
  P[8] = (u64x2){(uint64_t)g_w2f + w * 6144, (uint64_t)g_w2q + w * 6144};
  P[9] = (u64x2){(uint64_t)g_w2r + e * 1024, (uint64_t)g_w2f + e * 1024};
  P[10] = (u64x2){(uint64_t)grn_g + w * 256, (uint64_t)g_cs + w * 384 + 2048};
  P[11] = (u64x2){(uint64_t)g_buf + G_PUB + b * 256, (uint64_t)g_buf + G_PUB + w * 16384};
  P[12] = (u64x2){cso(e) | (uint64_t)(cso(e1) + (e & 1) * 32) << 32, (uint64_t)g_cs};
  P[13] = (u64x2){sc[3] | (uint64_t)1 << (32 + (b >> 3) % K2_NAT), 0};
}
__global__ __launch_bounds__(256, 1) void k1_fc1(const u16* __restrict__ x, char* __restrict__ hws, const u16* __restrict__ out, const u16* __restrict__ grn_g) {
  extern __shared__ __attribute__((aligned(16))) char smem[];
  const unsigned w = __builtin_amdgcn_readfirstlane(threadIdx.x >> 6);
  if (blockIdx.x < 8) {
    if (w < 2) k2_params(x, hws, out, grn_g, w * 64 + __builtin_amdgcn_mbcnt_hi(~0u, __builtin_amdgcn_mbcnt_lo(~0u, 0u)));
    return;
  }
  const unsigned lds0 = lds_addr(smem);
  const unsigned b = blockIdx.x - 8;
  const unsigned dir = (b >> 3) & 1;
  TS(0); MK_START(1);
  const uint64_t xp = (uint64_t)x, hp = (uint64_t)hws, gp = (uint64_t)g_buf, wp = gp + w * K1_WCH;
  const unsigned d = dir ? ~0u : 1u, wrap = dir ? ~0u : 48u, rst = dir ? 47u : 0u, pos0 = dir ? K1_SMAX - 1 : 0u;
  const unsigned xo0 = (12 * ((b >> 3) * 4 + (b & 3)) + w) * 49152, xs1 = (1536 + w + (dir ? 8 : 0)) * 49152, sb1 = 1536 * w + (dir ? K1_SB1 : 0);
  const unsigned r0o = ((b >> 3) * 4 + (b & 3)) * H_RS, tsel = 4 * (b & 3), s2 = (b >> 2) & 1;
  const uint64_t ap = gp + K1_G_ACC + (b % K1_NREP) * 12288;
  const unsigned ca0 = w == 0 ? dir * 64 : K1_G_DUM - K1_G_CNT + (4 * b + w) * 64;
  const unsigned pm = lds0 + K1_PF_OFF, pf = ((b >> 3) * 4 + w) * 1024;
  unsigned nb;
  asm volatile(
R"(.macro M t,f,w
.if \f < 30
v_mfma_f32_16x16x32_f16 v[56+4*\t:56+4*\t+3], v[128+4*\f:128+4*\f+3], a[216+4*\w:216+4*\w+3], v[56+4*\t:56+4*\t+3]
.else
v_mfma_f32_16x16x32_f16 v[56+4*\t:56+4*\t+3], a[4*\f-4*30:4*\f-4*30+3], a[216+4*\w:216+4*\w+3], v[56+4*\t:56+4*\t+3]
.endif
.endm
.macro Z t,f,w
.if \f < 30
v_mfma_f32_16x16x32_f16 v[56+4*\t:56+4*\t+3], v[128+4*\f:128+4*\f+3], a[216+4*\w:216+4*\w+3], v[252:255]
.else
v_mfma_f32_16x16x32_f16 v[56+4*\t:56+4*\t+3], a[4*\f-4*30:4*\f-4*30+3], a[216+4*\w:216+4*\w+3], v[252:255]
.endif
.endm
.macro LZ a,f
.if \f < 30
v_mfma_f32_16x16x32_bf16 v[36:36+3], v[\a:\a+3], v[128+4*\f:128+4*\f+3], 0
.else
v_mfma_f32_16x16x32_bf16 v[36:36+3], v[\a:\a+3], a[4*\f-4*30:4*\f-4*30+3], 0
.endif
.endm
.macro LS a,f
.if \f < 30
v_mfma_f32_16x16x32_bf16 v[36:36+3], v[\a:\a+3], v[128+4*\f:128+4*\f+3], v[36:36+3]
.else
v_mfma_f32_16x16x32_bf16 v[36:36+3], v[\a:\a+3], a[4*\f-4*30:4*\f-4*30+3], v[36:36+3]
.endif
.endm
.macro LD d,a,f
.if \f < 30
v_mfma_f32_16x16x32_bf16 v[\d:\d+3], a[\a:\a+3], v[128+4*\f:128+4*\f+3], v[36:36+3]
.else
v_mfma_f32_16x16x32_bf16 v[\d:\d+3], a[\a:\a+3], a[4*\f-4*30:4*\f-4*30+3], v[36:36+3]
.endif
.endm
.macro GZ f
.if \f < 30
v_mfma_f32_16x16x32_f16 v[52:55], v[128+4*\f:128+4*\f+3], v[128+4*\f:128+4*\f+3], 0
.else
v_mfma_f32_16x16x32_f16 v[52:55], a[4*\f-4*30:4*\f-4*30+3], a[4*\f-4*30:4*\f-4*30+3], 0
.endif
.endm
.macro GS f
.if \f < 30
v_mfma_f32_16x16x32_f16 v[52:55], v[128+4*\f:128+4*\f+3], v[128+4*\f:128+4*\f+3], v[52:55]
.else
v_mfma_f32_16x16x32_f16 v[52:55], a[4*\f-4*30:4*\f-4*30+3], a[4*\f-4*30:4*\f-4*30+3], v[52:55]
.endif
.endm
.macro CL p,z,s
v_fma_mixlo_f16 v[\p], v[\z], v[\s], 0 op_sel_hi:[0,1,0]
.endm
.macro CH p,z,s
v_fma_mixhi_f16 v[\p], v[\z], v[\s], 0 op_sel:[0,1,0] op_sel_hi:[0,1,0]
.endm
.macro XL f,o
.if \f < 30
global_load_dwordx4 v[128+4*\f:128+4*\f+3], v3, s[46:47] offset:\o
.else
global_load_dwordx4 a[4*\f-4*30:4*\f-4*30+3], v3, s[46:47] offset:\o
.endif
.endm
.macro XN f,o
.if \f < 30
global_load_dwordx4 v[128+4*\f:128+4*\f+3], v3, s[46:47] offset:\o nt
.else
global_load_dwordx4 a[4*\f-4*30:4*\f-4*30+3], v3, s[46:47] offset:\o nt
.endif
.endm
.macro F t,z
v_fma_f32 v[\t], -v[\z], v[\z], s36
.endm
.macro U t,z
v_mul_f32 v[\t], v[\z], v[\t]
.endm
.macro E t
v_exp_f32 v[\t], v[\t]
.endm
.macro A t
v_add_f32 v[\t], 1.0, v[\t]
.endm
.macro R t
v_rcp_f32 v[\t], v[\t]
.endm
.macro C d,a,b
v_cvt_pk_bf16_f32 v[\d], v[\a], v[\b]
.endm
.macro S0 q,t
v_mul_f32 v[\q], v[\t], v[\t]
.endm
.macro S q,t
v_fmac_f32 v[\q], v[\t], v[\t]
.endm
.macro HC d,z
v_cvt_pk_f16_f32 v[\d], v[\z], v[\z+1]
.endm
.macro HF t,p
v_pk_fma_f16 v[\t], v[\p], v[\p], s36 neg_lo:[1,0,0] neg_hi:[1,0,0]
.endm
.macro HM t,p
v_pk_mul_f16 v[\t], v[\p], v[\t]
.endm
.macro HE d,s
v_exp_f16 v[\d], v[\s]
.endm
.macro HG d,s
v_exp_f16_sdwa v[\d], v[\s] dst_sel:WORD_1 dst_unused:UNUSED_PRESERVE src0_sel:WORD_1
.endm
.macro HA t
v_pk_add_f16 v[\t], v[\t], 1.0 op_sel_hi:[1,0]
.endm
.macro HR d,s
v_rcp_f16 v[\d], v[\s]
.endm
.macro HQ d,s
v_rcp_f16_sdwa v[\d], v[\s] dst_sel:WORD_1 dst_unused:UNUSED_PRESERVE src0_sel:WORD_1
.endm
.macro HP d,s
v_rcp_f16_sdwa v[\d], v[\s] dst_sel:DWORD dst_unused:UNUSED_PAD src0_sel:WORD_1
.endm
.macro HY t,z
v_fma_mix_f32 v[\t], v[\z], v[\t], 0 op_sel_hi:[0,1,0]
.endm
.macro HK p
v_and_b32 v[\p], s56, v[\p]
.endm
.macro LQ p
v_lshrrev_b32 v[\p], 2, v[\p]
.endm
.macro LL t,z
v_lshlrev_b32_sdwa v[\t], 1, v[\z] dst_sel:DWORD dst_unused:UNUSED_PAD src0_sel:DWORD src1_sel:WORD_1
.endm
.macro LH t,p
v_and_b32_sdwa v[\t], s56, v[\p] dst_sel:DWORD dst_unused:UNUSED_PAD src0_sel:DWORD src1_sel:WORD_1
.endm
.macro LA t
ds_read_u16 v[\t], v[\t] offset:5376
.endm
.macro LC_ t
ds_read_u16_d16_hi v[\t], v[\t] offset:5376
.endm
.macro LO p,a,b
v_bfi_b32 v[\p], s36, v[\a], v[\b]
.endm
.macro HZ q,p
v_pk_mul_f16 v[\q], v[\p], v[\p]
.endm
.macro HS q,p
v_pk_fma_f16 v[\q], v[\p], v[\p], v[\q]
.endm
.macro HT q,p
v_fma_mix_f32 v[\q], v[\p], v[\p], v[\q] op_sel:[1,1,0] op_sel_hi:[1,1,0]
.endm
.macro HX q
v_fma_mix_f32 v[\q], v[\q], 1.0, v[\q] op_sel:[0,0,1] op_sel_hi:[1,0,1]
.endm
.macro UX t,z
v_mul_f32_e64 v[\t], v[\z], v[\t]
.endm
.macro EX t
v_exp_f32_e64 v[\t], v[\t]
.endm
.macro AX t
v_add_f32_e64 v[\t], 1.0, v[\t]
.endm
.macro RX t
v_rcp_f32_e64 v[\t], v[\t]
.endm
.macro TX q,t
v_mul_f32_e64 v[\q], v[\t], v[\t]
.endm
.macro SX q,t
v_fmac_f32_e64 v[\q], v[\t], v[\t]
.endm
.macro AW a,s
v_accvgpr_write_b32 a[\a], v[\s]
.endm
.macro RA d,v,o
ds_read_b128 a[\d:\d+3], v[\v] offset:\o
.endm
.macro RV d,v,o
ds_read_b128 v[\d:\d+3], v[\v] offset:\o
.endm
.macro W2 a,b,o
ds_write2st64_b32 v4, v[\a], v[\b] offset0:\o offset1:\o+4
.endm
.macro W1 a,o
ds_write_b32 v4, v[\a] offset:\o
.endm
.macro DW o
global_load_lds_dwordx4 v0, s[40:41] offset:\o
.endm
.macro DF
global_load_lds_dwordx4 v0, s[70:71]
.endm
.macro DX
global_load_lds_dword v3, s[46:47]
.endm
.macro ST d
.if ((\d - 104) / 4) & 1
global_store_dwordx4 v2, v[\d:\d+3], s[42:43] nt sc1
.else
global_store_dwordx2 v31, v[\d:\d+1], s[42:43] nt sc1
.endif
.endm
.macro SU d
.if ((\d - 104) / 4) & 1
global_store_dwordx4 v2, v[\d:\d+3], s[42:43]  sc1
.else
global_store_dwordx2 v31, v[\d:\d+1], s[42:43]  sc1
.endif
.endm
.macro SP
global_atomic_add_f64 v6, v[14:15], s[48:49]
.endm
.macro AT
global_atomic_add v11, v9, v10, s[68:69] sc0
.endm
.macro PFA
s_getpc_b64 s[70:71]
s_add_u32 s70, s70, 7f-.
s_addc_u32 s71, s71, 0
s_andn2_b32 s70, s70, 0x7f
s_min_u32 s53, %[pf], 8f-7f-1152
s_andn2_b32 s53, s53, 0x7f
s_add_u32 s70, s70, s53
s_addc_u32 s71, s71, 0
.endm
.macro PFN
s_getpc_b64 s[70:71]
s_add_u32 s70, s70, k2_code@rel32@lo-1020
s_addc_u32 s71, s71, k2_code@rel32@hi-1012
s_add_u32 s70, s70, %[pf]
s_addc_u32 s71, s71, 0
s_andn2_b32 s70, s70, 0x7f
.endm
.macro PFW
s_getpc_b64 s[70:71]
s_add_u32 s70, s70, g_w2q@rel32@lo+4
s_addc_u32 s71, s71, g_w2q@rel32@hi+12
s_add_u32 s70, s70, %[pf]
s_addc_u32 s71, s71, 0
.endm
.macro PF0
s_getpc_b64 s[70:71]
s_andn2_b32 s70, s70, 0x7f
s_min_u32 s53, %[pf], 7f-.-1152
s_andn2_b32 s53, s53, 0x7f
s_add_u32 s70, s70, s53
s_addc_u32 s71, s71, 0
.endm
.macro WL n
s_waitcnt lgkmcnt(\n)
.endm
.macro WV n
s_waitcnt vmcnt(\n)
.endm
.macro LU t,p
v_pk_fma_f16 v[\t], v[\p], v34, s67
.endm
.macro CF d,a,b
v_cvt_pk_f16_f32 v[\d], v[\a], v[\b]
.endm
.macro XS d
v_pk_fma_f16 v[\d], v[\d], v13, s56
.endm
.macro AR d,a
v_accvgpr_read_b32 v[\d], a[\a]
.endm
.macro DQ o
global_load_lds_dword v23, s[40:41] offset:\o
.endm
.macro GE
v_cmp_eq_u32 vcc, 0, v35
s_nop 4
s_cbranch_vccz 9f
v_mov_b32 v35, 14720
s_mov_b64 exec, 1
global_store_dword v35, v10, s[68:69] sc1
s_mov_b64 exec, -1
9:
.endm
.macro BW f
.if \f < 30
ds_write_b128 v12, v[128+4*\f:128+4*\f+3]
.else
ds_write_b128 v12, a[4*\f-120:4*\f-120+3]
.endif
.endm
.macro BR f
.if \f < 30
ds_read_b128 v[128+4*\f:128+4*\f+3], v20
.else
ds_read_b128 a[4*\f-120:4*\f-120+3], v20
.endif
.endm
.macro LK d,a,f,h
.if \f < 30
v_mfma_f32_4x4x4_16b_bf16 v[\d:\d+3], a[\a:\a+1], v[128+4*\f+\h:128+4*\f+\h+1], v[36:39]
.else
v_mfma_f32_4x4x4_16b_bf16 v[\d:\d+3], a[\a:\a+1], a[4*\f-120+\h:4*\f-120+\h+1], v[36:39]
.endif
.endm
.macro PK d,a,b,s
v_perm_b32 v[\d], v[\a], v[\b], v[\s]
.endm
.macro PO d
v_lshl_or_b32 v[\d], v[\d], 4, v[\d-4]
.endm
.macro LG a,b
v_cmp_eq_f32 vcc, v[\a], v[\b]
v_mov_b32 v[\b], 0x7f800000
s_nop 0
v_cndmask_b32 v[\a], v[\b], v[\a], vcc
.endm
.macro DY
s_mov_b32 s72, m0
s_mov_b32 m0, %[pm]
s_nop 0
global_load_lds_dword v3, s[46:47]
s_mov_b32 m0, s72
s_nop 0
.endm
.macro FP
ds_write_b32 v6, v19 offset:61440
.endm
.macro FR
ds_read_b32 v15, v31 offset:61440
.endm
k1_code:
v_mbcnt_lo_u32_b32 v16, -1, 0
v_mbcnt_hi_u32_b32 v16, -1, v16
v_lshlrev_b32 v0, 4, v16
v_and_b32 v17, 15, v16
v_lshrrev_b32 v18, 4, v16
s_add_u32 s68, %[glo], 1239040
s_addc_u32 s69, %[ghi], 0
s_mov_b32 s53, %[r0o]
s_load_dword s67, s[68:69], 16320
s_add_u32 s82, %[hlo], s53
s_addc_u32 s83, %[hhi], 0
s_add_u32 s84, s82, 8192
s_addc_u32 s85, s83, 0
s_and_b32 s90, %[hlo], 0x1fffff
s_add_u32 s90, s90, s53
s_mov_b32 s91, 3072
v_mov_b32 v11, s90
v_mad_u32_u24 v11, v0, s91, v11
v_lshrrev_b32 v11, 21, v11
v_lshl_add_u32 v11, v11, 2, 64
v_writelane_b32 v11, %[tsel], 63
s_sub_u32 s53, s82, %[hlo]
s_lshr_b32 s53, s53, 2
s_and_b32 s90, %[xlo], 0x1fffff
s_add_u32 s53, s53, s90
s_add_u32 s53, s53, 0x20048000
s_lshr_b32 s53, s53, 21
s_lshl_b32 s90, s53, 2
v_writelane_b32 v11, s90, 48
global_load_dword v11, v11, s[68:69] offset:-2048
v_lshlrev_b32 v19, 1, v18
s_movk_i32 s53, 0x78
v_bfe_u32 v21, v16, 2, 2
v_lshrrev_b32 v19, v19, s53
v_mul_u32_u24 v21, 0x300, v21
v_xor_b32 v19, v19, v16
v_and_b32 v19, 3, v19
v_lshl_add_u32 v21, v19, 4, v21
s_mov_b32 s97, 3072
s_mov_b32 s50, %[pos0]
s_mov_b32 s51, %[pos0]
s_mov_b32 s52, %[pos0]
s_add_u32 s74, %[lds], 0
s_add_u32 s75, s74, 163840
s_mov_b32 s37, s74
s_mul_i32 s53, %[w], 6400
s_add_u32 s76, s74, s53
s_add_u32 s77, s76, 163840
s_mov_b32 s38, s76
s_mul_i32 s60, %[w], 1536
s_add_u32 s61, s60, 512
s_add_u32 s62, s60, 16384
s_add_u32 s63, s60, 16896
s_add_u32 s64, s60, 32768
s_add_u32 s65, s60, 33280
v_lshlrev_b32 v23, 2, v16
s_lshl_b32 s53, %[w], 11
s_min_u32 s53, s53, 5120
s_add_u32 s72, s53, 1228800
s_add_u32 s40, %[glo], s72
s_addc_u32 s41, %[ghi], 0
s_add_u32 s53, s53, %[lds]
s_add_u32 m0, s53, 58368
s_nop 0
DW 0
DW 1024
s_nop 0 ;PS
s_mul_i32 s53, s50, 25600
s_add_u32 s40, %[wlo], s53
s_addc_u32 s41, %[whi], 0
)"
R"(s_mov_b32 m0, s38
s_nop 0
DW 0
DW 1024
DW 2048
s_add_u32 s40, s40, 3072
s_addc_u32 s41, s41, 0
s_add_u32 m0, s38, 3072
s_nop 0
DW 0
DW 1024
DW 2048
DQ 3072
s_add_u32 s50, s50, %[d]
s_cmp_eq_u32 s50, %[wrap]
s_cselect_b32 s50, %[rst], s50
s_add_u32 s38, s38, 32768
s_cmp_eq_u32 s38, s77
s_cselect_b32 s38, s76, s38
v_add_u32 v1, s37, v0
v_lshl_add_u32 v19, v18, 2, v17
v_and_b32 v19, 15, v19
v_lshlrev_b32 v19, 4, v19
v_lshl_add_u32 v2, v18, 8, v19
s_lshl_b32 s53, %[w], 2
s_add_u32 s53, s53, %[lds]
s_add_u32 s53, s53, 123904
s_add_u32 s53, %[lds], 123904
v_bfe_u32 v4, v17, 1, 2
v_and_b32 v5, 8, v17
v_add3_u32 v4, v4, v5, %[w]
v_lshl_add_u32 v4, v18, 2, v4
v_and_b32 v4, 15, v4
v_lshl_add_u32 v4, v17, 4, v4
v_lshl_add_u32 v4, v4, 2, s53
s_lshl_b32 s53, %[w], 9
s_add_u32 s53, s53, %[lds]
s_add_u32 s53, s53, 123904
v_lshlrev_b32 v5, 3, v16
v_add_u32 v5, s53, v5
s_lshl_b32 s53, %[w], 6
v_and_b32 v6, 0x38, v16
v_add_u32 v6, s53, v6
s_add_u32 s53, %[lds], 58368
v_and_b32 v19, 3, v16
v_lshl_add_u32 v19, v18, 2, v19
v_lshl_add_u32 v7, v19, 4, s53
v_lshlrev_b32 v8, 4, v18
v_add_u32 v8, s53, v8
v_mov_b32 v10, 1
s_mov_b32 s36, 0xffff
s_mov_b32 s56, 0x5550555
s_mov_b32 s39, 32768
s_mov_b32 s54, 0x01010101
s_mov_b32 s55, 0x01010101
s_mov_b32 %[nb], 0
v_and_b32 v28, 12, v17
v_lshl_add_u32 v28, v28, 2, v17
v_lshlrev_b32 v28, 2, v28
v_mov_b32 v34, 0x74007400
v_mov_b32 v35, -1
v_and_b32 v22, 0xfffffcff, v1
v_lshrrev_b32 v31, 1, v2
v_mov_b32 v32, 0x06020400
v_mov_b32 v33, 0x07030501
s_waitcnt lgkmcnt(0)
s_mov_b32 s78, 0xaaaaaaaa
s_mov_b32 s79, 0xaaaaaaaa
s_mov_b32 s80, 0xcccccccc
s_mov_b32 s81, 0xcccccccc
s_and_b32 s53, %[w], 1
s_lshl_b32 s53, s53, 10
s_lshr_b32 s72, %[w], 1
s_mul_i32 s72, s72, 32768
s_add_u32 s72, s72, s53
s_add_u32 s72, s72, 128000
s_movk_i32 s53, 0x78
v_lshrrev_b32 v12, 5, v0
v_and_b32 v12, 6, v12
v_bfe_u32 v12, s53, v12, 2
v_lshrrev_b32 v20, 8, v0
v_xor_b32 v20, v12, v20
v_lshl_add_u32 v20, v20, 4, s72
v_and_b32 v12, 0xf0, v0
v_lshl_add_u32 v20, v12, 2, v20
v_add_u32 v12, s72, v0
WV 9
v_bcnt_u32_b32 v11, v11, 0
v_cmp_lt_u32 s[90:91], 2, v11
s_nop 0
s_lshr_b32 s96, s91, 31
s_xor_b32 s96, s96, %[s2]
s_sub_u32 s53, 0, s96
s_xor_b32 s90, s90, s53
s_xor_b32 s91, s91, s53
s_add_u32 s53, %[xo0], 0
s_add_u32 s46, %[xlo], s53
s_addc_u32 s47, %[xhi], 0
s_bitcmp1_b64 s[90:91], 48
s_cselect_b32 s58, 16, 0
s_lshr_b32 s58, 0xfe987610, s58
v_lshrrev_b32 v3, 6, v0
v_and_b32 v3, 12, v3
v_bfe_u32 v3, s58, v3, 4
v_mad_u32_u24 v3, v3, s97, v21
XL 12,0
XL 13,64
XL 14,128
XL 15,192
XL 16,256
XL 17,320
XL 18,384
XL 19,448
XL 20,512
XL 21,576
XL 22,640
XL 23,704
s_mul_i32 s53, s96, 8320
s_cmp_lt_u32 %[ca0], 128
s_cselect_b32 s53, s53, 0
s_add_u32 s73, s53, %[ca0]
v_mov_b32 v9, s73
WV 19
s_barrier
v_lshrrev_b32 v19, 8, v31
v_sub_u32 v19, 0, v19
ds_write_b32 v31, v19 offset:61440
s_nop 1
s_nop 0
v_mov_b32 v19, 0
WV 11
BW 12
RV 40,8,6144
RV 44,8,6208
RV 48,8,6272
RV 248,8,6336
BR 12
WV 10
BW 13
s_nop 1
BR 13
WL 2
LZ 40,12
WV 9
BW 14
RV 40,8,6400
s_nop 0
BR 14
s_add_u32 s53, %[xo0], 0
s_add_u32 s46, %[xlo], s53
s_addc_u32 s47, %[xhi], 0
s_bitcmp1_b64 s[90:91], 48
s_cselect_b32 s58, 16, 0
s_lshr_b32 s58, 0xda43cb52, s58
v_lshrrev_b32 v3, 6, v0
v_and_b32 v3, 12, v3
v_bfe_u32 v3, s58, v3, 4
v_mad_u32_u24 v3, v3, s97, v21
WL 3
LS 44,13
WV 8
BW 15
RV 44,8,6464
s_nop 0
BR 15
XL 24,0
WL 3
LS 48,14
WV 8
BW 16
RV 48,8,6528
s_nop 0
BR 16
WL 3
LS 248,15
WV 7
BW 17
RV 248,8,6592
s_nop 0
BR 17
XL 25,64
WL 3
LS 40,16
WV 7
BW 18
RV 40,8,6656
s_nop 0
BR 18
XL 26,128
WL 3
LS 44,17
WV 7
BW 19
RV 44,8,6720
s_nop 0
BR 19
WL 3
LS 48,18
WV 6
BW 20
RV 48,8,6784
s_nop 0
BR 20
XL 27,192
WL 3
LS 248,19
WV 6
BW 21
RV 248,8,6848
s_nop 0
BR 21
XL 28,256
WL 3
LS 40,20
WV 6
BW 22
RA 240,7,0
s_nop 0
BR 22
WL 3
LS 44,21
WV 5
BW 23
RA 244,7,256
s_nop 0
BR 23
XL 29,320
WL 3
LS 48,22
RA 248,7,512
XL 30,384
WL 1
LS 248,23
RA 252,7,768
s_nop 6
v_mul_f32 v36, 0xbb2aaaab, v36
v_mul_f32 v37, 0xbb2aaaab, v37
v_mul_f32 v38, 0xbb2aaaab, v38
v_mul_f32 v39, 0xbb2aaaab, v39
s_nop 1
LK 40,240,12,0
LK 44,242,12,2
RA 240,7,1024
XL 31,448
LK 48,244,13,0
s_nop 0
CF 176,40,41
LK 248,246,13,2
WL 2
RA 244,7,1280
CF 177,42,43
CF 178,44,45
CF 179,46,47
XL 32,512
LK 40,248,14,0
CF 180,48,49
LK 44,250,14,2
RA 248,7,1536
CF 181,50,51
GZ 12
WL 3
XL 33,576
CF 182,248,249
CF 183,250,251
LK 48,252,15,0
CF 184,40,41
LK 248,254,15,2
RA 252,7,1792
CF 185,42,43
XL 34,640
GS 13
WL 3
CF 186,44,45
CF 187,46,47
LK 40,240,16,0
CF 188,48,49
LK 44,242,16,2
WL 2
RA 240,7,2048
CF 189,50,51
XL 35,704
GS 14
s_mul_i32 s53, s50, 25600
s_add_u32 s40, %[wlo], s53
s_addc_u32 s41, %[whi], 0
s_mov_b32 m0, s38
CF 190,248,249
CF 191,250,251
LK 48,244,17,0
CF 192,40,41
LK 248,246,17,2
RA 244,7,2304
CF 193,42,43
GS 15
WL 3
DW 0
CF 194,44,45
CF 195,46,47
LK 40,248,18,0
CF 196,48,49
LK 44,250,18,2
RA 248,7,2560
CF 197,50,51
DW 1024
GS 16
WL 3
CF 198,248,249
CF 199,250,251
LK 48,252,19,0
CF 200,40,41
LK 248,254,19,2
WL 2
RA 252,7,2816
CF 201,42,43
DW 2048
GS 17
s_add_u32 s40, s40, 3072
s_addc_u32 s41, s41, 0
s_add_u32 m0, s38, 3072
CF 202,44,45
CF 203,46,47
LK 40,240,20,0
CF 204,48,49
LK 44,242,20,2
CF 205,50,51
GS 18
WL 2
DW 0
CF 206,248,249
CF 207,250,251
LK 48,244,21,0
CF 208,40,41
LK 248,246,21,2
CF 209,42,43
DW 1024
GS 19
WL 1
CF 210,44,45
CF 211,46,47
LK 40,248,22,0
CF 212,48,49
LK 44,250,22,2
CF 213,50,51
DW 2048
GS 20
WL 0
DQ 3072
CF 214,248,249
CF 215,250,251
LK 48,252,23,0
CF 216,40,41
LK 248,254,23,2
CF 217,42,43
GS 21
s_add_u32 s50, s50, %[d]
s_cmp_eq_u32 s50, %[wrap]
s_cselect_b32 s50, %[rst], s50
CF 218,44,45
CF 219,46,47
RV 36,8,6912
s_nop 0
GS 22
s_add_u32 s38, s38, 32768
s_cmp_eq_u32 s38, s77
s_cselect_b32 s38, s76, s38
CF 220,48,49
CF 221,50,51
CF 222,248,249
CF 223,250,251
s_nop 1
GS 23
s_nop 7
v_cndmask_b32 v52, v52, v53, s[78:79]
v_cndmask_b32 v54, v54, v55, s[78:79]
v_cndmask_b32 v52, v52, v54, s[80:81]
v_mul_f32 v52, 0x3b2aaaab, v52
WL 0
v_med3_f32 v53, v52, v39, v37
LG 52,53
s_nop 0
C 52,52,52
v_lshlrev_b32 v52, 16, v52
v_add_f32 v52, v36, v52
C 52,52,52
v_lshlrev_b32 v52, 16, v52
v_sqrt_f32 v52, v52
s_nop 0
C 52,52,52
v_lshlrev_b32 v52, 16, v52
v_rcp_f32 v52, v52
s_nop 0
v_mul_f32 v52, v38, v52
CF 52,52,52
ds_bpermute_b32 v13, v28, v52
WV 18
BW 24
RV 40,8,6144
RV 44,8,6208
RV 48,8,6272
BR 24
)"
R"(WV 17
BW 25
s_nop 1
BR 25
WL 7
XS 176
WL 2
LZ 40,24
s_and_b32 s53, %[w], 1
s_mul_i32 s53, s53, 3072
s_lshr_b32 s72, %[w], 1
s_lshl_b32 s40, s72, 16
s_add_u32 s40, s40, s53
s_add_u32 m0, s40, 25600
s_lshl_b32 s72, s72, 13
s_add_u32 s53, s53, s72
s_add_u32 s53, s53, 1382400
s_add_u32 s40, %[glo], s53
s_addc_u32 s41, %[ghi], 0
XS 177
WV 16
BW 26
XS 178
RV 40,8,6336
XS 179
BR 26
XS 180
WL 3
LS 44,25
WV 15
DW 0
XS 181
BW 27
XS 182
RV 44,8,6400
XS 183
BR 27
v_min_u32 v35, v35, v13
WL 3
LS 48,26
WV 15
DW 1024
XS 184
BW 28
XS 185
RV 48,8,6464
XS 186
BR 28
XS 187
WL 3
LS 40,27
WV 15
XS 188
BW 29
XS 189
RV 40,8,6528
XS 190
XS 191
BR 29
XS 192
WL 3
LS 44,28
WV 14
DW 2048
XS 193
BW 30
XS 194
RV 44,8,6592
XS 195
BR 30
XS 196
WL 3
LS 48,29
WV 14
DW 3072
XS 197
BW 31
XS 198
RV 48,8,6656
XS 199
BR 31
XS 200
WL 3
LS 40,30
WV 13
XS 201
BW 32
XS 202
RV 40,8,6720
XS 203
BR 32
XS 204
WL 3
LS 44,31
PF0
s_mov_b32 m0, %[pm]
s_nop 0
DF
PFA
XS 205
BW 33
XS 206
XS 207
RV 44,8,6784
XS 208
BR 33
XS 209
WL 3
LS 48,32
s_mul_i32 s53, s50, 25600
s_add_u32 s40, %[wlo], s53
s_addc_u32 s41, %[whi], 0
s_mov_b32 m0, s38
XS 210
WV 13
BW 34
XS 211
RV 48,8,6848
XS 212
RA 240,7,0
XS 213
BR 34
XS 214
WL 4
LS 40,33
WV 12
XS 215
BW 35
XS 216
RA 244,7,256
XS 217
BR 35
XS 218
WL 3
LS 44,34
DW 0
XS 219
RA 248,7,512
XS 220
WL 1
LS 48,35
DW 1024
XS 221
RA 252,7,768
XS 222
XS 223
s_nop 2
v_mul_f32 v36, 0xbb2aaaab, v36
v_mul_f32 v37, 0xbb2aaaab, v37
v_mul_f32 v38, 0xbb2aaaab, v38
v_mul_f32 v39, 0xbb2aaaab, v39
s_nop 1
LK 40,240,24,0
LK 44,242,24,2
RA 240,7,1024
LK 48,244,25,0
s_nop 1
CF 224,40,41
LK 248,246,25,2
WL 2
RA 244,7,1280
CF 225,42,43
CF 226,44,45
CF 227,46,47
DW 2048
LK 40,248,26,0
CF 228,48,49
LK 44,250,26,2
RA 248,7,1536
CF 229,50,51
s_add_u32 s40, s40, 3072
s_addc_u32 s41, s41, 0
s_add_u32 m0, s38, 3072
GZ 24
WL 3
CF 230,248,249
CF 231,250,251
LK 48,252,27,0
CF 232,40,41
LK 248,254,27,2
RA 252,7,1792
CF 233,42,43
DW 0
GS 25
WL 3
DW 1024
CF 234,44,45
CF 235,46,47
LK 40,240,28,0
CF 236,48,49
LK 44,242,28,2
RA 240,7,2048
CF 237,50,51
GS 26
WL 3
DW 2048
CF 238,248,249
CF 239,250,251
LK 48,244,29,0
CF 240,40,41
LK 248,246,29,2
RA 244,7,2304
CF 241,42,43
DQ 3072
GS 27
WL 3
CF 242,44,45
CF 243,46,47
LK 40,248,30,0
CF 244,48,49
LK 44,250,30,2
WL 2
RA 248,7,2560
CF 245,50,51
s_add_u32 s50, s50, %[d]
s_cmp_eq_u32 s50, %[wrap]
s_cselect_b32 s50, %[rst], s50
GS 28
s_add_u32 s38, s38, 32768
s_cmp_eq_u32 s38, s77
s_cselect_b32 s38, s76, s38
CF 246,248,249
CF 247,250,251
LK 48,252,31,0
CF 152,40,41
LK 248,254,31,2
WL 2
RA 252,7,2816
CF 153,42,43
GS 29
s_add_u32 s53, %[xo0], 196608
s_add_u32 s46, %[xlo], s53
s_addc_u32 s47, %[xhi], 0
s_bitcmp1_b64 s[90:91], 48
s_cselect_b32 s58, 16, 0
s_lshr_b32 s58, 0xfe987610, s58
v_lshrrev_b32 v3, 6, v0
v_and_b32 v3, 12, v3
v_bfe_u32 v3, s58, v3, 4
v_mad_u32_u24 v3, v3, s97, v21
CF 154,44,45
CF 155,46,47
LK 40,240,32,0
CF 156,48,49
LK 44,242,32,2
CF 157,50,51
XL 36,0
GS 6
WL 2
CF 158,248,249
CF 159,250,251
LK 48,244,33,0
CF 160,40,41
LK 248,246,33,2
CF 161,42,43
XL 37,64
GS 7
WL 1
XL 38,128
CF 162,44,45
CF 163,46,47
LK 40,248,34,0
CF 164,48,49
LK 44,250,34,2
CF 165,50,51
GS 8
WL 0
XL 39,192
CF 166,248,249
CF 167,250,251
LK 48,252,35,0
CF 168,40,41
LK 248,254,35,2
CF 169,42,43
XL 40,256
GS 9
CF 170,44,45
CF 171,46,47
RV 36,8,6912
s_nop 0
GS 10
XL 41,320
CF 172,48,49
CF 173,50,51
CF 174,248,249
CF 175,250,251
s_nop 1
GS 11
XL 42,384
WV 15
s_barrier
RA 216,1,0
RA 220,1,1024
RA 224,1,2048
RA 228,1,3072
RV 252,22,5120
v_cndmask_b32 v52, v52, v53, s[78:79]
v_cndmask_b32 v54, v54, v55, s[78:79]
v_cndmask_b32 v52, v52, v54, s[80:81]
v_mul_f32 v52, 0x3b2aaaab, v52
WL 5
v_med3_f32 v53, v52, v39, v37
LG 52,53
s_nop 0
C 52,52,52
v_lshlrev_b32 v52, 16, v52
v_add_f32 v52, v36, v52
C 52,52,52
XL 43,448
v_lshlrev_b32 v52, 16, v52
v_sqrt_f32 v52, v52
s_nop 0
C 52,52,52
v_lshlrev_b32 v52, 16, v52
v_rcp_f32 v52, v52
XL 44,512
v_mul_f32 v52, v38, v52
CF 52,52,52
ds_bpermute_b32 v13, v28, v52
WL 0
XS 224
XS 225
XS 226
XS 227
XS 228
XS 229
XS 230
XL 45,576
XS 231
v_min_u32 v35, v35, v13
XS 232
XS 233
XS 234
XL 46,640
XS 235
XS 236
XS 237
XS 238
XS 239
XL 47,704
Z 0,12,0
XS 240
XS 241
XS 242
XS 243
XS 244
XS 245
XS 246
XS 247
XS 152
XS 153
XS 154
XS 155
M 0,13,1
s_mul_i32 s53, s50, 25600
RA 232,1,4096
AW 0,152
AW 1,153
Z 1,24,0
s_add_u32 s40, %[wlo], s53
AW 2,154
AW 3,155
M 1,25,1
s_addc_u32 s41, %[whi], 0
RA 236,1,6144
XS 156
XS 157
M 0,14,2
s_mov_b32 m0, s38
s_nop 0
DW 0
XS 158
XS 159
AW 4,156
M 0,15,3
RA 216,1,7168
AW 5,157
AW 6,158
M 1,26,2
DW 1024
AW 7,159
XS 160
M 1,27,3
WL 2
RA 220,1,8192
XS 161
XS 162
M 0,16,4
WL 2
XS 163
AW 8,160
M 0,17,5
RA 224,1,9216
AW 9,161
AW 10,162
AW 11,163
M 1,28,4
WL 1
DW 2048
XS 164
)"
R"(XS 165
M 1,29,5
s_add_u32 s40, s40, 3072
RA 228,1,10240
XS 166
XS 167
M 0,18,0
s_addc_u32 s41, s41, 0
WV 14
BW 36
AW 12,164
AW 13,165
M 0,19,1
s_add_u32 m0, s38, 3072
RA 232,1,11264
RV 40,8,6144
AW 14,166
AW 15,167
M 1,30,0
RV 252,22,5392
RV 44,8,6208
XS 168
XS 169
M 1,31,1
WL 6
RA 236,1,12288
RV 48,8,6272
XS 170
XS 171
AW 16,168
M 0,20,2
WL 7
DW 0
BR 36
AW 17,169
AW 18,170
M 0,21,3
RA 216,1,13312
AW 19,171
XS 172
M 1,32,2
DW 1024
XS 173
XS 174
M 1,33,3
WL 7
RA 220,1,14336
XS 175
WV 15
BW 37
s_nop 1
BR 37
AW 20,172
M 0,22,4
WL 6
DW 2048
AW 21,173
AW 22,174
AW 23,175
M 0,23,5
WL 4
RA 224,1,15360
LZ 40,36
WV 15
BW 38
RV 40,8,6336
s_nop 0
BR 38
M 1,34,4
DQ 3072
M 1,35,5
WL 4
RA 228,1,16384
LS 44,37
WV 15
BW 39
RV 44,8,6400
Z 6,12,0
s_add_u32 s50, s50, %[d]
BR 39
LL 16,56
LL 104,57
M 6,13,1
s_cmp_eq_u32 s50, %[wrap]
LA 16
RA 232,1,17408
WL 6
LS 48,38
s_cselect_b32 s50, %[rst], s50
WV 14
BW 40
RV 48,8,6464
Z 7,24,0
s_add_u32 s38, s38, 32768
LC_ 104
BR 40
M 7,25,1
s_cmp_eq_u32 s38, s77
RA 236,1,18432
WL 7
LS 40,39
s_cselect_b32 s38, s76, s38
WV 13
BW 41
RV 40,8,6528
M 6,14,2
s_add_u32 s37, s37, 32768
BR 41
M 6,15,3
s_cmp_eq_u32 s37, s75
RA 216,1,19456
WL 5
LS 44,40
s_cselect_b32 s37, s74, s37
WV 12
BW 42
M 7,26,2
s_add_u32 s53, %[xo0], 196608
RV 44,8,6592
BR 42
LL 17,58
LL 105,59
M 7,27,3
s_add_u32 s46, %[xlo], s53
LA 17
RA 220,1,20480
s_addc_u32 s47, %[xhi], 0
s_bitcmp1_b64 s[90:91], 48
s_cselect_b32 s58, 16, 0
s_lshr_b32 s58, 0xda43cb52, s58
v_lshrrev_b32 v3, 6, v0
v_and_b32 v3, 12, v3
v_bfe_u32 v3, s58, v3, 4
v_mad_u32_u24 v3, v3, s97, v21
WL 6
LS 48,41
WV 10
BW 43
M 6,16,4
WL 3
LC_ 105
XL 48,0
RV 48,8,6656
BR 43
M 6,17,5
s_mul_i32 s53, s51, 24
RA 224,1,21504
LS 40,42
s_bitcmp1_b32 s51, 0
BW 44
M 7,28,4
WV 10
RV 40,8,6720
LO 104,16,104
LU 16,104
M 7,29,5
s_addc_u32 s53, s53, 0
RA 228,1,22528
BR 44
HZ 24,16
WL 5
LS 44,43
s_lshl_b32 s53, s53, 11
BW 45
M 6,18,0
s_bitcmp1_b64 s[90:91], s51
LL 18,60
LL 108,61
s_cselect_b64 s[44:45], s[84:85], s[82:83]
s_add_u32 s44, s44, s53
s_addc_u32 s45, s45, 0
s_add_u32 s51, s51, %[d]
s_cmp_eq_u32 s51, %[wrap]
s_cselect_b32 s51, %[rst], s51
XL 49,64
RV 44,8,6784
M 6,19,1
WL 2
LA 18
RA 232,1,23552
BR 45
LS 48,44
WV 10
BW 46
M 7,30,0
LC_ 108
XL 50,128
M 7,31,1
RA 236,1,24576
RV 48,8,6848
RA 240,7,0
M 6,20,2
BR 46
LO 105,17,105
LU 17,105
M 6,21,3
WL 6
XL 51,192
HS 24,17
LS 40,45
WV 11
BW 47
RA 244,7,256
M 7,32,2
LL 16,62
LL 109,63
v_add_u32 v1, s37, v0
BR 47
M 7,33,3
WL 3
LA 16
RA 216,1,0
XL 52,256
LS 44,46
RA 248,7,512
v_and_b32 v22, 0xfffffcff, v1
M 6,22,4
LC_ 109
RA 220,1,1024
M 6,23,5
WL 5
RV 252,22,5120
LS 48,47
RA 252,7,768
s_nop 6
v_mul_f32 v36, 0xbb2aaaab, v36
v_mul_f32 v37, 0xbb2aaaab, v37
v_mul_f32 v38, 0xbb2aaaab, v38
M 7,34,4
XL 53,320
LO 108,18,108
LU 18,108
v_mul_f32 v39, 0xbb2aaaab, v39
M 7,35,5
s_barrier
RA 224,1,2048
HS 24,18
WL 2
Z 0,12,0
LL 17,80
LL 106,81
M 0,13,1
s_mul_i32 s53, s50, 25600
LA 17
RA 228,1,3072
LK 40,240,36,0
LK 44,242,36,2
s_add_u32 s40, %[wlo], s53
RA 240,7,1024
LK 48,244,37,0
s_nop 0
CF 128,40,41
LK 248,246,37,2
s_addc_u32 s41, %[whi], 0
RA 244,7,1280
CF 129,42,43
CF 130,44,45
CF 131,46,47
LK 40,248,38,0
CF 132,48,49
LK 44,250,38,2
s_mov_b32 m0, s38
RA 248,7,1536
CF 133,50,51
GZ 0
WL 6
CF 134,248,249
CF 135,250,251
LK 48,252,39,0
CF 136,40,41
LK 248,254,39,2
RA 252,7,1792
CF 137,42,43
GS 1
CF 138,44,45
CF 139,46,47
Z 1,24,0
LC_ 106
RA 232,1,4096
M 1,25,1
WL 8
RA 236,1,6144
M 0,14,2
WL 7
LO 109,16,109
LU 16,109
M 0,15,3
WL 6
RA 216,1,7168
HS 24,16
LK 40,240,40,0
CF 140,48,49
LK 44,242,40,2
RA 240,7,2048
CF 141,50,51
GS 2
WL 7
CF 142,248,249
CF 143,250,251
LK 48,244,41,0
CF 144,40,41
LK 248,246,41,2
RA 244,7,2304
CF 145,42,43
GS 3
WL 7
CF 146,44,45
CF 147,46,47
LK 40,248,42,0
CF 148,48,49
LK 44,250,42,2
RA 248,7,2560
CF 149,50,51
GS 4
CF 150,248,249
CF 151,250,251
M 1,26,2
DW 0
LL 18,82
LL 107,83
M 1,27,3
WL 7
LA 18
RA 220,1,8192
LK 48,252,43,0
CF 152,40,41
LK 248,254,43,2
RA 252,7,2816
CF 153,42,43
GS 5
WL 8
CF 154,44,45
CF 155,46,47
M 0,16,4
WL 7
LC_ 107
DW 1024
M 0,17,5
WL 6
RA 224,1,9216
HX 24
LK 40,240,44,0
CF 156,48,49
LK 44,242,44,2
CF 157,50,51
GS 6
CF 158,248,249
CF 159,250,251
M 1,28,4
LO 17,17,106
LU 106,17
W1 24,0
M 1,29,5
WL 7
RA 228,1,10240
DW 2048
HZ 25,106
LK 48,244,45,0
CF 160,40,41
LK 248,246,45,2
CF 161,42,43
GS 7
WL 5
CF 162,44,45
CF 163,46,47
M 0,18,0
s_add_u32 s40, s40, 3072
PK 106,17,104,33
PK 104,17,104,32
LL 16,84
LL 110,85
M 0,19,1
s_addc_u32 s41, s41, 0
LA 16
RA 232,1,11264
LK 40,248,46,0
CF 164,48,49
LK 44,250,46,2
s_add_u32 m0, s38, 3072
CF 165,50,51
GS 8
CF 166,248,249
CF 167,250,251
M 1,30,0
LC_ 110
RV 252,22,5392
M 1,31,1
WL 6
RA 236,1,12288
M 0,20,2
WL 5
LO 18,18,107
LU 107,18
M 0,21,3
RA 216,1,13312
DW 0
HS 25,107
LK 48,252,47,0
CF 168,40,41
LK 248,254,47,2
)"
R"(CF 169,42,43
GS 9
CF 170,44,45
CF 171,46,47
M 1,32,2
PK 107,18,105,33
PK 105,18,105,32
LL 17,86
LL 111,87
M 1,33,3
WL 1
LA 17
RA 220,1,14336
DW 1024
M 0,22,4
s_add_u32 s42, s44, s60
LC_ 111
RV 36,8,6912
s_addc_u32 s43, s45, 0
M 0,23,5
RA 224,1,15360
GS 10
CF 172,48,49
CF 173,50,51
M 1,34,4
LO 16,16,110
LU 110,16
ST 104
DW 2048
M 1,35,5
WL 5
RA 228,1,16384
HS 25,110
Z 6,12,0
WL 4
PK 110,16,108,33
PK 108,16,108,32
LL 18,56
LL 104,57
M 6,13,1
WL 1
LA 18
DQ 3072
PO 110
CF 174,248,249
CF 175,250,251
s_nop 1
GS 11
s_add_u32 s50, s50, %[d]
RA 232,1,17408
s_nop 5
v_cndmask_b32 v52, v52, v53, s[78:79]
Z 7,24,0
s_cmp_eq_u32 s50, %[wrap]
LC_ 104
v_cndmask_b32 v54, v54, v55, s[78:79]
M 7,25,1
s_cselect_b32 s50, %[rst], s50
RA 236,1,18432
v_cndmask_b32 v52, v52, v54, s[80:81]
v_mul_f32 v52, 0x3b2aaaab, v52
M 6,14,2
WL 2
LO 17,17,111
LU 111,17
v_med3_f32 v53, v52, v39, v37
LG 52,53
M 6,15,3
s_add_u32 s38, s38, 32768
RA 216,1,19456
HS 25,111
C 52,52,52
v_lshlrev_b32 v52, 16, v52
M 7,26,2
s_cmp_eq_u32 s38, s77
PK 111,17,109,33
PK 109,17,109,32
LL 16,58
LL 105,59
s_cselect_b32 s38, s76, s38
v_add_f32 v52, v36, v52
C 52,52,52
M 7,27,3
s_add_u32 s37, s37, 32768
LA 16
PO 111
s_cmp_eq_u32 s37, s75
s_cselect_b32 s37, s74, s37
v_lshlrev_b32 v52, 16, v52
v_sqrt_f32 v52, v52
M 6,16,4
WL 2
LC_ 105
RA 220,1,20480
XL 54,384
C 52,52,52
v_lshlrev_b32 v52, 16, v52
M 6,17,5
s_add_u32 s42, s44, s61
RA 224,1,21504
HX 25
s_addc_u32 s43, s45, 0
v_rcp_f32 v52, v52
s_nop 0
v_mul_f32 v52, v38, v52
M 7,28,4
LO 104,18,104
LU 18,104
W1 25,1024
CF 52,52,52
M 7,29,5
WL 2
RA 228,1,22528
HZ 24,18
ST 108
XL 55,448
ds_bpermute_b32 v13, v28, v52
M 6,18,0
s_mul_i32 s53, s51, 24
LL 17,60
LL 108,61
s_bitcmp1_b32 s51, 0
s_addc_u32 s53, s53, 0
s_lshl_b32 s53, s53, 11
s_bitcmp1_b64 s[90:91], s51
s_cselect_b64 s[44:45], s[84:85], s[82:83]
s_add_u32 s44, s44, s53
s_addc_u32 s45, s45, 0
s_add_u32 s51, s51, %[d]
s_cmp_eq_u32 s51, %[wrap]
s_cselect_b32 s51, %[rst], s51
M 6,19,1
LA 17
RA 232,1,23552
M 7,30,0
WL 5
LC_ 108
XL 56,512
v_add_u32 v4, s39, v4
M 7,31,1
WV 17
RA 236,1,24576
s_nop 1
v_add_u32 v1, s37, v0
BW 48
M 6,20,2
WL 6
XL 57,576
RV 40,8,6144
RV 44,8,6208
LO 105,16,105
LU 16,105
M 6,21,3
RA 216,1,0
RV 48,8,6272
HS 24,16
M 7,32,2
WL 5
BR 48
LL 18,62
LL 109,63
M 7,33,3
WV 16
LA 18
XL 58,640
BW 49
s_nop 1
BR 49
M 6,22,4
s_lshl_b32 s53, s52, 8
LC_ 109
v_and_b32 v22, 0xfffffcff, v1
XS 128
M 6,23,5
s_add_u32 s48, %[alo], s53
RA 220,1,1024
XS 129
XS 130
XS 131
WL 5
LZ 40,48
s_addc_u32 s49, %[ahi], 0
BW 50
M 7,34,4
WV 16
LO 108,17,108
LU 17,108
s_add_u32 s52, s52, %[d]
s_cmp_eq_u32 s52, %[wrap]
s_cselect_b32 s52, %[rst], s52
XL 59,704
RV 40,8,6336
BR 50
AW 24,128
AW 25,129
M 7,35,5
s_barrier
RV 252,22,5120
HS 24,17
AW 26,130
AW 27,131
WL 0
Z 0,12,0
s_sub_u32 s39, 0, s39
RA 224,1,2048
LL 16,80
LL 106,81
XS 132
XS 133
M 0,13,1
s_mul_i32 s53, s50, 25600
LA 16
RA 228,1,3072
XS 134
XS 135
AW 28,132
LS 44,49
s_add_u32 s40, %[wlo], s53
BW 51
RV 44,8,6400
s_nop 0
BR 51
Z 1,24,0
s_addc_u32 s41, %[whi], 0
LC_ 106
RA 232,1,4096
AW 29,133
AW 30,134
M 1,25,1
s_mov_b32 m0, s38
RA 236,1,6144
ds_read_b64 v[14:15], v5 offset:0
AW 31,135
v_min_u32 v35, v35, v13
LS 48,50
WV 16
BW 52
RV 48,8,6464
WL 11
M 0,14,2
WL 9
BR 52
LO 109,18,109
LU 18,109
XS 136
XS 137
M 0,15,3
WL 7
RA 216,1,7168
HS 24,18
XS 138
XS 139
AW 32,136
LS 40,51
WV 15
BW 53
M 1,26,2
DW 0
RV 40,8,6528
LL 17,82
LL 107,83
AW 33,137
AW 34,138
M 1,27,3
WL 8
LA 17
RA 220,1,8192
v_subrev_u32 v5, s39, v5
BR 53
AW 35,139
XS 140
M 0,16,4
WL 10
LC_ 107
DW 1024
XS 141
XS 142
M 0,17,5
WL 7
RA 224,1,9216
v_add_f32 v14, v14, v15
s_nop 1
v_add_f32_dpp v14, v14, v14 quad_perm:[1,0,3,2] row_mask:0xf bank_mask:0xf
s_nop 1
v_add_f32_dpp v14, v14, v14 quad_perm:[2,3,0,1] row_mask:0xf bank_mask:0xf
s_nop 1
v_add_f32_dpp v14, v14, v14 row_half_mirror row_mask:0xf bank_mask:0xf
HX 24
XS 143
AW 36,140
AW 37,141
LS 44,52
WV 8
BW 54
RV 44,8,6592
M 1,28,4
LO 16,16,106
LU 106,16
v_cvt_f64_f32 v[14:15], v14
W1 24,0
BR 54
AW 38,142
AW 39,143
M 1,29,5
WL 6
RA 228,1,10240
HZ 25,106
s_mov_b64 exec, s[54:55]
SP
s_mov_b64 exec, -1
DW 2048
XS 144
XS 145
LS 48,53
s_add_u32 s40, s40, 3072
WV 8
BW 55
M 0,18,0
s_addc_u32 s41, s41, 0
RV 48,8,6656
PK 106,16,104,33
PK 104,16,104,32
LL 18,84
LL 110,85
XS 146
XS 147
M 0,19,1
s_add_u32 m0, s38, 3072
LA 18
RA 232,1,11264
BR 55
AW 40,144
AW 41,145
AW 42,146
M 1,30,0
LC_ 110
RV 252,22,5392
AW 43,147
XS 148
M 1,31,1
WL 8
RA 236,1,12288
XS 149
XS 150
LS 40,54
WV 7
BW 56
RV 40,8,6720
M 0,20,2
WL 10
DW 0
BR 56
LO 17,17,107
LU 107,17
XS 151
AW 44,148
M 0,21,3
WL 6
RA 216,1,13312
HS 25,107
AW 45,149
AW 46,150
AW 47,151
LS 44,55
WV 7
BW 57
M 1,32,2
WL 5
DW 1024
RV 44,8,6784
PK 107,17,105,33
PK 105,17,105,32
LL 16,86
LL 111,87
XS 152
XS 153
M 1,33,3
s_add_u32 s42, s44, s60
LA 16
RA 220,1,14336
BR 57
XS 154
XS 155
M 0,22,4
s_addc_u32 s43, s45, 0
LC_ 111
DW 2048
AW 48,152
AW 49,153
M 0,23,5
WL 7
RA 224,1,15360
AW 50,154
AW 51,155
XS 156
LS 48,56
WV 8
BW 58
RV 48,8,6848
M 1,34,4
)"
R"(LO 18,18,110
LU 110,18
ST 104
RA 240,7,0
XS 157
XS 158
M 1,35,5
WL 5
RA 228,1,16384
DQ 3072
BR 58
HS 25,110
XS 159
AW 52,156
LS 40,57
WV 9
BW 59
Z 6,12,0
s_add_u32 s50, s50, %[d]
PK 110,18,108,33
PK 108,18,108,32
LL 17,56
LL 104,57
AW 53,157
AW 54,158
M 6,13,1
s_cmp_eq_u32 s50, %[wrap]
LA 17
RA 244,7,256
PO 110
AW 55,159
XS 160
Z 7,24,0
s_cselect_b32 s50, %[rst], s50
LC_ 104
RA 232,1,17408
BR 59
XS 161
XS 162
XS 163
M 7,25,1
s_add_u32 s38, s38, 32768
RA 236,1,18432
AW 56,160
AW 57,161
WL 7
LS 44,58
s_cmp_eq_u32 s38, s77
RA 248,7,512
AW 58,162
M 6,14,2
WL 2
LO 16,16,111
LU 111,16
s_cselect_b32 s38, s76, s38
AW 59,163
M 6,15,3
s_add_u32 s37, s37, 32768
RA 216,1,19456
HS 25,111
XS 164
XS 165
LS 48,59
s_cmp_eq_u32 s37, s75
RA 252,7,768
XS 166
M 7,26,2
s_cselect_b32 s37, s74, s37
PK 111,16,109,33
PK 109,16,109,32
LL 18,58
LL 105,59
XS 167
AW 60,164
M 7,27,3
s_add_u32 s53, %[xo0], 393216
LA 18
PO 111
s_add_u32 s46, %[xlo], s53
s_addc_u32 s47, %[xhi], 0
s_bitcmp1_b64 s[90:91], 48
s_cselect_b32 s58, 16, 0
s_lshr_b32 s58, 0xfe987610, s58
v_lshrrev_b32 v3, 6, v0
v_and_b32 v3, 12, v3
v_bfe_u32 v3, s58, v3, 4
v_mad_u32_u24 v3, v3, s97, v21
AW 61,165
AW 62,166
M 6,16,4
s_add_u32 s42, s44, s61
LC_ 105
RA 220,1,20480
AW 63,167
XS 168
WL 6
M 6,17,5
s_addc_u32 s43, s45, 0
RA 224,1,21504
XL 60,0
HX 25
XS 169
XS 170
M 7,28,4
LO 104,17,104
LU 17,104
W1 25,1024
XS 171
AW 64,168
AW 65,169
M 7,29,5
WL 2
RA 228,1,22528
HZ 24,17
ST 108
AW 66,170
AW 67,171
M 6,18,0
s_mul_i32 s53, s51, 24
LL 16,60
LL 108,61
s_bitcmp1_b32 s51, 0
s_addc_u32 s53, s53, 0
s_lshl_b32 s53, s53, 11
s_bitcmp1_b64 s[90:91], s51
s_cselect_b64 s[44:45], s[84:85], s[82:83]
s_add_u32 s44, s44, s53
s_addc_u32 s45, s45, 0
s_add_u32 s51, s51, %[d]
s_cmp_eq_u32 s51, %[wrap]
s_cselect_b32 s51, %[rst], s51
XL 61,64
XS 172
XS 173
M 6,19,1
LA 16
RA 232,1,23552
XS 174
XS 175
M 7,30,0
LC_ 108
XL 62,128
v_add_u32 v4, s39, v4
AW 68,172
AW 69,173
AW 70,174
M 7,31,1
RA 236,1,24576
s_nop 1
v_add_u32 v1, s37, v0
AW 71,175
v_mul_f32 v36, 0xbb2aaaab, v36
v_mul_f32 v37, 0xbb2aaaab, v37
v_mul_f32 v38, 0xbb2aaaab, v38
v_mul_f32 v39, 0xbb2aaaab, v39
s_nop 1
LK 40,240,48,0
LK 44,242,48,2
RA 240,7,1024
LK 48,244,49,0
s_nop 1
CF 128,40,41
LK 248,246,49,2
RA 244,7,1280
CF 129,42,43
CF 130,44,45
CF 131,46,47
LK 40,248,50,0
CF 132,48,49
LK 44,250,50,2
RA 248,7,1536
CF 133,50,51
GZ 0
CF 134,248,249
CF 135,250,251
LK 48,252,51,0
CF 136,40,41
LK 248,254,51,2
RA 252,7,1792
CF 137,42,43
GS 1
WL 8
CF 138,44,45
CF 139,46,47
M 6,20,2
LO 105,18,105
LU 18,105
M 6,21,3
WL 3
RA 216,1,0
XL 63,192
HS 24,18
LK 40,240,52,0
CF 140,48,49
LK 44,242,52,2
RA 240,7,2048
CF 141,50,51
GS 2
WL 4
CF 142,248,249
CF 143,250,251
LK 48,244,53,0
CF 144,40,41
LK 248,246,53,2
RA 244,7,2304
CF 145,42,43
GS 3
WL 4
CF 146,44,45
CF 147,46,47
LK 40,248,54,0
CF 148,48,49
LK 44,250,54,2
RA 248,7,2560
CF 149,50,51
GS 4
CF 150,248,249
CF 151,250,251
M 7,32,2
LL 17,62
LL 109,63
M 7,33,3
WL 4
LA 17
XL 64,256
LK 48,252,55,0
CF 152,40,41
LK 248,254,55,2
s_lshl_b32 s53, s52, 8
RA 252,7,2816
CF 153,42,43
GS 5
s_add_u32 s48, %[alo], s53
CF 154,44,45
CF 155,46,47
M 6,22,4
s_addc_u32 s49, %[ahi], 0
LC_ 109
v_and_b32 v22, 0xfffffcff, v1
M 6,23,5
s_add_u32 s52, s52, %[d]
RA 220,1,1024
M 7,34,4
LO 108,16,108
LU 16,108
s_cmp_eq_u32 s52, %[wrap]
s_cselect_b32 s52, %[rst], s52
XL 65,320
M 7,35,5
s_barrier
RV 252,22,5120
HS 24,16
WL 0
Z 0,12,0
s_sub_u32 s39, 0, s39
RA 224,1,2048
LL 18,80
LL 106,81
M 0,13,1
s_mul_i32 s53, s50, 25600
LA 18
RA 228,1,3072
LK 40,240,56,0
CF 156,48,49
LK 44,242,56,2
s_add_u32 s40, %[wlo], s53
CF 157,50,51
GS 6
s_addc_u32 s41, %[whi], 0
CF 158,248,249
CF 159,250,251
LK 48,244,57,0
CF 160,40,41
LK 248,246,57,2
s_mov_b32 m0, s38
CF 161,42,43
GS 7
CF 162,44,45
CF 163,46,47
Z 1,24,0
LC_ 106
RA 232,1,4096
M 1,25,1
RA 236,1,6144
ds_read_b64 v[14:15], v5 offset:0
LK 40,248,58,0
CF 164,48,49
LK 44,250,58,2
CF 165,50,51
GS 8
WL 6
CF 166,248,249
CF 167,250,251
M 0,14,2
WL 4
LO 109,17,109
LU 17,109
M 0,15,3
RA 216,1,7168
HS 24,17
LK 48,252,59,0
CF 168,40,41
LK 248,254,59,2
CF 169,42,43
v_subrev_u32 v5, s39, v5
GS 9
CF 170,44,45
CF 171,46,47
M 1,26,2
DW 0
LL 16,82
LL 107,83
M 1,27,3
WL 3
LA 16
RA 220,1,8192
M 0,16,4
WL 4
LC_ 107
DW 1024
M 0,17,5
RA 224,1,9216
RV 36,8,6912
HX 24
GS 10
CF 172,48,49
CF 173,50,51
M 1,28,4
LO 18,18,106
LU 106,18
W1 24,0
M 1,29,5
WL 4
RA 228,1,10240
HZ 25,106
v_add_f32 v14, v14, v15
s_nop 1
v_add_f32_dpp v14, v14, v14 quad_perm:[1,0,3,2] row_mask:0xf bank_mask:0xf
s_nop 1
v_add_f32_dpp v14, v14, v14 quad_perm:[2,3,0,1] row_mask:0xf bank_mask:0xf
s_nop 1
v_add_f32_dpp v14, v14, v14 row_half_mirror row_mask:0xf bank_mask:0xf
v_cvt_f64_f32 v[14:15], v14
s_mov_b64 exec, s[54:55]
SP
s_mov_b64 exec, -1
DW 2048
M 0,18,0
s_add_u32 s40, s40, 3072
PK 106,18,104,33
PK 104,18,104,32
LL 17,84
LL 110,85
M 0,19,1
s_addc_u32 s41, s41, 0
LA 17
RA 232,1,11264
M 1,30,0
s_add_u32 m0, s38, 3072
LC_ 110
RV 252,22,5392
M 1,31,1
WL 7
RA 236,1,12288
CF 174,248,249
CF 175,250,251
M 0,20,2
WL 5
LO 16,16,107
LU 107,16
M 0,21,3
RA 216,1,13312
DW 0
HS 25,107
GS 11
WL 4
DW 1024
s_nop 5
v_cndmask_b32 v52, v52, v53, s[78:79]
v_cndmask_b32 v54, v54, v55, s[78:79]
v_cndmask_b32 v52, v52, v54, s[80:81]
M 1,32,2
s_add_u32 s42, s44, s60
PK 107,16,105,33
PK 105,16,105,32
LL 18,86
LL 111,87
v_mul_f32 v52, 0x3b2aaaab, v52
M 1,33,3
s_addc_u32 s43, s45, 0
LA 18
RA 220,1,14336
v_med3_f32 v53, v52, v39, v37
M 0,22,4
WL 3
LC_ 111
ST 104
LG 52,53
s_nop 0
C 52,52,52
)"
R"(M 0,23,5
RA 224,1,15360
v_lshlrev_b32 v52, 16, v52
M 1,34,4
DW 2048
LO 17,17,110
LU 110,17
v_add_f32 v52, v36, v52
M 1,35,5
WL 4
RA 228,1,16384
HS 25,110
C 52,52,52
v_lshlrev_b32 v52, 16, v52
Z 6,12,0
WL 3
PK 110,17,108,33
PK 108,17,108,32
LL 16,56
LL 104,57
v_sqrt_f32 v52, v52
M 6,13,1
WL 1
LA 16
DQ 3072
PO 110
C 52,52,52
Z 7,24,0
s_add_u32 s50, s50, %[d]
LC_ 104
RA 232,1,17408
v_lshlrev_b32 v52, 16, v52
v_rcp_f32 v52, v52
M 7,25,1
s_cmp_eq_u32 s50, %[wrap]
RA 236,1,18432
s_cselect_b32 s50, %[rst], s50
v_mul_f32 v52, v38, v52
M 6,14,2
WL 4
LO 18,18,111
LU 111,18
CF 52,52,52
M 6,15,3
s_add_u32 s38, s38, 32768
RA 216,1,19456
HS 25,111
ds_bpermute_b32 v13, v28, v52
M 7,26,2
s_cmp_eq_u32 s38, s77
PK 111,18,109,33
PK 109,18,109,32
LL 17,58
LL 105,59
s_cselect_b32 s38, s76, s38
M 7,27,3
s_add_u32 s37, s37, 32768
LA 17
PO 111
s_cmp_eq_u32 s37, s75
s_cselect_b32 s37, s74, s37
WL 4
M 6,16,4
WL 3
LC_ 105
RA 220,1,20480
XL 66,384
M 6,17,5
s_add_u32 s42, s44, s61
RA 224,1,21504
HX 25
s_addc_u32 s43, s45, 0
M 7,28,4
LO 104,16,104
LU 16,104
W1 25,1024
M 7,29,5
WL 2
RA 228,1,22528
HZ 24,16
ST 108
XL 67,448
M 6,18,0
s_mul_i32 s53, s51, 24
LL 18,60
LL 108,61
s_bitcmp1_b32 s51, 0
s_addc_u32 s53, s53, 0
s_lshl_b32 s53, s53, 11
s_bitcmp1_b64 s[90:91], s51
s_cselect_b64 s[44:45], s[84:85], s[82:83]
s_add_u32 s44, s44, s53
s_addc_u32 s45, s45, 0
s_add_u32 s51, s51, %[d]
s_cmp_eq_u32 s51, %[wrap]
s_cselect_b32 s51, %[rst], s51
M 6,19,1
LA 18
RA 232,1,23552
M 7,30,0
LC_ 108
XL 68,512
v_add_u32 v4, s39, v4
XS 128
M 7,31,1
WL 5
RA 236,1,24576
s_nop 1
v_add_u32 v1, s37, v0
XS 129
XS 130
M 6,20,2
WL 4
XL 69,576
LO 105,17,105
LU 17,105
XS 131
M 6,21,3
RA 216,1,0
HS 24,17
AW 72,128
AW 73,129
M 7,32,2
LL 16,62
LL 109,63
AW 74,130
AW 75,131
M 7,33,3
WL 1
LA 16
XL 70,640
XS 132
XS 133
M 6,22,4
s_lshl_b32 s53, s52, 8
LC_ 109
v_and_b32 v22, 0xfffffcff, v1
XS 134
M 6,23,5
s_add_u32 s48, %[alo], s53
RA 220,1,1024
XS 135
AW 76,132
M 7,34,4
WV 22
LO 108,18,108
LU 18,108
s_addc_u32 s49, %[ahi], 0
s_add_u32 s52, s52, %[d]
s_cmp_eq_u32 s52, %[wrap]
s_cselect_b32 s52, %[rst], s52
XL 71,704
AW 77,133
AW 78,134
M 7,35,5
s_barrier
RV 252,22,5120
HS 24,18
AW 79,135
v_min_u32 v35, v35, v13
WL 0
Z 0,12,0
s_sub_u32 s39, 0, s39
LL 17,80
LL 106,81
XS 136
XS 137
M 0,13,1
s_mul_i32 s53, s50, 25600
LA 17
RA 224,1,2048
XS 138
Z 1,24,0
s_add_u32 s40, %[wlo], s53
LC_ 106
RA 228,1,3072
XS 139
AW 80,136
M 1,25,1
s_addc_u32 s41, %[whi], 0
RA 232,1,4096
AW 81,137
AW 82,138
Z 2,36,0
LO 109,16,109
LU 16,109
AW 83,139
XS 140
M 2,37,1
s_mov_b32 m0, s38
ds_read_b64 v[14:15], v5 offset:0
HS 24,16
XS 141
Z 3,48,0
WL 2
LL 18,82
LL 107,83
XS 142
XS 143
M 3,49,1
LA 18
RA 236,1,6144
AW 84,140
AW 85,141
M 0,14,2
LC_ 107
DW 0
AW 86,142
AW 87,143
M 0,15,3
v_subrev_u32 v5, s39, v5
HX 24
XS 144
XS 145
M 1,26,2
LO 17,17,106
LU 106,17
XS 146
M 1,27,3
RA 216,1,7168
HZ 25,106
XS 147
AW 88,144
M 2,38,2
PK 106,17,104,33
PK 104,17,104,32
LL 16,84
LL 110,85
AW 89,145
AW 90,146
M 2,39,3
LA 16
W1 24,0
AW 91,147
XS 148
M 3,50,2
WL 4
LC_ 110
DW 1024
XS 149
M 3,51,3
RA 220,1,8192
v_add_f32 v14, v14, v15
XS 150
XS 151
M 0,16,4
WL 5
LO 18,18,107
LU 107,18
AW 92,148
AW 93,149
M 0,17,5
DW 2048
HS 25,107
v_add_f32_dpp v14, v14, v14 quad_perm:[1,0,3,2] row_mask:0xf bank_mask:0xf
AW 94,150
AW 95,151
M 1,28,4
s_add_u32 s42, s44, s60
PK 107,18,105,33
PK 105,18,105,32
LL 17,86
LL 111,87
XS 152
M 1,29,5
s_addc_u32 s43, s45, 0
LA 17
RA 224,1,9216
v_add_f32_dpp v14, v14, v14 quad_perm:[2,3,0,1] row_mask:0xf bank_mask:0xf
XS 153
XS 154
M 2,40,4
LC_ 111
ST 104
XS 155
AW 96,152
M 2,41,5
WV 26
BW 60
v_add_f32_dpp v14, v14, v14 row_half_mirror row_mask:0xf bank_mask:0xf
AW 97,153
AW 98,154
M 3,52,4
WL 4
LO 16,16,110
LU 110,16
AW 99,155
XS 156
M 3,53,5
s_add_u32 s40, s40, 3072
RA 228,1,10240
HS 25,110
v_cvt_f64_f32 v[14:15], v14
XS 157
M 0,18,0
s_addc_u32 s41, s41, 0
PK 110,16,108,33
PK 108,16,108,32
XS 158
XS 159
M 0,19,1
s_add_u32 m0, s38, 3072
s_mov_b64 exec, s[54:55]
SP
s_mov_b64 exec, -1
PO 110
AW 100,156
AW 101,157
M 1,30,0
RV 40,8,6144
RV 44,8,6208
AW 102,158
AW 103,159
M 1,31,1
RA 232,1,11264
RV 48,8,6272
XS 160
M 2,42,0
WL 6
LO 17,17,111
LU 111,17
XS 161
XS 162
M 2,43,1
RV 252,22,5392
HS 25,111
XS 163
AW 104,160
M 3,54,0
WL 5
PK 111,17,109,33
PK 109,17,109,32
AW 105,161
AW 106,162
M 3,55,1
RA 236,1,12288
PO 111
AW 107,163
M 0,20,2
WV 25
DW 0
BR 60
XS 164
XS 165
M 0,21,3
s_add_u32 s42, s44, s61
BW 61
XS 166
XS 167
M 1,32,2
s_addc_u32 s43, s45, 0
ST 108
BR 61
AW 108,164
AW 109,165
M 1,33,3
RA 216,1,13312
HX 25
AW 110,166
AW 111,167
M 2,44,2
W1 25,1024
XS 168
M 2,45,3
DW 1024
v_add_u32 v4, s39, v4
XS 169
XS 170
M 3,56,2
WL 5
XS 171
AW 112,168
M 3,57,3
WL 4
RA 220,1,14336
AW 113,169
AW 114,170
LZ 40,60
WV 27
BW 62
M 0,22,4
DW 2048
RV 40,8,6336
AW 115,171
M 0,23,5
WL 5
BR 62
XS 172
XS 173
LS 44,61
WV 27
BW 63
M 1,34,4
RV 44,8,6400
BR 63
XS 174
XS 175
M 1,35,5
RA 224,1,15360
AW 116,172
)"
R"(AW 117,173
M 2,46,4
LL 18,56
LL 104,57
AW 118,174
AW 119,175
M 2,47,5
LA 18
DQ 3072
M 3,58,4
WL 8
LC_ 104
M 3,59,5
WV 27
RA 228,1,16384
Z 6,12,0
WL 7
M 6,13,1
s_add_u32 s50, s50, %[d]
LS 48,62
s_cmp_eq_u32 s50, %[wrap]
BW 64
Z 7,24,0
s_cselect_b32 s50, %[rst], s50
RV 48,8,6464
LL 16,58
LL 105,59
M 7,25,1
WL 6
LA 16
RA 232,1,17408
BR 64
LS 40,63
WV 26
BW 65
Z 8,36,0
LC_ 105
RV 40,8,6528
M 8,37,1
WV 16
BR 65
Z 9,48,0
WL 9
LO 104,18,104
LU 18,104
M 9,49,1
s_add_u32 s38, s38, 32768
RA 236,1,18432
HZ 24,18
M 6,14,2
s_cmp_eq_u32 s38, s77
LL 17,60
LL 108,61
M 6,15,3
s_cselect_b32 s38, s76, s38
LA 17
WL 6
LS 44,64
s_add_u32 s37, s37, 32768
BW 66
M 7,26,2
s_cmp_eq_u32 s37, s75
LC_ 108
RV 44,8,6592
M 7,27,3
s_cselect_b32 s37, s74, s37
RA 216,1,19456
BR 66
M 8,38,2
WL 7
LO 105,16,105
LU 16,105
M 8,39,3
s_add_u32 s53, %[xo0], 393216
HS 24,16
LS 48,65
s_add_u32 s46, %[xlo], s53
WV 14
BW 67
M 9,50,2
WL 7
LL 18,62
LL 109,63
M 9,51,3
s_addc_u32 s47, %[xhi], 0
LA 18
RA 220,1,20480
RV 48,8,6656
M 6,16,4
s_bitcmp1_b64 s[90:91], 48
LC_ 109
BR 67
M 6,17,5
s_cselect_b32 s58, 16, 0
M 7,28,4
WL 9
LO 108,17,108
LU 17,108
s_lshr_b32 s58, 0xda43cb52, s58
v_lshrrev_b32 v3, 6, v0
v_and_b32 v3, 12, v3
v_bfe_u32 v3, s58, v3, 4
v_mad_u32_u24 v3, v3, s97, v21
M 7,29,5
WL 6
RA 224,1,21504
HS 24,17
LS 40,66
WV 13
BW 68
M 8,40,4
RV 40,8,6720
LL 16,64
LL 112,65
M 8,41,5
WV 12
LA 16
XL 72,0
BR 68
M 9,52,4
WL 5
LC_ 112
M 9,53,5
s_mul_i32 s53, s51, 24
RA 228,1,22528
LS 44,67
s_bitcmp1_b32 s51, 0
BW 69
M 6,18,0
RV 44,8,6784
LO 109,18,109
LU 18,109
M 6,19,1
s_addc_u32 s53, s53, 0
BR 69
HS 24,18
s_lshl_b32 s53, s53, 11
s_bitcmp1_b64 s[90:91], s51
s_cselect_b64 s[44:45], s[84:85], s[82:83]
s_add_u32 s44, s44, s53
s_addc_u32 s45, s45, 0
s_add_u32 s51, s51, %[d]
s_cmp_eq_u32 s51, %[wrap]
s_cselect_b32 s51, %[rst], s51
M 7,30,0
LL 17,66
LL 113,67
M 7,31,1
WL 5
LA 17
RA 232,1,23552
LS 48,68
WV 12
BW 70
M 8,42,0
WV 11
LC_ 113
XL 73,64
M 8,43,1
s_lshl_b32 s53, s52, 8
RV 48,8,6848
RA 240,7,0
M 9,54,0
WL 6
LO 112,16,112
LU 16,112
M 9,55,1
s_add_u32 s48, %[alo], s53
RA 236,1,24576
BR 70
HS 24,16
LS 40,69
s_addc_u32 s49, %[ahi], 0
BW 71
M 6,20,2
s_add_u32 s52, s52, %[d]
LL 18,68
LL 116,69
s_cmp_eq_u32 s52, %[wrap]
s_cselect_b32 s52, %[rst], s52
M 6,21,3
LA 18
XL 74,128
M 7,32,2
LC_ 116
XL 75,192
RA 244,7,256
M 7,33,3
BR 71
v_add_u32 v1, s37, v0
M 8,44,2
WL 9
LO 113,17,113
LU 17,113
M 8,45,3
WL 5
RA 216,1,0
HS 24,17
LS 44,70
RA 248,7,512
v_and_b32 v22, 0xfffffcff, v1
M 9,56,2
LL 16,70
LL 117,71
M 9,57,3
WL 2
LA 16
RA 220,1,1024
LS 48,71
RA 252,7,768
M 6,22,4
LC_ 117
RV 252,22,5120
M 6,23,5
XL 76,256
s_nop 1
v_mul_f32 v36, 0xbb2aaaab, v36
v_mul_f32 v37, 0xbb2aaaab, v37
M 7,34,4
LO 116,18,116
LU 18,116
v_mul_f32 v38, 0xbb2aaaab, v38
M 7,35,5
s_barrier
RA 224,1,2048
HS 24,18
v_mul_f32 v39, 0xbb2aaaab, v39
s_nop 1
LK 40,240,60,0
LK 44,242,60,2
s_sub_u32 s39, 0, s39
RA 240,7,1024
LK 48,244,61,0
s_nop 0
CF 128,40,41
LK 248,246,61,2
RA 244,7,1280
CF 129,42,43
M 8,46,4
LL 17,80
LL 106,81
M 8,47,5
LA 17
ds_read_b64 v[14:15], v5 offset:0
M 9,58,4
WL 8
LC_ 106
XL 77,320
M 9,59,5
WL 6
RA 228,1,3072
v_subrev_u32 v5, s39, v5
Z 0,12,0
LO 117,16,117
LU 16,117
CF 130,44,45
M 0,13,1
s_mul_i32 s53, s50, 25600
HS 24,16
CF 131,46,47
LK 40,248,62,0
CF 132,48,49
LK 44,250,62,2
s_add_u32 s40, %[wlo], s53
RA 248,7,1536
CF 133,50,51
GZ 0
s_addc_u32 s41, %[whi], 0
CF 134,248,249
CF 135,250,251
LK 48,252,63,0
CF 136,40,41
LK 248,254,63,2
s_mov_b32 m0, s38
RA 252,7,1792
CF 137,42,43
GS 1
WL 7
CF 138,44,45
CF 139,46,47
LK 40,240,64,0
CF 140,48,49
LK 44,242,64,2
RA 240,7,2048
CF 141,50,51
GS 2
CF 142,248,249
CF 143,250,251
Z 1,24,0
LL 18,82
LL 107,83
M 1,25,1
LA 18
RA 232,1,4096
Z 2,36,0
LC_ 107
HX 24
M 2,37,1
WL 10
W1 24,0
LK 48,244,65,0
CF 144,40,41
LK 248,246,65,2
RA 244,7,2304
CF 145,42,43
GS 3
CF 146,44,45
CF 147,46,47
Z 3,48,0
WL 8
LO 17,17,106
LU 106,17
M 3,49,1
RA 236,1,6144
HZ 25,106
M 0,14,2
LL 16,84
LL 110,85
M 0,15,3
WL 8
LA 16
PK 106,17,104,33
LK 40,248,66,0
CF 148,48,49
LK 44,250,66,2
RA 248,7,2560
CF 149,50,51
GS 4
CF 150,248,249
CF 151,250,251
M 1,26,2
LC_ 110
PK 104,17,104,32
M 1,27,3
RA 216,1,7168
v_add_f32 v14, v14, v15
M 2,38,2
WL 7
LO 18,18,107
LU 107,18
v_add_f32_dpp v14, v14, v14 quad_perm:[1,0,3,2] row_mask:0xf bank_mask:0xf
M 2,39,3
DW 0
HS 25,107
LK 48,252,67,0
CF 152,40,41
LK 248,254,67,2
s_add_u32 s42, s44, s60
RA 252,7,2816
CF 153,42,43
GS 5
s_addc_u32 s43, s45, 0
CF 154,44,45
CF 155,46,47
M 3,50,2
WL 5
LL 17,86
LL 111,87
v_add_f32_dpp v14, v14, v14 quad_perm:[2,3,0,1] row_mask:0xf bank_mask:0xf
M 3,51,3
LA 17
PK 107,18,105,33
M 0,16,4
LC_ 111
PK 105,18,105,32
v_add_f32_dpp v14, v14, v14 row_half_mirror row_mask:0xf bank_mask:0xf
M 0,17,5
RA 220,1,8192
v_cvt_f64_f32 v[14:15], v14
M 1,28,4
WL 5
LO 16,16,110
LU 110,16
M 1,29,5
RA 224,1,9216
DW 1024
HS 25,110
LK 40,240,68,0
CF 156,48,49
LK 44,242,68,2
CF 157,50,51
GS 6
CF 158,248,249
CF 159,250,251
M 2,40,4
LL 18,88
LL 114,89
s_mov_b64 exec, s[54:55]
SP
s_mov_b64 exec, -1
M 2,41,5
LA 18
PK 110,16,108,33
M 3,52,4
WL 2
LC_ 114
PK 108,16,108,32
M 3,53,5
RA 228,1,10240
PO 110
)"
R"(LK 48,244,69,0
CF 160,40,41
LK 248,246,69,2
CF 161,42,43
GS 7
CF 162,44,45
CF 163,46,47
M 0,18,0
LO 17,17,111
LU 111,17
ST 104
DW 2048
M 0,19,1
s_add_u32 s40, s40, 3072
HS 25,111
M 1,30,0
s_addc_u32 s41, s41, 0
LL 16,90
LL 115,91
M 1,31,1
s_add_u32 m0, s38, 3072
LA 16
PK 111,17,109,33
LK 40,248,70,0
CF 164,48,49
LK 44,250,70,2
s_add_u32 s42, s44, s61
CF 165,50,51
GS 8
s_addc_u32 s43, s45, 0
CF 166,248,249
CF 167,250,251
M 2,42,0
LC_ 115
PK 109,17,109,32
M 2,43,1
RA 232,1,11264
PO 111
M 3,54,0
WL 3
LO 18,18,114
LU 114,18
M 3,55,1
RV 252,22,5392
HS 25,114
LK 48,252,71,0
CF 168,40,41
LK 248,254,71,2
CF 169,42,43
GS 9
CF 170,44,45
CF 171,46,47
M 0,20,2
LL 17,92
LL 118,93
M 0,21,3
LA 17
PK 114,18,112,33
M 1,32,2
LC_ 118
PK 112,18,112,32
M 1,33,3
RA 236,1,12288
RV 36,8,6912
GS 10
RA 216,1,13312
M 2,44,2
WL 7
DW 0
LO 16,16,115
LU 115,16
M 2,45,3
ST 108
HS 25,115
M 3,56,2
WL 2
LL 18,94
LL 119,95
M 3,57,3
LA 18
PK 115,16,113,33
M 0,22,4
LC_ 119
PK 113,16,113,32
M 0,23,5
RA 220,1,14336
DW 1024
M 1,34,4
LO 17,17,118
LU 118,17
M 1,35,5
s_add_u32 s42, s44, s62
RA 224,1,15360
HS 25,118
s_addc_u32 s43, s45, 0
M 2,46,4
LL 16,56
LL 104,57
M 2,47,5
LA 16
PK 118,17,116,33
M 3,58,4
WL 2
LC_ 104
PK 116,17,116,32
M 3,59,5
RA 228,1,16384
DW 2048
PO 118
CF 172,48,49
Z 6,12,0
LO 18,18,119
LU 119,18
ST 112
CF 173,50,51
M 6,13,1
DQ 3072
HS 25,119
CF 174,248,249
CF 175,250,251
Z 7,24,0
s_add_u32 s42, s44, s63
LL 17,58
LL 105,59
M 7,25,1
s_addc_u32 s43, s45, 0
LA 17
PK 119,18,117,33
GS 11
RA 232,1,17408
HX 25
s_nop 5
v_cndmask_b32 v52, v52, v53, s[78:79]
v_cndmask_b32 v54, v54, v55, s[78:79]
Z 8,36,0
s_add_u32 s50, s50, %[d]
LC_ 105
PK 117,18,117,32
M 8,37,1
s_cmp_eq_u32 s50, %[wrap]
W1 25,1024
PO 119
v_cndmask_b32 v52, v52, v54, s[80:81]
Z 9,48,0
WL 4
LO 104,16,104
LU 16,104
v_mul_f32 v52, 0x3b2aaaab, v52
M 9,49,1
s_cselect_b32 s50, %[rst], s50
RA 236,1,18432
HZ 24,16
v_med3_f32 v53, v52, v39, v37
M 6,14,2
LL 18,60
LL 108,61
LG 52,53
M 6,15,3
LA 18
ST 116
C 52,52,52
M 7,26,2
s_add_u32 s37, s37, 32768
LC_ 108
v_add_u32 v4, s39, v4
v_lshlrev_b32 v52, 16, v52
M 7,27,3
s_cmp_eq_u32 s37, s75
RA 216,1,19456
v_add_f32 v52, v36, v52
M 8,38,2
WL 5
LO 105,17,105
LU 17,105
C 52,52,52
M 8,39,3
s_cselect_b32 s37, s74, s37
HS 24,17
v_lshlrev_b32 v52, 16, v52
M 9,50,2
WL 3
LL 16,62
LL 109,63
v_sqrt_f32 v52, v52
M 9,51,3
s_add_u32 s38, s38, 32768
LA 16
RA 220,1,20480
C 52,52,52
M 6,16,4
s_cmp_eq_u32 s38, s77
LC_ 109
s_cselect_b32 s38, s76, s38
v_lshlrev_b32 v52, 16, v52
M 6,17,5
XL 78,384
v_rcp_f32 v52, v52
M 7,28,4
WL 4
LO 108,18,108
LU 18,108
v_mul_f32 v52, v38, v52
M 7,29,5
s_mul_i32 s53, s51, 24
RA 224,1,21504
HS 24,18
CF 52,52,52
M 8,40,4
s_bitcmp1_b32 s51, 0
LL 17,64
LL 112,65
M 8,41,5
s_addc_u32 s53, s53, 0
LA 17
ds_bpermute_b32 v13, v28, v52
M 9,52,4
WL 4
LC_ 112
M 9,53,5
s_lshl_b32 s53, s53, 11
RA 228,1,22528
M 6,18,0
WL 5
LO 109,16,109
LU 16,109
M 6,19,1
s_bitcmp1_b64 s[90:91], s51
HS 24,16
s_cselect_b64 s[44:45], s[84:85], s[82:83]
s_add_u32 s44, s44, s53
s_addc_u32 s45, s45, 0
s_add_u32 s51, s51, %[d]
s_cmp_eq_u32 s51, %[wrap]
s_cselect_b32 s51, %[rst], s51
M 7,30,0
LL 18,66
LL 113,67
M 7,31,1
WV 18
LA 18
RA 232,1,23552
XL 79,448
M 8,42,0
s_lshl_b32 s53, s52, 8
LC_ 113
M 8,43,1
s_add_u32 s48, %[alo], s53
BW 72
M 9,54,0
WL 4
LO 112,17,112
LU 17,112
M 9,55,1
s_addc_u32 s49, %[ahi], 0
RA 236,1,24576
RV 40,8,6144
HS 24,17
XS 128
M 6,20,2
s_add_u32 s52, s52, %[d]
LL 16,68
LL 116,69
s_cmp_eq_u32 s52, %[wrap]
s_cselect_b32 s52, %[rst], s52
RV 44,8,6208
XS 129
M 6,21,3
LA 16
XL 80,512
XS 130
M 7,32,2
LC_ 116
XL 81,576
RV 48,8,6272
XS 131
M 7,33,3
BR 72
v_add_u32 v1, s37, v0
AW 120,128
M 8,44,2
WL 8
LO 113,18,113
LU 18,113
AW 121,129
M 8,45,3
WV 20
RA 216,1,0
HS 24,18
AW 122,130
BW 73
M 9,56,2
WL 8
BR 73
LL 17,70
LL 117,71
AW 123,131
M 9,57,3
WL 3
LA 17
XL 82,640
XS 132
XS 133
M 6,22,4
s_barrier
LC_ 117
v_and_b32 v22, 0xfffffcff, v1
XS 134
M 6,23,5
s_sub_u32 s39, 0, s39
RA 220,1,1024
XS 135
LZ 40,72
WV 20
BW 74
M 7,34,4
LO 116,16,116
LU 16,116
AW 124,132
M 7,35,5
RV 252,22,5120
RV 40,8,6336
HS 24,16
AW 125,133
M 8,46,4
BR 74
LL 18,80
LL 106,81
AW 126,134
M 8,47,5
WL 7
LA 18
RA 224,1,2048
AW 127,135
LS 44,73
WV 19
BW 75
M 9,58,4
WL 7
LC_ 106
ds_read_b64 v[14:15], v5 offset:0
v_min_u32 v35, v35, v13
M 9,59,5
WL 7
RA 228,1,3072
RV 44,8,6400
XS 136
Z 0,12,0
WV 18
LO 117,17,117
LU 17,117
XS 137
M 0,13,1
WL 7
XL 83,704
HS 24,17
XS 138
Z 1,24,0
s_mul_i32 s53, s50, 25600
BR 75
LL 16,82
LL 107,83
XS 139
M 1,25,1
s_add_u32 s40, %[wlo], s53
LA 16
RA 232,1,4096
AW 128,136
LS 48,74
s_addc_u32 s41, %[whi], 0
BW 76
Z 2,36,0
s_mov_b32 m0, s38
LC_ 107
RV 48,8,6464
AW 129,137
M 2,37,1
BR 76
v_subrev_u32 v5, s39, v5
AW 130,138
Z 3,48,0
WL 8
LO 18,18,106
LU 106,18
AW 131,139
M 3,49,1
RA 236,1,6144
HZ 25,106
XS 140
M 0,14,2
LL 17,84
LL 110,85
XS 141
M 0,15,3
WL 7
LA 17
PK 106,18,104,33
HX 24
XS 142
LS 40,75
WV 18
)"
R"(BW 77
M 1,26,2
LC_ 110
PK 104,18,104,32
W1 24,0
XS 143
M 1,27,3
RA 216,1,7168
RV 40,8,6528
AW 132,140
M 2,38,2
WL 9
DW 0
LO 16,16,107
LU 107,16
AW 133,141
M 2,39,3
WV 6
DW 1024
HS 25,107
AW 134,142
M 3,50,2
WL 6
BR 77
LL 18,86
LL 111,87
AW 135,143
M 3,51,3
s_add_u32 s42, s44, s60
LA 18
PK 107,16,105,33
v_add_f32 v14, v14, v15
s_nop 1
v_add_f32_dpp v14, v14, v14 quad_perm:[1,0,3,2] row_mask:0xf bank_mask:0xf
s_nop 1
v_add_f32_dpp v14, v14, v14 quad_perm:[2,3,0,1] row_mask:0xf bank_mask:0xf
XS 144
LS 44,76
s_addc_u32 s43, s45, 0
BW 78
M 0,16,4
LC_ 111
PK 105,16,105,32
v_add_f32_dpp v14, v14, v14 row_half_mirror row_mask:0xf bank_mask:0xf
XS 145
M 0,17,5
RA 220,1,8192
RV 44,8,6592
XS 146
M 1,28,4
WL 9
LO 17,17,110
LU 110,17
v_cvt_f64_f32 v[14:15], v14
XS 147
M 1,29,5
RA 224,1,9216
HS 25,110
AW 136,144
M 2,40,4
LL 16,88
LL 114,89
s_mov_b64 exec, s[54:55]
SP
s_mov_b64 exec, -1
DW 2048
BR 78
AW 137,145
M 2,41,5
WL 7
LA 16
PK 110,17,108,33
AW 138,146
LS 48,77
WV 8
BW 79
M 3,52,4
WL 5
LC_ 114
PK 108,17,108,32
AW 139,147
M 3,53,5
RA 228,1,10240
PO 110
XS 148
M 0,18,0
LO 18,18,111
LU 111,18
ST 104
RV 48,8,6656
XS 149
M 0,19,1
s_add_u32 s40, s40, 3072
BR 79
HS 25,111
s_addc_u32 s41, s41, 0
s_add_u32 m0, s38, 3072
XS 150
M 1,30,0
LL 17,90
LL 115,91
XS 151
M 1,31,1
WL 6
LA 17
PK 111,18,109,33
AW 140,148
LS 40,78
WV 8
BW 80
M 2,42,0
LC_ 115
PK 109,18,109,32
AW 141,149
M 2,43,1
RA 232,1,11264
DW 0
PO 111
AW 142,150
M 3,54,0
WL 6
RV 40,8,6720
LO 16,16,114
LU 114,16
AW 143,151
M 3,55,1
s_add_u32 s42, s44, s61
RV 252,22,5392
HS 25,114
XS 152
M 0,20,2
s_addc_u32 s43, s45, 0
BR 80
LL 18,92
LL 118,93
XS 153
M 0,21,3
WL 7
LA 18
DW 1024
PK 114,16,112,33
XS 154
LS 44,79
WV 9
BW 81
M 1,32,2
LC_ 118
PK 112,16,112,32
XS 155
M 1,33,3
RA 236,1,12288
RV 44,8,6784
AW 144,152
M 2,44,2
WL 9
LO 17,17,115
LU 115,17
AW 145,153
M 2,45,3
RA 216,1,13312
HS 25,115
ST 108
BR 81
AW 146,154
M 3,56,2
WL 3
DW 2048
LL 16,94
LL 119,95
AW 147,155
M 3,57,3
LA 16
PK 115,17,113,33
XS 156
LS 48,80
WV 10
BW 82
M 0,22,4
LC_ 119
PK 113,17,113,32
XS 157
M 0,23,5
WV 9
RA 220,1,14336
RV 48,8,6848
XS 158
M 1,34,4
WL 5
DQ 3072
LO 18,18,118
LU 118,18
XS 159
M 1,35,5
s_add_u32 s42, s44, s62
RA 224,1,15360
HS 25,118
s_addc_u32 s43, s45, 0
RA 240,7,0
AW 148,156
M 2,46,4
s_add_u32 s50, s50, %[d]
LL 17,56
LL 104,57
AW 149,157
M 2,47,5
s_cmp_eq_u32 s50, %[wrap]
LA 17
BR 82
PK 118,18,116,33
AW 150,158
LS 40,81
s_cselect_b32 s50, %[rst], s50
BW 83
M 3,58,4
WL 6
LC_ 104
PK 116,18,116,32
AW 151,159
M 3,59,5
RA 228,1,16384
PO 118
XS 160
Z 6,12,0
WL 3
LO 16,16,119
LU 119,16
ST 112
XS 161
M 6,13,1
s_add_u32 s38, s38, 32768
RA 244,7,256
HS 25,119
XS 162
Z 7,24,0
s_cmp_eq_u32 s38, s77
LL 18,58
LL 105,59
s_cselect_b32 s38, s76, s38
XS 163
M 7,25,1
s_mul_i32 s53, s50, 25600
LA 18
BR 83
PK 119,16,117,33
AW 152,160
LS 44,82
s_add_u32 s40, %[wlo], s53
RA 248,7,512
AW 153,161
Z 8,36,0
s_addc_u32 s41, %[whi], 0
LC_ 105
PK 117,16,117,32
M 8,37,1
s_mov_b32 m0, s38
RA 232,1,17408
PO 119
AW 154,162
Z 9,48,0
WL 3
LO 104,17,104
LU 17,104
AW 155,163
M 9,49,1
s_add_u32 s42, s44, s63
RA 236,1,18432
HZ 24,17
XS 164
LS 48,83
s_addc_u32 s43, s45, 0
RA 252,7,768
XS 165
M 6,14,2
LL 16,60
LL 108,61
M 6,15,3
LA 16
DW 0
HX 25
XS 166
M 7,26,2
LC_ 108
W1 25,1024
XS 167
M 7,27,3
RA 216,1,19456
v_add_u32 v4, s39, v4
AW 156,164
M 8,38,2
WL 7
LO 105,18,105
LU 18,105
AW 157,165
M 8,39,3
ST 116
HS 24,18
AW 158,166
M 9,50,2
WL 5
LL 17,62
LL 109,63
AW 159,167
M 9,51,3
LA 17
RA 220,1,20480
DW 1024
XS 168
XS 169
M 6,16,4
s_add_u32 s37, s37, 32768
LC_ 109
s_cmp_eq_u32 s37, s75
s_cselect_b32 s37, s74, s37
XS 170
M 6,17,5
DW 2048
XS 171
M 7,28,4
WL 5
LO 108,16,108
LU 16,108
AW 160,168
M 7,29,5
s_add_u32 s40, s40, 3072
RA 224,1,21504
HS 24,16
AW 161,169
M 8,40,4
s_addc_u32 s41, s41, 0
LL 18,64
LL 112,65
AW 162,170
M 8,41,5
s_add_u32 m0, s38, 3072
LA 18
AW 163,171
M 9,52,4
WL 3
LC_ 112
XS 172
M 9,53,5
s_mul_i32 s53, s51, 24
RA 228,1,22528
XS 173
M 6,18,0
WL 4
LO 109,17,109
LU 17,109
XS 174
M 6,19,1
s_bitcmp1_b32 s51, 0
HS 24,17
s_addc_u32 s53, s53, 0
s_lshl_b32 s53, s53, 11
s_bitcmp1_b64 s[90:91], s51
s_cselect_b64 s[44:45], s[84:85], s[82:83]
s_add_u32 s44, s44, s53
s_addc_u32 s45, s45, 0
s_add_u32 s51, s51, %[d]
s_cmp_eq_u32 s51, %[wrap]
s_cselect_b32 s51, %[rst], s51
XS 175
M 7,30,0
LL 16,66
LL 113,67
AW 164,172
M 7,31,1
LA 16
RA 232,1,23552
AW 165,173
M 8,42,0
LC_ 113
DW 0
AW 166,174
M 8,43,1
s_lshl_b32 s53, s52, 8
AW 167,175
v_mul_f32 v36, 0xbb2aaaab, v36
v_mul_f32 v37, 0xbb2aaaab, v37
v_mul_f32 v38, 0xbb2aaaab, v38
v_mul_f32 v39, 0xbb2aaaab, v39
s_nop 1
LK 40,240,72,0
LK 44,242,72,2
s_add_u32 s48, %[alo], s53
RA 240,7,1024
LK 48,244,73,0
s_nop 0
CF 128,40,41
LK 248,246,73,2
s_addc_u32 s49, %[ahi], 0
RA 244,7,1280
CF 129,42,43
CF 130,44,45
CF 131,46,47
LK 40,248,74,0
CF 132,48,49
LK 44,250,74,2
s_add_u32 s52, s52, %[d]
RA 248,7,1536
)"
R"(CF 133,50,51
GZ 0
s_cmp_eq_u32 s52, %[wrap]
CF 134,248,249
CF 135,250,251
LK 48,252,75,0
CF 136,40,41
LK 248,254,75,2
s_cselect_b32 s52, %[rst], s52
RA 252,7,1792
CF 137,42,43
GS 1
CF 138,44,45
CF 139,46,47
M 9,54,0
WL 7
LO 112,18,112
LU 18,112
M 9,55,1
WL 3
RA 236,1,24576
HS 24,18
LK 40,240,76,0
CF 140,48,49
LK 44,242,76,2
RA 240,7,2048
CF 141,50,51
GS 2
WL 4
CF 142,248,249
CF 143,250,251
LK 48,244,77,0
CF 144,40,41
LK 248,246,77,2
RA 244,7,2304
CF 145,42,43
GS 3
WL 4
CF 146,44,45
CF 147,46,47
LK 40,248,78,0
CF 148,48,49
LK 44,250,78,2
RA 248,7,2560
CF 149,50,51
GS 4
CF 150,248,249
CF 151,250,251
M 6,20,2
DW 1024
LL 17,68
LL 116,69
M 6,21,3
LA 17
DW 2048
M 7,32,2
LC_ 116
v_add_u32 v1, s37, v0
M 7,33,3
RA 216,1,0
v_and_b32 v22, 0xfffffcff, v1
M 8,44,2
LO 113,16,113
LU 16,113
M 8,45,3
WL 7
DQ 3072
HS 24,16
LK 48,252,79,0
CF 152,40,41
LK 248,254,79,2
RA 252,7,2816
CF 153,42,43
GS 5
CF 154,44,45
CF 155,46,47
M 9,56,2
WL 7
LL 18,70
LL 117,71
M 9,57,3
s_add_u32 s50, s50, %[d]
LA 18
RA 220,1,1024
M 6,22,4
s_cmp_eq_u32 s50, %[wrap]
LC_ 117
RV 252,22,5120
M 6,23,5
s_cselect_b32 s50, %[rst], s50
M 7,34,4
WL 6
LO 116,17,116
LU 17,116
M 7,35,5
s_barrier
RA 224,1,2048
HS 24,17
LK 40,240,80,0
CF 156,48,49
LK 44,242,80,2
s_sub_u32 s39, 0, s39
CF 157,50,51
GS 6
s_add_u32 s38, s38, 32768
CF 158,248,249
CF 159,250,251
M 8,46,4
s_cmp_eq_u32 s38, s77
LL 16,80
LL 106,81
M 8,47,5
s_cselect_b32 s38, s76, s38
LA 16
ds_read_b64 v[14:15], v5 offset:0
M 9,58,4
WL 5
LC_ 106
v_subrev_u32 v5, s39, v5
M 9,59,5
WL 4
RA 228,1,3072
Z 0,12,0
LO 117,18,117
LU 18,117
M 0,13,1
s_mul_i32 s53, s50, 25600
HS 24,18
LK 48,244,81,0
CF 160,40,41
LK 248,246,81,2
s_add_u32 s40, %[wlo], s53
CF 161,42,43
GS 7
s_addc_u32 s41, %[whi], 0
CF 162,44,45
CF 163,46,47
Z 1,24,0
s_mov_b32 m0, s38
LL 17,82
LL 107,83
M 1,25,1
LA 17
RA 232,1,4096
Z 2,36,0
LC_ 107
HX 24
M 2,37,1
W1 24,0
LK 40,248,82,0
CF 164,48,49
LK 44,250,82,2
CF 165,50,51
GS 8
CF 166,248,249
CF 167,250,251
Z 3,48,0
WL 4
LO 16,16,106
LU 106,16
M 3,49,1
RA 236,1,6144
HZ 25,106
M 0,14,2
LL 18,84
LL 110,85
M 0,15,3
LA 18
PK 106,16,104,33
M 1,26,2
LC_ 110
PK 104,16,104,32
M 1,27,3
RA 216,1,7168
v_add_f32 v14, v14, v15
LK 48,252,83,0
CF 168,40,41
LK 248,254,83,2
CF 169,42,43
v_add_f32_dpp v14, v14, v14 quad_perm:[1,0,3,2] row_mask:0xf bank_mask:0xf
GS 9
CF 170,44,45
CF 171,46,47
M 2,38,2
WL 5
LO 17,17,107
LU 107,17
M 2,39,3
DW 0
HS 25,107
M 3,50,2
WL 3
LL 16,86
LL 111,87
v_add_f32_dpp v14, v14, v14 quad_perm:[2,3,0,1] row_mask:0xf bank_mask:0xf
M 3,51,3
s_add_u32 s42, s44, s60
LA 16
PK 107,17,105,33
M 0,16,4
s_addc_u32 s43, s45, 0
LC_ 111
PK 105,17,105,32
v_add_f32_dpp v14, v14, v14 row_half_mirror row_mask:0xf bank_mask:0xf
M 0,17,5
RA 220,1,8192
RV 36,8,6912
GS 10
CF 172,48,49
CF 173,50,51
M 1,28,4
WL 5
LO 18,18,110
LU 110,18
v_cvt_f64_f32 v[14:15], v14
M 1,29,5
RA 224,1,9216
HS 25,110
M 2,40,4
LL 17,88
LL 114,89
s_mov_b64 exec, s[54:55]
SP
s_mov_b64 exec, -1
M 2,41,5
LA 17
PK 110,18,108,33
M 3,52,4
WL 3
LC_ 114
PK 108,18,108,32
M 3,53,5
RA 228,1,10240
PO 110
M 0,18,0
LO 16,16,111
LU 111,16
ST 104
M 0,19,1
DW 1024
HS 25,111
M 1,30,0
s_add_u32 s42, s44, s61
LL 18,90
LL 115,91
M 1,31,1
s_addc_u32 s43, s45, 0
LA 18
PK 111,16,109,33
M 2,42,0
LC_ 115
PK 109,16,109,32
M 2,43,1
RA 232,1,11264
PO 111
CF 174,248,249
M 3,54,0
WL 3
LO 17,17,114
LU 114,17
CF 175,250,251
M 3,55,1
RV 252,22,5392
HS 25,114
GS 11
RA 236,1,12288
s_nop 6
v_cndmask_b32 v52, v52, v53, s[78:79]
M 0,20,2
LL 16,92
LL 118,93
M 0,21,3
LA 16
PK 114,17,112,33
v_cndmask_b32 v54, v54, v55, s[78:79]
M 1,32,2
LC_ 118
PK 112,17,112,32
v_cndmask_b32 v52, v52, v54, s[80:81]
M 1,33,3
RA 216,1,13312
DW 2048
M 2,44,2
WL 6
LO 18,18,115
LU 115,18
v_mul_f32 v52, 0x3b2aaaab, v52
M 2,45,3
ST 108
HS 25,115
v_med3_f32 v53, v52, v39, v37
M 3,56,2
WL 3
LL 17,94
LL 119,95
M 3,57,3
s_add_u32 s42, s44, s62
LA 17
PK 115,18,113,33
LG 52,53
M 0,22,4
s_addc_u32 s43, s45, 0
LC_ 119
PK 113,18,113,32
C 52,52,52
M 0,23,5
s_add_u32 s40, s40, 3072
RA 220,1,14336
v_lshlrev_b32 v52, 16, v52
M 1,34,4
WL 4
LO 16,16,118
LU 118,16
M 1,35,5
s_addc_u32 s41, s41, 0
RA 224,1,15360
HS 25,118
v_add_f32 v52, v36, v52
M 2,46,4
s_add_u32 m0, s38, 3072
LL 18,56
LL 104,57
M 2,47,5
LA 18
PK 118,16,116,33
C 52,52,52
M 3,58,4
WL 2
LC_ 104
PK 116,16,116,32
v_lshlrev_b32 v52, 16, v52
M 3,59,5
RA 228,1,16384
PO 118
Z 6,12,0
LO 17,17,119
LU 119,17
ST 112
v_sqrt_f32 v52, v52
M 6,13,1
DW 0
HS 25,119
C 52,52,52
Z 7,24,0
s_add_u32 s42, s44, s63
LL 16,58
LL 105,59
M 7,25,1
s_addc_u32 s43, s45, 0
LA 16
PK 119,17,117,33
v_lshlrev_b32 v52, 16, v52
Z 8,36,0
LC_ 105
PK 117,17,117,32
v_rcp_f32 v52, v52
M 8,37,1
RA 232,1,17408
PO 119
Z 9,48,0
WL 3
LO 104,18,104
LU 18,104
v_mul_f32 v52, v38, v52
M 9,49,1
RA 236,1,18432
HZ 24,18
CF 52,52,52
M 6,14,2
LL 17,60
LL 108,61
M 6,15,3
LA 17
ST 116
HX 25
ds_bpermute_b32 v13, v28, v52
M 7,26,2
LC_ 108
W1 25,1024
M 7,27,3
RA 216,1,19456
v_add_u32 v4, s39, v4
M 8,38,2
WL 7
LO 105,16,105
LU 16,105
M 8,39,3
DW 1024
HS 24,16
M 9,50,2
WL 5
LL 18,62
LL 109,63
M 9,51,3
s_add_u32 s37, s37, 32768
LA 18
RA 220,1,20480
M 6,16,4
s_cmp_eq_u32 s37, s75
LC_ 109
)"
R"(s_cselect_b32 s37, s74, s37
M 6,17,5
DW 2048
M 7,28,4
WL 5
LO 108,17,108
LU 17,108
M 7,29,5
s_mul_i32 s53, s51, 24
RA 224,1,21504
HS 24,17
XS 128
M 8,40,4
s_bitcmp1_b32 s51, 0
LL 16,64
LL 112,65
XS 129
M 8,41,5
s_addc_u32 s53, s53, 0
LA 16
XS 130
M 9,52,4
WL 3
LC_ 112
XS 131
M 9,53,5
s_lshl_b32 s53, s53, 11
RA 228,1,22528
AW 168,128
M 6,18,0
WL 4
LO 109,18,109
LU 18,109
AW 169,129
M 6,19,1
s_bitcmp1_b64 s[90:91], s51
HS 24,18
s_cselect_b64 s[44:45], s[84:85], s[82:83]
s_add_u32 s44, s44, s53
s_addc_u32 s45, s45, 0
s_add_u32 s51, s51, %[d]
s_cmp_eq_u32 s51, %[wrap]
s_cselect_b32 s51, %[rst], s51
AW 170,130
M 7,30,0
s_lshl_b32 s53, s52, 8
LL 17,66
LL 113,67
AW 171,131
M 7,31,1
s_add_u32 s48, %[alo], s53
LA 17
RA 232,1,23552
XS 132
M 8,42,0
s_addc_u32 s49, %[ahi], 0
LC_ 113
XS 133
M 8,43,1
s_add_u32 s52, s52, %[d]
XS 134
XS 135
M 9,54,0
WL 3
LO 112,16,112
LU 16,112
M 9,55,1
s_cmp_eq_u32 s52, %[wrap]
RA 236,1,24576
HS 24,16
AW 172,132
M 6,20,2
s_cselect_b32 s52, %[rst], s52
DQ 3072
LL 18,68
LL 116,69
AW 173,133
M 6,21,3
LA 18
v_add_u32 v1, s37, v0
AW 174,134
M 7,32,2
s_add_u32 s50, s50, %[d]
LC_ 116
AW 175,135
M 7,33,3
s_cmp_eq_u32 s50, %[wrap]
RA 216,1,0
v_and_b32 v22, 0xfffffcff, v1
v_min_u32 v35, v35, v13
M 8,44,2
WL 4
LO 113,17,113
LU 17,113
XS 136
M 8,45,3
s_cselect_b32 s50, %[rst], s50
HS 24,17
XS 137
M 9,56,2
WL 3
LL 16,70
LL 117,71
XS 138
M 9,57,3
WV 21
LA 16
RA 220,1,1024
XS 139
M 6,22,4
s_barrier
LC_ 117
RV 252,22,5120
AW 176,136
M 6,23,5
s_sub_u32 s39, 0, s39
AW 177,137
AW 178,138
M 7,34,4
WL 5
LO 116,18,116
LU 18,116
M 7,35,5
s_add_u32 s38, s38, 32768
RA 224,1,2048
HS 24,18
AW 179,139
M 8,46,4
s_cmp_eq_u32 s38, s77
LL 17,80
LL 106,81
XS 140
M 8,47,5
s_cselect_b32 s38, s76, s38
LA 17
XS 141
M 9,58,4
WL 4
LC_ 106
XS 142
M 9,59,5
WL 3
RA 228,1,3072
XS 143
Z 0,12,0
LO 117,16,117
LU 16,117
M 0,13,1
HS 24,16
AW 180,140
Z 1,24,0
LL 18,82
LL 107,83
AW 181,141
M 1,25,1
LA 18
RA 232,1,4096
AW 182,142
Z 2,36,0
LC_ 107
HX 24
AW 183,143
M 2,37,1
W1 24,0
XS 144
Z 3,48,0
WL 5
LO 17,17,106
LU 106,17
XS 145
M 3,49,1
HZ 25,106
XS 146
Z 4,60,0
LL 16,84
LL 110,85
XS 147
M 4,61,1
LA 16
PK 106,17,104,33
AW 184,144
Z 5,72,0
WL 5
LC_ 110
PK 104,17,104,32
AW 185,145
M 5,73,1
RA 236,1,6144
AW 186,146
M 0,14,2
WL 4
LO 18,18,107
LU 107,18
AW 187,147
M 0,15,3
s_add_u32 s42, s44, s60
HS 25,107
XS 148
M 1,26,2
s_addc_u32 s43, s45, 0
LL 17,86
LL 111,87
XS 149
M 1,27,3
LA 17
PK 107,18,105,33
XS 150
M 2,38,2
LC_ 111
PK 105,18,105,32
XS 151
M 2,39,3
RA 216,1,7168
AW 188,148
M 3,50,2
WL 4
LO 16,16,110
LU 110,16
AW 189,149
M 3,51,3
ST 104
HS 25,110
AW 190,150
M 4,62,2
LL 18,88
LL 114,89
AW 191,151
M 4,63,3
LA 18
PK 110,16,108,33
XS 152
M 5,74,2
WL 4
LC_ 114
PK 108,16,108,32
XS 153
M 5,75,3
s_mov_b32 m0, %[pm]
RA 220,1,8192
PO 110
XS 154
M 0,16,4
WL 4
LO 17,17,111
LU 111,17
XS 155
M 0,17,5
DF
HS 25,111
AW 192,152
M 1,28,4
s_add_u32 s42, s44, s61
LL 16,90
LL 115,91
AW 193,153
M 1,29,5
s_addc_u32 s43, s45, 0
LA 16
PK 111,17,109,33
AW 194,154
M 2,40,4
LC_ 115
PK 109,17,109,32
AW 195,155
M 2,41,5
RA 224,1,9216
PO 111
XS 156
M 3,52,4
WL 4
LO 18,18,114
LU 114,18
XS 157
M 3,53,5
ST 108
HS 25,114
XS 158
M 4,64,4
LL 17,92
LL 118,93
XS 159
M 4,65,5
LA 17
PK 114,18,112,33
AW 196,156
M 5,76,4
WL 4
LC_ 118
PK 112,18,112,32
AW 197,157
M 5,77,5
RA 228,1,10240
AW 198,158
M 0,18,0
WL 4
LO 16,16,115
LU 115,16
AW 199,159
M 0,19,1
s_add_u32 s42, s44, s62
HS 25,115
XS 160
M 1,30,0
s_addc_u32 s43, s45, 0
LL 18,94
LL 119,95
XS 161
M 1,31,1
LA 18
PK 115,16,113,33
XS 162
M 2,42,0
LC_ 119
PK 113,16,113,32
XS 163
M 2,43,1
RA 232,1,11264
AW 200,160
M 3,54,0
WL 4
LO 17,17,118
LU 118,17
AW 201,161
M 3,55,1
RV 252,22,5392
HS 25,118
AW 202,162
M 4,66,0
ST 112
AW 203,163
M 4,67,1
RA 236,1,12288
PK 118,17,116,33
XS 164
M 5,78,0
WL 5
PK 116,17,116,32
XS 165
M 5,79,1
PO 118
XS 166
M 0,20,2
WL 3
LO 18,18,119
LU 119,18
XS 167
M 0,21,3
s_add_u32 s42, s44, s63
HS 25,119
AW 204,164
M 1,32,2
s_addc_u32 s43, s45, 0
HX 25
AW 205,165
M 1,33,3
RA 216,1,13312
PK 119,18,117,33
AW 206,166
M 2,44,2
W1 25,1024
PK 117,18,117,32
AW 207,167
M 2,45,3
PO 119
v_add_u32 v4, s39, v4
XS 168
M 3,56,2
ST 116
XS 169
M 3,57,3
XS 170
XS 171
M 4,68,2
AW 208,168
AW 209,169
M 4,69,3
RA 220,1,14336
AW 210,170
M 5,80,2
WL 3
AW 211,171
XS 172
M 5,81,3
XS 173
XS 174
M 0,22,4
XS 175
AW 212,172
M 0,23,5
AW 213,173
AW 214,174
M 1,34,4
AW 215,175
M 1,35,5
RA 224,1,15360
M 2,46,4
s_nop 0
LL 16,56
LL 104,57
M 2,47,5
)"
R"(LA 16
ds_read_b64 v[14:15], v5 offset:0
M 3,58,4
LC_ 104
v_subrev_u32 v5, s39, v5
M 3,59,5
M 4,70,4
M 4,71,5
RA 228,1,16384
M 5,82,4
WL 5
LL 17,58
LL 105,59
M 5,83,5
LA 17
Z 6,12,0
LC_ 105
M 6,13,1
Z 7,24,0
WL 3
LO 104,16,104
LU 16,104
M 7,25,1
RA 232,1,17408
HZ 24,16
Z 8,36,0
LL 18,60
LL 108,61
M 8,37,1
s_mul_i32 s53, s50, 25600
LA 18
v_add_f32 v14, v14, v15
Z 9,48,0
s_add_u32 s40, %[wlo], s53
LC_ 108
v_add_f32_dpp v14, v14, v14 quad_perm:[1,0,3,2] row_mask:0xf bank_mask:0xf
M 9,49,1
s_addc_u32 s41, %[whi], 0
v_add_f32_dpp v14, v14, v14 quad_perm:[2,3,0,1] row_mask:0xf bank_mask:0xf
Z 10,60,0
WL 3
LO 105,17,105
LU 17,105
M 10,61,1
s_mov_b32 m0, s38
RA 236,1,18432
HS 24,17
Z 11,72,0
LL 16,62
LL 109,63
M 11,73,1
LA 16
DW 0
M 6,14,2
LC_ 109
v_add_f32_dpp v14, v14, v14 row_half_mirror row_mask:0xf bank_mask:0xf
M 6,15,3
v_cvt_f64_f32 v[14:15], v14
M 7,26,2
WL 3
LO 108,18,108
LU 18,108
M 7,27,3
s_add_u32 s37, s37, 32768
RA 216,1,19456
HS 24,18
M 8,38,2
s_cmp_eq_u32 s37, s75
LL 17,64
LL 112,65
M 8,39,3
s_cselect_b32 s37, s74, s37
LA 17
s_mov_b64 exec, s[54:55]
SP
s_mov_b64 exec, -1
M 9,50,2
LC_ 112
DW 1024
M 9,51,3
M 10,62,2
WL 3
LO 109,16,109
LU 16,109
M 10,63,3
RA 220,1,20480
HS 24,16
M 11,74,2
LL 18,66
LL 113,67
M 11,75,3
LA 18
DW 2048
M 6,16,4
LC_ 113
M 6,17,5
M 7,28,4
WL 3
LO 112,17,112
LU 17,112
M 7,29,5
s_mul_i32 s53, s51, 24
RA 224,1,21504
HS 24,17
M 8,40,4
s_bitcmp1_b32 s51, 0
LL 16,68
LL 116,69
M 8,41,5
s_addc_u32 s53, s53, 0
LA 16
M 9,52,4
s_lshl_b32 s53, s53, 11
LC_ 116
M 9,53,5
s_bitcmp1_b64 s[90:91], s51
M 10,64,4
WL 3
LO 113,18,113
LU 18,113
M 10,65,5
s_cselect_b64 s[44:45], s[84:85], s[82:83]
RA 228,1,22528
HS 24,18
M 11,76,4
LL 17,70
LL 117,71
M 11,77,5
s_add_u32 s44, s44, s53
LA 17
M 6,18,0
s_addc_u32 s45, s45, 0
LC_ 117
M 6,19,1
s_add_u32 s51, s51, %[d]
M 7,30,0
WL 3
LO 116,16,116
LU 16,116
M 7,31,1
s_cmp_eq_u32 s51, %[wrap]
RA 232,1,23552
HS 24,16
M 8,42,0
s_cselect_b32 s51, %[rst], s51
LL 18,72
LL 120,73
M 8,43,1
s_add_u32 s40, s40, 3072
LA 18
M 9,54,0
s_addc_u32 s41, s41, 0
LC_ 120
s_add_u32 m0, s38, 3072
M 9,55,1
DW 0
M 10,66,0
WL 3
LO 117,17,117
LU 17,117
M 10,67,1
RA 236,1,24576
HS 24,17
M 11,78,0
LL 16,74
LL 121,75
M 11,79,1
LA 16
DW 1024
M 6,20,2
LC_ 121
DW 2048
M 6,21,3
s_lshl_b32 s53, s52, 8
v_add_u32 v1, s37, v0
M 7,32,2
WL 3
LO 120,18,120
LU 18,120
M 7,33,3
s_add_u32 s48, %[alo], s53
RA 216,1,0
HS 24,18
M 8,44,2
s_addc_u32 s49, %[ahi], 0
LL 17,76
LL 124,77
M 8,45,3
s_add_u32 s52, s52, %[d]
LA 17
v_and_b32 v22, 0xfffffcff, v1
M 9,56,2
s_cmp_eq_u32 s52, %[wrap]
LC_ 124
M 9,57,3
s_cselect_b32 s52, %[rst], s52
DQ 3072
M 10,68,2
WL 3
LO 121,16,121
LU 16,121
M 10,69,3
s_add_u32 s50, s50, %[d]
RA 220,1,1024
HS 24,16
M 11,80,2
LL 18,78
LL 125,79
M 11,81,3
s_cmp_eq_u32 s50, %[wrap]
LA 18
RV 252,22,5120
M 6,22,4
s_cselect_b32 s50, %[rst], s50
LC_ 125
M 6,23,5
WV 25
M 7,34,4
WL 4
LO 124,17,124
LU 17,124
M 7,35,5
s_add_u32 s38, s38, 32768
RA 224,1,2048
HS 24,17
M 8,46,4
s_cmp_eq_u32 s38, s77
LL 16,80
LL 106,81
M 8,47,5
s_cselect_b32 s38, s76, s38
LA 16
M 9,58,4
s_barrier
LC_ 106
M 9,59,5
s_sub_u32 s39, 0, s39
M 10,70,4
WL 3
LO 125,18,125
LU 18,125
M 10,71,5
RA 228,1,3072
HS 24,18
M 11,82,4
LL 17,82
LL 107,83
M 11,83,5
LA 17
HX 24
Z 0,12,0
LC_ 107
W1 24,0
M 0,13,1
Z 1,24,0
WL 4
LO 16,16,106
LU 106,16
M 1,25,1
RA 232,1,4096
HZ 25,106
Z 2,36,0
LL 18,84
LL 110,85
M 2,37,1
LA 18
PK 106,16,104,33
Z 3,48,0
LC_ 110
PK 104,16,104,32
M 3,49,1
Z 4,60,0
WL 4
LO 17,17,107
LU 107,17
M 4,61,1
s_add_u32 s42, s44, s60
RA 236,1,6144
HS 25,107
Z 5,72,0
LL 16,86
LL 111,87
M 5,73,1
s_addc_u32 s43, s45, 0
LA 16
PK 107,17,105,33
M 0,14,2
LC_ 111
PK 105,17,105,32
M 0,15,3
ST 104
M 1,26,2
WL 3
LO 18,18,110
LU 110,18
M 1,27,3
RA 216,1,7168
HS 25,110
M 2,38,2
LL 17,88
LL 114,89
M 2,39,3
LA 17
PK 110,18,108,33
M 3,50,2
LC_ 114
PK 108,18,108,32
M 3,51,3
PO 110
M 4,62,2
WL 3
LO 16,16,111
LU 111,16
M 4,63,3
s_add_u32 s42, s44, s61
RA 220,1,8192
HS 25,111
M 5,74,2
LL 18,90
LL 115,91
M 5,75,3
s_addc_u32 s43, s45, 0
LA 18
PK 111,16,109,33
M 0,16,4
s_mov_b32 m0, %[pm]
LC_ 115
PK 109,16,109,32
M 0,17,5
DF
PO 111
M 1,28,4
WL 3
LO 17,17,114
LU 114,17
M 1,29,5
RA 224,1,9216
HS 25,114
M 2,40,4
LL 16,92
LL 118,93
M 2,41,5
LA 16
PK 114,17,112,33
M 3,52,4
LC_ 118
PK 112,17,112,32
M 3,53,5
ST 108
M 4,64,4
WL 3
LO 18,18,115
LU 115,18
M 4,65,5
s_add_u32 s42, s44, s62
RA 228,1,10240
HS 25,115
M 5,76,4
LL 17,94
LL 119,95
M 5,77,5
s_addc_u32 s43, s45, 0
LA 17
PK 115,18,113,33
M 0,18,0
LC_ 119
PK 113,18,113,32
M 0,19,1
ST 112
M 1,30,0
WL 3
LO 16,16,118
LU 118,16
M 1,31,1
RA 232,1,11264
HS 25,118
M 2,42,0
LL 18,96
LL 122,97
M 2,43,1
LA 18
PK 118,16,116,33
M 3,54,0
LC_ 122
PK 116,16,116,32
M 3,55,1
RV 252,22,5392
PO 118
M 4,66,0
WL 4
LO 17,17,119
LU 119,17
M 4,67,1
s_add_u32 s42, s44, s63
RA 236,1,12288
HS 25,119
M 5,78,0
LL 16,98
LL 123,99
M 5,79,1
)"
R"(s_addc_u32 s43, s45, 0
LA 16
PK 119,17,117,33
M 0,20,2
LC_ 123
PK 117,17,117,32
M 0,21,3
PO 119
M 1,32,2
WL 4
LO 18,18,122
LU 122,18
M 1,33,3
RA 216,1,13312
HS 25,122
M 2,44,2
LL 17,100
LL 126,101
M 2,45,3
LA 17
PK 122,18,120,33
M 3,56,2
LC_ 126
PK 120,18,120,32
M 3,57,3
ST 116
M 4,68,2
WL 3
LO 16,16,123
LU 123,16
M 4,69,3
s_add_u32 s42, s44, s64
RA 220,1,14336
HS 25,123
M 5,80,2
LL 18,102
LL 127,103
M 5,81,3
s_addc_u32 s43, s45, 0
LA 18
PK 123,16,121,33
M 0,22,4
LC_ 127
PK 121,16,121,32
M 0,23,5
ST 120
M 1,34,4
WL 3
LO 17,17,126
LU 126,17
M 1,35,5
RA 224,1,15360
HS 25,126
M 2,46,4
LL 16,56
LL 104,57
M 2,47,5
LA 16
PK 126,17,124,33
M 3,58,4
LC_ 104
PK 124,17,124,32
M 3,59,5
ds_read_b64 v[14:15], v5 offset:0
PO 126
M 4,70,4
WL 4
LO 18,18,127
LU 127,18
M 4,71,5
s_add_u32 s42, s44, s65
RA 228,1,16384
HS 25,127
M 5,82,4
LL 17,58
LL 105,59
M 5,83,5
s_addc_u32 s43, s45, 0
LA 17
PK 127,18,125,33
Z 6,12,0
LC_ 105
PK 125,18,125,32
M 6,13,1
PO 127
v_subrev_u32 v5, s39, v5
Z 7,24,0
WL 4
LO 104,16,104
LU 16,104
M 7,25,1
RA 232,1,17408
HZ 24,16
Z 8,36,0
WL 4
LL 18,60
LL 108,61
M 8,37,1
s_mul_i32 s53, s50, 25600
LA 18
v_add_f32 v14, v14, v15
Z 9,48,0
s_add_u32 s40, %[wlo], s53
LC_ 108
v_add_f32_dpp v14, v14, v14 quad_perm:[1,0,3,2] row_mask:0xf bank_mask:0xf
M 9,49,1
s_addc_u32 s41, %[whi], 0
v_add_f32_dpp v14, v14, v14 quad_perm:[2,3,0,1] row_mask:0xf bank_mask:0xf
HX 25
Z 10,60,0
WL 3
LO 105,17,105
LU 17,105
M 10,61,1
s_mov_b32 m0, s38
RA 236,1,18432
HS 24,17
Z 11,72,0
LL 16,62
LL 109,63
M 11,73,1
LA 16
W1 25,1024
M 6,14,2
LC_ 109
ST 124
M 6,15,3
DW 0
v_add_f32_dpp v14, v14, v14 row_half_mirror row_mask:0xf bank_mask:0xf
M 7,26,2
WL 4
LO 108,18,108
LU 18,108
M 7,27,3
s_add_u32 s37, s37, 32768
RA 216,1,19456
HS 24,18
M 8,38,2
s_cmp_eq_u32 s37, s75
LL 17,64
LL 112,65
M 8,39,3
s_cselect_b32 s37, s74, s37
LA 17
DW 1024
M 9,50,2
LC_ 112
v_cvt_f64_f32 v[14:15], v14
M 9,51,3
s_mov_b64 exec, s[54:55]
SP
s_mov_b64 exec, -1
v_add_u32 v4, s39, v4
M 10,62,2
WL 3
LO 109,16,109
LU 16,109
M 10,63,3
RA 220,1,20480
HS 24,16
M 11,74,2
LL 18,66
LL 113,67
M 11,75,3
LA 18
DW 2048
M 6,16,4
LC_ 113
M 6,17,5
M 7,28,4
WL 3
LO 112,17,112
LU 17,112
M 7,29,5
s_mul_i32 s53, s51, 24
RA 224,1,21504
HS 24,17
M 8,40,4
s_bitcmp1_b32 s51, 0
LL 16,68
LL 116,69
M 8,41,5
s_addc_u32 s53, s53, 0
LA 16
M 9,52,4
s_lshl_b32 s53, s53, 11
LC_ 116
M 9,53,5
s_bitcmp1_b64 s[90:91], s51
M 10,64,4
WL 3
LO 113,18,113
LU 18,113
M 10,65,5
s_cselect_b64 s[44:45], s[84:85], s[82:83]
RA 228,1,22528
HS 24,18
M 11,76,4
LL 17,70
LL 117,71
M 11,77,5
s_add_u32 s44, s44, s53
LA 17
M 6,18,0
s_addc_u32 s45, s45, 0
LC_ 117
M 6,19,1
s_add_u32 s51, s51, %[d]
M 7,30,0
WL 3
LO 116,16,116
LU 16,116
M 7,31,1
s_cmp_eq_u32 s51, %[wrap]
RA 232,1,23552
HS 24,16
M 8,42,0
s_cselect_b32 s51, %[rst], s51
LL 18,72
LL 120,73
M 8,43,1
s_add_u32 s40, s40, 3072
LA 18
M 9,54,0
s_addc_u32 s41, s41, 0
LC_ 120
s_add_u32 m0, s38, 3072
M 9,55,1
DW 0
M 10,66,0
WL 3
LO 117,17,117
LU 17,117
M 10,67,1
RA 236,1,24576
HS 24,17
M 11,78,0
LL 16,74
LL 121,75
M 11,79,1
LA 16
DW 1024
M 6,20,2
LC_ 121
DW 2048
M 6,21,3
s_lshl_b32 s53, s52, 8
v_add_u32 v1, s37, v0
M 7,32,2
WL 3
LO 120,18,120
LU 18,120
M 7,33,3
s_add_u32 s48, %[alo], s53
RA 216,1,0
HS 24,18
M 8,44,2
s_addc_u32 s49, %[ahi], 0
LL 17,76
LL 124,77
M 8,45,3
s_add_u32 s52, s52, %[d]
LA 17
v_and_b32 v22, 0xfffffcff, v1
M 9,56,2
s_cmp_eq_u32 s52, %[wrap]
LC_ 124
M 9,57,3
s_cselect_b32 s52, %[rst], s52
DQ 3072
M 10,68,2
WL 3
LO 121,16,121
LU 16,121
M 10,69,3
s_add_u32 s50, s50, %[d]
RA 220,1,1024
HS 24,16
M 11,80,2
LL 18,78
LL 125,79
M 11,81,3
s_cmp_eq_u32 s50, %[wrap]
LA 18
RV 252,22,5120
M 6,22,4
s_cselect_b32 s50, %[rst], s50
LC_ 125
M 6,23,5
WV 28
v_add_u32 v19, 1, v19
M 7,34,4
WL 4
LO 124,17,124
LU 17,124
M 7,35,5
s_add_u32 s38, s38, 32768
RA 224,1,2048
HS 24,17
M 8,46,4
s_cmp_eq_u32 s38, s77
LL 16,80
LL 106,81
M 8,47,5
s_cselect_b32 s38, s76, s38
LA 16
FP
M 9,58,4
s_sub_u32 s39, 0, s39
LC_ 106
M 9,59,5
M 10,70,4
WL 4
LO 125,18,125
LU 18,125
M 10,71,5
RA 228,1,3072
HS 24,18
M 11,82,4
LL 17,82
LL 107,83
M 11,83,5
s_mov_b32 s66, 30
1:
LA 17
HX 24
Z 0,12,0
LC_ 107
M 0,13,1
Z 1,24,0
WL 3
LO 16,16,106
LU 106,16
M 1,25,1
RA 232,1,4096
HZ 25,106
Z 2,36,0
LL 18,84
LL 110,85
M 2,37,1
LA 18
PK 106,16,104,33
Z 3,48,0
LC_ 110
PK 104,16,104,32
M 3,49,1
Z 4,60,0
WL 3
LO 17,17,107
LU 107,17
M 4,61,1
s_add_u32 s42, s44, s60
RA 236,1,6144
HS 25,107
Z 5,72,0
LL 16,86
LL 111,87
M 5,73,1
s_addc_u32 s43, s45, 0
LA 16
PK 107,17,105,33
M 0,14,2
LC_ 111
PK 105,17,105,32
M 0,15,3
ST 104
M 1,26,2
WL 3
LO 18,18,110
LU 110,18
M 1,27,3
RA 216,1,7168
HS 25,110
M 2,38,2
LL 17,88
LL 114,89
M 2,39,3
LA 17
PK 110,18,108,33
M 3,50,2
LC_ 114
PK 108,18,108,32
M 3,51,3
FR
PO 110
M 4,62,2
WL 4
LO 16,16,111
LU 111,16
M 4,63,3
s_add_u32 s42, s44, s61
RA 220,1,8192
HS 25,111
M 5,74,2
LL 18,90
LL 115,91
M 5,75,3
s_addc_u32 s43, s45, 0
LA 18
PK 111,16,109,33
)"
R"(M 0,16,4
s_mov_b32 m0, %[pm]
LC_ 115
PK 109,16,109,32
M 0,17,5
DF
PO 111
M 1,28,4
WL 4
LO 17,17,114
LU 114,17
M 1,29,5
RA 224,1,9216
HS 25,114
M 2,40,4
LL 16,92
LL 118,93
M 2,41,5
LA 16
PK 114,17,112,33
M 3,52,4
LC_ 118
PK 112,17,112,32
M 3,53,5
ST 108
M 4,64,4
WL 3
LO 18,18,115
LU 115,18
M 4,65,5
s_add_u32 s42, s44, s62
RA 228,1,10240
HS 25,115
M 5,76,4
LL 17,94
LL 119,95
M 5,77,5
s_addc_u32 s43, s45, 0
LA 17
PK 115,18,113,33
M 0,18,0
LC_ 119
PK 113,18,113,32
M 0,19,1
ST 112
M 1,30,0
WL 3
LO 16,16,118
LU 118,16
M 1,31,1
RA 232,1,11264
HS 25,118
M 2,42,0
LL 18,96
LL 122,97
M 2,43,1
LA 18
PK 118,16,116,33
v_cmp_gt_u32 vcc, v19, v15
M 3,54,0
LC_ 122
PK 116,16,116,32
M 3,55,1
RV 252,22,5392
PO 118
M 4,66,0
WL 4
LO 17,17,119
LU 119,17
M 4,67,1
s_cbranch_vccnz .Lk1s0
.Lk1r0:
RA 236,1,12288
HS 25,119
M 5,78,0
LL 16,98
LL 123,99
M 5,79,1
s_add_u32 s42, s44, s63
LA 16
PK 119,17,117,33
M 0,20,2
s_addc_u32 s43, s45, 0
LC_ 123
PK 117,17,117,32
M 0,21,3
W1 24,0
PO 119
M 1,32,2
WL 5
LO 18,18,122
LU 122,18
M 1,33,3
RA 216,1,13312
HS 25,122
M 2,44,2
LL 17,100
LL 126,101
M 2,45,3
LA 17
PK 122,18,120,33
M 3,56,2
LC_ 126
PK 120,18,120,32
M 3,57,3
ST 116
M 4,68,2
WL 4
LO 16,16,123
LU 123,16
M 4,69,3
s_add_u32 s42, s44, s64
RA 220,1,14336
HS 25,123
M 5,80,2
LL 18,102
LL 127,103
M 5,81,3
s_addc_u32 s43, s45, 0
LA 18
PK 123,16,121,33
M 0,22,4
LC_ 127
PK 121,16,121,32
M 0,23,5
ST 120
M 1,34,4
WL 3
LO 17,17,126
LU 126,17
M 1,35,5
RA 224,1,15360
HS 25,126
M 2,46,4
LL 16,56
LL 104,57
M 2,47,5
LA 16
PK 126,17,124,33
M 3,58,4
LC_ 104
PK 124,17,124,32
M 3,59,5
ds_read_b64 v[14:15], v5 offset:0
PO 126
M 4,70,4
WL 4
LO 18,18,127
LU 127,18
M 4,71,5
s_add_u32 s42, s44, s65
RA 228,1,16384
HS 25,127
M 5,82,4
LL 17,58
LL 105,59
M 5,83,5
s_addc_u32 s43, s45, 0
LA 17
PK 127,18,125,33
Z 6,12,0
LC_ 105
PK 125,18,125,32
M 6,13,1
PO 127
v_subrev_u32 v5, s39, v5
Z 7,24,0
WL 4
LO 104,16,104
LU 16,104
M 7,25,1
RA 232,1,17408
HZ 24,16
Z 8,36,0
WL 4
LL 18,60
LL 108,61
M 8,37,1
s_mul_i32 s53, s50, 25600
LA 18
v_add_f32 v14, v14, v15
Z 9,48,0
s_add_u32 s40, %[wlo], s53
LC_ 108
v_add_f32_dpp v14, v14, v14 quad_perm:[1,0,3,2] row_mask:0xf bank_mask:0xf
M 9,49,1
s_addc_u32 s41, %[whi], 0
Z 10,60,0
WL 3
LO 105,17,105
LU 17,105
M 10,61,1
s_mov_b32 m0, s38
RA 236,1,18432
HS 24,17
Z 11,72,0
LL 16,62
LL 109,63
M 11,73,1
LA 16
ST 124
M 6,14,2
LC_ 109
DW 0
M 6,15,3
v_add_f32_dpp v14, v14, v14 quad_perm:[2,3,0,1] row_mask:0xf bank_mask:0xf
HX 25
M 7,26,2
WL 3
LO 108,18,108
LU 18,108
W1 25,1024
M 7,27,3
s_add_u32 s37, s37, 32768
RA 216,1,19456
HS 24,18
M 8,38,2
s_cmp_eq_u32 s37, s75
LL 17,64
LL 112,65
M 8,39,3
s_cselect_b32 s37, s74, s37
LA 17
DW 1024
M 9,50,2
LC_ 112
v_add_f32_dpp v14, v14, v14 row_half_mirror row_mask:0xf bank_mask:0xf
M 9,51,3
v_cvt_f64_f32 v[14:15], v14
v_add_u32 v4, s39, v4
M 10,62,2
WL 4
LO 109,16,109
LU 16,109
M 10,63,3
RA 220,1,20480
HS 24,16
M 11,74,2
LL 18,66
LL 113,67
M 11,75,3
LA 18
s_mov_b64 exec, s[54:55]
SP
s_mov_b64 exec, -1
M 6,16,4
LC_ 113
DW 2048
M 6,17,5
M 7,28,4
WL 3
LO 112,17,112
LU 17,112
M 7,29,5
s_mul_i32 s53, s51, 24
RA 224,1,21504
HS 24,17
M 8,40,4
s_bitcmp1_b32 s51, 0
LL 16,68
LL 116,69
M 8,41,5
s_addc_u32 s53, s53, 0
LA 16
M 9,52,4
s_lshl_b32 s53, s53, 11
LC_ 116
M 9,53,5
s_bitcmp1_b64 s[90:91], s51
M 10,64,4
WL 3
LO 113,18,113
LU 18,113
M 10,65,5
s_cselect_b64 s[44:45], s[84:85], s[82:83]
RA 228,1,22528
HS 24,18
M 11,76,4
LL 17,70
LL 117,71
M 11,77,5
s_add_u32 s44, s44, s53
LA 17
M 6,18,0
s_addc_u32 s45, s45, 0
LC_ 117
M 6,19,1
s_add_u32 s51, s51, %[d]
M 7,30,0
WL 3
LO 116,16,116
LU 16,116
M 7,31,1
s_cmp_eq_u32 s51, %[wrap]
RA 232,1,23552
HS 24,16
M 8,42,0
s_cselect_b32 s51, %[rst], s51
LL 18,72
LL 120,73
M 8,43,1
s_add_u32 s40, s40, 3072
LA 18
M 9,54,0
s_addc_u32 s41, s41, 0
LC_ 120
s_add_u32 m0, s38, 3072
M 9,55,1
DW 0
M 10,66,0
WL 3
LO 117,17,117
LU 17,117
M 10,67,1
RA 236,1,24576
HS 24,17
M 11,78,0
LL 16,74
LL 121,75
M 11,79,1
LA 16
DW 1024
M 6,20,2
LC_ 121
DW 2048
M 6,21,3
s_lshl_b32 s53, s52, 8
v_add_u32 v1, s37, v0
M 7,32,2
WL 3
LO 120,18,120
LU 18,120
M 7,33,3
s_add_u32 s48, %[alo], s53
RA 216,1,0
HS 24,18
M 8,44,2
s_addc_u32 s49, %[ahi], 0
LL 17,76
LL 124,77
M 8,45,3
s_add_u32 s52, s52, %[d]
LA 17
v_and_b32 v22, 0xfffffcff, v1
M 9,56,2
s_cmp_eq_u32 s52, %[wrap]
LC_ 124
M 9,57,3
s_cselect_b32 s52, %[rst], s52
DQ 3072
M 10,68,2
WL 3
LO 121,16,121
LU 16,121
M 10,69,3
s_add_u32 s50, s50, %[d]
RA 220,1,1024
HS 24,16
M 11,80,2
LL 18,78
LL 125,79
M 11,81,3
s_cmp_eq_u32 s50, %[wrap]
LA 18
RV 252,22,5120
M 6,22,4
s_cselect_b32 s50, %[rst], s50
LC_ 125
M 6,23,5
WV 30
v_add_u32 v19, 1, v19
M 7,34,4
WL 4
LO 124,17,124
LU 17,124
M 7,35,5
s_add_u32 s38, s38, 32768
RA 224,1,2048
HS 24,17
M 8,46,4
s_cmp_eq_u32 s38, s77
LL 16,80
LL 106,81
M 8,47,5
s_cselect_b32 s38, s76, s38
LA 16
FP
M 9,58,4
s_sub_u32 s39, 0, s39
LC_ 106
M 9,59,5
M 10,70,4
WL 4
LO 125,18,125
LU 18,125
M 10,71,5
RA 228,1,3072
HS 24,18
M 11,82,4
s_sub_u32 s66, s66, 1
)"
R"(LL 17,82
LL 107,83
M 11,83,5
s_cbranch_scc0 1b
7:
LA 17
HX 24
Z 0,12,0
LC_ 107
M 0,13,1
Z 1,24,0
WL 3
LO 16,16,106
LU 106,16
M 1,25,1
RA 232,1,4096
HZ 25,106
Z 2,36,0
LL 18,84
LL 110,85
M 2,37,1
LA 18
PK 106,16,104,33
Z 3,48,0
LC_ 110
PK 104,16,104,32
M 3,49,1
Z 4,60,0
WL 3
LO 17,17,107
LU 107,17
M 4,61,1
s_add_u32 s42, s44, s60
RA 236,1,6144
HS 25,107
Z 5,72,0
LL 16,86
LL 111,87
M 5,73,1
s_addc_u32 s43, s45, 0
LA 16
PK 107,17,105,33
M 0,14,2
LC_ 111
PK 105,17,105,32
M 0,15,3
ST 104
M 1,26,2
WL 3
LO 18,18,110
LU 110,18
M 1,27,3
RA 216,1,7168
HS 25,110
M 2,38,2
LL 17,88
LL 114,89
M 2,39,3
LA 17
PK 110,18,108,33
M 3,50,2
LC_ 114
PK 108,18,108,32
M 3,51,3
FR
PO 110
M 4,62,2
WL 4
LO 16,16,111
LU 111,16
M 4,63,3
s_add_u32 s42, s44, s61
RA 220,1,8192
HS 25,111
M 5,74,2
LL 18,90
LL 115,91
M 5,75,3
s_addc_u32 s43, s45, 0
LA 18
PK 111,16,109,33
M 0,16,4
s_mov_b32 m0, %[pm]
LC_ 115
PK 109,16,109,32
M 0,17,5
DF
PO 111
M 1,28,4
WL 4
LO 17,17,114
LU 114,17
M 1,29,5
RA 224,1,9216
HS 25,114
M 2,40,4
LL 16,92
LL 118,93
M 2,41,5
LA 16
PK 114,17,112,33
M 3,52,4
LC_ 118
PK 112,17,112,32
M 3,53,5
ST 108
M 4,64,4
WL 3
LO 18,18,115
LU 115,18
M 4,65,5
s_add_u32 s42, s44, s62
RA 228,1,10240
HS 25,115
M 5,76,4
LL 17,94
LL 119,95
M 5,77,5
s_addc_u32 s43, s45, 0
LA 17
PK 115,18,113,33
M 0,18,0
LC_ 119
PK 113,18,113,32
M 0,19,1
ST 112
M 1,30,0
WL 3
LO 16,16,118
LU 118,16
M 1,31,1
RA 232,1,11264
HS 25,118
M 2,42,0
LL 18,96
LL 122,97
M 2,43,1
LA 18
PK 118,16,116,33
v_cmp_gt_u32 vcc, v19, v15
M 3,54,0
LC_ 122
PK 116,16,116,32
M 3,55,1
RV 252,22,5392
PO 118
M 4,66,0
WL 4
LO 17,17,119
LU 119,17
M 4,67,1
s_cbranch_vccnz .Lk1s1
.Lk1r1:
RA 236,1,12288
HS 25,119
M 5,78,0
LL 16,98
LL 123,99
M 5,79,1
s_add_u32 s42, s44, s63
LA 16
PK 119,17,117,33
M 0,20,2
s_addc_u32 s43, s45, 0
LC_ 123
PK 117,17,117,32
M 0,21,3
W1 24,0
PO 119
M 1,32,2
WL 5
LO 18,18,122
LU 122,18
M 1,33,3
RA 216,1,13312
HS 25,122
M 2,44,2
LL 17,100
LL 126,101
M 2,45,3
LA 17
PK 122,18,120,33
M 3,56,2
LC_ 126
PK 120,18,120,32
M 3,57,3
ST 116
M 4,68,2
WL 4
LO 16,16,123
LU 123,16
M 4,69,3
s_add_u32 s42, s44, s64
RA 220,1,14336
HS 25,123
M 5,80,2
LL 18,102
LL 127,103
M 5,81,3
s_addc_u32 s43, s45, 0
LA 18
PK 123,16,121,33
M 0,22,4
LC_ 127
PK 121,16,121,32
M 0,23,5
ST 120
M 1,34,4
WL 3
LO 17,17,126
LU 126,17
M 1,35,5
RA 224,1,15360
HS 25,126
M 2,46,4
LL 16,56
LL 104,57
M 2,47,5
LA 16
PK 126,17,124,33
M 3,58,4
LC_ 104
PK 124,17,124,32
M 3,59,5
ds_read_b64 v[14:15], v5 offset:0
PO 126
M 4,70,4
WL 4
LO 18,18,127
LU 127,18
M 4,71,5
s_add_u32 s42, s44, s65
RA 228,1,16384
HS 25,127
M 5,82,4
LL 17,58
LL 105,59
M 5,83,5
s_addc_u32 s43, s45, 0
LA 17
PK 127,18,125,33
Z 6,12,0
LC_ 105
PK 125,18,125,32
M 6,13,1
PO 127
v_subrev_u32 v5, s39, v5
Z 7,24,0
WL 4
LO 104,16,104
LU 16,104
M 7,25,1
RA 232,1,17408
HZ 24,16
Z 8,36,0
WL 4
LL 18,60
LL 108,61
M 8,37,1
s_mul_i32 s53, s50, 25600
LA 18
v_add_f32 v14, v14, v15
Z 9,48,0
s_add_u32 s40, %[wlo], s53
LC_ 108
v_add_f32_dpp v14, v14, v14 quad_perm:[1,0,3,2] row_mask:0xf bank_mask:0xf
M 9,49,1
s_addc_u32 s41, %[whi], 0
Z 10,60,0
WL 3
LO 105,17,105
LU 17,105
M 10,61,1
s_mov_b32 m0, s38
RA 236,1,18432
HS 24,17
Z 11,72,0
LL 16,62
LL 109,63
M 11,73,1
LA 16
ST 124
M 6,14,2
LC_ 109
DW 0
M 6,15,3
v_add_f32_dpp v14, v14, v14 quad_perm:[2,3,0,1] row_mask:0xf bank_mask:0xf
HX 25
M 7,26,2
WL 3
LO 108,18,108
LU 18,108
W1 25,1024
M 7,27,3
s_add_u32 s37, s37, 32768
RA 216,1,19456
HS 24,18
M 8,38,2
s_cmp_eq_u32 s37, s75
LL 17,64
LL 112,65
M 8,39,3
s_cselect_b32 s37, s74, s37
LA 17
DW 1024
M 9,50,2
LC_ 112
v_add_f32_dpp v14, v14, v14 row_half_mirror row_mask:0xf bank_mask:0xf
M 9,51,3
v_cvt_f64_f32 v[14:15], v14
v_add_u32 v4, s39, v4
s_mov_b64 exec, 1
AT
s_mov_b64 exec, -1
M 10,62,2
WL 4
LO 109,16,109
LU 16,109
M 10,63,3
RA 220,1,20480
HS 24,16
M 11,74,2
LL 18,66
LL 113,67
M 11,75,3
LA 18
s_mov_b64 exec, s[54:55]
SP
s_mov_b64 exec, -1
M 6,16,4
LC_ 113
DW 2048
M 6,17,5
M 7,28,4
WL 3
LO 112,17,112
LU 17,112
M 7,29,5
s_mul_i32 s53, s51, 24
RA 224,1,21504
HS 24,17
M 8,40,4
s_bitcmp1_b32 s51, 0
LL 16,68
LL 116,69
M 8,41,5
s_addc_u32 s53, s53, 0
LA 16
M 9,52,4
s_lshl_b32 s53, s53, 11
LC_ 116
M 9,53,5
s_bitcmp1_b64 s[90:91], s51
M 10,64,4
WL 3
LO 113,18,113
LU 18,113
M 10,65,5
s_cselect_b64 s[44:45], s[84:85], s[82:83]
RA 228,1,22528
HS 24,18
M 11,76,4
LL 17,70
LL 117,71
M 11,77,5
s_add_u32 s44, s44, s53
LA 17
M 6,18,0
s_addc_u32 s45, s45, 0
LC_ 117
M 6,19,1
s_add_u32 s51, s51, %[d]
M 7,30,0
WL 3
LO 116,16,116
LU 16,116
M 7,31,1
s_cmp_eq_u32 s51, %[wrap]
RA 232,1,23552
HS 24,16
M 8,42,0
s_cselect_b32 s51, %[rst], s51
LL 18,72
LL 120,73
M 8,43,1
s_add_u32 s40, s40, 3072
LA 18
M 9,54,0
s_addc_u32 s41, s41, 0
LC_ 120
s_add_u32 m0, s38, 3072
M 9,55,1
DW 0
M 10,66,0
WL 3
LO 117,17,117
LU 17,117
M 10,67,1
RA 236,1,24576
HS 24,17
)"
R"(M 11,78,0
LL 16,74
LL 121,75
M 11,79,1
LA 16
DW 1024
M 6,20,2
LC_ 121
DW 2048
M 6,21,3
s_lshl_b32 s53, s52, 8
v_add_u32 v1, s37, v0
M 7,32,2
WL 3
LO 120,18,120
LU 18,120
M 7,33,3
s_add_u32 s48, %[alo], s53
RA 216,1,0
HS 24,18
M 8,44,2
s_addc_u32 s49, %[ahi], 0
LL 17,76
LL 124,77
M 8,45,3
s_add_u32 s52, s52, %[d]
LA 17
v_and_b32 v22, 0xfffffcff, v1
M 9,56,2
s_cmp_eq_u32 s52, %[wrap]
LC_ 124
M 9,57,3
s_cselect_b32 s52, %[rst], s52
DQ 3072
M 10,68,2
WL 3
LO 121,16,121
LU 16,121
M 10,69,3
s_add_u32 s50, s50, %[d]
RA 220,1,1024
HS 24,16
M 11,80,2
LL 18,78
LL 125,79
M 11,81,3
s_cmp_eq_u32 s50, %[wrap]
LA 18
RV 252,22,5120
M 6,22,4
s_cselect_b32 s50, %[rst], s50
LC_ 125
M 6,23,5
WV 31
v_add_u32 v19, 1, v19
M 7,34,4
WL 4
LO 124,17,124
LU 17,124
M 7,35,5
s_add_u32 s38, s38, 32768
RA 224,1,2048
HS 24,17
M 8,46,4
s_cmp_eq_u32 s38, s77
LL 16,80
LL 106,81
M 8,47,5
s_cselect_b32 s38, s76, s38
LA 16
FP
M 9,58,4
s_sub_u32 s39, 0, s39
LC_ 106
M 9,59,5
M 10,70,4
WL 4
LO 125,18,125
LU 18,125
M 10,71,5
RA 228,1,3072
HS 24,18
M 11,82,4
LL 17,82
LL 107,83
M 11,83,5
LA 17
HX 24
Z 0,12,0
LC_ 107
M 0,13,1
Z 1,24,0
WL 3
LO 16,16,106
LU 106,16
M 1,25,1
RA 232,1,4096
HZ 25,106
Z 2,36,0
LL 18,84
LL 110,85
M 2,37,1
LA 18
PK 106,16,104,33
Z 3,48,0
LC_ 110
PK 104,16,104,32
M 3,49,1
Z 4,60,0
WL 3
LO 17,17,107
LU 107,17
M 4,61,1
s_add_u32 s42, s44, s60
RA 236,1,6144
HS 25,107
Z 5,72,0
LL 16,86
LL 111,87
M 5,73,1
s_addc_u32 s43, s45, 0
LA 16
PK 107,17,105,33
M 0,14,2
LC_ 111
PK 105,17,105,32
M 0,15,3
ST 104
M 1,26,2
WL 3
LO 18,18,110
LU 110,18
M 1,27,3
RA 216,1,7168
HS 25,110
M 2,38,2
LL 17,88
LL 114,89
M 2,39,3
LA 17
PK 110,18,108,33
M 3,50,2
LC_ 114
PK 108,18,108,32
M 3,51,3
FR
PO 110
M 4,62,2
WL 4
LO 16,16,111
LU 111,16
M 4,63,3
s_add_u32 s42, s44, s61
RA 220,1,8192
HS 25,111
M 5,74,2
LL 18,90
LL 115,91
M 5,75,3
s_addc_u32 s43, s45, 0
LA 18
PK 111,16,109,33
M 0,16,4
s_mov_b32 m0, %[pm]
LC_ 115
PK 109,16,109,32
M 0,17,5
DF
PO 111
M 1,28,4
WL 4
LO 17,17,114
LU 114,17
M 1,29,5
RA 224,1,9216
HS 25,114
M 2,40,4
LL 16,92
LL 118,93
M 2,41,5
LA 16
PK 114,17,112,33
M 3,52,4
LC_ 118
PK 112,17,112,32
M 3,53,5
ST 108
M 4,64,4
WL 3
LO 18,18,115
LU 115,18
M 4,65,5
s_add_u32 s42, s44, s62
RA 228,1,10240
HS 25,115
M 5,76,4
LL 17,94
LL 119,95
M 5,77,5
s_addc_u32 s43, s45, 0
LA 17
PK 115,18,113,33
M 0,18,0
LC_ 119
PK 113,18,113,32
M 0,19,1
ST 112
M 1,30,0
WL 3
LO 16,16,118
LU 118,16
M 1,31,1
RA 232,1,11264
HS 25,118
M 2,42,0
LL 18,96
LL 122,97
M 2,43,1
LA 18
PK 118,16,116,33
v_cmp_gt_u32 vcc, v19, v15
M 3,54,0
LC_ 122
PK 116,16,116,32
M 3,55,1
RV 252,22,5392
PO 118
M 4,66,0
WL 4
LO 17,17,119
LU 119,17
M 4,67,1
s_cbranch_vccnz .Lk1s2
.Lk1r2:
RA 236,1,12288
HS 25,119
M 5,78,0
LL 16,98
LL 123,99
M 5,79,1
s_add_u32 s42, s44, s63
LA 16
PK 119,17,117,33
M 0,20,2
s_addc_u32 s43, s45, 0
LC_ 123
PK 117,17,117,32
M 0,21,3
W1 24,0
PO 119
M 1,32,2
WL 5
LO 18,18,122
LU 122,18
M 1,33,3
RA 216,1,13312
HS 25,122
M 2,44,2
LL 17,100
LL 126,101
M 2,45,3
LA 17
PK 122,18,120,33
M 3,56,2
LC_ 126
PK 120,18,120,32
M 3,57,3
ST 116
M 4,68,2
WL 4
LO 16,16,123
LU 123,16
M 4,69,3
s_add_u32 s42, s44, s64
RA 220,1,14336
HS 25,123
M 5,80,2
LL 18,102
LL 127,103
M 5,81,3
s_addc_u32 s43, s45, 0
LA 18
PK 123,16,121,33
M 0,22,4
LC_ 127
PK 121,16,121,32
M 0,23,5
ST 120
M 1,34,4
WL 3
LO 17,17,126
LU 126,17
M 1,35,5
RA 224,1,15360
HS 25,126
M 2,46,4
LL 16,56
LL 104,57
M 2,47,5
LA 16
PK 126,17,124,33
M 3,58,4
LC_ 104
PK 124,17,124,32
M 3,59,5
ds_read_b64 v[14:15], v5 offset:0
PO 126
M 4,70,4
WL 4
LO 18,18,127
LU 127,18
M 4,71,5
s_add_u32 s42, s44, s65
RA 228,1,16384
HS 25,127
M 5,82,4
LL 17,58
LL 105,59
M 5,83,5
s_addc_u32 s43, s45, 0
LA 17
PK 127,18,125,33
Z 6,12,0
LC_ 105
PK 125,18,125,32
M 6,13,1
PO 127
v_subrev_u32 v5, s39, v5
Z 7,24,0
WL 4
LO 104,16,104
LU 16,104
M 7,25,1
RA 232,1,17408
HZ 24,16
Z 8,36,0
WL 4
LL 18,60
LL 108,61
M 8,37,1
s_mul_i32 s53, s50, 25600
LA 18
v_add_f32 v14, v14, v15
Z 9,48,0
s_add_u32 s40, %[wlo], s53
LC_ 108
v_add_f32_dpp v14, v14, v14 quad_perm:[1,0,3,2] row_mask:0xf bank_mask:0xf
M 9,49,1
s_addc_u32 s41, %[whi], 0
Z 10,60,0
WL 3
LO 105,17,105
LU 17,105
M 10,61,1
s_mov_b32 m0, s38
RA 236,1,18432
HS 24,17
Z 11,72,0
LL 16,62
LL 109,63
M 11,73,1
LA 16
ST 124
M 6,14,2
LC_ 109
DW 0
M 6,15,3
WV 14
v_add_f32_dpp v14, v14, v14 quad_perm:[2,3,0,1] row_mask:0xf bank_mask:0xf
HX 25
M 7,26,2
WL 3
LO 108,18,108
LU 18,108
W1 25,1024
M 7,27,3
s_add_u32 s37, s37, 32768
RA 216,1,19456
HS 24,18
v_readfirstlane_b32 s72, v11
M 8,38,2
s_cmp_eq_u32 s37, s75
LL 17,64
LL 112,65
v_mov_b32 v12, s72
M 8,39,3
s_cselect_b32 s37, s74, s37
LA 17
W2 12,12,16
M 9,50,2
LC_ 112
DW 1024
M 9,51,3
v_add_f32_dpp v14, v14, v14 row_half_mirror row_mask:0xf bank_mask:0xf
v_add_u32 v4, s39, v4
M 10,62,2
WL 5
LO 109,16,109
LU 16,109
M 10,63,3
)"
R"(RA 220,1,20480
HS 24,16
M 11,74,2
LL 18,66
LL 113,67
M 11,75,3
LA 18
DW 2048
M 6,16,4
LC_ 113
v_cvt_f64_f32 v[14:15], v14
M 6,17,5
s_mov_b64 exec, s[54:55]
SP
s_mov_b64 exec, -1
M 7,28,4
WL 3
LO 112,17,112
LU 17,112
M 7,29,5
s_mul_i32 s53, s51, 24
RA 224,1,21504
HS 24,17
M 8,40,4
s_bitcmp1_b32 s51, 0
LL 16,68
LL 116,69
M 8,41,5
s_addc_u32 s53, s53, 0
LA 16
M 9,52,4
s_lshl_b32 s53, s53, 11
LC_ 116
M 9,53,5
s_bitcmp1_b64 s[90:91], s51
M 10,64,4
WL 3
LO 113,18,113
LU 18,113
M 10,65,5
s_cselect_b64 s[44:45], s[84:85], s[82:83]
RA 228,1,22528
HS 24,18
M 11,76,4
LL 17,70
LL 117,71
M 11,77,5
s_add_u32 s44, s44, s53
LA 17
M 6,18,0
s_addc_u32 s45, s45, 0
LC_ 117
M 6,19,1
s_add_u32 s51, s51, %[d]
M 7,30,0
WL 3
LO 116,16,116
LU 16,116
M 7,31,1
s_cmp_eq_u32 s51, %[wrap]
RA 232,1,23552
HS 24,16
M 8,42,0
s_cselect_b32 s51, %[rst], s51
LL 18,72
LL 120,73
M 8,43,1
s_add_u32 s40, s40, 3072
LA 18
M 9,54,0
s_addc_u32 s41, s41, 0
LC_ 120
s_add_u32 m0, s38, 3072
M 9,55,1
DW 0
M 10,66,0
WL 3
LO 117,17,117
LU 17,117
M 10,67,1
RA 236,1,24576
HS 24,17
M 11,78,0
LL 16,74
LL 121,75
M 11,79,1
LA 16
DW 1024
M 6,20,2
LC_ 121
DW 2048
M 6,21,3
s_lshl_b32 s53, s52, 8
v_add_u32 v1, s37, v0
M 7,32,2
WL 3
LO 120,18,120
LU 18,120
M 7,33,3
s_add_u32 s48, %[alo], s53
RA 216,1,0
HS 24,18
M 8,44,2
s_addc_u32 s49, %[ahi], 0
LL 17,76
LL 124,77
M 8,45,3
s_add_u32 s52, s52, %[d]
LA 17
v_and_b32 v22, 0xfffffcff, v1
M 9,56,2
s_cmp_eq_u32 s52, %[wrap]
LC_ 124
M 9,57,3
s_cselect_b32 s52, %[rst], s52
DQ 3072
M 10,68,2
WL 3
LO 121,16,121
LU 16,121
M 10,69,3
s_add_u32 s50, s50, %[d]
RA 220,1,1024
HS 24,16
M 11,80,2
LL 18,78
LL 125,79
M 11,81,3
s_cmp_eq_u32 s50, %[wrap]
LA 18
RV 252,22,5120
M 6,22,4
s_cselect_b32 s50, %[rst], s50
LC_ 125
M 6,23,5
v_add_u32 v19, 1, v19
M 7,34,4
WL 4
LO 124,17,124
LU 17,124
M 7,35,5
s_add_u32 s38, s38, 32768
RA 224,1,2048
HS 24,17
M 8,46,4
s_cmp_eq_u32 s38, s77
LL 16,80
LL 106,81
M 8,47,5
s_cselect_b32 s38, s76, s38
LA 16
FP
M 9,58,4
s_sub_u32 s39, 0, s39
LC_ 106
M 9,59,5
M 10,70,4
WL 4
LO 125,18,125
LU 18,125
M 10,71,5
RA 228,1,3072
HS 24,18
M 11,82,4
LL 17,82
LL 107,83
M 11,83,5
LA 17
HX 24
Z 0,12,0
LC_ 107
M 0,13,1
Z 1,24,0
WL 3
LO 16,16,106
LU 106,16
M 1,25,1
RA 232,1,4096
HZ 25,106
Z 2,36,0
LL 18,84
LL 110,85
M 2,37,1
LA 18
PK 106,16,104,33
Z 3,48,0
LC_ 110
PK 104,16,104,32
M 3,49,1
Z 4,60,0
WL 3
LO 17,17,107
LU 107,17
M 4,61,1
s_add_u32 s42, s44, s60
RA 236,1,6144
HS 25,107
Z 5,72,0
LL 16,86
LL 111,87
M 5,73,1
s_addc_u32 s43, s45, 0
LA 16
PK 107,17,105,33
M 0,14,2
LC_ 111
PK 105,17,105,32
M 0,15,3
ST 104
M 1,26,2
WL 3
LO 18,18,110
LU 110,18
M 1,27,3
RA 216,1,7168
HS 25,110
M 2,38,2
LL 17,88
LL 114,89
M 2,39,3
LA 17
PK 110,18,108,33
M 3,50,2
LC_ 114
PK 108,18,108,32
M 3,51,3
FR
PO 110
M 4,62,2
WL 4
LO 16,16,111
LU 111,16
M 4,63,3
s_add_u32 s42, s44, s61
RA 220,1,8192
HS 25,111
M 5,74,2
LL 18,90
LL 115,91
M 5,75,3
s_addc_u32 s43, s45, 0
LA 18
PK 111,16,109,33
M 0,16,4
s_mov_b32 m0, %[pm]
LC_ 115
PK 109,16,109,32
M 0,17,5
DF
PO 111
M 1,28,4
WL 4
LO 17,17,114
LU 114,17
M 1,29,5
RA 224,1,9216
HS 25,114
M 2,40,4
LL 16,92
LL 118,93
M 2,41,5
LA 16
PK 114,17,112,33
M 3,52,4
LC_ 118
PK 112,17,112,32
M 3,53,5
ST 108
M 4,64,4
WL 3
LO 18,18,115
LU 115,18
M 4,65,5
s_add_u32 s42, s44, s62
RA 228,1,10240
HS 25,115
M 5,76,4
LL 17,94
LL 119,95
M 5,77,5
s_addc_u32 s43, s45, 0
LA 17
PK 115,18,113,33
M 0,18,0
LC_ 119
PK 113,18,113,32
M 0,19,1
ST 112
M 1,30,0
WL 3
LO 16,16,118
LU 118,16
M 1,31,1
RA 232,1,11264
HS 25,118
M 2,42,0
LL 18,96
LL 122,97
M 2,43,1
LA 18
PK 118,16,116,33
v_cmp_gt_u32 vcc, v19, v15
M 3,54,0
LC_ 122
PK 116,16,116,32
M 3,55,1
RV 252,22,5392
PO 118
M 4,66,0
WL 4
LO 17,17,119
LU 119,17
M 4,67,1
s_cbranch_vccnz .Lk1s3
.Lk1r3:
RA 236,1,12288
HS 25,119
M 5,78,0
LL 16,98
LL 123,99
M 5,79,1
s_add_u32 s42, s44, s63
LA 16
PK 119,17,117,33
M 0,20,2
s_addc_u32 s43, s45, 0
LC_ 123
PK 117,17,117,32
M 0,21,3
ds_read_b32 v20, v5 offset:4096
PO 119
M 1,32,2
WL 5
LO 18,18,122
LU 122,18
M 1,33,3
RA 216,1,13312
HS 25,122
M 2,44,2
LL 17,100
LL 126,101
M 2,45,3
LA 17
PK 122,18,120,33
M 3,56,2
LC_ 126
PK 120,18,120,32
M 3,57,3
M 4,68,2
WL 4
LO 16,16,123
LU 123,16
M 4,69,3
RA 220,1,14336
HS 25,123
M 5,80,2
LL 18,102
LL 127,103
M 5,81,3
LA 18
PK 123,16,121,33
M 0,22,4
LC_ 127
PK 121,16,121,32
W1 24,0
M 0,23,5
M 1,34,4
WL 4
LO 17,17,126
LU 126,17
M 1,35,5
RA 224,1,15360
HS 25,126
M 2,46,4
LL 16,56
LL 104,57
M 2,47,5
LA 16
PK 126,17,124,33
M 3,58,4
LC_ 104
PK 124,17,124,32
M 3,59,5
PO 126
v_readfirstlane_b32 s72, v20
M 4,70,4
WL 4
LO 18,18,127
LU 127,18
M 4,71,5
s_sub_u32 s53, 63, s72
RA 228,1,16384
HS 25,127
M 5,82,4
LL 17,58
LL 105,59
M 5,83,5
s_cmp_eq_u32 %[d], 1
LA 17
PK 127,18,125,33
)"
R"(Z 6,12,0
s_cselect_b32 s72, s72, s53
LC_ 105
PK 125,18,125,32
M 6,13,1
s_and_b32 s72, s72, 63
ds_read_b64 v[14:15], v5 offset:0
PO 127
Z 7,24,0
WL 4
LO 104,16,104
LU 16,104
M 7,25,1
s_add_u32 s53, s72, 128
RA 232,1,17408
HZ 24,16
Z 8,36,0
s_mul_i32 s53, s53, 2359296
LL 18,60
LL 108,61
M 8,37,1
s_add_u32 s86, %[hlo], s53
LA 18
v_subrev_u32 v5, s39, v5
Z 9,48,0
s_addc_u32 s87, %[hhi], 0
LC_ 108
HX 25
M 9,49,1
s_add_u32 s88, s86, 8192
Z 10,60,0
WL 4
LO 105,17,105
LU 17,105
M 10,61,1
s_addc_u32 s89, s87, 0
RA 236,1,18432
HS 24,17
Z 11,72,0
WL 4
LL 16,62
LL 109,63
M 11,73,1
s_and_b32 s92, %[hlo], 0x1fffff
LA 16
W1 25,1024
s_add_u32 s92, s92, s53
s_mov_b32 s93, 3072
ST 116
s_add_u32 s42, s44, s64
s_addc_u32 s43, s45, 0
s_mul_i32 s57, s72, 589824
s_add_u32 s57, s57, %[xs1]
s_lshl_b32 s72, s72, 1
s_add_u32 s72, s72, s96
s_lshl_b32 s53, s72, 6
s_add_u32 s53, s53, 128
s_cmp_lt_u32 %[ca0], 128
s_cselect_b32 s73, s53, %[ca0]
ST 120
v_mov_b32 v9, s73
v_mov_b32 v11, s92
M 6,14,2
s_add_u32 s42, s44, s65
LC_ 109
v_add_f32 v14, v14, v15
s_addc_u32 s43, s45, 0
v_mad_u32_u24 v11, v0, s93, v11
M 6,15,3
v_add_f32_dpp v14, v14, v14 quad_perm:[1,0,3,2] row_mask:0xf bank_mask:0xf
v_add_u32 v4, s39, v4
M 7,26,2
WL 4
LO 108,18,108
LU 18,108
M 7,27,3
RA 216,1,19456
HS 24,18
v_lshrrev_b32 v11, 21, v11
M 8,38,2
LL 17,64
LL 112,65
M 8,39,3
LA 17
v_add_f32_dpp v14, v14, v14 quad_perm:[2,3,0,1] row_mask:0xf bank_mask:0xf
M 9,50,2
LC_ 112
v_add_f32_dpp v14, v14, v14 row_half_mirror row_mask:0xf bank_mask:0xf
v_lshl_add_u32 v11, v11, 2, 64
M 9,51,3
s_mul_i32 s53, s50, 25600
v_cvt_f64_f32 v[14:15], v14
M 10,62,2
WL 3
LO 109,16,109
LU 16,109
s_add_u32 s40, %[wlo], s53
s_addc_u32 s41, %[whi], 0
s_mov_b32 m0, s38
M 10,63,3
RA 220,1,20480
HS 24,16
v_writelane_b32 v11, %[tsel], 63
M 11,74,2
LL 18,66
LL 113,67
ST 124
M 11,75,3
s_sub_u32 s53, s86, %[hlo]
LA 18
s_mov_b64 exec, s[54:55]
SP
s_mov_b64 exec, -1
M 6,16,4
s_lshr_b32 s53, s53, 2
LC_ 113
s_and_b32 s92, %[xlo], 0x1fffff
s_add_u32 s53, s53, s92
s_add_u32 s53, s53, 0x20048000
s_lshr_b32 s53, s53, 21
s_lshl_b32 s92, s53, 2
M 6,17,5
DW 0
v_writelane_b32 v11, s92, 48
M 7,28,4
WL 3
LO 112,17,112
LU 17,112
M 7,29,5
s_add_u32 s37, s37, 32768
RA 224,1,21504
HS 24,17
M 8,40,4
s_cmp_eq_u32 s37, s75
LL 16,68
LL 116,69
M 8,41,5
s_cselect_b32 s37, s74, s37
LA 16
global_load_dword v11, v11, s[68:69] offset:-2048
M 9,52,4
LC_ 116
DW 1024
M 9,53,5
DW 2048
M 10,64,4
WL 3
LO 113,18,113
LU 18,113
M 10,65,5
s_mul_i32 s53, s51, 24
RA 228,1,22528
HS 24,18
M 11,76,4
LL 17,70
LL 117,71
M 11,77,5
s_bitcmp1_b32 s51, 0
LA 17
M 6,18,0
s_addc_u32 s53, s53, 0
LC_ 117
M 6,19,1
s_lshl_b32 s53, s53, 11
M 7,30,0
WL 3
LO 116,16,116
LU 16,116
M 7,31,1
s_bitcmp1_b64 s[90:91], s51
RA 232,1,23552
HS 24,16
M 8,42,0
s_cselect_b64 s[44:45], s[84:85], s[82:83]
LL 18,72
LL 120,73
M 8,43,1
s_add_u32 s44, s44, s53
LA 18
s_addc_u32 s45, s45, 0
s_add_u32 s51, s51, %[d]
s_cmp_eq_u32 s51, %[wrap]
s_cselect_b32 s51, %[rst], s51
M 9,54,0
s_add_u32 s40, s40, 3072
LC_ 120
s_addc_u32 s41, s41, 0
s_add_u32 m0, s38, 3072
M 9,55,1
DW 0
M 10,66,0
WL 3
LO 117,17,117
LU 17,117
M 10,67,1
RA 236,1,24576
HS 24,17
M 11,78,0
LL 16,74
LL 121,75
M 11,79,1
LA 16
DW 1024
M 6,20,2
LC_ 121
DW 2048
M 6,21,3
s_lshl_b32 s53, s52, 8
v_add_u32 v1, s37, v0
M 7,32,2
WL 3
LO 120,18,120
LU 18,120
M 7,33,3
s_add_u32 s48, %[alo], s53
RA 216,1,0
HS 24,18
M 8,44,2
s_addc_u32 s49, %[ahi], 0
LL 17,76
LL 124,77
M 8,45,3
s_add_u32 s52, s52, %[d]
LA 17
v_and_b32 v22, 0xfffffcff, v1
M 9,56,2
s_cmp_eq_u32 s52, %[wrap]
LC_ 124
M 9,57,3
s_cselect_b32 s52, %[rst], s52
DQ 3072
M 10,68,2
WL 3
LO 121,16,121
LU 16,121
M 10,69,3
s_add_u32 s50, s50, %[d]
RA 220,1,1024
HS 24,16
M 11,80,2
LL 18,78
LL 125,79
M 11,81,3
s_cmp_eq_u32 s50, %[wrap]
LA 18
RV 252,22,5120
M 6,22,4
s_cselect_b32 s50, %[rst], s50
LC_ 125
M 6,23,5
WV 31
v_add_u32 v19, 1, v19
M 7,34,4
WL 4
LO 124,17,124
LU 17,124
M 7,35,5
s_add_u32 s38, s38, 32768
RA 224,1,2048
HS 24,17
M 8,46,4
s_cmp_eq_u32 s38, s77
LL 16,80
LL 106,81
M 8,47,5
s_cselect_b32 s38, s76, s38
LA 16
FP
M 9,58,4
s_sub_u32 s39, 0, s39
LC_ 106
M 9,59,5
M 10,70,4
WL 4
LO 125,18,125
LU 18,125
M 10,71,5
RA 228,1,3072
HS 24,18
M 11,82,4
LL 17,82
LL 107,83
M 11,83,5
LA 17
HX 24
Z 0,12,0
LC_ 107
M 0,13,1
Z 1,24,0
WL 3
LO 16,16,106
LU 106,16
M 1,25,1
RA 232,1,4096
HZ 25,106
Z 2,36,0
LL 18,84
LL 110,85
M 2,37,1
LA 18
PK 106,16,104,33
Z 3,48,0
LC_ 110
PK 104,16,104,32
M 3,49,1
Z 4,60,0
WL 3
LO 17,17,107
LU 107,17
M 4,61,1
s_add_u32 s42, s44, s60
RA 236,1,6144
HS 25,107
Z 5,72,0
LL 16,86
LL 111,87
M 5,73,1
s_addc_u32 s43, s45, 0
LA 16
PK 107,17,105,33
M 0,14,2
LC_ 111
PK 105,17,105,32
M 0,15,3
ST 104
M 1,26,2
WL 3
LO 18,18,110
LU 110,18
M 1,27,3
RA 216,1,7168
HS 25,110
M 2,38,2
LL 17,88
LL 114,89
M 2,39,3
LA 17
PK 110,18,108,33
M 3,50,2
LC_ 114
PK 108,18,108,32
M 3,51,3
FR
PO 110
M 4,62,2
WL 4
LO 16,16,111
LU 111,16
M 4,63,3
s_add_u32 s42, s44, s61
RA 220,1,8192
HS 25,111
M 5,74,2
LL 18,90
LL 115,91
M 5,75,3
s_addc_u32 s43, s45, 0
LA 18
PK 111,16,109,33
M 0,16,4
s_mov_b32 m0, %[pm]
LC_ 115
PK 109,16,109,32
M 0,17,5
DF
PO 111
M 1,28,4
WL 4
LO 17,17,114
LU 114,17
M 1,29,5
RA 224,1,9216
HS 25,114
M 2,40,4
LL 16,92
LL 118,93
M 2,41,5
LA 16
PK 114,17,112,33
M 3,52,4
LC_ 118
PK 112,17,112,32
M 3,53,5
ST 108
M 4,64,4
WL 3
LO 18,18,115
LU 115,18
M 4,65,5
s_add_u32 s42, s44, s62
RA 228,1,10240
HS 25,115
M 5,76,4
LL 17,94
LL 119,95
M 5,77,5
s_addc_u32 s43, s45, 0
LA 17
PK 115,18,113,33
M 0,18,0
)"
R"(LC_ 119
PK 113,18,113,32
M 0,19,1
ST 112
M 1,30,0
WL 3
LO 16,16,118
LU 118,16
M 1,31,1
RA 232,1,11264
HS 25,118
M 2,42,0
LL 18,96
LL 122,97
M 2,43,1
LA 18
PK 118,16,116,33
v_cmp_gt_u32 vcc, v19, v15
M 3,54,0
LC_ 122
PK 116,16,116,32
M 3,55,1
RV 252,22,5392
PO 118
M 4,66,0
WL 4
LO 17,17,119
LU 119,17
M 4,67,1
s_cbranch_vccnz .Lk1s4
.Lk1r4:
RA 236,1,12288
HS 25,119
M 5,78,0
LL 16,98
LL 123,99
M 5,79,1
s_add_u32 s42, s44, s63
LA 16
PK 119,17,117,33
M 0,20,2
s_addc_u32 s43, s45, 0
LC_ 123
PK 117,17,117,32
M 0,21,3
W1 24,0
PO 119
M 1,32,2
WL 5
LO 18,18,122
LU 122,18
M 1,33,3
RA 216,1,13312
HS 25,122
M 2,44,2
LL 17,100
LL 126,101
M 2,45,3
LA 17
PK 122,18,120,33
M 3,56,2
LC_ 126
PK 120,18,120,32
M 3,57,3
ST 116
M 4,68,2
WL 4
LO 16,16,123
LU 123,16
M 4,69,3
s_add_u32 s42, s44, s64
RA 220,1,14336
HS 25,123
M 5,80,2
LL 18,102
LL 127,103
M 5,81,3
s_addc_u32 s43, s45, 0
LA 18
PK 123,16,121,33
M 0,22,4
LC_ 127
PK 121,16,121,32
M 0,23,5
ST 120
M 1,34,4
WL 3
LO 17,17,126
LU 126,17
M 1,35,5
RA 224,1,15360
HS 25,126
M 2,46,4
LL 16,56
LL 104,57
M 2,47,5
LA 16
PK 126,17,124,33
M 3,58,4
LC_ 104
PK 124,17,124,32
M 3,59,5
ds_read_b64 v[14:15], v5 offset:0
PO 126
M 4,70,4
WL 4
LO 18,18,127
LU 127,18
M 4,71,5
s_add_u32 s42, s44, s65
RA 228,1,16384
HS 25,127
M 5,82,4
LL 17,58
LL 105,59
M 5,83,5
s_addc_u32 s43, s45, 0
LA 17
PK 127,18,125,33
Z 6,12,0
LC_ 105
PK 125,18,125,32
M 6,13,1
PO 127
v_subrev_u32 v5, s39, v5
Z 7,24,0
WL 4
LO 104,16,104
LU 16,104
M 7,25,1
RA 232,1,17408
HZ 24,16
Z 8,36,0
WL 4
LL 18,60
LL 108,61
M 8,37,1
s_mul_i32 s53, s50, 25600
LA 18
v_add_f32 v14, v14, v15
Z 9,48,0
s_add_u32 s40, %[wlo], s53
LC_ 108
v_add_f32_dpp v14, v14, v14 quad_perm:[1,0,3,2] row_mask:0xf bank_mask:0xf
M 9,49,1
s_addc_u32 s41, %[whi], 0
Z 10,60,0
WL 3
LO 105,17,105
LU 17,105
M 10,61,1
s_mov_b32 m0, s38
RA 236,1,18432
HS 24,17
Z 11,72,0
LL 16,62
LL 109,63
M 11,73,1
LA 16
ST 124
M 6,14,2
LC_ 109
DW 0
M 6,15,3
v_add_f32_dpp v14, v14, v14 quad_perm:[2,3,0,1] row_mask:0xf bank_mask:0xf
HX 25
M 7,26,2
WL 3
LO 108,18,108
LU 18,108
W1 25,1024
M 7,27,3
s_add_u32 s37, s37, 32768
RA 216,1,19456
HS 24,18
M 8,38,2
s_cmp_eq_u32 s37, s75
LL 17,64
LL 112,65
M 8,39,3
s_cselect_b32 s37, s74, s37
LA 17
DW 1024
M 9,50,2
LC_ 112
v_add_f32_dpp v14, v14, v14 row_half_mirror row_mask:0xf bank_mask:0xf
M 9,51,3
v_cvt_f64_f32 v[14:15], v14
v_add_u32 v4, s39, v4
M 10,62,2
WL 4
LO 109,16,109
LU 16,109
M 10,63,3
RA 220,1,20480
HS 24,16
M 11,74,2
LL 18,66
LL 113,67
M 11,75,3
LA 18
s_mov_b64 exec, s[54:55]
SP
s_mov_b64 exec, -1
M 6,16,4
LC_ 113
DW 2048
M 6,17,5
s_and_b32 s53, %[w], 1
M 7,28,4
WL 3
LO 112,17,112
LU 17,112
M 7,29,5
s_lshl_b32 s53, s53, 10
RA 224,1,21504
HS 24,17
M 8,40,4
s_lshr_b32 s72, %[w], 1
LL 16,68
LL 116,69
M 8,41,5
s_mul_i32 s72, s72, 32768
LA 16
M 9,52,4
s_add_u32 s72, s72, s53
LC_ 116
M 9,53,5
s_add_u32 s72, s72, 128000
M 10,64,4
WL 3
LO 113,18,113
LU 18,113
M 10,65,5
s_movk_i32 s53, 0x78
RA 228,1,22528
HS 24,18
M 11,76,4
LL 17,70
LL 117,71
M 11,77,5
LA 17
v_lshrrev_b32 v12, 5, v0
M 6,18,0
LC_ 117
v_and_b32 v12, 6, v12
M 6,19,1
v_bfe_u32 v12, s53, v12, 2
M 7,30,0
WL 3
LO 116,16,116
LU 16,116
M 7,31,1
RA 232,1,23552
HS 24,16
v_lshrrev_b32 v20, 8, v0
v_xor_b32 v20, v12, v20
v_lshl_add_u32 v20, v20, 4, s72
v_and_b32 v12, 0xf0, v0
v_lshl_add_u32 v20, v12, 2, v20
v_add_u32 v12, s72, v0
M 8,42,0
s_mul_i32 s53, s51, 24
LL 18,72
LL 120,73
M 8,43,1
s_bitcmp1_b32 s51, 0
LA 18
s_addc_u32 s53, s53, 0
s_lshl_b32 s53, s53, 11
s_bitcmp1_b64 s[90:91], s51
s_cselect_b64 s[44:45], s[84:85], s[82:83]
s_add_u32 s44, s44, s53
s_addc_u32 s45, s45, 0
s_add_u32 s51, s51, %[d]
s_cmp_eq_u32 s51, %[wrap]
s_cselect_b32 s51, %[rst], s51
M 9,54,0
s_add_u32 s40, s40, 3072
LC_ 120
s_addc_u32 s41, s41, 0
s_add_u32 m0, s38, 3072
M 9,55,1
DW 0
M 10,66,0
WL 3
LO 117,17,117
LU 17,117
M 10,67,1
RA 236,1,24576
HS 24,17
M 11,78,0
LL 16,74
LL 121,75
M 11,79,1
LA 16
DW 1024
M 6,20,2
LC_ 121
DW 2048
M 6,21,3
s_lshl_b32 s53, s52, 8
v_add_u32 v1, s37, v0
M 7,32,2
WL 3
LO 120,18,120
LU 18,120
M 7,33,3
s_add_u32 s48, %[alo], s53
RA 216,1,0
HS 24,18
M 8,44,2
s_addc_u32 s49, %[ahi], 0
LL 17,76
LL 124,77
M 8,45,3
s_add_u32 s52, s52, %[d]
LA 17
v_and_b32 v22, 0xfffffcff, v1
M 9,56,2
s_cmp_eq_u32 s52, %[wrap]
LC_ 124
M 9,57,3
s_cselect_b32 s52, %[rst], s52
DQ 3072
M 10,68,2
WL 3
LO 121,16,121
LU 16,121
M 10,69,3
s_add_u32 s50, s50, %[d]
RA 220,1,1024
HS 24,16
M 11,80,2
LL 18,78
LL 125,79
M 11,81,3
s_cmp_eq_u32 s50, %[wrap]
LA 18
RV 252,22,5120
M 6,22,4
s_cselect_b32 s50, %[rst], s50
LC_ 125
M 6,23,5
WV 31
v_add_u32 v19, 1, v19
M 7,34,4
WL 4
LO 124,17,124
LU 17,124
M 7,35,5
s_add_u32 s38, s38, 32768
RA 224,1,2048
HS 24,17
M 8,46,4
s_cmp_eq_u32 s38, s77
LL 16,80
LL 106,81
M 8,47,5
s_cselect_b32 s38, s76, s38
LA 16
FP
M 9,58,4
s_sub_u32 s39, 0, s39
LC_ 106
M 9,59,5
M 10,70,4
WL 4
LO 125,18,125
LU 18,125
M 10,71,5
RA 228,1,3072
HS 24,18
M 11,82,4
LL 17,82
LL 107,83
M 11,83,5
LA 17
HX 24
Z 0,12,0
LC_ 107
M 0,13,1
Z 1,24,0
WL 3
LO 16,16,106
LU 106,16
M 1,25,1
RA 232,1,4096
HZ 25,106
Z 2,36,0
LL 18,84
LL 110,85
M 2,37,1
LA 18
PK 106,16,104,33
Z 3,48,0
LC_ 110
PK 104,16,104,32
M 3,49,1
WV 21
)"
R"(v_bcnt_u32_b32 v11, v11, 0
Z 4,60,0
WL 3
LO 17,17,107
LU 107,17
M 4,61,1
RA 236,1,6144
HS 25,107
Z 5,72,0
LL 16,86
LL 111,87
M 5,73,1
LA 16
PK 107,17,105,33
M 0,14,2
LC_ 111
PK 105,17,105,32
M 0,15,3
v_cmp_lt_u32 s[92:93], 2, v11
M 1,26,2
WL 3
LO 18,18,110
LU 110,18
M 1,27,3
s_nop 0
RA 216,1,7168
HS 25,110
M 2,38,2
s_add_u32 s42, s44, s60
LL 17,88
LL 114,89
M 2,39,3
s_addc_u32 s43, s45, 0
LA 17
PK 110,18,108,33
M 3,50,2
s_sub_u32 s53, 0, s96
LC_ 114
PK 108,18,108,32
M 3,51,3
s_xor_b32 s92, s92, s53
FR
PO 110
M 4,62,2
WL 4
LO 16,16,111
LU 111,16
M 4,63,3
s_xor_b32 s93, s93, s53
RA 220,1,8192
HS 25,111
M 5,74,2
LL 18,90
LL 115,91
M 5,75,3
s_mul_i32 s53, %[d], 0
LA 18
PK 111,16,109,33
M 0,16,4
s_add_u32 s53, s53, s57
LC_ 115
PK 109,16,109,32
M 0,17,5
s_add_u32 s46, %[xlo], s53
PO 111
M 1,28,4
WL 4
LO 17,17,114
LU 114,17
M 1,29,5
s_addc_u32 s47, %[xhi], 0
RA 224,1,9216
HS 25,114
s_bitcmp1_b64 s[92:93], 48
s_cselect_b32 s58, 16, 0
s_lshr_b32 s58, 0xfe987610, s58
v_lshrrev_b32 v3, 6, v0
v_and_b32 v3, 12, v3
v_bfe_u32 v3, s58, v3, 4
v_mad_u32_u24 v3, v3, s97, v21
M 2,40,4
LL 16,92
LL 118,93
ST 104
M 2,41,5
LA 16
PK 114,17,112,33
M 3,52,4
LC_ 118
PK 112,17,112,32
M 3,53,5
WL 6
XL 0,0
v_cmp_gt_u32 vcc, v19, v15
M 4,64,4
WL 3
LO 18,18,115
LU 115,18
M 4,65,5
s_add_u32 s42, s44, s61
RA 228,1,10240
HS 25,115
M 5,76,4
LL 17,94
LL 119,95
M 5,77,5
s_addc_u32 s43, s45, 0
LA 17
PK 115,18,113,33
M 0,18,0
s_mov_b32 m0, %[pm]
LC_ 119
PK 113,18,113,32
M 0,19,1
s_cbranch_vccnz .Lk1s5
.Lk1r5:
DF
M 1,30,0
WL 3
LO 16,16,118
LU 118,16
M 1,31,1
RA 232,1,11264
HS 25,118
M 2,42,0
LL 18,96
LL 122,97
M 2,43,1
LA 18
PK 118,16,116,33
M 3,54,0
LC_ 122
XL 1,64
PK 116,16,116,32
M 3,55,1
RV 252,22,5392
PO 118
M 4,66,0
WL 4
LO 17,17,119
LU 119,17
M 4,67,1
RA 236,1,12288
HS 25,119
M 5,78,0
LL 16,98
LL 123,99
ST 108
M 5,79,1
s_add_u32 s42, s44, s62
LA 16
PK 119,17,117,33
s_addc_u32 s43, s45, 0
M 0,20,2
LC_ 123
PK 117,17,117,32
M 0,21,3
W1 24,0
XL 2,128
PO 119
M 1,32,2
WL 5
LO 18,18,122
LU 122,18
M 1,33,3
RA 216,1,13312
HS 25,122
M 2,44,2
LL 17,100
LL 126,101
ST 112
M 2,45,3
LA 17
PK 122,18,120,33
M 3,56,2
LC_ 126
PK 120,18,120,32
M 3,57,3
XL 3,192
M 4,68,2
WL 4
LO 16,16,123
LU 123,16
M 4,69,3
s_add_u32 s42, s44, s63
RA 220,1,14336
HS 25,123
M 5,80,2
LL 18,102
LL 127,103
M 5,81,3
s_addc_u32 s43, s45, 0
LA 18
PK 123,16,121,33
M 0,22,4
LC_ 127
PK 121,16,121,32
M 0,23,5
XL 4,256
M 1,34,4
WL 3
LO 17,17,126
LU 126,17
M 1,35,5
RA 224,1,15360
HS 25,126
M 2,46,4
LL 16,56
LL 104,57
M 2,47,5
LA 16
PK 126,17,124,33
M 3,58,4
LC_ 104
PK 124,17,124,32
M 3,59,5
ST 116
PO 126
M 4,70,4
WL 3
LO 18,18,127
LU 127,18
M 4,71,5
s_add_u32 s42, s44, s64
RA 228,1,16384
HS 25,127
M 5,82,4
LL 17,58
LL 105,59
M 5,83,5
s_addc_u32 s43, s45, 0
LA 17
PK 127,18,125,33
Z 6,12,0
LC_ 105
XL 5,320
PK 125,18,125,32
M 6,13,1
ST 120
PO 127
Z 7,24,0
WL 3
LO 104,16,104
LU 16,104
M 7,25,1
RA 232,1,17408
HZ 24,16
Z 8,36,0
LL 18,60
LL 108,61
M 8,37,1
LA 18
ds_read_b64 v[14:15], v5 offset:0
Z 9,48,0
LC_ 108
XL 6,384
M 9,49,1
s_add_u32 s42, s44, s65
v_subrev_u32 v5, s39, v5
HX 25
Z 10,60,0
WL 4
LO 105,17,105
LU 17,105
M 10,61,1
s_addc_u32 s43, s45, 0
RA 236,1,18432
HS 24,17
Z 11,72,0
LL 16,62
LL 109,63
M 11,73,1
LA 16
W1 25,1024
M 6,14,2
LC_ 109
XL 7,448
M 6,15,3
s_mul_i32 s53, s50, 25600
v_add_u32 v4, s39, v4
M 7,26,2
WL 4
LO 108,18,108
LU 18,108
M 7,27,3
s_add_u32 s40, %[wlo], s53
RA 216,1,19456
HS 24,18
M 8,38,2
s_addc_u32 s41, %[whi], 0
LL 17,64
LL 112,65
M 8,39,3
s_mov_b32 m0, s38
LA 17
ST 124
M 9,50,2
LC_ 112
XL 8,512
M 9,51,3
DW 0
v_add_f32 v14, v14, v15
s_nop 1
v_add_f32_dpp v14, v14, v14 quad_perm:[1,0,3,2] row_mask:0xf bank_mask:0xf
M 10,62,2
WL 3
LO 109,16,109
LU 16,109
M 10,63,3
s_add_u32 s37, s37, 32768
RA 220,1,20480
HS 24,16
v_add_f32_dpp v14, v14, v14 quad_perm:[2,3,0,1] row_mask:0xf bank_mask:0xf
M 11,74,2
LL 18,66
LL 113,67
M 11,75,3
s_cmp_eq_u32 s37, s75
LA 18
v_add_f32_dpp v14, v14, v14 row_half_mirror row_mask:0xf bank_mask:0xf
M 6,16,4
s_cselect_b32 s37, s74, s37
LC_ 113
XL 9,576
M 6,17,5
DW 1024
v_cvt_f64_f32 v[14:15], v14
M 7,28,4
WL 3
LO 112,17,112
LU 17,112
M 7,29,5
RA 224,1,21504
HS 24,17
M 8,40,4
LL 16,68
LL 116,69
M 8,41,5
LA 16
s_mov_b64 exec, s[54:55]
SP
s_mov_b64 exec, -1
M 9,52,4
LC_ 116
DW 2048
M 9,53,5
XL 10,640
M 10,64,4
WL 3
LO 113,18,113
LU 18,113
M 10,65,5
s_mul_i32 s53, s51, 24
RA 228,1,22528
HS 24,18
M 11,76,4
LL 17,70
LL 117,71
M 11,77,5
s_bitcmp1_b32 s51, 0
LA 17
M 6,18,0
s_addc_u32 s53, s53, 0
LC_ 117
M 6,19,1
s_lshl_b32 s53, s53, 11
M 7,30,0
WL 3
LO 116,16,116
LU 16,116
M 7,31,1
s_bitcmp1_b64 s[90:91], s51
RA 232,1,23552
HS 24,16
M 8,42,0
s_cselect_b64 s[44:45], s[84:85], s[82:83]
LL 18,72
LL 120,73
M 8,43,1
s_add_u32 s44, s44, s53
LA 18
s_addc_u32 s45, s45, 0
s_add_u32 s51, s51, %[d]
s_cmp_eq_u32 s51, %[wrap]
s_cselect_b32 s51, %[rst], s51
M 9,54,0
s_add_u32 s40, s40, 3072
LC_ 120
s_addc_u32 s41, s41, 0
s_add_u32 m0, s38, 3072
M 9,55,1
XL 11,704
M 10,66,0
WL 3
LO 117,17,117
LU 17,117
M 10,67,1
RA 236,1,24576
HS 24,17
M 11,78,0
)"
R"(LL 16,74
LL 121,75
M 11,79,1
LA 16
DW 0
M 6,20,2
LC_ 121
DW 1024
M 6,21,3
DW 2048
v_add_u32 v1, s37, v0
M 7,32,2
WL 3
LO 120,18,120
LU 18,120
M 7,33,3
s_lshl_b32 s53, s52, 8
RA 216,1,0
HS 24,18
M 8,44,2
s_add_u32 s48, %[alo], s53
LL 17,76
LL 124,77
M 8,45,3
s_addc_u32 s49, %[ahi], 0
LA 17
v_and_b32 v22, 0xfffffcff, v1
M 9,56,2
s_add_u32 s52, s52, %[d]
LC_ 124
M 9,57,3
s_cmp_eq_u32 s52, %[wrap]
M 10,68,2
WL 3
LO 121,16,121
LU 16,121
M 10,69,3
s_cselect_b32 s52, %[rst], s52
RA 220,1,1024
HS 24,16
M 11,80,2
LL 18,78
LL 125,79
M 11,81,3
LA 18
RV 252,22,5120
M 6,22,4
LC_ 125
DQ 3072
M 6,23,5
s_add_u32 s50, s50, %[d]
M 7,34,4
WL 4
LO 124,17,124
LU 17,124
M 7,35,5
s_cmp_eq_u32 s50, %[wrap]
RA 224,1,2048
HS 24,17
M 8,46,4
s_cselect_b32 s50, %[rst], s50
LL 16,80
LL 106,81
M 8,47,5
LA 16
v_add_u32 v19, 1, v19
M 9,58,4
s_add_u32 s38, s38, 32768
LC_ 106
M 9,59,5
M 10,70,4
WL 3
LO 125,18,125
LU 18,125
M 10,71,5
RA 228,1,3072
HS 24,18
M 11,82,4
LL 17,82
LL 107,83
M 11,83,5
LA 17
HX 24
Z 0,12,0
s_cmp_eq_u32 s38, s77
LC_ 107
M 0,13,1
s_cselect_b32 s38, s76, s38
WV 42
FP
Z 1,24,0
WL 4
LO 16,16,106
LU 106,16
M 1,25,1
s_sub_u32 s39, 0, s39
RA 232,1,4096
HZ 25,106
Z 2,36,0
LL 18,84
LL 110,85
M 2,37,1
LA 18
PK 106,16,104,33
Z 3,48,0
LC_ 110
PK 104,16,104,32
M 3,49,1
Z 4,60,0
WL 4
LO 17,17,107
LU 107,17
M 4,61,1
s_add_u32 s42, s44, s60
RA 236,1,6144
HS 25,107
Z 5,72,0
LL 16,86
LL 111,87
M 5,73,1
s_addc_u32 s43, s45, 0
LA 16
PK 107,17,105,33
M 0,14,2
LC_ 111
PK 105,17,105,32
M 0,15,3
ST 104
M 1,26,2
WL 3
LO 18,18,110
LU 110,18
M 1,27,3
RA 216,1,7168
HS 25,110
M 2,38,2
LL 17,88
LL 114,89
M 2,39,3
LA 17
PK 110,18,108,33
M 3,50,2
LC_ 114
PK 108,18,108,32
M 3,51,3
FR
PO 110
M 4,62,2
WL 4
LO 16,16,111
LU 111,16
M 4,63,3
s_add_u32 s42, s44, s61
RA 220,1,8192
HS 25,111
M 5,74,2
LL 18,90
LL 115,91
M 5,75,3
s_addc_u32 s43, s45, 0
LA 18
PK 111,16,109,33
M 0,16,4
s_mov_b32 m0, %[pm]
LC_ 115
PK 109,16,109,32
M 0,17,5
DF
PO 111
M 1,28,4
WL 4
LO 17,17,114
LU 114,17
M 1,29,5
RA 224,1,9216
HS 25,114
M 2,40,4
LL 16,92
LL 118,93
M 2,41,5
LA 16
PK 114,17,112,33
M 3,52,4
LC_ 118
PK 112,17,112,32
M 3,53,5
WL 6
ST 108
v_cmp_gt_u32 vcc, v19, v15
M 4,64,4
WL 3
LO 18,18,115
LU 115,18
M 4,65,5
s_add_u32 s42, s44, s62
RA 228,1,10240
HS 25,115
M 5,76,4
LL 17,94
LL 119,95
M 5,77,5
s_cbranch_vccnz .Lk1s6
.Lk1r6:
LA 17
PK 115,18,113,33
M 0,18,0
s_addc_u32 s43, s45, 0
LC_ 119
PK 113,18,113,32
M 0,19,1
W1 24,0
M 1,30,0
WL 4
LO 16,16,118
LU 118,16
M 1,31,1
RA 232,1,11264
HS 25,118
M 2,42,0
LL 18,96
LL 122,97
M 2,43,1
LA 18
PK 118,16,116,33
M 3,54,0
LC_ 122
PK 116,16,116,32
M 3,55,1
RV 252,22,5392
PO 118
M 4,66,0
WL 5
LO 17,17,119
LU 119,17
M 4,67,1
RA 236,1,12288
HS 25,119
M 5,78,0
LL 16,98
LL 123,99
M 5,79,1
LA 16
PK 119,17,117,33
M 0,20,2
WV 28
LC_ 123
PK 117,17,117,32
BW 0
M 0,21,3
ST 112
PO 119
M 1,32,2
WL 5
RV 40,8,6144
LO 18,18,122
LU 122,18
M 1,33,3
s_add_u32 s42, s44, s63
RA 216,1,13312
RV 44,8,6208
HS 25,122
M 2,44,2
s_addc_u32 s43, s45, 0
LL 17,100
LL 126,101
M 2,45,3
LA 17
RV 48,8,6272
PK 122,18,120,33
M 3,56,2
LC_ 126
BR 0
PK 120,18,120,32
M 3,57,3
WV 25
ST 116
BW 1
M 4,68,2
WL 9
LO 16,16,123
LU 123,16
M 4,69,3
s_add_u32 s42, s44, s64
RA 220,1,14336
BR 1
HS 25,123
M 5,80,2
WL 3
LL 18,102
LL 127,103
M 5,81,3
s_addc_u32 s43, s45, 0
LA 18
PK 123,16,121,33
LZ 40,0
s_bitcmp1_b64 s[92:93], 48
BW 2
M 0,22,4
s_cselect_b32 s58, 16, 0
LC_ 127
PK 121,16,121,32
M 0,23,5
s_lshr_b32 s58, 0xda43cb52, s58
RV 40,8,6336
BR 2
M 1,34,4
LO 17,17,126
LU 126,17
M 1,35,5
WL 5
RA 224,1,15360
HS 25,126
LS 44,1
WV 24
BW 3
M 2,46,4
LL 16,56
LL 104,57
M 2,47,5
LA 16
PK 126,17,124,33
M 3,58,4
LC_ 104
RV 44,8,6400
PK 124,17,124,32
M 3,59,5
ds_read_b64 v[14:15], v5 offset:0
BR 3
PO 126
M 4,70,4
WL 9
LO 18,18,127
LU 127,18
M 4,71,5
WL 7
RA 228,1,16384
HS 25,127
LS 48,2
WV 23
BW 4
M 5,82,4
LL 17,58
LL 105,59
M 5,83,5
LA 17
RV 48,8,6464
PK 127,18,125,33
Z 6,12,0
LC_ 105
PK 125,18,125,32
M 6,13,1
BR 4
PO 127
v_and_b32 v11, 0x1f0, v0
v_min_u32 v11, 368, v11
v_lshlrev_b32 v11, 3, v11
Z 7,24,0
WL 9
LO 104,16,104
LU 16,104
M 7,25,1
WL 6
RA 232,1,17408
HZ 24,16
LS 40,3
WV 21
BW 5
Z 8,36,0
LL 18,60
LL 108,61
ST 120
M 8,37,1
LA 18
RV 40,8,6528
Z 9,48,0
LC_ 108
BR 5
M 9,49,1
WL 6
v_subrev_u32 v5, s39, v5
HX 25
LS 44,4
WV 20
BW 6
Z 10,60,0
LO 105,17,105
LU 17,105
v_lshrrev_b32 v3, 9, v0
v_lshl_add_u32 v3, v3, 2, 0
v_bfe_u32 v3, s58, v3, 4
v_mad_u32_u24 v3, v3, s97, v11
M 10,61,1
s_add_u32 s42, s44, s65
RA 236,1,18432
HS 24,17
Z 11,72,0
WV 19
RV 44,8,6592
LL 16,62
LL 109,63
)"
R"(M 11,73,1
s_addc_u32 s43, s45, 0
LA 16
W1 25,1024
BR 6
M 6,14,2
WL 6
LC_ 109
DY
M 6,15,3
s_mul_i32 s53, s50, 25600
v_add_f32 v14, v14, v15
v_add_u32 v4, s39, v4
LS 48,5
s_add_u32 s40, %[wlo], s53
BW 7
M 7,26,2
LO 108,18,108
LU 18,108
M 7,27,3
s_addc_u32 s41, %[whi], 0
RA 216,1,19456
HS 24,18
M 8,38,2
s_mov_b32 m0, s38
RV 48,8,6656
LL 17,64
LL 112,65
M 8,39,3
LA 17
ST 124
BR 7
M 9,50,2
LC_ 112
v_add_f32_dpp v14, v14, v14 quad_perm:[1,0,3,2] row_mask:0xf bank_mask:0xf
M 9,51,3
WL 7
v_add_f32_dpp v14, v14, v14 quad_perm:[2,3,0,1] row_mask:0xf bank_mask:0xf
v_lshrrev_b32 v3, 9, v0
LS 40,6
WV 19
BW 8
M 10,62,2
WL 7
LO 109,16,109
LU 16,109
M 10,63,3
RA 220,1,20480
RV 40,8,6720
HS 24,16
M 11,74,2
BR 8
LL 18,66
LL 113,67
M 11,75,3
WV 17
LA 18
v_add_f32_dpp v14, v14, v14 row_half_mirror row_mask:0xf bank_mask:0xf
v_lshl_add_u32 v3, v3, 2, 8
v_bfe_u32 v3, s58, v3, 4
v_mad_u32_u24 v3, v3, s97, v11
M 6,16,4
WL 6
LC_ 113
DW 0
M 6,17,5
s_add_u32 s37, s37, 32768
v_cvt_f64_f32 v[14:15], v14
LS 44,7
s_cmp_eq_u32 s37, s75
BW 9
M 7,28,4
WL 7
RV 44,8,6784
LO 112,17,112
LU 17,112
M 7,29,5
s_cselect_b32 s37, s74, s37
RA 224,1,21504
HS 24,17
M 8,40,4
BR 9
LL 16,68
LL 116,69
M 8,41,5
WL 6
LA 16
s_mov_b64 exec, s[54:55]
SP
s_mov_b64 exec, -1
LS 48,8
WV 15
BW 10
M 9,52,4
LC_ 116
DY
M 9,53,5
DW 1024
RV 48,8,6848
M 10,64,4
WL 8
LO 113,18,113
LU 18,113
M 10,65,5
RA 228,1,22528
RA 240,7,0
HS 24,18
M 11,76,4
WV 16
BR 10
LL 17,70
LL 117,71
M 11,77,5
WL 7
LA 17
DW 2048
M 6,18,0
s_mul_i32 s53, s51, 24
LC_ 117
M 6,19,1
s_bitcmp1_b32 s51, 0
LS 40,9
s_addc_u32 s53, s53, 0
BW 11
M 7,30,0
WL 3
RA 244,7,256
LO 116,16,116
LU 16,116
M 7,31,1
s_lshl_b32 s53, s53, 11
RA 232,1,23552
HS 24,16
M 8,42,0
s_bitcmp1_b64 s[90:91], s51
BR 11
LL 18,72
LL 120,73
M 8,43,1
s_cselect_b64 s[44:45], s[84:85], s[82:83]
LA 18
s_add_u32 s44, s44, s53
s_addc_u32 s45, s45, 0
s_add_u32 s51, s51, %[d]
s_cmp_eq_u32 s51, %[wrap]
s_cselect_b32 s51, %[rst], s51
LS 44,10
s_add_u32 s40, s40, 3072
RA 248,7,512
M 9,54,0
s_addc_u32 s41, s41, 0
LC_ 120
s_add_u32 m0, s38, 3072
M 9,55,1
DW 0
M 10,66,0
WL 7
LO 117,17,117
LU 17,117
M 10,67,1
WL 3
RA 236,1,24576
HS 24,17
LS 48,11
RA 252,7,768
M 11,78,0
LL 16,74
LL 121,75
s_nop 3
v_mul_f32 v36, 0xbb2aaaab, v36
M 11,79,1
LA 16
DW 1024
v_mul_f32 v37, 0xbb2aaaab, v37
M 6,20,2
LC_ 121
DW 2048
v_mul_f32 v38, 0xbb2aaaab, v38
v_mul_f32 v39, 0xbb2aaaab, v39
M 6,21,3
s_lshl_b32 s53, s52, 8
v_add_u32 v1, s37, v0
LK 40,240,0,0
LK 44,242,0,2
s_add_u32 s48, %[alo], s53
RA 240,7,1024
LK 48,244,1,0
s_nop 0
CF 128,40,41
LK 248,246,1,2
s_addc_u32 s49, %[ahi], 0
RA 244,7,1280
CF 129,42,43
M 7,32,2
WL 6
LO 120,18,120
LU 18,120
M 7,33,3
s_add_u32 s52, s52, %[d]
RA 216,1,0
HS 24,18
M 8,44,2
s_cmp_eq_u32 s52, %[wrap]
LL 17,76
LL 124,77
M 8,45,3
s_cselect_b32 s52, %[rst], s52
LA 17
DQ 3072
M 9,56,2
LC_ 124
v_and_b32 v22, 0xfffffcff, v1
CF 130,44,45
M 9,57,3
CF 131,46,47
LK 40,248,2,0
CF 132,48,49
LK 44,250,2,2
RA 248,7,1536
CF 133,50,51
GZ 0
WL 8
CF 134,248,249
CF 135,250,251
LK 48,252,3,0
CF 136,40,41
LK 248,254,3,2
RA 252,7,1792
CF 137,42,43
GS 1
WL 6
CF 138,44,45
CF 139,46,47
LK 40,240,4,0
CF 140,48,49
LK 44,242,4,2
RA 240,7,2048
CF 141,50,51
GS 2
WL 6
CF 142,248,249
CF 143,250,251
LK 48,244,5,0
CF 144,40,41
LK 248,246,5,2
RA 244,7,2304
CF 145,42,43
GS 3
CF 146,44,45
CF 147,46,47
M 10,68,2
WL 3
LO 121,16,121
LU 16,121
M 10,69,3
s_add_u32 s50, s50, %[d]
RA 220,1,1024
HS 24,16
LK 40,248,6,0
WL 3
CF 148,48,49
LK 44,250,6,2
s_cmp_eq_u32 s50, %[wrap]
RA 248,7,2560
CF 149,50,51
GS 4
s_cselect_b32 s50, %[rst], s50
CF 150,248,249
CF 151,250,251
LK 48,252,7,0
CF 152,40,41
LK 248,254,7,2
RA 252,7,2816
CF 153,42,43
GS 5
WL 4
CF 154,44,45
CF 155,46,47
LK 40,240,8,0
CF 156,48,49
LK 44,242,8,2
CF 157,50,51
GS 6
CF 158,248,249
CF 159,250,251
M 11,80,2
LL 18,78
LL 125,79
M 11,81,3
WL 3
LA 18
RV 252,22,5120
LK 48,244,9,0
CF 160,40,41
LK 248,246,9,2
CF 161,42,43
GS 7
CF 162,44,45
CF 163,46,47
M 6,22,4
LC_ 125
M 6,23,5
v_add_u32 v19, 1, v19
M 7,34,4
WL 4
LO 124,17,124
LU 17,124
M 7,35,5
s_add_u32 s38, s38, 32768
RA 224,1,2048
HS 24,17
M 8,46,4
s_cmp_eq_u32 s38, s77
LL 16,80
LL 106,81
M 8,47,5
s_cselect_b32 s38, s76, s38
LA 16
FP
LK 40,248,10,0
CF 164,48,49
LK 44,250,10,2
s_sub_u32 s39, 0, s39
CF 165,50,51
GS 8
CF 166,248,249
CF 167,250,251
M 9,58,4
LC_ 106
M 9,59,5
M 10,70,4
WL 4
LO 125,18,125
LU 18,125
M 10,71,5
RA 228,1,3072
HS 24,18
LK 48,252,11,0
CF 168,40,41
LK 248,254,11,2
CF 169,42,43
GS 9
CF 170,44,45
CF 171,46,47
M 11,82,4
LL 17,82
LL 107,83
M 11,83,5
LA 17
HX 24
Z 0,12,0
LC_ 107
RV 36,8,6912
M 0,13,1
GS 10
CF 172,48,49
Z 1,24,0
WL 4
LO 16,16,106
LU 106,16
M 1,25,1
RA 232,1,4096
HZ 25,106
Z 2,36,0
LL 18,84
LL 110,85
M 2,37,1
LA 18
PK 106,16,104,33
Z 3,48,0
LC_ 110
PK 104,16,104,32
M 3,49,1
CF 173,50,51
CF 174,248,249
Z 4,60,0
WL 4
LO 17,17,107
LU 107,17
M 4,61,1
s_add_u32 s42, s44, s60
RA 236,1,6144
HS 25,107
Z 5,72,0
LL 16,86
LL 111,87
M 5,73,1
s_addc_u32 s43, s45, 0
LA 16
PK 107,17,105,33
M 0,14,2
LC_ 111
PK 105,17,105,32
M 0,15,3
ST 104
CF 175,250,251
M 1,26,2
WL 3
LO 18,18,110
LU 110,18
)"
R"(M 1,27,3
RA 216,1,7168
HS 25,110
GS 11
M 2,38,2
LL 17,88
LL 114,89
M 2,39,3
LA 17
PK 110,18,108,33
M 3,50,2
LC_ 114
PK 108,18,108,32
M 3,51,3
FR
PO 110
M 4,62,2
WL 4
LO 16,16,111
LU 111,16
M 4,63,3
s_add_u32 s42, s44, s61
RA 220,1,8192
HS 25,111
M 5,74,2
LL 18,90
LL 115,91
M 5,75,3
s_addc_u32 s43, s45, 0
LA 18
PK 111,16,109,33
M 0,16,4
s_mov_b32 m0, %[pm]
LC_ 115
PK 109,16,109,32
M 0,17,5
DF
PO 111
M 1,28,4
WL 4
LO 17,17,114
LU 114,17
M 1,29,5
RA 224,1,9216
HS 25,114
M 2,40,4
LL 16,92
LL 118,93
M 2,41,5
LA 16
PK 114,17,112,33
M 3,52,4
LC_ 118
PK 112,17,112,32
M 3,53,5
WL 6
ST 108
v_cmp_gt_u32 vcc, v19, v15
M 4,64,4
WL 3
LO 18,18,115
LU 115,18
M 4,65,5
s_add_u32 s42, s44, s62
RA 228,1,10240
HS 25,115
M 5,76,4
LL 17,94
LL 119,95
v_cndmask_b32 v52, v52, v53, s[78:79]
M 5,77,5
s_cbranch_vccnz .Lk1s7
.Lk1r7:
LA 17
PK 115,18,113,33
M 0,18,0
s_addc_u32 s43, s45, 0
LC_ 119
PK 113,18,113,32
v_cndmask_b32 v54, v54, v55, s[78:79]
M 0,19,1
W1 24,0
v_cndmask_b32 v52, v52, v54, s[80:81]
M 1,30,0
WL 4
LO 16,16,118
LU 118,16
v_mul_f32 v52, 0x3b2aaaab, v52
M 1,31,1
RA 232,1,11264
HS 25,118
v_med3_f32 v53, v52, v39, v37
M 2,42,0
LL 18,96
LL 122,97
LG 52,53
M 2,43,1
LA 18
PK 118,16,116,33
C 52,52,52
M 3,54,0
LC_ 122
PK 116,16,116,32
v_lshlrev_b32 v52, 16, v52
M 3,55,1
RV 252,22,5392
PO 118
v_add_f32 v52, v36, v52
M 4,66,0
WL 5
LO 17,17,119
LU 119,17
C 52,52,52
M 4,67,1
RA 236,1,12288
HS 25,119
M 5,78,0
LL 16,98
LL 123,99
v_lshlrev_b32 v52, 16, v52
M 5,79,1
LA 16
PK 119,17,117,33
v_sqrt_f32 v52, v52
M 0,20,2
LC_ 123
PK 117,17,117,32
C 52,52,52
M 0,21,3
ST 112
PO 119
v_lshlrev_b32 v52, 16, v52
M 1,32,2
WL 4
LO 18,18,122
LU 122,18
v_rcp_f32 v52, v52
M 1,33,3
s_add_u32 s42, s44, s63
RA 216,1,13312
HS 25,122
v_mul_f32 v52, v38, v52
M 2,44,2
s_addc_u32 s43, s45, 0
LL 17,100
LL 126,101
CF 52,52,52
M 2,45,3
LA 17
PK 122,18,120,33
ds_bpermute_b32 v13, v28, v52
M 3,56,2
LC_ 126
PK 120,18,120,32
M 3,57,3
ST 116
M 4,68,2
WL 4
LO 16,16,123
LU 123,16
M 4,69,3
s_add_u32 s42, s44, s64
RA 220,1,14336
HS 25,123
M 5,80,2
LL 18,102
LL 127,103
M 5,81,3
s_addc_u32 s43, s45, 0
LA 18
PK 123,16,121,33
M 0,22,4
LC_ 127
PK 121,16,121,32
M 0,23,5
ST 120
M 1,34,4
WL 3
LO 17,17,126
LU 126,17
M 1,35,5
RA 224,1,15360
HS 25,126
XS 128
M 2,46,4
LL 16,56
LL 104,57
M 2,47,5
LA 16
PK 126,17,124,33
M 3,58,4
LC_ 104
PK 124,17,124,32
XS 129
M 3,59,5
ds_read_b64 v[14:15], v5 offset:0
PO 126
M 4,70,4
WL 4
LO 18,18,127
LU 127,18
M 4,71,5
s_add_u32 s42, s44, s65
RA 228,1,16384
HS 25,127
XS 130
M 5,82,4
LL 17,58
LL 105,59
M 5,83,5
s_addc_u32 s43, s45, 0
LA 17
PK 127,18,125,33
Z 6,12,0
LC_ 105
PK 125,18,125,32
XS 131
M 6,13,1
PO 127
v_subrev_u32 v5, s39, v5
Z 7,24,0
WL 4
LO 104,16,104
LU 16,104
XS 132
M 7,25,1
RA 232,1,17408
HZ 24,16
Z 8,36,0
WL 4
LL 18,60
LL 108,61
M 8,37,1
s_mul_i32 s53, s50, 25600
LA 18
v_add_f32 v14, v14, v15
XS 133
Z 9,48,0
s_add_u32 s40, %[wlo], s53
LC_ 108
v_add_f32_dpp v14, v14, v14 quad_perm:[1,0,3,2] row_mask:0xf bank_mask:0xf
M 9,49,1
s_addc_u32 s41, %[whi], 0
v_add_f32_dpp v14, v14, v14 quad_perm:[2,3,0,1] row_mask:0xf bank_mask:0xf
HX 25
Z 10,60,0
WL 3
LO 105,17,105
LU 17,105
XS 134
M 10,61,1
s_mov_b32 m0, s38
RA 236,1,18432
HS 24,17
Z 11,72,0
LL 16,62
LL 109,63
M 11,73,1
LA 16
W1 25,1024
XS 135
M 6,14,2
LC_ 109
ST 124
M 6,15,3
DW 0
v_add_f32_dpp v14, v14, v14 row_half_mirror row_mask:0xf bank_mask:0xf
v_min_u32 v35, v35, v13
M 7,26,2
WL 4
LO 108,18,108
LU 18,108
M 7,27,3
s_add_u32 s37, s37, 32768
RA 216,1,19456
HS 24,18
M 8,38,2
s_cmp_eq_u32 s37, s75
LL 17,64
LL 112,65
XS 136
M 8,39,3
s_cselect_b32 s37, s74, s37
LA 17
v_cvt_f64_f32 v[14:15], v14
M 9,50,2
s_mul_i32 s53, %[d], 0
LC_ 112
s_mov_b64 exec, s[54:55]
SP
s_mov_b64 exec, -1
M 9,51,3
s_add_u32 s53, s53, s57
v_add_u32 v4, s39, v4
XS 137
M 10,62,2
WL 3
LO 109,16,109
LU 16,109
M 10,63,3
s_add_u32 s46, %[xlo], s53
RA 220,1,20480
HS 24,16
M 11,74,2
LL 18,66
LL 113,67
XS 138
M 11,75,3
s_addc_u32 s47, %[xhi], 0
LA 18
XS 139
M 6,16,4
s_bitcmp1_b64 s[92:93], 48
LC_ 113
XS 140
M 6,17,5
s_cselect_b32 s58, 16, 0
XS 141
XS 142
M 7,28,4
WL 3
LO 112,17,112
LU 17,112
M 7,29,5
s_lshr_b32 s58, 0xda43cb52, s58
RA 224,1,21504
HS 24,17
M 8,40,4
LL 16,68
LL 116,69
M 8,41,5
LA 16
v_lshrrev_b32 v3, 6, v0
M 9,52,4
LC_ 116
v_and_b32 v3, 12, v3
v_bfe_u32 v3, s58, v3, 4
v_mad_u32_u24 v3, v3, s97, v21
M 9,53,5
DW 1024
XS 143
M 10,64,4
WL 3
LO 113,18,113
LU 18,113
M 10,65,5
RA 228,1,22528
HS 24,18
M 11,76,4
LL 17,70
LL 117,71
M 11,77,5
LA 17
XL 12,0
M 6,18,0
LC_ 117
DW 2048
M 6,19,1
XL 13,64
XS 144
M 7,30,0
WL 3
LO 116,16,116
LU 16,116
M 7,31,1
s_mul_i32 s53, s51, 24
RA 232,1,23552
HS 24,16
M 8,42,0
s_bitcmp1_b32 s51, 0
LL 18,72
LL 120,73
M 8,43,1
s_addc_u32 s53, s53, 0
LA 18
s_lshl_b32 s53, s53, 11
s_bitcmp1_b64 s[90:91], s51
s_cselect_b64 s[44:45], s[84:85], s[82:83]
s_add_u32 s44, s44, s53
s_addc_u32 s45, s45, 0
s_add_u32 s51, s51, %[d]
s_cmp_eq_u32 s51, %[wrap]
s_cselect_b32 s51, %[rst], s51
XS 145
M 9,54,0
s_add_u32 s40, s40, 3072
LC_ 120
s_addc_u32 s41, s41, 0
s_add_u32 m0, s38, 3072
M 9,55,1
XL 14,128
XS 146
M 10,66,0
WL 3
LO 117,17,117
LU 17,117
M 10,67,1
RA 236,1,24576
HS 24,17
M 11,78,0
)"
R"(LL 16,74
LL 121,75
XS 147
M 11,79,1
LA 16
DW 0
M 6,20,2
LC_ 121
XL 15,192
M 6,21,3
DW 1024
v_add_u32 v1, s37, v0
XS 148
M 7,32,2
WL 3
LO 120,18,120
LU 18,120
M 7,33,3
RA 216,1,0
HS 24,18
M 8,44,2
LL 17,76
LL 124,77
XS 149
M 8,45,3
LA 17
XL 16,256
M 9,56,2
LC_ 124
DW 2048
XS 150
M 9,57,3
XL 17,320
v_and_b32 v22, 0xfffffcff, v1
M 10,68,2
WL 3
LO 121,16,121
LU 16,121
M 10,69,3
s_lshl_b32 s53, s52, 8
RA 220,1,1024
HS 24,16
XS 151
M 11,80,2
LL 18,78
LL 125,79
M 11,81,3
s_add_u32 s48, %[alo], s53
LA 18
RV 252,22,5120
M 6,22,4
s_addc_u32 s49, %[ahi], 0
LC_ 125
XS 152
M 6,23,5
s_add_u32 s52, s52, %[d]
XS 153
XS 154
M 7,34,4
WL 4
LO 124,17,124
LU 17,124
M 7,35,5
s_cmp_eq_u32 s52, %[wrap]
RA 224,1,2048
HS 24,17
M 8,46,4
s_cselect_b32 s52, %[rst], s52
LL 16,80
LL 106,81
M 8,47,5
LA 16
DQ 3072
M 9,58,4
LC_ 106
XL 18,384
M 9,59,5
s_add_u32 s50, s50, %[d]
XS 155
XS 156
M 10,70,4
WL 3
LO 125,18,125
LU 18,125
M 10,71,5
RA 228,1,3072
HS 24,18
M 11,82,4
LL 17,82
LL 107,83
M 11,83,5
LA 17
HX 24
s_cmp_eq_u32 s50, %[wrap]
s_cselect_b32 s50, %[rst], s50
XL 19,448
Z 2,36,0
WV 40
LC_ 107
XL 20,512
v_add_u32 v19, 1, v19
s_add_u32 s38, s38, 32768
s_cmp_eq_u32 s38, s77
s_cselect_b32 s38, s76, s38
FP
s_sub_u32 s39, 0, s39
M 2,37,1
XL 21,576
XS 157
Z 3,48,0
WL 4
LO 16,16,106
LU 106,16
M 3,49,1
RA 232,1,4096
HZ 25,106
Z 4,60,0
LL 18,84
LL 110,85
XS 158
M 4,61,1
LA 18
PK 106,16,104,33
Z 5,72,0
WL 5
LC_ 110
XL 22,640
PK 104,16,104,32
M 5,73,1
RA 236,1,6144
XS 159
M 2,38,2
WL 5
LO 17,17,107
LU 107,17
M 2,39,3
FR
HS 25,107
M 3,50,2
LL 16,86
LL 111,87
XS 160
M 3,51,3
LA 16
PK 107,17,105,33
M 4,62,2
LC_ 111
XL 23,704
PK 105,17,105,32
XS 161
M 4,63,3
s_add_u32 s42, s44, s60
RA 216,1,7168
XS 162
M 5,74,2
WL 4
LO 18,18,110
LU 110,18
M 5,75,3
s_addc_u32 s43, s45, 0
RA 220,1,8192
HS 25,110
M 2,40,4
LL 17,88
LL 114,89
M 2,41,5
LA 17
PK 110,18,108,33
M 3,52,4
LC_ 114
PK 108,18,108,32
XS 163
M 3,53,5
RA 224,1,9216
PO 110
M 4,64,4
WL 5
LO 16,16,111
LU 111,16
ST 104
XS 164
M 4,65,5
s_add_u32 s42, s44, s61
HS 25,111
v_cmp_gt_u32 vcc, v19, v15
M 5,76,4
WL 3
LL 18,90
LL 115,91
M 5,77,5
s_addc_u32 s43, s45, 0
LA 18
PK 111,16,109,33
XS 165
M 2,42,0
s_cbranch_vccnz .Lk1s8
.Lk1r8:
LC_ 115
PK 109,16,109,32
M 2,43,1
WV 17
RA 228,1,10240
PO 111
BW 12
M 3,54,0
WL 5
LO 17,17,114
LU 114,17
XS 166
M 3,55,1
RA 232,1,11264
RV 40,8,6144
HS 25,114
M 4,66,0
RV 44,8,6208
LL 16,92
LL 118,93
M 4,67,1
LA 16
RV 48,8,6272
PK 114,17,112,33
XS 167
M 5,78,0
WL 6
LC_ 118
BR 12
PK 112,17,112,32
M 5,79,1
WV 15
RV 252,22,5392
ds_read_b64 v[14:15], v5 offset:0
W1 24,0
BW 13
XS 168
M 2,44,2
BR 13
LO 18,18,115
LU 115,18
M 2,45,3
WL 5
RA 236,1,12288
HS 25,115
LZ 40,12
WV 14
BW 14
M 3,56,2
LL 17,94
LL 119,95
XS 169
M 3,57,3
LA 17
RV 40,8,6336
PK 115,18,113,33
M 4,68,2
LC_ 119
BR 14
PK 113,18,113,32
M 4,69,3
WL 6
RA 216,1,13312
v_subrev_u32 v5, s39, v5
XS 170
LS 44,13
WV 12
BW 15
M 5,80,2
WL 7
LO 16,16,118
LU 118,16
ST 108
M 5,81,3
s_add_u32 s42, s44, s62
RA 220,1,14336
HS 25,118
s_addc_u32 s43, s45, 0
RV 44,8,6400
M 2,46,4
BR 15
LL 18,96
LL 122,97
XS 171
M 2,47,5
WL 5
LA 18
PK 118,16,116,33
LS 48,14
WV 11
BW 16
M 3,58,4
LC_ 122
PK 116,16,116,32
XS 172
M 3,59,5
RA 224,1,15360
RV 48,8,6464
PO 118
M 4,70,4
LO 17,17,119
LU 119,17
ST 112
M 4,71,5
s_add_u32 s42, s44, s63
BR 16
HS 25,119
XS 173
M 5,82,4
WL 6
LL 16,98
LL 123,99
M 5,83,5
s_addc_u32 s43, s45, 0
LA 16
PK 119,17,117,33
LS 40,15
WV 10
BW 17
Z 8,36,0
LC_ 123
PK 117,17,117,32
v_add_f32 v14, v14, v15
s_nop 1
v_add_f32_dpp v14, v14, v14 quad_perm:[1,0,3,2] row_mask:0xf bank_mask:0xf
s_nop 1
v_add_f32_dpp v14, v14, v14 quad_perm:[2,3,0,1] row_mask:0xf bank_mask:0xf
s_nop 1
v_add_f32_dpp v14, v14, v14 row_half_mirror row_mask:0xf bank_mask:0xf
v_cvt_f64_f32 v[14:15], v14
s_mov_b64 exec, s[54:55]
SP
s_mov_b64 exec, -1
RV 40,8,6528
XS 174
M 8,37,1
WV 9
RA 228,1,16384
BR 17
PO 119
Z 9,48,0
WL 6
LO 18,18,122
LU 122,18
XS 175
M 9,49,1
s_mul_i32 s53, %[d], 196608
RA 232,1,17408
HS 25,122
LS 44,16
s_add_u32 s53, s53, s57
BW 18
Z 10,60,0
s_add_u32 s46, %[xlo], s53
RV 44,8,6592
LL 17,100
LL 126,101
M 10,61,1
s_addc_u32 s47, %[xhi], 0
LA 17
BR 18
PK 122,18,120,33
Z 0,72,0
WL 5
LC_ 126
PK 120,18,120,32
M 0,73,1
s_bitcmp1_b64 s[92:93], 48
RA 236,1,18432
LS 48,17
s_cselect_b32 s58, 16, 0
WV 8
BW 19
M 8,38,2
RV 48,8,6656
LO 16,16,123
LU 123,16
M 8,39,3
s_lshr_b32 s58, 0xfe987610, s58
BR 19
HS 25,123
v_lshrrev_b32 v3, 6, v0
v_and_b32 v3, 12, v3
v_bfe_u32 v3, s58, v3, 4
v_mad_u32_u24 v3, v3, s97, v21
M 9,50,2
s_mul_i32 s53, s50, 25600
LL 18,102
LL 127,103
s_add_u32 s40, %[wlo], s53
s_addc_u32 s41, %[whi], 0
s_mov_b32 m0, s38
M 9,51,3
WL 5
LA 18
PK 123,16,121,33
LS 40,18
WV 7
BW 20
M 10,62,2
LC_ 127
RV 40,8,6720
PK 121,16,121,32
)"
R"(M 10,63,3
WV 6
RA 216,1,19456
BR 20
DW 0
M 0,74,2
WL 6
LO 17,17,126
LU 126,17
ST 116
M 0,75,3
s_add_u32 s42, s44, s64
RA 220,1,20480
HS 25,126
s_addc_u32 s43, s45, 0
LS 44,19
s_add_u32 s37, s37, 32768
BW 21
M 8,40,4
s_cmp_eq_u32 s37, s75
LL 16,64
LL 112,65
s_cselect_b32 s37, s74, s37
M 8,41,5
LA 16
XL 24,0
DW 1024
RV 44,8,6784
PK 126,17,124,33
M 9,52,4
LC_ 112
BR 21
PK 124,17,124,32
M 9,53,5
WL 6
RA 224,1,21504
PO 126
LS 48,20
WV 9
BW 22
M 10,64,4
LO 18,18,127
LU 127,18
ST 120
M 10,65,5
WV 9
DW 2048
RV 48,8,6848
HS 25,127
M 0,76,4
WL 8
RA 240,7,0
LL 17,66
LL 113,67
M 0,77,5
s_add_u32 s40, s40, 3072
LA 17
BR 22
PK 127,18,125,33
M 8,42,0
s_addc_u32 s41, s41, 0
LC_ 113
PK 125,18,125,32
s_add_u32 m0, s38, 3072
M 8,43,1
WL 7
RA 228,1,22528
XL 25,64
PO 127
LS 40,21
s_add_u32 s42, s44, s65
BW 23
M 9,54,0
RA 244,7,256
LO 112,16,112
LU 16,112
M 9,55,1
s_addc_u32 s43, s45, 0
RA 232,1,23552
BR 23
HZ 24,16
M 10,66,0
WL 6
DW 0
LL 18,68
LL 116,69
M 10,67,1
s_lshl_b32 s53, s52, 8
LA 18
HX 25
LS 44,22
s_add_u32 s48, %[alo], s53
RA 248,7,512
M 0,78,0
WL 2
LC_ 116
W1 25,1024
M 0,79,1
s_addc_u32 s49, %[ahi], 0
RA 236,1,24576
v_add_u32 v4, s39, v4
LS 48,23
s_add_u32 s52, s52, %[d]
RA 252,7,768
M 8,44,2
LO 113,17,113
LU 17,113
s_cmp_eq_u32 s52, %[wrap]
s_cselect_b32 s52, %[rst], s52
DW 1024
v_mul_f32 v36, 0xbb2aaaab, v36
M 8,45,3
XL 26,128
HS 24,17
v_mul_f32 v37, 0xbb2aaaab, v37
M 9,56,2
LL 16,70
LL 117,71
v_mul_f32 v38, 0xbb2aaaab, v38
v_mul_f32 v39, 0xbb2aaaab, v39
M 9,57,3
LA 16
DW 2048
LK 40,240,12,0
LK 44,242,12,2
RA 240,7,1024
v_add_u32 v1, s37, v0
LK 48,244,13,0
s_nop 0
CF 176,40,41
LK 248,246,13,2
RA 244,7,1280
CF 177,42,43
M 10,68,2
LC_ 117
RA 216,1,0
M 10,69,3
ST 124
v_and_b32 v22, 0xfffffcff, v1
M 0,80,2
WL 6
LO 116,18,116
LU 18,116
M 0,81,3
s_mul_i32 s53, s51, 24
RA 220,1,1024
HS 24,18
s_bitcmp1_b32 s51, 0
s_addc_u32 s53, s53, 0
s_lshl_b32 s53, s53, 11
s_bitcmp1_b64 s[90:91], s51
s_cselect_b64 s[44:45], s[84:85], s[82:83]
s_add_u32 s44, s44, s53
s_addc_u32 s45, s45, 0
s_add_u32 s51, s51, %[d]
s_cmp_eq_u32 s51, %[wrap]
s_cselect_b32 s51, %[rst], s51
DQ 3072
CF 178,44,45
M 8,46,4
LL 17,72
LL 120,73
CF 179,46,47
M 8,47,5
LA 17
RV 252,22,5120
LK 40,248,14,0
CF 180,48,49
LK 44,250,14,2
RA 248,7,1536
CF 181,50,51
GZ 12
WL 9
CF 182,248,249
CF 183,250,251
LK 48,252,15,0
CF 184,40,41
LK 248,254,15,2
RA 252,7,1792
CF 185,42,43
GS 13
WL 8
CF 186,44,45
CF 187,46,47
LK 40,240,16,0
CF 188,48,49
LK 44,242,16,2
RA 240,7,2048
CF 189,50,51
GS 14
WL 8
CF 190,248,249
CF 191,250,251
LK 48,244,17,0
CF 192,40,41
LK 248,246,17,2
RA 244,7,2304
CF 193,42,43
GS 15
CF 194,44,45
CF 195,46,47
M 9,58,4
WL 3
LC_ 120
XL 27,192
M 9,59,5
s_add_u32 s50, s50, %[d]
RA 224,1,2048
s_cmp_eq_u32 s50, %[wrap]
s_cselect_b32 s50, %[rst], s50
v_add_u32 v19, 1, v19
LK 40,248,18,0
WL 4
CF 196,48,49
LK 44,250,18,2
s_add_u32 s38, s38, 32768
RA 248,7,2560
CF 197,50,51
GS 16
s_cmp_eq_u32 s38, s77
CF 198,248,249
CF 199,250,251
LK 48,252,19,0
CF 200,40,41
LK 248,254,19,2
s_cselect_b32 s38, s76, s38
RA 252,7,2816
CF 201,42,43
GS 17
WL 5
CF 202,44,45
CF 203,46,47
LK 40,240,20,0
CF 204,48,49
LK 44,242,20,2
CF 205,50,51
GS 18
CF 206,248,249
CF 207,250,251
M 10,70,4
LO 117,16,117
LU 16,117
M 10,71,5
FP
HS 24,16
M 0,82,4
LL 18,74
LL 121,75
M 0,83,5
s_sub_u32 s39, 0, s39
LA 18
RA 228,1,3072
Z 2,36,0
LC_ 121
XL 28,256
M 2,37,1
WL 8
LK 48,244,21,0
CF 208,40,41
LK 248,246,21,2
CF 209,42,43
GS 19
CF 210,44,45
CF 211,46,47
Z 3,48,0
WL 7
LO 120,17,120
LU 17,120
M 3,49,1
WL 5
RA 232,1,4096
HS 24,17
LK 40,248,22,0
CF 212,48,49
LK 44,250,22,2
CF 213,50,51
GS 20
CF 214,248,249
CF 215,250,251
Z 4,60,0
LL 16,76
LL 124,77
M 4,61,1
LA 16
FR
Z 6,72,0
WL 4
LC_ 124
XL 29,320
M 6,73,1
RA 236,1,6144
LK 48,252,23,0
CF 216,40,41
LK 248,254,23,2
CF 217,42,43
GS 21
CF 218,44,45
CF 219,46,47
M 2,38,2
WL 5
LO 121,18,121
LU 18,121
M 2,39,3
RV 36,8,6912
HS 24,18
M 3,50,2
LL 17,78
LL 125,79
M 3,51,3
LA 17
RA 216,1,7168
GS 22
M 4,62,2
WL 5
LC_ 125
v_cmp_gt_u32 vcc, v19, v15
M 4,63,3
XL 30,384
CF 220,48,49
M 6,74,2
WL 4
LO 124,16,124
LU 16,124
M 6,75,3
RA 220,1,8192
HS 24,16
M 2,40,4
s_cbranch_vccnz .Lk1s9
.Lk1r9:
LL 18,88
LL 114,89
M 2,41,5
LA 18
ds_read_b64 v[14:15], v5 offset:0
M 3,52,4
LC_ 114
v_subrev_u32 v5, s39, v5
M 3,53,5
RA 224,1,9216
CF 221,50,51
M 4,64,4
WL 5
LO 125,17,125
LU 17,125
M 4,65,5
XL 31,448
HS 24,17
M 6,76,4
WL 4
LL 16,90
LL 115,91
M 6,77,5
LA 16
RA 228,1,10240
M 2,42,0
LC_ 115
HX 24
M 2,43,1
W1 24,0
CF 222,248,249
M 3,54,0
WL 5
LO 18,18,114
LU 114,18
M 3,55,1
RA 232,1,11264
HZ 25,114
M 4,66,0
LL 17,92
LL 118,93
M 4,67,1
LA 17
PK 114,18,112,33
M 6,78,0
WL 4
LC_ 118
PK 112,18,112,32
M 6,79,1
RV 252,22,5392
v_add_f32 v14, v14, v15
M 2,44,2
WL 5
LO 16,16,115
LU 115,16
M 2,45,3
RA 236,1,12288
HS 25,115
M 3,56,2
LL 18,94
LL 119,95
M 3,57,3
LA 18
PK 115,16,113,33
M 4,68,2
LC_ 119
XL 32,512
PK 113,16,113,32
v_add_f32_dpp v14, v14, v14 quad_perm:[1,0,3,2] row_mask:0xf bank_mask:0xf
M 4,69,3
s_add_u32 s42, s44, s62
)"
R"(RA 216,1,13312
v_add_f32_dpp v14, v14, v14 quad_perm:[2,3,0,1] row_mask:0xf bank_mask:0xf
M 6,80,2
WL 3
LO 17,17,118
LU 118,17
CF 223,250,251
M 6,81,3
s_addc_u32 s43, s45, 0
RA 220,1,14336
HS 25,118
GS 23
ST 112
v_add_f32_dpp v14, v14, v14 row_half_mirror row_mask:0xf bank_mask:0xf
s_nop 5
v_cndmask_b32 v52, v52, v53, s[78:79]
M 2,46,4
LL 16,96
LL 122,97
v_cndmask_b32 v54, v54, v55, s[78:79]
M 2,47,5
LA 16
PK 118,17,116,33
v_cndmask_b32 v52, v52, v54, s[80:81]
M 3,58,4
LC_ 122
PK 116,17,116,32
v_cvt_f64_f32 v[14:15], v14
v_mul_f32 v52, 0x3b2aaaab, v52
M 3,59,5
RA 224,1,15360
PO 118
v_med3_f32 v53, v52, v39, v37
M 4,70,4
WL 5
LO 18,18,119
LU 119,18
s_mov_b64 exec, s[54:55]
SP
s_mov_b64 exec, -1
XL 33,576
LG 52,53
s_nop 0
C 52,52,52
M 4,71,5
s_add_u32 s42, s44, s63
HS 25,119
v_lshlrev_b32 v52, 16, v52
M 6,82,4
WL 3
LL 17,98
LL 123,99
v_add_f32 v52, v36, v52
M 6,83,5
s_addc_u32 s43, s45, 0
LA 17
PK 119,18,117,33
C 52,52,52
Z 8,36,0
LC_ 123
PK 117,18,117,32
v_lshlrev_b32 v52, 16, v52
M 8,37,1
RA 228,1,16384
PO 119
v_sqrt_f32 v52, v52
s_nop 0
C 52,52,52
Z 9,48,0
WL 4
LO 16,16,122
LU 122,16
v_lshlrev_b32 v52, 16, v52
M 9,49,1
RA 232,1,17408
HS 25,122
v_rcp_f32 v52, v52
Z 10,60,0
XL 34,640
LL 18,56
LL 126,57
v_mul_f32 v52, v38, v52
M 10,61,1
LA 18
PK 122,16,120,33
CF 52,52,52
Z 11,72,0
WL 2
LC_ 126
PK 120,16,120,32
ds_bpermute_b32 v13, v28, v52
M 11,73,1
RA 236,1,18432
M 8,38,2
LO 17,17,123
LU 123,17
M 8,39,3
ST 116
HS 25,123
M 9,50,2
s_mul_i32 s53, s50, 25600
LL 16,58
LL 127,59
s_add_u32 s40, %[wlo], s53
s_addc_u32 s41, %[whi], 0
s_mov_b32 m0, s38
M 9,51,3
LA 16
PK 123,17,121,33
M 10,62,2
LC_ 127
PK 121,17,121,32
M 10,63,3
RA 216,1,19456
XL 35,704
DW 0
M 11,74,2
WL 3
LO 18,18,126
LU 126,18
M 11,75,3
s_add_u32 s42, s44, s64
RA 220,1,20480
HS 25,126
s_addc_u32 s43, s45, 0
XS 176
M 8,40,4
s_add_u32 s37, s37, 32768
LL 17,64
LL 112,65
s_cmp_eq_u32 s37, s75
s_cselect_b32 s37, s74, s37
M 8,41,5
LA 17
DW 1024
PK 126,18,124,33
XS 177
M 9,52,4
LC_ 112
PK 124,18,124,32
M 9,53,5
RA 224,1,21504
PO 126
XS 178
M 10,64,4
WL 5
LO 16,16,127
LU 127,16
ST 120
M 10,65,5
DW 2048
HS 25,127
XS 179
M 11,76,4
WL 3
LL 18,66
LL 113,67
M 11,77,5
s_add_u32 s40, s40, 3072
LA 18
PK 127,16,125,33
XS 180
M 8,42,0
s_addc_u32 s41, s41, 0
LC_ 113
PK 125,16,125,32
s_add_u32 m0, s38, 3072
M 8,43,1
s_add_u32 s42, s44, s65
RA 228,1,22528
PO 127
XS 181
M 9,54,0
WL 4
LO 112,17,112
LU 17,112
M 9,55,1
s_addc_u32 s43, s45, 0
RA 232,1,23552
HZ 24,17
XS 182
M 10,66,0
DW 0
LL 16,68
LL 116,69
M 10,67,1
s_lshl_b32 s53, s52, 8
LA 16
HX 25
XS 183
M 11,78,0
WL 2
LC_ 116
W1 25,1024
M 11,79,1
s_add_u32 s48, %[alo], s53
RA 236,1,24576
v_add_u32 v4, s39, v4
v_min_u32 v35, v35, v13
M 8,44,2
LO 113,18,113
LU 18,113
s_addc_u32 s49, %[ahi], 0
s_add_u32 s52, s52, %[d]
s_cmp_eq_u32 s52, %[wrap]
s_cselect_b32 s52, %[rst], s52
DW 1024
M 8,45,3
ST 124
HS 24,18
XS 184
M 9,56,2
s_mul_i32 s53, s51, 24
LL 17,70
LL 117,71
s_bitcmp1_b32 s51, 0
s_addc_u32 s53, s53, 0
s_lshl_b32 s53, s53, 11
s_bitcmp1_b64 s[90:91], s51
s_cselect_b64 s[44:45], s[84:85], s[82:83]
s_add_u32 s44, s44, s53
s_addc_u32 s45, s45, 0
s_add_u32 s51, s51, %[d]
s_cmp_eq_u32 s51, %[wrap]
s_cselect_b32 s51, %[rst], s51
M 9,57,3
LA 17
DW 2048
XS 185
M 10,68,2
LC_ 117
DQ 3072
M 10,69,3
s_add_u32 s50, s50, %[d]
v_add_u32 v1, s37, v0
XS 186
M 11,80,2
WL 2
LO 116,16,116
LU 16,116
M 11,81,3
s_cmp_eq_u32 s50, %[wrap]
RA 216,1,0
HS 24,16
XS 187
M 8,46,4
s_cselect_b32 s50, %[rst], s50
LL 18,72
LL 120,73
M 8,47,5
LA 18
v_and_b32 v22, 0xfffffcff, v1
XS 188
M 9,58,4
LC_ 120
RA 220,1,1024
M 9,59,5
RV 252,22,5120
v_add_u32 v19, 1, v19
XS 189
M 10,70,4
WL 5
LO 117,17,117
LU 17,117
M 10,71,5
s_add_u32 s38, s38, 32768
RA 224,1,2048
HS 24,17
XS 190
M 11,82,4
WL 1
LL 16,74
LL 121,75
XS 191
M 11,83,5
s_cmp_eq_u32 s38, s77
LA 16
RA 228,1,3072
s_cselect_b32 s38, s76, s38
FP
s_sub_u32 s39, 0, s39
Z 0,0,0
LC_ 121
XS 192
M 0,1,1
XS 193
XS 194
Z 1,12,0
LO 120,18,120
LU 18,120
M 1,13,1
RA 232,1,4096
HS 24,18
Z 2,36,0
LL 17,80
LL 124,81
M 2,37,1
LA 17
XS 195
Z 3,48,0
LC_ 124
XS 196
M 3,49,1
XS 197
XS 198
Z 4,60,0
WL 3
LO 121,16,121
LU 16,121
M 4,61,1
RA 236,1,6144
HS 24,16
Z 5,72,0
LL 18,82
LL 125,83
M 5,73,1
LA 18
XS 199
M 0,2,2
LC_ 125
XS 200
M 0,3,3
XS 201
XS 202
M 1,14,2
WL 3
LO 124,17,124
LU 17,124
M 1,15,3
RA 216,1,7168
HS 24,17
M 2,38,2
LL 16,88
LL 114,89
M 2,39,3
LA 16
FR
M 3,50,2
LC_ 114
XS 203
M 3,51,3
WV 31
BW 24
M 4,62,2
WL 5
LO 125,18,125
LU 18,125
M 4,63,3
RA 220,1,8192
HS 24,18
M 5,74,2
LL 17,90
LL 115,91
M 5,75,3
s_mov_b32 m0, %[pm]
LA 17
DF
M 0,4,4
LC_ 115
RV 40,8,6144
XS 204
M 0,5,5
RV 44,8,6208
RV 48,8,6272
M 1,16,4
WL 7
LO 16,16,114
LU 114,16
XS 205
M 1,17,5
RA 224,1,9216
HZ 25,114
M 2,40,4
LL 18,92
LL 118,93
XS 206
M 2,41,5
LA 18
PK 114,16,112,33
XS 207
M 3,52,4
LC_ 118
PK 112,16,112,32
M 3,53,5
BR 24
v_cmp_gt_u32 vcc, v19, v15
XS 208
M 4,64,4
WL 7
LO 17,17,115
LU 115,17
M 4,65,5
s_add_u32 s42, s44, s62
RA 228,1,10240
HS 25,115
XS 209
M 5,76,4
LL 16,94
LL 119,95
M 5,77,5
)"
R"(s_cbranch_vccnz .Lk1s10
.Lk1r10:
LA 16
PK 115,17,113,33
XS 210
M 0,6,0
s_addc_u32 s43, s45, 0
LC_ 119
PK 113,17,113,32
M 0,7,1
ST 112
HX 24
XS 211
M 1,18,0
WL 4
LO 18,18,118
LU 118,18
M 1,19,1
RA 232,1,11264
HS 25,118
XS 212
M 2,42,0
LL 17,96
LL 122,97
M 2,43,1
LA 17
PK 118,18,116,33
XS 213
M 3,54,0
LC_ 122
PK 116,18,116,32
M 3,55,1
RV 252,22,5392
PO 118
XS 214
M 4,66,0
WL 4
LO 16,16,119
LU 119,16
M 4,67,1
s_add_u32 s42, s44, s63
RA 236,1,12288
HS 25,119
XS 215
M 5,78,0
LL 18,98
LL 123,99
M 5,79,1
s_addc_u32 s43, s45, 0
LA 18
PK 119,16,117,33
XS 216
M 0,8,2
LC_ 123
PK 117,16,117,32
M 0,9,3
W1 24,0
PO 119
XS 217
M 1,20,2
WL 5
LO 17,17,122
LU 122,17
M 1,21,3
RA 216,1,13312
HS 25,122
XS 218
M 2,44,2
LL 16,100
LL 126,101
M 2,45,3
LA 16
PK 122,17,120,33
XS 219
M 3,56,2
LC_ 126
PK 120,17,120,32
M 3,57,3
WV 26
ST 116
XS 220
BW 25
M 4,68,2
WL 5
LO 18,18,123
LU 123,18
M 4,69,3
s_add_u32 s42, s44, s64
RA 220,1,14336
BR 25
HS 25,123
XS 221
LZ 40,24
s_addc_u32 s43, s45, 0
BW 26
M 5,80,2
LL 17,102
LL 127,103
M 5,81,3
s_bitcmp1_b64 s[92:93], 48
LA 17
PK 123,18,121,33
XS 222
M 0,10,4
s_cselect_b32 s58, 16, 0
LC_ 127
PK 121,18,121,32
XS 223
M 0,11,5
s_lshr_b32 s58, 0xda43cb52, s58
RV 40,8,6336
BR 26
M 1,22,4
WL 8
LO 16,16,126
LU 126,16
M 1,23,5
WL 5
RA 224,1,15360
HS 25,126
LS 44,25
WV 23
BW 27
M 2,46,4
LL 18,56
LL 104,57
M 2,47,5
LA 18
PK 126,16,124,33
M 3,58,4
LC_ 104
RV 44,8,6400
PK 124,16,124,32
M 3,59,5
ds_read_b64 v[14:15], v5 offset:0
BR 27
PO 126
M 4,70,4
WL 9
LO 17,17,127
LU 127,17
M 4,71,5
WL 7
RA 228,1,16384
HS 25,127
LS 48,26
WV 22
BW 28
M 5,82,4
LL 16,58
LL 105,59
M 5,83,5
LA 16
RV 48,8,6464
PK 127,17,125,33
Z 6,0,0
LC_ 105
PK 125,17,125,32
M 6,1,1
BR 28
PO 127
v_and_b32 v11, 0x1f0, v0
v_min_u32 v11, 368, v11
v_lshlrev_b32 v11, 3, v11
Z 7,12,0
WL 9
LO 104,18,104
LU 18,104
M 7,13,1
WL 6
RA 232,1,17408
HZ 26,18
LS 40,27
WV 21
BW 29
Z 8,36,0
LL 17,60
LL 108,61
ST 120
M 8,37,1
LA 17
RV 40,8,6528
Z 9,48,0
LC_ 108
BR 29
M 9,49,1
WL 6
v_subrev_u32 v5, s39, v5
HX 25
LS 44,28
WV 21
BW 30
Z 10,60,0
LO 105,16,105
LU 16,105
v_lshrrev_b32 v3, 9, v0
v_lshl_add_u32 v3, v3, 2, 0
v_bfe_u32 v3, s58, v3, 4
v_mad_u32_u24 v3, v3, s97, v11
M 10,61,1
s_add_u32 s42, s44, s65
RA 236,1,18432
HS 26,16
Z 11,72,0
RV 44,8,6592
LL 18,62
LL 109,63
M 11,73,1
s_addc_u32 s43, s45, 0
LA 18
W1 25,1024
BR 30
M 6,2,2
WV 20
LC_ 109
DY
M 6,3,3
WL 7
ST 124
v_add_f32 v14, v14, v15
LS 48,29
s_mul_i32 s53, s50, 25600
BW 31
M 7,14,2
LO 108,17,108
LU 17,108
M 7,15,3
s_add_u32 s40, %[wlo], s53
RA 216,1,19456
HS 26,17
M 8,38,2
s_addc_u32 s41, %[whi], 0
RV 48,8,6656
LL 16,64
LL 112,65
M 8,39,3
s_mov_b32 m0, s38
LA 16
BR 31
M 9,50,2
LC_ 112
v_add_f32_dpp v14, v14, v14 quad_perm:[1,0,3,2] row_mask:0xf bank_mask:0xf
M 9,51,3
WL 7
v_add_f32_dpp v14, v14, v14 quad_perm:[2,3,0,1] row_mask:0xf bank_mask:0xf
v_add_u32 v4, s39, v4
LS 40,30
WV 21
BW 32
M 10,62,2
WL 7
LO 109,18,109
LU 18,109
M 10,63,3
RA 220,1,20480
RV 40,8,6720
HS 26,18
M 11,74,2
BR 32
LL 17,66
LL 113,67
M 11,75,3
WV 18
LA 17
v_add_f32_dpp v14, v14, v14 row_half_mirror row_mask:0xf bank_mask:0xf
v_lshrrev_b32 v3, 9, v0
v_lshl_add_u32 v3, v3, 2, 8
v_bfe_u32 v3, s58, v3, 4
v_mad_u32_u24 v3, v3, s97, v11
M 6,4,4
WL 6
LC_ 113
DW 0
M 6,5,5
s_add_u32 s37, s37, 32768
v_cvt_f64_f32 v[14:15], v14
HX 26
LS 44,31
s_cmp_eq_u32 s37, s75
BW 33
M 7,16,4
WL 7
RV 44,8,6784
LO 112,16,112
LU 16,112
M 7,17,5
s_cselect_b32 s37, s74, s37
RA 224,1,21504
HZ 24,16
M 8,40,4
BR 33
LL 18,68
LL 116,69
M 8,41,5
WL 6
LA 18
s_mov_b64 exec, s[54:55]
SP
s_mov_b64 exec, -1
LS 48,32
WV 19
BW 34
M 9,52,4
LC_ 116
DY
M 9,53,5
DW 1024
RV 48,8,6848
M 10,64,4
WL 8
LO 113,17,113
LU 17,113
M 10,65,5
RA 228,1,22528
RA 240,7,0
HS 24,17
M 11,76,4
WV 19
BR 34
LL 16,70
LL 117,71
M 11,77,5
WL 7
LA 16
DW 2048
M 6,6,0
s_mul_i32 s53, s51, 24
LC_ 117
M 6,7,1
s_bitcmp1_b32 s51, 0
LS 40,33
s_addc_u32 s53, s53, 0
BW 35
M 7,18,0
WL 3
RA 244,7,256
LO 116,18,116
LU 18,116
M 7,19,1
s_lshl_b32 s53, s53, 11
RA 232,1,23552
HS 24,18
M 8,42,0
s_bitcmp1_b64 s[90:91], s51
BR 35
LL 17,72
LL 120,73
M 8,43,1
s_cselect_b64 s[44:45], s[84:85], s[82:83]
LA 17
s_add_u32 s44, s44, s53
s_addc_u32 s45, s45, 0
s_bitcmp1_b64 s[92:93], s51
s_cselect_b64 s[94:95], s[88:89], s[86:87]
s_add_u32 s94, s94, s53
s_addc_u32 s95, s95, 0
s_add_u32 s51, s51, %[d]
s_cmp_eq_u32 s51, %[wrap]
s_cselect_b32 s51, %[rst], s51
s_mul_i32 s53, %[d], 0
s_add_u32 s60, s53, %[sb1]
s_add_u32 s61, s60, 512
LS 44,34
s_add_u32 s40, s40, 3072
RA 248,7,512
M 9,54,0
s_addc_u32 s41, s41, 0
LC_ 120
s_add_u32 m0, s38, 3072
M 9,55,1
DW 0
M 10,66,0
WL 7
LO 117,16,117
LU 16,117
M 10,67,1
WL 3
RA 236,1,24576
HS 24,16
LS 48,35
RA 252,7,768
M 11,78,0
LL 18,74
LL 121,75
s_nop 3
v_mul_f32 v36, 0xbb2aaaab, v36
M 11,79,1
LA 18
DW 1024
v_mul_f32 v37, 0xbb2aaaab, v37
v_mul_f32 v38, 0xbb2aaaab, v38
M 6,8,2
LC_ 121
DW 2048
v_mul_f32 v39, 0xbb2aaaab, v39
M 6,9,3
s_lshl_b32 s53, s52, 8
v_add_u32 v1, s37, v0
LK 40,240,24,0
LK 44,242,24,2
)"
R"(s_add_u32 s48, %[alo], s53
RA 240,7,1024
LK 48,244,25,0
s_nop 0
CF 224,40,41
LK 248,246,25,2
s_addc_u32 s49, %[ahi], 0
RA 244,7,1280
CF 225,42,43
M 7,20,2
WL 6
LO 120,17,120
LU 17,120
M 7,21,3
s_add_u32 s52, s52, %[d]
RA 216,1,0
HS 24,17
M 8,44,2
s_cmp_eq_u32 s52, %[wrap]
LL 16,76
LL 124,77
CF 226,44,45
M 8,45,3
s_cselect_b32 s52, %[rst], s52
LA 16
DQ 3072
CF 227,46,47
LK 40,248,26,0
CF 228,48,49
LK 44,250,26,2
RA 248,7,1536
CF 229,50,51
GZ 24
WL 7
CF 230,248,249
CF 231,250,251
LK 48,252,27,0
CF 232,40,41
LK 248,254,27,2
RA 252,7,1792
CF 233,42,43
GS 25
WL 5
CF 234,44,45
CF 235,46,47
LK 40,240,28,0
CF 236,48,49
LK 44,242,28,2
RA 240,7,2048
CF 237,50,51
GS 26
WL 5
CF 238,248,249
CF 239,250,251
LK 48,244,29,0
CF 240,40,41
LK 248,246,29,2
RA 244,7,2304
CF 241,42,43
GS 27
CF 242,44,45
CF 243,46,47
M 9,56,2
LC_ 124
v_and_b32 v22, 0xfffffcff, v1
M 9,57,3
WL 4
LK 40,248,30,0
CF 244,48,49
LK 44,250,30,2
RA 248,7,2560
CF 245,50,51
GS 28
WL 4
CF 246,248,249
CF 247,250,251
LK 48,252,31,0
CF 40,40,41
LK 248,254,31,2
RA 252,7,2816
CF 42,42,43
GS 29
CF 44,44,45
CF 46,46,47
M 10,68,2
LO 121,18,121
LU 18,121
M 10,69,3
s_add_u32 s50, s50, %[d]
RA 220,1,1024
HS 24,18
M 11,80,2
LL 17,78
LL 125,79
M 11,81,3
s_cmp_eq_u32 s50, %[wrap]
LA 17
RV 252,22,5120
M 6,10,4
s_cselect_b32 s50, %[rst], s50
LC_ 125
AW 0,40
M 6,11,5
v_add_u32 v19, 1, v19
AW 1,42
M 7,22,4
WL 6
LO 124,16,124
LU 16,124
M 7,23,5
s_add_u32 s38, s38, 32768
RA 224,1,2048
HS 24,16
M 8,46,4
s_cmp_eq_u32 s38, s77
LL 18,80
LL 106,81
M 8,47,5
s_cselect_b32 s38, s76, s38
LA 18
FP
M 9,58,4
s_sub_u32 s39, 0, s39
LC_ 106
AW 2,44
M 9,59,5
AW 3,46
LK 40,240,32,0
CF 48,48,49
LK 44,242,32,2
CF 50,50,51
GS 30
CF 248,248,249
CF 250,250,251
M 10,70,4
WL 4
LO 125,17,125
LU 17,125
M 10,71,5
RA 228,1,3072
HS 24,17
M 11,82,4
LL 16,82
LL 107,83
M 11,83,5
LA 16
HX 24
Z 0,0,0
LC_ 107
AW 4,48
M 0,1,1
AW 5,50
AW 6,248
Z 1,12,0
WL 3
LO 18,18,106
LU 106,18
M 1,13,1
RA 232,1,4096
HZ 27,106
Z 2,36,0
LL 17,84
LL 110,85
M 2,37,1
LA 17
PK 106,18,104,33
Z 3,48,0
LC_ 110
PK 104,18,104,32
M 3,49,1
AW 7,250
LK 48,244,33,0
CF 40,40,41
LK 248,246,33,2
CF 42,42,43
GS 31
CF 44,44,45
CF 46,46,47
Z 4,60,0
WL 3
LO 16,16,107
LU 107,16
M 4,61,1
s_add_u32 s42, s94, s60
RA 236,1,6144
HS 27,107
Z 5,72,0
LL 18,86
LL 111,87
M 5,73,1
s_addc_u32 s43, s95, 0
LA 18
PK 107,16,105,33
M 0,2,2
LC_ 111
PK 105,16,105,32
M 0,3,3
SU 104
AW 8,40
M 1,14,2
WL 3
LO 17,17,110
LU 110,17
M 1,15,3
RA 216,1,7168
HS 27,110
M 2,38,2
LL 16,88
LL 114,89
M 2,39,3
LA 16
PK 110,17,108,33
AW 9,42
AW 10,44
M 3,50,2
LC_ 114
PK 108,17,108,32
AW 11,46
M 3,51,3
FR
PO 110
LK 40,248,34,0
CF 48,48,49
LK 44,250,34,2
CF 50,50,51
GS 32
CF 248,248,249
CF 250,250,251
M 4,62,2
WL 4
LO 18,18,111
LU 111,18
M 4,63,3
s_add_u32 s42, s94, s61
RA 220,1,8192
HS 27,111
M 5,74,2
LL 17,90
LL 115,91
AW 12,48
AW 13,50
M 5,75,3
s_addc_u32 s43, s95, 0
LA 17
PK 111,18,109,33
AW 14,248
AW 15,250
LK 48,252,35,0
CF 40,40,41
LK 248,254,35,2
s_mov_b32 m0, %[pm]
CF 42,42,43
HX 27
GS 33
CF 44,44,45
CF 46,46,47
M 0,4,4
LC_ 115
PK 109,18,109,32
M 0,5,5
DF
PO 111
M 1,16,4
WL 4
LO 16,16,114
LU 114,16
M 1,17,5
RA 224,1,9216
HZ 25,114
AW 16,40
M 2,40,4
LL 18,92
LL 118,93
AW 17,42
AW 18,44
M 2,41,5
LA 18
RV 36,8,6912
PK 114,16,112,33
AW 19,46
M 3,52,4
LC_ 118
PK 112,16,112,32
M 3,53,5
WL 7
SU 108
v_cmp_gt_u32 vcc, v19, v15
GS 34
CF 48,48,49
CF 50,50,51
CF 248,248,249
M 4,64,4
WL 4
LO 17,17,115
LU 115,17
CF 250,250,251
AW 20,48
M 4,65,5
s_add_u32 s42, s44, s62
RA 228,1,10240
HS 25,115
AW 21,50
AW 22,248
M 5,76,4
LL 16,94
LL 119,95
AW 23,250
M 5,77,5
s_cbranch_vccnz .Lk1s11
.Lk1r11:
LA 16
PK 115,17,113,33
GS 35
s_addc_u32 s43, s45, 0
W1 24,0
s_nop 5
v_cndmask_b32 v52, v52, v53, s[78:79]
M 0,6,0
LC_ 119
PK 113,17,113,32
v_cndmask_b32 v54, v54, v55, s[78:79]
M 0,7,1
W1 26,2048
v_cndmask_b32 v52, v52, v54, s[80:81]
M 1,18,0
WL 5
LO 18,18,118
LU 118,18
v_mul_f32 v52, 0x3b2aaaab, v52
M 1,19,1
RA 232,1,11264
HS 25,118
v_med3_f32 v53, v52, v39, v37
M 2,42,0
LL 17,96
LL 122,97
LG 52,53
M 2,43,1
LA 17
PK 118,18,116,33
C 52,52,52
M 3,54,0
LC_ 122
PK 116,18,116,32
v_lshlrev_b32 v52, 16, v52
M 3,55,1
RV 252,22,5392
PO 118
v_add_f32 v52, v36, v52
M 4,66,0
WL 5
LO 16,16,119
LU 119,16
C 52,52,52
M 4,67,1
RA 236,1,12288
HS 25,119
M 5,78,0
LL 18,98
LL 123,99
v_lshlrev_b32 v52, 16, v52
M 5,79,1
LA 18
PK 119,16,117,33
v_sqrt_f32 v52, v52
M 0,8,2
LC_ 123
PK 117,16,117,32
C 52,52,52
M 0,9,3
ST 112
PO 119
v_lshlrev_b32 v52, 16, v52
M 1,20,2
WL 4
LO 17,17,122
LU 122,17
v_rcp_f32 v52, v52
M 1,21,3
s_add_u32 s42, s44, s63
RA 216,1,13312
HS 25,122
v_mul_f32 v52, v38, v52
M 2,44,2
s_addc_u32 s43, s45, 0
LL 16,100
LL 126,101
CF 52,52,52
M 2,45,3
LA 16
PK 122,17,120,33
ds_bpermute_b32 v13, v28, v52
M 3,56,2
LC_ 126
PK 120,17,120,32
M 3,57,3
ST 116
AR 248,0
M 4,68,2
WL 4
)"
R"(LO 18,18,123
LU 123,18
M 4,69,3
s_add_u32 s42, s44, s64
RA 220,1,14336
HS 25,123
M 5,80,2
LL 17,102
LL 127,103
M 5,81,3
s_addc_u32 s43, s45, 0
LA 17
PK 123,18,121,33
M 0,10,4
LC_ 127
PK 121,18,121,32
M 0,11,5
ST 120
AR 249,1
M 1,22,4
WL 3
LO 16,16,126
LU 126,16
M 1,23,5
RA 224,1,15360
HS 25,126
M 2,46,4
LL 18,56
LL 104,57
M 2,47,5
LA 18
PK 126,16,124,33
M 3,58,4
LC_ 104
PK 124,16,124,32
AR 250,2
M 3,59,5
ds_read_b64 v[14:15], v5 offset:0
PO 126
M 4,70,4
WL 4
LO 17,17,127
LU 127,17
AR 251,3
M 4,71,5
s_add_u32 s42, s44, s65
RA 228,1,16384
HS 25,127
AR 52,4
M 5,82,4
LL 16,58
LL 105,59
AR 53,5
M 5,83,5
s_addc_u32 s43, s45, 0
LA 16
PK 127,17,125,33
Z 6,0,0
LC_ 105
PK 125,17,125,32
AR 54,6
M 6,1,1
PO 127
v_subrev_u32 v5, s39, v5
AR 55,7
Z 7,12,0
WL 4
LO 104,18,104
LU 18,104
XS 224
M 7,13,1
RA 232,1,17408
HZ 26,18
Z 8,36,0
WL 4
LL 17,60
LL 108,61
XS 225
M 8,37,1
s_mul_i32 s53, s50, 25600
LA 17
v_add_f32 v14, v14, v15
XS 226
Z 9,48,0
s_add_u32 s40, %[wlo], s53
LC_ 108
v_add_f32_dpp v14, v14, v14 quad_perm:[1,0,3,2] row_mask:0xf bank_mask:0xf
M 9,49,1
s_addc_u32 s41, %[whi], 0
v_add_f32_dpp v14, v14, v14 quad_perm:[2,3,0,1] row_mask:0xf bank_mask:0xf
HX 25
XS 227
Z 10,60,0
WL 3
LO 105,16,105
LU 16,105
XS 228
M 10,61,1
s_mov_b32 m0, s38
RA 236,1,18432
HS 26,16
XS 229
Z 11,72,0
LL 18,62
LL 109,63
M 11,73,1
LA 18
W1 25,1024
XS 230
M 6,2,2
LC_ 109
W1 27,3072
XS 231
M 6,3,3
ST 124
v_add_f32_dpp v14, v14, v14 row_half_mirror row_mask:0xf bank_mask:0xf
v_min_u32 v35, v35, v13
M 7,14,2
WL 5
LO 108,17,108
LU 17,108
M 7,15,3
RA 216,1,19456
HS 26,17
XS 232
M 8,38,2
LL 16,64
LL 112,65
XS 233
M 8,39,3
LA 16
DW 0
XS 234
M 9,50,2
s_add_u32 s37, s37, 32768
LC_ 112
v_cvt_f64_f32 v[14:15], v14
M 9,51,3
s_cmp_eq_u32 s37, s75
s_mov_b64 exec, s[54:55]
SP
s_mov_b64 exec, -1
v_add_u32 v4, s39, v4
XS 235
M 10,62,2
WL 4
LO 109,18,109
LU 18,109
XS 236
M 10,63,3
s_cselect_b32 s37, s74, s37
RA 220,1,20480
HS 26,18
M 11,74,2
LL 17,66
LL 113,67
XS 237
M 11,75,3
s_mul_i32 s53, %[d], 196608
LA 17
HX 26
XS 238
M 6,4,4
s_add_u32 s53, s53, s57
LC_ 113
XS 239
M 6,5,5
s_add_u32 s46, %[xlo], s53
XS 240
XS 241
M 7,16,4
WL 3
LO 112,16,112
LU 16,112
M 7,17,5
s_addc_u32 s47, %[xhi], 0
RA 224,1,21504
HZ 24,16
M 8,40,4
s_bitcmp1_b64 s[92:93], 48
LL 18,68
LL 116,69
XS 242
M 8,41,5
s_cselect_b32 s58, 16, 0
LA 18
XS 243
M 9,52,4
s_lshr_b32 s58, 0xda43cb52, s58
LC_ 116
v_lshrrev_b32 v3, 6, v0
v_and_b32 v3, 12, v3
v_bfe_u32 v3, s58, v3, 4
v_mad_u32_u24 v3, v3, s97, v21
M 9,53,5
DW 1024
XS 244
M 10,64,4
WL 3
LO 113,17,113
LU 17,113
XS 245
M 10,65,5
RA 228,1,22528
HS 24,17
M 11,76,4
LL 16,70
LL 117,71
XS 246
M 11,77,5
LA 16
XL 36,0
XS 247
M 6,6,0
LC_ 117
DW 2048
M 6,7,1
XL 37,64
XS 248
M 7,18,0
WL 3
LO 116,18,116
LU 18,116
XS 249
M 7,19,1
s_mul_i32 s53, s51, 24
RA 232,1,23552
HS 24,18
XS 250
M 8,42,0
s_bitcmp1_b32 s51, 0
LL 17,72
LL 120,73
M 8,43,1
s_addc_u32 s53, s53, 0
LA 17
s_lshl_b32 s53, s53, 11
s_bitcmp1_b64 s[90:91], s51
s_cselect_b64 s[44:45], s[84:85], s[82:83]
s_add_u32 s44, s44, s53
s_addc_u32 s45, s45, 0
s_bitcmp1_b64 s[92:93], s51
s_cselect_b64 s[94:95], s[88:89], s[86:87]
s_add_u32 s94, s94, s53
s_addc_u32 s95, s95, 0
s_add_u32 s51, s51, %[d]
s_cmp_eq_u32 s51, %[wrap]
s_cselect_b32 s51, %[rst], s51
XS 251
M 9,54,0
s_add_u32 s40, s40, 3072
LC_ 120
s_addc_u32 s41, s41, 0
s_add_u32 m0, s38, 3072
AW 0,248
M 9,55,1
XL 38,128
AW 1,249
M 10,66,0
WL 3
LO 117,16,117
LU 16,117
M 10,67,1
RA 236,1,24576
HS 24,16
AW 2,250
M 11,78,0
LL 18,74
LL 121,75
AW 3,251
M 11,79,1
LA 18
DW 0
XS 52
M 6,8,2
LC_ 121
XL 39,192
M 6,9,3
DW 1024
v_add_u32 v1, s37, v0
XS 53
M 7,20,2
WL 3
LO 120,17,120
LU 17,120
XS 54
M 7,21,3
RA 216,1,0
HS 24,17
M 8,44,2
LL 16,76
LL 124,77
XS 55
M 8,45,3
LA 16
XL 40,256
AW 4,52
M 9,56,2
LC_ 124
DW 2048
AW 5,53
M 9,57,3
XL 41,320
v_and_b32 v22, 0xfffffcff, v1
M 10,68,2
WL 3
LO 121,18,121
LU 18,121
AW 6,54
M 10,69,3
s_lshl_b32 s53, s52, 8
RA 220,1,1024
HS 24,18
AW 7,55
M 11,80,2
LL 17,78
LL 125,79
AR 248,8
M 11,81,3
s_add_u32 s48, %[alo], s53
LA 17
RV 252,22,5120
M 6,10,4
s_addc_u32 s49, %[ahi], 0
LC_ 125
AR 249,9
M 6,11,5
s_add_u32 s52, s52, %[d]
AR 250,10
AR 251,11
M 7,22,4
WL 4
LO 124,16,124
LU 16,124
M 7,23,5
s_cmp_eq_u32 s52, %[wrap]
RA 224,1,2048
HS 24,16
M 8,46,4
s_cselect_b32 s52, %[rst], s52
LL 18,80
LL 106,81
XS 248
M 8,47,5
LA 18
DQ 3072
XS 249
M 9,58,4
LC_ 106
XL 42,384
M 9,59,5
s_add_u32 s50, s50, %[d]
XS 250
XS 251
M 10,70,4
WL 3
LO 125,17,125
LU 17,125
M 10,71,5
RA 228,1,3072
HS 24,17
AW 8,248
M 11,82,4
LL 16,82
LL 107,83
M 11,83,5
LA 16
HX 24
s_cmp_eq_u32 s50, %[wrap]
s_cselect_b32 s50, %[rst], s50
XL 43,448
AW 9,249
Z 0,0,0
WV 38
LC_ 107
XL 44,512
v_add_u32 v19, 1, v19
s_add_u32 s38, s38, 32768
s_cmp_eq_u32 s38, s77
s_cselect_b32 s38, s76, s38
FP
s_sub_u32 s39, 0, s39
AW 10,250
M 0,1,1
XL 45,576
AW 11,251
Z 1,12,0
WL 4
LO 18,18,106
LU 106,18
M 1,13,1
RA 232,1,4096
HZ 27,106
AR 52,12
Z 4,60,0
LL 17,84
LL 110,85
AR 53,13
M 4,61,1
LA 17
)"
R"(PK 106,18,104,33
AR 54,14
Z 5,72,0
WL 5
LC_ 110
XL 46,640
PK 104,18,104,32
M 5,73,1
RA 236,1,6144
AR 55,15
M 0,2,2
WL 5
LO 16,16,107
LU 107,16
XS 52
M 0,3,3
FR
HS 27,107
M 1,14,2
LL 18,86
LL 111,87
XS 53
M 1,15,3
LA 18
PK 107,16,105,33
XS 54
M 4,62,2
LC_ 111
XL 47,704
PK 105,16,105,32
XS 55
M 4,63,3
s_add_u32 s42, s94, s60
RA 216,1,7168
AW 12,52
M 5,74,2
WL 4
LO 17,17,110
LU 110,17
M 5,75,3
s_addc_u32 s43, s95, 0
RA 220,1,8192
HS 27,110
AW 13,53
M 0,4,4
LL 16,88
LL 114,89
AW 14,54
M 0,5,5
LA 16
PK 110,17,108,33
M 1,16,4
LC_ 114
PK 108,17,108,32
AW 15,55
M 1,17,5
RA 224,1,9216
PO 110
AR 248,16
M 4,64,4
WL 5
LO 18,18,111
LU 111,18
SU 104
AR 249,17
M 4,65,5
s_add_u32 s42, s94, s61
HS 27,111
v_cmp_gt_u32 vcc, v19, v15
M 5,76,4
WL 3
LL 17,90
LL 115,91
AR 250,18
M 5,77,5
s_addc_u32 s43, s95, 0
LA 17
PK 111,18,109,33
AR 251,19
M 0,6,0
s_cbranch_vccnz .Lk1s12
.Lk1r12:
LC_ 115
PK 109,18,109,32
M 0,7,1
WV 17
RA 228,1,10240
PO 111
XS 248
BW 36
M 1,18,0
WL 5
LO 16,16,114
LU 114,16
XS 249
M 1,19,1
RA 232,1,11264
RV 40,8,6144
HZ 25,114
XS 250
M 4,66,0
RV 44,8,6208
LL 18,92
LL 118,93
M 4,67,1
LA 18
RV 48,8,6272
PK 114,16,112,33
XS 251
M 5,78,0
WL 6
LC_ 118
BR 36
PK 112,16,112,32
AW 16,248
M 5,79,1
WV 15
RV 252,22,5392
ds_read_b64 v[14:15], v5 offset:0
W1 24,0
W1 26,2048
HX 27
BW 37
AW 17,249
M 0,8,2
BR 37
LO 17,17,115
LU 115,17
M 0,9,3
WL 6
RA 236,1,12288
HS 25,115
AW 18,250
LZ 40,36
WV 14
BW 38
M 1,20,2
LL 16,94
LL 119,95
AW 19,251
M 1,21,3
LA 16
RV 40,8,6336
PK 115,17,113,33
AR 52,20
M 4,68,2
LC_ 119
BR 38
PK 113,17,113,32
M 4,69,3
WL 6
RA 216,1,13312
AR 53,21
LS 44,37
WV 12
BW 39
M 5,80,2
WL 7
LO 18,18,118
LU 118,18
SU 108
AR 54,22
M 5,81,3
s_add_u32 s42, s44, s62
RA 220,1,14336
HS 25,118
s_addc_u32 s43, s45, 0
RV 44,8,6400
M 0,10,4
BR 39
LL 17,96
LL 122,97
AR 55,23
M 0,11,5
WL 5
LA 17
PK 118,18,116,33
XS 52
LS 48,38
WV 11
BW 40
M 1,22,4
LC_ 122
PK 116,18,116,32
XS 53
M 1,23,5
RA 224,1,15360
RV 48,8,6464
PO 118
M 4,70,4
LO 16,16,119
LU 119,16
ST 112
XS 54
M 4,71,5
s_add_u32 s42, s44, s63
BR 40
HS 25,119
XS 55
M 5,82,4
WL 6
LL 18,98
LL 123,99
AW 20,52
M 5,83,5
s_addc_u32 s43, s45, 0
LA 18
PK 119,16,117,33
LS 40,39
WV 10
BW 41
Z 6,0,0
LC_ 123
PK 117,16,117,32
v_add_f32 v14, v14, v15
s_nop 1
v_add_f32_dpp v14, v14, v14 quad_perm:[1,0,3,2] row_mask:0xf bank_mask:0xf
s_nop 1
v_add_f32_dpp v14, v14, v14 quad_perm:[2,3,0,1] row_mask:0xf bank_mask:0xf
s_nop 1
v_add_f32_dpp v14, v14, v14 row_half_mirror row_mask:0xf bank_mask:0xf
v_cvt_f64_f32 v[14:15], v14
s_mov_b64 exec, s[54:55]
SP
s_mov_b64 exec, -1
s_nop 0
ds_read_b64 v[14:15], v5 offset:2048
RV 40,8,6528
AW 21,53
M 6,1,1
WV 9
RA 228,1,16384
BR 41
PO 119
AW 22,54
Z 7,12,0
WL 7
LO 17,17,122
LU 122,17
AW 23,55
M 7,13,1
s_mul_i32 s53, %[d], 393216
RA 232,1,17408
HS 25,122
LS 44,40
s_add_u32 s53, s53, s57
BW 42
Z 10,60,0
s_add_u32 s46, %[xlo], s53
RV 44,8,6592
LL 16,100
LL 126,101
M 10,61,1
s_addc_u32 s47, %[xhi], 0
LA 16
BR 42
PK 122,17,120,33
Z 2,72,0
WL 5
LC_ 126
PK 120,17,120,32
M 2,73,1
s_bitcmp1_b64 s[92:93], 48
RA 236,1,18432
v_subrev_u32 v5, s39, v5
LS 48,41
s_cselect_b32 s58, 16, 0
WV 8
BW 43
M 6,2,2
RV 48,8,6656
LO 18,18,123
LU 123,18
M 6,3,3
s_lshr_b32 s58, 0xfe987610, s58
BR 43
HS 25,123
v_lshrrev_b32 v3, 6, v0
v_and_b32 v3, 12, v3
v_bfe_u32 v3, s58, v3, 4
v_mad_u32_u24 v3, v3, s97, v21
M 7,14,2
s_mul_i32 s53, s50, 25600
LL 17,102
LL 127,103
s_add_u32 s40, %[wlo], s53
s_addc_u32 s41, %[whi], 0
s_mov_b32 m0, s38
M 7,15,3
WL 5
LA 17
PK 123,18,121,33
LS 40,42
WV 7
BW 44
M 10,62,2
LC_ 127
RV 40,8,6720
PK 121,18,121,32
M 10,63,3
WV 6
RA 216,1,19456
BR 44
DW 0
M 2,74,2
WL 6
LO 16,16,126
LU 126,16
ST 116
M 2,75,3
s_add_u32 s42, s44, s64
RA 220,1,20480
HS 25,126
s_addc_u32 s43, s45, 0
LS 44,43
s_add_u32 s37, s37, 32768
BW 45
M 6,4,4
s_cmp_eq_u32 s37, s75
LL 18,56
LL 104,57
s_cselect_b32 s37, s74, s37
M 6,5,5
LA 18
PK 126,16,124,33
v_add_f32 v14, v14, v15
s_nop 1
v_add_f32_dpp v14, v14, v14 quad_perm:[1,0,3,2] row_mask:0xf bank_mask:0xf
s_nop 1
v_add_f32_dpp v14, v14, v14 quad_perm:[2,3,0,1] row_mask:0xf bank_mask:0xf
s_nop 1
v_add_f32_dpp v14, v14, v14 row_half_mirror row_mask:0xf bank_mask:0xf
v_cvt_f64_f32 v[14:15], v14
s_mov_b64 exec, s[54:55]
SP
s_mov_b64 exec, -1
XL 48,0
DW 1024
RV 44,8,6784
M 7,16,4
LC_ 104
BR 45
PK 124,16,124,32
M 7,17,5
WL 6
RA 224,1,21504
PO 126
LS 48,44
WV 10
BW 46
M 10,64,4
LO 17,17,127
LU 127,17
ST 120
M 10,65,5
WV 10
DW 2048
RV 48,8,6848
HS 25,127
M 2,76,4
WL 8
RA 240,7,0
LL 16,58
LL 105,59
M 2,77,5
s_add_u32 s40, s40, 3072
LA 16
BR 46
PK 127,17,125,33
M 6,6,0
s_addc_u32 s41, s41, 0
LC_ 105
PK 125,17,125,32
s_add_u32 m0, s38, 3072
M 6,7,1
WL 7
RA 228,1,22528
XL 49,64
PO 127
LS 40,45
s_add_u32 s42, s44, s65
BW 47
M 7,18,0
RA 244,7,256
LO 104,18,104
LU 18,104
M 7,19,1
s_addc_u32 s43, s45, 0
RA 232,1,23552
BR 47
HZ 26,18
M 10,66,0
WL 6
DW 0
LL 17,60
LL 108,61
M 10,67,1
s_lshl_b32 s53, s52, 8
LA 17
HX 25
)"
R"(LS 44,46
s_add_u32 s48, %[alo], s53
RA 248,7,512
M 2,78,0
WL 2
LC_ 108
W1 25,1024
W1 27,3072
M 2,79,1
s_addc_u32 s49, %[ahi], 0
RA 236,1,24576
v_add_u32 v4, s39, v4
LS 48,47
s_add_u32 s52, s52, %[d]
RA 252,7,768
M 6,8,2
LO 105,16,105
LU 16,105
s_cmp_eq_u32 s52, %[wrap]
s_cselect_b32 s52, %[rst], s52
DW 1024
v_mul_f32 v36, 0xbb2aaaab, v36
M 6,9,3
XL 50,128
HS 26,16
v_mul_f32 v37, 0xbb2aaaab, v37
v_mul_f32 v38, 0xbb2aaaab, v38
M 7,20,2
LL 18,62
LL 109,63
v_mul_f32 v39, 0xbb2aaaab, v39
M 7,21,3
LA 18
DW 2048
LK 40,240,36,0
LK 44,242,36,2
RA 240,7,1024
v_add_u32 v1, s37, v0
LK 48,244,37,0
s_nop 0
CF 40,40,41
LK 248,246,37,2
RA 244,7,1280
CF 42,42,43
M 10,68,2
LC_ 109
RA 216,1,0
M 10,69,3
ST 124
v_and_b32 v22, 0xfffffcff, v1
CF 44,44,45
CF 46,46,47
M 2,80,2
WL 6
LO 108,17,108
LU 17,108
AW 24,40
AW 25,42
M 2,81,3
s_mul_i32 s53, s51, 24
RA 220,1,1024
HS 26,17
s_bitcmp1_b32 s51, 0
s_addc_u32 s53, s53, 0
s_lshl_b32 s53, s53, 11
s_bitcmp1_b64 s[90:91], s51
s_cselect_b64 s[44:45], s[84:85], s[82:83]
s_add_u32 s44, s44, s53
s_addc_u32 s45, s45, 0
s_bitcmp1_b64 s[92:93], s51
s_cselect_b64 s[94:95], s[88:89], s[86:87]
s_add_u32 s94, s94, s53
s_addc_u32 s95, s95, 0
s_add_u32 s51, s51, %[d]
s_cmp_eq_u32 s51, %[wrap]
s_cselect_b32 s51, %[rst], s51
DQ 3072
AW 26,44
AW 27,46
LK 40,248,38,0
CF 48,48,49
LK 44,250,38,2
RA 248,7,1536
CF 50,50,51
GZ 36
CF 248,248,249
CF 250,250,251
M 6,10,4
LL 16,72
LL 120,73
M 6,11,5
LA 16
RV 252,22,5120
M 7,22,4
WL 4
LC_ 120
XL 51,192
AW 28,48
M 7,23,5
s_add_u32 s50, s50, %[d]
RA 224,1,2048
s_cmp_eq_u32 s50, %[wrap]
s_cselect_b32 s50, %[rst], s50
v_add_u32 v19, 1, v19
AW 29,50
AW 30,248
AW 31,250
LK 48,252,39,0
CF 40,40,41
LK 248,254,39,2
s_add_u32 s38, s38, 32768
RA 252,7,1792
CF 42,42,43
GS 37
s_cmp_eq_u32 s38, s77
CF 44,44,45
CF 46,46,47
M 10,70,4
LO 109,18,109
LU 18,109
M 10,71,5
s_cselect_b32 s38, s76, s38
FP
HS 26,18
M 2,82,4
WL 4
LL 17,74
LL 121,75
M 2,83,5
s_sub_u32 s39, 0, s39
LA 17
RA 228,1,3072
AW 32,40
AW 33,42
Z 0,0,0
LC_ 121
XL 52,256
AW 34,44
AW 35,46
M 0,1,1
HX 26
LK 40,240,40,0
CF 48,48,49
LK 44,242,40,2
RA 240,7,2048
CF 50,50,51
GS 38
CF 248,248,249
CF 250,250,251
Z 1,12,0
WL 7
LO 120,16,120
LU 16,120
M 1,13,1
RA 232,1,4096
HZ 24,16
AW 36,48
Z 4,60,0
LL 18,76
LL 124,77
AW 37,50
AW 38,248
M 4,61,1
LA 18
FR
AW 39,250
LK 48,244,41,0
CF 40,40,41
LK 248,246,41,2
RA 244,7,2304
CF 42,42,43
GS 39
CF 44,44,45
CF 46,46,47
Z 8,72,0
WL 6
LC_ 124
XL 53,320
M 8,73,1
RA 236,1,6144
AW 40,40
M 0,2,2
WL 7
LO 121,17,121
LU 17,121
AW 41,42
M 0,3,3
HS 24,17
AW 42,44
AW 43,46
LK 40,248,42,0
CF 48,48,49
LK 44,250,42,2
RA 248,7,2560
CF 50,50,51
GS 40
CF 248,248,249
CF 250,250,251
M 1,14,2
LL 16,78
LL 125,79
M 1,15,3
LA 16
RA 216,1,7168
M 4,62,2
WL 6
LC_ 125
v_cmp_gt_u32 vcc, v19, v15
M 4,63,3
XL 54,384
AW 44,48
AW 45,50
AW 46,248
M 8,74,2
WL 4
LO 124,18,124
LU 18,124
AW 47,250
M 8,75,3
RA 220,1,8192
HS 24,18
LK 48,252,43,0
CF 40,40,41
LK 248,254,43,2
RA 252,7,2816
CF 42,42,43
GS 41
CF 44,44,45
CF 46,46,47
M 0,4,4
s_cbranch_vccnz .Lk1s13
.Lk1r13:
LL 17,80
LL 106,81
M 0,5,5
LA 17
ds_read_b64 v[14:15], v5 offset:0
AW 48,40
AW 49,42
M 1,16,4
LC_ 106
AW 50,44
AW 51,46
M 1,17,5
RA 224,1,9216
LK 40,240,44,0
CF 48,48,49
LK 44,242,44,2
CF 50,50,51
GS 42
CF 248,248,249
CF 250,250,251
M 4,64,4
WL 6
LO 125,16,125
LU 16,125
M 4,65,5
XL 55,448
HS 24,16
AW 52,48
M 8,76,4
WL 5
LL 18,82
LL 107,83
AW 53,50
AW 54,248
AW 55,250
M 8,77,5
LA 18
RA 228,1,10240
LK 48,244,45,0
CF 40,40,41
LK 248,246,45,2
CF 42,42,43
HX 24
GS 43
CF 44,44,45
CF 46,46,47
M 0,6,0
LC_ 107
W1 24,0
M 0,7,1
W1 26,2048
AW 56,40
M 1,18,0
WL 6
LO 17,17,106
LU 106,17
AW 57,42
AW 58,44
M 1,19,1
RA 232,1,11264
HZ 27,106
AW 59,46
LK 40,248,46,0
CF 48,48,49
LK 44,250,46,2
CF 50,50,51
v_add_f32 v14, v14, v15
GS 44
CF 248,248,249
CF 250,250,251
M 4,66,0
LL 16,84
LL 110,85
M 4,67,1
LA 16
PK 106,17,104,33
M 8,78,0
WL 5
LC_ 110
PK 104,17,104,32
AW 60,48
AW 61,50
AW 62,248
M 8,79,1
RV 252,22,5392
v_add_f32_dpp v14, v14, v14 quad_perm:[1,0,3,2] row_mask:0xf bank_mask:0xf
AW 63,250
LK 48,252,47,0
CF 40,40,41
LK 248,254,47,2
CF 42,42,43
v_add_f32_dpp v14, v14, v14 quad_perm:[2,3,0,1] row_mask:0xf bank_mask:0xf
GS 45
CF 44,44,45
CF 46,46,47
M 0,8,2
WL 6
LO 18,18,107
LU 107,18
M 0,9,3
RA 236,1,12288
HS 27,107
M 1,20,2
LL 17,86
LL 111,87
AW 64,40
AW 65,42
M 1,21,3
LA 17
RV 36,8,6912
PK 107,18,105,33
AW 66,44
AW 67,46
M 4,68,2
LC_ 111
XL 56,512
PK 105,18,105,32
M 4,69,3
s_add_u32 s42, s94, s60
RA 216,1,13312
v_add_f32_dpp v14, v14, v14 row_half_mirror row_mask:0xf bank_mask:0xf
GS 46
s_addc_u32 s43, s95, 0
CF 48,48,49
CF 50,50,51
CF 248,248,249
CF 250,250,251
M 8,80,2
WL 4
LO 16,16,110
LU 110,16
AW 68,48
AW 69,50
AW 70,248
M 8,81,3
RA 220,1,14336
HS 27,110
AW 71,250
s_nop 1
GS 47
SU 104
v_cvt_f64_f32 v[14:15], v14
s_nop 5
v_cndmask_b32 v52, v52, v53, s[78:79]
M 0,10,4
LL 18,96
LL 122,97
v_cndmask_b32 v54, v54, v55, s[78:79]
M 0,11,5
LA 18
PK 110,16,108,33
v_cndmask_b32 v52, v52, v54, s[80:81]
M 1,22,4
LC_ 122
PK 108,16,108,32
v_mul_f32 v52, 0x3b2aaaab, v52
M 1,23,5
WL 5
RA 224,1,15360
PO 110
v_med3_f32 v53, v52, v39, v37
M 4,70,4
WL 5
LO 17,17,111
LU 111,17
s_mov_b64 exec, s[54:55]
SP
s_mov_b64 exec, -1
)"
R"(XL 57,576
LG 52,53
s_nop 0
C 52,52,52
M 4,71,5
s_add_u32 s42, s94, s61
ds_read_b64 v[14:15], v5 offset:2048
HS 27,111
v_lshlrev_b32 v52, 16, v52
M 8,82,4
WL 4
LL 16,98
LL 123,99
v_add_f32 v52, v36, v52
M 8,83,5
s_addc_u32 s43, s95, 0
LA 16
PK 111,17,109,33
C 52,52,52
Z 6,0,0
LC_ 123
PK 109,17,109,32
v_lshlrev_b32 v52, 16, v52
M 6,1,1
RA 228,1,16384
PO 111
v_sqrt_f32 v52, v52
s_nop 0
C 52,52,52
Z 7,12,0
WL 5
LO 18,18,122
LU 122,18
v_lshlrev_b32 v52, 16, v52
M 7,13,1
RA 232,1,17408
HZ 25,122
v_rcp_f32 v52, v52
Z 10,60,0
XL 58,640
LL 17,64
LL 126,65
v_mul_f32 v52, v38, v52
M 10,61,1
LA 17
PK 122,18,120,33
v_subrev_u32 v5, s39, v5
HX 27
CF 52,52,52
Z 11,72,0
WL 2
LC_ 126
PK 120,18,120,32
ds_bpermute_b32 v13, v28, v52
M 11,73,1
RA 236,1,18432
v_add_f32 v14, v14, v15
M 6,2,2
LO 16,16,123
LU 123,16
M 6,3,3
SU 108
HS 25,123
v_add_f32_dpp v14, v14, v14 quad_perm:[1,0,3,2] row_mask:0xf bank_mask:0xf
M 7,14,2
s_mul_i32 s53, s50, 25600
LL 18,66
LL 127,67
s_add_u32 s40, %[wlo], s53
s_addc_u32 s41, %[whi], 0
s_mov_b32 m0, s38
M 7,15,3
LA 18
PK 123,16,121,33
v_add_f32_dpp v14, v14, v14 quad_perm:[2,3,0,1] row_mask:0xf bank_mask:0xf
M 10,62,2
LC_ 127
PK 121,16,121,32
M 10,63,3
RA 216,1,19456
XL 59,704
DW 0
v_add_f32_dpp v14, v14, v14 row_half_mirror row_mask:0xf bank_mask:0xf
M 11,74,2
WL 3
LO 17,17,126
LU 126,17
M 11,75,3
s_add_u32 s42, s44, s64
RA 220,1,20480
HS 25,126
v_cvt_f64_f32 v[14:15], v14
s_addc_u32 s43, s45, 0
AR 248,24
M 6,4,4
s_add_u32 s37, s37, 32768
LL 16,56
LL 104,57
s_cmp_eq_u32 s37, s75
s_cselect_b32 s37, s74, s37
AR 249,25
M 6,5,5
LA 16
PK 126,17,124,33
s_mov_b64 exec, s[54:55]
SP
s_mov_b64 exec, -1
DW 1024
AR 250,26
M 7,16,4
LC_ 104
PK 124,17,124,32
AR 251,27
AR 52,28
M 7,17,5
RA 224,1,21504
PO 126
AR 53,29
M 10,64,4
WL 5
LO 18,18,127
LU 127,18
ST 120
AR 54,30
AR 55,31
M 10,65,5
DW 2048
HS 25,127
XS 248
M 11,76,4
WL 3
LL 17,58
LL 105,59
XS 249
XS 250
M 11,77,5
s_add_u32 s40, s40, 3072
LA 17
PK 127,18,125,33
XS 251
M 6,6,0
s_addc_u32 s41, s41, 0
LC_ 105
PK 125,18,125,32
s_add_u32 m0, s38, 3072
AW 24,248
AW 25,249
M 6,7,1
s_add_u32 s42, s44, s65
RA 228,1,22528
PO 127
AW 26,250
M 7,18,0
WL 4
LO 104,16,104
LU 16,104
AW 27,251
XS 52
M 7,19,1
s_addc_u32 s43, s45, 0
RA 232,1,23552
HZ 26,16
XS 53
M 10,66,0
DW 0
LL 18,60
LL 108,61
XS 54
XS 55
M 10,67,1
s_lshl_b32 s53, s52, 8
LA 18
HX 25
AW 28,52
M 11,78,0
WL 2
LC_ 108
W1 25,1024
W1 27,3072
AW 29,53
AW 30,54
M 11,79,1
s_add_u32 s48, %[alo], s53
RA 236,1,24576
v_add_u32 v4, s39, v4
AW 31,55
M 6,8,2
LO 105,17,105
LU 17,105
s_addc_u32 s49, %[ahi], 0
s_add_u32 s52, s52, %[d]
s_cmp_eq_u32 s52, %[wrap]
s_cselect_b32 s52, %[rst], s52
DW 1024
v_min_u32 v35, v35, v13
AR 248,32
M 6,9,3
ST 124
HS 26,17
AR 249,33
M 7,20,2
s_mul_i32 s53, s51, 24
LL 16,62
LL 109,63
s_bitcmp1_b32 s51, 0
s_addc_u32 s53, s53, 0
s_lshl_b32 s53, s53, 11
s_bitcmp1_b64 s[90:91], s51
s_cselect_b64 s[44:45], s[84:85], s[82:83]
s_add_u32 s44, s44, s53
s_addc_u32 s45, s45, 0
s_bitcmp1_b64 s[92:93], s51
s_cselect_b64 s[94:95], s[88:89], s[86:87]
s_add_u32 s94, s94, s53
s_addc_u32 s95, s95, 0
s_add_u32 s51, s51, %[d]
s_cmp_eq_u32 s51, %[wrap]
s_cselect_b32 s51, %[rst], s51
AR 250,34
AR 251,35
M 7,21,3
LA 16
DW 2048
XS 248
M 10,68,2
LC_ 109
DQ 3072
XS 249
XS 250
M 10,69,3
s_add_u32 s50, s50, %[d]
v_add_u32 v1, s37, v0
XS 251
M 11,80,2
WL 2
LO 108,18,108
LU 18,108
AW 32,248
AW 33,249
M 11,81,3
s_cmp_eq_u32 s50, %[wrap]
RA 216,1,0
HS 26,18
AW 34,250
M 6,10,4
s_cselect_b32 s50, %[rst], s50
LL 17,72
LL 120,73
AW 35,251
AR 52,36
M 6,11,5
LA 17
v_and_b32 v22, 0xfffffcff, v1
AR 53,37
M 7,22,4
LC_ 120
RA 220,1,1024
AR 54,38
AR 55,39
M 7,23,5
RV 252,22,5120
v_add_u32 v19, 1, v19
XS 52
M 10,70,4
WL 5
LO 109,16,109
LU 16,109
XS 53
XS 54
M 10,71,5
s_add_u32 s38, s38, 32768
RA 224,1,2048
HS 26,16
XS 55
M 11,82,4
WL 1
LL 18,74
LL 121,75
AW 36,52
AW 37,53
M 11,83,5
s_cmp_eq_u32 s38, s77
LA 18
RA 228,1,3072
s_cselect_b32 s38, s76, s38
FP
s_sub_u32 s39, 0, s39
AW 38,54
Z 0,0,0
LC_ 121
HX 26
AW 39,55
M 0,1,1
AR 248,40
AR 249,41
Z 1,12,0
LO 120,17,120
LU 17,120
AR 250,42
M 1,13,1
RA 232,1,4096
HZ 24,17
AR 251,43
XS 248
Z 2,24,0
LL 16,88
LL 124,89
XS 249
M 2,25,1
LA 16
XS 250
XS 251
Z 3,36,0
LC_ 124
AW 40,248
M 3,37,1
AW 41,249
AW 42,250
Z 4,60,0
WL 3
LO 121,18,121
LU 18,121
AW 43,251
M 4,61,1
RA 236,1,6144
HS 24,18
AR 52,44
AR 53,45
Z 5,72,0
LL 17,90
LL 125,91
AR 54,46
M 5,73,1
LA 17
AR 55,47
XS 52
M 0,2,2
LC_ 125
XS 53
M 0,3,3
XS 54
XS 55
M 1,14,2
WL 3
LO 124,16,124
LU 16,124
AW 44,52
M 1,15,3
RA 216,1,7168
HS 24,16
AW 45,53
AW 46,54
M 2,26,2
LL 18,80
LL 106,81
AW 47,55
M 2,27,3
LA 18
FR
AR 248,48
AR 249,49
M 3,38,2
LC_ 106
AR 250,50
M 3,39,3
WV 32
BW 48
AR 251,51
XS 248
M 4,62,2
WL 5
LO 125,17,125
LU 17,125
XS 249
M 4,63,3
RA 220,1,8192
HS 24,17
XS 250
XS 251
M 5,74,2
LL 16,82
LL 107,83
AW 48,248
M 5,75,3
s_mov_b32 m0, %[pm]
LA 16
DF
AW 49,249
AW 50,250
M 0,4,4
LC_ 107
RV 40,8,6144
AW 51,251
M 0,5,5
RV 44,8,6208
RV 48,8,6272
AR 52,52
AR 53,53
M 1,16,4
WL 7
)"
R"(LO 18,18,106
LU 106,18
AR 54,54
M 1,17,5
RA 224,1,9216
HZ 27,106
AR 55,55
XS 52
M 2,28,4
LL 17,84
LL 110,85
XS 53
M 2,29,5
LA 17
PK 106,18,104,33
XS 54
XS 55
M 3,40,4
LC_ 110
PK 104,18,104,32
AW 52,52
M 3,41,5
BR 48
v_cmp_gt_u32 vcc, v19, v15
AW 53,53
AW 54,54
M 4,64,4
WL 7
LO 16,16,107
LU 107,16
AW 55,55
M 4,65,5
s_add_u32 s42, s94, s60
RA 228,1,10240
HS 27,107
AR 248,56
M 5,76,4
LL 18,86
LL 111,87
AR 249,57
AR 250,58
M 5,77,5
s_cbranch_vccnz .Lk1s14
.Lk1r14:
LA 18
PK 107,16,105,33
AR 251,59
M 0,6,0
s_addc_u32 s43, s95, 0
LC_ 111
PK 105,16,105,32
XS 248
XS 249
M 0,7,1
SU 104
HX 24
XS 250
M 1,18,0
WL 4
LO 17,17,110
LU 110,17
XS 251
AW 56,248
M 1,19,1
RA 232,1,11264
HS 27,110
AW 57,249
M 2,30,0
LL 16,96
LL 122,97
AW 58,250
AW 59,251
M 2,31,1
LA 16
PK 110,17,108,33
AR 52,60
M 3,42,0
LC_ 122
PK 108,17,108,32
AR 53,61
AR 54,62
M 3,43,1
RV 252,22,5392
PO 110
AR 55,63
M 4,66,0
WL 4
LO 18,18,111
LU 111,18
XS 52
XS 53
M 4,67,1
s_add_u32 s42, s94, s61
RA 236,1,12288
HS 27,111
XS 54
M 5,78,0
LL 17,98
LL 123,99
XS 55
AW 60,52
M 5,79,1
s_addc_u32 s43, s95, 0
LA 17
PK 111,18,109,33
AW 61,53
M 0,8,2
LC_ 123
PK 109,18,109,32
AW 62,54
AW 63,55
M 0,9,3
W1 24,0
PO 111
AR 248,64
M 1,20,2
WL 5
LO 16,16,122
LU 122,16
W1 26,2048
AR 249,65
AR 250,66
M 1,21,3
RA 216,1,13312
HZ 25,122
AR 251,67
M 2,32,2
LL 18,100
LL 126,101
XS 248
XS 249
M 2,33,3
LA 18
PK 122,16,120,33
XS 250
M 3,44,2
LC_ 126
PK 120,16,120,32
XS 251
AW 64,248
M 3,45,3
WV 27
SU 108
HX 27
AW 65,249
BW 49
M 4,68,2
WL 6
LO 17,17,123
LU 123,17
AW 66,250
AW 67,251
M 4,69,3
s_add_u32 s42, s44, s64
RA 220,1,14336
BR 49
HS 25,123
AR 52,68
LZ 40,48
s_addc_u32 s43, s45, 0
BW 50
M 5,80,2
LL 16,102
LL 127,103
AR 53,69
AR 54,70
M 5,81,3
s_bitcmp1_b64 s[92:93], 48
LA 16
PK 123,17,121,33
AR 55,71
M 0,10,4
s_cselect_b32 s58, 16, 0
LC_ 127
PK 121,17,121,32
XS 52
XS 53
M 0,11,5
s_lshr_b32 s58, 0xda43cb52, s58
RV 40,8,6336
BR 50
XS 54
M 1,22,4
WL 8
LO 18,18,126
LU 126,18
XS 55
AW 68,52
M 1,23,5
WL 5
RA 224,1,15360
HS 25,126
AW 69,53
LS 44,49
WV 24
BW 51
M 2,34,4
LL 17,56
LL 104,57
AW 70,54
AW 71,55
M 2,35,5
LA 17
PK 126,18,124,33
M 3,46,4
LC_ 104
RV 44,8,6400
PK 124,18,124,32
M 3,47,5
ds_read_b64 v[14:15], v5 offset:0
BR 51
PO 126
M 4,70,4
WL 9
LO 16,16,127
LU 127,16
M 4,71,5
WL 7
RA 228,1,16384
HS 25,127
LS 48,50
WV 23
BW 52
M 5,82,4
LL 18,58
LL 105,59
M 5,83,5
LA 18
RV 48,8,6464
PK 127,16,125,33
Z 6,0,0
LC_ 105
PK 125,16,125,32
M 6,1,1
BR 52
PO 127
v_and_b32 v11, 0x1f0, v0
v_min_u32 v11, 368, v11
v_lshlrev_b32 v11, 3, v11
Z 7,12,0
WL 9
LO 104,17,104
LU 17,104
M 7,13,1
WL 6
RA 232,1,17408
HZ 26,17
LS 40,51
WV 22
BW 53
Z 8,24,0
LL 16,60
LL 108,61
ST 120
M 8,25,1
LA 16
RV 40,8,6528
Z 9,36,0
LC_ 108
BR 53
M 9,37,1
WL 6
v_add_f32 v14, v14, v15
HX 25
LS 44,52
WV 22
BW 54
Z 10,60,0
LO 105,18,105
LU 18,105
v_lshrrev_b32 v3, 9, v0
v_lshl_add_u32 v3, v3, 2, 0
v_bfe_u32 v3, s58, v3, 4
v_mad_u32_u24 v3, v3, s97, v11
M 10,61,1
s_add_u32 s42, s44, s65
RA 236,1,18432
HS 26,18
Z 11,72,0
RV 44,8,6592
LL 17,62
LL 109,63
M 11,73,1
s_addc_u32 s43, s45, 0
LA 17
W1 25,1024
BR 54
M 6,2,2
LC_ 109
W1 27,3072
M 6,3,3
WL 8
DY
v_add_f32_dpp v14, v14, v14 quad_perm:[1,0,3,2] row_mask:0xf bank_mask:0xf
LS 48,53
WV 22
BW 55
M 7,14,2
LO 108,16,108
LU 16,108
M 7,15,3
RA 216,1,19456
HS 26,16
M 8,26,2
WV 21
RV 48,8,6656
LL 18,64
LL 112,65
M 8,27,3
WL 5
LA 18
ST 124
BR 55
M 9,38,2
s_mul_i32 s53, s50, 25600
LC_ 112
v_add_f32_dpp v14, v14, v14 quad_perm:[2,3,0,1] row_mask:0xf bank_mask:0xf
M 9,39,3
s_add_u32 s40, %[wlo], s53
v_add_f32_dpp v14, v14, v14 row_half_mirror row_mask:0xf bank_mask:0xf
v_add_u32 v4, s39, v4
LS 40,54
s_addc_u32 s41, %[whi], 0
BW 56
M 10,62,2
WL 8
LO 109,17,109
LU 17,109
s_mov_b32 m0, s38
M 10,63,3
RA 220,1,20480
RV 40,8,6720
HS 26,17
M 11,74,2
BR 56
LL 16,66
LL 113,67
M 11,75,3
WV 19
LA 16
v_cvt_f64_f32 v[14:15], v14
v_lshrrev_b32 v3, 9, v0
v_lshl_add_u32 v3, v3, 2, 8
v_bfe_u32 v3, s58, v3, 4
v_mad_u32_u24 v3, v3, s97, v11
M 6,4,4
WL 6
LC_ 113
DW 0
M 6,5,5
s_add_u32 s37, s37, 32768
s_mov_b64 exec, s[54:55]
SP
s_mov_b64 exec, -1
LS 44,55
s_cmp_eq_u32 s37, s75
BW 57
M 7,16,4
WL 7
RV 44,8,6784
LO 112,18,112
LU 18,112
M 7,17,5
s_cselect_b32 s37, s74, s37
RA 224,1,21504
HZ 29,18
M 8,28,4
BR 57
LL 17,68
LL 116,69
M 8,29,5
WL 6
LA 17
ds_read_b64 v[14:15], v5 offset:2048
LS 48,56
WV 20
BW 58
M 9,40,4
LC_ 116
DY
M 9,41,5
DW 1024
RV 48,8,6848
M 10,64,4
WL 9
LO 113,16,113
LU 16,113
M 10,65,5
RA 228,1,22528
RA 240,7,0
HS 29,16
M 11,76,4
WV 20
BR 58
LL 18,70
LL 117,71
M 11,77,5
WL 8
LA 18
DW 2048
)"
R"(M 6,6,0
s_mul_i32 s53, s51, 24
LC_ 117
v_subrev_u32 v5, s39, v5
M 6,7,1
s_bitcmp1_b32 s51, 0
LS 40,57
s_addc_u32 s53, s53, 0
BW 59
M 7,18,0
WL 3
RA 244,7,256
LO 116,17,116
LU 17,116
M 7,19,1
s_lshl_b32 s53, s53, 11
RA 232,1,23552
HS 29,17
M 8,30,0
s_bitcmp1_b64 s[90:91], s51
BR 59
LL 16,72
LL 120,73
M 8,31,1
s_cselect_b64 s[44:45], s[84:85], s[82:83]
LA 16
v_add_f32 v14, v14, v15
s_add_u32 s44, s44, s53
s_addc_u32 s45, s45, 0
s_bitcmp1_b64 s[92:93], s51
s_cselect_b64 s[94:95], s[88:89], s[86:87]
s_add_u32 s94, s94, s53
s_addc_u32 s95, s95, 0
s_add_u32 s51, s51, %[d]
s_cmp_eq_u32 s51, %[wrap]
s_cselect_b32 s51, %[rst], s51
s_mul_i32 s53, %[d], 16384
s_add_u32 s62, s53, %[sb1]
s_add_u32 s63, s62, 512
LS 44,58
s_add_u32 s40, s40, 3072
RA 248,7,512
v_add_f32_dpp v14, v14, v14 quad_perm:[1,0,3,2] row_mask:0xf bank_mask:0xf
M 9,42,0
s_addc_u32 s41, s41, 0
LC_ 120
v_add_f32_dpp v14, v14, v14 quad_perm:[2,3,0,1] row_mask:0xf bank_mask:0xf
s_add_u32 m0, s38, 3072
M 9,43,1
DW 0
v_add_f32_dpp v14, v14, v14 row_half_mirror row_mask:0xf bank_mask:0xf
M 10,66,0
WL 7
LO 117,18,117
LU 18,117
M 10,67,1
WL 3
RA 236,1,24576
HS 29,18
LS 48,59
RA 252,7,768
v_cvt_f64_f32 v[14:15], v14
M 11,78,0
LL 17,74
LL 121,75
s_nop 2
v_mul_f32 v36, 0xbb2aaaab, v36
M 11,79,1
LA 17
DW 1024
v_mul_f32 v37, 0xbb2aaaab, v37
v_mul_f32 v38, 0xbb2aaaab, v38
M 6,8,2
LC_ 121
s_mov_b64 exec, s[54:55]
SP
s_mov_b64 exec, -1
v_mul_f32 v39, 0xbb2aaaab, v39
M 6,9,3
DW 2048
v_add_u32 v1, s37, v0
LK 40,240,48,0
LK 44,242,48,2
s_lshl_b32 s53, s52, 8
RA 240,7,1024
v_pk_add_f16 v26, v26, v29
LK 48,244,49,0
CF 40,40,41
LK 248,246,49,2
s_add_u32 s48, %[alo], s53
RA 244,7,1280
CF 42,42,43
M 7,20,2
WL 6
LO 120,16,120
LU 16,120
M 7,21,3
s_addc_u32 s49, %[ahi], 0
RA 216,1,0
HZ 24,16
CF 44,44,45
M 8,32,2
s_add_u32 s52, s52, %[d]
LL 18,76
LL 124,77
CF 46,46,47
AW 72,40
M 8,33,3
s_cmp_eq_u32 s52, %[wrap]
LA 18
v_and_b32 v22, 0xfffffcff, v1
AW 73,42
AW 74,44
M 9,44,2
s_cselect_b32 s52, %[rst], s52
LC_ 124
DQ 3072
AW 75,46
M 9,45,3
HX 26
LK 40,248,50,0
CF 48,48,49
LK 44,250,50,2
RA 248,7,1536
CF 50,50,51
GZ 48
CF 248,248,249
CF 250,250,251
M 10,68,2
WL 6
LO 121,17,121
LU 17,121
M 10,69,3
s_add_u32 s50, s50, %[d]
RA 220,1,1024
HS 24,17
AW 76,48
M 11,80,2
LL 16,78
LL 125,79
AW 77,50
AW 78,248
AW 79,250
M 11,81,3
s_cmp_eq_u32 s50, %[wrap]
LA 16
RV 252,22,5120
LK 48,252,51,0
CF 40,40,41
LK 248,254,51,2
s_cselect_b32 s50, %[rst], s50
RA 252,7,1792
CF 42,42,43
GS 49
CF 44,44,45
CF 46,46,47
M 6,10,4
LC_ 125
AW 80,40
M 6,11,5
v_add_u32 v19, 1, v19
AW 81,42
M 7,22,4
WL 6
LO 124,18,124
LU 18,124
M 7,23,5
s_add_u32 s38, s38, 32768
RA 224,1,2048
HS 24,18
AW 82,44
AW 83,46
LK 40,240,52,0
CF 48,48,49
LK 44,242,52,2
s_cmp_eq_u32 s38, s77
RA 240,7,2048
CF 50,50,51
GS 50
s_cselect_b32 s38, s76, s38
CF 248,248,249
CF 250,250,251
M 8,34,4
LL 17,80
LL 106,81
M 8,35,5
LA 17
FP
M 9,46,4
s_sub_u32 s39, 0, s39
LC_ 106
AW 84,48
M 9,47,5
AW 85,50
AW 86,248
M 10,70,4
WL 5
LO 125,16,125
LU 16,125
AW 87,250
M 10,71,5
RA 228,1,3072
HS 24,16
LK 48,244,53,0
CF 40,40,41
LK 248,246,53,2
RA 244,7,2304
CF 42,42,43
GS 51
CF 44,44,45
CF 46,46,47
M 11,82,4
LL 18,82
LL 107,83
M 11,83,5
LA 18
HX 24
Z 0,0,0
LC_ 107
AW 88,40
M 0,1,1
AW 89,42
AW 90,44
AW 91,46
LK 40,248,54,0
CF 48,48,49
LK 44,250,54,2
RA 248,7,2560
CF 50,50,51
GS 52
CF 248,248,249
CF 250,250,251
Z 1,12,0
WL 5
LO 17,17,106
LU 106,17
M 1,13,1
RA 232,1,4096
HZ 27,106
Z 2,24,0
LL 16,84
LL 110,85
M 2,25,1
LA 16
PK 106,17,104,33
AW 92,48
Z 3,36,0
LC_ 110
PK 104,17,104,32
AW 93,50
AW 94,248
AW 95,250
M 3,37,1
LK 48,252,55,0
CF 40,40,41
LK 248,254,55,2
RA 252,7,2816
CF 42,42,43
GS 53
CF 44,44,45
CF 46,46,47
Z 4,60,0
WL 5
LO 18,18,107
LU 107,18
M 4,61,1
s_add_u32 s42, s94, s60
RA 236,1,6144
HS 27,107
Z 5,72,0
LL 17,86
LL 111,87
AW 96,40
AW 97,42
M 5,73,1
s_addc_u32 s43, s95, 0
LA 17
PK 107,18,105,33
AW 98,44
AW 99,46
LK 40,240,56,0
CF 48,48,49
LK 44,242,56,2
CF 50,50,51
GS 54
CF 248,248,249
CF 250,250,251
M 0,2,2
LC_ 111
PK 105,18,105,32
M 0,3,3
SU 104
AW 100,48
M 1,14,2
WL 4
LO 16,16,110
LU 110,16
M 1,15,3
RA 216,1,7168
HS 27,110
AW 101,50
AW 102,248
M 2,26,2
LL 18,88
LL 114,89
AW 103,250
M 2,27,3
LA 18
PK 110,16,108,33
LK 48,244,57,0
CF 40,40,41
LK 248,246,57,2
CF 42,42,43
GS 55
CF 44,44,45
CF 46,46,47
M 3,38,2
LC_ 114
PK 108,16,108,32
M 3,39,3
FR
PO 110
AW 104,40
M 4,62,2
WL 4
LO 17,17,111
LU 111,17
AW 105,42
AW 106,44
AW 107,46
M 4,63,3
s_add_u32 s42, s94, s61
RA 220,1,8192
HS 27,111
LK 40,248,58,0
CF 48,48,49
LK 44,250,58,2
s_addc_u32 s43, s95, 0
CF 50,50,51
GS 56
CF 248,248,249
CF 250,250,251
M 5,74,2
LL 16,90
LL 115,91
M 5,75,3
s_mov_b32 m0, %[pm]
LA 16
PK 111,17,109,33
M 0,4,4
LC_ 115
PK 109,17,109,32
AW 108,48
AW 109,50
M 0,5,5
DF
PO 111
AW 110,248
AW 111,250
LK 48,252,59,0
CF 40,40,41
LK 248,254,59,2
CF 42,42,43
GS 57
CF 44,44,45
CF 46,46,47
M 1,16,4
WL 4
LO 18,18,114
LU 114,18
M 1,17,5
RA 224,1,9216
HZ 29,114
M 2,28,4
LL 17,92
LL 118,93
AW 112,40
M 2,29,5
LA 17
PK 114,18,112,33
AW 113,42
AW 114,44
M 3,40,4
LC_ 118
RV 36,8,6912
PK 112,18,112,32
AW 115,46
M 3,41,5
WL 7
SU 108
v_cmp_gt_u32 vcc, v19, v15
GS 58
CF 48,48,49
CF 50,50,51
M 4,64,4
WL 4
LO 16,16,115
LU 115,16
CF 248,248,249
CF 250,250,251
)"
R"(AW 116,48
M 4,65,5
s_add_u32 s42, s94, s62
RA 228,1,10240
HS 29,115
AW 117,50
AW 118,248
M 5,76,4
LL 18,94
LL 119,95
AW 119,250
M 5,77,5
s_cbranch_vccnz .Lk1s15
.Lk1r15:
LA 18
PK 115,16,113,33
GS 59
s_addc_u32 s43, s95, 0
W1 24,0
s_nop 5
v_cndmask_b32 v52, v52, v53, s[78:79]
M 0,6,0
LC_ 119
PK 113,16,113,32
v_cndmask_b32 v54, v54, v55, s[78:79]
M 0,7,1
W1 26,2048
v_cndmask_b32 v52, v52, v54, s[80:81]
M 1,18,0
WL 6
LO 17,17,118
LU 118,17
v_mul_f32 v52, 0x3b2aaaab, v52
M 1,19,1
WL 5
RA 232,1,11264
HS 29,118
v_med3_f32 v53, v52, v39, v37
M 2,30,0
LL 16,96
LL 122,97
LG 52,53
M 2,31,1
LA 16
PK 118,17,116,33
C 52,52,52
M 3,42,0
LC_ 122
PK 116,17,116,32
v_lshlrev_b32 v52, 16, v52
M 3,43,1
RV 252,22,5392
PO 118
v_add_f32 v52, v36, v52
M 4,66,0
WL 5
LO 18,18,119
LU 119,18
C 52,52,52
M 4,67,1
RA 236,1,12288
HS 29,119
M 5,78,0
LL 17,98
LL 123,99
v_lshlrev_b32 v52, 16, v52
M 5,79,1
LA 17
PK 119,18,117,33
v_sqrt_f32 v52, v52
M 0,8,2
LC_ 123
PK 117,18,117,32
C 52,52,52
M 0,9,3
SU 112
PO 119
v_lshlrev_b32 v52, 16, v52
M 1,20,2
WL 4
LO 16,16,122
LU 122,16
v_rcp_f32 v52, v52
M 1,21,3
s_add_u32 s42, s94, s63
RA 216,1,13312
HZ 25,122
v_mul_f32 v52, v38, v52
M 2,32,2
s_addc_u32 s43, s95, 0
LL 18,100
LL 126,101
CF 52,52,52
M 2,33,3
LA 18
PK 122,16,120,33
ds_bpermute_b32 v13, v28, v52
M 3,44,2
LC_ 126
PK 120,16,120,32
M 3,45,3
SU 116
v_pk_add_f16 v27, v27, v29
M 4,68,2
WL 4
LO 17,17,123
LU 123,17
M 4,69,3
s_add_u32 s42, s44, s64
RA 220,1,14336
HS 25,123
M 5,80,2
LL 16,102
LL 127,103
M 5,81,3
s_addc_u32 s43, s45, 0
LA 16
PK 123,17,121,33
M 0,10,4
LC_ 127
PK 121,17,121,32
M 0,11,5
ST 120
HX 27
M 1,22,4
WL 3
LO 18,18,126
LU 126,18
M 1,23,5
RA 224,1,15360
HS 25,126
AR 248,72
M 2,34,4
LL 17,56
LL 104,57
AR 249,73
M 2,35,5
LA 17
PK 126,18,124,33
AR 250,74
M 3,46,4
LC_ 104
PK 124,18,124,32
AR 251,75
M 3,47,5
ds_read_b64 v[14:15], v5 offset:0
PO 126
AR 52,76
M 4,70,4
WL 4
LO 16,16,127
LU 127,16
AR 53,77
M 4,71,5
s_add_u32 s42, s44, s65
RA 228,1,16384
HS 25,127
AR 54,78
M 5,82,4
LL 18,58
LL 105,59
AR 55,79
M 5,83,5
s_addc_u32 s43, s45, 0
LA 18
PK 127,16,125,33
XS 248
Z 6,0,0
LC_ 105
PK 125,16,125,32
XS 249
M 6,1,1
PO 127
HX 25
XS 250
Z 7,12,0
WL 4
LO 104,17,104
LU 17,104
XS 251
AW 72,248
M 7,13,1
RA 232,1,17408
HZ 26,17
AW 73,249
Z 8,24,0
WL 4
LL 16,60
LL 108,61
AW 74,250
M 8,25,1
s_mul_i32 s53, s50, 25600
LA 16
W1 25,1024
AW 75,251
Z 9,36,0
s_add_u32 s40, %[wlo], s53
LC_ 108
W1 27,3072
XS 52
M 9,37,1
s_addc_u32 s41, %[whi], 0
v_add_f32 v14, v14, v15
v_add_u32 v4, s39, v4
XS 53
Z 10,60,0
WL 5
LO 105,18,105
LU 18,105
XS 54
M 10,61,1
s_mov_b32 m0, s38
RA 236,1,18432
HS 26,18
XS 55
Z 11,72,0
LL 17,62
LL 109,63
AW 76,52
M 11,73,1
LA 17
ST 124
AW 77,53
M 6,2,2
LC_ 109
DW 0
AW 78,54
AW 79,55
M 6,3,3
v_add_f32_dpp v14, v14, v14 quad_perm:[1,0,3,2] row_mask:0xf bank_mask:0xf
v_min_u32 v35, v35, v13
M 7,14,2
WL 4
LO 108,16,108
LU 16,108
AR 248,80
M 7,15,3
s_add_u32 s37, s37, 32768
RA 216,1,19456
HS 26,16
AR 249,81
M 8,26,2
s_cmp_eq_u32 s37, s75
LL 18,64
LL 112,65
AR 250,82
M 8,27,3
s_cselect_b32 s37, s74, s37
LA 18
v_add_f32_dpp v14, v14, v14 quad_perm:[2,3,0,1] row_mask:0xf bank_mask:0xf
AR 251,83
M 9,38,2
s_mul_i32 s53, %[d], 393216
LC_ 112
v_add_f32_dpp v14, v14, v14 row_half_mirror row_mask:0xf bank_mask:0xf
XS 248
M 9,39,3
s_add_u32 s53, s53, s57
v_cvt_f64_f32 v[14:15], v14
XS 249
M 10,62,2
WL 3
LO 109,17,109
LU 17,109
XS 250
M 10,63,3
s_add_u32 s46, %[xlo], s53
RA 220,1,20480
HS 26,17
XS 251
M 11,74,2
LL 16,66
LL 113,67
AW 80,248
AW 81,249
M 11,75,3
s_addc_u32 s47, %[xhi], 0
LA 16
s_mov_b64 exec, s[54:55]
SP
s_mov_b64 exec, -1
AW 82,250
M 6,4,4
s_bitcmp1_b64 s[92:93], 48
LC_ 113
ds_read_b64 v[14:15], v5 offset:2048
AW 83,251
M 6,5,5
s_cselect_b32 s58, 16, 0
v_subrev_u32 v5, s39, v5
AR 52,84
M 7,16,4
WL 4
LO 112,18,112
LU 18,112
AR 53,85
M 7,17,5
s_lshr_b32 s58, 0xda43cb52, s58
RA 224,1,21504
HZ 29,18
AR 54,86
M 8,28,4
LL 17,68
LL 116,69
AR 55,87
M 8,29,5
LA 17
v_lshrrev_b32 v3, 6, v0
XS 52
M 9,40,4
LC_ 116
v_and_b32 v3, 12, v3
v_bfe_u32 v3, s58, v3, 4
v_mad_u32_u24 v3, v3, s97, v21
XS 53
M 9,41,5
DW 1024
XS 54
M 10,64,4
WL 4
LO 113,16,113
LU 16,113
XS 55
AW 84,52
M 10,65,5
RA 228,1,22528
HS 29,16
AW 85,53
M 11,76,4
LL 18,70
LL 117,71
AW 86,54
M 11,77,5
LA 18
XL 60,0
AW 87,55
M 6,6,0
LC_ 117
DW 2048
AR 248,88
M 6,7,1
WL 6
XL 61,64
v_add_f32 v14, v14, v15
AR 249,89
M 7,18,0
WL 3
LO 116,17,116
LU 17,116
AR 250,90
M 7,19,1
s_mul_i32 s53, s51, 24
RA 232,1,23552
HS 29,17
AR 251,91
M 8,30,0
s_bitcmp1_b32 s51, 0
LL 16,72
LL 120,73
XS 248
M 8,31,1
s_addc_u32 s53, s53, 0
LA 16
v_add_f32_dpp v14, v14, v14 quad_perm:[1,0,3,2] row_mask:0xf bank_mask:0xf
s_lshl_b32 s53, s53, 11
s_bitcmp1_b64 s[90:91], s51
s_cselect_b64 s[44:45], s[84:85], s[82:83]
s_add_u32 s44, s44, s53
s_addc_u32 s45, s45, 0
s_bitcmp1_b64 s[92:93], s51
s_cselect_b64 s[94:95], s[88:89], s[86:87]
s_add_u32 s94, s94, s53
s_addc_u32 s95, s95, 0
s_add_u32 s51, s51, %[d]
s_cmp_eq_u32 s51, %[wrap]
s_cselect_b32 s51, %[rst], s51
XS 249
M 9,42,0
s_add_u32 s40, s40, 3072
LC_ 120
v_add_f32_dpp v14, v14, v14 quad_perm:[2,3,0,1] row_mask:0xf bank_mask:0xf
s_addc_u32 s41, s41, 0
s_add_u32 m0, s38, 3072
XS 250
XS 251
M 9,43,1
XL 62,128
v_add_f32_dpp v14, v14, v14 row_half_mirror row_mask:0xf bank_mask:0xf
AW 88,248
M 10,66,0
WL 3
LO 117,18,117
LU 18,117
AW 89,249
M 10,67,1
RA 236,1,24576
HS 29,18
AW 90,250
M 11,78,0
LL 17,74
)"
R"(LL 121,75
AW 91,251
M 11,79,1
LA 17
DW 0
AR 52,92
M 6,8,2
LC_ 121
XL 63,192
AR 53,93
M 6,9,3
DW 1024
v_add_u32 v1, s37, v0
AR 54,94
M 7,20,2
WL 3
LO 120,16,120
LU 16,120
AR 55,95
M 7,21,3
RA 216,1,0
HZ 24,16
XS 52
M 8,32,2
LL 18,76
LL 124,77
v_cvt_f64_f32 v[14:15], v14
XS 53
XS 54
M 8,33,3
LA 18
s_mov_b64 exec, s[54:55]
SP
s_mov_b64 exec, -1
XS 55
M 9,44,2
LC_ 124
XL 64,256
AW 92,52
M 9,45,3
DW 2048
v_and_b32 v22, 0xfffffcff, v1
AW 93,53
M 10,68,2
WL 3
LO 121,17,121
LU 17,121
AW 94,54
M 10,69,3
RA 220,1,1024
HS 24,17
AW 95,55
M 11,80,2
LL 16,78
LL 125,79
AR 248,96
M 11,81,3
LA 16
RV 252,22,5120
AR 249,97
M 6,10,4
LC_ 125
XL 65,320
AR 250,98
M 6,11,5
s_lshl_b32 s53, s52, 8
v_pk_add_f16 v26, v26, v29
AR 251,99
M 7,22,4
WL 4
LO 124,18,124
LU 18,124
HX 26
XS 248
XS 249
M 7,23,5
s_add_u32 s48, %[alo], s53
RA 224,1,2048
HS 24,18
XS 250
M 8,34,4
s_addc_u32 s49, %[ahi], 0
LL 17,80
LL 106,81
s_add_u32 s52, s52, %[d]
s_cmp_eq_u32 s52, %[wrap]
s_cselect_b32 s52, %[rst], s52
XS 251
M 8,35,5
LA 17
DQ 3072
AW 96,248
M 9,46,4
LC_ 106
XL 66,384
AW 97,249
M 9,47,5
s_add_u32 s50, s50, %[d]
AW 98,250
AW 99,251
M 10,70,4
WL 3
LO 125,16,125
LU 16,125
M 10,71,5
RA 228,1,3072
HS 24,16
AR 52,100
M 11,82,4
LL 18,82
LL 107,83
AR 53,101
M 11,83,5
LA 18
HX 24
s_cmp_eq_u32 s50, %[wrap]
s_cselect_b32 s50, %[rst], s50
XL 67,448
AR 54,102
Z 0,0,0
WV 40
LC_ 107
XL 68,512
v_add_u32 v19, 1, v19
s_add_u32 s38, s38, 32768
s_cmp_eq_u32 s38, s77
s_cselect_b32 s38, s76, s38
FP
s_sub_u32 s39, 0, s39
AR 55,103
XS 52
M 0,1,1
XL 69,576
XS 53
Z 1,12,0
WL 4
LO 17,17,106
LU 106,17
XS 54
M 1,13,1
RA 232,1,4096
HZ 27,106
XS 55
Z 2,24,0
LL 16,84
LL 110,85
AW 100,52
M 2,25,1
LA 16
PK 106,17,104,33
AW 101,53
Z 3,36,0
WL 5
LC_ 110
XL 70,640
PK 104,17,104,32
AW 102,54
M 3,37,1
RA 236,1,6144
AW 103,55
M 0,2,2
WL 5
LO 18,18,107
LU 107,18
AR 248,104
M 0,3,3
FR
HS 27,107
AR 249,105
M 1,14,2
LL 17,86
LL 111,87
AR 250,106
AR 251,107
M 1,15,3
LA 17
PK 107,18,105,33
XS 248
M 2,26,2
LC_ 111
XL 71,704
PK 105,18,105,32
XS 249
M 2,27,3
s_add_u32 s42, s94, s60
RA 216,1,7168
XS 250
M 3,38,2
WL 4
LO 16,16,110
LU 110,16
XS 251
M 3,39,3
s_addc_u32 s43, s95, 0
RA 220,1,8192
HS 27,110
AW 104,248
M 0,4,4
LL 18,88
LL 114,89
AW 105,249
M 0,5,5
LA 18
PK 110,16,108,33
AW 106,250
M 1,16,4
LC_ 114
PK 108,16,108,32
AW 107,251
M 1,17,5
RA 224,1,9216
PO 110
AR 52,108
M 2,28,4
WL 5
LO 17,17,111
LU 111,17
SU 104
AR 53,109
AR 54,110
M 2,29,5
s_add_u32 s42, s94, s61
HS 27,111
v_cmp_gt_u32 vcc, v19, v15
AR 55,111
M 3,40,4
WL 3
LL 16,90
LL 115,91
XS 52
M 3,41,5
s_addc_u32 s43, s95, 0
LA 16
PK 111,17,109,33
XS 53
M 0,6,0
s_cbranch_vccnz .Lk1s16
.Lk1r16:
LC_ 115
PK 109,17,109,32
XS 54
M 0,7,1
WV 18
RA 228,1,10240
PO 111
XS 55
BW 60
M 1,18,0
WL 5
LO 18,18,114
LU 114,18
AW 108,52
M 1,19,1
RA 232,1,11264
RV 40,8,6144
HZ 29,114
AW 109,53
M 2,30,0
RV 44,8,6208
LL 17,92
LL 118,93
AW 110,54
M 2,31,1
LA 17
RV 48,8,6272
PK 114,18,112,33
AW 111,55
M 3,42,0
WL 6
LC_ 118
BR 60
PK 112,18,112,32
AR 248,112
AR 249,113
M 3,43,1
WV 16
RV 252,22,5392
ds_read_b64 v[14:15], v5 offset:0
W1 24,0
W1 26,2048
BW 61
AR 250,114
M 0,8,2
BR 61
LO 16,16,115
LU 115,16
AR 251,115
M 0,9,3
WL 6
RA 236,1,12288
HS 29,115
XS 248
LZ 40,60
WV 15
BW 62
M 1,20,2
LL 18,94
LL 119,95
XS 249
M 1,21,3
LA 18
RV 40,8,6336
PK 115,16,113,33
XS 250
M 2,32,2
LC_ 119
BR 62
PK 113,16,113,32
XS 251
M 2,33,3
WL 6
RA 216,1,13312
AW 112,248
LS 44,61
WV 13
BW 63
M 3,44,2
WL 7
LO 17,17,118
LU 118,17
SU 108
AW 113,249
M 3,45,3
s_add_u32 s42, s94, s62
RA 220,1,14336
HS 29,118
s_addc_u32 s43, s95, 0
RV 44,8,6400
AW 114,250
M 0,10,4
BR 63
LL 16,96
LL 122,97
AW 115,251
AR 52,116
M 0,11,5
WL 5
LA 16
PK 118,17,116,33
AR 53,117
LS 48,62
WV 11
BW 64
M 1,22,4
LC_ 122
PK 116,17,116,32
AR 54,118
M 1,23,5
RA 224,1,15360
RV 48,8,6464
PO 118
AR 55,119
M 2,34,4
LO 18,18,119
LU 119,18
SU 112
XS 52
M 2,35,5
s_add_u32 s42, s94, s63
BR 64
HS 29,119
XS 53
M 3,46,4
WL 6
LL 17,98
LL 123,99
XS 54
M 3,47,5
s_addc_u32 s43, s95, 0
LA 17
PK 119,18,117,33
XS 55
LS 40,63
WV 10
BW 65
Z 6,0,0
LC_ 123
PK 117,18,117,32
v_add_f32 v14, v14, v15
s_nop 1
v_add_f32_dpp v14, v14, v14 quad_perm:[1,0,3,2] row_mask:0xf bank_mask:0xf
s_nop 1
v_add_f32_dpp v14, v14, v14 quad_perm:[2,3,0,1] row_mask:0xf bank_mask:0xf
s_nop 1
v_add_f32_dpp v14, v14, v14 row_half_mirror row_mask:0xf bank_mask:0xf
v_cvt_f64_f32 v[14:15], v14
s_mov_b64 exec, s[54:55]
SP
s_mov_b64 exec, -1
s_nop 0
ds_read_b64 v[14:15], v5 offset:2048
RV 40,8,6528
AW 116,52
M 6,1,1
WL 5
RA 228,1,16384
BR 65
PO 119
AW 117,53
LS 44,64
)"
R"(WV 9
BW 66
Z 7,12,0
LO 16,16,122
LU 122,16
AW 118,54
AW 119,55
M 7,13,1
s_mul_i32 s53, s50, 25600
RA 232,1,17408
HZ 25,122
Z 8,24,0
s_add_u32 s40, %[wlo], s53
RV 44,8,6592
LL 18,100
LL 126,101
M 8,25,1
s_addc_u32 s41, %[whi], 0
LA 18
BR 66
PK 122,16,120,33
v_pk_add_f16 v27, v27, v29
Z 9,36,0
WL 5
LC_ 126
PK 120,16,120,32
HX 27
M 9,37,1
s_mov_b32 m0, s38
RA 236,1,18432
v_subrev_u32 v5, s39, v5
LS 48,65
WV 8
BW 67
M 6,2,2
RV 48,8,6656
LO 17,17,123
LU 123,17
M 6,3,3
DW 0
BR 67
HS 25,123
M 7,14,2
LL 16,102
LL 127,103
M 7,15,3
WL 5
LA 16
PK 123,17,121,33
LS 40,66
WV 8
BW 68
M 8,26,2
LC_ 127
RV 40,8,6720
PK 121,17,121,32
M 8,27,3
WV 7
RA 216,1,19456
BR 68
M 9,38,2
WL 6
LO 18,18,126
LU 126,18
SU 116
M 9,39,3
s_add_u32 s42, s44, s64
RA 220,1,20480
HS 25,126
s_addc_u32 s43, s45, 0
LS 44,67
s_add_u32 s37, s37, 32768
BW 69
M 6,4,4
s_cmp_eq_u32 s37, s75
LL 17,56
LL 104,57
s_cselect_b32 s37, s74, s37
M 6,5,5
LA 17
PK 126,18,124,33
v_add_f32 v14, v14, v15
s_nop 1
v_add_f32_dpp v14, v14, v14 quad_perm:[1,0,3,2] row_mask:0xf bank_mask:0xf
s_nop 1
v_add_f32_dpp v14, v14, v14 quad_perm:[2,3,0,1] row_mask:0xf bank_mask:0xf
s_nop 1
v_add_f32_dpp v14, v14, v14 row_half_mirror row_mask:0xf bank_mask:0xf
v_cvt_f64_f32 v[14:15], v14
s_mov_b64 exec, s[54:55]
SP
s_mov_b64 exec, -1
DW 1024
RV 44,8,6784
M 7,16,4
LC_ 104
BR 69
PK 124,18,124,32
M 7,17,5
WL 6
RA 224,1,21504
PO 126
LS 48,68
WV 9
BW 70
M 8,28,4
LO 16,16,127
LU 127,16
ST 120
M 8,29,5
WV 9
DW 2048
RV 48,8,6848
HS 25,127
M 9,40,4
WL 3
RA 240,7,0
LL 18,58
LL 105,59
M 9,41,5
s_add_u32 s40, s40, 3072
LA 18
BR 70
PK 127,16,125,33
M 6,6,0
s_addc_u32 s41, s41, 0
LC_ 105
PK 125,16,125,32
s_add_u32 m0, s38, 3072
M 6,7,1
s_add_u32 s42, s44, s65
RA 228,1,22528
PO 127
LS 40,69
s_addc_u32 s43, s45, 0
BW 71
M 7,18,0
RA 244,7,256
LO 104,17,104
LU 17,104
M 7,19,1
RA 232,1,23552
BR 71
HZ 26,17
M 8,30,0
WL 6
DW 0
LL 16,60
LL 108,61
M 8,31,1
s_lshl_b32 s53, s52, 8
LA 16
HX 25
LS 44,70
s_add_u32 s48, %[alo], s53
RA 248,7,512
M 9,42,0
WL 2
LC_ 108
W1 25,1024
W1 27,3072
M 9,43,1
s_addc_u32 s49, %[ahi], 0
RA 236,1,24576
v_add_u32 v4, s39, v4
LS 48,71
s_add_u32 s52, s52, %[d]
RA 252,7,768
M 6,8,2
LO 105,18,105
LU 18,105
s_cmp_eq_u32 s52, %[wrap]
s_cselect_b32 s52, %[rst], s52
DW 1024
v_mul_f32 v36, 0xbb2aaaab, v36
M 6,9,3
DW 2048
HS 26,18
v_mul_f32 v37, 0xbb2aaaab, v37
v_mul_f32 v38, 0xbb2aaaab, v38
M 7,20,2
LL 17,62
LL 109,63
v_mul_f32 v39, 0xbb2aaaab, v39
M 7,21,3
LA 17
ST 124
LK 40,240,60,0
LK 44,242,60,2
s_mul_i32 s53, s51, 24
RA 240,7,1024
v_add_u32 v1, s37, v0
LK 48,244,61,0
CF 40,40,41
LK 248,246,61,2
s_bitcmp1_b32 s51, 0
RA 244,7,1280
CF 42,42,43
M 8,32,2
s_addc_u32 s53, s53, 0
LC_ 109
RA 216,1,0
M 8,33,3
s_lshl_b32 s53, s53, 11
v_and_b32 v22, 0xfffffcff, v1
CF 44,44,45
CF 46,46,47
M 9,44,2
WL 6
LO 108,16,108
LU 16,108
AW 120,40
AW 121,42
M 9,45,3
s_bitcmp1_b64 s[92:93], s51
RA 220,1,1024
HS 26,16
s_cselect_b64 s[94:95], s[88:89], s[86:87]
s_add_u32 s94, s94, s53
s_addc_u32 s95, s95, 0
s_add_u32 s51, s51, %[d]
s_cmp_eq_u32 s51, %[wrap]
s_cselect_b32 s51, %[rst], s51
DQ 3072
AW 122,44
AW 123,46
LK 40,248,62,0
CF 48,48,49
LK 44,250,62,2
s_add_u32 s50, s50, %[d]
RA 248,7,1536
CF 50,50,51
GZ 60
s_cmp_eq_u32 s50, %[wrap]
CF 248,248,249
CF 250,250,251
M 6,10,4
s_cselect_b32 s50, %[rst], s50
LL 18,64
LL 112,65
M 6,11,5
LA 18
RV 252,22,5120
M 7,22,4
WL 7
LC_ 112
v_add_u32 v19, 1, v19
AW 124,48
M 7,23,5
s_add_u32 s38, s38, 32768
RA 224,1,2048
AW 125,50
AW 126,248
AW 127,250
LK 48,252,63,0
CF 40,40,41
LK 248,254,63,2
s_cmp_eq_u32 s38, s77
RA 252,7,1792
CF 42,42,43
GS 61
s_cselect_b32 s38, s76, s38
CF 44,44,45
CF 46,46,47
M 8,34,4
WL 7
LO 109,17,109
LU 17,109
M 8,35,5
FP
HS 26,17
M 9,46,4
WL 4
LL 16,66
LL 113,67
M 9,47,5
s_sub_u32 s39, 0, s39
LA 16
RA 228,1,3072
AW 128,40
AW 129,42
Z 0,0,0
LC_ 113
AW 130,44
AW 131,46
M 0,1,1
LK 40,240,64,0
CF 48,48,49
LK 44,242,64,2
RA 240,7,2048
CF 50,50,51
GS 62
CF 248,248,249
CF 250,250,251
Z 1,12,0
WL 7
LO 112,18,112
LU 18,112
M 1,13,1
RA 232,1,4096
HZ 29,18
AW 132,48
Z 2,24,0
LL 17,68
LL 116,69
AW 133,50
AW 134,248
M 2,25,1
LA 17
FR
AW 135,250
LK 48,244,65,0
CF 40,40,41
LK 248,246,65,2
RA 244,7,2304
CF 42,42,43
GS 63
CF 44,44,45
CF 46,46,47
Z 10,36,0
WL 6
LC_ 116
AW 136,40
M 10,37,1
RA 236,1,6144
AW 137,42
M 0,2,2
WL 7
LO 113,16,113
LU 16,113
M 0,3,3
HS 29,16
AW 138,44
AW 139,46
LK 40,248,66,0
CF 48,48,49
LK 44,250,66,2
RA 248,7,2560
CF 50,50,51
GS 64
CF 248,248,249
CF 250,250,251
M 1,14,2
LL 18,70
LL 117,71
M 1,15,3
LA 18
RA 216,1,7168
M 2,26,2
WL 6
LC_ 117
v_cmp_gt_u32 vcc, v19, v15
M 2,27,3
AW 140,48
AW 141,50
AW 142,248
M 10,38,2
WL 4
LO 116,17,116
LU 17,116
AW 143,250
M 10,39,3
RA 220,1,8192
HS 29,17
LK 48,252,67,0
CF 40,40,41
LK 248,254,67,2
RA 252,7,2816
CF 42,42,43
GS 65
CF 44,44,45
CF 46,46,47
M 0,4,4
s_cbranch_vccnz .Lk1s17
.Lk1r17:
LL 16,80
LL 106,81
M 0,5,5
LA 16
ds_read_b64 v[14:15], v5 offset:0
AW 144,40
AW 145,42
M 1,16,4
LC_ 106
AW 146,44
AW 147,46
M 1,17,5
RA 224,1,9216
LK 40,240,68,0
CF 48,48,49
LK 44,242,68,2
CF 50,50,51
GS 66
CF 248,248,249
CF 250,250,251
M 2,28,4
WL 6
LO 117,18,117
LU 18,117
M 2,29,5
)"
R"(HS 29,18
AW 148,48
M 10,40,4
WL 5
LL 17,82
LL 107,83
AW 149,50
AW 150,248
AW 151,250
M 10,41,5
LA 17
RA 228,1,10240
LK 48,244,69,0
CF 40,40,41
LK 248,246,69,2
CF 42,42,43
v_pk_add_f16 v26, v26, v29
GS 67
CF 44,44,45
CF 46,46,47
M 0,6,0
LC_ 107
HX 26
M 0,7,1
W1 26,2048
AW 152,40
M 1,18,0
WL 5
LO 16,16,106
LU 106,16
AW 153,42
AW 154,44
M 1,19,1
RA 232,1,11264
HZ 27,106
AW 155,46
LK 40,248,70,0
CF 48,48,49
LK 44,250,70,2
CF 50,50,51
v_add_f32 v14, v14, v15
GS 68
CF 248,248,249
CF 250,250,251
M 2,30,0
LL 18,84
LL 110,85
M 2,31,1
LA 18
PK 106,16,104,33
M 10,42,0
WL 4
LC_ 110
PK 104,16,104,32
AW 156,48
AW 157,50
AW 158,248
M 10,43,1
RV 252,22,5392
v_add_f32_dpp v14, v14, v14 quad_perm:[1,0,3,2] row_mask:0xf bank_mask:0xf
AW 159,250
LK 48,252,71,0
CF 40,40,41
LK 248,254,71,2
CF 42,42,43
v_add_f32_dpp v14, v14, v14 quad_perm:[2,3,0,1] row_mask:0xf bank_mask:0xf
GS 69
CF 44,44,45
CF 46,46,47
M 0,8,2
WL 5
LO 17,17,107
LU 107,17
M 0,9,3
s_add_u32 s42, s94, s60
RA 236,1,12288
HS 27,107
M 1,20,2
s_addc_u32 s43, s95, 0
LL 16,86
LL 111,87
AW 160,40
AW 161,42
M 1,21,3
LA 16
RV 36,8,6912
PK 107,17,105,33
AW 162,44
AW 163,46
M 2,32,2
LC_ 111
PK 105,17,105,32
M 2,33,3
RA 216,1,13312
v_add_f32_dpp v14, v14, v14 row_half_mirror row_mask:0xf bank_mask:0xf
GS 70
CF 48,48,49
CF 50,50,51
CF 248,248,249
CF 250,250,251
M 10,44,2
WL 4
LO 18,18,110
LU 110,18
AW 164,48
AW 165,50
AW 166,248
M 10,45,3
RA 220,1,14336
HS 27,110
AW 167,250
s_nop 1
GS 71
SU 104
v_cvt_f64_f32 v[14:15], v14
s_nop 5
v_cndmask_b32 v52, v52, v53, s[78:79]
M 0,10,4
LL 17,88
LL 114,89
v_cndmask_b32 v54, v54, v55, s[78:79]
M 0,11,5
LA 17
PK 110,18,108,33
v_cndmask_b32 v52, v52, v54, s[80:81]
M 1,22,4
LC_ 114
PK 108,18,108,32
v_mul_f32 v52, 0x3b2aaaab, v52
M 1,23,5
WL 5
RA 224,1,15360
PO 110
v_med3_f32 v53, v52, v39, v37
M 2,34,4
WL 5
LO 16,16,111
LU 111,16
s_mov_b64 exec, s[54:55]
SP
s_mov_b64 exec, -1
LG 52,53
s_nop 0
C 52,52,52
M 2,35,5
s_add_u32 s42, s94, s61
ds_read_b64 v[14:15], v5 offset:2048
HS 27,111
v_lshlrev_b32 v52, 16, v52
M 10,46,4
WL 4
LL 18,90
LL 115,91
v_add_f32 v52, v36, v52
M 10,47,5
s_addc_u32 s43, s95, 0
LA 18
PK 111,16,109,33
C 52,52,52
Z 6,0,0
LC_ 115
PK 109,16,109,32
v_lshlrev_b32 v52, 16, v52
M 6,1,1
RA 228,1,16384
PO 111
v_sqrt_f32 v52, v52
s_nop 0
C 52,52,52
Z 7,12,0
WL 5
LO 17,17,114
LU 114,17
v_lshlrev_b32 v52, 16, v52
M 7,13,1
RA 232,1,17408
HZ 29,114
v_rcp_f32 v52, v52
Z 8,24,0
LL 16,92
LL 118,93
v_mul_f32 v52, v38, v52
M 8,25,1
LA 16
PK 114,17,112,33
v_subrev_u32 v5, s39, v5
CF 52,52,52
Z 11,36,0
WL 2
LC_ 118
PK 112,17,112,32
ds_bpermute_b32 v13, v28, v52
M 11,37,1
RA 236,1,18432
v_add_f32 v14, v14, v15
M 6,2,2
LO 18,18,115
LU 115,18
M 6,3,3
SU 108
HS 29,115
v_add_f32_dpp v14, v14, v14 quad_perm:[1,0,3,2] row_mask:0xf bank_mask:0xf
M 7,14,2
s_mul_i32 s53, s50, 25600
LL 17,94
LL 119,95
s_add_u32 s40, %[wlo], s53
s_addc_u32 s41, %[whi], 0
s_mov_b32 m0, s38
M 7,15,3
LA 17
PK 115,18,113,33
v_add_f32_dpp v14, v14, v14 quad_perm:[2,3,0,1] row_mask:0xf bank_mask:0xf
M 8,26,2
LC_ 119
PK 113,18,113,32
M 8,27,3
RA 216,1,19456
DW 0
v_add_f32_dpp v14, v14, v14 row_half_mirror row_mask:0xf bank_mask:0xf
M 11,38,2
WL 3
LO 16,16,118
LU 118,16
M 11,39,3
s_add_u32 s42, s94, s62
RA 220,1,20480
HS 29,118
v_cvt_f64_f32 v[14:15], v14
s_addc_u32 s43, s95, 0
AR 248,120
M 6,4,4
s_add_u32 s37, s37, 32768
LL 18,56
LL 104,57
s_cmp_eq_u32 s37, s75
s_cselect_b32 s37, s74, s37
AR 249,121
M 6,5,5
LA 18
PK 118,16,116,33
s_mov_b64 exec, s[54:55]
SP
s_mov_b64 exec, -1
DW 1024
AR 250,122
M 7,16,4
LC_ 104
PK 116,16,116,32
AR 251,123
AR 52,124
M 7,17,5
RA 224,1,21504
PO 118
AR 53,125
M 8,28,4
WL 5
LO 17,17,119
LU 119,17
SU 112
AR 54,126
AR 55,127
M 8,29,5
DW 2048
HS 29,119
XS 248
M 11,40,4
WL 3
LL 16,58
LL 105,59
XS 249
M 11,41,5
s_add_u32 s40, s40, 3072
LA 16
PK 119,17,117,33
XS 250
XS 251
M 6,6,0
s_addc_u32 s41, s41, 0
LC_ 105
PK 117,17,117,32
s_add_u32 m0, s38, 3072
AW 120,248
M 6,7,1
s_add_u32 s42, s94, s63
RA 228,1,22528
PO 119
AW 121,249
AW 122,250
M 7,18,0
WL 4
LO 104,18,104
LU 18,104
AW 123,251
M 7,19,1
s_addc_u32 s43, s95, 0
RA 232,1,23552
HZ 26,18
XS 52
XS 53
M 8,30,0
DW 0
LL 17,60
LL 108,61
XS 54
M 8,31,1
s_lshl_b32 s53, s52, 8
LA 17
v_pk_add_f16 v27, v27, v29
XS 55
M 11,42,0
WL 2
LC_ 108
HX 27
AW 124,52
AW 125,53
M 11,43,1
s_add_u32 s48, %[alo], s53
RA 236,1,24576
W1 27,3072
AW 126,54
M 6,8,2
LO 105,16,105
LU 16,105
s_addc_u32 s49, %[ahi], 0
s_add_u32 s52, s52, %[d]
s_cmp_eq_u32 s52, %[wrap]
s_cselect_b32 s52, %[rst], s52
DW 1024
AW 127,55
v_min_u32 v35, v35, v13
M 6,9,3
SU 116
HS 26,16
AR 248,128
M 7,20,2
s_mul_i32 s53, s51, 24
LL 18,62
LL 109,63
s_bitcmp1_b32 s51, 0
s_addc_u32 s53, s53, 0
s_lshl_b32 s53, s53, 11
s_bitcmp1_b64 s[92:93], s51
s_cselect_b64 s[94:95], s[88:89], s[86:87]
s_add_u32 s94, s94, s53
s_addc_u32 s95, s95, 0
s_add_u32 s51, s51, %[d]
s_cmp_eq_u32 s51, %[wrap]
s_cselect_b32 s51, %[rst], s51
AR 249,129
AR 250,130
M 7,21,3
LA 18
DW 2048
AR 251,131
M 8,32,2
LC_ 109
DQ 3072
XS 248
M 8,33,3
s_add_u32 s50, s50, %[d]
v_add_u32 v1, s37, v0
v_add_u32 v4, s39, v4
XS 249
XS 250
M 11,44,2
WL 3
LO 108,17,108
LU 17,108
XS 251
M 11,45,3
s_cmp_eq_u32 s50, %[wrap]
RA 216,1,0
HS 26,17
AW 128,248
AW 129,249
M 6,10,4
s_cselect_b32 s50, %[rst], s50
LL 16,64
LL 112,65
AW 130,250
M 6,11,5
LA 16
v_and_b32 v22, 0xfffffcff, v1
AW 131,251
AR 52,132
M 7,22,4
LC_ 112
RA 220,1,1024
AR 53,133
M 7,23,5
RV 252,22,5120
v_add_u32 v19, 1, v19
AR 54,134
M 8,34,4
WL 5
LO 109,18,109
LU 18,109
AR 55,135
XS 52
)"
R"(M 8,35,5
s_add_u32 s38, s38, 32768
RA 224,1,2048
HS 26,18
XS 53
M 11,46,4
WL 1
LL 17,66
LL 113,67
XS 54
XS 55
M 11,47,5
s_cmp_eq_u32 s38, s77
LA 17
RA 228,1,3072
s_cselect_b32 s38, s76, s38
FP
s_sub_u32 s39, 0, s39
AW 132,52
Z 0,0,0
LC_ 113
AW 133,53
AW 134,54
M 0,1,1
AW 135,55
AR 248,136
Z 1,12,0
LO 112,16,112
LU 16,112
M 1,13,1
RA 232,1,4096
HZ 29,16
AR 249,137
AR 250,138
Z 2,24,0
LL 18,96
LL 116,97
AR 251,139
M 2,25,1
LA 18
XS 248
XS 249
Z 3,36,0
LC_ 116
XS 250
M 3,37,1
XS 251
AW 136,248
Z 4,48,0
WL 3
LO 113,17,113
LU 17,113
AW 137,249
M 4,49,1
RA 236,1,6144
HS 29,17
AW 138,250
Z 5,60,0
LL 16,98
LL 117,99
AW 139,251
AR 52,140
M 5,61,1
LA 16
AR 53,141
M 0,2,2
LC_ 117
AR 54,142
AR 55,143
M 0,3,3
XS 52
XS 53
M 1,14,2
WL 3
LO 116,18,116
LU 18,116
XS 54
M 1,15,3
RA 216,1,7168
HS 29,18
XS 55
M 2,26,2
LL 17,80
LL 106,81
AW 140,52
M 2,27,3
LA 17
FR
AW 141,53
AW 142,54
M 3,38,2
LC_ 106
AW 143,55
M 3,39,3
AR 248,144
AR 249,145
M 4,50,2
WL 4
LO 117,16,117
LU 16,117
AR 250,146
M 4,51,3
RA 220,1,8192
HS 29,16
AR 251,147
M 5,62,2
LL 18,82
LL 107,83
XS 248
XS 249
M 5,63,3
LA 18
v_pk_add_f16 v26, v26, v29
XS 250
M 0,4,4
WL 3
LC_ 107
v_cmp_gt_u32 vcc, v19, v15
XS 251
AW 144,248
M 0,5,5
HX 26
AW 145,249
M 1,16,4
WL 3
LO 17,17,106
LU 106,17
AW 146,250
AW 147,251
M 1,17,5
RA 224,1,9216
HZ 27,106
AR 52,148
M 2,28,4
s_cbranch_vccnz .Lk1s18
.Lk1r18:
LL 16,84
LL 110,85
AR 53,149
M 2,29,5
LA 16
PK 106,17,104,33
AR 54,150
AR 55,151
M 3,40,4
LC_ 110
PK 104,17,104,32
XS 52
M 3,41,5
W1 26,2048
XS 53
XS 54
M 4,52,4
WL 4
LO 18,18,107
LU 107,18
XS 55
M 4,53,5
s_add_u32 s42, s94, s60
RA 228,1,10240
HS 27,107
AW 148,52
AW 149,53
M 5,64,4
LL 17,86
LL 111,87
AW 150,54
M 5,65,5
s_addc_u32 s43, s95, 0
LA 17
PK 107,18,105,33
AW 151,55
M 0,6,0
LC_ 111
PK 105,18,105,32
AR 248,152
AR 249,153
M 0,7,1
SU 104
AR 250,154
M 1,18,0
WL 4
LO 16,16,110
LU 110,16
AR 251,155
XS 248
M 1,19,1
RA 232,1,11264
HS 27,110
XS 249
M 2,30,0
LL 18,88
LL 114,89
XS 250
XS 251
M 2,31,1
LA 18
PK 110,16,108,33
AW 152,248
M 3,42,0
LC_ 114
PK 108,16,108,32
AW 153,249
M 3,43,1
RV 252,22,5392
PO 110
AW 154,250
AW 155,251
M 4,54,0
WL 4
LO 17,17,111
LU 111,17
AR 52,156
M 4,55,1
s_add_u32 s42, s94, s61
RA 236,1,12288
HS 27,111
AR 53,157
AR 54,158
M 5,66,0
LL 16,90
LL 115,91
AR 55,159
M 5,67,1
s_addc_u32 s43, s95, 0
LA 16
PK 111,17,109,33
XS 52
XS 53
M 0,8,2
LC_ 115
PK 109,17,109,32
XS 54
M 0,9,3
PO 111
XS 55
M 1,20,2
WL 4
LO 18,18,114
LU 114,18
AW 156,52
AW 157,53
M 1,21,3
RA 216,1,13312
HZ 29,114
AW 158,54
M 2,32,2
LL 17,100
LL 118,101
AW 159,55
AR 248,160
M 2,33,3
LA 17
PK 114,18,112,33
AR 249,161
M 3,44,2
LC_ 118
PK 112,18,112,32
AR 250,162
AR 251,163
M 3,45,3
SU 108
XS 248
M 4,56,2
WL 3
LO 16,16,115
LU 115,16
XS 249
M 4,57,3
s_add_u32 s42, s94, s62
RA 220,1,14336
HS 29,115
XS 250
XS 251
M 5,68,2
LL 18,102
LL 119,103
AW 160,248
M 5,69,3
s_addc_u32 s43, s95, 0
LA 18
PK 115,16,113,33
AW 161,249
AW 162,250
M 0,10,4
LC_ 119
PK 113,16,113,32
AW 163,251
M 0,11,5
SU 112
AR 52,164
AR 53,165
M 1,22,4
WL 3
LO 17,17,118
LU 118,17
AR 54,166
M 1,23,5
RA 224,1,15360
HS 29,118
AR 55,167
M 2,34,4
LL 16,56
LL 104,57
XS 52
XS 53
M 2,35,5
LA 16
PK 118,17,116,33
XS 54
M 3,46,4
LC_ 104
PK 116,17,116,32
XS 55
AW 164,52
M 3,47,5
ds_read_b64 v[14:15], v5 offset:2048
PO 118
AW 165,53
M 4,58,4
WL 4
LO 18,18,119
LU 119,18
AW 166,54
AW 167,55
M 4,59,5
s_add_u32 s42, s94, s63
RA 228,1,16384
HS 29,119
M 5,70,4
LL 17,58
LL 105,59
M 5,71,5
s_addc_u32 s43, s95, 0
LA 17
PK 119,18,117,33
Z 6,0,0
LC_ 105
PK 117,18,117,32
M 6,1,1
PO 119
v_subrev_u32 v5, s39, v5
Z 7,12,0
WL 4
LO 104,16,104
LU 16,104
M 7,13,1
RA 232,1,17408
HZ 26,16
Z 8,24,0
LL 18,60
LL 108,61
M 8,25,1
WL 4
LA 18
SU 116
Z 9,36,0
s_mul_i32 s53, s50, 25600
LC_ 108
v_add_f32 v14, v14, v15
M 9,37,1
s_add_u32 s40, %[wlo], s53
v_add_f32_dpp v14, v14, v14 quad_perm:[1,0,3,2] row_mask:0xf bank_mask:0xf
v_pk_add_f16 v27, v27, v29
Z 10,48,0
WL 3
LO 105,17,105
LU 17,105
M 10,49,1
s_addc_u32 s41, %[whi], 0
RA 236,1,18432
HS 26,17
Z 11,60,0
LL 16,62
LL 109,63
M 11,61,1
s_mov_b32 m0, s38
LA 16
DW 0
M 6,2,2
LC_ 109
v_add_f32_dpp v14, v14, v14 quad_perm:[2,3,0,1] row_mask:0xf bank_mask:0xf
M 6,3,3
s_nop 0
v_add_f32_dpp v14, v14, v14 row_half_mirror row_mask:0xf bank_mask:0xf
HX 27
M 7,14,2
WL 3
LO 108,18,108
LU 18,108
M 7,15,3
s_add_u32 s37, s37, 32768
RA 216,1,19456
HS 26,18
W1 27,3072
M 8,26,2
s_cmp_eq_u32 s37, s75
LL 17,64
LL 112,65
M 8,27,3
)"
R"(s_cselect_b32 s37, s74, s37
LA 17
DW 1024
M 9,38,2
LC_ 112
v_cvt_f64_f32 v[14:15], v14
M 9,39,3
s_mov_b64 exec, s[54:55]
SP
s_mov_b64 exec, -1
v_add_u32 v4, s39, v4
M 10,50,2
WL 4
LO 109,16,109
LU 16,109
M 10,51,3
RA 220,1,20480
HS 26,16
M 11,62,2
LL 18,66
LL 113,67
M 11,63,3
LA 18
DW 2048
M 6,4,4
LC_ 113
M 6,5,5
M 7,16,4
WL 3
LO 112,17,112
LU 17,112
M 7,17,5
s_mul_i32 s53, s51, 24
RA 224,1,21504
HZ 29,17
M 8,28,4
s_bitcmp1_b32 s51, 0
LL 16,68
LL 116,69
M 8,29,5
s_addc_u32 s53, s53, 0
LA 16
M 9,40,4
s_lshl_b32 s53, s53, 11
LC_ 116
M 9,41,5
s_bitcmp1_b64 s[92:93], s51
M 10,52,4
WL 3
LO 113,18,113
LU 18,113
M 10,53,5
s_cselect_b64 s[94:95], s[88:89], s[86:87]
RA 228,1,22528
HS 29,18
M 11,64,4
LL 17,70
LL 117,71
M 11,65,5
s_add_u32 s94, s94, s53
LA 17
M 6,6,0
s_addc_u32 s95, s95, 0
LC_ 117
M 6,7,1
s_add_u32 s51, s51, %[d]
M 7,18,0
WL 3
LO 116,16,116
LU 16,116
M 7,19,1
s_cmp_eq_u32 s51, %[wrap]
RA 232,1,23552
HS 29,16
M 8,30,0
s_cselect_b32 s51, %[rst], s51
LL 18,72
LL 120,73
M 8,31,1
s_mul_i32 s53, %[d], 32768
LA 18
s_add_u32 s64, s53, %[sb1]
s_add_u32 s65, s64, 512
M 9,42,0
s_add_u32 s40, s40, 3072
LC_ 120
s_addc_u32 s41, s41, 0
s_add_u32 m0, s38, 3072
M 9,43,1
DW 0
M 10,54,0
WL 3
LO 117,17,117
LU 17,117
M 10,55,1
RA 236,1,24576
HS 29,17
M 11,66,0
LL 16,74
LL 121,75
M 11,67,1
LA 16
DW 1024
M 6,8,2
LC_ 121
DW 2048
M 6,9,3
s_lshl_b32 s53, s52, 8
v_add_u32 v1, s37, v0
M 7,20,2
WL 3
LO 120,18,120
LU 18,120
M 7,21,3
s_add_u32 s48, %[alo], s53
RA 216,1,0
HZ 30,18
M 8,32,2
s_addc_u32 s49, %[ahi], 0
LL 17,76
LL 124,77
M 8,33,3
s_add_u32 s52, s52, %[d]
LA 17
v_and_b32 v22, 0xfffffcff, v1
M 9,44,2
s_cmp_eq_u32 s52, %[wrap]
LC_ 124
M 9,45,3
s_cselect_b32 s52, %[rst], s52
DQ 3072
M 10,56,2
WL 3
LO 121,16,121
LU 16,121
M 10,57,3
s_add_u32 s50, s50, %[d]
RA 220,1,1024
HS 30,16
M 11,68,2
LL 18,78
LL 125,79
M 11,69,3
s_cmp_eq_u32 s50, %[wrap]
LA 18
RV 252,22,5120
M 6,10,4
s_cselect_b32 s50, %[rst], s50
LC_ 125
M 6,11,5
WV 25
v_add_u32 v19, 1, v19
M 7,22,4
WL 4
LO 124,17,124
LU 17,124
M 7,23,5
s_add_u32 s38, s38, 32768
RA 224,1,2048
HS 30,17
M 8,34,4
s_cmp_eq_u32 s38, s77
LL 16,80
LL 106,81
M 8,35,5
s_cselect_b32 s38, s76, s38
LA 16
FP
M 9,46,4
s_sub_u32 s39, 0, s39
LC_ 106
M 9,47,5
M 10,58,4
WL 4
LO 125,18,125
LU 18,125
M 10,59,5
RA 228,1,3072
HS 30,18
M 11,70,4
LL 17,82
LL 107,83
M 11,71,5
LA 17
v_pk_add_f16 v26, v26, v30
Z 0,0,0
LC_ 107
v_pk_add_f16 v26, v26, v29
M 0,1,1
HX 26
Z 1,12,0
WL 3
LO 16,16,106
LU 106,16
M 1,13,1
RA 232,1,4096
HZ 27,106
Z 2,24,0
LL 18,84
LL 110,85
M 2,25,1
LA 18
PK 106,16,104,33
Z 3,36,0
LC_ 110
PK 104,16,104,32
M 3,37,1
Z 4,48,0
WL 3
LO 17,17,107
LU 107,17
M 4,49,1
s_add_u32 s42, s94, s60
RA 236,1,6144
HS 27,107
Z 5,60,0
LL 16,86
LL 111,87
M 5,61,1
s_addc_u32 s43, s95, 0
LA 16
PK 107,17,105,33
M 0,2,2
LC_ 111
PK 105,17,105,32
M 0,3,3
SU 104
M 1,14,2
WL 3
LO 18,18,110
LU 110,18
M 1,15,3
RA 216,1,7168
HS 27,110
M 2,26,2
LL 17,88
LL 114,89
M 2,27,3
LA 17
PK 110,18,108,33
M 3,38,2
LC_ 114
PK 108,18,108,32
M 3,39,3
FR
PO 110
M 4,50,2
WL 4
LO 16,16,111
LU 111,16
M 4,51,3
s_add_u32 s42, s94, s61
RA 220,1,8192
HS 27,111
M 5,62,2
LL 18,90
LL 115,91
M 5,63,3
s_addc_u32 s43, s95, 0
LA 18
PK 111,16,109,33
M 0,4,4
PFN
LC_ 115
PK 109,16,109,32
M 0,5,5
s_mov_b32 m0, %[pm]
s_nop 0
DF
PO 111
M 1,16,4
WL 4
LO 17,17,114
LU 114,17
M 1,17,5
RA 224,1,9216
HZ 29,114
M 2,28,4
LL 16,92
LL 118,93
M 2,29,5
LA 16
PK 114,17,112,33
M 3,40,4
LC_ 118
PK 112,17,112,32
M 3,41,5
WL 6
SU 108
v_cmp_gt_u32 vcc, v19, v15
M 4,52,4
WL 3
LO 18,18,115
LU 115,18
M 4,53,5
s_add_u32 s42, s94, s62
RA 228,1,10240
HS 29,115
M 5,64,4
LL 17,94
LL 119,95
M 5,65,5
s_cbranch_vccnz .Lk1s19
.Lk1r19:
LA 17
PK 115,18,113,33
M 0,6,0
s_addc_u32 s43, s95, 0
LC_ 119
PK 113,18,113,32
M 0,7,1
W1 26,2048
M 1,18,0
WL 4
LO 16,16,118
LU 118,16
M 1,19,1
RA 232,1,11264
HS 29,118
M 2,30,0
LL 18,96
LL 122,97
M 2,31,1
LA 18
PK 118,16,116,33
M 3,42,0
LC_ 122
PK 116,16,116,32
M 3,43,1
RV 252,22,5392
PO 118
M 4,54,0
WL 5
LO 17,17,119
LU 119,17
M 4,55,1
RA 236,1,12288
HS 29,119
M 5,66,0
LL 16,98
LL 123,99
M 5,67,1
LA 16
PK 119,17,117,33
M 0,8,2
LC_ 123
PK 117,17,117,32
M 0,9,3
SU 112
PO 119
M 1,20,2
WL 4
LO 18,18,122
LU 122,18
M 1,21,3
s_add_u32 s42, s94, s63
RA 216,1,13312
HZ 30,122
M 2,32,2
s_addc_u32 s43, s95, 0
LL 17,100
LL 126,101
M 2,33,3
LA 17
PK 122,18,120,33
M 3,44,2
LC_ 126
PK 120,18,120,32
M 3,45,3
SU 116
M 4,56,2
WL 3
LO 16,16,123
LU 123,16
M 4,57,3
s_add_u32 s42, s94, s64
RA 220,1,14336
HS 30,123
M 5,68,2
LL 18,102
LL 127,103
M 5,69,3
s_addc_u32 s43, s95, 0
LA 18
PK 123,16,121,33
M 0,10,4
LC_ 127
PK 121,16,121,32
M 0,11,5
SU 120
M 1,22,4
WL 3
LO 17,17,126
LU 126,17
M 1,23,5
RA 224,1,15360
HS 30,126
M 2,34,4
LL 16,56
LL 104,57
)"
R"(M 2,35,5
LA 16
PK 126,17,124,33
M 3,46,4
LC_ 104
PK 124,17,124,32
M 3,47,5
ds_read_b64 v[14:15], v5 offset:2048
PO 126
M 4,58,4
WL 4
LO 18,18,127
LU 127,18
M 4,59,5
s_add_u32 s42, s94, s65
RA 228,1,16384
HS 30,127
M 5,70,4
LL 17,58
LL 105,59
M 5,71,5
s_addc_u32 s43, s95, 0
LA 17
PK 127,18,125,33
Z 6,0,0
LC_ 105
PK 125,18,125,32
M 6,1,1
PO 127
v_subrev_u32 v5, s39, v5
Z 7,12,0
WL 4
LO 104,16,104
LU 16,104
M 7,13,1
RA 232,1,17408
HZ 26,16
Z 8,24,0
WL 4
LL 18,60
LL 108,61
M 8,25,1
s_mul_i32 s53, s50, 25600
LA 18
v_add_f32 v14, v14, v15
Z 9,36,0
s_add_u32 s40, %[wlo], s53
LC_ 108
v_add_f32_dpp v14, v14, v14 quad_perm:[1,0,3,2] row_mask:0xf bank_mask:0xf
M 9,37,1
s_addc_u32 s41, %[whi], 0
v_add_f32_dpp v14, v14, v14 quad_perm:[2,3,0,1] row_mask:0xf bank_mask:0xf
v_pk_add_f16 v27, v27, v30
Z 10,48,0
WL 3
LO 105,17,105
LU 17,105
M 10,49,1
s_mov_b32 m0, s38
RA 236,1,18432
HS 26,17
Z 11,60,0
LL 16,62
LL 109,63
M 11,61,1
LA 16
SU 124
M 6,2,2
LC_ 109
DW 0
M 6,3,3
v_add_f32_dpp v14, v14, v14 row_half_mirror row_mask:0xf bank_mask:0xf
v_pk_add_f16 v27, v27, v29
M 7,14,2
WL 3
LO 108,18,108
LU 18,108
M 7,15,3
s_add_u32 s37, s37, 32768
RA 216,1,19456
HS 26,18
HX 27
M 8,26,2
s_cmp_eq_u32 s37, s75
LL 17,64
LL 112,65
W1 27,3072
M 8,27,3
s_cselect_b32 s37, s74, s37
LA 17
DW 1024
M 9,38,2
LC_ 112
v_cvt_f64_f32 v[14:15], v14
M 9,39,3
s_mov_b64 exec, s[54:55]
SP
s_mov_b64 exec, -1
v_add_u32 v4, s39, v4
s_mov_b64 exec, 1
AT
s_mov_b64 exec, -1
M 10,50,2
WL 4
LO 109,16,109
LU 16,109
M 10,51,3
RA 220,1,20480
HS 26,16
M 11,62,2
LL 18,66
LL 113,67
M 11,63,3
LA 18
DW 2048
M 6,4,4
LC_ 113
M 6,5,5
M 7,16,4
WL 3
LO 112,17,112
LU 17,112
M 7,17,5
s_mul_i32 s53, s51, 24
RA 224,1,21504
HZ 29,17
M 8,28,4
s_bitcmp1_b32 s51, 0
LL 16,68
LL 116,69
M 8,29,5
s_addc_u32 s53, s53, 0
LA 16
M 9,40,4
s_lshl_b32 s53, s53, 11
LC_ 116
M 9,41,5
s_bitcmp1_b64 s[92:93], s51
M 10,52,4
WL 3
LO 113,18,113
LU 18,113
M 10,53,5
s_cselect_b64 s[94:95], s[88:89], s[86:87]
RA 228,1,22528
HS 29,18
M 11,64,4
LL 17,70
LL 117,71
M 11,65,5
s_add_u32 s94, s94, s53
LA 17
M 6,6,0
s_addc_u32 s95, s95, 0
LC_ 117
M 6,7,1
s_add_u32 s51, s51, %[d]
M 7,18,0
WL 3
LO 116,16,116
LU 16,116
M 7,19,1
s_cmp_eq_u32 s51, %[wrap]
RA 232,1,23552
HS 29,16
M 8,30,0
s_cselect_b32 s51, %[rst], s51
LL 18,72
LL 120,73
M 8,31,1
s_add_u32 s40, s40, 3072
LA 18
M 9,42,0
s_addc_u32 s41, s41, 0
LC_ 120
s_add_u32 m0, s38, 3072
M 9,43,1
DW 0
M 10,54,0
WL 3
LO 117,17,117
LU 17,117
M 10,55,1
RA 236,1,24576
HS 29,17
M 11,66,0
LL 16,74
LL 121,75
M 11,67,1
LA 16
DW 1024
M 6,8,2
LC_ 121
DW 2048
M 6,9,3
s_lshl_b32 s53, s52, 8
v_add_u32 v1, s37, v0
M 7,20,2
WL 3
LO 120,18,120
LU 18,120
M 7,21,3
s_add_u32 s48, %[alo], s53
RA 216,1,0
HZ 30,18
M 8,32,2
s_addc_u32 s49, %[ahi], 0
LL 17,76
LL 124,77
M 8,33,3
s_add_u32 s52, s52, %[d]
LA 17
v_and_b32 v22, 0xfffffcff, v1
M 9,44,2
s_cmp_eq_u32 s52, %[wrap]
LC_ 124
M 9,45,3
s_cselect_b32 s52, %[rst], s52
DQ 3072
M 10,56,2
WL 3
LO 121,16,121
LU 16,121
M 10,57,3
s_add_u32 s50, s50, %[d]
RA 220,1,1024
HS 30,16
M 11,68,2
LL 18,78
LL 125,79
M 11,69,3
s_cmp_eq_u32 s50, %[wrap]
LA 18
RV 252,22,5120
M 6,10,4
s_cselect_b32 s50, %[rst], s50
LC_ 125
M 6,11,5
WV 28
M 7,22,4
WL 4
LO 124,17,124
LU 17,124
M 7,23,5
s_add_u32 s38, s38, 32768
RA 224,1,2048
HS 30,17
M 8,34,4
s_cmp_eq_u32 s38, s77
LL 16,80
LL 106,81
M 8,35,5
s_cselect_b32 s38, s76, s38
LA 16
M 9,46,4
s_barrier
LC_ 106
M 9,47,5
s_sub_u32 s39, 0, s39
M 10,58,4
WL 3
LO 125,18,125
LU 18,125
M 10,59,5
RA 228,1,3072
HS 30,18
M 11,70,4
LL 17,82
LL 107,83
M 11,71,5
LA 17
v_pk_add_f16 v26, v26, v30
Z 0,0,0
LC_ 107
v_pk_add_f16 v26, v26, v29
M 0,1,1
HX 26
Z 1,12,0
WL 3
LO 16,16,106
LU 106,16
M 1,13,1
RA 232,1,4096
HZ 27,106
Z 2,24,0
LL 18,84
LL 110,85
M 2,25,1
LA 18
PK 106,16,104,33
Z 3,36,0
LC_ 110
PK 104,16,104,32
M 3,37,1
W1 26,2048
Z 4,48,0
WL 4
LO 17,17,107
LU 107,17
M 4,49,1
s_add_u32 s42, s94, s60
RA 236,1,6144
HS 27,107
Z 5,60,0
LL 16,86
LL 111,87
M 5,61,1
s_addc_u32 s43, s95, 0
LA 16
PK 107,17,105,33
M 0,2,2
LC_ 111
PK 105,17,105,32
M 0,3,3
SU 104
M 1,14,2
WL 4
LO 18,18,110
LU 110,18
M 1,15,3
RA 216,1,7168
HS 27,110
M 2,26,2
LL 17,88
LL 114,89
M 2,27,3
LA 17
PK 110,18,108,33
M 3,38,2
LC_ 114
PK 108,18,108,32
M 3,39,3
PO 110
M 4,50,2
WL 3
LO 16,16,111
LU 111,16
M 4,51,3
s_add_u32 s42, s94, s61
RA 220,1,8192
HS 27,111
M 5,62,2
LL 18,90
LL 115,91
M 5,63,3
s_addc_u32 s43, s95, 0
LA 18
PK 111,16,109,33
M 0,4,4
PFW
LC_ 115
PK 109,16,109,32
M 0,5,5
s_mov_b32 m0, %[pm]
s_nop 0
DF
PO 111
M 1,16,4
WL 3
LO 17,17,114
LU 114,17
M 1,17,5
RA 224,1,9216
HZ 29,114
M 2,28,4
LL 16,92
LL 118,93
M 2,29,5
LA 16
PK 114,17,112,33
M 3,40,4
LC_ 118
PK 112,17,112,32
M 3,41,5
SU 108
M 4,52,4
WL 3
LO 18,18,115
LU 115,18
M 4,53,5
s_add_u32 s42, s94, s62
RA 228,1,10240
HS 29,115
M 5,64,4
LL 17,94
LL 119,95
M 5,65,5
s_addc_u32 s43, s95, 0
LA 17
PK 115,18,113,33
M 0,6,0
LC_ 119
PK 113,18,113,32
M 0,7,1
SU 112
M 1,18,0
WL 3
LO 16,16,118
LU 118,16
M 1,19,1
)"
R"(RA 232,1,11264
HS 29,118
M 2,30,0
LL 18,96
LL 122,97
M 2,31,1
LA 18
PK 118,16,116,33
M 3,42,0
LC_ 122
PK 116,16,116,32
M 3,43,1
RV 252,22,5392
PO 118
M 4,54,0
WL 4
LO 17,17,119
LU 119,17
M 4,55,1
s_add_u32 s42, s94, s63
RA 236,1,12288
HS 29,119
M 5,66,0
LL 16,98
LL 123,99
M 5,67,1
s_addc_u32 s43, s95, 0
LA 16
PK 119,17,117,33
M 0,8,2
LC_ 123
PK 117,17,117,32
M 0,9,3
PO 119
M 1,20,2
WL 4
LO 18,18,122
LU 122,18
M 1,21,3
RA 216,1,13312
HZ 30,122
M 2,32,2
LL 17,100
LL 126,101
M 2,33,3
LA 17
PK 122,18,120,33
M 3,44,2
LC_ 126
PK 120,18,120,32
M 3,45,3
SU 116
M 4,56,2
WL 3
LO 16,16,123
LU 123,16
M 4,57,3
s_add_u32 s42, s94, s64
RA 220,1,14336
HS 30,123
M 5,68,2
LL 18,102
LL 127,103
M 5,69,3
s_addc_u32 s43, s95, 0
LA 18
PK 123,16,121,33
M 0,10,4
LC_ 127
PK 121,16,121,32
M 0,11,5
SU 120
M 1,22,4
WL 3
LO 17,17,126
LU 126,17
M 1,23,5
RA 224,1,15360
HS 30,126
M 2,34,4
LL 16,56
LL 104,57
M 2,35,5
LA 16
PK 126,17,124,33
M 3,46,4
LC_ 104
PK 124,17,124,32
M 3,47,5
ds_read_b64 v[14:15], v5 offset:2048
PO 126
M 4,58,4
WL 4
LO 18,18,127
LU 127,18
M 4,59,5
s_add_u32 s42, s94, s65
RA 228,1,16384
HS 30,127
M 5,70,4
LL 17,58
LL 105,59
M 5,71,5
s_addc_u32 s43, s95, 0
LA 17
PK 127,18,125,33
Z 6,0,0
LC_ 105
PK 125,18,125,32
M 6,1,1
PO 127
v_subrev_u32 v5, s39, v5
Z 7,12,0
WL 4
LO 104,16,104
LU 16,104
M 7,13,1
RA 232,1,17408
HZ 26,16
Z 8,24,0
WL 4
LL 18,60
LL 108,61
M 8,25,1
s_mul_i32 s53, s50, 25600
LA 18
v_add_f32 v14, v14, v15
Z 9,36,0
s_add_u32 s40, %[wlo], s53
LC_ 108
v_add_f32_dpp v14, v14, v14 quad_perm:[1,0,3,2] row_mask:0xf bank_mask:0xf
M 9,37,1
s_addc_u32 s41, %[whi], 0
Z 10,48,0
WL 3
LO 105,17,105
LU 17,105
M 10,49,1
s_mov_b32 m0, s38
RA 236,1,18432
HS 26,17
Z 11,60,0
LL 16,62
LL 109,63
M 11,61,1
LA 16
SU 124
M 6,2,2
LC_ 109
DW 0
M 6,3,3
WV 13
v_add_f32_dpp v14, v14, v14 quad_perm:[2,3,0,1] row_mask:0xf bank_mask:0xf
v_pk_add_f16 v27, v27, v30
M 7,14,2
WL 3
LO 108,18,108
LU 18,108
v_pk_add_f16 v27, v27, v29
M 7,15,3
s_add_u32 s37, s37, 32768
RA 216,1,19456
HS 26,18
HX 27
M 8,26,2
s_cmp_eq_u32 s37, s75
LL 17,64
LL 112,65
W1 27,3072
M 8,27,3
s_cselect_b32 s37, s74, s37
LA 17
DW 1024
v_readfirstlane_b32 s72, v11
M 9,38,2
LC_ 112
v_add_f32_dpp v14, v14, v14 row_half_mirror row_mask:0xf bank_mask:0xf
v_mov_b32 v12, s72
M 9,39,3
W2 12,12,16
v_cvt_f64_f32 v[14:15], v14
M 10,50,2
WL 5
LO 109,16,109
LU 16,109
M 10,51,3
RA 220,1,20480
HS 26,16
M 11,62,2
LL 18,66
LL 113,67
M 11,63,3
LA 18
s_mov_b64 exec, s[54:55]
SP
s_mov_b64 exec, -1
M 6,4,4
LC_ 113
DW 2048
M 6,5,5
v_add_u32 v4, s39, v4
s_mov_b64 exec, 1
AT
s_mov_b64 exec, -1
M 7,16,4
WL 4
LO 112,17,112
LU 17,112
M 7,17,5
s_mul_i32 s53, s51, 24
RA 224,1,21504
HZ 29,17
M 8,28,4
s_bitcmp1_b32 s51, 0
LL 16,68
LL 116,69
M 8,29,5
s_addc_u32 s53, s53, 0
LA 16
M 9,40,4
s_lshl_b32 s53, s53, 11
LC_ 116
M 9,41,5
s_bitcmp1_b64 s[92:93], s51
M 10,52,4
WL 3
LO 113,18,113
LU 18,113
M 10,53,5
s_cselect_b64 s[94:95], s[88:89], s[86:87]
RA 228,1,22528
HS 29,18
M 11,64,4
LL 17,70
LL 117,71
M 11,65,5
s_add_u32 s94, s94, s53
LA 17
M 6,6,0
s_addc_u32 s95, s95, 0
LC_ 117
M 6,7,1
s_add_u32 s51, s51, %[d]
M 7,18,0
WL 3
LO 116,16,116
LU 16,116
M 7,19,1
s_cmp_eq_u32 s51, %[wrap]
RA 232,1,23552
HS 29,16
M 8,30,0
s_cselect_b32 s51, %[rst], s51
LL 18,72
LL 120,73
M 8,31,1
s_add_u32 s40, s40, 3072
LA 18
M 9,42,0
s_addc_u32 s41, s41, 0
LC_ 120
s_add_u32 m0, s38, 3072
M 9,43,1
DW 0
M 10,54,0
WL 3
LO 117,17,117
LU 17,117
M 10,55,1
RA 236,1,24576
HS 29,17
M 11,66,0
LL 16,74
LL 121,75
M 11,67,1
LA 16
DW 1024
M 6,8,2
LC_ 121
DW 2048
M 6,9,3
s_lshl_b32 s53, s52, 8
v_add_u32 v1, s37, v0
M 7,20,2
WL 3
LO 120,18,120
LU 18,120
M 7,21,3
s_add_u32 s48, %[alo], s53
RA 216,1,0
HZ 30,18
M 8,32,2
s_addc_u32 s49, %[ahi], 0
LL 17,76
LL 124,77
M 8,33,3
s_add_u32 s52, s52, %[d]
LA 17
v_and_b32 v22, 0xfffffcff, v1
M 9,44,2
s_cmp_eq_u32 s52, %[wrap]
LC_ 124
M 9,45,3
s_cselect_b32 s52, %[rst], s52
DQ 3072
M 10,56,2
WL 3
LO 121,16,121
LU 16,121
M 10,57,3
s_add_u32 s50, s50, %[d]
RA 220,1,1024
HS 30,16
M 11,68,2
LL 18,78
LL 125,79
M 11,69,3
s_cmp_eq_u32 s50, %[wrap]
LA 18
RV 252,22,5120
M 6,10,4
s_cselect_b32 s50, %[rst], s50
LC_ 125
M 6,11,5
M 7,22,4
WL 4
LO 124,17,124
LU 17,124
M 7,23,5
s_add_u32 s38, s38, 32768
RA 224,1,2048
HS 30,17
M 8,34,4
s_cmp_eq_u32 s38, s77
LL 16,80
LL 106,81
M 8,35,5
s_cselect_b32 s38, s76, s38
LA 16
M 9,46,4
s_barrier
LC_ 106
M 9,47,5
s_sub_u32 s39, 0, s39
M 10,58,4
WL 3
LO 125,18,125
LU 18,125
M 10,59,5
RA 228,1,3072
HS 30,18
M 11,70,4
LL 17,82
LL 107,83
M 11,71,5
2:
LA 17
v_pk_add_f16 v26, v26, v30
Z 0,0,0
LC_ 107
ds_read_b32 v20, v5 offset:4096
M 0,1,1
v_pk_add_f16 v26, v26, v29
Z 1,12,0
WL 4
LO 16,16,106
LU 106,16
M 1,13,1
RA 232,1,4096
HZ 27,106
Z 2,24,0
LL 18,84
LL 110,85
M 2,25,1
LA 18
PK 106,16,104,33
Z 3,36,0
LC_ 110
PK 104,16,104,32
M 3,37,1
HX 26
Z 4,48,0
WL 4
LO 17,17,107
LU 107,17
M 4,49,1
s_add_u32 s42, s94, s60
RA 236,1,6144
HS 27,107
Z 5,60,0
LL 16,86
LL 111,87
M 5,61,1
s_addc_u32 s43, s95, 0
LA 16
PK 107,17,105,33
M 0,2,2
LC_ 111
PK 105,17,105,32
M 0,3,3
)"
R"(SU 104
M 1,14,2
WL 3
LO 18,18,110
LU 110,18
M 1,15,3
RA 216,1,7168
HS 27,110
M 2,26,2
LL 17,88
LL 114,89
W1 26,2048
M 2,27,3
LA 17
PK 110,18,108,33
M 3,38,2
LC_ 114
PK 108,18,108,32
M 3,39,3
PO 110
v_readfirstlane_b32 s59, v20
M 4,50,2
WL 4
LO 16,16,111
LU 111,16
M 4,51,3
s_add_u32 s42, s94, s61
RA 220,1,8192
HS 27,111
M 5,62,2
LL 18,90
LL 115,91
M 5,63,3
s_addc_u32 s43, s95, 0
LA 18
PK 111,16,109,33
M 0,4,4
LC_ 115
PK 109,16,109,32
M 0,5,5
PO 111
M 1,16,4
WL 3
LO 17,17,114
LU 114,17
M 1,17,5
RA 224,1,9216
HZ 29,114
M 2,28,4
LL 16,92
LL 118,93
M 2,29,5
LA 16
PK 114,17,112,33
M 3,40,4
LC_ 118
PK 112,17,112,32
M 3,41,5
SU 108
M 4,52,4
WL 3
LO 18,18,115
LU 115,18
M 4,53,5
s_add_u32 s42, s94, s62
RA 228,1,10240
HS 29,115
M 5,64,4
LL 17,94
LL 119,95
M 5,65,5
s_addc_u32 s43, s95, 0
LA 17
PK 115,18,113,33
M 0,6,0
LC_ 119
PK 113,18,113,32
M 0,7,1
SU 112
M 1,18,0
WL 3
LO 16,16,118
LU 118,16
M 1,19,1
RA 232,1,11264
HS 29,118
M 2,30,0
LL 18,96
LL 122,97
M 2,31,1
LA 18
PK 118,16,116,33
M 3,42,0
LC_ 122
PK 116,16,116,32
M 3,43,1
RV 252,22,5392
PO 118
M 4,54,0
WL 4
LO 17,17,119
LU 119,17
M 4,55,1
s_add_u32 s42, s94, s63
RA 236,1,12288
HS 29,119
M 5,66,0
LL 16,98
LL 123,99
M 5,67,1
s_addc_u32 s43, s95, 0
LA 16
PK 119,17,117,33
M 0,8,2
LC_ 123
PK 117,17,117,32
M 0,9,3
PO 119
M 1,20,2
WL 4
LO 18,18,122
LU 122,18
M 1,21,3
RA 216,1,13312
HZ 30,122
M 2,32,2
LL 17,100
LL 126,101
M 2,33,3
LA 17
PK 122,18,120,33
M 3,44,2
LC_ 126
PK 120,18,120,32
M 3,45,3
SU 116
M 4,56,2
WL 3
LO 16,16,123
LU 123,16
M 4,57,3
s_add_u32 s42, s94, s64
RA 220,1,14336
HS 30,123
M 5,68,2
LL 18,102
LL 127,103
M 5,69,3
s_addc_u32 s43, s95, 0
LA 18
PK 123,16,121,33
M 0,10,4
LC_ 127
PK 121,16,121,32
M 0,11,5
SU 120
M 1,22,4
WL 3
LO 17,17,126
LU 126,17
M 1,23,5
RA 224,1,15360
HS 30,126
M 2,34,4
LL 16,56
LL 104,57
M 2,35,5
LA 16
PK 126,17,124,33
M 3,46,4
LC_ 104
PK 124,17,124,32
M 3,47,5
ds_read_b64 v[14:15], v5 offset:2048
PO 126
M 4,58,4
WL 4
LO 18,18,127
LU 127,18
M 4,59,5
s_add_u32 s42, s94, s65
RA 228,1,16384
HS 30,127
M 5,70,4
LL 17,58
LL 105,59
M 5,71,5
s_addc_u32 s43, s95, 0
LA 17
PK 127,18,125,33
Z 6,0,0
LC_ 105
PK 125,18,125,32
M 6,1,1
PO 127
v_subrev_u32 v5, s39, v5
Z 7,12,0
WL 4
LO 104,16,104
LU 16,104
M 7,13,1
RA 232,1,17408
HZ 26,16
Z 8,24,0
WL 4
LL 18,60
LL 108,61
M 8,25,1
s_mul_i32 s53, s50, 25600
LA 18
v_add_f32 v14, v14, v15
Z 9,36,0
s_add_u32 s40, %[wlo], s53
LC_ 108
v_add_f32_dpp v14, v14, v14 quad_perm:[1,0,3,2] row_mask:0xf bank_mask:0xf
M 9,37,1
s_addc_u32 s41, %[whi], 0
Z 10,48,0
WL 3
LO 105,17,105
LU 17,105
M 10,49,1
s_mov_b32 m0, s38
RA 236,1,18432
HS 26,17
Z 11,60,0
LL 16,62
LL 109,63
M 11,61,1
LA 16
SU 124
M 6,2,2
LC_ 109
DW 0
M 6,3,3
WV 11
v_add_f32_dpp v14, v14, v14 quad_perm:[2,3,0,1] row_mask:0xf bank_mask:0xf
v_pk_add_f16 v27, v27, v30
M 7,14,2
WL 3
LO 108,18,108
LU 18,108
v_pk_add_f16 v27, v27, v29
M 7,15,3
s_add_u32 s37, s37, 32768
RA 216,1,19456
HS 26,18
HX 27
M 8,26,2
s_cmp_eq_u32 s37, s75
LL 17,64
LL 112,65
W1 27,3072
M 8,27,3
s_cselect_b32 s37, s74, s37
LA 17
DW 1024
v_readfirstlane_b32 s72, v11
M 9,38,2
LC_ 112
v_add_f32_dpp v14, v14, v14 row_half_mirror row_mask:0xf bank_mask:0xf
v_mov_b32 v12, s72
M 9,39,3
s_max_u32 s59, s59, %[nb]
W2 12,12,16
v_cvt_f64_f32 v[14:15], v14
M 10,50,2
WL 5
LO 109,16,109
LU 16,109
M 10,51,3
s_add_u32 %[nb], %[nb], 1
RA 220,1,20480
HS 26,16
M 11,62,2
LL 18,66
LL 113,67
M 11,63,3
LA 18
s_mov_b64 exec, s[54:55]
SP
s_mov_b64 exec, -1
M 6,4,4
LC_ 113
DW 2048
M 6,5,5
v_add_u32 v4, s39, v4
s_mov_b64 exec, 1
AT
s_mov_b64 exec, -1
M 7,16,4
WL 4
LO 112,17,112
LU 17,112
M 7,17,5
s_mul_i32 s53, s51, 24
RA 224,1,21504
HZ 29,17
M 8,28,4
s_bitcmp1_b32 s51, 0
LL 16,68
LL 116,69
M 8,29,5
s_addc_u32 s53, s53, 0
LA 16
M 9,40,4
s_lshl_b32 s53, s53, 11
LC_ 116
M 9,41,5
s_bitcmp1_b64 s[92:93], s51
M 10,52,4
WL 3
LO 113,18,113
LU 18,113
M 10,53,5
s_cselect_b64 s[94:95], s[88:89], s[86:87]
RA 228,1,22528
HS 29,18
M 11,64,4
LL 17,70
LL 117,71
M 11,65,5
s_add_u32 s94, s94, s53
LA 17
M 6,6,0
s_addc_u32 s95, s95, 0
LC_ 117
M 6,7,1
s_add_u32 s51, s51, %[d]
M 7,18,0
WL 3
LO 116,16,116
LU 16,116
M 7,19,1
s_cmp_eq_u32 s51, %[wrap]
RA 232,1,23552
HS 29,16
M 8,30,0
s_cselect_b32 s51, %[rst], s51
LL 18,72
LL 120,73
M 8,31,1
s_add_u32 s40, s40, 3072
LA 18
M 9,42,0
s_addc_u32 s41, s41, 0
LC_ 120
s_add_u32 m0, s38, 3072
M 9,43,1
DW 0
M 10,54,0
WL 3
LO 117,17,117
LU 17,117
M 10,55,1
RA 236,1,24576
HS 29,17
M 11,66,0
LL 16,74
LL 121,75
M 11,67,1
LA 16
DW 1024
M 6,8,2
LC_ 121
DW 2048
M 6,9,3
s_lshl_b32 s53, s52, 8
v_add_u32 v1, s37, v0
M 7,20,2
WL 3
LO 120,18,120
LU 18,120
M 7,21,3
s_add_u32 s48, %[alo], s53
RA 216,1,0
HZ 30,18
M 8,32,2
s_addc_u32 s49, %[ahi], 0
LL 17,76
LL 124,77
M 8,33,3
s_add_u32 s52, s52, %[d]
LA 17
v_and_b32 v22, 0xfffffcff, v1
M 9,44,2
s_cmp_eq_u32 s52, %[wrap]
LC_ 124
M 9,45,3
s_cselect_b32 s52, %[rst], s52
DQ 3072
M 10,56,2
WL 3
LO 121,16,121
LU 16,121
M 10,57,3
s_add_u32 s50, s50, %[d]
RA 220,1,1024
HS 30,16
M 11,68,2
)"
R"(LL 18,78
LL 125,79
M 11,69,3
s_cmp_eq_u32 s50, %[wrap]
LA 18
RV 252,22,5120
M 6,10,4
s_cselect_b32 s50, %[rst], s50
LC_ 125
M 6,11,5
M 7,22,4
WL 4
LO 124,17,124
LU 17,124
M 7,23,5
s_add_u32 s38, s38, 32768
RA 224,1,2048
HS 30,17
M 8,34,4
s_cmp_eq_u32 s38, s77
LL 16,80
LL 106,81
M 8,35,5
s_cselect_b32 s38, s76, s38
LA 16
M 9,46,4
s_barrier
LC_ 106
M 9,47,5
s_sub_u32 s39, 0, s39
M 10,58,4
WL 3
LO 125,18,125
LU 18,125
M 10,59,5
RA 228,1,3072
HS 30,18
M 11,70,4
s_cmp_lt_u32 s59, 32
LL 17,82
LL 107,83
M 11,71,5
s_cbranch_scc1 2b
LA 17
LC_ 107
ds_read_b64 v[14:15], v5 offset:2048
v_pk_add_f16 v26, v26, v30
v_pk_add_f16 v26, v26, v29
HX 26
v_subrev_u32 v5, s39, v5
W1 26,2048
WL 0
LO 16,16,106
LU 106,16
HZ 27,106
LL 18,84
LA 18
LL 110,85
PK 106,16,104,33
v_add_f32 v14, v14, v15
s_nop 1
v_add_f32_dpp v14, v14, v14 quad_perm:[1,0,3,2] row_mask:0xf bank_mask:0xf
s_nop 1
v_add_f32_dpp v14, v14, v14 quad_perm:[2,3,0,1] row_mask:0xf bank_mask:0xf
s_nop 1
v_add_f32_dpp v14, v14, v14 row_half_mirror row_mask:0xf bank_mask:0xf
v_cvt_f64_f32 v[14:15], v14
s_mov_b64 exec, s[54:55]
SP
s_mov_b64 exec, -1
LC_ 110
PK 104,16,104,32
LO 17,17,107
LU 107,17
s_add_u32 s42, s94, s60
s_addc_u32 s43, s95, 0
HS 27,107
LL 16,86
LA 16
LL 111,87
LC_ 111
PK 107,17,105,33
PK 105,17,105,32
SU 104
WL 0
LO 18,18,110
LU 110,18
HS 27,110
LL 17,88
LA 17
LL 114,89
LC_ 114
PK 110,18,108,33
PK 108,18,108,32
PO 110
LO 16,16,111
LU 111,16
HS 27,111
s_add_u32 s42, s94, s61
s_addc_u32 s43, s95, 0
LL 18,90
LA 18
LL 115,91
LC_ 115
PK 111,16,109,33
PK 109,16,109,32
PO 111
WL 0
LO 17,17,114
LU 114,17
HZ 29,114
LL 16,92
LA 16
LL 118,93
LC_ 118
PK 114,17,112,33
PK 112,17,112,32
SU 108
LO 18,18,115
LU 115,18
s_add_u32 s42, s94, s62
s_addc_u32 s43, s95, 0
HS 29,115
LL 17,94
LA 17
LL 119,95
LC_ 119
PK 115,18,113,33
PK 113,18,113,32
SU 112
WL 0
LO 16,16,118
LU 118,16
HS 29,118
LL 18,96
LA 18
LL 122,97
LC_ 122
PK 118,16,116,33
PK 116,16,116,32
PO 118
LO 17,17,119
LU 119,17
HS 29,119
s_add_u32 s42, s94, s63
s_addc_u32 s43, s95, 0
LL 16,98
LA 16
LL 123,99
LC_ 123
PK 119,17,117,33
PK 117,17,117,32
PO 119
WL 0
LO 18,18,122
LU 122,18
HZ 30,122
LL 17,100
LA 17
LL 126,101
LC_ 126
PK 122,18,120,33
PK 120,18,120,32
SU 116
LO 16,16,123
LU 123,16
s_add_u32 s42, s94, s64
s_addc_u32 s43, s95, 0
HS 30,123
LL 18,102
LA 18
LL 127,103
LC_ 127
PK 123,16,121,33
PK 121,16,121,32
SU 120
WL 0
LO 17,17,126
LU 126,17
HS 30,126
PK 126,17,124,33
PK 124,17,124,32
PO 126
LO 18,18,127
LU 127,18
HS 30,127
s_add_u32 s42, s94, s65
s_addc_u32 s43, s95, 0
v_pk_add_f16 v27, v27, v30
PK 127,18,125,33
v_pk_add_f16 v27, v27, v29
PK 125,18,125,32
HX 27
PO 127
W1 27,3072
s_nop 1
v_add_u32 v4, s39, v4
SU 124
s_lshl_b32 s53, s52, 8
s_add_u32 s48, %[alo], s53
s_addc_u32 s49, %[ahi], 0
s_add_u32 s52, s52, %[d]
s_cmp_eq_u32 s52, %[wrap]
s_cselect_b32 s52, %[rst], s52
WL 0
WV 0
s_barrier
s_sub_u32 s39, 0, s39
ds_read_b64 v[14:15], v5 offset:2048
s_nop 1
v_subrev_u32 v5, s39, v5
WL 0
v_add_f32 v14, v14, v15
s_nop 1
v_add_f32_dpp v14, v14, v14 quad_perm:[1,0,3,2] row_mask:0xf bank_mask:0xf
s_nop 1
v_add_f32_dpp v14, v14, v14 quad_perm:[2,3,0,1] row_mask:0xf bank_mask:0xf
s_nop 1
v_add_f32_dpp v14, v14, v14 row_half_mirror row_mask:0xf bank_mask:0xf
v_cvt_f64_f32 v[14:15], v14
s_mov_b64 exec, s[54:55]
SP
s_mov_b64 exec, -1
GE
s_waitcnt vmcnt(0) lgkmcnt(0)
8:
s_branch 6f
.Lk1s0:
FR
s_waitcnt lgkmcnt(0)
v_cmp_gt_u32 vcc, v19, v15
s_nop 4
s_cbranch_vccnz .Lk1s0
s_branch .Lk1r0
.Lk1s1:
FR
s_waitcnt lgkmcnt(0)
v_cmp_gt_u32 vcc, v19, v15
s_nop 4
s_cbranch_vccnz .Lk1s1
s_branch .Lk1r1
.Lk1s2:
FR
s_waitcnt lgkmcnt(0)
v_cmp_gt_u32 vcc, v19, v15
s_nop 4
s_cbranch_vccnz .Lk1s2
s_branch .Lk1r2
.Lk1s3:
FR
s_waitcnt lgkmcnt(0)
v_cmp_gt_u32 vcc, v19, v15
s_nop 4
s_cbranch_vccnz .Lk1s3
s_branch .Lk1r3
.Lk1s4:
FR
s_waitcnt lgkmcnt(0)
v_cmp_gt_u32 vcc, v19, v15
s_nop 4
s_cbranch_vccnz .Lk1s4
s_branch .Lk1r4
.Lk1s5:
FR
s_waitcnt lgkmcnt(0)
v_cmp_gt_u32 vcc, v19, v15
s_nop 4
s_cbranch_vccnz .Lk1s5
s_branch .Lk1r5
.Lk1s6:
FR
s_waitcnt lgkmcnt(0)
v_cmp_gt_u32 vcc, v19, v15
s_nop 4
s_cbranch_vccnz .Lk1s6
s_branch .Lk1r6
.Lk1s7:
FR
s_waitcnt lgkmcnt(0)
v_cmp_gt_u32 vcc, v19, v15
s_nop 4
s_cbranch_vccnz .Lk1s7
s_branch .Lk1r7
.Lk1s8:
FR
s_waitcnt lgkmcnt(0)
v_cmp_gt_u32 vcc, v19, v15
s_nop 4
s_cbranch_vccnz .Lk1s8
s_branch .Lk1r8
.Lk1s9:
FR
s_waitcnt lgkmcnt(0)
v_cmp_gt_u32 vcc, v19, v15
s_nop 4
s_cbranch_vccnz .Lk1s9
s_branch .Lk1r9
.Lk1s10:
FR
s_waitcnt lgkmcnt(0)
v_cmp_gt_u32 vcc, v19, v15
s_nop 4
s_cbranch_vccnz .Lk1s10
s_branch .Lk1r10
.Lk1s11:
FR
s_waitcnt lgkmcnt(0)
v_cmp_gt_u32 vcc, v19, v15
s_nop 4
s_cbranch_vccnz .Lk1s11
s_branch .Lk1r11
.Lk1s12:
FR
s_waitcnt lgkmcnt(0)
v_cmp_gt_u32 vcc, v19, v15
s_nop 4
s_cbranch_vccnz .Lk1s12
s_branch .Lk1r12
.Lk1s13:
FR
s_waitcnt lgkmcnt(0)
v_cmp_gt_u32 vcc, v19, v15
s_nop 4
s_cbranch_vccnz .Lk1s13
s_branch .Lk1r13
.Lk1s14:
FR
s_waitcnt lgkmcnt(0)
v_cmp_gt_u32 vcc, v19, v15
s_nop 4
s_cbranch_vccnz .Lk1s14
s_branch .Lk1r14
.Lk1s15:
FR
s_waitcnt lgkmcnt(0)
v_cmp_gt_u32 vcc, v19, v15
s_nop 4
s_cbranch_vccnz .Lk1s15
s_branch .Lk1r15
.Lk1s16:
FR
s_waitcnt lgkmcnt(0)
v_cmp_gt_u32 vcc, v19, v15
s_nop 4
s_cbranch_vccnz .Lk1s16
s_branch .Lk1r16
.Lk1s17:
FR
s_waitcnt lgkmcnt(0)
v_cmp_gt_u32 vcc, v19, v15
s_nop 4
s_cbranch_vccnz .Lk1s17
s_branch .Lk1r17
.Lk1s18:
FR
s_waitcnt lgkmcnt(0)
v_cmp_gt_u32 vcc, v19, v15
s_nop 4
s_cbranch_vccnz .Lk1s18
s_branch .Lk1r18
.Lk1s19:
FR
s_waitcnt lgkmcnt(0)
v_cmp_gt_u32 vcc, v19, v15
s_nop 4
s_cbranch_vccnz .Lk1s19
s_branch .Lk1r19
6:
.purgem M
.purgem Z
.purgem LZ
.purgem LS
.purgem LD
.purgem GZ
.purgem GS
.purgem CL
.purgem CH
.purgem XL
.purgem XN
.purgem F
.purgem U
.purgem E
.purgem A
.purgem R
.purgem C
.purgem S0
.purgem S
.purgem HC
.purgem HF
.purgem HM
.purgem HE
.purgem HG
.purgem HA
.purgem HR
.purgem HQ
.purgem HP
.purgem HY
.purgem HK
.purgem LQ
.purgem LL
)"
R"(.purgem LH
.purgem LA
.purgem LC_
.purgem LO
.purgem HZ
.purgem HS
.purgem HT
.purgem HX
.purgem UX
.purgem EX
.purgem AX
.purgem RX
.purgem TX
.purgem SX
.purgem AW
.purgem RA
.purgem RV
.purgem W2
.purgem W1
.purgem DW
.purgem DF
.purgem DX
.purgem ST
.purgem SU
.purgem SP
.purgem AT
.purgem PFA
.purgem PFN
.purgem PFW
.purgem PF0
.purgem WL
.purgem WV
.purgem LU
.purgem CF
.purgem XS
.purgem AR
.purgem DQ
.purgem GE
.purgem BW
.purgem BR
.purgem LK
.purgem PK
.purgem PO
.purgem LG
.purgem DY
.purgem FP
.purgem FR
)"
      : [nb] "=&s"(nb)
      : [xlo] "s"((unsigned)xp), [xhi] "s"((unsigned)(xp >> 32)), [hlo] "s"((unsigned)hp), [hhi] "s"((unsigned)(hp >> 32)),
        [glo] "s"((unsigned)gp), [ghi] "s"((unsigned)(gp >> 32)), [wlo] "s"((unsigned)wp), [whi] "s"((unsigned)(wp >> 32)),
        [w] "s"(w), [lds] "s"(lds0), [d] "s"(d), [wrap] "s"(wrap), [rst] "s"(rst), [pos0] "s"(pos0), [xo0] "s"(xo0), [xs1] "s"(xs1), [sb1] "s"(sb1), [r0o] "s"(r0o), [tsel] "s"(tsel), [s2] "s"(s2),
        [alo] "s"((unsigned)ap), [ahi] "s"((unsigned)(ap >> 32)), [ca0] "s"(ca0), [pm] "s"(pm), [pf] "s"(pf)
      : K1_CLOB);
  TS(6);
  MK_END(1);
}

typedef __bf16 bf16x8_t __attribute__((ext_vector_type(8)));
DI f32x4 mfma16(u32x4 a, u32x4 b, f32x4 c) {
  return __builtin_amdgcn_mfma_f32_16x16x32_bf16(__builtin_bit_cast(bf16x8_t, a), __builtin_bit_cast(bf16x8_t, b), c, 0, 0, 0);
}
DI float grn_gx(double s, float scale) { return round_bf16(__builtin_amdgcn_sqrtf(round_bf16(round_bf16((float)s * scale) + 1e-6f))); }
DI void slow_fc1(const u16* __restrict__ x, const u16* __restrict__ dw, char* __restrict__ hws, float* sacc, int w) {
  const int lane = __builtin_amdgcn_mbcnt_hi(~0u, __builtin_amdgcn_mbcnt_lo(~0u, 0u)), m = lane & 15, g = lane >> 4;
  for (int i = lane; i < HID; i += 64) sacc[i] = 0.f;
#pragma unroll 1
  for (int it = 0; it < 9; ++it) {
    const int sub = blockIdx.x * 36 + it * 4 + w;
    const unsigned px = sub_pixel(x, sub, m);
    u32x4 A[12];
    float sum = 0.f;
    const u16* dq = dw;
    asm volatile("" : "+s"(dq));
#pragma unroll
    for (int ks = 0; ks < 12; ++ks) {
      const u32x4 v = *(const u32x4*)(x + (size_t)px * CH + 32 * ks + 8 * g);
      const u32x4 d = *(const u32x4*)(dq + 32 * ks + 8 * g);
#pragma unroll
      for (int j = 0; j < 4; ++j) {
        A[ks][j] = pack_bf16(bf_lo(v[j]) * bf_lo(d[j]), bf_hi(v[j]) * bf_hi(d[j]));
        sum += bf_lo(A[ks][j]) + bf_hi(A[ks][j]);
      }
      asm volatile("" : "+v"(A[ks]));
    }
    sum += __shfl_xor(sum, 16); sum += __shfl_xor(sum, 32);
    const float mean = sum * (1.0f / CH);
    float var = 0.f;
#pragma unroll
    for (int ks = 0; ks < 12; ++ks) {
#pragma unroll
      for (int j = 0; j < 4; ++j) {
        const float a = bf_lo(A[ks][j]) - mean, b = bf_hi(A[ks][j]) - mean;
        var += a * a + b * b;
      }
      asm volatile("" : "+v"(A[ks]));
    }
    var += __shfl_xor(var, 16); var += __shfl_xor(var, 32);
    const float mb = round_bf16(mean);
    const float rd = 1.0f / round_bf16(sqrtf(round_bf16(round_bf16(var * (1.0f / CH)) + 1e-5f)));
#pragma unroll
    for (int ks = 0; ks < 12; ++ks)
#pragma unroll
      for (int j = 0; j < 4; ++j) A[ks][j] = pack_bf16((bf_lo(A[ks][j]) - mb) * rd, (bf_hi(A[ks][j]) - mb) * rd);
#pragma unroll 1
    for (int tc = 0; tc < 96; ++tc) {
      f32x4 acc = {0.f, 0.f, 0.f, 0.f};
#pragma unroll
      for (int ks = 0; ks < 12; ++ks) acc = mfma16(A[ks], *(const u32x4*)(g_buf + (size_t)(tc * 12 + ks) * 1024 + lane * 16), acc);
      float q = 0.f;
#pragma unroll
      for (int r = 0; r < 4; ++r) { acc[r] = gelu_tanh(acc[r]); q = __builtin_fmaf(acc[r], acc[r], q); }
      typedef unsigned u32x2_ __attribute__((ext_vector_type(2)));
      *(u32x2_*)(hws + h_piece(hws, sub, tc >> 1) + g * 256 + ((m + 4 * g) & 15) * 16 + (tc & 1) * 8) = (u32x2_){pack_bf16(acc[0], acc[1]), pack_bf16(acc[2], acc[3])};
      q += __shfl_xor(q, 16); q += __shfl_xor(q, 32);
      if (g == 0) sacc[tc * 16 + m] += q;
    }
  }
  __builtin_amdgcn_wave_barrier();
  __builtin_amdgcn_fence(__ATOMIC_ACQ_REL, "workgroup");
  for (int i = lane; i < HID; i += 64) atomicAdd((double*)(g_buf + G_ACC2) + i, (double)sacc[i]);
}
DI void slow_bar(unsigned* c, int w) {
  const int lane = __builtin_amdgcn_mbcnt_hi(~0u, __builtin_amdgcn_mbcnt_lo(~0u, 0u));
  asm volatile("s_waitcnt vmcnt(0)" ::: "memory");
  __syncthreads();
  if (w == 0 && lane == 0) {
    __threadfence();
    asm volatile("s_waitcnt vmcnt(0)" ::: "memory");
    __hip_atomic_fetch_add(c, 1u, __ATOMIC_RELAXED, __HIP_MEMORY_SCOPE_AGENT);
    for (int i = 0; i < (1 << 22) && __hip_atomic_load(c, __ATOMIC_RELAXED, __HIP_MEMORY_SCOPE_AGENT) < (unsigned)NWG; ++i) __builtin_amdgcn_s_sleep(32);
    __threadfence();
    asm volatile("s_waitcnt vmcnt(0)" ::: "memory");
  }
  __syncthreads();
}

#define K2_CLOB "memory", "vcc", "scc", "s48", "s49", "s50", "s51", "s52", "s53", "s54", "s55", "s56", "s57", "s58", "s59", "s60", "s61", "s62", "s63", "s64", "s65", "s66", "s67", \
  "s68", "s69", "s70", "s71", "s72", "s73", "s74", "s75", "s76", "s77", "s78", "s79", "s80", "s81", "s84", "s85", "s86", "s87", "s88", "s89", "s90", "s91", "s92", "s93", "s94", "s95", "s96", "s97", "s98", "s99", "s38", "s39", "s40", "s41", "s42", "s43", "s44", "s45", "s46", "s47", VA_CLOB
DI void slow_fc2(const u16* __restrict__ x, const char* __restrict__ hws, u16* __restrict__ out, const u16* __restrict__ grn_g, const u16* __restrict__ w2,
                 char* smem, int w) {
  float* sc = (float*)smem;
  float* red = sc + HID;
  u16* wf = (u16*)(smem + 8192);
  const int lane = __builtin_amdgcn_mbcnt_hi(~0u, __builtin_amdgcn_mbcnt_lo(~0u, 0u)), tid = w * 64 + lane, m = lane & 15, g = lane >> 4;
  __syncthreads();
  float part = 0.f;
  for (int c = tid; c < HID; c += 256) {
    const float gx = grn_gx(((const double*)(g_buf + G_ACC2))[c], 1.0f);
    sc[c] = gx; part += gx;
  }
#pragma unroll
  for (int o = 32; o > 0; o >>= 1) part += __shfl_xor(part, o);
  if (lane == 0) red[tid >> 6] = part;
  __syncthreads();
  const float gmean = round_bf16(round_bf16(((red[0] + red[1]) + (red[2] + red[3])) * (1.0f / HID)) + 1e-6f);
  for (int c = tid; c < HID; c += 256) sc[c] = (1.0f + ld_bf16(grn_g + c) * round_bf16(sc[c] / gmean)) ;
#pragma unroll 1
  for (int it = 0; it < 9; ++it) {
    const int sub = blockIdx.x * 36 + it * 4 + (tid >> 6);
    f32x4 acc[24];
#pragma unroll
    for (int i = 0; i < 24; ++i) acc[i] = (f32x4){0.f, 0.f, 0.f, 0.f};
#pragma unroll 1
    for (int t = 0; t < 48; ++t) {
      __syncthreads();
      for (int e = tid; e < 32 * CH; e += 256) {
        const int k = e / CH, n = e % CH;
        wf[((n >> 4) * 64 + (k >> 3) * 16 + (n & 15)) * 8 + (k & 7)] = (u16)pack_bf16(sc[32 * t + k] * ld_bf16(w2 + (size_t)(32 * t + k) * CH + n), 0.f);
      }
      __syncthreads();
      const char* hp = hws + h_piece(hws, sub, t) + (m >> 2) * 256 + (g >> 1) * 8 + 2 * (m & 3);
      u32x4 A;
#pragma unroll
      for (int j = 0; j < 4; ++j) {
        unsigned v[2];
#pragma unroll
        for (int k = 0; k < 2; ++k) {
          v[k] = *(const u16*)(hp + ((8 * (g & 1) + 2 * j + k + (m & 12)) & 15) * 16);
        }
        A[j] = v[0] | (v[1] << 16);
      }
#pragma unroll
      for (int i = 0; i < 24; ++i) acc[i] = mfma16(A, *(const u32x4*)(wf + (i * 64 + lane) * 8), acc[i]);
    }
#pragma unroll
    for (int i = 0; i < 24; ++i) {
      const int n = 16 * i + m;
      const float* bp = g_cs + 4 * 384 + cs_index(n);
      const float bias = (bp[0] + bp[384]) + (bp[768] + bp[1152]);
#pragma unroll
      for (int r = 0; r < 4; ++r) {
        const size_t o = (size_t)(sub_pixel(x, sub, 4 * g) + r) * CH + n;
        out[o] = (u16)pack_bf16(acc[i][r] + bias + ld_bf16(x + o), 0.f);
      }
    }
  }
}
__global__ __launch_bounds__(256, 1) void k2_fc2(const u16* __restrict__ x, char* __restrict__ hws, u16* __restrict__ out,
                                                 const u16* __restrict__ grn_g, const u16* __restrict__ w2, const u16* __restrict__ dw,
                                                 const u16* __restrict__ w1) {
  extern __shared__ __attribute__((aligned(16))) char smem[];
  const int w = __builtin_amdgcn_readfirstlane(threadIdx.x >> 6);
  const unsigned pg = w & 1, nh = w >> 1;
  const unsigned lds0 = lds_addr(smem) + 1024;
  MK_START(4); TS(9);
  const uint64_t pp = uni64((uint64_t)g_prm + (blockIdx.x * 4 + w) * 256), ap = (uint64_t)g_buf + K1_G_ACC + w * 1024;
  const unsigned wm = lds0 + w * 6144, ab = lds0 + nh * 12288;
  const unsigned hb = lds0 + K2_HB + pg * 2 * K2_RSZ, hm = hb + nh * K2_RSZ;
  const unsigned bm = w * 384, bb = lds0 + K2_E_OFF + nh * 768;
  const unsigned pm = lds0 + K2_PF_OFF, sb = lds0 + K2_S16_OFF, sw = sb + w * 256;
  uint64_t flg;
#define K2_OPS [pp] "s"(pp), [alo] "s"((unsigned)ap), [ahi] "s"((unsigned)(ap >> 32)), [wm] "s"(wm), [hm] "s"(hm), [bm] "s"(bm), [ab] "s"(ab), [hb] "s"(hb), [bb] "s"(bb), \
         [sb] "s"(sb), [sw] "s"(sw), [pm] "s"(pm)
  asm volatile(
R"(.macro MA t,a,b
v_mfma_f32_16x16x32_f16 a[\t:\t+3], v[\b:\b+3], a[\a:\a+3], a[\t:\t+3]
.endm
.macro MV t,a,b
v_mfma_f32_16x16x32_f16 v[\t:\t+3], v[\b:\b+3], a[\a:\a+3], v[\t:\t+3]
.endm
.macro NA t,a,b
v_mfma_f32_16x16x32_f16 a[\t:\t+3], a[\b:\b+3], a[\a:\a+3], a[\t:\t+3]
.endm
.macro NV t,a,b
v_mfma_f32_16x16x32_f16 v[\t:\t+3], a[\b:\b+3], a[\a:\a+3], v[\t:\t+3]
.endm
.macro AN d,s
v_and_b32 v[\d], s93, v[\s]
.endm
.macro SH d
v_lshrrev_b32 v[\d], 4, v[\d]
.endm
.macro PA_ d,h,r
v_perm_b32 v[\d], v[\h], v[\r], s54
.endm
.macro PB_ d,h,r
v_perm_b32 v[\d], v[\h], v[\r], s55
.endm
.macro AW a,s
v_accvgpr_write_b32 a[\a], v[\s]
.endm
.macro AR d,a
v_accvgpr_read_b32 v[\d], a[\a]
.endm
.macro UL d,x
v_lshlrev_b32 v[\d], 16, v[\x]
.endm
.macro UH d,x
v_and_b32 v[\d], s38, v[\x]
.endm
.macro AD d,b
v_fmac_f32 v[\d], s53, v[\b]
.endm
.macro EF d,e
v_fma_f32 v[\d], v[\d], s39, v[\e]
.endm
.macro TY d,o
ds_read_b64_tr_b16 v[\d:\d+1], v2 offset:\o
.endm
.macro TX d,o
ds_read_b64_tr_b16 v[\d:\d+1], v51 offset:\o
.endm
.macro RA d,o
ds_read_b128 a[\d:\d+3], v1 offset:\o
.endm
.macro RC d,o
ds_read_b128 a[\d:\d+3], v5 offset:\o
.endm
.macro BV d,o
ds_read_b128 v[\d:\d+3], v4 offset:\o
.endm
.macro BA d,o
ds_read_b128 a[\d:\d+3], v4 offset:\o
.endm
.macro PA a
s_mov_b32 m0, %[pm]
s_getpc_b64 s[46:47]
s_andn2_b32 s46, s46, 0x7f
s_add_u32 s46, s46, s81
s_addc_u32 s47, s47, 0
s_add_u32 s46, s46, \a
s_addc_u32 s47, s47, 0
.endm
.macro DF
global_load_lds_dwordx4 v0, s[46:47]
.endm
.macro DW o
global_load_lds_dwordx4 v0, s[40:41] offset:\o sc1
.endm
.macro DR o
global_load_lds_dwordx4 v0, s[40:41] offset:\o
.endm
.macro DH o
global_load_lds_dwordx4 v0, s[40:41] offset:\o nt
.endm
.macro DG o
s_mov_b32 exec_hi, 0
global_load_lds_dwordx4 v0, s[40:41] offset:\o nt
s_mov_b32 exec_hi, -1
.endm
.macro XL d,o
global_load_dwordx2 v[\d:\d+1], v47, s[44:45] offset:\o
.endm
.macro ST d,o
global_store_dwordx2 v50, v[\d:\d+1], s[42:43] offset:\o nt sc1
.endm
.macro SA d,o
global_store_dwordx2 v50, a[\d:\d+1], s[42:43] offset:\o nt sc1
.endm
.macro CV d,a,b
v_cvt_pk_bf16_f32 v[\d], v[\a], v[\b]
.endm
.macro RS d,o
ds_read_b128 v[\d:\d+3], v46 offset:\o
.endm
.macro PM d,s
v_pk_mul_f16 v[\d], v[\d], v[\s]
.endm
.macro PF d,n
v_pk_fma_f16 v[\d], v[\d], v[14+\n], v[22+\n]
.endm
.macro FS
global_store_dword v0, v54, s[96:97] sc1
.endm
.macro FL
global_load_dword v54, v55, s[98:99] sc1
.endm
.macro FB
s_cbranch_vccnz 21b
22:
.endm
.macro FC
s_cbranch_vccnz 26b
27:
.endm
.macro FG
s_or_b64 s[54:55], s[44:45], vcc
s_cbranch_scc1 23b
24:
.endm
.macro FX
s_branch 25f
21:
s_sleep 4
global_load_dword v54, v55, s[98:99] sc1
s_waitcnt vmcnt(0)
v_cmp_eq_u32 vcc, 0, v54
s_sub_u32 s80, s80, 1
s_cbranch_scc1 22f
s_nop 2
s_cbranch_vccnz 21b
s_branch 22f
26:
s_sleep 4
global_load_dword v54, v55, s[98:99] sc1
s_waitcnt vmcnt(0)
v_cmp_ne_u32 vcc, 2, v54
s_sub_u32 s80, s80, 1
s_cbranch_scc1 27f
s_nop 2
s_cbranch_vccnz 26b
s_branch 27f
23:
v_mov_b32 v10, 1
s_mov_b64 exec, 1
global_store_dword v0, v10, s[78:79] offset:384 sc1
s_mov_b64 exec, -1
s_nop 4
s_branch 24f
25:
.endm
.macro FP
ds_inc_u32 v0, v128
.endm
.macro FR
ds_read_b32 v10, v0
.endm
.macro FQ n
v_cmp_gt_u32 vcc, \n, v10
.endm
.macro FZ n
s_cbranch_vccnz .Lk2s\n
.Lk2r\n:
.endm
.macro FY n
.Lk2s\n:
FR
s_waitcnt lgkmcnt(0)
FQ \n
s_nop 4
s_cbranch_vccnz .Lk2s\n
s_branch .Lk2r\n
.endm
.macro WL n
s_waitcnt lgkmcnt(\n)
.endm
.macro WV n
s_waitcnt vmcnt(\n)
.endm
.macro M n
.if \n < 82
v_mfma_f32_16x16x32_f16 a[36+4*(\n/2):36+4*(\n/2)+3], v[56+8*((\n/2)%%6)+4*(\n&1):56+8*((\n/2)%%6)+4*(\n&1)+3], a[8*((\n/12)%%4)+4*(\n&1):8*((\n/12)%%4)+4*(\n&1)+3], a[36+4*(\n/2):36+4*(\n/2)+3]
.else
v_mfma_f32_16x16x32_f16 v[-32+4*(\n/2):-32+4*(\n/2)+3], v[56+8*((\n/2)%%6)+4*(\n&1):56+8*((\n/2)%%6)+4*(\n&1)+3], a[8*((\n/12)%%4)+4*(\n&1):8*((\n/12)%%4)+4*(\n&1)+3], v[-32+4*(\n/2):-32+4*(\n/2)+3]
.endif
.endm
.macro R1 n
.if \n < 64
RA 8*(((\n%%24)/2+2*((\n/24)&1))%%4)+4*(\n&1),1024*\n
.else
RC 8*(((\n%%24)/2+2*((\n/24)&1))%%4)+4*(\n&1),1024*\n-49152
.endif
.endm
.macro R n
R1 \n
R1 (\n+1)
.endm
.macro P n
.if \n & 2
PB_ 56+\n,104+8*(\n/16)+(\n&4)+2*((\n/8)&1)+(\n&1),56+(\n|2)
.else
PA_ 56+\n,104+8*(\n/16)+(\n&4)+2*((\n/8)&1)+(\n&1),56+(\n|2)
.endif
.endm
.macro Q n
P \n
P (\n+1)
.endm
.macro mw k
s_add_u32 m0, %[wm], 1024*\k
.endm
.macro mh k
s_add_u32 m0, %[hm], 512*\k
.endm
.macro ad r,k
s_add_u32 s40, s[\r], 512*\k
s_addc_u32 s41, s[\r+1], 0
.endm
.macro ae d,r,k
s_add_u32 s[\d], s[\r], 512*\k
s_addc_u32 s[\d+1], s[\r+1], 0
.endm
.macro bc m,b,d,r
s_bitcmp1_b32 s[\m], \b
s_cselect_b64 s[\d:\d+1], s[\r+2:\r+3], s[\r:\r+1]
.endm
.macro W k
DW 1024*\k
.endm
.macro H k
DH 1024*\k
.endm
k2_code:
s_load_dwordx16 s[48:63], %[pp], 0x0
s_load_dwordx16 s[64:79], %[pp], 0x40
s_load_dwordx16 s[84:99], %[pp], 0x80
s_load_dword s42, %[pp], 0xc0
v_mbcnt_lo_u32_b32 v7, -1, 0
v_mbcnt_hi_u32_b32 v7, -1, v7
v_lshlrev_b32 v0, 4, v7
v_add_u32 v88, 0x1000, v0
v_add_u32 v89, 0x2000, v0
s_mov_b32 s44, %[alo]
s_mov_b32 s45, %[ahi]
global_load_dwordx4 v[132:135], v0, s[44:45]
global_load_dwordx4 v[144:147], v88, s[44:45]
global_load_dwordx4 v[148:151], v89, s[44:45]
ae 44,44,24
global_load_dwordx4 v[168:171], v0, s[44:45]
global_load_dwordx4 v[172:175], v88, s[44:45]
global_load_dwordx4 v[192:195], v89, s[44:45]
s_waitcnt lgkmcnt(0)
bc 49,0,66,60
mh 0
ad 66,0
H 0
DG 1024
ae 44,44,24
global_load_dwordx4 v[196:199], v0, s[44:45]
global_load_dwordx4 v[216:219], v88, s[44:45]
global_load_dwordx4 v[220:223], v89, s[44:45]
ae 44,44,24
global_load_dwordx4 v[224:227], v0, s[44:45]
global_load_dwordx4 v[228:231], v88, s[44:45]
global_load_dwordx4 v[240:243], v89, s[44:45]
v_and_b32 v8, 15, v7
v_lshrrev_b32 v9, 4, v7
v_lshlrev_b32 v10, 2, v7
v_add_u32 v202, s65, v0
global_load_dword v244, v10, s[92:93] offset:0
global_load_dword v245, v10, s[92:93] offset:1024
global_load_dword v246, v10, s[92:93] offset:2048
global_load_dwordx4 v[152:155], v0, s[88:89]
s_mov_b64 exec, 0xff
global_load_dwordx4 v[156:159], v202, s[88:89]
s_mov_b64 exec, -1
s_branch 28f
FY 4
FY 8
FY 12
FY 16
FY 20
FY 24
FY 28
FY 32
FY 36
FY 40
FY 44
FY 48
FY 52
FY 56
FY 60
FY 64
FY 68
FY 72
FY 76
FY 80
FY 84
FY 88
FY 92
FY 96
FY 100
FY 104
FY 108
FY 112
FY 116
FY 120
FY 124
FY 128
28:
FX
s_mov_b32 s92, s64
s_mov_b32 s93, 0x0f0f0f0f
v_add_u32 v1, %[ab], v0
v_add_u32 v5, 0xc000, v1
v_and_b32 v10, 3, v7
v_lshrrev_b32 v11, 2, v7
v_lshl_add_u32 v11, v10, 2, v11
v_and_b32 v11, 15, v11
mw 0
ad 86,0
DR 0
v_lshl_add_u32 v11, v10, 4, v11
v_lshl_add_u32 v2, v11, 4, %[hb]
v_lshl_add_u32 v51, v11, 3, %[hb]
v_lshl_add_u32 v4, v8, 4, %[bb]
s_mov_b32 s38, 0xffff0000
s_mov_b32 s80, 0x40000
s_mov_b32 s81, s52
s_mov_b32 s39, s54
v_lshl_add_u32 v46, v9, 4, %[sb]
v_add_u32 v46, 8192, v46
v_bfe_u32 v10, v7, 1, 2
DR 1024
v_bfe_u32 v11, v7, 3, 1
v_and_b32 v12, 1, v7
v_lshl_add_u32 v11, v11, 1, v12
v_lshl_add_u32 v10, v10, 2, v11
v_lshl_add_u32 v10, v9, 4, v10
v_lshl_add_u32 v90, v10, 2, %[sw]
DR 2048
v_lshl_add_u32 v91, v7, 2, %[sw]
s_and_b32 s42, s51, 0xffff
s_lshr_b32 s43, s51, 16
s_add_u32 s42, s42, %[sb]
s_add_u32 s43, s43, %[sb]
v_lshl_add_u32 v200, v9, 4, s42
v_mov_b32 v201, s43
v_mov_b32 v128, 0x362aaa8f
v_mov_b32 v129, 0x362aaa8f
v_mov_b32 v130, 0x362aaa8f
v_mov_b32 v131, 0x362aaa8f
mw 3
ad 86,6
DR 0
AW 32,128
AW 33,129
AW 34,130
AW 35,131
v_mov_b32 v95, 0
ds_write_b32 v0, v95
s_nop 1
v_mov_b32 v95, 0x7f800000
s_mov_b32 s44, 0x44100000
WV 19
v_add_f64 v[132:133], v[132:133], v[168:169]
WV 18
v_add_f64 v[144:145], v[144:145], v[172:173]
DR 1024
WV 18
v_add_f64 v[148:149], v[148:149], v[192:193]
v_add_f64 v[134:135], v[134:135], v[170:171]
v_add_f64 v[146:147], v[146:147], v[174:175]
v_add_f64 v[150:151], v[150:151], v[194:195]
WV 15
v_add_f64 v[132:133], v[132:133], v[196:197]
WV 14
v_add_f64 v[144:145], v[144:145], v[216:217]
DR 2048
WV 14
v_add_f64 v[148:149], v[148:149], v[220:221]
v_add_f64 v[134:135], v[134:135], v[198:199]
v_add_f64 v[146:147], v[146:147], v[218:219]
v_add_f64 v[150:151], v[150:151], v[222:223]
WV 13
v_add_f64 v[132:133], v[132:133], v[224:225]
WV 12
v_add_f64 v[144:145], v[144:145], v[228:229]
bc 49,1,66,60
mh 9
ad 66,192
H 0
WV 12
v_add_f64 v[148:149], v[148:149], v[240:241]
)"
R"(v_add_f64 v[134:135], v[134:135], v[226:227]
v_add_f64 v[146:147], v[146:147], v[230:231]
v_add_f64 v[150:151], v[150:151], v[242:243]
v_cvt_f32_f64 v248, v[132:133]
v_cvt_f32_f64 v252, v[144:145]
DG 1024
v_cvt_f32_f64 v72, v[148:149]
v_cvt_f32_f64 v249, v[134:135]
v_cvt_f32_f64 v253, v[146:147]
v_cvt_f32_f64 v73, v[150:151]
v_mul_f32 v250, s55, v248
v_mul_f32 v254, s55, v252
bc 48,1,64,56
mh 12
ad 64,192
H 0
v_mul_f32 v74, s55, v72
v_mul_f32 v251, s55, v249
v_mul_f32 v255, s55, v253
v_mul_f32 v75, s55, v73
CV 251,250,251
CV 255,254,255
DG 1024
CV 75,74,75
UL 250,251
UL 254,255
UL 74,75
UH 251,251
UH 255,255
mw 24
ad 86,48
DR 0
UH 75,75
v_add_f32 v250, 0x358637bd, v250
v_add_f32 v254, 0x358637bd, v254
v_add_f32 v74, 0x358637bd, v74
v_add_f32 v251, 0x358637bd, v251
v_add_f32 v255, 0x358637bd, v255
DR 1024
v_add_f32 v75, 0x358637bd, v75
CV 251,250,251
CV 255,254,255
CV 75,74,75
UL 250,251
UL 254,255
DR 2048
UL 74,75
UH 251,251
UH 255,255
UH 75,75
v_sqrt_f32 v250, v250
v_sqrt_f32 v254, v254
mw 27
ad 86,54
DR 0
v_sqrt_f32 v74, v74
v_sqrt_f32 v251, v251
v_sqrt_f32 v255, v255
v_sqrt_f32 v75, v75
CV 251,250,251
CV 255,254,255
DR 1024
CV 75,74,75
UL 250,251
UL 254,255
UL 74,75
UH 251,251
UH 255,255
DR 2048
UH 75,75
v_cmp_le_f32 vcc, s44, v248
v_cmp_le_f32 s[54:55], s44, v249
s_nop 0
v_cndmask_b32 v248, v95, v250, vcc
v_cndmask_b32 v249, v95, v251, s[54:55]
v_add_f32 v248, v248, v249
v_cmp_le_f32 vcc, s44, v252
v_cmp_le_f32 s[54:55], s44, v253
s_nop 0
v_cndmask_b32 v252, v95, v254, vcc
v_cndmask_b32 v253, v95, v255, s[54:55]
v_add_f32 v252, v252, v253
v_cmp_le_f32 vcc, s44, v72
v_cmp_le_f32 s[54:55], s44, v73
s_nop 0
v_cndmask_b32 v72, v95, v74, vcc
v_cndmask_b32 v73, v95, v75, s[54:55]
bc 49,2,66,60
mh 18
ad 66,384
H 0
v_add_f32 v72, v72, v73
v_add_f32 v92, v248, v252
v_add_f32 v92, v92, v72
s_nop 1
v_add_f32_dpp v92, v92, v92 row_shr:1 row_mask:0xf bank_mask:0xf
s_nop 1
v_add_f32_dpp v92, v92, v92 row_shr:2 row_mask:0xf bank_mask:0xf
s_nop 1
v_add_f32_dpp v92, v92, v92 row_shr:4 row_mask:0xf bank_mask:0xf
s_nop 1
v_add_f32_dpp v92, v92, v92 row_shr:8 row_mask:0xf bank_mask:0xf
s_nop 1
v_add_f32_dpp v92, v92, v92 row_bcast:15 row_mask:0xa bank_mask:0xf
s_nop 1
v_add_f32_dpp v92, v92, v92 row_bcast:31 row_mask:0xc bank_mask:0xf
ds_write_b32 v91, v92 offset:7168
v_mov_b32 v10, %[sb]
WL 0
s_barrier
ds_read_b32 v96, v10 offset:7420
ds_read_b32 v97, v10 offset:7676
ds_read_b32 v98, v10 offset:7932
ds_read_b32 v99, v10 offset:8188
WV 21
UL 76,244
WV 20
UL 80,245
WV 19
UL 84,246
UH 77,244
DG 1024
UH 81,245
UH 85,246
v_mov_b32 v93, 0
WL 0
v_add_f32 v96, v96, v97
v_add_f32 v98, v98, v99
v_add_f32 v96, v96, v98
bc 48,2,64,56
mh 21
ad 64,384
H 0
v_mul_f32 v94, 0x3a2aaaab, v96
v_cmp_nlt_f32 s[44:45], v96, v95
CV 94,94,94
UL 94,94
v_add_f32 v94, 0x358637bd, v94
CV 94,94,94
UL 94,94
H 1
v_rcp_f32 v94, v94
s_nop 0
v_mul_f32 v250, v94, v250
v_mul_f32 v254, v94, v254
v_mul_f32 v74, v94, v74
v_mul_f32 v251, v94, v251
v_mul_f32 v255, v94, v255
v_mul_f32 v75, v94, v75
H 2
CV 251,250,251
CV 255,254,255
CV 75,74,75
UL 250,251
UL 254,255
UL 74,75
mw 48
ad 86,96
DR 0
UH 251,251
UH 255,255
UH 75,75
v_fma_f32 v250, v76, v250, 1.0
v_fma_f32 v254, v80, v254, 1.0
v_fma_f32 v74, v84, v74, 1.0
DR 1024
v_fma_f32 v251, v77, v251, 1.0
v_fma_f32 v255, v81, v255, 1.0
v_fma_f32 v75, v85, v75, 1.0
v_cvt_pk_f16_f32 v76, v250, v251
v_cvt_pk_f16_f32 v80, v254, v255
v_cvt_pk_f16_f32 v84, v74, v75
ds_write_b32 v90, v76 offset:8192
ds_write_b32 v90, v80 offset:1024
ds_write_b32 v90, v84 offset:2048
v_max3_f32 v93, |v250|, |v251|, v93
DR 2048
v_max3_f32 v93, |v254|, |v255|, v93
v_max3_f32 v93, |v74|, |v75|, v93
v_cmp_lt_f32 vcc, 0x43000000, v93
WL 0
WV 16
s_barrier
TY 106,520
TY 110,14344
TX 58,0
TX 62,13824
TY 66,512
TY 70,14336
RS 14,0
RS 18,64
ds_read_b128 v[176:179], v200
ds_read_b128 v[180:183], v201
R 0
R 2
FG
s_mov_b32 s54, 0x06020400
s_mov_b32 s55, 0x07030501
WL 13
AN 104,106
AN 105,107
SH 106
SH 107
mw 51
ad 86,102
DR 0
AN 106,106
AN 107,107
WL 12
AN 108,110
AN 109,111
SH 110
SH 111
DR 1024
AN 110,110
AN 111,111
WL 10
Q 0
Q 2
DR 2048
Q 4
Q 6
R 4
R 6
WL 7
v_pk_mul_f16 v22, v14, s92
v_pk_mul_f16 v23, v15, s92
v_pk_mul_f16 v24, v16, s92
v_pk_mul_f16 v25, v17, s92
v_pk_mul_f16 v26, v18, s92
v_pk_mul_f16 v27, v19, s92
v_pk_mul_f16 v28, v20, s92
v_pk_mul_f16 v29, v21, s92
PF 56,0
PF 57,1
PF 58,2
PF 59,3
PF 60,4
PF 61,5
PF 62,6
PF 63,7
Q 8
Q 10
Q 12
Q 14
PF 64,0
PF 65,1
PF 66,2
PF 67,3
PF 68,4
PF 69,5
PF 70,6
PF 71,7
PM 152,176
PM 153,177
PM 154,178
PM 155,179
PM 156,180
PM 157,181
PM 158,182
PM 159,183
s_mov_b32 s42, 0x1c001c00
v_pk_mul_f16 v152, v152, s42
v_pk_mul_f16 v153, v153, s42
v_pk_mul_f16 v154, v154, s42
v_pk_mul_f16 v155, v155, s42
v_pk_mul_f16 v156, v156, s42
v_pk_mul_f16 v157, v157, s42
v_pk_mul_f16 v158, v158, s42
v_pk_mul_f16 v159, v159, s42
global_store_dwordx4 v0, v[152:155], s[90:91] sc1
s_mov_b64 exec, 0xff
global_store_dwordx4 v202, v[156:159], s[90:91] sc1
s_mov_b64 exec, -1
s_mov_b32 s42, 0x3c003c00
v_dot2_f32_f16 v52, v152, s42, 0
v_dot2_f32_f16 v53, v156, s42, 0
s_nop 1
v_dot2_f32_f16 v52, v153, s42, v52
v_dot2_f32_f16 v53, v157, s42, v53
s_nop 1
v_dot2_f32_f16 v52, v154, s42, v52
v_dot2_f32_f16 v53, v158, s42, v53
s_nop 1
v_dot2_f32_f16 v52, v155, s42, v52
v_dot2_f32_f16 v53, v159, s42, v53
s_nop 1
WL 0
v_mfma_f32_16x16x32_f16 a[36:39], v[56:59], a[0:3], a[32:35]
mw 72
RS 14,128
RS 18,192
TY 106,5128
M 1
ad 86,144
DR 0
TY 110,18952
v_mfma_f32_16x16x32_f16 a[60:63], v[56:59], a[8:11], a[32:35]
DR 1024
TX 32,4608
TX 36,18432
M 13
DR 2048
TY 40,5120
TY 44,18944
v_mfma_f32_16x16x32_f16 a[40:43], v[64:67], a[0:3], a[32:35]
mw 75
M 3
R 8
v_mfma_f32_16x16x32_f16 a[64:67], v[64:67], a[8:11], a[32:35]
ad 86,150
DR 0
M 15
R 10
v_mfma_f32_16x16x32_f16 a[84:87], v[56:59], a[16:19], a[32:35]
WL 10
DR 1024
v_pk_mul_f16 v22, v14, s92
M 25
DR 2048
v_pk_mul_f16 v23, v15, s92
v_mfma_f32_16x16x32_f16 a[88:91], v[64:67], a[16:19], a[32:35]
v_pk_mul_f16 v24, v16, s92
v_pk_mul_f16 v25, v17, s92
M 27
v_pk_mul_f16 v26, v18, s92
v_pk_mul_f16 v27, v19, s92
v_mfma_f32_16x16x32_f16 a[108:111], v[56:59], a[24:27], a[32:35]
WV 19
v_pk_mul_f16 v28, v20, s92
v_pk_mul_f16 v29, v21, s92
M 37
WL 0
AN 104,106
AN 105,107
v_mfma_f32_16x16x32_f16 a[112:115], v[64:67], a[24:27], a[32:35]
s_barrier
R 24
M 39
R 26
v_mfma_f32_16x16x32_f16 a[132:135], v[56:59], a[0:3], a[32:35]
mw 0
SH 106
SH 107
M 49
ad 86,192
DR 0
v_mfma_f32_16x16x32_f16 a[136:139], v[64:67], a[0:3], a[32:35]
DR 1024
AN 106,106
M 51
R 28
v_mfma_f32_16x16x32_f16 a[156:159], v[56:59], a[8:11], a[32:35]
DR 2048
AN 107,107
M 61
mw 3
AN 108,110
AN 109,111
v_mfma_f32_16x16x32_f16 a[160:163], v[64:67], a[8:11], a[32:35]
ad 86,198
DR 0
M 63
WL 4
R 30
v_mfma_f32_16x16x32_f16 a[180:183], v[56:59], a[16:19], a[32:35]
DR 1024
SH 110
M 73
DR 2048
SH 111
v_mfma_f32_16x16x32_f16 a[184:187], v[64:67], a[16:19], a[32:35]
AN 110,110
AN 111,111
M 75
WL 4
R 32
v_mfma_f32_16x16x32_f16 v[136:139], v[56:59], a[24:27], v[128:131]
PA_ 30,104,32
PA_ 31,105,33
M 85
PB_ 32,104,32
PB_ 33,105,33
v_mfma_f32_16x16x32_f16 v[140:143], v[64:67], a[24:27], v[128:131]
PA_ 34,108,36
PA_ 35,109,37
M 87
WL 4
R 34
v_mfma_f32_16x16x32_f16 v[160:163], v[56:59], a[0:3], v[128:131]
PB_ 36,108,36
PB_ 37,109,37
M 97
PF 30,0
PF 31,1
v_mfma_f32_16x16x32_f16 v[164:167], v[64:67], a[0:3], v[128:131]
PF 32,2
PF 33,3
M 99
WL 4
)"
R"(PF 34,4
PF 35,5
v_mfma_f32_16x16x32_f16 v[184:187], v[56:59], a[8:11], v[128:131]
WV 14
PF 36,6
PF 37,7
M 109
WL 0
AW 200,30
AW 201,31
v_mfma_f32_16x16x32_f16 v[188:191], v[64:67], a[8:11], v[128:131]
s_barrier
R 48
TY 114,6664
M 111
R 50
TY 118,20488
v_mfma_f32_16x16x32_f16 v[208:211], v[56:59], a[16:19], v[128:131]
mw 24
AW 202,32
AW 203,33
M 121
ad 86,240
DR 0
v_mfma_f32_16x16x32_f16 v[232:235], v[56:59], a[24:27], v[128:131]
DR 1024
AW 204,34
M 133
DR 2048
AW 205,35
v_mfma_f32_16x16x32_f16 v[212:215], v[64:67], a[16:19], v[128:131]
mw 27
AW 206,36
AW 207,37
PA_ 38,106,40
PA_ 39,107,41
PB_ 40,106,40
PB_ 41,107,41
M 123
WL 0
R 52
PA_ 42,110,44
PA_ 43,111,45
PB_ 44,110,44
PB_ 45,111,45
AN 112,114
AN 113,115
SH 114
SH 115
v_mfma_f32_16x16x32_f16 v[236:239], v[64:67], a[24:27], v[128:131]
ad 86,246
DR 0
PF 38,0
PF 39,1
PF 40,2
PF 41,3
AN 114,114
AN 115,115
AN 116,118
AN 117,119
M 135
R 54
PF 42,4
PF 43,5
PF 44,6
PF 45,7
SH 118
SH 119
AN 118,118
AN 119,119
NA 36,0,200
DR 1024
AW 208,38
AW 209,39
AW 210,40
AW 211,41
AW 212,42
NA 36,4,204
DR 2048
AW 213,43
AW 214,44
AW 215,45
NA 60,8,200
bc 49,3,66,60
TX 32,6144
TX 36,19968
NA 60,12,204
mh 0
TY 40,6656
TY 44,20480
WL 2
PA_ 30,112,32
PA_ 31,113,33
PB_ 32,112,32
PB_ 33,113,33
PA_ 34,116,36
PA_ 35,117,37
PB_ 36,116,36
PB_ 37,117,37
NA 40,0,208
ad 66,576
H 0
PF 30,0
PF 31,1
PF 32,2
PF 33,3
PF 34,4
PF 35,5
PF 36,6
PF 37,7
NA 40,4,212
WL 0
DG 1024
AW 216,30
AW 217,31
AW 218,32
AW 219,33
AW 220,34
AW 221,35
AW 222,36
AW 223,37
NA 64,8,208
bc 48,3,64,56
TY 106,9736
TY 110,23560
NA 64,12,212
mh 3
PA_ 38,114,40
PA_ 39,115,41
PB_ 40,114,40
PB_ 41,115,41
PA_ 42,118,44
PA_ 43,119,45
PB_ 44,118,44
PB_ 45,119,45
PF 38,0
PF 39,1
v_mfma_f32_16x16x32_f16 a[44:47], a[216:219], a[0:3], a[32:35]
ad 64,576
H 0
PF 40,2
PF 41,3
PF 42,4
PF 43,5
PF 44,6
PF 45,7
AW 224,38
AW 225,39
NA 44,4,220
H 1
AW 226,40
AW 227,41
AW 228,42
AW 229,43
AW 230,44
AW 231,45
v_mfma_f32_16x16x32_f16 a[68:71], a[216:219], a[8:11], a[32:35]
H 2
RS 14,256
TX 58,9216
NA 68,12,220
RS 18,320
TX 62,23040
TY 66,9728
v_mfma_f32_16x16x32_f16 a[48:51], a[224:227], a[0:3], a[32:35]
WL 5
TY 70,23552
AN 104,106
NA 48,4,228
R 56
v_mfma_f32_16x16x32_f16 a[72:75], a[224:227], a[8:11], a[32:35]
AN 105,107
SH 106
NA 72,12,228
R 58
NA 84,16,200
SH 107
AN 106,106
NA 84,20,204
WL 7
v_pk_mul_f16 v22, v14, s92
v_pk_mul_f16 v23, v15, s92
NA 88,16,208
v_pk_mul_f16 v24, v16, s92
v_pk_mul_f16 v25, v17, s92
NA 88,20,212
v_pk_mul_f16 v26, v18, s92
v_pk_mul_f16 v27, v19, s92
v_mfma_f32_16x16x32_f16 a[92:95], a[216:219], a[16:19], a[32:35]
v_pk_mul_f16 v28, v20, s92
v_pk_mul_f16 v29, v21, s92
NA 92,20,220
AN 107,107
AN 108,110
v_mfma_f32_16x16x32_f16 a[96:99], a[224:227], a[16:19], a[32:35]
AN 109,111
SH 110
NA 96,20,228
SH 111
AN 110,110
NA 108,24,200
WL 5
AN 111,111
P 0
NA 108,28,204
Q 1
NA 112,24,208
Q 3
NA 112,28,212
Q 5
v_mfma_f32_16x16x32_f16 a[116:119], a[216:219], a[24:27], a[32:35]
WV 17
P 7
PF 56,0
NA 116,28,220
WL 0
PF 57,1
PF 58,2
v_mfma_f32_16x16x32_f16 a[120:123], a[224:227], a[24:27], a[32:35]
s_barrier
R 72
TY 114,11272
NA 120,28,228
R 74
TY 118,25096
NA 132,0,200
mw 48
v_mov_b32 v54, 1
s_mov_b64 exec, 1
FS
s_mov_b64 exec, -1
NA 132,4,204
ad 86,288
DR 0
TX 74,10752
NA 136,0,208
PF 59,3
PF 60,4
TX 78,24576
NA 136,4,212
DR 1024
PF 61,5
TY 82,11264
v_mfma_f32_16x16x32_f16 a[140:143], a[216:219], a[0:3], a[32:35]
PF 62,6
PF 63,7
TY 86,25088
NA 140,4,220
DR 2048
P 8
TY 122,12808
v_mfma_f32_16x16x32_f16 a[144:147], a[224:227], a[0:3], a[32:35]
Q 9
TY 126,26632
NA 144,4,228
WL 6
R 76
TX 90,12288
NA 156,8,200
mw 51
Q 11
TX 94,26112
NA 156,12,204
ad 86,294
DR 0
TY 98,12800
NA 160,8,208
Q 13
TY 102,26624
NA 160,12,212
DR 1024
P 15
v_mfma_f32_16x16x32_f16 a[164:167], a[216:219], a[8:11], a[32:35]
PF 64,0
PF 65,1
NA 164,12,220
DR 2048
PF 66,2
v_mfma_f32_16x16x32_f16 a[168:171], a[224:227], a[8:11], a[32:35]
PF 67,3
PF 68,4
NA 168,12,228
R 78
NA 180,16,200
bc 49,4,66,60
PF 69,5
NA 180,20,204
mh 9
PF 70,6
PF 71,7
NA 184,16,208
ad 66,768
H 0
NA 184,20,212
AN 112,114
AN 113,115
v_mfma_f32_16x16x32_f16 a[188:191], a[216:219], a[16:19], a[32:35]
DG 1024
NA 188,20,220
SH 114
SH 115
v_mfma_f32_16x16x32_f16 a[192:195], a[224:227], a[16:19], a[32:35]
bc 48,4,64,56
AN 114,114
NA 192,20,228
WL 2
R 80
NV 136,24,200
mh 12
AN 115,115
AN 116,118
NV 136,28,204
ad 64,768
H 0
NV 140,24,208
AN 117,119
SH 118
NV 140,28,212
H 1
SH 119
v_mfma_f32_16x16x32_f16 v[144:147], a[216:219], a[24:27], v[128:131]
AN 118,118
AN 119,119
NV 144,28,220
H 2
P 16
v_mfma_f32_16x16x32_f16 v[148:151], a[224:227], a[24:27], v[128:131]
Q 17
NV 148,28,228
R 82
NV 160,0,200
Q 19
NV 160,4,204
Q 21
NV 164,0,208
P 23
PF 72,0
NV 164,4,212
PF 73,1
PF 74,2
v_mfma_f32_16x16x32_f16 v[168:171], a[216:219], a[0:3], v[128:131]
PF 75,3
PF 76,4
NV 168,4,220
PF 77,5
PF 78,6
v_mfma_f32_16x16x32_f16 v[172:175], a[224:227], a[0:3], v[128:131]
PF 79,7
P 24
NV 172,4,228
WL 2
Q 25
NV 184,8,200
Q 27
NV 184,12,204
Q 29
NV 188,8,208
P 31
PF 80,0
NV 188,12,212
PF 81,1
PF 82,2
v_mfma_f32_16x16x32_f16 v[192:195], a[216:219], a[8:11], v[128:131]
WV 23
PF 83,3
PF 84,4
NV 192,12,220
WL 0
PF 85,5
PF 86,6
v_mfma_f32_16x16x32_f16 v[196:199], a[224:227], a[8:11], v[128:131]
s_barrier
R 0
NV 196,12,228
R 2
NV 208,16,200
mw 72
PF 87,7
AN 120,122
NV 208,20,204
ad 86,336
DR 0
NV 232,24,200
AN 121,123
SH 122
NV 232,28,204
DR 1024
SH 123
NV 212,16,208
AN 122,122
AN 123,123
NV 212,20,212
DR 2048
AN 124,126
NV 236,24,208
AN 125,127
SH 126
NV 236,28,212
mw 75
SH 127
)"
R"(AN 126,126
v_mfma_f32_16x16x32_f16 v[216:219], a[216:219], a[16:19], v[128:131]
ad 86,342
DR 0
NV 216,20,220
AN 127,127
P 32
v_mfma_f32_16x16x32_f16 v[240:243], a[216:219], a[24:27], v[128:131]
DR 1024
P 33
NV 240,28,220
Q 34
v_mfma_f32_16x16x32_f16 v[220:223], a[224:227], a[16:19], v[128:131]
DR 2048
P 36
NV 220,20,228
R 4
v_mfma_f32_16x16x32_f16 v[244:247], a[224:227], a[24:27], v[128:131]
Q 37
NV 244,28,228
WL 2
R 6
M 0
P 39
PF 88,0
M 1
PF 89,1
PF 90,2
M 12
PF 91,3
PF 92,4
M 13
PF 93,5
PF 94,6
M 2
PF 95,7
P 40
M 3
Q 41
M 14
Q 43
M 15
Q 45
M 4
P 47
PF 96,0
M 5
PF 97,1
PF 98,2
M 16
PF 99,3
PF 100,4
M 17
PF 101,5
PF 102,6
M 6
PF 103,7
M 7
RS 14,384
RS 18,448
M 18
M 19
v_mfma_f32_16x16x32_f16 a[52:55], v[88:91], a[0:3], a[32:35]
v_lshrrev_b32 v9, 8, v0
v_bfe_u32 v8, v0, 4, 4
M 9
v_lshlrev_b32 v10, 2, v9
v_mfma_f32_16x16x32_f16 a[76:79], v[88:91], a[8:11], a[32:35]
s_mov_b32 s42, 0xcb527610
M 21
s_movk_i32 s43, 0xc00
WL 0
v_pk_mul_f16 v22, v14, s92
v_pk_mul_f16 v23, v15, s92
v_mfma_f32_16x16x32_f16 a[56:59], v[96:99], a[0:3], a[32:35]
v_pk_mul_f16 v24, v16, s92
v_pk_mul_f16 v25, v17, s92
M 11
R 8
v_mfma_f32_16x16x32_f16 a[80:83], v[96:99], a[8:11], a[32:35]
v_pk_mul_f16 v26, v18, s92
v_pk_mul_f16 v27, v19, s92
M 23
R 10
M 24
v_pk_mul_f16 v28, v20, s92
v_pk_mul_f16 v29, v21, s92
M 25
v_lshlrev_b32 v11, 3, v8
v_bfe_u32 v3, s42, v10, 4
M 26
v_add_u32 v10, 16, v10
v_mad_u32_u24 v3, v3, s43, v11
M 27
v_bfe_u32 v48, s42, v10, 4
v_add_u32 v10, 1, v9
M 28
v_mad_u32_u24 v48, v48, s43, v11
v_and_b32 v10, 2, v10
M 29
v_sub_u32 v10, 1, v10
v_mul_i32_i24 v49, s43, v10
M 30
M 31
v_mfma_f32_16x16x32_f16 a[100:103], v[88:91], a[16:19], a[32:35]
M 33
v_mfma_f32_16x16x32_f16 a[104:107], v[96:99], a[16:19], a[32:35]
M 35
M 36
M 37
M 38
M 39
M 40
M 41
M 42
M 43
WV 18
v_mfma_f32_16x16x32_f16 a[124:127], v[88:91], a[24:27], a[32:35]
WL 0
M 45
s_barrier
R 24
TY 106,520
v_mfma_f32_16x16x32_f16 a[128:131], v[96:99], a[24:27], a[32:35]
TY 110,14344
TX 32,0
TX 36,13824
M 47
R 26
TY 40,512
M 48
TY 44,14336
TY 114,2056
TY 118,15880
M 49
M 50
M 51
M 52
WL 9
AN 104,106
AN 105,107
M 53
bc 49,5,66,60
SH 106
M 54
mh 18
SH 107
AN 106,106
M 55
ad 66,960
H 0
v_mfma_f32_16x16x32_f16 a[148:151], v[88:91], a[0:3], a[32:35]
WL 8
AN 107,107
AN 108,110
M 57
AN 109,111
SH 110
v_mfma_f32_16x16x32_f16 a[152:155], v[96:99], a[0:3], a[32:35]
SH 111
AN 110,110
M 59
R 28
M 60
DG 1024
M 61
WL 5
AN 111,111
PA_ 30,104,32
M 62
PA_ 31,105,33
PB_ 32,104,32
M 63
PB_ 33,105,33
PA_ 34,108,36
M 64
PA_ 35,109,37
PB_ 36,108,36
M 65
PB_ 37,109,37
PF 30,0
M 66
v_lshlrev_b32 v55, 4, v0
PF 31,1
M 67
FL
PF 32,2
v_mfma_f32_16x16x32_f16 a[172:175], v[88:91], a[8:11], a[32:35]
PF 33,3
PF 34,4
M 69
PF 35,5
PF 36,6
v_mfma_f32_16x16x32_f16 a[176:179], v[96:99], a[8:11], a[32:35]
PF 37,7
AW 200,30
M 71
R 30
M 72
AW 201,31
AW 202,32
M 73
AW 203,33
AW 204,34
M 74
AW 205,35
AW 206,36
M 75
WL 2
AW 207,37
PA_ 38,106,40
M 76
PA_ 39,107,41
PB_ 40,106,40
TX 32,1536
M 77
PB_ 41,107,41
PA_ 42,110,44
TX 36,15360
M 78
PA_ 43,111,45
PB_ 44,110,44
M 79
PB_ 45,111,45
PF 38,0
v_mfma_f32_16x16x32_f16 a[196:199], v[88:91], a[16:19], a[32:35]
PF 39,1
PF 40,2
M 81
PF 41,3
PF 42,4
v_mfma_f32_16x16x32_f16 v[132:135], v[96:99], a[16:19], v[128:131]
PF 43,5
PF 44,6
M 83
R 32
M 84
PF 45,7
AW 208,38
M 85
bc 48,5,64,56
AW 209,39
M 86
mh 21
AW 210,40
AW 211,41
M 87
ad 64,960
H 0
M 88
AW 212,42
AW 213,43
M 89
AW 214,44
AW 215,45
M 90
TY 40,2048
TY 44,15872
TY 122,3592
M 91
H 1
AN 112,114
TY 126,17416
v_mfma_f32_16x16x32_f16 v[152:155], v[88:91], a[24:27], v[128:131]
AN 113,115
SH 114
M 93
SH 115
AN 114,114
v_mfma_f32_16x16x32_f16 v[156:159], v[96:99], a[24:27], v[128:131]
AN 115,115
AN 116,118
M 95
R 34
M 96
H 2
AN 117,119
M 97
SH 118
SH 119
M 98
AN 118,118
AN 119,119
M 99
WL 6
PA_ 30,112,32
PA_ 31,113,33
M 100
PB_ 32,112,32
PB_ 33,113,33
M 101
PA_ 34,116,36
PA_ 35,117,37
M 102
PB_ 36,116,36
PB_ 37,117,37
M 103
PF 30,0
PF 31,1
v_mfma_f32_16x16x32_f16 v[176:179], v[88:91], a[0:3], v[128:131]
PF 32,2
PF 33,3
M 105
PF 34,4
PF 35,5
v_mfma_f32_16x16x32_f16 v[180:183], v[96:99], a[0:3], v[128:131]
PF 36,6
PF 37,7
M 107
AW 216,30
AW 217,31
M 108
AW 218,32
AW 219,33
M 109
AW 220,34
AW 221,35
M 110
WV 3
v_cmp_eq_u32 vcc, 0, v54
AW 222,36
M 111
WL 0
AW 223,37
PA_ 38,114,40
M 112
PA_ 39,115,41
PB_ 40,114,40
TX 32,3072
M 113
FB
PB_ 41,115,41
PA_ 42,118,44
TX 36,16896
M 114
PA_ 43,119,45
PB_ 44,118,44
M 115
PB_ 45,119,45
PF 38,0
v_mfma_f32_16x16x32_f16 v[200:203], v[88:91], a[8:11], v[128:131]
PF 39,1
PF 40,2
M 117
s_barrier
R 48
v_mfma_f32_16x16x32_f16 v[204:207], v[96:99], a[8:11], v[128:131]
mw 0
PF 41,3
PF 42,4
M 119
R 50
M 120
ad 84,384
W 0
M 121
W 1
PF 43,5
M 132
W 2
PF 44,6
M 133
mw 3
PF 45,7
AW 224,38
M 122
ad 84,390
W 0
M 123
W 1
AW 225,39
M 134
W 2
AW 226,40
M 135
mw 24
AW 227,41
AW 228,42
M 124
ad 84,432
W 0
M 125
W 1
AW 229,43
M 136
W 2
AW 230,44
M 137
mw 27
AW 231,45
AN 120,122
M 126
ad 84,438
W 0
TY 40,3584
M 127
W 1
TY 44,17408
M 138
W 2
)"
R"(AN 121,123
M 139
SH 122
SH 123
v_mfma_f32_16x16x32_f16 v[224:227], v[88:91], a[16:19], v[128:131]
AN 122,122
AN 123,123
M 129
AN 124,126
AN 125,127
v_mfma_f32_16x16x32_f16 v[248:251], v[88:91], a[24:27], v[128:131]
SH 126
SH 127
M 141
AN 126,126
AN 127,127
v_mfma_f32_16x16x32_f16 v[228:231], v[96:99], a[16:19], v[128:131]
WL 2
PA_ 30,120,32
PA_ 31,121,33
M 131
R 52
v_mfma_f32_16x16x32_f16 v[252:255], v[96:99], a[24:27], v[128:131]
PB_ 32,120,32
PB_ 33,121,33
M 143
R 54
NA 36,0,200
PA_ 34,124,36
PA_ 35,125,37
NA 36,4,204
PB_ 36,124,36
PB_ 37,125,37
NA 60,8,200
PF 30,0
PF 31,1
NA 60,12,204
PF 32,2
PF 33,3
NA 40,0,208
PF 34,4
PF 35,5
NA 40,4,212
PF 36,6
PF 37,7
NA 64,8,208
AW 232,30
AW 233,31
NA 64,12,212
AW 234,32
AW 235,33
NA 44,0,216
AW 236,34
AW 237,35
NA 44,4,220
WL 4
AW 238,36
AW 239,37
PA_ 38,122,40
NA 68,8,216
PA_ 39,123,41
PB_ 40,122,40
PB_ 41,123,41
NA 68,12,220
PA_ 42,126,44
PA_ 43,127,45
PB_ 44,126,44
NA 48,0,224
PB_ 45,127,45
PF 38,0
PF 39,1
NA 48,4,228
PF 40,2
PF 41,3
PF 42,4
NA 72,8,224
PF 43,5
PF 44,6
PF 45,7
NA 72,12,228
AW 240,38
AW 241,39
AW 242,40
NA 52,0,232
AW 243,41
AW 244,42
AW 245,43
NA 52,4,236
AW 246,44
AW 247,45
NA 76,8,232
NA 76,12,236
NA 56,0,240
NA 56,4,244
R 56
NA 80,8,240
NA 80,12,244
WL 2
R 58
NA 84,16,200
NA 84,20,204
NA 88,16,208
NA 88,20,212
NA 92,16,216
NA 92,20,220
NA 96,16,224
NA 96,20,228
NA 100,16,232
NA 100,20,236
NA 104,16,240
NA 104,20,244
NA 108,24,200
NA 108,28,204
NA 112,24,208
NA 112,28,212
NA 116,24,216
NA 116,28,220
NA 120,24,224
NA 120,28,228
NA 124,24,232
WL 0
NA 124,28,236
s_barrier
R 72
TY 106,5128
NA 128,24,240
mw 48
TY 110,18952
TX 58,4608
TX 62,18432
NA 128,28,244
R 74
TY 66,5120
NA 132,0,200
ad 84,480
W 0
TY 70,18944
NA 132,4,204
v_lshrrev_b32 v8, 1, v0
s_mov_b64 exec, s[76:77]
global_load_dwordx2 v[6:7], v8, s[78:79] sc1
s_mov_b64 exec, -1
NA 136,0,208
TY 114,6664
TY 118,20488
TX 74,6144
NA 136,4,212
WL 7
TX 78,19968
TY 82,6656
TY 86,20480
NA 140,0,216
W 1
AN 104,106
TY 122,8200
NA 140,4,220
AN 105,107
SH 106
TY 126,22024
NA 144,0,224
SH 107
AN 106,106
TX 90,7680
NA 144,4,228
AN 107,107
AN 108,110
TX 94,21504
NA 148,0,232
W 2
AN 109,111
TY 98,8192
NA 148,4,236
WL 9
SH 110
SH 111
TY 102,22016
NA 152,0,240
AN 110,110
AN 111,111
NA 152,4,244
R 76
NA 156,8,200
mw 51
Q 0
NA 156,12,204
ad 84,486
W 0
NA 160,8,208
Q 2
NA 160,12,212
Q 4
NA 164,8,216
Q 6
NA 164,12,220
W 1
P 8
NA 168,8,224
Q 9
NA 168,12,228
Q 11
NA 172,8,232
Q 13
NA 172,12,236
W 2
P 15
NA 176,8,240
AN 112,114
AN 113,115
NA 176,12,244
R 78
NA 180,16,200
SH 114
SH 115
NA 180,20,204
bc 49,6,66,60
AN 114,114
NA 184,16,208
mh 0
AN 115,115
AN 116,118
NA 184,20,212
ad 66,1152
H 0
NA 188,16,216
AN 117,119
SH 118
NA 188,20,220
SH 119
AN 118,118
NA 192,16,224
WL 2
AN 119,119
P 16
NA 192,20,228
DG 1024
NA 196,16,232
Q 17
NA 196,20,236
Q 19
NV 132,16,240
Q 21
NV 132,20,244
R 80
NV 136,24,200
bc 48,6,64,56
P 23
NV 136,28,204
mh 3
Q 24
NV 140,24,208
ad 64,1152
H 0
NV 140,28,212
Q 26
NV 144,24,216
Q 28
NV 144,28,220
Q 30
NV 148,24,224
H 1
AN 120,122
NV 148,28,228
AN 121,123
SH 122
NV 152,24,232
SH 123
AN 122,122
NV 152,28,236
AN 123,123
AN 124,126
NV 156,24,240
H 2
AN 125,127
NV 156,28,244
R 82
NV 160,0,200
SH 126
SH 127
NV 160,4,204
AN 126,126
AN 127,127
NV 164,0,208
Q 32
NV 164,4,212
Q 34
NV 168,0,216
Q 36
NV 168,4,220
Q 38
NV 172,0,224
Q 40
NV 172,4,228
Q 42
NV 176,0,232
Q 44
NV 176,4,236
Q 46
NV 180,0,240
NV 180,4,244
WL 2
NV 184,8,200
NV 184,12,204
NV 188,8,208
NV 188,12,212
NV 192,8,216
NV 192,12,220
NV 196,8,224
NV 196,12,228
WV 18
NV 200,8,232
WL 0
NV 200,12,236
s_barrier
R 0
NV 204,8,240
mw 72
NV 204,12,244
R 2
NV 208,16,200
ad 84,528
W 0
NV 208,20,204
NV 232,24,200
NV 232,28,204
NV 212,16,208
W 1
NV 212,20,212
NV 236,24,208
NV 236,28,212
NV 216,16,216
W 2
NV 216,20,220
ae 44,70,0
NV 240,24,216
s_bitcmp1_b32 s50, 0
s_cselect_b32 s51, 24576, 0
v_add_u32 v47, s51, v3
NV 240,28,220
XL 14,0
NV 220,16,224
mw 75
NV 220,20,228
ad 84,534
W 0
NV 244,24,224
XL 16,768
NV 244,28,228
NV 224,16,232
NV 224,20,236
W 1
NV 248,24,232
NV 248,28,236
NV 228,16,240
XL 18,1536
NV 228,20,244
R 4
NV 252,24,240
W 2
NV 252,28,244
WL 2
R 6
M 0
M 1
M 12
XL 20,2304
M 13
M 2
M 3
WV 20
v_or_b32 v6, v6, v7
s_mov_b64 exec, s[76:77]
v_cmp_ne_u32 %[flg], 0, v6
s_mov_b64 exec, -1
M 14
M 15
M 4
M 5
M 16
M 17
M 6
M 7
M 18
M 19
M 8
M 9
M 20
M 21
M 10
M 11
R 8
M 22
M 23
WL 4
R 10
M 24
M 25
M 26
M 27
M 28
M 29
)"
R"(M 30
M 31
M 32
M 33
M 34
M 35
WL 4
M 36
M 37
M 38
M 39
M 40
M 41
M 42
M 43
WV 15
M 44
WL 0
M 45
s_barrier
R 24
M 46
mw 0
M 47
R 26
M 48
ad 84,576
W 0
M 49
TY 106,9736
TY 110,23560
M 50
M 51
W 1
M 52
M 53
M 54
W 2
M 55
M 56
M 57
mw 3
M 58
ad 84,582
W 0
M 59
R 28
M 60
WL 3
AN 104,106
AN 105,107
M 61
W 1
SH 106
M 62
SH 107
AN 106,106
M 63
WL 2
AN 107,107
AN 108,110
M 64
W 2
AN 109,111
M 65
SH 110
SH 111
M 66
AN 110,110
AN 111,111
M 67
bc 49,7,66,60
TY 114,11272
M 68
mh 9
TY 118,25096
M 69
ad 66,1344
H 0
M 70
M 71
R 30
M 72
DG 1024
M 73
M 74
M 75
bc 48,7,64,56
TY 122,12808
M 76
mh 12
TY 126,26632
M 77
ad 64,1344
H 0
M 78
WL 5
AN 112,114
AN 113,115
M 79
SH 114
SH 115
M 80
H 1
AN 114,114
M 81
WL 4
AN 115,115
AN 116,118
M 82
AN 117,119
SH 118
M 83
R 32
M 84
H 2
SH 119
M 85
AN 118,118
AN 119,119
M 86
WL 3
AN 120,122
AN 121,123
M 87
s_load_dwordx4 s[88:91], %[pp], 0xc0
s_load_dwordx2 s[86:87], %[pp], 0xd0
SH 122
M 88
SH 123
AN 122,122
M 89
WL 2
AN 123,123
AN 124,126
M 90
AN 125,127
SH 126
M 91
SH 127
AN 126,126
M 92
AN 127,127
M 93
M 94
M 95
R 34
M 96
M 97
M 98
M 99
M 100
M 101
M 102
M 103
M 104
M 105
M 106
M 107
R 48
M 108
M 109
M 110
M 111
M 112
M 113
M 114
M 115
M 116
s_waitcnt lgkmcnt(0)
M 117
M 118
M 119
R 50
M 120
v_mov_b32 v54, v52
M 121
M 132
M 133
TX 58,9216
TX 62,23040
M 122
v_permlane32_swap_b32 v52, v54
M 123
M 134
M 135
TY 66,9728
TY 70,23552
M 124
v_add_f32 v52, v52, v54
v_mov_b32 v54, v52
M 125
M 136
ae 44,70,0
M 137
TX 74,10752
TX 78,24576
M 126
s_bfe_u32 s51, s50, 0x10001
WL 4
Q 0
M 127
Q 2
M 138
Q 4
M 139
TY 82,11264
TY 86,25088
M 128
Q 6
M 129
WL 4
Q 8
M 140
Q 10
M 141
TX 90,12288
TX 94,26112
M 130
Q 12
M 131
R 52
M 142
Q 14
M 143
TY 98,12800
TY 102,26624
M 0
R 54
M 1
WL 10
Q 16
M 12
Q 18
M 13
Q 20
M 2
Q 22
M 3
WL 8
Q 24
M 14
Q 26
M 15
Q 28
M 4
Q 30
M 5
WL 6
Q 32
M 16
Q 34
M 17
Q 36
M 6
Q 38
M 7
WL 2
Q 40
M 18
Q 42
M 19
Q 44
M 8
Q 46
M 9
v_permlane16_swap_b32 v52, v54
v_mad_i32_i24 v47, v49, s51, v48
M 20
XL 22,0
M 21
XL 24,768
M 10
XL 26,1536
v_add_f32 v52, v52, v54
M 11
R 56
M 22
XL 28,2304
M 23
R 58
M 24
v_permlane16_swap_b32 v52, v53
v_mov_b32 v54, s88
M 25
v_mov_b32 v53, s89
M 26
M 27
M 28
v_permlane16_swap_b32 v54, v53
v_bfe_u32 v53, v0, 2, 6
M 29
M 30
M 31
v_add_u32 v54, v53, v54
M 32
M 33
WV 9
M 34
WL 0
FP
M 35
M 36
M 37
M 38
M 39
M 40
UL 6,14
UH 7,14
M 41
AR 8,36
AR 9,60
M 42
AD 8,6
AD 9,7
M 43
AW 36,8
AW 60,9
M 44
UL 6,15
UH 7,15
M 45
FR
AR 8,84
M 46
AR 9,108
AD 8,6
M 47
AD 9,7
AW 84,8
M 48
AW 108,9
UL 6,16
M 49
UH 7,16
AR 8,37
M 50
AR 9,61
AD 8,6
M 51
AD 9,7
AW 37,8
M 52
AW 61,9
UL 6,17
M 53
WL 0
FQ 4
UH 7,17
M 54
AR 8,85
AR 9,109
M 55
AD 8,6
AD 9,7
M 56
FZ 4
R 72
M 57
mw 24
R 74
M 58
TY 106,520
TY 110,14344
M 59
R 76
M 60
ad 84,624
W 0
M 61
AW 85,8
AW 109,9
M 62
UL 6,18
UH 7,18
M 63
W 1
AR 8,38
M 64
AR 9,62
AD 8,6
M 65
AD 9,7
AW 38,8
M 66
W 2
AW 62,9
M 67
WL 3
AN 104,106
AN 105,107
M 68
SH 106
SH 107
M 69
mw 27
AN 106,106
AN 107,107
M 70
WL 2
)"
R"(AN 108,110
AN 109,111
M 71
R 78
M 72
SH 110
SH 111
M 73
AN 110,110
AN 111,111
M 74
TY 114,2056
TY 118,15880
M 75
ad 84,630
W 0
M 76
UL 6,19
UH 7,19
M 77
AR 8,86
AR 9,110
M 78
W 1
AD 8,6
M 79
AD 9,7
AW 86,8
M 80
AW 110,9
UL 6,20
M 81
W 2
UH 7,20
M 82
AR 8,39
AR 9,63
M 83
WL 1
R 80
M 84
mw 48
AN 112,114
AN 113,115
M 85
SH 114
SH 115
M 86
AN 114,114
AN 115,115
M 87
WL 2
AN 116,118
AN 117,119
M 88
SH 118
SH 119
M 89
AN 118,118
AN 119,119
M 90
TY 122,3592
TY 126,17416
M 91
ad 84,672
M 92
W 0
AD 8,6
M 93
AD 9,7
AW 39,8
M 94
AW 63,9
UL 6,21
M 95
R 82
M 96
W 1
UH 7,21
M 97
AR 8,87
AR 9,111
M 98
AD 8,6
AD 9,7
M 99
WL 3
AN 120,122
AN 121,123
M 100
SH 122
SH 123
M 101
AN 122,122
AN 123,123
M 102
WL 2
AN 124,126
AN 125,127
M 103
SH 126
SH 127
M 104
AN 126,126
AN 127,127
M 105
W 2
AW 87,8
M 106
AW 111,9
M 107
R 0
M 108
mw 51
M 109
ad 84,678
W 0
M 110
M 111
M 112
W 1
M 113
M 114
M 115
W 2
M 116
M 117
M 118
bc 49,8,66,60
M 119
WL 2
R 2
M 120
mh 18
s_bitcmp1_b32 s87, 0
s_cselect_b32 exec_lo, 0xffffff, 0
s_mov_b32 exec_hi, 0
global_atomic_add_f32 v54, v52, s[90:91]
s_mov_b64 exec, -1
M 121
ad 66,1536
H 0
M 132
M 133
TX 58,0
TX 62,13824
M 122
DG 1024
M 123
M 134
M 135
TY 66,512
TY 70,14336
M 124
bc 48,8,64,56
M 125
mh 21
M 136
ad 64,1536
H 0
M 137
TX 74,1536
TX 78,15360
M 126
WL 4
Q 0
M 127
Q 2
M 138
Q 4
M 139
TY 82,2048
TY 86,15872
M 128
Q 6
M 129
WL 4
Q 8
M 140
Q 10
M 141
TX 90,3072
TX 94,16896
M 130
Q 12
M 131
R 4
M 142
Q 14
M 143
TY 98,3584
TY 102,17408
M 0
R 6
M 1
WL 10
Q 16
M 12
Q 18
M 13
Q 20
M 2
Q 22
M 3
WL 8
Q 24
M 14
Q 26
M 15
Q 28
M 4
Q 30
M 5
WL 6
Q 32
M 16
Q 34
M 17
Q 36
M 6
Q 38
M 7
WL 2
Q 40
M 18
Q 42
M 19
Q 44
M 8
Q 46
M 9
H 1
M 20
ae 44,68,0
M 21
s_bitcmp1_b32 s50, 2
s_cselect_b32 s51, 24576, 0
v_add_u32 v47, s51, v3
M 10
H 2
XL 30,0
M 11
R 8
M 22
XL 32,768
M 23
R 10
M 24
XL 34,1536
M 25
XL 36,2304
M 26
M 27
M 28
M 29
M 30
M 31
M 32
M 33
WV 10
M 34
WL 0
FP
M 35
M 36
M 37
s_bitcmp1_b32 s87, 1
s_cselect_b32 exec_lo, 0xffffff, 0
s_mov_b32 exec_hi, 0
global_atomic_add_f32 v54, v52, s[90:91]
s_mov_b64 exec, -1
M 38
M 39
M 40
M 41
M 42
UL 6,22
UH 7,22
M 43
AR 8,40
AR 9,64
M 44
AD 8,6
AD 9,7
M 45
FR
AW 40,8
M 46
AW 64,9
UL 6,23
M 47
UH 7,23
AR 8,88
M 48
AR 9,112
AD 8,6
M 49
AD 9,7
AW 88,8
M 50
AW 112,9
UL 6,24
M 51
UH 7,24
AR 8,41
M 52
AR 9,65
AD 8,6
M 53
WL 0
FQ 8
AD 9,7
M 54
AW 41,8
AW 65,9
M 55
UL 6,25
UH 7,25
M 56
FZ 8
R 24
M 57
mw 72
R 26
M 58
TY 106,5128
TY 110,18952
M 59
R 28
M 60
ad 84,720
W 0
M 61
AR 8,89
AR 9,113
M 62
AD 8,6
AD 9,7
M 63
W 1
AW 89,8
M 64
AW 113,9
UL 6,26
M 65
UH 7,26
AR 8,42
M 66
W 2
AR 9,66
M 67
WL 3
AN 104,106
AN 105,107
M 68
SH 106
SH 107
M 69
mw 75
AN 106,106
AN 107,107
M 70
WL 2
AN 108,110
AN 109,111
M 71
R 30
M 72
SH 110
SH 111
M 73
AN 110,110
AN 111,111
M 74
TY 114,6664
TY 118,20488
M 75
ad 84,726
W 0
M 76
AD 8,6
AD 9,7
M 77
AW 42,8
AW 66,9
M 78
W 1
UL 6,27
M 79
UH 7,27
AR 8,90
M 80
AR 9,114
AD 8,6
M 81
W 2
AD 9,7
M 82
AW 90,8
AW 114,9
)"
R"(M 83
WL 1
R 32
M 84
mw 0
AN 112,114
AN 113,115
M 85
SH 114
SH 115
M 86
AN 114,114
AN 115,115
M 87
WL 2
AN 116,118
AN 117,119
M 88
SH 118
SH 119
M 89
AN 118,118
AN 119,119
M 90
TY 122,8200
TY 126,22024
M 91
ad 84,768
M 92
W 0
UL 6,28
M 93
UH 7,28
AR 8,43
M 94
AR 9,67
AD 8,6
M 95
R 34
M 96
W 1
AD 9,7
M 97
AW 43,8
AW 67,9
M 98
UL 6,29
UH 7,29
M 99
WL 3
AN 120,122
AN 121,123
M 100
SH 122
SH 123
M 101
AN 122,122
AN 123,123
M 102
WL 2
AN 124,126
AN 125,127
M 103
SH 126
SH 127
M 104
AN 126,126
AN 127,127
M 105
W 2
AR 8,91
M 106
AR 9,115
AD 8,6
M 107
R 48
M 108
mw 3
AD 9,7
AW 91,8
M 109
ad 84,774
W 0
M 110
AW 115,9
M 111
M 112
W 1
M 113
M 114
M 115
W 2
M 116
M 117
M 118
bc 49,9,66,60
M 119
WL 2
R 50
M 120
mh 0
s_bitcmp1_b32 s87, 2
s_cselect_b32 exec_lo, 0xffffff, 0
s_mov_b32 exec_hi, 0
global_atomic_add_f32 v54, v52, s[90:91]
s_mov_b64 exec, -1
M 121
ad 66,1728
H 0
M 132
M 133
TX 58,4608
TX 62,18432
M 122
DG 1024
M 123
M 134
M 135
TY 66,5120
TY 70,18944
M 124
bc 48,9,64,56
M 125
mh 3
M 136
ad 64,1728
H 0
M 137
TX 74,6144
TX 78,19968
M 126
WL 4
Q 0
M 127
Q 2
M 138
Q 4
M 139
TY 82,6656
TY 86,20480
M 128
Q 6
M 129
WL 4
Q 8
M 140
Q 10
M 141
TX 90,7680
TX 94,21504
M 130
Q 12
M 131
R 52
M 142
Q 14
M 143
TY 98,8192
TY 102,22016
M 0
R 54
M 1
WL 10
Q 16
M 12
Q 18
M 13
Q 20
M 2
Q 22
M 3
WL 8
Q 24
M 14
Q 26
M 15
Q 28
M 4
Q 30
M 5
WL 6
Q 32
M 16
Q 34
M 17
Q 36
M 6
Q 38
M 7
WL 2
Q 40
M 18
Q 42
M 19
Q 44
M 8
Q 46
M 9
H 1
M 20
ae 44,68,0
M 21
s_bfe_u32 s51, s50, 0x10003
v_mad_i32_i24 v47, v49, s51, v48
XL 38,0
M 10
H 2
XL 40,768
M 11
R 56
M 22
XL 42,1536
M 23
R 58
M 24
XL 44,2304
M 25
M 26
M 27
M 28
M 29
M 30
M 31
M 32
M 33
WV 10
M 34
WL 0
FP
M 35
M 36
M 37
s_bitcmp1_b32 s87, 3
s_cselect_b32 exec_lo, 0xffffff, 0
s_mov_b32 exec_hi, 0
global_atomic_add_f32 v54, v52, s[90:91]
s_mov_b64 exec, -1
M 38
M 39
M 40
M 41
M 42
M 43
M 44
UL 6,30
UH 7,30
M 45
FR
AR 8,44
M 46
AR 9,68
AD 8,6
M 47
AD 9,7
AW 44,8
M 48
AW 68,9
UL 6,31
M 49
UH 7,31
AR 8,92
M 50
AR 9,116
AD 8,6
M 51
AD 9,7
AW 92,8
M 52
AW 116,9
UL 6,32
M 53
WL 0
FQ 12
UH 7,32
M 54
AR 8,45
AR 9,69
M 55
AD 8,6
AD 9,7
M 56
FZ 12
R 72
M 57
mw 24
R 74
M 58
TY 106,9736
TY 110,23560
M 59
R 76
M 60
ad 84,816
W 0
M 61
AW 45,8
AW 69,9
M 62
UL 6,33
UH 7,33
M 63
W 1
AR 8,93
M 64
AR 9,117
AD 8,6
M 65
AD 9,7
AW 93,8
M 66
W 2
AW 117,9
M 67
WL 3
AN 104,106
AN 105,107
M 68
SH 106
SH 107
M 69
mw 27
AN 106,106
AN 107,107
M 70
WL 2
AN 108,110
AN 109,111
M 71
R 78
M 72
SH 110
SH 111
M 73
AN 110,110
AN 111,111
M 74
TY 114,11272
TY 118,25096
M 75
ad 84,822
W 0
M 76
UL 6,34
UH 7,34
M 77
AR 8,46
AR 9,70
M 78
W 1
AD 8,6
M 79
AD 9,7
AW 46,8
M 80
AW 70,9
UL 6,35
M 81
W 2
UH 7,35
M 82
AR 8,94
AR 9,118
M 83
WL 1
R 80
M 84
mw 48
AN 112,114
AN 113,115
M 85
SH 114
SH 115
M 86
AN 114,114
AN 115,115
M 87
WL 2
AN 116,118
AN 117,119
M 88
SH 118
SH 119
M 89
AN 118,118
AN 119,119
M 90
TY 122,12808
TY 126,26632
M 91
ad 84,864
M 92
W 0
AD 8,6
M 93
AD 9,7
AW 94,8
M 94
AW 118,9
UL 6,36
M 95
)"
R"(R 82
M 96
W 1
UH 7,36
M 97
AR 8,47
AR 9,71
M 98
AD 8,6
AD 9,7
M 99
WL 3
AN 120,122
AN 121,123
M 100
SH 122
SH 123
M 101
AN 122,122
AN 123,123
M 102
WL 2
AN 124,126
AN 125,127
M 103
SH 126
SH 127
M 104
AN 126,126
AN 127,127
M 105
W 2
AW 47,8
M 106
AW 71,9
UL 6,37
M 107
R 0
M 108
mw 51
UH 7,37
AR 8,95
M 109
ad 84,870
W 0
M 110
AR 9,119
AD 8,6
M 111
AD 9,7
AW 95,8
M 112
W 1
AW 119,9
M 113
M 114
M 115
W 2
M 116
M 117
M 118
bc 49,10,66,60
M 119
WL 2
R 2
M 120
mh 9
s_bitcmp1_b32 s87, 4
s_cselect_b32 exec_lo, 0xffffff, 0
s_mov_b32 exec_hi, 0
global_atomic_add_f32 v54, v52, s[90:91]
s_mov_b64 exec, -1
M 121
ad 66,1920
H 0
M 132
M 133
TX 58,9216
TX 62,23040
M 122
DG 1024
M 123
M 134
M 135
TY 66,9728
TY 70,23552
M 124
bc 48,10,64,56
M 125
mh 12
M 136
ad 64,1920
H 0
M 137
TX 74,10752
TX 78,24576
M 126
WL 4
Q 0
M 127
Q 2
M 138
Q 4
M 139
TY 82,11264
TY 86,25088
M 128
Q 6
M 129
WL 4
Q 8
M 140
Q 10
M 141
TX 90,12288
TX 94,26112
M 130
Q 12
M 131
R 4
M 142
Q 14
M 143
TY 98,12800
TY 102,26624
M 0
R 6
M 1
WL 10
Q 16
M 12
Q 18
M 13
Q 20
M 2
Q 22
M 3
WL 8
Q 24
M 14
Q 26
M 15
Q 28
M 4
Q 30
M 5
WL 6
Q 32
M 16
Q 34
M 17
Q 36
M 6
Q 38
M 7
WL 2
Q 40
M 18
Q 42
M 19
Q 44
M 8
Q 46
M 9
H 1
M 20
ae 44,68,96
M 21
s_bitcmp1_b32 s50, 4
s_cselect_b32 s51, 24576, 0
v_add_u32 v47, s51, v3
M 10
H 2
XL 14,0
M 11
R 8
M 22
XL 16,768
M 23
R 10
PA 12288
DF
M 24
XL 18,1536
M 25
XL 20,2304
M 26
M 27
M 28
M 29
M 30
M 31
M 32
M 33
WV 11
M 34
WL 0
FP
M 35
M 36
M 37
s_bitcmp1_b32 s87, 5
s_cselect_b32 exec_lo, 0xffffff, 0
s_mov_b32 exec_hi, 0
global_atomic_add_f32 v54, v52, s[90:91]
s_mov_b64 exec, -1
M 38
M 39
M 40
M 41
M 42
M 43
M 44
M 45
FR
M 46
UL 6,38
UH 7,38
M 47
AR 8,48
AR 9,72
M 48
AD 8,6
AD 9,7
M 49
AW 48,8
AW 72,9
M 50
UL 6,39
UH 7,39
M 51
AR 8,96
AR 9,120
M 52
AD 8,6
AD 9,7
M 53
WL 0
FQ 16
AW 96,8
M 54
AW 120,9
UL 6,40
M 55
UH 7,40
AR 8,49
M 56
FZ 16
R 24
M 57
mw 72
R 26
M 58
TY 106,520
TY 110,14344
M 59
R 28
M 60
ad 84,912
W 0
M 61
AR 9,73
AD 8,6
M 62
AD 9,7
AW 49,8
M 63
W 1
AW 73,9
M 64
UL 6,41
UH 7,41
M 65
AR 8,97
AR 9,121
M 66
W 2
AD 8,6
M 67
WL 3
AN 104,106
AN 105,107
M 68
SH 106
SH 107
M 69
mw 75
AN 106,106
AN 107,107
M 70
WL 2
AN 108,110
AN 109,111
M 71
R 30
M 72
SH 110
SH 111
M 73
AN 110,110
AN 111,111
M 74
TY 114,2056
TY 118,15880
M 75
ad 84,918
W 0
M 76
AD 9,7
AW 97,8
M 77
AW 121,9
UL 6,42
M 78
W 1
UH 7,42
M 79
AR 8,50
AR 9,74
M 80
AD 8,6
AD 9,7
M 81
W 2
AW 50,8
M 82
AW 74,9
UL 6,43
M 83
WL 1
R 32
M 84
mw 0
AN 112,114
AN 113,115
M 85
SH 114
SH 115
M 86
AN 114,114
AN 115,115
M 87
WL 2
AN 116,118
AN 117,119
M 88
SH 118
SH 119
M 89
AN 118,118
AN 119,119
M 90
TY 122,3592
TY 126,17416
M 91
ad 84,960
M 92
W 0
UH 7,43
M 93
AR 8,98
AR 9,122
M 94
AD 8,6
AD 9,7
M 95
R 34
M 96
W 1
AW 98,8
M 97
AW 122,9
UL 6,44
M 98
UH 7,44
AR 8,51
M 99
WL 3
AN 120,122
AN 121,123
M 100
SH 122
SH 123
M 101
AN 122,122
AN 123,123
M 102
WL 2
AN 124,126
AN 125,127
M 103
SH 126
SH 127
M 104
AN 126,126
AN 127,127
M 105
W 2
AR 9,75
M 106
)"
R"(AD 8,6
AD 9,7
M 107
R 48
M 108
mw 3
AW 51,8
AW 75,9
M 109
ad 84,966
W 0
M 110
UL 6,45
UH 7,45
M 111
AR 8,99
AR 9,123
M 112
W 1
AD 8,6
M 113
AD 9,7
AW 99,8
M 114
AW 123,9
M 115
W 2
M 116
M 117
M 118
bc 49,11,66,60
M 119
WL 2
R 50
M 120
mh 18
s_bitcmp1_b32 s87, 6
s_cselect_b32 exec_lo, 0xffffff, 0
s_mov_b32 exec_hi, 0
global_atomic_add_f32 v54, v52, s[90:91]
s_mov_b64 exec, -1
M 121
ad 66,2112
H 0
M 132
M 133
TX 58,0
TX 62,13824
M 122
DG 1024
M 123
M 134
M 135
TY 66,512
TY 70,14336
M 124
bc 48,11,64,56
M 125
mh 21
M 136
ad 64,2112
H 0
M 137
TX 74,1536
TX 78,15360
M 126
WL 4
Q 0
M 127
Q 2
M 138
Q 4
M 139
TY 82,2048
TY 86,15872
M 128
Q 6
M 129
WL 4
Q 8
M 140
Q 10
M 141
TX 90,3072
TX 94,16896
M 130
Q 12
M 131
R 52
M 142
Q 14
M 143
TY 98,3584
TY 102,17408
M 0
R 54
M 1
WL 10
Q 16
M 12
Q 18
M 13
Q 20
M 2
Q 22
M 3
WL 8
Q 24
M 14
Q 26
M 15
Q 28
M 4
Q 30
M 5
WL 6
Q 32
M 16
Q 34
M 17
Q 36
M 6
Q 38
M 7
WL 2
Q 40
M 18
Q 42
M 19
Q 44
M 8
Q 46
M 9
H 1
M 20
ae 44,68,96
M 21
s_bfe_u32 s51, s50, 0x10005
v_mad_i32_i24 v47, v49, s51, v48
XL 22,0
M 10
H 2
XL 24,768
M 11
R 56
M 22
XL 26,1536
M 23
R 58
M 24
XL 28,2304
M 25
M 26
M 27
M 28
M 29
M 30
M 31
M 32
M 33
WV 10
M 34
WL 0
FP
M 35
M 36
M 37
s_bitcmp1_b32 s87, 7
s_cselect_b32 exec_lo, 0xffffff, 0
s_mov_b32 exec_hi, 0
global_atomic_add_f32 v54, v52, s[90:91]
s_mov_b64 exec, -1
M 38
M 39
M 40
M 41
M 42
M 43
M 44
M 45
FR
M 46
M 47
M 48
UL 6,14
UH 7,14
M 49
AR 8,52
AR 9,76
M 50
AD 8,6
AD 9,7
M 51
AW 52,8
AW 76,9
M 52
UL 6,15
UH 7,15
M 53
WL 0
FQ 20
AR 8,100
M 54
AR 9,124
AD 8,6
M 55
AD 9,7
AW 100,8
M 56
FZ 20
R 72
M 57
mw 24
R 74
M 58
TY 106,5128
TY 110,18952
M 59
R 76
M 60
ad 84,1008
W 0
M 61
AW 124,9
UL 6,16
M 62
UH 7,16
AR 8,53
M 63
W 1
AR 9,77
M 64
AD 8,6
AD 9,7
M 65
AW 53,8
AW 77,9
M 66
W 2
UL 6,17
M 67
WL 3
AN 104,106
AN 105,107
M 68
SH 106
SH 107
M 69
mw 27
AN 106,106
AN 107,107
M 70
WL 2
AN 108,110
AN 109,111
M 71
R 78
M 72
SH 110
SH 111
M 73
AN 110,110
AN 111,111
M 74
TY 114,6664
TY 118,20488
M 75
ad 84,1014
W 0
M 76
UH 7,17
AR 8,101
M 77
AR 9,125
AD 8,6
M 78
W 1
AD 9,7
M 79
AW 101,8
AW 125,9
M 80
UL 6,18
UH 7,18
M 81
W 2
AR 8,54
M 82
AR 9,78
AD 8,6
M 83
WL 1
R 80
M 84
mw 48
AN 112,114
AN 113,115
M 85
SH 114
SH 115
M 86
AN 114,114
AN 115,115
M 87
WL 2
AN 116,118
AN 117,119
M 88
SH 118
SH 119
M 89
AN 118,118
AN 119,119
M 90
TY 122,8200
TY 126,22024
M 91
ad 84,1056
M 92
W 0
AD 9,7
M 93
AW 54,8
AW 78,9
M 94
UL 6,19
UH 7,19
M 95
R 82
M 96
W 1
AR 8,102
M 97
AR 9,126
AD 8,6
M 98
AD 9,7
AW 102,8
M 99
WL 3
AN 120,122
AN 121,123
M 100
SH 122
SH 123
M 101
AN 122,122
AN 123,123
M 102
WL 2
AN 124,126
AN 125,127
M 103
SH 126
SH 127
M 104
AN 126,126
AN 127,127
M 105
W 2
AW 126,9
M 106
UL 6,20
UH 7,20
M 107
R 0
M 108
mw 51
AR 8,55
AR 9,79
M 109
ad 84,1062
W 0
M 110
AD 8,6
AD 9,7
M 111
AW 55,8
AW 79,9
M 112
W 1
UL 6,21
M 113
UH 7,21
AR 8,103
M 114
AR 9,127
AD 8,6
M 115
W 2
AD 9,7
M 116
AW 103,8
AW 127,9
M 117
M 118
bc 49,12,66,60
M 119
WL 2
R 2
)"
R"(M 120
mh 0
M 121
ad 66,2304
H 0
M 132
M 133
TX 58,4608
TX 62,18432
M 122
DG 1024
M 123
M 134
M 135
TY 66,5120
TY 70,18944
M 124
bc 48,12,64,56
M 125
mh 3
M 136
ad 64,2304
H 0
M 137
TX 74,6144
TX 78,19968
M 126
WL 4
Q 0
M 127
Q 2
M 138
Q 4
M 139
TY 82,6656
TY 86,20480
M 128
Q 6
M 129
WL 4
Q 8
M 140
Q 10
M 141
TX 90,7680
TX 94,21504
M 130
Q 12
M 131
R 4
M 142
Q 14
M 143
TY 98,8192
TY 102,22016
M 0
R 6
M 1
WL 10
Q 16
M 12
Q 18
M 13
Q 20
M 2
Q 22
M 3
WL 8
Q 24
M 14
Q 26
M 15
Q 28
M 4
Q 30
M 5
WL 6
Q 32
M 16
Q 34
M 17
Q 36
M 6
Q 38
M 7
WL 2
Q 40
M 18
Q 42
M 19
Q 44
M 8
Q 46
M 9
H 1
M 20
M 21
M 10
H 2
M 11
R 8
M 22
M 23
R 10
PA 12288
DF
M 24
M 25
M 26
M 27
M 28
M 29
M 30
M 31
M 32
ae 44,70,0
M 33
WV 6
M 34
WL 0
FP
M 35
M 36
s_bitcmp1_b32 s50, 0
s_cselect_b32 s51, 24576, 0
v_add_u32 v47, s51, v3
M 37
XL 30,128
M 38
XL 32,896
M 39
M 40
M 41
M 42
M 43
M 44
XL 34,1664
M 45
FR
M 46
M 47
M 48
M 49
M 50
XL 36,2432
UL 6,22
M 51
UH 7,22
AR 8,56
M 52
AR 9,80
AD 8,6
M 53
WL 0
FQ 24
AD 9,7
M 54
AW 56,8
AW 80,9
M 55
UL 6,23
UH 7,23
M 56
FZ 24
R 24
M 57
mw 72
R 26
M 58
TY 106,9736
TY 110,23560
M 59
R 28
M 60
ad 84,1104
W 0
M 61
AR 8,104
AR 9,128
M 62
AD 8,6
AD 9,7
M 63
W 1
AW 104,8
M 64
AW 128,9
UL 6,24
M 65
UH 7,24
AR 8,57
M 66
W 2
AR 9,81
M 67
WL 3
AN 104,106
AN 105,107
M 68
SH 106
SH 107
M 69
mw 75
AN 106,106
AN 107,107
M 70
WL 2
AN 108,110
AN 109,111
M 71
R 30
M 72
SH 110
SH 111
M 73
AN 110,110
AN 111,111
M 74
TY 114,11272
TY 118,25096
M 75
ad 84,1110
W 0
M 76
AD 8,6
AD 9,7
M 77
AW 57,8
AW 81,9
M 78
W 1
UL 6,25
M 79
UH 7,25
AR 8,105
M 80
AR 9,129
AD 8,6
M 81
W 2
AD 9,7
M 82
AW 105,8
AW 129,9
M 83
WL 1
R 32
M 84
mw 0
AN 112,114
AN 113,115
M 85
SH 114
SH 115
M 86
AN 114,114
AN 115,115
M 87
WL 2
AN 116,118
AN 117,119
M 88
SH 118
SH 119
M 89
AN 118,118
AN 119,119
M 90
TY 122,12808
TY 126,26632
M 91
ad 84,1152
M 92
W 0
UL 6,26
M 93
UH 7,26
AR 8,58
M 94
AR 9,82
AD 8,6
M 95
R 34
M 96
W 1
AD 9,7
M 97
AW 58,8
AW 82,9
M 98
UL 6,27
UH 7,27
M 99
WL 3
AN 120,122
AN 121,123
M 100
SH 122
SH 123
M 101
AN 122,122
AN 123,123
M 102
WL 2
AN 124,126
AN 125,127
M 103
SH 126
SH 127
M 104
AN 126,126
AN 127,127
M 105
W 2
AR 8,106
M 106
AR 9,130
AD 8,6
M 107
R 48
M 108
mw 3
AD 9,7
AW 106,8
M 109
ad 84,1158
W 0
M 110
AW 130,9
UL 6,28
M 111
UH 7,28
AR 8,59
M 112
W 1
AR 9,83
M 113
AD 8,6
AD 9,7
M 114
AW 59,8
AW 83,9
M 115
W 2
UL 6,29
M 116
UH 7,29
AR 8,107
M 117
AR 9,131
AD 8,6
M 118
bc 49,13,66,60
AD 9,7
M 119
WL 0
R 50
M 120
mh 9
AW 107,8
AW 131,9
M 121
ad 66,2496
H 0
M 132
M 133
TX 58,9216
TX 62,23040
M 122
DG 1024
M 123
M 134
M 135
TY 66,9728
TY 70,23552
M 124
bc 48,13,64,56
M 125
mh 12
M 136
ad 64,2496
H 0
M 137
TX 74,10752
TX 78,24576
M 126
WL 4
Q 0
M 127
Q 2
M 138
Q 4
M 139
TY 82,11264
TY 86,25088
M 128
Q 6
M 129
WL 4
Q 8
)"
R"(M 140
Q 10
M 141
TX 90,12288
TX 94,26112
M 130
Q 12
M 131
R 52
M 142
Q 14
M 143
TY 98,12800
TY 102,26624
M 0
R 54
M 1
WL 10
Q 16
M 12
Q 18
M 13
Q 20
M 2
Q 22
M 3
WL 8
Q 24
M 14
Q 26
M 15
Q 28
M 4
Q 30
M 5
WL 6
Q 32
M 16
Q 34
M 17
Q 36
M 6
Q 38
M 7
WL 2
Q 40
M 18
Q 42
M 19
Q 44
M 8
Q 46
M 9
H 1
M 20
M 21
M 10
H 2
M 11
R 56
M 22
M 23
R 58
M 24
M 25
M 26
M 27
M 28
M 29
M 30
M 31
M 32
M 33
WV 5
M 34
WL 0
FP
M 35
M 36
ae 44,70,0
M 37
s_bfe_u32 s51, s50, 0x10001
v_mad_i32_i24 v47, v49, s51, v48
XL 38,128
M 38
M 39
M 40
XL 40,896
M 41
M 42
M 43
M 44
M 45
FR
M 46
XL 42,1664
M 47
M 48
M 49
M 50
M 51
M 52
XL 44,2432
M 53
WL 0
FQ 28
M 54
M 55
M 56
FZ 28
R 72
M 57
R 74
M 58
TY 106,520
TY 110,14344
M 59
R 76
M 60
mw 24
v_mov_b32 v54, 2
s_mov_b64 exec, 1
FS
s_mov_b64 exec, -1
M 61
ad 84,1200
W 0
M 62
M 63
M 64
W 1
M 65
M 66
M 67
WL 3
AN 104,106
AN 105,107
M 68
SH 106
SH 107
M 69
AN 106,106
AN 107,107
M 70
WL 2
AN 108,110
AN 109,111
M 71
R 78
M 72
SH 110
SH 111
M 73
AN 110,110
AN 111,111
M 74
TY 114,2056
TY 118,15880
M 75
W 2
M 76
M 77
M 78
mw 27
M 79
ad 84,1206
W 0
M 80
M 81
M 82
W 1
M 83
R 80
M 84
WL 3
AN 112,114
AN 113,115
M 85
SH 114
SH 115
M 86
AN 114,114
AN 115,115
M 87
WL 2
AN 116,118
AN 117,119
M 88
SH 118
SH 119
M 89
AN 118,118
AN 119,119
M 90
TY 122,3592
TY 126,17416
M 91
W 2
UL 6,30
M 92
UH 7,30
AR 8,132
M 93
AR 9,156
AD 8,6
M 94
mw 48
AD 9,7
AW 132,8
M 95
R 82
M 96
ad 84,1248
W 0
M 97
AW 156,9
UL 6,31
M 98
UH 7,31
AR 8,180
M 99
WL 3
AN 120,122
AN 121,123
M 100
SH 122
SH 123
M 101
AN 122,122
AN 123,123
M 102
WL 2
AN 124,126
AN 125,127
M 103
SH 126
SH 127
M 104
AN 126,126
AN 127,127
M 105
W 1
AD 8,6
M 106
AD 136,7
AW 180,8
M 107
R 0
M 108
W 2
UL 6,32
M 109
UH 7,32
AR 8,133
M 110
AR 9,157
AD 8,6
M 111
mw 51
AD 9,7
AW 133,8
M 112
ad 84,1254
W 0
M 113
AW 157,9
UL 6,33
M 114
UH 7,33
AR 8,181
M 115
W 1
AD 8,6
M 116
AD 137,7
AW 181,8
M 117
UL 6,34
UH 7,34
M 118
W 2
AR 8,134
M 119
WL 0
R 2
M 120
AR 9,158
AD 8,6
M 121
bc 49,14,66,60
AD 9,7
M 132
mh 18
AW 134,8
AW 158,9
M 133
TX 58,0
TX 62,13824
M 122
ad 66,2688
H 0
M 123
UL 6,35
UH 7,35
M 134
AR 8,182
AD 8,6
M 135
TY 66,512
TY 70,14336
M 124
DG 1024
M 125
AD 138,7
AW 182,8
M 136
UL 6,36
UH 7,36
M 137
TX 74,1536
TX 78,15360
M 126
WL 4
Q 0
M 127
Q 2
M 138
Q 4
M 139
TY 82,2048
TY 86,15872
M 128
Q 6
M 129
WL 4
Q 8
M 140
Q 10
M 141
TX 90,3072
TX 94,16896
M 130
Q 12
M 131
R 4
M 142
Q 14
M 143
TY 98,3584
TY 102,17408
M 0
R 6
M 1
WL 10
Q 16
M 12
Q 18
M 13
Q 20
M 2
Q 22
M 3
WL 8
Q 24
M 14
Q 26
M 15
Q 28
M 4
Q 30
M 5
WL 6
Q 32
M 16
Q 34
M 17
Q 36
M 6
Q 38
M 7
WL 2
Q 40
M 18
Q 42
M 19
Q 44
M 8
Q 46
M 9
bc 48,14,64,56
M 20
mh 21
AR 8,135
AR 9,159
M 21
ad 64,2688
H 0
M 10
AD 8,6
AD 9,7
)"
R"(M 11
R 8
M 22
H 1
AW 135,8
M 23
R 10
M 24
AW 159,9
UL 6,37
M 25
H 2
UH 7,37
M 26
AR 8,183
AD 8,6
M 27
AD 139,7
AW 183,8
M 28
PA 12288
DF
M 29
M 30
M 31
M 32
M 33
WV 6
M 34
WL 0
FP
M 35
M 36
ae 44,68,0
M 37
s_bitcmp1_b32 s50, 2
s_cselect_b32 s51, 24576, 0
v_add_u32 v47, s51, v3
M 38
XL 14,128
M 39
M 40
M 41
M 42
XL 16,896
M 43
M 44
M 45
FR
M 46
M 47
M 48
XL 18,1664
M 49
M 50
M 51
M 52
M 53
WL 0
FQ 32
M 54
XL 20,2432
M 55
M 56
FZ 32
R 24
M 57
mw 72
R 26
M 58
TY 106,5128
TY 110,18952
M 59
R 28
M 60
ad 84,1296
W 0
M 61
M 62
M 63
W 1
M 64
M 65
M 66
W 2
M 67
WL 3
AN 104,106
AN 105,107
M 68
SH 106
SH 107
M 69
mw 75
AN 106,106
AN 107,107
M 70
WL 2
AN 108,110
AN 109,111
M 71
R 30
M 72
SH 110
SH 111
M 73
AN 110,110
AN 111,111
M 74
TY 114,6664
TY 118,20488
M 75
ad 84,1302
W 0
M 76
M 77
M 78
W 1
M 79
M 80
M 81
W 2
M 82
M 83
R 32
M 84
mw 0
WL 3
AN 112,114
AN 113,115
M 85
SH 114
SH 115
M 86
AN 114,114
AN 115,115
M 87
WL 2
AN 116,118
AN 117,119
M 88
SH 118
SH 119
M 89
AN 118,118
AN 119,119
M 90
TY 122,8200
TY 126,22024
M 91
ad 84,1344
M 92
W 0
UL 6,38
M 93
UH 7,38
AR 8,136
M 94
AR 9,160
AD 8,6
M 95
R 34
M 96
W 1
AD 9,7
M 97
AW 136,8
AW 160,9
M 98
UL 6,39
UH 7,39
M 99
WL 3
AN 120,122
AN 121,123
M 100
SH 122
SH 123
M 101
AN 122,122
AN 123,123
M 102
WL 2
AN 124,126
AN 125,127
M 103
SH 126
SH 127
M 104
AN 126,126
AN 127,127
M 105
W 2
AR 8,184
M 106
AD 8,6
AD 140,7
M 107
R 48
M 108
mw 3
AW 184,8
UL 6,40
M 109
ad 84,1350
W 0
M 110
UH 7,40
AR 8,137
M 111
AR 9,161
AD 8,6
M 112
W 1
AD 9,7
M 113
AW 137,8
AW 161,9
M 114
UL 6,41
UH 7,41
M 115
W 2
AR 8,185
M 116
AD 8,6
AD 141,7
M 117
AW 185,8
UL 6,42
M 118
bc 49,15,66,60
UH 7,42
M 119
WL 0
R 50
M 120
mh 0
AR 8,138
AR 9,162
M 121
ad 66,2880
H 0
M 132
AD 8,6
AD 9,7
M 133
TX 58,4608
TX 62,18432
M 122
DG 1024
M 123
AW 138,8
AW 162,9
M 134
UL 6,43
UH 7,43
M 135
TY 66,5120
TY 70,18944
M 124
bc 48,15,64,56
AR 8,186
M 125
mh 3
AD 8,6
AD 142,7
M 136
ad 64,2880
H 0
M 137
TX 74,6144
TX 78,19968
M 126
WL 4
Q 0
M 127
Q 2
M 138
Q 4
M 139
TY 82,6656
TY 86,20480
M 128
Q 6
M 129
WL 4
Q 8
M 140
Q 10
M 141
TX 90,7680
TX 94,21504
M 130
Q 12
M 131
R 52
M 142
Q 14
M 143
TY 98,8192
TY 102,22016
M 0
R 54
M 1
WL 10
Q 16
M 12
Q 18
M 13
Q 20
M 2
Q 22
M 3
WL 8
Q 24
M 14
Q 26
M 15
Q 28
M 4
Q 30
M 5
WL 6
Q 32
M 16
Q 34
M 17
Q 36
M 6
Q 38
M 7
WL 2
Q 40
M 18
Q 42
M 19
Q 44
M 8
Q 46
M 9
H 1
AW 186,8
M 20
UL 6,44
UH 7,44
M 21
AR 8,139
AR 9,163
M 10
H 2
AD 8,6
M 11
R 56
M 22
AD 9,7
AW 139,8
M 23
R 58
M 24
AW 163,9
UL 6,45
M 25
UH 7,45
AR 8,187
M 26
AD 8,6
AD 143,7
M 27
AW 187,8
M 28
M 29
M 30
M 31
M 32
M 33
WV 5
M 34
WL 0
FP
M 35
M 36
M 37
M 38
ae 44,68,0
M 39
s_bfe_u32 s51, s50, 0x10003
v_mad_i32_i24 v47, v49, s51, v48
XL 22,128
M 40
M 41
M 42
M 43
M 44
XL 24,896
M 45
FR
M 46
M 47
M 48
)"
R"(M 49
M 50
XL 26,1664
M 51
M 52
M 53
WL 0
FQ 36
M 54
M 55
M 56
FZ 36
R 72
M 57
mw 24
R 74
M 58
TY 106,9736
TY 110,23560
M 59
R 76
M 60
ad 84,1392
W 0
M 61
XL 28,2432
M 62
M 63
W 1
M 64
M 65
M 66
W 2
M 67
WL 3
AN 104,106
AN 105,107
M 68
SH 106
SH 107
M 69
mw 27
AN 106,106
AN 107,107
M 70
WL 2
AN 108,110
AN 109,111
M 71
R 78
M 72
SH 110
SH 111
M 73
AN 110,110
AN 111,111
M 74
TY 114,11272
TY 118,25096
M 75
ad 84,1398
W 0
M 76
M 77
M 78
W 1
M 79
M 80
M 81
W 2
M 82
M 83
R 80
M 84
mw 48
WL 3
AN 112,114
AN 113,115
M 85
SH 114
SH 115
M 86
AN 114,114
AN 115,115
M 87
WL 2
AN 116,118
AN 117,119
M 88
SH 118
SH 119
M 89
AN 118,118
AN 119,119
M 90
TY 122,12808
TY 126,26632
M 91
ad 84,1440
M 92
W 0
UL 6,14
M 93
UH 7,14
AR 8,140
M 94
AR 9,164
AD 8,6
M 95
R 82
M 96
W 1
AD 9,7
M 97
AW 140,8
AW 164,9
M 98
UL 6,15
UH 7,15
M 99
WL 3
AN 120,122
AN 121,123
M 100
SH 122
SH 123
M 101
AN 122,122
AN 123,123
M 102
WL 2
AN 124,126
AN 125,127
M 103
SH 126
SH 127
M 104
AN 126,126
AN 127,127
M 105
W 2
AR 8,188
M 106
AD 8,6
AD 144,7
M 107
R 0
M 108
mw 51
AW 188,8
UL 6,16
M 109
ad 84,1446
W 0
M 110
UH 7,16
AR 8,141
M 111
AR 9,165
AD 8,6
M 112
W 1
AD 9,7
M 113
AW 141,8
AW 165,9
M 114
UL 6,17
UH 7,17
M 115
W 2
AR 8,189
M 116
AD 8,6
AD 145,7
M 117
AW 189,8
UL 6,18
M 118
bc 49,16,66,60
UH 7,18
M 119
WL 0
R 2
M 120
mh 9
AR 8,142
AR 9,166
M 121
ad 66,3072
H 0
M 132
AD 8,6
AD 9,7
M 133
TX 58,9216
TX 62,23040
M 122
DG 1024
M 123
AW 142,8
AW 166,9
M 134
UL 6,19
UH 7,19
M 135
TY 66,9728
TY 70,23552
M 124
bc 48,16,64,56
AR 8,190
M 125
mh 12
AD 8,6
AD 146,7
M 136
ad 64,3072
H 0
M 137
TX 74,10752
TX 78,24576
M 126
WL 4
Q 0
M 127
Q 2
M 138
Q 4
M 139
TY 82,11264
TY 86,25088
M 128
Q 6
M 129
WL 4
Q 8
M 140
Q 10
M 141
TX 90,12288
TX 94,26112
M 130
Q 12
M 131
R 4
M 142
Q 14
M 143
TY 98,12800
TY 102,26624
M 0
R 6
M 1
WL 10
Q 16
M 12
Q 18
M 13
Q 20
M 2
Q 22
M 3
WL 8
Q 24
M 14
Q 26
M 15
Q 28
M 4
Q 30
M 5
WL 6
Q 32
M 16
Q 34
M 17
Q 36
M 6
Q 38
M 7
WL 2
Q 40
M 18
Q 42
M 19
Q 44
M 8
Q 46
M 9
H 1
AW 190,8
M 20
UL 6,20
UH 7,20
M 21
AR 8,143
AR 9,167
M 10
H 2
AD 8,6
M 11
R 8
M 22
AD 9,7
AW 143,8
M 23
R 10
PA 12288
DF
M 24
AW 167,9
UL 6,21
M 25
UH 7,21
AR 8,191
M 26
AD 8,6
AD 147,7
M 27
AW 191,8
M 28
M 29
M 30
M 31
M 32
M 33
WV 6
M 34
WL 0
FP
M 35
M 36
M 37
M 38
M 39
M 40
ae 44,68,96
M 41
s_bitcmp1_b32 s50, 4
s_cselect_b32 s51, 24576, 0
v_add_u32 v47, s51, v3
M 42
XL 30,128
M 43
M 44
M 45
FR
M 46
XL 32,896
M 47
M 48
M 49
M 50
M 51
M 52
XL 34,1664
M 53
WL 0
FQ 40
M 54
M 55
M 56
FZ 40
R 24
M 57
mw 72
R 26
M 58
TY 106,520
TY 110,14344
M 59
R 28
M 60
ad 84,1488
W 0
M 61
XL 36,2432
v_lshlrev_b32 v55, 4, v0
M 62
FL
M 63
W 1
M 64
M 65
M 66
W 2
M 67
WL 3
AN 104,106
AN 105,107
M 68
SH 106
SH 107
M 69
mw 75
AN 106,106
AN 107,107
M 70
WL 2
AN 108,110
)"
R"(AN 109,111
M 71
R 30
M 72
SH 110
SH 111
M 73
AN 110,110
AN 111,111
M 74
TY 114,2056
TY 118,15880
M 75
ad 84,1494
W 0
M 76
M 77
M 78
W 1
M 79
M 80
M 81
W 2
M 82
M 83
R 32
M 84
mw 0
WL 3
AN 112,114
AN 113,115
M 85
SH 114
SH 115
M 86
AN 114,114
AN 115,115
M 87
WL 2
AN 116,118
AN 117,119
M 88
SH 118
SH 119
M 89
AN 118,118
AN 119,119
M 90
TY 122,3592
TY 126,17416
M 91
ad 84,1536
M 92
W 0
M 93
M 94
UL 6,22
UH 7,22
M 95
R 34
M 96
W 1
AR 8,144
M 97
AR 9,168
AD 8,6
M 98
AD 9,7
AW 144,8
M 99
WL 3
AN 120,122
AN 121,123
M 100
SH 122
SH 123
M 101
AN 122,122
AN 123,123
M 102
WL 2
AN 124,126
AN 125,127
M 103
SH 126
SH 127
M 104
AN 126,126
AN 127,127
M 105
W 2
AW 168,9
M 106
UL 6,23
UH 7,23
M 107
R 48
M 108
mw 3
AR 8,192
AD 8,6
M 109
ad 84,1542
W 0
M 110
AD 148,7
AW 192,8
M 111
UL 6,24
UH 7,24
M 112
W 1
AR 8,145
M 113
AR 9,169
AD 8,6
M 114
AD 9,7
AW 145,8
M 115
W 2
AW 169,9
M 116
UL 6,25
UH 7,25
M 117
AR 8,193
AD 8,6
M 118
bc 49,17,66,60
AD 149,7
M 119
WL 0
R 50
M 120
mh 18
AW 193,8
UL 6,26
M 121
ad 66,3264
H 0
M 132
UH 7,26
AR 8,146
M 133
TX 58,0
TX 62,13824
M 122
DG 1024
M 123
AR 9,170
AD 8,6
M 134
AD 9,7
AW 146,8
M 135
TY 66,512
TY 70,14336
M 124
bc 48,17,64,56
AW 170,9
M 125
mh 21
UL 6,27
UH 7,27
M 136
ad 64,3264
H 0
M 137
TX 74,1536
TX 78,15360
M 126
WL 4
Q 0
M 127
Q 2
M 138
Q 4
M 139
TY 82,2048
TY 86,15872
M 128
Q 6
M 129
WL 4
Q 8
M 140
Q 10
M 141
TX 90,3072
TX 94,16896
M 130
Q 12
M 131
R 52
M 142
Q 14
M 143
TY 98,3584
TY 102,17408
M 0
R 54
M 1
WL 10
Q 16
M 12
Q 18
M 13
Q 20
M 2
Q 22
M 3
WL 8
Q 24
M 14
Q 26
M 15
Q 28
M 4
Q 30
M 5
WL 6
Q 32
M 16
Q 34
M 17
Q 36
M 6
Q 38
M 7
WL 2
Q 40
M 18
Q 42
M 19
Q 44
M 8
Q 46
M 9
H 1
AR 8,194
M 20
AD 8,6
AD 150,7
M 21
AW 194,8
UL 6,28
M 10
H 2
UH 7,28
M 11
R 56
M 22
AR 8,147
AR 9,171
M 23
R 58
M 24
AD 8,6
AD 9,7
M 25
AW 147,8
AW 171,9
M 26
UL 6,29
UH 7,29
M 27
AR 8,195
AD 8,6
M 28
AD 151,7
AW 195,8
M 29
M 30
M 31
M 32
M 33
WV 5
M 34
WL 0
FP
M 35
M 36
M 37
M 38
M 39
M 40
M 41
M 42
ae 44,68,96
M 43
s_bfe_u32 s51, s50, 0x10005
v_mad_i32_i24 v47, v49, s51, v48
XL 38,128
M 44
M 45
FR
M 46
M 47
M 48
XL 40,896
M 49
M 50
M 51
M 52
M 53
WL 0
FQ 44
M 54
XL 42,1664
M 55
M 56
FZ 44
R 72
M 57
mw 24
R 74
M 58
TY 106,5128
TY 110,18952
M 59
R 76
M 60
ad 84,1584
W 0
M 61
XL 44,2432
M 62
M 63
W 1
M 64
M 65
M 66
W 2
M 67
WL 3
AN 104,106
AN 105,107
M 68
SH 106
SH 107
M 69
mw 27
AN 106,106
AN 107,107
M 70
WL 2
AN 108,110
AN 109,111
M 71
R 78
M 72
SH 110
SH 111
M 73
AN 110,110
AN 111,111
M 74
TY 114,6664
TY 118,20488
M 75
ad 84,1590
W 0
M 76
M 77
M 78
W 1
M 79
M 80
M 81
W 2
M 82
M 83
R 80
M 84
mw 48
WL 3
AN 112,114
AN 113,115
M 85
SH 114
SH 115
M 86
AN 114,114
AN 115,115
M 87
WL 2
AN 116,118
AN 117,119
M 88
SH 118
SH 119
M 89
AN 118,118
AN 119,119
M 90
TY 122,8200
TY 126,22024
M 91
ad 84,1632
)"
R"(M 92
W 0
M 93
M 94
M 95
R 82
M 96
W 1
UL 6,30
M 97
UH 7,30
AR 8,148
M 98
AR 9,172
AD 8,6
M 99
WL 3
AN 120,122
AN 121,123
M 100
SH 122
SH 123
M 101
AN 122,122
AN 123,123
M 102
WL 2
AN 124,126
AN 125,127
M 103
SH 126
SH 127
M 104
AN 126,126
AN 127,127
M 105
W 2
AD 9,7
M 106
AW 148,8
AW 172,9
M 107
R 0
M 108
mw 51
UL 6,31
UH 7,31
M 109
ad 84,1638
W 0
M 110
AR 8,196
AD 8,6
M 111
AD 152,7
AW 196,8
M 112
W 1
UL 6,32
M 113
UH 7,32
AR 8,149
M 114
AR 9,173
AD 8,6
M 115
W 2
AD 9,7
M 116
AW 149,8
AW 173,9
M 117
UL 6,33
UH 7,33
M 118
bc 49,18,66,60
AR 8,197
M 119
WL 0
R 2
M 120
mh 0
AD 8,6
AD 153,7
M 121
ad 66,3456
H 0
M 132
AW 197,8
UL 6,34
M 133
TX 58,4608
TX 62,18432
M 122
DG 1024
M 123
UH 7,34
AR 8,150
M 134
AR 9,174
AD 8,6
M 135
TY 66,5120
TY 70,18944
M 124
bc 48,18,64,56
AD 9,7
M 125
mh 3
AW 150,8
AW 174,9
M 136
ad 64,3456
H 0
M 137
TX 74,6144
TX 78,19968
M 126
WL 4
Q 0
M 127
Q 2
M 138
Q 4
M 139
TY 82,6656
TY 86,20480
M 128
Q 6
M 129
WL 4
Q 8
M 140
Q 10
M 141
TX 90,7680
TX 94,21504
M 130
Q 12
M 131
R 4
M 142
Q 14
M 143
TY 98,8192
TY 102,22016
M 0
R 6
M 1
WL 10
Q 16
M 12
Q 18
M 13
Q 20
M 2
Q 22
M 3
WL 8
Q 24
M 14
Q 26
M 15
Q 28
M 4
Q 30
M 5
WL 6
Q 32
M 16
Q 34
M 17
Q 36
M 6
Q 38
M 7
WL 2
Q 40
M 18
Q 42
M 19
Q 44
M 8
Q 46
M 9
H 1
UL 6,35
M 20
UH 7,35
AR 8,198
M 21
AD 8,6
AD 154,7
M 10
H 2
AW 198,8
M 11
R 8
M 22
UL 6,36
UH 7,36
M 23
R 10
PA 12288
DF
M 24
AR 8,151
AR 9,175
M 25
AD 8,6
AD 9,7
M 26
AW 151,8
AW 175,9
M 27
v_cmp_ne_u32 vcc, 2, v54
UL 6,37
M 28
UH 7,37
AR 8,199
M 29
AD 8,6
AD 155,7
M 30
FC
AW 199,8
M 31
M 32
M 33
WV 6
M 34
WL 0
FP
M 35
M 36
M 37
M 38
M 39
M 40
M 41
M 42
M 43
M 44
M 45
FR
M 46
M 47
M 48
M 49
M 50
M 51
M 52
M 53
WL 0
FQ 48
M 54
M 55
M 56
FZ 48
R 24
M 57
mw 72
R 26
M 58
TY 106,9736
TY 110,23560
M 59
R 28
M 60
ad 84,1680
W 0
M 61
M 62
M 63
W 1
M 64
M 65
M 66
W 2
M 67
WL 3
AN 104,106
AN 105,107
M 68
SH 106
SH 107
M 69
mw 75
AN 106,106
AN 107,107
M 70
WL 2
AN 108,110
AN 109,111
M 71
R 30
M 72
SH 110
SH 111
M 73
AN 110,110
AN 111,111
M 74
TY 114,11272
TY 118,25096
M 75
ad 84,1686
W 0
M 76
ae 44,70,0
M 77
s_bitcmp1_b32 s50, 0
s_cselect_b32 s51, 24576, 0
v_add_u32 v47, s51, v3
M 78
W 1
XL 14,256
M 79
XL 16,1024
M 80
M 81
W 2
M 82
XL 18,1792
M 83
WL 1
R 32
M 84
mw 0
AN 112,114
AN 113,115
M 85
SH 114
SH 115
M 86
AN 114,114
AN 115,115
M 87
WL 2
AN 116,118
AN 117,119
M 88
SH 118
SH 119
M 89
AN 118,118
AN 119,119
M 90
TY 122,12808
TY 126,26632
M 91
ad 84,1728
M 92
W 0
XL 20,2560
M 93
M 94
M 95
R 34
M 96
W 1
M 97
M 98
UL 6,38
UH 7,38
M 99
WL 3
AN 120,122
AN 121,123
M 100
SH 122
SH 123
M 101
AN 122,122
AN 123,123
M 102
WL 2
AN 124,126
AN 125,127
M 103
SH 126
SH 127
M 104
AN 126,126
AN 127,127
M 105
W 2
AR 8,152
M 106
AR 9,176
AD 8,6
M 107
R 48
M 108
mw 3
AD 9,7
AW 152,8
M 109
ad 84,1734
W 0
)"
R"(M 110
AW 176,9
UL 6,39
M 111
UH 7,39
AD 132,6
M 112
W 1
AD 156,7
M 113
UL 6,40
UH 7,40
M 114
AR 8,153
AR 9,177
M 115
W 2
AD 8,6
M 116
AD 9,7
AW 153,8
M 117
AW 177,9
UL 6,41
M 118
bc 49,19,66,60
UH 7,41
M 119
WL 0
R 50
M 120
mh 9
AD 133,6
AD 157,7
M 121
ad 66,3648
H 0
M 132
UL 6,42
UH 7,42
M 133
TX 58,9216
TX 62,23040
M 122
DG 1024
M 123
AR 8,154
AR 9,178
M 134
AD 8,6
AD 9,7
M 135
TY 66,9728
TY 70,23552
M 124
bc 48,19,64,56
AW 154,8
M 125
mh 12
AW 178,9
UL 6,43
M 136
ad 64,3648
H 0
M 137
TX 74,10752
TX 78,24576
M 126
WL 4
Q 0
M 127
Q 2
M 138
Q 4
M 139
TY 82,11264
TY 86,25088
M 128
Q 6
M 129
WL 4
Q 8
M 140
Q 10
M 141
TX 90,12288
TX 94,26112
M 130
Q 12
M 131
R 52
M 142
Q 14
M 143
TY 98,12800
TY 102,26624
M 0
R 54
M 1
WL 10
Q 16
M 12
Q 18
M 13
Q 20
M 2
Q 22
M 3
WL 8
Q 24
M 14
Q 26
M 15
Q 28
M 4
Q 30
M 5
WL 6
Q 32
M 16
Q 34
M 17
Q 36
M 6
Q 38
M 7
WL 2
Q 40
M 18
Q 42
M 19
Q 44
M 8
Q 46
M 9
H 1
UH 7,43
M 20
AD 134,6
AD 158,7
M 21
UL 6,44
UH 7,44
M 10
H 2
AR 8,155
M 11
R 56
M 22
AR 9,179
AD 8,6
M 23
R 58
v_min_u32 v52, 752, v0
v_lshrrev_b32 v52, 1, v52
v_add_u32 v53, 0x1800, v52
global_load_dwordx2 a[200:201], v52, s[94:95] offset:-2048 sc1
global_load_dwordx2 a[202:203], v52, s[94:95] offset:-512 sc1
global_load_dwordx2 a[204:205], v52, s[94:95] offset:1024 sc1
global_load_dwordx2 a[206:207], v52, s[94:95] offset:2560 sc1
global_load_dwordx2 a[208:209], v53, s[94:95] offset:-2048 sc1
global_load_dwordx2 a[210:211], v53, s[94:95] offset:-512 sc1
global_load_dwordx2 a[212:213], v53, s[94:95] offset:1024 sc1
global_load_dwordx2 a[214:215], v53, s[94:95] offset:2560 sc1
M 24
AD 9,7
AW 155,8
M 25
AW 179,9
UL 6,45
M 26
UH 7,45
AD 135,6
M 27
AD 159,7
M 28
M 29
M 30
M 31
M 32
M 33
WV 13
M 34
WL 0
FP
M 35
M 36
M 37
M 38
M 39
M 40
M 41
M 42
M 43
M 44
M 45
FR
M 46
M 47
M 48
M 49
M 50
M 51
M 52
M 53
WL 0
FQ 52
M 54
M 55
M 56
FZ 52
R 72
M 57
mw 24
R 74
M 58
TY 106,520
TY 110,14344
M 59
R 76
M 60
ad 84,1776
W 0
M 61
M 62
M 63
W 1
M 64
M 65
M 66
W 2
M 67
WL 3
AN 104,106
AN 105,107
M 68
SH 106
SH 107
M 69
mw 27
AN 106,106
AN 107,107
M 70
WL 2
AN 108,110
AN 109,111
M 71
R 78
M 72
SH 110
SH 111
M 73
AN 110,110
AN 111,111
M 74
TY 114,2056
TY 118,15880
M 75
ad 84,1782
W 0
M 76
ae 44,70,0
M 77
s_bfe_u32 s51, s50, 0x10001
v_mad_i32_i24 v47, v49, s51, v48
XL 22,256
M 78
W 1
M 79
M 80
XL 24,1024
M 81
W 2
M 82
M 83
WL 1
R 80
M 84
mw 48
AN 112,114
AN 113,115
M 85
SH 114
SH 115
M 86
AN 114,114
AN 115,115
M 87
WL 2
AN 116,118
AN 117,119
M 88
SH 118
SH 119
M 89
AN 118,118
AN 119,119
M 90
TY 122,3592
TY 126,17416
M 91
ad 84,1824
M 92
W 0
XL 26,1792
M 93
XL 28,2560
M 94
M 95
R 82
M 96
W 1
M 97
M 98
M 99
WL 3
AN 120,122
AN 121,123
M 100
SH 122
SH 123
M 101
AN 122,122
AN 123,123
M 102
WL 2
AN 124,126
AN 125,127
M 103
SH 126
SH 127
M 104
AN 126,126
AN 127,127
M 105
W 2
M 106
M 107
R 0
M 108
mw 51
M 109
ad 84,1830
W 0
M 110
M 111
M 112
W 1
M 113
M 114
M 115
W 2
M 116
M 117
M 118
bc 49,20,66,60
M 119
WL 2
R 2
M 120
mh 18
M 121
ad 66,3840
H 0
M 132
M 133
TX 58,0
TX 62,13824
M 122
DG 1024
M 123
M 134
UL 6,14
UH 7,14
M 135
TY 66,512
TY 70,14336
M 124
bc 48,20,64,56
AD 160,6
M 125
mh 21
AD 184,7
UL 6,15
M 136
ad 64,3840
H 0
M 137
TX 74,1536
TX 78,15360
M 126
WL 4
Q 0
M 127
Q 2
M 138
Q 4
M 139
TY 82,2048
TY 86,15872
)"
R"(M 128
Q 6
M 129
WL 4
Q 8
M 140
Q 10
M 141
TX 90,3072
TX 94,16896
M 130
Q 12
M 131
R 4
M 142
Q 14
M 143
TY 98,3584
TY 102,17408
M 0
R 6
M 1
WL 10
Q 16
M 12
Q 18
M 13
Q 20
M 2
Q 22
M 3
WL 8
Q 24
M 14
Q 26
M 15
Q 28
M 4
Q 30
M 5
WL 6
Q 32
M 16
Q 34
M 17
Q 36
M 6
Q 38
M 7
WL 2
Q 40
M 18
Q 42
M 19
Q 44
M 8
Q 46
M 9
H 1
UH 7,15
M 20
AD 208,6
AD 232,7
M 21
UL 6,16
UH 7,16
M 10
H 2
AD 161,6
M 11
R 8
M 22
AD 185,7
UL 6,17
M 23
R 10
PA 12288
DF
M 24
UH 7,17
AD 209,6
M 25
AD 233,7
UL 6,18
M 26
UH 7,18
AD 162,6
M 27
AD 186,7
UL 6,19
M 28
UH 7,19
AD 210,6
M 29
AD 234,7
UL 6,20
M 30
UH 7,20
AD 163,6
M 31
AD 187,7
UL 6,21
M 32
UH 7,21
AD 211,6
M 33
WV 6
AD 235,7
M 34
WL 0
FP
M 35
M 36
M 37
M 38
M 39
M 40
M 41
M 42
M 43
M 44
M 45
FR
M 46
M 47
M 48
M 49
M 50
M 51
M 52
M 53
WL 0
FQ 56
M 54
M 55
M 56
FZ 56
R 24
M 57
mw 72
R 26
M 58
TY 106,5128
TY 110,18952
M 59
R 28
M 60
ad 84,1872
W 0
M 61
M 62
M 63
W 1
M 64
M 65
M 66
W 2
M 67
WL 3
AN 104,106
AN 105,107
M 68
SH 106
SH 107
M 69
mw 75
AN 106,106
AN 107,107
M 70
WL 2
AN 108,110
AN 109,111
M 71
R 30
M 72
SH 110
SH 111
M 73
AN 110,110
AN 111,111
M 74
TY 114,6664
TY 118,20488
M 75
ad 84,1878
W 0
M 76
M 77
M 78
W 1
M 79
ae 44,68,0
M 80
s_bitcmp1_b32 s50, 2
s_cselect_b32 s51, 24576, 0
v_add_u32 v47, s51, v3
M 81
W 2
XL 30,256
M 82
M 83
WL 1
R 32
M 84
mw 0
AN 112,114
AN 113,115
M 85
SH 114
SH 115
M 86
AN 114,114
AN 115,115
M 87
WL 2
AN 116,118
AN 117,119
M 88
SH 118
SH 119
M 89
AN 118,118
AN 119,119
M 90
TY 122,8200
TY 126,22024
M 91
ad 84,1920
M 92
W 0
XL 32,1024
M 93
XL 34,1792
M 94
M 95
R 34
M 96
W 1
XL 36,2560
M 97
M 98
M 99
WL 3
AN 120,122
AN 121,123
M 100
SH 122
SH 123
M 101
AN 122,122
AN 123,123
M 102
WL 2
AN 124,126
AN 125,127
M 103
SH 126
SH 127
M 104
AN 126,126
AN 127,127
M 105
W 2
M 106
M 107
R 48
M 108
mw 3
M 109
ad 84,1926
W 0
M 110
M 111
M 112
W 1
M 113
M 114
M 115
W 2
M 116
M 117
M 118
bc 49,21,66,60
M 119
WL 2
R 50
M 120
mh 0
M 121
ad 66,4032
H 0
M 132
v_mov_b32 v54, s39
v_mul_f32 v54, 0xb62aaa8f, v54
M 133
TX 58,4608
TX 62,18432
M 122
DG 1024
M 123
v_add_u32 v52, %[bm], v52
v_bfe_u32 v53, v52, 6, 2
M 134
v_and_b32 v55, 63, v52
v_add_u32 v53, v53, v55
M 135
TY 66,5120
TY 70,18944
M 124
bc 48,21,64,56
v_and_b32 v52, 0x700, v52
M 125
mh 3
v_lshl_add_u32 v52, v53, 2, v52
v_add_u32 v52, %[sb], v52
M 136
ad 64,4032
H 0
M 137
TX 74,6144
TX 78,19968
M 126
WL 4
Q 0
M 127
Q 2
M 138
Q 4
M 139
TY 82,6656
TY 86,20480
M 128
Q 6
M 129
WL 4
Q 8
M 140
Q 10
M 141
TX 90,7680
TX 94,21504
M 130
Q 12
M 131
R 52
M 142
Q 14
M 143
TY 98,8192
TY 102,22016
M 0
R 54
M 1
WL 10
Q 16
M 12
Q 18
M 13
Q 20
M 2
Q 22
M 3
WL 8
Q 24
M 14
Q 26
M 15
Q 28
M 4
Q 30
M 5
WL 6
Q 32
M 16
Q 34
M 17
Q 36
M 6
Q 38
M 7
WL 2
Q 40
M 18
Q 42
M 19
Q 44
M 8
Q 46
M 9
H 1
AR 12,214
M 20
AR 13,215
AR 8,208
M 21
AR 9,209
v_add_f32 v12, v12, v8
M 10
H 2
v_add_f32 v13, v13, v9
)"
R"(M 11
R 56
M 22
AR 8,210
AR 9,211
M 23
R 58
M 24
v_add_f32 v12, v12, v8
v_add_f32 v13, v13, v9
M 25
AR 8,212
AR 9,213
M 26
v_add_f32 v12, v12, v8
v_add_f32 v13, v13, v9
M 27
AR 6,206
AR 7,207
M 28
v_add_f32 v12, v12, v54
v_add_f32 v13, v13, v54
M 29
v_fma_f32 v8, v6, s86, v12
v_fma_f32 v9, v7, s86, v13
M 30
ds_write_b32 v52, v8 offset:1024
ds_write_b32 v52, v9 offset:1040
M 31
AR 8,200
AR 9,201
M 32
v_add_f32 v6, v6, v8
v_add_f32 v7, v7, v9
M 33
WV 5
v_fma_f32 v8, v6, s86, v12
v_fma_f32 v9, v7, s86, v13
M 34
WL 2
FP
ds_write_b32 v52, v8 offset:2560
M 35
ds_write_b32 v52, v9 offset:2576
AR 8,202
M 36
AR 9,203
v_add_f32 v6, v6, v8
M 37
v_add_f32 v7, v7, v9
v_fma_f32 v8, v6, s86, v12
M 38
ds_write_b32 v52, v8 offset:4096
v_fma_f32 v9, v7, s86, v13
M 39
ds_write_b32 v52, v9 offset:4112
AR 8,204
M 40
AR 9,205
v_add_f32 v6, v6, v8
M 41
v_add_f32 v7, v7, v9
v_fma_f32 v8, v6, s86, v12
M 42
ds_write_b32 v52, v8 offset:5632
v_fma_f32 v9, v7, s86, v13
M 43
ds_write_b32 v52, v9 offset:5648
M 44
UL 6,22
UH 7,22
M 45
FR
AD 164,6
M 46
AD 188,7
UL 6,23
M 47
UH 7,23
AD 212,6
M 48
AD 236,7
UL 6,24
M 49
UH 7,24
AD 165,6
M 50
AD 189,7
UL 6,25
M 51
UH 7,25
AD 213,6
M 52
AD 237,7
UL 6,26
M 53
WL 0
FQ 60
UH 7,26
M 54
AD 166,6
AD 190,7
M 55
UL 6,27
UH 7,27
M 56
FZ 60
R 72
M 57
mw 24
R 74
M 58
TY 106,9736
TY 110,23560
M 59
R 76
M 60
ad 84,1968
W 0
M 61
AD 214,6
AD 238,7
M 62
UL 6,28
UH 7,28
M 63
W 1
AD 167,6
M 64
AD 191,7
UL 6,29
M 65
UH 7,29
AD 215,6
M 66
W 2
AD 239,7
M 67
WL 3
AN 104,106
AN 105,107
M 68
SH 106
SH 107
M 69
mw 27
AN 106,106
AN 107,107
M 70
WL 2
AN 108,110
AN 109,111
M 71
R 78
M 72
SH 110
SH 111
M 73
AN 110,110
AN 111,111
M 74
TY 114,11272
TY 118,25096
M 75
ad 84,1974
W 0
M 76
M 77
M 78
W 1
M 79
M 80
M 81
W 2
M 82
ae 44,68,0
M 83
WL 1
R 80
M 84
mw 48
AN 112,114
AN 113,115
M 85
s_bfe_u32 s51, s50, 0x10003
SH 114
SH 115
M 86
AN 114,114
AN 115,115
M 87
WL 2
AN 116,118
AN 117,119
M 88
SH 118
SH 119
M 89
AN 118,118
AN 119,119
M 90
TY 122,12808
TY 126,26632
M 91
ad 84,2016
M 92
W 0
v_mad_i32_i24 v47, v49, s51, v48
M 93
XL 38,256
M 94
XL 40,1024
M 95
R 82
M 96
W 1
XL 42,1792
M 97
M 98
M 99
WL 3
AN 120,122
AN 121,123
M 100
SH 122
SH 123
M 101
AN 122,122
AN 123,123
M 102
WL 2
AN 124,126
AN 125,127
M 103
SH 126
SH 127
M 104
AN 126,126
AN 127,127
M 105
W 2
XL 44,2560
M 106
M 107
R 0
M 108
mw 51
M 109
ad 84,2022
W 0
M 110
M 111
M 112
W 1
M 113
M 114
M 115
W 2
M 116
M 117
M 118
bc 49,22,66,60
M 119
WL 2
R 2
M 120
mh 9
M 121
ad 66,4224
H 0
M 132
M 133
TX 58,9216
TX 62,23040
M 122
DG 1024
M 123
M 134
M 135
TY 66,9728
TY 70,23552
M 124
bc 48,22,64,56
M 125
mh 12
M 136
ad 64,4224
H 0
M 137
TX 74,10752
TX 78,24576
M 126
WL 4
Q 0
M 127
Q 2
M 138
Q 4
M 139
TY 82,11264
TY 86,25088
M 128
Q 6
M 129
WL 4
Q 8
M 140
Q 10
M 141
TX 90,12288
TX 94,26112
M 130
Q 12
M 131
R 4
M 142
Q 14
M 143
TY 98,12800
TY 102,26624
M 0
R 6
M 1
WL 10
Q 16
M 12
Q 18
M 13
Q 20
M 2
Q 22
M 3
WL 8
Q 24
M 14
Q 26
M 15
Q 28
M 4
Q 30
M 5
WL 6
Q 32
M 16
Q 34
M 17
Q 36
M 6
Q 38
M 7
WL 2
Q 40
M 18
Q 42
M 19
Q 44
M 8
Q 46
M 9
H 1
UL 6,30
M 20
UH 7,30
AD 168,6
M 21
AD 192,7
UL 6,31
M 10
H 2
UH 7,31
M 11
R 8
M 22
AD 216,6
AD 240,7
M 23
R 10
PA 12288
DF
M 24
UL 6,32
UH 7,32
M 25
AD 169,6
AD 193,7
M 26
UL 6,33
UH 7,33
M 27
AD 217,6
AD 241,7
M 28
UL 6,34
UH 7,34
M 29
AD 170,6
AD 194,7
M 30
UL 6,35
UH 7,35
)"
R"(M 31
AD 218,6
AD 242,7
M 32
UL 6,36
UH 7,36
M 33
WV 6
AD 171,6
AD 195,7
M 34
WL 0
FP
UL 6,37
M 35
UH 7,37
AD 219,6
M 36
AD 243,7
M 37
M 38
M 39
M 40
M 41
M 42
M 43
M 44
M 45
FR
M 46
M 47
M 48
M 49
M 50
M 51
M 52
M 53
WL 0
FQ 64
M 54
M 55
M 56
FZ 64
R 24
M 57
mw 72
R 26
M 58
TY 106,520
TY 110,14344
M 59
R 28
M 60
ad 84,2064
W 0
M 61
M 62
M 63
W 1
M 64
M 65
M 66
W 2
M 67
WL 3
AN 104,106
AN 105,107
M 68
SH 106
SH 107
M 69
mw 75
AN 106,106
AN 107,107
M 70
WL 2
AN 108,110
AN 109,111
M 71
R 30
M 72
SH 110
SH 111
M 73
AN 110,110
AN 111,111
M 74
TY 114,2056
TY 118,15880
M 75
ad 84,2070
W 0
M 76
M 77
M 78
W 1
M 79
M 80
M 81
W 2
M 82
M 83
R 32
M 84
mw 0
WL 3
AN 112,114
AN 113,115
M 85
SH 114
SH 115
M 86
AN 114,114
AN 115,115
M 87
WL 2
AN 116,118
AN 117,119
M 88
SH 118
SH 119
M 89
AN 118,118
AN 119,119
M 90
TY 122,3592
TY 126,17416
M 91
ad 84,2112
M 92
W 0
M 93
ae 44,68,96
M 94
s_bitcmp1_b32 s50, 4
s_cselect_b32 s51, 24576, 0
v_add_u32 v47, s51, v3
M 95
R 34
M 96
W 1
XL 14,256
M 97
XL 16,1024
M 98
XL 18,1792
M 99
WL 3
AN 120,122
AN 121,123
M 100
SH 122
SH 123
M 101
AN 122,122
AN 123,123
M 102
WL 2
AN 124,126
AN 125,127
M 103
SH 126
SH 127
M 104
AN 126,126
AN 127,127
M 105
W 2
XL 20,2560
M 106
M 107
R 48
M 108
mw 3
M 109
ad 84,2118
W 0
M 110
M 111
M 112
W 1
M 113
M 114
M 115
W 2
M 116
M 117
M 118
bc 49,23,66,60
M 119
WL 2
R 50
M 120
mh 18
M 121
ad 66,4416
H 0
M 132
M 133
TX 58,0
TX 62,13824
M 122
DG 1024
M 123
M 134
M 135
TY 66,512
TY 70,14336
M 124
bc 48,23,64,56
M 125
mh 21
M 136
ad 64,4416
H 0
M 137
TX 74,1536
TX 78,15360
M 126
WL 4
Q 0
M 127
Q 2
M 138
Q 4
M 139
TY 82,2048
TY 86,15872
M 128
Q 6
M 129
WL 4
Q 8
M 140
Q 10
M 141
TX 90,3072
TX 94,16896
M 130
Q 12
M 131
R 52
M 142
Q 14
M 143
TY 98,3584
TY 102,17408
M 0
R 54
M 1
WL 10
Q 16
M 12
Q 18
M 13
Q 20
M 2
Q 22
M 3
WL 8
Q 24
M 14
Q 26
M 15
Q 28
M 4
Q 30
M 5
WL 6
Q 32
M 16
Q 34
M 17
Q 36
M 6
Q 38
M 7
WL 2
Q 40
M 18
Q 42
M 19
Q 44
M 8
Q 46
M 9
H 1
UL 6,38
M 20
UH 7,38
AD 172,6
M 21
AD 196,7
UL 6,39
M 10
H 2
UH 7,39
M 11
R 56
M 22
AD 220,6
AD 244,7
M 23
R 58
M 24
UL 6,40
UH 7,40
M 25
AD 173,6
AD 197,7
M 26
UL 6,41
UH 7,41
M 27
AD 221,6
AD 245,7
M 28
UL 6,42
UH 7,42
M 29
AD 174,6
AD 198,7
M 30
UL 6,43
UH 7,43
M 31
AD 222,6
AD 246,7
M 32
UL 6,44
UH 7,44
M 33
WV 5
AD 175,6
AD 199,7
M 34
WL 0
FP
UL 6,45
M 35
UH 7,45
AD 223,6
M 36
AD 247,7
M 37
M 38
M 39
M 40
M 41
M 42
M 43
M 44
M 45
FR
M 46
M 47
M 48
M 49
M 50
M 51
M 52
M 53
WL 0
FQ 68
M 54
M 55
M 56
FZ 68
R 72
M 57
mw 24
R 74
M 58
TY 106,5128
TY 110,18952
M 59
R 76
M 60
ad 84,2160
W 0
M 61
M 62
M 63
W 1
M 64
M 65
M 66
W 2
M 67
WL 3
AN 104,106
AN 105,107
M 68
SH 106
SH 107
M 69
mw 27
AN 106,106
AN 107,107
M 70
)"
R"(WL 2
AN 108,110
AN 109,111
M 71
R 78
M 72
SH 110
SH 111
M 73
AN 110,110
AN 111,111
M 74
TY 114,6664
TY 118,20488
M 75
ad 84,2166
W 0
M 76
M 77
M 78
W 1
M 79
M 80
M 81
W 2
M 82
M 83
R 80
M 84
mw 48
WL 3
AN 112,114
AN 113,115
M 85
SH 114
SH 115
M 86
AN 114,114
AN 115,115
M 87
WL 2
AN 116,118
AN 117,119
M 88
SH 118
SH 119
M 89
AN 118,118
AN 119,119
M 90
TY 122,8200
TY 126,22024
M 91
ad 84,2208
M 92
W 0
M 93
ae 44,68,96
M 94
s_bfe_u32 s51, s50, 0x10005
v_mad_i32_i24 v47, v49, s51, v48
XL 22,256
M 95
R 82
M 96
W 1
XL 24,1024
M 97
M 98
M 99
WL 3
AN 120,122
AN 121,123
M 100
SH 122
SH 123
M 101
AN 122,122
AN 123,123
M 102
WL 2
AN 124,126
AN 125,127
M 103
SH 126
SH 127
M 104
AN 126,126
AN 127,127
M 105
W 2
XL 26,1792
M 106
M 107
R 0
M 108
mw 51
XL 28,2560
M 109
ad 84,2214
W 0
M 110
M 111
M 112
W 1
M 113
M 114
M 115
W 2
M 116
M 117
M 118
bc 49,0,66,60
M 119
WL 2
R 2
M 120
mh 0
M 121
ad 66,32
H 0
M 132
M 133
TX 58,4608
TX 62,18432
M 122
DG 1024
M 123
M 134
M 135
TY 66,5120
TY 70,18944
M 124
bc 48,0,64,56
M 125
mh 3
M 136
ad 64,0
H 0
M 137
TX 74,6144
TX 78,19968
M 126
WL 4
Q 0
M 127
Q 2
M 138
Q 4
M 139
TY 82,6656
TY 86,20480
M 128
Q 6
M 129
WL 4
Q 8
M 140
Q 10
M 141
TX 90,7680
TX 94,21504
M 130
Q 12
M 131
R 4
M 142
Q 14
M 143
TY 98,8192
TY 102,22016
M 0
R 6
M 1
WL 10
Q 16
M 12
Q 18
M 13
Q 20
M 2
Q 22
M 3
WL 8
Q 24
M 14
Q 26
M 15
Q 28
M 4
Q 30
M 5
WL 6
Q 32
M 16
Q 34
M 17
Q 36
M 6
Q 38
M 7
WL 2
Q 40
M 18
Q 42
M 19
Q 44
M 8
Q 46
M 9
H 1
UL 6,14
M 20
UH 7,14
AD 176,6
M 21
AD 200,7
UL 6,15
M 10
H 2
UH 7,15
M 11
R 8
M 22
AD 224,6
AD 248,7
M 23
R 10
PA 12288
DF
M 24
UL 6,16
UH 7,16
M 25
AD 177,6
AD 201,7
M 26
UL 6,17
UH 7,17
M 27
AD 225,6
AD 249,7
M 28
UL 6,18
UH 7,18
M 29
AD 178,6
AD 202,7
M 30
UL 6,19
UH 7,19
M 31
AD 226,6
AD 250,7
M 32
UL 6,20
UH 7,20
M 33
WV 6
AD 179,6
AD 203,7
M 34
WL 0
FP
UL 6,21
M 35
UH 7,21
AD 227,6
M 36
AD 251,7
M 37
M 38
M 39
M 40
M 41
M 42
M 43
M 44
M 45
FR
M 46
M 47
M 48
M 49
M 50
M 51
M 52
M 53
WL 0
FQ 72
M 54
M 55
M 56
FZ 72
R 24
M 57
mw 72
R 26
M 58
TY 106,9736
TY 110,23560
M 59
R 28
M 60
ad 84,2256
W 0
M 61
M 62
M 63
W 1
M 64
M 65
M 66
W 2
M 67
WL 3
AN 104,106
AN 105,107
M 68
SH 106
SH 107
M 69
mw 75
AN 106,106
AN 107,107
M 70
WL 2
AN 108,110
AN 109,111
M 71
R 30
M 72
SH 110
SH 111
M 73
AN 110,110
AN 111,111
M 74
TY 114,11272
TY 118,25096
M 75
ad 84,2262
W 0
M 76
M 77
M 78
W 1
M 79
M 80
M 81
W 2
M 82
M 83
R 32
M 84
mw 0
WL 3
AN 112,114
AN 113,115
M 85
SH 114
SH 115
M 86
AN 114,114
AN 115,115
M 87
WL 2
AN 116,118
AN 117,119
M 88
SH 118
SH 119
M 89
AN 118,118
AN 119,119
M 90
TY 122,12808
TY 126,26632
M 91
ad 84,0
M 92
W 0
M 93
M 94
M 95
R 34
M 96
W 1
M 97
M 98
M 99
WL 3
AN 120,122
AN 121,123
M 100
SH 122
SH 123
M 101
AN 122,122
AN 123,123
)"
R"(M 102
WL 2
AN 124,126
AN 125,127
M 103
SH 126
SH 127
M 104
AN 126,126
AN 127,127
M 105
W 2
M 106
M 107
R 48
M 108
mw 3
M 109
ad 84,6
W 0
M 110
M 111
M 112
W 1
M 113
M 114
M 115
W 2
M 116
M 117
M 118
bc 49,1,66,60
M 119
WL 2
R 50
M 120
mh 9
M 121
ad 66,224
H 0
M 132
M 133
TX 58,9216
TX 62,23040
M 122
DG 1024
M 123
M 134
M 135
TY 66,9728
TY 70,23552
M 124
bc 48,1,64,56
M 125
mh 12
M 136
ad 64,224
H 0
M 137
TX 74,10752
TX 78,24576
M 126
WL 4
Q 0
M 127
Q 2
M 138
Q 4
M 139
TY 82,11264
TY 86,25088
M 128
Q 6
M 129
WL 4
Q 8
M 140
Q 10
M 141
TX 90,12288
TX 94,26112
M 130
Q 12
M 131
R 52
M 142
Q 14
M 143
TY 98,12800
TY 102,26624
M 0
R 54
M 1
WL 10
Q 16
M 12
Q 18
M 13
Q 20
M 2
Q 22
M 3
WL 8
Q 24
M 14
Q 26
M 15
Q 28
M 4
Q 30
M 5
WL 6
Q 32
M 16
Q 34
M 17
Q 36
M 6
Q 38
M 7
WL 2
Q 40
M 18
Q 42
M 19
Q 44
M 8
Q 46
M 9
DG 1024
M 20
UL 6,22
UH 7,22
M 21
AD 180,6
AD 204,7
M 10
mh 15
UL 6,23
UH 7,23
M 11
R 56
M 22
ad 64,195
H 0
M 23
R 58
M 24
AD 228,6
AD 252,7
M 25
DG 1024
M 26
UL 6,24
UH 7,24
BV 52,0
M 27
AD 181,6
AD 205,7
M 28
UL 6,25
UH 7,25
M 29
AD 229,6
AD 253,7
M 30
UL 6,26
UH 7,26
M 31
AD 182,6
AD 206,7
M 32
UL 6,27
UH 7,27
M 33
WV 6
AD 230,6
AD 254,7
M 34
WL 1
FP
UL 6,28
M 35
UH 7,28
AD 183,6
M 36
AD 207,7
UL 6,29
M 37
UH 7,29
AD 231,6
M 38
AD 255,7
M 39
M 40
ds_read_b64 v[12:13], v4 offset:256
ds_read_b64 v[46:47], v4 offset:264
M 41
AR 6,36
AR 7,60
M 42
AR 8,84
AR 9,108
M 43
WL 3
EF 6,52
EF 7,53
M 44
EF 8,54
EF 9,55
M 45
FR
CV 30,6,7
M 46
CV 31,8,9
AR 6,37
M 47
AR 7,61
AR 8,85
M 48
AR 9,109
EF 6,52
M 49
EF 7,53
EF 8,54
M 50
EF 9,55
CV 32,6,7
M 51
CV 33,8,9
AR 6,38
M 52
AR 7,62
AR 8,86
M 53
WL 0
FQ 76
AR 9,110
M 54
EF 6,52
EF 7,53
M 55
EF 8,54
EF 9,55
M 56
FZ 76
R 72
M 57
mw 24
R 74
M 58
TY 106,520
TY 110,14344
M 59
R 76
M 60
ad 84,48
W 0
M 61
CV 34,6,7
CV 35,8,9
M 62
AR 6,39
AR 7,63
M 63
W 1
AR 8,87
M 64
AR 9,111
EF 6,52
M 65
EF 7,53
EF 8,54
M 66
W 2
EF 9,55
M 67
WL 3
AN 104,106
AN 105,107
M 68
SH 106
SH 107
M 69
mw 27
AN 106,106
AN 107,107
M 70
WL 2
AN 108,110
AN 109,111
M 71
R 78
M 72
SH 110
SH 111
M 73
AN 110,110
AN 111,111
M 74
TY 114,2056
TY 118,15880
M 75
ad 84,54
W 0
M 76
CV 36,6,7
CV 37,8,9
M 77
ae 42,74,0
AR 6,40
M 78
W 1
AR 7,64
M 79
s_bitcmp1_b32 s50, 0
s_cselect_b32 s52, 24576, 0
v_add_u32 v50, s52, v3
ST 30,0
M 80
AR 8,88
AR 9,112
M 81
W 2
EF 6,52
M 82
EF 7,53
EF 8,54
M 83
WL 1
R 80
M 84
mw 48
AN 112,114
AN 113,115
M 85
SH 114
SH 115
M 86
AN 114,114
AN 115,115
M 87
WL 2
AN 116,118
AN 117,119
M 88
SH 118
SH 119
M 89
AN 118,118
AN 119,119
M 90
TY 122,3592
TY 126,17416
M 91
ad 84,96
ST 32,768
M 92
W 0
EF 9,55
M 93
CV 38,6,7
CV 39,8,9
M 94
AR 6,41
AR 7,65
M 95
R 82
M 96
W 1
AR 8,89
M 97
AR 9,113
EF 6,52
M 98
EF 7,53
EF 8,54
M 99
WL 3
AN 120,122
AN 121,123
M 100
SH 122
SH 123
M 101
AN 122,122
AN 123,123
M 102
WL 2
AN 124,126
AN 125,127
M 103
SH 126
SH 127
ST 34,1536
M 104
AN 126,126
AN 127,127
M 105
W 2
)"
R"(EF 9,55
M 106
CV 40,6,7
CV 41,8,9
M 107
R 0
M 108
mw 51
AR 6,42
AR 7,66
M 109
ad 84,102
W 0
M 110
AR 8,90
AR 9,114
M 111
EF 6,52
EF 7,53
M 112
W 1
EF 8,54
M 113
EF 9,55
CV 42,6,7
M 114
CV 43,8,9
AR 6,43
M 115
W 2
ST 36,2304
M 116
AR 7,67
AR 8,91
M 117
AR 9,115
EF 6,52
M 118
bc 49,2,66,60
EF 7,53
M 119
WL 0
R 2
M 120
mh 18
EF 8,54
EF 9,55
M 121
ad 66,416
H 0
M 132
CV 44,6,7
CV 45,8,9
M 133
TX 58,0
TX 62,13824
BV 52,512
M 122
DG 1024
M 123
AR 6,132
AR 7,156
M 134
AR 8,180
EF 6,12
M 135
TY 66,512
TY 70,14336
M 124
bc 48,2,64,56
ae 42,74,0
M 125
mh 21
EF 7,13
EF 8,46
M 136
ad 64,416
H 0
M 137
WL 3
TX 74,1536
TX 78,15360
M 126
s_bfe_u32 s52, s50, 0x10001
Q 0
v_mad_i32_i24 v50, v49, s52, v48
M 127
Q 2
ST 38,0
M 138
Q 4
M 139
TY 82,2048
TY 86,15872
M 128
Q 6
M 129
WL 4
Q 8
M 140
Q 10
M 141
TX 90,3072
TX 94,16896
M 130
Q 12
M 131
R 4
M 142
Q 14
M 143
TY 98,3584
TY 102,17408
v_mfma_f32_16x16x32_f16 a[36:39], v[56:59], a[0:3], a[32:35]
R 6
M 1
WL 10
Q 16
ST 40,768
v_mfma_f32_16x16x32_f16 a[60:63], v[56:59], a[8:11], a[32:35]
Q 18
M 13
Q 20
v_mfma_f32_16x16x32_f16 a[40:43], v[64:67], a[0:3], a[32:35]
Q 22
M 3
WL 8
Q 24
v_mfma_f32_16x16x32_f16 a[64:67], v[64:67], a[8:11], a[32:35]
Q 26
M 15
Q 28
M 4
Q 30
M 5
WL 6
Q 32
M 16
Q 34
M 17
Q 36
M 6
Q 38
M 7
WL 1
Q 40
ST 42,1536
M 18
Q 42
M 19
Q 44
M 8
Q 46
M 9
H 1
EF 136,47
M 20
CV 14,6,7
CV 15,8,136
M 21
AR 6,133
AR 7,157
M 10
H 2
AR 8,181
M 11
R 8
M 22
EF 6,12
EF 7,13
M 23
R 10
PA 12288
DF
v_mfma_f32_16x16x32_f16 a[84:87], v[56:59], a[16:19], a[32:35]
EF 8,46
EF 137,47
M 25
ST 44,2304
CV 16,6,7
v_mfma_f32_16x16x32_f16 a[88:91], v[64:67], a[16:19], a[32:35]
CV 17,8,137
AR 6,134
M 27
AR 7,158
AR 8,182
M 28
EF 6,12
EF 7,13
M 29
EF 8,46
EF 138,47
M 30
CV 18,6,7
CV 19,8,138
M 31
AR 6,135
AR 7,159
M 32
AR 8,183
EF 6,12
M 33
WV 11
EF 7,13
EF 8,46
M 34
WL 0
FP
EF 139,47
M 35
CV 20,6,7
CV 21,8,139
v_mfma_f32_16x16x32_f16 a[108:111], v[56:59], a[24:27], a[32:35]
AR 6,136
AR 7,160
AR 8,184
EF 6,12
M 37
ae 42,74,0
EF 7,13
EF 8,46
EF 140,47
v_mfma_f32_16x16x32_f16 a[112:115], v[64:67], a[24:27], a[32:35]
s_bitcmp1_b32 s50, 0
s_cselect_b32 s52, 24576, 0
v_add_u32 v50, s52, v3
ST 14,128
CV 22,6,7
M 39
CV 23,8,140
AR 6,137
AR 7,161
AR 8,185
M 40
EF 6,12
EF 7,13
EF 8,46
EF 141,47
M 41
CV 24,6,7
CV 25,8,141
AR 6,138
AR 7,162
M 42
AR 8,186
EF 6,12
EF 7,13
EF 8,46
M 43
EF 142,47
CV 26,6,7
CV 27,8,142
AR 6,139
M 44
AR 7,163
AR 8,187
EF 6,12
EF 7,13
M 45
FR
EF 8,46
EF 143,47
CV 28,6,7
M 46
CV 29,8,143
M 47
ds_read_b64 v[12:13], v4 offset:1536
ds_read_b64 v[46:47], v4 offset:1544
v_mfma_f32_16x16x32_f16 a[132:135], v[56:59], a[0:3], a[32:35]
EF 160,52
EF 184,53
M 49
EF 208,54
EF 232,55
v_mfma_f32_16x16x32_f16 a[136:139], v[64:67], a[0:3], a[32:35]
ST 16,896
CV 30,160,184
M 51
CV 31,208,232
EF 161,52
M 52
EF 185,53
EF 209,54
M 53
WL 2
FQ 80
EF 233,55
M 54
CV 32,161,185
CV 33,209,233
M 55
EF 162,52
EF 186,53
M 56
FZ 80
R 24
M 57
mw 72
R 26
M 58
TY 106,5128
TY 110,18952
M 59
R 28
v_mfma_f32_16x16x32_f16 a[156:159], v[56:59], a[8:11], a[32:35]
ad 84,144
W 0
M 61
EF 210,54
EF 234,55
v_mfma_f32_16x16x32_f16 a[160:163], v[64:67], a[8:11], a[32:35]
ST 18,1664
CV 34,162,186
M 63
W 1
CV 35,210,234
M 64
EF 163,52
EF 187,53
M 65
EF 211,54
EF 235,55
M 66
W 2
CV 36,163,187
M 67
WL 3
AN 104,106
AN 105,107
M 68
SH 106
SH 107
M 69
mw 75
AN 106,106
AN 107,107
M 70
WL 2
AN 108,110
AN 109,111
M 71
R 30
v_mfma_f32_16x16x32_f16 a[180:183], v[56:59], a[16:19], a[32:35]
SH 110
SH 111
M 73
AN 110,110
AN 111,111
v_mfma_f32_16x16x32_f16 a[184:187], v[64:67], a[16:19], a[32:35]
TY 114,6664
TY 118,20488
ST 20,2432
M 75
ad 84,150
W 0
M 76
CV 37,211,235
M 77
EF 164,52
EF 188,53
EF 212,54
EF 236,55
M 78
W 1
CV 38,164,188
CV 39,212,236
EF 165,52
M 79
EF 189,53
EF 213,54
EF 237,55
CV 40,165,189
M 80
CV 41,213,237
EF 166,52
EF 190,53
EF 214,54
M 81
W 2
EF 238,55
CV 42,166,190
CV 43,214,238
M 82
EF 167,52
EF 191,53
EF 215,54
EF 239,55
M 83
WL 0
R 32
CV 44,167,191
CV 45,215,239
v_mfma_f32_16x16x32_f16 v[136:139], v[56:59], a[24:27], v[128:131]
mw 0
AN 112,114
AN 113,115
BV 52,1792
M 85
SH 114
SH 115
)"
R"(AR 6,44
v_mfma_f32_16x16x32_f16 v[140:143], v[64:67], a[24:27], v[128:131]
AN 114,114
AN 115,115
AR 7,68
M 87
AN 116,118
AN 117,119
AR 8,92
M 88
SH 118
SH 119
AR 9,116
M 89
AN 118,118
AN 119,119
EF 6,12
M 90
TY 122,8200
TY 126,22024
EF 7,13
M 91
ad 84,192
EF 8,46
M 92
W 0
ae 42,74,0
M 93
s_bfe_u32 s52, s50, 0x10001
v_mad_i32_i24 v50, v49, s52, v48
ST 22,128
EF 9,47
M 94
CV 14,6,7
CV 15,8,9
AR 6,45
M 95
R 34
AR 7,69
v_mfma_f32_16x16x32_f16 v[160:163], v[56:59], a[0:3], v[128:131]
W 1
AR 8,93
AR 9,117
M 97
EF 6,12
EF 7,13
EF 8,46
v_mfma_f32_16x16x32_f16 v[164:167], v[64:67], a[0:3], v[128:131]
EF 9,47
CV 16,6,7
CV 17,8,9
M 99
WL 3
AN 120,122
AN 121,123
AR 6,46
M 100
SH 122
SH 123
AR 7,70
M 101
AN 122,122
AN 123,123
AR 8,94
M 102
WL 2
AN 124,126
AN 125,127
AR 9,118
M 103
SH 126
SH 127
EF 6,12
M 104
AN 126,126
AN 127,127
EF 7,13
M 105
W 2
ST 24,896
EF 8,46
M 106
EF 9,47
CV 18,6,7
CV 19,8,9
M 107
R 48
AR 6,47
v_mfma_f32_16x16x32_f16 v[184:187], v[56:59], a[8:11], v[128:131]
mw 3
AR 7,71
AR 8,95
AR 9,119
M 109
ad 84,198
W 0
EF 6,12
v_mfma_f32_16x16x32_f16 v[188:191], v[64:67], a[8:11], v[128:131]
EF 7,13
EF 8,46
EF 9,47
M 111
CV 20,6,7
CV 21,8,9
M 112
W 1
AR 6,48
AR 7,72
M 113
AR 8,96
AR 9,120
EF 6,12
M 114
EF 7,13
EF 8,46
EF 9,47
M 115
W 2
CV 6,6,7
CV 7,8,9
M 116
AW 200,6
AW 201,7
AR 6,49
M 117
ST 26,1664
AR 7,73
AR 8,97
M 118
bc 49,3,66,60
AR 9,121
EF 6,12
M 119
WL 0
R 50
EF 7,13
v_mfma_f32_16x16x32_f16 v[208:211], v[56:59], a[16:19], v[128:131]
mh 0
EF 8,46
EF 9,47
CV 6,6,7
M 121
ad 66,608
H 0
CV 7,8,9
v_mfma_f32_16x16x32_f16 v[232:235], v[56:59], a[24:27], v[128:131]
AW 202,6
AW 203,7
AR 6,50
M 133
TX 58,4608
TX 62,18432
AR 7,74
v_mfma_f32_16x16x32_f16 v[212:215], v[64:67], a[16:19], v[128:131]
DG 1024
AR 8,98
M 123
AR 9,122
EF 6,12
EF 7,13
v_mfma_f32_16x16x32_f16 v[236:239], v[64:67], a[24:27], v[128:131]
EF 8,46
EF 9,47
CV 6,6,7
M 135
TY 66,5120
TY 70,18944
CV 7,8,9
M 124
bc 48,3,64,56
AW 204,6
AW 205,7
M 125
mh 3
ST 28,2432
AR 6,51
AR 7,75
M 136
ad 64,608
H 0
AR 8,99
M 137
TX 74,6144
TX 78,19968
AR 9,123
M 126
WL 4
Q 0
EF 6,12
M 127
Q 2
EF 7,13
M 138
Q 4
EF 8,46
M 139
TY 82,6656
TY 86,20480
EF 9,47
M 128
Q 6
CV 6,6,7
M 129
WL 4
Q 8
CV 7,8,9
M 140
Q 10
AW 206,6
M 141
TX 90,7680
TX 94,21504
AW 207,7
M 130
Q 12
ds_read_b64 v[12:13], v4 offset:2048
M 131
R 52
ds_read_b64 v[46:47], v4 offset:2056
M 142
Q 14
M 143
TY 98,8192
TY 102,22016
M 0
R 54
M 1
WL 12
Q 16
M 12
Q 18
M 13
Q 20
M 2
Q 22
M 3
WL 10
Q 24
M 14
Q 26
M 15
Q 28
v_mfma_f32_16x16x32_f16 a[44:47], v[72:75], a[0:3], a[32:35]
Q 30
M 5
WL 7
Q 32
v_mfma_f32_16x16x32_f16 a[68:71], v[72:75], a[8:11], a[32:35]
Q 34
M 17
Q 36
v_mfma_f32_16x16x32_f16 a[48:51], v[80:83], a[0:3], a[32:35]
Q 38
M 7
WL 2
Q 40
v_mfma_f32_16x16x32_f16 a[72:75], v[80:83], a[8:11], a[32:35]
Q 42
M 19
Q 44
M 8
Q 46
M 9
H 1
ae 42,74,0
M 20
s_bitcmp1_b32 s50, 0
s_cselect_b32 s52, 24576, 0
v_add_u32 v50, s52, v3
ST 30,256
M 21
AR 6,140
AR 7,164
M 10
H 2
AR 8,188
M 11
R 56
M 22
EF 6,52
EF 7,53
M 23
R 58
M 24
EF 8,54
EF 144,55
M 25
CV 22,6,7
CV 23,8,144
M 26
AR 6,141
AR 7,165
M 27
AR 8,189
EF 6,52
v_mfma_f32_16x16x32_f16 a[92:95], v[72:75], a[16:19], a[32:35]
EF 7,53
EF 8,54
M 29
EF 145,55
CV 24,6,7
v_mfma_f32_16x16x32_f16 a[96:99], v[80:83], a[16:19], a[32:35]
ST 32,1024
CV 25,8,145
M 31
AR 6,142
AR 7,166
M 32
AR 8,190
EF 6,52
M 33
WV 9
EF 7,53
EF 8,54
M 34
WL 0
FP
EF 146,55
M 35
CV 26,6,7
CV 27,8,146
M 36
AR 6,143
AR 7,167
M 37
AR 8,191
EF 6,52
M 38
EF 7,53
EF 8,54
M 39
EF 147,55
CV 28,6,7
v_mfma_f32_16x16x32_f16 a[116:119], v[72:75], a[24:27], a[32:35]
CV 29,8,147
M 41
AR 6,144
AR 7,168
AR 8,192
EF 6,52
v_mfma_f32_16x16x32_f16 a[120:123], v[80:83], a[24:27], a[32:35]
ST 34,1792
EF 7,53
EF 8,54
EF 148,55
M 43
CV 6,6,7
CV 7,8,148
AW 208,6
AW 209,7
M 44
AR 6,145
AR 7,169
AR 8,193
EF 6,52
M 45
FR
EF 7,53
EF 8,54
EF 149,55
M 46
CV 6,6,7
CV 7,8,149
AW 210,6
AW 211,7
M 47
AR 6,146
AR 7,170
AR 8,194
EF 6,52
M 48
EF 7,53
EF 8,54
EF 150,55
CV 6,6,7
M 49
CV 7,8,150
AW 212,6
AW 213,7
AR 6,147
M 50
AR 7,171
AR 8,195
EF 6,52
EF 7,53
M 51
EF 8,54
EF 151,55
CV 6,6,7
CV 7,8,151
v_mfma_f32_16x16x32_f16 a[140:143], v[72:75], a[0:3], a[32:35]
AW 214,6
AW 215,7
M 53
WL 0
FQ 84
BV 52,3072
EF 168,12
v_mfma_f32_16x16x32_f16 a[144:147], v[80:83], a[0:3], a[32:35]
ST 36,2560
)"
R"(EF 192,13
EF 216,46
M 55
EF 240,47
CV 6,168,192
CV 7,216,240
M 56
FZ 84
R 72
AW 216,6
M 57
mw 24
R 74
AW 217,7
M 58
TY 106,9736
TY 110,23560
EF 169,12
M 59
R 76
EF 193,13
M 60
ad 84,240
W 0
EF 217,46
M 61
EF 241,47
CV 6,169,193
CV 7,217,241
M 62
AW 218,6
AW 219,7
EF 170,12
M 63
W 1
EF 194,13
EF 218,46
v_mfma_f32_16x16x32_f16 a[164:167], v[72:75], a[8:11], a[32:35]
EF 242,47
CV 6,170,194
CV 7,218,242
M 65
AW 220,6
AW 221,7
EF 171,12
v_mfma_f32_16x16x32_f16 a[168:171], v[80:83], a[8:11], a[32:35]
WL 3
W 2
ae 42,74,0
M 67
s_bfe_u32 s52, s50, 0x10001
AN 104,106
AN 105,107
v_mad_i32_i24 v50, v49, s52, v48
M 68
SH 106
SH 107
ST 38,256
M 69
mw 27
AN 106,106
AN 107,107
EF 195,13
M 70
WL 0
AN 108,110
AN 109,111
EF 219,46
M 71
R 78
EF 243,47
M 72
SH 110
SH 111
CV 6,171,195
M 73
AN 110,110
AN 111,111
CV 7,219,243
M 74
TY 114,11272
TY 118,25096
AW 222,6
M 75
ad 84,246
W 0
AW 223,7
v_mfma_f32_16x16x32_f16 a[188:191], v[72:75], a[16:19], a[32:35]
EF 172,12
EF 196,13
EF 220,46
M 77
EF 244,47
CV 30,172,196
CV 31,220,244
v_mfma_f32_16x16x32_f16 a[192:195], v[80:83], a[16:19], a[32:35]
W 1
EF 173,12
EF 197,13
M 79
EF 221,46
EF 245,47
CV 32,173,197
M 80
ST 40,1024
CV 33,221,245
EF 174,12
M 81
W 2
EF 198,13
EF 222,46
M 82
EF 246,47
CV 34,174,198
CV 35,222,246
M 83
WL 0
R 80
EF 175,12
M 84
mw 48
AN 112,114
AN 113,115
EF 199,13
M 85
SH 114
SH 115
EF 223,46
M 86
AN 114,114
AN 115,115
EF 247,47
M 87
AN 116,118
AN 117,119
CV 36,175,199
v_mfma_f32_16x16x32_f16 v[144:147], v[72:75], a[24:27], v[128:131]
SH 118
SH 119
CV 37,223,247
M 89
AN 118,118
AN 119,119
ds_read_b64 v[12:13], v4 offset:3328
v_mfma_f32_16x16x32_f16 v[148:151], v[80:83], a[24:27], v[128:131]
TY 122,12808
TY 126,26632
ds_read_b64 v[46:47], v4 offset:3336
M 91
ad 84,288
AR 6,52
M 92
W 0
ST 42,1792
AR 7,76
M 93
AR 8,100
AR 9,124
EF 6,52
M 94
EF 7,53
EF 8,54
EF 9,55
M 95
R 82
CV 6,6,7
M 96
W 1
CV 7,8,9
AW 224,6
M 97
AW 225,7
AR 6,53
AR 7,77
M 98
AR 8,101
AR 9,125
EF 6,52
M 99
WL 4
AN 120,122
AN 121,123
EF 7,53
v_mfma_f32_16x16x32_f16 v[168:171], v[72:75], a[0:3], v[128:131]
SH 122
SH 123
EF 8,54
M 101
AN 122,122
AN 123,123
EF 9,55
v_mfma_f32_16x16x32_f16 v[172:175], v[80:83], a[0:3], v[128:131]
WL 2
AN 124,126
AN 125,127
CV 6,6,7
M 103
SH 126
SH 127
CV 7,8,9
M 104
AN 126,126
AN 127,127
ST 44,2560
M 105
W 2
AW 226,6
AW 227,7
M 106
AR 6,54
AR 7,78
AR 8,102
M 107
R 0
AR 9,126
M 108
mw 51
EF 6,52
EF 7,53
EF 8,54
M 109
ad 84,294
W 0
EF 9,55
M 110
CV 6,6,7
CV 7,8,9
AW 228,6
M 111
AW 229,7
AR 6,55
AR 7,79
v_mfma_f32_16x16x32_f16 v[192:195], v[72:75], a[8:11], v[128:131]
W 1
AR 8,103
AR 9,127
M 113
EF 6,52
EF 7,53
EF 8,54
v_mfma_f32_16x16x32_f16 v[196:199], v[80:83], a[8:11], v[128:131]
EF 9,55
CV 6,6,7
CV 7,8,9
M 115
W 2
AW 230,6
AW 231,7
M 116
ae 42,72,0
AR 6,56
AR 7,80
M 117
s_bitcmp1_b32 s50, 2
s_cselect_b32 s52, 24576, 0
v_add_u32 v50, s52, v3
ST 14,0
M 118
bc 49,4,66,60
AR 8,104
AR 9,128
M 119
WL 0
R 2
EF 6,52
M 120
mh 9
EF 7,53
EF 8,54
EF 9,55
M 121
ad 66,800
H 0
CV 38,6,7
M 132
CV 39,8,9
AR 6,57
AR 7,81
M 133
TX 58,9216
TX 62,23040
AR 8,105
M 122
DG 1024
AR 9,129
M 123
EF 6,52
EF 7,53
EF 8,54
M 134
EF 9,55
CV 40,6,7
CV 41,8,9
M 135
TY 66,9728
TY 70,23552
AR 6,58
v_mfma_f32_16x16x32_f16 v[216:219], v[72:75], a[16:19], v[128:131]
bc 48,4,64,56
AR 7,82
AR 8,106
M 125
mh 12
ST 16,768
AR 9,130
EF 6,52
v_mfma_f32_16x16x32_f16 v[240:243], v[72:75], a[24:27], v[128:131]
ad 64,800
H 0
EF 7,53
M 137
TX 74,10752
TX 78,24576
EF 8,54
v_mfma_f32_16x16x32_f16 v[220:223], v[80:83], a[16:19], v[128:131]
WL 4
Q 0
EF 9,55
M 127
Q 2
CV 42,6,7
v_mfma_f32_16x16x32_f16 v[244:247], v[80:83], a[24:27], v[128:131]
Q 4
CV 43,8,9
M 139
TY 82,11264
TY 86,25088
AR 6,59
M 128
Q 6
AR 7,83
M 129
WL 4
Q 8
AR 8,107
M 140
Q 10
AR 9,131
M 141
TX 90,12288
TX 94,26112
EF 6,52
M 130
Q 12
EF 7,53
M 131
R 4
ST 18,1536
M 142
Q 14
EF 8,54
M 143
TY 98,12800
TY 102,26624
EF 9,55
M 0
R 6
CV 44,6,7
M 1
WL 8
Q 16
CV 45,8,9
M 12
Q 18
BV 52,3584
M 13
Q 20
M 2
Q 22
M 3
Q 24
M 14
Q 26
M 15
Q 28
M 4
Q 30
M 5
WL 5
Q 32
ST 20,2304
M 16
Q 34
M 17
Q 36
M 6
Q 38
M 7
WL 1
Q 40
M 18
Q 42
M 19
Q 44
v_mfma_f32_16x16x32_f16 a[52:55], v[88:91], a[0:3], a[32:35]
Q 46
M 9
H 1
AR 6,148
v_mfma_f32_16x16x32_f16 a[76:79], v[88:91], a[8:11], a[32:35]
)"
R"(AR 7,172
AR 8,196
M 21
EF 6,12
EF 7,13
v_mfma_f32_16x16x32_f16 a[56:59], v[96:99], a[0:3], a[32:35]
H 2
EF 8,46
M 11
R 8
v_mfma_f32_16x16x32_f16 a[80:83], v[96:99], a[8:11], a[32:35]
ae 42,72,0
ST 22,128
M 23
R 10
PA 12288
DF
M 24
EF 152,47
CV 6,6,7
M 25
CV 7,8,152
AW 232,6
M 26
AW 233,7
AR 6,149
M 27
AR 7,173
AR 8,197
M 28
EF 6,12
EF 7,13
M 29
EF 8,46
EF 153,47
M 30
CV 6,6,7
CV 7,8,153
M 31
AW 234,6
AW 235,7
v_mfma_f32_16x16x32_f16 a[100:103], v[88:91], a[16:19], a[32:35]
AR 6,150
AR 7,174
M 33
WV 11
AR 8,198
EF 6,12
v_mfma_f32_16x16x32_f16 a[104:107], v[96:99], a[16:19], a[32:35]
WL 0
FP
ST 24,896
M 35
EF 7,13
EF 8,46
M 36
EF 154,47
CV 6,6,7
M 37
CV 7,8,154
AW 236,6
M 38
AW 237,7
AR 6,151
M 39
AR 7,175
AR 8,199
M 40
EF 6,12
EF 7,13
M 41
EF 8,46
EF 155,47
M 42
CV 6,6,7
CV 7,8,155
M 43
AW 238,6
AW 239,7
v_mfma_f32_16x16x32_f16 a[124:127], v[88:91], a[24:27], a[32:35]
AR 6,152
AR 7,176
EF 6,12
M 45
FR
EF 7,13
EF 132,46
v_mfma_f32_16x16x32_f16 a[128:131], v[96:99], a[24:27], a[32:35]
ST 26,1664
EF 156,47
CV 14,6,7
M 47
CV 15,132,156
AR 6,153
AR 7,177
M 48
EF 6,12
EF 7,13
EF 133,46
M 49
EF 157,47
CV 16,6,7
CV 17,133,157
M 50
AR 6,154
AR 7,178
EF 6,12
M 51
EF 7,13
EF 134,46
EF 158,47
M 52
CV 18,6,7
CV 19,134,158
AR 6,155
M 53
WL 0
FQ 88
AR 7,179
EF 6,12
M 54
EF 7,13
EF 135,46
EF 159,47
M 55
CV 20,6,7
CV 21,135,159
v_mfma_f32_16x16x32_f16 a[148:151], v[88:91], a[0:3], a[32:35]
FZ 88
R 24
EF 176,52
M 57
mw 72
R 26
EF 200,53
v_mfma_f32_16x16x32_f16 a[152:155], v[96:99], a[0:3], a[32:35]
TY 106,520
TY 110,14344
ST 28,2432
M 59
R 28
EF 224,54
M 60
ad 84,336
W 0
EF 248,55
M 61
CV 6,176,200
CV 7,224,248
AW 240,6
M 62
AW 241,7
EF 177,52
EF 201,53
M 63
W 1
EF 225,54
EF 249,55
M 64
CV 6,177,201
CV 7,225,249
AW 242,6
M 65
AW 243,7
EF 178,52
EF 202,53
M 66
W 2
EF 226,54
EF 250,55
M 67
WL 3
AN 104,106
AN 105,107
CV 6,178,202
v_mfma_f32_16x16x32_f16 a[172:175], v[88:91], a[8:11], a[32:35]
SH 106
SH 107
CV 7,226,250
M 69
mw 75
AN 106,106
AN 107,107
AW 244,6
v_mfma_f32_16x16x32_f16 a[176:179], v[96:99], a[8:11], a[32:35]
WL 0
AN 108,110
AN 109,111
AW 245,7
M 71
R 30
EF 179,52
M 72
SH 110
SH 111
EF 203,53
M 73
AN 110,110
AN 111,111
EF 227,54
M 74
TY 114,2056
TY 118,15880
EF 251,55
M 75
ad 84,342
W 0
CV 6,179,203
M 76
ae 42,72,0
CV 7,227,251
AW 246,6
M 77
s_bfe_u32 s52, s50, 0x10003
v_mad_i32_i24 v50, v49, s52, v48
ST 30,256
AW 247,7
M 78
W 1
EF 180,52
EF 204,53
M 79
EF 228,54
EF 252,55
CV 6,180,204
v_mfma_f32_16x16x32_f16 a[196:199], v[88:91], a[16:19], a[32:35]
CV 7,228,252
AW 248,6
AW 249,7
M 81
W 2
EF 181,52
EF 205,53
v_mfma_f32_16x16x32_f16 v[132:135], v[96:99], a[16:19], v[128:131]
EF 229,54
EF 253,55
CV 6,181,205
M 83
WL 0
R 32
CV 7,229,253
M 84
mw 0
AN 112,114
AN 113,115
AW 250,6
M 85
SH 114
SH 115
AW 251,7
M 86
AN 114,114
AN 115,115
EF 182,52
M 87
AN 116,118
AN 117,119
EF 206,53
M 88
SH 118
SH 119
EF 230,54
M 89
AN 118,118
AN 119,119
ST 32,1024
M 90
TY 122,3592
TY 126,17416
EF 254,55
M 91
ad 84,384
CV 6,182,206
v_mfma_f32_16x16x32_f16 v[152:155], v[88:91], a[24:27], v[128:131]
W 0
CV 7,230,254
AW 252,6
M 93
AW 253,7
EF 183,52
EF 207,53
v_mfma_f32_16x16x32_f16 v[156:159], v[96:99], a[24:27], v[128:131]
EF 231,54
EF 255,55
CV 6,183,207
M 95
R 34
CV 7,231,255
M 96
W 1
AW 254,6
AW 255,7
M 97
M 98
M 99
WL 3
AN 120,122
AN 121,123
M 100
SH 122
SH 123
M 101
AN 122,122
AN 123,123
ST 34,1792
M 102
WL 2
AN 124,126
AN 125,127
M 103
SH 126
SH 127
v_mfma_f32_16x16x32_f16 v[176:179], v[88:91], a[0:3], v[128:131]
AN 126,126
AN 127,127
M 105
W 2
v_mfma_f32_16x16x32_f16 v[180:183], v[96:99], a[0:3], v[128:131]
M 107
R 48
M 108
mw 3
M 109
ad 84,390
W 0
M 110
M 111
M 112
W 1
M 113
ST 36,2560
M 114
M 115
W 2
v_mfma_f32_16x16x32_f16 v[200:203], v[88:91], a[8:11], v[128:131]
M 117
v_mfma_f32_16x16x32_f16 v[204:207], v[96:99], a[8:11], v[128:131]
bc 49,5,66,60
M 119
WL 2
R 50
M 120
mh 18
M 121
ad 66,992
H 0
M 132
M 133
TX 58,0
TX 62,13824
M 122
DG 1024
M 123
ae 42,72,96
M 134
s_bfe_u32 s52, s50, 0x10005
v_mad_i32_i24 v50, v49, s52, v48
ST 38,0
M 135
TY 66,512
TY 70,14336
M 124
bc 48,5,64,56
M 125
mh 21
M 136
ad 64,992
H 0
M 137
TX 74,1536
TX 78,15360
M 126
WL 4
Q 0
M 127
Q 2
M 138
Q 4
M 139
TY 82,2048
TY 86,15872
v_mfma_f32_16x16x32_f16 v[224:227], v[88:91], a[16:19], v[128:131]
Q 6
M 129
WL 4
Q 8
v_mfma_f32_16x16x32_f16 v[248:251], v[88:91], a[24:27], v[128:131]
Q 10
ST 40,768
M 141
TX 90,3072
TX 94,16896
v_mfma_f32_16x16x32_f16 v[228:231], v[96:99], a[16:19], v[128:131]
Q 12
M 131
R 52
v_mfma_f32_16x16x32_f16 v[252:255], v[96:99], a[24:27], v[128:131]
Q 14
M 143
TY 98,3584
)"
R"(TY 102,17408
M 0
R 54
M 1
WL 10
Q 16
M 12
Q 18
M 13
Q 20
M 2
Q 22
M 3
WL 8
Q 24
M 14
Q 26
ST 42,1536
M 15
Q 28
M 4
Q 30
M 5
WL 6
Q 32
M 16
Q 34
M 17
Q 36
M 6
Q 38
M 7
WL 1
Q 40
M 18
Q 42
M 19
Q 44
M 8
Q 46
M 9
H 1
M 20
ST 44,2304
M 21
M 10
H 2
M 11
R 56
M 22
M 23
R 58
M 24
M 25
M 26
M 27
M 28
M 29
M 30
ae 42,72,96
ST 14,128
M 31
M 32
M 33
WV 10
M 34
WL 0
FP
M 35
M 36
M 37
M 38
M 39
M 40
M 41
M 42
ST 16,896
M 43
M 44
M 45
FR
M 46
M 47
M 48
M 49
M 50
M 51
M 52
M 53
WL 0
FQ 92
M 54
ST 18,1664
M 55
M 56
FZ 92
R 72
M 57
mw 24
R 74
M 58
TY 106,5128
TY 110,18952
M 59
R 76
M 60
ad 84,432
W 0
M 61
M 62
M 63
W 1
M 64
M 65
M 66
W 2
ST 20,2432
M 67
WL 3
AN 104,106
AN 105,107
M 68
SH 106
SH 107
M 69
mw 27
AN 106,106
AN 107,107
M 70
WL 2
AN 108,110
AN 109,111
M 71
R 78
M 72
SH 110
SH 111
M 73
AN 110,110
AN 111,111
M 74
TY 114,6664
TY 118,20488
M 75
ad 84,438
W 0
M 76
M 77
M 78
W 1
M 79
M 80
M 81
W 2
M 82
M 83
R 80
M 84
mw 48
WL 3
AN 112,114
AN 113,115
M 85
SH 114
SH 115
M 86
AN 114,114
AN 115,115
M 87
WL 2
AN 116,118
AN 117,119
M 88
SH 118
SH 119
M 89
AN 118,118
AN 119,119
M 90
TY 122,8200
TY 126,22024
M 91
ad 84,480
M 92
W 0
M 93
M 94
M 95
R 82
M 96
W 1
M 97
M 98
M 99
WL 3
AN 120,122
AN 121,123
M 100
SH 122
SH 123
M 101
AN 122,122
AN 123,123
M 102
WL 2
AN 124,126
AN 125,127
M 103
SH 126
SH 127
M 104
AN 126,126
AN 127,127
M 105
W 2
M 106
M 107
R 0
M 108
mw 51
M 109
ad 84,486
W 0
M 110
M 111
M 112
W 1
M 113
M 114
M 115
W 2
M 116
M 117
M 118
bc 49,6,66,60
M 119
WL 2
R 2
M 120
mh 0
M 121
ad 66,1184
H 0
M 132
M 133
TX 58,4608
TX 62,18432
M 122
DG 1024
M 123
M 134
M 135
TY 66,5120
TY 70,18944
M 124
bc 48,6,64,56
M 125
mh 3
ae 44,70,384
M 136
ad 64,1184
H 0
M 137
TX 74,6144
TX 78,19968
M 126
WL 4
Q 0
M 127
Q 2
M 138
Q 4
M 139
TY 82,6656
TY 86,20480
M 128
Q 6
M 129
WL 4
Q 8
M 140
Q 10
M 141
TX 90,7680
TX 94,21504
M 130
Q 12
M 131
R 4
M 142
Q 14
M 143
TY 98,8192
TY 102,22016
M 0
R 6
M 1
WL 10
Q 16
M 12
Q 18
M 13
Q 20
M 2
Q 22
M 3
WL 8
Q 24
M 14
Q 26
M 15
Q 28
M 4
Q 30
M 5
WL 6
Q 32
M 16
Q 34
M 17
Q 36
M 6
Q 38
M 7
WL 2
Q 40
M 18
Q 42
M 19
Q 44
M 8
Q 46
M 9
H 1
M 20
ae 42,72,0
M 21
s_bfe_u32 s52, s50, 0x10003
v_mad_i32_i24 v50, v49, s52, v48
SA 200,0
M 10
H 2
M 11
R 8
M 22
s_bitcmp1_b32 s50, 6
s_cselect_b32 s51, 24576, 0
M 23
R 10
PA 12288
DF
M 24
v_add_u32 v47, s51, v3
XL 22,0
M 25
XL 24,768
M 26
XL 26,1536
M 27
XL 28,2304
M 28
M 29
M 30
M 31
M 32
M 33
WV 11
M 34
WL 0
FP
M 35
M 36
M 37
M 38
M 39
M 40
M 41
M 42
M 43
M 44
M 45
FR
M 46
M 47
M 48
M 49
M 50
M 51
M 52
M 53
WL 0
FQ 96
M 54
M 55
M 56
FZ 96
R 24
M 57
mw 72
R 26
)"
R"(M 58
TY 106,9736
TY 110,23560
M 59
R 28
M 60
ad 84,528
W 0
M 61
M 62
M 63
W 1
M 64
M 65
M 66
W 2
M 67
WL 3
AN 104,106
AN 105,107
M 68
SH 106
SH 107
M 69
mw 75
AN 106,106
AN 107,107
M 70
WL 2
AN 108,110
AN 109,111
M 71
R 30
M 72
SH 110
SH 111
M 73
AN 110,110
AN 111,111
M 74
TY 114,11272
TY 118,25096
M 75
ad 84,534
W 0
M 76
M 77
M 78
W 1
M 79
M 80
M 81
W 2
M 82
M 83
R 32
M 84
mw 0
WL 3
AN 112,114
AN 113,115
M 85
SH 114
SH 115
M 86
AN 114,114
AN 115,115
M 87
WL 2
AN 116,118
AN 117,119
M 88
SH 118
SH 119
M 89
AN 118,118
AN 119,119
M 90
TY 122,12808
TY 126,26632
M 91
ad 84,576
M 92
W 0
SA 202,768
M 93
M 94
M 95
R 34
M 96
W 1
M 97
M 98
M 99
WL 3
AN 120,122
AN 121,123
M 100
SH 122
SH 123
M 101
AN 122,122
AN 123,123
M 102
WL 2
AN 124,126
AN 125,127
M 103
SH 126
SH 127
M 104
AN 126,126
AN 127,127
M 105
W 2
M 106
M 107
R 48
M 108
mw 3
M 109
ad 84,582
W 0
M 110
M 111
M 112
W 1
M 113
M 114
M 115
W 2
M 116
M 117
M 118
bc 49,7,66,60
M 119
WL 2
R 50
M 120
mh 9
M 121
ad 66,1376
H 0
M 132
M 133
TX 58,9216
TX 62,23040
M 122
DG 1024
M 123
M 134
M 135
TY 66,9728
TY 70,23552
M 124
bc 48,7,64,56
M 125
mh 12
M 136
ad 64,1376
H 0
M 137
TX 74,10752
TX 78,24576
M 126
WL 4
Q 0
M 127
Q 2
M 138
Q 4
M 139
TY 82,11264
TY 86,25088
M 128
Q 6
M 129
WL 4
Q 8
M 140
Q 10
M 141
TX 90,12288
TX 94,26112
M 130
Q 12
M 131
R 52
M 142
Q 14
M 143
TY 98,12800
TY 102,26624
M 0
R 54
M 1
WL 10
Q 16
M 12
Q 18
M 13
Q 20
M 2
Q 22
M 3
WL 8
Q 24
M 14
Q 26
M 15
Q 28
M 4
Q 30
M 5
WL 6
Q 32
M 16
Q 34
M 17
Q 36
M 6
Q 38
M 7
WL 2
Q 40
M 18
Q 42
M 19
Q 44
M 8
Q 46
M 9
H 1
SA 204,1536
M 20
ae 44,70,384
M 21
s_bfe_u32 s51, s50, 0x10007
v_mad_i32_i24 v47, v49, s51, v48
XL 30,0
M 10
H 2
XL 32,768
M 11
R 56
M 22
XL 34,1536
M 23
R 58
M 24
XL 36,2304
M 25
M 26
M 27
M 28
M 29
M 30
M 31
M 32
M 33
WV 10
M 34
WL 0
FP
M 35
M 36
M 37
M 38
M 39
M 40
UL 6,22
UH 7,22
M 41
AR 8,36
AR 9,60
M 42
AD 8,6
AD 9,7
M 43
AW 36,8
AW 60,9
M 44
UL 6,23
UH 7,23
M 45
FR
AR 8,84
M 46
AR 9,108
AD 8,6
M 47
AD 9,7
AW 84,8
M 48
AW 108,9
UL 6,24
M 49
UH 7,24
AR 8,37
M 50
AR 9,61
AD 8,6
M 51
AD 9,7
AW 37,8
M 52
AW 61,9
UL 6,25
M 53
WL 0
FQ 100
UH 7,25
M 54
AR 8,85
AR 9,109
M 55
AD 8,6
AD 9,7
M 56
FZ 100
R 72
M 57
mw 24
R 74
M 58
TY 106,520
TY 110,14344
M 59
R 76
M 60
ad 84,624
W 0
M 61
AW 85,8
AW 109,9
M 62
UL 6,26
UH 7,26
M 63
W 1
AR 8,38
M 64
AR 9,62
AD 8,6
M 65
AD 9,7
AW 38,8
M 66
W 2
AW 62,9
M 67
WL 3
AN 104,106
AN 105,107
M 68
SH 106
SH 107
M 69
mw 27
AN 106,106
AN 107,107
M 70
WL 2
AN 108,110
AN 109,111
M 71
R 78
M 72
SH 110
SH 111
M 73
AN 110,110
AN 111,111
M 74
TY 114,2056
TY 118,15880
M 75
ad 84,630
W 0
M 76
UL 6,27
UH 7,27
M 77
AR 8,86
AR 9,110
M 78
W 1
AD 8,6
M 79
AD 9,7
AW 86,8
M 80
AW 110,9
UL 6,28
M 81
W 2
SA 206,2304
M 82
UH 7,28
AR 8,39
)"
R"(M 83
WL 1
R 80
M 84
mw 48
AN 112,114
AN 113,115
M 85
SH 114
SH 115
M 86
AN 114,114
AN 115,115
M 87
WL 2
AN 116,118
AN 117,119
M 88
SH 118
SH 119
M 89
AN 118,118
AN 119,119
M 90
TY 122,3592
TY 126,17416
M 91
ad 84,672
M 92
W 0
AR 9,63
M 93
AD 8,6
AD 9,7
M 94
AW 39,8
AW 63,9
M 95
R 82
M 96
W 1
UL 6,29
M 97
UH 7,29
AR 8,87
M 98
AR 9,111
AD 8,6
M 99
WL 3
AN 120,122
AN 121,123
M 100
SH 122
SH 123
M 101
AN 122,122
AN 123,123
M 102
WL 2
AN 124,126
AN 125,127
M 103
SH 126
SH 127
M 104
AN 126,126
AN 127,127
M 105
W 2
AD 9,7
M 106
AW 87,8
AW 111,9
M 107
R 0
M 108
mw 51
M 109
ad 84,678
W 0
M 110
M 111
M 112
W 1
M 113
M 114
M 115
W 2
M 116
M 117
M 118
bc 49,8,66,60
M 119
WL 2
R 2
M 120
mh 18
M 121
ad 66,1568
H 0
M 132
M 133
TX 58,0
TX 62,13824
M 122
DG 1024
M 123
M 134
M 135
TY 66,512
TY 70,14336
M 124
bc 48,8,64,56
M 125
mh 21
M 136
ad 64,1568
H 0
M 137
TX 74,1536
TX 78,15360
M 126
WL 4
Q 0
M 127
Q 2
M 138
Q 4
M 139
TY 82,2048
TY 86,15872
M 128
Q 6
M 129
WL 4
Q 8
M 140
Q 10
M 141
TX 90,3072
TX 94,16896
M 130
Q 12
M 131
R 4
M 142
Q 14
M 143
TY 98,3584
TY 102,17408
M 0
R 6
M 1
WL 10
Q 16
M 12
Q 18
M 13
Q 20
M 2
Q 22
M 3
WL 8
Q 24
M 14
Q 26
M 15
Q 28
M 4
Q 30
M 5
WL 6
Q 32
M 16
Q 34
M 17
Q 36
M 6
Q 38
M 7
WL 2
Q 40
M 18
Q 42
M 19
Q 44
M 8
Q 46
M 9
H 1
M 20
ae 42,72,0
M 21
SA 208,128
M 10
H 2
M 11
R 8
M 22
ae 44,68,384
M 23
R 10
PA 12288
DF
M 24
s_bitcmp1_b32 s50, 8
s_cselect_b32 s51, 24576, 0
v_add_u32 v47, s51, v3
M 25
XL 38,0
M 26
XL 40,768
M 27
XL 42,1536
M 28
XL 44,2304
M 29
M 30
M 31
M 32
M 33
WV 11
M 34
WL 0
FP
M 35
M 36
M 37
M 38
M 39
M 40
M 41
M 42
UL 6,30
UH 7,30
M 43
AR 8,40
AR 9,64
M 44
AD 8,6
AD 9,7
M 45
FR
AW 40,8
M 46
AW 64,9
UL 6,31
M 47
UH 7,31
AR 8,88
M 48
AR 9,112
AD 8,6
M 49
AD 9,7
AW 88,8
M 50
AW 112,9
UL 6,32
M 51
UH 7,32
AR 8,41
M 52
AR 9,65
AD 8,6
M 53
WL 0
FQ 104
AD 9,7
M 54
AW 41,8
AW 65,9
M 55
UL 6,33
UH 7,33
M 56
FZ 104
R 24
M 57
mw 72
R 26
M 58
TY 106,5128
TY 110,18952
M 59
R 28
M 60
ad 84,720
W 0
M 61
AR 8,89
AR 9,113
M 62
AD 8,6
AD 9,7
M 63
W 1
AW 89,8
M 64
AW 113,9
UL 6,34
M 65
UH 7,34
AR 8,42
M 66
W 2
AR 9,66
M 67
WL 3
AN 104,106
AN 105,107
M 68
SH 106
SH 107
M 69
mw 75
AN 106,106
AN 107,107
M 70
WL 2
AN 108,110
AN 109,111
M 71
R 30
M 72
SH 110
SH 111
M 73
AN 110,110
AN 111,111
M 74
TY 114,6664
TY 118,20488
M 75
ad 84,726
W 0
M 76
AD 8,6
AD 9,7
M 77
AW 42,8
AW 66,9
M 78
W 1
UL 6,35
M 79
UH 7,35
AR 8,90
M 80
AR 9,114
AD 8,6
M 81
W 2
AD 9,7
M 82
AW 90,8
AW 114,9
M 83
WL 1
R 32
M 84
mw 0
AN 112,114
AN 113,115
M 85
SH 114
SH 115
M 86
AN 114,114
AN 115,115
M 87
WL 2
AN 116,118
AN 117,119
M 88
SH 118
SH 119
M 89
AN 118,118
AN 119,119
M 90
TY 122,8200
TY 126,22024
M 91
ad 84,768
M 92
W 0
SA 210,896
M 93
UL 6,36
UH 7,36
M 94
AR 8,43
AR 9,67
M 95
R 34
M 96
W 1
AD 8,6
)"
R"(M 97
AD 9,7
AW 43,8
M 98
AW 67,9
UL 6,37
M 99
WL 3
AN 120,122
AN 121,123
M 100
SH 122
SH 123
M 101
AN 122,122
AN 123,123
M 102
WL 2
AN 124,126
AN 125,127
M 103
SH 126
SH 127
M 104
AN 126,126
AN 127,127
M 105
W 2
UH 7,37
M 106
AR 8,91
AR 9,115
M 107
R 48
M 108
mw 3
AD 8,6
AD 9,7
M 109
ad 84,774
W 0
M 110
AW 91,8
AW 115,9
M 111
M 112
W 1
M 113
M 114
M 115
W 2
M 116
M 117
M 118
bc 49,9,66,60
M 119
WL 2
R 50
M 120
mh 0
M 121
ad 66,1760
H 0
M 132
M 133
TX 58,4608
TX 62,18432
M 122
DG 1024
M 123
M 134
M 135
TY 66,5120
TY 70,18944
M 124
bc 48,9,64,56
M 125
mh 3
M 136
ad 64,1760
H 0
M 137
TX 74,6144
TX 78,19968
M 126
WL 4
Q 0
M 127
Q 2
M 138
Q 4
M 139
TY 82,6656
TY 86,20480
M 128
Q 6
M 129
WL 4
Q 8
M 140
Q 10
M 141
TX 90,7680
TX 94,21504
M 130
Q 12
M 131
R 52
M 142
Q 14
M 143
TY 98,8192
TY 102,22016
M 0
R 54
M 1
WL 10
Q 16
M 12
Q 18
M 13
Q 20
M 2
Q 22
M 3
WL 8
Q 24
M 14
Q 26
M 15
Q 28
M 4
Q 30
M 5
WL 6
Q 32
M 16
Q 34
M 17
Q 36
M 6
Q 38
M 7
WL 2
Q 40
M 18
Q 42
M 19
Q 44
M 8
Q 46
M 9
H 1
SA 212,1664
M 20
ae 44,68,384
M 21
s_bfe_u32 s51, s50, 0x10009
v_mad_i32_i24 v47, v49, s51, v48
XL 14,0
M 10
H 2
XL 16,768
M 11
R 56
M 22
XL 18,1536
M 23
R 58
M 24
XL 20,2304
M 25
M 26
M 27
M 28
M 29
M 30
M 31
M 32
M 33
WV 10
M 34
WL 0
FP
M 35
M 36
M 37
M 38
M 39
M 40
M 41
M 42
M 43
M 44
UL 6,38
UH 7,38
M 45
FR
AR 8,44
M 46
AR 9,68
AD 8,6
M 47
AD 9,7
AW 44,8
M 48
AW 68,9
UL 6,39
M 49
UH 7,39
AR 8,92
M 50
AR 9,116
AD 8,6
M 51
AD 9,7
AW 92,8
M 52
AW 116,9
UL 6,40
M 53
WL 0
FQ 108
UH 7,40
M 54
AR 8,45
AR 9,69
M 55
AD 8,6
AD 9,7
M 56
FZ 108
R 72
M 57
mw 24
R 74
M 58
TY 106,9736
TY 110,23560
M 59
R 76
M 60
ad 84,816
W 0
M 61
AW 45,8
AW 69,9
M 62
UL 6,41
UH 7,41
M 63
W 1
AR 8,93
M 64
AR 9,117
AD 8,6
M 65
AD 9,7
AW 93,8
M 66
W 2
AW 117,9
M 67
WL 3
AN 104,106
AN 105,107
M 68
SH 106
SH 107
M 69
mw 27
AN 106,106
AN 107,107
M 70
WL 2
AN 108,110
AN 109,111
M 71
R 78
M 72
SH 110
SH 111
M 73
AN 110,110
AN 111,111
M 74
TY 114,11272
TY 118,25096
M 75
ad 84,822
W 0
M 76
UL 6,42
UH 7,42
M 77
AR 8,46
AR 9,70
M 78
W 1
AD 8,6
M 79
AD 9,7
AW 46,8
M 80
AW 70,9
UL 6,43
M 81
W 2
SA 214,2432
M 82
UH 7,43
AR 8,94
M 83
WL 1
R 80
M 84
mw 48
AN 112,114
AN 113,115
M 85
SH 114
SH 115
M 86
AN 114,114
AN 115,115
M 87
WL 2
AN 116,118
AN 117,119
M 88
SH 118
SH 119
M 89
AN 118,118
AN 119,119
M 90
TY 122,12808
TY 126,26632
M 91
ad 84,864
M 92
W 0
AR 9,118
M 93
AD 8,6
AD 9,7
M 94
AW 94,8
AW 118,9
M 95
R 82
M 96
W 1
UL 6,44
M 97
UH 7,44
AR 8,47
M 98
AR 9,71
AD 8,6
M 99
WL 3
AN 120,122
AN 121,123
M 100
SH 122
SH 123
M 101
AN 122,122
AN 123,123
M 102
WL 2
AN 124,126
AN 125,127
M 103
SH 126
SH 127
M 104
AN 126,126
AN 127,127
M 105
W 2
AD 9,7
M 106
AW 47,8
AW 71,9
M 107
R 0
M 108
mw 51
UL 6,45
UH 7,45
M 109
ad 84,870
W 0
M 110
AR 8,95
AR 9,119
M 111
AD 8,6
)"
R"(AD 9,7
M 112
W 1
AW 95,8
M 113
AW 119,9
M 114
M 115
W 2
M 116
M 117
M 118
bc 49,10,66,60
M 119
WL 2
R 2
M 120
mh 9
M 121
ad 66,1952
H 0
M 132
M 133
TX 58,9216
TX 62,23040
M 122
DG 1024
M 123
M 134
M 135
TY 66,9728
TY 70,23552
M 124
bc 48,10,64,56
M 125
mh 12
M 136
ad 64,1952
H 0
M 137
TX 74,10752
TX 78,24576
M 126
WL 4
Q 0
M 127
Q 2
M 138
Q 4
M 139
TY 82,11264
TY 86,25088
M 128
Q 6
M 129
WL 4
Q 8
M 140
Q 10
M 141
TX 90,12288
TX 94,26112
M 130
Q 12
M 131
R 4
M 142
Q 14
M 143
TY 98,12800
TY 102,26624
M 0
R 6
M 1
WL 10
Q 16
M 12
Q 18
M 13
Q 20
M 2
Q 22
M 3
WL 8
Q 24
M 14
Q 26
M 15
Q 28
M 4
Q 30
M 5
WL 6
Q 32
M 16
Q 34
M 17
Q 36
M 6
Q 38
M 7
WL 2
Q 40
M 18
Q 42
M 19
Q 44
M 8
Q 46
M 9
H 1
M 20
ae 42,72,0
M 21
s_bitcmp1_b32 s50, 2
s_cselect_b32 s52, 24576, 0
v_add_u32 v50, s52, v3
M 10
H 2
SA 216,256
M 11
R 8
M 22
ae 44,68,480
M 23
R 10
PA 12288
DF
M 24
s_bitcmp1_b32 s50, 10
s_cselect_b32 s51, 24576, 0
v_add_u32 v47, s51, v3
M 25
XL 22,0
M 26
XL 24,768
M 27
XL 26,1536
M 28
XL 28,2304
M 29
M 30
M 31
M 32
M 33
WV 11
M 34
WL 0
FP
M 35
M 36
M 37
M 38
M 39
M 40
M 41
M 42
M 43
M 44
M 45
FR
M 46
UL 6,14
UH 7,14
M 47
AR 8,48
AR 9,72
M 48
AD 8,6
AD 9,7
M 49
AW 48,8
AW 72,9
M 50
UL 6,15
UH 7,15
M 51
AR 8,96
AR 9,120
M 52
AD 8,6
AD 9,7
M 53
WL 0
FQ 112
AW 96,8
M 54
AW 120,9
UL 6,16
M 55
UH 7,16
AR 8,49
M 56
FZ 112
R 24
M 57
mw 72
R 26
M 58
TY 106,520
TY 110,14344
M 59
R 28
M 60
ad 84,912
W 0
M 61
AR 9,73
AD 8,6
M 62
AD 9,7
AW 49,8
M 63
W 1
AW 73,9
M 64
UL 6,17
UH 7,17
M 65
AR 8,97
AR 9,121
M 66
W 2
AD 8,6
M 67
WL 3
AN 104,106
AN 105,107
M 68
SH 106
SH 107
M 69
mw 75
AN 106,106
AN 107,107
M 70
WL 2
AN 108,110
AN 109,111
M 71
R 30
M 72
SH 110
SH 111
M 73
AN 110,110
AN 111,111
M 74
TY 114,2056
TY 118,15880
M 75
ad 84,918
W 0
M 76
AD 9,7
AW 97,8
M 77
AW 121,9
UL 6,18
M 78
W 1
UH 7,18
M 79
AR 8,50
AR 9,74
M 80
AD 8,6
AD 9,7
M 81
W 2
AW 50,8
M 82
AW 74,9
UL 6,19
M 83
WL 1
R 32
M 84
mw 0
AN 112,114
AN 113,115
M 85
SH 114
SH 115
M 86
AN 114,114
AN 115,115
M 87
WL 2
AN 116,118
AN 117,119
M 88
SH 118
SH 119
M 89
AN 118,118
AN 119,119
M 90
TY 122,3592
TY 126,17416
M 91
ad 84,960
M 92
W 0
SA 218,1024
M 93
UH 7,19
AR 8,98
M 94
AR 9,122
AD 8,6
M 95
R 34
M 96
W 1
AD 9,7
M 97
AW 98,8
AW 122,9
M 98
UL 6,20
UH 7,20
M 99
WL 3
AN 120,122
AN 121,123
M 100
SH 122
SH 123
M 101
AN 122,122
AN 123,123
M 102
WL 2
AN 124,126
AN 125,127
M 103
SH 126
SH 127
M 104
AN 126,126
AN 127,127
M 105
W 2
AR 8,51
M 106
AR 9,75
AD 8,6
M 107
R 48
M 108
mw 3
AD 9,7
AW 51,8
M 109
ad 84,966
W 0
M 110
AW 75,9
UL 6,21
M 111
UH 7,21
AR 8,99
M 112
W 1
AR 9,123
M 113
AD 8,6
AD 9,7
M 114
AW 99,8
AW 123,9
M 115
W 2
M 116
M 117
M 118
bc 49,11,66,60
M 119
WL 2
R 50
M 120
mh 18
M 121
ad 66,2144
H 0
M 132
M 133
TX 58,0
TX 62,13824
M 122
DG 1024
M 123
M 134
M 135
TY 66,512
TY 70,14336
M 124
bc 48,11,64,56
M 125
)"
R"(mh 21
M 136
ad 64,2144
H 0
M 137
TX 74,1536
TX 78,15360
M 126
WL 4
Q 0
M 127
Q 2
M 138
Q 4
M 139
TY 82,2048
TY 86,15872
M 128
Q 6
M 129
WL 4
Q 8
M 140
Q 10
M 141
TX 90,3072
TX 94,16896
M 130
Q 12
M 131
R 52
M 142
Q 14
M 143
TY 98,3584
TY 102,17408
M 0
R 54
M 1
WL 10
Q 16
M 12
Q 18
M 13
Q 20
M 2
Q 22
M 3
WL 8
Q 24
M 14
Q 26
M 15
Q 28
M 4
Q 30
M 5
WL 6
Q 32
M 16
Q 34
M 17
Q 36
M 6
Q 38
M 7
WL 2
Q 40
M 18
Q 42
M 19
Q 44
M 8
Q 46
M 9
H 1
SA 220,1792
M 20
ae 44,68,480
M 21
s_bfe_u32 s51, s50, 0x1000b
v_mad_i32_i24 v47, v49, s51, v48
XL 30,0
M 10
H 2
XL 32,768
M 11
R 56
M 22
XL 34,1536
M 23
R 58
M 24
XL 36,2304
M 25
M 26
M 27
M 28
M 29
M 30
M 31
M 32
M 33
WV 10
M 34
WL 0
FP
M 35
M 36
M 37
M 38
M 39
M 40
M 41
M 42
M 43
M 44
M 45
FR
M 46
M 47
M 48
UL 6,22
UH 7,22
M 49
AR 8,52
AR 9,76
M 50
AD 8,6
AD 9,7
M 51
AW 52,8
AW 76,9
M 52
UL 6,23
UH 7,23
M 53
WL 0
FQ 116
AR 8,100
M 54
AR 9,124
AD 8,6
M 55
AD 9,7
AW 100,8
M 56
FZ 116
R 72
M 57
mw 24
R 74
M 58
TY 106,5128
TY 110,18952
M 59
R 76
M 60
ad 84,1008
W 0
M 61
AW 124,9
UL 6,24
M 62
UH 7,24
AR 8,53
M 63
W 1
AR 9,77
M 64
AD 8,6
AD 9,7
M 65
AW 53,8
AW 77,9
M 66
W 2
UL 6,25
M 67
WL 3
AN 104,106
AN 105,107
M 68
SH 106
SH 107
M 69
mw 27
AN 106,106
AN 107,107
M 70
WL 2
AN 108,110
AN 109,111
M 71
R 78
M 72
SH 110
SH 111
M 73
AN 110,110
AN 111,111
M 74
TY 114,6664
TY 118,20488
M 75
ad 84,1014
W 0
M 76
UH 7,25
AR 8,101
M 77
AR 9,125
AD 8,6
M 78
W 1
AD 9,7
M 79
AW 101,8
AW 125,9
M 80
UL 6,26
UH 7,26
M 81
W 2
SA 222,2560
M 82
AR 8,54
AR 9,78
M 83
WL 1
R 80
M 84
mw 48
AN 112,114
AN 113,115
M 85
SH 114
SH 115
M 86
AN 114,114
AN 115,115
M 87
WL 2
AN 116,118
AN 117,119
M 88
SH 118
SH 119
M 89
AN 118,118
AN 119,119
M 90
TY 122,8200
TY 126,22024
M 91
ad 84,1056
M 92
W 0
AD 8,6
M 93
AD 9,7
AW 54,8
M 94
AW 78,9
UL 6,27
M 95
R 82
M 96
W 1
UH 7,27
M 97
AR 8,102
AR 9,126
M 98
AD 8,6
AD 9,7
M 99
WL 3
AN 120,122
AN 121,123
M 100
SH 122
SH 123
M 101
AN 122,122
AN 123,123
M 102
WL 2
AN 124,126
AN 125,127
M 103
SH 126
SH 127
M 104
AN 126,126
AN 127,127
M 105
W 2
AW 102,8
M 106
AW 126,9
UL 6,28
M 107
R 0
M 108
mw 51
UH 7,28
AR 8,55
M 109
ad 84,1062
W 0
M 110
AR 9,79
AD 8,6
M 111
AD 9,7
AW 55,8
M 112
W 1
AW 79,9
M 113
UL 6,29
UH 7,29
M 114
AR 8,103
AR 9,127
M 115
W 2
AD 8,6
M 116
AD 9,7
AW 103,8
M 117
AW 127,9
M 118
bc 49,12,66,60
M 119
WL 1
R 2
M 120
mh 0
M 121
ad 66,2336
H 0
M 132
M 133
TX 58,4608
TX 62,18432
M 122
DG 1024
M 123
M 134
M 135
TY 66,5120
TY 70,18944
M 124
bc 48,12,64,56
M 125
mh 3
M 136
ad 64,2336
H 0
M 137
TX 74,6144
TX 78,19968
M 126
WL 4
Q 0
M 127
Q 2
M 138
Q 4
M 139
TY 82,6656
TY 86,20480
M 128
Q 6
M 129
WL 4
Q 8
M 140
Q 10
M 141
TX 90,7680
TX 94,21504
M 130
Q 12
M 131
R 4
M 142
Q 14
M 143
TY 98,8192
TY 102,22016
M 0
R 6
M 1
WL 10
Q 16
M 12
)"
R"(Q 18
M 13
Q 20
M 2
Q 22
M 3
WL 8
Q 24
M 14
Q 26
M 15
Q 28
M 4
Q 30
M 5
WL 6
Q 32
M 16
Q 34
M 17
Q 36
M 6
Q 38
M 7
WL 2
Q 40
M 18
Q 42
M 19
Q 44
M 8
Q 46
M 9
H 1
M 20
ae 42,72,96
M 21
s_bitcmp1_b32 s50, 4
s_cselect_b32 s52, 24576, 0
v_add_u32 v50, s52, v3
M 10
H 2
SA 224,0
M 11
R 8
M 22
M 23
R 10
PA 12288
DF
M 24
M 25
M 26
M 27
M 28
M 29
M 30
M 31
M 32
ae 44,70,384
M 33
WV 7
M 34
WL 0
FP
M 35
M 36
s_bitcmp1_b32 s50, 6
s_cselect_b32 s51, 24576, 0
v_add_u32 v47, s51, v3
M 37
XL 38,128
M 38
XL 40,896
M 39
M 40
M 41
M 42
M 43
M 44
XL 42,1664
M 45
FR
M 46
M 47
M 48
M 49
M 50
XL 44,2432
UL 6,30
M 51
UH 7,30
AR 8,56
M 52
AR 9,80
AD 8,6
M 53
WL 0
FQ 120
AD 9,7
M 54
AW 56,8
AW 80,9
M 55
UL 6,31
UH 7,31
M 56
FZ 120
R 24
M 57
mw 72
R 26
M 58
TY 106,9736
TY 110,23560
M 59
R 28
M 60
ad 84,1104
W 0
M 61
AR 8,104
AR 9,128
M 62
AD 8,6
AD 9,7
M 63
W 1
AW 104,8
M 64
AW 128,9
UL 6,32
M 65
UH 7,32
AR 8,57
M 66
W 2
AR 9,81
M 67
WL 3
AN 104,106
AN 105,107
M 68
SH 106
SH 107
M 69
mw 75
AN 106,106
AN 107,107
M 70
WL 2
AN 108,110
AN 109,111
M 71
R 30
M 72
SH 110
SH 111
M 73
AN 110,110
AN 111,111
M 74
TY 114,11272
TY 118,25096
M 75
ad 84,1110
W 0
M 76
AD 8,6
AD 9,7
M 77
AW 57,8
AW 81,9
M 78
W 1
UL 6,33
M 79
UH 7,33
AR 8,105
M 80
AR 9,129
AD 8,6
M 81
W 2
AD 9,7
M 82
AW 105,8
AW 129,9
M 83
WL 1
R 32
M 84
mw 0
AN 112,114
AN 113,115
M 85
SH 114
SH 115
M 86
AN 114,114
AN 115,115
M 87
WL 2
AN 116,118
AN 117,119
M 88
SH 118
SH 119
M 89
AN 118,118
AN 119,119
M 90
TY 122,12808
TY 126,26632
M 91
ad 84,1152
M 92
W 0
SA 226,768
M 93
UL 6,34
UH 7,34
M 94
AR 8,58
AR 9,82
M 95
R 34
M 96
W 1
AD 8,6
M 97
AD 9,7
AW 58,8
M 98
AW 82,9
UL 6,35
M 99
WL 3
AN 120,122
AN 121,123
M 100
SH 122
SH 123
M 101
AN 122,122
AN 123,123
M 102
WL 2
AN 124,126
AN 125,127
M 103
SH 126
SH 127
M 104
AN 126,126
AN 127,127
M 105
W 2
UH 7,35
M 106
AR 8,106
AR 9,130
M 107
R 48
M 108
mw 3
AD 8,6
AD 9,7
M 109
ad 84,1158
W 0
M 110
AW 106,8
AW 130,9
M 111
UL 6,36
UH 7,36
M 112
W 1
AR 8,59
M 113
AR 9,83
AD 8,6
M 114
AD 9,7
AW 59,8
M 115
W 2
AW 83,9
M 116
UL 6,37
UH 7,37
M 117
AR 8,107
AR 9,131
M 118
bc 49,13,66,60
AD 8,6
M 119
WL 0
R 50
M 120
mh 9
AD 9,7
AW 107,8
M 121
ad 66,2528
H 0
M 132
AW 131,9
M 133
TX 58,9216
TX 62,23040
M 122
DG 1024
M 123
M 134
M 135
TY 66,9728
TY 70,23552
M 124
bc 48,13,64,56
M 125
mh 12
M 136
ad 64,2528
H 0
M 137
TX 74,10752
TX 78,24576
M 126
WL 4
Q 0
M 127
Q 2
M 138
Q 4
M 139
TY 82,11264
TY 86,25088
M 128
Q 6
M 129
WL 4
Q 8
M 140
Q 10
M 141
TX 90,12288
TX 94,26112
M 130
Q 12
M 131
R 52
M 142
Q 14
M 143
TY 98,12800
TY 102,26624
M 0
R 54
M 1
WL 10
Q 16
M 12
Q 18
M 13
Q 20
M 2
Q 22
M 3
WL 8
Q 24
M 14
Q 26
M 15
Q 28
M 4
Q 30
M 5
WL 6
Q 32
M 16
Q 34
M 17
Q 36
M 6
Q 38
M 7
WL 2
Q 40
M 18
Q 42
M 19
Q 44
M 8
Q 46
M 9
H 1
SA 228,1536
)"
R"(M 20
M 21
M 10
H 2
M 11
R 56
M 22
M 23
R 58
M 24
M 25
M 26
M 27
M 28
M 29
M 30
M 31
M 32
M 33
WV 6
M 34
WL 0
FP
M 35
M 36
ae 44,70,384
M 37
s_bfe_u32 s51, s50, 0x10007
v_mad_i32_i24 v47, v49, s51, v48
XL 14,128
M 38
M 39
M 40
XL 16,896
M 41
M 42
M 43
M 44
M 45
FR
M 46
XL 18,1664
M 47
M 48
M 49
M 50
M 51
M 52
XL 20,2432
M 53
WL 0
FQ 124
M 54
M 55
M 56
FZ 124
R 72
M 57
mw 24
R 74
M 58
TY 106,520
TY 110,14344
M 59
R 76
M 60
ad 84,1200
W 0
M 61
M 62
M 63
W 1
M 64
M 65
M 66
W 2
M 67
WL 3
AN 104,106
AN 105,107
M 68
SH 106
SH 107
M 69
mw 27
AN 106,106
AN 107,107
M 70
WL 2
AN 108,110
AN 109,111
M 71
R 78
M 72
SH 110
SH 111
M 73
AN 110,110
AN 111,111
M 74
TY 114,2056
TY 118,15880
M 75
ad 84,1206
W 0
M 76
M 77
M 78
W 1
M 79
M 80
M 81
W 2
SA 230,2304
M 82
M 83
R 80
M 84
mw 48
WL 3
AN 112,114
AN 113,115
M 85
SH 114
SH 115
M 86
AN 114,114
AN 115,115
M 87
WL 2
AN 116,118
AN 117,119
M 88
SH 118
SH 119
M 89
AN 118,118
AN 119,119
M 90
TY 122,3592
TY 126,17416
M 91
ad 84,1248
M 92
W 0
UL 6,38
M 93
UH 7,38
AR 8,132
M 94
AR 9,156
AD 8,6
M 95
R 82
M 96
W 1
AD 9,7
M 97
AW 132,8
AW 156,9
M 98
UL 6,39
UH 7,39
M 99
WL 3
AN 120,122
AN 121,123
M 100
SH 122
SH 123
M 101
AN 122,122
AN 123,123
M 102
WL 2
AN 124,126
AN 125,127
M 103
SH 126
SH 127
M 104
AN 126,126
AN 127,127
M 105
W 2
AR 8,180
M 106
AD 8,6
AD 136,7
M 107
R 0
M 108
mw 51
AW 180,8
UL 6,40
M 109
ad 84,1254
W 0
M 110
UH 7,40
AR 8,133
M 111
AR 9,157
AD 8,6
M 112
W 1
AD 9,7
M 113
AW 133,8
AW 157,9
M 114
UL 6,41
UH 7,41
M 115
W 2
AR 8,181
M 116
AD 8,6
AD 137,7
M 117
AW 181,8
UL 6,42
M 118
bc 49,14,66,60
UH 7,42
M 119
WL 0
R 2
M 120
mh 18
AR 8,134
AR 9,158
M 121
ad 66,2720
H 0
M 132
AD 8,6
AD 9,7
M 133
TX 58,0
TX 62,13824
M 122
DG 1024
M 123
AW 134,8
AW 158,9
M 134
UL 6,43
UH 7,43
M 135
TY 66,512
TY 70,14336
M 124
bc 48,14,64,56
AR 8,182
M 125
mh 21
AD 8,6
AD 138,7
M 136
ad 64,2720
H 0
M 137
TX 74,1536
TX 78,15360
M 126
WL 4
Q 0
M 127
Q 2
M 138
Q 4
M 139
TY 82,2048
TY 86,15872
M 128
Q 6
M 129
WL 4
Q 8
M 140
Q 10
M 141
TX 90,3072
TX 94,16896
M 130
Q 12
M 131
R 4
M 142
Q 14
M 143
TY 98,3584
TY 102,17408
M 0
R 6
M 1
WL 10
Q 16
M 12
Q 18
M 13
Q 20
M 2
Q 22
M 3
WL 8
Q 24
M 14
Q 26
M 15
Q 28
M 4
Q 30
M 5
WL 6
Q 32
M 16
Q 34
M 17
Q 36
M 6
Q 38
M 7
WL 2
Q 40
M 18
Q 42
M 19
Q 44
M 8
Q 46
M 9
H 1
AW 182,8
M 20
ae 42,72,96
M 21
SA 232,128
UL 6,44
M 10
H 2
UH 7,44
M 11
R 8
M 22
AR 8,135
AR 9,159
M 23
R 10
PA 12288
DF
M 24
AD 8,6
AD 9,7
M 25
AW 135,8
AW 159,9
M 26
UL 6,45
UH 7,45
M 27
AR 8,183
AD 8,6
M 28
AD 139,7
AW 183,8
M 29
M 30
M 31
M 32
M 33
WV 7
M 34
WL 0
FP
M 35
M 36
ae 44,68,384
M 37
s_bitcmp1_b32 s50, 8
s_cselect_b32 s51, 24576, 0
v_add_u32 v47, s51, v3
M 38
XL 22,128
M 39
M 40
M 41
M 42
XL 24,896
M 43
M 44
M 45
FR
M 46
M 47
M 48
XL 26,1664
M 49
M 50
)"
R"(M 51
M 52
M 53
WL 0
FQ 128
M 54
XL 28,2432
M 55
M 56
FZ 128
R 24
M 57
mw 72
R 26
M 58
TY 106,5128
TY 110,18952
M 59
R 28
M 60
ad 84,1296
W 0
M 61
M 62
M 63
W 1
M 64
M 65
M 66
W 2
M 67
WL 3
AN 104,106
AN 105,107
M 68
SH 106
SH 107
M 69
mw 75
AN 106,106
AN 107,107
M 70
WL 2
AN 108,110
AN 109,111
M 71
R 30
M 72
SH 110
SH 111
M 73
AN 110,110
AN 111,111
M 74
TY 114,6664
TY 118,20488
M 75
ad 84,1302
W 0
M 76
M 77
M 78
W 1
M 79
M 80
M 81
W 2
M 82
M 83
R 32
M 84
mw 0
WL 3
AN 112,114
AN 113,115
M 85
SH 114
SH 115
M 86
AN 114,114
AN 115,115
M 87
WL 2
AN 116,118
AN 117,119
M 88
SH 118
SH 119
M 89
AN 118,118
AN 119,119
M 90
TY 122,8200
TY 126,22024
M 91
ad 84,1344
M 92
W 0
SA 234,896
M 93
UL 6,14
UH 7,14
M 94
AR 8,136
AR 9,160
M 95
R 34
M 96
W 1
AD 8,6
M 97
AD 9,7
AW 136,8
M 98
AW 160,9
UL 6,15
M 99
WL 3
AN 120,122
AN 121,123
M 100
SH 122
SH 123
M 101
AN 122,122
AN 123,123
M 102
WL 2
AN 124,126
AN 125,127
M 103
SH 126
SH 127
M 104
AN 126,126
AN 127,127
M 105
W 2
UH 7,15
M 106
AR 8,184
AD 8,6
M 107
R 48
M 108
mw 3
AD 140,7
AW 184,8
M 109
ad 84,1350
W 0
M 110
UL 6,16
UH 7,16
M 111
AR 8,137
AR 9,161
M 112
W 1
AD 8,6
M 113
AD 9,7
AW 137,8
M 114
AW 161,9
UL 6,17
M 115
W 2
UH 7,17
M 116
AR 8,185
AD 8,6
M 117
AD 141,7
AW 185,8
M 118
bc 49,15,66,60
UL 6,18
M 119
WL 0
R 50
M 120
mh 0
UH 7,18
AR 8,138
M 121
ad 66,2912
H 0
M 132
AR 9,162
AD 8,6
M 133
TX 58,4608
TX 62,18432
M 122
DG 1024
M 123
AD 9,7
AW 138,8
M 134
AW 162,9
UL 6,19
M 135
TY 66,5120
TY 70,18944
M 124
bc 48,15,64,56
UH 7,19
M 125
mh 3
AR 8,186
AD 8,6
M 136
ad 64,2912
H 0
M 137
TX 74,6144
TX 78,19968
M 126
WL 4
Q 0
M 127
Q 2
M 138
Q 4
M 139
TY 82,6656
TY 86,20480
M 128
Q 6
M 129
WL 4
Q 8
M 140
Q 10
M 141
TX 90,7680
TX 94,21504
M 130
Q 12
M 131
R 52
M 142
Q 14
M 143
TY 98,8192
TY 102,22016
M 0
R 54
M 1
WL 10
Q 16
M 12
Q 18
M 13
Q 20
M 2
Q 22
M 3
WL 8
Q 24
M 14
Q 26
M 15
Q 28
M 4
Q 30
M 5
WL 6
Q 32
M 16
Q 34
M 17
Q 36
M 6
Q 38
M 7
WL 2
Q 40
M 18
Q 42
M 19
Q 44
M 8
Q 46
M 9
H 1
SA 236,1664
M 20
AD 142,7
AW 186,8
M 21
UL 6,20
UH 7,20
M 10
H 2
AR 8,139
M 11
R 56
M 22
AR 9,163
AD 8,6
M 23
R 58
M 24
AD 9,7
AW 139,8
M 25
AW 163,9
UL 6,21
M 26
UH 7,21
AR 8,187
M 27
AD 8,6
AD 143,7
M 28
AW 187,8
M 29
M 30
M 31
M 32
M 33
WV 6
M 34
WL 0
FP
M 35
M 36
M 37
M 38
ae 44,68,384
M 39
s_bfe_u32 s51, s50, 0x10009
v_mad_i32_i24 v47, v49, s51, v48
XL 30,128
M 40
M 41
M 42
M 43
M 44
XL 32,896
M 45
FR
M 46
M 47
M 48
M 49
M 50
XL 34,1664
M 51
M 52
M 53
WL 0
FQ 132
M 54
M 55
M 56
FZ 132
R 72
M 57
mw 24
R 74
M 58
TY 106,9736
TY 110,23560
M 59
R 76
M 60
ad 84,1392
W 0
M 61
XL 36,2432
M 62
M 63
W 1
M 64
M 65
M 66
W 2
M 67
WL 3
AN 104,106
AN 105,107
M 68
SH 106
SH 107
M 69
mw 27
AN 106,106
AN 107,107
M 70
WL 2
AN 108,110
AN 109,111
M 71
R 78
M 72
SH 110
SH 111
)"
R"(M 73
AN 110,110
AN 111,111
M 74
TY 114,11272
TY 118,25096
M 75
ad 84,1398
W 0
M 76
M 77
M 78
W 1
M 79
M 80
M 81
W 2
SA 238,2432
M 82
M 83
R 80
M 84
mw 48
WL 3
AN 112,114
AN 113,115
M 85
SH 114
SH 115
M 86
AN 114,114
AN 115,115
M 87
WL 2
AN 116,118
AN 117,119
M 88
SH 118
SH 119
M 89
AN 118,118
AN 119,119
M 90
TY 122,12808
TY 126,26632
M 91
ad 84,1440
M 92
W 0
UL 6,22
M 93
UH 7,22
AR 8,140
M 94
AR 9,164
AD 8,6
M 95
R 82
M 96
W 1
AD 9,7
M 97
AW 140,8
AW 164,9
M 98
UL 6,23
UH 7,23
M 99
WL 3
AN 120,122
AN 121,123
M 100
SH 122
SH 123
M 101
AN 122,122
AN 123,123
M 102
WL 2
AN 124,126
AN 125,127
M 103
SH 126
SH 127
M 104
AN 126,126
AN 127,127
M 105
W 2
AR 8,188
M 106
AD 8,6
AD 144,7
M 107
R 0
M 108
mw 51
AW 188,8
UL 6,24
M 109
ad 84,1446
W 0
M 110
UH 7,24
AR 8,141
M 111
AR 9,165
AD 8,6
M 112
W 1
AD 9,7
M 113
AW 141,8
AW 165,9
M 114
UL 6,25
UH 7,25
M 115
W 2
AR 8,189
M 116
AD 8,6
AD 145,7
M 117
AW 189,8
UL 6,26
M 118
bc 49,16,66,60
UH 7,26
M 119
WL 0
R 2
M 120
mh 9
AR 8,142
AR 9,166
M 121
ad 66,3104
H 0
M 132
AD 8,6
AD 9,7
M 133
TX 58,9216
TX 62,23040
M 122
DG 1024
M 123
AW 142,8
AW 166,9
M 134
UL 6,27
UH 7,27
M 135
TY 66,9728
TY 70,23552
M 124
bc 48,16,64,56
AR 8,190
M 125
mh 12
AD 8,6
AD 146,7
M 136
ad 64,3104
H 0
M 137
TX 74,10752
TX 78,24576
M 126
WL 4
Q 0
M 127
Q 2
M 138
Q 4
M 139
TY 82,11264
TY 86,25088
M 128
Q 6
M 129
WL 4
Q 8
M 140
Q 10
M 141
TX 90,12288
TX 94,26112
M 130
Q 12
M 131
R 4
M 142
Q 14
M 143
TY 98,12800
TY 102,26624
M 0
R 6
M 1
WL 10
Q 16
M 12
Q 18
M 13
Q 20
M 2
Q 22
M 3
WL 8
Q 24
M 14
Q 26
M 15
Q 28
M 4
Q 30
M 5
WL 6
Q 32
M 16
Q 34
M 17
Q 36
M 6
Q 38
M 7
WL 2
Q 40
M 18
Q 42
M 19
Q 44
M 8
Q 46
M 9
H 1
AW 190,8
M 20
ae 42,72,96
M 21
SA 240,256
UL 6,28
M 10
H 2
UH 7,28
M 11
R 8
M 22
AR 8,143
AR 9,167
M 23
R 10
PA 12288
DF
M 24
AD 8,6
AD 9,7
M 25
AW 143,8
AW 167,9
M 26
UL 6,29
UH 7,29
M 27
AR 8,191
AD 8,6
M 28
AD 147,7
AW 191,8
M 29
M 30
M 31
M 32
M 33
WV 7
M 34
WL 0
FP
M 35
M 36
M 37
M 38
M 39
M 40
ae 44,68,480
M 41
s_bitcmp1_b32 s50, 10
s_cselect_b32 s51, 24576, 0
v_add_u32 v47, s51, v3
M 42
XL 38,128
M 43
M 44
M 45
FR
M 46
XL 40,896
M 47
M 48
M 49
M 50
M 51
M 52
XL 42,1664
M 53
WL 0
FQ 136
M 54
M 55
M 56
FZ 136
R 24
M 57
mw 72
R 26
M 58
TY 106,520
TY 110,14344
M 59
R 28
M 60
ad 84,1488
W 0
M 61
XL 44,2432
M 62
M 63
W 1
M 64
M 65
M 66
W 2
M 67
WL 3
AN 104,106
AN 105,107
M 68
SH 106
SH 107
M 69
mw 75
AN 106,106
AN 107,107
M 70
WL 2
AN 108,110
AN 109,111
M 71
R 30
M 72
SH 110
SH 111
M 73
AN 110,110
AN 111,111
M 74
TY 114,2056
TY 118,15880
M 75
ad 84,1494
W 0
M 76
M 77
M 78
W 1
M 79
M 80
M 81
W 2
M 82
M 83
R 32
M 84
mw 0
WL 3
AN 112,114
AN 113,115
M 85
SH 114
SH 115
M 86
AN 114,114
AN 115,115
M 87
WL 2
AN 116,118
AN 117,119
M 88
SH 118
SH 119
M 89
AN 118,118
AN 119,119
M 90
TY 122,3592
TY 126,17416
M 91
ad 84,1536
)"
R"(M 92
W 0
SA 242,1024
M 93
M 94
UL 6,30
UH 7,30
M 95
R 34
M 96
W 1
AR 8,144
M 97
AR 9,168
AD 8,6
M 98
AD 9,7
AW 144,8
M 99
WL 3
AN 120,122
AN 121,123
M 100
SH 122
SH 123
M 101
AN 122,122
AN 123,123
M 102
WL 2
AN 124,126
AN 125,127
M 103
SH 126
SH 127
M 104
AN 126,126
AN 127,127
M 105
W 2
AW 168,9
M 106
UL 6,31
UH 7,31
M 107
R 48
M 108
mw 3
AR 8,192
AD 8,6
M 109
ad 84,1542
W 0
M 110
AD 148,7
AW 192,8
M 111
UL 6,32
UH 7,32
M 112
W 1
AR 8,145
M 113
AR 9,169
AD 8,6
M 114
AD 9,7
AW 145,8
M 115
W 2
AW 169,9
M 116
UL 6,33
UH 7,33
M 117
AR 8,193
AD 8,6
M 118
bc 49,17,66,60
AD 149,7
M 119
WL 0
R 50
M 120
mh 18
AW 193,8
UL 6,34
M 121
ad 66,3296
H 0
M 132
UH 7,34
AR 8,146
M 133
TX 58,0
TX 62,13824
M 122
DG 1024
M 123
AR 9,170
AD 8,6
M 134
AD 9,7
AW 146,8
M 135
TY 66,512
TY 70,14336
M 124
bc 48,17,64,56
AW 170,9
M 125
mh 21
UL 6,35
UH 7,35
M 136
ad 64,3296
H 0
M 137
TX 74,1536
TX 78,15360
M 126
WL 4
Q 0
M 127
Q 2
M 138
Q 4
M 139
TY 82,2048
TY 86,15872
M 128
Q 6
M 129
WL 4
Q 8
M 140
Q 10
M 141
TX 90,3072
TX 94,16896
M 130
Q 12
M 131
R 52
M 142
Q 14
M 143
TY 98,3584
TY 102,17408
M 0
R 54
M 1
WL 10
Q 16
M 12
Q 18
M 13
Q 20
M 2
Q 22
M 3
WL 8
Q 24
M 14
Q 26
M 15
Q 28
M 4
Q 30
M 5
WL 6
Q 32
M 16
Q 34
M 17
Q 36
M 6
Q 38
M 7
WL 2
Q 40
M 18
Q 42
M 19
Q 44
M 8
Q 46
M 9
H 1
SA 244,1792
M 20
AR 8,194
AD 8,6
M 21
AD 150,7
AW 194,8
M 10
H 2
UL 6,36
M 11
R 56
M 22
UH 7,36
AR 8,147
M 23
R 58
M 24
AR 9,171
AD 8,6
M 25
AD 9,7
AW 147,8
M 26
AW 171,9
UL 6,37
M 27
UH 7,37
AR 8,195
M 28
AD 8,6
AD 151,7
M 29
AW 195,8
M 30
M 31
M 32
M 33
WV 6
M 34
WL 0
FP
M 35
M 36
M 37
M 38
M 39
M 40
M 41
M 42
ae 44,68,480
M 43
s_bfe_u32 s51, s50, 0x1000b
v_mad_i32_i24 v47, v49, s51, v48
XL 14,128
M 44
M 45
FR
M 46
M 47
M 48
XL 16,896
M 49
M 50
M 51
M 52
M 53
WL 0
FQ 140
M 54
XL 18,1664
M 55
M 56
FZ 140
R 72
M 57
mw 24
R 74
M 58
TY 106,5128
TY 110,18952
M 59
R 76
M 60
ad 84,1584
W 0
M 61
XL 20,2432
M 62
M 63
W 1
M 64
M 65
M 66
W 2
M 67
WL 3
AN 104,106
AN 105,107
M 68
SH 106
SH 107
M 69
mw 27
AN 106,106
AN 107,107
M 70
WL 2
AN 108,110
AN 109,111
M 71
R 78
M 72
SH 110
SH 111
M 73
AN 110,110
AN 111,111
M 74
TY 114,6664
TY 118,20488
M 75
ad 84,1590
W 0
M 76
M 77
M 78
W 1
M 79
M 80
M 81
W 2
SA 246,2560
M 82
M 83
R 80
M 84
mw 48
WL 3
AN 112,114
AN 113,115
M 85
SH 114
SH 115
M 86
AN 114,114
AN 115,115
M 87
WL 2
AN 116,118
AN 117,119
M 88
SH 118
SH 119
M 89
AN 118,118
AN 119,119
M 90
TY 122,8200
TY 126,22024
M 91
ad 84,1632
M 92
W 0
M 93
M 94
M 95
R 82
M 96
W 1
UL 6,38
M 97
UH 7,38
AR 8,148
M 98
AR 9,172
AD 8,6
M 99
WL 3
AN 120,122
AN 121,123
M 100
SH 122
SH 123
M 101
AN 122,122
AN 123,123
M 102
WL 2
AN 124,126
AN 125,127
M 103
SH 126
SH 127
M 104
AN 126,126
AN 127,127
M 105
W 2
AD 9,7
M 106
AW 148,8
AW 172,9
M 107
R 0
M 108
mw 51
UL 6,39
UH 7,39
M 109
ad 84,1638
)"
R"(W 0
M 110
AR 8,196
AD 8,6
M 111
AD 152,7
AW 196,8
M 112
W 1
UL 6,40
M 113
UH 7,40
AR 8,149
M 114
AR 9,173
AD 8,6
M 115
W 2
AD 9,7
M 116
AW 149,8
AW 173,9
M 117
UL 6,41
UH 7,41
M 118
bc 49,18,66,60
AR 8,197
M 119
WL 0
R 2
M 120
mh 0
AD 8,6
AD 153,7
M 121
ad 66,3488
H 0
M 132
AW 197,8
UL 6,42
M 133
TX 58,4608
TX 62,18432
M 122
DG 1024
M 123
UH 7,42
AR 8,150
M 134
AR 9,174
AD 8,6
M 135
TY 66,5120
TY 70,18944
M 124
bc 48,18,64,56
AD 9,7
M 125
mh 3
AW 150,8
AW 174,9
M 136
ad 64,3488
H 0
M 137
TX 74,6144
TX 78,19968
M 126
WL 4
Q 0
M 127
Q 2
M 138
Q 4
M 139
TY 82,6656
TY 86,20480
M 128
Q 6
M 129
WL 4
Q 8
M 140
Q 10
M 141
TX 90,7680
TX 94,21504
M 130
Q 12
M 131
R 4
M 142
Q 14
M 143
TY 98,8192
TY 102,22016
M 0
R 6
M 1
WL 10
Q 16
M 12
Q 18
M 13
Q 20
M 2
Q 22
M 3
WL 8
Q 24
M 14
Q 26
M 15
Q 28
M 4
Q 30
M 5
WL 6
Q 32
M 16
Q 34
M 17
Q 36
M 6
Q 38
M 7
WL 2
Q 40
M 18
Q 42
M 19
Q 44
M 8
Q 46
M 9
H 1
UL 6,43
M 20
ae 42,72,96
M 21
s_bfe_u32 s52, s50, 0x10005
v_mad_i32_i24 v50, v49, s52, v48
SA 248,256
M 10
H 2
UH 7,43
M 11
R 8
M 22
AR 8,198
AD 8,6
M 23
R 10
PA 12288
DF
M 24
AD 154,7
AW 198,8
M 25
UL 6,44
UH 7,44
M 26
AR 8,151
AR 9,175
M 27
AD 8,6
AD 9,7
M 28
AW 151,8
AW 175,9
M 29
UL 6,45
UH 7,45
M 30
AR 8,199
AD 8,6
M 31
AD 155,7
AW 199,8
M 32
M 33
WV 7
M 34
WL 0
FP
M 35
M 36
M 37
M 38
M 39
M 40
M 41
M 42
M 43
M 44
M 45
FR
M 46
M 47
M 48
M 49
M 50
M 51
M 52
M 53
WL 0
FQ 144
M 54
M 55
M 56
FZ 144
R 24
M 57
mw 72
R 26
M 58
TY 106,9736
TY 110,23560
M 59
R 28
M 60
ad 84,1680
W 0
M 61
M 62
M 63
W 1
M 64
M 65
M 66
W 2
M 67
WL 3
AN 104,106
AN 105,107
M 68
SH 106
SH 107
M 69
mw 75
AN 106,106
AN 107,107
M 70
WL 2
AN 108,110
AN 109,111
M 71
R 30
M 72
SH 110
SH 111
M 73
AN 110,110
AN 111,111
M 74
TY 114,11272
TY 118,25096
M 75
ad 84,1686
W 0
M 76
ae 44,70,384
M 77
s_bitcmp1_b32 s50, 6
s_cselect_b32 s51, 24576, 0
v_add_u32 v47, s51, v3
M 78
W 1
XL 22,256
M 79
XL 24,1024
M 80
M 81
W 2
M 82
XL 26,1792
M 83
WL 1
R 32
M 84
mw 0
AN 112,114
AN 113,115
M 85
SH 114
SH 115
M 86
AN 114,114
AN 115,115
M 87
WL 2
AN 116,118
AN 117,119
M 88
SH 118
SH 119
M 89
AN 118,118
AN 119,119
M 90
TY 122,12808
TY 126,26632
M 91
ad 84,1728
M 92
W 0
SA 250,1024
M 93
XL 28,2560
M 94
M 95
R 34
M 96
W 1
M 97
M 98
UL 6,14
UH 7,14
M 99
WL 3
AN 120,122
AN 121,123
M 100
SH 122
SH 123
M 101
AN 122,122
AN 123,123
M 102
WL 2
AN 124,126
AN 125,127
M 103
SH 126
SH 127
M 104
AN 126,126
AN 127,127
M 105
W 2
AR 8,152
M 106
AR 9,176
AD 8,6
M 107
R 48
M 108
mw 3
AD 9,7
AW 152,8
M 109
ad 84,1734
W 0
M 110
AW 176,9
UL 6,15
M 111
UH 7,15
AD 132,6
M 112
W 1
AD 156,7
M 113
UL 6,16
UH 7,16
M 114
AR 8,153
AR 9,177
M 115
W 2
AD 8,6
M 116
AD 9,7
AW 153,8
M 117
AW 177,9
UL 6,17
M 118
bc 49,19,66,60
UH 7,17
M 119
WL 0
R 50
M 120
mh 9
AD 133,6
AD 157,7
M 121
ad 66,3680
H 0
M 132
UL 6,18
UH 7,18
M 133
TX 58,9216
TX 62,23040
M 122
DG 1024
M 123
)"
R"(AR 8,154
AR 9,178
M 134
AD 8,6
AD 9,7
M 135
TY 66,9728
TY 70,23552
M 124
bc 48,19,64,56
AW 154,8
M 125
mh 12
AW 178,9
UL 6,19
M 136
ad 64,3680
H 0
M 137
TX 74,10752
TX 78,24576
M 126
WL 4
Q 0
M 127
Q 2
M 138
Q 4
M 139
TY 82,11264
TY 86,25088
M 128
Q 6
M 129
WL 4
Q 8
M 140
Q 10
M 141
TX 90,12288
TX 94,26112
M 130
Q 12
M 131
R 52
M 142
Q 14
M 143
TY 98,12800
TY 102,26624
M 0
R 54
M 1
WL 10
Q 16
M 12
Q 18
M 13
Q 20
M 2
Q 22
M 3
WL 8
Q 24
M 14
Q 26
M 15
Q 28
M 4
Q 30
M 5
WL 6
Q 32
M 16
Q 34
M 17
Q 36
M 6
Q 38
M 7
WL 2
Q 40
M 18
Q 42
M 19
Q 44
M 8
Q 46
M 9
H 1
SA 252,1792
M 20
UH 7,19
AD 134,6
M 21
AD 158,7
UL 6,20
M 10
H 2
UH 7,20
M 11
R 56
M 22
AR 8,155
AR 9,179
M 23
R 58
M 24
AD 8,6
AD 9,7
M 25
AW 155,8
AW 179,9
M 26
UL 6,21
UH 7,21
M 27
AD 135,6
AD 159,7
M 28
M 29
M 30
M 31
M 32
M 33
WV 6
M 34
WL 0
FP
M 35
M 36
M 37
M 38
M 39
M 40
M 41
M 42
M 43
M 44
M 45
FR
M 46
M 47
M 48
M 49
M 50
M 51
M 52
M 53
WL 0
FQ 148
M 54
M 55
M 56
FZ 148
R 72
M 57
mw 24
R 74
M 58
TY 106,520
TY 110,14344
M 59
R 76
M 60
ad 84,1776
W 0
M 61
M 62
M 63
W 1
M 64
M 65
M 66
W 2
M 67
WL 3
AN 104,106
AN 105,107
M 68
SH 106
SH 107
M 69
mw 27
AN 106,106
AN 107,107
M 70
WL 2
AN 108,110
AN 109,111
M 71
R 78
M 72
SH 110
SH 111
M 73
AN 110,110
AN 111,111
M 74
TY 114,2056
TY 118,15880
M 75
ad 84,1782
W 0
M 76
ae 44,70,384
M 77
s_bfe_u32 s51, s50, 0x10007
v_mad_i32_i24 v47, v49, s51, v48
XL 30,256
M 78
W 1
M 79
M 80
XL 32,1024
M 81
W 2
SA 254,2560
M 82
M 83
WL 1
R 80
M 84
mw 48
AN 112,114
AN 113,115
M 85
SH 114
SH 115
M 86
AN 114,114
AN 115,115
M 87
WL 2
AN 116,118
AN 117,119
M 88
SH 118
SH 119
M 89
AN 118,118
AN 119,119
M 90
TY 122,3592
TY 126,17416
M 91
ad 84,1824
M 92
W 0
XL 34,1792
M 93
XL 36,2560
M 94
M 95
R 82
M 96
W 1
M 97
M 98
M 99
WL 3
AN 120,122
AN 121,123
M 100
SH 122
SH 123
M 101
AN 122,122
AN 123,123
M 102
WL 2
AN 124,126
AN 125,127
M 103
SH 126
SH 127
M 104
AN 126,126
AN 127,127
M 105
W 2
M 106
M 107
R 0
M 108
mw 51
M 109
ad 84,1830
W 0
M 110
M 111
M 112
W 1
M 113
M 114
M 115
W 2
M 116
M 117
M 118
bc 49,20,66,60
M 119
WL 2
R 2
M 120
mh 18
M 121
ad 66,3872
H 0
M 132
M 133
TX 58,0
TX 62,13824
M 122
DG 1024
M 123
M 134
UL 6,22
UH 7,22
M 135
TY 66,512
TY 70,14336
M 124
bc 48,20,64,56
AD 160,6
M 125
mh 21
AD 184,7
UL 6,23
M 136
ad 64,3872
H 0
M 137
TX 74,1536
TX 78,15360
M 126
WL 4
Q 0
M 127
Q 2
M 138
Q 4
M 139
TY 82,2048
TY 86,15872
M 128
Q 6
M 129
WL 4
Q 8
M 140
Q 10
M 141
TX 90,3072
TX 94,16896
M 130
Q 12
M 131
R 4
M 142
Q 14
M 143
TY 98,3584
TY 102,17408
M 0
R 6
M 1
WL 10
Q 16
M 12
Q 18
M 13
Q 20
M 2
Q 22
M 3
WL 8
Q 24
M 14
Q 26
M 15
Q 28
M 4
Q 30
M 5
WL 6
Q 32
M 16
Q 34
M 17
Q 36
M 6
Q 38
M 7
WL 2
Q 40
M 18
Q 42
M 19
Q 44
)"
R"(M 8
Q 46
M 9
H 1
UH 7,23
M 20
AD 208,6
AD 232,7
M 21
UL 6,24
UH 7,24
M 10
H 2
AD 161,6
M 11
R 8
M 22
AD 185,7
UL 6,25
M 23
R 10
PA 12288
DF
M 24
UH 7,25
AD 209,6
M 25
AD 233,7
UL 6,26
M 26
UH 7,26
AD 162,6
M 27
AD 186,7
UL 6,27
M 28
UH 7,27
AD 210,6
M 29
AD 234,7
UL 6,28
M 30
UH 7,28
AD 163,6
M 31
AD 187,7
UL 6,29
M 32
UH 7,29
AD 211,6
M 33
WV 6
AD 235,7
M 34
WL 0
FP
M 35
M 36
M 37
M 38
M 39
M 40
M 41
M 42
M 43
M 44
M 45
FR
M 46
M 47
M 48
M 49
M 50
M 51
M 52
M 53
WL 0
FQ 152
M 54
M 55
M 56
FZ 152
R 24
M 57
mw 72
R 26
M 58
TY 106,5128
TY 110,18952
M 59
R 28
M 60
ad 84,1872
W 0
M 61
M 62
M 63
W 1
M 64
M 65
M 66
W 2
M 67
WL 3
AN 104,106
AN 105,107
M 68
SH 106
SH 107
M 69
mw 75
AN 106,106
AN 107,107
M 70
WL 2
AN 108,110
AN 109,111
M 71
R 30
M 72
SH 110
SH 111
M 73
AN 110,110
AN 111,111
M 74
TY 114,6664
TY 118,20488
M 75
ad 84,1878
W 0
M 76
M 77
M 78
W 1
M 79
ae 44,68,384
M 80
s_bitcmp1_b32 s50, 8
s_cselect_b32 s51, 24576, 0
v_add_u32 v47, s51, v3
M 81
W 2
XL 38,256
M 82
M 83
WL 1
R 32
M 84
mw 0
AN 112,114
AN 113,115
M 85
SH 114
SH 115
M 86
AN 114,114
AN 115,115
M 87
WL 2
AN 116,118
AN 117,119
M 88
SH 118
SH 119
M 89
AN 118,118
AN 119,119
M 90
TY 122,8200
TY 126,22024
M 91
ad 84,1920
M 92
W 0
XL 40,1024
M 93
XL 42,1792
M 94
M 95
R 34
M 96
W 1
XL 44,2560
M 97
M 98
M 99
WL 3
AN 120,122
AN 121,123
M 100
SH 122
SH 123
M 101
AN 122,122
AN 123,123
M 102
WL 2
AN 124,126
AN 125,127
M 103
SH 126
SH 127
M 104
AN 126,126
AN 127,127
M 105
W 2
M 106
M 107
R 48
M 108
mw 3
M 109
ad 84,1926
W 0
M 110
M 111
M 112
W 1
M 113
M 114
M 115
W 2
M 116
M 117
M 118
bc 49,21,66,60
M 119
WL 2
R 50
M 120
mh 0
M 121
ad 66,4064
H 0
M 132
M 133
TX 58,4608
TX 62,18432
M 122
DG 1024
M 123
M 134
M 135
TY 66,5120
TY 70,18944
M 124
bc 48,21,64,56
M 125
mh 3
M 136
ad 64,4064
H 0
M 137
TX 74,6144
TX 78,19968
M 126
WL 4
Q 0
M 127
Q 2
M 138
Q 4
M 139
TY 82,6656
TY 86,20480
M 128
Q 6
M 129
WL 4
Q 8
M 140
Q 10
M 141
TX 90,7680
TX 94,21504
M 130
Q 12
M 131
R 52
M 142
Q 14
M 143
TY 98,8192
TY 102,22016
M 0
R 54
M 1
WL 10
Q 16
M 12
Q 18
M 13
Q 20
M 2
Q 22
M 3
WL 8
Q 24
M 14
Q 26
M 15
Q 28
M 4
Q 30
M 5
WL 6
Q 32
M 16
Q 34
M 17
Q 36
M 6
Q 38
M 7
WL 2
Q 40
M 18
Q 42
M 19
Q 44
M 8
Q 46
M 9
H 1
UL 6,30
M 20
UH 7,30
AD 164,6
M 21
AD 188,7
UL 6,31
M 10
H 2
UH 7,31
M 11
R 56
M 22
AD 212,6
AD 236,7
M 23
R 58
M 24
UL 6,32
UH 7,32
M 25
AD 165,6
AD 189,7
M 26
UL 6,33
UH 7,33
M 27
AD 213,6
AD 237,7
M 28
UL 6,34
UH 7,34
M 29
AD 166,6
AD 190,7
M 30
UL 6,35
UH 7,35
M 31
AD 214,6
AD 238,7
M 32
UL 6,36
UH 7,36
M 33
WV 5
AD 167,6
AD 191,7
M 34
WL 0
FP
UL 6,37
M 35
UH 7,37
AD 215,6
M 36
AD 239,7
M 37
M 38
M 39
M 40
M 41
M 42
M 43
M 44
M 45
FR
M 46
M 47
M 48
M 49
M 50
M 51
M 52
)"
R"(M 53
WL 0
FQ 156
M 54
M 55
M 56
FZ 156
R 72
M 57
mw 24
R 74
M 58
TY 106,9736
TY 110,23560
M 59
R 76
M 60
ad 84,1968
W 0
M 61
M 62
M 63
W 1
M 64
M 65
M 66
W 2
M 67
WL 3
AN 104,106
AN 105,107
M 68
SH 106
SH 107
M 69
mw 27
AN 106,106
AN 107,107
M 70
WL 2
AN 108,110
AN 109,111
M 71
R 78
M 72
SH 110
SH 111
M 73
AN 110,110
AN 111,111
M 74
TY 114,11272
TY 118,25096
M 75
ad 84,1974
W 0
M 76
M 77
M 78
W 1
M 79
M 80
M 81
W 2
M 82
ae 44,68,384
M 83
WL 1
R 80
M 84
mw 48
AN 112,114
AN 113,115
M 85
s_bfe_u32 s51, s50, 0x10009
SH 114
SH 115
M 86
AN 114,114
AN 115,115
M 87
WL 2
AN 116,118
AN 117,119
M 88
SH 118
SH 119
M 89
AN 118,118
AN 119,119
M 90
TY 122,12808
TY 126,26632
M 91
ad 84,2016
M 92
W 0
v_mad_i32_i24 v47, v49, s51, v48
M 93
XL 14,256
M 94
XL 16,1024
M 95
R 82
M 96
W 1
XL 18,1792
M 97
M 98
M 99
WL 3
AN 120,122
AN 121,123
M 100
SH 122
SH 123
M 101
AN 122,122
AN 123,123
M 102
WL 2
AN 124,126
AN 125,127
M 103
SH 126
SH 127
M 104
AN 126,126
AN 127,127
M 105
W 2
XL 20,2560
M 106
M 107
R 0
M 108
mw 51
M 109
ad 84,2022
W 0
M 110
M 111
M 112
W 1
M 113
M 114
M 115
W 2
M 116
M 117
M 118
bc 49,22,66,60
M 119
WL 2
R 2
M 120
mh 9
M 121
ad 66,4256
H 0
M 132
M 133
TX 58,9216
TX 62,23040
M 122
DG 1024
M 123
M 134
M 135
TY 66,9728
TY 70,23552
M 124
bc 48,22,64,56
M 125
mh 12
M 136
ad 64,4256
H 0
M 137
TX 74,10752
TX 78,24576
M 126
WL 4
Q 0
M 127
Q 2
M 138
Q 4
M 139
TY 82,11264
TY 86,25088
M 128
Q 6
M 129
WL 4
Q 8
M 140
Q 10
M 141
TX 90,12288
TX 94,26112
M 130
Q 12
M 131
R 4
M 142
Q 14
M 143
TY 98,12800
TY 102,26624
M 0
R 6
M 1
WL 10
Q 16
M 12
Q 18
M 13
Q 20
M 2
Q 22
M 3
WL 8
Q 24
M 14
Q 26
M 15
Q 28
M 4
Q 30
M 5
WL 6
Q 32
M 16
Q 34
M 17
Q 36
M 6
Q 38
M 7
WL 2
Q 40
M 18
Q 42
M 19
Q 44
M 8
Q 46
M 9
H 1
UL 6,38
M 20
UH 7,38
AD 168,6
M 21
AD 192,7
UL 6,39
M 10
H 2
UH 7,39
M 11
R 8
M 22
AD 216,6
AD 240,7
M 23
R 10
PA 12288
DF
M 24
UL 6,40
UH 7,40
M 25
AD 169,6
AD 193,7
M 26
UL 6,41
UH 7,41
M 27
AD 217,6
AD 241,7
M 28
UL 6,42
UH 7,42
M 29
AD 170,6
AD 194,7
M 30
UL 6,43
UH 7,43
M 31
AD 218,6
AD 242,7
M 32
UL 6,44
UH 7,44
M 33
WV 6
AD 171,6
AD 195,7
M 34
WL 0
FP
UL 6,45
M 35
UH 7,45
AD 219,6
M 36
AD 243,7
M 37
M 38
M 39
M 40
M 41
M 42
M 43
M 44
M 45
FR
M 46
M 47
M 48
M 49
M 50
M 51
M 52
M 53
WL 0
FQ 160
M 54
M 55
M 56
FZ 160
R 24
M 57
mw 72
R 26
M 58
TY 106,520
TY 110,14344
M 59
R 28
M 60
ad 84,2064
W 0
M 61
M 62
M 63
W 1
M 64
M 65
M 66
W 2
M 67
WL 3
AN 104,106
AN 105,107
M 68
SH 106
SH 107
M 69
mw 75
AN 106,106
AN 107,107
M 70
WL 2
AN 108,110
AN 109,111
M 71
R 30
M 72
SH 110
SH 111
M 73
AN 110,110
AN 111,111
M 74
TY 114,2056
TY 118,15880
M 75
ad 84,2070
W 0
M 76
M 77
M 78
W 1
M 79
M 80
M 81
W 2
M 82
M 83
R 32
M 84
mw 0
WL 3
AN 112,114
AN 113,115
M 85
SH 114
)"
R"(SH 115
M 86
AN 114,114
AN 115,115
M 87
WL 2
AN 116,118
AN 117,119
M 88
SH 118
SH 119
M 89
AN 118,118
AN 119,119
M 90
TY 122,3592
TY 126,17416
M 91
ad 84,2112
M 92
W 0
M 93
ae 44,68,480
M 94
s_bitcmp1_b32 s50, 10
s_cselect_b32 s51, 24576, 0
v_add_u32 v47, s51, v3
M 95
R 34
M 96
W 1
XL 22,256
M 97
XL 24,1024
M 98
XL 26,1792
M 99
WL 3
AN 120,122
AN 121,123
M 100
SH 122
SH 123
M 101
AN 122,122
AN 123,123
M 102
WL 2
AN 124,126
AN 125,127
M 103
SH 126
SH 127
M 104
AN 126,126
AN 127,127
M 105
W 2
XL 28,2560
M 106
M 107
R 48
M 108
mw 3
M 109
ad 84,2118
W 0
M 110
M 111
M 112
W 1
M 113
M 114
M 115
W 2
M 116
M 117
M 118
bc 49,23,66,60
M 119
WL 2
R 50
M 120
mh 18
M 121
ad 66,4448
H 0
M 132
M 133
TX 58,0
TX 62,13824
M 122
DG 1024
M 123
M 134
M 135
TY 66,512
TY 70,14336
M 124
bc 48,23,64,56
M 125
mh 21
M 136
ad 64,4448
H 0
M 137
TX 74,1536
TX 78,15360
M 126
WL 4
Q 0
M 127
Q 2
M 138
Q 4
M 139
TY 82,2048
TY 86,15872
M 128
Q 6
M 129
WL 4
Q 8
M 140
Q 10
M 141
TX 90,3072
TX 94,16896
M 130
Q 12
M 131
R 52
M 142
Q 14
M 143
TY 98,3584
TY 102,17408
M 0
R 54
M 1
WL 10
Q 16
M 12
Q 18
M 13
Q 20
M 2
Q 22
M 3
WL 8
Q 24
M 14
Q 26
M 15
Q 28
M 4
Q 30
M 5
WL 6
Q 32
M 16
Q 34
M 17
Q 36
M 6
Q 38
M 7
WL 2
Q 40
M 18
Q 42
M 19
Q 44
M 8
Q 46
M 9
H 1
UL 6,14
M 20
UH 7,14
AD 172,6
M 21
AD 196,7
UL 6,15
M 10
H 2
UH 7,15
M 11
R 56
M 22
AD 220,6
AD 244,7
M 23
R 58
M 24
UL 6,16
UH 7,16
M 25
AD 173,6
AD 197,7
M 26
UL 6,17
UH 7,17
M 27
AD 221,6
AD 245,7
M 28
UL 6,18
UH 7,18
M 29
AD 174,6
AD 198,7
M 30
UL 6,19
UH 7,19
M 31
AD 222,6
AD 246,7
M 32
UL 6,20
UH 7,20
M 33
WV 5
AD 175,6
AD 199,7
M 34
WL 0
FP
UL 6,21
M 35
UH 7,21
AD 223,6
M 36
AD 247,7
M 37
M 38
M 39
M 40
M 41
M 42
M 43
M 44
M 45
FR
M 46
M 47
M 48
M 49
M 50
M 51
M 52
M 53
WL 0
FQ 164
M 54
M 55
M 56
FZ 164
R 72
M 57
mw 24
R 74
M 58
TY 106,5128
TY 110,18952
M 59
R 76
M 60
ad 84,2160
W 0
M 61
M 62
M 63
W 1
M 64
M 65
M 66
W 2
M 67
WL 3
AN 104,106
AN 105,107
M 68
SH 106
SH 107
M 69
mw 27
AN 106,106
AN 107,107
M 70
WL 2
AN 108,110
AN 109,111
M 71
R 78
M 72
SH 110
SH 111
M 73
AN 110,110
AN 111,111
M 74
TY 114,6664
TY 118,20488
M 75
ad 84,2166
W 0
M 76
M 77
M 78
W 1
M 79
M 80
M 81
W 2
M 82
M 83
R 80
M 84
mw 48
WL 3
AN 112,114
AN 113,115
M 85
SH 114
SH 115
M 86
AN 114,114
AN 115,115
M 87
WL 2
AN 116,118
AN 117,119
M 88
SH 118
SH 119
M 89
AN 118,118
AN 119,119
M 90
TY 122,8200
TY 126,22024
M 91
ad 84,2208
M 92
W 0
M 93
ae 44,68,480
M 94
s_bfe_u32 s51, s50, 0x1000b
v_mad_i32_i24 v47, v49, s51, v48
XL 30,256
M 95
R 82
M 96
W 1
XL 32,1024
M 97
M 98
M 99
WL 3
AN 120,122
AN 121,123
M 100
SH 122
SH 123
M 101
AN 122,122
AN 123,123
M 102
WL 2
AN 124,126
AN 125,127
M 103
SH 126
SH 127
M 104
AN 126,126
AN 127,127
M 105
W 2
XL 34,1792
M 106
M 107
R 0
M 108
mw 51
XL 36,2560
M 109
ad 84,2214
W 0
M 110
M 111
M 112
W 1
M 113
M 114
M 115
W 2
M 116
)"
R"(M 117
M 118
bc 49,0,66,60
M 119
WL 2
R 2
M 120
mh 0
M 121
ad 66,64
H 0
M 132
M 133
TX 58,4608
TX 62,18432
M 122
DG 1024
M 123
M 134
M 135
TY 66,5120
TY 70,18944
M 124
bc 48,0,64,56
M 125
mh 3
M 136
ad 64,32
H 0
M 137
TX 74,6144
TX 78,19968
M 126
WL 4
Q 0
M 127
Q 2
M 138
Q 4
M 139
TY 82,6656
TY 86,20480
M 128
Q 6
M 129
WL 4
Q 8
M 140
Q 10
M 141
TX 90,7680
TX 94,21504
M 130
Q 12
M 131
R 4
M 142
Q 14
M 143
TY 98,8192
TY 102,22016
M 0
R 6
M 1
WL 10
Q 16
M 12
Q 18
M 13
Q 20
M 2
Q 22
M 3
WL 8
Q 24
M 14
Q 26
M 15
Q 28
M 4
Q 30
M 5
WL 6
Q 32
M 16
Q 34
M 17
Q 36
M 6
Q 38
M 7
WL 2
Q 40
M 18
Q 42
M 19
Q 44
M 8
Q 46
M 9
H 1
UL 6,22
M 20
UH 7,22
AD 176,6
M 21
AD 200,7
UL 6,23
M 10
H 2
UH 7,23
M 11
R 8
M 22
AD 224,6
AD 248,7
M 23
R 10
PA 12288
DF
M 24
UL 6,24
UH 7,24
M 25
AD 177,6
AD 201,7
M 26
UL 6,25
UH 7,25
M 27
AD 225,6
AD 249,7
M 28
UL 6,26
UH 7,26
M 29
AD 178,6
AD 202,7
M 30
UL 6,27
UH 7,27
M 31
AD 226,6
AD 250,7
M 32
UL 6,28
UH 7,28
M 33
WV 6
AD 179,6
AD 203,7
M 34
WL 0
FP
UL 6,29
M 35
UH 7,29
AD 227,6
M 36
AD 251,7
M 37
M 38
M 39
M 40
M 41
M 42
M 43
M 44
M 45
FR
M 46
M 47
M 48
M 49
M 50
M 51
M 52
M 53
WL 0
FQ 168
M 54
M 55
M 56
FZ 168
R 24
M 57
mw 72
R 26
M 58
TY 106,9736
TY 110,23560
M 59
R 28
M 60
ad 84,2256
W 0
M 61
M 62
M 63
W 1
M 64
M 65
M 66
W 2
M 67
WL 3
AN 104,106
AN 105,107
M 68
SH 106
SH 107
M 69
mw 75
AN 106,106
AN 107,107
M 70
WL 2
AN 108,110
AN 109,111
M 71
R 30
M 72
SH 110
SH 111
M 73
AN 110,110
AN 111,111
M 74
TY 114,11272
TY 118,25096
M 75
ad 84,2262
W 0
M 76
M 77
M 78
W 1
M 79
M 80
M 81
W 2
M 82
M 83
R 32
M 84
mw 0
WL 3
AN 112,114
AN 113,115
M 85
SH 114
SH 115
M 86
AN 114,114
AN 115,115
M 87
WL 2
AN 116,118
AN 117,119
M 88
SH 118
SH 119
M 89
AN 118,118
AN 119,119
M 90
TY 122,12808
TY 126,26632
M 91
ad 84,0
M 92
W 0
M 93
M 94
M 95
R 34
M 96
W 1
M 97
M 98
M 99
WL 3
AN 120,122
AN 121,123
M 100
SH 122
SH 123
M 101
AN 122,122
AN 123,123
M 102
WL 2
AN 124,126
AN 125,127
M 103
SH 126
SH 127
M 104
AN 126,126
AN 127,127
M 105
W 2
M 106
M 107
R 48
M 108
mw 3
M 109
ad 84,6
W 0
M 110
M 111
M 112
W 1
M 113
M 114
M 115
W 2
M 116
M 117
M 118
bc 49,1,66,60
M 119
WL 2
R 50
M 120
mh 9
M 121
ad 66,256
H 0
M 132
M 133
TX 58,9216
TX 62,23040
M 122
DG 1024
M 123
M 134
M 135
TY 66,9728
TY 70,23552
M 124
bc 48,1,64,56
M 125
mh 12
M 136
ad 64,256
H 0
M 137
TX 74,10752
TX 78,24576
M 126
WL 4
Q 0
M 127
Q 2
M 138
Q 4
M 139
TY 82,11264
TY 86,25088
M 128
Q 6
M 129
WL 4
Q 8
M 140
Q 10
M 141
TX 90,12288
TX 94,26112
M 130
Q 12
M 131
R 52
M 142
Q 14
M 143
TY 98,12800
TY 102,26624
M 0
R 54
M 1
WL 10
Q 16
M 12
Q 18
M 13
Q 20
M 2
Q 22
M 3
WL 8
Q 24
M 14
Q 26
M 15
Q 28
M 4
Q 30
)"
R"(M 5
WL 6
Q 32
M 16
Q 34
M 17
Q 36
M 6
Q 38
M 7
WL 2
Q 40
M 18
Q 42
M 19
Q 44
M 8
Q 46
M 9
DG 1024
M 20
UL 6,30
UH 7,30
M 21
AD 180,6
AD 204,7
M 10
mh 15
UL 6,31
UH 7,31
M 11
R 56
M 22
ad 64,227
H 0
M 23
R 58
M 24
AD 228,6
AD 252,7
M 25
DG 1024
M 26
UL 6,32
UH 7,32
BV 52,4608
M 27
AD 181,6
AD 205,7
M 28
UL 6,33
UH 7,33
M 29
AD 229,6
AD 253,7
M 30
UL 6,34
UH 7,34
M 31
AD 182,6
AD 206,7
M 32
UL 6,35
UH 7,35
M 33
WV 6
AD 230,6
AD 254,7
M 34
WL 1
FP
UL 6,36
M 35
UH 7,36
AD 183,6
M 36
AD 207,7
UL 6,37
M 37
UH 7,37
AD 231,6
M 38
AD 255,7
M 39
M 40
ds_read_b64 v[12:13], v4 offset:4864
ds_read_b64 v[46:47], v4 offset:4872
M 41
AR 6,36
AR 7,60
M 42
AR 8,84
AR 9,108
M 43
WL 3
EF 6,52
EF 7,53
M 44
EF 8,54
EF 9,55
M 45
FR
CV 38,6,7
M 46
CV 39,8,9
AR 6,37
M 47
AR 7,61
AR 8,85
M 48
AR 9,109
EF 6,52
M 49
EF 7,53
EF 8,54
M 50
EF 9,55
CV 40,6,7
M 51
CV 41,8,9
AR 6,38
M 52
AR 7,62
AR 8,86
M 53
WL 0
FQ 172
AR 9,110
M 54
EF 6,52
EF 7,53
M 55
EF 8,54
EF 9,55
M 56
FZ 172
R 72
M 57
mw 24
R 74
M 58
TY 106,520
TY 110,14344
M 59
R 76
M 60
ad 84,48
W 0
M 61
CV 42,6,7
CV 43,8,9
M 62
AR 6,39
AR 7,63
M 63
W 1
AR 8,87
M 64
AR 9,111
EF 6,52
M 65
EF 7,53
EF 8,54
M 66
W 2
EF 9,55
M 67
WL 3
AN 104,106
AN 105,107
M 68
SH 106
SH 107
M 69
mw 27
AN 106,106
AN 107,107
M 70
WL 2
AN 108,110
AN 109,111
M 71
R 78
M 72
SH 110
SH 111
M 73
AN 110,110
AN 111,111
M 74
TY 114,2056
TY 118,15880
M 75
ad 84,54
W 0
M 76
CV 44,6,7
CV 45,8,9
M 77
ae 42,74,384
AR 6,40
M 78
W 1
AR 7,64
M 79
s_bitcmp1_b32 s50, 6
s_cselect_b32 s52, 24576, 0
v_add_u32 v50, s52, v3
ST 38,0
M 80
AR 8,88
AR 9,112
M 81
W 2
EF 6,52
M 82
EF 7,53
EF 8,54
M 83
WL 1
R 80
M 84
mw 48
AN 112,114
AN 113,115
M 85
SH 114
SH 115
M 86
AN 114,114
AN 115,115
M 87
WL 2
AN 116,118
AN 117,119
M 88
SH 118
SH 119
M 89
AN 118,118
AN 119,119
M 90
TY 122,3592
TY 126,17416
M 91
ad 84,96
ST 40,768
M 92
W 0
EF 9,55
M 93
CV 14,6,7
CV 15,8,9
M 94
AR 6,41
AR 7,65
M 95
R 82
M 96
W 1
AR 8,89
M 97
AR 9,113
EF 6,52
M 98
EF 7,53
EF 8,54
M 99
WL 3
AN 120,122
AN 121,123
M 100
SH 122
SH 123
M 101
AN 122,122
AN 123,123
M 102
WL 2
AN 124,126
AN 125,127
M 103
SH 126
SH 127
ST 42,1536
M 104
AN 126,126
AN 127,127
M 105
W 2
EF 9,55
M 106
CV 16,6,7
CV 17,8,9
M 107
R 0
M 108
mw 51
AR 6,42
AR 7,66
M 109
ad 84,102
W 0
M 110
AR 8,90
AR 9,114
M 111
EF 6,52
EF 7,53
M 112
W 1
EF 8,54
M 113
EF 9,55
CV 18,6,7
M 114
CV 19,8,9
AR 6,43
M 115
W 2
ST 44,2304
M 116
AR 7,67
AR 8,91
M 117
AR 9,115
EF 6,52
M 118
bc 49,2,66,60
EF 7,53
M 119
WL 0
R 2
M 120
mh 18
EF 8,54
EF 9,55
M 121
ad 66,448
H 0
M 132
CV 20,6,7
CV 21,8,9
M 133
TX 58,0
TX 62,13824
BV 52,5120
M 122
DG 1024
M 123
AR 6,132
AR 7,156
M 134
AR 8,180
EF 6,12
M 135
TY 66,512
TY 70,14336
M 124
bc 48,2,64,56
ae 42,74,384
M 125
mh 21
EF 7,13
EF 8,46
M 136
ad 64,448
H 0
M 137
WL 3
TX 74,1536
TX 78,15360
M 126
s_bfe_u32 s52, s50, 0x10007
Q 0
v_mad_i32_i24 v50, v49, s52, v48
M 127
Q 2
ST 14,0
M 138
Q 4
M 139
TY 82,2048
TY 86,15872
M 128
Q 6
M 129
WL 4
Q 8
M 140
Q 10
M 141
TX 90,3072
TX 94,16896
M 130
Q 12
M 131
R 4
M 142
Q 14
)"
R"(M 143
TY 98,3584
TY 102,17408
v_mfma_f32_16x16x32_f16 a[36:39], v[56:59], a[0:3], a[32:35]
R 6
M 1
WL 10
Q 16
ST 16,768
v_mfma_f32_16x16x32_f16 a[60:63], v[56:59], a[8:11], a[32:35]
Q 18
M 13
Q 20
v_mfma_f32_16x16x32_f16 a[40:43], v[64:67], a[0:3], a[32:35]
Q 22
M 3
WL 8
Q 24
v_mfma_f32_16x16x32_f16 a[64:67], v[64:67], a[8:11], a[32:35]
Q 26
M 15
Q 28
M 4
Q 30
M 5
WL 6
Q 32
M 16
Q 34
M 17
Q 36
M 6
Q 38
M 7
WL 1
Q 40
ST 18,1536
M 18
Q 42
M 19
Q 44
M 8
Q 46
M 9
H 1
EF 136,47
M 20
CV 22,6,7
CV 23,8,136
M 21
AR 6,133
AR 7,157
M 10
H 2
AR 8,181
M 11
R 8
M 22
EF 6,12
EF 7,13
M 23
R 10
PA 12288
DF
v_mfma_f32_16x16x32_f16 a[84:87], v[56:59], a[16:19], a[32:35]
EF 8,46
EF 137,47
M 25
ST 20,2304
CV 24,6,7
v_mfma_f32_16x16x32_f16 a[88:91], v[64:67], a[16:19], a[32:35]
CV 25,8,137
AR 6,134
M 27
AR 7,158
AR 8,182
M 28
EF 6,12
EF 7,13
M 29
EF 8,46
EF 138,47
M 30
CV 26,6,7
CV 27,8,138
M 31
AR 6,135
AR 7,159
M 32
AR 8,183
EF 6,12
M 33
WV 11
EF 7,13
EF 8,46
M 34
WL 0
FP
EF 139,47
M 35
CV 28,6,7
CV 29,8,139
v_mfma_f32_16x16x32_f16 a[108:111], v[56:59], a[24:27], a[32:35]
AR 6,136
AR 7,160
AR 8,184
EF 6,12
M 37
ae 42,74,384
EF 7,13
EF 8,46
EF 140,47
v_mfma_f32_16x16x32_f16 a[112:115], v[64:67], a[24:27], a[32:35]
s_bitcmp1_b32 s50, 6
s_cselect_b32 s52, 24576, 0
v_add_u32 v50, s52, v3
ST 22,128
CV 30,6,7
M 39
CV 31,8,140
AR 6,137
AR 7,161
AR 8,185
M 40
EF 6,12
EF 7,13
EF 8,46
EF 141,47
M 41
CV 32,6,7
CV 33,8,141
AR 6,138
AR 7,162
M 42
AR 8,186
EF 6,12
EF 7,13
EF 8,46
M 43
EF 142,47
CV 34,6,7
CV 35,8,142
AR 6,139
M 44
AR 7,163
AR 8,187
EF 6,12
EF 7,13
M 45
FR
EF 8,46
EF 143,47
CV 36,6,7
M 46
CV 37,8,143
M 47
ds_read_b64 v[12:13], v4 offset:4608
ds_read_b64 v[46:47], v4 offset:4616
v_mfma_f32_16x16x32_f16 a[132:135], v[56:59], a[0:3], a[32:35]
EF 160,52
EF 184,53
M 49
EF 208,54
EF 232,55
v_mfma_f32_16x16x32_f16 a[136:139], v[64:67], a[0:3], a[32:35]
ST 24,896
CV 38,160,184
M 51
CV 39,208,232
EF 161,52
M 52
EF 185,53
EF 209,54
M 53
WL 2
FQ 176
EF 233,55
M 54
CV 40,161,185
CV 41,209,233
M 55
EF 162,52
EF 186,53
M 56
FZ 176
R 24
M 57
mw 72
R 26
M 58
TY 106,5128
TY 110,18952
M 59
R 28
v_mfma_f32_16x16x32_f16 a[156:159], v[56:59], a[8:11], a[32:35]
ad 84,144
W 0
M 61
EF 210,54
EF 234,55
v_mfma_f32_16x16x32_f16 a[160:163], v[64:67], a[8:11], a[32:35]
ST 26,1664
CV 42,162,186
M 63
W 1
CV 43,210,234
M 64
EF 163,52
EF 187,53
M 65
EF 211,54
EF 235,55
M 66
W 2
CV 44,163,187
M 67
WL 3
AN 104,106
AN 105,107
M 68
SH 106
SH 107
M 69
mw 75
AN 106,106
AN 107,107
M 70
WL 2
AN 108,110
AN 109,111
M 71
R 30
v_mfma_f32_16x16x32_f16 a[180:183], v[56:59], a[16:19], a[32:35]
SH 110
SH 111
M 73
AN 110,110
AN 111,111
v_mfma_f32_16x16x32_f16 a[184:187], v[64:67], a[16:19], a[32:35]
TY 114,6664
TY 118,20488
ST 28,2432
M 75
ad 84,150
W 0
M 76
CV 45,211,235
M 77
EF 164,52
EF 188,53
EF 212,54
EF 236,55
M 78
W 1
CV 14,164,188
CV 15,212,236
EF 165,52
M 79
EF 189,53
EF 213,54
EF 237,55
CV 16,165,189
M 80
CV 17,213,237
EF 166,52
EF 190,53
EF 214,54
M 81
W 2
EF 238,55
CV 18,166,190
CV 19,214,238
M 82
EF 167,52
EF 191,53
EF 215,54
EF 239,55
M 83
WL 0
R 32
CV 20,167,191
CV 21,215,239
v_mfma_f32_16x16x32_f16 v[136:139], v[56:59], a[24:27], v[128:131]
mw 0
AN 112,114
AN 113,115
BV 52,4864
M 85
SH 114
SH 115
AR 6,44
v_mfma_f32_16x16x32_f16 v[140:143], v[64:67], a[24:27], v[128:131]
AN 114,114
AN 115,115
AR 7,68
M 87
AN 116,118
AN 117,119
AR 8,92
M 88
SH 118
SH 119
AR 9,116
M 89
AN 118,118
AN 119,119
EF 6,12
M 90
TY 122,8200
TY 126,22024
EF 7,13
M 91
ad 84,192
EF 8,46
M 92
W 0
ae 42,74,384
M 93
s_bfe_u32 s52, s50, 0x10007
v_mad_i32_i24 v50, v49, s52, v48
ST 30,128
EF 9,47
M 94
CV 22,6,7
CV 23,8,9
AR 6,45
M 95
R 34
AR 7,69
v_mfma_f32_16x16x32_f16 v[160:163], v[56:59], a[0:3], v[128:131]
W 1
AR 8,93
AR 9,117
M 97
EF 6,12
EF 7,13
EF 8,46
v_mfma_f32_16x16x32_f16 v[164:167], v[64:67], a[0:3], v[128:131]
EF 9,47
CV 24,6,7
CV 25,8,9
M 99
WL 3
AN 120,122
AN 121,123
AR 6,46
M 100
SH 122
SH 123
AR 7,70
M 101
AN 122,122
AN 123,123
AR 8,94
M 102
WL 2
AN 124,126
AN 125,127
AR 9,118
M 103
SH 126
SH 127
EF 6,12
M 104
AN 126,126
AN 127,127
EF 7,13
M 105
W 2
ST 32,896
EF 8,46
M 106
EF 9,47
CV 26,6,7
CV 27,8,9
M 107
R 48
AR 6,47
v_mfma_f32_16x16x32_f16 v[184:187], v[56:59], a[8:11], v[128:131]
mw 3
AR 7,71
AR 8,95
AR 9,119
M 109
ad 84,198
W 0
EF 6,12
v_mfma_f32_16x16x32_f16 v[188:191], v[64:67], a[8:11], v[128:131]
EF 7,13
EF 8,46
EF 9,47
M 111
CV 28,6,7
CV 29,8,9
M 112
W 1
AR 6,48
AR 7,72
M 113
AR 8,96
)"
R"(AR 9,120
EF 6,12
M 114
EF 7,13
EF 8,46
EF 9,47
M 115
W 2
CV 6,6,7
CV 7,8,9
M 116
AW 200,6
AW 201,7
AR 6,49
M 117
ST 34,1664
AR 7,73
AR 8,97
M 118
bc 49,3,66,60
AR 9,121
EF 6,12
M 119
WL 0
R 50
EF 7,13
v_mfma_f32_16x16x32_f16 v[208:211], v[56:59], a[16:19], v[128:131]
mh 0
EF 8,46
EF 9,47
CV 6,6,7
M 121
ad 66,640
H 0
CV 7,8,9
v_mfma_f32_16x16x32_f16 v[232:235], v[56:59], a[24:27], v[128:131]
AW 202,6
AW 203,7
AR 6,50
M 133
TX 58,4608
TX 62,18432
AR 7,74
v_mfma_f32_16x16x32_f16 v[212:215], v[64:67], a[16:19], v[128:131]
DG 1024
AR 8,98
M 123
AR 9,122
EF 6,12
EF 7,13
v_mfma_f32_16x16x32_f16 v[236:239], v[64:67], a[24:27], v[128:131]
EF 8,46
EF 9,47
CV 6,6,7
M 135
TY 66,5120
TY 70,18944
CV 7,8,9
M 124
bc 48,3,64,56
AW 204,6
AW 205,7
M 125
mh 3
ST 36,2432
AR 6,51
AR 7,75
M 136
ad 64,640
H 0
AR 8,99
M 137
TX 74,6144
TX 78,19968
AR 9,123
M 126
WL 4
Q 0
EF 6,12
M 127
Q 2
EF 7,13
M 138
Q 4
EF 8,46
M 139
TY 82,6656
TY 86,20480
EF 9,47
M 128
Q 6
CV 6,6,7
M 129
WL 4
Q 8
CV 7,8,9
M 140
Q 10
AW 206,6
M 141
TX 90,7680
TX 94,21504
AW 207,7
M 130
Q 12
ds_read_b64 v[12:13], v4 offset:5120
M 131
R 52
ds_read_b64 v[46:47], v4 offset:5128
M 142
Q 14
M 143
TY 98,8192
TY 102,22016
M 0
R 54
M 1
WL 12
Q 16
M 12
Q 18
M 13
Q 20
M 2
Q 22
M 3
WL 10
Q 24
M 14
Q 26
M 15
Q 28
v_mfma_f32_16x16x32_f16 a[44:47], v[72:75], a[0:3], a[32:35]
Q 30
M 5
WL 7
Q 32
v_mfma_f32_16x16x32_f16 a[68:71], v[72:75], a[8:11], a[32:35]
Q 34
M 17
Q 36
v_mfma_f32_16x16x32_f16 a[48:51], v[80:83], a[0:3], a[32:35]
Q 38
M 7
WL 2
Q 40
v_mfma_f32_16x16x32_f16 a[72:75], v[80:83], a[8:11], a[32:35]
Q 42
M 19
Q 44
M 8
Q 46
M 9
H 1
ae 42,74,384
M 20
s_bitcmp1_b32 s50, 6
s_cselect_b32 s52, 24576, 0
v_add_u32 v50, s52, v3
ST 38,256
M 21
AR 6,140
AR 7,164
M 10
H 2
AR 8,188
M 11
R 56
M 22
EF 6,52
EF 7,53
M 23
R 58
M 24
EF 8,54
EF 144,55
M 25
CV 30,6,7
CV 31,8,144
M 26
AR 6,141
AR 7,165
M 27
AR 8,189
EF 6,52
v_mfma_f32_16x16x32_f16 a[92:95], v[72:75], a[16:19], a[32:35]
EF 7,53
EF 8,54
M 29
EF 145,55
CV 32,6,7
v_mfma_f32_16x16x32_f16 a[96:99], v[80:83], a[16:19], a[32:35]
ST 40,1024
CV 33,8,145
M 31
AR 6,142
AR 7,166
M 32
AR 8,190
EF 6,52
M 33
WV 9
EF 7,53
EF 8,54
M 34
WL 0
FP
EF 146,55
M 35
CV 34,6,7
CV 35,8,146
M 36
AR 6,143
AR 7,167
M 37
AR 8,191
EF 6,52
M 38
EF 7,53
EF 8,54
M 39
EF 147,55
CV 36,6,7
v_mfma_f32_16x16x32_f16 a[116:119], v[72:75], a[24:27], a[32:35]
CV 37,8,147
M 41
AR 6,144
AR 7,168
AR 8,192
EF 6,52
v_mfma_f32_16x16x32_f16 a[120:123], v[80:83], a[24:27], a[32:35]
ST 42,1792
EF 7,53
EF 8,54
EF 148,55
M 43
CV 6,6,7
CV 7,8,148
AW 208,6
AW 209,7
M 44
AR 6,145
AR 7,169
AR 8,193
EF 6,52
M 45
FR
EF 7,53
EF 8,54
EF 149,55
M 46
CV 6,6,7
CV 7,8,149
AW 210,6
AW 211,7
M 47
AR 6,146
AR 7,170
AR 8,194
EF 6,52
M 48
EF 7,53
EF 8,54
EF 150,55
CV 6,6,7
M 49
CV 7,8,150
AW 212,6
AW 213,7
AR 6,147
M 50
AR 7,171
AR 8,195
EF 6,52
EF 7,53
M 51
EF 8,54
EF 151,55
CV 6,6,7
CV 7,8,151
v_mfma_f32_16x16x32_f16 a[140:143], v[72:75], a[0:3], a[32:35]
AW 214,6
AW 215,7
M 53
WL 0
FQ 180
BV 52,4608
EF 168,12
v_mfma_f32_16x16x32_f16 a[144:147], v[80:83], a[0:3], a[32:35]
ST 44,2560
EF 192,13
EF 216,46
M 55
EF 240,47
CV 6,168,192
CV 7,216,240
M 56
FZ 180
R 72
AW 216,6
M 57
mw 24
R 74
AW 217,7
M 58
TY 106,9736
TY 110,23560
EF 169,12
M 59
R 76
EF 193,13
M 60
ad 84,240
W 0
EF 217,46
M 61
EF 241,47
CV 6,169,193
CV 7,217,241
M 62
AW 218,6
AW 219,7
EF 170,12
M 63
W 1
EF 194,13
EF 218,46
v_mfma_f32_16x16x32_f16 a[164:167], v[72:75], a[8:11], a[32:35]
EF 242,47
CV 6,170,194
CV 7,218,242
M 65
AW 220,6
AW 221,7
EF 171,12
v_mfma_f32_16x16x32_f16 a[168:171], v[80:83], a[8:11], a[32:35]
WL 3
W 2
ae 42,74,384
M 67
s_bfe_u32 s52, s50, 0x10007
AN 104,106
AN 105,107
v_mad_i32_i24 v50, v49, s52, v48
M 68
SH 106
SH 107
ST 14,256
M 69
mw 27
AN 106,106
AN 107,107
EF 195,13
M 70
WL 0
AN 108,110
AN 109,111
EF 219,46
M 71
R 78
EF 243,47
M 72
SH 110
SH 111
CV 6,171,195
M 73
AN 110,110
AN 111,111
CV 7,219,243
M 74
TY 114,11272
TY 118,25096
AW 222,6
M 75
ad 84,246
W 0
AW 223,7
v_mfma_f32_16x16x32_f16 a[188:191], v[72:75], a[16:19], a[32:35]
EF 172,12
EF 196,13
EF 220,46
M 77
EF 244,47
CV 38,172,196
CV 39,220,244
v_mfma_f32_16x16x32_f16 a[192:195], v[80:83], a[16:19], a[32:35]
W 1
EF 173,12
EF 197,13
M 79
EF 221,46
EF 245,47
CV 40,173,197
M 80
ST 16,1024
CV 41,221,245
EF 174,12
M 81
W 2
EF 198,13
)"
R"(EF 222,46
M 82
EF 246,47
CV 42,174,198
CV 43,222,246
M 83
WL 0
R 80
EF 175,12
M 84
mw 48
AN 112,114
AN 113,115
EF 199,13
M 85
SH 114
SH 115
EF 223,46
M 86
AN 114,114
AN 115,115
EF 247,47
M 87
AN 116,118
AN 117,119
CV 44,175,199
v_mfma_f32_16x16x32_f16 v[144:147], v[72:75], a[24:27], v[128:131]
SH 118
SH 119
CV 45,223,247
M 89
AN 118,118
AN 119,119
ds_read_b64 v[12:13], v4 offset:4864
v_mfma_f32_16x16x32_f16 v[148:151], v[80:83], a[24:27], v[128:131]
TY 122,12808
TY 126,26632
ds_read_b64 v[46:47], v4 offset:4872
M 91
ad 84,288
AR 6,52
M 92
W 0
ST 18,1792
AR 7,76
M 93
AR 8,100
AR 9,124
EF 6,52
M 94
EF 7,53
EF 8,54
EF 9,55
M 95
R 82
CV 6,6,7
M 96
W 1
CV 7,8,9
AW 224,6
M 97
AW 225,7
AR 6,53
AR 7,77
M 98
AR 8,101
AR 9,125
EF 6,52
M 99
WL 4
AN 120,122
AN 121,123
EF 7,53
v_mfma_f32_16x16x32_f16 v[168:171], v[72:75], a[0:3], v[128:131]
SH 122
SH 123
EF 8,54
M 101
AN 122,122
AN 123,123
EF 9,55
v_mfma_f32_16x16x32_f16 v[172:175], v[80:83], a[0:3], v[128:131]
WL 2
AN 124,126
AN 125,127
CV 6,6,7
M 103
SH 126
SH 127
CV 7,8,9
M 104
AN 126,126
AN 127,127
ST 20,2560
M 105
W 2
AW 226,6
AW 227,7
M 106
AR 6,54
AR 7,78
AR 8,102
M 107
R 0
AR 9,126
M 108
mw 51
EF 6,52
EF 7,53
EF 8,54
M 109
ad 84,294
W 0
EF 9,55
M 110
CV 6,6,7
CV 7,8,9
AW 228,6
M 111
AW 229,7
AR 6,55
AR 7,79
v_mfma_f32_16x16x32_f16 v[192:195], v[72:75], a[8:11], v[128:131]
W 1
AR 8,103
AR 9,127
M 113
EF 6,52
EF 7,53
EF 8,54
v_mfma_f32_16x16x32_f16 v[196:199], v[80:83], a[8:11], v[128:131]
EF 9,55
CV 6,6,7
CV 7,8,9
M 115
W 2
AW 230,6
AW 231,7
M 116
ae 42,72,384
AR 6,56
AR 7,80
M 117
s_bitcmp1_b32 s50, 8
s_cselect_b32 s52, 24576, 0
v_add_u32 v50, s52, v3
ST 22,0
M 118
bc 49,4,66,60
AR 8,104
AR 9,128
M 119
WL 0
R 2
EF 6,52
M 120
mh 9
EF 7,53
EF 8,54
EF 9,55
M 121
ad 66,832
H 0
CV 14,6,7
M 132
CV 15,8,9
AR 6,57
AR 7,81
M 133
TX 58,9216
TX 62,23040
AR 8,105
M 122
DG 1024
AR 9,129
M 123
EF 6,52
EF 7,53
EF 8,54
M 134
EF 9,55
CV 16,6,7
CV 17,8,9
M 135
TY 66,9728
TY 70,23552
AR 6,58
v_mfma_f32_16x16x32_f16 v[216:219], v[72:75], a[16:19], v[128:131]
bc 48,4,64,56
AR 7,82
AR 8,106
M 125
mh 12
ST 24,768
AR 9,130
EF 6,52
v_mfma_f32_16x16x32_f16 v[240:243], v[72:75], a[24:27], v[128:131]
ad 64,832
H 0
EF 7,53
M 137
TX 74,10752
TX 78,24576
EF 8,54
v_mfma_f32_16x16x32_f16 v[220:223], v[80:83], a[16:19], v[128:131]
WL 4
Q 0
EF 9,55
M 127
Q 2
CV 18,6,7
v_mfma_f32_16x16x32_f16 v[244:247], v[80:83], a[24:27], v[128:131]
Q 4
CV 19,8,9
M 139
TY 82,11264
TY 86,25088
AR 6,59
M 128
Q 6
AR 7,83
M 129
WL 4
Q 8
AR 8,107
M 140
Q 10
AR 9,131
M 141
TX 90,12288
TX 94,26112
EF 6,52
M 130
Q 12
EF 7,53
M 131
R 4
ST 26,1536
M 142
Q 14
EF 8,54
M 143
TY 98,12800
TY 102,26624
EF 9,55
M 0
R 6
CV 20,6,7
M 1
WL 8
Q 16
CV 21,8,9
M 12
Q 18
BV 52,5120
M 13
Q 20
M 2
Q 22
M 3
Q 24
M 14
Q 26
M 15
Q 28
M 4
Q 30
M 5
WL 5
Q 32
ST 28,2304
M 16
Q 34
M 17
Q 36
M 6
Q 38
M 7
WL 1
Q 40
M 18
Q 42
M 19
Q 44
v_mfma_f32_16x16x32_f16 a[52:55], v[88:91], a[0:3], a[32:35]
Q 46
M 9
H 1
AR 6,148
v_mfma_f32_16x16x32_f16 a[76:79], v[88:91], a[8:11], a[32:35]
AR 7,172
AR 8,196
M 21
EF 6,12
EF 7,13
v_mfma_f32_16x16x32_f16 a[56:59], v[96:99], a[0:3], a[32:35]
H 2
EF 8,46
M 11
R 8
v_mfma_f32_16x16x32_f16 a[80:83], v[96:99], a[8:11], a[32:35]
ae 42,72,384
ST 30,128
M 23
R 10
PA 12288
DF
M 24
EF 152,47
CV 6,6,7
M 25
CV 7,8,152
AW 232,6
M 26
AW 233,7
AR 6,149
M 27
AR 7,173
AR 8,197
M 28
EF 6,12
EF 7,13
M 29
EF 8,46
EF 153,47
M 30
CV 6,6,7
CV 7,8,153
M 31
AW 234,6
AW 235,7
v_mfma_f32_16x16x32_f16 a[100:103], v[88:91], a[16:19], a[32:35]
AR 6,150
AR 7,174
M 33
WV 11
AR 8,198
EF 6,12
v_mfma_f32_16x16x32_f16 a[104:107], v[96:99], a[16:19], a[32:35]
WL 0
FP
ST 32,896
M 35
EF 7,13
EF 8,46
M 36
EF 154,47
CV 6,6,7
M 37
CV 7,8,154
AW 236,6
M 38
AW 237,7
AR 6,151
M 39
AR 7,175
AR 8,199
M 40
EF 6,12
EF 7,13
M 41
EF 8,46
EF 155,47
M 42
CV 6,6,7
CV 7,8,155
M 43
AW 238,6
AW 239,7
v_mfma_f32_16x16x32_f16 a[124:127], v[88:91], a[24:27], a[32:35]
AR 6,152
AR 7,176
EF 6,12
M 45
FR
EF 7,13
EF 132,46
v_mfma_f32_16x16x32_f16 a[128:131], v[96:99], a[24:27], a[32:35]
ST 34,1664
EF 156,47
CV 22,6,7
M 47
CV 23,132,156
AR 6,153
AR 7,177
M 48
EF 6,12
EF 7,13
EF 133,46
M 49
EF 157,47
CV 24,6,7
CV 25,133,157
M 50
AR 6,154
AR 7,178
EF 6,12
M 51
EF 7,13
EF 134,46
)"
R"(EF 158,47
M 52
CV 26,6,7
CV 27,134,158
AR 6,155
M 53
WL 0
FQ 184
AR 7,179
EF 6,12
M 54
EF 7,13
EF 135,46
EF 159,47
M 55
CV 28,6,7
CV 29,135,159
v_mfma_f32_16x16x32_f16 a[148:151], v[88:91], a[0:3], a[32:35]
FZ 184
R 24
EF 176,52
M 57
mw 72
R 26
EF 200,53
v_mfma_f32_16x16x32_f16 a[152:155], v[96:99], a[0:3], a[32:35]
TY 106,520
TY 110,14344
ST 36,2432
M 59
R 28
EF 224,54
M 60
ad 84,336
W 0
EF 248,55
M 61
CV 6,176,200
CV 7,224,248
AW 240,6
M 62
AW 241,7
EF 177,52
EF 201,53
M 63
W 1
EF 225,54
EF 249,55
M 64
CV 6,177,201
CV 7,225,249
AW 242,6
M 65
AW 243,7
EF 178,52
EF 202,53
M 66
W 2
EF 226,54
EF 250,55
M 67
WL 3
AN 104,106
AN 105,107
CV 6,178,202
v_mfma_f32_16x16x32_f16 a[172:175], v[88:91], a[8:11], a[32:35]
SH 106
SH 107
CV 7,226,250
M 69
mw 75
AN 106,106
AN 107,107
AW 244,6
v_mfma_f32_16x16x32_f16 a[176:179], v[96:99], a[8:11], a[32:35]
WL 0
AN 108,110
AN 109,111
AW 245,7
M 71
R 30
EF 179,52
M 72
SH 110
SH 111
EF 203,53
M 73
AN 110,110
AN 111,111
EF 227,54
M 74
TY 114,2056
TY 118,15880
EF 251,55
M 75
ad 84,342
W 0
CV 6,179,203
M 76
ae 42,72,384
CV 7,227,251
AW 246,6
M 77
s_bfe_u32 s52, s50, 0x10009
v_mad_i32_i24 v50, v49, s52, v48
ST 38,256
AW 247,7
M 78
W 1
EF 180,52
EF 204,53
M 79
EF 228,54
EF 252,55
CV 6,180,204
v_mfma_f32_16x16x32_f16 a[196:199], v[88:91], a[16:19], a[32:35]
CV 7,228,252
AW 248,6
AW 249,7
M 81
W 2
EF 181,52
EF 205,53
v_mfma_f32_16x16x32_f16 v[132:135], v[96:99], a[16:19], v[128:131]
EF 229,54
EF 253,55
CV 6,181,205
M 83
WL 0
R 32
CV 7,229,253
M 84
mw 0
AN 112,114
AN 113,115
AW 250,6
M 85
SH 114
SH 115
AW 251,7
M 86
AN 114,114
AN 115,115
EF 182,52
M 87
AN 116,118
AN 117,119
EF 206,53
M 88
SH 118
SH 119
EF 230,54
M 89
AN 118,118
AN 119,119
ST 40,1024
M 90
TY 122,3592
TY 126,17416
EF 254,55
M 91
ad 84,384
CV 6,182,206
v_mfma_f32_16x16x32_f16 v[152:155], v[88:91], a[24:27], v[128:131]
W 0
CV 7,230,254
AW 252,6
M 93
AW 253,7
EF 183,52
EF 207,53
v_mfma_f32_16x16x32_f16 v[156:159], v[96:99], a[24:27], v[128:131]
EF 231,54
EF 255,55
CV 6,183,207
M 95
R 34
CV 7,231,255
M 96
W 1
AW 254,6
AW 255,7
M 97
M 98
M 99
WL 3
AN 120,122
AN 121,123
M 100
SH 122
SH 123
M 101
AN 122,122
AN 123,123
ST 42,1792
M 102
WL 2
AN 124,126
AN 125,127
M 103
SH 126
SH 127
v_mfma_f32_16x16x32_f16 v[176:179], v[88:91], a[0:3], v[128:131]
AN 126,126
AN 127,127
M 105
W 2
v_mfma_f32_16x16x32_f16 v[180:183], v[96:99], a[0:3], v[128:131]
M 107
R 48
M 108
mw 3
M 109
ad 84,390
W 0
M 110
M 111
M 112
W 1
M 113
ST 44,2560
M 114
M 115
W 2
v_mfma_f32_16x16x32_f16 v[200:203], v[88:91], a[8:11], v[128:131]
M 117
v_mfma_f32_16x16x32_f16 v[204:207], v[96:99], a[8:11], v[128:131]
bc 49,5,66,60
M 119
WL 2
R 50
M 120
mh 18
M 121
ad 66,1024
H 0
M 132
M 133
TX 58,0
TX 62,13824
M 122
DG 1024
M 123
ae 42,72,480
M 134
s_bfe_u32 s52, s50, 0x1000b
v_mad_i32_i24 v50, v49, s52, v48
ST 14,0
M 135
TY 66,512
TY 70,14336
M 124
bc 48,5,64,56
M 125
mh 21
M 136
ad 64,1024
H 0
M 137
TX 74,1536
TX 78,15360
M 126
WL 4
Q 0
M 127
Q 2
M 138
Q 4
M 139
TY 82,2048
TY 86,15872
v_mfma_f32_16x16x32_f16 v[224:227], v[88:91], a[16:19], v[128:131]
Q 6
M 129
WL 4
Q 8
v_mfma_f32_16x16x32_f16 v[248:251], v[88:91], a[24:27], v[128:131]
Q 10
ST 16,768
M 141
TX 90,3072
TX 94,16896
v_mfma_f32_16x16x32_f16 v[228:231], v[96:99], a[16:19], v[128:131]
Q 12
M 131
R 52
v_mfma_f32_16x16x32_f16 v[252:255], v[96:99], a[24:27], v[128:131]
Q 14
M 143
TY 98,3584
TY 102,17408
M 0
R 54
M 1
WL 10
Q 16
M 12
Q 18
M 13
Q 20
M 2
Q 22
M 3
WL 8
Q 24
M 14
Q 26
ST 18,1536
M 15
Q 28
M 4
Q 30
M 5
WL 6
Q 32
M 16
Q 34
M 17
Q 36
M 6
Q 38
M 7
WL 1
Q 40
M 18
Q 42
M 19
Q 44
M 8
Q 46
M 9
H 1
M 20
ST 20,2304
M 21
M 10
H 2
M 11
R 56
M 22
M 23
R 58
M 24
M 25
M 26
M 27
M 28
M 29
M 30
ae 42,72,480
ST 22,128
M 31
M 32
M 33
WV 10
M 34
WL 0
FP
M 35
M 36
M 37
M 38
M 39
M 40
M 41
M 42
ST 24,896
M 43
M 44
M 45
FR
M 46
M 47
M 48
M 49
M 50
M 51
M 52
M 53
WL 0
FQ 188
M 54
ST 26,1664
M 55
M 56
FZ 188
R 72
M 57
mw 24
R 74
M 58
TY 106,5128
TY 110,18952
M 59
R 76
M 60
ad 84,432
W 0
M 61
M 62
)"
R"(M 63
W 1
M 64
M 65
M 66
W 2
ST 28,2432
M 67
WL 3
AN 104,106
AN 105,107
M 68
SH 106
SH 107
M 69
mw 27
AN 106,106
AN 107,107
M 70
WL 2
AN 108,110
AN 109,111
M 71
R 78
M 72
SH 110
SH 111
M 73
AN 110,110
AN 111,111
M 74
TY 114,6664
TY 118,20488
M 75
ad 84,438
W 0
M 76
M 77
M 78
W 1
M 79
M 80
M 81
W 2
M 82
M 83
R 80
M 84
mw 48
WL 3
AN 112,114
AN 113,115
M 85
SH 114
SH 115
M 86
AN 114,114
AN 115,115
M 87
WL 2
AN 116,118
AN 117,119
M 88
SH 118
SH 119
M 89
AN 118,118
AN 119,119
M 90
TY 122,8200
TY 126,22024
M 91
ad 84,480
M 92
W 0
M 93
M 94
M 95
R 82
M 96
W 1
M 97
M 98
M 99
WL 3
AN 120,122
AN 121,123
M 100
SH 122
SH 123
M 101
AN 122,122
AN 123,123
M 102
WL 2
AN 124,126
AN 125,127
M 103
SH 126
SH 127
M 104
AN 126,126
AN 127,127
M 105
W 2
M 106
M 107
R 0
M 108
mw 51
M 109
ad 84,486
W 0
M 110
M 111
M 112
W 1
M 113
M 114
M 115
W 2
M 116
M 117
M 118
bc 49,6,66,60
M 119
WL 2
R 2
M 120
mh 0
M 121
ad 66,1216
H 0
M 132
M 133
TX 58,4608
TX 62,18432
M 122
DG 1024
M 123
M 134
M 135
TY 66,5120
TY 70,18944
M 124
bc 48,6,64,56
M 125
mh 3
ae 44,70,768
M 136
ad 64,1216
H 0
M 137
TX 74,6144
TX 78,19968
M 126
WL 4
Q 0
M 127
Q 2
M 138
Q 4
M 139
TY 82,6656
TY 86,20480
M 128
Q 6
M 129
WL 4
Q 8
M 140
Q 10
M 141
TX 90,7680
TX 94,21504
M 130
Q 12
M 131
R 4
M 142
Q 14
M 143
TY 98,8192
TY 102,22016
M 0
R 6
M 1
WL 10
Q 16
M 12
Q 18
M 13
Q 20
M 2
Q 22
M 3
WL 8
Q 24
M 14
Q 26
M 15
Q 28
M 4
Q 30
M 5
WL 6
Q 32
M 16
Q 34
M 17
Q 36
M 6
Q 38
M 7
WL 2
Q 40
M 18
Q 42
M 19
Q 44
M 8
Q 46
M 9
H 1
M 20
ae 42,72,384
M 21
s_bfe_u32 s52, s50, 0x10009
v_mad_i32_i24 v50, v49, s52, v48
SA 200,0
M 10
H 2
M 11
R 8
M 22
s_bitcmp1_b32 s50, 12
s_cselect_b32 s51, 24576, 0
M 23
R 10
PA 12288
DF
M 24
v_add_u32 v47, s51, v3
XL 30,0
M 25
XL 32,768
M 26
XL 34,1536
M 27
XL 36,2304
M 28
M 29
M 30
M 31
M 32
M 33
WV 11
M 34
WL 0
FP
M 35
M 36
M 37
M 38
M 39
M 40
M 41
M 42
M 43
M 44
M 45
FR
M 46
M 47
M 48
M 49
M 50
M 51
M 52
M 53
WL 0
FQ 192
M 54
M 55
M 56
FZ 192
R 24
M 57
mw 72
R 26
M 58
TY 106,9736
TY 110,23560
M 59
R 28
M 60
ad 84,528
W 0
M 61
M 62
M 63
W 1
M 64
M 65
M 66
W 2
M 67
WL 3
AN 104,106
AN 105,107
M 68
SH 106
SH 107
M 69
mw 75
AN 106,106
AN 107,107
M 70
WL 2
AN 108,110
AN 109,111
M 71
R 30
M 72
SH 110
SH 111
M 73
AN 110,110
AN 111,111
M 74
TY 114,11272
TY 118,25096
M 75
ad 84,534
W 0
M 76
M 77
M 78
W 1
M 79
M 80
M 81
W 2
M 82
M 83
R 32
M 84
mw 0
WL 3
AN 112,114
AN 113,115
M 85
SH 114
SH 115
M 86
AN 114,114
AN 115,115
M 87
WL 2
AN 116,118
AN 117,119
M 88
SH 118
SH 119
M 89
AN 118,118
AN 119,119
M 90
TY 122,12808
TY 126,26632
M 91
ad 84,576
M 92
W 0
SA 202,768
M 93
M 94
M 95
R 34
M 96
W 1
M 97
M 98
M 99
WL 3
AN 120,122
AN 121,123
M 100
SH 122
SH 123
M 101
AN 122,122
AN 123,123
M 102
WL 2
AN 124,126
AN 125,127
M 103
SH 126
SH 127
)"
R"(M 104
AN 126,126
AN 127,127
M 105
W 2
M 106
M 107
R 48
M 108
mw 3
M 109
ad 84,582
W 0
M 110
M 111
M 112
W 1
M 113
M 114
M 115
W 2
M 116
M 117
M 118
bc 49,7,66,60
M 119
WL 2
R 50
M 120
mh 9
M 121
ad 66,1408
H 0
M 132
M 133
TX 58,9216
TX 62,23040
M 122
DG 1024
M 123
M 134
M 135
TY 66,9728
TY 70,23552
M 124
bc 48,7,64,56
M 125
mh 12
M 136
ad 64,1408
H 0
M 137
TX 74,10752
TX 78,24576
M 126
WL 4
Q 0
M 127
Q 2
M 138
Q 4
M 139
TY 82,11264
TY 86,25088
M 128
Q 6
M 129
WL 4
Q 8
M 140
Q 10
M 141
TX 90,12288
TX 94,26112
M 130
Q 12
M 131
R 52
M 142
Q 14
M 143
TY 98,12800
TY 102,26624
M 0
R 54
M 1
WL 10
Q 16
M 12
Q 18
M 13
Q 20
M 2
Q 22
M 3
WL 8
Q 24
M 14
Q 26
M 15
Q 28
M 4
Q 30
M 5
WL 6
Q 32
M 16
Q 34
M 17
Q 36
M 6
Q 38
M 7
WL 2
Q 40
M 18
Q 42
M 19
Q 44
M 8
Q 46
M 9
H 1
SA 204,1536
M 20
ae 44,70,768
M 21
s_bfe_u32 s51, s50, 0x1000d
v_mad_i32_i24 v47, v49, s51, v48
XL 38,0
M 10
H 2
XL 40,768
M 11
R 56
M 22
XL 42,1536
M 23
R 58
M 24
XL 44,2304
M 25
M 26
M 27
M 28
M 29
M 30
M 31
M 32
M 33
WV 10
M 34
WL 0
FP
M 35
M 36
M 37
M 38
M 39
M 40
UL 6,30
UH 7,30
M 41
AR 8,36
AR 9,60
M 42
AD 8,6
AD 9,7
M 43
AW 36,8
AW 60,9
M 44
UL 6,31
UH 7,31
M 45
FR
AR 8,84
M 46
AR 9,108
AD 8,6
M 47
AD 9,7
AW 84,8
M 48
AW 108,9
UL 6,32
M 49
UH 7,32
AR 8,37
M 50
AR 9,61
AD 8,6
M 51
AD 9,7
AW 37,8
M 52
AW 61,9
UL 6,33
M 53
WL 0
FQ 196
UH 7,33
M 54
AR 8,85
AR 9,109
M 55
AD 8,6
AD 9,7
M 56
FZ 196
R 72
M 57
mw 24
R 74
M 58
TY 106,520
TY 110,14344
M 59
R 76
M 60
ad 84,624
W 0
M 61
AW 85,8
AW 109,9
M 62
UL 6,34
UH 7,34
M 63
W 1
AR 8,38
M 64
AR 9,62
AD 8,6
M 65
AD 9,7
AW 38,8
M 66
W 2
AW 62,9
M 67
WL 3
AN 104,106
AN 105,107
M 68
SH 106
SH 107
M 69
mw 27
AN 106,106
AN 107,107
M 70
WL 2
AN 108,110
AN 109,111
M 71
R 78
M 72
SH 110
SH 111
M 73
AN 110,110
AN 111,111
M 74
TY 114,2056
TY 118,15880
M 75
ad 84,630
W 0
M 76
UL 6,35
UH 7,35
M 77
AR 8,86
AR 9,110
M 78
W 1
AD 8,6
M 79
AD 9,7
AW 86,8
M 80
AW 110,9
UL 6,36
M 81
W 2
SA 206,2304
M 82
UH 7,36
AR 8,39
M 83
WL 1
R 80
M 84
mw 48
AN 112,114
AN 113,115
M 85
SH 114
SH 115
M 86
AN 114,114
AN 115,115
M 87
WL 2
AN 116,118
AN 117,119
M 88
SH 118
SH 119
M 89
AN 118,118
AN 119,119
M 90
TY 122,3592
TY 126,17416
M 91
ad 84,672
M 92
W 0
AR 9,63
M 93
AD 8,6
AD 9,7
M 94
AW 39,8
AW 63,9
M 95
R 82
M 96
W 1
UL 6,37
M 97
UH 7,37
AR 8,87
M 98
AR 9,111
AD 8,6
M 99
WL 3
AN 120,122
AN 121,123
M 100
SH 122
SH 123
M 101
AN 122,122
AN 123,123
M 102
WL 2
AN 124,126
AN 125,127
M 103
SH 126
SH 127
M 104
AN 126,126
AN 127,127
M 105
W 2
AD 9,7
M 106
AW 87,8
AW 111,9
M 107
R 0
M 108
mw 51
M 109
ad 84,678
W 0
M 110
M 111
M 112
W 1
M 113
M 114
M 115
W 2
M 116
M 117
M 118
bc 49,8,66,60
M 119
WL 2
R 2
M 120
mh 18
M 121
ad 66,1600
H 0
M 132
M 133
TX 58,0
TX 62,13824
M 122
DG 1024
M 123
M 134
M 135
)"
R"(TY 66,512
TY 70,14336
M 124
bc 48,8,64,56
M 125
mh 21
M 136
ad 64,1600
H 0
M 137
TX 74,1536
TX 78,15360
M 126
WL 4
Q 0
M 127
Q 2
M 138
Q 4
M 139
TY 82,2048
TY 86,15872
M 128
Q 6
M 129
WL 4
Q 8
M 140
Q 10
M 141
TX 90,3072
TX 94,16896
M 130
Q 12
M 131
R 4
M 142
Q 14
M 143
TY 98,3584
TY 102,17408
M 0
R 6
M 1
WL 10
Q 16
M 12
Q 18
M 13
Q 20
M 2
Q 22
M 3
WL 8
Q 24
M 14
Q 26
M 15
Q 28
M 4
Q 30
M 5
WL 6
Q 32
M 16
Q 34
M 17
Q 36
M 6
Q 38
M 7
WL 2
Q 40
M 18
Q 42
M 19
Q 44
M 8
Q 46
M 9
H 1
M 20
ae 42,72,384
M 21
SA 208,128
M 10
H 2
M 11
R 8
M 22
ae 44,68,768
M 23
R 10
PA 12288
DF
M 24
s_bitcmp1_b32 s50, 14
s_cselect_b32 s51, 24576, 0
v_add_u32 v47, s51, v3
M 25
XL 14,0
M 26
XL 16,768
M 27
XL 18,1536
M 28
XL 20,2304
M 29
M 30
M 31
M 32
M 33
WV 11
M 34
WL 0
FP
M 35
M 36
M 37
M 38
M 39
M 40
M 41
M 42
UL 6,38
UH 7,38
M 43
AR 8,40
AR 9,64
M 44
AD 8,6
AD 9,7
M 45
FR
AW 40,8
M 46
AW 64,9
UL 6,39
M 47
UH 7,39
AR 8,88
M 48
AR 9,112
AD 8,6
M 49
AD 9,7
AW 88,8
M 50
AW 112,9
UL 6,40
M 51
UH 7,40
AR 8,41
M 52
AR 9,65
AD 8,6
M 53
WL 0
FQ 200
AD 9,7
M 54
AW 41,8
AW 65,9
M 55
UL 6,41
UH 7,41
M 56
FZ 200
R 24
M 57
mw 72
R 26
M 58
TY 106,5128
TY 110,18952
M 59
R 28
M 60
ad 84,720
W 0
M 61
AR 8,89
AR 9,113
M 62
AD 8,6
AD 9,7
M 63
W 1
AW 89,8
M 64
AW 113,9
UL 6,42
M 65
UH 7,42
AR 8,42
M 66
W 2
AR 9,66
M 67
WL 3
AN 104,106
AN 105,107
M 68
SH 106
SH 107
M 69
mw 75
AN 106,106
AN 107,107
M 70
WL 2
AN 108,110
AN 109,111
M 71
R 30
M 72
SH 110
SH 111
M 73
AN 110,110
AN 111,111
M 74
TY 114,6664
TY 118,20488
M 75
ad 84,726
W 0
M 76
AD 8,6
AD 9,7
M 77
AW 42,8
AW 66,9
M 78
W 1
UL 6,43
M 79
UH 7,43
AR 8,90
M 80
AR 9,114
AD 8,6
M 81
W 2
AD 9,7
M 82
AW 90,8
AW 114,9
M 83
WL 1
R 32
M 84
mw 0
AN 112,114
AN 113,115
M 85
SH 114
SH 115
M 86
AN 114,114
AN 115,115
M 87
WL 2
AN 116,118
AN 117,119
M 88
SH 118
SH 119
M 89
AN 118,118
AN 119,119
M 90
TY 122,8200
TY 126,22024
M 91
ad 84,768
M 92
W 0
SA 210,896
M 93
UL 6,44
UH 7,44
M 94
AR 8,43
AR 9,67
M 95
R 34
M 96
W 1
AD 8,6
M 97
AD 9,7
AW 43,8
M 98
AW 67,9
UL 6,45
M 99
WL 3
AN 120,122
AN 121,123
M 100
SH 122
SH 123
M 101
AN 122,122
AN 123,123
M 102
WL 2
AN 124,126
AN 125,127
M 103
SH 126
SH 127
M 104
AN 126,126
AN 127,127
M 105
W 2
UH 7,45
M 106
AR 8,91
AR 9,115
M 107
R 48
M 108
mw 3
AD 8,6
AD 9,7
M 109
ad 84,774
W 0
M 110
AW 91,8
AW 115,9
M 111
M 112
W 1
M 113
M 114
M 115
W 2
M 116
M 117
M 118
bc 49,9,66,60
M 119
WL 2
R 50
M 120
mh 0
M 121
ad 66,1792
H 0
M 132
M 133
TX 58,4608
TX 62,18432
M 122
DG 1024
M 123
M 134
M 135
TY 66,5120
TY 70,18944
M 124
bc 48,9,64,56
M 125
mh 3
M 136
ad 64,1792
H 0
M 137
TX 74,6144
TX 78,19968
M 126
WL 4
Q 0
M 127
Q 2
M 138
Q 4
M 139
TY 82,6656
TY 86,20480
M 128
Q 6
M 129
WL 4
Q 8
M 140
Q 10
M 141
TX 90,7680
TX 94,21504
M 130
Q 12
M 131
R 52
M 142
Q 14
)"
R"(M 143
TY 98,8192
TY 102,22016
M 0
R 54
M 1
WL 10
Q 16
M 12
Q 18
M 13
Q 20
M 2
Q 22
M 3
WL 8
Q 24
M 14
Q 26
M 15
Q 28
M 4
Q 30
M 5
WL 6
Q 32
M 16
Q 34
M 17
Q 36
M 6
Q 38
M 7
WL 2
Q 40
M 18
Q 42
M 19
Q 44
M 8
Q 46
M 9
H 1
SA 212,1664
M 20
ae 44,68,768
M 21
s_bfe_u32 s51, s50, 0x1000f
v_mad_i32_i24 v47, v49, s51, v48
XL 22,0
M 10
H 2
XL 24,768
M 11
R 56
M 22
XL 26,1536
M 23
R 58
M 24
XL 28,2304
M 25
M 26
M 27
M 28
M 29
M 30
M 31
M 32
M 33
WV 10
M 34
WL 0
FP
M 35
M 36
M 37
M 38
M 39
M 40
M 41
M 42
M 43
M 44
UL 6,14
UH 7,14
M 45
FR
AR 8,44
M 46
AR 9,68
AD 8,6
M 47
AD 9,7
AW 44,8
M 48
AW 68,9
UL 6,15
M 49
UH 7,15
AR 8,92
M 50
AR 9,116
AD 8,6
M 51
AD 9,7
AW 92,8
M 52
AW 116,9
UL 6,16
M 53
WL 0
FQ 204
UH 7,16
M 54
AR 8,45
AR 9,69
M 55
AD 8,6
AD 9,7
M 56
FZ 204
R 72
M 57
mw 24
R 74
M 58
TY 106,9736
TY 110,23560
M 59
R 76
M 60
ad 84,816
W 0
M 61
AW 45,8
AW 69,9
M 62
UL 6,17
UH 7,17
M 63
W 1
AR 8,93
M 64
AR 9,117
AD 8,6
M 65
AD 9,7
AW 93,8
M 66
W 2
AW 117,9
M 67
WL 3
AN 104,106
AN 105,107
M 68
SH 106
SH 107
M 69
mw 27
AN 106,106
AN 107,107
M 70
WL 2
AN 108,110
AN 109,111
M 71
R 78
M 72
SH 110
SH 111
M 73
AN 110,110
AN 111,111
M 74
TY 114,11272
TY 118,25096
M 75
ad 84,822
W 0
M 76
UL 6,18
UH 7,18
M 77
AR 8,46
AR 9,70
M 78
W 1
AD 8,6
M 79
AD 9,7
AW 46,8
M 80
AW 70,9
UL 6,19
M 81
W 2
SA 214,2432
M 82
UH 7,19
AR 8,94
M 83
WL 1
R 80
M 84
mw 48
AN 112,114
AN 113,115
M 85
SH 114
SH 115
M 86
AN 114,114
AN 115,115
M 87
WL 2
AN 116,118
AN 117,119
M 88
SH 118
SH 119
M 89
AN 118,118
AN 119,119
M 90
TY 122,12808
TY 126,26632
M 91
ad 84,864
M 92
W 0
AR 9,118
M 93
AD 8,6
AD 9,7
M 94
AW 94,8
AW 118,9
M 95
R 82
M 96
W 1
UL 6,20
M 97
UH 7,20
AR 8,47
M 98
AR 9,71
AD 8,6
M 99
WL 3
AN 120,122
AN 121,123
M 100
SH 122
SH 123
M 101
AN 122,122
AN 123,123
M 102
WL 2
AN 124,126
AN 125,127
M 103
SH 126
SH 127
M 104
AN 126,126
AN 127,127
M 105
W 2
AD 9,7
M 106
AW 47,8
AW 71,9
M 107
R 0
M 108
mw 51
UL 6,21
UH 7,21
M 109
ad 84,870
W 0
M 110
AR 8,95
AR 9,119
M 111
AD 8,6
AD 9,7
M 112
W 1
AW 95,8
M 113
AW 119,9
M 114
M 115
W 2
M 116
M 117
M 118
bc 49,10,66,60
M 119
WL 2
R 2
M 120
mh 9
M 121
ad 66,1984
H 0
M 132
M 133
TX 58,9216
TX 62,23040
M 122
DG 1024
M 123
M 134
M 135
TY 66,9728
TY 70,23552
M 124
bc 48,10,64,56
M 125
mh 12
M 136
ad 64,1984
H 0
M 137
TX 74,10752
TX 78,24576
M 126
WL 4
Q 0
M 127
Q 2
M 138
Q 4
M 139
TY 82,11264
TY 86,25088
M 128
Q 6
M 129
WL 4
Q 8
M 140
Q 10
M 141
TX 90,12288
TX 94,26112
M 130
Q 12
M 131
R 4
M 142
Q 14
M 143
TY 98,12800
TY 102,26624
M 0
R 6
M 1
WL 10
Q 16
M 12
Q 18
M 13
Q 20
M 2
Q 22
M 3
WL 8
Q 24
M 14
Q 26
M 15
Q 28
M 4
Q 30
M 5
WL 6
Q 32
M 16
Q 34
M 17
Q 36
M 6
Q 38
M 7
WL 2
Q 40
M 18
Q 42
M 19
Q 44
M 8
Q 46
M 9
)"
R"(H 1
M 20
ae 42,72,384
M 21
s_bitcmp1_b32 s50, 8
s_cselect_b32 s52, 24576, 0
v_add_u32 v50, s52, v3
M 10
H 2
SA 216,256
M 11
R 8
M 22
ae 44,68,864
M 23
R 10
PA 12288
DF
M 24
s_bitcmp1_b32 s50, 16
s_cselect_b32 s51, 24576, 0
v_add_u32 v47, s51, v3
M 25
XL 30,0
M 26
XL 32,768
M 27
XL 34,1536
M 28
XL 36,2304
M 29
M 30
M 31
M 32
M 33
WV 11
M 34
WL 0
FP
M 35
M 36
M 37
M 38
M 39
M 40
M 41
M 42
M 43
M 44
M 45
FR
M 46
UL 6,22
UH 7,22
M 47
AR 8,48
AR 9,72
M 48
AD 8,6
AD 9,7
M 49
AW 48,8
AW 72,9
M 50
UL 6,23
UH 7,23
M 51
AR 8,96
AR 9,120
M 52
AD 8,6
AD 9,7
M 53
WL 0
FQ 208
AW 96,8
M 54
AW 120,9
UL 6,24
M 55
UH 7,24
AR 8,49
M 56
FZ 208
R 24
M 57
mw 72
R 26
M 58
TY 106,520
TY 110,14344
M 59
R 28
M 60
ad 84,912
W 0
M 61
AR 9,73
AD 8,6
M 62
AD 9,7
AW 49,8
M 63
W 1
AW 73,9
M 64
UL 6,25
UH 7,25
M 65
AR 8,97
AR 9,121
M 66
W 2
AD 8,6
M 67
WL 3
AN 104,106
AN 105,107
M 68
SH 106
SH 107
M 69
mw 75
AN 106,106
AN 107,107
M 70
WL 2
AN 108,110
AN 109,111
M 71
R 30
M 72
SH 110
SH 111
M 73
AN 110,110
AN 111,111
M 74
TY 114,2056
TY 118,15880
M 75
ad 84,918
W 0
M 76
AD 9,7
AW 97,8
M 77
AW 121,9
UL 6,26
M 78
W 1
UH 7,26
M 79
AR 8,50
AR 9,74
M 80
AD 8,6
AD 9,7
M 81
W 2
AW 50,8
M 82
AW 74,9
UL 6,27
M 83
WL 1
R 32
M 84
mw 0
AN 112,114
AN 113,115
M 85
SH 114
SH 115
M 86
AN 114,114
AN 115,115
M 87
WL 2
AN 116,118
AN 117,119
M 88
SH 118
SH 119
M 89
AN 118,118
AN 119,119
M 90
TY 122,3592
TY 126,17416
M 91
ad 84,960
M 92
W 0
SA 218,1024
M 93
UH 7,27
AR 8,98
M 94
AR 9,122
AD 8,6
M 95
R 34
M 96
W 1
AD 9,7
M 97
AW 98,8
AW 122,9
M 98
UL 6,28
UH 7,28
M 99
WL 3
AN 120,122
AN 121,123
M 100
SH 122
SH 123
M 101
AN 122,122
AN 123,123
M 102
WL 2
AN 124,126
AN 125,127
M 103
SH 126
SH 127
M 104
AN 126,126
AN 127,127
M 105
W 2
AR 8,51
M 106
AR 9,75
AD 8,6
M 107
R 48
M 108
mw 3
AD 9,7
AW 51,8
M 109
ad 84,966
W 0
M 110
AW 75,9
UL 6,29
M 111
UH 7,29
AR 8,99
M 112
W 1
AR 9,123
M 113
AD 8,6
AD 9,7
M 114
AW 99,8
AW 123,9
M 115
W 2
M 116
M 117
M 118
bc 49,11,66,60
M 119
WL 2
R 50
M 120
mh 18
M 121
ad 66,2176
H 0
M 132
M 133
TX 58,0
TX 62,13824
M 122
DG 1024
M 123
M 134
M 135
TY 66,512
TY 70,14336
M 124
bc 48,11,64,56
M 125
mh 21
M 136
ad 64,2176
H 0
M 137
TX 74,1536
TX 78,15360
M 126
WL 4
Q 0
M 127
Q 2
M 138
Q 4
M 139
TY 82,2048
TY 86,15872
M 128
Q 6
M 129
WL 4
Q 8
M 140
Q 10
M 141
TX 90,3072
TX 94,16896
M 130
Q 12
M 131
R 52
M 142
Q 14
M 143
TY 98,3584
TY 102,17408
M 0
R 54
M 1
WL 10
Q 16
M 12
Q 18
M 13
Q 20
M 2
Q 22
M 3
WL 8
Q 24
M 14
Q 26
M 15
Q 28
M 4
Q 30
M 5
WL 6
Q 32
M 16
Q 34
M 17
Q 36
M 6
Q 38
M 7
WL 2
Q 40
M 18
Q 42
M 19
Q 44
M 8
Q 46
M 9
H 1
SA 220,1792
M 20
ae 44,68,864
M 21
s_bfe_u32 s51, s50, 0x10011
v_mad_i32_i24 v47, v49, s51, v48
XL 38,0
M 10
H 2
XL 40,768
M 11
R 56
M 22
XL 42,1536
M 23
R 58
M 24
XL 44,2304
M 25
M 26
M 27
M 28
M 29
M 30
M 31
M 32
M 33
WV 10
M 34
WL 0
FP
M 35
M 36
M 37
)"
R"(M 38
M 39
M 40
M 41
M 42
M 43
M 44
M 45
FR
M 46
M 47
M 48
UL 6,30
UH 7,30
M 49
AR 8,52
AR 9,76
M 50
AD 8,6
AD 9,7
M 51
AW 52,8
AW 76,9
M 52
UL 6,31
UH 7,31
M 53
WL 0
FQ 212
AR 8,100
M 54
AR 9,124
AD 8,6
M 55
AD 9,7
AW 100,8
M 56
FZ 212
R 72
M 57
mw 24
R 74
M 58
TY 106,5128
TY 110,18952
M 59
R 76
M 60
ad 84,1008
W 0
M 61
AW 124,9
UL 6,32
M 62
UH 7,32
AR 8,53
M 63
W 1
AR 9,77
M 64
AD 8,6
AD 9,7
M 65
AW 53,8
AW 77,9
M 66
W 2
UL 6,33
M 67
WL 3
AN 104,106
AN 105,107
M 68
SH 106
SH 107
M 69
mw 27
AN 106,106
AN 107,107
M 70
WL 2
AN 108,110
AN 109,111
M 71
R 78
M 72
SH 110
SH 111
M 73
AN 110,110
AN 111,111
M 74
TY 114,6664
TY 118,20488
M 75
ad 84,1014
W 0
M 76
UH 7,33
AR 8,101
M 77
AR 9,125
AD 8,6
M 78
W 1
AD 9,7
M 79
AW 101,8
AW 125,9
M 80
UL 6,34
UH 7,34
M 81
W 2
SA 222,2560
M 82
AR 8,54
AR 9,78
M 83
WL 1
R 80
M 84
mw 48
AN 112,114
AN 113,115
M 85
SH 114
SH 115
M 86
AN 114,114
AN 115,115
M 87
WL 2
AN 116,118
AN 117,119
M 88
SH 118
SH 119
M 89
AN 118,118
AN 119,119
M 90
TY 122,8200
TY 126,22024
M 91
ad 84,1056
M 92
W 0
AD 8,6
M 93
AD 9,7
AW 54,8
M 94
AW 78,9
UL 6,35
M 95
R 82
M 96
W 1
UH 7,35
M 97
AR 8,102
AR 9,126
M 98
AD 8,6
AD 9,7
M 99
WL 3
AN 120,122
AN 121,123
M 100
SH 122
SH 123
M 101
AN 122,122
AN 123,123
M 102
WL 2
AN 124,126
AN 125,127
M 103
SH 126
SH 127
M 104
AN 126,126
AN 127,127
M 105
W 2
AW 102,8
M 106
AW 126,9
UL 6,36
M 107
R 0
M 108
mw 51
UH 7,36
AR 8,55
M 109
ad 84,1062
W 0
M 110
AR 9,79
AD 8,6
M 111
AD 9,7
AW 55,8
M 112
W 1
AW 79,9
M 113
UL 6,37
UH 7,37
M 114
AR 8,103
AR 9,127
M 115
W 2
AD 8,6
M 116
AD 9,7
AW 103,8
M 117
AW 127,9
M 118
bc 49,12,66,60
M 119
WL 1
R 2
M 120
mh 0
M 121
ad 66,2368
H 0
M 132
M 133
TX 58,4608
TX 62,18432
M 122
DG 1024
M 123
M 134
M 135
TY 66,5120
TY 70,18944
M 124
bc 48,12,64,56
M 125
mh 3
M 136
ad 64,2368
H 0
M 137
TX 74,6144
TX 78,19968
M 126
WL 4
Q 0
M 127
Q 2
M 138
Q 4
M 139
TY 82,6656
TY 86,20480
M 128
Q 6
M 129
WL 4
Q 8
M 140
Q 10
M 141
TX 90,7680
TX 94,21504
M 130
Q 12
M 131
R 4
M 142
Q 14
M 143
TY 98,8192
TY 102,22016
M 0
R 6
M 1
WL 10
Q 16
M 12
Q 18
M 13
Q 20
M 2
Q 22
M 3
WL 8
Q 24
M 14
Q 26
M 15
Q 28
M 4
Q 30
M 5
WL 6
Q 32
M 16
Q 34
M 17
Q 36
M 6
Q 38
M 7
WL 2
Q 40
M 18
Q 42
M 19
Q 44
M 8
Q 46
M 9
H 1
M 20
ae 42,72,480
M 21
s_bitcmp1_b32 s50, 10
s_cselect_b32 s52, 24576, 0
v_add_u32 v50, s52, v3
M 10
H 2
SA 224,0
M 11
R 8
M 22
M 23
R 10
PA 12288
DF
M 24
M 25
M 26
M 27
M 28
M 29
M 30
M 31
M 32
ae 44,70,768
M 33
WV 7
M 34
WL 0
FP
M 35
M 36
s_bitcmp1_b32 s50, 12
s_cselect_b32 s51, 24576, 0
v_add_u32 v47, s51, v3
M 37
XL 14,128
M 38
XL 16,896
M 39
M 40
M 41
M 42
M 43
M 44
XL 18,1664
M 45
FR
M 46
M 47
M 48
M 49
M 50
XL 20,2432
UL 6,38
M 51
UH 7,38
AR 8,56
M 52
AR 9,80
AD 8,6
M 53
WL 0
FQ 216
AD 9,7
M 54
AW 56,8
AW 80,9
M 55
UL 6,39
UH 7,39
M 56
FZ 216
R 24
M 57
)"
R"(mw 72
R 26
M 58
TY 106,9736
TY 110,23560
M 59
R 28
M 60
ad 84,1104
W 0
M 61
AR 8,104
AR 9,128
M 62
AD 8,6
AD 9,7
M 63
W 1
AW 104,8
M 64
AW 128,9
UL 6,40
M 65
UH 7,40
AR 8,57
M 66
W 2
AR 9,81
M 67
WL 3
AN 104,106
AN 105,107
M 68
SH 106
SH 107
M 69
mw 75
AN 106,106
AN 107,107
M 70
WL 2
AN 108,110
AN 109,111
M 71
R 30
M 72
SH 110
SH 111
M 73
AN 110,110
AN 111,111
M 74
TY 114,11272
TY 118,25096
M 75
ad 84,1110
W 0
M 76
AD 8,6
AD 9,7
M 77
AW 57,8
AW 81,9
M 78
W 1
UL 6,41
M 79
UH 7,41
AR 8,105
M 80
AR 9,129
AD 8,6
M 81
W 2
AD 9,7
M 82
AW 105,8
AW 129,9
M 83
WL 1
R 32
M 84
mw 0
AN 112,114
AN 113,115
M 85
SH 114
SH 115
M 86
AN 114,114
AN 115,115
M 87
WL 2
AN 116,118
AN 117,119
M 88
SH 118
SH 119
M 89
AN 118,118
AN 119,119
M 90
TY 122,12808
TY 126,26632
M 91
ad 84,1152
M 92
W 0
SA 226,768
M 93
UL 6,42
UH 7,42
M 94
AR 8,58
AR 9,82
M 95
R 34
M 96
W 1
AD 8,6
M 97
AD 9,7
AW 58,8
M 98
AW 82,9
UL 6,43
M 99
WL 3
AN 120,122
AN 121,123
M 100
SH 122
SH 123
M 101
AN 122,122
AN 123,123
M 102
WL 2
AN 124,126
AN 125,127
M 103
SH 126
SH 127
M 104
AN 126,126
AN 127,127
M 105
W 2
UH 7,43
M 106
AR 8,106
AR 9,130
M 107
R 48
M 108
mw 3
AD 8,6
AD 9,7
M 109
ad 84,1158
W 0
M 110
AW 106,8
AW 130,9
M 111
UL 6,44
UH 7,44
M 112
W 1
AR 8,59
M 113
AR 9,83
AD 8,6
M 114
AD 9,7
AW 59,8
M 115
W 2
AW 83,9
M 116
UL 6,45
UH 7,45
M 117
AR 8,107
AR 9,131
M 118
bc 49,13,66,60
AD 8,6
M 119
WL 0
R 50
M 120
mh 9
AD 9,7
AW 107,8
M 121
ad 66,2560
H 0
M 132
AW 131,9
M 133
TX 58,9216
TX 62,23040
M 122
DG 1024
M 123
M 134
M 135
TY 66,9728
TY 70,23552
M 124
bc 48,13,64,56
M 125
mh 12
M 136
ad 64,2560
H 0
M 137
TX 74,10752
TX 78,24576
M 126
WL 4
Q 0
M 127
Q 2
M 138
Q 4
M 139
TY 82,11264
TY 86,25088
M 128
Q 6
M 129
WL 4
Q 8
M 140
Q 10
M 141
TX 90,12288
TX 94,26112
M 130
Q 12
M 131
R 52
M 142
Q 14
M 143
TY 98,12800
TY 102,26624
M 0
R 54
M 1
WL 10
Q 16
M 12
Q 18
M 13
Q 20
M 2
Q 22
M 3
WL 8
Q 24
M 14
Q 26
M 15
Q 28
M 4
Q 30
M 5
WL 6
Q 32
M 16
Q 34
M 17
Q 36
M 6
Q 38
M 7
WL 2
Q 40
M 18
Q 42
M 19
Q 44
M 8
Q 46
M 9
H 1
SA 228,1536
M 20
M 21
M 10
H 2
M 11
R 56
M 22
M 23
R 58
M 24
M 25
M 26
M 27
M 28
M 29
M 30
M 31
M 32
M 33
WV 6
M 34
WL 0
FP
M 35
M 36
ae 44,70,768
M 37
s_bfe_u32 s51, s50, 0x1000d
v_mad_i32_i24 v47, v49, s51, v48
XL 22,128
M 38
M 39
M 40
XL 24,896
M 41
M 42
M 43
M 44
M 45
FR
M 46
XL 26,1664
M 47
M 48
M 49
M 50
M 51
M 52
XL 28,2432
M 53
WL 0
FQ 220
M 54
M 55
M 56
FZ 220
R 72
M 57
mw 24
R 74
M 58
TY 106,520
TY 110,14344
M 59
R 76
M 60
ad 84,1200
W 0
M 61
M 62
M 63
W 1
M 64
M 65
M 66
W 2
M 67
WL 3
AN 104,106
AN 105,107
M 68
SH 106
SH 107
M 69
mw 27
AN 106,106
AN 107,107
M 70
WL 2
AN 108,110
AN 109,111
M 71
R 78
M 72
SH 110
SH 111
M 73
AN 110,110
AN 111,111
M 74
TY 114,2056
TY 118,15880
M 75
ad 84,1206
W 0
M 76
M 77
M 78
W 1
M 79
)"
R"(M 80
M 81
W 2
SA 230,2304
M 82
M 83
R 80
M 84
mw 48
WL 3
AN 112,114
AN 113,115
M 85
SH 114
SH 115
M 86
AN 114,114
AN 115,115
M 87
WL 2
AN 116,118
AN 117,119
M 88
SH 118
SH 119
M 89
AN 118,118
AN 119,119
M 90
TY 122,3592
TY 126,17416
M 91
ad 84,1248
M 92
W 0
UL 6,14
M 93
UH 7,14
AR 8,132
M 94
AR 9,156
AD 8,6
M 95
R 82
M 96
W 1
AD 9,7
M 97
AW 132,8
AW 156,9
M 98
UL 6,15
UH 7,15
M 99
WL 3
AN 120,122
AN 121,123
M 100
SH 122
SH 123
M 101
AN 122,122
AN 123,123
M 102
WL 2
AN 124,126
AN 125,127
M 103
SH 126
SH 127
M 104
AN 126,126
AN 127,127
M 105
W 2
AR 8,180
M 106
AD 8,6
AD 136,7
M 107
R 0
M 108
mw 51
AW 180,8
UL 6,16
M 109
ad 84,1254
W 0
M 110
UH 7,16
AR 8,133
M 111
AR 9,157
AD 8,6
M 112
W 1
AD 9,7
M 113
AW 133,8
AW 157,9
M 114
UL 6,17
UH 7,17
M 115
W 2
AR 8,181
M 116
AD 8,6
AD 137,7
M 117
AW 181,8
UL 6,18
M 118
bc 49,14,66,60
UH 7,18
M 119
WL 0
R 2
M 120
mh 18
AR 8,134
AR 9,158
M 121
ad 66,2752
H 0
M 132
AD 8,6
AD 9,7
M 133
TX 58,0
TX 62,13824
M 122
DG 1024
M 123
AW 134,8
AW 158,9
M 134
UL 6,19
UH 7,19
M 135
TY 66,512
TY 70,14336
M 124
bc 48,14,64,56
AR 8,182
M 125
mh 21
AD 8,6
AD 138,7
M 136
ad 64,2752
H 0
M 137
TX 74,1536
TX 78,15360
M 126
WL 4
Q 0
M 127
Q 2
M 138
Q 4
M 139
TY 82,2048
TY 86,15872
M 128
Q 6
M 129
WL 4
Q 8
M 140
Q 10
M 141
TX 90,3072
TX 94,16896
M 130
Q 12
M 131
R 4
M 142
Q 14
M 143
TY 98,3584
TY 102,17408
M 0
R 6
M 1
WL 10
Q 16
M 12
Q 18
M 13
Q 20
M 2
Q 22
M 3
WL 8
Q 24
M 14
Q 26
M 15
Q 28
M 4
Q 30
M 5
WL 6
Q 32
M 16
Q 34
M 17
Q 36
M 6
Q 38
M 7
WL 2
Q 40
M 18
Q 42
M 19
Q 44
M 8
Q 46
M 9
H 1
AW 182,8
M 20
ae 42,72,480
M 21
SA 232,128
UL 6,20
M 10
H 2
UH 7,20
M 11
R 8
M 22
AR 8,135
AR 9,159
M 23
R 10
PA 12288
DF
M 24
AD 8,6
AD 9,7
M 25
AW 135,8
AW 159,9
M 26
UL 6,21
UH 7,21
M 27
AR 8,183
AD 8,6
M 28
AD 139,7
AW 183,8
M 29
M 30
M 31
M 32
M 33
WV 7
M 34
WL 0
FP
M 35
M 36
ae 44,68,768
M 37
s_bitcmp1_b32 s50, 14
s_cselect_b32 s51, 24576, 0
v_add_u32 v47, s51, v3
M 38
XL 30,128
M 39
M 40
M 41
M 42
XL 32,896
M 43
M 44
M 45
FR
M 46
M 47
M 48
XL 34,1664
M 49
M 50
M 51
M 52
M 53
WL 0
FQ 224
M 54
XL 36,2432
M 55
M 56
FZ 224
R 24
M 57
mw 72
R 26
M 58
TY 106,5128
TY 110,18952
M 59
R 28
M 60
ad 84,1296
W 0
M 61
M 62
M 63
W 1
M 64
M 65
M 66
W 2
M 67
WL 3
AN 104,106
AN 105,107
M 68
SH 106
SH 107
M 69
mw 75
AN 106,106
AN 107,107
M 70
WL 2
AN 108,110
AN 109,111
M 71
R 30
M 72
SH 110
SH 111
M 73
AN 110,110
AN 111,111
M 74
TY 114,6664
TY 118,20488
M 75
ad 84,1302
W 0
M 76
M 77
M 78
W 1
M 79
M 80
M 81
W 2
M 82
M 83
R 32
M 84
mw 0
WL 3
AN 112,114
AN 113,115
M 85
SH 114
SH 115
M 86
AN 114,114
AN 115,115
M 87
WL 2
AN 116,118
AN 117,119
M 88
SH 118
SH 119
M 89
AN 118,118
AN 119,119
M 90
TY 122,8200
TY 126,22024
M 91
ad 84,1344
M 92
W 0
SA 234,896
M 93
UL 6,22
UH 7,22
M 94
AR 8,136
AR 9,160
M 95
R 34
M 96
W 1
AD 8,6
)"
R"(M 97
AD 9,7
AW 136,8
M 98
AW 160,9
UL 6,23
M 99
WL 3
AN 120,122
AN 121,123
M 100
SH 122
SH 123
M 101
AN 122,122
AN 123,123
M 102
WL 2
AN 124,126
AN 125,127
M 103
SH 126
SH 127
M 104
AN 126,126
AN 127,127
M 105
W 2
UH 7,23
M 106
AR 8,184
AD 8,6
M 107
R 48
M 108
mw 3
AD 140,7
AW 184,8
M 109
ad 84,1350
W 0
M 110
UL 6,24
UH 7,24
M 111
AR 8,137
AR 9,161
M 112
W 1
AD 8,6
M 113
AD 9,7
AW 137,8
M 114
AW 161,9
UL 6,25
M 115
W 2
UH 7,25
M 116
AR 8,185
AD 8,6
M 117
AD 141,7
AW 185,8
M 118
bc 49,15,66,60
UL 6,26
M 119
WL 0
R 50
M 120
mh 0
UH 7,26
AR 8,138
M 121
ad 66,2944
H 0
M 132
AR 9,162
AD 8,6
M 133
TX 58,4608
TX 62,18432
M 122
DG 1024
M 123
AD 9,7
AW 138,8
M 134
AW 162,9
UL 6,27
M 135
TY 66,5120
TY 70,18944
M 124
bc 48,15,64,56
UH 7,27
M 125
mh 3
AR 8,186
AD 8,6
M 136
ad 64,2944
H 0
M 137
TX 74,6144
TX 78,19968
M 126
WL 4
Q 0
M 127
Q 2
M 138
Q 4
M 139
TY 82,6656
TY 86,20480
M 128
Q 6
M 129
WL 4
Q 8
M 140
Q 10
M 141
TX 90,7680
TX 94,21504
M 130
Q 12
M 131
R 52
M 142
Q 14
M 143
TY 98,8192
TY 102,22016
M 0
R 54
M 1
WL 10
Q 16
M 12
Q 18
M 13
Q 20
M 2
Q 22
M 3
WL 8
Q 24
M 14
Q 26
M 15
Q 28
M 4
Q 30
M 5
WL 6
Q 32
M 16
Q 34
M 17
Q 36
M 6
Q 38
M 7
WL 2
Q 40
M 18
Q 42
M 19
Q 44
M 8
Q 46
M 9
H 1
SA 236,1664
M 20
AD 142,7
AW 186,8
M 21
UL 6,28
UH 7,28
M 10
H 2
AR 8,139
M 11
R 56
M 22
AR 9,163
AD 8,6
M 23
R 58
M 24
AD 9,7
AW 139,8
M 25
AW 163,9
UL 6,29
M 26
UH 7,29
AR 8,187
M 27
AD 8,6
AD 143,7
M 28
AW 187,8
M 29
M 30
M 31
M 32
M 33
WV 6
M 34
WL 0
FP
M 35
M 36
M 37
M 38
ae 44,68,768
M 39
s_bfe_u32 s51, s50, 0x1000f
v_mad_i32_i24 v47, v49, s51, v48
XL 38,128
M 40
M 41
M 42
M 43
M 44
XL 40,896
M 45
FR
M 46
M 47
M 48
M 49
M 50
XL 42,1664
M 51
M 52
M 53
WL 0
FQ 228
M 54
M 55
M 56
FZ 228
R 72
M 57
mw 24
R 74
M 58
TY 106,9736
TY 110,23560
M 59
R 76
M 60
ad 84,1392
W 0
M 61
XL 44,2432
M 62
M 63
W 1
M 64
M 65
M 66
W 2
M 67
WL 3
AN 104,106
AN 105,107
M 68
SH 106
SH 107
M 69
mw 27
AN 106,106
AN 107,107
M 70
WL 2
AN 108,110
AN 109,111
M 71
R 78
M 72
SH 110
SH 111
M 73
AN 110,110
AN 111,111
M 74
TY 114,11272
TY 118,25096
M 75
ad 84,1398
W 0
M 76
M 77
M 78
W 1
M 79
M 80
M 81
W 2
SA 238,2432
M 82
M 83
R 80
M 84
mw 48
WL 3
AN 112,114
AN 113,115
M 85
SH 114
SH 115
M 86
AN 114,114
AN 115,115
M 87
WL 2
AN 116,118
AN 117,119
M 88
SH 118
SH 119
M 89
AN 118,118
AN 119,119
M 90
TY 122,12808
TY 126,26632
M 91
ad 84,1440
M 92
W 0
UL 6,30
M 93
UH 7,30
AR 8,140
M 94
AR 9,164
AD 8,6
M 95
R 82
M 96
W 1
AD 9,7
M 97
AW 140,8
AW 164,9
M 98
UL 6,31
UH 7,31
M 99
WL 3
AN 120,122
AN 121,123
M 100
SH 122
SH 123
M 101
AN 122,122
AN 123,123
M 102
WL 2
AN 124,126
AN 125,127
M 103
SH 126
SH 127
M 104
AN 126,126
AN 127,127
M 105
W 2
AR 8,188
M 106
AD 8,6
AD 144,7
M 107
R 0
M 108
mw 51
AW 188,8
UL 6,32
M 109
ad 84,1446
W 0
M 110
UH 7,32
AR 8,141
M 111
AR 9,165
AD 8,6
M 112
W 1
)"
R"(AD 9,7
M 113
AW 141,8
AW 165,9
M 114
UL 6,33
UH 7,33
M 115
W 2
AR 8,189
M 116
AD 8,6
AD 145,7
M 117
AW 189,8
UL 6,34
M 118
bc 49,16,66,60
UH 7,34
M 119
WL 0
R 2
M 120
mh 9
AR 8,142
AR 9,166
M 121
ad 66,3136
H 0
M 132
AD 8,6
AD 9,7
M 133
TX 58,9216
TX 62,23040
M 122
DG 1024
M 123
AW 142,8
AW 166,9
M 134
UL 6,35
UH 7,35
M 135
TY 66,9728
TY 70,23552
M 124
bc 48,16,64,56
AR 8,190
M 125
mh 12
AD 8,6
AD 146,7
M 136
ad 64,3136
H 0
M 137
TX 74,10752
TX 78,24576
M 126
WL 4
Q 0
M 127
Q 2
M 138
Q 4
M 139
TY 82,11264
TY 86,25088
M 128
Q 6
M 129
WL 4
Q 8
M 140
Q 10
M 141
TX 90,12288
TX 94,26112
M 130
Q 12
M 131
R 4
M 142
Q 14
M 143
TY 98,12800
TY 102,26624
M 0
R 6
M 1
WL 10
Q 16
M 12
Q 18
M 13
Q 20
M 2
Q 22
M 3
WL 8
Q 24
M 14
Q 26
M 15
Q 28
M 4
Q 30
M 5
WL 6
Q 32
M 16
Q 34
M 17
Q 36
M 6
Q 38
M 7
WL 2
Q 40
M 18
Q 42
M 19
Q 44
M 8
Q 46
M 9
H 1
AW 190,8
M 20
ae 42,72,480
M 21
SA 240,256
UL 6,36
M 10
H 2
UH 7,36
M 11
R 8
M 22
AR 8,143
AR 9,167
M 23
R 10
PA 12288
DF
M 24
AD 8,6
AD 9,7
M 25
AW 143,8
AW 167,9
M 26
UL 6,37
UH 7,37
M 27
AR 8,191
AD 8,6
M 28
AD 147,7
AW 191,8
M 29
M 30
M 31
M 32
M 33
WV 7
M 34
WL 0
FP
M 35
M 36
M 37
M 38
M 39
M 40
ae 44,68,864
M 41
s_bitcmp1_b32 s50, 16
s_cselect_b32 s51, 24576, 0
v_add_u32 v47, s51, v3
M 42
XL 14,128
M 43
M 44
M 45
FR
M 46
XL 16,896
M 47
M 48
M 49
M 50
M 51
M 52
XL 18,1664
M 53
WL 0
FQ 232
M 54
M 55
M 56
FZ 232
R 24
M 57
mw 72
R 26
M 58
TY 106,520
TY 110,14344
M 59
R 28
M 60
ad 84,1488
W 0
M 61
XL 20,2432
M 62
M 63
W 1
M 64
M 65
M 66
W 2
M 67
WL 3
AN 104,106
AN 105,107
M 68
SH 106
SH 107
M 69
mw 75
AN 106,106
AN 107,107
M 70
WL 2
AN 108,110
AN 109,111
M 71
R 30
M 72
SH 110
SH 111
M 73
AN 110,110
AN 111,111
M 74
TY 114,2056
TY 118,15880
M 75
ad 84,1494
W 0
M 76
M 77
M 78
W 1
M 79
M 80
M 81
W 2
M 82
M 83
R 32
M 84
mw 0
WL 3
AN 112,114
AN 113,115
M 85
SH 114
SH 115
M 86
AN 114,114
AN 115,115
M 87
WL 2
AN 116,118
AN 117,119
M 88
SH 118
SH 119
M 89
AN 118,118
AN 119,119
M 90
TY 122,3592
TY 126,17416
M 91
ad 84,1536
M 92
W 0
SA 242,1024
M 93
M 94
UL 6,38
UH 7,38
M 95
R 34
M 96
W 1
AR 8,144
M 97
AR 9,168
AD 8,6
M 98
AD 9,7
AW 144,8
M 99
WL 3
AN 120,122
AN 121,123
M 100
SH 122
SH 123
M 101
AN 122,122
AN 123,123
M 102
WL 2
AN 124,126
AN 125,127
M 103
SH 126
SH 127
M 104
AN 126,126
AN 127,127
M 105
W 2
AW 168,9
M 106
UL 6,39
UH 7,39
M 107
R 48
M 108
mw 3
AR 8,192
AD 8,6
M 109
ad 84,1542
W 0
M 110
AD 148,7
AW 192,8
M 111
UL 6,40
UH 7,40
M 112
W 1
AR 8,145
M 113
AR 9,169
AD 8,6
M 114
AD 9,7
AW 145,8
M 115
W 2
AW 169,9
M 116
UL 6,41
UH 7,41
M 117
AR 8,193
AD 8,6
M 118
bc 49,17,66,60
AD 149,7
M 119
WL 0
R 50
M 120
mh 18
AW 193,8
UL 6,42
M 121
ad 66,3328
H 0
M 132
UH 7,42
AR 8,146
M 133
TX 58,0
TX 62,13824
M 122
DG 1024
M 123
AR 9,170
AD 8,6
M 134
AD 9,7
AW 146,8
M 135
TY 66,512
TY 70,14336
M 124
bc 48,17,64,56
AW 170,9
)"
R"(M 125
mh 21
UL 6,43
UH 7,43
M 136
ad 64,3328
H 0
M 137
TX 74,1536
TX 78,15360
M 126
WL 4
Q 0
M 127
Q 2
M 138
Q 4
M 139
TY 82,2048
TY 86,15872
M 128
Q 6
M 129
WL 4
Q 8
M 140
Q 10
M 141
TX 90,3072
TX 94,16896
M 130
Q 12
M 131
R 52
M 142
Q 14
M 143
TY 98,3584
TY 102,17408
M 0
R 54
M 1
WL 10
Q 16
M 12
Q 18
M 13
Q 20
M 2
Q 22
M 3
WL 8
Q 24
M 14
Q 26
M 15
Q 28
M 4
Q 30
M 5
WL 6
Q 32
M 16
Q 34
M 17
Q 36
M 6
Q 38
M 7
WL 2
Q 40
M 18
Q 42
M 19
Q 44
M 8
Q 46
M 9
H 1
SA 244,1792
M 20
AR 8,194
AD 8,6
M 21
AD 150,7
AW 194,8
M 10
H 2
UL 6,44
M 11
R 56
M 22
UH 7,44
AR 8,147
M 23
R 58
M 24
AR 9,171
AD 8,6
M 25
AD 9,7
AW 147,8
M 26
AW 171,9
UL 6,45
M 27
UH 7,45
AR 8,195
M 28
AD 8,6
AD 151,7
M 29
AW 195,8
M 30
M 31
M 32
M 33
WV 6
M 34
WL 0
FP
M 35
M 36
M 37
M 38
M 39
M 40
M 41
M 42
ae 44,68,864
M 43
s_bfe_u32 s51, s50, 0x10011
v_mad_i32_i24 v47, v49, s51, v48
XL 22,128
M 44
M 45
FR
M 46
M 47
M 48
XL 24,896
M 49
M 50
M 51
M 52
M 53
WL 0
FQ 236
M 54
XL 26,1664
M 55
M 56
FZ 236
R 72
M 57
mw 24
R 74
M 58
TY 106,5128
TY 110,18952
M 59
R 76
M 60
ad 84,1584
W 0
M 61
XL 28,2432
M 62
M 63
W 1
M 64
M 65
M 66
W 2
M 67
WL 3
AN 104,106
AN 105,107
M 68
SH 106
SH 107
M 69
mw 27
AN 106,106
AN 107,107
M 70
WL 2
AN 108,110
AN 109,111
M 71
R 78
M 72
SH 110
SH 111
M 73
AN 110,110
AN 111,111
M 74
TY 114,6664
TY 118,20488
M 75
ad 84,1590
W 0
M 76
M 77
M 78
W 1
M 79
M 80
M 81
W 2
SA 246,2560
M 82
M 83
R 80
M 84
mw 48
WL 3
AN 112,114
AN 113,115
M 85
SH 114
SH 115
M 86
AN 114,114
AN 115,115
M 87
WL 2
AN 116,118
AN 117,119
M 88
SH 118
SH 119
M 89
AN 118,118
AN 119,119
M 90
TY 122,8200
TY 126,22024
M 91
ad 84,1632
M 92
W 0
M 93
M 94
M 95
R 82
M 96
W 1
UL 6,14
M 97
UH 7,14
AR 8,148
M 98
AR 9,172
AD 8,6
M 99
WL 3
AN 120,122
AN 121,123
M 100
SH 122
SH 123
M 101
AN 122,122
AN 123,123
M 102
WL 2
AN 124,126
AN 125,127
M 103
SH 126
SH 127
M 104
AN 126,126
AN 127,127
M 105
W 2
AD 9,7
M 106
AW 148,8
AW 172,9
M 107
R 0
M 108
mw 51
UL 6,15
UH 7,15
M 109
ad 84,1638
W 0
M 110
AR 8,196
AD 8,6
M 111
AD 152,7
AW 196,8
M 112
W 1
UL 6,16
M 113
UH 7,16
AR 8,149
M 114
AR 9,173
AD 8,6
M 115
W 2
AD 9,7
M 116
AW 149,8
AW 173,9
M 117
UL 6,17
UH 7,17
M 118
bc 49,18,66,60
AR 8,197
M 119
WL 0
R 2
M 120
mh 0
AD 8,6
AD 153,7
M 121
ad 66,3520
H 0
M 132
AW 197,8
UL 6,18
M 133
TX 58,4608
TX 62,18432
M 122
DG 1024
M 123
UH 7,18
AR 8,150
M 134
AR 9,174
AD 8,6
M 135
TY 66,5120
TY 70,18944
M 124
bc 48,18,64,56
AD 9,7
M 125
mh 3
AW 150,8
AW 174,9
M 136
ad 64,3520
H 0
M 137
TX 74,6144
TX 78,19968
M 126
WL 4
Q 0
M 127
Q 2
M 138
Q 4
M 139
TY 82,6656
TY 86,20480
M 128
Q 6
M 129
WL 4
Q 8
M 140
Q 10
M 141
TX 90,7680
TX 94,21504
M 130
Q 12
M 131
R 4
M 142
Q 14
M 143
TY 98,8192
TY 102,22016
M 0
R 6
M 1
WL 10
Q 16
M 12
Q 18
M 13
Q 20
M 2
Q 22
M 3
WL 8
)"
R"(Q 24
M 14
Q 26
M 15
Q 28
M 4
Q 30
M 5
WL 6
Q 32
M 16
Q 34
M 17
Q 36
M 6
Q 38
M 7
WL 2
Q 40
M 18
Q 42
M 19
Q 44
M 8
Q 46
M 9
H 1
UL 6,19
M 20
ae 42,72,480
M 21
s_bfe_u32 s52, s50, 0x1000b
v_mad_i32_i24 v50, v49, s52, v48
SA 248,256
M 10
H 2
UH 7,19
M 11
R 8
M 22
AR 8,198
AD 8,6
M 23
R 10
PA 12288
DF
M 24
AD 154,7
AW 198,8
M 25
UL 6,20
UH 7,20
M 26
AR 8,151
AR 9,175
M 27
AD 8,6
AD 9,7
M 28
AW 151,8
AW 175,9
M 29
UL 6,21
UH 7,21
M 30
AR 8,199
AD 8,6
M 31
AD 155,7
AW 199,8
M 32
M 33
WV 7
M 34
WL 0
FP
M 35
M 36
M 37
M 38
M 39
M 40
M 41
M 42
M 43
M 44
M 45
FR
M 46
M 47
M 48
M 49
M 50
M 51
M 52
M 53
WL 0
FQ 240
M 54
M 55
M 56
FZ 240
R 24
M 57
mw 72
R 26
M 58
TY 106,9736
TY 110,23560
M 59
R 28
M 60
ad 84,1680
W 0
M 61
M 62
M 63
W 1
M 64
M 65
M 66
W 2
M 67
WL 3
AN 104,106
AN 105,107
M 68
SH 106
SH 107
M 69
mw 75
AN 106,106
AN 107,107
M 70
WL 2
AN 108,110
AN 109,111
M 71
R 30
M 72
SH 110
SH 111
M 73
AN 110,110
AN 111,111
M 74
TY 114,11272
TY 118,25096
M 75
ad 84,1686
W 0
M 76
ae 44,70,768
M 77
s_bitcmp1_b32 s50, 12
s_cselect_b32 s51, 24576, 0
v_add_u32 v47, s51, v3
M 78
W 1
XL 30,256
M 79
XL 32,1024
M 80
M 81
W 2
M 82
XL 34,1792
M 83
WL 1
R 32
M 84
mw 0
AN 112,114
AN 113,115
M 85
SH 114
SH 115
M 86
AN 114,114
AN 115,115
M 87
WL 2
AN 116,118
AN 117,119
M 88
SH 118
SH 119
M 89
AN 118,118
AN 119,119
M 90
TY 122,12808
TY 126,26632
M 91
ad 84,1728
M 92
W 0
SA 250,1024
M 93
XL 36,2560
M 94
M 95
R 34
M 96
W 1
M 97
M 98
UL 6,22
UH 7,22
M 99
WL 3
AN 120,122
AN 121,123
M 100
SH 122
SH 123
M 101
AN 122,122
AN 123,123
M 102
WL 2
AN 124,126
AN 125,127
M 103
SH 126
SH 127
M 104
AN 126,126
AN 127,127
M 105
W 2
AR 8,152
M 106
AR 9,176
AD 8,6
M 107
R 48
M 108
mw 3
AD 9,7
AW 152,8
M 109
ad 84,1734
W 0
M 110
AW 176,9
UL 6,23
M 111
UH 7,23
AD 132,6
M 112
W 1
AD 156,7
M 113
UL 6,24
UH 7,24
M 114
AR 8,153
AR 9,177
M 115
W 2
AD 8,6
M 116
AD 9,7
AW 153,8
M 117
AW 177,9
UL 6,25
M 118
bc 49,19,66,60
UH 7,25
M 119
WL 0
R 50
M 120
mh 9
AD 133,6
AD 157,7
M 121
ad 66,3712
H 0
M 132
UL 6,26
UH 7,26
M 133
TX 58,9216
TX 62,23040
M 122
DG 1024
M 123
AR 8,154
AR 9,178
M 134
AD 8,6
AD 9,7
M 135
TY 66,9728
TY 70,23552
M 124
bc 48,19,64,56
AW 154,8
M 125
mh 12
AW 178,9
UL 6,27
M 136
ad 64,3712
H 0
M 137
TX 74,10752
TX 78,24576
M 126
WL 4
Q 0
M 127
Q 2
M 138
Q 4
M 139
TY 82,11264
TY 86,25088
M 128
Q 6
M 129
WL 4
Q 8
M 140
Q 10
M 141
TX 90,12288
TX 94,26112
M 130
Q 12
M 131
R 52
M 142
Q 14
M 143
TY 98,12800
TY 102,26624
M 0
R 54
M 1
WL 10
Q 16
M 12
Q 18
M 13
Q 20
M 2
Q 22
M 3
WL 8
Q 24
M 14
Q 26
M 15
Q 28
M 4
Q 30
M 5
WL 6
Q 32
M 16
Q 34
M 17
Q 36
M 6
Q 38
M 7
WL 2
Q 40
M 18
Q 42
M 19
Q 44
M 8
Q 46
M 9
H 1
SA 252,1792
M 20
UH 7,27
AD 134,6
M 21
AD 158,7
UL 6,28
M 10
H 2
UH 7,28
M 11
R 56
M 22
AR 8,155
AR 9,179
M 23
R 58
M 24
AD 8,6
AD 9,7
)"
R"(M 25
AW 155,8
AW 179,9
M 26
UL 6,29
UH 7,29
M 27
AD 135,6
AD 159,7
M 28
M 29
M 30
M 31
M 32
M 33
WV 6
M 34
WL 0
FP
M 35
M 36
M 37
M 38
M 39
M 40
M 41
M 42
M 43
M 44
M 45
FR
M 46
M 47
M 48
M 49
M 50
M 51
M 52
M 53
WL 0
FQ 244
M 54
M 55
M 56
FZ 244
R 72
M 57
mw 24
R 74
M 58
TY 106,520
TY 110,14344
M 59
R 76
M 60
ad 84,1776
W 0
M 61
M 62
M 63
W 1
M 64
M 65
M 66
W 2
M 67
WL 3
AN 104,106
AN 105,107
M 68
SH 106
SH 107
M 69
mw 27
AN 106,106
AN 107,107
M 70
WL 2
AN 108,110
AN 109,111
M 71
R 78
M 72
SH 110
SH 111
M 73
AN 110,110
AN 111,111
M 74
TY 114,2056
TY 118,15880
M 75
ad 84,1782
W 0
M 76
ae 44,70,768
M 77
s_bfe_u32 s51, s50, 0x1000d
v_mad_i32_i24 v47, v49, s51, v48
XL 38,256
M 78
W 1
M 79
M 80
XL 40,1024
M 81
W 2
SA 254,2560
M 82
M 83
WL 1
R 80
M 84
mw 48
AN 112,114
AN 113,115
M 85
SH 114
SH 115
M 86
AN 114,114
AN 115,115
M 87
WL 2
AN 116,118
AN 117,119
M 88
SH 118
SH 119
M 89
AN 118,118
AN 119,119
M 90
TY 122,3592
TY 126,17416
M 91
ad 84,1824
M 92
W 0
XL 42,1792
M 93
XL 44,2560
M 94
M 95
R 82
M 96
W 1
M 97
M 98
M 99
WL 3
AN 120,122
AN 121,123
M 100
SH 122
SH 123
M 101
AN 122,122
AN 123,123
M 102
WL 2
AN 124,126
AN 125,127
M 103
SH 126
SH 127
M 104
AN 126,126
AN 127,127
M 105
W 2
M 106
M 107
R 0
M 108
mw 51
M 109
ad 84,1830
W 0
M 110
M 111
M 112
W 1
M 113
M 114
M 115
W 2
M 116
M 117
M 118
bc 49,20,66,60
M 119
WL 2
R 2
M 120
mh 18
M 121
ad 66,3904
H 0
M 132
M 133
TX 58,0
TX 62,13824
M 122
DG 1024
M 123
M 134
UL 6,30
UH 7,30
M 135
TY 66,512
TY 70,14336
M 124
bc 48,20,64,56
AD 160,6
M 125
mh 21
AD 184,7
UL 6,31
M 136
ad 64,3904
H 0
M 137
TX 74,1536
TX 78,15360
M 126
WL 4
Q 0
M 127
Q 2
M 138
Q 4
M 139
TY 82,2048
TY 86,15872
M 128
Q 6
M 129
WL 4
Q 8
M 140
Q 10
M 141
TX 90,3072
TX 94,16896
M 130
Q 12
M 131
R 4
M 142
Q 14
M 143
TY 98,3584
TY 102,17408
M 0
R 6
M 1
WL 10
Q 16
M 12
Q 18
M 13
Q 20
M 2
Q 22
M 3
WL 8
Q 24
M 14
Q 26
M 15
Q 28
M 4
Q 30
M 5
WL 6
Q 32
M 16
Q 34
M 17
Q 36
M 6
Q 38
M 7
WL 2
Q 40
M 18
Q 42
M 19
Q 44
M 8
Q 46
M 9
H 1
UH 7,31
M 20
AD 208,6
AD 232,7
M 21
UL 6,32
UH 7,32
M 10
H 2
AD 161,6
M 11
R 8
M 22
AD 185,7
UL 6,33
M 23
R 10
M 24
UH 7,33
AD 209,6
M 25
AD 233,7
UL 6,34
M 26
UH 7,34
AD 162,6
M 27
AD 186,7
UL 6,35
M 28
UH 7,35
AD 210,6
M 29
AD 234,7
UL 6,36
M 30
UH 7,36
AD 163,6
M 31
AD 187,7
UL 6,37
M 32
UH 7,37
AD 211,6
M 33
WV 5
AD 235,7
M 34
WL 0
FP
M 35
M 36
M 37
M 38
M 39
M 40
M 41
M 42
M 43
M 44
M 45
FR
M 46
M 47
M 48
M 49
M 50
M 51
M 52
M 53
WL 0
FQ 248
M 54
M 55
M 56
FZ 248
R 24
M 57
mw 72
R 26
M 58
TY 106,5128
TY 110,18952
M 59
R 28
M 60
ad 84,1872
W 0
M 61
M 62
M 63
W 1
M 64
M 65
M 66
W 2
M 67
WL 3
AN 104,106
AN 105,107
M 68
SH 106
SH 107
M 69
mw 75
AN 106,106
)"
R"(AN 107,107
M 70
WL 2
AN 108,110
AN 109,111
M 71
R 30
M 72
SH 110
SH 111
M 73
AN 110,110
AN 111,111
M 74
TY 114,6664
TY 118,20488
M 75
ad 84,1878
W 0
M 76
M 77
M 78
W 1
M 79
ae 44,68,768
M 80
s_bitcmp1_b32 s50, 14
s_cselect_b32 s51, 24576, 0
v_add_u32 v47, s51, v3
M 81
W 2
XL 14,256
M 82
M 83
WL 1
R 32
M 84
mw 0
AN 112,114
AN 113,115
M 85
SH 114
SH 115
M 86
AN 114,114
AN 115,115
M 87
WL 2
AN 116,118
AN 117,119
M 88
SH 118
SH 119
M 89
AN 118,118
AN 119,119
M 90
TY 122,8200
TY 126,22024
M 91
ad 84,1920
M 92
W 0
XL 16,1024
M 93
XL 18,1792
M 94
M 95
R 34
M 96
W 1
XL 20,2560
M 97
M 98
M 99
WL 3
AN 120,122
AN 121,123
M 100
SH 122
SH 123
M 101
AN 122,122
AN 123,123
M 102
WL 2
AN 124,126
AN 125,127
M 103
SH 126
SH 127
M 104
AN 126,126
AN 127,127
M 105
W 2
M 106
M 107
R 48
M 108
mw 3
M 109
ad 84,1926
W 0
M 110
M 111
M 112
W 1
M 113
M 114
M 115
W 2
M 116
M 117
M 118
bc 49,21,66,60
M 119
WL 2
R 50
M 120
mh 0
M 121
ad 66,4096
H 0
M 132
M 133
TX 58,4608
TX 62,18432
M 122
DG 1024
M 123
M 134
M 135
TY 66,5120
TY 70,18944
M 124
bc 48,21,64,56
M 125
mh 3
M 136
ad 64,4096
H 0
M 137
TX 74,6144
TX 78,19968
M 126
WL 4
Q 0
M 127
Q 2
M 138
Q 4
M 139
TY 82,6656
TY 86,20480
M 128
Q 6
M 129
WL 4
Q 8
M 140
Q 10
M 141
TX 90,7680
TX 94,21504
M 130
Q 12
M 131
R 52
M 142
Q 14
M 143
TY 98,8192
TY 102,22016
M 0
R 54
M 1
WL 10
Q 16
M 12
Q 18
M 13
Q 20
M 2
Q 22
M 3
WL 8
Q 24
M 14
Q 26
M 15
Q 28
M 4
Q 30
M 5
WL 6
Q 32
M 16
Q 34
M 17
Q 36
M 6
Q 38
M 7
WL 2
Q 40
M 18
Q 42
M 19
Q 44
M 8
Q 46
M 9
H 1
UL 6,38
M 20
UH 7,38
AD 164,6
M 21
AD 188,7
UL 6,39
M 10
H 2
UH 7,39
M 11
R 56
M 22
AD 212,6
AD 236,7
M 23
R 58
M 24
UL 6,40
UH 7,40
M 25
AD 165,6
AD 189,7
M 26
UL 6,41
UH 7,41
M 27
AD 213,6
AD 237,7
M 28
UL 6,42
UH 7,42
M 29
AD 166,6
AD 190,7
M 30
UL 6,43
UH 7,43
M 31
AD 214,6
AD 238,7
M 32
UL 6,44
UH 7,44
M 33
WV 5
AD 167,6
AD 191,7
M 34
WL 0
FP
UL 6,45
M 35
UH 7,45
AD 215,6
M 36
AD 239,7
M 37
M 38
M 39
M 40
M 41
M 42
M 43
M 44
M 45
FR
M 46
M 47
M 48
M 49
M 50
M 51
M 52
M 53
WL 0
FQ 252
M 54
M 55
M 56
FZ 252
R 72
M 57
mw 24
R 74
M 58
TY 106,9736
TY 110,23560
M 59
R 76
M 60
ad 84,1968
W 0
M 61
M 62
M 63
W 1
M 64
M 65
M 66
W 2
M 67
WL 3
AN 104,106
AN 105,107
M 68
SH 106
SH 107
M 69
mw 27
AN 106,106
AN 107,107
M 70
WL 2
AN 108,110
AN 109,111
M 71
R 78
M 72
SH 110
SH 111
M 73
AN 110,110
AN 111,111
M 74
TY 114,11272
TY 118,25096
M 75
ad 84,1974
W 0
M 76
M 77
M 78
W 1
M 79
M 80
M 81
W 2
M 82
ae 44,68,768
M 83
WL 1
R 80
M 84
mw 48
AN 112,114
AN 113,115
M 85
s_bfe_u32 s51, s50, 0x1000f
SH 114
SH 115
M 86
AN 114,114
AN 115,115
M 87
WL 2
AN 116,118
AN 117,119
M 88
SH 118
SH 119
M 89
AN 118,118
AN 119,119
M 90
TY 122,12808
TY 126,26632
M 91
ad 84,2016
M 92
W 0
v_mad_i32_i24 v47, v49, s51, v48
M 93
XL 22,256
M 94
XL 24,1024
M 95
R 82
M 96
W 1
XL 26,1792
M 97
M 98
M 99
WL 3
AN 120,122
)"
R"(AN 121,123
M 100
SH 122
SH 123
M 101
AN 122,122
AN 123,123
M 102
WL 2
AN 124,126
AN 125,127
M 103
SH 126
SH 127
M 104
AN 126,126
AN 127,127
M 105
W 2
XL 28,2560
M 106
M 107
R 0
M 108
mw 51
M 109
ad 84,2022
W 0
M 110
M 111
M 112
W 1
M 113
M 114
M 115
W 2
M 116
M 117
M 118
bc 49,22,66,60
M 119
WL 2
R 2
M 120
mh 9
M 121
ad 66,4288
H 0
M 132
M 133
TX 58,9216
TX 62,23040
M 122
DG 1024
M 123
M 134
M 135
TY 66,9728
TY 70,23552
M 124
bc 48,22,64,56
M 125
mh 12
M 136
ad 64,4288
H 0
M 137
TX 74,10752
TX 78,24576
M 126
WL 4
Q 0
M 127
Q 2
M 138
Q 4
M 139
TY 82,11264
TY 86,25088
M 128
Q 6
M 129
WL 4
Q 8
M 140
Q 10
M 141
TX 90,12288
TX 94,26112
M 130
Q 12
M 131
R 4
M 142
Q 14
M 143
TY 98,12800
TY 102,26624
M 0
R 6
M 1
WL 10
Q 16
M 12
Q 18
M 13
Q 20
M 2
Q 22
M 3
WL 8
Q 24
M 14
Q 26
M 15
Q 28
M 4
Q 30
M 5
WL 6
Q 32
M 16
Q 34
M 17
Q 36
M 6
Q 38
M 7
WL 2
Q 40
M 18
Q 42
M 19
Q 44
M 8
Q 46
M 9
H 1
UL 6,14
M 20
UH 7,14
AD 168,6
M 21
AD 192,7
UL 6,15
M 10
H 2
UH 7,15
M 11
R 8
M 22
AD 216,6
AD 240,7
M 23
R 10
M 24
UL 6,16
UH 7,16
M 25
AD 169,6
AD 193,7
M 26
UL 6,17
UH 7,17
M 27
AD 217,6
AD 241,7
M 28
UL 6,18
UH 7,18
M 29
AD 170,6
AD 194,7
M 30
UL 6,19
UH 7,19
M 31
AD 218,6
AD 242,7
M 32
UL 6,20
UH 7,20
M 33
WV 5
AD 171,6
AD 195,7
M 34
WL 0
FP
UL 6,21
M 35
UH 7,21
AD 219,6
M 36
AD 243,7
M 37
M 38
M 39
M 40
M 41
M 42
M 43
M 44
M 45
FR
M 46
M 47
M 48
M 49
M 50
M 51
M 52
M 53
WL 0
FQ 256
M 54
M 55
M 56
FZ 256
R 24
M 57
mw 72
R 26
M 58
TY 106,520
TY 110,14344
M 59
R 28
M 60
ad 84,2064
W 0
M 61
M 62
M 63
W 1
M 64
M 65
M 66
W 2
M 67
WL 3
AN 104,106
AN 105,107
M 68
SH 106
SH 107
M 69
mw 75
AN 106,106
AN 107,107
M 70
WL 2
AN 108,110
AN 109,111
M 71
R 30
M 72
SH 110
SH 111
M 73
AN 110,110
AN 111,111
M 74
TY 114,2056
TY 118,15880
M 75
ad 84,2070
W 0
M 76
M 77
M 78
W 1
M 79
M 80
M 81
W 2
M 82
M 83
R 32
M 84
mw 0
WL 3
AN 112,114
AN 113,115
M 85
SH 114
SH 115
M 86
AN 114,114
AN 115,115
M 87
WL 2
AN 116,118
AN 117,119
M 88
SH 118
SH 119
M 89
AN 118,118
AN 119,119
M 90
TY 122,3592
TY 126,17416
M 91
ad 84,2112
M 92
W 0
M 93
ae 44,68,864
M 94
s_bitcmp1_b32 s50, 16
s_cselect_b32 s51, 24576, 0
v_add_u32 v47, s51, v3
M 95
R 34
M 96
W 1
XL 30,256
M 97
XL 32,1024
M 98
XL 34,1792
M 99
WL 3
AN 120,122
AN 121,123
M 100
SH 122
SH 123
M 101
AN 122,122
AN 123,123
M 102
WL 2
AN 124,126
AN 125,127
M 103
SH 126
SH 127
M 104
AN 126,126
AN 127,127
M 105
W 2
XL 36,2560
M 106
M 107
R 48
M 108
mw 3
M 109
ad 84,2118
W 0
M 110
M 111
M 112
W 1
M 113
M 114
M 115
W 2
M 116
M 117
M 118
bc 49,23,66,60
M 119
WL 2
R 50
M 120
mh 18
M 121
ad 66,4480
H 0
M 132
M 133
TX 58,0
TX 62,13824
M 122
DG 1024
M 123
M 134
M 135
TY 66,512
TY 70,14336
M 124
bc 48,23,64,56
M 125
mh 21
M 136
ad 64,4480
H 0
M 137
TX 74,1536
TX 78,15360
M 126
WL 4
Q 0
M 127
Q 2
M 138
)"
R"(Q 4
M 139
TY 82,2048
TY 86,15872
M 128
Q 6
M 129
WL 4
Q 8
M 140
Q 10
M 141
TX 90,3072
TX 94,16896
M 130
Q 12
M 131
R 52
M 142
Q 14
M 143
TY 98,3584
TY 102,17408
M 0
R 54
M 1
WL 10
Q 16
M 12
Q 18
M 13
Q 20
M 2
Q 22
M 3
WL 8
Q 24
M 14
Q 26
M 15
Q 28
M 4
Q 30
M 5
WL 6
Q 32
M 16
Q 34
M 17
Q 36
M 6
Q 38
M 7
WL 2
Q 40
M 18
Q 42
M 19
Q 44
M 8
Q 46
M 9
H 1
UL 6,22
M 20
UH 7,22
AD 172,6
M 21
AD 196,7
UL 6,23
M 10
H 2
UH 7,23
M 11
R 56
M 22
AD 220,6
AD 244,7
M 23
R 58
M 24
UL 6,24
UH 7,24
M 25
AD 173,6
AD 197,7
M 26
UL 6,25
UH 7,25
M 27
AD 221,6
AD 245,7
M 28
UL 6,26
UH 7,26
M 29
AD 174,6
AD 198,7
M 30
UL 6,27
UH 7,27
M 31
AD 222,6
AD 246,7
M 32
UL 6,28
UH 7,28
M 33
WV 5
AD 175,6
AD 199,7
M 34
WL 0
FP
UL 6,29
M 35
UH 7,29
AD 223,6
M 36
AD 247,7
M 37
M 38
M 39
M 40
M 41
M 42
M 43
M 44
M 45
FR
M 46
M 47
M 48
M 49
M 50
M 51
M 52
M 53
WL 0
FQ 260
M 54
M 55
M 56
FZ 260
R 72
M 57
mw 24
R 74
M 58
TY 106,5128
TY 110,18952
M 59
R 76
M 60
ad 84,2160
W 0
M 61
M 62
M 63
W 1
M 64
M 65
M 66
W 2
M 67
WL 3
AN 104,106
AN 105,107
M 68
SH 106
SH 107
M 69
mw 27
AN 106,106
AN 107,107
M 70
WL 2
AN 108,110
AN 109,111
M 71
R 78
M 72
SH 110
SH 111
M 73
AN 110,110
AN 111,111
M 74
TY 114,6664
TY 118,20488
M 75
ad 84,2166
W 0
M 76
M 77
M 78
W 1
M 79
M 80
M 81
W 2
M 82
M 83
R 80
M 84
mw 48
WL 3
AN 112,114
AN 113,115
M 85
SH 114
SH 115
M 86
AN 114,114
AN 115,115
M 87
WL 2
AN 116,118
AN 117,119
M 88
SH 118
SH 119
M 89
AN 118,118
AN 119,119
M 90
TY 122,8200
TY 126,22024
M 91
ad 84,2208
M 92
W 0
M 93
ae 44,68,864
M 94
s_bfe_u32 s51, s50, 0x10011
v_mad_i32_i24 v47, v49, s51, v48
XL 38,256
M 95
R 82
M 96
W 1
XL 40,1024
M 97
M 98
M 99
WL 3
AN 120,122
AN 121,123
M 100
SH 122
SH 123
M 101
AN 122,122
AN 123,123
M 102
WL 2
AN 124,126
AN 125,127
M 103
SH 126
SH 127
M 104
AN 126,126
AN 127,127
M 105
W 2
XL 42,1792
M 106
M 107
R 0
M 108
mw 51
XL 44,2560
M 109
ad 84,2214
W 0
M 110
M 111
M 112
W 1
M 113
M 114
M 115
W 2
M 116
M 117
M 118
bc 48,0,64,56
M 119
WL 2
R 2
M 120
mh 3
M 121
ad 64,64
H 0
M 132
M 133
TX 58,4608
TX 62,18432
M 122
H 1
M 123
M 134
M 135
TY 66,5120
TY 70,18944
M 124
H 2
M 125
M 136
M 137
TX 74,6144
TX 78,19968
M 126
WL 4
Q 0
M 127
Q 2
M 138
Q 4
M 139
TY 82,6656
TY 86,20480
M 128
Q 6
M 129
WL 4
Q 8
M 140
Q 10
M 141
TX 90,7680
TX 94,21504
M 130
Q 12
M 131
R 4
M 142
Q 14
M 143
TY 98,8192
TY 102,22016
M 0
R 6
M 1
WL 10
Q 16
M 12
Q 18
M 13
Q 20
M 2
Q 22
M 3
WL 8
Q 24
M 14
Q 26
M 15
Q 28
M 4
Q 30
M 5
WL 6
Q 32
M 16
Q 34
M 17
Q 36
M 6
Q 38
M 7
WL 2
Q 40
M 18
Q 42
M 19
Q 44
M 8
Q 46
M 9
UL 6,30
UH 7,30
M 20
AD 176,6
AD 200,7
M 21
UL 6,31
UH 7,31
M 10
AD 224,6
AD 248,7
M 11
R 8
M 22
UL 6,32
UH 7,32
M 23
)"
R"(R 10
M 24
AD 177,6
AD 201,7
M 25
UL 6,33
UH 7,33
M 26
AD 225,6
AD 249,7
M 27
UL 6,34
UH 7,34
M 28
AD 178,6
AD 202,7
M 29
UL 6,35
UH 7,35
M 30
AD 226,6
AD 250,7
M 31
UL 6,36
UH 7,36
M 32
AD 179,6
AD 203,7
M 33
WV 3
UL 6,37
UH 7,37
M 34
WL 0
FP
AD 227,6
M 35
AD 251,7
M 36
M 37
M 38
M 39
M 40
M 41
M 42
M 43
M 44
M 45
FR
M 46
M 47
M 48
M 49
M 50
M 51
M 52
M 53
WL 0
FQ 264
M 54
M 55
M 56
FZ 264
R 24
M 57
mw 72
R 26
M 58
TY 106,9736
TY 110,23560
M 59
R 28
M 60
ad 84,2256
W 0
M 61
M 62
M 63
W 1
M 64
M 65
M 66
W 2
M 67
WL 3
AN 104,106
AN 105,107
M 68
SH 106
SH 107
M 69
mw 75
AN 106,106
AN 107,107
M 70
WL 2
AN 108,110
AN 109,111
M 71
R 30
M 72
SH 110
SH 111
M 73
AN 110,110
AN 111,111
M 74
TY 114,11272
TY 118,25096
M 75
ad 84,2262
W 0
M 76
M 77
M 78
W 1
M 79
M 80
M 81
W 2
M 82
M 83
R 32
M 84
mw 0
WL 3
AN 112,114
AN 113,115
M 85
SH 114
SH 115
M 86
AN 114,114
AN 115,115
M 87
WL 2
AN 116,118
AN 117,119
M 88
SH 118
SH 119
M 89
AN 118,118
AN 119,119
M 90
TY 122,12808
TY 126,26632
M 91
ad 84,0
M 92
W 0
M 93
M 94
M 95
R 34
M 96
W 1
M 97
M 98
M 99
WL 3
AN 120,122
AN 121,123
M 100
SH 122
SH 123
M 101
AN 122,122
AN 123,123
M 102
WL 2
AN 124,126
AN 125,127
M 103
SH 126
SH 127
M 104
AN 126,126
AN 127,127
M 105
W 2
M 106
M 107
R 48
M 108
mw 3
M 109
ad 84,6
W 0
M 110
M 111
M 112
W 1
M 113
M 114
M 115
W 2
M 116
M 117
M 118
bc 48,1,64,56
M 119
WL 2
R 50
M 120
mh 15
M 121
ad 64,259
H 0
M 132
M 133
TX 58,9216
TX 62,23040
M 122
DG 1024
M 123
M 134
M 135
TY 66,9728
TY 70,23552
M 124
M 125
M 136
M 137
TX 74,10752
TX 78,24576
M 126
WL 4
Q 0
M 127
Q 2
M 138
Q 4
M 139
TY 82,11264
TY 86,25088
M 128
Q 6
M 129
WL 4
Q 8
M 140
Q 10
M 141
TX 90,12288
TX 94,26112
M 130
Q 12
M 131
R 52
M 142
Q 14
M 143
TY 98,12800
TY 102,26624
M 0
R 54
M 1
WL 10
Q 16
M 12
Q 18
M 13
Q 20
M 2
Q 22
M 3
WL 8
Q 24
M 14
Q 26
M 15
Q 28
M 4
Q 30
M 5
WL 6
Q 32
M 16
Q 34
M 17
Q 36
M 6
Q 38
M 7
WL 2
Q 40
M 18
Q 42
M 19
Q 44
M 8
Q 46
M 9
UL 6,38
UH 7,38
M 20
AD 180,6
AD 204,7
M 21
UL 6,39
UH 7,39
M 10
AD 228,6
AD 252,7
M 11
R 56
M 22
UL 6,40
UH 7,40
M 23
R 58
M 24
AD 181,6
AD 205,7
M 25
UL 6,41
UH 7,41
M 26
AD 229,6
AD 253,7
BV 52,4608
M 27
UL 6,42
UH 7,42
M 28
AD 182,6
AD 206,7
M 29
UL 6,43
UH 7,43
M 30
AD 230,6
AD 254,7
M 31
UL 6,44
UH 7,44
M 32
AD 183,6
AD 207,7
M 33
WV 8
UL 6,45
UH 7,45
M 34
WL 1
FP
AD 231,6
M 35
AD 255,7
M 36
M 37
M 38
M 39
M 40
ds_read_b64 v[12:13], v4 offset:4864
ds_read_b64 v[46:47], v4 offset:4872
M 41
AR 6,36
AR 7,60
M 42
AR 8,84
AR 9,108
M 43
WL 3
EF 6,52
EF 7,53
M 44
EF 8,54
EF 9,55
M 45
FR
CV 14,6,7
M 46
CV 15,8,9
AR 6,37
M 47
AR 7,61
AR 8,85
M 48
AR 9,109
EF 6,52
M 49
EF 7,53
EF 8,54
M 50
EF 9,55
CV 16,6,7
M 51
CV 17,8,9
AR 6,38
M 52
AR 7,62
AR 8,86
M 53
WL 0
FQ 268
AR 9,110
M 54
EF 6,52
)"
R"(EF 7,53
M 55
EF 8,54
EF 9,55
M 56
FZ 268
R 72
M 57
mw 24
R 74
M 58
TY 114,2056
TY 118,15880
M 59
R 76
M 60
ad 84,48
W 0
M 61
CV 18,6,7
CV 19,8,9
M 62
AR 6,39
AR 7,63
M 63
W 1
AR 8,87
M 64
AR 9,111
EF 6,52
M 65
EF 7,53
EF 8,54
M 66
W 2
EF 9,55
M 67
WL 3
AN 112,114
AN 113,115
M 68
SH 114
SH 115
M 69
mw 27
AN 114,114
AN 115,115
M 70
WL 2
AN 116,118
AN 117,119
M 71
R 78
M 72
SH 118
SH 119
M 73
AN 118,118
AN 119,119
M 74
TY 122,3592
TY 126,17416
M 75
ad 84,54
W 0
M 76
CV 20,6,7
CV 21,8,9
M 77
ae 42,74,768
AR 6,40
M 78
W 1
AR 7,64
M 79
s_bitcmp1_b32 s50, 12
s_cselect_b32 s52, 24576, 0
v_add_u32 v50, s52, v3
M 80
ST 14,0
AR 8,88
M 81
W 2
AR 9,112
M 82
EF 6,52
EF 7,53
M 83
WL 1
R 80
M 84
mw 48
AN 120,122
AN 121,123
M 85
SH 122
SH 123
M 86
AN 122,122
AN 123,123
M 87
WL 2
AN 124,126
AN 125,127
M 88
SH 126
SH 127
M 89
AN 126,126
AN 127,127
M 90
ad 84,96
W 0
M 91
ST 16,768
EF 8,54
M 92
EF 9,55
CV 22,6,7
M 93
W 1
CV 23,8,9
M 94
AR 6,41
AR 7,65
M 95
R 82
M 96
W 2
ST 18,1536
M 97
AR 8,89
AR 9,113
M 98
EF 6,52
EF 7,53
M 99
mw 51
EF 8,54
EF 9,55
M 100
ad 84,102
W 0
M 101
ST 20,2304
CV 24,6,7
M 102
CV 25,8,9
AR 6,42
M 103
W 1
AR 7,66
M 104
AR 8,90
AR 9,114
M 105
EF 6,52
EF 7,53
M 106
W 2
EF 8,54
M 107
EF 9,55
CV 26,6,7
M 108
CV 27,8,9
AR 6,43
M 109
AR 7,67
AR 8,91
M 110
AR 9,115
EF 6,52
M 111
EF 7,53
EF 8,54
M 112
EF 9,55
CV 28,6,7
M 113
CV 29,8,9
M 114
ae 42,74,768
s_bfe_u32 s52, s50, 0x1000d
BV 52,5120
M 115
WV 18
v_mad_i32_i24 v50, v49, s52, v48
ST 22,0
M 116
WL 1
AR 6,132
AR 7,156
M 117
s_barrier
R 0
M 118
mw 72
AR 8,180
EF 6,12
M 119
R 2
M 120
ad 84,144
W 0
M 121
ST 24,768
EF 7,13
M 132
EF 8,46
EF 136,47
M 133
CV 30,6,7
CV 31,8,136
M 122
W 1
AR 6,133
M 123
ST 26,1536
AR 7,157
M 134
AR 8,181
EF 6,12
M 135
EF 7,13
EF 8,46
M 124
W 2
EF 137,47
M 125
ST 28,2304
CV 32,6,7
M 136
CV 33,8,137
AR 6,134
M 137
TX 74,1536
TX 78,15360
M 126
mw 75
AR 7,158
AR 8,182
M 127
ad 84,150
W 0
M 138
EF 6,12
EF 7,13
M 139
TY 82,2048
TY 86,15872
M 128
EF 8,46
EF 138,47
M 129
W 1
CV 34,6,7
M 140
CV 35,8,138
AR 6,135
M 141
WL 2
TX 90,3072
TX 94,16896
P 16
M 130
Q 17
P 19
M 131
R 4
P 20
M 142
Q 21
P 23
M 143
WL 4
TY 98,3584
TY 102,17408
P 24
M 4
R 6
P 25
M 5
Q 26
P 28
M 16
Q 29
P 31
M 17
WL 6
Q 32
M 6
Q 34
M 7
Q 36
M 18
Q 38
M 19
WL 2
Q 40
M 8
Q 42
M 9
Q 44
M 20
Q 46
M 21
W 2
AR 7,159
M 10
AR 8,183
EF 6,12
M 11
R 8
M 22
EF 7,13
EF 8,46
M 23
R 10
M 28
EF 139,47
CV 36,6,7
M 29
CV 37,8,139
M 30
ae 42,74,768
AR 6,136
M 31
s_bitcmp1_b32 s50, 12
s_cselect_b32 s52, 24576, 0
v_add_u32 v50, s52, v3
M 32
ST 30,128
AR 7,160
M 33
AR 8,184
EF 6,12
M 34
EF 7,13
EF 8,46
M 35
WL 4
EF 140,47
CV 38,6,7
M 40
ST 32,896
CV 39,8,140
M 41
AR 6,137
AR 7,161
M 42
AR 8,185
EF 6,12
M 43
EF 7,13
EF 8,46
M 44
WV 21
ST 34,1664
EF 141,47
M 45
WL 0
CV 40,6,7
CV 41,8,141
M 46
s_barrier
R 24
M 47
R 26
M 52
TY 122,8200
TY 126,22024
M 53
ST 36,2432
AR 6,138
M 54
AR 7,162
AR 8,186
M 55
EF 6,12
EF 7,13
M 56
EF 8,46
EF 142,47
M 57
CV 42,6,7
CV 43,8,142
M 58
AR 6,139
AR 7,163
M 59
R 28
M 64
AR 8,187
EF 6,12
M 65
WL 3
AN 120,122
AN 121,123
M 66
)"
R"(SH 122
SH 123
M 67
AN 122,122
AN 123,123
M 68
WL 2
AN 124,126
AN 125,127
M 69
SH 126
SH 127
M 70
AN 126,126
AN 127,127
M 71
R 30
M 76
EF 7,13
EF 8,46
M 77
EF 143,47
CV 44,6,7
M 78
CV 45,8,143
M 79
ae 42,74,768
ds_read_b64 v[12:13], v4 offset:4608
ds_read_b64 v[46:47], v4 offset:4616
M 80
s_bfe_u32 s52, s50, 0x1000d
v_mad_i32_i24 v50, v49, s52, v48
ST 38,128
M 81
EF 160,52
EF 184,53
M 82
EF 208,54
EF 232,55
M 83
R 32
M 88
TX 58,7680
TX 62,21504
M 89
ST 40,896
CV 14,160,184
M 90
CV 15,208,232
EF 161,52
M 91
EF 185,53
EF 209,54
M 92
TY 66,8192
TY 70,22016
M 93
ST 42,1664
EF 233,55
M 94
CV 16,161,185
CV 17,209,233
M 95
WL 7
R 34
M 100
EF 162,52
EF 186,53
M 101
WL 4
PA_ 56,120,58
PA_ 57,121,59
M 102
PB_ 58,120,58
PB_ 59,121,59
M 103
PA_ 60,124,62
PA_ 61,125,63
M 104
PB_ 62,124,62
PB_ 63,125,63
M 105
WL 2
PA_ 64,122,66
PA_ 65,123,67
M 106
PB_ 66,122,66
PB_ 67,123,67
M 107
PA_ 68,126,70
PA_ 69,127,71
M 112
PB_ 70,126,70
PB_ 71,127,71
M 113
ST 44,2432
EF 210,54
M 114
EF 234,55
CV 18,162,186
M 115
CV 19,210,234
EF 163,52
M 116
WV 18
EF 187,53
EF 211,54
M 117
WL 0
EF 235,55
CV 20,163,187
M 118
s_barrier
R 48
M 119
R 50
M 124
CV 21,211,235
M 125
ae 42,74,768
EF 164,52
M 136
s_bitcmp1_b32 s50, 12
s_cselect_b32 s52, 24576, 0
v_add_u32 v50, s52, v3
M 137
ST 14,256
EF 188,53
M 126
EF 212,54
EF 236,55
M 127
CV 22,164,188
CV 23,212,236
M 138
EF 165,52
EF 189,53
M 139
ST 16,1024
EF 213,54
M 128
EF 237,55
CV 24,165,189
M 129
CV 25,213,237
EF 166,52
M 140
EF 190,53
EF 214,54
M 141
ST 18,1792
EF 238,55
M 130
CV 26,166,190
CV 27,214,238
M 131
R 52
M 142
EF 167,52
EF 191,53
M 143
WL 2
R 54
MA 52,0,56
ST 20,2560
EF 215,54
MA 52,4,60
EF 239,55
CV 28,167,191
MA 76,8,56
CV 29,215,239
MA 76,12,60
BV 52,4864
AR 6,44
MA 56,0,64
ae 42,74,768
AR 7,68
MA 56,4,68
s_bfe_u32 s52, s50, 0x1000d
R 56
MA 80,8,64
v_mad_i32_i24 v50, v49, s52, v48
ST 22,256
MA 80,12,68
WL 5
R 58
MA 100,16,56
AR 8,92
AR 9,116
MA 100,20,60
EF 6,12
EF 7,13
MA 104,16,64
ST 24,1024
EF 8,46
MA 104,20,68
WL 5
EF 9,47
CV 30,6,7
MA 124,24,56
WV 14
CV 31,8,9
AR 6,45
MA 124,28,60
WL 0
AR 7,69
AR 8,93
MA 128,24,64
s_barrier
R 72
MA 128,28,68
R 74
MA 148,0,56
ST 26,1792
AR 9,117
MA 148,4,60
EF 6,12
EF 7,13
MA 152,0,64
EF 8,46
EF 9,47
MA 152,4,68
R 76
MA 172,8,56
ST 28,2560
CV 32,6,7
MA 172,12,60
CV 33,8,9
AR 6,46
MA 176,8,64
AR 7,70
AR 8,94
MA 176,12,68
WL 4
R 78
MA 196,16,56
AR 9,118
EF 6,12
MA 196,20,60
EF 7,13
EF 8,46
MV 132,16,64
EF 9,47
CV 34,6,7
MV 132,20,68
WL 4
R 80
MV 152,24,56
CV 35,8,9
AR 6,47
MV 152,28,60
AR 7,71
AR 8,95
MV 156,24,64
AR 9,119
EF 6,12
MV 156,28,68
WL 4
R 82
MV 176,0,56
EF 7,13
EF 8,46
MV 176,4,60
EF 9,47
CV 36,6,7
MV 180,0,64
CV 37,8,9
MV 180,4,68
WL 4
AR 6,48
AR 7,72
AR 8,96
AR 9,120
MV 200,8,56
ae 42,72,768
EF 6,12
EF 7,13
EF 8,46
MV 200,12,60
s_bitcmp1_b32 s50, 14
s_cselect_b32 s52, 24576, 0
v_add_u32 v50, s52, v3
EF 9,47
CV 38,6,7
MV 204,8,64
ST 30,0
CV 39,8,9
AR 6,49
AR 7,73
MV 204,12,68
WL 0
AR 8,97
AR 9,121
EF 6,12
EF 7,13
MV 224,16,56
EF 8,46
EF 9,47
CV 40,6,7
CV 41,8,9
MV 224,20,60
AR 6,50
AR 7,74
AR 8,98
AR 9,122
MV 248,24,56
ST 32,768
EF 6,12
EF 7,13
EF 8,46
MV 248,28,60
EF 9,47
CV 42,6,7
CV 43,8,9
AR 6,51
MV 228,16,64
AR 7,75
AR 8,99
AR 9,123
EF 6,12
MV 228,20,68
EF 7,13
EF 8,46
EF 9,47
CV 44,6,7
MV 252,24,64
ST 34,1536
CV 45,8,9
MV 252,28,68
WV 13
ST 36,2304
ae 42,72,768
s_bfe_u32 s52, s50, 0x1000f
v_mad_i32_i24 v50, v49, s52, v48
ST 38,0
ST 40,768
ST 42,1536
ST 44,2304
ds_read_b64 v[12:13], v4 offset:5120
ds_read_b64 v[46:47], v4 offset:5128
AR 6,140
AR 7,164
AR 8,188
EF 6,52
EF 7,53
EF 8,54
EF 144,55
CV 14,6,7
CV 15,8,144
AR 6,141
AR 7,165
AR 8,189
EF 6,52
EF 7,53
EF 8,54
EF 145,55
CV 16,6,7
CV 17,8,145
AR 6,142
AR 7,166
AR 8,190
EF 6,52
EF 7,53
EF 8,54
EF 146,55
CV 18,6,7
CV 19,8,146
AR 6,143
AR 7,167
AR 8,191
EF 6,52
EF 7,53
EF 8,54
EF 147,55
CV 20,6,7
CV 21,8,147
ae 42,72,768
s_bitcmp1_b32 s50, 14
s_cselect_b32 s52, 24576, 0
v_add_u32 v50, s52, v3
ST 14,128
ST 16,896
ST 18,1664
ST 20,2432
AR 6,144
AR 7,168
AR 8,192
EF 6,52
EF 7,53
EF 8,54
EF 148,55
CV 22,6,7
CV 23,8,148
AR 6,145
AR 7,169
AR 8,193
EF 6,52
EF 7,53
EF 8,54
EF 149,55
)"
R"(CV 24,6,7
CV 25,8,149
AR 6,146
AR 7,170
AR 8,194
EF 6,52
EF 7,53
EF 8,54
EF 150,55
CV 26,6,7
CV 27,8,150
AR 6,147
AR 7,171
AR 8,195
EF 6,52
EF 7,53
EF 8,54
EF 151,55
CV 28,6,7
CV 29,8,151
ae 42,72,768
s_bfe_u32 s52, s50, 0x1000f
v_mad_i32_i24 v50, v49, s52, v48
ST 22,128
ST 24,896
ST 26,1664
ST 28,2432
BV 52,4608
WL 1
EF 168,12
EF 192,13
EF 216,46
EF 240,47
CV 30,168,192
CV 31,216,240
EF 169,12
EF 193,13
EF 217,46
EF 241,47
CV 32,169,193
CV 33,217,241
EF 170,12
EF 194,13
EF 218,46
EF 242,47
CV 34,170,194
CV 35,218,242
EF 171,12
EF 195,13
EF 219,46
EF 243,47
CV 36,171,195
CV 37,219,243
ae 42,72,768
s_bitcmp1_b32 s50, 14
s_cselect_b32 s52, 24576, 0
v_add_u32 v50, s52, v3
ST 30,256
ST 32,1024
ST 34,1792
ST 36,2560
EF 172,12
EF 196,13
EF 220,46
EF 244,47
CV 38,172,196
CV 39,220,244
EF 173,12
EF 197,13
EF 221,46
EF 245,47
CV 40,173,197
CV 41,221,245
EF 174,12
EF 198,13
EF 222,46
EF 246,47
CV 42,174,198
CV 43,222,246
EF 175,12
EF 199,13
EF 223,46
EF 247,47
CV 44,175,199
CV 45,223,247
ae 42,72,768
s_bfe_u32 s52, s50, 0x1000f
v_mad_i32_i24 v50, v49, s52, v48
ST 38,256
ST 40,1024
ST 42,1792
ST 44,2560
ds_read_b64 v[12:13], v4 offset:4864
ds_read_b64 v[46:47], v4 offset:4872
AR 6,52
AR 7,76
AR 8,100
AR 9,124
WL 2
EF 6,52
EF 7,53
EF 8,54
EF 9,55
CV 14,6,7
CV 15,8,9
AR 6,53
AR 7,77
AR 8,101
AR 9,125
EF 6,52
EF 7,53
EF 8,54
EF 9,55
CV 16,6,7
CV 17,8,9
AR 6,54
AR 7,78
AR 8,102
AR 9,126
EF 6,52
EF 7,53
EF 8,54
EF 9,55
CV 18,6,7
CV 19,8,9
AR 6,55
AR 7,79
AR 8,103
AR 9,127
EF 6,52
EF 7,53
EF 8,54
EF 9,55
CV 20,6,7
CV 21,8,9
ae 42,72,864
s_bitcmp1_b32 s50, 16
s_cselect_b32 s52, 24576, 0
v_add_u32 v50, s52, v3
ST 14,0
ST 16,768
ST 18,1536
ST 20,2304
AR 6,56
AR 7,80
AR 8,104
AR 9,128
EF 6,52
EF 7,53
EF 8,54
EF 9,55
CV 22,6,7
CV 23,8,9
AR 6,57
AR 7,81
AR 8,105
AR 9,129
EF 6,52
EF 7,53
EF 8,54
EF 9,55
CV 24,6,7
CV 25,8,9
AR 6,58
AR 7,82
AR 8,106
AR 9,130
EF 6,52
EF 7,53
EF 8,54
EF 9,55
CV 26,6,7
CV 27,8,9
AR 6,59
AR 7,83
AR 8,107
AR 9,131
EF 6,52
EF 7,53
EF 8,54
EF 9,55
CV 28,6,7
CV 29,8,9
ae 42,72,864
s_bfe_u32 s52, s50, 0x10011
v_mad_i32_i24 v50, v49, s52, v48
ST 22,0
ST 24,768
ST 26,1536
ST 28,2304
BV 52,5120
AR 6,148
AR 7,172
AR 8,196
WL 1
EF 6,12
EF 7,13
EF 8,46
EF 152,47
CV 30,6,7
CV 31,8,152
AR 6,149
AR 7,173
AR 8,197
EF 6,12
EF 7,13
EF 8,46
EF 153,47
CV 32,6,7
CV 33,8,153
AR 6,150
AR 7,174
AR 8,198
EF 6,12
EF 7,13
EF 8,46
EF 154,47
CV 34,6,7
CV 35,8,154
AR 6,151
AR 7,175
AR 8,199
EF 6,12
EF 7,13
EF 8,46
EF 155,47
CV 36,6,7
CV 37,8,155
ae 42,72,864
s_bitcmp1_b32 s50, 16
s_cselect_b32 s52, 24576, 0
v_add_u32 v50, s52, v3
ST 30,128
ST 32,896
ST 34,1664
ST 36,2432
AR 6,152
AR 7,176
EF 6,12
EF 7,13
EF 132,46
EF 156,47
CV 38,6,7
CV 39,132,156
AR 6,153
AR 7,177
EF 6,12
EF 7,13
EF 133,46
EF 157,47
CV 40,6,7
CV 41,133,157
AR 6,154
AR 7,178
EF 6,12
EF 7,13
EF 134,46
EF 158,47
CV 42,6,7
CV 43,134,158
AR 6,155
AR 7,179
EF 6,12
EF 7,13
EF 135,46
EF 159,47
CV 44,6,7
CV 45,135,159
ae 42,72,864
s_bfe_u32 s52, s50, 0x10011
v_mad_i32_i24 v50, v49, s52, v48
ST 38,128
ST 40,896
ST 42,1664
ST 44,2432
WL 0
EF 176,52
EF 200,53
EF 224,54
EF 248,55
CV 14,176,200
CV 15,224,248
EF 177,52
EF 201,53
EF 225,54
EF 249,55
CV 16,177,201
CV 17,225,249
EF 178,52
EF 202,53
EF 226,54
EF 250,55
CV 18,178,202
CV 19,226,250
EF 179,52
EF 203,53
EF 227,54
EF 251,55
CV 20,179,203
CV 21,227,251
ae 42,72,864
s_bitcmp1_b32 s50, 16
s_cselect_b32 s52, 24576, 0
v_add_u32 v50, s52, v3
ST 14,256
ST 16,1024
ST 18,1792
ST 20,2560
EF 180,52
EF 204,53
EF 228,54
EF 252,55
CV 22,180,204
CV 23,228,252
EF 181,52
EF 205,53
EF 229,54
EF 253,55
CV 24,181,205
CV 25,229,253
EF 182,52
EF 206,53
EF 230,54
EF 254,55
CV 26,182,206
CV 27,230,254
EF 183,52
EF 207,53
EF 231,54
EF 255,55
CV 28,183,207
CV 29,231,255
ae 42,72,864
s_bfe_u32 s52, s50, 0x10011
v_mad_i32_i24 v50, v49, s52, v48
ST 22,256
ST 24,1024
ST 26,1792
ST 28,2560
s_waitcnt vmcnt(0) lgkmcnt(0)
s_branch 29f
FY 132
FY 136
FY 140
FY 144
FY 148
FY 152
FY 156
FY 160
FY 164
FY 168
FY 172
FY 176
FY 180
FY 184
FY 188
FY 192
FY 196
FY 200
FY 204
FY 208
FY 212
FY 216
FY 220
FY 224
FY 228
FY 232
FY 236
FY 240
FY 244
FY 248
FY 252
FY 256
FY 260
FY 264
FY 268
29:
.purgem MA
.purgem MV
.purgem NA
.purgem NV
.purgem AN
.purgem SH
.purgem PA_
.purgem PB_
.purgem AW
.purgem AR
.purgem UL
.purgem UH
.purgem AD
.purgem EF
.purgem TY
.purgem TX
.purgem RA
.purgem RC
.purgem BV
.purgem BA
.purgem PA
.purgem DF
.purgem DW
)"
R"(.purgem DR
.purgem DH
.purgem DG
.purgem XL
.purgem ST
.purgem SA
.purgem CV
.purgem RS
.purgem PM
.purgem PF
.purgem FS
.purgem FL
.purgem FB
.purgem FC
.purgem FG
.purgem FX
.purgem FP
.purgem FR
.purgem FQ
.purgem FZ
.purgem FY
.purgem WL
.purgem WV
.purgem M
.purgem R1
.purgem R
.purgem P
.purgem Q
.purgem mw
.purgem mh
.purgem ad
.purgem ae
.purgem bc
.purgem W
.purgem H
)"
      : [flg] "=&s"(flg) : K2_OPS : K2_CLOB);
  if (flg) {
    const unsigned lane = __builtin_amdgcn_mbcnt_hi(~0u, __builtin_amdgcn_mbcnt_lo(~0u, 0u));
    const u16* const volatile __attribute__((address_space(4)))* ka = (const u16* const volatile __attribute__((address_space(4)))*)__builtin_amdgcn_kernarg_segment_ptr();
    {
      const u16* w1p = ka[6];
#pragma unroll 1
      for (unsigned f = blockIdx.x * 4 + w; f < 1152; f += 1024) {
        const u16* p = w1p + (32 * (f % 12) + 8 * (lane >> 4)) * HID + 16 * (f / 12) + (lane & 15);
        u32x4 v;
#pragma unroll
        for (int j = 0; j < 4; ++j) v[j] = p[2 * j * HID] | (unsigned)p[(2 * j + 1) * HID] << 16;
        *(u32x4*)(g_buf + f * 1024 + lane * 16) = v;
      }
    }
    slow_bar((unsigned*)(g_buf + G_BAR), w);
    slow_fc1(ka[0], ka[5], (char*)ka[1], (float*)smem + w * HID, w);
    slow_bar((unsigned*)(g_buf + G_BAR + 64), w);
    slow_fc2(ka[0], (const char*)ka[1], (u16*)ka[2], ka[3], ka[4], smem, w);
  }
  TS(15);
  MK_END(4);
}

extern "C" void kernel_launch(void* const* d_in, const int* in_sizes, int n_in,
                              void* d_out, int out_size, void* d_ws, size_t ws_size,
                              hipStream_t stream) {
  const u16* x = (const u16*)d_in[0];
  const u16* dw = (const u16*)d_in[1];
  const u16* w1 = (const u16*)d_in[2];
  const u16* w2 = (const u16*)d_in[3];
  const u16* grn_g = (const u16*)d_in[4];
  const u16* grn_b = (const u16*)d_in[5];
  char* ws = (char*)d_ws;
  if (ws_size < H_BYTES) return;
  (void)hipFuncSetAttribute((const void*)k1_fc1, hipFuncAttributeMaxDynamicSharedMemorySize, K1_LDS);
  (void)hipFuncSetAttribute((const void*)k2_fc2, hipFuncAttributeMaxDynamicSharedMemorySize, K2_LDS);

  k0_prep<<<K0_NB + NPAGE + NPAGX, 256, 0, stream>>>(w1, dw, ws, (const char*)x, grn_b, w2);
  k1_fc1<<<NWG + 8, 256, K1_LDS, stream>>>(x, ws, (const u16*)d_out, grn_g);
  k2_fc2<<<NWG, 256, K2_LDS, stream>>>(x, ws, (u16*)d_out, grn_g, w2, dw, w1);
}
```

```cpp
#include <hip/hip_runtime.h>
#include <hip/hip_bf16.h>
#include <stdint.h>
#include <stdio.h>

typedef short bf16x8 __attribute__((ext_vector_type(8)));
typedef short s16x4 __attribute__((ext_vector_type(4)));
typedef short v4i16_t __attribute__((ext_vector_type(4)));
typedef float f32x16 __attribute__((ext_vector_type(16)));
typedef float f32x4 __attribute__((ext_vector_type(4)));
typedef float f32x2 __attribute__((ext_vector_type(2)));
typedef unsigned u32x4 __attribute__((ext_vector_type(4)));
typedef __bf16 bf16x2_t __attribute__((ext_vector_type(2)));
typedef unsigned short u16;

constexpr float TZ = 175.f, TH = 175.f, TW2 = 175.f;
#define DI __device__ __forceinline__
#define LAS __attribute__((address_space(3)))

constexpr int NPIX = 147456;
constexpr int CH = 384;
constexpr int HID = 1536;
constexpr int NTILE = NPIX / 32;
constexpr int NWG = 256;
constexpr int TPW = NTILE / NWG;

constexpr size_t H_BYTES = (size_t)NPIX * HID * 2;
constexpr unsigned K1_LDS = 163840, K1_PF_OFF = 130048, K1_SMAX = 12, K1_NREP = 4, K1_G_TAB = 1228800, K1_G_CNT = 1239040, K1_G_LOC = 1236992, K1_G_DUM = 1255424,
  K1_G_ACC = 1320960, K1_G_LUT = 1382400, K1_G_BYTES = 1398784;
constexpr bool K1_LUT = true;
constexpr float K1_ZS = 16.0f;
constexpr unsigned K1_GST = 25600, K1_WCH = 6400, K1_ZC = 1365;
constexpr unsigned K1_SB1 = 32768;
constexpr bool K1_F2 = true;
constexpr unsigned K2_SHT = 12483;
constexpr int K2_LDS = 163840, K2_HB = 98304, K2_RSZ = 13824, K2_E_OFF = 153600, K2_PF_OFF = 159744, K2_S16_OFF = 152576, K2_S16_D0 = 8192, K2_NAT = 8;

__device__ __attribute__((aligned(256))) char g_buf[K1_G_BYTES];
__device__ __attribute__((aligned(256))) float g_cs[8 * 384];
__device__ __attribute__((aligned(256))) char g_w2r[96 * 12 * 1024];
__device__ __attribute__((aligned(256))) char g_w2f[96 * 12 * 1024];
__device__ __attribute__((aligned(256))) char g_w2q[16 * 12 * 1024];
__device__ __attribute__((aligned(256))) float g_sc[64];
constexpr unsigned G_FLAG = K1_G_CNT + 14336;
constexpr unsigned G_PUB = K1_G_DUM;
__device__ __attribute__((aligned(256))) unsigned g_prm[1024 * 64];
constexpr unsigned G_STMP = K1_G_CNT + 12288;
constexpr unsigned G_BAR = K1_G_CNT + 13312;
constexpr unsigned H_TS = 49152, H_RS = 48 * H_TS, NPAGE = 217, NPAGX = 55;
#define TS(i) do { } while (0)
#define KST(i) do { } while (0)
#define MK_START(i) do { } while (0)
#define MK_END(i) do { } while (0)

DI unsigned pack_bf16(float a, float b) {
  f32x2 v = {a, b};
  bf16x2_t r = __builtin_convertvector(v, bf16x2_t);
  return __builtin_bit_cast(unsigned, r);
}
typedef _Float16 f16x2_t __attribute__((ext_vector_type(2)));
DI unsigned pack_f16(float a, float b) {
  f32x2 v = {a, b};
  f16x2_t r = __builtin_convertvector(v, f16x2_t);
  return __builtin_bit_cast(unsigned, r);
}
DI float bf_lo(unsigned u) { return __uint_as_float(u << 16); }
DI float bf_hi(unsigned u) { return __uint_as_float(u & 0xffff0000u); }
DI float round_bf16(float a) { return bf_lo(pack_bf16(a, 0.f)); }
DI float dot2acc(unsigned a, unsigned b, float c) {
  return __builtin_amdgcn_fdot2_f32_bf16(__builtin_bit_cast(bf16x2_t, a), __builtin_bit_cast(bf16x2_t, b), c, false);
}
DI float ld_bf16(const u16* p) { return __uint_as_float((unsigned)(*p) << 16); }

DI s16x4 tr_read(const char* p) {
  return __builtin_bit_cast(s16x4, __builtin_amdgcn_ds_read_tr16_b64_v4i16((LAS v4i16_t*)p));
}

#define MFMA32(a, b, c) __builtin_amdgcn_mfma_f32_32x32x16_bf16((a), (b), (c), 0, 0, 0)
#define BAR() __builtin_amdgcn_s_barrier()

DI void dma16(unsigned voff, const char* sbase, unsigned lds_dst) {
  unsigned keep;
  asm volatile("s_mov_b32 %0, m0\n\ts_mov_b32 m0, %3\n\ts_nop 0\n\tglobal_load_lds_dwordx4 %1, %2\n\ts_mov_b32 m0, %0"
               : "=&s"(keep) : "v"(voff), "s"(sbase), "s"(lds_dst) : "memory");
}
DI unsigned lds_addr(const char* p) { return (unsigned)(size_t)(const LAS char*)p; }

DI float gelu_tanh(float x) {
  const float c0 = -2.0f * 0.7978845608028654f * 1.4426950408889634f;
  const float c1 = c0 * 0.044715f;
  float x2 = x * x;
  float t = __builtin_fmaf(x2, c1, c0);
  float e = __builtin_amdgcn_exp2f(x * t);
  float r = __builtin_amdgcn_rcpf(1.0f + e);
  return x * r;
}

DI void gload_b128_asm(u32x4& dst, const char* sbase, unsigned voff) {
  asm volatile("global_load_dwordx4 %0, %1, %2" : "=v"(dst) : "v"(voff), "s"(sbase) : "memory");
}
DI void lds_rd128(u32x4& dst, unsigned addr, int off) {
  asm volatile("ds_read_b128 %0, %1 offset:%2" : "=v"(dst) : "v"(addr), "i"(off));
}
DI void lds_rd128_far(u32x4& dst, unsigned voff, unsigned sbase, int imm) {
  unsigned t;
  asm volatile("v_add_u32 %1, %3, %2\n\tv_add_u32 %1, %4, %1\n\tds_read_b128 %0, %1" : "=v"(dst), "=&v"(t) : "v"(voff), "s"(sbase), "i"(imm));
}
DI void gload16i(u32x4& dst, const char* sbase, unsigned voff, int imm) {
  unsigned t;
  asm volatile("v_add_u32 %1, %4, %2\n\tglobal_load_dwordx4 %0, %1, %3" : "=v"(dst), "=&v"(t) : "v"(voff), "s"(sbase), "i"(imm) : "memory");
}
DI void dma16i(const char* sbase, unsigned voff, int immv, unsigned lbase, int imml) {
  unsigned t, keep;
  asm volatile("s_mov_b32 %1, m0\n\ts_add_u32 m0, %4, %6\n\tv_add_u32 %0, %5, %2\n\tglobal_load_lds_dwordx4 %0, %3\n\ts_mov_b32 m0, %1"
               : "=&v"(t), "=&s"(keep) : "v"(voff), "s"(sbase), "s"(lbase), "i"(immv), "i"(imml) : "memory", "scc");
}

DI void st16_wt(void* p, u32x4 v) { asm volatile("global_store_dwordx4 %0, %1, off\n\ts_nop 1" :: "v"(p), "v"(v) : "memory"); }
template <int I> struct IC { static constexpr int value = I; };
template <int N, int I = 0, typename F> DI void static_for(F&& f) {
  if constexpr (I < N) { f(IC<I>{}); static_for<N, I + 1>(f); }
}
template <int C, int M> DI float dpp_add(float v) { return v + __int_as_float(__builtin_amdgcn_update_dpp(0, __float_as_int(v), C, M, 0xf, false)); }
DI float wave_sum63(float v) {
  v = dpp_add<0x111, 0xf>(v); v = dpp_add<0x112, 0xf>(v); v = dpp_add<0x114, 0xf>(v); v = dpp_add<0x118, 0xf>(v);
  v = dpp_add<0x142, 0xa>(v); return dpp_add<0x143, 0xc>(v);
}
DI unsigned cs_index(unsigned n) { return (n & ~63u) + (n & 3) * 16 + ((n >> 2) & 15); }
DI uint64_t uni64(uint64_t v) { return (uint64_t)__builtin_amdgcn_readfirstlane(v >> 32) << 32 | (unsigned)__builtin_amdgcn_readfirstlane(v); }
DI unsigned wg_side(unsigned b) { return (((const unsigned*)(g_buf + K1_G_LOC))[b & 3] ^ (b >> 2)) & 1; }
DI unsigned h_flip(const char* ws, unsigned region, unsigned t) { return __popc(((const unsigned*)(g_buf + K1_G_LOC + 64))[(((unsigned)(size_t)ws & 0x1fffffu) + region * H_RS + t * H_TS) >> 21]) > 2; }
DI unsigned x_flip(const u16* x, unsigned region) { return __popc(((const unsigned*)(g_buf + K1_G_LOC + 1024))[(((unsigned)(size_t)x & 0x1fffffu) + (12 * region + 6) * 49152u) >> 21]) > 2; }
DI unsigned sub_pixel(const u16* x, unsigned sub, unsigned m) {
  const unsigned blk = sub / 24, u = sub % 24;
  const unsigned region = blk < 256 ? (blk >> 3) * 4 + (blk & 3) : 128 + ((blk - 256) >> 1), side = blk < 256 ? wg_side(blk) : blk & 1;
  return (12 * region + (u >> 1)) * 64 + ((0xda43cb52fe987610ull >> (32 * (u & 1) + 16 * (side ^ x_flip(x, region)) + (m & 12))) & 15) * 4 + (m & 3);
}
DI unsigned h_piece(const char* ws, unsigned sub, unsigned t) {
  const unsigned blk = sub / 24, u = sub % 24;
  const unsigned region = blk < 256 ? (blk >> 3) * 4 + (blk & 3) : 128 + ((blk - 256) >> 1), side = blk < 256 ? wg_side(blk) : blk & 1;
  return region * H_RS + t * H_TS + (u >> 3) * 16384 + (u & 7) * 1024 + ((side ^ h_flip(ws, region, t)) << 13);
}
constexpr unsigned K0_NB = 145 + 24 + 96 + 16 + 48;
constexpr float U24 = 0x1p-24f, EPS = 1e-5f;
DI float wave_all(float v) { return __int_as_float(__builtin_amdgcn_readlane(__float_as_int(wave_sum63(v)), 63)); }
DI float pow2f(float v) { return __uint_as_float(__float_as_uint(v) & 0x7f800000u); }
DI u32x4 w_load(const u16* w) { return *(const u32x4*)(w + ((threadIdx.x >> 3) & 7) * 73408 + 36800 + (threadIdx.x & 7) * 8); }
DI float w_ms(u32x4 a) {
  float q = 0.f;
#pragma unroll
  for (int i = 0; i < 4; ++i) { const float lo = bf_lo(a[i]), hi = bf_hi(a[i]); q = __builtin_fmaf(lo, lo, __builtin_fmaf(hi, hi, q)); }
  return wave_all(q) * (1.0f / 512);
}
DI float w1_scale(float ms) { return pow2f(300.f * U24 * __builtin_amdgcn_rsqf(ms)); }
DI float w2_scale(float ms) { return TW2 * U24 * __builtin_amdgcn_rsqf(ms); }
struct Sc { float w1, zs, g, h, vlo, vhi, c; };
DI Sc scales(const u16* w1, const u16* x, const u16* dw) {
  const u32x4 a = w_load(w1);
  const unsigned l = threadIdx.x & 63;
  float s = 0.f, q = 0.f;
#pragma unroll
  for (int k = 0; k < 6; ++k) {
    const u32x4 v = *(const u32x4*)(x + (9001 + 18397 * (l >> 3)) * CH + 64 * k + 8 * (l & 7)), d = *(const u32x4*)(dw + 64 * k + 8 * (l & 7));
#pragma unroll
    for (int i = 0; i < 4; ++i) { const float lo = bf_lo(v[i]) * bf_lo(d[i]), hi = bf_hi(v[i]) * bf_hi(d[i]); s += lo + hi; q = __builtin_fmaf(lo, lo, __builtin_fmaf(hi, hi, q)); }
  }
  s = dpp_add<0x141, 0xf>(dpp_add<0x4e, 0xf>(dpp_add<0xb1, 0xf>(s))); q = dpp_add<0x141, 0xf>(dpp_add<0x4e, 0xf>(dpp_add<0xb1, 0xf>(q)));
  const float var = fmaxf(q * (1.0f / CH) - s * s * (1.0f / (CH * CH)), 0.f);
  const float r2 = wave_all(var * __builtin_amdgcn_rcpf(var + EPS)) * (1.0f / 64), rden = __builtin_amdgcn_exp2f(wave_all(__builtin_amdgcn_logf(var + EPS)) * (-1.0f / 128));
  const float s1 = w_ms(a), p2 = r2 * CH * s1;
  Sc c;
  c.w1 = w1_scale(s1);
  c.g = TZ * U24 * __builtin_amdgcn_rsqf(r2);
  c.h = TH * U24 * __builtin_amdgcn_rsqf(p2 * (0.25f + 0.5f * p2 * __builtin_amdgcn_rcpf(1.0f + 2.0f * p2)));
  c.c = fminf(__builtin_ceilf(0.1701f * 0x1p24f * c.h) + 1.0f, 1400.f);
  c.zs = fminf(pow2f(c.g * 2530.f), pow2f(c.g * 0x1p12f * rden));
  const float e = EPS * c.zs * c.zs, z2 = r2 * (660.f * 660.f / (TZ * TZ));
  c.vlo = 4.2f * c.g * c.g - e;
  c.vhi = fminf(0x1.ep29f * c.g * c.g - e, z2 < 1.0f ? e * z2 * __builtin_amdgcn_rcpf(1.0f - z2) : 0x1p120f);
  return c;
}
__global__ __launch_bounds__(256) void k0_prep(const u16* __restrict__ w1, const u16* __restrict__ dw, const char* __restrict__ ws, const char* __restrict__ x,
                                                const u16* __restrict__ grn_b, const u16* __restrict__ w2) {
  MK_START(0);
  uint64_t pc; int d1;
  asm volatile("s_getpc_b64 %0\n1:\n\ts_mov_b32 %1, k1_code-1b" : "=s"(pc), "=s"(d1));
  if (threadIdx.x >= 192) pc += (int64_t)d1 + ((int)(blockIdx.x >> 3) - 3) * 8192;
  unsigned pfd = 0;
  if (blockIdx.x < 64 && (threadIdx.x < 150 || (blockIdx.x < 48 && threadIdx.x >= 192))) pfd = *(const __attribute__((address_space(1))) unsigned*)((pc & ~127ull) + threadIdx.x * 128);
#define K0_END() do { asm volatile("" :: "v"(pfd)); MK_END(0); } while (0)
  unsigned xcc;
  asm volatile("s_getreg_b32 %0, hwreg(HW_REG_XCC_ID)" : "=s"(xcc));
  xcc = (xcc >> 2) & 1;
  if (blockIdx.x < 4 && threadIdx.x == 0) ((unsigned*)(g_buf + K1_G_LOC))[blockIdx.x] = 7 * xcc;
  constexpr unsigned NP = NPAGE + NPAGX;
  __shared__ Sc ssc;
  __shared__ float ss2;
  const bool sw = threadIdx.x >= 192;
  const unsigned bi = blockIdx.x;
  const unsigned bx = bi < NP ? bi + K0_NB : bi - NP;
  if (bx >= K0_NB) {
    __shared__ unsigned lat[8];
    const unsigned pg = bx - K0_NB, w = threadIdx.x >> 6, lane = threadIdx.x & 63;
    auto gran = [&](unsigned p) {
      const bool h = p < NPAGE;
      const char* buf = h ? ws : x;
      const int o0 = (int)((h ? p : p - NPAGE) << 21) - (int)((unsigned)(size_t)buf & 0x1fffffu);
      const unsigned al = h ? 49152 : 12 * 49152;
      const unsigned q = ((((unsigned)max(o0, 0) + al - 1) >> (h ? 14 : 16)) * (h ? 87382u : 29128u)) >> 18;
      return buf + min(q * al, (unsigned)(h ? H_BYTES : H_BYTES / 4) - 16384u);
    };
    if (lane < 5) {
      const char* a = gran(lane < 4 ? min(pg + NP - 1 - (lane >> 1), pg - 1 - (lane >> 1)) : pg) + (lane & 1) * 8192 + w * 2048 + (lane < 4 ? 1024 : 0);
      const char* b = a;
      const char* own = (const char*)uni64(__builtin_amdgcn_readlane((uint64_t)a >> 32, 4) * 4294967296ull + (unsigned)__builtin_amdgcn_readlane((unsigned)(uint64_t)a, 4));
      unsigned long long t0, t1, t2;
      int n = 2;
      asm volatile("" : "+s"(n));
#pragma unroll 1
      do {
        unsigned v;
        asm volatile("s_cmp_eq_u32 %6, 2\n\ts_cbranch_scc1 1f\n\ts_sleep 4\n1:\n\ts_memtime %0\n\ts_waitcnt lgkmcnt(0)\n\tglobal_load_dword %3, %4, off sc1\n\ts_waitcnt vmcnt(0)\n\ts_memtime %1\n\ts_waitcnt lgkmcnt(0)\n\t"
                     "global_load_dword %3, %5, off sc1\n\ts_waitcnt vmcnt(0)\n\ts_memtime %2\n\ts_waitcnt lgkmcnt(0)" : "=&s"(t0), "=&s"(t1), "=&s"(t2), "=&v"(v) : "v"(a), "v"(b), "s"(n) : "memory", "scc");
        a = own + 1024 + (w & 1) * 8192; b = a + 8192 - (w & 1) * 16384;
      } while (--n);
      lat[w + 4 * (w & 1)] = (unsigned)(t1 - t0); lat[w + 4 - 4 * (w & 1)] = (unsigned)(t2 - t1);
    }
    __syncthreads();
    if (threadIdx.x == 0) {
      auto fastest = [&](const unsigned* l) { unsigned m = 9999; for (int i = 0; i < 4; ++i) m = l[i] < 300 ? m : min(m, l[i]); return (int)m; };
      const int df = fastest(lat) - fastest(lat + 4);
      const unsigned v = xcc ^ (df > 0), sure = abs(df) > 80;
      char* e = g_buf + K1_G_LOC + 64 + 4 * (pg < NPAGE ? pg : pg + 240 - NPAGE);
      *(u16*)(e + 2) = v ? 3 + 4 * sure : 1 - sure;
      if (pg == 0 || pg == NPAGE) e[0] = v; else e[-3] = v ^ 1;
      if (pg == NPAGE - 1 || pg == NP - 1) e[1] = v; else e[4] = v ^ 1;
    }
    K0_END();
    return;
  }
  if (bx >= 145 + 24 + 96 + 16) {
    __shared__ __attribute__((aligned(16))) float red[2][16][32];
    const int t = bx - 281, c8 = (threadIdx.x & 3) * 8;
    u32x4 sm, v[6];
    if (sw) sm = w_load(w1);
#pragma unroll
    for (int k = 0; k < 6; ++k) v[k] = *(const u32x4*)(w1 + (size_t)(64 * k + (threadIdx.x >> 2)) * HID + 32 * t + c8);
    if (sw) ss2 = w1_scale(w_ms(sm));
    __syncthreads();
    const float s1 = ss2;
    float a[8], q[8];
#pragma unroll
    for (int i = 0; i < 8; ++i) {
      a[i] = q[i] = 0.f;
#pragma unroll
      for (int k = 0; k < 6; ++k) {
        const float f = (float)(_Float16)(s1 * (i & 1 ? bf_hi(v[k][i >> 1]) : bf_lo(v[k][i >> 1]))) * 0x1p24f;
        a[i] += f; q[i] = __builtin_fmaf(f, f, q[i]);
      }
      a[i] = dpp_add<0x118, 0xf>(dpp_add<0x114, 0xf>(a[i])); q[i] = dpp_add<0x118, 0xf>(dpp_add<0x114, 0xf>(q[i]));
    }
    if ((threadIdx.x & 12) == 12) {
#pragma unroll
      for (int i = 0; i < 2; ++i) {
        *(f32x4*)&red[0][threadIdx.x >> 4][c8 + 4 * i] = (f32x4){a[4 * i], a[4 * i + 1], a[4 * i + 2], a[4 * i + 3]};
        *(f32x4*)&red[1][threadIdx.x >> 4][c8 + 4 * i] = (f32x4){q[4 * i], q[4 * i + 1], q[4 * i + 2], q[4 * i + 3]};
      }
    }
    __syncthreads();
    if (threadIdx.x < 64) {
      const int n = threadIdx.x & 31;
      float s = 0.f;
#pragma unroll
      for (int i = 0; i < 16; ++i) s += red[threadIdx.x >> 5][i][n];
      const float c = (0.f - (float)K1_ZC * s) * 0x1p-48f;
      char* p = g_buf + t * K1_GST + 5120;
      if (threadIdx.x < 32) *(f32x4*)(p + 272 * (n >> 4) + (n & 15) * 16) = (f32x4){c, c, c, c};
      const bool any = __ballot(threadIdx.x >= 32 && !(s >= 384.f * 64 * 64 && s <= 384.f * 1024 * 1024)) != 0;
      if (threadIdx.x == 0) g_buf[G_FLAG + 4 * 98 + t] = any;
    }
    K0_END();
    return;
  }
  if (bx >= 145 + 24 + 96) {
    if (sw) ssc = scales(w1, (const u16*)x, dw);
    __syncthreads();
    const Sc sc = ssc;
    const unsigned e = (bx - 265) * 256 + threadIdx.x;
    const float u = 1.0f / (sc.g * sc.w1);
    float r[2];
#pragma unroll
    for (int i = 0; i < 2; ++i) {
      const unsigned c = 2 * e + i;
      r[i] = __builtin_fmaf(sc.h, gelu_tanh(u * __uint_as_float((c >> 12) << 31 | ((c & 4095) + 79 * 128) << 16 | 0x8000u)), sc.c * U24);
      if (!(r[i] >= 0.f && r[i] < 8189.f * U24)) r[i] = __builtin_inff();
    }
    *(unsigned*)(g_buf + K1_G_LUT + 4 * e) = pack_f16(r[0], r[1]);
    K0_END();
    return;
  }
  if (bx >= 145 + 24) {
    __shared__ __attribute__((aligned(16))) u16 tw[32][200];
    const int kappa = (bx - 169) >> 1, nh = (bx - 169) & 1;
    u32x4 sm, v[3];
    if (sw) sm = w_load(w2);
#pragma unroll
    for (int i = 0; i < 3; ++i) { const int e = threadIdx.x + 256 * i; v[i] = *(const u32x4*)(w2 + (size_t)(32 * kappa + e / 24) * CH + 192 * nh + (e % 24) * 8); }
    if (sw) ss2 = w2_scale(w_ms(sm));
#pragma unroll
    for (int i = 0; i < 3; ++i) { const int e = threadIdx.x + 256 * i; *(u32x4*)&tw[e / 24][(e % 24) * 8] = v[i]; }
    __syncthreads();
    const int lane = threadIdx.x & 63, g = lane >> 4;
    const float s2 = 256.f * ss2;
    bool bad = false;
#pragma unroll
    for (int i = 0; i < 3; ++i) {
      const int fi = (threadIdx.x >> 6) + 4 * i, n = 64 * (fi >> 2) + 4 * (lane & 15) + (fi & 3);
      float wv[8];
      unsigned bits = 0;
#pragma unroll
      for (int j = 0; j < 8; ++j) {
        wv[j] = ld_bf16(&tw[16 * (j >> 2) + 4 * g + (j & 3)][n]);
        bits |= wv[j] != 0.f ? 256u << j : 0u;
        wv[j] *= s2;
        bits |= fabsf(wv[j]) >= 48 * 256 * U24 ? 1u << j : 0u;
        bad |= !(fabsf(wv[j]) <= 500.f);
      }
      const unsigned o = (((kappa & ~1) + fi / 6) * 24 + nh * 12 + (fi % 6) * 2 + (kappa & 1)) * 1024 + lane * 16;
      st16_wt(g_w2r + o, (u32x4){pack_f16(wv[0], wv[1]), pack_f16(wv[2], wv[3]), pack_f16(wv[4], wv[5]), pack_f16(wv[6], wv[7])});
      constexpr float q = 1.0f / 256;
      if (kappa < 8) st16_wt(g_w2q + o, (u32x4){pack_f16(q * wv[0], q * wv[1]), pack_f16(q * wv[2], q * wv[3]), pack_f16(q * wv[4], q * wv[5]), pack_f16(q * wv[6], q * wv[7])});
      static_for<4>([&](auto ic) { bits |= __builtin_amdgcn_update_dpp(0u, bits, 0x120 + (1 << decltype(ic)::value), 0xf, 0xf, false); });
      bad |= ((bits >> 8) & ~bits & 255u) != 0;
    }
    const bool any = __ballot(bad) != 0;
    if (lane == 0) ((char*)(g_buf + G_FLAG))[4 * (bx - 169) + (threadIdx.x >> 6)] = any;
    K0_END();
    return;
  }
  if (bx >= 145) {
    __shared__ __attribute__((aligned(16))) float red[16][64];
    const int c0 = 64 * ((bx - 145) >> 2), rg = (bx - 145) & 3, c8 = (threadIdx.x & 7) * 8;
    float a[8];
#pragma unroll
    for (int i = 0; i < 8; ++i) a[i] = 0.f;
#pragma unroll
    for (int k = 0; k < 12; ++k) {
      const int ch = 384 * rg + 32 * k + (threadIdx.x >> 3);
      const float b = ld_bf16(grn_b + ch);
      const u32x4 v = *(const u32x4*)(w2 + (size_t)ch * CH + c0 + c8);
#pragma unroll
      for (int i = 0; i < 4; ++i) { a[2 * i] = __builtin_fmaf(b, bf_lo(v[i]), a[2 * i]); a[2 * i + 1] = __builtin_fmaf(b, bf_hi(v[i]), a[2 * i + 1]); }
    }
#pragma unroll
    for (int i = 0; i < 8; ++i) a[i] = dpp_add<0x118, 0xf>(a[i]);
    if (threadIdx.x & 8) {
      *(f32x4*)&red[threadIdx.x >> 4][c8] = (f32x4){a[0], a[1], a[2], a[3]};
      *(f32x4*)&red[threadIdx.x >> 4][c8 + 4] = (f32x4){a[4], a[5], a[6], a[7]};
    }
    __syncthreads();
    if (threadIdx.x < 64) {
      float s = 0.f;
#pragma unroll
      for (int i = 0; i < 16; ++i) s += red[i][threadIdx.x];
      g_cs[(4 + rg) * 384 + cs_index(c0 + threadIdx.x)] = s;
      g_cs[64 * (bx - 145) + threadIdx.x] = 0.f;
    }
    K0_END();
    return;
  }
  const unsigned bid = bx;
  if (bid == 0) {
    if (sw) ssc = scales(w1, (const u16*)x, dw);
    if ((threadIdx.x >> 6) == 2) ss2 = w2_scale(w_ms(w_load(w2)));
#pragma unroll
    for (int i = 0; i < 4; ++i) if (threadIdx.x + 256 * i < 896) *(u32x4*)(g_buf + K1_G_CNT + (threadIdx.x + 256 * i) * 16) = (u32x4){0u, 0u, 0u, 0u};
    *(unsigned*)(g_buf + G_PUB + threadIdx.x * 256) = 0u;
    const int e = threadIdx.x, e2 = e < 192 ? 32 * (e >> 4) + 8 * ((e & 15) >> 2) : 8 * min(e - 192, 47);
    const u32x4 d = *(const u32x4*)(dw + e2);
    __syncthreads();
    const Sc sc = ssc;
    const float s2 = ss2;
    u32x4 v;
#pragma unroll
    for (int k = 0; k < 4; ++k) v[k] = pack_bf16(sc.zs * bf_lo(d[k]), sc.zs * bf_hi(d[k]));
    if (e < 192) {
#pragma unroll
      for (int k = 0; k < 4; ++k) v[k] &= (k & 1) != ((e >> 1) & 1) ? 0u : e & 1 ? 0xffff0000u : 0xffffu;
    }
    if (e >= 240) v = (u32x4){__float_as_uint(EPS * sc.zs * sc.zs), __float_as_uint(sc.vhi), __float_as_uint(sc.g), __float_as_uint(sc.vlo)};
    *(u32x4*)(g_buf + K1_G_TAB + (e < 192 ? e : e + 192) * 16) = v;
    auto sane = [](float v) { return v >= 0x1p-45f && v <= 0x1p20f; };
    if (threadIdx.x < 2) ((unsigned*)(g_buf + G_FLAG))[96 + threadIdx.x] = !(sane(sc.w1) && sane(sc.zs) && sane(sc.g) && sane(sc.h) && sane(s2));
    if (threadIdx.x == 0) {
      const float ik = 1.0f / (sc.h * s2);
      *(f32x4*)g_sc = (f32x4){sc.h * s2, ik, 0x1p-28f / (sc.h * sc.h), -sc.c * U24 * ik};
      g_sc[4] = __uint_as_float(pack_f16(-sc.c * U24, -sc.c * U24));
      *(unsigned*)(g_buf + K1_G_CNT + 16320) = pack_f16(-sc.c * 0x1p-10f, -sc.c * 0x1p-10f);
    }
    if (threadIdx.x < 48) *(u32x4*)(g_buf + threadIdx.x * K1_GST + 5376) = (u32x4){pack_f16(sc.c * U24, 0.f), 0u, 0u, 0u};
    K0_END();
    return;
  }
  __shared__ __attribute__((aligned(16))) u16 tl[32][136];
  const int e = (bid - 1) * 256 + threadIdx.x, ks = (bid - 1) % 12, cb = (bid - 1) / 12;
  u32x4 sm, v[2];
  if (sw) sm = w_load(w1);
#pragma unroll
  for (int i = 0; i < 2; ++i) v[i] = *(const u32x4*)(w1 + (size_t)(32 * ks + (threadIdx.x >> 4) + 16 * i) * HID + 128 * cb + (threadIdx.x & 15) * 8);
  if (sw) ss2 = w1_scale(w_ms(sm));
#pragma unroll
  for (int i = 0; i < 2; ++i) *(u32x4*)&tl[(threadIdx.x >> 4) + 16 * i][(threadIdx.x & 15) * 8] = v[i];
  if (e < (int)(K1_NREP + 1) * 768) *(u32x4*)(g_buf + K1_G_ACC + e * 16) = (u32x4){0u, 0u, 0u, 0u};
  __syncthreads();
  const float s1 = ss2;
#pragma unroll
  for (int i = 0; i < 2; ++i) {
    const int lane = threadIdx.x & 63, f = (threadIdx.x >> 6) + 4 * i, n = 16 * f + (lane & 15), k0 = 8 * (lane >> 4);
    u32x4 P;
#pragma unroll
    for (int jj = 0; jj < 4; ++jj) P[jj] = pack_f16(s1 * ld_bf16(&tl[k0 + 2 * jj][n]), s1 * ld_bf16(&tl[k0 + 2 * jj + 1][n]));
    const int fs = (f & 1) * 12 + ks;
    st16_wt(g_buf + (4 * cb + (f >> 1)) * K1_GST + (fs + (fs >= 5)) * 1024 + lane * 16, P);
  }
  K0_END();
}

constexpr unsigned G_ACC2 = K1_G_ACC + K1_NREP * 12288;

#define VA_CLOB \
  "v0", "v1", "v2", "v3", "v4", "v5", "v6", "v7", "v8", "v9", "v10", "v11", "v12", "v13", "v14", "v15", "v16", "v17", "v18", "v19", "v20", "v21", "v22", "v23", "v24", "v25", "v26", "v27", "v28", "v29", "v30", "v31", \
  "v32", "v33", "v34", "v35", "v36", "v37", "v38", "v39", "v40", "v41", "v42", "v43", "v44", "v45", "v46", "v47", "v48", "v49", "v50", "v51", "v52", "v53", "v54", "v55", "v56", "v57", "v58", "v59", "v60", "v61", "v62", "v63", \
  "v64", "v65", "v66", "v67", "v68", "v69", "v70", "v71", "v72", "v73", "v74", "v75", "v76", "v77", "v78", "v79", "v80", "v81", "v82", "v83", "v84", "v85", "v86", "v87", "v88", "v89", "v90", "v91", "v92", "v93", "v94", "v95", \
  "v96", "v97", "v98", "v99", "v100", "v101", "v102", "v103", "v104", "v105", "v106", "v107", "v108", "v109", "v110", "v111", "v112", "v113", "v114", "v115", "v116", "v117", "v118", "v119", "v120", "v121", "v122", "v123", "v124", "v125", "v126", "v127", \
  "v128", "v129", "v130", "v131", "v132", "v133", "v134", "v135", "v136", "v137", "v138", "v139", "v140", "v141", "v142", "v143", "v144", "v145", "v146", "v147", "v148", "v149", "v150", "v151", "v152", "v153", "v154", "v155", "v156", "v157", "v158", "v159", \
  "v160", "v161", "v162", "v163", "v164", "v165", "v166", "v167", "v168", "v169", "v170", "v171", "v172", "v173", "v174", "v175", "v176", "v177", "v178", "v179", "v180", "v181", "v182", "v183", "v184", "v185", "v186", "v187", "v188", "v189", "v190", "v191", \
  "v192", "v193", "v194", "v195", "v196", "v197", "v198", "v199", "v200", "v201", "v202", "v203", "v204", "v205", "v206", "v207", "v208", "v209", "v210", "v211", "v212", "v213", "v214", "v215", "v216", "v217", "v218", "v219", "v220", "v221", "v222", "v223", \
  "v224", "v225", "v226", "v227", "v228", "v229", "v230", "v231", "v232", "v233", "v234", "v235", "v236", "v237", "v238", "v239", "v240", "v241", "v242", "v243", "v244", "v245", "v246", "v247", "v248", "v249", "v250", "v251", "v252", "v253", "v254", "v255", \
  "a0", "a1", "a2", "a3", "a4", "a5", "a6", "a7", "a8", "a9", "a10", "a11", "a12", "a13", "a14", "a15", "a16", "a17", "a18", "a19", "a20", "a21", "a22", "a23", "a24", "a25", "a26", "a27", "a28", "a29", "a30", "a31", \
  "a32", "a33", "a34", "a35", "a36", "a37", "a38", "a39", "a40", "a41", "a42", "a43", "a44", "a45", "a46", "a47", "a48", "a49", "a50", "a51", "a52", "a53", "a54", "a55", "a56", "a57", "a58", "a59", "a60", "a61", "a62", "a63", \
  "a64", "a65", "a66", "a67", "a68", "a69", "a70", "a71", "a72", "a73", "a74", "a75", "a76", "a77", "a78", "a79", "a80", "a81", "a82", "a83", "a84", "a85", "a86", "a87", "a88", "a89", "a90", "a91", "a92", "a93", "a94", "a95", \
  "a96", "a97", "a98", "a99", "a100", "a101", "a102", "a103", "a104", "a105", "a106", "a107", "a108", "a109", "a110", "a111", "a112", "a113", "a114", "a115", "a116", "a117", "a118", "a119", "a120", "a121", "a122", "a123", "a124", "a125", "a126", "a127", \
  "a128", "a129", "a130", "a131", "a132", "a133", "a134", "a135", "a136", "a137", "a138", "a139", "a140", "a141", "a142", "a143", "a144", "a145", "a146", "a147", "a148", "a149", "a150", "a151", "a152", "a153", "a154", "a155", "a156", "a157", "a158", "a159", \
  "a160", "a161", "a162", "a163", "a164", "a165", "a166", "a167", "a168", "a169", "a170", "a171", "a172", "a173", "a174", "a175", "a176", "a177", "a178", "a179", "a180", "a181", "a182", "a183", "a184", "a185", "a186", "a187", "a188", "a189", "a190", "a191", \
  "a192", "a193", "a194", "a195", "a196", "a197", "a198", "a199", "a200", "a201", "a202", "a203", "a204", "a205", "a206", "a207", "a208", "a209", "a210", "a211", "a212", "a213", "a214", "a215", "a216", "a217", "a218", "a219", "a220", "a221", "a222", "a223", \
  "a224", "a225", "a226", "a227", "a228", "a229", "a230", "a231", "a232", "a233", "a234", "a235", "a236", "a237", "a238", "a239", "a240", "a241", "a242", "a243", "a244", "a245", "a246", "a247", "a248", "a249", "a250", "a251", "a252", "a253", "a254", "a255"
#define K1_CLOB "memory", "vcc", "scc", "s36", "s37", "s38", "s39", "s40", "s41", "s42", "s43", "s44", "s45", "s46", "s47", "s48", "s49", "s50", "s51", "s52", "s53", "s54", "s55", "s56", "s57", "s58", "s59", "s60", "s61", "s62", "s63", "s64", "s65", "s66", "s67", "s68", "s69", "s70", "s71", "s72", "s73", "s74", "s75", "s76", "s77", "s78", "s79", "s80", "s81", "s82", "s83", "s84", "s85", "s86", "s87", "s88", "s89", "s90", "s91", "s92", "s93", "s94", "s95", "s96", "s97", VA_CLOB
DI void k2_params(const u16* x, const char* hws, const u16* out, const u16* grn_g, unsigned tid) {
  const unsigned e = (tid >> 2) * 32 + blockIdx.x * 4 + (tid & 3);
  const unsigned b = e >> 2, w = e & 3, pg = w & 1, nh = w >> 1, q = (b >> 3) * 4 + (b & 3), rs = 128 + (q >> 1), hf = q & 1, side = wg_side(b);
  auto fmask = [&](unsigned region) {
    const unsigned o = ((unsigned)(size_t)hws & 0x1fffffu) + region * H_RS + nh * H_TS;
    const unsigned* p = (const unsigned*)(g_buf + K1_G_LOC + 64) + (o >> 21);
    const unsigned r = 0x200000u + 2 * H_TS - 1 - (o & 0x1fffffu), d1 = ((r >> 15) * 171) >> 9, d2 = (((r + 0x200000u) >> 15) * 171) >> 9;
    const unsigned a1 = ~0u << d1, a2 = d2 < 32 ? ~0u << d2 : 0u;
    return ((__popc(p[0]) > 2 ? ~a1 : 0u) | (__popc(p[1]) > 2 ? a1 & ~a2 : 0u) | (__popc(p[2]) > 2 ? a2 : 0u)) ^ (0u - side);
  };
  const unsigned f0 = fmask(q), f1 = fmask(rs);
  const unsigned fx = ((side ^ x_flip(x, q)) ? ~K2_SHT : 0u) | ((side ^ x_flip(x, rs)) ? K2_SHT : 0u);
  const uint64_t h0 = (uint64_t)hws + q * H_RS + nh * (H_TS + 2048) + pg * 3072, h1 = (uint64_t)hws + rs * H_RS + nh * (H_TS + 2048) + hf * 3072 + pg * 1536;
  const unsigned p0 = (12 * q + 2 * pg) * 49152 + nh * 384, p1 = (12 * rs + 2 * hf + pg) * 49152 + nh * 384;
  const unsigned e1 = 1024 + (e >> 3);
  auto kap = [](unsigned f) { return f / 48 * 2 + (f & 1); };
  auto sco = [&](unsigned f) { return kap(f) * 64 + (kap(f) < 16 ? K2_S16_D0 : 0); };
  auto cso = [&](unsigned f) { const unsigned r = f % 48; return ((f < 192 ? min(f / 48, 2u) : 3) * 384 + (r / 12 % 2 * 12 + r / 24 * 6 + r % 12 / 2) * 16) * 4; };
  typedef uint64_t u64x2 __attribute__((ext_vector_type(2)));
  u64x2* P = (u64x2*)(g_prm + 64 * e);
  P[0] = (u64x2){f0 | (uint64_t)f1 << 32, fx | (uint64_t)(sco(e) | (sco(e1) + (e & 6) * 8) << 16) << 32};
  const u32x4 sc = *(const u32x4*)g_sc;
  P[1] = (u64x2){(((b >> 3) * 4 + w) & 7) * 1024 | (uint64_t)sc[0] << 32, sc[1] | (uint64_t)sc[2] << 32};
  P[2] = (u64x2){h0, h0 + 8192};
  P[3] = (u64x2){h1, h1 + 8192};
  P[4] = (u64x2){__float_as_uint(g_sc[4]) | (uint64_t)((e1 - e) * 1024 + (e & 7) * 128) << 32, 0};
  P[5] = (u64x2){(uint64_t)x + p0, (uint64_t)x + p1};
  P[6] = (u64x2){(uint64_t)out + p0, (uint64_t)out + p1};
  P[7] = (u64x2){0x7fffffffffffffull, (uint64_t)g_buf + G_FLAG};
  P[8] = (u64x2){(uint64_t)g_w2f + w * 6144, (uint64_t)g_w2q + w * 6144};
  P[9] = (u64x2){(uint64_t)g_w2r + e * 1024, (uint64_t)g_w2f + e * 1024};
  P[10] = (u64x2){(uint64_t)grn_g + w * 256, (uint64_t)g_cs + w * 384 + 2048};
  P[11] = (u64x2){(uint64_t)g_buf + G_PUB + b * 256, (uint64_t)g_buf + G_PUB + w * 16384};
  P[12] = (u64x2){cso(e) | (uint64_t)(cso(e1) + (e & 1) * 32) << 32, (uint64_t)g_cs};
  P[13] = (u64x2){sc[3] | (uint64_t)1 << (32 + (b >> 3) % K2_NAT), 0};
}
__global__ __launch_bounds__(256, 1) void k1_fc1(const u16* __restrict__ x, char* __restrict__ hws, const u16* __restrict__ out, const u16* __restrict__ grn_g) {
  extern __shared__ __attribute__((aligned(16))) char smem[];
  const unsigned w = __builtin_amdgcn_readfirstlane(threadIdx.x >> 6);
  if (blockIdx.x < 8) {
    if (w < 2) k2_params(x, hws, out, grn_g, w * 64 + __builtin_amdgcn_mbcnt_hi(~0u, __builtin_amdgcn_mbcnt_lo(~0u, 0u)));
    return;
  }
  const unsigned lds0 = lds_addr(smem);
  const unsigned b = blockIdx.x - 8;
  const unsigned dir = (b >> 3) & 1;
  TS(0); MK_START(1);
  const uint64_t xp = (uint64_t)x, hp = (uint64_t)hws, gp = (uint64_t)g_buf, wp = gp + w * K1_WCH;
  const unsigned d = dir ? ~0u : 1u, wrap = dir ? ~0u : 48u, rst = dir ? 47u : 0u, pos0 = dir ? K1_SMAX - 1 : 0u;
  const unsigned xo0 = (12 * ((b >> 3) * 4 + (b & 3)) + w) * 49152, xs1 = (1536 + w + (dir ? 8 : 0)) * 49152, sb1 = 1536 * w + (dir ? K1_SB1 : 0);
  const unsigned r0o = ((b >> 3) * 4 + (b & 3)) * H_RS, tsel = 4 * (b & 3), s2 = (b >> 2) & 1;
  const uint64_t ap = gp + K1_G_ACC + (b % K1_NREP) * 12288;
  const unsigned ca0 = w == 0 ? dir * 64 : K1_G_DUM - K1_G_CNT + (4 * b + w) * 64;
  const unsigned pm = lds0 + K1_PF_OFF, pf = ((b >> 3) * 4 + w) * 1024;
  unsigned nb;
  asm volatile(
R"(.macro M t,f,w
.if \f < 30
v_mfma_f32_16x16x32_f16 v[56+4*\t:56+4*\t+3], v[128+4*\f:128+4*\f+3], a[216+4*\w:216+4*\w+3], v[56+4*\t:56+4*\t+3]
.else
v_mfma_f32_16x16x32_f16 v[56+4*\t:56+4*\t+3], a[4*\f-4*30:4*\f-4*30+3], a[216+4*\w:216+4*\w+3], v[56+4*\t:56+4*\t+3]
.endif
.endm
.macro Z t,f,w
.if \f < 30
v_mfma_f32_16x16x32_f16 v[56+4*\t:56+4*\t+3], v[128+4*\f:128+4*\f+3], a[216+4*\w:216+4*\w+3], v[252:255]
.else
v_mfma_f32_16x16x32_f16 v[56+4*\t:56+4*\t+3], a[4*\f-4*30:4*\f-4*30+3], a[216+4*\w:216+4*\w+3], v[252:255]
.endif
.endm
.macro LZ a,f
.if \f < 30
v_mfma_f32_16x16x32_bf16 v[36:36+3], v[\a:\a+3], v[128+4*\f:128+4*\f+3], 0
.else
v_mfma_f32_16x16x32_bf16 v[36:36+3], v[\a:\a+3], a[4*\f-4*30:4*\f-4*30+3], 0
.endif
.endm
.macro LS a,f
.if \f < 30
v_mfma_f32_16x16x32_bf16 v[36:36+3], v[\a:\a+3], v[128+4*\f:128+4*\f+3], v[36:36+3]
.else
v_mfma_f32_16x16x32_bf16 v[36:36+3], v[\a:\a+3], a[4*\f-4*30:4*\f-4*30+3], v[36:36+3]
.endif
.endm
.macro LD d,a,f
.if \f < 30
v_mfma_f32_16x16x32_bf16 v[\d:\d+3], a[\a:\a+3], v[128+4*\f:128+4*\f+3], v[36:36+3]
.else
v_mfma_f32_16x16x32_bf16 v[\d:\d+3], a[\a:\a+3], a[4*\f-4*30:4*\f-4*30+3], v[36:36+3]
.endif
.endm
.macro GZ f
.if \f < 30
v_mfma_f32_16x16x32_f16 v[52:55], v[128+4*\f:128+4*\f+3], v[128+4*\f:128+4*\f+3], 0
.else
v_mfma_f32_16x16x32_f16 v[52:55], a[4*\f-4*30:4*\f-4*30+3], a[4*\f-4*30:4*\f-4*30+3], 0
.endif
.endm
.macro GS f
.if \f < 30
v_mfma_f32_16x16x32_f16 v[52:55], v[128+4*\f:128+4*\f+3], v[128+4*\f:128+4*\f+3], v[52:55]
.else
v_mfma_f32_16x16x32_f16 v[52:55], a[4*\f-4*30:4*\f-4*30+3], a[4*\f-4*30:4*\f-4*30+3], v[52:55]
.endif
.endm
.macro CL p,z,s
v_fma_mixlo_f16 v[\p], v[\z], v[\s], 0 op_sel_hi:[0,1,0]
.endm
.macro CH p,z,s
v_fma_mixhi_f16 v[\p], v[\z], v[\s], 0 op_sel:[0,1,0] op_sel_hi:[0,1,0]
.endm
.macro XL f,o
.if \f < 30
global_load_dwordx4 v[128+4*\f:128+4*\f+3], v3, s[46:47] offset:\o
.else
global_load_dwordx4 a[4*\f-4*30:4*\f-4*30+3], v3, s[46:47] offset:\o
.endif
.endm
.macro XN f,o
.if \f < 30
global_load_dwordx4 v[128+4*\f:128+4*\f+3], v3, s[46:47] offset:\o nt
.else
global_load_dwordx4 a[4*\f-4*30:4*\f-4*30+3], v3, s[46:47] offset:\o nt
.endif
.endm
.macro F t,z
v_fma_f32 v[\t], -v[\z], v[\z], s36
.endm
.macro U t,z
v_mul_f32 v[\t], v[\z], v[\t]
.endm
.macro E t
v_exp_f32 v[\t], v[\t]
.endm
.macro A t
v_add_f32 v[\t], 1.0, v[\t]
.endm
.macro R t
v_rcp_f32 v[\t], v[\t]
.endm
.macro C d,a,b
v_cvt_pk_bf16_f32 v[\d], v[\a], v[\b]
.endm
.macro S0 q,t
v_mul_f32 v[\q], v[\t], v[\t]
.endm
.macro S q,t
v_fmac_f32 v[\q], v[\t], v[\t]
.endm
.macro HC d,z
v_cvt_pk_f16_f32 v[\d], v[\z], v[\z+1]
.endm
.macro HF t,p
v_pk_fma_f16 v[\t], v[\p], v[\p], s36 neg_lo:[1,0,0] neg_hi:[1,0,0]
.endm
.macro HM t,p
v_pk_mul_f16 v[\t], v[\p], v[\t]
.endm
.macro HE d,s
v_exp_f16 v[\d], v[\s]
.endm
.macro HG d,s
v_exp_f16_sdwa v[\d], v[\s] dst_sel:WORD_1 dst_unused:UNUSED_PRESERVE src0_sel:WORD_1
.endm
.macro HA t
v_pk_add_f16 v[\t], v[\t], 1.0 op_sel_hi:[1,0]
.endm
.macro HR d,s
v_rcp_f16 v[\d], v[\s]
.endm
.macro HQ d,s
v_rcp_f16_sdwa v[\d], v[\s] dst_sel:WORD_1 dst_unused:UNUSED_PRESERVE src0_sel:WORD_1
.endm
.macro HP d,s
v_rcp_f16_sdwa v[\d], v[\s] dst_sel:DWORD dst_unused:UNUSED_PAD src0_sel:WORD_1
.endm
.macro HY t,z
v_fma_mix_f32 v[\t], v[\z], v[\t], 0 op_sel_hi:[0,1,0]
.endm
.macro HK p
v_and_b32 v[\p], s56, v[\p]
.endm
.macro LQ p
v_lshrrev_b32 v[\p], 2, v[\p]
.endm
.macro LL t,z
v_lshlrev_b32_sdwa v[\t], 1, v[\z] dst_sel:DWORD dst_unused:UNUSED_PAD src0_sel:DWORD src1_sel:WORD_1
.endm
.macro LH t,p
v_and_b32_sdwa v[\t], s56, v[\p] dst_sel:DWORD dst_unused:UNUSED_PAD src0_sel:DWORD src1_sel:WORD_1
.endm
.macro LA t
ds_read_u16 v[\t], v[\t] offset:5376
.endm
.macro LC_ t
ds_read_u16_d16_hi v[\t], v[\t] offset:5376
.endm
.macro LO p,a,b
v_bfi_b32 v[\p], s36, v[\a], v[\b]
.endm
.macro HZ q,p
v_pk_mul_f16 v[\q], v[\p], v[\p]
.endm
.macro HS q,p
v_pk_fma_f16 v[\q], v[\p], v[\p], v[\q]
.endm
.macro HT q,p
v_fma_mix_f32 v[\q], v[\p], v[\p], v[\q] op_sel:[1,1,0] op_sel_hi:[1,1,0]
.endm
.macro HX q
v_fma_mix_f32 v[\q], v[\q], 1.0, v[\q] op_sel:[0,0,1] op_sel_hi:[1,0,1]
.endm
.macro UX t,z
v_mul_f32_e64 v[\t], v[\z], v[\t]
.endm
.macro EX t
v_exp_f32_e64 v[\t], v[\t]
.endm
.macro AX t
v_add_f32_e64 v[\t], 1.0, v[\t]
.endm
.macro RX t
v_rcp_f32_e64 v[\t], v[\t]
.endm
.macro TX q,t
v_mul_f32_e64 v[\q], v[\t], v[\t]
.endm
.macro SX q,t
v_fmac_f32_e64 v[\q], v[\t], v[\t]
.endm
.macro AW a,s
v_accvgpr_write_b32 a[\a], v[\s]
.endm
.macro RA d,v,o
ds_read_b128 a[\d:\d+3], v[\v] offset:\o
.endm
.macro RV d,v,o
ds_read_b128 v[\d:\d+3], v[\v] offset:\o
.endm
.macro W2 a,b,o
ds_write2st64_b32 v4, v[\a], v[\b] offset0:\o offset1:\o+4
.endm
.macro W1 a,o
ds_write_b32 v4, v[\a] offset:\o
.endm
.macro DW o
global_load_lds_dwordx4 v0, s[40:41] offset:\o
.endm
.macro DF
global_load_lds_dwordx4 v0, s[70:71]
.endm
.macro DX
global_load_lds_dword v3, s[46:47]
.endm
.macro ST d
.if ((\d - 104) / 4) & 1
global_store_dwordx4 v2, v[\d:\d+3], s[42:43] nt sc1
.else
global_store_dwordx2 v31, v[\d:\d+1], s[42:43] nt sc1
.endif
.endm
.macro SU d
.if ((\d - 104) / 4) & 1
global_store_dwordx4 v2, v[\d:\d+3], s[42:43]  sc1
.else
global_store_dwordx2 v31, v[\d:\d+1], s[42:43]  sc1
.endif
.endm
.macro SP
global_atomic_add_f64 v6, v[14:15], s[48:49]
.endm
.macro AT
global_atomic_add v11, v9, v10, s[68:69] sc0
.endm
.macro PFA
s_getpc_b64 s[70:71]
s_add_u32 s70, s70, 7f-.
s_addc_u32 s71, s71, 0
s_andn2_b32 s70, s70, 0x7f
s_min_u32 s53, %[pf], 8f-7f-1152
s_andn2_b32 s53, s53, 0x7f
s_add_u32 s70, s70, s53
s_addc_u32 s71, s71, 0
.endm
.macro PFN
s_getpc_b64 s[70:71]
s_add_u32 s70, s70, k2_code@rel32@lo-1020
s_addc_u32 s71, s71, k2_code@rel32@hi-1012
s_add_u32 s70, s70, %[pf]
s_addc_u32 s71, s71, 0
s_andn2_b32 s70, s70, 0x7f
.endm
.macro PFW
s_getpc_b64 s[70:71]
s_add_u32 s70, s70, g_w2q@rel32@lo+4
s_addc_u32 s71, s71, g_w2q@rel32@hi+12
s_add_u32 s70, s70, %[pf]
s_addc_u32 s71, s71, 0
.endm
.macro PF0
s_getpc_b64 s[70:71]
s_andn2_b32 s70, s70, 0x7f
s_min_u32 s53, %[pf], 7f-.-1152
s_andn2_b32 s53, s53, 0x7f
s_add_u32 s70, s70, s53
s_addc_u32 s71, s71, 0
.endm
.macro WL n
s_waitcnt lgkmcnt(\n)
.endm
.macro WV n
s_waitcnt vmcnt(\n)
.endm
.macro LU t,p
v_pk_fma_f16 v[\t], v[\p], v34, s67
.endm
.macro CF d,a,b
v_cvt_pk_f16_f32 v[\d], v[\a], v[\b]
.endm
.macro XS d
v_pk_fma_f16 v[\d], v[\d], v13, s56
.endm
.macro AR d,a
v_accvgpr_read_b32 v[\d], a[\a]
.endm
.macro DQ o
global_load_lds_dword v23, s[40:41] offset:\o
.endm
.macro GE
v_cmp_eq_u32 vcc, 0, v35
s_nop 4
s_cbranch_vccz 9f
v_mov_b32 v35, 14720
s_mov_b64 exec, 1
global_store_dword v35, v10, s[68:69] sc1
s_mov_b64 exec, -1
9:
.endm
.macro BW f
.if \f < 30
ds_write_b128 v12, v[128+4*\f:128+4*\f+3]
.else
ds_write_b128 v12, a[4*\f-120:4*\f-120+3]
.endif
.endm
.macro BR f
.if \f < 30
ds_read_b128 v[128+4*\f:128+4*\f+3], v20
.else
ds_read_b128 a[4*\f-120:4*\f-120+3], v20
.endif
.endm
.macro LK d,a,f,h
.if \f < 30
v_mfma_f32_4x4x4_16b_bf16 v[\d:\d+3], a[\a:\a+1], v[128+4*\f+\h:128+4*\f+\h+1], v[36:39]
.else
v_mfma_f32_4x4x4_16b_bf16 v[\d:\d+3], a[\a:\a+1], a[4*\f-120+\h:4*\f-120+\h+1], v[36:39]
.endif
.endm
.macro PK d,a,b,s
v_perm_b32 v[\d], v[\a], v[\b], v[\s]
.endm
.macro PO d
v_lshl_or_b32 v[\d], v[\d], 4, v[\d-4]
.endm
.macro LG a,b
v_cmp_eq_f32 vcc, v[\a], v[\b]
v_mov_b32 v[\b], 0x7f800000
s_nop 0
v_cndmask_b32 v[\a], v[\b], v[\a], vcc
.endm
.macro DY
s_mov_b32 s72, m0
s_mov_b32 m0, %[pm]
s_nop 0
global_load_lds_dword v3, s[46:47]
s_mov_b32 m0, s72
s_nop 0
.endm
.macro FP
ds_write_b32 v6, v19 offset:61440
.endm
.macro FR
ds_read_b32 v15, v31 offset:61440
.endm
k1_code:
v_mbcnt_lo_u32_b32 v16, -1, 0
v_mbcnt_hi_u32_b32 v16, -1, v16
v_lshlrev_b32 v0, 4, v16
v_and_b32 v17, 15, v16
v_lshrrev_b32 v18, 4, v16
s_add_u32 s68, %[glo], 1239040
s_addc_u32 s69, %[ghi], 0
s_mov_b32 s53, %[r0o]
s_load_dword s67, s[68:69], 16320
s_add_u32 s82, %[hlo], s53
s_addc_u32 s83, %[hhi], 0
s_add_u32 s84, s82, 8192
s_addc_u32 s85, s83, 0
s_and_b32 s90, %[hlo], 0x1fffff
s_add_u32 s90, s90, s53
s_mov_b32 s91, 3072
v_mov_b32 v11, s90
v_mad_u32_u24 v11, v0, s91, v11
v_lshrrev_b32 v11, 21, v11
v_lshl_add_u32 v11, v11, 2, 64
v_writelane_b32 v11, %[tsel], 63
s_sub_u32 s53, s82, %[hlo]
s_lshr_b32 s53, s53, 2
s_and_b32 s90, %[xlo], 0x1fffff
s_add_u32 s53, s53, s90
s_add_u32 s53, s53, 0x20048000
s_lshr_b32 s53, s53, 21
s_lshl_b32 s90, s53, 2
v_writelane_b32 v11, s90, 48
global_load_dword v11, v11, s[68:69] offset:-2048
v_lshlrev_b32 v19, 1, v18
s_movk_i32 s53, 0x78
v_bfe_u32 v21, v16, 2, 2
v_lshrrev_b32 v19, v19, s53
v_mul_u32_u24 v21, 0x300, v21
v_xor_b32 v19, v19, v16
v_and_b32 v19, 3, v19
v_lshl_add_u32 v21, v19, 4, v21
s_mov_b32 s97, 3072
s_mov_b32 s50, %[pos0]
s_mov_b32 s51, %[pos0]
s_mov_b32 s52, %[pos0]
s_add_u32 s74, %[lds], 0
s_add_u32 s75, s74, 163840
s_mov_b32 s37, s74
s_mul_i32 s53, %[w], 6400
s_add_u32 s76, s74, s53
s_add_u32 s77, s76, 163840
s_mov_b32 s38, s76
s_mul_i32 s60, %[w], 1536
s_add_u32 s61, s60, 512
s_add_u32 s62, s60, 16384
s_add_u32 s63, s60, 16896
s_add_u32 s64, s60, 32768
s_add_u32 s65, s60, 33280
v_lshlrev_b32 v23, 2, v16
s_lshl_b32 s53, %[w], 11
s_min_u32 s53, s53, 5120
s_add_u32 s72, s53, 1228800
s_add_u32 s40, %[glo], s72
s_addc_u32 s41, %[ghi], 0
s_add_u32 s53, s53, %[lds]
s_add_u32 m0, s53, 58368
s_nop 0
DW 0
DW 1024
s_nop 0 ;PS
s_mul_i32 s53, s50, 25600
s_add_u32 s40, %[wlo], s53
s_addc_u32 s41, %[whi], 0
)"
R"(s_mov_b32 m0, s38
s_nop 0
DW 0
DW 1024
DW 2048
s_add_u32 s40, s40, 3072
s_addc_u32 s41, s41, 0
s_add_u32 m0, s38, 3072
s_nop 0
DW 0
DW 1024
DW 2048
DQ 3072
s_add_u32 s50, s50, %[d]
s_cmp_eq_u32 s50, %[wrap]
s_cselect_b32 s50, %[rst], s50
s_add_u32 s38, s38, 32768
s_cmp_eq_u32 s38, s77
s_cselect_b32 s38, s76, s38
v_add_u32 v1, s37, v0
v_lshl_add_u32 v19, v18, 2, v17
v_and_b32 v19, 15, v19
v_lshlrev_b32 v19, 4, v19
v_lshl_add_u32 v2, v18, 8, v19
s_lshl_b32 s53, %[w], 2
s_add_u32 s53, s53, %[lds]
s_add_u32 s53, s53, 123904
s_add_u32 s53, %[lds], 123904
v_bfe_u32 v4, v17, 1, 2
v_and_b32 v5, 8, v17
v_add3_u32 v4, v4, v5, %[w]
v_lshl_add_u32 v4, v18, 2, v4
v_and_b32 v4, 15, v4
v_lshl_add_u32 v4, v17, 4, v4
v_lshl_add_u32 v4, v4, 2, s53
s_lshl_b32 s53, %[w], 9
s_add_u32 s53, s53, %[lds]
s_add_u32 s53, s53, 123904
v_lshlrev_b32 v5, 3, v16
v_add_u32 v5, s53, v5
s_lshl_b32 s53, %[w], 6
v_and_b32 v6, 0x38, v16
v_add_u32 v6, s53, v6
s_add_u32 s53, %[lds], 58368
v_and_b32 v19, 3, v16
v_lshl_add_u32 v19, v18, 2, v19
v_lshl_add_u32 v7, v19, 4, s53
v_lshlrev_b32 v8, 4, v18
v_add_u32 v8, s53, v8
v_mov_b32 v10, 1
s_mov_b32 s36, 0xffff
s_mov_b32 s56, 0x5550555
s_mov_b32 s39, 32768
s_mov_b32 s54, 0x01010101
s_mov_b32 s55, 0x01010101
s_mov_b32 %[nb], 0
v_and_b32 v28, 12, v17
v_lshl_add_u32 v28, v28, 2, v17
v_lshlrev_b32 v28, 2, v28
v_mov_b32 v34, 0x74007400
v_mov_b32 v35, -1
v_and_b32 v22, 0xfffffcff, v1
v_lshrrev_b32 v31, 1, v2
v_mov_b32 v32, 0x06020400
v_mov_b32 v33, 0x07030501
s_waitcnt lgkmcnt(0)
s_mov_b32 s78, 0xaaaaaaaa
s_mov_b32 s79, 0xaaaaaaaa
s_mov_b32 s80, 0xcccccccc
s_mov_b32 s81, 0xcccccccc
s_and_b32 s53, %[w], 1
s_lshl_b32 s53, s53, 10
s_lshr_b32 s72, %[w], 1
s_mul_i32 s72, s72, 32768
s_add_u32 s72, s72, s53
s_add_u32 s72, s72, 128000
s_movk_i32 s53, 0x78
v_lshrrev_b32 v12, 5, v0
v_and_b32 v12, 6, v12
v_bfe_u32 v12, s53, v12, 2
v_lshrrev_b32 v20, 8, v0
v_xor_b32 v20, v12, v20
v_lshl_add_u32 v20, v20, 4, s72
v_and_b32 v12, 0xf0, v0
v_lshl_add_u32 v20, v12, 2, v20
v_add_u32 v12, s72, v0
WV 9
v_bcnt_u32_b32 v11, v11, 0
v_cmp_lt_u32 s[90:91], 2, v11
s_nop 0
s_lshr_b32 s96, s91, 31
s_xor_b32 s96, s96, %[s2]
s_sub_u32 s53, 0, s96
s_xor_b32 s90, s90, s53
s_xor_b32 s91, s91, s53
s_add_u32 s53, %[xo0], 0
s_add_u32 s46, %[xlo], s53
s_addc_u32 s47, %[xhi], 0
s_bitcmp1_b64 s[90:91], 48
s_cselect_b32 s58, 16, 0
s_lshr_b32 s58, 0xfe987610, s58
v_lshrrev_b32 v3, 6, v0
v_and_b32 v3, 12, v3
v_bfe_u32 v3, s58, v3, 4
v_mad_u32_u24 v3, v3, s97, v21
XL 12,0
XL 13,64
XL 14,128
XL 15,192
XL 16,256
XL 17,320
XL 18,384
XL 19,448
XL 20,512
XL 21,576
XL 22,640
XL 23,704
s_mul_i32 s53, s96, 8320
s_cmp_lt_u32 %[ca0], 128
s_cselect_b32 s53, s53, 0
s_add_u32 s73, s53, %[ca0]
v_mov_b32 v9, s73
WV 19
s_barrier
v_lshrrev_b32 v19, 8, v31
v_sub_u32 v19, 0, v19
ds_write_b32 v31, v19 offset:61440
s_nop 1
s_nop 0
v_mov_b32 v19, 0
WV 11
BW 12
RV 40,8,6144
RV 44,8,6208
RV 48,8,6272
RV 248,8,6336
BR 12
WV 10
BW 13
s_nop 1
BR 13
WL 2
LZ 40,12
WV 9
BW 14
RV 40,8,6400
s_nop 0
BR 14
s_add_u32 s53, %[xo0], 0
s_add_u32 s46, %[xlo], s53
s_addc_u32 s47, %[xhi], 0
s_bitcmp1_b64 s[90:91], 48
s_cselect_b32 s58, 16, 0
s_lshr_b32 s58, 0xda43cb52, s58
v_lshrrev_b32 v3, 6, v0
v_and_b32 v3, 12, v3
v_bfe_u32 v3, s58, v3, 4
v_mad_u32_u24 v3, v3, s97, v21
WL 3
LS 44,13
WV 8
BW 15
RV 44,8,6464
s_nop 0
BR 15
XL 24,0
WL 3
LS 48,14
WV 8
BW 16
RV 48,8,6528
s_nop 0
BR 16
WL 3
LS 248,15
WV 7
BW 17
RV 248,8,6592
s_nop 0
BR 17
XL 25,64
WL 3
LS 40,16
WV 7
BW 18
RV 40,8,6656
s_nop 0
BR 18
XL 26,128
WL 3
LS 44,17
WV 7
BW 19
RV 44,8,6720
s_nop 0
BR 19
WL 3
LS 48,18
WV 6
BW 20
RV 48,8,6784
s_nop 0
BR 20
XL 27,192
WL 3
LS 248,19
WV 6
BW 21
RV 248,8,6848
s_nop 0
BR 21
XL 28,256
WL 3
LS 40,20
WV 6
BW 22
RA 240,7,0
s_nop 0
BR 22
WL 3
LS 44,21
WV 5
BW 23
RA 244,7,256
s_nop 0
BR 23
XL 29,320
WL 3
LS 48,22
RA 248,7,512
XL 30,384
WL 1
LS 248,23
RA 252,7,768
s_nop 6
v_mul_f32 v36, 0xbb2aaaab, v36
v_mul_f32 v37, 0xbb2aaaab, v37
v_mul_f32 v38, 0xbb2aaaab, v38
v_mul_f32 v39, 0xbb2aaaab, v39
s_nop 1
LK 40,240,12,0
LK 44,242,12,2
RA 240,7,1024
XL 31,448
LK 48,244,13,0
s_nop 0
CF 176,40,41
LK 248,246,13,2
WL 2
RA 244,7,1280
CF 177,42,43
CF 178,44,45
CF 179,46,47
XL 32,512
LK 40,248,14,0
CF 180,48,49
LK 44,250,14,2
RA 248,7,1536
CF 181,50,51
GZ 12
WL 3
XL 33,576
CF 182,248,249
CF 183,250,251
LK 48,252,15,0
CF 184,40,41
LK 248,254,15,2
RA 252,7,1792
CF 185,42,43
XL 34,640
GS 13
WL 3
CF 186,44,45
CF 187,46,47
LK 40,240,16,0
CF 188,48,49
LK 44,242,16,2
WL 2
RA 240,7,2048
CF 189,50,51
XL 35,704
GS 14
s_mul_i32 s53, s50, 25600
s_add_u32 s40, %[wlo], s53
s_addc_u32 s41, %[whi], 0
s_mov_b32 m0, s38
CF 190,248,249
CF 191,250,251
LK 48,244,17,0
CF 192,40,41
LK 248,246,17,2
RA 244,7,2304
CF 193,42,43
GS 15
WL 3
DW 0
CF 194,44,45
CF 195,46,47
LK 40,248,18,0
CF 196,48,49
LK 44,250,18,2
RA 248,7,2560
CF 197,50,51
DW 1024
GS 16
WL 3
CF 198,248,249
CF 199,250,251
LK 48,252,19,0
CF 200,40,41
LK 248,254,19,2
WL 2
RA 252,7,2816
CF 201,42,43
DW 2048
GS 17
s_add_u32 s40, s40, 3072
s_addc_u32 s41, s41, 0
s_add_u32 m0, s38, 3072
CF 202,44,45
CF 203,46,47
LK 40,240,20,0
CF 204,48,49
LK 44,242,20,2
CF 205,50,51
GS 18
WL 2
DW 0
CF 206,248,249
CF 207,250,251
LK 48,244,21,0
CF 208,40,41
LK 248,246,21,2
CF 209,42,43
DW 1024
GS 19
WL 1
CF 210,44,45
CF 211,46,47
LK 40,248,22,0
CF 212,48,49
LK 44,250,22,2
CF 213,50,51
DW 2048
GS 20
WL 0
DQ 3072
CF 214,248,249
CF 215,250,251
LK 48,252,23,0
CF 216,40,41
LK 248,254,23,2
CF 217,42,43
GS 21
s_add_u32 s50, s50, %[d]
s_cmp_eq_u32 s50, %[wrap]
s_cselect_b32 s50, %[rst], s50
CF 218,44,45
CF 219,46,47
RV 36,8,6912
s_nop 0
GS 22
s_add_u32 s38, s38, 32768
s_cmp_eq_u32 s38, s77
s_cselect_b32 s38, s76, s38
CF 220,48,49
CF 221,50,51
CF 222,248,249
CF 223,250,251
s_nop 1
GS 23
s_nop 7
v_cndmask_b32 v52, v52, v53, s[78:79]
v_cndmask_b32 v54, v54, v55, s[78:79]
v_cndmask_b32 v52, v52, v54, s[80:81]
v_mul_f32 v52, 0x3b2aaaab, v52
WL 0
v_med3_f32 v53, v52, v39, v37
LG 52,53
s_nop 0
C 52,52,52
v_lshlrev_b32 v52, 16, v52
v_add_f32 v52, v36, v52
C 52,52,52
v_lshlrev_b32 v52, 16, v52
v_sqrt_f32 v52, v52
s_nop 0
C 52,52,52
v_lshlrev_b32 v52, 16, v52
v_rcp_f32 v52, v52
s_nop 0
v_mul_f32 v52, v38, v52
CF 52,52,52
ds_bpermute_b32 v13, v28, v52
WV 18
BW 24
RV 40,8,6144
RV 44,8,6208
RV 48,8,6272
BR 24
)"
R"(WV 17
BW 25
s_nop 1
BR 25
WL 7
XS 176
WL 2
LZ 40,24
s_and_b32 s53, %[w], 1
s_mul_i32 s53, s53, 3072
s_lshr_b32 s72, %[w], 1
s_lshl_b32 s40, s72, 16
s_add_u32 s40, s40, s53
s_add_u32 m0, s40, 25600
s_lshl_b32 s72, s72, 13
s_add_u32 s53, s53, s72
s_add_u32 s53, s53, 1382400
s_add_u32 s40, %[glo], s53
s_addc_u32 s41, %[ghi], 0
XS 177
WV 16
BW 26
XS 178
RV 40,8,6336
XS 179
BR 26
XS 180
WL 3
LS 44,25
WV 15
DW 0
XS 181
BW 27
XS 182
RV 44,8,6400
XS 183
BR 27
v_min_u32 v35, v35, v13
WL 3
LS 48,26
WV 15
DW 1024
XS 184
BW 28
XS 185
RV 48,8,6464
XS 186
BR 28
XS 187
WL 3
LS 40,27
WV 15
XS 188
BW 29
XS 189
RV 40,8,6528
XS 190
XS 191
BR 29
XS 192
WL 3
LS 44,28
WV 14
DW 2048
XS 193
BW 30
XS 194
RV 44,8,6592
XS 195
BR 30
XS 196
WL 3
LS 48,29
WV 14
DW 3072
XS 197
BW 31
XS 198
RV 48,8,6656
XS 199
BR 31
XS 200
WL 3
LS 40,30
WV 13
XS 201
BW 32
XS 202
RV 40,8,6720
XS 203
BR 32
XS 204
WL 3
LS 44,31
PF0
s_mov_b32 m0, %[pm]
s_nop 0
DF
PFA
XS 205
BW 33
XS 206
XS 207
RV 44,8,6784
XS 208
BR 33
XS 209
WL 3
LS 48,32
s_mul_i32 s53, s50, 25600
s_add_u32 s40, %[wlo], s53
s_addc_u32 s41, %[whi], 0
s_mov_b32 m0, s38
XS 210
WV 13
BW 34
XS 211
RV 48,8,6848
XS 212
RA 240,7,0
XS 213
BR 34
XS 214
WL 4
LS 40,33
WV 12
XS 215
BW 35
XS 216
RA 244,7,256
XS 217
BR 35
XS 218
WL 3
LS 44,34
DW 0
XS 219
RA 248,7,512
XS 220
WL 1
LS 48,35
DW 1024
XS 221
RA 252,7,768
XS 222
XS 223
s_nop 2
v_mul_f32 v36, 0xbb2aaaab, v36
v_mul_f32 v37, 0xbb2aaaab, v37
v_mul_f32 v38, 0xbb2aaaab, v38
v_mul_f32 v39, 0xbb2aaaab, v39
s_nop 1
LK 40,240,24,0
LK 44,242,24,2
RA 240,7,1024
LK 48,244,25,0
s_nop 1
CF 224,40,41
LK 248,246,25,2
WL 2
RA 244,7,1280
CF 225,42,43
CF 226,44,45
CF 227,46,47
DW 2048
LK 40,248,26,0
CF 228,48,49
LK 44,250,26,2
RA 248,7,1536
CF 229,50,51
s_add_u32 s40, s40, 3072
s_addc_u32 s41, s41, 0
s_add_u32 m0, s38, 3072
GZ 24
WL 3
CF 230,248,249
CF 231,250,251
LK 48,252,27,0
CF 232,40,41
LK 248,254,27,2
RA 252,7,1792
CF 233,42,43
DW 0
GS 25
WL 3
DW 1024
CF 234,44,45
CF 235,46,47
LK 40,240,28,0
CF 236,48,49
LK 44,242,28,2
RA 240,7,2048
CF 237,50,51
GS 26
WL 3
DW 2048
CF 238,248,249
CF 239,250,251
LK 48,244,29,0
CF 240,40,41
LK 248,246,29,2
RA 244,7,2304
CF 241,42,43
DQ 3072
GS 27
WL 3
CF 242,44,45
CF 243,46,47
LK 40,248,30,0
CF 244,48,49
LK 44,250,30,2
WL 2
RA 248,7,2560
CF 245,50,51
s_add_u32 s50, s50, %[d]
s_cmp_eq_u32 s50, %[wrap]
s_cselect_b32 s50, %[rst], s50
GS 28
s_add_u32 s38, s38, 32768
s_cmp_eq_u32 s38, s77
s_cselect_b32 s38, s76, s38
CF 246,248,249
CF 247,250,251
LK 48,252,31,0
CF 152,40,41
LK 248,254,31,2
WL 2
RA 252,7,2816
CF 153,42,43
GS 29
s_add_u32 s53, %[xo0], 196608
s_add_u32 s46, %[xlo], s53
s_addc_u32 s47, %[xhi], 0
s_bitcmp1_b64 s[90:91], 48
s_cselect_b32 s58, 16, 0
s_lshr_b32 s58, 0xfe987610, s58
v_lshrrev_b32 v3, 6, v0
v_and_b32 v3, 12, v3
v_bfe_u32 v3, s58, v3, 4
v_mad_u32_u24 v3, v3, s97, v21
CF 154,44,45
CF 155,46,47
LK 40,240,32,0
CF 156,48,49
LK 44,242,32,2
CF 157,50,51
XL 36,0
GS 6
WL 2
CF 158,248,249
CF 159,250,251
LK 48,244,33,0
CF 160,40,41
LK 248,246,33,2
CF 161,42,43
XL 37,64
GS 7
WL 1
XL 38,128
CF 162,44,45
CF 163,46,47
LK 40,248,34,0
CF 164,48,49
LK 44,250,34,2
CF 165,50,51
GS 8
WL 0
XL 39,192
CF 166,248,249
CF 167,250,251
LK 48,252,35,0
CF 168,40,41
LK 248,254,35,2
CF 169,42,43
XL 40,256
GS 9
CF 170,44,45
CF 171,46,47
RV 36,8,6912
s_nop 0
GS 10
XL 41,320
CF 172,48,49
CF 173,50,51
CF 174,248,249
CF 175,250,251
s_nop 1
GS 11
XL 42,384
WV 15
s_barrier
RA 216,1,0
RA 220,1,1024
RA 224,1,2048
RA 228,1,3072
RV 252,22,5120
v_cndmask_b32 v52, v52, v53, s[78:79]
v_cndmask_b32 v54, v54, v55, s[78:79]
v_cndmask_b32 v52, v52, v54, s[80:81]
v_mul_f32 v52, 0x3b2aaaab, v52
WL 5
v_med3_f32 v53, v52, v39, v37
LG 52,53
s_nop 0
C 52,52,52
v_lshlrev_b32 v52, 16, v52
v_add_f32 v52, v36, v52
C 52,52,52
XL 43,448
v_lshlrev_b32 v52, 16, v52
v_sqrt_f32 v52, v52
s_nop 0
C 52,52,52
v_lshlrev_b32 v52, 16, v52
v_rcp_f32 v52, v52
XL 44,512
v_mul_f32 v52, v38, v52
CF 52,52,52
ds_bpermute_b32 v13, v28, v52
WL 0
XS 224
XS 225
XS 226
XS 227
XS 228
XS 229
XS 230
XL 45,576
XS 231
v_min_u32 v35, v35, v13
XS 232
XS 233
XS 234
XL 46,640
XS 235
XS 236
XS 237
XS 238
XS 239
XL 47,704
Z 0,12,0
XS 240
XS 241
XS 242
XS 243
XS 244
XS 245
XS 246
XS 247
XS 152
XS 153
XS 154
XS 155
M 0,13,1
s_mul_i32 s53, s50, 25600
RA 232,1,4096
AW 0,152
AW 1,153
Z 1,24,0
s_add_u32 s40, %[wlo], s53
AW 2,154
AW 3,155
M 1,25,1
s_addc_u32 s41, %[whi], 0
RA 236,1,6144
XS 156
XS 157
M 0,14,2
s_mov_b32 m0, s38
s_nop 0
DW 0
XS 158
XS 159
AW 4,156
M 0,15,3
RA 216,1,7168
AW 5,157
AW 6,158
M 1,26,2
DW 1024
AW 7,159
XS 160
M 1,27,3
WL 2
RA 220,1,8192
XS 161
XS 162
M 0,16,4
WL 2
XS 163
AW 8,160
M 0,17,5
RA 224,1,9216
AW 9,161
AW 10,162
AW 11,163
M 1,28,4
WL 1
DW 2048
XS 164
)"
R"(XS 165
M 1,29,5
s_add_u32 s40, s40, 3072
RA 228,1,10240
XS 166
XS 167
M 0,18,0
s_addc_u32 s41, s41, 0
AW 12,164
AW 13,165
M 0,19,1
s_add_u32 m0, s38, 3072
RA 232,1,11264
AW 14,166
AW 15,167
M 1,30,0
RV 252,22,5392
XS 168
XS 169
M 1,31,1
WL 3
RA 236,1,12288
XS 170
XS 171
AW 16,168
M 0,20,2
WL 3
DW 0
AW 17,169
AW 18,170
M 0,21,3
RA 216,1,13312
AW 19,171
XS 172
M 1,32,2
DW 1024
XS 173
XS 174
M 1,33,3
WL 3
RA 220,1,14336
XS 175
AW 20,172
M 0,22,4
WL 2
DW 2048
AW 21,173
AW 22,174
AW 23,175
M 0,23,5
RA 224,1,15360
M 1,34,4
DQ 3072
M 1,35,5
WL 1
RA 228,1,16384
Z 6,12,0
s_add_u32 s50, s50, %[d]
LL 16,56
LL 104,57
M 6,13,1
s_cmp_eq_u32 s50, %[wrap]
LA 16
RA 232,1,17408
Z 7,24,0
s_cselect_b32 s50, %[rst], s50
LC_ 104
M 7,25,1
s_add_u32 s38, s38, 32768
RA 236,1,18432
WL 5
M 6,14,2
s_cmp_eq_u32 s38, s77
WV 18
BW 36
s_cselect_b32 s38, s76, s38
WL 5
M 6,15,3
s_add_u32 s37, s37, 32768
RA 216,1,19456
RV 40,8,6144
M 7,26,2
s_cmp_eq_u32 s37, s75
LL 17,58
LL 105,59
M 7,27,3
s_cselect_b32 s37, s74, s37
LA 17
RA 220,1,20480
s_add_u32 s53, %[xo0], 196608
s_add_u32 s46, %[xlo], s53
s_addc_u32 s47, %[xhi], 0
s_bitcmp1_b64 s[90:91], 48
s_cselect_b32 s58, 16, 0
s_lshr_b32 s58, 0xda43cb52, s58
v_lshrrev_b32 v3, 6, v0
v_and_b32 v3, 12, v3
v_bfe_u32 v3, s58, v3, 4
RV 44,8,6208
RV 48,8,6272
v_mad_u32_u24 v3, v3, s97, v21
WL 9
M 6,16,4
WL 7
LC_ 105
XL 48,0
BR 36
M 6,17,5
s_mul_i32 s53, s51, 24
RA 224,1,21504
WV 18
BW 37
s_nop 1
BR 37
M 7,28,4
WV 17
LO 104,16,104
LU 16,104
M 7,29,5
s_bitcmp1_b32 s51, 0
RA 228,1,22528
HZ 24,16
WL 4
LZ 40,36
s_addc_u32 s53, s53, 0
BW 38
RV 40,8,6336
M 6,18,0
s_lshl_b32 s53, s53, 11
LL 18,60
LL 108,61
s_bitcmp1_b64 s[90:91], s51
s_cselect_b64 s[44:45], s[84:85], s[82:83]
s_add_u32 s44, s44, s53
s_addc_u32 s45, s45, 0
s_add_u32 s51, s51, %[d]
s_cmp_eq_u32 s51, %[wrap]
s_cselect_b32 s51, %[rst], s51
XL 49,64
BR 38
M 6,19,1
WL 4
LA 18
RA 232,1,23552
LS 44,37
WV 17
BW 39
M 7,30,0
LC_ 108
XL 50,128
RV 44,8,6400
M 7,31,1
RA 236,1,24576
BR 39
WL 7
LS 48,38
WV 17
BW 40
M 6,20,2
LO 105,17,105
LU 17,105
M 6,21,3
XL 51,192
RV 48,8,6464
BR 40
HS 24,17
M 7,32,2
LL 16,62
LL 109,63
v_add_u32 v1, s37, v0
M 7,33,3
WL 3
LA 16
RA 216,1,0
XL 52,256
LS 40,39
WV 18
BW 41
RV 40,8,6528
M 6,22,4
WL 4
LC_ 109
XL 53,320
v_and_b32 v22, 0xfffffcff, v1
BR 41
M 6,23,5
s_barrier
RA 220,1,1024
LS 44,40
WV 18
BW 42
M 7,34,4
RV 44,8,6592
LO 108,18,108
LU 18,108
M 7,35,5
RV 252,22,5120
BR 42
HS 24,18
WL 1
Z 0,12,0
WV 17
RA 224,1,2048
LL 17,80
LL 106,81
M 0,13,1
s_mul_i32 s53, s50, 25600
LA 17
RA 228,1,3072
LS 48,41
s_add_u32 s40, %[wlo], s53
BW 43
RV 48,8,6656
s_nop 0
BR 43
Z 1,24,0
s_addc_u32 s41, %[whi], 0
LC_ 106
RA 232,1,4096
M 1,25,1
s_mov_b32 m0, s38
RA 236,1,6144
WL 9
LS 40,42
WV 16
BW 44
RV 40,8,6720
WL 10
M 0,14,2
WL 8
BR 44
LO 109,16,109
LU 16,109
M 0,15,3
WL 6
RA 216,1,7168
HS 24,16
LS 44,43
WV 15
BW 45
M 1,26,2
DW 0
RV 44,8,6784
LL 18,82
LL 107,83
M 1,27,3
WL 3
LA 18
RA 220,1,8192
BR 45
LS 48,44
WV 15
BW 46
M 0,16,4
LC_ 107
DW 1024
M 0,17,5
RA 224,1,9216
RV 48,8,6848
RA 240,7,0
HX 24
M 1,28,4
LO 17,17,106
LU 106,17
W1 24,0
BR 46
M 1,29,5
WL 7
RA 228,1,10240
DW 2048
HZ 25,106
LS 40,45
WV 16
BW 47
M 0,18,0
s_add_u32 s40, s40, 3072
RA 244,7,256
PK 106,17,104,33
PK 104,17,104,32
LL 16,84
LL 110,85
M 0,19,1
s_addc_u32 s41, s41, 0
LA 16
RA 232,1,11264
BR 47
WL 6
LS 44,46
s_add_u32 m0, s38, 3072
RA 248,7,512
M 1,30,0
LC_ 110
RV 252,22,5392
M 1,31,1
RA 236,1,12288
WL 4
LS 48,47
RA 252,7,768
M 0,20,2
LO 18,18,107
LU 107,18
s_nop 3
v_mul_f32 v36, 0xbb2aaaab, v36
v_mul_f32 v37, 0xbb2aaaab, v37
v_mul_f32 v38, 0xbb2aaaab, v38
M 0,21,3
RA 216,1,13312
DW 0
HS 25,107
v_mul_f32 v39, 0xbb2aaaab, v39
s_nop 1
LK 40,240,36,0
LK 44,242,36,2
RA 240,7,1024
LK 48,244,37,0
s_nop 1
CF 128,40,41
LK 248,246,37,2
RA 244,7,1280
CF 129,42,43
M 1,32,2
PK 107,18,105,33
PK 105,18,105,32
LL 17,86
LL 111,87
M 1,33,3
WL 7
LA 17
RA 220,1,14336
DW 1024
CF 130,44,45
CF 131,46,47
LK 40,248,38,0
WL 5
CF 132,48,49
LK 44,250,38,2
s_add_u32 s42, s44, s60
RA 248,7,1536
CF 133,50,51
GZ 0
s_addc_u32 s43, s45, 0
CF 134,248,249
CF 135,250,251
LK 48,252,39,0
CF 136,40,41
LK 248,254,39,2
RA 252,7,1792
CF 137,42,43
GS 1
WL 5
CF 138,44,45
CF 139,46,47
LK 40,240,40,0
CF 140,48,49
LK 44,242,40,2
RA 240,7,2048
CF 141,50,51
GS 2
WL 5
CF 142,248,249
CF 143,250,251
LK 48,244,41,0
CF 144,40,41
LK 248,246,41,2
RA 244,7,2304
CF 145,42,43
GS 3
CF 146,44,45
CF 147,46,47
M 0,22,4
LC_ 111
ST 104
M 0,23,5
WL 4
RA 224,1,15360
LK 40,248,42,0
CF 148,48,49
LK 44,250,42,2
RA 248,7,2560
CF 149,50,51
GS 4
CF 150,248,249
CF 151,250,251
M 1,34,4
DW 2048
LO 16,16,110
LU 110,16
M 1,35,5
WL 5
RA 228,1,16384
HS 25,110
LK 48,252,43,0
CF 152,40,41
LK 248,254,43,2
RA 252,7,2816
CF 153,42,43
GS 5
CF 154,44,45
CF 155,46,47
Z 6,12,0
)"
R"(PK 110,16,108,33
PK 108,16,108,32
LL 18,56
LL 104,57
M 6,13,1
WL 6
LA 18
DQ 3072
PO 110
LK 40,240,44,0
CF 156,48,49
LK 44,242,44,2
s_add_u32 s50, s50, %[d]
CF 157,50,51
GS 6
s_cmp_eq_u32 s50, %[wrap]
CF 158,248,249
CF 159,250,251
Z 7,24,0
s_cselect_b32 s50, %[rst], s50
LC_ 104
RA 232,1,17408
M 7,25,1
WL 8
RA 236,1,18432
LK 48,244,45,0
CF 160,40,41
LK 248,246,45,2
CF 161,42,43
GS 7
WL 7
CF 162,44,45
CF 163,46,47
M 6,14,2
WL 5
LO 17,17,111
LU 111,17
M 6,15,3
s_add_u32 s38, s38, 32768
RA 216,1,19456
HS 25,111
LK 40,248,46,0
WL 5
CF 164,48,49
LK 44,250,46,2
s_cmp_eq_u32 s38, s77
CF 165,50,51
GS 8
s_cselect_b32 s38, s76, s38
CF 166,248,249
CF 167,250,251
M 7,26,2
s_add_u32 s37, s37, 32768
PK 111,17,109,33
PK 109,17,109,32
LL 16,58
LL 105,59
M 7,27,3
s_cmp_eq_u32 s37, s75
LA 16
PO 111
s_cselect_b32 s37, s74, s37
LK 48,252,47,0
CF 168,40,41
LK 248,254,47,2
CF 169,42,43
HX 25
GS 9
WL 3
CF 170,44,45
CF 171,46,47
M 6,16,4
WL 2
LC_ 105
RA 220,1,20480
XL 54,384
M 6,17,5
s_add_u32 s42, s44, s61
RA 224,1,21504
RV 36,8,6912
s_addc_u32 s43, s45, 0
GS 10
CF 172,48,49
CF 173,50,51
M 7,28,4
LO 104,18,104
LU 18,104
W1 25,1024
M 7,29,5
WL 3
RA 228,1,22528
HZ 24,18
ST 108
XL 55,448
M 6,18,0
s_mul_i32 s53, s51, 24
LL 17,60
LL 108,61
s_bitcmp1_b32 s51, 0
s_addc_u32 s53, s53, 0
s_lshl_b32 s53, s53, 11
s_bitcmp1_b64 s[90:91], s51
s_cselect_b64 s[44:45], s[84:85], s[82:83]
s_add_u32 s44, s44, s53
s_addc_u32 s45, s45, 0
s_add_u32 s51, s51, %[d]
s_cmp_eq_u32 s51, %[wrap]
s_cselect_b32 s51, %[rst], s51
M 6,19,1
LA 17
RA 232,1,23552
M 7,30,0
LC_ 108
XL 56,512
v_add_u32 v4, s39, v4
CF 174,248,249
CF 175,250,251
M 7,31,1
RA 236,1,24576
s_nop 1
v_add_u32 v1, s37, v0
GS 11
WL 7
XL 57,576
s_nop 5
v_cndmask_b32 v52, v52, v53, s[78:79]
v_cndmask_b32 v54, v54, v55, s[78:79]
M 6,20,2
WL 4
LO 105,16,105
LU 16,105
v_cndmask_b32 v52, v52, v54, s[80:81]
v_mul_f32 v52, 0x3b2aaaab, v52
M 6,21,3
RA 216,1,0
HS 24,16
v_med3_f32 v53, v52, v39, v37
M 7,32,2
LL 18,62
LL 109,63
LG 52,53
s_nop 0
C 52,52,52
M 7,33,3
WL 1
LA 18
XL 58,640
v_lshlrev_b32 v52, 16, v52
v_add_f32 v52, v36, v52
M 6,22,4
s_lshl_b32 s53, s52, 8
LC_ 109
v_and_b32 v22, 0xfffffcff, v1
C 52,52,52
v_lshlrev_b32 v52, 16, v52
M 6,23,5
s_add_u32 s48, %[alo], s53
RA 220,1,1024
v_sqrt_f32 v52, v52
s_nop 0
C 52,52,52
M 7,34,4
WV 20
LO 108,17,108
LU 17,108
s_addc_u32 s49, %[ahi], 0
s_add_u32 s52, s52, %[d]
s_cmp_eq_u32 s52, %[wrap]
s_cselect_b32 s52, %[rst], s52
XL 59,704
v_lshlrev_b32 v52, 16, v52
v_rcp_f32 v52, v52
M 7,35,5
s_barrier
RV 252,22,5120
HS 24,17
v_mul_f32 v52, v38, v52
CF 52,52,52
WL 0
Z 0,12,0
s_sub_u32 s39, 0, s39
RA 224,1,2048
LL 16,80
LL 106,81
ds_bpermute_b32 v13, v28, v52
M 0,13,1
s_mul_i32 s53, s50, 25600
LA 16
RA 228,1,3072
Z 1,24,0
s_add_u32 s40, %[wlo], s53
LC_ 106
RA 232,1,4096
M 1,25,1
s_addc_u32 s41, %[whi], 0
RA 236,1,6144
ds_read_b64 v[14:15], v5 offset:0
WL 7
M 0,14,2
LO 109,18,109
LU 18,109
s_mov_b32 m0, s38
WL 4
M 0,15,3
RA 216,1,7168
HS 24,18
M 1,26,2
DW 0
LL 17,82
LL 107,83
M 1,27,3
WL 3
LA 17
RA 220,1,8192
v_subrev_u32 v5, s39, v5
M 0,16,4
WL 4
LC_ 107
DW 1024
M 0,17,5
WL 4
RA 224,1,9216
v_add_f32 v14, v14, v15
s_nop 1
v_add_f32_dpp v14, v14, v14 quad_perm:[1,0,3,2] row_mask:0xf bank_mask:0xf
s_nop 1
v_add_f32_dpp v14, v14, v14 quad_perm:[2,3,0,1] row_mask:0xf bank_mask:0xf
s_nop 1
v_add_f32_dpp v14, v14, v14 row_half_mirror row_mask:0xf bank_mask:0xf
HX 24
XS 128
M 1,28,4
WV 21
LO 16,16,106
LU 106,16
v_cvt_f64_f32 v[14:15], v14
W1 24,0
XS 129
BW 48
XS 130
M 1,29,5
WL 4
RA 228,1,10240
HZ 25,106
s_mov_b64 exec, s[54:55]
SP
s_mov_b64 exec, -1
DW 2048
RV 40,8,6144
RV 44,8,6208
XS 131
AW 24,128
AW 25,129
M 0,18,0
s_add_u32 s40, s40, 3072
RV 48,8,6272
PK 106,16,104,33
PK 104,16,104,32
LL 18,84
LL 110,85
AW 26,130
AW 27,131
M 0,19,1
s_addc_u32 s41, s41, 0
LA 18
RA 232,1,11264
s_add_u32 m0, s38, 3072
BR 48
XS 132
XS 133
XS 134
BW 49
M 1,30,0
LC_ 110
RV 252,22,5392
BR 49
XS 135
AW 28,132
M 1,31,1
WL 10
RA 236,1,12288
AW 29,133
AW 30,134
AW 31,135
WL 5
LZ 40,48
WV 22
BW 50
M 0,20,2
DW 0
RV 40,8,6336
LO 17,17,107
LU 107,17
v_min_u32 v35, v35, v13
XS 136
XS 137
M 0,21,3
WL 3
RA 216,1,13312
BR 50
HS 25,107
XS 138
XS 139
LS 44,49
WV 22
BW 51
M 1,32,2
WL 5
DW 1024
PK 107,17,105,33
PK 105,17,105,32
LL 16,86
LL 111,87
AW 32,136
AW 33,137
AW 34,138
M 1,33,3
s_add_u32 s42, s44, s60
LA 16
RA 220,1,14336
RV 44,8,6400
BR 51
AW 35,139
XS 140
M 0,22,4
s_addc_u32 s43, s45, 0
LC_ 111
DW 2048
XS 141
XS 142
XS 143
M 0,23,5
WL 6
RA 224,1,15360
AW 36,140
AW 37,141
AW 38,142
LS 48,50
WV 23
BW 52
RV 48,8,6464
M 1,34,4
LO 18,18,110
LU 110,18
ST 104
BR 52
AW 39,143
XS 144
M 1,35,5
WL 5
RA 228,1,16384
DQ 3072
HS 25,110
XS 145
XS 146
XS 147
LS 40,51
WV 15
BW 53
Z 6,12,0
s_add_u32 s50, s50, %[d]
RV 40,8,6528
PK 110,18,108,33
PK 108,18,108,32
LL 17,56
LL 104,57
AW 40,144
AW 41,145
M 6,13,1
s_cmp_eq_u32 s50, %[wrap]
LA 17
BR 53
PO 110
AW 42,146
AW 43,147
XS 148
WL 5
LS 44,52
s_cselect_b32 s50, %[rst], s50
BW 54
Z 7,24,0
s_add_u32 s38, s38, 32768
LC_ 104
RA 232,1,17408
XS 149
XS 150
XS 151
M 7,25,1
s_cmp_eq_u32 s38, s77
RA 236,1,18432
RV 44,8,6592
BR 54
AW 44,148
AW 45,149
M 6,14,2
WL 6
LO 16,16,111
LU 111,16
s_cselect_b32 s38, s76, s38
AW 46,150
)"
R"(AW 47,151
XS 152
M 6,15,3
s_add_u32 s37, s37, 32768
RA 216,1,19456
HS 25,111
XS 153
XS 154
LS 48,53
s_cmp_eq_u32 s37, s75
WV 13
BW 55
RV 48,8,6656
M 7,26,2
s_cselect_b32 s37, s74, s37
BR 55
PK 111,16,109,33
PK 109,16,109,32
LL 18,58
LL 105,59
XS 155
AW 48,152
AW 49,153
M 7,27,3
s_add_u32 s53, %[xo0], 393216
LA 18
PO 111
s_add_u32 s46, %[xlo], s53
s_addc_u32 s47, %[xhi], 0
s_bitcmp1_b64 s[90:91], 48
s_cselect_b32 s58, 16, 0
s_lshr_b32 s58, 0xfe987610, s58
v_lshrrev_b32 v3, 6, v0
v_and_b32 v3, 12, v3
v_bfe_u32 v3, s58, v3, 4
v_mad_u32_u24 v3, v3, s97, v21
AW 50,154
AW 51,155
WL 5
LS 40,54
s_add_u32 s42, s44, s61
WV 12
BW 56
M 6,16,4
s_addc_u32 s43, s45, 0
LC_ 105
RA 220,1,20480
RV 40,8,6720
XS 156
XS 157
XS 158
M 6,17,5
WL 5
RA 224,1,21504
BR 56
XL 60,0
HX 25
XS 159
AW 52,156
AW 53,157
LS 44,55
WV 12
BW 57
M 7,28,4
LO 104,17,104
LU 17,104
W1 25,1024
AW 54,158
AW 55,159
M 7,29,5
WL 5
RA 228,1,22528
HZ 24,17
ST 108
RV 44,8,6784
BR 57
XS 160
XS 161
XS 162
M 6,18,0
s_mul_i32 s53, s51, 24
LL 16,60
LL 108,61
s_bitcmp1_b32 s51, 0
s_addc_u32 s53, s53, 0
s_lshl_b32 s53, s53, 11
s_bitcmp1_b64 s[90:91], s51
s_cselect_b64 s[44:45], s[84:85], s[82:83]
s_add_u32 s44, s44, s53
s_addc_u32 s45, s45, 0
s_add_u32 s51, s51, %[d]
s_cmp_eq_u32 s51, %[wrap]
s_cselect_b32 s51, %[rst], s51
XL 61,64
XS 163
AW 56,160
M 6,19,1
WL 5
LA 16
RA 232,1,23552
AW 57,161
AW 58,162
AW 59,163
LS 48,56
WV 13
BW 58
RV 48,8,6848
M 7,30,0
LC_ 108
XL 62,128
v_add_u32 v4, s39, v4
RA 240,7,0
XS 164
XS 165
XS 166
M 7,31,1
WL 6
RA 236,1,24576
BR 58
XS 167
AW 60,164
LS 40,57
WV 13
BW 59
M 6,20,2
LO 105,18,105
LU 18,105
AW 61,165
AW 62,166
AW 63,167
M 6,21,3
XL 63,192
RA 244,7,256
BR 59
HS 24,18
XS 168
XS 169
M 7,32,2
LL 17,62
LL 109,63
v_add_u32 v1, s37, v0
XS 170
XS 171
AW 64,168
M 7,33,3
WL 0
LA 17
RA 216,1,0
XL 64,256
AW 65,169
AW 66,170
AW 67,171
LS 44,58
s_lshl_b32 s53, s52, 8
RA 248,7,512
XS 172
LS 48,59
s_add_u32 s48, %[alo], s53
RA 252,7,768
XS 173
M 6,22,4
s_addc_u32 s49, %[ahi], 0
LC_ 109
v_and_b32 v22, 0xfffffcff, v1
M 6,23,5
s_add_u32 s52, s52, %[d]
RA 220,1,1024
XS 174
XS 175
AW 68,172
M 7,34,4
LO 108,16,108
LU 16,108
s_cmp_eq_u32 s52, %[wrap]
s_cselect_b32 s52, %[rst], s52
XL 65,320
AW 69,173
AW 70,174
M 7,35,5
s_barrier
RV 252,22,5120
HS 24,16
AW 71,175
v_mul_f32 v36, 0xbb2aaaab, v36
v_mul_f32 v37, 0xbb2aaaab, v37
v_mul_f32 v38, 0xbb2aaaab, v38
v_mul_f32 v39, 0xbb2aaaab, v39
WL 0
Z 0,12,0
s_sub_u32 s39, 0, s39
RA 224,1,2048
LL 18,80
LL 106,81
M 0,13,1
s_mul_i32 s53, s50, 25600
LA 18
RA 228,1,3072
LK 40,240,48,0
LK 44,242,48,2
s_add_u32 s40, %[wlo], s53
RA 240,7,1024
LK 48,244,49,0
s_nop 0
CF 128,40,41
LK 248,246,49,2
s_addc_u32 s41, %[whi], 0
RA 244,7,1280
CF 129,42,43
CF 130,44,45
CF 131,46,47
LK 40,248,50,0
CF 132,48,49
LK 44,250,50,2
s_mov_b32 m0, s38
RA 248,7,1536
CF 133,50,51
GZ 0
CF 134,248,249
CF 135,250,251
LK 48,252,51,0
CF 136,40,41
LK 248,254,51,2
RA 252,7,1792
CF 137,42,43
GS 1
CF 138,44,45
CF 139,46,47
Z 1,24,0
LC_ 106
RA 232,1,4096
M 1,25,1
WL 8
RA 236,1,6144
ds_read_b64 v[14:15], v5 offset:0
M 0,14,2
WL 8
LO 109,17,109
LU 17,109
M 0,15,3
WL 7
RA 216,1,7168
HS 24,17
LK 40,240,52,0
CF 140,48,49
LK 44,242,52,2
RA 240,7,2048
CF 141,50,51
GS 2
WL 8
CF 142,248,249
CF 143,250,251
LK 48,244,53,0
CF 144,40,41
LK 248,246,53,2
RA 244,7,2304
CF 145,42,43
GS 3
WL 8
CF 146,44,45
CF 147,46,47
LK 40,248,54,0
CF 148,48,49
LK 44,250,54,2
RA 248,7,2560
CF 149,50,51
GS 4
WL 8
CF 150,248,249
CF 151,250,251
LK 48,252,55,0
CF 152,40,41
LK 248,254,55,2
RA 252,7,2816
CF 153,42,43
GS 5
CF 154,44,45
CF 155,46,47
M 1,26,2
DW 0
LL 16,82
LL 107,83
M 1,27,3
WL 3
LA 16
RA 220,1,8192
v_subrev_u32 v5, s39, v5
LK 40,240,56,0
CF 156,48,49
LK 44,242,56,2
CF 157,50,51
HX 24
GS 6
CF 158,248,249
CF 159,250,251
M 0,16,4
LC_ 107
DW 1024
M 0,17,5
WL 5
RA 224,1,9216
v_add_f32 v14, v14, v15
s_nop 1
v_add_f32_dpp v14, v14, v14 quad_perm:[1,0,3,2] row_mask:0xf bank_mask:0xf
s_nop 1
v_add_f32_dpp v14, v14, v14 quad_perm:[2,3,0,1] row_mask:0xf bank_mask:0xf
s_nop 1
v_add_f32_dpp v14, v14, v14 row_half_mirror row_mask:0xf bank_mask:0xf
LK 48,244,57,0
CF 160,40,41
LK 248,246,57,2
CF 161,42,43
v_cvt_f64_f32 v[14:15], v14
GS 7
CF 162,44,45
CF 163,46,47
M 1,28,4
LO 18,18,106
LU 106,18
W1 24,0
M 1,29,5
WL 6
RA 228,1,10240
HZ 25,106
s_mov_b64 exec, s[54:55]
SP
s_mov_b64 exec, -1
DW 2048
LK 40,248,58,0
CF 164,48,49
LK 44,250,58,2
CF 165,50,51
GS 8
WL 4
CF 166,248,249
CF 167,250,251
M 0,18,0
s_add_u32 s40, s40, 3072
PK 106,18,104,33
PK 104,18,104,32
LL 17,84
LL 110,85
M 0,19,1
s_addc_u32 s41, s41, 0
LA 17
RA 232,1,11264
LK 48,252,59,0
CF 168,40,41
LK 248,254,59,2
s_add_u32 m0, s38, 3072
CF 169,42,43
GS 9
CF 170,44,45
CF 171,46,47
M 1,30,0
LC_ 110
RV 252,22,5392
M 1,31,1
RA 236,1,12288
RV 36,8,6912
GS 10
WL 8
CF 172,48,49
CF 173,50,51
M 0,20,2
WL 6
LO 16,16,107
LU 107,16
M 0,21,3
RA 216,1,13312
DW 0
HS 25,107
M 1,32,2
PK 107,16,105,33
PK 105,16,105,32
LL 18,86
LL 111,87
M 1,33,3
WL 2
LA 18
RA 220,1,14336
DW 1024
M 0,22,4
s_add_u32 s42, s44, s60
LC_ 111
s_addc_u32 s43, s45, 0
M 0,23,5
RA 224,1,15360
CF 174,248,249
CF 175,250,251
s_nop 1
GS 11
ST 104
M 1,34,4
DW 2048
LO 17,17,110
LU 110,17
s_nop 2
v_cndmask_b32 v52, v52, v53, s[78:79]
v_cndmask_b32 v54, v54, v55, s[78:79]
M 1,35,5
)"
R"(WL 4
RA 228,1,16384
HS 25,110
v_cndmask_b32 v52, v52, v54, s[80:81]
Z 6,12,0
WL 3
PK 110,17,108,33
PK 108,17,108,32
LL 16,56
LL 104,57
v_mul_f32 v52, 0x3b2aaaab, v52
v_med3_f32 v53, v52, v39, v37
M 6,13,1
WL 1
LA 16
DQ 3072
PO 110
LG 52,53
s_nop 0
C 52,52,52
Z 7,24,0
s_add_u32 s50, s50, %[d]
LC_ 104
RA 232,1,17408
v_lshlrev_b32 v52, 16, v52
M 7,25,1
s_cmp_eq_u32 s50, %[wrap]
RA 236,1,18432
s_cselect_b32 s50, %[rst], s50
v_add_f32 v52, v36, v52
C 52,52,52
M 6,14,2
WL 4
LO 18,18,111
LU 111,18
v_lshlrev_b32 v52, 16, v52
M 6,15,3
s_add_u32 s38, s38, 32768
RA 216,1,19456
HS 25,111
v_sqrt_f32 v52, v52
s_nop 0
C 52,52,52
M 7,26,2
s_cmp_eq_u32 s38, s77
PK 111,18,109,33
PK 109,18,109,32
LL 17,58
LL 105,59
s_cselect_b32 s38, s76, s38
v_lshlrev_b32 v52, 16, v52
M 7,27,3
s_add_u32 s37, s37, 32768
LA 17
PO 111
s_cmp_eq_u32 s37, s75
s_cselect_b32 s37, s74, s37
v_rcp_f32 v52, v52
s_nop 0
v_mul_f32 v52, v38, v52
WL 3
M 6,16,4
WL 2
LC_ 105
RA 220,1,20480
XL 66,384
CF 52,52,52
ds_bpermute_b32 v13, v28, v52
M 6,17,5
s_add_u32 s42, s44, s61
RA 224,1,21504
HX 25
s_addc_u32 s43, s45, 0
M 7,28,4
LO 104,16,104
LU 16,104
W1 25,1024
M 7,29,5
WL 3
RA 228,1,22528
HZ 24,16
ST 108
XL 67,448
M 6,18,0
s_mul_i32 s53, s51, 24
LL 18,60
LL 108,61
s_bitcmp1_b32 s51, 0
s_addc_u32 s53, s53, 0
s_lshl_b32 s53, s53, 11
s_bitcmp1_b64 s[90:91], s51
s_cselect_b64 s[44:45], s[84:85], s[82:83]
s_add_u32 s44, s44, s53
s_addc_u32 s45, s45, 0
s_add_u32 s51, s51, %[d]
s_cmp_eq_u32 s51, %[wrap]
s_cselect_b32 s51, %[rst], s51
M 6,19,1
LA 18
RA 232,1,23552
M 7,30,0
LC_ 108
XL 68,512
v_add_u32 v4, s39, v4
M 7,31,1
WL 5
RA 236,1,24576
s_nop 1
v_add_u32 v1, s37, v0
M 6,20,2
WL 4
XL 69,576
LO 105,17,105
LU 17,105
M 6,21,3
RA 216,1,0
HS 24,17
M 7,32,2
LL 16,62
LL 109,63
XS 128
M 7,33,3
WL 1
LA 16
XL 70,640
XS 129
XS 130
M 6,22,4
s_lshl_b32 s53, s52, 8
LC_ 109
v_and_b32 v22, 0xfffffcff, v1
XS 131
AW 72,128
M 6,23,5
s_add_u32 s48, %[alo], s53
RA 220,1,1024
AW 73,129
AW 74,130
M 7,34,4
WV 22
LO 108,18,108
LU 18,108
s_addc_u32 s49, %[ahi], 0
s_add_u32 s52, s52, %[d]
s_cmp_eq_u32 s52, %[wrap]
s_cselect_b32 s52, %[rst], s52
XL 71,704
AW 75,131
XS 132
M 7,35,5
s_barrier
RV 252,22,5120
HS 24,18
XS 133
XS 134
WL 0
Z 0,12,0
s_sub_u32 s39, 0, s39
LL 17,80
LL 106,81
XS 135
AW 76,132
M 0,13,1
s_mul_i32 s53, s50, 25600
LA 17
RA 224,1,2048
AW 77,133
AW 78,134
Z 1,24,0
s_add_u32 s40, %[wlo], s53
LC_ 106
RA 228,1,3072
AW 79,135
M 1,25,1
s_addc_u32 s41, %[whi], 0
RA 232,1,4096
v_min_u32 v35, v35, v13
XS 136
Z 2,36,0
LO 109,16,109
LU 16,109
XS 137
XS 138
M 2,37,1
s_mov_b32 m0, s38
ds_read_b64 v[14:15], v5 offset:0
HS 24,16
XS 139
AW 80,136
Z 3,48,0
WL 2
LL 18,82
LL 107,83
AW 81,137
AW 82,138
M 3,49,1
LA 18
RA 236,1,6144
AW 83,139
XS 140
M 0,14,2
LC_ 107
DW 0
XS 141
XS 142
M 0,15,3
v_subrev_u32 v5, s39, v5
HX 24
XS 143
AW 84,140
M 1,26,2
LO 17,17,106
LU 106,17
AW 85,141
AW 86,142
M 1,27,3
RA 216,1,7168
HZ 25,106
AW 87,143
XS 144
M 2,38,2
PK 106,17,104,33
PK 104,17,104,32
LL 16,84
LL 110,85
XS 145
M 2,39,3
LA 16
W1 24,0
XS 146
XS 147
M 3,50,2
WL 4
LC_ 110
DW 1024
AW 88,144
AW 89,145
M 3,51,3
RA 220,1,8192
v_add_f32 v14, v14, v15
AW 90,146
AW 91,147
M 0,16,4
WL 5
LO 18,18,107
LU 107,18
XS 148
XS 149
M 0,17,5
DW 2048
HS 25,107
v_add_f32_dpp v14, v14, v14 quad_perm:[1,0,3,2] row_mask:0xf bank_mask:0xf
XS 150
XS 151
M 1,28,4
s_add_u32 s42, s44, s60
PK 107,18,105,33
PK 105,18,105,32
LL 17,86
LL 111,87
AW 92,148
AW 93,149
M 1,29,5
s_addc_u32 s43, s45, 0
LA 17
RA 224,1,9216
v_add_f32_dpp v14, v14, v14 quad_perm:[2,3,0,1] row_mask:0xf bank_mask:0xf
AW 94,150
AW 95,151
M 2,40,4
LC_ 111
ST 104
XS 152
XS 153
M 2,41,5
v_add_f32_dpp v14, v14, v14 row_half_mirror row_mask:0xf bank_mask:0xf
XS 154
XS 155
M 3,52,4
WL 3
LO 16,16,110
LU 110,16
AW 96,152
AW 97,153
M 3,53,5
s_add_u32 s40, s40, 3072
RA 228,1,10240
HS 25,110
v_cvt_f64_f32 v[14:15], v14
AW 98,154
M 0,18,0
s_addc_u32 s41, s41, 0
PK 110,16,108,33
PK 108,16,108,32
AW 99,155
XS 156
M 0,19,1
s_add_u32 m0, s38, 3072
s_mov_b64 exec, s[54:55]
SP
s_mov_b64 exec, -1
PO 110
XS 157
XS 158
M 1,30,0
XS 159
AW 100,156
M 1,31,1
RA 232,1,11264
AW 101,157
AW 102,158
M 2,42,0
WL 2
LO 17,17,111
LU 111,17
AW 103,159
XS 160
M 2,43,1
RV 252,22,5392
HS 25,111
XS 161
XS 162
M 3,54,0
WL 2
PK 111,17,109,33
PK 109,17,109,32
XS 163
AW 104,160
M 3,55,1
RA 236,1,12288
PO 111
AW 105,161
AW 106,162
M 0,20,2
DW 0
HX 25
AW 107,163
XS 164
M 0,21,3
s_add_u32 s42, s44, s61
W1 25,1024
XS 165
XS 166
M 1,32,2
s_addc_u32 s43, s45, 0
ST 108
v_add_u32 v4, s39, v4
XS 167
M 1,33,3
RA 216,1,13312
AW 108,164
AW 109,165
M 2,44,2
DW 1024
AW 110,166
AW 111,167
M 2,45,3
XS 168
XS 169
M 3,56,2
WL 2
XS 170
XS 171
M 3,57,3
RA 220,1,14336
AW 112,168
AW 113,169
M 0,22,4
DW 2048
AW 114,170
AW 115,171
M 0,23,5
XS 172
XS 173
M 1,34,4
XS 174
XS 175
M 1,35,5
RA 224,1,15360
AW 116,172
AW 117,173
M 2,46,4
LL 18,56
LL 104,57
AW 118,174
AW 119,175
M 2,47,5
LA 18
DQ 3072
M 3,58,4
WL 2
LC_ 104
M 3,59,5
RA 228,1,16384
Z 6,12,0
WV 32
BW 60
M 6,13,1
s_add_u32 s50, s50, %[d]
RV 40,8,6144
)"
R"(RV 44,8,6208
Z 7,24,0
s_cmp_eq_u32 s50, %[wrap]
LL 16,58
LL 105,59
M 7,25,1
s_cselect_b32 s50, %[rst], s50
LA 16
RA 232,1,17408
Z 8,36,0
LC_ 105
RV 48,8,6272
M 8,37,1
BR 60
Z 9,48,0
WL 8
LO 104,18,104
LU 18,104
M 9,49,1
s_add_u32 s38, s38, 32768
RA 236,1,18432
HZ 24,18
M 6,14,2
s_cmp_eq_u32 s38, s77
LL 17,60
LL 108,61
M 6,15,3
s_cselect_b32 s38, s76, s38
LA 17
M 7,26,2
s_add_u32 s37, s37, 32768
LC_ 108
M 7,27,3
s_cmp_eq_u32 s37, s75
RA 216,1,19456
M 8,38,2
WL 6
LO 105,16,105
LU 16,105
M 8,39,3
s_cselect_b32 s37, s74, s37
HS 24,16
M 9,50,2
WL 3
LL 18,62
LL 109,63
M 9,51,3
s_add_u32 s53, %[xo0], 393216
LA 18
RA 220,1,20480
M 6,16,4
s_add_u32 s46, %[xlo], s53
LC_ 109
M 6,17,5
s_addc_u32 s47, %[xhi], 0
WV 30
BW 61
M 7,28,4
WL 5
LO 108,17,108
LU 17,108
s_bitcmp1_b64 s[90:91], 48
s_cselect_b32 s58, 16, 0
s_lshr_b32 s58, 0xda43cb52, s58
v_lshrrev_b32 v3, 6, v0
v_and_b32 v3, 12, v3
v_bfe_u32 v3, s58, v3, 4
BR 61
v_mad_u32_u24 v3, v3, s97, v21
M 7,29,5
RA 224,1,21504
HS 24,17
LZ 40,60
WV 29
BW 62
M 8,40,4
LL 16,64
LL 112,65
M 8,41,5
LA 16
XL 72,0
M 9,52,4
WL 6
LC_ 112
RV 40,8,6336
M 9,53,5
s_mul_i32 s53, s51, 24
RA 228,1,22528
BR 62
M 6,18,0
WL 7
LO 109,18,109
LU 18,109
M 6,19,1
s_bitcmp1_b32 s51, 0
HS 24,18
s_addc_u32 s53, s53, 0
s_lshl_b32 s53, s53, 11
s_bitcmp1_b64 s[90:91], s51
s_cselect_b64 s[44:45], s[84:85], s[82:83]
s_add_u32 s44, s44, s53
s_addc_u32 s45, s45, 0
s_add_u32 s51, s51, %[d]
s_cmp_eq_u32 s51, %[wrap]
s_cselect_b32 s51, %[rst], s51
LS 44,61
WV 29
BW 63
M 7,30,0
LL 17,66
LL 113,67
M 7,31,1
WV 28
LA 17
RA 232,1,23552
RV 44,8,6400
M 8,42,0
WL 4
LC_ 113
XL 73,64
M 8,43,1
s_lshl_b32 s53, s52, 8
BR 63
LS 48,62
s_add_u32 s48, %[alo], s53
BW 64
M 9,54,0
LO 112,16,112
LU 16,112
M 9,55,1
s_addc_u32 s49, %[ahi], 0
RA 236,1,24576
HS 24,16
M 6,20,2
s_add_u32 s52, s52, %[d]
LL 18,68
LL 116,69
s_cmp_eq_u32 s52, %[wrap]
s_cselect_b32 s52, %[rst], s52
M 6,21,3
LA 18
XL 74,128
RV 48,8,6464
M 7,32,2
LC_ 116
XL 75,192
BR 64
M 7,33,3
WL 6
v_add_u32 v1, s37, v0
LS 40,63
WV 30
BW 65
M 8,44,2
LO 113,17,113
LU 17,113
M 8,45,3
RA 216,1,0
HS 24,17
M 9,56,2
WL 2
RV 40,8,6528
LL 16,70
LL 117,71
M 9,57,3
WV 21
LA 16
XL 76,256
BR 65
M 6,22,4
s_barrier
LC_ 117
v_and_b32 v22, 0xfffffcff, v1
M 6,23,5
s_sub_u32 s39, 0, s39
RA 220,1,1024
LS 44,64
WV 21
BW 66
M 7,34,4
LO 116,18,116
LU 18,116
M 7,35,5
RV 252,22,5120
RV 44,8,6592
HS 24,18
M 8,46,4
LL 17,80
LL 106,81
M 8,47,5
LA 17
RA 224,1,2048
BR 66
M 9,58,4
WL 6
LC_ 106
ds_read_b64 v[14:15], v5 offset:0
M 9,59,5
WL 6
RA 228,1,3072
v_subrev_u32 v5, s39, v5
Z 0,12,0
LO 117,16,117
LU 16,117
M 0,13,1
WV 18
XL 77,320
HS 24,16
LS 48,65
s_mul_i32 s53, s50, 25600
BW 67
RV 48,8,6656
Z 1,24,0
s_add_u32 s40, %[wlo], s53
BR 67
LL 18,82
LL 107,83
M 1,25,1
s_addc_u32 s41, %[whi], 0
LA 18
RA 232,1,4096
WL 8
LS 40,66
s_mov_b32 m0, s38
BW 68
Z 2,36,0
LC_ 107
RV 40,8,6720
M 2,37,1
BR 68
HX 24
Z 3,48,0
WL 9
LO 17,17,106
LU 106,17
M 3,49,1
RA 236,1,6144
HZ 25,106
M 0,14,2
LL 16,84
LL 110,85
M 0,15,3
WL 7
LA 16
PK 106,17,104,33
LS 44,67
WV 18
BW 69
M 1,26,2
LC_ 110
PK 104,17,104,32
W1 24,0
M 1,27,3
WV 17
RA 216,1,7168
RV 44,8,6784
M 2,38,2
WL 9
LO 18,18,107
LU 107,18
M 2,39,3
WL 7
DW 0
BR 69
HS 25,107
LS 48,68
s_add_u32 s42, s44, s60
BW 70
M 3,50,2
WL 8
LL 17,86
LL 111,87
M 3,51,3
s_addc_u32 s43, s45, 0
LA 17
PK 107,18,105,33
v_add_f32 v14, v14, v15
s_nop 1
v_add_f32_dpp v14, v14, v14 quad_perm:[1,0,3,2] row_mask:0xf bank_mask:0xf
s_nop 1
v_add_f32_dpp v14, v14, v14 quad_perm:[2,3,0,1] row_mask:0xf bank_mask:0xf
M 0,16,4
LC_ 111
PK 105,18,105,32
v_add_f32_dpp v14, v14, v14 row_half_mirror row_mask:0xf bank_mask:0xf
M 0,17,5
RA 220,1,8192
RV 48,8,6848
M 1,28,4
WL 9
RA 240,7,0
LO 16,16,110
LU 110,16
v_cvt_f64_f32 v[14:15], v14
M 1,29,5
RA 224,1,9216
DW 1024
BR 70
HS 25,110
M 2,40,4
LL 18,88
LL 114,89
s_mov_b64 exec, s[54:55]
SP
s_mov_b64 exec, -1
M 2,41,5
WL 8
LA 18
PK 110,16,108,33
LS 40,69
WV 19
BW 71
M 3,52,4
WL 6
LC_ 114
PK 108,16,108,32
M 3,53,5
RA 228,1,10240
RA 244,7,256
PO 110
M 0,18,0
WL 5
LO 17,17,111
LU 111,17
ST 104
DW 2048
M 0,19,1
s_add_u32 s40, s40, 3072
BR 71
HS 25,111
M 1,30,0
s_addc_u32 s41, s41, 0
LL 16,90
LL 115,91
M 1,31,1
s_add_u32 m0, s38, 3072
LA 16
PK 111,17,109,33
LS 44,70
s_add_u32 s42, s44, s61
RA 248,7,512
M 2,42,0
s_addc_u32 s43, s45, 0
LC_ 115
PK 109,17,109,32
M 2,43,1
WL 3
RA 232,1,11264
PO 111
LS 48,71
RA 252,7,768
M 3,54,0
LO 18,18,114
LU 114,18
M 3,55,1
RV 252,22,5392
HS 25,114
s_nop 0
v_mul_f32 v36, 0xbb2aaaab, v36
M 0,20,2
LL 17,92
LL 118,93
v_mul_f32 v37, 0xbb2aaaab, v37
v_mul_f32 v38, 0xbb2aaaab, v38
M 0,21,3
LA 17
PK 114,18,112,33
v_mul_f32 v39, 0xbb2aaaab, v39
s_nop 1
LK 40,240,60,0
LK 44,242,60,2
RA 240,7,1024
LK 48,244,61,0
s_nop 1
CF 128,40,41
LK 248,246,61,2
RA 244,7,1280
CF 129,42,43
M 1,32,2
LC_ 118
PK 112,18,112,32
M 1,33,3
RA 236,1,12288
CF 130,44,45
M 2,44,2
WL 8
DW 0
LO 16,16,115
LU 115,16
M 2,45,3
RA 216,1,13312
HS 25,115
ST 108
CF 131,46,47
LK 40,248,62,0
CF 132,48,49
LK 44,250,62,2
RA 248,7,1536
CF 133,50,51
GZ 0
)"
R"(WL 8
CF 134,248,249
CF 135,250,251
LK 48,252,63,0
CF 136,40,41
LK 248,254,63,2
RA 252,7,1792
CF 137,42,43
GS 1
WL 6
CF 138,44,45
CF 139,46,47
LK 40,240,64,0
CF 140,48,49
LK 44,242,64,2
RA 240,7,2048
CF 141,50,51
GS 2
CF 142,248,249
CF 143,250,251
M 3,56,2
WL 4
LL 18,94
LL 119,95
M 3,57,3
LA 18
PK 115,16,113,33
LK 48,244,65,0
CF 144,40,41
LK 248,246,65,2
RA 244,7,2304
CF 145,42,43
GS 3
CF 146,44,45
CF 147,46,47
M 0,22,4
LC_ 119
PK 113,16,113,32
M 0,23,5
RA 220,1,14336
DW 1024
M 1,34,4
WL 6
LO 17,17,118
LU 118,17
M 1,35,5
s_add_u32 s42, s44, s62
RA 224,1,15360
HS 25,118
s_addc_u32 s43, s45, 0
LK 40,248,66,0
CF 148,48,49
LK 44,250,66,2
RA 248,7,2560
CF 149,50,51
GS 4
CF 150,248,249
CF 151,250,251
M 2,46,4
LL 16,56
LL 104,57
M 2,47,5
WL 7
LA 16
PK 118,17,116,33
LK 48,252,67,0
CF 152,40,41
LK 248,254,67,2
RA 252,7,2816
CF 153,42,43
GS 5
CF 154,44,45
CF 155,46,47
M 3,58,4
WL 4
LC_ 104
PK 116,17,116,32
M 3,59,5
RA 228,1,16384
DW 2048
PO 118
Z 6,12,0
LO 18,18,119
LU 119,18
ST 112
M 6,13,1
DQ 3072
HS 25,119
LK 40,240,68,0
CF 156,48,49
LK 44,242,68,2
s_add_u32 s42, s44, s63
CF 157,50,51
GS 6
s_addc_u32 s43, s45, 0
CF 158,248,249
CF 159,250,251
Z 7,24,0
LL 17,58
LL 105,59
M 7,25,1
LA 17
PK 119,18,117,33
Z 8,36,0
s_add_u32 s50, s50, %[d]
LC_ 105
PK 117,18,117,32
M 8,37,1
s_cmp_eq_u32 s50, %[wrap]
RA 232,1,17408
PO 119
LK 48,244,69,0
CF 160,40,41
LK 248,246,69,2
s_cselect_b32 s50, %[rst], s50
CF 161,42,43
HX 25
GS 7
CF 162,44,45
CF 163,46,47
Z 9,48,0
WL 3
LO 104,16,104
LU 16,104
M 9,49,1
RA 236,1,18432
HZ 24,16
M 6,14,2
LL 18,60
LL 108,61
M 6,15,3
LA 18
W1 25,1024
LK 40,248,70,0
CF 164,48,49
LK 44,250,70,2
CF 165,50,51
GS 8
CF 166,248,249
CF 167,250,251
M 7,26,2
LC_ 108
ST 116
M 7,27,3
s_add_u32 s37, s37, 32768
RA 216,1,19456
v_add_u32 v4, s39, v4
LK 48,252,71,0
CF 168,40,41
LK 248,254,71,2
s_cmp_eq_u32 s37, s75
CF 169,42,43
GS 9
s_cselect_b32 s37, s74, s37
CF 170,44,45
CF 171,46,47
M 8,38,2
WL 6
LO 105,17,105
LU 17,105
M 8,39,3
s_add_u32 s38, s38, 32768
RV 36,8,6912
HS 24,17
M 9,50,2
WL 5
LL 16,62
LL 109,63
M 9,51,3
s_cmp_eq_u32 s38, s77
LA 16
RA 220,1,20480
GS 10
s_cselect_b32 s38, s76, s38
CF 172,48,49
M 6,16,4
LC_ 109
XL 78,384
M 6,17,5
CF 173,50,51
CF 174,248,249
M 7,28,4
WL 5
LO 108,18,108
LU 18,108
M 7,29,5
s_mul_i32 s53, s51, 24
RA 224,1,21504
HS 24,18
M 8,40,4
s_bitcmp1_b32 s51, 0
LL 17,64
LL 112,65
M 8,41,5
s_addc_u32 s53, s53, 0
LA 17
CF 175,250,251
M 9,52,4
WL 3
LC_ 112
M 9,53,5
s_lshl_b32 s53, s53, 11
RA 228,1,22528
GS 11
s_bitcmp1_b64 s[90:91], s51
M 6,18,0
WL 4
LO 109,16,109
LU 16,109
M 6,19,1
s_cselect_b64 s[44:45], s[84:85], s[82:83]
HS 24,16
s_add_u32 s44, s44, s53
s_addc_u32 s45, s45, 0
s_add_u32 s51, s51, %[d]
s_cmp_eq_u32 s51, %[wrap]
s_cselect_b32 s51, %[rst], s51
M 7,30,0
LL 18,66
LL 113,67
M 7,31,1
LA 18
RA 232,1,23552
XL 79,448
M 8,42,0
s_lshl_b32 s53, s52, 8
LC_ 113
v_cndmask_b32 v52, v52, v53, s[78:79]
M 8,43,1
s_add_u32 s48, %[alo], s53
v_cndmask_b32 v54, v54, v55, s[78:79]
v_cndmask_b32 v52, v52, v54, s[80:81]
M 9,54,0
WL 3
LO 112,17,112
LU 17,112
M 9,55,1
s_addc_u32 s49, %[ahi], 0
RA 236,1,24576
HS 24,17
M 6,20,2
s_add_u32 s52, s52, %[d]
LL 16,68
LL 116,69
s_cmp_eq_u32 s52, %[wrap]
s_cselect_b32 s52, %[rst], s52
v_mul_f32 v52, 0x3b2aaaab, v52
M 6,21,3
LA 16
XL 80,512
M 7,32,2
LC_ 116
XL 81,576
v_med3_f32 v53, v52, v39, v37
M 7,33,3
v_add_u32 v1, s37, v0
LG 52,53
M 8,44,2
WL 3
LO 113,18,113
LU 18,113
C 52,52,52
M 8,45,3
RA 216,1,0
HS 24,18
v_lshlrev_b32 v52, 16, v52
M 9,56,2
WL 3
LL 17,70
LL 117,71
v_add_f32 v52, v36, v52
M 9,57,3
WV 22
LA 17
XL 82,640
C 52,52,52
M 6,22,4
s_barrier
LC_ 117
v_and_b32 v22, 0xfffffcff, v1
v_lshlrev_b32 v52, 16, v52
M 6,23,5
s_sub_u32 s39, 0, s39
RA 220,1,1024
v_sqrt_f32 v52, v52
M 7,34,4
WL 4
LO 116,16,116
LU 16,116
C 52,52,52
M 7,35,5
RV 252,22,5120
HS 24,16
v_lshlrev_b32 v52, 16, v52
M 8,46,4
LL 18,80
LL 106,81
v_rcp_f32 v52, v52
M 8,47,5
LA 18
RA 224,1,2048
v_mul_f32 v52, v38, v52
M 9,58,4
WL 3
LC_ 106
ds_read_b64 v[14:15], v5 offset:0
CF 52,52,52
M 9,59,5
WL 4
RA 228,1,3072
ds_bpermute_b32 v13, v28, v52
Z 0,12,0
LO 117,17,117
LU 17,117
M 0,13,1
XL 83,704
HS 24,17
Z 1,24,0
s_mul_i32 s53, s50, 25600
LL 16,82
LL 107,83
M 1,25,1
s_add_u32 s40, %[wlo], s53
LA 16
RA 232,1,4096
Z 2,36,0
s_addc_u32 s41, %[whi], 0
LC_ 107
v_subrev_u32 v5, s39, v5
M 2,37,1
s_mov_b32 m0, s38
HX 24
Z 3,48,0
WL 4
LO 18,18,106
LU 106,18
M 3,49,1
RA 236,1,6144
HZ 25,106
M 0,14,2
LL 17,84
LL 110,85
M 0,15,3
WL 4
LA 17
PK 106,18,104,33
XS 128
M 1,26,2
LC_ 110
PK 104,18,104,32
W1 24,0
XS 129
M 1,27,3
RA 216,1,7168
v_add_f32 v14, v14, v15
XS 130
M 2,38,2
WL 5
DW 0
LO 16,16,107
LU 107,16
v_add_f32_dpp v14, v14, v14 quad_perm:[1,0,3,2] row_mask:0xf bank_mask:0xf
XS 131
M 2,39,3
DW 1024
HS 25,107
AW 120,128
M 3,50,2
WL 4
LL 18,86
LL 111,87
v_add_f32_dpp v14, v14, v14 quad_perm:[2,3,0,1] row_mask:0xf bank_mask:0xf
AW 121,129
M 3,51,3
s_add_u32 s42, s44, s60
LA 18
PK 107,16,105,33
AW 122,130
M 0,16,4
s_addc_u32 s43, s45, 0
LC_ 111
PK 105,16,105,32
v_add_f32_dpp v14, v14, v14 row_half_mirror row_mask:0xf bank_mask:0xf
AW 123,131
XS 132
M 0,17,5
RA 220,1,8192
v_cvt_f64_f32 v[14:15], v14
XS 133
M 1,28,4
WL 5
LO 17,17,110
LU 110,17
XS 134
M 1,29,5
RA 224,1,9216
HS 25,110
XS 135
M 2,40,4
WV 25
)"
R"(LL 16,88
LL 114,89
s_mov_b64 exec, s[54:55]
SP
s_mov_b64 exec, -1
DW 2048
BW 72
AW 124,132
M 2,41,5
LA 16
RV 40,8,6144
PK 110,17,108,33
AW 125,133
M 3,52,4
WL 4
LC_ 114
RV 44,8,6208
PK 108,17,108,32
AW 126,134
AW 127,135
M 3,53,5
RA 228,1,10240
PO 110
v_min_u32 v35, v35, v13
M 0,18,0
LO 18,18,111
LU 111,18
ST 104
RV 48,8,6272
XS 136
M 0,19,1
s_add_u32 s40, s40, 3072
BR 72
HS 25,111
s_addc_u32 s41, s41, 0
s_add_u32 m0, s38, 3072
XS 137
M 1,30,0
WV 27
LL 17,90
LL 115,91
XS 138
BW 73
M 1,31,1
LA 17
PK 111,18,109,33
XS 139
M 2,42,0
WV 26
LC_ 115
BR 73
PK 109,18,109,32
AW 128,136
M 2,43,1
WL 4
RA 232,1,11264
DW 0
PO 111
AW 129,137
AW 130,138
LZ 40,72
s_add_u32 s42, s44, s61
BW 74
M 3,54,0
LO 16,16,114
LU 114,16
AW 131,139
M 3,55,1
s_addc_u32 s43, s45, 0
RV 252,22,5392
HS 25,114
XS 140
M 0,20,2
RV 40,8,6336
LL 18,92
LL 118,93
XS 141
M 0,21,3
LA 18
DW 1024
BR 74
PK 114,16,112,33
XS 142
M 1,32,2
LC_ 118
PK 112,16,112,32
XS 143
M 1,33,3
WL 7
RA 236,1,12288
AW 132,140
AW 133,141
LS 44,73
WV 27
BW 75
M 2,44,2
LO 17,17,115
LU 115,17
AW 134,142
M 2,45,3
RA 216,1,13312
HS 25,115
ST 108
RV 44,8,6400
AW 135,143
M 3,56,2
WL 3
DW 2048
BR 75
LL 16,94
LL 119,95
XS 144
M 3,57,3
LA 16
PK 115,17,113,33
XS 145
LS 48,74
WV 28
BW 76
M 0,22,4
LC_ 119
PK 113,17,113,32
XS 146
M 0,23,5
WV 27
RA 220,1,14336
RV 48,8,6464
XS 147
M 1,34,4
WL 5
DQ 3072
LO 18,18,118
LU 118,18
AW 136,144
AW 137,145
M 1,35,5
s_add_u32 s42, s44, s62
RA 224,1,15360
HS 25,118
s_addc_u32 s43, s45, 0
AW 138,146
M 2,46,4
s_add_u32 s50, s50, %[d]
BR 76
LL 17,56
LL 104,57
AW 139,147
M 2,47,5
s_cmp_eq_u32 s50, %[wrap]
LA 17
PK 118,18,116,33
XS 148
LS 40,75
s_cselect_b32 s50, %[rst], s50
BW 77
M 3,58,4
WL 5
LC_ 104
PK 116,18,116,32
XS 149
M 3,59,5
WV 15
RA 228,1,16384
PO 118
XS 150
Z 6,12,0
WL 4
LO 16,16,119
LU 119,16
ST 112
RV 40,8,6528
XS 151
AW 140,148
M 6,13,1
s_add_u32 s38, s38, 32768
BR 77
HS 25,119
AW 141,149
Z 7,24,0
s_cmp_eq_u32 s38, s77
LL 18,58
LL 105,59
s_cselect_b32 s38, s76, s38
AW 142,150
M 7,25,1
s_mul_i32 s53, s50, 25600
LA 18
PK 119,16,117,33
AW 143,151
LS 44,76
s_add_u32 s40, %[wlo], s53
BW 78
Z 8,36,0
s_addc_u32 s41, %[whi], 0
LC_ 105
PK 117,16,117,32
XS 152
M 8,37,1
s_mov_b32 m0, s38
RA 232,1,17408
RV 44,8,6592
PO 119
XS 153
Z 9,48,0
WL 5
BR 78
LO 104,17,104
LU 17,104
XS 154
M 9,49,1
s_add_u32 s42, s44, s63
RA 236,1,18432
HZ 24,17
XS 155
AW 144,152
LS 48,77
s_addc_u32 s43, s45, 0
WV 15
BW 79
M 6,14,2
LL 16,60
LL 108,61
AW 145,153
M 6,15,3
LA 16
DW 0
HX 25
AW 146,154
M 7,26,2
LC_ 108
W1 25,1024
RV 48,8,6656
AW 147,155
M 7,27,3
RA 216,1,19456
BR 79
XS 156
M 8,38,2
WL 11
LO 105,18,105
LU 18,105
XS 157
M 8,39,3
WL 8
ST 116
HS 24,18
XS 158
XS 159
LS 40,78
WV 16
BW 80
M 9,50,2
WL 8
LL 17,62
LL 109,63
AW 148,156
M 9,51,3
LA 17
RA 220,1,20480
DW 1024
AW 149,157
M 6,16,4
s_add_u32 s37, s37, 32768
LC_ 109
RV 40,8,6720
s_cmp_eq_u32 s37, s75
s_cselect_b32 s37, s74, s37
AW 150,158
M 6,17,5
WL 5
DW 2048
BR 80
AW 151,159
LS 44,79
WV 17
BW 81
M 7,28,4
LO 108,16,108
LU 16,108
v_add_u32 v4, s39, v4
XS 160
M 7,29,5
s_add_u32 s40, s40, 3072
RA 224,1,21504
HS 24,16
XS 161
M 8,40,4
s_addc_u32 s41, s41, 0
LL 18,64
LL 112,65
XS 162
XS 163
M 8,41,5
s_add_u32 m0, s38, 3072
LA 18
RV 44,8,6784
AW 152,160
M 9,52,4
WL 7
LC_ 112
BR 81
AW 153,161
M 9,53,5
s_mul_i32 s53, s51, 24
RA 228,1,22528
AW 154,162
M 6,18,0
WL 7
LO 109,17,109
LU 17,109
AW 155,163
M 6,19,1
s_bitcmp1_b32 s51, 0
HS 24,17
s_addc_u32 s53, s53, 0
s_lshl_b32 s53, s53, 11
s_bitcmp1_b64 s[90:91], s51
s_cselect_b64 s[44:45], s[84:85], s[82:83]
s_add_u32 s44, s44, s53
s_addc_u32 s45, s45, 0
s_add_u32 s51, s51, %[d]
s_cmp_eq_u32 s51, %[wrap]
s_cselect_b32 s51, %[rst], s51
XS 164
LS 48,80
WV 16
BW 82
M 7,30,0
RV 48,8,6848
LL 16,66
LL 113,67
XS 165
XS 166
M 7,31,1
WV 15
LA 16
RA 232,1,23552
XS 167
M 8,42,0
WL 5
LC_ 113
DW 0
RA 240,7,0
AW 156,164
M 8,43,1
s_lshl_b32 s53, s52, 8
BR 82
AW 157,165
LS 40,81
s_add_u32 s48, %[alo], s53
BW 83
M 9,54,0
WL 8
LO 112,18,112
LU 18,112
AW 158,166
M 9,55,1
s_addc_u32 s49, %[ahi], 0
RA 236,1,24576
HS 24,18
AW 159,167
M 6,20,2
s_add_u32 s52, s52, %[d]
LL 17,68
LL 116,69
s_cmp_eq_u32 s52, %[wrap]
s_cselect_b32 s52, %[rst], s52
DW 1024
XS 168
M 6,21,3
LA 17
DW 2048
RA 244,7,256
XS 169
XS 170
M 7,32,2
LC_ 116
BR 83
XS 171
M 7,33,3
WL 6
DQ 3072
v_add_u32 v1, s37, v0
AW 160,168
LS 44,82
RA 248,7,512
AW 161,169
M 8,44,2
LO 113,16,113
LU 16,113
M 8,45,3
RA 216,1,0
HS 24,16
AW 162,170
M 9,56,2
WL 2
LL 18,70
LL 117,71
AW 163,171
M 9,57,3
)"
R"(s_add_u32 s50, s50, %[d]
LA 18
v_and_b32 v22, 0xfffffcff, v1
XS 172
XS 173
LS 48,83
s_cmp_eq_u32 s50, %[wrap]
RA 252,7,768
XS 174
M 6,22,4
s_cselect_b32 s50, %[rst], s50
LC_ 117
RA 220,1,1024
M 6,23,5
s_barrier
RV 252,22,5120
XS 175
M 7,34,4
LO 116,17,116
LU 17,116
AW 164,172
M 7,35,5
s_sub_u32 s39, 0, s39
RA 224,1,2048
HS 24,17
AW 165,173
M 8,46,4
s_add_u32 s38, s38, 32768
LL 16,80
LL 106,81
AW 166,174
M 8,47,5
s_cmp_eq_u32 s38, s77
LA 16
ds_read_b64 v[14:15], v5 offset:0
AW 167,175
v_mul_f32 v36, 0xbb2aaaab, v36
v_mul_f32 v37, 0xbb2aaaab, v37
v_mul_f32 v38, 0xbb2aaaab, v38
v_mul_f32 v39, 0xbb2aaaab, v39
s_nop 1
LK 40,240,72,0
LK 44,242,72,2
s_cselect_b32 s38, s76, s38
RA 240,7,1024
LK 48,244,73,0
s_nop 0
CF 128,40,41
LK 248,246,73,2
RA 244,7,1280
CF 129,42,43
M 9,58,4
WL 6
LC_ 106
v_subrev_u32 v5, s39, v5
M 9,59,5
WL 6
RA 228,1,3072
CF 130,44,45
Z 0,12,0
LO 117,18,117
LU 18,117
CF 131,46,47
M 0,13,1
s_mul_i32 s53, s50, 25600
HS 24,18
LK 40,248,74,0
CF 132,48,49
LK 44,250,74,2
s_add_u32 s40, %[wlo], s53
RA 248,7,1536
CF 133,50,51
GZ 0
s_addc_u32 s41, %[whi], 0
CF 134,248,249
CF 135,250,251
LK 48,252,75,0
CF 136,40,41
LK 248,254,75,2
s_mov_b32 m0, s38
RA 252,7,1792
CF 137,42,43
GS 1
WL 5
CF 138,44,45
CF 139,46,47
LK 40,240,76,0
CF 140,48,49
LK 44,242,76,2
RA 240,7,2048
CF 141,50,51
GS 2
WL 5
CF 142,248,249
CF 143,250,251
LK 48,244,77,0
CF 144,40,41
LK 248,246,77,2
RA 244,7,2304
CF 145,42,43
GS 3
CF 146,44,45
CF 147,46,47
Z 1,24,0
LL 17,82
LL 107,83
M 1,25,1
LA 17
RA 232,1,4096
Z 2,36,0
LC_ 107
HX 24
M 2,37,1
WL 6
W1 24,0
LK 40,248,78,0
CF 148,48,49
LK 44,250,78,2
RA 248,7,2560
CF 149,50,51
GS 4
CF 150,248,249
CF 151,250,251
Z 3,48,0
LO 16,16,106
LU 106,16
M 3,49,1
RA 236,1,6144
HZ 25,106
M 0,14,2
LL 18,84
LL 110,85
M 0,15,3
WL 8
LA 18
PK 106,16,104,33
LK 48,252,79,0
CF 152,40,41
LK 248,254,79,2
RA 252,7,2816
CF 153,42,43
GS 5
CF 154,44,45
CF 155,46,47
M 1,26,2
LC_ 110
PK 104,16,104,32
M 1,27,3
RA 216,1,7168
v_add_f32 v14, v14, v15
M 2,38,2
WL 7
LO 17,17,107
LU 107,17
v_add_f32_dpp v14, v14, v14 quad_perm:[1,0,3,2] row_mask:0xf bank_mask:0xf
M 2,39,3
DW 0
HS 25,107
LK 40,240,80,0
CF 156,48,49
LK 44,242,80,2
s_add_u32 s42, s44, s60
CF 157,50,51
v_add_f32_dpp v14, v14, v14 quad_perm:[2,3,0,1] row_mask:0xf bank_mask:0xf
GS 6
s_addc_u32 s43, s45, 0
CF 158,248,249
CF 159,250,251
M 3,50,2
WL 4
LL 16,86
LL 111,87
M 3,51,3
LA 16
PK 107,17,105,33
M 0,16,4
LC_ 111
PK 105,17,105,32
v_add_f32_dpp v14, v14, v14 row_half_mirror row_mask:0xf bank_mask:0xf
M 0,17,5
RA 220,1,8192
v_cvt_f64_f32 v[14:15], v14
LK 48,244,81,0
CF 160,40,41
LK 248,246,81,2
CF 161,42,43
GS 7
CF 162,44,45
CF 163,46,47
M 1,28,4
WL 4
LO 18,18,110
LU 110,18
M 1,29,5
RA 224,1,9216
HS 25,110
M 2,40,4
LL 17,88
LL 114,89
s_mov_b64 exec, s[54:55]
SP
s_mov_b64 exec, -1
M 2,41,5
LA 17
PK 110,18,108,33
LK 40,248,82,0
CF 164,48,49
LK 44,250,82,2
CF 165,50,51
GS 8
CF 166,248,249
CF 167,250,251
M 3,52,4
WL 2
LC_ 114
PK 108,18,108,32
M 3,53,5
RA 228,1,10240
PO 110
M 0,18,0
LO 16,16,111
LU 111,16
ST 104
M 0,19,1
DW 1024
HS 25,111
LK 48,252,83,0
CF 168,40,41
LK 248,254,83,2
s_add_u32 s42, s44, s61
CF 169,42,43
GS 9
s_addc_u32 s43, s45, 0
CF 170,44,45
CF 171,46,47
M 1,30,0
LL 18,90
LL 115,91
M 1,31,1
LA 18
PK 111,16,109,33
M 2,42,0
LC_ 115
PK 109,16,109,32
M 2,43,1
RA 232,1,11264
PO 111
M 3,54,0
WL 3
LO 17,17,114
LU 114,17
M 3,55,1
RV 252,22,5392
HS 25,114
M 0,20,2
LL 16,92
LL 118,93
M 0,21,3
LA 16
PK 114,17,112,33
M 1,32,2
LC_ 118
PK 112,17,112,32
M 1,33,3
RA 236,1,12288
RV 36,8,6912
DW 2048
GS 10
CF 172,48,49
CF 173,50,51
M 2,44,2
WL 6
LO 18,18,115
LU 115,18
M 2,45,3
RA 216,1,13312
HS 25,115
ST 108
M 3,56,2
WL 2
LL 17,94
LL 119,95
M 3,57,3
s_add_u32 s42, s44, s62
LA 17
PK 115,18,113,33
M 0,22,4
s_addc_u32 s43, s45, 0
LC_ 119
PK 113,18,113,32
M 0,23,5
s_add_u32 s40, s40, 3072
RA 220,1,14336
CF 174,248,249
M 1,34,4
LO 16,16,118
LU 118,16
CF 175,250,251
M 1,35,5
s_addc_u32 s41, s41, 0
RA 224,1,15360
HS 25,118
GS 11
s_add_u32 m0, s38, 3072
ST 112
s_nop 5
v_cndmask_b32 v52, v52, v53, s[78:79]
M 2,46,4
LL 18,56
LL 104,57
v_cndmask_b32 v54, v54, v55, s[78:79]
M 2,47,5
LA 18
PK 118,16,116,33
v_cndmask_b32 v52, v52, v54, s[80:81]
M 3,58,4
WL 2
LC_ 104
PK 116,16,116,32
M 3,59,5
RA 228,1,16384
PO 118
v_mul_f32 v52, 0x3b2aaaab, v52
Z 6,12,0
LO 17,17,119
LU 119,17
v_med3_f32 v53, v52, v39, v37
M 6,13,1
DW 0
HS 25,119
LG 52,53
Z 7,24,0
s_add_u32 s42, s44, s63
LL 16,58
LL 105,59
C 52,52,52
M 7,25,1
s_addc_u32 s43, s45, 0
LA 16
PK 119,17,117,33
Z 8,36,0
LC_ 105
PK 117,17,117,32
v_lshlrev_b32 v52, 16, v52
M 8,37,1
RA 232,1,17408
PO 119
v_add_f32 v52, v36, v52
Z 9,48,0
WL 3
LO 104,18,104
LU 18,104
C 52,52,52
M 9,49,1
RA 236,1,18432
HZ 24,18
M 6,14,2
LL 17,60
LL 108,61
v_lshlrev_b32 v52, 16, v52
M 6,15,3
LA 17
ST 116
HX 25
v_sqrt_f32 v52, v52
M 7,26,2
LC_ 108
W1 25,1024
C 52,52,52
M 7,27,3
RA 216,1,19456
v_add_u32 v4, s39, v4
v_lshlrev_b32 v52, 16, v52
M 8,38,2
WL 6
LO 105,16,105
LU 16,105
M 8,39,3
DW 1024
HS 24,16
v_rcp_f32 v52, v52
M 9,50,2
WL 4
LL 18,62
LL 109,63
v_mul_f32 v52, v38, v52
M 9,51,3
s_add_u32 s37, s37, 32768
LA 18
RA 220,1,20480
CF 52,52,52
M 6,16,4
s_cmp_eq_u32 s37, s75
LC_ 109
ds_bpermute_b32 v13, v28, v52
s_cselect_b32 s37, s74, s37
M 6,17,5
DW 2048
M 7,28,4
WL 6
)"
R"(LO 108,17,108
LU 17,108
M 7,29,5
s_mul_i32 s53, s51, 24
RA 224,1,21504
HS 24,17
M 8,40,4
s_bitcmp1_b32 s51, 0
LL 16,64
LL 112,65
M 8,41,5
s_addc_u32 s53, s53, 0
LA 16
M 9,52,4
WL 4
LC_ 112
M 9,53,5
s_lshl_b32 s53, s53, 11
RA 228,1,22528
M 6,18,0
WL 4
LO 109,18,109
LU 18,109
M 6,19,1
s_bitcmp1_b64 s[90:91], s51
HS 24,18
s_cselect_b64 s[44:45], s[84:85], s[82:83]
s_add_u32 s44, s44, s53
s_addc_u32 s45, s45, 0
s_add_u32 s51, s51, %[d]
s_cmp_eq_u32 s51, %[wrap]
s_cselect_b32 s51, %[rst], s51
M 7,30,0
s_lshl_b32 s53, s52, 8
LL 17,66
LL 113,67
M 7,31,1
s_add_u32 s48, %[alo], s53
LA 17
RA 232,1,23552
XS 128
M 8,42,0
s_addc_u32 s49, %[ahi], 0
LC_ 113
XS 129
M 8,43,1
s_add_u32 s52, s52, %[d]
XS 130
XS 131
M 9,54,0
WL 3
LO 112,16,112
LU 16,112
M 9,55,1
s_cmp_eq_u32 s52, %[wrap]
RA 236,1,24576
HS 24,16
AW 168,128
AW 169,129
M 6,20,2
s_cselect_b32 s52, %[rst], s52
DQ 3072
LL 18,68
LL 116,69
AW 170,130
M 6,21,3
LA 18
v_add_u32 v1, s37, v0
AW 171,131
M 7,32,2
s_add_u32 s50, s50, %[d]
LC_ 116
XS 132
M 7,33,3
s_cmp_eq_u32 s50, %[wrap]
RA 216,1,0
v_and_b32 v22, 0xfffffcff, v1
XS 133
M 8,44,2
WL 4
LO 113,17,113
LU 17,113
XS 134
M 8,45,3
s_cselect_b32 s50, %[rst], s50
HS 24,17
XS 135
M 9,56,2
WL 3
LL 16,70
LL 117,71
AW 172,132
M 9,57,3
WV 21
LA 16
RA 220,1,1024
AW 173,133
M 6,22,4
s_barrier
LC_ 117
RV 252,22,5120
AW 174,134
M 6,23,5
s_sub_u32 s39, 0, s39
AW 175,135
v_min_u32 v35, v35, v13
M 7,34,4
WL 5
LO 116,18,116
LU 18,116
M 7,35,5
s_add_u32 s38, s38, 32768
RA 224,1,2048
HS 24,18
XS 136
M 8,46,4
s_cmp_eq_u32 s38, s77
LL 17,80
LL 106,81
XS 137
M 8,47,5
s_cselect_b32 s38, s76, s38
LA 17
XS 138
XS 139
M 9,58,4
WL 4
LC_ 106
AW 176,136
M 9,59,5
WL 3
RA 228,1,3072
AW 177,137
Z 0,12,0
LO 117,16,117
LU 16,117
AW 178,138
M 0,13,1
HS 24,16
AW 179,139
Z 1,24,0
LL 18,82
LL 107,83
XS 140
M 1,25,1
LA 18
RA 232,1,4096
XS 141
Z 2,36,0
LC_ 107
HX 24
XS 142
M 2,37,1
W1 24,0
XS 143
Z 3,48,0
WL 5
LO 17,17,106
LU 106,17
AW 180,140
M 3,49,1
HZ 25,106
AW 181,141
Z 4,60,0
LL 16,84
LL 110,85
AW 182,142
M 4,61,1
LA 16
PK 106,17,104,33
AW 183,143
Z 5,72,0
WL 5
LC_ 110
PK 104,17,104,32
XS 144
M 5,73,1
RA 236,1,6144
XS 145
XS 146
M 0,14,2
WL 4
LO 18,18,107
LU 107,18
XS 147
M 0,15,3
s_add_u32 s42, s44, s60
HS 25,107
AW 184,144
M 1,26,2
s_addc_u32 s43, s45, 0
LL 17,86
LL 111,87
AW 185,145
M 1,27,3
LA 17
PK 107,18,105,33
AW 186,146
M 2,38,2
LC_ 111
PK 105,18,105,32
AW 187,147
M 2,39,3
RA 216,1,7168
XS 148
M 3,50,2
WL 4
LO 16,16,110
LU 110,16
XS 149
M 3,51,3
ST 104
HS 25,110
XS 150
M 4,62,2
LL 18,88
LL 114,89
XS 151
M 4,63,3
LA 18
PK 110,16,108,33
AW 188,148
M 5,74,2
WL 4
LC_ 114
PK 108,16,108,32
AW 189,149
M 5,75,3
s_mov_b32 m0, %[pm]
RA 220,1,8192
PO 110
AW 190,150
M 0,16,4
WL 4
LO 17,17,111
LU 111,17
AW 191,151
M 0,17,5
DF
HS 25,111
XS 152
M 1,28,4
s_add_u32 s42, s44, s61
LL 16,90
LL 115,91
XS 153
XS 154
M 1,29,5
s_addc_u32 s43, s45, 0
LA 16
PK 111,17,109,33
XS 155
M 2,40,4
LC_ 115
PK 109,17,109,32
AW 192,152
M 2,41,5
RA 224,1,9216
PO 111
AW 193,153
M 3,52,4
WL 4
LO 18,18,114
LU 114,18
AW 194,154
M 3,53,5
ST 108
HS 25,114
AW 195,155
M 4,64,4
LL 17,92
LL 118,93
XS 156
M 4,65,5
LA 17
PK 114,18,112,33
XS 157
M 5,76,4
WL 4
LC_ 118
PK 112,18,112,32
XS 158
M 5,77,5
RA 228,1,10240
XS 159
M 0,18,0
WL 4
LO 16,16,115
LU 115,16
AW 196,156
M 0,19,1
s_add_u32 s42, s44, s62
HS 25,115
AW 197,157
M 1,30,0
s_addc_u32 s43, s45, 0
LL 18,94
LL 119,95
AW 198,158
M 1,31,1
LA 18
PK 115,16,113,33
AW 199,159
M 2,42,0
LC_ 119
PK 113,16,113,32
XS 160
XS 161
M 2,43,1
RA 232,1,11264
XS 162
M 3,54,0
WL 4
LO 17,17,118
LU 118,17
XS 163
M 3,55,1
RV 252,22,5392
HS 25,118
AW 200,160
M 4,66,0
ST 112
AW 201,161
M 4,67,1
RA 236,1,12288
PK 118,17,116,33
AW 202,162
M 5,78,0
WL 5
PK 116,17,116,32
AW 203,163
M 5,79,1
PO 118
XS 164
M 0,20,2
WL 3
LO 18,18,119
LU 119,18
XS 165
M 0,21,3
s_add_u32 s42, s44, s63
HS 25,119
XS 166
M 1,32,2
s_addc_u32 s43, s45, 0
HX 25
XS 167
M 1,33,3
RA 216,1,13312
PK 119,18,117,33
AW 204,164
M 2,44,2
W1 25,1024
PK 117,18,117,32
AW 205,165
M 2,45,3
PO 119
v_add_u32 v4, s39, v4
AW 206,166
M 3,56,2
ST 116
AW 207,167
XS 168
M 3,57,3
XS 169
XS 170
M 4,68,2
XS 171
AW 208,168
M 4,69,3
RA 220,1,14336
AW 209,169
M 5,80,2
WL 3
AW 210,170
AW 211,171
M 5,81,3
XS 172
XS 173
M 0,22,4
XS 174
XS 175
M 0,23,5
AW 212,172
AW 213,173
M 1,34,4
AW 214,174
AW 215,175
M 1,35,5
RA 224,1,15360
M 2,46,4
LL 16,56
LL 104,57
M 2,47,5
LA 16
ds_read_b64 v[14:15], v5 offset:0
M 3,58,4
LC_ 104
v_subrev_u32 v5, s39, v5
M 3,59,5
)"
R"(M 4,70,4
M 4,71,5
RA 228,1,16384
M 5,82,4
WL 5
LL 17,58
LL 105,59
M 5,83,5
LA 17
Z 6,12,0
LC_ 105
M 6,13,1
Z 7,24,0
WL 3
LO 104,16,104
LU 16,104
M 7,25,1
RA 232,1,17408
HZ 24,16
Z 8,36,0
LL 18,60
LL 108,61
M 8,37,1
s_mul_i32 s53, s50, 25600
LA 18
v_add_f32 v14, v14, v15
Z 9,48,0
s_add_u32 s40, %[wlo], s53
LC_ 108
v_add_f32_dpp v14, v14, v14 quad_perm:[1,0,3,2] row_mask:0xf bank_mask:0xf
M 9,49,1
s_addc_u32 s41, %[whi], 0
v_add_f32_dpp v14, v14, v14 quad_perm:[2,3,0,1] row_mask:0xf bank_mask:0xf
Z 10,60,0
WL 3
LO 105,17,105
LU 17,105
M 10,61,1
s_mov_b32 m0, s38
RA 236,1,18432
HS 24,17
Z 11,72,0
LL 16,62
LL 109,63
M 11,73,1
LA 16
DW 0
M 6,14,2
LC_ 109
v_add_f32_dpp v14, v14, v14 row_half_mirror row_mask:0xf bank_mask:0xf
M 6,15,3
v_cvt_f64_f32 v[14:15], v14
M 7,26,2
WL 3
LO 108,18,108
LU 18,108
M 7,27,3
s_add_u32 s37, s37, 32768
RA 216,1,19456
HS 24,18
M 8,38,2
s_cmp_eq_u32 s37, s75
LL 17,64
LL 112,65
M 8,39,3
s_cselect_b32 s37, s74, s37
LA 17
s_mov_b64 exec, s[54:55]
SP
s_mov_b64 exec, -1
M 9,50,2
LC_ 112
DW 1024
M 9,51,3
M 10,62,2
WL 3
LO 109,16,109
LU 16,109
M 10,63,3
RA 220,1,20480
HS 24,16
M 11,74,2
LL 18,66
LL 113,67
M 11,75,3
LA 18
DW 2048
M 6,16,4
LC_ 113
M 6,17,5
M 7,28,4
WL 3
LO 112,17,112
LU 17,112
M 7,29,5
s_mul_i32 s53, s51, 24
RA 224,1,21504
HS 24,17
M 8,40,4
s_bitcmp1_b32 s51, 0
LL 16,68
LL 116,69
M 8,41,5
s_addc_u32 s53, s53, 0
LA 16
M 9,52,4
s_lshl_b32 s53, s53, 11
LC_ 116
M 9,53,5
s_bitcmp1_b64 s[90:91], s51
M 10,64,4
WL 3
LO 113,18,113
LU 18,113
M 10,65,5
s_cselect_b64 s[44:45], s[84:85], s[82:83]
RA 228,1,22528
HS 24,18
M 11,76,4
LL 17,70
LL 117,71
M 11,77,5
s_add_u32 s44, s44, s53
LA 17
M 6,18,0
s_addc_u32 s45, s45, 0
LC_ 117
M 6,19,1
s_add_u32 s51, s51, %[d]
M 7,30,0
WL 3
LO 116,16,116
LU 16,116
M 7,31,1
s_cmp_eq_u32 s51, %[wrap]
RA 232,1,23552
HS 24,16
M 8,42,0
s_cselect_b32 s51, %[rst], s51
LL 18,72
LL 120,73
M 8,43,1
s_add_u32 s40, s40, 3072
LA 18
M 9,54,0
s_addc_u32 s41, s41, 0
LC_ 120
s_add_u32 m0, s38, 3072
M 9,55,1
DW 0
M 10,66,0
WL 3
LO 117,17,117
LU 17,117
M 10,67,1
RA 236,1,24576
HS 24,17
M 11,78,0
LL 16,74
LL 121,75
M 11,79,1
LA 16
DW 1024
M 6,20,2
LC_ 121
DW 2048
M 6,21,3
s_lshl_b32 s53, s52, 8
v_add_u32 v1, s37, v0
M 7,32,2
WL 3
LO 120,18,120
LU 18,120
M 7,33,3
s_add_u32 s48, %[alo], s53
RA 216,1,0
HS 24,18
M 8,44,2
s_addc_u32 s49, %[ahi], 0
LL 17,76
LL 124,77
M 8,45,3
s_add_u32 s52, s52, %[d]
LA 17
v_and_b32 v22, 0xfffffcff, v1
M 9,56,2
s_cmp_eq_u32 s52, %[wrap]
LC_ 124
M 9,57,3
s_cselect_b32 s52, %[rst], s52
DQ 3072
M 10,68,2
WL 3
LO 121,16,121
LU 16,121
M 10,69,3
s_add_u32 s50, s50, %[d]
RA 220,1,1024
HS 24,16
M 11,80,2
LL 18,78
LL 125,79
M 11,81,3
s_cmp_eq_u32 s50, %[wrap]
LA 18
RV 252,22,5120
M 6,22,4
s_cselect_b32 s50, %[rst], s50
LC_ 125
M 6,23,5
WV 25
M 7,34,4
WL 4
LO 124,17,124
LU 17,124
M 7,35,5
s_add_u32 s38, s38, 32768
RA 224,1,2048
HS 24,17
M 8,46,4
s_cmp_eq_u32 s38, s77
LL 16,80
LL 106,81
M 8,47,5
s_cselect_b32 s38, s76, s38
LA 16
M 9,58,4
s_barrier
LC_ 106
M 9,59,5
s_sub_u32 s39, 0, s39
M 10,70,4
WL 3
LO 125,18,125
LU 18,125
M 10,71,5
RA 228,1,3072
HS 24,18
M 11,82,4
LL 17,82
LL 107,83
M 11,83,5
LA 17
HX 24
Z 0,12,0
LC_ 107
W1 24,0
M 0,13,1
Z 1,24,0
WL 4
LO 16,16,106
LU 106,16
M 1,25,1
RA 232,1,4096
HZ 25,106
Z 2,36,0
LL 18,84
LL 110,85
M 2,37,1
LA 18
PK 106,16,104,33
Z 3,48,0
LC_ 110
PK 104,16,104,32
M 3,49,1
Z 4,60,0
WL 4
LO 17,17,107
LU 107,17
M 4,61,1
s_add_u32 s42, s44, s60
RA 236,1,6144
HS 25,107
Z 5,72,0
LL 16,86
LL 111,87
M 5,73,1
s_addc_u32 s43, s45, 0
LA 16
PK 107,17,105,33
M 0,14,2
LC_ 111
PK 105,17,105,32
M 0,15,3
ST 104
M 1,26,2
WL 3
LO 18,18,110
LU 110,18
M 1,27,3
RA 216,1,7168
HS 25,110
M 2,38,2
LL 17,88
LL 114,89
M 2,39,3
LA 17
PK 110,18,108,33
M 3,50,2
LC_ 114
PK 108,18,108,32
M 3,51,3
PO 110
M 4,62,2
WL 3
LO 16,16,111
LU 111,16
M 4,63,3
s_add_u32 s42, s44, s61
RA 220,1,8192
HS 25,111
M 5,74,2
LL 18,90
LL 115,91
M 5,75,3
s_addc_u32 s43, s45, 0
LA 18
PK 111,16,109,33
M 0,16,4
s_mov_b32 m0, %[pm]
LC_ 115
PK 109,16,109,32
M 0,17,5
DF
PO 111
M 1,28,4
WL 3
LO 17,17,114
LU 114,17
M 1,29,5
RA 224,1,9216
HS 25,114
M 2,40,4
LL 16,92
LL 118,93
M 2,41,5
LA 16
PK 114,17,112,33
M 3,52,4
LC_ 118
PK 112,17,112,32
M 3,53,5
ST 108
M 4,64,4
WL 3
LO 18,18,115
LU 115,18
M 4,65,5
s_add_u32 s42, s44, s62
RA 228,1,10240
HS 25,115
M 5,76,4
LL 17,94
LL 119,95
M 5,77,5
s_addc_u32 s43, s45, 0
LA 17
PK 115,18,113,33
M 0,18,0
LC_ 119
PK 113,18,113,32
M 0,19,1
ST 112
M 1,30,0
WL 3
LO 16,16,118
LU 118,16
M 1,31,1
RA 232,1,11264
HS 25,118
M 2,42,0
LL 18,96
LL 122,97
M 2,43,1
LA 18
PK 118,16,116,33
M 3,54,0
LC_ 122
PK 116,16,116,32
M 3,55,1
RV 252,22,5392
PO 118
M 4,66,0
WL 4
LO 17,17,119
LU 119,17
M 4,67,1
s_add_u32 s42, s44, s63
RA 236,1,12288
HS 25,119
M 5,78,0
LL 16,98
LL 123,99
M 5,79,1
s_addc_u32 s43, s45, 0
LA 16
PK 119,17,117,33
M 0,20,2
LC_ 123
PK 117,17,117,32
)"
R"(M 0,21,3
PO 119
M 1,32,2
WL 4
LO 18,18,122
LU 122,18
M 1,33,3
RA 216,1,13312
HS 25,122
M 2,44,2
LL 17,100
LL 126,101
M 2,45,3
LA 17
PK 122,18,120,33
M 3,56,2
LC_ 126
PK 120,18,120,32
M 3,57,3
ST 116
M 4,68,2
WL 3
LO 16,16,123
LU 123,16
M 4,69,3
s_add_u32 s42, s44, s64
RA 220,1,14336
HS 25,123
M 5,80,2
LL 18,102
LL 127,103
M 5,81,3
s_addc_u32 s43, s45, 0
LA 18
PK 123,16,121,33
M 0,22,4
LC_ 127
PK 121,16,121,32
M 0,23,5
ST 120
M 1,34,4
WL 3
LO 17,17,126
LU 126,17
M 1,35,5
RA 224,1,15360
HS 25,126
M 2,46,4
LL 16,56
LL 104,57
M 2,47,5
LA 16
PK 126,17,124,33
M 3,58,4
LC_ 104
PK 124,17,124,32
M 3,59,5
ds_read_b64 v[14:15], v5 offset:0
PO 126
M 4,70,4
WL 4
LO 18,18,127
LU 127,18
M 4,71,5
s_add_u32 s42, s44, s65
RA 228,1,16384
HS 25,127
M 5,82,4
LL 17,58
LL 105,59
M 5,83,5
s_addc_u32 s43, s45, 0
LA 17
PK 127,18,125,33
Z 6,12,0
LC_ 105
PK 125,18,125,32
M 6,13,1
PO 127
v_subrev_u32 v5, s39, v5
Z 7,24,0
WL 4
LO 104,16,104
LU 16,104
M 7,25,1
RA 232,1,17408
HZ 24,16
Z 8,36,0
WL 4
LL 18,60
LL 108,61
M 8,37,1
s_mul_i32 s53, s50, 25600
LA 18
v_add_f32 v14, v14, v15
Z 9,48,0
s_add_u32 s40, %[wlo], s53
LC_ 108
v_add_f32_dpp v14, v14, v14 quad_perm:[1,0,3,2] row_mask:0xf bank_mask:0xf
M 9,49,1
s_addc_u32 s41, %[whi], 0
v_add_f32_dpp v14, v14, v14 quad_perm:[2,3,0,1] row_mask:0xf bank_mask:0xf
HX 25
Z 10,60,0
WL 3
LO 105,17,105
LU 17,105
M 10,61,1
s_mov_b32 m0, s38
RA 236,1,18432
HS 24,17
Z 11,72,0
LL 16,62
LL 109,63
M 11,73,1
LA 16
W1 25,1024
M 6,14,2
LC_ 109
ST 124
M 6,15,3
DW 0
v_add_f32_dpp v14, v14, v14 row_half_mirror row_mask:0xf bank_mask:0xf
M 7,26,2
WL 4
LO 108,18,108
LU 18,108
M 7,27,3
s_add_u32 s37, s37, 32768
RA 216,1,19456
HS 24,18
M 8,38,2
s_cmp_eq_u32 s37, s75
LL 17,64
LL 112,65
M 8,39,3
s_cselect_b32 s37, s74, s37
LA 17
DW 1024
M 9,50,2
LC_ 112
v_cvt_f64_f32 v[14:15], v14
M 9,51,3
s_mov_b64 exec, s[54:55]
SP
s_mov_b64 exec, -1
v_add_u32 v4, s39, v4
M 10,62,2
WL 3
LO 109,16,109
LU 16,109
M 10,63,3
RA 220,1,20480
HS 24,16
M 11,74,2
LL 18,66
LL 113,67
M 11,75,3
LA 18
DW 2048
M 6,16,4
LC_ 113
M 6,17,5
M 7,28,4
WL 3
LO 112,17,112
LU 17,112
M 7,29,5
s_mul_i32 s53, s51, 24
RA 224,1,21504
HS 24,17
M 8,40,4
s_bitcmp1_b32 s51, 0
LL 16,68
LL 116,69
M 8,41,5
s_addc_u32 s53, s53, 0
LA 16
M 9,52,4
s_lshl_b32 s53, s53, 11
LC_ 116
M 9,53,5
s_bitcmp1_b64 s[90:91], s51
M 10,64,4
WL 3
LO 113,18,113
LU 18,113
M 10,65,5
s_cselect_b64 s[44:45], s[84:85], s[82:83]
RA 228,1,22528
HS 24,18
M 11,76,4
LL 17,70
LL 117,71
M 11,77,5
s_add_u32 s44, s44, s53
LA 17
M 6,18,0
s_addc_u32 s45, s45, 0
LC_ 117
M 6,19,1
s_add_u32 s51, s51, %[d]
M 7,30,0
WL 3
LO 116,16,116
LU 16,116
M 7,31,1
s_cmp_eq_u32 s51, %[wrap]
RA 232,1,23552
HS 24,16
M 8,42,0
s_cselect_b32 s51, %[rst], s51
LL 18,72
LL 120,73
M 8,43,1
s_add_u32 s40, s40, 3072
LA 18
M 9,54,0
s_addc_u32 s41, s41, 0
LC_ 120
s_add_u32 m0, s38, 3072
M 9,55,1
DW 0
M 10,66,0
WL 3
LO 117,17,117
LU 17,117
M 10,67,1
RA 236,1,24576
HS 24,17
M 11,78,0
LL 16,74
LL 121,75
M 11,79,1
LA 16
DW 1024
M 6,20,2
LC_ 121
DW 2048
M 6,21,3
s_lshl_b32 s53, s52, 8
v_add_u32 v1, s37, v0
M 7,32,2
WL 3
LO 120,18,120
LU 18,120
M 7,33,3
s_add_u32 s48, %[alo], s53
RA 216,1,0
HS 24,18
M 8,44,2
s_addc_u32 s49, %[ahi], 0
LL 17,76
LL 124,77
M 8,45,3
s_add_u32 s52, s52, %[d]
LA 17
v_and_b32 v22, 0xfffffcff, v1
M 9,56,2
s_cmp_eq_u32 s52, %[wrap]
LC_ 124
M 9,57,3
s_cselect_b32 s52, %[rst], s52
DQ 3072
M 10,68,2
WL 3
LO 121,16,121
LU 16,121
M 10,69,3
s_add_u32 s50, s50, %[d]
RA 220,1,1024
HS 24,16
M 11,80,2
LL 18,78
LL 125,79
M 11,81,3
s_cmp_eq_u32 s50, %[wrap]
LA 18
RV 252,22,5120
M 6,22,4
s_cselect_b32 s50, %[rst], s50
LC_ 125
M 6,23,5
WV 28
v_add_u32 v19, 1, v19
M 7,34,4
WL 4
LO 124,17,124
LU 17,124
M 7,35,5
s_add_u32 s38, s38, 32768
RA 224,1,2048
HS 24,17
M 8,46,4
s_cmp_eq_u32 s38, s77
LL 16,80
LL 106,81
M 8,47,5
s_cselect_b32 s38, s76, s38
LA 16
FP
M 9,58,4
s_sub_u32 s39, 0, s39
LC_ 106
M 9,59,5
M 10,70,4
WL 4
LO 125,18,125
LU 18,125
M 10,71,5
RA 228,1,3072
HS 24,18
M 11,82,4
LL 17,82
LL 107,83
M 11,83,5
s_mov_b32 s66, 28
1:
LA 17
HX 24
Z 0,12,0
LC_ 107
M 0,13,1
Z 1,24,0
WL 3
LO 16,16,106
LU 106,16
M 1,25,1
RA 232,1,4096
HZ 25,106
Z 2,36,0
LL 18,84
LL 110,85
M 2,37,1
LA 18
PK 106,16,104,33
Z 3,48,0
LC_ 110
PK 104,16,104,32
M 3,49,1
Z 4,60,0
WL 3
LO 17,17,107
LU 107,17
M 4,61,1
s_add_u32 s42, s44, s60
RA 236,1,6144
HS 25,107
Z 5,72,0
LL 16,86
LL 111,87
M 5,73,1
s_addc_u32 s43, s45, 0
LA 16
PK 107,17,105,33
M 0,14,2
LC_ 111
PK 105,17,105,32
M 0,15,3
ST 104
M 1,26,2
WL 3
LO 18,18,110
LU 110,18
M 1,27,3
RA 216,1,7168
HS 25,110
M 2,38,2
LL 17,88
LL 114,89
M 2,39,3
LA 17
PK 110,18,108,33
M 3,50,2
LC_ 114
PK 108,18,108,32
M 3,51,3
FR
PO 110
M 4,62,2
WL 4
LO 16,16,111
LU 111,16
M 4,63,3
s_add_u32 s42, s44, s61
RA 220,1,8192
HS 25,111
M 5,74,2
LL 18,90
LL 115,91
M 5,75,3
s_addc_u32 s43, s45, 0
LA 18
PK 111,16,109,33
M 0,16,4
s_mov_b32 m0, %[pm]
LC_ 115
PK 109,16,109,32
M 0,17,5
DF
)"
R"(PO 111
M 1,28,4
WL 4
LO 17,17,114
LU 114,17
M 1,29,5
RA 224,1,9216
HS 25,114
M 2,40,4
LL 16,92
LL 118,93
M 2,41,5
LA 16
PK 114,17,112,33
M 3,52,4
LC_ 118
PK 112,17,112,32
M 3,53,5
ST 108
M 4,64,4
WL 3
LO 18,18,115
LU 115,18
M 4,65,5
s_add_u32 s42, s44, s62
RA 228,1,10240
HS 25,115
M 5,76,4
LL 17,94
LL 119,95
M 5,77,5
s_addc_u32 s43, s45, 0
LA 17
PK 115,18,113,33
M 0,18,0
LC_ 119
PK 113,18,113,32
M 0,19,1
ST 112
M 1,30,0
WL 3
LO 16,16,118
LU 118,16
M 1,31,1
RA 232,1,11264
HS 25,118
M 2,42,0
LL 18,96
LL 122,97
M 2,43,1
LA 18
PK 118,16,116,33
v_cmp_gt_u32 vcc, v19, v15
M 3,54,0
LC_ 122
PK 116,16,116,32
M 3,55,1
RV 252,22,5392
PO 118
M 4,66,0
WL 4
LO 17,17,119
LU 119,17
M 4,67,1
s_cbranch_vccnz .Lk1s0
.Lk1r0:
RA 236,1,12288
HS 25,119
M 5,78,0
LL 16,98
LL 123,99
M 5,79,1
s_add_u32 s42, s44, s63
LA 16
PK 119,17,117,33
M 0,20,2
s_addc_u32 s43, s45, 0
LC_ 123
PK 117,17,117,32
M 0,21,3
W1 24,0
PO 119
M 1,32,2
WL 5
LO 18,18,122
LU 122,18
M 1,33,3
RA 216,1,13312
HS 25,122
M 2,44,2
LL 17,100
LL 126,101
M 2,45,3
LA 17
PK 122,18,120,33
M 3,56,2
LC_ 126
PK 120,18,120,32
M 3,57,3
ST 116
M 4,68,2
WL 4
LO 16,16,123
LU 123,16
M 4,69,3
s_add_u32 s42, s44, s64
RA 220,1,14336
HS 25,123
M 5,80,2
LL 18,102
LL 127,103
M 5,81,3
s_addc_u32 s43, s45, 0
LA 18
PK 123,16,121,33
M 0,22,4
LC_ 127
PK 121,16,121,32
M 0,23,5
ST 120
M 1,34,4
WL 3
LO 17,17,126
LU 126,17
M 1,35,5
RA 224,1,15360
HS 25,126
M 2,46,4
LL 16,56
LL 104,57
M 2,47,5
LA 16
PK 126,17,124,33
M 3,58,4
LC_ 104
PK 124,17,124,32
M 3,59,5
ds_read_b64 v[14:15], v5 offset:0
PO 126
M 4,70,4
WL 4
LO 18,18,127
LU 127,18
M 4,71,5
s_add_u32 s42, s44, s65
RA 228,1,16384
HS 25,127
M 5,82,4
LL 17,58
LL 105,59
M 5,83,5
s_addc_u32 s43, s45, 0
LA 17
PK 127,18,125,33
Z 6,12,0
LC_ 105
PK 125,18,125,32
M 6,13,1
PO 127
v_subrev_u32 v5, s39, v5
Z 7,24,0
WL 4
LO 104,16,104
LU 16,104
M 7,25,1
RA 232,1,17408
HZ 24,16
Z 8,36,0
WL 4
LL 18,60
LL 108,61
M 8,37,1
s_mul_i32 s53, s50, 25600
LA 18
v_add_f32 v14, v14, v15
Z 9,48,0
s_add_u32 s40, %[wlo], s53
LC_ 108
v_add_f32_dpp v14, v14, v14 quad_perm:[1,0,3,2] row_mask:0xf bank_mask:0xf
M 9,49,1
s_addc_u32 s41, %[whi], 0
Z 10,60,0
WL 3
LO 105,17,105
LU 17,105
M 10,61,1
s_mov_b32 m0, s38
RA 236,1,18432
HS 24,17
Z 11,72,0
LL 16,62
LL 109,63
M 11,73,1
LA 16
ST 124
M 6,14,2
LC_ 109
DW 0
M 6,15,3
v_add_f32_dpp v14, v14, v14 quad_perm:[2,3,0,1] row_mask:0xf bank_mask:0xf
HX 25
M 7,26,2
WL 3
LO 108,18,108
LU 18,108
W1 25,1024
M 7,27,3
s_add_u32 s37, s37, 32768
RA 216,1,19456
HS 24,18
M 8,38,2
s_cmp_eq_u32 s37, s75
LL 17,64
LL 112,65
M 8,39,3
s_cselect_b32 s37, s74, s37
LA 17
DW 1024
M 9,50,2
LC_ 112
v_add_f32_dpp v14, v14, v14 row_half_mirror row_mask:0xf bank_mask:0xf
M 9,51,3
v_cvt_f64_f32 v[14:15], v14
v_add_u32 v4, s39, v4
M 10,62,2
WL 4
LO 109,16,109
LU 16,109
M 10,63,3
RA 220,1,20480
HS 24,16
M 11,74,2
LL 18,66
LL 113,67
M 11,75,3
LA 18
s_mov_b64 exec, s[54:55]
SP
s_mov_b64 exec, -1
M 6,16,4
LC_ 113
DW 2048
M 6,17,5
M 7,28,4
WL 3
LO 112,17,112
LU 17,112
M 7,29,5
s_mul_i32 s53, s51, 24
RA 224,1,21504
HS 24,17
M 8,40,4
s_bitcmp1_b32 s51, 0
LL 16,68
LL 116,69
M 8,41,5
s_addc_u32 s53, s53, 0
LA 16
M 9,52,4
s_lshl_b32 s53, s53, 11
LC_ 116
M 9,53,5
s_bitcmp1_b64 s[90:91], s51
M 10,64,4
WL 3
LO 113,18,113
LU 18,113
M 10,65,5
s_cselect_b64 s[44:45], s[84:85], s[82:83]
RA 228,1,22528
HS 24,18
M 11,76,4
LL 17,70
LL 117,71
M 11,77,5
s_add_u32 s44, s44, s53
LA 17
M 6,18,0
s_addc_u32 s45, s45, 0
LC_ 117
M 6,19,1
s_add_u32 s51, s51, %[d]
M 7,30,0
WL 3
LO 116,16,116
LU 16,116
M 7,31,1
s_cmp_eq_u32 s51, %[wrap]
RA 232,1,23552
HS 24,16
M 8,42,0
s_cselect_b32 s51, %[rst], s51
LL 18,72
LL 120,73
M 8,43,1
s_add_u32 s40, s40, 3072
LA 18
M 9,54,0
s_addc_u32 s41, s41, 0
LC_ 120
s_add_u32 m0, s38, 3072
M 9,55,1
DW 0
M 10,66,0
WL 3
LO 117,17,117
LU 17,117
M 10,67,1
RA 236,1,24576
HS 24,17
M 11,78,0
LL 16,74
LL 121,75
M 11,79,1
LA 16
DW 1024
M 6,20,2
LC_ 121
DW 2048
M 6,21,3
s_lshl_b32 s53, s52, 8
v_add_u32 v1, s37, v0
M 7,32,2
WL 3
LO 120,18,120
LU 18,120
M 7,33,3
s_add_u32 s48, %[alo], s53
RA 216,1,0
HS 24,18
M 8,44,2
s_addc_u32 s49, %[ahi], 0
LL 17,76
LL 124,77
M 8,45,3
s_add_u32 s52, s52, %[d]
LA 17
v_and_b32 v22, 0xfffffcff, v1
M 9,56,2
s_cmp_eq_u32 s52, %[wrap]
LC_ 124
M 9,57,3
s_cselect_b32 s52, %[rst], s52
DQ 3072
M 10,68,2
WL 3
LO 121,16,121
LU 16,121
M 10,69,3
s_add_u32 s50, s50, %[d]
RA 220,1,1024
HS 24,16
M 11,80,2
LL 18,78
LL 125,79
M 11,81,3
s_cmp_eq_u32 s50, %[wrap]
LA 18
RV 252,22,5120
M 6,22,4
s_cselect_b32 s50, %[rst], s50
LC_ 125
M 6,23,5
WV 30
v_add_u32 v19, 1, v19
M 7,34,4
WL 4
LO 124,17,124
LU 17,124
M 7,35,5
s_add_u32 s38, s38, 32768
RA 224,1,2048
HS 24,17
M 8,46,4
s_cmp_eq_u32 s38, s77
LL 16,80
LL 106,81
M 8,47,5
s_cselect_b32 s38, s76, s38
LA 16
FP
M 9,58,4
s_sub_u32 s39, 0, s39
LC_ 106
M 9,59,5
M 10,70,4
WL 4
LO 125,18,125
LU 18,125
M 10,71,5
RA 228,1,3072
HS 24,18
M 11,82,4
s_sub_u32 s66, s66, 1
LL 17,82
LL 107,83
M 11,83,5
s_cbranch_scc0 1b
7:
LA 17
)"
R"(HX 24
Z 0,12,0
LC_ 107
M 0,13,1
Z 1,24,0
WL 3
LO 16,16,106
LU 106,16
M 1,25,1
RA 232,1,4096
HZ 25,106
Z 2,36,0
LL 18,84
LL 110,85
M 2,37,1
LA 18
PK 106,16,104,33
Z 3,48,0
LC_ 110
PK 104,16,104,32
M 3,49,1
Z 4,60,0
WL 3
LO 17,17,107
LU 107,17
M 4,61,1
s_add_u32 s42, s44, s60
RA 236,1,6144
HS 25,107
Z 5,72,0
LL 16,86
LL 111,87
M 5,73,1
s_addc_u32 s43, s45, 0
LA 16
PK 107,17,105,33
M 0,14,2
LC_ 111
PK 105,17,105,32
M 0,15,3
ST 104
M 1,26,2
WL 3
LO 18,18,110
LU 110,18
M 1,27,3
RA 216,1,7168
HS 25,110
M 2,38,2
LL 17,88
LL 114,89
M 2,39,3
LA 17
PK 110,18,108,33
M 3,50,2
LC_ 114
PK 108,18,108,32
M 3,51,3
FR
PO 110
M 4,62,2
WL 4
LO 16,16,111
LU 111,16
M 4,63,3
s_add_u32 s42, s44, s61
RA 220,1,8192
HS 25,111
M 5,74,2
LL 18,90
LL 115,91
M 5,75,3
s_addc_u32 s43, s45, 0
LA 18
PK 111,16,109,33
M 0,16,4
s_mov_b32 m0, %[pm]
LC_ 115
PK 109,16,109,32
M 0,17,5
DF
PO 111
M 1,28,4
WL 4
LO 17,17,114
LU 114,17
M 1,29,5
RA 224,1,9216
HS 25,114
M 2,40,4
LL 16,92
LL 118,93
M 2,41,5
LA 16
PK 114,17,112,33
M 3,52,4
LC_ 118
PK 112,17,112,32
M 3,53,5
ST 108
M 4,64,4
WL 3
LO 18,18,115
LU 115,18
M 4,65,5
s_add_u32 s42, s44, s62
RA 228,1,10240
HS 25,115
M 5,76,4
LL 17,94
LL 119,95
M 5,77,5
s_addc_u32 s43, s45, 0
LA 17
PK 115,18,113,33
M 0,18,0
LC_ 119
PK 113,18,113,32
M 0,19,1
ST 112
M 1,30,0
WL 3
LO 16,16,118
LU 118,16
M 1,31,1
RA 232,1,11264
HS 25,118
M 2,42,0
LL 18,96
LL 122,97
M 2,43,1
LA 18
PK 118,16,116,33
v_cmp_gt_u32 vcc, v19, v15
M 3,54,0
LC_ 122
PK 116,16,116,32
M 3,55,1
RV 252,22,5392
PO 118
M 4,66,0
WL 4
LO 17,17,119
LU 119,17
M 4,67,1
s_cbranch_vccnz .Lk1s1
.Lk1r1:
RA 236,1,12288
HS 25,119
M 5,78,0
LL 16,98
LL 123,99
M 5,79,1
s_add_u32 s42, s44, s63
LA 16
PK 119,17,117,33
M 0,20,2
s_addc_u32 s43, s45, 0
LC_ 123
PK 117,17,117,32
M 0,21,3
W1 24,0
PO 119
M 1,32,2
WL 5
LO 18,18,122
LU 122,18
M 1,33,3
RA 216,1,13312
HS 25,122
M 2,44,2
LL 17,100
LL 126,101
M 2,45,3
LA 17
PK 122,18,120,33
M 3,56,2
LC_ 126
PK 120,18,120,32
M 3,57,3
ST 116
M 4,68,2
WL 4
LO 16,16,123
LU 123,16
M 4,69,3
s_add_u32 s42, s44, s64
RA 220,1,14336
HS 25,123
M 5,80,2
LL 18,102
LL 127,103
M 5,81,3
s_addc_u32 s43, s45, 0
LA 18
PK 123,16,121,33
M 0,22,4
LC_ 127
PK 121,16,121,32
M 0,23,5
ST 120
M 1,34,4
WL 3
LO 17,17,126
LU 126,17
M 1,35,5
RA 224,1,15360
HS 25,126
M 2,46,4
LL 16,56
LL 104,57
M 2,47,5
LA 16
PK 126,17,124,33
M 3,58,4
LC_ 104
PK 124,17,124,32
M 3,59,5
ds_read_b64 v[14:15], v5 offset:0
PO 126
M 4,70,4
WL 4
LO 18,18,127
LU 127,18
M 4,71,5
s_add_u32 s42, s44, s65
RA 228,1,16384
HS 25,127
M 5,82,4
LL 17,58
LL 105,59
M 5,83,5
s_addc_u32 s43, s45, 0
LA 17
PK 127,18,125,33
Z 6,12,0
LC_ 105
PK 125,18,125,32
M 6,13,1
PO 127
v_subrev_u32 v5, s39, v5
Z 7,24,0
WL 4
LO 104,16,104
LU 16,104
M 7,25,1
RA 232,1,17408
HZ 24,16
Z 8,36,0
WL 4
LL 18,60
LL 108,61
M 8,37,1
s_mul_i32 s53, s50, 25600
LA 18
v_add_f32 v14, v14, v15
Z 9,48,0
s_add_u32 s40, %[wlo], s53
LC_ 108
v_add_f32_dpp v14, v14, v14 quad_perm:[1,0,3,2] row_mask:0xf bank_mask:0xf
M 9,49,1
s_addc_u32 s41, %[whi], 0
Z 10,60,0
WL 3
LO 105,17,105
LU 17,105
M 10,61,1
s_mov_b32 m0, s38
RA 236,1,18432
HS 24,17
Z 11,72,0
LL 16,62
LL 109,63
M 11,73,1
LA 16
ST 124
M 6,14,2
LC_ 109
DW 0
M 6,15,3
v_add_f32_dpp v14, v14, v14 quad_perm:[2,3,0,1] row_mask:0xf bank_mask:0xf
HX 25
M 7,26,2
WL 3
LO 108,18,108
LU 18,108
W1 25,1024
M 7,27,3
s_add_u32 s37, s37, 32768
RA 216,1,19456
HS 24,18
M 8,38,2
s_cmp_eq_u32 s37, s75
LL 17,64
LL 112,65
M 8,39,3
s_cselect_b32 s37, s74, s37
LA 17
DW 1024
M 9,50,2
LC_ 112
v_add_f32_dpp v14, v14, v14 row_half_mirror row_mask:0xf bank_mask:0xf
M 9,51,3
v_cvt_f64_f32 v[14:15], v14
v_add_u32 v4, s39, v4
s_mov_b64 exec, 1
AT
s_mov_b64 exec, -1
M 10,62,2
WL 4
LO 109,16,109
LU 16,109
M 10,63,3
RA 220,1,20480
HS 24,16
M 11,74,2
LL 18,66
LL 113,67
M 11,75,3
LA 18
s_mov_b64 exec, s[54:55]
SP
s_mov_b64 exec, -1
M 6,16,4
LC_ 113
DW 2048
M 6,17,5
M 7,28,4
WL 3
LO 112,17,112
LU 17,112
M 7,29,5
s_mul_i32 s53, s51, 24
RA 224,1,21504
HS 24,17
M 8,40,4
s_bitcmp1_b32 s51, 0
LL 16,68
LL 116,69
M 8,41,5
s_addc_u32 s53, s53, 0
LA 16
M 9,52,4
s_lshl_b32 s53, s53, 11
LC_ 116
M 9,53,5
s_bitcmp1_b64 s[90:91], s51
M 10,64,4
WL 3
LO 113,18,113
LU 18,113
M 10,65,5
s_cselect_b64 s[44:45], s[84:85], s[82:83]
RA 228,1,22528
HS 24,18
M 11,76,4
LL 17,70
LL 117,71
M 11,77,5
s_add_u32 s44, s44, s53
LA 17
M 6,18,0
s_addc_u32 s45, s45, 0
LC_ 117
M 6,19,1
s_add_u32 s51, s51, %[d]
M 7,30,0
WL 3
LO 116,16,116
LU 16,116
M 7,31,1
s_cmp_eq_u32 s51, %[wrap]
RA 232,1,23552
HS 24,16
M 8,42,0
s_cselect_b32 s51, %[rst], s51
LL 18,72
LL 120,73
M 8,43,1
s_add_u32 s40, s40, 3072
LA 18
M 9,54,0
s_addc_u32 s41, s41, 0
LC_ 120
s_add_u32 m0, s38, 3072
M 9,55,1
DW 0
M 10,66,0
WL 3
LO 117,17,117
LU 17,117
M 10,67,1
RA 236,1,24576
HS 24,17
M 11,78,0
LL 16,74
LL 121,75
M 11,79,1
LA 16
DW 1024
)"
R"(M 6,20,2
LC_ 121
DW 2048
M 6,21,3
s_lshl_b32 s53, s52, 8
v_add_u32 v1, s37, v0
M 7,32,2
WL 3
LO 120,18,120
LU 18,120
M 7,33,3
s_add_u32 s48, %[alo], s53
RA 216,1,0
HS 24,18
M 8,44,2
s_addc_u32 s49, %[ahi], 0
LL 17,76
LL 124,77
M 8,45,3
s_add_u32 s52, s52, %[d]
LA 17
v_and_b32 v22, 0xfffffcff, v1
M 9,56,2
s_cmp_eq_u32 s52, %[wrap]
LC_ 124
M 9,57,3
s_cselect_b32 s52, %[rst], s52
DQ 3072
M 10,68,2
WL 3
LO 121,16,121
LU 16,121
M 10,69,3
s_add_u32 s50, s50, %[d]
RA 220,1,1024
HS 24,16
M 11,80,2
LL 18,78
LL 125,79
M 11,81,3
s_cmp_eq_u32 s50, %[wrap]
LA 18
RV 252,22,5120
M 6,22,4
s_cselect_b32 s50, %[rst], s50
LC_ 125
M 6,23,5
WV 31
v_add_u32 v19, 1, v19
M 7,34,4
WL 4
LO 124,17,124
LU 17,124
M 7,35,5
s_add_u32 s38, s38, 32768
RA 224,1,2048
HS 24,17
M 8,46,4
s_cmp_eq_u32 s38, s77
LL 16,80
LL 106,81
M 8,47,5
s_cselect_b32 s38, s76, s38
LA 16
FP
M 9,58,4
s_sub_u32 s39, 0, s39
LC_ 106
M 9,59,5
M 10,70,4
WL 4
LO 125,18,125
LU 18,125
M 10,71,5
RA 228,1,3072
HS 24,18
M 11,82,4
LL 17,82
LL 107,83
M 11,83,5
LA 17
HX 24
Z 0,12,0
LC_ 107
M 0,13,1
Z 1,24,0
WL 3
LO 16,16,106
LU 106,16
M 1,25,1
RA 232,1,4096
HZ 25,106
Z 2,36,0
LL 18,84
LL 110,85
M 2,37,1
LA 18
PK 106,16,104,33
Z 3,48,0
LC_ 110
PK 104,16,104,32
M 3,49,1
Z 4,60,0
WL 3
LO 17,17,107
LU 107,17
M 4,61,1
s_add_u32 s42, s44, s60
RA 236,1,6144
HS 25,107
Z 5,72,0
LL 16,86
LL 111,87
M 5,73,1
s_addc_u32 s43, s45, 0
LA 16
PK 107,17,105,33
M 0,14,2
LC_ 111
PK 105,17,105,32
M 0,15,3
ST 104
M 1,26,2
WL 3
LO 18,18,110
LU 110,18
M 1,27,3
RA 216,1,7168
HS 25,110
M 2,38,2
LL 17,88
LL 114,89
M 2,39,3
LA 17
PK 110,18,108,33
M 3,50,2
LC_ 114
PK 108,18,108,32
M 3,51,3
FR
PO 110
M 4,62,2
WL 4
LO 16,16,111
LU 111,16
M 4,63,3
s_add_u32 s42, s44, s61
RA 220,1,8192
HS 25,111
M 5,74,2
LL 18,90
LL 115,91
M 5,75,3
s_addc_u32 s43, s45, 0
LA 18
PK 111,16,109,33
M 0,16,4
s_mov_b32 m0, %[pm]
LC_ 115
PK 109,16,109,32
M 0,17,5
DF
PO 111
M 1,28,4
WL 4
LO 17,17,114
LU 114,17
M 1,29,5
RA 224,1,9216
HS 25,114
M 2,40,4
LL 16,92
LL 118,93
M 2,41,5
LA 16
PK 114,17,112,33
M 3,52,4
LC_ 118
PK 112,17,112,32
M 3,53,5
ST 108
M 4,64,4
WL 3
LO 18,18,115
LU 115,18
M 4,65,5
s_add_u32 s42, s44, s62
RA 228,1,10240
HS 25,115
M 5,76,4
LL 17,94
LL 119,95
M 5,77,5
s_addc_u32 s43, s45, 0
LA 17
PK 115,18,113,33
M 0,18,0
LC_ 119
PK 113,18,113,32
M 0,19,1
ST 112
M 1,30,0
WL 3
LO 16,16,118
LU 118,16
M 1,31,1
RA 232,1,11264
HS 25,118
M 2,42,0
LL 18,96
LL 122,97
M 2,43,1
LA 18
PK 118,16,116,33
v_cmp_gt_u32 vcc, v19, v15
M 3,54,0
LC_ 122
PK 116,16,116,32
M 3,55,1
RV 252,22,5392
PO 118
M 4,66,0
WL 4
LO 17,17,119
LU 119,17
M 4,67,1
s_cbranch_vccnz .Lk1s2
.Lk1r2:
RA 236,1,12288
HS 25,119
M 5,78,0
LL 16,98
LL 123,99
M 5,79,1
s_add_u32 s42, s44, s63
LA 16
PK 119,17,117,33
M 0,20,2
s_addc_u32 s43, s45, 0
LC_ 123
PK 117,17,117,32
M 0,21,3
W1 24,0
PO 119
M 1,32,2
WL 5
LO 18,18,122
LU 122,18
M 1,33,3
RA 216,1,13312
HS 25,122
M 2,44,2
LL 17,100
LL 126,101
M 2,45,3
LA 17
PK 122,18,120,33
M 3,56,2
LC_ 126
PK 120,18,120,32
M 3,57,3
ST 116
M 4,68,2
WL 4
LO 16,16,123
LU 123,16
M 4,69,3
s_add_u32 s42, s44, s64
RA 220,1,14336
HS 25,123
M 5,80,2
LL 18,102
LL 127,103
M 5,81,3
s_addc_u32 s43, s45, 0
LA 18
PK 123,16,121,33
M 0,22,4
LC_ 127
PK 121,16,121,32
M 0,23,5
ST 120
M 1,34,4
WL 3
LO 17,17,126
LU 126,17
M 1,35,5
RA 224,1,15360
HS 25,126
M 2,46,4
LL 16,56
LL 104,57
M 2,47,5
LA 16
PK 126,17,124,33
M 3,58,4
LC_ 104
PK 124,17,124,32
M 3,59,5
ds_read_b64 v[14:15], v5 offset:0
PO 126
M 4,70,4
WL 4
LO 18,18,127
LU 127,18
M 4,71,5
s_add_u32 s42, s44, s65
RA 228,1,16384
HS 25,127
M 5,82,4
LL 17,58
LL 105,59
M 5,83,5
s_addc_u32 s43, s45, 0
LA 17
PK 127,18,125,33
Z 6,12,0
LC_ 105
PK 125,18,125,32
M 6,13,1
PO 127
v_subrev_u32 v5, s39, v5
Z 7,24,0
WL 4
LO 104,16,104
LU 16,104
M 7,25,1
RA 232,1,17408
HZ 24,16
Z 8,36,0
WL 4
LL 18,60
LL 108,61
M 8,37,1
s_mul_i32 s53, s50, 25600
LA 18
v_add_f32 v14, v14, v15
Z 9,48,0
s_add_u32 s40, %[wlo], s53
LC_ 108
v_add_f32_dpp v14, v14, v14 quad_perm:[1,0,3,2] row_mask:0xf bank_mask:0xf
M 9,49,1
s_addc_u32 s41, %[whi], 0
Z 10,60,0
WL 3
LO 105,17,105
LU 17,105
M 10,61,1
s_mov_b32 m0, s38
RA 236,1,18432
HS 24,17
Z 11,72,0
LL 16,62
LL 109,63
M 11,73,1
LA 16
ST 124
M 6,14,2
LC_ 109
DW 0
M 6,15,3
WV 14
v_add_f32_dpp v14, v14, v14 quad_perm:[2,3,0,1] row_mask:0xf bank_mask:0xf
HX 25
M 7,26,2
WL 3
LO 108,18,108
LU 18,108
W1 25,1024
M 7,27,3
s_add_u32 s37, s37, 32768
RA 216,1,19456
HS 24,18
v_readfirstlane_b32 s72, v11
M 8,38,2
s_cmp_eq_u32 s37, s75
LL 17,64
LL 112,65
v_mov_b32 v12, s72
M 8,39,3
s_cselect_b32 s37, s74, s37
LA 17
W2 12,12,16
M 9,50,2
LC_ 112
DW 1024
M 9,51,3
v_add_f32_dpp v14, v14, v14 row_half_mirror row_mask:0xf bank_mask:0xf
v_add_u32 v4, s39, v4
M 10,62,2
WL 5
LO 109,16,109
LU 16,109
M 10,63,3
RA 220,1,20480
HS 24,16
M 11,74,2
LL 18,66
LL 113,67
M 11,75,3
)"
R"(LA 18
DW 2048
M 6,16,4
LC_ 113
v_cvt_f64_f32 v[14:15], v14
M 6,17,5
s_mov_b64 exec, s[54:55]
SP
s_mov_b64 exec, -1
M 7,28,4
WL 3
LO 112,17,112
LU 17,112
M 7,29,5
s_mul_i32 s53, s51, 24
RA 224,1,21504
HS 24,17
M 8,40,4
s_bitcmp1_b32 s51, 0
LL 16,68
LL 116,69
M 8,41,5
s_addc_u32 s53, s53, 0
LA 16
M 9,52,4
s_lshl_b32 s53, s53, 11
LC_ 116
M 9,53,5
s_bitcmp1_b64 s[90:91], s51
M 10,64,4
WL 3
LO 113,18,113
LU 18,113
M 10,65,5
s_cselect_b64 s[44:45], s[84:85], s[82:83]
RA 228,1,22528
HS 24,18
M 11,76,4
LL 17,70
LL 117,71
M 11,77,5
s_add_u32 s44, s44, s53
LA 17
M 6,18,0
s_addc_u32 s45, s45, 0
LC_ 117
M 6,19,1
s_add_u32 s51, s51, %[d]
M 7,30,0
WL 3
LO 116,16,116
LU 16,116
M 7,31,1
s_cmp_eq_u32 s51, %[wrap]
RA 232,1,23552
HS 24,16
M 8,42,0
s_cselect_b32 s51, %[rst], s51
LL 18,72
LL 120,73
M 8,43,1
s_add_u32 s40, s40, 3072
LA 18
M 9,54,0
s_addc_u32 s41, s41, 0
LC_ 120
s_add_u32 m0, s38, 3072
M 9,55,1
DW 0
M 10,66,0
WL 3
LO 117,17,117
LU 17,117
M 10,67,1
RA 236,1,24576
HS 24,17
M 11,78,0
LL 16,74
LL 121,75
M 11,79,1
LA 16
DW 1024
M 6,20,2
LC_ 121
DW 2048
M 6,21,3
s_lshl_b32 s53, s52, 8
v_add_u32 v1, s37, v0
M 7,32,2
WL 3
LO 120,18,120
LU 18,120
M 7,33,3
s_add_u32 s48, %[alo], s53
RA 216,1,0
HS 24,18
M 8,44,2
s_addc_u32 s49, %[ahi], 0
LL 17,76
LL 124,77
M 8,45,3
s_add_u32 s52, s52, %[d]
LA 17
v_and_b32 v22, 0xfffffcff, v1
M 9,56,2
s_cmp_eq_u32 s52, %[wrap]
LC_ 124
M 9,57,3
s_cselect_b32 s52, %[rst], s52
DQ 3072
M 10,68,2
WL 3
LO 121,16,121
LU 16,121
M 10,69,3
s_add_u32 s50, s50, %[d]
RA 220,1,1024
HS 24,16
M 11,80,2
LL 18,78
LL 125,79
M 11,81,3
s_cmp_eq_u32 s50, %[wrap]
LA 18
RV 252,22,5120
M 6,22,4
s_cselect_b32 s50, %[rst], s50
LC_ 125
M 6,23,5
v_add_u32 v19, 1, v19
M 7,34,4
WL 4
LO 124,17,124
LU 17,124
M 7,35,5
s_add_u32 s38, s38, 32768
RA 224,1,2048
HS 24,17
M 8,46,4
s_cmp_eq_u32 s38, s77
LL 16,80
LL 106,81
M 8,47,5
s_cselect_b32 s38, s76, s38
LA 16
FP
M 9,58,4
s_sub_u32 s39, 0, s39
LC_ 106
M 9,59,5
M 10,70,4
WL 4
LO 125,18,125
LU 18,125
M 10,71,5
RA 228,1,3072
HS 24,18
M 11,82,4
LL 17,82
LL 107,83
M 11,83,5
LA 17
HX 24
Z 0,12,0
LC_ 107
M 0,13,1
Z 1,24,0
WL 3
LO 16,16,106
LU 106,16
M 1,25,1
RA 232,1,4096
HZ 25,106
Z 2,36,0
LL 18,84
LL 110,85
M 2,37,1
LA 18
PK 106,16,104,33
Z 3,48,0
LC_ 110
PK 104,16,104,32
M 3,49,1
Z 4,60,0
WL 3
LO 17,17,107
LU 107,17
M 4,61,1
s_add_u32 s42, s44, s60
RA 236,1,6144
HS 25,107
Z 5,72,0
LL 16,86
LL 111,87
M 5,73,1
s_addc_u32 s43, s45, 0
LA 16
PK 107,17,105,33
M 0,14,2
LC_ 111
PK 105,17,105,32
M 0,15,3
ST 104
M 1,26,2
WL 3
LO 18,18,110
LU 110,18
M 1,27,3
RA 216,1,7168
HS 25,110
M 2,38,2
LL 17,88
LL 114,89
M 2,39,3
LA 17
PK 110,18,108,33
M 3,50,2
LC_ 114
PK 108,18,108,32
M 3,51,3
FR
PO 110
M 4,62,2
WL 4
LO 16,16,111
LU 111,16
M 4,63,3
s_add_u32 s42, s44, s61
RA 220,1,8192
HS 25,111
M 5,74,2
LL 18,90
LL 115,91
M 5,75,3
s_addc_u32 s43, s45, 0
LA 18
PK 111,16,109,33
M 0,16,4
s_mov_b32 m0, %[pm]
LC_ 115
PK 109,16,109,32
M 0,17,5
DF
PO 111
M 1,28,4
WL 4
LO 17,17,114
LU 114,17
M 1,29,5
RA 224,1,9216
HS 25,114
M 2,40,4
LL 16,92
LL 118,93
M 2,41,5
LA 16
PK 114,17,112,33
M 3,52,4
LC_ 118
PK 112,17,112,32
M 3,53,5
ST 108
M 4,64,4
WL 3
LO 18,18,115
LU 115,18
M 4,65,5
s_add_u32 s42, s44, s62
RA 228,1,10240
HS 25,115
M 5,76,4
LL 17,94
LL 119,95
M 5,77,5
s_addc_u32 s43, s45, 0
LA 17
PK 115,18,113,33
M 0,18,0
LC_ 119
PK 113,18,113,32
M 0,19,1
ST 112
M 1,30,0
WL 3
LO 16,16,118
LU 118,16
M 1,31,1
RA 232,1,11264
HS 25,118
M 2,42,0
LL 18,96
LL 122,97
M 2,43,1
LA 18
PK 118,16,116,33
v_cmp_gt_u32 vcc, v19, v15
M 3,54,0
LC_ 122
PK 116,16,116,32
M 3,55,1
RV 252,22,5392
PO 118
M 4,66,0
WL 4
LO 17,17,119
LU 119,17
M 4,67,1
s_cbranch_vccnz .Lk1s3
.Lk1r3:
RA 236,1,12288
HS 25,119
M 5,78,0
LL 16,98
LL 123,99
M 5,79,1
s_add_u32 s42, s44, s63
LA 16
PK 119,17,117,33
M 0,20,2
s_addc_u32 s43, s45, 0
LC_ 123
PK 117,17,117,32
M 0,21,3
ds_read_b32 v20, v5 offset:4096
PO 119
M 1,32,2
WL 5
LO 18,18,122
LU 122,18
M 1,33,3
RA 216,1,13312
HS 25,122
M 2,44,2
LL 17,100
LL 126,101
M 2,45,3
LA 17
PK 122,18,120,33
M 3,56,2
LC_ 126
PK 120,18,120,32
M 3,57,3
M 4,68,2
WL 4
LO 16,16,123
LU 123,16
M 4,69,3
RA 220,1,14336
HS 25,123
M 5,80,2
LL 18,102
LL 127,103
M 5,81,3
LA 18
PK 123,16,121,33
M 0,22,4
LC_ 127
PK 121,16,121,32
W1 24,0
M 0,23,5
M 1,34,4
WL 4
LO 17,17,126
LU 126,17
M 1,35,5
RA 224,1,15360
HS 25,126
M 2,46,4
LL 16,56
LL 104,57
M 2,47,5
LA 16
PK 126,17,124,33
M 3,58,4
LC_ 104
PK 124,17,124,32
M 3,59,5
PO 126
v_readfirstlane_b32 s72, v20
M 4,70,4
WL 4
LO 18,18,127
LU 127,18
M 4,71,5
s_sub_u32 s53, 63, s72
RA 228,1,16384
HS 25,127
M 5,82,4
LL 17,58
LL 105,59
M 5,83,5
s_cmp_eq_u32 %[d], 1
LA 17
PK 127,18,125,33
Z 6,12,0
s_cselect_b32 s72, s72, s53
LC_ 105
PK 125,18,125,32
M 6,13,1
s_and_b32 s72, s72, 63
)"
R"(ds_read_b64 v[14:15], v5 offset:0
PO 127
Z 7,24,0
WL 4
LO 104,16,104
LU 16,104
M 7,25,1
s_add_u32 s53, s72, 128
RA 232,1,17408
HZ 24,16
Z 8,36,0
s_mul_i32 s53, s53, 2359296
LL 18,60
LL 108,61
M 8,37,1
s_add_u32 s86, %[hlo], s53
LA 18
v_subrev_u32 v5, s39, v5
Z 9,48,0
s_addc_u32 s87, %[hhi], 0
LC_ 108
HX 25
M 9,49,1
s_add_u32 s88, s86, 8192
Z 10,60,0
WL 4
LO 105,17,105
LU 17,105
M 10,61,1
s_addc_u32 s89, s87, 0
RA 236,1,18432
HS 24,17
Z 11,72,0
WL 4
LL 16,62
LL 109,63
M 11,73,1
s_and_b32 s92, %[hlo], 0x1fffff
LA 16
W1 25,1024
s_add_u32 s92, s92, s53
s_mov_b32 s93, 3072
ST 116
s_add_u32 s42, s44, s64
s_addc_u32 s43, s45, 0
s_mul_i32 s57, s72, 589824
s_add_u32 s57, s57, %[xs1]
s_lshl_b32 s72, s72, 1
s_add_u32 s72, s72, s96
s_lshl_b32 s53, s72, 6
s_add_u32 s53, s53, 128
s_cmp_lt_u32 %[ca0], 128
s_cselect_b32 s73, s53, %[ca0]
ST 120
v_mov_b32 v9, s73
v_mov_b32 v11, s92
M 6,14,2
s_add_u32 s42, s44, s65
LC_ 109
v_add_f32 v14, v14, v15
s_addc_u32 s43, s45, 0
v_mad_u32_u24 v11, v0, s93, v11
M 6,15,3
v_add_f32_dpp v14, v14, v14 quad_perm:[1,0,3,2] row_mask:0xf bank_mask:0xf
v_add_u32 v4, s39, v4
M 7,26,2
WL 4
LO 108,18,108
LU 18,108
M 7,27,3
RA 216,1,19456
HS 24,18
v_lshrrev_b32 v11, 21, v11
M 8,38,2
LL 17,64
LL 112,65
M 8,39,3
LA 17
v_add_f32_dpp v14, v14, v14 quad_perm:[2,3,0,1] row_mask:0xf bank_mask:0xf
M 9,50,2
LC_ 112
v_add_f32_dpp v14, v14, v14 row_half_mirror row_mask:0xf bank_mask:0xf
v_lshl_add_u32 v11, v11, 2, 64
M 9,51,3
s_mul_i32 s53, s50, 25600
v_cvt_f64_f32 v[14:15], v14
M 10,62,2
WL 3
LO 109,16,109
LU 16,109
s_add_u32 s40, %[wlo], s53
s_addc_u32 s41, %[whi], 0
s_mov_b32 m0, s38
M 10,63,3
RA 220,1,20480
HS 24,16
v_writelane_b32 v11, %[tsel], 63
M 11,74,2
LL 18,66
LL 113,67
ST 124
M 11,75,3
s_sub_u32 s53, s86, %[hlo]
LA 18
s_mov_b64 exec, s[54:55]
SP
s_mov_b64 exec, -1
M 6,16,4
s_lshr_b32 s53, s53, 2
LC_ 113
s_and_b32 s92, %[xlo], 0x1fffff
s_add_u32 s53, s53, s92
s_add_u32 s53, s53, 0x20048000
s_lshr_b32 s53, s53, 21
s_lshl_b32 s92, s53, 2
M 6,17,5
DW 0
v_writelane_b32 v11, s92, 48
M 7,28,4
WL 3
LO 112,17,112
LU 17,112
M 7,29,5
s_add_u32 s37, s37, 32768
RA 224,1,21504
HS 24,17
M 8,40,4
s_cmp_eq_u32 s37, s75
LL 16,68
LL 116,69
M 8,41,5
s_cselect_b32 s37, s74, s37
LA 16
global_load_dword v11, v11, s[68:69] offset:-2048
M 9,52,4
LC_ 116
DW 1024
M 9,53,5
DW 2048
M 10,64,4
WL 3
LO 113,18,113
LU 18,113
M 10,65,5
s_mul_i32 s53, s51, 24
RA 228,1,22528
HS 24,18
M 11,76,4
LL 17,70
LL 117,71
M 11,77,5
s_bitcmp1_b32 s51, 0
LA 17
M 6,18,0
s_addc_u32 s53, s53, 0
LC_ 117
M 6,19,1
s_lshl_b32 s53, s53, 11
M 7,30,0
WL 3
LO 116,16,116
LU 16,116
M 7,31,1
s_bitcmp1_b64 s[90:91], s51
RA 232,1,23552
HS 24,16
M 8,42,0
s_cselect_b64 s[44:45], s[84:85], s[82:83]
LL 18,72
LL 120,73
M 8,43,1
s_add_u32 s44, s44, s53
LA 18
s_addc_u32 s45, s45, 0
s_add_u32 s51, s51, %[d]
s_cmp_eq_u32 s51, %[wrap]
s_cselect_b32 s51, %[rst], s51
M 9,54,0
s_add_u32 s40, s40, 3072
LC_ 120
s_addc_u32 s41, s41, 0
s_add_u32 m0, s38, 3072
M 9,55,1
DW 0
M 10,66,0
WL 3
LO 117,17,117
LU 17,117
M 10,67,1
RA 236,1,24576
HS 24,17
M 11,78,0
LL 16,74
LL 121,75
M 11,79,1
LA 16
DW 1024
M 6,20,2
LC_ 121
DW 2048
M 6,21,3
s_lshl_b32 s53, s52, 8
v_add_u32 v1, s37, v0
M 7,32,2
WL 3
LO 120,18,120
LU 18,120
M 7,33,3
s_add_u32 s48, %[alo], s53
RA 216,1,0
HS 24,18
M 8,44,2
s_addc_u32 s49, %[ahi], 0
LL 17,76
LL 124,77
M 8,45,3
s_add_u32 s52, s52, %[d]
LA 17
v_and_b32 v22, 0xfffffcff, v1
M 9,56,2
s_cmp_eq_u32 s52, %[wrap]
LC_ 124
M 9,57,3
s_cselect_b32 s52, %[rst], s52
DQ 3072
M 10,68,2
WL 3
LO 121,16,121
LU 16,121
M 10,69,3
s_add_u32 s50, s50, %[d]
RA 220,1,1024
HS 24,16
M 11,80,2
LL 18,78
LL 125,79
M 11,81,3
s_cmp_eq_u32 s50, %[wrap]
LA 18
RV 252,22,5120
M 6,22,4
s_cselect_b32 s50, %[rst], s50
LC_ 125
M 6,23,5
WV 31
v_add_u32 v19, 1, v19
M 7,34,4
WL 4
LO 124,17,124
LU 17,124
M 7,35,5
s_add_u32 s38, s38, 32768
RA 224,1,2048
HS 24,17
M 8,46,4
s_cmp_eq_u32 s38, s77
LL 16,80
LL 106,81
M 8,47,5
s_cselect_b32 s38, s76, s38
LA 16
FP
M 9,58,4
s_sub_u32 s39, 0, s39
LC_ 106
M 9,59,5
M 10,70,4
WL 4
LO 125,18,125
LU 18,125
M 10,71,5
RA 228,1,3072
HS 24,18
M 11,82,4
LL 17,82
LL 107,83
M 11,83,5
LA 17
HX 24
Z 0,12,0
LC_ 107
M 0,13,1
Z 1,24,0
WL 3
LO 16,16,106
LU 106,16
M 1,25,1
RA 232,1,4096
HZ 25,106
Z 2,36,0
LL 18,84
LL 110,85
M 2,37,1
LA 18
PK 106,16,104,33
Z 3,48,0
LC_ 110
PK 104,16,104,32
M 3,49,1
Z 4,60,0
WL 3
LO 17,17,107
LU 107,17
M 4,61,1
s_add_u32 s42, s44, s60
RA 236,1,6144
HS 25,107
Z 5,72,0
LL 16,86
LL 111,87
M 5,73,1
s_addc_u32 s43, s45, 0
LA 16
PK 107,17,105,33
M 0,14,2
LC_ 111
PK 105,17,105,32
M 0,15,3
ST 104
M 1,26,2
WL 3
LO 18,18,110
LU 110,18
M 1,27,3
RA 216,1,7168
HS 25,110
M 2,38,2
LL 17,88
LL 114,89
M 2,39,3
LA 17
PK 110,18,108,33
M 3,50,2
LC_ 114
PK 108,18,108,32
M 3,51,3
FR
PO 110
M 4,62,2
WL 4
LO 16,16,111
LU 111,16
M 4,63,3
s_add_u32 s42, s44, s61
RA 220,1,8192
HS 25,111
M 5,74,2
LL 18,90
LL 115,91
M 5,75,3
s_addc_u32 s43, s45, 0
LA 18
PK 111,16,109,33
M 0,16,4
s_mov_b32 m0, %[pm]
LC_ 115
PK 109,16,109,32
M 0,17,5
DF
PO 111
M 1,28,4
WL 4
LO 17,17,114
LU 114,17
M 1,29,5
RA 224,1,9216
HS 25,114
M 2,40,4
LL 16,92
LL 118,93
M 2,41,5
LA 16
PK 114,17,112,33
M 3,52,4
LC_ 118
PK 112,17,112,32
M 3,53,5
ST 108
M 4,64,4
WL 3
LO 18,18,115
LU 115,18
M 4,65,5
s_add_u32 s42, s44, s62
RA 228,1,10240
HS 25,115
M 5,76,4
LL 17,94
LL 119,95
M 5,77,5
s_addc_u32 s43, s45, 0
LA 17
PK 115,18,113,33
M 0,18,0
LC_ 119
PK 113,18,113,32
M 0,19,1
ST 112
M 1,30,0
WL 3
)"
R"(LO 16,16,118
LU 118,16
M 1,31,1
RA 232,1,11264
HS 25,118
M 2,42,0
LL 18,96
LL 122,97
M 2,43,1
LA 18
PK 118,16,116,33
v_cmp_gt_u32 vcc, v19, v15
M 3,54,0
LC_ 122
PK 116,16,116,32
M 3,55,1
RV 252,22,5392
PO 118
M 4,66,0
WL 4
LO 17,17,119
LU 119,17
M 4,67,1
s_cbranch_vccnz .Lk1s4
.Lk1r4:
RA 236,1,12288
HS 25,119
M 5,78,0
LL 16,98
LL 123,99
M 5,79,1
s_add_u32 s42, s44, s63
LA 16
PK 119,17,117,33
M 0,20,2
s_addc_u32 s43, s45, 0
LC_ 123
PK 117,17,117,32
M 0,21,3
W1 24,0
PO 119
M 1,32,2
WL 5
LO 18,18,122
LU 122,18
M 1,33,3
RA 216,1,13312
HS 25,122
M 2,44,2
LL 17,100
LL 126,101
M 2,45,3
LA 17
PK 122,18,120,33
M 3,56,2
LC_ 126
PK 120,18,120,32
M 3,57,3
ST 116
M 4,68,2
WL 4
LO 16,16,123
LU 123,16
M 4,69,3
s_add_u32 s42, s44, s64
RA 220,1,14336
HS 25,123
M 5,80,2
LL 18,102
LL 127,103
M 5,81,3
s_addc_u32 s43, s45, 0
LA 18
PK 123,16,121,33
M 0,22,4
LC_ 127
PK 121,16,121,32
M 0,23,5
ST 120
M 1,34,4
WL 3
LO 17,17,126
LU 126,17
M 1,35,5
RA 224,1,15360
HS 25,126
M 2,46,4
LL 16,56
LL 104,57
M 2,47,5
LA 16
PK 126,17,124,33
M 3,58,4
LC_ 104
PK 124,17,124,32
M 3,59,5
ds_read_b64 v[14:15], v5 offset:0
PO 126
M 4,70,4
WL 4
LO 18,18,127
LU 127,18
M 4,71,5
s_add_u32 s42, s44, s65
RA 228,1,16384
HS 25,127
M 5,82,4
LL 17,58
LL 105,59
M 5,83,5
s_addc_u32 s43, s45, 0
LA 17
PK 127,18,125,33
Z 6,12,0
LC_ 105
PK 125,18,125,32
M 6,13,1
PO 127
v_subrev_u32 v5, s39, v5
Z 7,24,0
WL 4
LO 104,16,104
LU 16,104
M 7,25,1
RA 232,1,17408
HZ 24,16
Z 8,36,0
WL 4
LL 18,60
LL 108,61
M 8,37,1
s_mul_i32 s53, s50, 25600
LA 18
v_add_f32 v14, v14, v15
Z 9,48,0
s_add_u32 s40, %[wlo], s53
LC_ 108
v_add_f32_dpp v14, v14, v14 quad_perm:[1,0,3,2] row_mask:0xf bank_mask:0xf
M 9,49,1
s_addc_u32 s41, %[whi], 0
Z 10,60,0
WL 3
LO 105,17,105
LU 17,105
M 10,61,1
s_mov_b32 m0, s38
RA 236,1,18432
HS 24,17
Z 11,72,0
LL 16,62
LL 109,63
M 11,73,1
LA 16
ST 124
M 6,14,2
LC_ 109
DW 0
M 6,15,3
v_add_f32_dpp v14, v14, v14 quad_perm:[2,3,0,1] row_mask:0xf bank_mask:0xf
HX 25
M 7,26,2
WL 3
LO 108,18,108
LU 18,108
W1 25,1024
M 7,27,3
s_add_u32 s37, s37, 32768
RA 216,1,19456
HS 24,18
M 8,38,2
s_cmp_eq_u32 s37, s75
LL 17,64
LL 112,65
M 8,39,3
s_cselect_b32 s37, s74, s37
LA 17
DW 1024
M 9,50,2
LC_ 112
v_add_f32_dpp v14, v14, v14 row_half_mirror row_mask:0xf bank_mask:0xf
M 9,51,3
v_cvt_f64_f32 v[14:15], v14
v_add_u32 v4, s39, v4
M 10,62,2
WL 4
LO 109,16,109
LU 16,109
M 10,63,3
RA 220,1,20480
HS 24,16
M 11,74,2
LL 18,66
LL 113,67
M 11,75,3
LA 18
s_mov_b64 exec, s[54:55]
SP
s_mov_b64 exec, -1
M 6,16,4
LC_ 113
DW 2048
M 6,17,5
s_and_b32 s53, %[w], 1
M 7,28,4
WL 3
LO 112,17,112
LU 17,112
M 7,29,5
s_lshl_b32 s53, s53, 10
RA 224,1,21504
HS 24,17
M 8,40,4
s_lshr_b32 s72, %[w], 1
LL 16,68
LL 116,69
M 8,41,5
s_mul_i32 s72, s72, 32768
LA 16
M 9,52,4
s_add_u32 s72, s72, s53
LC_ 116
M 9,53,5
s_add_u32 s72, s72, 128000
M 10,64,4
WL 3
LO 113,18,113
LU 18,113
M 10,65,5
s_movk_i32 s53, 0x78
RA 228,1,22528
HS 24,18
M 11,76,4
LL 17,70
LL 117,71
M 11,77,5
LA 17
v_lshrrev_b32 v12, 5, v0
M 6,18,0
LC_ 117
v_and_b32 v12, 6, v12
M 6,19,1
v_bfe_u32 v12, s53, v12, 2
M 7,30,0
WL 3
LO 116,16,116
LU 16,116
M 7,31,1
RA 232,1,23552
HS 24,16
v_lshrrev_b32 v20, 8, v0
v_xor_b32 v20, v12, v20
v_lshl_add_u32 v20, v20, 4, s72
v_and_b32 v12, 0xf0, v0
v_lshl_add_u32 v20, v12, 2, v20
v_add_u32 v12, s72, v0
M 8,42,0
s_mul_i32 s53, s51, 24
LL 18,72
LL 120,73
M 8,43,1
s_bitcmp1_b32 s51, 0
LA 18
s_addc_u32 s53, s53, 0
s_lshl_b32 s53, s53, 11
s_bitcmp1_b64 s[90:91], s51
s_cselect_b64 s[44:45], s[84:85], s[82:83]
s_add_u32 s44, s44, s53
s_addc_u32 s45, s45, 0
s_add_u32 s51, s51, %[d]
s_cmp_eq_u32 s51, %[wrap]
s_cselect_b32 s51, %[rst], s51
M 9,54,0
s_add_u32 s40, s40, 3072
LC_ 120
s_addc_u32 s41, s41, 0
s_add_u32 m0, s38, 3072
M 9,55,1
DW 0
M 10,66,0
WL 3
LO 117,17,117
LU 17,117
M 10,67,1
RA 236,1,24576
HS 24,17
M 11,78,0
LL 16,74
LL 121,75
M 11,79,1
LA 16
DW 1024
M 6,20,2
LC_ 121
DW 2048
M 6,21,3
s_lshl_b32 s53, s52, 8
v_add_u32 v1, s37, v0
M 7,32,2
WL 3
LO 120,18,120
LU 18,120
M 7,33,3
s_add_u32 s48, %[alo], s53
RA 216,1,0
HS 24,18
M 8,44,2
s_addc_u32 s49, %[ahi], 0
LL 17,76
LL 124,77
M 8,45,3
s_add_u32 s52, s52, %[d]
LA 17
v_and_b32 v22, 0xfffffcff, v1
M 9,56,2
s_cmp_eq_u32 s52, %[wrap]
LC_ 124
M 9,57,3
s_cselect_b32 s52, %[rst], s52
DQ 3072
M 10,68,2
WL 3
LO 121,16,121
LU 16,121
M 10,69,3
s_add_u32 s50, s50, %[d]
RA 220,1,1024
HS 24,16
M 11,80,2
LL 18,78
LL 125,79
M 11,81,3
s_cmp_eq_u32 s50, %[wrap]
LA 18
RV 252,22,5120
M 6,22,4
s_cselect_b32 s50, %[rst], s50
LC_ 125
M 6,23,5
WV 31
v_add_u32 v19, 1, v19
M 7,34,4
WL 4
LO 124,17,124
LU 17,124
M 7,35,5
s_add_u32 s38, s38, 32768
RA 224,1,2048
HS 24,17
M 8,46,4
s_cmp_eq_u32 s38, s77
LL 16,80
LL 106,81
M 8,47,5
s_cselect_b32 s38, s76, s38
LA 16
FP
M 9,58,4
s_sub_u32 s39, 0, s39
LC_ 106
M 9,59,5
M 10,70,4
WL 4
LO 125,18,125
LU 18,125
M 10,71,5
RA 228,1,3072
HS 24,18
M 11,82,4
LL 17,82
LL 107,83
M 11,83,5
LA 17
HX 24
Z 0,12,0
LC_ 107
M 0,13,1
Z 1,24,0
WL 3
LO 16,16,106
LU 106,16
M 1,25,1
RA 232,1,4096
HZ 25,106
Z 2,36,0
LL 18,84
LL 110,85
M 2,37,1
LA 18
PK 106,16,104,33
Z 3,48,0
LC_ 110
PK 104,16,104,32
M 3,49,1
WV 21
v_bcnt_u32_b32 v11, v11, 0
Z 4,60,0
WL 3
LO 17,17,107
LU 107,17
M 4,61,1
)"
R"(RA 236,1,6144
HS 25,107
Z 5,72,0
LL 16,86
LL 111,87
M 5,73,1
LA 16
PK 107,17,105,33
M 0,14,2
LC_ 111
PK 105,17,105,32
M 0,15,3
M 1,26,2
WL 3
LO 18,18,110
LU 110,18
M 1,27,3
RA 216,1,7168
HS 25,110
M 2,38,2
LL 17,88
LL 114,89
M 2,39,3
LA 17
PK 110,18,108,33
M 3,50,2
LC_ 114
PK 108,18,108,32
M 3,51,3
FR
PO 110
v_cmp_lt_u32 s[92:93], 2, v11
M 4,62,2
WL 4
LO 16,16,111
LU 111,16
M 4,63,3
s_nop 0
RA 220,1,8192
HS 25,111
M 5,74,2
LL 18,90
LL 115,91
M 5,75,3
s_add_u32 s42, s44, s60
LA 18
PK 111,16,109,33
s_addc_u32 s43, s45, 0
s_sub_u32 s53, 0, s96
s_xor_b32 s92, s92, s53
s_xor_b32 s93, s93, s53
M 0,16,4
s_mul_i32 s53, %[d], 0
LC_ 115
PK 109,16,109,32
M 0,17,5
s_add_u32 s53, s53, s57
PO 111
M 1,28,4
WL 4
LO 17,17,114
LU 114,17
M 1,29,5
s_add_u32 s46, %[xlo], s53
RA 224,1,9216
HS 25,114
s_addc_u32 s47, %[xhi], 0
s_bitcmp1_b64 s[92:93], 48
s_cselect_b32 s58, 16, 0
s_lshr_b32 s58, 0xfe987610, s58
v_lshrrev_b32 v3, 6, v0
v_and_b32 v3, 12, v3
v_bfe_u32 v3, s58, v3, 4
v_mad_u32_u24 v3, v3, s97, v21
M 2,40,4
LL 16,92
LL 118,93
ST 104
M 2,41,5
s_add_u32 s42, s44, s61
LA 16
PK 114,17,112,33
M 3,52,4
s_addc_u32 s43, s45, 0
LC_ 118
PK 112,17,112,32
M 3,53,5
s_mov_b32 m0, %[pm]
M 4,64,4
WL 3
LO 18,18,115
LU 115,18
M 4,65,5
RA 228,1,10240
HS 25,115
M 5,76,4
LL 17,94
LL 119,95
M 5,77,5
LA 17
PK 115,18,113,33
M 0,18,0
LC_ 119
PK 113,18,113,32
M 0,19,1
DF
M 1,30,0
WL 3
LO 16,16,118
LU 118,16
M 1,31,1
RA 232,1,11264
HS 25,118
M 2,42,0
LL 18,96
LL 122,97
M 2,43,1
LA 18
PK 118,16,116,33
v_cmp_gt_u32 vcc, v19, v15
M 3,54,0
LC_ 122
PK 116,16,116,32
M 3,55,1
RV 252,22,5392
PO 118
M 4,66,0
WL 4
LO 17,17,119
LU 119,17
M 4,67,1
s_cbranch_vccnz .Lk1s5
.Lk1r5:
RA 236,1,12288
HS 25,119
M 5,78,0
LL 16,98
LL 123,99
ST 108
XL 0,0
M 5,79,1
s_add_u32 s42, s44, s62
LA 16
PK 119,17,117,33
s_addc_u32 s43, s45, 0
M 0,20,2
LC_ 123
PK 117,17,117,32
M 0,21,3
W1 24,0
PO 119
M 1,32,2
WL 5
LO 18,18,122
LU 122,18
M 1,33,3
RA 216,1,13312
HS 25,122
M 2,44,2
LL 17,100
LL 126,101
ST 112
M 2,45,3
s_add_u32 s42, s44, s63
LA 17
PK 122,18,120,33
M 3,56,2
s_addc_u32 s43, s45, 0
LC_ 126
PK 120,18,120,32
M 3,57,3
M 4,68,2
WL 4
LO 16,16,123
LU 123,16
M 4,69,3
RA 220,1,14336
HS 25,123
M 5,80,2
LL 18,102
LL 127,103
M 5,81,3
LA 18
PK 123,16,121,33
M 0,22,4
LC_ 127
PK 121,16,121,32
M 0,23,5
M 1,34,4
WL 3
LO 17,17,126
LU 126,17
M 1,35,5
RA 224,1,15360
HS 25,126
M 2,46,4
LL 16,56
LL 104,57
M 2,47,5
LA 16
XL 1,64
PK 126,17,124,33
M 3,58,4
LC_ 104
PK 124,17,124,32
M 3,59,5
ST 116
PO 126
M 4,70,4
WL 3
LO 18,18,127
LU 127,18
M 4,71,5
s_add_u32 s42, s44, s64
RA 228,1,16384
HS 25,127
M 5,82,4
LL 17,58
LL 105,59
M 5,83,5
s_addc_u32 s43, s45, 0
LA 17
PK 127,18,125,33
Z 6,12,0
LC_ 105
PK 125,18,125,32
M 6,13,1
ST 120
PO 127
Z 7,24,0
WL 3
LO 104,16,104
LU 16,104
M 7,25,1
s_add_u32 s42, s44, s65
RA 232,1,17408
HZ 24,16
Z 8,36,0
s_addc_u32 s43, s45, 0
LL 18,60
LL 108,61
M 8,37,1
LA 18
ds_read_b64 v[14:15], v5 offset:0
Z 9,48,0
LC_ 108
XL 2,128
M 9,49,1
s_mul_i32 s53, s50, 25600
Z 10,60,0
WL 4
LO 105,17,105
LU 17,105
M 10,61,1
s_add_u32 s40, %[wlo], s53
RA 236,1,18432
HS 24,17
Z 11,72,0
LL 16,62
LL 109,63
M 11,73,1
s_addc_u32 s41, %[whi], 0
LA 16
v_subrev_u32 v5, s39, v5
M 6,14,2
s_mov_b32 m0, s38
LC_ 109
ST 124
M 6,15,3
DW 0
HX 25
M 7,26,2
WL 3
LO 108,18,108
LU 18,108
W1 25,1024
M 7,27,3
s_add_u32 s37, s37, 32768
RA 216,1,19456
HS 24,18
M 8,38,2
s_cmp_eq_u32 s37, s75
LL 17,64
LL 112,65
M 8,39,3
s_cselect_b32 s37, s74, s37
LA 17
XL 3,192
M 9,50,2
LC_ 112
DW 1024
M 9,51,3
v_add_f32 v14, v14, v15
s_nop 1
v_add_f32_dpp v14, v14, v14 quad_perm:[1,0,3,2] row_mask:0xf bank_mask:0xf
v_add_u32 v4, s39, v4
M 10,62,2
WL 4
LO 109,16,109
LU 16,109
M 10,63,3
RA 220,1,20480
HS 24,16
v_add_f32_dpp v14, v14, v14 quad_perm:[2,3,0,1] row_mask:0xf bank_mask:0xf
M 11,74,2
LL 18,66
LL 113,67
M 11,75,3
LA 18
DW 2048
M 6,16,4
LC_ 113
v_add_f32_dpp v14, v14, v14 row_half_mirror row_mask:0xf bank_mask:0xf
M 6,17,5
v_cvt_f64_f32 v[14:15], v14
M 7,28,4
WL 3
LO 112,17,112
LU 17,112
M 7,29,5
s_mul_i32 s53, s51, 24
RA 224,1,21504
HS 24,17
M 8,40,4
s_bitcmp1_b32 s51, 0
LL 16,68
LL 116,69
M 8,41,5
s_addc_u32 s53, s53, 0
LA 16
s_mov_b64 exec, s[54:55]
SP
s_mov_b64 exec, -1
M 9,52,4
s_lshl_b32 s53, s53, 11
LC_ 116
M 9,53,5
s_bitcmp1_b64 s[90:91], s51
M 10,64,4
WL 3
LO 113,18,113
LU 18,113
M 10,65,5
s_cselect_b64 s[44:45], s[84:85], s[82:83]
RA 228,1,22528
HS 24,18
M 11,76,4
LL 17,70
LL 117,71
M 11,77,5
s_add_u32 s44, s44, s53
LA 17
M 6,18,0
s_addc_u32 s45, s45, 0
LC_ 117
M 6,19,1
s_add_u32 s51, s51, %[d]
M 7,30,0
WL 3
LO 116,16,116
LU 16,116
M 7,31,1
s_cmp_eq_u32 s51, %[wrap]
RA 232,1,23552
HS 24,16
M 8,42,0
s_cselect_b32 s51, %[rst], s51
LL 18,72
LL 120,73
M 8,43,1
s_add_u32 s40, s40, 3072
LA 18
M 9,54,0
s_addc_u32 s41, s41, 0
LC_ 120
s_add_u32 m0, s38, 3072
M 9,55,1
DW 0
M 10,66,0
WL 3
LO 117,17,117
LU 17,117
M 10,67,1
RA 236,1,24576
HS 24,17
M 11,78,0
LL 16,74
LL 121,75
M 11,79,1
LA 16
XL 4,256
M 6,20,2
LC_ 121
DW 1024
M 6,21,3
DW 2048
v_add_u32 v1, s37, v0
M 7,32,2
WL 3
LO 120,18,120
)"
R"(LU 18,120
M 7,33,3
s_lshl_b32 s53, s52, 8
RA 216,1,0
HS 24,18
M 8,44,2
s_add_u32 s48, %[alo], s53
LL 17,76
LL 124,77
M 8,45,3
s_addc_u32 s49, %[ahi], 0
LA 17
v_and_b32 v22, 0xfffffcff, v1
M 9,56,2
s_add_u32 s52, s52, %[d]
LC_ 124
M 9,57,3
s_cmp_eq_u32 s52, %[wrap]
M 10,68,2
WL 3
LO 121,16,121
LU 16,121
M 10,69,3
s_cselect_b32 s52, %[rst], s52
RA 220,1,1024
HS 24,16
M 11,80,2
LL 18,78
LL 125,79
M 11,81,3
LA 18
RV 252,22,5120
M 6,22,4
LC_ 125
DQ 3072
M 6,23,5
XL 5,320
M 7,34,4
WL 4
LO 124,17,124
LU 17,124
M 7,35,5
s_add_u32 s50, s50, %[d]
RA 224,1,2048
HS 24,17
M 8,46,4
s_cmp_eq_u32 s50, %[wrap]
LL 16,80
LL 106,81
M 8,47,5
s_cselect_b32 s50, %[rst], s50
LA 16
v_add_u32 v19, 1, v19
M 9,58,4
s_add_u32 s38, s38, 32768
LC_ 106
M 9,59,5
M 10,70,4
WL 3
LO 125,18,125
LU 18,125
M 10,71,5
RA 228,1,3072
HS 24,18
M 11,82,4
LL 17,82
LL 107,83
M 11,83,5
LA 17
HX 24
Z 0,12,0
s_cmp_eq_u32 s38, s77
LC_ 107
M 0,13,1
s_cselect_b32 s38, s76, s38
WV 36
FP
Z 1,24,0
WL 4
LO 16,16,106
LU 106,16
M 1,25,1
s_sub_u32 s39, 0, s39
RA 232,1,4096
HZ 25,106
Z 2,36,0
LL 18,84
LL 110,85
M 2,37,1
LA 18
PK 106,16,104,33
Z 3,48,0
LC_ 110
PK 104,16,104,32
M 3,49,1
XL 6,384
Z 4,60,0
WL 4
LO 17,17,107
LU 107,17
M 4,61,1
s_add_u32 s42, s44, s60
RA 236,1,6144
HS 25,107
Z 5,72,0
LL 16,86
LL 111,87
M 5,73,1
s_addc_u32 s43, s45, 0
LA 16
PK 107,17,105,33
M 0,14,2
LC_ 111
PK 105,17,105,32
M 0,15,3
ST 104
M 1,26,2
WL 3
LO 18,18,110
LU 110,18
M 1,27,3
RA 216,1,7168
HS 25,110
M 2,38,2
LL 17,88
LL 114,89
M 2,39,3
LA 17
PK 110,18,108,33
M 3,50,2
LC_ 114
PK 108,18,108,32
M 3,51,3
FR
PO 110
M 4,62,2
WL 4
LO 16,16,111
LU 111,16
M 4,63,3
RA 220,1,8192
HS 25,111
M 5,74,2
LL 18,90
LL 115,91
M 5,75,3
LA 18
PK 111,16,109,33
M 0,16,4
LC_ 115
PK 109,16,109,32
M 0,17,5
XL 7,448
PO 111
M 1,28,4
WL 4
LO 17,17,114
LU 114,17
M 1,29,5
s_add_u32 s42, s44, s61
RA 224,1,9216
HS 25,114
M 2,40,4
s_addc_u32 s43, s45, 0
LL 16,92
LL 118,93
M 2,41,5
s_mov_b32 m0, %[pm]
LA 16
PK 114,17,112,33
M 3,52,4
LC_ 118
PK 112,17,112,32
M 3,53,5
M 4,64,4
WL 3
LO 18,18,115
LU 115,18
M 4,65,5
RA 228,1,10240
HS 25,115
M 5,76,4
LL 17,94
LL 119,95
M 5,77,5
LA 17
PK 115,18,113,33
M 0,18,0
LC_ 119
PK 113,18,113,32
M 0,19,1
DF
M 1,30,0
WL 3
LO 16,16,118
LU 118,16
M 1,31,1
RA 232,1,11264
HS 25,118
M 2,42,0
LL 18,96
LL 122,97
M 2,43,1
LA 18
PK 118,16,116,33
v_cmp_gt_u32 vcc, v19, v15
M 3,54,0
LC_ 122
PK 116,16,116,32
M 3,55,1
RV 252,22,5392
PO 118
M 4,66,0
WL 4
LO 17,17,119
LU 119,17
M 4,67,1
s_cbranch_vccnz .Lk1s6
.Lk1r6:
RA 236,1,12288
HS 25,119
M 5,78,0
LL 16,98
LL 123,99
ST 108
M 5,79,1
s_add_u32 s42, s44, s62
LA 16
PK 119,17,117,33
s_addc_u32 s43, s45, 0
M 0,20,2
LC_ 123
PK 117,17,117,32
M 0,21,3
W1 24,0
PO 119
M 1,32,2
WL 5
LO 18,18,122
LU 122,18
M 1,33,3
RA 216,1,13312
HS 25,122
M 2,44,2
LL 17,100
LL 126,101
ST 112
M 2,45,3
LA 17
PK 122,18,120,33
M 3,56,2
LC_ 126
XL 8,512
PK 120,18,120,32
M 3,57,3
s_add_u32 s42, s44, s63
M 4,68,2
WL 4
LO 16,16,123
LU 123,16
M 4,69,3
s_addc_u32 s43, s45, 0
RA 220,1,14336
HS 25,123
M 5,80,2
LL 18,102
LL 127,103
M 5,81,3
LA 18
PK 123,16,121,33
M 0,22,4
LC_ 127
PK 121,16,121,32
M 0,23,5
ST 116
M 1,34,4
WL 3
LO 17,17,126
LU 126,17
M 1,35,5
s_add_u32 s42, s44, s64
RA 224,1,15360
HS 25,126
M 2,46,4
s_addc_u32 s43, s45, 0
LL 16,56
LL 104,57
M 2,47,5
LA 16
PK 126,17,124,33
M 3,58,4
LC_ 104
PK 124,17,124,32
M 3,59,5
XL 9,576
PO 126
M 4,70,4
WL 3
LO 18,18,127
LU 127,18
M 4,71,5
RA 228,1,16384
HS 25,127
M 5,82,4
LL 17,58
LL 105,59
M 5,83,5
LA 17
PK 127,18,125,33
Z 6,12,0
LC_ 105
PK 125,18,125,32
M 6,13,1
ST 120
PO 127
Z 7,24,0
WL 3
LO 104,16,104
LU 16,104
M 7,25,1
s_add_u32 s42, s44, s65
RA 232,1,17408
HZ 24,16
Z 8,36,0
s_addc_u32 s43, s45, 0
LL 18,60
LL 108,61
M 8,37,1
s_mul_i32 s53, s50, 25600
LA 18
ds_read_b64 v[14:15], v5 offset:0
Z 9,48,0
s_add_u32 s40, %[wlo], s53
LC_ 108
v_subrev_u32 v5, s39, v5
M 9,49,1
s_addc_u32 s41, %[whi], 0
Z 10,60,0
WL 4
LO 105,17,105
LU 17,105
M 10,61,1
s_mov_b32 m0, s38
RA 236,1,18432
HS 24,17
Z 11,72,0
LL 16,62
LL 109,63
M 11,73,1
LA 16
XL 10,640
M 6,14,2
LC_ 109
ST 124
M 6,15,3
DW 0
HX 25
M 7,26,2
WL 3
LO 108,18,108
LU 18,108
W1 25,1024
M 7,27,3
s_add_u32 s37, s37, 32768
RA 216,1,19456
HS 24,18
M 8,38,2
s_cmp_eq_u32 s37, s75
LL 17,64
LL 112,65
M 8,39,3
s_cselect_b32 s37, s74, s37
LA 17
DW 1024
M 9,50,2
LC_ 112
v_add_f32 v14, v14, v15
s_nop 1
v_add_f32_dpp v14, v14, v14 quad_perm:[1,0,3,2] row_mask:0xf bank_mask:0xf
M 9,51,3
s_nop 0
v_add_f32_dpp v14, v14, v14 quad_perm:[2,3,0,1] row_mask:0xf bank_mask:0xf
v_add_u32 v4, s39, v4
M 10,62,2
WL 4
LO 109,16,109
LU 16,109
M 10,63,3
RA 220,1,20480
HS 24,16
M 11,74,2
LL 18,66
LL 113,67
M 11,75,3
LA 18
DW 2048
M 6,16,4
LC_ 113
XL 11,704
v_add_f32_dpp v14, v14, v14 row_half_mirror row_mask:0xf bank_mask:0xf
)"
R"(M 6,17,5
v_cvt_f64_f32 v[14:15], v14
M 7,28,4
WL 3
LO 112,17,112
LU 17,112
M 7,29,5
s_mul_i32 s53, s51, 24
RA 224,1,21504
HS 24,17
M 8,40,4
s_bitcmp1_b32 s51, 0
LL 16,68
LL 116,69
M 8,41,5
s_addc_u32 s53, s53, 0
LA 16
s_mov_b64 exec, s[54:55]
SP
s_mov_b64 exec, -1
M 9,52,4
s_lshl_b32 s53, s53, 11
LC_ 116
M 9,53,5
s_bitcmp1_b64 s[90:91], s51
M 10,64,4
WL 3
LO 113,18,113
LU 18,113
M 10,65,5
s_cselect_b64 s[44:45], s[84:85], s[82:83]
RA 228,1,22528
HS 24,18
M 11,76,4
LL 17,70
LL 117,71
M 11,77,5
s_add_u32 s44, s44, s53
LA 17
M 6,18,0
s_addc_u32 s45, s45, 0
LC_ 117
M 6,19,1
s_add_u32 s51, s51, %[d]
M 7,30,0
WL 3
LO 116,16,116
LU 16,116
M 7,31,1
s_cmp_eq_u32 s51, %[wrap]
RA 232,1,23552
HS 24,16
M 8,42,0
s_cselect_b32 s51, %[rst], s51
LL 18,72
LL 120,73
M 8,43,1
s_add_u32 s40, s40, 3072
LA 18
M 9,54,0
s_addc_u32 s41, s41, 0
LC_ 120
s_add_u32 m0, s38, 3072
M 9,55,1
DW 0
M 10,66,0
WL 3
LO 117,17,117
LU 17,117
M 10,67,1
RA 236,1,24576
HS 24,17
M 11,78,0
LL 16,74
LL 121,75
M 11,79,1
LA 16
DW 1024
M 6,20,2
s_bitcmp1_b64 s[92:93], 48
LC_ 121
M 6,21,3
s_cselect_b32 s58, 16, 0
v_add_u32 v1, s37, v0
M 7,32,2
WL 3
LO 120,18,120
LU 18,120
M 7,33,3
s_lshr_b32 s58, 0xda43cb52, s58
RA 216,1,0
HS 24,18
M 8,44,2
LL 17,76
LL 124,77
M 8,45,3
LA 17
v_and_b32 v22, 0xfffffcff, v1
M 9,56,2
LC_ 124
v_and_b32 v11, 0x1f0, v0
M 9,57,3
v_min_u32 v11, 368, v11
M 10,68,2
WL 3
LO 121,16,121
LU 16,121
M 10,69,3
RA 220,1,1024
HS 24,16
M 11,80,2
LL 18,78
LL 125,79
M 11,81,3
LA 18
RV 252,22,5120
DW 2048
v_lshlrev_b32 v11, 3, v11
M 6,22,4
s_lshl_b32 s53, s52, 8
LC_ 125
M 6,23,5
s_add_u32 s48, %[alo], s53
M 7,34,4
WL 4
LO 124,17,124
LU 17,124
M 7,35,5
s_addc_u32 s49, %[ahi], 0
RA 224,1,2048
HS 24,17
M 8,46,4
s_add_u32 s52, s52, %[d]
LL 16,80
LL 106,81
s_cmp_eq_u32 s52, %[wrap]
s_cselect_b32 s52, %[rst], s52
M 8,47,5
LA 16
DQ 3072
M 9,58,4
s_add_u32 s50, s50, %[d]
LC_ 106
M 9,59,5
M 10,70,4
WL 3
LO 125,18,125
LU 18,125
M 10,71,5
RA 228,1,3072
HS 24,18
M 11,82,4
LL 17,82
LL 107,83
M 11,83,5
LA 17
HX 24
s_cmp_eq_u32 s50, %[wrap]
s_cselect_b32 s50, %[rst], s50
Z 0,12,0
LC_ 107
v_add_u32 v19, 1, v19
M 0,13,1
s_add_u32 s38, s38, 32768
Z 1,24,0
WL 3
LO 16,16,106
LU 106,16
M 1,25,1
s_cmp_eq_u32 s38, s77
RA 232,1,4096
HZ 25,106
Z 2,36,0
s_cselect_b32 s38, s76, s38
LL 18,84
LL 110,85
M 2,37,1
LA 18
PK 106,16,104,33
Z 3,48,0
LC_ 110
PK 104,16,104,32
M 3,49,1
WV 42
FP
s_sub_u32 s39, 0, s39
Z 4,60,0
WL 4
LO 17,17,107
LU 107,17
M 4,61,1
s_add_u32 s42, s44, s60
RA 236,1,6144
HS 25,107
Z 5,72,0
LL 16,86
LL 111,87
M 5,73,1
s_addc_u32 s43, s45, 0
LA 16
PK 107,17,105,33
M 0,14,2
LC_ 111
PK 105,17,105,32
M 0,15,3
ST 104
M 1,26,2
WL 4
LO 18,18,110
LU 110,18
M 1,27,3
RA 216,1,7168
HS 25,110
M 2,38,2
LL 17,88
LL 114,89
M 2,39,3
LA 17
PK 110,18,108,33
M 3,50,2
LC_ 114
PK 108,18,108,32
M 3,51,3
FR
PO 110
M 4,62,2
WL 4
LO 16,16,111
LU 111,16
M 4,63,3
s_add_u32 s42, s44, s61
RA 220,1,8192
HS 25,111
M 5,74,2
LL 18,90
LL 115,91
M 5,75,3
s_addc_u32 s43, s45, 0
LA 18
PK 111,16,109,33
M 0,16,4
s_mov_b32 m0, %[pm]
LC_ 115
PK 109,16,109,32
M 0,17,5
DF
PO 111
M 1,28,4
WL 4
LO 17,17,114
LU 114,17
M 1,29,5
RA 224,1,9216
HS 25,114
M 2,40,4
LL 16,92
LL 118,93
M 2,41,5
LA 16
PK 114,17,112,33
M 3,52,4
LC_ 118
PK 112,17,112,32
M 3,53,5
WL 6
ST 108
v_cmp_gt_u32 vcc, v19, v15
M 4,64,4
WL 3
LO 18,18,115
LU 115,18
M 4,65,5
s_add_u32 s42, s44, s62
RA 228,1,10240
HS 25,115
M 5,76,4
LL 17,94
LL 119,95
M 5,77,5
s_cbranch_vccnz .Lk1s7
.Lk1r7:
LA 17
PK 115,18,113,33
M 0,18,0
s_addc_u32 s43, s45, 0
LC_ 119
PK 113,18,113,32
M 0,19,1
W1 24,0
M 1,30,0
WL 4
LO 16,16,118
LU 118,16
M 1,31,1
RA 232,1,11264
HS 25,118
M 2,42,0
LL 18,96
LL 122,97
M 2,43,1
LA 18
PK 118,16,116,33
M 3,54,0
LC_ 122
PK 116,16,116,32
M 3,55,1
RV 252,22,5392
PO 118
M 4,66,0
WL 5
LO 17,17,119
LU 119,17
M 4,67,1
RA 236,1,12288
HS 25,119
M 5,78,0
LL 16,98
LL 123,99
M 5,79,1
LA 16
PK 119,17,117,33
M 0,20,2
LC_ 123
PK 117,17,117,32
M 0,21,3
ST 112
PO 119
M 1,32,2
WL 4
LO 18,18,122
LU 122,18
M 1,33,3
RA 216,1,13312
HS 25,122
M 2,44,2
LL 17,100
LL 126,101
M 2,45,3
LA 17
PK 122,18,120,33
M 3,56,2
LC_ 126
PK 120,18,120,32
M 3,57,3
v_lshrrev_b32 v3, 9, v0
M 4,68,2
WL 3
LO 16,16,123
LU 123,16
M 4,69,3
RA 220,1,14336
HS 25,123
v_lshl_add_u32 v3, v3, 2, 0
v_bfe_u32 v3, s58, v3, 4
v_mad_u32_u24 v3, v3, s97, v11
M 5,80,2
LL 18,102
LL 127,103
M 5,81,3
s_add_u32 s42, s44, s63
LA 18
PK 123,16,121,33
M 0,22,4
s_addc_u32 s43, s45, 0
LC_ 127
PK 121,16,121,32
M 0,23,5
ST 116
M 1,34,4
WL 3
LO 17,17,126
LU 126,17
M 1,35,5
s_add_u32 s42, s44, s64
RA 224,1,15360
HS 25,126
M 2,46,4
s_addc_u32 s43, s45, 0
LL 16,56
LL 104,57
M 2,47,5
LA 16
PK 126,17,124,33
M 3,58,4
LC_ 104
PK 124,17,124,32
M 3,59,5
ST 120
PO 126
M 4,70,4
WL 3
LO 18,18,127
LU 127,18
M 4,71,5
s_add_u32 s42, s44, s65
RA 228,1,16384
HS 25,127
M 5,82,4
LL 17,58
LL 105,59
)"
R"(M 5,83,5
s_addc_u32 s43, s45, 0
LA 17
PK 127,18,125,33
Z 6,12,0
LC_ 105
PK 125,18,125,32
M 6,13,1
ds_read_b64 v[14:15], v5 offset:0
PO 127
Z 7,24,0
WL 4
LO 104,16,104
LU 16,104
M 7,25,1
RA 232,1,17408
HZ 24,16
Z 8,36,0
LL 18,60
LL 108,61
M 8,37,1
s_mul_i32 s53, s50, 25600
LA 18
v_subrev_u32 v5, s39, v5
Z 9,48,0
s_add_u32 s40, %[wlo], s53
LC_ 108
HX 25
M 9,49,1
s_addc_u32 s41, %[whi], 0
W1 25,1024
Z 10,60,0
WL 5
LO 105,17,105
LU 17,105
M 10,61,1
s_mov_b32 m0, s38
RA 236,1,18432
HS 24,17
Z 11,72,0
LL 16,62
LL 109,63
M 11,73,1
LA 16
ST 124
M 6,14,2
LC_ 109
DW 0
M 6,15,3
WL 7
v_add_f32 v14, v14, v15
v_add_u32 v4, s39, v4
M 7,26,2
WL 4
LO 108,18,108
LU 18,108
M 7,27,3
s_add_u32 s37, s37, 32768
RA 216,1,19456
HS 24,18
M 8,38,2
s_cmp_eq_u32 s37, s75
LL 17,64
LL 112,65
M 8,39,3
s_cselect_b32 s37, s74, s37
LA 17
DW 1024
M 9,50,2
LC_ 112
v_add_f32_dpp v14, v14, v14 quad_perm:[1,0,3,2] row_mask:0xf bank_mask:0xf
M 9,51,3
s_nop 0
v_add_f32_dpp v14, v14, v14 quad_perm:[2,3,0,1] row_mask:0xf bank_mask:0xf
M 10,62,2
WL 3
LO 109,16,109
LU 16,109
M 10,63,3
RA 220,1,20480
HS 24,16
M 11,74,2
LL 18,66
LL 113,67
M 11,75,3
LA 18
DW 2048
M 6,16,4
LC_ 113
v_add_f32_dpp v14, v14, v14 row_half_mirror row_mask:0xf bank_mask:0xf
M 6,17,5
v_cvt_f64_f32 v[14:15], v14
M 7,28,4
WL 3
LO 112,17,112
LU 17,112
M 7,29,5
s_mul_i32 s53, s51, 24
RA 224,1,21504
HS 24,17
M 8,40,4
s_bitcmp1_b32 s51, 0
LL 16,68
LL 116,69
M 8,41,5
s_addc_u32 s53, s53, 0
LA 16
s_mov_b64 exec, s[54:55]
SP
s_mov_b64 exec, -1
M 9,52,4
s_lshl_b32 s53, s53, 11
LC_ 116
M 9,53,5
s_bitcmp1_b64 s[90:91], s51
M 10,64,4
WL 3
LO 113,18,113
LU 18,113
M 10,65,5
s_cselect_b64 s[44:45], s[84:85], s[82:83]
RA 228,1,22528
HS 24,18
M 11,76,4
LL 17,70
LL 117,71
M 11,77,5
s_add_u32 s44, s44, s53
LA 17
M 6,18,0
s_addc_u32 s45, s45, 0
LC_ 117
M 6,19,1
s_add_u32 s51, s51, %[d]
M 7,30,0
WL 3
LO 116,16,116
LU 16,116
M 7,31,1
s_cmp_eq_u32 s51, %[wrap]
RA 232,1,23552
HS 24,16
M 8,42,0
s_cselect_b32 s51, %[rst], s51
LL 18,72
LL 120,73
M 8,43,1
s_add_u32 s40, s40, 3072
LA 18
M 9,54,0
s_addc_u32 s41, s41, 0
LC_ 120
s_add_u32 m0, s38, 3072
M 9,55,1
DW 0
M 10,66,0
WL 3
LO 117,17,117
LU 17,117
M 10,67,1
RA 236,1,24576
HS 24,17
M 11,78,0
LL 16,74
LL 121,75
M 11,79,1
LA 16
DW 1024
M 6,20,2
LC_ 121
DY
M 6,21,3
DW 2048
v_add_u32 v1, s37, v0
M 7,32,2
WL 3
LO 120,18,120
LU 18,120
M 7,33,3
s_lshl_b32 s53, s52, 8
RA 216,1,0
HS 24,18
M 8,44,2
s_add_u32 s48, %[alo], s53
LL 17,76
LL 124,77
M 8,45,3
s_addc_u32 s49, %[ahi], 0
LA 17
v_and_b32 v22, 0xfffffcff, v1
M 9,56,2
s_add_u32 s52, s52, %[d]
LC_ 124
M 9,57,3
s_cmp_eq_u32 s52, %[wrap]
M 10,68,2
WL 3
LO 121,16,121
LU 16,121
M 10,69,3
s_cselect_b32 s52, %[rst], s52
RA 220,1,1024
HS 24,16
M 11,80,2
LL 18,78
LL 125,79
M 11,81,3
LA 18
RV 252,22,5120
M 6,22,4
LC_ 125
DQ 3072
M 6,23,5
s_add_u32 s50, s50, %[d]
M 7,34,4
WL 4
LO 124,17,124
LU 17,124
M 7,35,5
s_cmp_eq_u32 s50, %[wrap]
RA 224,1,2048
HS 24,17
M 8,46,4
s_cselect_b32 s50, %[rst], s50
LL 16,80
LL 106,81
M 8,47,5
LA 16
v_add_u32 v19, 1, v19
M 9,58,4
s_add_u32 s38, s38, 32768
LC_ 106
M 9,59,5
M 10,70,4
WL 3
LO 125,18,125
LU 18,125
M 10,71,5
RA 228,1,3072
HS 24,18
M 11,82,4
LL 17,82
LL 107,83
M 11,83,5
LA 17
HX 24
Z 0,12,0
s_cmp_eq_u32 s38, s77
LC_ 107
M 0,13,1
s_cselect_b32 s38, s76, s38
WV 38
FP
Z 1,24,0
WL 4
LO 16,16,106
LU 106,16
M 1,25,1
s_sub_u32 s39, 0, s39
RA 232,1,4096
HZ 25,106
Z 2,36,0
LL 18,84
LL 110,85
M 2,37,1
LA 18
PK 106,16,104,33
Z 3,48,0
LC_ 110
PK 104,16,104,32
M 3,49,1
Z 4,60,0
WL 4
LO 17,17,107
LU 107,17
M 4,61,1
s_add_u32 s42, s44, s60
RA 236,1,6144
HS 25,107
Z 5,72,0
LL 16,86
LL 111,87
M 5,73,1
s_addc_u32 s43, s45, 0
LA 16
PK 107,17,105,33
M 0,14,2
LC_ 111
PK 105,17,105,32
M 0,15,3
ST 104
M 1,26,2
WL 3
LO 18,18,110
LU 110,18
M 1,27,3
RA 216,1,7168
HS 25,110
M 2,38,2
LL 17,88
LL 114,89
M 2,39,3
LA 17
PK 110,18,108,33
M 3,50,2
LC_ 114
PK 108,18,108,32
M 3,51,3
FR
PO 110
M 4,62,2
WL 4
LO 16,16,111
LU 111,16
M 4,63,3
s_add_u32 s42, s44, s61
RA 220,1,8192
HS 25,111
M 5,74,2
LL 18,90
LL 115,91
M 5,75,3
s_addc_u32 s43, s45, 0
LA 18
PK 111,16,109,33
M 0,16,4
s_mov_b32 m0, %[pm]
LC_ 115
PK 109,16,109,32
M 0,17,5
DF
PO 111
M 1,28,4
WL 4
LO 17,17,114
LU 114,17
M 1,29,5
RA 224,1,9216
HS 25,114
M 2,40,4
LL 16,92
LL 118,93
M 2,41,5
LA 16
PK 114,17,112,33
M 3,52,4
LC_ 118
PK 112,17,112,32
M 3,53,5
WL 6
ST 108
v_cmp_gt_u32 vcc, v19, v15
M 4,64,4
WL 3
LO 18,18,115
LU 115,18
M 4,65,5
s_add_u32 s42, s44, s62
RA 228,1,10240
HS 25,115
M 5,76,4
LL 17,94
LL 119,95
M 5,77,5
s_cbranch_vccnz .Lk1s8
.Lk1r8:
LA 17
PK 115,18,113,33
M 0,18,0
s_addc_u32 s43, s45, 0
LC_ 119
PK 113,18,113,32
M 0,19,1
W1 24,0
M 1,30,0
WL 4
LO 16,16,118
LU 118,16
M 1,31,1
RA 232,1,11264
HS 25,118
M 2,42,0
LL 18,96
LL 122,97
M 2,43,1
LA 18
PK 118,16,116,33
M 3,54,0
LC_ 122
PK 116,16,116,32
M 3,55,1
RV 252,22,5392
PO 118
M 4,66,0
WL 5
LO 17,17,119
LU 119,17
M 4,67,1
RA 236,1,12288
HS 25,119
)"
R"(M 5,78,0
LL 16,98
LL 123,99
M 5,79,1
LA 16
PK 119,17,117,33
M 0,20,2
LC_ 123
PK 117,17,117,32
BW 0
M 0,21,3
ST 112
PO 119
M 1,32,2
WL 5
RV 40,8,6144
LO 18,18,122
LU 122,18
M 1,33,3
RA 216,1,13312
RV 44,8,6208
HS 25,122
M 2,44,2
LL 17,100
LL 126,101
M 2,45,3
LA 17
PK 122,18,120,33
v_lshrrev_b32 v3, 9, v0
v_lshl_add_u32 v3, v3, 2, 8
v_bfe_u32 v3, s58, v3, 4
v_mad_u32_u24 v3, v3, s97, v11
RV 48,8,6272
M 3,56,2
s_add_u32 s42, s44, s63
LC_ 126
BR 0
PK 120,18,120,32
M 3,57,3
s_addc_u32 s43, s45, 0
ST 116
BW 1
M 4,68,2
WL 9
LO 16,16,123
LU 123,16
M 4,69,3
s_add_u32 s42, s44, s64
RA 220,1,14336
BR 1
HS 25,123
M 5,80,2
WL 3
LL 18,102
LL 127,103
M 5,81,3
s_addc_u32 s43, s45, 0
LA 18
PK 123,16,121,33
LZ 40,0
BW 2
M 0,22,4
LC_ 127
PK 121,16,121,32
M 0,23,5
ST 120
RV 40,8,6336
M 1,34,4
BR 2
LO 17,17,126
LU 126,17
M 1,35,5
WL 5
RA 224,1,15360
HS 25,126
LS 44,1
BW 3
M 2,46,4
LL 16,56
LL 104,57
M 2,47,5
LA 16
PK 126,17,124,33
M 3,58,4
LC_ 104
RV 44,8,6400
PK 124,17,124,32
M 3,59,5
ds_read_b64 v[14:15], v5 offset:0
BR 3
PO 126
M 4,70,4
WL 7
LO 18,18,127
LU 127,18
M 4,71,5
s_add_u32 s42, s44, s65
RA 228,1,16384
HS 25,127
LS 48,2
s_addc_u32 s43, s45, 0
BW 4
M 5,82,4
LL 17,58
LL 105,59
M 5,83,5
LA 17
RV 48,8,6464
PK 127,18,125,33
Z 6,12,0
LC_ 105
PK 125,18,125,32
M 6,13,1
BR 4
PO 127
Z 7,24,0
WL 9
LO 104,16,104
LU 16,104
M 7,25,1
WL 6
RA 232,1,17408
HZ 24,16
LS 40,3
WV 43
BW 5
Z 8,36,0
WV 42
LL 18,60
LL 108,61
M 8,37,1
s_mul_i32 s53, s50, 25600
LA 18
RV 40,8,6528
Z 9,48,0
s_add_u32 s40, %[wlo], s53
LC_ 108
BR 5
M 9,49,1
s_addc_u32 s41, %[whi], 0
v_subrev_u32 v5, s39, v5
HX 25
WL 6
LS 44,4
s_mov_b32 m0, s38
BW 6
Z 10,60,0
LO 105,17,105
LU 17,105
M 10,61,1
RA 236,1,18432
HS 24,17
Z 11,72,0
RV 44,8,6592
LL 16,62
LL 109,63
M 11,73,1
LA 16
W1 25,1024
BR 6
M 6,14,2
LC_ 109
ST 124
M 6,15,3
WL 7
DW 0
v_add_f32 v14, v14, v15
LS 48,5
WV 42
BW 7
M 7,26,2
LO 108,18,108
LU 18,108
M 7,27,3
s_add_u32 s37, s37, 32768
RA 216,1,19456
HS 24,18
M 8,38,2
s_cmp_eq_u32 s37, s75
RV 48,8,6656
LL 17,64
LL 112,65
M 8,39,3
s_cselect_b32 s37, s74, s37
LA 17
DW 1024
BR 7
M 9,50,2
LC_ 112
v_add_f32_dpp v14, v14, v14 quad_perm:[1,0,3,2] row_mask:0xf bank_mask:0xf
M 9,51,3
WL 7
v_add_f32_dpp v14, v14, v14 quad_perm:[2,3,0,1] row_mask:0xf bank_mask:0xf
v_add_u32 v4, s39, v4
LS 40,6
WV 39
BW 8
M 10,62,2
WL 7
LO 109,16,109
LU 16,109
M 10,63,3
RA 220,1,20480
RV 40,8,6720
HS 24,16
M 11,74,2
BR 8
LL 18,66
LL 113,67
M 11,75,3
LA 18
DW 2048
M 6,16,4
LC_ 113
v_add_f32_dpp v14, v14, v14 row_half_mirror row_mask:0xf bank_mask:0xf
M 6,17,5
WL 7
v_cvt_f64_f32 v[14:15], v14
LS 44,7
WV 36
BW 9
M 7,28,4
WL 3
RV 44,8,6784
LO 112,17,112
LU 17,112
M 7,29,5
s_mul_i32 s53, s51, 24
RA 224,1,21504
HS 24,17
M 8,40,4
s_bitcmp1_b32 s51, 0
BR 9
LL 16,68
LL 116,69
M 8,41,5
s_addc_u32 s53, s53, 0
LA 16
s_mov_b64 exec, s[54:55]
SP
s_mov_b64 exec, -1
LS 48,8
s_lshl_b32 s53, s53, 11
BW 10
M 9,52,4
s_bitcmp1_b64 s[90:91], s51
LC_ 116
RV 48,8,6848
M 9,53,5
s_cselect_b64 s[44:45], s[84:85], s[82:83]
RA 240,7,0
BR 10
M 10,64,4
WL 10
LO 113,18,113
LU 18,113
M 10,65,5
s_add_u32 s44, s44, s53
RA 228,1,22528
HS 24,18
M 11,76,4
WL 7
LL 17,70
LL 117,71
M 11,77,5
s_addc_u32 s45, s45, 0
LA 17
M 6,18,0
s_add_u32 s51, s51, %[d]
LC_ 117
M 6,19,1
s_cmp_eq_u32 s51, %[wrap]
LS 40,9
s_cselect_b32 s51, %[rst], s51
WV 32
BW 11
M 7,30,0
WL 4
RA 244,7,256
LO 116,16,116
LU 16,116
M 7,31,1
s_add_u32 s40, s40, 3072
RA 232,1,23552
HS 24,16
M 8,42,0
s_addc_u32 s41, s41, 0
BR 11
LL 18,72
LL 120,73
M 8,43,1
s_add_u32 m0, s38, 3072
LA 18
DW 0
LS 44,10
RA 248,7,512
M 9,54,0
LC_ 120
DY
M 9,55,1
DW 1024
M 10,66,0
WL 7
LO 117,17,117
LU 17,117
M 10,67,1
WL 3
RA 236,1,24576
HS 24,17
LS 48,11
RA 252,7,768
M 11,78,0
LL 16,74
LL 121,75
s_nop 3
v_mul_f32 v36, 0xbb2aaaab, v36
M 11,79,1
LA 16
DW 2048
v_mul_f32 v37, 0xbb2aaaab, v37
M 6,20,2
LC_ 121
v_mul_f32 v38, 0xbb2aaaab, v38
v_mul_f32 v39, 0xbb2aaaab, v39
M 6,21,3
s_lshl_b32 s53, s52, 8
v_add_u32 v1, s37, v0
LK 40,240,0,0
LK 44,242,0,2
s_add_u32 s48, %[alo], s53
RA 240,7,1024
LK 48,244,1,0
s_nop 0
CF 128,40,41
LK 248,246,1,2
s_addc_u32 s49, %[ahi], 0
RA 244,7,1280
CF 129,42,43
M 7,32,2
WL 6
LO 120,18,120
LU 18,120
M 7,33,3
s_add_u32 s52, s52, %[d]
RA 216,1,0
HS 24,18
M 8,44,2
s_cmp_eq_u32 s52, %[wrap]
LL 17,76
LL 124,77
M 8,45,3
s_cselect_b32 s52, %[rst], s52
LA 17
DQ 3072
M 9,56,2
LC_ 124
v_and_b32 v22, 0xfffffcff, v1
CF 130,44,45
M 9,57,3
CF 131,46,47
LK 40,248,2,0
CF 132,48,49
LK 44,250,2,2
RA 248,7,1536
CF 133,50,51
GZ 0
WL 8
CF 134,248,249
CF 135,250,251
LK 48,252,3,0
CF 136,40,41
LK 248,254,3,2
RA 252,7,1792
CF 137,42,43
GS 1
WL 6
CF 138,44,45
CF 139,46,47
LK 40,240,4,0
CF 140,48,49
LK 44,242,4,2
RA 240,7,2048
CF 141,50,51
GS 2
WL 6
CF 142,248,249
CF 143,250,251
LK 48,244,5,0
CF 144,40,41
LK 248,246,5,2
RA 244,7,2304
CF 145,42,43
GS 3
CF 146,44,45
CF 147,46,47
M 10,68,2
WL 3
LO 121,16,121
LU 16,121
M 10,69,3
)"
R"(s_add_u32 s50, s50, %[d]
RA 220,1,1024
HS 24,16
LK 40,248,6,0
WL 3
CF 148,48,49
LK 44,250,6,2
s_cmp_eq_u32 s50, %[wrap]
RA 248,7,2560
CF 149,50,51
GS 4
s_cselect_b32 s50, %[rst], s50
CF 150,248,249
CF 151,250,251
LK 48,252,7,0
CF 152,40,41
LK 248,254,7,2
RA 252,7,2816
CF 153,42,43
GS 5
WL 4
CF 154,44,45
CF 155,46,47
LK 40,240,8,0
CF 156,48,49
LK 44,242,8,2
CF 157,50,51
GS 6
CF 158,248,249
CF 159,250,251
M 11,80,2
LL 18,78
LL 125,79
M 11,81,3
WL 3
LA 18
RV 252,22,5120
LK 48,244,9,0
CF 160,40,41
LK 248,246,9,2
CF 161,42,43
GS 7
CF 162,44,45
CF 163,46,47
M 6,22,4
LC_ 125
M 6,23,5
WL 4
v_add_u32 v19, 1, v19
M 7,34,4
WV 32
LO 124,17,124
LU 17,124
M 7,35,5
s_add_u32 s38, s38, 32768
RA 224,1,2048
HS 24,17
M 8,46,4
s_cmp_eq_u32 s38, s77
LL 16,80
LL 106,81
M 8,47,5
s_cselect_b32 s38, s76, s38
LA 16
FP
LK 40,248,10,0
CF 164,48,49
LK 44,250,10,2
s_sub_u32 s39, 0, s39
CF 165,50,51
GS 8
CF 166,248,249
CF 167,250,251
M 9,58,4
LC_ 106
M 9,59,5
M 10,70,4
WL 4
LO 125,18,125
LU 18,125
M 10,71,5
RA 228,1,3072
HS 24,18
LK 48,252,11,0
CF 168,40,41
LK 248,254,11,2
CF 169,42,43
GS 9
CF 170,44,45
CF 171,46,47
M 11,82,4
LL 17,82
LL 107,83
M 11,83,5
LA 17
HX 24
Z 0,12,0
LC_ 107
RV 36,8,6912
M 0,13,1
GS 10
CF 172,48,49
Z 1,24,0
WL 4
LO 16,16,106
LU 106,16
M 1,25,1
RA 232,1,4096
HZ 25,106
Z 2,36,0
LL 18,84
LL 110,85
M 2,37,1
LA 18
PK 106,16,104,33
Z 3,48,0
LC_ 110
PK 104,16,104,32
M 3,49,1
CF 173,50,51
CF 174,248,249
Z 4,60,0
WL 4
LO 17,17,107
LU 107,17
M 4,61,1
s_add_u32 s42, s44, s60
RA 236,1,6144
HS 25,107
Z 5,72,0
LL 16,86
LL 111,87
M 5,73,1
s_addc_u32 s43, s45, 0
LA 16
PK 107,17,105,33
M 0,14,2
LC_ 111
PK 105,17,105,32
M 0,15,3
ST 104
CF 175,250,251
M 1,26,2
WL 3
LO 18,18,110
LU 110,18
M 1,27,3
RA 216,1,7168
HS 25,110
GS 11
M 2,38,2
LL 17,88
LL 114,89
M 2,39,3
LA 17
PK 110,18,108,33
M 3,50,2
LC_ 114
PK 108,18,108,32
M 3,51,3
FR
PO 110
M 4,62,2
WL 4
LO 16,16,111
LU 111,16
M 4,63,3
s_add_u32 s42, s44, s61
RA 220,1,8192
HS 25,111
M 5,74,2
LL 18,90
LL 115,91
M 5,75,3
s_addc_u32 s43, s45, 0
LA 18
PK 111,16,109,33
M 0,16,4
s_mov_b32 m0, %[pm]
LC_ 115
PK 109,16,109,32
M 0,17,5
DF
PO 111
M 1,28,4
WL 4
LO 17,17,114
LU 114,17
M 1,29,5
RA 224,1,9216
HS 25,114
M 2,40,4
LL 16,92
LL 118,93
M 2,41,5
LA 16
PK 114,17,112,33
M 3,52,4
LC_ 118
PK 112,17,112,32
M 3,53,5
WL 6
ST 108
v_cmp_gt_u32 vcc, v19, v15
M 4,64,4
WL 3
LO 18,18,115
LU 115,18
M 4,65,5
s_add_u32 s42, s44, s62
RA 228,1,10240
HS 25,115
M 5,76,4
LL 17,94
LL 119,95
v_cndmask_b32 v52, v52, v53, s[78:79]
M 5,77,5
s_cbranch_vccnz .Lk1s9
.Lk1r9:
LA 17
PK 115,18,113,33
M 0,18,0
s_addc_u32 s43, s45, 0
LC_ 119
PK 113,18,113,32
v_cndmask_b32 v54, v54, v55, s[78:79]
M 0,19,1
W1 24,0
v_cndmask_b32 v52, v52, v54, s[80:81]
M 1,30,0
WL 4
LO 16,16,118
LU 118,16
v_mul_f32 v52, 0x3b2aaaab, v52
M 1,31,1
RA 232,1,11264
HS 25,118
v_med3_f32 v53, v52, v39, v37
M 2,42,0
LL 18,96
LL 122,97
LG 52,53
M 2,43,1
LA 18
PK 118,16,116,33
C 52,52,52
M 3,54,0
LC_ 122
PK 116,16,116,32
v_lshlrev_b32 v52, 16, v52
M 3,55,1
RV 252,22,5392
PO 118
v_add_f32 v52, v36, v52
M 4,66,0
WL 5
LO 17,17,119
LU 119,17
C 52,52,52
M 4,67,1
RA 236,1,12288
HS 25,119
M 5,78,0
LL 16,98
LL 123,99
v_lshlrev_b32 v52, 16, v52
M 5,79,1
LA 16
PK 119,17,117,33
v_sqrt_f32 v52, v52
M 0,20,2
LC_ 123
PK 117,17,117,32
C 52,52,52
M 0,21,3
ST 112
PO 119
v_lshlrev_b32 v52, 16, v52
M 1,32,2
WL 4
LO 18,18,122
LU 122,18
v_rcp_f32 v52, v52
M 1,33,3
s_add_u32 s42, s44, s63
RA 216,1,13312
HS 25,122
v_mul_f32 v52, v38, v52
M 2,44,2
s_addc_u32 s43, s45, 0
LL 17,100
LL 126,101
CF 52,52,52
M 2,45,3
LA 17
PK 122,18,120,33
ds_bpermute_b32 v13, v28, v52
M 3,56,2
LC_ 126
PK 120,18,120,32
M 3,57,3
ST 116
M 4,68,2
WL 4
LO 16,16,123
LU 123,16
M 4,69,3
s_add_u32 s42, s44, s64
RA 220,1,14336
HS 25,123
M 5,80,2
LL 18,102
LL 127,103
M 5,81,3
s_addc_u32 s43, s45, 0
LA 18
PK 123,16,121,33
M 0,22,4
LC_ 127
PK 121,16,121,32
M 0,23,5
ST 120
M 1,34,4
WL 3
LO 17,17,126
LU 126,17
M 1,35,5
RA 224,1,15360
HS 25,126
XS 128
M 2,46,4
LL 16,56
LL 104,57
M 2,47,5
LA 16
PK 126,17,124,33
M 3,58,4
LC_ 104
PK 124,17,124,32
XS 129
M 3,59,5
ds_read_b64 v[14:15], v5 offset:0
PO 126
M 4,70,4
WL 4
LO 18,18,127
LU 127,18
M 4,71,5
s_add_u32 s42, s44, s65
RA 228,1,16384
HS 25,127
XS 130
M 5,82,4
LL 17,58
LL 105,59
M 5,83,5
s_addc_u32 s43, s45, 0
LA 17
PK 127,18,125,33
Z 6,12,0
LC_ 105
PK 125,18,125,32
XS 131
M 6,13,1
PO 127
v_subrev_u32 v5, s39, v5
Z 7,24,0
WL 4
LO 104,16,104
LU 16,104
XS 132
M 7,25,1
RA 232,1,17408
HZ 24,16
Z 8,36,0
WL 4
LL 18,60
LL 108,61
M 8,37,1
s_mul_i32 s53, s50, 25600
LA 18
v_add_f32 v14, v14, v15
XS 133
Z 9,48,0
s_add_u32 s40, %[wlo], s53
LC_ 108
v_add_f32_dpp v14, v14, v14 quad_perm:[1,0,3,2] row_mask:0xf bank_mask:0xf
M 9,49,1
s_addc_u32 s41, %[whi], 0
v_add_f32_dpp v14, v14, v14 quad_perm:[2,3,0,1] row_mask:0xf bank_mask:0xf
HX 25
Z 10,60,0
WL 3
LO 105,17,105
LU 17,105
XS 134
M 10,61,1
s_mov_b32 m0, s38
RA 236,1,18432
HS 24,17
)"
R"(Z 11,72,0
LL 16,62
LL 109,63
M 11,73,1
LA 16
W1 25,1024
XS 135
M 6,14,2
LC_ 109
ST 124
M 6,15,3
DW 0
v_add_f32_dpp v14, v14, v14 row_half_mirror row_mask:0xf bank_mask:0xf
v_min_u32 v35, v35, v13
M 7,26,2
WL 4
LO 108,18,108
LU 18,108
M 7,27,3
s_add_u32 s37, s37, 32768
RA 216,1,19456
HS 24,18
M 8,38,2
s_cmp_eq_u32 s37, s75
LL 17,64
LL 112,65
XS 136
M 8,39,3
s_cselect_b32 s37, s74, s37
LA 17
v_cvt_f64_f32 v[14:15], v14
M 9,50,2
s_mul_i32 s53, %[d], 0
LC_ 112
s_mov_b64 exec, s[54:55]
SP
s_mov_b64 exec, -1
M 9,51,3
s_add_u32 s53, s53, s57
v_add_u32 v4, s39, v4
XS 137
M 10,62,2
WL 3
LO 109,16,109
LU 16,109
M 10,63,3
s_add_u32 s46, %[xlo], s53
RA 220,1,20480
HS 24,16
M 11,74,2
LL 18,66
LL 113,67
XS 138
M 11,75,3
s_addc_u32 s47, %[xhi], 0
LA 18
XS 139
M 6,16,4
s_bitcmp1_b64 s[92:93], 48
LC_ 113
XS 140
M 6,17,5
s_cselect_b32 s58, 16, 0
XS 141
XS 142
M 7,28,4
WL 3
LO 112,17,112
LU 17,112
M 7,29,5
s_lshr_b32 s58, 0xda43cb52, s58
RA 224,1,21504
HS 24,17
M 8,40,4
LL 16,68
LL 116,69
M 8,41,5
LA 16
v_lshrrev_b32 v3, 6, v0
M 9,52,4
LC_ 116
v_and_b32 v3, 12, v3
v_bfe_u32 v3, s58, v3, 4
v_mad_u32_u24 v3, v3, s97, v21
M 9,53,5
DW 1024
XS 143
M 10,64,4
WL 3
LO 113,18,113
LU 18,113
M 10,65,5
RA 228,1,22528
HS 24,18
M 11,76,4
LL 17,70
LL 117,71
M 11,77,5
LA 17
XL 12,0
M 6,18,0
LC_ 117
DW 2048
M 6,19,1
XL 13,64
XS 144
M 7,30,0
WL 3
LO 116,16,116
LU 16,116
M 7,31,1
s_mul_i32 s53, s51, 24
RA 232,1,23552
HS 24,16
M 8,42,0
s_bitcmp1_b32 s51, 0
LL 18,72
LL 120,73
M 8,43,1
s_addc_u32 s53, s53, 0
LA 18
s_lshl_b32 s53, s53, 11
s_bitcmp1_b64 s[90:91], s51
s_cselect_b64 s[44:45], s[84:85], s[82:83]
s_add_u32 s44, s44, s53
s_addc_u32 s45, s45, 0
s_add_u32 s51, s51, %[d]
s_cmp_eq_u32 s51, %[wrap]
s_cselect_b32 s51, %[rst], s51
XS 145
M 9,54,0
s_add_u32 s40, s40, 3072
LC_ 120
s_addc_u32 s41, s41, 0
s_add_u32 m0, s38, 3072
M 9,55,1
XL 14,128
XS 146
M 10,66,0
WL 3
LO 117,17,117
LU 17,117
M 10,67,1
RA 236,1,24576
HS 24,17
M 11,78,0
LL 16,74
LL 121,75
XS 147
M 11,79,1
LA 16
DW 0
M 6,20,2
LC_ 121
XL 15,192
M 6,21,3
DW 1024
v_add_u32 v1, s37, v0
XS 148
M 7,32,2
WL 3
LO 120,18,120
LU 18,120
M 7,33,3
RA 216,1,0
HS 24,18
M 8,44,2
LL 17,76
LL 124,77
XS 149
M 8,45,3
LA 17
XL 16,256
M 9,56,2
LC_ 124
DW 2048
XS 150
M 9,57,3
XL 17,320
v_and_b32 v22, 0xfffffcff, v1
M 10,68,2
WL 3
LO 121,16,121
LU 16,121
M 10,69,3
s_lshl_b32 s53, s52, 8
RA 220,1,1024
HS 24,16
XS 151
M 11,80,2
LL 18,78
LL 125,79
M 11,81,3
s_add_u32 s48, %[alo], s53
LA 18
RV 252,22,5120
M 6,22,4
s_addc_u32 s49, %[ahi], 0
LC_ 125
XS 152
M 6,23,5
s_add_u32 s52, s52, %[d]
XS 153
XS 154
M 7,34,4
WL 4
LO 124,17,124
LU 17,124
M 7,35,5
s_cmp_eq_u32 s52, %[wrap]
RA 224,1,2048
HS 24,17
M 8,46,4
s_cselect_b32 s52, %[rst], s52
LL 16,80
LL 106,81
M 8,47,5
LA 16
DQ 3072
M 9,58,4
LC_ 106
XL 18,384
M 9,59,5
s_add_u32 s50, s50, %[d]
XS 155
XS 156
M 10,70,4
WL 3
LO 125,18,125
LU 18,125
M 10,71,5
RA 228,1,3072
HS 24,18
M 11,82,4
LL 17,82
LL 107,83
M 11,83,5
LA 17
HX 24
s_cmp_eq_u32 s50, %[wrap]
s_cselect_b32 s50, %[rst], s50
XL 19,448
Z 2,36,0
WV 39
LC_ 107
XL 20,512
v_add_u32 v19, 1, v19
s_add_u32 s38, s38, 32768
s_cmp_eq_u32 s38, s77
s_cselect_b32 s38, s76, s38
FP
s_sub_u32 s39, 0, s39
M 2,37,1
XL 21,576
XS 157
Z 3,48,0
WL 4
LO 16,16,106
LU 106,16
M 3,49,1
RA 232,1,4096
HZ 25,106
Z 4,60,0
LL 18,84
LL 110,85
XS 158
M 4,61,1
LA 18
PK 106,16,104,33
Z 5,72,0
WL 5
LC_ 110
XL 22,640
PK 104,16,104,32
M 5,73,1
RA 236,1,6144
XS 159
M 2,38,2
WL 5
LO 17,17,107
LU 107,17
M 2,39,3
FR
HS 25,107
M 3,50,2
LL 16,86
LL 111,87
XS 160
M 3,51,3
LA 16
PK 107,17,105,33
M 4,62,2
LC_ 111
XL 23,704
PK 105,17,105,32
XS 161
M 4,63,3
s_add_u32 s42, s44, s60
RA 216,1,7168
XS 162
M 5,74,2
WL 4
LO 18,18,110
LU 110,18
M 5,75,3
s_addc_u32 s43, s45, 0
RA 220,1,8192
HS 25,110
M 2,40,4
LL 17,88
LL 114,89
M 2,41,5
LA 17
PK 110,18,108,33
M 3,52,4
LC_ 114
PK 108,18,108,32
XS 163
M 3,53,5
RA 224,1,9216
PO 110
M 4,64,4
WL 5
LO 16,16,111
LU 111,16
ST 104
XS 164
M 4,65,5
s_add_u32 s42, s44, s61
HS 25,111
v_cmp_gt_u32 vcc, v19, v15
M 5,76,4
WL 3
LL 18,90
LL 115,91
M 5,77,5
s_addc_u32 s43, s45, 0
LA 18
PK 111,16,109,33
XS 165
M 2,42,0
s_cbranch_vccnz .Lk1s10
.Lk1r10:
LC_ 115
PK 109,16,109,32
M 2,43,1
WV 17
RA 228,1,10240
PO 111
BW 12
M 3,54,0
WL 5
LO 17,17,114
LU 114,17
XS 166
M 3,55,1
RA 232,1,11264
RV 40,8,6144
HS 25,114
M 4,66,0
RV 44,8,6208
LL 16,92
LL 118,93
M 4,67,1
LA 16
RV 48,8,6272
PK 114,17,112,33
XS 167
M 5,78,0
WL 6
LC_ 118
BR 12
PK 112,17,112,32
M 5,79,1
WV 15
RV 252,22,5392
ds_read_b64 v[14:15], v5 offset:0
W1 24,0
BW 13
XS 168
M 2,44,2
BR 13
LO 18,18,115
LU 115,18
M 2,45,3
WL 5
RA 236,1,12288
HS 25,115
LZ 40,12
WV 14
BW 14
M 3,56,2
LL 17,94
LL 119,95
XS 169
M 3,57,3
LA 17
RV 40,8,6336
PK 115,18,113,33
M 4,68,2
)"
R"(LC_ 119
BR 14
PK 113,18,113,32
M 4,69,3
WL 6
RA 216,1,13312
v_subrev_u32 v5, s39, v5
XS 170
LS 44,13
WV 12
BW 15
M 5,80,2
WL 7
LO 16,16,118
LU 118,16
ST 108
M 5,81,3
s_add_u32 s42, s44, s62
RA 220,1,14336
HS 25,118
s_addc_u32 s43, s45, 0
RV 44,8,6400
M 2,46,4
BR 15
LL 18,96
LL 122,97
XS 171
M 2,47,5
WL 5
LA 18
PK 118,16,116,33
LS 48,14
WV 11
BW 16
M 3,58,4
LC_ 122
PK 116,16,116,32
XS 172
M 3,59,5
RA 224,1,15360
RV 48,8,6464
PO 118
M 4,70,4
LO 17,17,119
LU 119,17
ST 112
M 4,71,5
s_add_u32 s42, s44, s63
BR 16
HS 25,119
XS 173
M 5,82,4
WL 6
LL 16,98
LL 123,99
M 5,83,5
s_addc_u32 s43, s45, 0
LA 16
PK 119,17,117,33
LS 40,15
WV 10
BW 17
Z 8,36,0
LC_ 123
PK 117,17,117,32
v_add_f32 v14, v14, v15
s_nop 1
v_add_f32_dpp v14, v14, v14 quad_perm:[1,0,3,2] row_mask:0xf bank_mask:0xf
s_nop 1
v_add_f32_dpp v14, v14, v14 quad_perm:[2,3,0,1] row_mask:0xf bank_mask:0xf
s_nop 1
v_add_f32_dpp v14, v14, v14 row_half_mirror row_mask:0xf bank_mask:0xf
v_cvt_f64_f32 v[14:15], v14
s_mov_b64 exec, s[54:55]
SP
s_mov_b64 exec, -1
RV 40,8,6528
XS 174
M 8,37,1
WV 9
RA 228,1,16384
BR 17
PO 119
Z 9,48,0
WL 6
LO 18,18,122
LU 122,18
XS 175
M 9,49,1
s_mul_i32 s53, %[d], 196608
RA 232,1,17408
HS 25,122
LS 44,16
s_add_u32 s53, s53, s57
BW 18
Z 10,60,0
s_add_u32 s46, %[xlo], s53
RV 44,8,6592
LL 17,100
LL 126,101
M 10,61,1
s_addc_u32 s47, %[xhi], 0
LA 17
BR 18
PK 122,18,120,33
Z 0,72,0
WL 5
LC_ 126
PK 120,18,120,32
M 0,73,1
s_bitcmp1_b64 s[92:93], 48
RA 236,1,18432
LS 48,17
s_cselect_b32 s58, 16, 0
WV 8
BW 19
M 8,38,2
RV 48,8,6656
LO 16,16,123
LU 123,16
M 8,39,3
s_lshr_b32 s58, 0xfe987610, s58
BR 19
HS 25,123
v_lshrrev_b32 v3, 6, v0
v_and_b32 v3, 12, v3
v_bfe_u32 v3, s58, v3, 4
v_mad_u32_u24 v3, v3, s97, v21
M 9,50,2
s_mul_i32 s53, s50, 25600
LL 18,102
LL 127,103
s_add_u32 s40, %[wlo], s53
s_addc_u32 s41, %[whi], 0
s_mov_b32 m0, s38
M 9,51,3
WL 5
LA 18
PK 123,16,121,33
LS 40,18
WV 7
BW 20
M 10,62,2
LC_ 127
RV 40,8,6720
PK 121,16,121,32
M 10,63,3
WV 6
RA 216,1,19456
BR 20
DW 0
M 0,74,2
WL 6
LO 17,17,126
LU 126,17
ST 116
M 0,75,3
s_add_u32 s42, s44, s64
RA 220,1,20480
HS 25,126
s_addc_u32 s43, s45, 0
LS 44,19
s_add_u32 s37, s37, 32768
BW 21
M 8,40,4
s_cmp_eq_u32 s37, s75
LL 16,64
LL 112,65
s_cselect_b32 s37, s74, s37
M 8,41,5
LA 16
XL 24,0
DW 1024
RV 44,8,6784
PK 126,17,124,33
M 9,52,4
LC_ 112
BR 21
PK 124,17,124,32
M 9,53,5
WL 6
RA 224,1,21504
PO 126
LS 48,20
WV 9
BW 22
M 10,64,4
LO 18,18,127
LU 127,18
ST 120
M 10,65,5
WV 9
DW 2048
RV 48,8,6848
HS 25,127
M 0,76,4
WL 8
RA 240,7,0
LL 17,66
LL 113,67
M 0,77,5
s_add_u32 s40, s40, 3072
LA 17
BR 22
PK 127,18,125,33
M 8,42,0
s_addc_u32 s41, s41, 0
LC_ 113
PK 125,18,125,32
s_add_u32 m0, s38, 3072
M 8,43,1
WL 7
RA 228,1,22528
XL 25,64
PO 127
LS 40,21
s_add_u32 s42, s44, s65
BW 23
M 9,54,0
RA 244,7,256
LO 112,16,112
LU 16,112
M 9,55,1
s_addc_u32 s43, s45, 0
RA 232,1,23552
BR 23
HZ 24,16
M 10,66,0
WL 6
DW 0
LL 18,68
LL 116,69
M 10,67,1
s_lshl_b32 s53, s52, 8
LA 18
HX 25
LS 44,22
s_add_u32 s48, %[alo], s53
RA 248,7,512
M 0,78,0
WL 2
LC_ 116
W1 25,1024
M 0,79,1
s_addc_u32 s49, %[ahi], 0
RA 236,1,24576
v_add_u32 v4, s39, v4
LS 48,23
s_add_u32 s52, s52, %[d]
RA 252,7,768
M 8,44,2
LO 113,17,113
LU 17,113
s_cmp_eq_u32 s52, %[wrap]
s_cselect_b32 s52, %[rst], s52
DW 1024
v_mul_f32 v36, 0xbb2aaaab, v36
M 8,45,3
XL 26,128
HS 24,17
v_mul_f32 v37, 0xbb2aaaab, v37
M 9,56,2
LL 16,70
LL 117,71
v_mul_f32 v38, 0xbb2aaaab, v38
v_mul_f32 v39, 0xbb2aaaab, v39
M 9,57,3
LA 16
DW 2048
LK 40,240,12,0
LK 44,242,12,2
RA 240,7,1024
v_add_u32 v1, s37, v0
LK 48,244,13,0
s_nop 0
CF 176,40,41
LK 248,246,13,2
RA 244,7,1280
CF 177,42,43
M 10,68,2
LC_ 117
RA 216,1,0
M 10,69,3
ST 124
v_and_b32 v22, 0xfffffcff, v1
M 0,80,2
WL 6
LO 116,18,116
LU 18,116
M 0,81,3
s_mul_i32 s53, s51, 24
RA 220,1,1024
HS 24,18
s_bitcmp1_b32 s51, 0
s_addc_u32 s53, s53, 0
s_lshl_b32 s53, s53, 11
s_bitcmp1_b64 s[90:91], s51
s_cselect_b64 s[44:45], s[84:85], s[82:83]
s_add_u32 s44, s44, s53
s_addc_u32 s45, s45, 0
s_add_u32 s51, s51, %[d]
s_cmp_eq_u32 s51, %[wrap]
s_cselect_b32 s51, %[rst], s51
DQ 3072
CF 178,44,45
M 8,46,4
LL 17,72
LL 120,73
CF 179,46,47
M 8,47,5
LA 17
RV 252,22,5120
LK 40,248,14,0
CF 180,48,49
LK 44,250,14,2
RA 248,7,1536
CF 181,50,51
GZ 12
WL 9
CF 182,248,249
CF 183,250,251
LK 48,252,15,0
CF 184,40,41
LK 248,254,15,2
RA 252,7,1792
CF 185,42,43
GS 13
WL 8
CF 186,44,45
CF 187,46,47
LK 40,240,16,0
CF 188,48,49
LK 44,242,16,2
RA 240,7,2048
CF 189,50,51
GS 14
WL 8
CF 190,248,249
CF 191,250,251
LK 48,244,17,0
CF 192,40,41
LK 248,246,17,2
RA 244,7,2304
CF 193,42,43
GS 15
CF 194,44,45
CF 195,46,47
M 9,58,4
WL 3
LC_ 120
XL 27,192
M 9,59,5
s_add_u32 s50, s50, %[d]
RA 224,1,2048
s_cmp_eq_u32 s50, %[wrap]
s_cselect_b32 s50, %[rst], s50
v_add_u32 v19, 1, v19
LK 40,248,18,0
WL 4
CF 196,48,49
LK 44,250,18,2
s_add_u32 s38, s38, 32768
RA 248,7,2560
CF 197,50,51
GS 16
s_cmp_eq_u32 s38, s77
CF 198,248,249
CF 199,250,251
LK 48,252,19,0
CF 200,40,41
LK 248,254,19,2
s_cselect_b32 s38, s76, s38
RA 252,7,2816
CF 201,42,43
GS 17
WL 5
CF 202,44,45
CF 203,46,47
LK 40,240,20,0
CF 204,48,49
LK 44,242,20,2
CF 205,50,51
GS 18
CF 206,248,249
CF 207,250,251
M 10,70,4
LO 117,16,117
LU 16,117
M 10,71,5
FP
HS 24,16
M 0,82,4
LL 18,74
LL 121,75
M 0,83,5
s_sub_u32 s39, 0, s39
LA 18
RA 228,1,3072
Z 2,36,0
LC_ 121
)"
R"(XL 28,256
M 2,37,1
WL 8
LK 48,244,21,0
CF 208,40,41
LK 248,246,21,2
CF 209,42,43
GS 19
CF 210,44,45
CF 211,46,47
Z 3,48,0
WL 7
LO 120,17,120
LU 17,120
M 3,49,1
WL 5
RA 232,1,4096
HS 24,17
LK 40,248,22,0
CF 212,48,49
LK 44,250,22,2
CF 213,50,51
GS 20
CF 214,248,249
CF 215,250,251
Z 4,60,0
LL 16,76
LL 124,77
M 4,61,1
LA 16
FR
Z 6,72,0
WL 4
LC_ 124
XL 29,320
M 6,73,1
RA 236,1,6144
LK 48,252,23,0
CF 216,40,41
LK 248,254,23,2
CF 217,42,43
GS 21
CF 218,44,45
CF 219,46,47
M 2,38,2
WL 5
LO 121,18,121
LU 18,121
M 2,39,3
RV 36,8,6912
HS 24,18
M 3,50,2
LL 17,78
LL 125,79
M 3,51,3
LA 17
RA 216,1,7168
GS 22
M 4,62,2
WL 5
LC_ 125
v_cmp_gt_u32 vcc, v19, v15
M 4,63,3
XL 30,384
CF 220,48,49
M 6,74,2
WL 4
LO 124,16,124
LU 16,124
M 6,75,3
RA 220,1,8192
HS 24,16
M 2,40,4
s_cbranch_vccnz .Lk1s11
.Lk1r11:
LL 18,88
LL 114,89
M 2,41,5
LA 18
ds_read_b64 v[14:15], v5 offset:0
M 3,52,4
LC_ 114
v_subrev_u32 v5, s39, v5
M 3,53,5
RA 224,1,9216
CF 221,50,51
M 4,64,4
WL 5
LO 125,17,125
LU 17,125
M 4,65,5
XL 31,448
HS 24,17
M 6,76,4
WL 4
LL 16,90
LL 115,91
M 6,77,5
LA 16
RA 228,1,10240
M 2,42,0
LC_ 115
HX 24
M 2,43,1
W1 24,0
CF 222,248,249
M 3,54,0
WL 5
LO 18,18,114
LU 114,18
M 3,55,1
RA 232,1,11264
HZ 25,114
M 4,66,0
LL 17,92
LL 118,93
M 4,67,1
LA 17
PK 114,18,112,33
M 6,78,0
WL 4
LC_ 118
PK 112,18,112,32
M 6,79,1
RV 252,22,5392
v_add_f32 v14, v14, v15
M 2,44,2
WL 5
LO 16,16,115
LU 115,16
M 2,45,3
RA 236,1,12288
HS 25,115
M 3,56,2
LL 18,94
LL 119,95
M 3,57,3
LA 18
PK 115,16,113,33
M 4,68,2
LC_ 119
XL 32,512
PK 113,16,113,32
v_add_f32_dpp v14, v14, v14 quad_perm:[1,0,3,2] row_mask:0xf bank_mask:0xf
M 4,69,3
s_add_u32 s42, s44, s62
RA 216,1,13312
v_add_f32_dpp v14, v14, v14 quad_perm:[2,3,0,1] row_mask:0xf bank_mask:0xf
M 6,80,2
WL 3
LO 17,17,118
LU 118,17
CF 223,250,251
M 6,81,3
s_addc_u32 s43, s45, 0
RA 220,1,14336
HS 25,118
GS 23
ST 112
v_add_f32_dpp v14, v14, v14 row_half_mirror row_mask:0xf bank_mask:0xf
s_nop 5
v_cndmask_b32 v52, v52, v53, s[78:79]
M 2,46,4
LL 16,96
LL 122,97
v_cndmask_b32 v54, v54, v55, s[78:79]
M 2,47,5
LA 16
PK 118,17,116,33
v_cndmask_b32 v52, v52, v54, s[80:81]
M 3,58,4
LC_ 122
PK 116,17,116,32
v_cvt_f64_f32 v[14:15], v14
v_mul_f32 v52, 0x3b2aaaab, v52
M 3,59,5
RA 224,1,15360
PO 118
v_med3_f32 v53, v52, v39, v37
M 4,70,4
WL 5
LO 18,18,119
LU 119,18
s_mov_b64 exec, s[54:55]
SP
s_mov_b64 exec, -1
XL 33,576
LG 52,53
s_nop 0
C 52,52,52
M 4,71,5
s_add_u32 s42, s44, s63
HS 25,119
v_lshlrev_b32 v52, 16, v52
M 6,82,4
WL 3
LL 17,98
LL 123,99
v_add_f32 v52, v36, v52
M 6,83,5
s_addc_u32 s43, s45, 0
LA 17
PK 119,18,117,33
C 52,52,52
Z 8,36,0
LC_ 123
PK 117,18,117,32
v_lshlrev_b32 v52, 16, v52
M 8,37,1
RA 228,1,16384
PO 119
v_sqrt_f32 v52, v52
s_nop 0
C 52,52,52
Z 9,48,0
WL 4
LO 16,16,122
LU 122,16
v_lshlrev_b32 v52, 16, v52
M 9,49,1
RA 232,1,17408
HS 25,122
v_rcp_f32 v52, v52
Z 10,60,0
XL 34,640
LL 18,56
LL 126,57
v_mul_f32 v52, v38, v52
M 10,61,1
LA 18
PK 122,16,120,33
CF 52,52,52
Z 11,72,0
WL 2
LC_ 126
PK 120,16,120,32
ds_bpermute_b32 v13, v28, v52
M 11,73,1
RA 236,1,18432
M 8,38,2
LO 17,17,123
LU 123,17
M 8,39,3
ST 116
HS 25,123
M 9,50,2
s_mul_i32 s53, s50, 25600
LL 16,58
LL 127,59
s_add_u32 s40, %[wlo], s53
s_addc_u32 s41, %[whi], 0
s_mov_b32 m0, s38
M 9,51,3
LA 16
PK 123,17,121,33
M 10,62,2
LC_ 127
PK 121,17,121,32
M 10,63,3
RA 216,1,19456
XL 35,704
DW 0
M 11,74,2
WL 3
LO 18,18,126
LU 126,18
M 11,75,3
s_add_u32 s42, s44, s64
RA 220,1,20480
HS 25,126
s_addc_u32 s43, s45, 0
XS 176
M 8,40,4
s_add_u32 s37, s37, 32768
LL 17,64
LL 112,65
s_cmp_eq_u32 s37, s75
s_cselect_b32 s37, s74, s37
M 8,41,5
LA 17
DW 1024
PK 126,18,124,33
XS 177
M 9,52,4
LC_ 112
PK 124,18,124,32
M 9,53,5
RA 224,1,21504
PO 126
XS 178
M 10,64,4
WL 5
LO 16,16,127
LU 127,16
ST 120
M 10,65,5
DW 2048
HS 25,127
XS 179
M 11,76,4
WL 3
LL 18,66
LL 113,67
M 11,77,5
s_add_u32 s40, s40, 3072
LA 18
PK 127,16,125,33
XS 180
M 8,42,0
s_addc_u32 s41, s41, 0
LC_ 113
PK 125,16,125,32
s_add_u32 m0, s38, 3072
M 8,43,1
s_add_u32 s42, s44, s65
RA 228,1,22528
PO 127
XS 181
M 9,54,0
WL 4
LO 112,17,112
LU 17,112
M 9,55,1
s_addc_u32 s43, s45, 0
RA 232,1,23552
HZ 24,17
XS 182
M 10,66,0
DW 0
LL 16,68
LL 116,69
M 10,67,1
s_lshl_b32 s53, s52, 8
LA 16
HX 25
XS 183
M 11,78,0
WL 2
LC_ 116
W1 25,1024
M 11,79,1
s_add_u32 s48, %[alo], s53
RA 236,1,24576
v_add_u32 v4, s39, v4
v_min_u32 v35, v35, v13
M 8,44,2
LO 113,18,113
LU 18,113
s_addc_u32 s49, %[ahi], 0
s_add_u32 s52, s52, %[d]
s_cmp_eq_u32 s52, %[wrap]
s_cselect_b32 s52, %[rst], s52
DW 1024
M 8,45,3
ST 124
HS 24,18
s_bitcmp1_b64 s[92:93], 48
s_cselect_b32 s58, 16, 0
s_lshr_b32 s58, 0xda43cb52, s58
v_and_b32 v11, 0x1f0, v0
v_min_u32 v11, 368, v11
v_lshlrev_b32 v11, 3, v11
XS 184
M 9,56,2
s_mul_i32 s53, s51, 24
LL 17,70
LL 117,71
s_bitcmp1_b32 s51, 0
s_addc_u32 s53, s53, 0
s_lshl_b32 s53, s53, 11
s_bitcmp1_b64 s[90:91], s51
s_cselect_b64 s[44:45], s[84:85], s[82:83]
s_add_u32 s44, s44, s53
s_addc_u32 s45, s45, 0
s_add_u32 s51, s51, %[d]
s_cmp_eq_u32 s51, %[wrap]
s_cselect_b32 s51, %[rst], s51
M 9,57,3
LA 17
DW 2048
XS 185
M 10,68,2
LC_ 117
DQ 3072
M 10,69,3
s_add_u32 s50, s50, %[d]
v_add_u32 v1, s37, v0
XS 186
M 11,80,2
WL 2
LO 116,16,116
LU 16,116
M 11,81,3
s_cmp_eq_u32 s50, %[wrap]
RA 216,1,0
HS 24,16
XS 187
M 8,46,4
s_cselect_b32 s50, %[rst], s50
LL 18,72
)"
R"(LL 120,73
M 8,47,5
LA 18
v_and_b32 v22, 0xfffffcff, v1
XS 188
M 9,58,4
LC_ 120
RA 220,1,1024
M 9,59,5
RV 252,22,5120
v_add_u32 v19, 1, v19
XS 189
M 10,70,4
WL 5
LO 117,17,117
LU 17,117
M 10,71,5
s_add_u32 s38, s38, 32768
RA 224,1,2048
HS 24,17
XS 190
M 11,82,4
WL 1
LL 16,74
LL 121,75
XS 191
M 11,83,5
s_cmp_eq_u32 s38, s77
LA 16
RA 228,1,3072
s_cselect_b32 s38, s76, s38
FP
s_sub_u32 s39, 0, s39
Z 0,0,0
LC_ 121
XS 192
M 0,1,1
XS 193
XS 194
Z 1,12,0
LO 120,18,120
LU 18,120
M 1,13,1
RA 232,1,4096
HS 24,18
Z 2,36,0
LL 17,80
LL 124,81
M 2,37,1
LA 17
XS 195
Z 3,48,0
LC_ 124
XS 196
M 3,49,1
XS 197
XS 198
Z 4,60,0
WL 3
LO 121,16,121
LU 16,121
M 4,61,1
RA 236,1,6144
HS 24,16
Z 5,72,0
LL 18,82
LL 125,83
M 5,73,1
LA 18
XS 199
M 0,2,2
LC_ 125
XS 200
M 0,3,3
XS 201
XS 202
M 1,14,2
WL 3
LO 124,17,124
LU 17,124
M 1,15,3
RA 216,1,7168
HS 24,17
M 2,38,2
LL 16,88
LL 114,89
M 2,39,3
LA 16
FR
M 3,50,2
LC_ 114
v_lshrrev_b32 v3, 9, v0
M 3,51,3
WV 31
BW 24
M 4,62,2
WL 5
LO 125,18,125
LU 18,125
M 4,63,3
RA 220,1,8192
HS 24,18
M 5,74,2
LL 17,90
LL 115,91
XS 203
M 5,75,3
LA 17
RV 40,8,6144
M 0,4,4
LC_ 115
RV 44,8,6208
XS 204
M 0,5,5
RV 48,8,6272
BR 24
M 1,16,4
WL 8
LO 16,16,114
LU 114,16
XS 205
M 1,17,5
RA 224,1,9216
HZ 25,114
M 2,40,4
LL 18,92
LL 118,93
XS 206
M 2,41,5
LA 18
PK 114,16,112,33
XS 207
M 3,52,4
LC_ 118
PK 112,16,112,32
v_lshl_add_u32 v3, v3, 2, 0
v_bfe_u32 v3, s58, v3, 4
v_mad_u32_u24 v3, v3, s97, v11
M 3,53,5
s_mov_b32 m0, %[pm]
s_nop 0
DF
v_cmp_gt_u32 vcc, v19, v15
XS 208
M 4,64,4
WL 6
LO 17,17,115
LU 115,17
M 4,65,5
s_add_u32 s42, s44, s62
RA 228,1,10240
HS 25,115
XS 209
M 5,76,4
LL 16,94
LL 119,95
M 5,77,5
s_cbranch_vccnz .Lk1s12
.Lk1r12:
LA 16
PK 115,17,113,33
XS 210
M 0,6,0
s_addc_u32 s43, s45, 0
LC_ 119
PK 113,17,113,32
M 0,7,1
ST 112
HX 24
XS 211
M 1,18,0
WL 3
LO 18,18,118
LU 118,18
M 1,19,1
RA 232,1,11264
HS 25,118
XS 212
M 2,42,0
LL 17,96
LL 122,97
M 2,43,1
LA 17
PK 118,18,116,33
XS 213
M 3,54,0
LC_ 122
PK 116,18,116,32
M 3,55,1
RV 252,22,5392
PO 118
XS 214
M 4,66,0
WL 4
LO 16,16,119
LU 119,16
M 4,67,1
s_add_u32 s42, s44, s63
RA 236,1,12288
HS 25,119
XS 215
M 5,78,0
LL 18,98
LL 123,99
M 5,79,1
s_addc_u32 s43, s45, 0
LA 18
PK 119,16,117,33
XS 216
M 0,8,2
LC_ 123
PK 117,16,117,32
M 0,9,3
W1 24,0
PO 119
XS 217
M 1,20,2
WL 5
LO 17,17,122
LU 122,17
M 1,21,3
RA 216,1,13312
HS 25,122
XS 218
M 2,44,2
LL 16,100
LL 126,101
M 2,45,3
LA 16
PK 122,17,120,33
XS 219
M 3,56,2
LC_ 126
PK 120,17,120,32
M 3,57,3
WV 29
ST 116
XS 220
BW 25
M 4,68,2
WL 5
LO 18,18,123
LU 123,18
M 4,69,3
RA 220,1,14336
BR 25
HS 25,123
XS 221
LZ 40,24
WV 27
BW 26
M 5,80,2
LL 17,102
LL 127,103
M 5,81,3
LA 17
PK 123,18,121,33
XS 222
M 0,10,4
LC_ 127
PK 121,18,121,32
XS 223
M 0,11,5
WV 23
DY
RV 40,8,6336
M 1,22,4
WL 4
BR 26
LO 16,16,126
LU 126,16
M 1,23,5
s_add_u32 s42, s44, s64
RA 224,1,15360
HS 25,126
LS 44,25
s_addc_u32 s43, s45, 0
BW 27
M 2,46,4
LL 18,56
LL 104,57
M 2,47,5
LA 18
PK 126,16,124,33
M 3,58,4
LC_ 104
RV 44,8,6400
PK 124,16,124,32
M 3,59,5
WV 23
ST 120
BR 27
PO 126
M 4,70,4
WL 6
LO 17,17,127
LU 127,17
M 4,71,5
s_add_u32 s42, s44, s65
RA 228,1,16384
HS 25,127
LS 48,26
s_addc_u32 s43, s45, 0
BW 28
M 5,82,4
LL 16,58
LL 105,59
M 5,83,5
LA 16
RV 48,8,6464
PK 127,17,125,33
Z 6,0,0
LC_ 105
PK 125,17,125,32
M 6,1,1
ds_read_b64 v[14:15], v5 offset:0
BR 28
PO 127
Z 7,12,0
WL 9
LO 104,18,104
LU 18,104
M 7,13,1
WL 7
RA 232,1,17408
HZ 26,18
LS 40,27
WV 23
BW 29
Z 8,36,0
WV 22
LL 17,60
LL 108,61
M 8,37,1
WL 2
LA 17
ST 124
RV 40,8,6528
Z 9,48,0
s_mul_i32 s53, s50, 25600
LC_ 108
BR 29
M 9,49,1
s_add_u32 s40, %[wlo], s53
v_subrev_u32 v5, s39, v5
HX 25
LS 44,28
s_addc_u32 s41, %[whi], 0
BW 30
Z 10,60,0
LO 105,16,105
LU 16,105
M 10,61,1
s_mov_b32 m0, s38
RA 236,1,18432
HS 26,16
Z 11,72,0
RV 44,8,6592
LL 18,62
LL 109,63
M 11,73,1
LA 18
W1 25,1024
BR 30
M 6,2,2
LC_ 109
DW 0
M 6,3,3
WL 7
v_add_f32 v14, v14, v15
v_add_u32 v4, s39, v4
LS 48,29
WV 23
BW 31
M 7,14,2
LO 108,17,108
LU 17,108
M 7,15,3
s_add_u32 s37, s37, 32768
RA 216,1,19456
HS 26,17
M 8,38,2
s_cmp_eq_u32 s37, s75
RV 48,8,6656
LL 16,64
LL 112,65
M 8,39,3
s_cselect_b32 s37, s74, s37
LA 16
DW 1024
BR 31
M 9,50,2
LC_ 112
v_add_f32_dpp v14, v14, v14 quad_perm:[1,0,3,2] row_mask:0xf bank_mask:0xf
M 9,51,3
WL 7
v_add_f32_dpp v14, v14, v14 quad_perm:[2,3,0,1] row_mask:0xf bank_mask:0xf
v_lshrrev_b32 v3, 9, v0
LS 40,30
WV 23
)"
R"(BW 32
M 10,62,2
WL 7
LO 109,18,109
LU 18,109
M 10,63,3
RA 220,1,20480
RV 40,8,6720
HS 26,18
M 11,74,2
BR 32
LL 17,66
LL 113,67
M 11,75,3
LA 17
v_add_f32_dpp v14, v14, v14 row_half_mirror row_mask:0xf bank_mask:0xf
M 6,4,4
WL 6
LC_ 113
v_cvt_f64_f32 v[14:15], v14
M 6,5,5
s_mov_b64 exec, s[54:55]
SP
s_mov_b64 exec, -1
HX 26
LS 44,31
WV 21
BW 33
M 7,16,4
WL 7
RV 44,8,6784
LO 112,16,112
LU 16,112
M 7,17,5
RA 224,1,21504
HZ 24,16
M 8,40,4
BR 33
LL 18,68
LL 116,69
M 8,41,5
WL 6
LA 18
v_lshl_add_u32 v3, v3, 2, 8
LS 48,32
WV 20
BW 34
M 9,52,4
LC_ 116
RV 48,8,6848
M 9,53,5
RA 240,7,0
BR 34
M 10,64,4
WL 10
LO 113,17,113
LU 17,113
M 10,65,5
RA 228,1,22528
HS 24,17
v_bfe_u32 v3, s58, v3, 4
v_mad_u32_u24 v3, v3, s97, v11
M 11,76,4
WV 18
LL 16,70
LL 117,71
M 11,77,5
WL 7
LA 16
DW 2048
M 6,6,0
s_mul_i32 s53, s51, 24
LC_ 117
M 6,7,1
s_bitcmp1_b32 s51, 0
LS 40,33
s_addc_u32 s53, s53, 0
BW 35
M 7,18,0
WL 4
RA 244,7,256
LO 116,18,116
LU 18,116
M 7,19,1
s_lshl_b32 s53, s53, 11
RA 232,1,23552
HS 24,18
M 8,42,0
s_bitcmp1_b64 s[90:91], s51
BR 35
LL 17,72
LL 120,73
M 8,43,1
s_cselect_b64 s[44:45], s[84:85], s[82:83]
LA 17
s_add_u32 s44, s44, s53
s_addc_u32 s45, s45, 0
s_bitcmp1_b64 s[92:93], s51
s_cselect_b64 s[94:95], s[88:89], s[86:87]
s_add_u32 s94, s94, s53
s_addc_u32 s95, s95, 0
s_add_u32 s51, s51, %[d]
s_cmp_eq_u32 s51, %[wrap]
s_cselect_b32 s51, %[rst], s51
s_mul_i32 s53, %[d], 0
s_add_u32 s60, s53, %[sb1]
s_add_u32 s61, s60, 512
LS 44,34
s_add_u32 s40, s40, 3072
RA 248,7,512
M 9,54,0
s_addc_u32 s41, s41, 0
LC_ 120
s_add_u32 m0, s38, 3072
M 9,55,1
DW 0
M 10,66,0
WL 7
LO 117,16,117
LU 16,117
M 10,67,1
WL 3
RA 236,1,24576
HS 24,16
LS 48,35
RA 252,7,768
M 11,78,0
LL 18,74
LL 121,75
s_nop 3
v_mul_f32 v36, 0xbb2aaaab, v36
M 11,79,1
LA 18
DW 1024
v_mul_f32 v37, 0xbb2aaaab, v37
v_mul_f32 v38, 0xbb2aaaab, v38
M 6,8,2
LC_ 121
DW 2048
v_mul_f32 v39, 0xbb2aaaab, v39
M 6,9,3
s_lshl_b32 s53, s52, 8
v_add_u32 v1, s37, v0
LK 40,240,24,0
LK 44,242,24,2
s_add_u32 s48, %[alo], s53
RA 240,7,1024
LK 48,244,25,0
s_nop 0
CF 224,40,41
LK 248,246,25,2
s_addc_u32 s49, %[ahi], 0
RA 244,7,1280
CF 225,42,43
M 7,20,2
WL 6
LO 120,17,120
LU 17,120
M 7,21,3
s_add_u32 s52, s52, %[d]
RA 216,1,0
HS 24,17
M 8,44,2
s_cmp_eq_u32 s52, %[wrap]
LL 16,76
LL 124,77
CF 226,44,45
M 8,45,3
s_cselect_b32 s52, %[rst], s52
LA 16
DQ 3072
CF 227,46,47
LK 40,248,26,0
CF 228,48,49
LK 44,250,26,2
RA 248,7,1536
CF 229,50,51
GZ 24
WL 7
CF 230,248,249
CF 231,250,251
LK 48,252,27,0
CF 232,40,41
LK 248,254,27,2
RA 252,7,1792
CF 233,42,43
GS 25
WL 5
CF 234,44,45
CF 235,46,47
LK 40,240,28,0
CF 236,48,49
LK 44,242,28,2
RA 240,7,2048
CF 237,50,51
GS 26
WL 5
CF 238,248,249
CF 239,250,251
LK 48,244,29,0
CF 240,40,41
LK 248,246,29,2
RA 244,7,2304
CF 241,42,43
GS 27
CF 242,44,45
CF 243,46,47
M 9,56,2
LC_ 124
v_and_b32 v22, 0xfffffcff, v1
M 9,57,3
WL 4
LK 40,248,30,0
CF 244,48,49
LK 44,250,30,2
RA 248,7,2560
CF 245,50,51
GS 28
WL 4
CF 246,248,249
CF 247,250,251
LK 48,252,31,0
CF 40,40,41
LK 248,254,31,2
RA 252,7,2816
CF 42,42,43
GS 29
CF 44,44,45
CF 46,46,47
M 10,68,2
LO 121,18,121
LU 18,121
M 10,69,3
RA 220,1,1024
HS 24,18
M 11,80,2
LL 17,78
LL 125,79
M 11,81,3
LA 17
RV 252,22,5120
M 6,10,4
LC_ 125
DY
M 6,11,5
s_add_u32 s50, s50, %[d]
AW 0,40
AW 1,42
M 7,22,4
WL 6
LO 124,16,124
LU 16,124
M 7,23,5
s_cmp_eq_u32 s50, %[wrap]
RA 224,1,2048
HS 24,16
M 8,46,4
s_cselect_b32 s50, %[rst], s50
LL 18,80
LL 106,81
M 8,47,5
LA 18
v_add_u32 v19, 1, v19
M 9,58,4
s_add_u32 s38, s38, 32768
LC_ 106
AW 2,44
M 9,59,5
AW 3,46
LK 40,240,32,0
CF 48,48,49
LK 44,242,32,2
CF 50,50,51
GS 30
CF 248,248,249
CF 250,250,251
M 10,70,4
WL 3
LO 125,17,125
LU 17,125
M 10,71,5
RA 228,1,3072
HS 24,17
M 11,82,4
LL 16,82
LL 107,83
M 11,83,5
LA 16
HX 24
Z 0,0,0
s_cmp_eq_u32 s38, s77
LC_ 107
AW 4,48
M 0,1,1
s_cselect_b32 s38, s76, s38
FP
AW 5,50
Z 1,12,0
WL 4
LO 18,18,106
LU 106,18
M 1,13,1
s_sub_u32 s39, 0, s39
RA 232,1,4096
HZ 27,106
Z 2,36,0
LL 17,84
LL 110,85
M 2,37,1
LA 17
PK 106,18,104,33
Z 3,48,0
LC_ 110
PK 104,18,104,32
M 3,49,1
AW 6,248
AW 7,250
LK 48,244,33,0
CF 40,40,41
LK 248,246,33,2
CF 42,42,43
GS 31
CF 44,44,45
CF 46,46,47
Z 4,60,0
WL 4
LO 16,16,107
LU 107,16
M 4,61,1
s_add_u32 s42, s94, s60
RA 236,1,6144
HS 27,107
Z 5,72,0
LL 18,86
LL 111,87
M 5,73,1
s_addc_u32 s43, s95, 0
LA 18
PK 107,16,105,33
M 0,2,2
LC_ 111
PK 105,16,105,32
M 0,3,3
SU 104
AW 8,40
M 1,14,2
WL 3
LO 17,17,110
LU 110,17
M 1,15,3
RA 216,1,7168
HS 27,110
M 2,38,2
LL 16,88
LL 114,89
M 2,39,3
LA 16
PK 110,17,108,33
AW 9,42
AW 10,44
M 3,50,2
LC_ 114
PK 108,17,108,32
AW 11,46
M 3,51,3
FR
PO 110
LK 40,248,34,0
CF 48,48,49
LK 44,250,34,2
CF 50,50,51
GS 32
CF 248,248,249
CF 250,250,251
M 4,62,2
WL 4
LO 18,18,111
LU 111,18
M 4,63,3
s_add_u32 s42, s94, s61
RA 220,1,8192
HS 27,111
M 5,74,2
LL 17,90
LL 115,91
AW 12,48
AW 13,50
M 5,75,3
s_addc_u32 s43, s95, 0
LA 17
PK 111,18,109,33
AW 14,248
AW 15,250
LK 48,252,35,0
CF 40,40,41
LK 248,254,35,2
s_mov_b32 m0, %[pm]
CF 42,42,43
HX 27
GS 33
)"
R"(CF 44,44,45
CF 46,46,47
M 0,4,4
LC_ 115
PK 109,18,109,32
M 0,5,5
DF
PO 111
M 1,16,4
WL 4
LO 16,16,114
LU 114,16
M 1,17,5
RA 224,1,9216
HZ 25,114
AW 16,40
M 2,40,4
LL 18,92
LL 118,93
AW 17,42
AW 18,44
M 2,41,5
LA 18
RV 36,8,6912
PK 114,16,112,33
AW 19,46
M 3,52,4
LC_ 118
PK 112,16,112,32
M 3,53,5
WL 7
SU 108
v_cmp_gt_u32 vcc, v19, v15
GS 34
CF 48,48,49
CF 50,50,51
CF 248,248,249
M 4,64,4
WL 4
LO 17,17,115
LU 115,17
CF 250,250,251
AW 20,48
M 4,65,5
s_add_u32 s42, s44, s62
RA 228,1,10240
HS 25,115
AW 21,50
AW 22,248
M 5,76,4
LL 16,94
LL 119,95
AW 23,250
M 5,77,5
s_cbranch_vccnz .Lk1s13
.Lk1r13:
LA 16
PK 115,17,113,33
GS 35
s_addc_u32 s43, s45, 0
W1 24,0
s_nop 5
v_cndmask_b32 v52, v52, v53, s[78:79]
M 0,6,0
LC_ 119
PK 113,17,113,32
v_cndmask_b32 v54, v54, v55, s[78:79]
M 0,7,1
W1 26,2048
v_cndmask_b32 v52, v52, v54, s[80:81]
M 1,18,0
WL 5
LO 18,18,118
LU 118,18
v_mul_f32 v52, 0x3b2aaaab, v52
M 1,19,1
RA 232,1,11264
HS 25,118
v_med3_f32 v53, v52, v39, v37
M 2,42,0
LL 17,96
LL 122,97
LG 52,53
M 2,43,1
LA 17
PK 118,18,116,33
C 52,52,52
M 3,54,0
LC_ 122
PK 116,18,116,32
v_lshlrev_b32 v52, 16, v52
M 3,55,1
RV 252,22,5392
PO 118
v_add_f32 v52, v36, v52
M 4,66,0
WL 5
LO 16,16,119
LU 119,16
C 52,52,52
M 4,67,1
RA 236,1,12288
HS 25,119
M 5,78,0
LL 18,98
LL 123,99
v_lshlrev_b32 v52, 16, v52
M 5,79,1
LA 18
PK 119,16,117,33
v_sqrt_f32 v52, v52
M 0,8,2
LC_ 123
PK 117,16,117,32
C 52,52,52
M 0,9,3
ST 112
PO 119
v_lshlrev_b32 v52, 16, v52
M 1,20,2
WL 4
LO 17,17,122
LU 122,17
v_rcp_f32 v52, v52
M 1,21,3
s_add_u32 s42, s44, s63
RA 216,1,13312
HS 25,122
v_mul_f32 v52, v38, v52
M 2,44,2
s_addc_u32 s43, s45, 0
LL 16,100
LL 126,101
CF 52,52,52
M 2,45,3
LA 16
PK 122,17,120,33
ds_bpermute_b32 v13, v28, v52
M 3,56,2
LC_ 126
PK 120,17,120,32
M 3,57,3
ST 116
AR 248,0
M 4,68,2
WL 4
LO 18,18,123
LU 123,18
M 4,69,3
s_add_u32 s42, s44, s64
RA 220,1,14336
HS 25,123
M 5,80,2
LL 17,102
LL 127,103
M 5,81,3
s_addc_u32 s43, s45, 0
LA 17
PK 123,18,121,33
M 0,10,4
LC_ 127
PK 121,18,121,32
M 0,11,5
ST 120
AR 249,1
M 1,22,4
WL 3
LO 16,16,126
LU 126,16
M 1,23,5
RA 224,1,15360
HS 25,126
M 2,46,4
LL 18,56
LL 104,57
M 2,47,5
LA 18
PK 126,16,124,33
M 3,58,4
LC_ 104
PK 124,16,124,32
AR 250,2
M 3,59,5
ds_read_b64 v[14:15], v5 offset:0
PO 126
M 4,70,4
WL 4
LO 17,17,127
LU 127,17
AR 251,3
M 4,71,5
s_add_u32 s42, s44, s65
RA 228,1,16384
HS 25,127
AR 52,4
M 5,82,4
LL 16,58
LL 105,59
AR 53,5
M 5,83,5
s_addc_u32 s43, s45, 0
LA 16
PK 127,17,125,33
Z 6,0,0
LC_ 105
PK 125,17,125,32
AR 54,6
M 6,1,1
PO 127
v_subrev_u32 v5, s39, v5
AR 55,7
Z 7,12,0
WL 4
LO 104,18,104
LU 18,104
XS 224
M 7,13,1
RA 232,1,17408
HZ 26,18
Z 8,36,0
WL 4
LL 17,60
LL 108,61
XS 225
M 8,37,1
s_mul_i32 s53, s50, 25600
LA 17
v_add_f32 v14, v14, v15
XS 226
Z 9,48,0
s_add_u32 s40, %[wlo], s53
LC_ 108
v_add_f32_dpp v14, v14, v14 quad_perm:[1,0,3,2] row_mask:0xf bank_mask:0xf
M 9,49,1
s_addc_u32 s41, %[whi], 0
v_add_f32_dpp v14, v14, v14 quad_perm:[2,3,0,1] row_mask:0xf bank_mask:0xf
HX 25
XS 227
Z 10,60,0
WL 3
LO 105,16,105
LU 16,105
XS 228
M 10,61,1
s_mov_b32 m0, s38
RA 236,1,18432
HS 26,16
XS 229
Z 11,72,0
LL 18,62
LL 109,63
M 11,73,1
LA 18
W1 25,1024
XS 230
M 6,2,2
LC_ 109
W1 27,3072
XS 231
M 6,3,3
ST 124
v_add_f32_dpp v14, v14, v14 row_half_mirror row_mask:0xf bank_mask:0xf
v_min_u32 v35, v35, v13
M 7,14,2
WL 5
LO 108,17,108
LU 17,108
M 7,15,3
RA 216,1,19456
HS 26,17
XS 232
M 8,38,2
LL 16,64
LL 112,65
XS 233
M 8,39,3
LA 16
DW 0
XS 234
M 9,50,2
s_add_u32 s37, s37, 32768
LC_ 112
v_cvt_f64_f32 v[14:15], v14
M 9,51,3
s_cmp_eq_u32 s37, s75
s_mov_b64 exec, s[54:55]
SP
s_mov_b64 exec, -1
v_add_u32 v4, s39, v4
XS 235
M 10,62,2
WL 4
LO 109,18,109
LU 18,109
XS 236
M 10,63,3
s_cselect_b32 s37, s74, s37
RA 220,1,20480
HS 26,18
M 11,74,2
LL 17,66
LL 113,67
XS 237
M 11,75,3
s_mul_i32 s53, %[d], 196608
LA 17
HX 26
XS 238
M 6,4,4
s_add_u32 s53, s53, s57
LC_ 113
XS 239
M 6,5,5
s_add_u32 s46, %[xlo], s53
XS 240
XS 241
M 7,16,4
WL 3
LO 112,16,112
LU 16,112
M 7,17,5
s_addc_u32 s47, %[xhi], 0
RA 224,1,21504
HZ 24,16
M 8,40,4
s_bitcmp1_b64 s[92:93], 48
LL 18,68
LL 116,69
XS 242
M 8,41,5
s_cselect_b32 s58, 16, 0
LA 18
XS 243
M 9,52,4
s_lshr_b32 s58, 0xda43cb52, s58
LC_ 116
v_lshrrev_b32 v3, 6, v0
v_and_b32 v3, 12, v3
v_bfe_u32 v3, s58, v3, 4
v_mad_u32_u24 v3, v3, s97, v21
M 9,53,5
DW 1024
XS 244
M 10,64,4
WL 3
LO 113,17,113
LU 17,113
XS 245
M 10,65,5
RA 228,1,22528
HS 24,17
M 11,76,4
LL 16,70
LL 117,71
XS 246
M 11,77,5
LA 16
XL 36,0
XS 247
M 6,6,0
LC_ 117
DW 2048
M 6,7,1
XL 37,64
XS 248
M 7,18,0
WL 3
LO 116,18,116
LU 18,116
XS 249
M 7,19,1
s_mul_i32 s53, s51, 24
RA 232,1,23552
HS 24,18
XS 250
M 8,42,0
s_bitcmp1_b32 s51, 0
LL 17,72
LL 120,73
M 8,43,1
s_addc_u32 s53, s53, 0
LA 17
s_lshl_b32 s53, s53, 11
s_bitcmp1_b64 s[90:91], s51
s_cselect_b64 s[44:45], s[84:85], s[82:83]
s_add_u32 s44, s44, s53
s_addc_u32 s45, s45, 0
s_bitcmp1_b64 s[92:93], s51
s_cselect_b64 s[94:95], s[88:89], s[86:87]
s_add_u32 s94, s94, s53
s_addc_u32 s95, s95, 0
s_add_u32 s51, s51, %[d]
s_cmp_eq_u32 s51, %[wrap]
s_cselect_b32 s51, %[rst], s51
XS 251
M 9,54,0
s_add_u32 s40, s40, 3072
LC_ 120
s_addc_u32 s41, s41, 0
s_add_u32 m0, s38, 3072
)"
R"(AW 0,248
M 9,55,1
XL 38,128
AW 1,249
M 10,66,0
WL 3
LO 117,16,117
LU 16,117
M 10,67,1
RA 236,1,24576
HS 24,16
AW 2,250
M 11,78,0
LL 18,74
LL 121,75
AW 3,251
M 11,79,1
LA 18
DW 0
XS 52
M 6,8,2
LC_ 121
XL 39,192
M 6,9,3
DW 1024
v_add_u32 v1, s37, v0
XS 53
M 7,20,2
WL 3
LO 120,17,120
LU 17,120
XS 54
M 7,21,3
RA 216,1,0
HS 24,17
M 8,44,2
LL 16,76
LL 124,77
XS 55
M 8,45,3
LA 16
XL 40,256
AW 4,52
M 9,56,2
LC_ 124
DW 2048
AW 5,53
M 9,57,3
XL 41,320
v_and_b32 v22, 0xfffffcff, v1
M 10,68,2
WL 3
LO 121,18,121
LU 18,121
AW 6,54
M 10,69,3
s_lshl_b32 s53, s52, 8
RA 220,1,1024
HS 24,18
AW 7,55
M 11,80,2
LL 17,78
LL 125,79
AR 248,8
M 11,81,3
s_add_u32 s48, %[alo], s53
LA 17
RV 252,22,5120
M 6,10,4
s_addc_u32 s49, %[ahi], 0
LC_ 125
AR 249,9
M 6,11,5
s_add_u32 s52, s52, %[d]
AR 250,10
AR 251,11
M 7,22,4
WL 4
LO 124,16,124
LU 16,124
M 7,23,5
s_cmp_eq_u32 s52, %[wrap]
RA 224,1,2048
HS 24,16
M 8,46,4
s_cselect_b32 s52, %[rst], s52
LL 18,80
LL 106,81
XS 248
M 8,47,5
LA 18
DQ 3072
XS 249
M 9,58,4
LC_ 106
XL 42,384
M 9,59,5
s_add_u32 s50, s50, %[d]
XS 250
XS 251
M 10,70,4
WL 3
LO 125,17,125
LU 17,125
M 10,71,5
RA 228,1,3072
HS 24,17
AW 8,248
M 11,82,4
LL 16,82
LL 107,83
M 11,83,5
LA 16
HX 24
s_cmp_eq_u32 s50, %[wrap]
s_cselect_b32 s50, %[rst], s50
XL 43,448
AW 9,249
Z 0,0,0
WV 38
LC_ 107
XL 44,512
v_add_u32 v19, 1, v19
s_add_u32 s38, s38, 32768
s_cmp_eq_u32 s38, s77
s_cselect_b32 s38, s76, s38
FP
s_sub_u32 s39, 0, s39
AW 10,250
M 0,1,1
XL 45,576
AW 11,251
Z 1,12,0
WL 4
LO 18,18,106
LU 106,18
M 1,13,1
RA 232,1,4096
HZ 27,106
AR 52,12
Z 4,60,0
LL 17,84
LL 110,85
AR 53,13
M 4,61,1
LA 17
PK 106,18,104,33
AR 54,14
Z 5,72,0
WL 5
LC_ 110
XL 46,640
PK 104,18,104,32
M 5,73,1
RA 236,1,6144
AR 55,15
M 0,2,2
WL 5
LO 16,16,107
LU 107,16
XS 52
M 0,3,3
FR
HS 27,107
M 1,14,2
LL 18,86
LL 111,87
XS 53
M 1,15,3
LA 18
PK 107,16,105,33
XS 54
M 4,62,2
LC_ 111
XL 47,704
PK 105,16,105,32
XS 55
M 4,63,3
s_add_u32 s42, s94, s60
RA 216,1,7168
AW 12,52
M 5,74,2
WL 4
LO 17,17,110
LU 110,17
M 5,75,3
s_addc_u32 s43, s95, 0
RA 220,1,8192
HS 27,110
AW 13,53
M 0,4,4
LL 16,88
LL 114,89
AW 14,54
M 0,5,5
LA 16
PK 110,17,108,33
M 1,16,4
LC_ 114
PK 108,17,108,32
AW 15,55
M 1,17,5
RA 224,1,9216
PO 110
AR 248,16
M 4,64,4
WL 5
LO 18,18,111
LU 111,18
SU 104
AR 249,17
M 4,65,5
s_add_u32 s42, s94, s61
HS 27,111
v_cmp_gt_u32 vcc, v19, v15
M 5,76,4
WL 3
LL 17,90
LL 115,91
AR 250,18
M 5,77,5
s_addc_u32 s43, s95, 0
LA 17
PK 111,18,109,33
AR 251,19
M 0,6,0
s_cbranch_vccnz .Lk1s14
.Lk1r14:
LC_ 115
PK 109,18,109,32
M 0,7,1
WV 17
RA 228,1,10240
PO 111
XS 248
BW 36
M 1,18,0
WL 5
LO 16,16,114
LU 114,16
XS 249
M 1,19,1
RA 232,1,11264
RV 40,8,6144
HZ 25,114
XS 250
M 4,66,0
RV 44,8,6208
LL 18,92
LL 118,93
M 4,67,1
LA 18
RV 48,8,6272
PK 114,16,112,33
XS 251
M 5,78,0
WL 6
LC_ 118
BR 36
PK 112,16,112,32
AW 16,248
M 5,79,1
WV 15
RV 252,22,5392
ds_read_b64 v[14:15], v5 offset:0
W1 24,0
W1 26,2048
HX 27
BW 37
AW 17,249
M 0,8,2
BR 37
LO 17,17,115
LU 115,17
M 0,9,3
WL 6
RA 236,1,12288
HS 25,115
AW 18,250
LZ 40,36
WV 14
BW 38
M 1,20,2
LL 16,94
LL 119,95
AW 19,251
M 1,21,3
LA 16
RV 40,8,6336
PK 115,17,113,33
AR 52,20
M 4,68,2
LC_ 119
BR 38
PK 113,17,113,32
M 4,69,3
WL 6
RA 216,1,13312
AR 53,21
LS 44,37
WV 12
BW 39
M 5,80,2
WL 7
LO 18,18,118
LU 118,18
SU 108
AR 54,22
M 5,81,3
s_add_u32 s42, s44, s62
RA 220,1,14336
HS 25,118
s_addc_u32 s43, s45, 0
RV 44,8,6400
M 0,10,4
BR 39
LL 17,96
LL 122,97
AR 55,23
M 0,11,5
WL 5
LA 17
PK 118,18,116,33
XS 52
LS 48,38
WV 11
BW 40
M 1,22,4
LC_ 122
PK 116,18,116,32
XS 53
M 1,23,5
RA 224,1,15360
RV 48,8,6464
PO 118
M 4,70,4
LO 16,16,119
LU 119,16
ST 112
XS 54
M 4,71,5
s_add_u32 s42, s44, s63
BR 40
HS 25,119
XS 55
M 5,82,4
WL 6
LL 18,98
LL 123,99
AW 20,52
M 5,83,5
s_addc_u32 s43, s45, 0
LA 18
PK 119,16,117,33
LS 40,39
WV 10
BW 41
Z 6,0,0
LC_ 123
PK 117,16,117,32
v_add_f32 v14, v14, v15
s_nop 1
v_add_f32_dpp v14, v14, v14 quad_perm:[1,0,3,2] row_mask:0xf bank_mask:0xf
s_nop 1
v_add_f32_dpp v14, v14, v14 quad_perm:[2,3,0,1] row_mask:0xf bank_mask:0xf
s_nop 1
v_add_f32_dpp v14, v14, v14 row_half_mirror row_mask:0xf bank_mask:0xf
v_cvt_f64_f32 v[14:15], v14
s_mov_b64 exec, s[54:55]
SP
s_mov_b64 exec, -1
s_nop 0
ds_read_b64 v[14:15], v5 offset:2048
RV 40,8,6528
AW 21,53
M 6,1,1
WV 9
RA 228,1,16384
BR 41
PO 119
AW 22,54
Z 7,12,0
WL 7
LO 17,17,122
LU 122,17
AW 23,55
M 7,13,1
s_mul_i32 s53, %[d], 393216
RA 232,1,17408
HS 25,122
LS 44,40
s_add_u32 s53, s53, s57
BW 42
Z 10,60,0
s_add_u32 s46, %[xlo], s53
RV 44,8,6592
LL 16,100
LL 126,101
M 10,61,1
s_addc_u32 s47, %[xhi], 0
)"
R"(LA 16
BR 42
PK 122,17,120,33
Z 2,72,0
WL 5
LC_ 126
PK 120,17,120,32
M 2,73,1
s_bitcmp1_b64 s[92:93], 48
RA 236,1,18432
v_subrev_u32 v5, s39, v5
LS 48,41
s_cselect_b32 s58, 16, 0
WV 8
BW 43
M 6,2,2
RV 48,8,6656
LO 18,18,123
LU 123,18
M 6,3,3
s_lshr_b32 s58, 0xfe987610, s58
BR 43
HS 25,123
v_lshrrev_b32 v3, 6, v0
v_and_b32 v3, 12, v3
v_bfe_u32 v3, s58, v3, 4
v_mad_u32_u24 v3, v3, s97, v21
M 7,14,2
s_mul_i32 s53, s50, 25600
LL 17,102
LL 127,103
s_add_u32 s40, %[wlo], s53
s_addc_u32 s41, %[whi], 0
s_mov_b32 m0, s38
M 7,15,3
WL 5
LA 17
PK 123,18,121,33
LS 40,42
WV 7
BW 44
M 10,62,2
LC_ 127
RV 40,8,6720
PK 121,18,121,32
M 10,63,3
WV 6
RA 216,1,19456
BR 44
DW 0
M 2,74,2
WL 6
LO 16,16,126
LU 126,16
ST 116
M 2,75,3
s_add_u32 s42, s44, s64
RA 220,1,20480
HS 25,126
s_addc_u32 s43, s45, 0
LS 44,43
s_add_u32 s37, s37, 32768
BW 45
M 6,4,4
s_cmp_eq_u32 s37, s75
LL 18,56
LL 104,57
s_cselect_b32 s37, s74, s37
M 6,5,5
LA 18
PK 126,16,124,33
v_add_f32 v14, v14, v15
s_nop 1
v_add_f32_dpp v14, v14, v14 quad_perm:[1,0,3,2] row_mask:0xf bank_mask:0xf
s_nop 1
v_add_f32_dpp v14, v14, v14 quad_perm:[2,3,0,1] row_mask:0xf bank_mask:0xf
s_nop 1
v_add_f32_dpp v14, v14, v14 row_half_mirror row_mask:0xf bank_mask:0xf
v_cvt_f64_f32 v[14:15], v14
s_mov_b64 exec, s[54:55]
SP
s_mov_b64 exec, -1
XL 48,0
DW 1024
RV 44,8,6784
M 7,16,4
LC_ 104
BR 45
PK 124,16,124,32
M 7,17,5
WL 6
RA 224,1,21504
PO 126
LS 48,44
WV 10
BW 46
M 10,64,4
LO 17,17,127
LU 127,17
ST 120
M 10,65,5
WV 10
DW 2048
RV 48,8,6848
HS 25,127
M 2,76,4
WL 8
RA 240,7,0
LL 16,58
LL 105,59
M 2,77,5
s_add_u32 s40, s40, 3072
LA 16
BR 46
PK 127,17,125,33
M 6,6,0
s_addc_u32 s41, s41, 0
LC_ 105
PK 125,17,125,32
s_add_u32 m0, s38, 3072
M 6,7,1
WL 7
RA 228,1,22528
XL 49,64
PO 127
LS 40,45
s_add_u32 s42, s44, s65
BW 47
M 7,18,0
RA 244,7,256
LO 104,18,104
LU 18,104
M 7,19,1
s_addc_u32 s43, s45, 0
RA 232,1,23552
BR 47
HZ 26,18
M 10,66,0
WL 6
DW 0
LL 17,60
LL 108,61
M 10,67,1
s_lshl_b32 s53, s52, 8
LA 17
HX 25
LS 44,46
s_add_u32 s48, %[alo], s53
RA 248,7,512
M 2,78,0
WL 2
LC_ 108
W1 25,1024
W1 27,3072
M 2,79,1
s_addc_u32 s49, %[ahi], 0
RA 236,1,24576
v_add_u32 v4, s39, v4
LS 48,47
s_add_u32 s52, s52, %[d]
RA 252,7,768
M 6,8,2
LO 105,16,105
LU 16,105
s_cmp_eq_u32 s52, %[wrap]
s_cselect_b32 s52, %[rst], s52
DW 1024
v_mul_f32 v36, 0xbb2aaaab, v36
M 6,9,3
XL 50,128
HS 26,16
v_mul_f32 v37, 0xbb2aaaab, v37
v_mul_f32 v38, 0xbb2aaaab, v38
M 7,20,2
LL 18,62
LL 109,63
v_mul_f32 v39, 0xbb2aaaab, v39
M 7,21,3
LA 18
DW 2048
LK 40,240,36,0
LK 44,242,36,2
RA 240,7,1024
v_add_u32 v1, s37, v0
LK 48,244,37,0
s_nop 0
CF 40,40,41
LK 248,246,37,2
RA 244,7,1280
CF 42,42,43
M 10,68,2
LC_ 109
RA 216,1,0
M 10,69,3
ST 124
v_and_b32 v22, 0xfffffcff, v1
CF 44,44,45
CF 46,46,47
M 2,80,2
WL 6
LO 108,17,108
LU 17,108
AW 24,40
AW 25,42
M 2,81,3
s_mul_i32 s53, s51, 24
RA 220,1,1024
HS 26,17
s_bitcmp1_b32 s51, 0
s_addc_u32 s53, s53, 0
s_lshl_b32 s53, s53, 11
s_bitcmp1_b64 s[90:91], s51
s_cselect_b64 s[44:45], s[84:85], s[82:83]
s_add_u32 s44, s44, s53
s_addc_u32 s45, s45, 0
s_bitcmp1_b64 s[92:93], s51
s_cselect_b64 s[94:95], s[88:89], s[86:87]
s_add_u32 s94, s94, s53
s_addc_u32 s95, s95, 0
s_add_u32 s51, s51, %[d]
s_cmp_eq_u32 s51, %[wrap]
s_cselect_b32 s51, %[rst], s51
DQ 3072
AW 26,44
AW 27,46
LK 40,248,38,0
CF 48,48,49
LK 44,250,38,2
RA 248,7,1536
CF 50,50,51
GZ 36
CF 248,248,249
CF 250,250,251
M 6,10,4
LL 16,72
LL 120,73
M 6,11,5
LA 16
RV 252,22,5120
M 7,22,4
WL 4
LC_ 120
XL 51,192
AW 28,48
M 7,23,5
s_add_u32 s50, s50, %[d]
RA 224,1,2048
s_cmp_eq_u32 s50, %[wrap]
s_cselect_b32 s50, %[rst], s50
v_add_u32 v19, 1, v19
AW 29,50
AW 30,248
AW 31,250
LK 48,252,39,0
CF 40,40,41
LK 248,254,39,2
s_add_u32 s38, s38, 32768
RA 252,7,1792
CF 42,42,43
GS 37
s_cmp_eq_u32 s38, s77
CF 44,44,45
CF 46,46,47
M 10,70,4
LO 109,18,109
LU 18,109
M 10,71,5
s_cselect_b32 s38, s76, s38
FP
HS 26,18
M 2,82,4
WL 4
LL 17,74
LL 121,75
M 2,83,5
s_sub_u32 s39, 0, s39
LA 17
RA 228,1,3072
AW 32,40
AW 33,42
Z 0,0,0
LC_ 121
XL 52,256
AW 34,44
AW 35,46
M 0,1,1
HX 26
LK 40,240,40,0
CF 48,48,49
LK 44,242,40,2
RA 240,7,2048
CF 50,50,51
GS 38
CF 248,248,249
CF 250,250,251
Z 1,12,0
WL 7
LO 120,16,120
LU 16,120
M 1,13,1
RA 232,1,4096
HZ 24,16
AW 36,48
Z 4,60,0
LL 18,76
LL 124,77
AW 37,50
AW 38,248
M 4,61,1
LA 18
FR
AW 39,250
LK 48,244,41,0
CF 40,40,41
LK 248,246,41,2
RA 244,7,2304
CF 42,42,43
GS 39
CF 44,44,45
CF 46,46,47
Z 8,72,0
WL 6
LC_ 124
XL 53,320
M 8,73,1
RA 236,1,6144
AW 40,40
M 0,2,2
WL 7
LO 121,17,121
LU 17,121
AW 41,42
M 0,3,3
HS 24,17
AW 42,44
AW 43,46
LK 40,248,42,0
CF 48,48,49
LK 44,250,42,2
RA 248,7,2560
CF 50,50,51
GS 40
CF 248,248,249
CF 250,250,251
M 1,14,2
LL 16,78
LL 125,79
M 1,15,3
LA 16
RA 216,1,7168
M 4,62,2
WL 6
LC_ 125
v_cmp_gt_u32 vcc, v19, v15
M 4,63,3
XL 54,384
AW 44,48
AW 45,50
AW 46,248
M 8,74,2
WL 4
LO 124,18,124
LU 18,124
AW 47,250
M 8,75,3
RA 220,1,8192
HS 24,18
LK 48,252,43,0
CF 40,40,41
LK 248,254,43,2
RA 252,7,2816
CF 42,42,43
GS 41
CF 44,44,45
CF 46,46,47
M 0,4,4
s_cbranch_vccnz .Lk1s15
.Lk1r15:
LL 17,80
LL 106,81
M 0,5,5
LA 17
ds_read_b64 v[14:15], v5 offset:0
AW 48,40
AW 49,42
M 1,16,4
LC_ 106
AW 50,44
AW 51,46
M 1,17,5
RA 224,1,9216
LK 40,240,44,0
CF 48,48,49
LK 44,242,44,2
CF 50,50,51
GS 42
CF 248,248,249
CF 250,250,251
M 4,64,4
WL 6
)"
R"(LO 125,16,125
LU 16,125
M 4,65,5
XL 55,448
HS 24,16
AW 52,48
M 8,76,4
WL 5
LL 18,82
LL 107,83
AW 53,50
AW 54,248
AW 55,250
M 8,77,5
LA 18
RA 228,1,10240
LK 48,244,45,0
CF 40,40,41
LK 248,246,45,2
CF 42,42,43
HX 24
GS 43
CF 44,44,45
CF 46,46,47
M 0,6,0
LC_ 107
W1 24,0
M 0,7,1
W1 26,2048
AW 56,40
M 1,18,0
WL 6
LO 17,17,106
LU 106,17
AW 57,42
AW 58,44
M 1,19,1
RA 232,1,11264
HZ 27,106
AW 59,46
LK 40,248,46,0
CF 48,48,49
LK 44,250,46,2
CF 50,50,51
v_add_f32 v14, v14, v15
GS 44
CF 248,248,249
CF 250,250,251
M 4,66,0
LL 16,84
LL 110,85
M 4,67,1
LA 16
PK 106,17,104,33
M 8,78,0
WL 5
LC_ 110
PK 104,17,104,32
AW 60,48
AW 61,50
AW 62,248
M 8,79,1
RV 252,22,5392
v_add_f32_dpp v14, v14, v14 quad_perm:[1,0,3,2] row_mask:0xf bank_mask:0xf
AW 63,250
LK 48,252,47,0
CF 40,40,41
LK 248,254,47,2
CF 42,42,43
v_add_f32_dpp v14, v14, v14 quad_perm:[2,3,0,1] row_mask:0xf bank_mask:0xf
GS 45
CF 44,44,45
CF 46,46,47
M 0,8,2
WL 6
LO 18,18,107
LU 107,18
M 0,9,3
RA 236,1,12288
HS 27,107
M 1,20,2
LL 17,86
LL 111,87
AW 64,40
AW 65,42
M 1,21,3
LA 17
RV 36,8,6912
PK 107,18,105,33
AW 66,44
AW 67,46
M 4,68,2
LC_ 111
XL 56,512
PK 105,18,105,32
M 4,69,3
s_add_u32 s42, s94, s60
RA 216,1,13312
v_add_f32_dpp v14, v14, v14 row_half_mirror row_mask:0xf bank_mask:0xf
GS 46
s_addc_u32 s43, s95, 0
CF 48,48,49
CF 50,50,51
CF 248,248,249
CF 250,250,251
M 8,80,2
WL 4
LO 16,16,110
LU 110,16
AW 68,48
AW 69,50
AW 70,248
M 8,81,3
RA 220,1,14336
HS 27,110
AW 71,250
s_nop 1
GS 47
SU 104
v_cvt_f64_f32 v[14:15], v14
s_nop 5
v_cndmask_b32 v52, v52, v53, s[78:79]
M 0,10,4
LL 18,96
LL 122,97
v_cndmask_b32 v54, v54, v55, s[78:79]
M 0,11,5
LA 18
PK 110,16,108,33
v_cndmask_b32 v52, v52, v54, s[80:81]
M 1,22,4
LC_ 122
PK 108,16,108,32
v_mul_f32 v52, 0x3b2aaaab, v52
M 1,23,5
WL 5
RA 224,1,15360
PO 110
v_med3_f32 v53, v52, v39, v37
M 4,70,4
WL 5
LO 17,17,111
LU 111,17
s_mov_b64 exec, s[54:55]
SP
s_mov_b64 exec, -1
XL 57,576
LG 52,53
s_nop 0
C 52,52,52
M 4,71,5
s_add_u32 s42, s94, s61
ds_read_b64 v[14:15], v5 offset:2048
HS 27,111
v_lshlrev_b32 v52, 16, v52
M 8,82,4
WL 4
LL 16,98
LL 123,99
v_add_f32 v52, v36, v52
M 8,83,5
s_addc_u32 s43, s95, 0
LA 16
PK 111,17,109,33
C 52,52,52
Z 6,0,0
LC_ 123
PK 109,17,109,32
v_lshlrev_b32 v52, 16, v52
M 6,1,1
RA 228,1,16384
PO 111
v_sqrt_f32 v52, v52
s_nop 0
C 52,52,52
Z 7,12,0
WL 5
LO 18,18,122
LU 122,18
v_lshlrev_b32 v52, 16, v52
M 7,13,1
RA 232,1,17408
HZ 25,122
v_rcp_f32 v52, v52
Z 10,60,0
XL 58,640
LL 17,64
LL 126,65
v_mul_f32 v52, v38, v52
M 10,61,1
LA 17
PK 122,18,120,33
v_subrev_u32 v5, s39, v5
HX 27
CF 52,52,52
Z 11,72,0
WL 2
LC_ 126
PK 120,18,120,32
ds_bpermute_b32 v13, v28, v52
M 11,73,1
RA 236,1,18432
v_add_f32 v14, v14, v15
M 6,2,2
LO 16,16,123
LU 123,16
M 6,3,3
SU 108
HS 25,123
v_add_f32_dpp v14, v14, v14 quad_perm:[1,0,3,2] row_mask:0xf bank_mask:0xf
M 7,14,2
s_mul_i32 s53, s50, 25600
LL 18,66
LL 127,67
s_add_u32 s40, %[wlo], s53
s_addc_u32 s41, %[whi], 0
s_mov_b32 m0, s38
M 7,15,3
LA 18
PK 123,16,121,33
v_add_f32_dpp v14, v14, v14 quad_perm:[2,3,0,1] row_mask:0xf bank_mask:0xf
M 10,62,2
LC_ 127
PK 121,16,121,32
M 10,63,3
RA 216,1,19456
XL 59,704
DW 0
v_add_f32_dpp v14, v14, v14 row_half_mirror row_mask:0xf bank_mask:0xf
M 11,74,2
WL 3
LO 17,17,126
LU 126,17
M 11,75,3
s_add_u32 s42, s44, s64
RA 220,1,20480
HS 25,126
v_cvt_f64_f32 v[14:15], v14
s_addc_u32 s43, s45, 0
AR 248,24
M 6,4,4
s_add_u32 s37, s37, 32768
LL 16,56
LL 104,57
s_cmp_eq_u32 s37, s75
s_cselect_b32 s37, s74, s37
AR 249,25
M 6,5,5
LA 16
PK 126,17,124,33
s_mov_b64 exec, s[54:55]
SP
s_mov_b64 exec, -1
DW 1024
AR 250,26
M 7,16,4
LC_ 104
PK 124,17,124,32
AR 251,27
AR 52,28
M 7,17,5
RA 224,1,21504
PO 126
AR 53,29
M 10,64,4
WL 5
LO 18,18,127
LU 127,18
ST 120
AR 54,30
AR 55,31
M 10,65,5
DW 2048
HS 25,127
XS 248
M 11,76,4
WL 3
LL 17,58
LL 105,59
XS 249
XS 250
M 11,77,5
s_add_u32 s40, s40, 3072
LA 17
PK 127,18,125,33
XS 251
M 6,6,0
s_addc_u32 s41, s41, 0
LC_ 105
PK 125,18,125,32
s_add_u32 m0, s38, 3072
AW 24,248
AW 25,249
M 6,7,1
s_add_u32 s42, s44, s65
RA 228,1,22528
PO 127
AW 26,250
M 7,18,0
WL 4
LO 104,16,104
LU 16,104
AW 27,251
XS 52
M 7,19,1
s_addc_u32 s43, s45, 0
RA 232,1,23552
HZ 26,16
XS 53
M 10,66,0
DW 0
LL 18,60
LL 108,61
XS 54
XS 55
M 10,67,1
s_lshl_b32 s53, s52, 8
LA 18
HX 25
AW 28,52
M 11,78,0
WL 2
LC_ 108
W1 25,1024
W1 27,3072
AW 29,53
AW 30,54
M 11,79,1
s_add_u32 s48, %[alo], s53
RA 236,1,24576
v_add_u32 v4, s39, v4
AW 31,55
M 6,8,2
LO 105,17,105
LU 17,105
s_addc_u32 s49, %[ahi], 0
s_add_u32 s52, s52, %[d]
s_cmp_eq_u32 s52, %[wrap]
s_cselect_b32 s52, %[rst], s52
DW 1024
v_min_u32 v35, v35, v13
AR 248,32
M 6,9,3
ST 124
HS 26,17
s_bitcmp1_b64 s[92:93], 48
s_cselect_b32 s58, 16, 0
s_lshr_b32 s58, 0xda43cb52, s58
v_and_b32 v11, 0x1f0, v0
v_min_u32 v11, 368, v11
v_lshlrev_b32 v11, 3, v11
AR 249,33
M 7,20,2
s_mul_i32 s53, s51, 24
LL 16,62
LL 109,63
s_bitcmp1_b32 s51, 0
s_addc_u32 s53, s53, 0
s_lshl_b32 s53, s53, 11
s_bitcmp1_b64 s[90:91], s51
s_cselect_b64 s[44:45], s[84:85], s[82:83]
s_add_u32 s44, s44, s53
s_addc_u32 s45, s45, 0
s_bitcmp1_b64 s[92:93], s51
s_cselect_b64 s[94:95], s[88:89], s[86:87]
s_add_u32 s94, s94, s53
s_addc_u32 s95, s95, 0
s_add_u32 s51, s51, %[d]
s_cmp_eq_u32 s51, %[wrap]
s_cselect_b32 s51, %[rst], s51
AR 250,34
AR 251,35
M 7,21,3
LA 16
DW 2048
XS 248
M 10,68,2
LC_ 109
DQ 3072
XS 249
XS 250
M 10,69,3
s_add_u32 s50, s50, %[d]
v_add_u32 v1, s37, v0
XS 251
M 11,80,2
WL 2
LO 108,18,108
LU 18,108
AW 32,248
AW 33,249
M 11,81,3
s_cmp_eq_u32 s50, %[wrap]
RA 216,1,0
HS 26,18
AW 34,250
M 6,10,4
s_cselect_b32 s50, %[rst], s50
LL 17,72
LL 120,73
)"
R"(AW 35,251
AR 52,36
M 6,11,5
LA 17
v_and_b32 v22, 0xfffffcff, v1
AR 53,37
M 7,22,4
LC_ 120
RA 220,1,1024
AR 54,38
AR 55,39
M 7,23,5
RV 252,22,5120
v_add_u32 v19, 1, v19
XS 52
M 10,70,4
WL 5
LO 109,16,109
LU 16,109
XS 53
XS 54
M 10,71,5
s_add_u32 s38, s38, 32768
RA 224,1,2048
HS 26,16
XS 55
M 11,82,4
WL 1
LL 18,74
LL 121,75
AW 36,52
AW 37,53
M 11,83,5
s_cmp_eq_u32 s38, s77
LA 18
RA 228,1,3072
s_cselect_b32 s38, s76, s38
FP
s_sub_u32 s39, 0, s39
AW 38,54
Z 0,0,0
LC_ 121
HX 26
AW 39,55
M 0,1,1
AR 248,40
AR 249,41
Z 1,12,0
LO 120,17,120
LU 17,120
AR 250,42
M 1,13,1
RA 232,1,4096
HZ 24,17
AR 251,43
XS 248
Z 2,24,0
LL 16,88
LL 124,89
XS 249
M 2,25,1
LA 16
XS 250
XS 251
Z 3,36,0
LC_ 124
AW 40,248
M 3,37,1
AW 41,249
AW 42,250
Z 4,60,0
WL 3
LO 121,18,121
LU 18,121
AW 43,251
M 4,61,1
RA 236,1,6144
HS 24,18
AR 52,44
AR 53,45
Z 5,72,0
LL 17,90
LL 125,91
AR 54,46
M 5,73,1
LA 17
AR 55,47
XS 52
M 0,2,2
LC_ 125
XS 53
M 0,3,3
XS 54
XS 55
M 1,14,2
WL 3
LO 124,16,124
LU 16,124
AW 44,52
M 1,15,3
RA 216,1,7168
HS 24,16
AW 45,53
AW 46,54
M 2,26,2
LL 18,80
LL 106,81
AW 47,55
M 2,27,3
LA 18
FR
AR 248,48
AR 249,49
M 3,38,2
LC_ 106
v_lshrrev_b32 v3, 9, v0
AR 250,50
M 3,39,3
WV 32
BW 48
AR 251,51
XS 248
M 4,62,2
WL 5
LO 125,17,125
LU 17,125
XS 249
M 4,63,3
RA 220,1,8192
HS 24,17
XS 250
XS 251
M 5,74,2
LL 16,82
LL 107,83
AW 48,248
M 5,75,3
LA 16
RV 40,8,6144
AW 49,249
AW 50,250
M 0,4,4
LC_ 107
RV 44,8,6208
AW 51,251
M 0,5,5
RV 48,8,6272
BR 48
AR 52,52
AR 53,53
M 1,16,4
WL 8
LO 18,18,106
LU 106,18
AR 54,54
M 1,17,5
RA 224,1,9216
HZ 27,106
AR 55,55
XS 52
M 2,28,4
LL 17,84
LL 110,85
XS 53
M 2,29,5
LA 17
PK 106,18,104,33
XS 54
XS 55
M 3,40,4
LC_ 110
PK 104,18,104,32
v_lshl_add_u32 v3, v3, 2, 0
v_bfe_u32 v3, s58, v3, 4
v_mad_u32_u24 v3, v3, s97, v11
AW 52,52
M 3,41,5
s_mov_b32 m0, %[pm]
s_nop 0
DF
v_cmp_gt_u32 vcc, v19, v15
AW 53,53
AW 54,54
M 4,64,4
WL 6
LO 16,16,107
LU 107,16
AW 55,55
M 4,65,5
s_add_u32 s42, s94, s60
RA 228,1,10240
HS 27,107
AR 248,56
M 5,76,4
LL 18,86
LL 111,87
AR 249,57
AR 250,58
M 5,77,5
s_cbranch_vccnz .Lk1s16
.Lk1r16:
LA 18
PK 107,16,105,33
AR 251,59
M 0,6,0
s_addc_u32 s43, s95, 0
LC_ 111
PK 105,16,105,32
XS 248
XS 249
M 0,7,1
SU 104
HX 24
XS 250
M 1,18,0
WL 3
LO 17,17,110
LU 110,17
XS 251
AW 56,248
M 1,19,1
RA 232,1,11264
HS 27,110
AW 57,249
M 2,30,0
LL 16,96
LL 122,97
AW 58,250
AW 59,251
M 2,31,1
LA 16
PK 110,17,108,33
AR 52,60
M 3,42,0
LC_ 122
PK 108,17,108,32
AR 53,61
AR 54,62
M 3,43,1
RV 252,22,5392
PO 110
AR 55,63
M 4,66,0
WL 4
LO 18,18,111
LU 111,18
XS 52
XS 53
M 4,67,1
s_add_u32 s42, s94, s61
RA 236,1,12288
HS 27,111
XS 54
M 5,78,0
LL 17,98
LL 123,99
XS 55
AW 60,52
M 5,79,1
s_addc_u32 s43, s95, 0
LA 17
PK 111,18,109,33
AW 61,53
M 0,8,2
LC_ 123
PK 109,18,109,32
AW 62,54
AW 63,55
M 0,9,3
W1 24,0
PO 111
AR 248,64
M 1,20,2
WL 5
LO 16,16,122
LU 122,16
W1 26,2048
AR 249,65
AR 250,66
M 1,21,3
RA 216,1,13312
HZ 25,122
AR 251,67
M 2,32,2
LL 18,100
LL 126,101
XS 248
XS 249
M 2,33,3
LA 18
PK 122,16,120,33
XS 250
M 3,44,2
LC_ 126
PK 120,16,120,32
XS 251
AW 64,248
M 3,45,3
WV 30
SU 108
HX 27
AW 65,249
BW 49
M 4,68,2
WL 6
LO 17,17,123
LU 123,17
AW 66,250
AW 67,251
M 4,69,3
RA 220,1,14336
BR 49
HS 25,123
AR 52,68
LZ 40,48
WV 28
BW 50
M 5,80,2
LL 16,102
LL 127,103
AR 53,69
AR 54,70
M 5,81,3
LA 16
PK 123,17,121,33
AR 55,71
M 0,10,4
LC_ 127
PK 121,17,121,32
XS 52
XS 53
M 0,11,5
WV 24
DY
RV 40,8,6336
XS 54
M 1,22,4
WL 4
BR 50
LO 18,18,126
LU 126,18
XS 55
AW 68,52
M 1,23,5
s_add_u32 s42, s44, s64
RA 224,1,15360
HS 25,126
AW 69,53
LS 44,49
s_addc_u32 s43, s45, 0
BW 51
M 2,34,4
LL 17,56
LL 104,57
AW 70,54
AW 71,55
M 2,35,5
LA 17
PK 126,18,124,33
M 3,46,4
LC_ 104
RV 44,8,6400
PK 124,18,124,32
M 3,47,5
WV 24
ST 120
BR 51
PO 126
M 4,70,4
WL 6
LO 16,16,127
LU 127,16
M 4,71,5
s_add_u32 s42, s44, s65
RA 228,1,16384
HS 25,127
LS 48,50
s_addc_u32 s43, s45, 0
BW 52
M 5,82,4
LL 18,58
LL 105,59
M 5,83,5
LA 18
RV 48,8,6464
PK 127,16,125,33
Z 6,0,0
LC_ 105
PK 125,16,125,32
M 6,1,1
ds_read_b64 v[14:15], v5 offset:0
BR 52
PO 127
Z 7,12,0
WL 9
LO 104,17,104
LU 17,104
M 7,13,1
WL 7
RA 232,1,17408
)"
R"(HZ 26,17
LS 40,51
WV 24
BW 53
Z 8,24,0
WV 23
LL 16,60
LL 108,61
M 8,25,1
WL 2
LA 16
ST 124
RV 40,8,6528
Z 9,36,0
s_mul_i32 s53, s50, 25600
LC_ 108
BR 53
M 9,37,1
s_add_u32 s40, %[wlo], s53
HX 25
LS 44,52
s_addc_u32 s41, %[whi], 0
BW 54
Z 10,60,0
LO 105,18,105
LU 18,105
M 10,61,1
s_mov_b32 m0, s38
RA 236,1,18432
HS 26,18
Z 11,72,0
RV 44,8,6592
LL 17,62
LL 109,63
M 11,73,1
LA 17
W1 25,1024
BR 54
M 6,2,2
LC_ 109
W1 27,3072
M 6,3,3
WL 8
DW 0
v_add_f32 v14, v14, v15
LS 48,53
WV 24
BW 55
M 7,14,2
LO 108,16,108
LU 16,108
M 7,15,3
s_add_u32 s37, s37, 32768
RA 216,1,19456
HS 26,16
M 8,26,2
s_cmp_eq_u32 s37, s75
RV 48,8,6656
LL 18,64
LL 112,65
M 8,27,3
s_cselect_b32 s37, s74, s37
LA 18
DW 1024
BR 55
M 9,38,2
LC_ 112
v_add_f32_dpp v14, v14, v14 quad_perm:[1,0,3,2] row_mask:0xf bank_mask:0xf
M 9,39,3
WL 8
v_add_f32_dpp v14, v14, v14 quad_perm:[2,3,0,1] row_mask:0xf bank_mask:0xf
v_add_u32 v4, s39, v4
LS 40,54
WV 24
BW 56
M 10,62,2
WL 8
LO 109,17,109
LU 17,109
M 10,63,3
RA 220,1,20480
RV 40,8,6720
HS 26,17
M 11,74,2
BR 56
LL 16,66
LL 113,67
M 11,75,3
LA 16
v_add_f32_dpp v14, v14, v14 row_half_mirror row_mask:0xf bank_mask:0xf
M 6,4,4
WL 6
LC_ 113
v_cvt_f64_f32 v[14:15], v14
M 6,5,5
s_mov_b64 exec, s[54:55]
SP
s_mov_b64 exec, -1
v_lshrrev_b32 v3, 9, v0
LS 44,55
WV 22
BW 57
M 7,16,4
WL 7
RV 44,8,6784
LO 112,18,112
LU 18,112
M 7,17,5
RA 224,1,21504
HZ 29,18
M 8,28,4
BR 57
LL 17,68
LL 116,69
M 8,29,5
WL 6
LA 17
ds_read_b64 v[14:15], v5 offset:2048
LS 48,56
WV 21
BW 58
M 9,40,4
LC_ 116
RV 48,8,6848
M 9,41,5
RA 240,7,0
BR 58
M 10,64,4
WL 11
LO 113,16,113
LU 16,113
M 10,65,5
RA 228,1,22528
HS 29,16
v_lshl_add_u32 v3, v3, 2, 8
v_bfe_u32 v3, s58, v3, 4
v_mad_u32_u24 v3, v3, s97, v11
M 11,76,4
WV 19
LL 18,70
LL 117,71
M 11,77,5
WL 8
LA 18
DW 2048
M 6,6,0
s_mul_i32 s53, s51, 24
LC_ 117
v_subrev_u32 v5, s39, v5
M 6,7,1
s_bitcmp1_b32 s51, 0
LS 40,57
s_addc_u32 s53, s53, 0
BW 59
M 7,18,0
WL 4
RA 244,7,256
LO 116,17,116
LU 17,116
M 7,19,1
s_lshl_b32 s53, s53, 11
RA 232,1,23552
HS 29,17
M 8,30,0
s_bitcmp1_b64 s[90:91], s51
BR 59
LL 16,72
LL 120,73
M 8,31,1
s_cselect_b64 s[44:45], s[84:85], s[82:83]
LA 16
v_add_f32 v14, v14, v15
s_add_u32 s44, s44, s53
s_addc_u32 s45, s45, 0
s_bitcmp1_b64 s[92:93], s51
s_cselect_b64 s[94:95], s[88:89], s[86:87]
s_add_u32 s94, s94, s53
s_addc_u32 s95, s95, 0
s_add_u32 s51, s51, %[d]
s_cmp_eq_u32 s51, %[wrap]
s_cselect_b32 s51, %[rst], s51
s_mul_i32 s53, %[d], 16384
s_add_u32 s62, s53, %[sb1]
s_add_u32 s63, s62, 512
LS 44,58
s_add_u32 s40, s40, 3072
RA 248,7,512
v_add_f32_dpp v14, v14, v14 quad_perm:[1,0,3,2] row_mask:0xf bank_mask:0xf
M 9,42,0
s_addc_u32 s41, s41, 0
LC_ 120
v_add_f32_dpp v14, v14, v14 quad_perm:[2,3,0,1] row_mask:0xf bank_mask:0xf
s_add_u32 m0, s38, 3072
M 9,43,1
DW 0
v_add_f32_dpp v14, v14, v14 row_half_mirror row_mask:0xf bank_mask:0xf
M 10,66,0
WL 7
LO 117,18,117
LU 18,117
M 10,67,1
WL 3
RA 236,1,24576
HS 29,18
LS 48,59
RA 252,7,768
v_cvt_f64_f32 v[14:15], v14
M 11,78,0
LL 17,74
LL 121,75
s_nop 2
v_mul_f32 v36, 0xbb2aaaab, v36
M 11,79,1
LA 17
DW 1024
v_mul_f32 v37, 0xbb2aaaab, v37
v_mul_f32 v38, 0xbb2aaaab, v38
M 6,8,2
LC_ 121
s_mov_b64 exec, s[54:55]
SP
s_mov_b64 exec, -1
v_mul_f32 v39, 0xbb2aaaab, v39
M 6,9,3
DW 2048
v_add_u32 v1, s37, v0
LK 40,240,48,0
LK 44,242,48,2
s_lshl_b32 s53, s52, 8
RA 240,7,1024
v_pk_add_f16 v26, v26, v29
LK 48,244,49,0
CF 40,40,41
LK 248,246,49,2
s_add_u32 s48, %[alo], s53
RA 244,7,1280
CF 42,42,43
M 7,20,2
WL 6
LO 120,16,120
LU 16,120
M 7,21,3
s_addc_u32 s49, %[ahi], 0
RA 216,1,0
HZ 24,16
CF 44,44,45
M 8,32,2
s_add_u32 s52, s52, %[d]
LL 18,76
LL 124,77
CF 46,46,47
AW 72,40
M 8,33,3
s_cmp_eq_u32 s52, %[wrap]
LA 18
v_and_b32 v22, 0xfffffcff, v1
AW 73,42
AW 74,44
M 9,44,2
s_cselect_b32 s52, %[rst], s52
LC_ 124
DQ 3072
AW 75,46
M 9,45,3
HX 26
LK 40,248,50,0
CF 48,48,49
LK 44,250,50,2
RA 248,7,1536
CF 50,50,51
GZ 48
CF 248,248,249
CF 250,250,251
M 10,68,2
WL 6
LO 121,17,121
LU 17,121
M 10,69,3
RA 220,1,1024
HS 24,17
AW 76,48
M 11,80,2
LL 16,78
LL 125,79
AW 77,50
AW 78,248
AW 79,250
M 11,81,3
LA 16
RV 252,22,5120
LK 48,252,51,0
CF 40,40,41
LK 248,254,51,2
RA 252,7,1792
CF 42,42,43
GS 49
CF 44,44,45
CF 46,46,47
M 6,10,4
LC_ 125
DY
M 6,11,5
s_add_u32 s50, s50, %[d]
AW 80,40
AW 81,42
M 7,22,4
WL 6
LO 124,18,124
LU 18,124
M 7,23,5
s_cmp_eq_u32 s50, %[wrap]
RA 224,1,2048
HS 24,18
AW 82,44
AW 83,46
LK 40,240,52,0
CF 48,48,49
LK 44,242,52,2
s_cselect_b32 s50, %[rst], s50
RA 240,7,2048
CF 50,50,51
GS 50
CF 248,248,249
CF 250,250,251
M 8,34,4
LL 17,80
LL 106,81
M 8,35,5
LA 17
v_add_u32 v19, 1, v19
M 9,46,4
s_add_u32 s38, s38, 32768
LC_ 106
AW 84,48
M 9,47,5
AW 85,50
AW 86,248
M 10,70,4
WL 4
LO 125,16,125
LU 16,125
AW 87,250
M 10,71,5
RA 228,1,3072
HS 24,16
LK 48,244,53,0
CF 40,40,41
LK 248,246,53,2
RA 244,7,2304
CF 42,42,43
GS 51
CF 44,44,45
CF 46,46,47
M 11,82,4
LL 18,82
LL 107,83
M 11,83,5
LA 18
HX 24
Z 0,0,0
s_cmp_eq_u32 s38, s77
LC_ 107
AW 88,40
M 0,1,1
s_cselect_b32 s38, s76, s38
FP
AW 89,42
AW 90,44
AW 91,46
LK 40,248,54,0
CF 48,48,49
LK 44,250,54,2
s_sub_u32 s39, 0, s39
RA 248,7,2560
CF 50,50,51
GS 52
CF 248,248,249
CF 250,250,251
Z 1,12,0
WL 6
LO 17,17,106
LU 106,17
M 1,13,1
RA 232,1,4096
HZ 27,106
Z 2,24,0
LL 16,84
LL 110,85
M 2,25,1
LA 16
PK 106,17,104,33
AW 92,48
Z 3,36,0
LC_ 110
PK 104,17,104,32
AW 93,50
AW 94,248
AW 95,250
M 3,37,1
LK 48,252,55,0
)"
R"(CF 40,40,41
LK 248,254,55,2
RA 252,7,2816
CF 42,42,43
GS 53
CF 44,44,45
CF 46,46,47
Z 4,60,0
WL 6
LO 18,18,107
LU 107,18
M 4,61,1
s_add_u32 s42, s94, s60
RA 236,1,6144
HS 27,107
Z 5,72,0
LL 17,86
LL 111,87
AW 96,40
AW 97,42
M 5,73,1
s_addc_u32 s43, s95, 0
LA 17
PK 107,18,105,33
AW 98,44
AW 99,46
LK 40,240,56,0
CF 48,48,49
LK 44,242,56,2
CF 50,50,51
GS 54
CF 248,248,249
CF 250,250,251
M 0,2,2
LC_ 111
PK 105,18,105,32
M 0,3,3
SU 104
AW 100,48
M 1,14,2
WL 4
LO 16,16,110
LU 110,16
M 1,15,3
RA 216,1,7168
HS 27,110
AW 101,50
AW 102,248
M 2,26,2
LL 18,88
LL 114,89
AW 103,250
M 2,27,3
LA 18
PK 110,16,108,33
LK 48,244,57,0
CF 40,40,41
LK 248,246,57,2
CF 42,42,43
GS 55
CF 44,44,45
CF 46,46,47
M 3,38,2
LC_ 114
PK 108,16,108,32
M 3,39,3
FR
PO 110
AW 104,40
M 4,62,2
WL 4
LO 17,17,111
LU 111,17
AW 105,42
AW 106,44
AW 107,46
M 4,63,3
s_add_u32 s42, s94, s61
RA 220,1,8192
HS 27,111
LK 40,248,58,0
CF 48,48,49
LK 44,250,58,2
s_addc_u32 s43, s95, 0
CF 50,50,51
GS 56
CF 248,248,249
CF 250,250,251
M 5,74,2
LL 16,90
LL 115,91
M 5,75,3
s_mov_b32 m0, %[pm]
LA 16
PK 111,17,109,33
M 0,4,4
LC_ 115
PK 109,17,109,32
AW 108,48
AW 109,50
M 0,5,5
DF
PO 111
AW 110,248
AW 111,250
LK 48,252,59,0
CF 40,40,41
LK 248,254,59,2
CF 42,42,43
GS 57
CF 44,44,45
CF 46,46,47
M 1,16,4
WL 4
LO 18,18,114
LU 114,18
M 1,17,5
RA 224,1,9216
HZ 29,114
M 2,28,4
LL 17,92
LL 118,93
AW 112,40
M 2,29,5
LA 17
PK 114,18,112,33
AW 113,42
AW 114,44
M 3,40,4
LC_ 118
RV 36,8,6912
PK 112,18,112,32
AW 115,46
M 3,41,5
WL 7
SU 108
v_cmp_gt_u32 vcc, v19, v15
GS 58
CF 48,48,49
CF 50,50,51
M 4,64,4
WL 4
LO 16,16,115
LU 115,16
CF 248,248,249
CF 250,250,251
AW 116,48
M 4,65,5
s_add_u32 s42, s94, s62
RA 228,1,10240
HS 29,115
AW 117,50
AW 118,248
M 5,76,4
LL 18,94
LL 119,95
AW 119,250
M 5,77,5
s_cbranch_vccnz .Lk1s17
.Lk1r17:
LA 18
PK 115,16,113,33
GS 59
s_addc_u32 s43, s95, 0
W1 24,0
s_nop 5
v_cndmask_b32 v52, v52, v53, s[78:79]
M 0,6,0
LC_ 119
PK 113,16,113,32
v_cndmask_b32 v54, v54, v55, s[78:79]
M 0,7,1
W1 26,2048
v_cndmask_b32 v52, v52, v54, s[80:81]
M 1,18,0
WL 6
LO 17,17,118
LU 118,17
v_mul_f32 v52, 0x3b2aaaab, v52
M 1,19,1
WL 5
RA 232,1,11264
HS 29,118
v_med3_f32 v53, v52, v39, v37
M 2,30,0
LL 16,96
LL 122,97
LG 52,53
M 2,31,1
LA 16
PK 118,17,116,33
C 52,52,52
M 3,42,0
LC_ 122
PK 116,17,116,32
v_lshlrev_b32 v52, 16, v52
M 3,43,1
RV 252,22,5392
PO 118
v_add_f32 v52, v36, v52
M 4,66,0
WL 5
LO 18,18,119
LU 119,18
C 52,52,52
M 4,67,1
RA 236,1,12288
HS 29,119
M 5,78,0
LL 17,98
LL 123,99
v_lshlrev_b32 v52, 16, v52
M 5,79,1
LA 17
PK 119,18,117,33
v_sqrt_f32 v52, v52
M 0,8,2
LC_ 123
PK 117,18,117,32
C 52,52,52
M 0,9,3
SU 112
PO 119
v_lshlrev_b32 v52, 16, v52
M 1,20,2
WL 4
LO 16,16,122
LU 122,16
v_rcp_f32 v52, v52
M 1,21,3
s_add_u32 s42, s94, s63
RA 216,1,13312
HZ 25,122
v_mul_f32 v52, v38, v52
M 2,32,2
s_addc_u32 s43, s95, 0
LL 18,100
LL 126,101
CF 52,52,52
M 2,33,3
LA 18
PK 122,16,120,33
ds_bpermute_b32 v13, v28, v52
M 3,44,2
LC_ 126
PK 120,16,120,32
M 3,45,3
SU 116
v_pk_add_f16 v27, v27, v29
M 4,68,2
WL 4
LO 17,17,123
LU 123,17
M 4,69,3
s_add_u32 s42, s44, s64
RA 220,1,14336
HS 25,123
M 5,80,2
LL 16,102
LL 127,103
M 5,81,3
s_addc_u32 s43, s45, 0
LA 16
PK 123,17,121,33
M 0,10,4
LC_ 127
PK 121,17,121,32
M 0,11,5
ST 120
HX 27
M 1,22,4
WL 3
LO 18,18,126
LU 126,18
M 1,23,5
RA 224,1,15360
HS 25,126
AR 248,72
M 2,34,4
LL 17,56
LL 104,57
AR 249,73
M 2,35,5
LA 17
PK 126,18,124,33
AR 250,74
M 3,46,4
LC_ 104
PK 124,18,124,32
AR 251,75
M 3,47,5
ds_read_b64 v[14:15], v5 offset:0
PO 126
AR 52,76
M 4,70,4
WL 4
LO 16,16,127
LU 127,16
AR 53,77
M 4,71,5
s_add_u32 s42, s44, s65
RA 228,1,16384
HS 25,127
AR 54,78
M 5,82,4
LL 18,58
LL 105,59
AR 55,79
M 5,83,5
s_addc_u32 s43, s45, 0
LA 18
PK 127,16,125,33
XS 248
Z 6,0,0
LC_ 105
PK 125,16,125,32
XS 249
M 6,1,1
PO 127
HX 25
XS 250
Z 7,12,0
WL 4
LO 104,17,104
LU 17,104
XS 251
AW 72,248
M 7,13,1
RA 232,1,17408
HZ 26,17
AW 73,249
Z 8,24,0
WL 4
LL 16,60
LL 108,61
AW 74,250
M 8,25,1
s_mul_i32 s53, s50, 25600
LA 16
W1 25,1024
AW 75,251
Z 9,36,0
s_add_u32 s40, %[wlo], s53
LC_ 108
W1 27,3072
XS 52
M 9,37,1
s_addc_u32 s41, %[whi], 0
v_add_f32 v14, v14, v15
v_add_u32 v4, s39, v4
XS 53
Z 10,60,0
WL 5
LO 105,18,105
LU 18,105
XS 54
M 10,61,1
s_mov_b32 m0, s38
RA 236,1,18432
HS 26,18
XS 55
Z 11,72,0
LL 17,62
LL 109,63
AW 76,52
M 11,73,1
LA 17
ST 124
AW 77,53
M 6,2,2
LC_ 109
DW 0
AW 78,54
AW 79,55
M 6,3,3
v_add_f32_dpp v14, v14, v14 quad_perm:[1,0,3,2] row_mask:0xf bank_mask:0xf
v_min_u32 v35, v35, v13
M 7,14,2
WL 4
LO 108,16,108
LU 16,108
AR 248,80
M 7,15,3
s_add_u32 s37, s37, 32768
RA 216,1,19456
HS 26,16
AR 249,81
M 8,26,2
s_cmp_eq_u32 s37, s75
LL 18,64
LL 112,65
AR 250,82
M 8,27,3
s_cselect_b32 s37, s74, s37
LA 18
v_add_f32_dpp v14, v14, v14 quad_perm:[2,3,0,1] row_mask:0xf bank_mask:0xf
AR 251,83
M 9,38,2
s_mul_i32 s53, %[d], 393216
LC_ 112
)"
R"(v_add_f32_dpp v14, v14, v14 row_half_mirror row_mask:0xf bank_mask:0xf
XS 248
M 9,39,3
s_add_u32 s53, s53, s57
v_cvt_f64_f32 v[14:15], v14
XS 249
M 10,62,2
WL 3
LO 109,17,109
LU 17,109
XS 250
M 10,63,3
s_add_u32 s46, %[xlo], s53
RA 220,1,20480
HS 26,17
XS 251
M 11,74,2
LL 16,66
LL 113,67
AW 80,248
AW 81,249
M 11,75,3
s_addc_u32 s47, %[xhi], 0
LA 16
s_mov_b64 exec, s[54:55]
SP
s_mov_b64 exec, -1
AW 82,250
M 6,4,4
s_bitcmp1_b64 s[92:93], 48
LC_ 113
ds_read_b64 v[14:15], v5 offset:2048
AW 83,251
M 6,5,5
s_cselect_b32 s58, 16, 0
v_subrev_u32 v5, s39, v5
AR 52,84
M 7,16,4
WL 4
LO 112,18,112
LU 18,112
AR 53,85
M 7,17,5
s_lshr_b32 s58, 0xda43cb52, s58
RA 224,1,21504
HZ 29,18
AR 54,86
M 8,28,4
LL 17,68
LL 116,69
AR 55,87
M 8,29,5
LA 17
v_lshrrev_b32 v3, 6, v0
XS 52
M 9,40,4
LC_ 116
v_and_b32 v3, 12, v3
v_bfe_u32 v3, s58, v3, 4
v_mad_u32_u24 v3, v3, s97, v21
XS 53
M 9,41,5
DW 1024
XS 54
M 10,64,4
WL 4
LO 113,16,113
LU 16,113
XS 55
AW 84,52
M 10,65,5
RA 228,1,22528
HS 29,16
AW 85,53
M 11,76,4
LL 18,70
LL 117,71
AW 86,54
M 11,77,5
LA 18
XL 60,0
AW 87,55
M 6,6,0
LC_ 117
DW 2048
AR 248,88
M 6,7,1
WL 6
XL 61,64
v_add_f32 v14, v14, v15
AR 249,89
M 7,18,0
WL 3
LO 116,17,116
LU 17,116
AR 250,90
M 7,19,1
s_mul_i32 s53, s51, 24
RA 232,1,23552
HS 29,17
AR 251,91
M 8,30,0
s_bitcmp1_b32 s51, 0
LL 16,72
LL 120,73
XS 248
M 8,31,1
s_addc_u32 s53, s53, 0
LA 16
v_add_f32_dpp v14, v14, v14 quad_perm:[1,0,3,2] row_mask:0xf bank_mask:0xf
s_lshl_b32 s53, s53, 11
s_bitcmp1_b64 s[90:91], s51
s_cselect_b64 s[44:45], s[84:85], s[82:83]
s_add_u32 s44, s44, s53
s_addc_u32 s45, s45, 0
s_bitcmp1_b64 s[92:93], s51
s_cselect_b64 s[94:95], s[88:89], s[86:87]
s_add_u32 s94, s94, s53
s_addc_u32 s95, s95, 0
s_add_u32 s51, s51, %[d]
s_cmp_eq_u32 s51, %[wrap]
s_cselect_b32 s51, %[rst], s51
XS 249
M 9,42,0
s_add_u32 s40, s40, 3072
LC_ 120
v_add_f32_dpp v14, v14, v14 quad_perm:[2,3,0,1] row_mask:0xf bank_mask:0xf
s_addc_u32 s41, s41, 0
s_add_u32 m0, s38, 3072
XS 250
XS 251
M 9,43,1
XL 62,128
v_add_f32_dpp v14, v14, v14 row_half_mirror row_mask:0xf bank_mask:0xf
AW 88,248
M 10,66,0
WL 3
LO 117,18,117
LU 18,117
AW 89,249
M 10,67,1
RA 236,1,24576
HS 29,18
AW 90,250
M 11,78,0
LL 17,74
LL 121,75
AW 91,251
M 11,79,1
LA 17
DW 0
AR 52,92
M 6,8,2
LC_ 121
XL 63,192
AR 53,93
M 6,9,3
DW 1024
v_add_u32 v1, s37, v0
AR 54,94
M 7,20,2
WL 3
LO 120,16,120
LU 16,120
AR 55,95
M 7,21,3
RA 216,1,0
HZ 24,16
XS 52
M 8,32,2
LL 18,76
LL 124,77
v_cvt_f64_f32 v[14:15], v14
XS 53
XS 54
M 8,33,3
LA 18
s_mov_b64 exec, s[54:55]
SP
s_mov_b64 exec, -1
XS 55
M 9,44,2
LC_ 124
XL 64,256
AW 92,52
M 9,45,3
DW 2048
v_and_b32 v22, 0xfffffcff, v1
AW 93,53
M 10,68,2
WL 3
LO 121,17,121
LU 17,121
AW 94,54
M 10,69,3
RA 220,1,1024
HS 24,17
AW 95,55
M 11,80,2
LL 16,78
LL 125,79
AR 248,96
M 11,81,3
LA 16
RV 252,22,5120
AR 249,97
M 6,10,4
LC_ 125
XL 65,320
AR 250,98
M 6,11,5
s_lshl_b32 s53, s52, 8
v_pk_add_f16 v26, v26, v29
AR 251,99
M 7,22,4
WL 4
LO 124,18,124
LU 18,124
HX 26
XS 248
XS 249
M 7,23,5
s_add_u32 s48, %[alo], s53
RA 224,1,2048
HS 24,18
XS 250
M 8,34,4
s_addc_u32 s49, %[ahi], 0
LL 17,80
LL 106,81
s_add_u32 s52, s52, %[d]
s_cmp_eq_u32 s52, %[wrap]
s_cselect_b32 s52, %[rst], s52
XS 251
M 8,35,5
LA 17
DQ 3072
AW 96,248
M 9,46,4
LC_ 106
XL 66,384
AW 97,249
M 9,47,5
s_add_u32 s50, s50, %[d]
AW 98,250
AW 99,251
M 10,70,4
WL 3
LO 125,16,125
LU 16,125
M 10,71,5
RA 228,1,3072
HS 24,16
AR 52,100
M 11,82,4
LL 18,82
LL 107,83
AR 53,101
M 11,83,5
LA 18
HX 24
s_cmp_eq_u32 s50, %[wrap]
s_cselect_b32 s50, %[rst], s50
XL 67,448
AR 54,102
Z 0,0,0
WV 40
LC_ 107
XL 68,512
v_add_u32 v19, 1, v19
s_add_u32 s38, s38, 32768
s_cmp_eq_u32 s38, s77
s_cselect_b32 s38, s76, s38
FP
s_sub_u32 s39, 0, s39
AR 55,103
XS 52
M 0,1,1
XL 69,576
XS 53
Z 1,12,0
WL 4
LO 17,17,106
LU 106,17
XS 54
M 1,13,1
RA 232,1,4096
HZ 27,106
XS 55
Z 2,24,0
LL 16,84
LL 110,85
AW 100,52
M 2,25,1
LA 16
PK 106,17,104,33
AW 101,53
Z 3,36,0
WL 5
LC_ 110
XL 70,640
PK 104,17,104,32
AW 102,54
M 3,37,1
RA 236,1,6144
AW 103,55
M 0,2,2
WL 5
LO 18,18,107
LU 107,18
AR 248,104
M 0,3,3
FR
HS 27,107
AR 249,105
M 1,14,2
LL 17,86
LL 111,87
AR 250,106
AR 251,107
M 1,15,3
LA 17
PK 107,18,105,33
XS 248
M 2,26,2
LC_ 111
XL 71,704
PK 105,18,105,32
XS 249
M 2,27,3
s_add_u32 s42, s94, s60
RA 216,1,7168
XS 250
M 3,38,2
WL 4
LO 16,16,110
LU 110,16
XS 251
M 3,39,3
s_addc_u32 s43, s95, 0
RA 220,1,8192
HS 27,110
AW 104,248
M 0,4,4
LL 18,88
LL 114,89
AW 105,249
M 0,5,5
LA 18
PK 110,16,108,33
AW 106,250
M 1,16,4
LC_ 114
PK 108,16,108,32
AW 107,251
M 1,17,5
RA 224,1,9216
PO 110
AR 52,108
M 2,28,4
WL 5
LO 17,17,111
LU 111,17
SU 104
AR 53,109
AR 54,110
M 2,29,5
s_add_u32 s42, s94, s61
HS 27,111
v_cmp_gt_u32 vcc, v19, v15
AR 55,111
M 3,40,4
WL 3
LL 16,90
LL 115,91
XS 52
M 3,41,5
s_addc_u32 s43, s95, 0
LA 16
PK 111,17,109,33
XS 53
M 0,6,0
s_cbranch_vccnz .Lk1s18
.Lk1r18:
LC_ 115
PK 109,17,109,32
XS 54
M 0,7,1
WV 18
RA 228,1,10240
PO 111
XS 55
BW 60
M 1,18,0
WL 5
LO 18,18,114
LU 114,18
AW 108,52
M 1,19,1
RA 232,1,11264
)"
R"(RV 40,8,6144
HZ 29,114
AW 109,53
M 2,30,0
RV 44,8,6208
LL 17,92
LL 118,93
AW 110,54
M 2,31,1
LA 17
RV 48,8,6272
PK 114,18,112,33
AW 111,55
M 3,42,0
WL 6
LC_ 118
BR 60
PK 112,18,112,32
AR 248,112
AR 249,113
M 3,43,1
WV 16
RV 252,22,5392
ds_read_b64 v[14:15], v5 offset:0
W1 24,0
W1 26,2048
BW 61
AR 250,114
M 0,8,2
BR 61
LO 16,16,115
LU 115,16
AR 251,115
M 0,9,3
WL 6
RA 236,1,12288
HS 29,115
XS 248
LZ 40,60
WV 15
BW 62
M 1,20,2
LL 18,94
LL 119,95
XS 249
M 1,21,3
LA 18
RV 40,8,6336
PK 115,16,113,33
XS 250
M 2,32,2
LC_ 119
BR 62
PK 113,16,113,32
XS 251
M 2,33,3
WL 6
RA 216,1,13312
AW 112,248
LS 44,61
WV 13
BW 63
M 3,44,2
WL 7
LO 17,17,118
LU 118,17
SU 108
AW 113,249
M 3,45,3
s_add_u32 s42, s94, s62
RA 220,1,14336
HS 29,118
s_addc_u32 s43, s95, 0
RV 44,8,6400
AW 114,250
M 0,10,4
BR 63
LL 16,96
LL 122,97
AW 115,251
AR 52,116
M 0,11,5
WL 5
LA 16
PK 118,17,116,33
AR 53,117
LS 48,62
WV 11
BW 64
M 1,22,4
LC_ 122
PK 116,17,116,32
AR 54,118
M 1,23,5
RA 224,1,15360
RV 48,8,6464
PO 118
AR 55,119
M 2,34,4
LO 18,18,119
LU 119,18
SU 112
XS 52
M 2,35,5
s_add_u32 s42, s94, s63
BR 64
HS 29,119
XS 53
M 3,46,4
WL 6
LL 17,98
LL 123,99
XS 54
M 3,47,5
s_addc_u32 s43, s95, 0
LA 17
PK 119,18,117,33
XS 55
LS 40,63
WV 10
BW 65
Z 6,0,0
LC_ 123
PK 117,18,117,32
v_add_f32 v14, v14, v15
s_nop 1
v_add_f32_dpp v14, v14, v14 quad_perm:[1,0,3,2] row_mask:0xf bank_mask:0xf
s_nop 1
v_add_f32_dpp v14, v14, v14 quad_perm:[2,3,0,1] row_mask:0xf bank_mask:0xf
s_nop 1
v_add_f32_dpp v14, v14, v14 row_half_mirror row_mask:0xf bank_mask:0xf
v_cvt_f64_f32 v[14:15], v14
s_mov_b64 exec, s[54:55]
SP
s_mov_b64 exec, -1
s_nop 0
ds_read_b64 v[14:15], v5 offset:2048
RV 40,8,6528
AW 116,52
M 6,1,1
WL 5
RA 228,1,16384
BR 65
PO 119
AW 117,53
LS 44,64
WV 9
BW 66
Z 7,12,0
LO 16,16,122
LU 122,16
AW 118,54
AW 119,55
M 7,13,1
s_mul_i32 s53, s50, 25600
RA 232,1,17408
HZ 25,122
Z 8,24,0
s_add_u32 s40, %[wlo], s53
RV 44,8,6592
LL 18,100
LL 126,101
M 8,25,1
s_addc_u32 s41, %[whi], 0
LA 18
BR 66
PK 122,16,120,33
v_pk_add_f16 v27, v27, v29
Z 9,36,0
WL 5
LC_ 126
PK 120,16,120,32
HX 27
M 9,37,1
s_mov_b32 m0, s38
RA 236,1,18432
v_subrev_u32 v5, s39, v5
LS 48,65
WV 8
BW 67
M 6,2,2
RV 48,8,6656
LO 17,17,123
LU 123,17
M 6,3,3
DW 0
BR 67
HS 25,123
M 7,14,2
LL 16,102
LL 127,103
M 7,15,3
WL 5
LA 16
PK 123,17,121,33
LS 40,66
WV 8
BW 68
M 8,26,2
LC_ 127
RV 40,8,6720
PK 121,17,121,32
M 8,27,3
WV 7
RA 216,1,19456
BR 68
M 9,38,2
WL 6
LO 18,18,126
LU 126,18
SU 116
M 9,39,3
s_add_u32 s42, s44, s64
RA 220,1,20480
HS 25,126
s_addc_u32 s43, s45, 0
LS 44,67
s_add_u32 s37, s37, 32768
BW 69
M 6,4,4
s_cmp_eq_u32 s37, s75
LL 17,56
LL 104,57
s_cselect_b32 s37, s74, s37
M 6,5,5
LA 17
PK 126,18,124,33
v_add_f32 v14, v14, v15
s_nop 1
v_add_f32_dpp v14, v14, v14 quad_perm:[1,0,3,2] row_mask:0xf bank_mask:0xf
s_nop 1
v_add_f32_dpp v14, v14, v14 quad_perm:[2,3,0,1] row_mask:0xf bank_mask:0xf
s_nop 1
v_add_f32_dpp v14, v14, v14 row_half_mirror row_mask:0xf bank_mask:0xf
v_cvt_f64_f32 v[14:15], v14
s_mov_b64 exec, s[54:55]
SP
s_mov_b64 exec, -1
DW 1024
RV 44,8,6784
M 7,16,4
LC_ 104
BR 69
PK 124,18,124,32
M 7,17,5
WL 6
RA 224,1,21504
PO 126
LS 48,68
WV 9
BW 70
M 8,28,4
LO 16,16,127
LU 127,16
ST 120
M 8,29,5
WV 9
DW 2048
RV 48,8,6848
HS 25,127
M 9,40,4
WL 3
RA 240,7,0
LL 18,58
LL 105,59
M 9,41,5
s_add_u32 s40, s40, 3072
LA 18
BR 70
PK 127,16,125,33
M 6,6,0
s_addc_u32 s41, s41, 0
LC_ 105
PK 125,16,125,32
s_add_u32 m0, s38, 3072
M 6,7,1
s_add_u32 s42, s44, s65
RA 228,1,22528
PO 127
LS 40,69
s_addc_u32 s43, s45, 0
BW 71
M 7,18,0
RA 244,7,256
LO 104,17,104
LU 17,104
M 7,19,1
RA 232,1,23552
BR 71
HZ 26,17
M 8,30,0
WL 6
DW 0
LL 16,60
LL 108,61
M 8,31,1
s_lshl_b32 s53, s52, 8
LA 16
HX 25
LS 44,70
s_add_u32 s48, %[alo], s53
RA 248,7,512
M 9,42,0
WL 2
LC_ 108
W1 25,1024
W1 27,3072
M 9,43,1
s_addc_u32 s49, %[ahi], 0
RA 236,1,24576
v_add_u32 v4, s39, v4
LS 48,71
s_add_u32 s52, s52, %[d]
RA 252,7,768
M 6,8,2
LO 105,18,105
LU 18,105
s_cmp_eq_u32 s52, %[wrap]
s_cselect_b32 s52, %[rst], s52
DW 1024
v_mul_f32 v36, 0xbb2aaaab, v36
M 6,9,3
DW 2048
HS 26,18
v_mul_f32 v37, 0xbb2aaaab, v37
v_mul_f32 v38, 0xbb2aaaab, v38
M 7,20,2
LL 17,62
LL 109,63
v_mul_f32 v39, 0xbb2aaaab, v39
M 7,21,3
LA 17
ST 124
LK 40,240,60,0
LK 44,242,60,2
s_mul_i32 s53, s51, 24
RA 240,7,1024
v_add_u32 v1, s37, v0
LK 48,244,61,0
CF 40,40,41
LK 248,246,61,2
s_bitcmp1_b32 s51, 0
RA 244,7,1280
CF 42,42,43
M 8,32,2
s_addc_u32 s53, s53, 0
LC_ 109
RA 216,1,0
M 8,33,3
s_lshl_b32 s53, s53, 11
v_and_b32 v22, 0xfffffcff, v1
CF 44,44,45
CF 46,46,47
M 9,44,2
WL 6
LO 108,16,108
LU 16,108
AW 120,40
AW 121,42
M 9,45,3
s_bitcmp1_b64 s[92:93], s51
RA 220,1,1024
HS 26,16
s_cselect_b64 s[94:95], s[88:89], s[86:87]
s_add_u32 s94, s94, s53
s_addc_u32 s95, s95, 0
s_add_u32 s51, s51, %[d]
s_cmp_eq_u32 s51, %[wrap]
s_cselect_b32 s51, %[rst], s51
DQ 3072
AW 122,44
AW 123,46
LK 40,248,62,0
CF 48,48,49
LK 44,250,62,2
s_add_u32 s50, s50, %[d]
RA 248,7,1536
CF 50,50,51
GZ 60
s_cmp_eq_u32 s50, %[wrap]
CF 248,248,249
CF 250,250,251
M 6,10,4
s_cselect_b32 s50, %[rst], s50
LL 18,64
LL 112,65
M 6,11,5
LA 18
RV 252,22,5120
M 7,22,4
WL 7
LC_ 112
v_add_u32 v19, 1, v19
AW 124,48
M 7,23,5
s_add_u32 s38, s38, 32768
RA 224,1,2048
AW 125,50
AW 126,248
AW 127,250
)"
R"(LK 48,252,63,0
CF 40,40,41
LK 248,254,63,2
s_cmp_eq_u32 s38, s77
RA 252,7,1792
CF 42,42,43
GS 61
s_cselect_b32 s38, s76, s38
CF 44,44,45
CF 46,46,47
M 8,34,4
WL 7
LO 109,17,109
LU 17,109
M 8,35,5
FP
HS 26,17
M 9,46,4
WL 4
LL 16,66
LL 113,67
M 9,47,5
s_sub_u32 s39, 0, s39
LA 16
RA 228,1,3072
AW 128,40
AW 129,42
Z 0,0,0
LC_ 113
AW 130,44
AW 131,46
M 0,1,1
LK 40,240,64,0
CF 48,48,49
LK 44,242,64,2
RA 240,7,2048
CF 50,50,51
GS 62
CF 248,248,249
CF 250,250,251
Z 1,12,0
WL 7
LO 112,18,112
LU 18,112
M 1,13,1
RA 232,1,4096
HZ 29,18
AW 132,48
Z 2,24,0
LL 17,68
LL 116,69
AW 133,50
AW 134,248
M 2,25,1
LA 17
FR
AW 135,250
LK 48,244,65,0
CF 40,40,41
LK 248,246,65,2
RA 244,7,2304
CF 42,42,43
GS 63
CF 44,44,45
CF 46,46,47
Z 10,36,0
WL 6
LC_ 116
AW 136,40
M 10,37,1
RA 236,1,6144
AW 137,42
M 0,2,2
WL 7
LO 113,16,113
LU 16,113
M 0,3,3
HS 29,16
AW 138,44
AW 139,46
LK 40,248,66,0
CF 48,48,49
LK 44,250,66,2
RA 248,7,2560
CF 50,50,51
GS 64
CF 248,248,249
CF 250,250,251
M 1,14,2
LL 18,70
LL 117,71
M 1,15,3
LA 18
RA 216,1,7168
M 2,26,2
WL 6
LC_ 117
v_cmp_gt_u32 vcc, v19, v15
M 2,27,3
AW 140,48
AW 141,50
AW 142,248
M 10,38,2
WL 4
LO 116,17,116
LU 17,116
AW 143,250
M 10,39,3
RA 220,1,8192
HS 29,17
LK 48,252,67,0
CF 40,40,41
LK 248,254,67,2
RA 252,7,2816
CF 42,42,43
GS 65
CF 44,44,45
CF 46,46,47
M 0,4,4
s_cbranch_vccnz .Lk1s19
.Lk1r19:
LL 16,80
LL 106,81
M 0,5,5
LA 16
ds_read_b64 v[14:15], v5 offset:0
AW 144,40
AW 145,42
M 1,16,4
LC_ 106
AW 146,44
AW 147,46
M 1,17,5
RA 224,1,9216
LK 40,240,68,0
CF 48,48,49
LK 44,242,68,2
CF 50,50,51
GS 66
CF 248,248,249
CF 250,250,251
M 2,28,4
WL 6
LO 117,18,117
LU 18,117
M 2,29,5
HS 29,18
AW 148,48
M 10,40,4
WL 5
LL 17,82
LL 107,83
AW 149,50
AW 150,248
AW 151,250
M 10,41,5
LA 17
RA 228,1,10240
LK 48,244,69,0
CF 40,40,41
LK 248,246,69,2
CF 42,42,43
v_pk_add_f16 v26, v26, v29
GS 67
CF 44,44,45
CF 46,46,47
M 0,6,0
LC_ 107
HX 26
M 0,7,1
W1 26,2048
AW 152,40
M 1,18,0
WL 5
LO 16,16,106
LU 106,16
AW 153,42
AW 154,44
M 1,19,1
RA 232,1,11264
HZ 27,106
AW 155,46
LK 40,248,70,0
CF 48,48,49
LK 44,250,70,2
CF 50,50,51
v_add_f32 v14, v14, v15
GS 68
CF 248,248,249
CF 250,250,251
M 2,30,0
LL 18,84
LL 110,85
M 2,31,1
LA 18
PK 106,16,104,33
M 10,42,0
WL 4
LC_ 110
PK 104,16,104,32
AW 156,48
AW 157,50
AW 158,248
M 10,43,1
RV 252,22,5392
v_add_f32_dpp v14, v14, v14 quad_perm:[1,0,3,2] row_mask:0xf bank_mask:0xf
AW 159,250
LK 48,252,71,0
CF 40,40,41
LK 248,254,71,2
CF 42,42,43
v_add_f32_dpp v14, v14, v14 quad_perm:[2,3,0,1] row_mask:0xf bank_mask:0xf
GS 69
CF 44,44,45
CF 46,46,47
M 0,8,2
WL 5
LO 17,17,107
LU 107,17
M 0,9,3
s_add_u32 s42, s94, s60
RA 236,1,12288
HS 27,107
M 1,20,2
s_addc_u32 s43, s95, 0
LL 16,86
LL 111,87
AW 160,40
AW 161,42
M 1,21,3
LA 16
RV 36,8,6912
PK 107,17,105,33
AW 162,44
AW 163,46
M 2,32,2
LC_ 111
PK 105,17,105,32
M 2,33,3
RA 216,1,13312
v_add_f32_dpp v14, v14, v14 row_half_mirror row_mask:0xf bank_mask:0xf
GS 70
CF 48,48,49
CF 50,50,51
CF 248,248,249
CF 250,250,251
M 10,44,2
WL 4
LO 18,18,110
LU 110,18
AW 164,48
AW 165,50
AW 166,248
M 10,45,3
RA 220,1,14336
HS 27,110
AW 167,250
s_nop 1
GS 71
SU 104
v_cvt_f64_f32 v[14:15], v14
s_nop 5
v_cndmask_b32 v52, v52, v53, s[78:79]
M 0,10,4
LL 17,88
LL 114,89
v_cndmask_b32 v54, v54, v55, s[78:79]
M 0,11,5
LA 17
PK 110,18,108,33
v_cndmask_b32 v52, v52, v54, s[80:81]
M 1,22,4
LC_ 114
PK 108,18,108,32
v_mul_f32 v52, 0x3b2aaaab, v52
M 1,23,5
WL 5
RA 224,1,15360
PO 110
v_med3_f32 v53, v52, v39, v37
M 2,34,4
WL 5
LO 16,16,111
LU 111,16
s_mov_b64 exec, s[54:55]
SP
s_mov_b64 exec, -1
LG 52,53
s_nop 0
C 52,52,52
M 2,35,5
s_add_u32 s42, s94, s61
ds_read_b64 v[14:15], v5 offset:2048
HS 27,111
v_lshlrev_b32 v52, 16, v52
M 10,46,4
WL 4
LL 18,90
LL 115,91
v_add_f32 v52, v36, v52
M 10,47,5
s_addc_u32 s43, s95, 0
LA 18
PK 111,16,109,33
C 52,52,52
Z 6,0,0
LC_ 115
PK 109,16,109,32
v_lshlrev_b32 v52, 16, v52
M 6,1,1
RA 228,1,16384
PO 111
v_sqrt_f32 v52, v52
s_nop 0
C 52,52,52
Z 7,12,0
WL 5
LO 17,17,114
LU 114,17
v_lshlrev_b32 v52, 16, v52
M 7,13,1
RA 232,1,17408
HZ 29,114
v_rcp_f32 v52, v52
Z 8,24,0
LL 16,92
LL 118,93
v_mul_f32 v52, v38, v52
M 8,25,1
LA 16
PK 114,17,112,33
v_subrev_u32 v5, s39, v5
CF 52,52,52
Z 11,36,0
WL 2
LC_ 118
PK 112,17,112,32
ds_bpermute_b32 v13, v28, v52
M 11,37,1
RA 236,1,18432
v_add_f32 v14, v14, v15
M 6,2,2
LO 18,18,115
LU 115,18
M 6,3,3
SU 108
HS 29,115
v_add_f32_dpp v14, v14, v14 quad_perm:[1,0,3,2] row_mask:0xf bank_mask:0xf
M 7,14,2
s_mul_i32 s53, s50, 25600
LL 17,94
LL 119,95
s_add_u32 s40, %[wlo], s53
s_addc_u32 s41, %[whi], 0
s_mov_b32 m0, s38
M 7,15,3
LA 17
PK 115,18,113,33
v_add_f32_dpp v14, v14, v14 quad_perm:[2,3,0,1] row_mask:0xf bank_mask:0xf
M 8,26,2
LC_ 119
PK 113,18,113,32
M 8,27,3
RA 216,1,19456
DW 0
v_add_f32_dpp v14, v14, v14 row_half_mirror row_mask:0xf bank_mask:0xf
M 11,38,2
WL 3
LO 16,16,118
LU 118,16
M 11,39,3
s_add_u32 s42, s94, s62
RA 220,1,20480
HS 29,118
v_cvt_f64_f32 v[14:15], v14
s_addc_u32 s43, s95, 0
AR 248,120
M 6,4,4
s_add_u32 s37, s37, 32768
LL 18,56
LL 104,57
s_cmp_eq_u32 s37, s75
s_cselect_b32 s37, s74, s37
AR 249,121
M 6,5,5
LA 18
PK 118,16,116,33
s_mov_b64 exec, s[54:55]
SP
s_mov_b64 exec, -1
DW 1024
AR 250,122
M 7,16,4
LC_ 104
PK 116,16,116,32
AR 251,123
AR 52,124
M 7,17,5
RA 224,1,21504
PO 118
)"
R"(AR 53,125
M 8,28,4
WL 5
LO 17,17,119
LU 119,17
SU 112
AR 54,126
AR 55,127
M 8,29,5
DW 2048
HS 29,119
XS 248
M 11,40,4
WL 3
LL 16,58
LL 105,59
XS 249
M 11,41,5
s_add_u32 s40, s40, 3072
LA 16
PK 119,17,117,33
XS 250
XS 251
M 6,6,0
s_addc_u32 s41, s41, 0
LC_ 105
PK 117,17,117,32
s_add_u32 m0, s38, 3072
AW 120,248
M 6,7,1
s_add_u32 s42, s94, s63
RA 228,1,22528
PO 119
AW 121,249
AW 122,250
M 7,18,0
WL 4
LO 104,18,104
LU 18,104
AW 123,251
M 7,19,1
s_addc_u32 s43, s95, 0
RA 232,1,23552
HZ 26,18
XS 52
XS 53
M 8,30,0
DW 0
LL 17,60
LL 108,61
XS 54
M 8,31,1
s_lshl_b32 s53, s52, 8
LA 17
v_pk_add_f16 v27, v27, v29
XS 55
M 11,42,0
WL 2
LC_ 108
HX 27
AW 124,52
AW 125,53
M 11,43,1
s_add_u32 s48, %[alo], s53
RA 236,1,24576
W1 27,3072
AW 126,54
M 6,8,2
LO 105,16,105
LU 16,105
s_addc_u32 s49, %[ahi], 0
s_add_u32 s52, s52, %[d]
s_cmp_eq_u32 s52, %[wrap]
s_cselect_b32 s52, %[rst], s52
DW 1024
AW 127,55
v_min_u32 v35, v35, v13
M 6,9,3
SU 116
HS 26,16
AR 248,128
M 7,20,2
s_mul_i32 s53, s51, 24
LL 18,62
LL 109,63
s_bitcmp1_b32 s51, 0
s_addc_u32 s53, s53, 0
s_lshl_b32 s53, s53, 11
s_bitcmp1_b64 s[92:93], s51
s_cselect_b64 s[94:95], s[88:89], s[86:87]
s_add_u32 s94, s94, s53
s_addc_u32 s95, s95, 0
s_add_u32 s51, s51, %[d]
s_cmp_eq_u32 s51, %[wrap]
s_cselect_b32 s51, %[rst], s51
AR 249,129
AR 250,130
M 7,21,3
LA 18
DW 2048
AR 251,131
M 8,32,2
LC_ 109
DQ 3072
XS 248
M 8,33,3
s_add_u32 s50, s50, %[d]
v_add_u32 v1, s37, v0
v_add_u32 v4, s39, v4
XS 249
XS 250
M 11,44,2
WL 3
LO 108,17,108
LU 17,108
XS 251
M 11,45,3
s_cmp_eq_u32 s50, %[wrap]
RA 216,1,0
HS 26,17
AW 128,248
AW 129,249
M 6,10,4
s_cselect_b32 s50, %[rst], s50
LL 16,64
LL 112,65
AW 130,250
M 6,11,5
LA 16
v_and_b32 v22, 0xfffffcff, v1
AW 131,251
AR 52,132
M 7,22,4
LC_ 112
RA 220,1,1024
AR 53,133
M 7,23,5
RV 252,22,5120
v_add_u32 v19, 1, v19
AR 54,134
M 8,34,4
WL 5
LO 109,18,109
LU 18,109
AR 55,135
XS 52
M 8,35,5
s_add_u32 s38, s38, 32768
RA 224,1,2048
HS 26,18
XS 53
M 11,46,4
WL 1
LL 17,66
LL 113,67
XS 54
XS 55
M 11,47,5
s_cmp_eq_u32 s38, s77
LA 17
RA 228,1,3072
s_cselect_b32 s38, s76, s38
FP
s_sub_u32 s39, 0, s39
AW 132,52
Z 0,0,0
LC_ 113
AW 133,53
AW 134,54
M 0,1,1
AW 135,55
AR 248,136
Z 1,12,0
LO 112,16,112
LU 16,112
M 1,13,1
RA 232,1,4096
HZ 29,16
AR 249,137
AR 250,138
Z 2,24,0
LL 18,96
LL 116,97
AR 251,139
M 2,25,1
LA 18
XS 248
XS 249
Z 3,36,0
LC_ 116
XS 250
M 3,37,1
XS 251
AW 136,248
Z 4,48,0
WL 3
LO 113,17,113
LU 17,113
AW 137,249
M 4,49,1
RA 236,1,6144
HS 29,17
AW 138,250
Z 5,60,0
LL 16,98
LL 117,99
AW 139,251
AR 52,140
M 5,61,1
LA 16
AR 53,141
M 0,2,2
LC_ 117
AR 54,142
AR 55,143
M 0,3,3
XS 52
XS 53
M 1,14,2
WL 3
LO 116,18,116
LU 18,116
XS 54
M 1,15,3
RA 216,1,7168
HS 29,18
XS 55
M 2,26,2
LL 17,80
LL 106,81
AW 140,52
M 2,27,3
LA 17
FR
AW 141,53
AW 142,54
M 3,38,2
LC_ 106
AW 143,55
M 3,39,3
AR 248,144
AR 249,145
M 4,50,2
WL 4
LO 117,16,117
LU 16,117
AR 250,146
M 4,51,3
RA 220,1,8192
HS 29,16
AR 251,147
M 5,62,2
LL 18,82
LL 107,83
XS 248
XS 249
M 5,63,3
LA 18
v_pk_add_f16 v26, v26, v29
XS 250
M 0,4,4
WL 3
LC_ 107
v_cmp_gt_u32 vcc, v19, v15
XS 251
AW 144,248
M 0,5,5
HX 26
AW 145,249
M 1,16,4
WL 3
LO 17,17,106
LU 106,17
AW 146,250
AW 147,251
M 1,17,5
RA 224,1,9216
HZ 27,106
AR 52,148
M 2,28,4
s_cbranch_vccnz .Lk1s20
.Lk1r20:
LL 16,84
LL 110,85
AR 53,149
M 2,29,5
LA 16
PK 106,17,104,33
AR 54,150
AR 55,151
M 3,40,4
LC_ 110
PK 104,17,104,32
XS 52
M 3,41,5
W1 26,2048
XS 53
XS 54
M 4,52,4
WL 4
LO 18,18,107
LU 107,18
XS 55
M 4,53,5
s_add_u32 s42, s94, s60
RA 228,1,10240
HS 27,107
AW 148,52
AW 149,53
M 5,64,4
LL 17,86
LL 111,87
AW 150,54
M 5,65,5
s_addc_u32 s43, s95, 0
LA 17
PK 107,18,105,33
AW 151,55
M 0,6,0
LC_ 111
PK 105,18,105,32
AR 248,152
AR 249,153
M 0,7,1
SU 104
AR 250,154
M 1,18,0
WL 4
LO 16,16,110
LU 110,16
AR 251,155
XS 248
M 1,19,1
RA 232,1,11264
HS 27,110
XS 249
M 2,30,0
LL 18,88
LL 114,89
XS 250
XS 251
M 2,31,1
LA 18
PK 110,16,108,33
AW 152,248
M 3,42,0
LC_ 114
PK 108,16,108,32
AW 153,249
M 3,43,1
RV 252,22,5392
PO 110
AW 154,250
AW 155,251
M 4,54,0
WL 4
LO 17,17,111
LU 111,17
AR 52,156
M 4,55,1
s_add_u32 s42, s94, s61
RA 236,1,12288
HS 27,111
AR 53,157
AR 54,158
M 5,66,0
LL 16,90
LL 115,91
AR 55,159
M 5,67,1
s_addc_u32 s43, s95, 0
LA 16
PK 111,17,109,33
XS 52
XS 53
M 0,8,2
LC_ 115
PK 109,17,109,32
XS 54
M 0,9,3
PO 111
XS 55
M 1,20,2
WL 4
LO 18,18,114
LU 114,18
AW 156,52
AW 157,53
M 1,21,3
RA 216,1,13312
HZ 29,114
AW 158,54
M 2,32,2
LL 17,100
LL 118,101
AW 159,55
AR 248,160
M 2,33,3
LA 17
PK 114,18,112,33
)"
R"(AR 249,161
M 3,44,2
LC_ 118
PK 112,18,112,32
AR 250,162
AR 251,163
M 3,45,3
SU 108
XS 248
M 4,56,2
WL 3
LO 16,16,115
LU 115,16
XS 249
M 4,57,3
s_add_u32 s42, s94, s62
RA 220,1,14336
HS 29,115
XS 250
XS 251
M 5,68,2
LL 18,102
LL 119,103
AW 160,248
M 5,69,3
s_addc_u32 s43, s95, 0
LA 18
PK 115,16,113,33
AW 161,249
AW 162,250
M 0,10,4
LC_ 119
PK 113,16,113,32
AW 163,251
M 0,11,5
SU 112
AR 52,164
AR 53,165
M 1,22,4
WL 3
LO 17,17,118
LU 118,17
AR 54,166
M 1,23,5
RA 224,1,15360
HS 29,118
AR 55,167
M 2,34,4
LL 16,56
LL 104,57
XS 52
XS 53
M 2,35,5
LA 16
PK 118,17,116,33
XS 54
M 3,46,4
LC_ 104
PK 116,17,116,32
XS 55
AW 164,52
M 3,47,5
ds_read_b64 v[14:15], v5 offset:2048
PO 118
AW 165,53
M 4,58,4
WL 4
LO 18,18,119
LU 119,18
AW 166,54
AW 167,55
M 4,59,5
s_add_u32 s42, s94, s63
RA 228,1,16384
HS 29,119
M 5,70,4
LL 17,58
LL 105,59
M 5,71,5
s_addc_u32 s43, s95, 0
LA 17
PK 119,18,117,33
Z 6,0,0
LC_ 105
PK 117,18,117,32
M 6,1,1
PO 119
v_subrev_u32 v5, s39, v5
Z 7,12,0
WL 4
LO 104,16,104
LU 16,104
M 7,13,1
RA 232,1,17408
HZ 26,16
Z 8,24,0
LL 18,60
LL 108,61
M 8,25,1
WL 4
LA 18
SU 116
Z 9,36,0
s_mul_i32 s53, s50, 25600
LC_ 108
v_add_f32 v14, v14, v15
M 9,37,1
s_add_u32 s40, %[wlo], s53
v_add_f32_dpp v14, v14, v14 quad_perm:[1,0,3,2] row_mask:0xf bank_mask:0xf
v_pk_add_f16 v27, v27, v29
Z 10,48,0
WL 3
LO 105,17,105
LU 17,105
M 10,49,1
s_addc_u32 s41, %[whi], 0
RA 236,1,18432
HS 26,17
Z 11,60,0
LL 16,62
LL 109,63
M 11,61,1
s_mov_b32 m0, s38
LA 16
DW 0
M 6,2,2
LC_ 109
v_add_f32_dpp v14, v14, v14 quad_perm:[2,3,0,1] row_mask:0xf bank_mask:0xf
M 6,3,3
s_nop 0
v_add_f32_dpp v14, v14, v14 row_half_mirror row_mask:0xf bank_mask:0xf
HX 27
M 7,14,2
WL 3
LO 108,18,108
LU 18,108
M 7,15,3
s_add_u32 s37, s37, 32768
RA 216,1,19456
HS 26,18
W1 27,3072
M 8,26,2
s_cmp_eq_u32 s37, s75
LL 17,64
LL 112,65
M 8,27,3
s_cselect_b32 s37, s74, s37
LA 17
DW 1024
M 9,38,2
LC_ 112
v_cvt_f64_f32 v[14:15], v14
M 9,39,3
s_mov_b64 exec, s[54:55]
SP
s_mov_b64 exec, -1
v_add_u32 v4, s39, v4
M 10,50,2
WL 4
LO 109,16,109
LU 16,109
M 10,51,3
RA 220,1,20480
HS 26,16
M 11,62,2
LL 18,66
LL 113,67
M 11,63,3
LA 18
DW 2048
M 6,4,4
LC_ 113
M 6,5,5
M 7,16,4
WL 3
LO 112,17,112
LU 17,112
M 7,17,5
s_mul_i32 s53, s51, 24
RA 224,1,21504
HZ 29,17
M 8,28,4
s_bitcmp1_b32 s51, 0
LL 16,68
LL 116,69
M 8,29,5
s_addc_u32 s53, s53, 0
LA 16
M 9,40,4
s_lshl_b32 s53, s53, 11
LC_ 116
M 9,41,5
s_bitcmp1_b64 s[92:93], s51
M 10,52,4
WL 3
LO 113,18,113
LU 18,113
M 10,53,5
s_cselect_b64 s[94:95], s[88:89], s[86:87]
RA 228,1,22528
HS 29,18
M 11,64,4
LL 17,70
LL 117,71
M 11,65,5
s_add_u32 s94, s94, s53
LA 17
M 6,6,0
s_addc_u32 s95, s95, 0
LC_ 117
M 6,7,1
s_add_u32 s51, s51, %[d]
M 7,18,0
WL 3
LO 116,16,116
LU 16,116
M 7,19,1
s_cmp_eq_u32 s51, %[wrap]
RA 232,1,23552
HS 29,16
M 8,30,0
s_cselect_b32 s51, %[rst], s51
LL 18,72
LL 120,73
M 8,31,1
s_mul_i32 s53, %[d], 32768
LA 18
s_add_u32 s64, s53, %[sb1]
s_add_u32 s65, s64, 512
M 9,42,0
s_add_u32 s40, s40, 3072
LC_ 120
s_addc_u32 s41, s41, 0
s_add_u32 m0, s38, 3072
M 9,43,1
DW 0
M 10,54,0
WL 3
LO 117,17,117
LU 17,117
M 10,55,1
RA 236,1,24576
HS 29,17
M 11,66,0
LL 16,74
LL 121,75
M 11,67,1
LA 16
DW 1024
M 6,8,2
LC_ 121
DW 2048
M 6,9,3
s_lshl_b32 s53, s52, 8
v_add_u32 v1, s37, v0
M 7,20,2
WL 3
LO 120,18,120
LU 18,120
M 7,21,3
s_add_u32 s48, %[alo], s53
RA 216,1,0
HZ 30,18
M 8,32,2
s_addc_u32 s49, %[ahi], 0
LL 17,76
LL 124,77
M 8,33,3
s_add_u32 s52, s52, %[d]
LA 17
v_and_b32 v22, 0xfffffcff, v1
M 9,44,2
s_cmp_eq_u32 s52, %[wrap]
LC_ 124
M 9,45,3
s_cselect_b32 s52, %[rst], s52
DQ 3072
M 10,56,2
WL 3
LO 121,16,121
LU 16,121
M 10,57,3
s_add_u32 s50, s50, %[d]
RA 220,1,1024
HS 30,16
M 11,68,2
LL 18,78
LL 125,79
M 11,69,3
s_cmp_eq_u32 s50, %[wrap]
LA 18
RV 252,22,5120
M 6,10,4
s_cselect_b32 s50, %[rst], s50
LC_ 125
M 6,11,5
WV 25
v_add_u32 v19, 1, v19
M 7,22,4
WL 4
LO 124,17,124
LU 17,124
M 7,23,5
s_add_u32 s38, s38, 32768
RA 224,1,2048
HS 30,17
M 8,34,4
s_cmp_eq_u32 s38, s77
LL 16,80
LL 106,81
M 8,35,5
s_cselect_b32 s38, s76, s38
LA 16
FP
M 9,46,4
s_sub_u32 s39, 0, s39
LC_ 106
M 9,47,5
M 10,58,4
WL 4
LO 125,18,125
LU 18,125
M 10,59,5
RA 228,1,3072
HS 30,18
M 11,70,4
LL 17,82
LL 107,83
M 11,71,5
LA 17
v_pk_add_f16 v26, v26, v30
Z 0,0,0
LC_ 107
v_pk_add_f16 v26, v26, v29
M 0,1,1
HX 26
Z 1,12,0
WL 3
LO 16,16,106
LU 106,16
M 1,13,1
RA 232,1,4096
HZ 27,106
Z 2,24,0
LL 18,84
LL 110,85
M 2,25,1
LA 18
PK 106,16,104,33
Z 3,36,0
LC_ 110
PK 104,16,104,32
M 3,37,1
Z 4,48,0
WL 3
LO 17,17,107
LU 107,17
M 4,49,1
s_add_u32 s42, s94, s60
RA 236,1,6144
HS 27,107
Z 5,60,0
LL 16,86
LL 111,87
M 5,61,1
s_addc_u32 s43, s95, 0
LA 16
PK 107,17,105,33
M 0,2,2
LC_ 111
PK 105,17,105,32
M 0,3,3
SU 104
M 1,14,2
WL 3
LO 18,18,110
LU 110,18
M 1,15,3
RA 216,1,7168
HS 27,110
M 2,26,2
LL 17,88
LL 114,89
M 2,27,3
LA 17
PK 110,18,108,33
M 3,38,2
LC_ 114
PK 108,18,108,32
M 3,39,3
FR
PO 110
M 4,50,2
WL 4
LO 16,16,111
LU 111,16
M 4,51,3
s_add_u32 s42, s94, s61
RA 220,1,8192
HS 27,111
)"
R"(M 5,62,2
LL 18,90
LL 115,91
M 5,63,3
s_addc_u32 s43, s95, 0
LA 18
PK 111,16,109,33
M 0,4,4
PFN
LC_ 115
PK 109,16,109,32
M 0,5,5
s_mov_b32 m0, %[pm]
s_nop 0
DF
PO 111
M 1,16,4
WL 4
LO 17,17,114
LU 114,17
M 1,17,5
RA 224,1,9216
HZ 29,114
M 2,28,4
LL 16,92
LL 118,93
M 2,29,5
LA 16
PK 114,17,112,33
M 3,40,4
LC_ 118
PK 112,17,112,32
M 3,41,5
WL 6
SU 108
v_cmp_gt_u32 vcc, v19, v15
M 4,52,4
WL 3
LO 18,18,115
LU 115,18
M 4,53,5
s_add_u32 s42, s94, s62
RA 228,1,10240
HS 29,115
M 5,64,4
LL 17,94
LL 119,95
M 5,65,5
s_cbranch_vccnz .Lk1s21
.Lk1r21:
LA 17
PK 115,18,113,33
M 0,6,0
s_addc_u32 s43, s95, 0
LC_ 119
PK 113,18,113,32
M 0,7,1
W1 26,2048
M 1,18,0
WL 4
LO 16,16,118
LU 118,16
M 1,19,1
RA 232,1,11264
HS 29,118
M 2,30,0
LL 18,96
LL 122,97
M 2,31,1
LA 18
PK 118,16,116,33
M 3,42,0
LC_ 122
PK 116,16,116,32
M 3,43,1
RV 252,22,5392
PO 118
M 4,54,0
WL 5
LO 17,17,119
LU 119,17
M 4,55,1
RA 236,1,12288
HS 29,119
M 5,66,0
LL 16,98
LL 123,99
M 5,67,1
LA 16
PK 119,17,117,33
M 0,8,2
LC_ 123
PK 117,17,117,32
M 0,9,3
SU 112
PO 119
M 1,20,2
WL 4
LO 18,18,122
LU 122,18
M 1,21,3
s_add_u32 s42, s94, s63
RA 216,1,13312
HZ 30,122
M 2,32,2
s_addc_u32 s43, s95, 0
LL 17,100
LL 126,101
M 2,33,3
LA 17
PK 122,18,120,33
M 3,44,2
LC_ 126
PK 120,18,120,32
M 3,45,3
SU 116
M 4,56,2
WL 3
LO 16,16,123
LU 123,16
M 4,57,3
s_add_u32 s42, s94, s64
RA 220,1,14336
HS 30,123
M 5,68,2
LL 18,102
LL 127,103
M 5,69,3
s_addc_u32 s43, s95, 0
LA 18
PK 123,16,121,33
M 0,10,4
LC_ 127
PK 121,16,121,32
M 0,11,5
SU 120
M 1,22,4
WL 3
LO 17,17,126
LU 126,17
M 1,23,5
RA 224,1,15360
HS 30,126
M 2,34,4
LL 16,56
LL 104,57
M 2,35,5
LA 16
PK 126,17,124,33
M 3,46,4
LC_ 104
PK 124,17,124,32
M 3,47,5
ds_read_b64 v[14:15], v5 offset:2048
PO 126
M 4,58,4
WL 4
LO 18,18,127
LU 127,18
M 4,59,5
s_add_u32 s42, s94, s65
RA 228,1,16384
HS 30,127
M 5,70,4
LL 17,58
LL 105,59
M 5,71,5
s_addc_u32 s43, s95, 0
LA 17
PK 127,18,125,33
Z 6,0,0
LC_ 105
PK 125,18,125,32
M 6,1,1
PO 127
v_subrev_u32 v5, s39, v5
Z 7,12,0
WL 4
LO 104,16,104
LU 16,104
M 7,13,1
RA 232,1,17408
HZ 26,16
Z 8,24,0
WL 4
LL 18,60
LL 108,61
M 8,25,1
s_mul_i32 s53, s50, 25600
LA 18
v_add_f32 v14, v14, v15
Z 9,36,0
s_add_u32 s40, %[wlo], s53
LC_ 108
v_add_f32_dpp v14, v14, v14 quad_perm:[1,0,3,2] row_mask:0xf bank_mask:0xf
M 9,37,1
s_addc_u32 s41, %[whi], 0
v_add_f32_dpp v14, v14, v14 quad_perm:[2,3,0,1] row_mask:0xf bank_mask:0xf
v_pk_add_f16 v27, v27, v30
Z 10,48,0
WL 3
LO 105,17,105
LU 17,105
M 10,49,1
s_mov_b32 m0, s38
RA 236,1,18432
HS 26,17
Z 11,60,0
LL 16,62
LL 109,63
M 11,61,1
LA 16
SU 124
M 6,2,2
LC_ 109
DW 0
M 6,3,3
v_add_f32_dpp v14, v14, v14 row_half_mirror row_mask:0xf bank_mask:0xf
v_pk_add_f16 v27, v27, v29
M 7,14,2
WL 3
LO 108,18,108
LU 18,108
M 7,15,3
s_add_u32 s37, s37, 32768
RA 216,1,19456
HS 26,18
HX 27
M 8,26,2
s_cmp_eq_u32 s37, s75
LL 17,64
LL 112,65
W1 27,3072
M 8,27,3
s_cselect_b32 s37, s74, s37
LA 17
DW 1024
M 9,38,2
LC_ 112
v_cvt_f64_f32 v[14:15], v14
M 9,39,3
s_mov_b64 exec, s[54:55]
SP
s_mov_b64 exec, -1
v_add_u32 v4, s39, v4
s_mov_b64 exec, 1
AT
s_mov_b64 exec, -1
M 10,50,2
WL 4
LO 109,16,109
LU 16,109
M 10,51,3
RA 220,1,20480
HS 26,16
M 11,62,2
LL 18,66
LL 113,67
M 11,63,3
LA 18
DW 2048
M 6,4,4
LC_ 113
M 6,5,5
M 7,16,4
WL 3
LO 112,17,112
LU 17,112
M 7,17,5
s_mul_i32 s53, s51, 24
RA 224,1,21504
HZ 29,17
M 8,28,4
s_bitcmp1_b32 s51, 0
LL 16,68
LL 116,69
M 8,29,5
s_addc_u32 s53, s53, 0
LA 16
M 9,40,4
s_lshl_b32 s53, s53, 11
LC_ 116
M 9,41,5
s_bitcmp1_b64 s[92:93], s51
M 10,52,4
WL 3
LO 113,18,113
LU 18,113
M 10,53,5
s_cselect_b64 s[94:95], s[88:89], s[86:87]
RA 228,1,22528
HS 29,18
M 11,64,4
LL 17,70
LL 117,71
M 11,65,5
s_add_u32 s94, s94, s53
LA 17
M 6,6,0
s_addc_u32 s95, s95, 0
LC_ 117
M 6,7,1
s_add_u32 s51, s51, %[d]
M 7,18,0
WL 3
LO 116,16,116
LU 16,116
M 7,19,1
s_cmp_eq_u32 s51, %[wrap]
RA 232,1,23552
HS 29,16
M 8,30,0
s_cselect_b32 s51, %[rst], s51
LL 18,72
LL 120,73
M 8,31,1
s_add_u32 s40, s40, 3072
LA 18
M 9,42,0
s_addc_u32 s41, s41, 0
LC_ 120
s_add_u32 m0, s38, 3072
M 9,43,1
DW 0
M 10,54,0
WL 3
LO 117,17,117
LU 17,117
M 10,55,1
RA 236,1,24576
HS 29,17
M 11,66,0
LL 16,74
LL 121,75
M 11,67,1
LA 16
DW 1024
M 6,8,2
LC_ 121
DW 2048
M 6,9,3
s_lshl_b32 s53, s52, 8
v_add_u32 v1, s37, v0
M 7,20,2
WL 3
LO 120,18,120
LU 18,120
M 7,21,3
s_add_u32 s48, %[alo], s53
RA 216,1,0
HZ 30,18
M 8,32,2
s_addc_u32 s49, %[ahi], 0
LL 17,76
LL 124,77
M 8,33,3
s_add_u32 s52, s52, %[d]
LA 17
v_and_b32 v22, 0xfffffcff, v1
M 9,44,2
s_cmp_eq_u32 s52, %[wrap]
LC_ 124
M 9,45,3
s_cselect_b32 s52, %[rst], s52
DQ 3072
M 10,56,2
WL 3
LO 121,16,121
LU 16,121
M 10,57,3
s_add_u32 s50, s50, %[d]
RA 220,1,1024
HS 30,16
M 11,68,2
LL 18,78
LL 125,79
M 11,69,3
s_cmp_eq_u32 s50, %[wrap]
LA 18
RV 252,22,5120
M 6,10,4
s_cselect_b32 s50, %[rst], s50
LC_ 125
M 6,11,5
WV 28
M 7,22,4
WL 4
LO 124,17,124
LU 17,124
M 7,23,5
s_add_u32 s38, s38, 32768
RA 224,1,2048
HS 30,17
M 8,34,4
s_cmp_eq_u32 s38, s77
LL 16,80
LL 106,81
M 8,35,5
s_cselect_b32 s38, s76, s38
LA 16
)"
R"(M 9,46,4
s_barrier
LC_ 106
M 9,47,5
s_sub_u32 s39, 0, s39
M 10,58,4
WL 3
LO 125,18,125
LU 18,125
M 10,59,5
RA 228,1,3072
HS 30,18
M 11,70,4
LL 17,82
LL 107,83
M 11,71,5
LA 17
v_pk_add_f16 v26, v26, v30
Z 0,0,0
LC_ 107
v_pk_add_f16 v26, v26, v29
M 0,1,1
HX 26
Z 1,12,0
WL 3
LO 16,16,106
LU 106,16
M 1,13,1
RA 232,1,4096
HZ 27,106
Z 2,24,0
LL 18,84
LL 110,85
M 2,25,1
LA 18
PK 106,16,104,33
Z 3,36,0
LC_ 110
PK 104,16,104,32
M 3,37,1
W1 26,2048
Z 4,48,0
WL 4
LO 17,17,107
LU 107,17
M 4,49,1
s_add_u32 s42, s94, s60
RA 236,1,6144
HS 27,107
Z 5,60,0
LL 16,86
LL 111,87
M 5,61,1
s_addc_u32 s43, s95, 0
LA 16
PK 107,17,105,33
M 0,2,2
LC_ 111
PK 105,17,105,32
M 0,3,3
SU 104
M 1,14,2
WL 4
LO 18,18,110
LU 110,18
M 1,15,3
RA 216,1,7168
HS 27,110
M 2,26,2
LL 17,88
LL 114,89
M 2,27,3
LA 17
PK 110,18,108,33
M 3,38,2
LC_ 114
PK 108,18,108,32
M 3,39,3
PO 110
M 4,50,2
WL 3
LO 16,16,111
LU 111,16
M 4,51,3
s_add_u32 s42, s94, s61
RA 220,1,8192
HS 27,111
M 5,62,2
LL 18,90
LL 115,91
M 5,63,3
s_addc_u32 s43, s95, 0
LA 18
PK 111,16,109,33
M 0,4,4
PFW
LC_ 115
PK 109,16,109,32
M 0,5,5
s_mov_b32 m0, %[pm]
s_nop 0
DF
PO 111
M 1,16,4
WL 3
LO 17,17,114
LU 114,17
M 1,17,5
RA 224,1,9216
HZ 29,114
M 2,28,4
LL 16,92
LL 118,93
M 2,29,5
LA 16
PK 114,17,112,33
M 3,40,4
LC_ 118
PK 112,17,112,32
M 3,41,5
SU 108
M 4,52,4
WL 3
LO 18,18,115
LU 115,18
M 4,53,5
s_add_u32 s42, s94, s62
RA 228,1,10240
HS 29,115
M 5,64,4
LL 17,94
LL 119,95
M 5,65,5
s_addc_u32 s43, s95, 0
LA 17
PK 115,18,113,33
M 0,6,0
LC_ 119
PK 113,18,113,32
M 0,7,1
SU 112
M 1,18,0
WL 3
LO 16,16,118
LU 118,16
M 1,19,1
RA 232,1,11264
HS 29,118
M 2,30,0
LL 18,96
LL 122,97
M 2,31,1
LA 18
PK 118,16,116,33
M 3,42,0
LC_ 122
PK 116,16,116,32
M 3,43,1
RV 252,22,5392
PO 118
M 4,54,0
WL 4
LO 17,17,119
LU 119,17
M 4,55,1
s_add_u32 s42, s94, s63
RA 236,1,12288
HS 29,119
M 5,66,0
LL 16,98
LL 123,99
M 5,67,1
s_addc_u32 s43, s95, 0
LA 16
PK 119,17,117,33
M 0,8,2
LC_ 123
PK 117,17,117,32
M 0,9,3
PO 119
M 1,20,2
WL 4
LO 18,18,122
LU 122,18
M 1,21,3
RA 216,1,13312
HZ 30,122
M 2,32,2
LL 17,100
LL 126,101
M 2,33,3
LA 17
PK 122,18,120,33
M 3,44,2
LC_ 126
PK 120,18,120,32
M 3,45,3
SU 116
M 4,56,2
WL 3
LO 16,16,123
LU 123,16
M 4,57,3
s_add_u32 s42, s94, s64
RA 220,1,14336
HS 30,123
M 5,68,2
LL 18,102
LL 127,103
M 5,69,3
s_addc_u32 s43, s95, 0
LA 18
PK 123,16,121,33
M 0,10,4
LC_ 127
PK 121,16,121,32
M 0,11,5
SU 120
M 1,22,4
WL 3
LO 17,17,126
LU 126,17
M 1,23,5
RA 224,1,15360
HS 30,126
M 2,34,4
LL 16,56
LL 104,57
M 2,35,5
LA 16
PK 126,17,124,33
M 3,46,4
LC_ 104
PK 124,17,124,32
M 3,47,5
ds_read_b64 v[14:15], v5 offset:2048
PO 126
M 4,58,4
WL 4
LO 18,18,127
LU 127,18
M 4,59,5
s_add_u32 s42, s94, s65
RA 228,1,16384
HS 30,127
M 5,70,4
LL 17,58
LL 105,59
M 5,71,5
s_addc_u32 s43, s95, 0
LA 17
PK 127,18,125,33
Z 6,0,0
LC_ 105
PK 125,18,125,32
M 6,1,1
PO 127
v_subrev_u32 v5, s39, v5
Z 7,12,0
WL 4
LO 104,16,104
LU 16,104
M 7,13,1
RA 232,1,17408
HZ 26,16
Z 8,24,0
WL 4
LL 18,60
LL 108,61
M 8,25,1
s_mul_i32 s53, s50, 25600
LA 18
v_add_f32 v14, v14, v15
Z 9,36,0
s_add_u32 s40, %[wlo], s53
LC_ 108
v_add_f32_dpp v14, v14, v14 quad_perm:[1,0,3,2] row_mask:0xf bank_mask:0xf
M 9,37,1
s_addc_u32 s41, %[whi], 0
Z 10,48,0
WL 3
LO 105,17,105
LU 17,105
M 10,49,1
s_mov_b32 m0, s38
RA 236,1,18432
HS 26,17
Z 11,60,0
LL 16,62
LL 109,63
M 11,61,1
LA 16
SU 124
M 6,2,2
LC_ 109
DW 0
M 6,3,3
WV 13
v_add_f32_dpp v14, v14, v14 quad_perm:[2,3,0,1] row_mask:0xf bank_mask:0xf
v_pk_add_f16 v27, v27, v30
M 7,14,2
WL 3
LO 108,18,108
LU 18,108
v_pk_add_f16 v27, v27, v29
M 7,15,3
s_add_u32 s37, s37, 32768
RA 216,1,19456
HS 26,18
HX 27
M 8,26,2
s_cmp_eq_u32 s37, s75
LL 17,64
LL 112,65
W1 27,3072
M 8,27,3
s_cselect_b32 s37, s74, s37
LA 17
DW 1024
v_readfirstlane_b32 s72, v11
M 9,38,2
LC_ 112
v_add_f32_dpp v14, v14, v14 row_half_mirror row_mask:0xf bank_mask:0xf
v_mov_b32 v12, s72
M 9,39,3
W2 12,12,16
v_cvt_f64_f32 v[14:15], v14
M 10,50,2
WL 5
LO 109,16,109
LU 16,109
M 10,51,3
RA 220,1,20480
HS 26,16
M 11,62,2
LL 18,66
LL 113,67
M 11,63,3
LA 18
s_mov_b64 exec, s[54:55]
SP
s_mov_b64 exec, -1
M 6,4,4
LC_ 113
DW 2048
M 6,5,5
v_add_u32 v4, s39, v4
s_mov_b64 exec, 1
AT
s_mov_b64 exec, -1
M 7,16,4
WL 4
LO 112,17,112
LU 17,112
M 7,17,5
s_mul_i32 s53, s51, 24
RA 224,1,21504
HZ 29,17
M 8,28,4
s_bitcmp1_b32 s51, 0
LL 16,68
LL 116,69
M 8,29,5
s_addc_u32 s53, s53, 0
LA 16
M 9,40,4
s_lshl_b32 s53, s53, 11
LC_ 116
M 9,41,5
s_bitcmp1_b64 s[92:93], s51
M 10,52,4
WL 3
LO 113,18,113
LU 18,113
M 10,53,5
s_cselect_b64 s[94:95], s[88:89], s[86:87]
RA 228,1,22528
HS 29,18
M 11,64,4
LL 17,70
LL 117,71
M 11,65,5
s_add_u32 s94, s94, s53
LA 17
M 6,6,0
s_addc_u32 s95, s95, 0
LC_ 117
M 6,7,1
s_add_u32 s51, s51, %[d]
M 7,18,0
WL 3
LO 116,16,116
LU 16,116
M 7,19,1
s_cmp_eq_u32 s51, %[wrap]
RA 232,1,23552
HS 29,16
M 8,30,0
s_cselect_b32 s51, %[rst], s51
LL 18,72
)"
R"(LL 120,73
M 8,31,1
s_add_u32 s40, s40, 3072
LA 18
M 9,42,0
s_addc_u32 s41, s41, 0
LC_ 120
s_add_u32 m0, s38, 3072
M 9,43,1
DW 0
M 10,54,0
WL 3
LO 117,17,117
LU 17,117
M 10,55,1
RA 236,1,24576
HS 29,17
M 11,66,0
LL 16,74
LL 121,75
M 11,67,1
LA 16
DW 1024
M 6,8,2
LC_ 121
DW 2048
M 6,9,3
s_lshl_b32 s53, s52, 8
v_add_u32 v1, s37, v0
M 7,20,2
WL 3
LO 120,18,120
LU 18,120
M 7,21,3
s_add_u32 s48, %[alo], s53
RA 216,1,0
HZ 30,18
M 8,32,2
s_addc_u32 s49, %[ahi], 0
LL 17,76
LL 124,77
M 8,33,3
s_add_u32 s52, s52, %[d]
LA 17
v_and_b32 v22, 0xfffffcff, v1
M 9,44,2
s_cmp_eq_u32 s52, %[wrap]
LC_ 124
M 9,45,3
s_cselect_b32 s52, %[rst], s52
DQ 3072
M 10,56,2
WL 3
LO 121,16,121
LU 16,121
M 10,57,3
s_add_u32 s50, s50, %[d]
RA 220,1,1024
HS 30,16
M 11,68,2
LL 18,78
LL 125,79
M 11,69,3
s_cmp_eq_u32 s50, %[wrap]
LA 18
RV 252,22,5120
M 6,10,4
s_cselect_b32 s50, %[rst], s50
LC_ 125
M 6,11,5
M 7,22,4
WL 4
LO 124,17,124
LU 17,124
M 7,23,5
s_add_u32 s38, s38, 32768
RA 224,1,2048
HS 30,17
M 8,34,4
s_cmp_eq_u32 s38, s77
LL 16,80
LL 106,81
M 8,35,5
s_cselect_b32 s38, s76, s38
LA 16
M 9,46,4
s_barrier
LC_ 106
M 9,47,5
s_sub_u32 s39, 0, s39
M 10,58,4
WL 3
LO 125,18,125
LU 18,125
M 10,59,5
RA 228,1,3072
HS 30,18
M 11,70,4
LL 17,82
LL 107,83
M 11,71,5
2:
LA 17
v_pk_add_f16 v26, v26, v30
Z 0,0,0
LC_ 107
ds_read_b32 v20, v5 offset:4096
M 0,1,1
v_pk_add_f16 v26, v26, v29
Z 1,12,0
WL 4
LO 16,16,106
LU 106,16
M 1,13,1
RA 232,1,4096
HZ 27,106
Z 2,24,0
LL 18,84
LL 110,85
M 2,25,1
LA 18
PK 106,16,104,33
Z 3,36,0
LC_ 110
PK 104,16,104,32
M 3,37,1
HX 26
Z 4,48,0
WL 4
LO 17,17,107
LU 107,17
M 4,49,1
s_add_u32 s42, s94, s60
RA 236,1,6144
HS 27,107
Z 5,60,0
LL 16,86
LL 111,87
M 5,61,1
s_addc_u32 s43, s95, 0
LA 16
PK 107,17,105,33
M 0,2,2
LC_ 111
PK 105,17,105,32
M 0,3,3
SU 104
M 1,14,2
WL 3
LO 18,18,110
LU 110,18
M 1,15,3
RA 216,1,7168
HS 27,110
M 2,26,2
LL 17,88
LL 114,89
W1 26,2048
M 2,27,3
LA 17
PK 110,18,108,33
M 3,38,2
LC_ 114
PK 108,18,108,32
M 3,39,3
PO 110
v_readfirstlane_b32 s59, v20
M 4,50,2
WL 4
LO 16,16,111
LU 111,16
M 4,51,3
s_add_u32 s42, s94, s61
RA 220,1,8192
HS 27,111
M 5,62,2
LL 18,90
LL 115,91
M 5,63,3
s_addc_u32 s43, s95, 0
LA 18
PK 111,16,109,33
M 0,4,4
LC_ 115
PK 109,16,109,32
M 0,5,5
PO 111
M 1,16,4
WL 3
LO 17,17,114
LU 114,17
M 1,17,5
RA 224,1,9216
HZ 29,114
M 2,28,4
LL 16,92
LL 118,93
M 2,29,5
LA 16
PK 114,17,112,33
M 3,40,4
LC_ 118
PK 112,17,112,32
M 3,41,5
SU 108
M 4,52,4
WL 3
LO 18,18,115
LU 115,18
M 4,53,5
s_add_u32 s42, s94, s62
RA 228,1,10240
HS 29,115
M 5,64,4
LL 17,94
LL 119,95
M 5,65,5
s_addc_u32 s43, s95, 0
LA 17
PK 115,18,113,33
M 0,6,0
LC_ 119
PK 113,18,113,32
M 0,7,1
SU 112
M 1,18,0
WL 3
LO 16,16,118
LU 118,16
M 1,19,1
RA 232,1,11264
HS 29,118
M 2,30,0
LL 18,96
LL 122,97
M 2,31,1
LA 18
PK 118,16,116,33
M 3,42,0
LC_ 122
PK 116,16,116,32
M 3,43,1
RV 252,22,5392
PO 118
M 4,54,0
WL 4
LO 17,17,119
LU 119,17
M 4,55,1
s_add_u32 s42, s94, s63
RA 236,1,12288
HS 29,119
M 5,66,0
LL 16,98
LL 123,99
M 5,67,1
s_addc_u32 s43, s95, 0
LA 16
PK 119,17,117,33
M 0,8,2
LC_ 123
PK 117,17,117,32
M 0,9,3
PO 119
M 1,20,2
WL 4
LO 18,18,122
LU 122,18
M 1,21,3
RA 216,1,13312
HZ 30,122
M 2,32,2
LL 17,100
LL 126,101
M 2,33,3
LA 17
PK 122,18,120,33
M 3,44,2
LC_ 126
PK 120,18,120,32
M 3,45,3
SU 116
M 4,56,2
WL 3
LO 16,16,123
LU 123,16
M 4,57,3
s_add_u32 s42, s94, s64
RA 220,1,14336
HS 30,123
M 5,68,2
LL 18,102
LL 127,103
M 5,69,3
s_addc_u32 s43, s95, 0
LA 18
PK 123,16,121,33
M 0,10,4
LC_ 127
PK 121,16,121,32
M 0,11,5
SU 120
M 1,22,4
WL 3
LO 17,17,126
LU 126,17
M 1,23,5
RA 224,1,15360
HS 30,126
M 2,34,4
LL 16,56
LL 104,57
M 2,35,5
LA 16
PK 126,17,124,33
M 3,46,4
LC_ 104
PK 124,17,124,32
M 3,47,5
ds_read_b64 v[14:15], v5 offset:2048
PO 126
M 4,58,4
WL 4
LO 18,18,127
LU 127,18
M 4,59,5
s_add_u32 s42, s94, s65
RA 228,1,16384
HS 30,127
M 5,70,4
LL 17,58
LL 105,59
M 5,71,5
s_addc_u32 s43, s95, 0
LA 17
PK 127,18,125,33
Z 6,0,0
LC_ 105
PK 125,18,125,32
M 6,1,1
PO 127
v_subrev_u32 v5, s39, v5
Z 7,12,0
WL 4
LO 104,16,104
LU 16,104
M 7,13,1
RA 232,1,17408
HZ 26,16
Z 8,24,0
WL 4
LL 18,60
LL 108,61
M 8,25,1
s_mul_i32 s53, s50, 25600
LA 18
v_add_f32 v14, v14, v15
Z 9,36,0
s_add_u32 s40, %[wlo], s53
LC_ 108
v_add_f32_dpp v14, v14, v14 quad_perm:[1,0,3,2] row_mask:0xf bank_mask:0xf
M 9,37,1
s_addc_u32 s41, %[whi], 0
Z 10,48,0
WL 3
LO 105,17,105
LU 17,105
M 10,49,1
s_mov_b32 m0, s38
RA 236,1,18432
HS 26,17
Z 11,60,0
LL 16,62
LL 109,63
M 11,61,1
LA 16
SU 124
M 6,2,2
LC_ 109
DW 0
M 6,3,3
WV 11
v_add_f32_dpp v14, v14, v14 quad_perm:[2,3,0,1] row_mask:0xf bank_mask:0xf
v_pk_add_f16 v27, v27, v30
M 7,14,2
WL 3
LO 108,18,108
LU 18,108
v_pk_add_f16 v27, v27, v29
M 7,15,3
s_add_u32 s37, s37, 32768
RA 216,1,19456
HS 26,18
HX 27
M 8,26,2
s_cmp_eq_u32 s37, s75
LL 17,64
LL 112,65
W1 27,3072
M 8,27,3
)"
R"(s_cselect_b32 s37, s74, s37
LA 17
DW 1024
v_readfirstlane_b32 s72, v11
M 9,38,2
LC_ 112
v_add_f32_dpp v14, v14, v14 row_half_mirror row_mask:0xf bank_mask:0xf
v_mov_b32 v12, s72
M 9,39,3
s_max_u32 s59, s59, %[nb]
W2 12,12,16
v_cvt_f64_f32 v[14:15], v14
M 10,50,2
WL 5
LO 109,16,109
LU 16,109
M 10,51,3
s_add_u32 %[nb], %[nb], 1
RA 220,1,20480
HS 26,16
M 11,62,2
LL 18,66
LL 113,67
M 11,63,3
LA 18
s_mov_b64 exec, s[54:55]
SP
s_mov_b64 exec, -1
M 6,4,4
LC_ 113
DW 2048
M 6,5,5
v_add_u32 v4, s39, v4
s_mov_b64 exec, 1
AT
s_mov_b64 exec, -1
M 7,16,4
WL 4
LO 112,17,112
LU 17,112
M 7,17,5
s_mul_i32 s53, s51, 24
RA 224,1,21504
HZ 29,17
M 8,28,4
s_bitcmp1_b32 s51, 0
LL 16,68
LL 116,69
M 8,29,5
s_addc_u32 s53, s53, 0
LA 16
M 9,40,4
s_lshl_b32 s53, s53, 11
LC_ 116
M 9,41,5
s_bitcmp1_b64 s[92:93], s51
M 10,52,4
WL 3
LO 113,18,113
LU 18,113
M 10,53,5
s_cselect_b64 s[94:95], s[88:89], s[86:87]
RA 228,1,22528
HS 29,18
M 11,64,4
LL 17,70
LL 117,71
M 11,65,5
s_add_u32 s94, s94, s53
LA 17
M 6,6,0
s_addc_u32 s95, s95, 0
LC_ 117
M 6,7,1
s_add_u32 s51, s51, %[d]
M 7,18,0
WL 3
LO 116,16,116
LU 16,116
M 7,19,1
s_cmp_eq_u32 s51, %[wrap]
RA 232,1,23552
HS 29,16
M 8,30,0
s_cselect_b32 s51, %[rst], s51
LL 18,72
LL 120,73
M 8,31,1
s_add_u32 s40, s40, 3072
LA 18
M 9,42,0
s_addc_u32 s41, s41, 0
LC_ 120
s_add_u32 m0, s38, 3072
M 9,43,1
DW 0
M 10,54,0
WL 3
LO 117,17,117
LU 17,117
M 10,55,1
RA 236,1,24576
HS 29,17
M 11,66,0
LL 16,74
LL 121,75
M 11,67,1
LA 16
DW 1024
M 6,8,2
LC_ 121
DW 2048
M 6,9,3
s_lshl_b32 s53, s52, 8
v_add_u32 v1, s37, v0
M 7,20,2
WL 3
LO 120,18,120
LU 18,120
M 7,21,3
s_add_u32 s48, %[alo], s53
RA 216,1,0
HZ 30,18
M 8,32,2
s_addc_u32 s49, %[ahi], 0
LL 17,76
LL 124,77
M 8,33,3
s_add_u32 s52, s52, %[d]
LA 17
v_and_b32 v22, 0xfffffcff, v1
M 9,44,2
s_cmp_eq_u32 s52, %[wrap]
LC_ 124
M 9,45,3
s_cselect_b32 s52, %[rst], s52
DQ 3072
M 10,56,2
WL 3
LO 121,16,121
LU 16,121
M 10,57,3
s_add_u32 s50, s50, %[d]
RA 220,1,1024
HS 30,16
M 11,68,2
LL 18,78
LL 125,79
M 11,69,3
s_cmp_eq_u32 s50, %[wrap]
LA 18
RV 252,22,5120
M 6,10,4
s_cselect_b32 s50, %[rst], s50
LC_ 125
M 6,11,5
M 7,22,4
WL 4
LO 124,17,124
LU 17,124
M 7,23,5
s_add_u32 s38, s38, 32768
RA 224,1,2048
HS 30,17
M 8,34,4
s_cmp_eq_u32 s38, s77
LL 16,80
LL 106,81
M 8,35,5
s_cselect_b32 s38, s76, s38
LA 16
M 9,46,4
s_barrier
LC_ 106
M 9,47,5
s_sub_u32 s39, 0, s39
M 10,58,4
WL 3
LO 125,18,125
LU 18,125
M 10,59,5
RA 228,1,3072
HS 30,18
M 11,70,4
s_cmp_lt_u32 s59, 32
LL 17,82
LL 107,83
M 11,71,5
s_cbranch_scc1 2b
LA 17
LC_ 107
ds_read_b64 v[14:15], v5 offset:2048
v_pk_add_f16 v26, v26, v30
v_pk_add_f16 v26, v26, v29
HX 26
v_subrev_u32 v5, s39, v5
W1 26,2048
WL 0
LO 16,16,106
LU 106,16
HZ 27,106
LL 18,84
LA 18
LL 110,85
PK 106,16,104,33
v_add_f32 v14, v14, v15
s_nop 1
v_add_f32_dpp v14, v14, v14 quad_perm:[1,0,3,2] row_mask:0xf bank_mask:0xf
s_nop 1
v_add_f32_dpp v14, v14, v14 quad_perm:[2,3,0,1] row_mask:0xf bank_mask:0xf
s_nop 1
v_add_f32_dpp v14, v14, v14 row_half_mirror row_mask:0xf bank_mask:0xf
v_cvt_f64_f32 v[14:15], v14
s_mov_b64 exec, s[54:55]
SP
s_mov_b64 exec, -1
LC_ 110
PK 104,16,104,32
LO 17,17,107
LU 107,17
s_add_u32 s42, s94, s60
s_addc_u32 s43, s95, 0
HS 27,107
LL 16,86
LA 16
LL 111,87
LC_ 111
PK 107,17,105,33
PK 105,17,105,32
SU 104
WL 0
LO 18,18,110
LU 110,18
HS 27,110
LL 17,88
LA 17
LL 114,89
LC_ 114
PK 110,18,108,33
PK 108,18,108,32
PO 110
LO 16,16,111
LU 111,16
HS 27,111
s_add_u32 s42, s94, s61
s_addc_u32 s43, s95, 0
LL 18,90
LA 18
LL 115,91
LC_ 115
PK 111,16,109,33
PK 109,16,109,32
PO 111
WL 0
LO 17,17,114
LU 114,17
HZ 29,114
LL 16,92
LA 16
LL 118,93
LC_ 118
PK 114,17,112,33
PK 112,17,112,32
SU 108
LO 18,18,115
LU 115,18
s_add_u32 s42, s94, s62
s_addc_u32 s43, s95, 0
HS 29,115
LL 17,94
LA 17
LL 119,95
LC_ 119
PK 115,18,113,33
PK 113,18,113,32
SU 112
WL 0
LO 16,16,118
LU 118,16
HS 29,118
LL 18,96
LA 18
LL 122,97
LC_ 122
PK 118,16,116,33
PK 116,16,116,32
PO 118
LO 17,17,119
LU 119,17
HS 29,119
s_add_u32 s42, s94, s63
s_addc_u32 s43, s95, 0
LL 16,98
LA 16
LL 123,99
LC_ 123
PK 119,17,117,33
PK 117,17,117,32
PO 119
WL 0
LO 18,18,122
LU 122,18
HZ 30,122
LL 17,100
LA 17
LL 126,101
LC_ 126
PK 122,18,120,33
PK 120,18,120,32
SU 116
LO 16,16,123
LU 123,16
s_add_u32 s42, s94, s64
s_addc_u32 s43, s95, 0
HS 30,123
LL 18,102
LA 18
LL 127,103
LC_ 127
PK 123,16,121,33
PK 121,16,121,32
SU 120
WL 0
LO 17,17,126
LU 126,17
HS 30,126
PK 126,17,124,33
PK 124,17,124,32
PO 126
LO 18,18,127
LU 127,18
HS 30,127
s_add_u32 s42, s94, s65
s_addc_u32 s43, s95, 0
v_pk_add_f16 v27, v27, v30
PK 127,18,125,33
v_pk_add_f16 v27, v27, v29
PK 125,18,125,32
HX 27
PO 127
W1 27,3072
s_nop 1
v_add_u32 v4, s39, v4
SU 124
s_lshl_b32 s53, s52, 8
s_add_u32 s48, %[alo], s53
s_addc_u32 s49, %[ahi], 0
s_add_u32 s52, s52, %[d]
s_cmp_eq_u32 s52, %[wrap]
s_cselect_b32 s52, %[rst], s52
WL 0
WV 0
s_barrier
s_sub_u32 s39, 0, s39
ds_read_b64 v[14:15], v5 offset:2048
s_nop 1
v_subrev_u32 v5, s39, v5
WL 0
v_add_f32 v14, v14, v15
s_nop 1
v_add_f32_dpp v14, v14, v14 quad_perm:[1,0,3,2] row_mask:0xf bank_mask:0xf
s_nop 1
v_add_f32_dpp v14, v14, v14 quad_perm:[2,3,0,1] row_mask:0xf bank_mask:0xf
s_nop 1
v_add_f32_dpp v14, v14, v14 row_half_mirror row_mask:0xf bank_mask:0xf
v_cvt_f64_f32 v[14:15], v14
s_mov_b64 exec, s[54:55]
SP
s_mov_b64 exec, -1
GE
s_waitcnt vmcnt(0) lgkmcnt(0)
8:
s_branch 6f
.Lk1s0:
FR
s_waitcnt lgkmcnt(0)
v_cmp_gt_u32 vcc, v19, v15
s_nop 4
s_cbranch_vccnz .Lk1s0
s_branch .Lk1r0
.Lk1s1:
FR
s_waitcnt lgkmcnt(0)
v_cmp_gt_u32 vcc, v19, v15
s_nop 4
s_cbranch_vccnz .Lk1s1
s_branch .Lk1r1
.Lk1s2:
FR
s_waitcnt lgkmcnt(0)
v_cmp_gt_u32 vcc, v19, v15
s_nop 4
s_cbranch_vccnz .Lk1s2
s_branch .Lk1r2
.Lk1s3:
FR
s_waitcnt lgkmcnt(0)
v_cmp_gt_u32 vcc, v19, v15
s_nop 4
s_cbranch_vccnz .Lk1s3
)"
R"(s_branch .Lk1r3
.Lk1s4:
FR
s_waitcnt lgkmcnt(0)
v_cmp_gt_u32 vcc, v19, v15
s_nop 4
s_cbranch_vccnz .Lk1s4
s_branch .Lk1r4
.Lk1s5:
FR
s_waitcnt lgkmcnt(0)
v_cmp_gt_u32 vcc, v19, v15
s_nop 4
s_cbranch_vccnz .Lk1s5
s_branch .Lk1r5
.Lk1s6:
FR
s_waitcnt lgkmcnt(0)
v_cmp_gt_u32 vcc, v19, v15
s_nop 4
s_cbranch_vccnz .Lk1s6
s_branch .Lk1r6
.Lk1s7:
FR
s_waitcnt lgkmcnt(0)
v_cmp_gt_u32 vcc, v19, v15
s_nop 4
s_cbranch_vccnz .Lk1s7
s_branch .Lk1r7
.Lk1s8:
FR
s_waitcnt lgkmcnt(0)
v_cmp_gt_u32 vcc, v19, v15
s_nop 4
s_cbranch_vccnz .Lk1s8
s_branch .Lk1r8
.Lk1s9:
FR
s_waitcnt lgkmcnt(0)
v_cmp_gt_u32 vcc, v19, v15
s_nop 4
s_cbranch_vccnz .Lk1s9
s_branch .Lk1r9
.Lk1s10:
FR
s_waitcnt lgkmcnt(0)
v_cmp_gt_u32 vcc, v19, v15
s_nop 4
s_cbranch_vccnz .Lk1s10
s_branch .Lk1r10
.Lk1s11:
FR
s_waitcnt lgkmcnt(0)
v_cmp_gt_u32 vcc, v19, v15
s_nop 4
s_cbranch_vccnz .Lk1s11
s_branch .Lk1r11
.Lk1s12:
FR
s_waitcnt lgkmcnt(0)
v_cmp_gt_u32 vcc, v19, v15
s_nop 4
s_cbranch_vccnz .Lk1s12
s_branch .Lk1r12
.Lk1s13:
FR
s_waitcnt lgkmcnt(0)
v_cmp_gt_u32 vcc, v19, v15
s_nop 4
s_cbranch_vccnz .Lk1s13
s_branch .Lk1r13
.Lk1s14:
FR
s_waitcnt lgkmcnt(0)
v_cmp_gt_u32 vcc, v19, v15
s_nop 4
s_cbranch_vccnz .Lk1s14
s_branch .Lk1r14
.Lk1s15:
FR
s_waitcnt lgkmcnt(0)
v_cmp_gt_u32 vcc, v19, v15
s_nop 4
s_cbranch_vccnz .Lk1s15
s_branch .Lk1r15
.Lk1s16:
FR
s_waitcnt lgkmcnt(0)
v_cmp_gt_u32 vcc, v19, v15
s_nop 4
s_cbranch_vccnz .Lk1s16
s_branch .Lk1r16
.Lk1s17:
FR
s_waitcnt lgkmcnt(0)
v_cmp_gt_u32 vcc, v19, v15
s_nop 4
s_cbranch_vccnz .Lk1s17
s_branch .Lk1r17
.Lk1s18:
FR
s_waitcnt lgkmcnt(0)
v_cmp_gt_u32 vcc, v19, v15
s_nop 4
s_cbranch_vccnz .Lk1s18
s_branch .Lk1r18
.Lk1s19:
FR
s_waitcnt lgkmcnt(0)
v_cmp_gt_u32 vcc, v19, v15
s_nop 4
s_cbranch_vccnz .Lk1s19
s_branch .Lk1r19
.Lk1s20:
FR
s_waitcnt lgkmcnt(0)
v_cmp_gt_u32 vcc, v19, v15
s_nop 4
s_cbranch_vccnz .Lk1s20
s_branch .Lk1r20
.Lk1s21:
FR
s_waitcnt lgkmcnt(0)
v_cmp_gt_u32 vcc, v19, v15
s_nop 4
s_cbranch_vccnz .Lk1s21
s_branch .Lk1r21
6:
.purgem M
.purgem Z
.purgem LZ
.purgem LS
.purgem LD
.purgem GZ
.purgem GS
.purgem CL
.purgem CH
.purgem XL
.purgem XN
.purgem F
.purgem U
.purgem E
.purgem A
.purgem R
.purgem C
.purgem S0
.purgem S
.purgem HC
.purgem HF
.purgem HM
.purgem HE
.purgem HG
.purgem HA
.purgem HR
.purgem HQ
.purgem HP
.purgem HY
.purgem HK
.purgem LQ
.purgem LL
.purgem LH
.purgem LA
.purgem LC_
.purgem LO
.purgem HZ
.purgem HS
.purgem HT
.purgem HX
.purgem UX
.purgem EX
.purgem AX
.purgem RX
.purgem TX
.purgem SX
.purgem AW
.purgem RA
.purgem RV
.purgem W2
.purgem W1
.purgem DW
.purgem DF
.purgem DX
.purgem ST
.purgem SU
.purgem SP
.purgem AT
.purgem PFA
.purgem PFN
.purgem PFW
.purgem PF0
.purgem WL
.purgem WV
.purgem LU
.purgem CF
.purgem XS
.purgem AR
.purgem DQ
.purgem GE
.purgem BW
.purgem BR
.purgem LK
.purgem PK
.purgem PO
.purgem LG
.purgem DY
.purgem FP
.purgem FR
)"
      : [nb] "=&s"(nb)
      : [xlo] "s"((unsigned)xp), [xhi] "s"((unsigned)(xp >> 32)), [hlo] "s"((unsigned)hp), [hhi] "s"((unsigned)(hp >> 32)),
        [glo] "s"((unsigned)gp), [ghi] "s"((unsigned)(gp >> 32)), [wlo] "s"((unsigned)wp), [whi] "s"((unsigned)(wp >> 32)),
        [w] "s"(w), [lds] "s"(lds0), [d] "s"(d), [wrap] "s"(wrap), [rst] "s"(rst), [pos0] "s"(pos0), [xo0] "s"(xo0), [xs1] "s"(xs1), [sb1] "s"(sb1), [r0o] "s"(r0o), [tsel] "s"(tsel), [s2] "s"(s2),
        [alo] "s"((unsigned)ap), [ahi] "s"((unsigned)(ap >> 32)), [ca0] "s"(ca0), [pm] "s"(pm), [pf] "s"(pf)
      : K1_CLOB);
  TS(6);
  MK_END(1);
}

typedef __bf16 bf16x8_t __attribute__((ext_vector_type(8)));
DI f32x4 mfma16(u32x4 a, u32x4 b, f32x4 c) {
  return __builtin_amdgcn_mfma_f32_16x16x32_bf16(__builtin_bit_cast(bf16x8_t, a), __builtin_bit_cast(bf16x8_t, b), c, 0, 0, 0);
}
DI float grn_gx(double s, float scale) { return round_bf16(__builtin_amdgcn_sqrtf(round_bf16(round_bf16((float)s * scale) + 1e-6f))); }
DI void slow_fc1(const u16* __restrict__ x, const u16* __restrict__ dw, char* __restrict__ hws, float* sacc, int w) {
  const int lane = __builtin_amdgcn_mbcnt_hi(~0u, __builtin_amdgcn_mbcnt_lo(~0u, 0u)), m = lane & 15, g = lane >> 4;
  for (int i = lane; i < HID; i += 64) sacc[i] = 0.f;
#pragma unroll 1
  for (int it = 0; it < 9; ++it) {
    const int sub = blockIdx.x * 36 + it * 4 + w;
    const unsigned px = sub_pixel(x, sub, m);
    u32x4 A[12];
    float sum = 0.f;
    const u16* dq = dw;
    asm volatile("" : "+s"(dq));
#pragma unroll
    for (int ks = 0; ks < 12; ++ks) {
      const u32x4 v = *(const u32x4*)(x + (size_t)px * CH + 32 * ks + 8 * g);
      const u32x4 d = *(const u32x4*)(dq + 32 * ks + 8 * g);
#pragma unroll
      for (int j = 0; j < 4; ++j) {
        A[ks][j] = pack_bf16(bf_lo(v[j]) * bf_lo(d[j]), bf_hi(v[j]) * bf_hi(d[j]));
        sum += bf_lo(A[ks][j]) + bf_hi(A[ks][j]);
      }
      asm volatile("" : "+v"(A[ks]));
    }
    sum += __shfl_xor(sum, 16); sum += __shfl_xor(sum, 32);
    const float mean = sum * (1.0f / CH);
    float var = 0.f;
#pragma unroll
    for (int ks = 0; ks < 12; ++ks) {
#pragma unroll
      for (int j = 0; j < 4; ++j) {
        const float a = bf_lo(A[ks][j]) - mean, b = bf_hi(A[ks][j]) - mean;
        var += a * a + b * b;
      }
      asm volatile("" : "+v"(A[ks]));
    }
    var += __shfl_xor(var, 16); var += __shfl_xor(var, 32);
    const float mb = round_bf16(mean);
    const float rd = 1.0f / round_bf16(sqrtf(round_bf16(round_bf16(var * (1.0f / CH)) + 1e-5f)));
#pragma unroll
    for (int ks = 0; ks < 12; ++ks)
#pragma unroll
      for (int j = 0; j < 4; ++j) A[ks][j] = pack_bf16((bf_lo(A[ks][j]) - mb) * rd, (bf_hi(A[ks][j]) - mb) * rd);
#pragma unroll 1
    for (int tc = 0; tc < 96; ++tc) {
      f32x4 acc = {0.f, 0.f, 0.f, 0.f};
#pragma unroll
      for (int ks = 0; ks < 12; ++ks) acc = mfma16(A[ks], *(const u32x4*)(g_buf + (size_t)(tc * 12 + ks) * 1024 + lane * 16), acc);
      float q = 0.f;
#pragma unroll
      for (int r = 0; r < 4; ++r) { acc[r] = gelu_tanh(acc[r]); q = __builtin_fmaf(acc[r], acc[r], q); }
      typedef unsigned u32x2_ __attribute__((ext_vector_type(2)));
      *(u32x2_*)(hws + h_piece(hws, sub, tc >> 1) + g * 256 + ((m + 4 * g) & 15) * 16 + (tc & 1) * 8) = (u32x2_){pack_bf16(acc[0], acc[1]), pack_bf16(acc[2], acc[3])};
      q += __shfl_xor(q, 16); q += __shfl_xor(q, 32);
      if (g == 0) sacc[tc * 16 + m] += q;
    }
  }
  __builtin_amdgcn_wave_barrier();
  __builtin_amdgcn_fence(__ATOMIC_ACQ_REL, "workgroup");
  for (int i = lane; i < HID; i += 64) atomicAdd((double*)(g_buf + G_ACC2) + i, (double)sacc[i]);
}
DI void slow_bar(unsigned* c, int w) {
  const int lane = __builtin_amdgcn_mbcnt_hi(~0u, __builtin_amdgcn_mbcnt_lo(~0u, 0u));
  asm volatile("s_waitcnt vmcnt(0)" ::: "memory");
  __syncthreads();
  if (w == 0 && lane == 0) {
    __threadfence();
    asm volatile("s_waitcnt vmcnt(0)" ::: "memory");
    __hip_atomic_fetch_add(c, 1u, __ATOMIC_RELAXED, __HIP_MEMORY_SCOPE_AGENT);
    for (int i = 0; i < (1 << 22) && __hip_atomic_load(c, __ATOMIC_RELAXED, __HIP_MEMORY_SCOPE_AGENT) < (unsigned)NWG; ++i) __builtin_amdgcn_s_sleep(32);
    __threadfence();
    asm volatile("s_waitcnt vmcnt(0)" ::: "memory");
  }
  __syncthreads();
}

#define K2_CLOB "memory", "vcc", "scc", "s48", "s49", "s50", "s51", "s52", "s53", "s54", "s55", "s56", "s57", "s58", "s59", "s60", "s61", "s62", "s63", "s64", "s65", "s66", "s67", \
  "s68", "s69", "s70", "s71", "s72", "s73", "s74", "s75", "s76", "s77", "s78", "s79", "s80", "s81", "s84", "s85", "s86", "s87", "s88", "s89", "s90", "s91", "s92", "s93", "s94", "s95", "s96", "s97", "s98", "s99", "s38", "s39", "s40", "s41", "s42", "s43", "s44", "s45", "s46", "s47", VA_CLOB
DI void slow_fc2(const u16* __restrict__ x, const char* __restrict__ hws, u16* __restrict__ out, const u16* __restrict__ grn_g, const u16* __restrict__ w2,
                 char* smem, int w) {
  float* sc = (float*)smem;
  float* red = sc + HID;
  u16* wf = (u16*)(smem + 8192);
  const int lane = __builtin_amdgcn_mbcnt_hi(~0u, __builtin_amdgcn_mbcnt_lo(~0u, 0u)), tid = w * 64 + lane, m = lane & 15, g = lane >> 4;
  __syncthreads();
  float part = 0.f;
  for (int c = tid; c < HID; c += 256) {
    const float gx = grn_gx(((const double*)(g_buf + G_ACC2))[c], 1.0f);
    sc[c] = gx; part += gx;
  }
#pragma unroll
  for (int o = 32; o > 0; o >>= 1) part += __shfl_xor(part, o);
  if (lane == 0) red[tid >> 6] = part;
  __syncthreads();
  const float gmean = round_bf16(round_bf16(((red[0] + red[1]) + (red[2] + red[3])) * (1.0f / HID)) + 1e-6f);
  for (int c = tid; c < HID; c += 256) sc[c] = (1.0f + ld_bf16(grn_g + c) * round_bf16(sc[c] / gmean)) ;
#pragma unroll 1
  for (int it = 0; it < 9; ++it) {
    const int sub = blockIdx.x * 36 + it * 4 + (tid >> 6);
    f32x4 acc[24];
#pragma unroll
    for (int i = 0; i < 24; ++i) acc[i] = (f32x4){0.f, 0.f, 0.f, 0.f};
#pragma unroll 1
    for (int t = 0; t < 48; ++t) {
      __syncthreads();
      for (int e = tid; e < 32 * CH; e += 256) {
        const int k = e / CH, n = e % CH;
        wf[((n >> 4) * 64 + (k >> 3) * 16 + (n & 15)) * 8 + (k & 7)] = (u16)pack_bf16(sc[32 * t + k] * ld_bf16(w2 + (size_t)(32 * t + k) * CH + n), 0.f);
      }
      __syncthreads();
      const char* hp = hws + h_piece(hws, sub, t) + (m >> 2) * 256 + (g >> 1) * 8 + 2 * (m & 3);
      u32x4 A;
#pragma unroll
      for (int j = 0; j < 4; ++j) {
        unsigned v[2];
#pragma unroll
        for (int k = 0; k < 2; ++k) {
          v[k] = *(const u16*)(hp + ((8 * (g & 1) + 2 * j + k + (m & 12)) & 15) * 16);
        }
        A[j] = v[0] | (v[1] << 16);
      }
#pragma unroll
      for (int i = 0; i < 24; ++i) acc[i] = mfma16(A, *(const u32x4*)(wf + (i * 64 + lane) * 8), acc[i]);
    }
#pragma unroll
    for (int i = 0; i < 24; ++i) {
      const int n = 16 * i + m;
      const float* bp = g_cs + 4 * 384 + cs_index(n);
      const float bias = (bp[0] + bp[384]) + (bp[768] + bp[1152]);
#pragma unroll
      for (int r = 0; r < 4; ++r) {
        const size_t o = (size_t)(sub_pixel(x, sub, 4 * g) + r) * CH + n;
        out[o] = (u16)pack_bf16(acc[i][r] + bias + ld_bf16(x + o), 0.f);
      }
    }
  }
}
__global__ __launch_bounds__(256, 1) void k2_fc2(const u16* __restrict__ x, char* __restrict__ hws, u16* __restrict__ out,
                                                 const u16* __restrict__ grn_g, const u16* __restrict__ w2, const u16* __restrict__ dw,
                                                 const u16* __restrict__ w1) {
  extern __shared__ __attribute__((aligned(16))) char smem[];
  const int w = __builtin_amdgcn_readfirstlane(threadIdx.x >> 6);
  const unsigned pg = w & 1, nh = w >> 1;
  const unsigned lds0 = lds_addr(smem) + 1024;
  MK_START(4); TS(9);
  const uint64_t pp = uni64((uint64_t)g_prm + (blockIdx.x * 4 + w) * 256), ap = (uint64_t)g_buf + K1_G_ACC + w * 1024;
  const unsigned wm = lds0 + w * 6144, ab = lds0 + nh * 12288;
  const unsigned hb = lds0 + K2_HB + pg * 2 * K2_RSZ, hm = hb + nh * K2_RSZ;
  const unsigned bm = w * 384, bb = lds0 + K2_E_OFF + nh * 768;
  const unsigned pm = lds0 + K2_PF_OFF, sb = lds0 + K2_S16_OFF, sw = sb + w * 256;
  uint64_t flg;
#define K2_OPS [pp] "s"(pp), [alo] "s"((unsigned)ap), [ahi] "s"((unsigned)(ap >> 32)), [wm] "s"(wm), [hm] "s"(hm), [bm] "s"(bm), [ab] "s"(ab), [hb] "s"(hb), [bb] "s"(bb), \
         [sb] "s"(sb), [sw] "s"(sw), [pm] "s"(pm)
  asm volatile(
R"(.macro MA t,a,b
v_mfma_f32_16x16x32_f16 a[\t:\t+3], v[\b:\b+3], a[\a:\a+3], a[\t:\t+3]
.endm
.macro MV t,a,b
v_mfma_f32_16x16x32_f16 v[\t:\t+3], v[\b:\b+3], a[\a:\a+3], v[\t:\t+3]
.endm
.macro NA t,a,b
v_mfma_f32_16x16x32_f16 a[\t:\t+3], a[\b:\b+3], a[\a:\a+3], a[\t:\t+3]
.endm
.macro NV t,a,b
v_mfma_f32_16x16x32_f16 v[\t:\t+3], a[\b:\b+3], a[\a:\a+3], v[\t:\t+3]
.endm
.macro AN d,s
v_and_b32 v[\d], s93, v[\s]
.endm
.macro SH d
v_lshrrev_b32 v[\d], 4, v[\d]
.endm
.macro PA_ d,h,r
v_perm_b32 v[\d], v[\h], v[\r], s54
.endm
.macro PB_ d,h,r
v_perm_b32 v[\d], v[\h], v[\r], s55
.endm
.macro AW a,s
v_accvgpr_write_b32 a[\a], v[\s]
.endm
.macro AR d,a
v_accvgpr_read_b32 v[\d], a[\a]
.endm
.macro UL d,x
v_lshlrev_b32 v[\d], 16, v[\x]
.endm
.macro UH d,x
v_and_b32 v[\d], s38, v[\x]
.endm
.macro AD d,b
v_fmac_f32 v[\d], s53, v[\b]
.endm
.macro EF d,e
v_fma_f32 v[\d], v[\d], s39, v[\e]
.endm
.macro TY d,o
ds_read_b64_tr_b16 v[\d:\d+1], v2 offset:\o
.endm
.macro TX d,o
ds_read_b64_tr_b16 v[\d:\d+1], v51 offset:\o
.endm
.macro RA d,o
ds_read_b128 a[\d:\d+3], v1 offset:\o
.endm
.macro RC d,o
ds_read_b128 a[\d:\d+3], v5 offset:\o
.endm
.macro BV d,o
ds_read_b128 v[\d:\d+3], v4 offset:\o
.endm
.macro BA d,o
ds_read_b128 a[\d:\d+3], v4 offset:\o
.endm
.macro PA a
s_mov_b32 m0, %[pm]
s_getpc_b64 s[46:47]
s_andn2_b32 s46, s46, 0x7f
s_add_u32 s46, s46, s81
s_addc_u32 s47, s47, 0
s_add_u32 s46, s46, \a
s_addc_u32 s47, s47, 0
.endm
.macro DF
global_load_lds_dwordx4 v0, s[46:47]
.endm
.macro DW o
global_load_lds_dwordx4 v0, s[40:41] offset:\o sc1
.endm
.macro DR o
global_load_lds_dwordx4 v0, s[40:41] offset:\o
.endm
.macro DH o
global_load_lds_dwordx4 v0, s[40:41] offset:\o nt
.endm
.macro DG o
s_mov_b32 exec_hi, 0
global_load_lds_dwordx4 v0, s[40:41] offset:\o nt
s_mov_b32 exec_hi, -1
.endm
.macro XL d,o
global_load_dwordx2 v[\d:\d+1], v47, s[44:45] offset:\o
.endm
.macro ST d,o
global_store_dwordx2 v50, v[\d:\d+1], s[42:43] offset:\o nt sc1
.endm
.macro SA d,o
global_store_dwordx2 v50, a[\d:\d+1], s[42:43] offset:\o nt sc1
.endm
.macro CV d,a,b
v_cvt_pk_bf16_f32 v[\d], v[\a], v[\b]
.endm
.macro RS d,o
ds_read_b128 v[\d:\d+3], v46 offset:\o
.endm
.macro PM d,s
v_pk_mul_f16 v[\d], v[\d], v[\s]
.endm
.macro PF d,n
v_pk_fma_f16 v[\d], v[\d], v[14+\n], v[22+\n]
.endm
.macro FS
global_store_dword v0, v54, s[96:97] sc1
.endm
.macro FL
global_load_dword v54, v55, s[98:99] sc1
.endm
.macro FB
s_cbranch_vccnz 21b
22:
.endm
.macro FC
s_cbranch_vccnz 26b
27:
.endm
.macro FG
s_or_b64 s[54:55], s[44:45], vcc
s_cbranch_scc1 23b
24:
.endm
.macro FX
s_branch 25f
21:
s_sleep 4
global_load_dword v54, v55, s[98:99] sc1
s_waitcnt vmcnt(0)
v_cmp_eq_u32 vcc, 0, v54
s_sub_u32 s80, s80, 1
s_cbranch_scc1 22f
s_nop 2
s_cbranch_vccnz 21b
s_branch 22f
26:
s_sleep 4
global_load_dword v54, v55, s[98:99] sc1
s_waitcnt vmcnt(0)
v_cmp_ne_u32 vcc, 2, v54
s_sub_u32 s80, s80, 1
s_cbranch_scc1 27f
s_nop 2
s_cbranch_vccnz 26b
s_branch 27f
23:
v_mov_b32 v10, 1
s_mov_b64 exec, 1
global_store_dword v0, v10, s[78:79] offset:384 sc1
s_mov_b64 exec, -1
s_nop 4
s_branch 24f
25:
.endm
.macro FP
ds_inc_u32 v0, v128
.endm
.macro FR
ds_read_b32 v10, v0
.endm
.macro FQ n
v_cmp_gt_u32 vcc, \n, v10
.endm
.macro FZ n
s_cbranch_vccnz .Lk2s\n
.Lk2r\n:
.endm
.macro FY n
.Lk2s\n:
FR
s_waitcnt lgkmcnt(0)
FQ \n
s_nop 4
s_cbranch_vccnz .Lk2s\n
s_branch .Lk2r\n
.endm
.macro WL n
s_waitcnt lgkmcnt(\n)
.endm
.macro WV n
s_waitcnt vmcnt(\n)
.endm
.macro M n
.if \n < 82
v_mfma_f32_16x16x32_f16 a[36+4*(\n/2):36+4*(\n/2)+3], v[56+8*((\n/2)%%6)+4*(\n&1):56+8*((\n/2)%%6)+4*(\n&1)+3], a[8*((\n/12)%%4)+4*(\n&1):8*((\n/12)%%4)+4*(\n&1)+3], a[36+4*(\n/2):36+4*(\n/2)+3]
.else
v_mfma_f32_16x16x32_f16 v[-32+4*(\n/2):-32+4*(\n/2)+3], v[56+8*((\n/2)%%6)+4*(\n&1):56+8*((\n/2)%%6)+4*(\n&1)+3], a[8*((\n/12)%%4)+4*(\n&1):8*((\n/12)%%4)+4*(\n&1)+3], v[-32+4*(\n/2):-32+4*(\n/2)+3]
.endif
.endm
.macro R1 n
.if \n < 64
RA 8*(((\n%%24)/2+2*((\n/24)&1))%%4)+4*(\n&1),1024*\n
.else
RC 8*(((\n%%24)/2+2*((\n/24)&1))%%4)+4*(\n&1),1024*\n-49152
.endif
.endm
.macro R n
R1 \n
R1 (\n+1)
.endm
.macro P n
.if \n & 2
PB_ 56+\n,104+8*(\n/16)+(\n&4)+2*((\n/8)&1)+(\n&1),56+(\n|2)
.else
PA_ 56+\n,104+8*(\n/16)+(\n&4)+2*((\n/8)&1)+(\n&1),56+(\n|2)
.endif
.endm
.macro Q n
P \n
P (\n+1)
.endm
.macro mw k
s_add_u32 m0, %[wm], 1024*\k
.endm
.macro mh k
s_add_u32 m0, %[hm], 512*\k
.endm
.macro ad r,k
s_add_u32 s40, s[\r], 512*\k
s_addc_u32 s41, s[\r+1], 0
.endm
.macro ae d,r,k
s_add_u32 s[\d], s[\r], 512*\k
s_addc_u32 s[\d+1], s[\r+1], 0
.endm
.macro bc m,b,d,r
s_bitcmp1_b32 s[\m], \b
s_cselect_b64 s[\d:\d+1], s[\r+2:\r+3], s[\r:\r+1]
.endm
.macro W k
DW 1024*\k
.endm
.macro H k
DH 1024*\k
.endm
k2_code:
s_load_dwordx16 s[48:63], %[pp], 0x0
s_load_dwordx16 s[64:79], %[pp], 0x40
s_load_dwordx16 s[84:99], %[pp], 0x80
s_load_dword s42, %[pp], 0xc0
v_mbcnt_lo_u32_b32 v7, -1, 0
v_mbcnt_hi_u32_b32 v7, -1, v7
v_lshlrev_b32 v0, 4, v7
v_add_u32 v88, 0x1000, v0
v_add_u32 v89, 0x2000, v0
s_mov_b32 s44, %[alo]
s_mov_b32 s45, %[ahi]
global_load_dwordx4 v[132:135], v0, s[44:45]
global_load_dwordx4 v[144:147], v88, s[44:45]
global_load_dwordx4 v[148:151], v89, s[44:45]
ae 44,44,24
global_load_dwordx4 v[168:171], v0, s[44:45]
global_load_dwordx4 v[172:175], v88, s[44:45]
global_load_dwordx4 v[192:195], v89, s[44:45]
s_waitcnt lgkmcnt(0)
bc 49,0,66,60
mh 0
ad 66,0
H 0
DG 1024
ae 44,44,24
global_load_dwordx4 v[196:199], v0, s[44:45]
global_load_dwordx4 v[216:219], v88, s[44:45]
global_load_dwordx4 v[220:223], v89, s[44:45]
ae 44,44,24
global_load_dwordx4 v[224:227], v0, s[44:45]
global_load_dwordx4 v[228:231], v88, s[44:45]
global_load_dwordx4 v[240:243], v89, s[44:45]
v_and_b32 v8, 15, v7
v_lshrrev_b32 v9, 4, v7
v_lshlrev_b32 v10, 2, v7
v_add_u32 v202, s65, v0
global_load_dword v244, v10, s[92:93] offset:0
global_load_dword v245, v10, s[92:93] offset:1024
global_load_dword v246, v10, s[92:93] offset:2048
global_load_dwordx4 v[152:155], v0, s[88:89]
s_mov_b64 exec, 0xff
global_load_dwordx4 v[156:159], v202, s[88:89]
s_mov_b64 exec, -1
s_branch 28f
FY 4
FY 8
FY 12
FY 16
FY 20
FY 24
FY 28
FY 32
FY 36
FY 40
FY 44
FY 48
FY 52
FY 56
FY 60
FY 64
FY 68
FY 72
FY 76
FY 80
FY 84
FY 88
FY 92
FY 96
FY 100
FY 104
FY 108
FY 112
FY 116
FY 120
FY 124
FY 128
28:
FX
s_mov_b32 s92, s64
s_mov_b32 s93, 0x0f0f0f0f
v_add_u32 v1, %[ab], v0
v_add_u32 v5, 0xc000, v1
v_and_b32 v10, 3, v7
v_lshrrev_b32 v11, 2, v7
v_lshl_add_u32 v11, v10, 2, v11
v_and_b32 v11, 15, v11
mw 0
ad 86,0
DR 0
v_lshl_add_u32 v11, v10, 4, v11
v_lshl_add_u32 v2, v11, 4, %[hb]
v_lshl_add_u32 v51, v11, 3, %[hb]
v_lshl_add_u32 v4, v8, 4, %[bb]
s_mov_b32 s38, 0xffff0000
s_mov_b32 s80, 0x40000
s_mov_b32 s81, s52
s_mov_b32 s39, s54
v_lshl_add_u32 v46, v9, 4, %[sb]
v_add_u32 v46, 8192, v46
v_bfe_u32 v10, v7, 1, 2
DR 1024
v_bfe_u32 v11, v7, 3, 1
v_and_b32 v12, 1, v7
v_lshl_add_u32 v11, v11, 1, v12
v_lshl_add_u32 v10, v10, 2, v11
v_lshl_add_u32 v10, v9, 4, v10
v_lshl_add_u32 v90, v10, 2, %[sw]
DR 2048
v_lshl_add_u32 v91, v7, 2, %[sw]
s_and_b32 s42, s51, 0xffff
s_lshr_b32 s43, s51, 16
s_add_u32 s42, s42, %[sb]
s_add_u32 s43, s43, %[sb]
v_lshl_add_u32 v200, v9, 4, s42
v_mov_b32 v201, s43
v_mov_b32 v128, 0x362aaa8f
v_mov_b32 v129, 0x362aaa8f
v_mov_b32 v130, 0x362aaa8f
v_mov_b32 v131, 0x362aaa8f
mw 3
ad 86,6
DR 0
AW 32,128
AW 33,129
AW 34,130
AW 35,131
v_mov_b32 v95, 0
ds_write_b32 v0, v95
s_nop 1
v_mov_b32 v95, 0x7f800000
s_mov_b32 s44, 0x44100000
WV 19
v_add_f64 v[132:133], v[132:133], v[168:169]
WV 18
v_add_f64 v[144:145], v[144:145], v[172:173]
DR 1024
WV 18
v_add_f64 v[148:149], v[148:149], v[192:193]
v_add_f64 v[134:135], v[134:135], v[170:171]
v_add_f64 v[146:147], v[146:147], v[174:175]
v_add_f64 v[150:151], v[150:151], v[194:195]
WV 15
v_add_f64 v[132:133], v[132:133], v[196:197]
WV 14
v_add_f64 v[144:145], v[144:145], v[216:217]
DR 2048
WV 14
v_add_f64 v[148:149], v[148:149], v[220:221]
v_add_f64 v[134:135], v[134:135], v[198:199]
v_add_f64 v[146:147], v[146:147], v[218:219]
v_add_f64 v[150:151], v[150:151], v[222:223]
WV 13
v_add_f64 v[132:133], v[132:133], v[224:225]
WV 12
v_add_f64 v[144:145], v[144:145], v[228:229]
bc 49,1,66,60
mh 9
ad 66,192
H 0
WV 12
v_add_f64 v[148:149], v[148:149], v[240:241]
)"
R"(v_add_f64 v[134:135], v[134:135], v[226:227]
v_add_f64 v[146:147], v[146:147], v[230:231]
v_add_f64 v[150:151], v[150:151], v[242:243]
v_cvt_f32_f64 v248, v[132:133]
v_cvt_f32_f64 v252, v[144:145]
DG 1024
v_cvt_f32_f64 v72, v[148:149]
v_cvt_f32_f64 v249, v[134:135]
v_cvt_f32_f64 v253, v[146:147]
v_cvt_f32_f64 v73, v[150:151]
v_mul_f32 v250, s55, v248
v_mul_f32 v254, s55, v252
bc 48,1,64,56
mh 12
ad 64,192
H 0
v_mul_f32 v74, s55, v72
v_mul_f32 v251, s55, v249
v_mul_f32 v255, s55, v253
v_mul_f32 v75, s55, v73
CV 251,250,251
CV 255,254,255
DG 1024
CV 75,74,75
UL 250,251
UL 254,255
UL 74,75
UH 251,251
UH 255,255
mw 24
ad 86,48
DR 0
UH 75,75
v_add_f32 v250, 0x358637bd, v250
v_add_f32 v254, 0x358637bd, v254
v_add_f32 v74, 0x358637bd, v74
v_add_f32 v251, 0x358637bd, v251
v_add_f32 v255, 0x358637bd, v255
DR 1024
v_add_f32 v75, 0x358637bd, v75
CV 251,250,251
CV 255,254,255
CV 75,74,75
UL 250,251
UL 254,255
DR 2048
UL 74,75
UH 251,251
UH 255,255
UH 75,75
v_sqrt_f32 v250, v250
v_sqrt_f32 v254, v254
mw 27
ad 86,54
DR 0
v_sqrt_f32 v74, v74
v_sqrt_f32 v251, v251
v_sqrt_f32 v255, v255
v_sqrt_f32 v75, v75
CV 251,250,251
CV 255,254,255
DR 1024
CV 75,74,75
UL 250,251
UL 254,255
UL 74,75
UH 251,251
UH 255,255
DR 2048
UH 75,75
v_cmp_le_f32 vcc, s44, v248
v_cmp_le_f32 s[54:55], s44, v249
s_nop 0
v_cndmask_b32 v248, v95, v250, vcc
v_cndmask_b32 v249, v95, v251, s[54:55]
v_add_f32 v248, v248, v249
v_cmp_le_f32 vcc, s44, v252
v_cmp_le_f32 s[54:55], s44, v253
s_nop 0
v_cndmask_b32 v252, v95, v254, vcc
v_cndmask_b32 v253, v95, v255, s[54:55]
v_add_f32 v252, v252, v253
v_cmp_le_f32 vcc, s44, v72
v_cmp_le_f32 s[54:55], s44, v73
s_nop 0
v_cndmask_b32 v72, v95, v74, vcc
v_cndmask_b32 v73, v95, v75, s[54:55]
bc 49,2,66,60
mh 18
ad 66,384
H 0
v_add_f32 v72, v72, v73
v_add_f32 v92, v248, v252
v_add_f32 v92, v92, v72
s_nop 1
v_add_f32_dpp v92, v92, v92 row_shr:1 row_mask:0xf bank_mask:0xf
s_nop 1
v_add_f32_dpp v92, v92, v92 row_shr:2 row_mask:0xf bank_mask:0xf
s_nop 1
v_add_f32_dpp v92, v92, v92 row_shr:4 row_mask:0xf bank_mask:0xf
s_nop 1
v_add_f32_dpp v92, v92, v92 row_shr:8 row_mask:0xf bank_mask:0xf
s_nop 1
v_add_f32_dpp v92, v92, v92 row_bcast:15 row_mask:0xa bank_mask:0xf
s_nop 1
v_add_f32_dpp v92, v92, v92 row_bcast:31 row_mask:0xc bank_mask:0xf
ds_write_b32 v91, v92 offset:7168
v_mov_b32 v10, %[sb]
WL 0
s_barrier
ds_read_b32 v96, v10 offset:7420
ds_read_b32 v97, v10 offset:7676
ds_read_b32 v98, v10 offset:7932
ds_read_b32 v99, v10 offset:8188
WV 21
UL 76,244
WV 20
UL 80,245
WV 19
UL 84,246
UH 77,244
DG 1024
UH 81,245
UH 85,246
v_mov_b32 v93, 0
WL 0
v_add_f32 v96, v96, v97
v_add_f32 v98, v98, v99
v_add_f32 v96, v96, v98
bc 48,2,64,56
mh 21
ad 64,384
H 0
v_mul_f32 v94, 0x3a2aaaab, v96
v_cmp_nlt_f32 s[44:45], v96, v95
CV 94,94,94
UL 94,94
v_add_f32 v94, 0x358637bd, v94
CV 94,94,94
UL 94,94
H 1
v_rcp_f32 v94, v94
s_nop 0
v_mul_f32 v250, v94, v250
v_mul_f32 v254, v94, v254
v_mul_f32 v74, v94, v74
v_mul_f32 v251, v94, v251
v_mul_f32 v255, v94, v255
v_mul_f32 v75, v94, v75
H 2
CV 251,250,251
CV 255,254,255
CV 75,74,75
UL 250,251
UL 254,255
UL 74,75
mw 48
ad 86,96
DR 0
UH 251,251
UH 255,255
UH 75,75
v_fma_f32 v250, v76, v250, 1.0
v_fma_f32 v254, v80, v254, 1.0
v_fma_f32 v74, v84, v74, 1.0
DR 1024
v_fma_f32 v251, v77, v251, 1.0
v_fma_f32 v255, v81, v255, 1.0
v_fma_f32 v75, v85, v75, 1.0
v_cvt_pk_f16_f32 v76, v250, v251
v_cvt_pk_f16_f32 v80, v254, v255
v_cvt_pk_f16_f32 v84, v74, v75
ds_write_b32 v90, v76 offset:8192
ds_write_b32 v90, v80 offset:1024
ds_write_b32 v90, v84 offset:2048
v_max3_f32 v93, |v250|, |v251|, v93
DR 2048
v_max3_f32 v93, |v254|, |v255|, v93
v_max3_f32 v93, |v74|, |v75|, v93
v_cmp_lt_f32 vcc, 0x43000000, v93
WL 0
WV 16
s_barrier
TY 106,520
TY 110,14344
TX 58,0
TX 62,13824
TY 66,512
TY 70,14336
RS 14,0
RS 18,64
ds_read_b128 v[176:179], v200
ds_read_b128 v[180:183], v201
R 0
R 2
FG
s_mov_b32 s54, 0x06020400
s_mov_b32 s55, 0x07030501
WL 13
AN 104,106
AN 105,107
SH 106
SH 107
mw 51
ad 86,102
DR 0
AN 106,106
AN 107,107
WL 12
AN 108,110
AN 109,111
SH 110
SH 111
DR 1024
AN 110,110
AN 111,111
WL 10
Q 0
Q 2
DR 2048
Q 4
Q 6
R 4
R 6
WL 7
v_pk_mul_f16 v22, v14, s92
v_pk_mul_f16 v23, v15, s92
v_pk_mul_f16 v24, v16, s92
v_pk_mul_f16 v25, v17, s92
v_pk_mul_f16 v26, v18, s92
v_pk_mul_f16 v27, v19, s92
v_pk_mul_f16 v28, v20, s92
v_pk_mul_f16 v29, v21, s92
PF 56,0
PF 57,1
PF 58,2
PF 59,3
PF 60,4
PF 61,5
PF 62,6
PF 63,7
Q 8
Q 10
Q 12
Q 14
PF 64,0
PF 65,1
PF 66,2
PF 67,3
PF 68,4
PF 69,5
PF 70,6
PF 71,7
PM 152,176
PM 153,177
PM 154,178
PM 155,179
PM 156,180
PM 157,181
PM 158,182
PM 159,183
s_mov_b32 s42, 0x1c001c00
v_pk_mul_f16 v152, v152, s42
v_pk_mul_f16 v153, v153, s42
v_pk_mul_f16 v154, v154, s42
v_pk_mul_f16 v155, v155, s42
v_pk_mul_f16 v156, v156, s42
v_pk_mul_f16 v157, v157, s42
v_pk_mul_f16 v158, v158, s42
v_pk_mul_f16 v159, v159, s42
global_store_dwordx4 v0, v[152:155], s[90:91] sc1
s_mov_b64 exec, 0xff
global_store_dwordx4 v202, v[156:159], s[90:91] sc1
s_mov_b64 exec, -1
s_mov_b32 s42, 0x3c003c00
v_dot2_f32_f16 v52, v152, s42, 0
v_dot2_f32_f16 v53, v156, s42, 0
s_nop 1
v_dot2_f32_f16 v52, v153, s42, v52
v_dot2_f32_f16 v53, v157, s42, v53
s_nop 1
v_dot2_f32_f16 v52, v154, s42, v52
v_dot2_f32_f16 v53, v158, s42, v53
s_nop 1
v_dot2_f32_f16 v52, v155, s42, v52
v_dot2_f32_f16 v53, v159, s42, v53
s_nop 1
WL 0
v_mfma_f32_16x16x32_f16 a[36:39], v[56:59], a[0:3], a[32:35]
mw 72
RS 14,128
RS 18,192
TY 106,5128
M 1
ad 86,144
DR 0
TY 110,18952
v_mfma_f32_16x16x32_f16 a[60:63], v[56:59], a[8:11], a[32:35]
DR 1024
TX 32,4608
TX 36,18432
M 13
DR 2048
TY 40,5120
TY 44,18944
v_mfma_f32_16x16x32_f16 a[40:43], v[64:67], a[0:3], a[32:35]
mw 75
M 3
R 8
v_mfma_f32_16x16x32_f16 a[64:67], v[64:67], a[8:11], a[32:35]
ad 86,150
DR 0
M 15
R 10
v_mfma_f32_16x16x32_f16 a[84:87], v[56:59], a[16:19], a[32:35]
WL 10
DR 1024
v_pk_mul_f16 v22, v14, s92
M 25
DR 2048
v_pk_mul_f16 v23, v15, s92
v_mfma_f32_16x16x32_f16 a[88:91], v[64:67], a[16:19], a[32:35]
v_pk_mul_f16 v24, v16, s92
v_pk_mul_f16 v25, v17, s92
M 27
v_pk_mul_f16 v26, v18, s92
v_pk_mul_f16 v27, v19, s92
v_mfma_f32_16x16x32_f16 a[108:111], v[56:59], a[24:27], a[32:35]
WV 19
v_pk_mul_f16 v28, v20, s92
v_pk_mul_f16 v29, v21, s92
M 37
WL 0
AN 104,106
AN 105,107
v_mfma_f32_16x16x32_f16 a[112:115], v[64:67], a[24:27], a[32:35]
s_barrier
R 24
M 39
R 26
v_mfma_f32_16x16x32_f16 a[132:135], v[56:59], a[0:3], a[32:35]
mw 0
SH 106
SH 107
M 49
ad 86,192
DR 0
v_mfma_f32_16x16x32_f16 a[136:139], v[64:67], a[0:3], a[32:35]
DR 1024
AN 106,106
M 51
R 28
v_mfma_f32_16x16x32_f16 a[156:159], v[56:59], a[8:11], a[32:35]
DR 2048
AN 107,107
M 61
mw 3
AN 108,110
AN 109,111
v_mfma_f32_16x16x32_f16 a[160:163], v[64:67], a[8:11], a[32:35]
ad 86,198
DR 0
M 63
WL 4
R 30
v_mfma_f32_16x16x32_f16 a[180:183], v[56:59], a[16:19], a[32:35]
DR 1024
SH 110
M 73
DR 2048
SH 111
v_mfma_f32_16x16x32_f16 a[184:187], v[64:67], a[16:19], a[32:35]
AN 110,110
AN 111,111
M 75
WL 4
R 32
v_mfma_f32_16x16x32_f16 v[136:139], v[56:59], a[24:27], v[128:131]
PA_ 30,104,32
PA_ 31,105,33
M 85
PB_ 32,104,32
PB_ 33,105,33
v_mfma_f32_16x16x32_f16 v[140:143], v[64:67], a[24:27], v[128:131]
PA_ 34,108,36
PA_ 35,109,37
M 87
WL 4
R 34
v_mfma_f32_16x16x32_f16 v[160:163], v[56:59], a[0:3], v[128:131]
PB_ 36,108,36
PB_ 37,109,37
M 97
PF 30,0
PF 31,1
v_mfma_f32_16x16x32_f16 v[164:167], v[64:67], a[0:3], v[128:131]
PF 32,2
PF 33,3
M 99
WL 4
)"
R"(PF 34,4
PF 35,5
v_mfma_f32_16x16x32_f16 v[184:187], v[56:59], a[8:11], v[128:131]
WV 14
PF 36,6
PF 37,7
M 109
WL 0
AW 200,30
AW 201,31
v_mfma_f32_16x16x32_f16 v[188:191], v[64:67], a[8:11], v[128:131]
s_barrier
R 48
TY 114,6664
M 111
R 50
TY 118,20488
v_mfma_f32_16x16x32_f16 v[208:211], v[56:59], a[16:19], v[128:131]
mw 24
AW 202,32
AW 203,33
M 121
ad 86,240
DR 0
v_mfma_f32_16x16x32_f16 v[232:235], v[56:59], a[24:27], v[128:131]
DR 1024
AW 204,34
M 133
DR 2048
AW 205,35
v_mfma_f32_16x16x32_f16 v[212:215], v[64:67], a[16:19], v[128:131]
mw 27
AW 206,36
AW 207,37
PA_ 38,106,40
PA_ 39,107,41
PB_ 40,106,40
PB_ 41,107,41
M 123
WL 0
R 52
PA_ 42,110,44
PA_ 43,111,45
PB_ 44,110,44
PB_ 45,111,45
AN 112,114
AN 113,115
SH 114
SH 115
v_mfma_f32_16x16x32_f16 v[236:239], v[64:67], a[24:27], v[128:131]
ad 86,246
DR 0
PF 38,0
PF 39,1
PF 40,2
PF 41,3
AN 114,114
AN 115,115
AN 116,118
AN 117,119
M 135
R 54
PF 42,4
PF 43,5
PF 44,6
PF 45,7
SH 118
SH 119
AN 118,118
AN 119,119
NA 36,0,200
DR 1024
AW 208,38
AW 209,39
AW 210,40
AW 211,41
AW 212,42
NA 36,4,204
DR 2048
AW 213,43
AW 214,44
AW 215,45
NA 60,8,200
bc 49,3,66,60
TX 32,6144
TX 36,19968
NA 60,12,204
mh 0
TY 40,6656
TY 44,20480
WL 2
PA_ 30,112,32
PA_ 31,113,33
PB_ 32,112,32
PB_ 33,113,33
PA_ 34,116,36
PA_ 35,117,37
PB_ 36,116,36
PB_ 37,117,37
NA 40,0,208
ad 66,576
H 0
PF 30,0
PF 31,1
PF 32,2
PF 33,3
PF 34,4
PF 35,5
PF 36,6
PF 37,7
NA 40,4,212
WL 0
DG 1024
AW 216,30
AW 217,31
AW 218,32
AW 219,33
AW 220,34
AW 221,35
AW 222,36
AW 223,37
NA 64,8,208
bc 48,3,64,56
TY 106,9736
TY 110,23560
NA 64,12,212
mh 3
PA_ 38,114,40
PA_ 39,115,41
PB_ 40,114,40
PB_ 41,115,41
PA_ 42,118,44
PA_ 43,119,45
PB_ 44,118,44
PB_ 45,119,45
PF 38,0
PF 39,1
v_mfma_f32_16x16x32_f16 a[44:47], a[216:219], a[0:3], a[32:35]
ad 64,576
H 0
PF 40,2
PF 41,3
PF 42,4
PF 43,5
PF 44,6
PF 45,7
AW 224,38
AW 225,39
NA 44,4,220
H 1
AW 226,40
AW 227,41
AW 228,42
AW 229,43
AW 230,44
AW 231,45
v_mfma_f32_16x16x32_f16 a[68:71], a[216:219], a[8:11], a[32:35]
H 2
RS 14,256
TX 58,9216
NA 68,12,220
RS 18,320
TX 62,23040
TY 66,9728
v_mfma_f32_16x16x32_f16 a[48:51], a[224:227], a[0:3], a[32:35]
WL 5
TY 70,23552
AN 104,106
NA 48,4,228
R 56
v_mfma_f32_16x16x32_f16 a[72:75], a[224:227], a[8:11], a[32:35]
AN 105,107
SH 106
NA 72,12,228
R 58
NA 84,16,200
SH 107
AN 106,106
NA 84,20,204
WL 7
v_pk_mul_f16 v22, v14, s92
v_pk_mul_f16 v23, v15, s92
NA 88,16,208
v_pk_mul_f16 v24, v16, s92
v_pk_mul_f16 v25, v17, s92
NA 88,20,212
v_pk_mul_f16 v26, v18, s92
v_pk_mul_f16 v27, v19, s92
v_mfma_f32_16x16x32_f16 a[92:95], a[216:219], a[16:19], a[32:35]
v_pk_mul_f16 v28, v20, s92
v_pk_mul_f16 v29, v21, s92
NA 92,20,220
AN 107,107
AN 108,110
v_mfma_f32_16x16x32_f16 a[96:99], a[224:227], a[16:19], a[32:35]
AN 109,111
SH 110
NA 96,20,228
SH 111
AN 110,110
NA 108,24,200
WL 5
AN 111,111
P 0
NA 108,28,204
Q 1
NA 112,24,208
Q 3
NA 112,28,212
Q 5
v_mfma_f32_16x16x32_f16 a[116:119], a[216:219], a[24:27], a[32:35]
WV 17
P 7
PF 56,0
NA 116,28,220
WL 0
PF 57,1
PF 58,2
v_mfma_f32_16x16x32_f16 a[120:123], a[224:227], a[24:27], a[32:35]
s_barrier
R 72
TY 114,11272
NA 120,28,228
R 74
TY 118,25096
NA 132,0,200
mw 48
v_mov_b32 v54, 1
s_mov_b64 exec, 1
FS
s_mov_b64 exec, -1
NA 132,4,204
ad 86,288
DR 0
TX 74,10752
NA 136,0,208
PF 59,3
PF 60,4
TX 78,24576
NA 136,4,212
DR 1024
PF 61,5
TY 82,11264
v_mfma_f32_16x16x32_f16 a[140:143], a[216:219], a[0:3], a[32:35]
PF 62,6
PF 63,7
TY 86,25088
NA 140,4,220
DR 2048
P 8
TY 122,12808
v_mfma_f32_16x16x32_f16 a[144:147], a[224:227], a[0:3], a[32:35]
Q 9
TY 126,26632
NA 144,4,228
WL 6
R 76
TX 90,12288
NA 156,8,200
mw 51
Q 11
TX 94,26112
NA 156,12,204
ad 86,294
DR 0
TY 98,12800
NA 160,8,208
Q 13
TY 102,26624
NA 160,12,212
DR 1024
P 15
v_mfma_f32_16x16x32_f16 a[164:167], a[216:219], a[8:11], a[32:35]
PF 64,0
PF 65,1
NA 164,12,220
DR 2048
PF 66,2
v_mfma_f32_16x16x32_f16 a[168:171], a[224:227], a[8:11], a[32:35]
PF 67,3
PF 68,4
NA 168,12,228
R 78
NA 180,16,200
bc 49,4,66,60
PF 69,5
NA 180,20,204
mh 9
PF 70,6
PF 71,7
NA 184,16,208
ad 66,768
H 0
NA 184,20,212
AN 112,114
AN 113,115
v_mfma_f32_16x16x32_f16 a[188:191], a[216:219], a[16:19], a[32:35]
DG 1024
NA 188,20,220
SH 114
SH 115
v_mfma_f32_16x16x32_f16 a[192:195], a[224:227], a[16:19], a[32:35]
bc 48,4,64,56
AN 114,114
NA 192,20,228
WL 2
R 80
NV 136,24,200
mh 12
AN 115,115
AN 116,118
NV 136,28,204
ad 64,768
H 0
NV 140,24,208
AN 117,119
SH 118
NV 140,28,212
H 1
SH 119
v_mfma_f32_16x16x32_f16 v[144:147], a[216:219], a[24:27], v[128:131]
AN 118,118
AN 119,119
NV 144,28,220
H 2
P 16
v_mfma_f32_16x16x32_f16 v[148:151], a[224:227], a[24:27], v[128:131]
Q 17
NV 148,28,228
R 82
NV 160,0,200
Q 19
NV 160,4,204
Q 21
NV 164,0,208
P 23
PF 72,0
NV 164,4,212
PF 73,1
PF 74,2
v_mfma_f32_16x16x32_f16 v[168:171], a[216:219], a[0:3], v[128:131]
PF 75,3
PF 76,4
NV 168,4,220
PF 77,5
PF 78,6
v_mfma_f32_16x16x32_f16 v[172:175], a[224:227], a[0:3], v[128:131]
PF 79,7
P 24
NV 172,4,228
WL 2
Q 25
NV 184,8,200
Q 27
NV 184,12,204
Q 29
NV 188,8,208
P 31
PF 80,0
NV 188,12,212
PF 81,1
PF 82,2
v_mfma_f32_16x16x32_f16 v[192:195], a[216:219], a[8:11], v[128:131]
WV 23
PF 83,3
PF 84,4
NV 192,12,220
WL 0
PF 85,5
PF 86,6
v_mfma_f32_16x16x32_f16 v[196:199], a[224:227], a[8:11], v[128:131]
s_barrier
R 0
NV 196,12,228
R 2
NV 208,16,200
mw 72
PF 87,7
AN 120,122
NV 208,20,204
ad 86,336
DR 0
NV 232,24,200
AN 121,123
SH 122
NV 232,28,204
DR 1024
SH 123
NV 212,16,208
AN 122,122
AN 123,123
NV 212,20,212
DR 2048
AN 124,126
NV 236,24,208
AN 125,127
SH 126
NV 236,28,212
mw 75
SH 127
)"
R"(AN 126,126
v_mfma_f32_16x16x32_f16 v[216:219], a[216:219], a[16:19], v[128:131]
ad 86,342
DR 0
NV 216,20,220
AN 127,127
P 32
v_mfma_f32_16x16x32_f16 v[240:243], a[216:219], a[24:27], v[128:131]
DR 1024
P 33
NV 240,28,220
Q 34
v_mfma_f32_16x16x32_f16 v[220:223], a[224:227], a[16:19], v[128:131]
DR 2048
P 36
NV 220,20,228
R 4
v_mfma_f32_16x16x32_f16 v[244:247], a[224:227], a[24:27], v[128:131]
Q 37
NV 244,28,228
WL 2
R 6
M 0
P 39
PF 88,0
M 1
PF 89,1
PF 90,2
M 12
PF 91,3
PF 92,4
M 13
PF 93,5
PF 94,6
M 2
PF 95,7
P 40
M 3
Q 41
M 14
Q 43
M 15
Q 45
M 4
P 47
PF 96,0
M 5
PF 97,1
PF 98,2
M 16
PF 99,3
PF 100,4
M 17
PF 101,5
PF 102,6
M 6
PF 103,7
M 7
RS 14,384
RS 18,448
M 18
M 19
v_mfma_f32_16x16x32_f16 a[52:55], v[88:91], a[0:3], a[32:35]
v_lshrrev_b32 v9, 8, v0
v_bfe_u32 v8, v0, 4, 4
M 9
v_lshlrev_b32 v10, 2, v9
v_mfma_f32_16x16x32_f16 a[76:79], v[88:91], a[8:11], a[32:35]
s_mov_b32 s42, 0xcb527610
M 21
s_movk_i32 s43, 0xc00
WL 0
v_pk_mul_f16 v22, v14, s92
v_pk_mul_f16 v23, v15, s92
v_mfma_f32_16x16x32_f16 a[56:59], v[96:99], a[0:3], a[32:35]
v_pk_mul_f16 v24, v16, s92
v_pk_mul_f16 v25, v17, s92
M 11
R 8
v_mfma_f32_16x16x32_f16 a[80:83], v[96:99], a[8:11], a[32:35]
v_pk_mul_f16 v26, v18, s92
v_pk_mul_f16 v27, v19, s92
M 23
R 10
M 24
v_pk_mul_f16 v28, v20, s92
v_pk_mul_f16 v29, v21, s92
M 25
v_lshlrev_b32 v11, 3, v8
v_bfe_u32 v3, s42, v10, 4
M 26
v_add_u32 v10, 16, v10
v_mad_u32_u24 v3, v3, s43, v11
M 27
v_bfe_u32 v48, s42, v10, 4
v_add_u32 v10, 1, v9
M 28
v_mad_u32_u24 v48, v48, s43, v11
v_and_b32 v10, 2, v10
M 29
v_sub_u32 v10, 1, v10
v_mul_i32_i24 v49, s43, v10
M 30
M 31
v_mfma_f32_16x16x32_f16 a[100:103], v[88:91], a[16:19], a[32:35]
M 33
v_mfma_f32_16x16x32_f16 a[104:107], v[96:99], a[16:19], a[32:35]
M 35
M 36
M 37
M 38
M 39
M 40
M 41
M 42
M 43
WV 18
v_mfma_f32_16x16x32_f16 a[124:127], v[88:91], a[24:27], a[32:35]
WL 0
M 45
s_barrier
R 24
TY 106,520
v_mfma_f32_16x16x32_f16 a[128:131], v[96:99], a[24:27], a[32:35]
TY 110,14344
TX 32,0
TX 36,13824
M 47
R 26
TY 40,512
M 48
TY 44,14336
TY 114,2056
TY 118,15880
M 49
M 50
M 51
M 52
WL 9
AN 104,106
AN 105,107
M 53
bc 49,5,66,60
SH 106
M 54
mh 18
SH 107
AN 106,106
M 55
ad 66,960
H 0
v_mfma_f32_16x16x32_f16 a[148:151], v[88:91], a[0:3], a[32:35]
WL 8
AN 107,107
AN 108,110
M 57
AN 109,111
SH 110
v_mfma_f32_16x16x32_f16 a[152:155], v[96:99], a[0:3], a[32:35]
SH 111
AN 110,110
M 59
R 28
M 60
DG 1024
M 61
WL 5
AN 111,111
PA_ 30,104,32
M 62
PA_ 31,105,33
PB_ 32,104,32
M 63
PB_ 33,105,33
PA_ 34,108,36
M 64
PA_ 35,109,37
PB_ 36,108,36
M 65
PB_ 37,109,37
PF 30,0
M 66
v_lshlrev_b32 v55, 4, v0
PF 31,1
M 67
FL
PF 32,2
v_mfma_f32_16x16x32_f16 a[172:175], v[88:91], a[8:11], a[32:35]
PF 33,3
PF 34,4
M 69
PF 35,5
PF 36,6
v_mfma_f32_16x16x32_f16 a[176:179], v[96:99], a[8:11], a[32:35]
PF 37,7
AW 200,30
M 71
R 30
M 72
AW 201,31
AW 202,32
M 73
AW 203,33
AW 204,34
M 74
AW 205,35
AW 206,36
M 75
WL 2
AW 207,37
PA_ 38,106,40
M 76
PA_ 39,107,41
PB_ 40,106,40
TX 32,1536
M 77
PB_ 41,107,41
PA_ 42,110,44
TX 36,15360
M 78
PA_ 43,111,45
PB_ 44,110,44
M 79
PB_ 45,111,45
PF 38,0
v_mfma_f32_16x16x32_f16 a[196:199], v[88:91], a[16:19], a[32:35]
PF 39,1
PF 40,2
M 81
PF 41,3
PF 42,4
v_mfma_f32_16x16x32_f16 v[132:135], v[96:99], a[16:19], v[128:131]
PF 43,5
PF 44,6
M 83
R 32
M 84
PF 45,7
AW 208,38
M 85
bc 48,5,64,56
AW 209,39
M 86
mh 21
AW 210,40
AW 211,41
M 87
ad 64,960
H 0
M 88
AW 212,42
AW 213,43
M 89
AW 214,44
AW 215,45
M 90
TY 40,2048
TY 44,15872
TY 122,3592
M 91
H 1
AN 112,114
TY 126,17416
v_mfma_f32_16x16x32_f16 v[152:155], v[88:91], a[24:27], v[128:131]
AN 113,115
SH 114
M 93
SH 115
AN 114,114
v_mfma_f32_16x16x32_f16 v[156:159], v[96:99], a[24:27], v[128:131]
AN 115,115
AN 116,118
M 95
R 34
M 96
H 2
AN 117,119
M 97
SH 118
SH 119
M 98
AN 118,118
AN 119,119
M 99
WL 6
PA_ 30,112,32
PA_ 31,113,33
M 100
PB_ 32,112,32
PB_ 33,113,33
M 101
PA_ 34,116,36
PA_ 35,117,37
M 102
PB_ 36,116,36
PB_ 37,117,37
M 103
PF 30,0
PF 31,1
v_mfma_f32_16x16x32_f16 v[176:179], v[88:91], a[0:3], v[128:131]
PF 32,2
PF 33,3
M 105
PF 34,4
PF 35,5
v_mfma_f32_16x16x32_f16 v[180:183], v[96:99], a[0:3], v[128:131]
PF 36,6
PF 37,7
M 107
AW 216,30
AW 217,31
M 108
AW 218,32
AW 219,33
M 109
AW 220,34
AW 221,35
M 110
WV 3
v_cmp_eq_u32 vcc, 0, v54
AW 222,36
M 111
WL 0
AW 223,37
PA_ 38,114,40
M 112
PA_ 39,115,41
PB_ 40,114,40
TX 32,3072
M 113
FB
PB_ 41,115,41
PA_ 42,118,44
TX 36,16896
M 114
PA_ 43,119,45
PB_ 44,118,44
M 115
PB_ 45,119,45
PF 38,0
v_mfma_f32_16x16x32_f16 v[200:203], v[88:91], a[8:11], v[128:131]
PF 39,1
PF 40,2
M 117
s_barrier
R 48
v_mfma_f32_16x16x32_f16 v[204:207], v[96:99], a[8:11], v[128:131]
mw 0
PF 41,3
PF 42,4
M 119
R 50
M 120
ad 84,384
W 0
M 121
W 1
PF 43,5
M 132
W 2
PF 44,6
M 133
mw 3
PF 45,7
AW 224,38
M 122
ad 84,390
W 0
M 123
W 1
AW 225,39
M 134
W 2
AW 226,40
M 135
mw 24
AW 227,41
AW 228,42
M 124
ad 84,432
W 0
M 125
W 1
AW 229,43
M 136
W 2
AW 230,44
M 137
mw 27
AW 231,45
AN 120,122
M 126
ad 84,438
W 0
TY 40,3584
M 127
W 1
TY 44,17408
M 138
W 2
)"
R"(AN 121,123
M 139
SH 122
SH 123
v_mfma_f32_16x16x32_f16 v[224:227], v[88:91], a[16:19], v[128:131]
AN 122,122
AN 123,123
M 129
AN 124,126
AN 125,127
v_mfma_f32_16x16x32_f16 v[248:251], v[88:91], a[24:27], v[128:131]
SH 126
SH 127
M 141
AN 126,126
AN 127,127
v_mfma_f32_16x16x32_f16 v[228:231], v[96:99], a[16:19], v[128:131]
WL 2
PA_ 30,120,32
PA_ 31,121,33
M 131
R 52
v_mfma_f32_16x16x32_f16 v[252:255], v[96:99], a[24:27], v[128:131]
PB_ 32,120,32
PB_ 33,121,33
M 143
R 54
NA 36,0,200
PA_ 34,124,36
PA_ 35,125,37
NA 36,4,204
PB_ 36,124,36
PB_ 37,125,37
NA 60,8,200
PF 30,0
PF 31,1
NA 60,12,204
PF 32,2
PF 33,3
NA 40,0,208
PF 34,4
PF 35,5
NA 40,4,212
PF 36,6
PF 37,7
NA 64,8,208
AW 232,30
AW 233,31
NA 64,12,212
AW 234,32
AW 235,33
NA 44,0,216
AW 236,34
AW 237,35
NA 44,4,220
WL 4
AW 238,36
AW 239,37
PA_ 38,122,40
NA 68,8,216
PA_ 39,123,41
PB_ 40,122,40
PB_ 41,123,41
NA 68,12,220
PA_ 42,126,44
PA_ 43,127,45
PB_ 44,126,44
NA 48,0,224
PB_ 45,127,45
PF 38,0
PF 39,1
NA 48,4,228
PF 40,2
PF 41,3
PF 42,4
NA 72,8,224
PF 43,5
PF 44,6
PF 45,7
NA 72,12,228
AW 240,38
AW 241,39
AW 242,40
NA 52,0,232
AW 243,41
AW 244,42
AW 245,43
NA 52,4,236
AW 246,44
AW 247,45
NA 76,8,232
NA 76,12,236
NA 56,0,240
NA 56,4,244
R 56
NA 80,8,240
NA 80,12,244
WL 2
R 58
NA 84,16,200
NA 84,20,204
NA 88,16,208
NA 88,20,212
NA 92,16,216
NA 92,20,220
NA 96,16,224
NA 96,20,228
NA 100,16,232
NA 100,20,236
NA 104,16,240
NA 104,20,244
NA 108,24,200
NA 108,28,204
NA 112,24,208
NA 112,28,212
NA 116,24,216
NA 116,28,220
NA 120,24,224
NA 120,28,228
NA 124,24,232
WL 0
NA 124,28,236
s_barrier
R 72
TY 106,5128
NA 128,24,240
mw 48
TY 110,18952
TX 58,4608
TX 62,18432
NA 128,28,244
R 74
TY 66,5120
NA 132,0,200
ad 84,480
W 0
TY 70,18944
NA 132,4,204
v_lshrrev_b32 v8, 1, v0
s_mov_b64 exec, s[76:77]
global_load_dwordx2 v[6:7], v8, s[78:79] sc1
s_mov_b64 exec, -1
NA 136,0,208
TY 114,6664
TY 118,20488
TX 74,6144
NA 136,4,212
WL 7
TX 78,19968
TY 82,6656
TY 86,20480
NA 140,0,216
W 1
AN 104,106
TY 122,8200
NA 140,4,220
AN 105,107
SH 106
TY 126,22024
NA 144,0,224
SH 107
AN 106,106
TX 90,7680
NA 144,4,228
AN 107,107
AN 108,110
TX 94,21504
NA 148,0,232
W 2
AN 109,111
TY 98,8192
NA 148,4,236
WL 9
SH 110
SH 111
TY 102,22016
NA 152,0,240
AN 110,110
AN 111,111
NA 152,4,244
R 76
NA 156,8,200
mw 51
Q 0
NA 156,12,204
ad 84,486
W 0
NA 160,8,208
Q 2
NA 160,12,212
Q 4
NA 164,8,216
Q 6
NA 164,12,220
W 1
P 8
NA 168,8,224
Q 9
NA 168,12,228
Q 11
NA 172,8,232
Q 13
NA 172,12,236
W 2
P 15
NA 176,8,240
AN 112,114
AN 113,115
NA 176,12,244
R 78
NA 180,16,200
SH 114
SH 115
NA 180,20,204
bc 49,6,66,60
AN 114,114
NA 184,16,208
mh 0
AN 115,115
AN 116,118
NA 184,20,212
ad 66,1152
H 0
NA 188,16,216
AN 117,119
SH 118
NA 188,20,220
SH 119
AN 118,118
NA 192,16,224
WL 2
AN 119,119
P 16
NA 192,20,228
DG 1024
NA 196,16,232
Q 17
NA 196,20,236
Q 19
NV 132,16,240
Q 21
NV 132,20,244
R 80
NV 136,24,200
bc 48,6,64,56
P 23
NV 136,28,204
mh 3
Q 24
NV 140,24,208
ad 64,1152
H 0
NV 140,28,212
Q 26
NV 144,24,216
Q 28
NV 144,28,220
Q 30
NV 148,24,224
H 1
AN 120,122
NV 148,28,228
AN 121,123
SH 122
NV 152,24,232
SH 123
AN 122,122
NV 152,28,236
AN 123,123
AN 124,126
NV 156,24,240
H 2
AN 125,127
NV 156,28,244
R 82
NV 160,0,200
SH 126
SH 127
NV 160,4,204
AN 126,126
AN 127,127
NV 164,0,208
Q 32
NV 164,4,212
Q 34
NV 168,0,216
Q 36
NV 168,4,220
Q 38
NV 172,0,224
Q 40
NV 172,4,228
Q 42
NV 176,0,232
Q 44
NV 176,4,236
Q 46
NV 180,0,240
NV 180,4,244
WL 2
NV 184,8,200
NV 184,12,204
NV 188,8,208
NV 188,12,212
NV 192,8,216
NV 192,12,220
NV 196,8,224
NV 196,12,228
WV 18
NV 200,8,232
WL 0
NV 200,12,236
s_barrier
R 0
NV 204,8,240
mw 72
NV 204,12,244
R 2
NV 208,16,200
ad 84,528
W 0
NV 208,20,204
NV 232,24,200
NV 232,28,204
NV 212,16,208
W 1
NV 212,20,212
NV 236,24,208
NV 236,28,212
NV 216,16,216
W 2
NV 216,20,220
ae 44,70,0
NV 240,24,216
s_bitcmp1_b32 s50, 0
s_cselect_b32 s51, 24576, 0
v_add_u32 v47, s51, v3
NV 240,28,220
XL 14,0
NV 220,16,224
mw 75
NV 220,20,228
ad 84,534
W 0
NV 244,24,224
XL 16,768
NV 244,28,228
NV 224,16,232
NV 224,20,236
W 1
NV 248,24,232
NV 248,28,236
NV 228,16,240
XL 18,1536
NV 228,20,244
R 4
NV 252,24,240
W 2
NV 252,28,244
WL 2
R 6
M 0
M 1
M 12
XL 20,2304
M 13
M 2
M 3
WV 20
v_or_b32 v6, v6, v7
s_mov_b64 exec, s[76:77]
v_cmp_ne_u32 %[flg], 0, v6
s_mov_b64 exec, -1
M 14
M 15
M 4
M 5
M 16
M 17
M 6
M 7
M 18
M 19
M 8
M 9
M 20
M 21
M 10
M 11
R 8
M 22
M 23
WL 4
R 10
M 24
M 25
M 26
M 27
M 28
M 29
)"
R"(M 30
M 31
M 32
M 33
M 34
M 35
WL 4
M 36
M 37
M 38
M 39
M 40
M 41
M 42
M 43
WV 15
M 44
WL 0
M 45
s_barrier
R 24
M 46
mw 0
M 47
R 26
M 48
ad 84,576
W 0
M 49
TY 106,9736
TY 110,23560
M 50
M 51
W 1
M 52
M 53
M 54
W 2
M 55
M 56
M 57
mw 3
M 58
ad 84,582
W 0
M 59
R 28
M 60
WL 3
AN 104,106
AN 105,107
M 61
W 1
SH 106
M 62
SH 107
AN 106,106
M 63
WL 2
AN 107,107
AN 108,110
M 64
W 2
AN 109,111
M 65
SH 110
SH 111
M 66
AN 110,110
AN 111,111
M 67
bc 49,7,66,60
TY 114,11272
M 68
mh 9
TY 118,25096
M 69
ad 66,1344
H 0
M 70
M 71
R 30
M 72
DG 1024
M 73
M 74
M 75
bc 48,7,64,56
TY 122,12808
M 76
mh 12
TY 126,26632
M 77
ad 64,1344
H 0
M 78
WL 5
AN 112,114
AN 113,115
M 79
SH 114
SH 115
M 80
H 1
AN 114,114
M 81
WL 4
AN 115,115
AN 116,118
M 82
AN 117,119
SH 118
M 83
R 32
M 84
H 2
SH 119
M 85
AN 118,118
AN 119,119
M 86
WL 3
AN 120,122
AN 121,123
M 87
s_load_dwordx4 s[88:91], %[pp], 0xc0
s_load_dwordx2 s[86:87], %[pp], 0xd0
SH 122
M 88
SH 123
AN 122,122
M 89
WL 2
AN 123,123
AN 124,126
M 90
AN 125,127
SH 126
M 91
SH 127
AN 126,126
M 92
AN 127,127
M 93
M 94
M 95
R 34
M 96
M 97
M 98
M 99
M 100
M 101
M 102
M 103
M 104
M 105
M 106
M 107
R 48
M 108
M 109
M 110
M 111
M 112
M 113
M 114
M 115
M 116
s_waitcnt lgkmcnt(0)
M 117
M 118
M 119
R 50
M 120
v_mov_b32 v54, v52
M 121
M 132
M 133
TX 58,9216
TX 62,23040
M 122
v_permlane32_swap_b32 v52, v54
M 123
M 134
M 135
TY 66,9728
TY 70,23552
M 124
v_add_f32 v52, v52, v54
v_mov_b32 v54, v52
M 125
M 136
ae 44,70,0
M 137
TX 74,10752
TX 78,24576
M 126
s_bfe_u32 s51, s50, 0x10001
WL 4
Q 0
M 127
Q 2
M 138
Q 4
M 139
TY 82,11264
TY 86,25088
M 128
Q 6
M 129
WL 4
Q 8
M 140
Q 10
M 141
TX 90,12288
TX 94,26112
M 130
Q 12
M 131
R 52
M 142
Q 14
M 143
TY 98,12800
TY 102,26624
M 0
R 54
M 1
WL 10
Q 16
M 12
Q 18
M 13
Q 20
M 2
Q 22
M 3
WL 8
Q 24
M 14
Q 26
M 15
Q 28
M 4
Q 30
M 5
WL 6
Q 32
M 16
Q 34
M 17
Q 36
M 6
Q 38
M 7
WL 2
Q 40
M 18
Q 42
M 19
Q 44
M 8
Q 46
M 9
v_permlane16_swap_b32 v52, v54
v_mad_i32_i24 v47, v49, s51, v48
M 20
XL 22,0
M 21
XL 24,768
M 10
XL 26,1536
v_add_f32 v52, v52, v54
M 11
R 56
M 22
XL 28,2304
M 23
R 58
M 24
v_permlane16_swap_b32 v52, v53
v_mov_b32 v54, s88
M 25
v_mov_b32 v53, s89
M 26
M 27
M 28
v_permlane16_swap_b32 v54, v53
v_bfe_u32 v53, v0, 2, 6
M 29
M 30
M 31
v_add_u32 v54, v53, v54
M 32
M 33
WV 9
M 34
WL 0
FP
M 35
M 36
M 37
M 38
M 39
M 40
UL 6,14
UH 7,14
M 41
AR 8,36
AR 9,60
M 42
AD 8,6
AD 9,7
M 43
AW 36,8
AW 60,9
M 44
UL 6,15
UH 7,15
M 45
FR
AR 8,84
M 46
AR 9,108
AD 8,6
M 47
AD 9,7
AW 84,8
M 48
AW 108,9
UL 6,16
M 49
UH 7,16
AR 8,37
M 50
AR 9,61
AD 8,6
M 51
AD 9,7
AW 37,8
M 52
AW 61,9
UL 6,17
M 53
WL 0
FQ 4
UH 7,17
M 54
AR 8,85
AR 9,109
M 55
AD 8,6
AD 9,7
M 56
FZ 4
R 72
M 57
mw 24
R 74
M 58
TY 106,520
TY 110,14344
M 59
R 76
M 60
ad 84,624
W 0
M 61
AW 85,8
AW 109,9
M 62
UL 6,18
UH 7,18
M 63
W 1
AR 8,38
M 64
AR 9,62
AD 8,6
M 65
AD 9,7
AW 38,8
M 66
W 2
AW 62,9
M 67
WL 3
AN 104,106
AN 105,107
M 68
SH 106
SH 107
M 69
mw 27
AN 106,106
AN 107,107
M 70
WL 2
)"
R"(AN 108,110
AN 109,111
M 71
R 78
M 72
SH 110
SH 111
M 73
AN 110,110
AN 111,111
M 74
TY 114,2056
TY 118,15880
M 75
ad 84,630
W 0
M 76
UL 6,19
UH 7,19
M 77
AR 8,86
AR 9,110
M 78
W 1
AD 8,6
M 79
AD 9,7
AW 86,8
M 80
AW 110,9
UL 6,20
M 81
W 2
UH 7,20
M 82
AR 8,39
AR 9,63
M 83
WL 1
R 80
M 84
mw 48
AN 112,114
AN 113,115
M 85
SH 114
SH 115
M 86
AN 114,114
AN 115,115
M 87
WL 2
AN 116,118
AN 117,119
M 88
SH 118
SH 119
M 89
AN 118,118
AN 119,119
M 90
TY 122,3592
TY 126,17416
M 91
ad 84,672
M 92
W 0
AD 8,6
M 93
AD 9,7
AW 39,8
M 94
AW 63,9
UL 6,21
M 95
R 82
M 96
W 1
UH 7,21
M 97
AR 8,87
AR 9,111
M 98
AD 8,6
AD 9,7
M 99
WL 3
AN 120,122
AN 121,123
M 100
SH 122
SH 123
M 101
AN 122,122
AN 123,123
M 102
WL 2
AN 124,126
AN 125,127
M 103
SH 126
SH 127
M 104
AN 126,126
AN 127,127
M 105
W 2
AW 87,8
M 106
AW 111,9
M 107
R 0
M 108
mw 51
M 109
ad 84,678
W 0
M 110
M 111
M 112
W 1
M 113
M 114
M 115
W 2
M 116
M 117
M 118
bc 49,8,66,60
M 119
WL 2
R 2
M 120
mh 18
s_bitcmp1_b32 s87, 0
s_cselect_b32 exec_lo, 0xffffff, 0
s_mov_b32 exec_hi, 0
global_atomic_add_f32 v54, v52, s[90:91]
s_mov_b64 exec, -1
M 121
ad 66,1536
H 0
M 132
M 133
TX 58,0
TX 62,13824
M 122
DG 1024
M 123
M 134
M 135
TY 66,512
TY 70,14336
M 124
bc 48,8,64,56
M 125
mh 21
M 136
ad 64,1536
H 0
M 137
TX 74,1536
TX 78,15360
M 126
WL 4
Q 0
M 127
Q 2
M 138
Q 4
M 139
TY 82,2048
TY 86,15872
M 128
Q 6
M 129
WL 4
Q 8
M 140
Q 10
M 141
TX 90,3072
TX 94,16896
M 130
Q 12
M 131
R 4
M 142
Q 14
M 143
TY 98,3584
TY 102,17408
M 0
R 6
M 1
WL 10
Q 16
M 12
Q 18
M 13
Q 20
M 2
Q 22
M 3
WL 8
Q 24
M 14
Q 26
M 15
Q 28
M 4
Q 30
M 5
WL 6
Q 32
M 16
Q 34
M 17
Q 36
M 6
Q 38
M 7
WL 2
Q 40
M 18
Q 42
M 19
Q 44
M 8
Q 46
M 9
H 1
M 20
ae 44,68,0
M 21
s_bitcmp1_b32 s50, 2
s_cselect_b32 s51, 24576, 0
v_add_u32 v47, s51, v3
M 10
H 2
XL 30,0
M 11
R 8
M 22
XL 32,768
M 23
R 10
M 24
XL 34,1536
M 25
XL 36,2304
M 26
M 27
M 28
M 29
M 30
M 31
M 32
M 33
WV 10
M 34
WL 0
FP
M 35
M 36
M 37
s_bitcmp1_b32 s87, 1
s_cselect_b32 exec_lo, 0xffffff, 0
s_mov_b32 exec_hi, 0
global_atomic_add_f32 v54, v52, s[90:91]
s_mov_b64 exec, -1
M 38
M 39
M 40
M 41
M 42
UL 6,22
UH 7,22
M 43
AR 8,40
AR 9,64
M 44
AD 8,6
AD 9,7
M 45
FR
AW 40,8
M 46
AW 64,9
UL 6,23
M 47
UH 7,23
AR 8,88
M 48
AR 9,112
AD 8,6
M 49
AD 9,7
AW 88,8
M 50
AW 112,9
UL 6,24
M 51
UH 7,24
AR 8,41
M 52
AR 9,65
AD 8,6
M 53
WL 0
FQ 8
AD 9,7
M 54
AW 41,8
AW 65,9
M 55
UL 6,25
UH 7,25
M 56
FZ 8
R 24
M 57
mw 72
R 26
M 58
TY 106,5128
TY 110,18952
M 59
R 28
M 60
ad 84,720
W 0
M 61
AR 8,89
AR 9,113
M 62
AD 8,6
AD 9,7
M 63
W 1
AW 89,8
M 64
AW 113,9
UL 6,26
M 65
UH 7,26
AR 8,42
M 66
W 2
AR 9,66
M 67
WL 3
AN 104,106
AN 105,107
M 68
SH 106
SH 107
M 69
mw 75
AN 106,106
AN 107,107
M 70
WL 2
AN 108,110
AN 109,111
M 71
R 30
M 72
SH 110
SH 111
M 73
AN 110,110
AN 111,111
M 74
TY 114,6664
TY 118,20488
M 75
ad 84,726
W 0
M 76
AD 8,6
AD 9,7
M 77
AW 42,8
AW 66,9
M 78
W 1
UL 6,27
M 79
UH 7,27
AR 8,90
M 80
AR 9,114
AD 8,6
M 81
W 2
AD 9,7
M 82
AW 90,8
AW 114,9
)"
R"(M 83
WL 1
R 32
M 84
mw 0
AN 112,114
AN 113,115
M 85
SH 114
SH 115
M 86
AN 114,114
AN 115,115
M 87
WL 2
AN 116,118
AN 117,119
M 88
SH 118
SH 119
M 89
AN 118,118
AN 119,119
M 90
TY 122,8200
TY 126,22024
M 91
ad 84,768
M 92
W 0
UL 6,28
M 93
UH 7,28
AR 8,43
M 94
AR 9,67
AD 8,6
M 95
R 34
M 96
W 1
AD 9,7
M 97
AW 43,8
AW 67,9
M 98
UL 6,29
UH 7,29
M 99
WL 3
AN 120,122
AN 121,123
M 100
SH 122
SH 123
M 101
AN 122,122
AN 123,123
M 102
WL 2
AN 124,126
AN 125,127
M 103
SH 126
SH 127
M 104
AN 126,126
AN 127,127
M 105
W 2
AR 8,91
M 106
AR 9,115
AD 8,6
M 107
R 48
M 108
mw 3
AD 9,7
AW 91,8
M 109
ad 84,774
W 0
M 110
AW 115,9
M 111
M 112
W 1
M 113
M 114
M 115
W 2
M 116
M 117
M 118
bc 49,9,66,60
M 119
WL 2
R 50
M 120
mh 0
s_bitcmp1_b32 s87, 2
s_cselect_b32 exec_lo, 0xffffff, 0
s_mov_b32 exec_hi, 0
global_atomic_add_f32 v54, v52, s[90:91]
s_mov_b64 exec, -1
M 121
ad 66,1728
H 0
M 132
M 133
TX 58,4608
TX 62,18432
M 122
DG 1024
M 123
M 134
M 135
TY 66,5120
TY 70,18944
M 124
bc 48,9,64,56
M 125
mh 3
M 136
ad 64,1728
H 0
M 137
TX 74,6144
TX 78,19968
M 126
WL 4
Q 0
M 127
Q 2
M 138
Q 4
M 139
TY 82,6656
TY 86,20480
M 128
Q 6
M 129
WL 4
Q 8
M 140
Q 10
M 141
TX 90,7680
TX 94,21504
M 130
Q 12
M 131
R 52
M 142
Q 14
M 143
TY 98,8192
TY 102,22016
M 0
R 54
M 1
WL 10
Q 16
M 12
Q 18
M 13
Q 20
M 2
Q 22
M 3
WL 8
Q 24
M 14
Q 26
M 15
Q 28
M 4
Q 30
M 5
WL 6
Q 32
M 16
Q 34
M 17
Q 36
M 6
Q 38
M 7
WL 2
Q 40
M 18
Q 42
M 19
Q 44
M 8
Q 46
M 9
H 1
M 20
ae 44,68,0
M 21
s_bfe_u32 s51, s50, 0x10003
v_mad_i32_i24 v47, v49, s51, v48
XL 38,0
M 10
H 2
XL 40,768
M 11
R 56
M 22
XL 42,1536
M 23
R 58
M 24
XL 44,2304
M 25
M 26
M 27
M 28
M 29
M 30
M 31
M 32
M 33
WV 10
M 34
WL 0
FP
M 35
M 36
M 37
s_bitcmp1_b32 s87, 3
s_cselect_b32 exec_lo, 0xffffff, 0
s_mov_b32 exec_hi, 0
global_atomic_add_f32 v54, v52, s[90:91]
s_mov_b64 exec, -1
M 38
M 39
M 40
M 41
M 42
M 43
M 44
UL 6,30
UH 7,30
M 45
FR
AR 8,44
M 46
AR 9,68
AD 8,6
M 47
AD 9,7
AW 44,8
M 48
AW 68,9
UL 6,31
M 49
UH 7,31
AR 8,92
M 50
AR 9,116
AD 8,6
M 51
AD 9,7
AW 92,8
M 52
AW 116,9
UL 6,32
M 53
WL 0
FQ 12
UH 7,32
M 54
AR 8,45
AR 9,69
M 55
AD 8,6
AD 9,7
M 56
FZ 12
R 72
M 57
mw 24
R 74
M 58
TY 106,9736
TY 110,23560
M 59
R 76
M 60
ad 84,816
W 0
M 61
AW 45,8
AW 69,9
M 62
UL 6,33
UH 7,33
M 63
W 1
AR 8,93
M 64
AR 9,117
AD 8,6
M 65
AD 9,7
AW 93,8
M 66
W 2
AW 117,9
M 67
WL 3
AN 104,106
AN 105,107
M 68
SH 106
SH 107
M 69
mw 27
AN 106,106
AN 107,107
M 70
WL 2
AN 108,110
AN 109,111
M 71
R 78
M 72
SH 110
SH 111
M 73
AN 110,110
AN 111,111
M 74
TY 114,11272
TY 118,25096
M 75
ad 84,822
W 0
M 76
UL 6,34
UH 7,34
M 77
AR 8,46
AR 9,70
M 78
W 1
AD 8,6
M 79
AD 9,7
AW 46,8
M 80
AW 70,9
UL 6,35
M 81
W 2
UH 7,35
M 82
AR 8,94
AR 9,118
M 83
WL 1
R 80
M 84
mw 48
AN 112,114
AN 113,115
M 85
SH 114
SH 115
M 86
AN 114,114
AN 115,115
M 87
WL 2
AN 116,118
AN 117,119
M 88
SH 118
SH 119
M 89
AN 118,118
AN 119,119
M 90
TY 122,12808
TY 126,26632
M 91
ad 84,864
M 92
W 0
AD 8,6
M 93
AD 9,7
AW 94,8
M 94
AW 118,9
UL 6,36
M 95
)"
R"(R 82
M 96
W 1
UH 7,36
M 97
AR 8,47
AR 9,71
M 98
AD 8,6
AD 9,7
M 99
WL 3
AN 120,122
AN 121,123
M 100
SH 122
SH 123
M 101
AN 122,122
AN 123,123
M 102
WL 2
AN 124,126
AN 125,127
M 103
SH 126
SH 127
M 104
AN 126,126
AN 127,127
M 105
W 2
AW 47,8
M 106
AW 71,9
UL 6,37
M 107
R 0
M 108
mw 51
UH 7,37
AR 8,95
M 109
ad 84,870
W 0
M 110
AR 9,119
AD 8,6
M 111
AD 9,7
AW 95,8
M 112
W 1
AW 119,9
M 113
M 114
M 115
W 2
M 116
M 117
M 118
bc 49,10,66,60
M 119
WL 2
R 2
M 120
mh 9
s_bitcmp1_b32 s87, 4
s_cselect_b32 exec_lo, 0xffffff, 0
s_mov_b32 exec_hi, 0
global_atomic_add_f32 v54, v52, s[90:91]
s_mov_b64 exec, -1
M 121
ad 66,1920
H 0
M 132
M 133
TX 58,9216
TX 62,23040
M 122
DG 1024
M 123
M 134
M 135
TY 66,9728
TY 70,23552
M 124
bc 48,10,64,56
M 125
mh 12
M 136
ad 64,1920
H 0
M 137
TX 74,10752
TX 78,24576
M 126
WL 4
Q 0
M 127
Q 2
M 138
Q 4
M 139
TY 82,11264
TY 86,25088
M 128
Q 6
M 129
WL 4
Q 8
M 140
Q 10
M 141
TX 90,12288
TX 94,26112
M 130
Q 12
M 131
R 4
M 142
Q 14
M 143
TY 98,12800
TY 102,26624
M 0
R 6
M 1
WL 10
Q 16
M 12
Q 18
M 13
Q 20
M 2
Q 22
M 3
WL 8
Q 24
M 14
Q 26
M 15
Q 28
M 4
Q 30
M 5
WL 6
Q 32
M 16
Q 34
M 17
Q 36
M 6
Q 38
M 7
WL 2
Q 40
M 18
Q 42
M 19
Q 44
M 8
Q 46
M 9
H 1
M 20
ae 44,68,96
M 21
s_bitcmp1_b32 s50, 4
s_cselect_b32 s51, 24576, 0
v_add_u32 v47, s51, v3
M 10
H 2
XL 14,0
M 11
R 8
M 22
XL 16,768
M 23
R 10
PA 12288
DF
M 24
XL 18,1536
M 25
XL 20,2304
M 26
M 27
M 28
M 29
M 30
M 31
M 32
M 33
WV 11
M 34
WL 0
FP
M 35
M 36
M 37
s_bitcmp1_b32 s87, 5
s_cselect_b32 exec_lo, 0xffffff, 0
s_mov_b32 exec_hi, 0
global_atomic_add_f32 v54, v52, s[90:91]
s_mov_b64 exec, -1
M 38
M 39
M 40
M 41
M 42
M 43
M 44
M 45
FR
M 46
UL 6,38
UH 7,38
M 47
AR 8,48
AR 9,72
M 48
AD 8,6
AD 9,7
M 49
AW 48,8
AW 72,9
M 50
UL 6,39
UH 7,39
M 51
AR 8,96
AR 9,120
M 52
AD 8,6
AD 9,7
M 53
WL 0
FQ 16
AW 96,8
M 54
AW 120,9
UL 6,40
M 55
UH 7,40
AR 8,49
M 56
FZ 16
R 24
M 57
mw 72
R 26
M 58
TY 106,520
TY 110,14344
M 59
R 28
M 60
ad 84,912
W 0
M 61
AR 9,73
AD 8,6
M 62
AD 9,7
AW 49,8
M 63
W 1
AW 73,9
M 64
UL 6,41
UH 7,41
M 65
AR 8,97
AR 9,121
M 66
W 2
AD 8,6
M 67
WL 3
AN 104,106
AN 105,107
M 68
SH 106
SH 107
M 69
mw 75
AN 106,106
AN 107,107
M 70
WL 2
AN 108,110
AN 109,111
M 71
R 30
M 72
SH 110
SH 111
M 73
AN 110,110
AN 111,111
M 74
TY 114,2056
TY 118,15880
M 75
ad 84,918
W 0
M 76
AD 9,7
AW 97,8
M 77
AW 121,9
UL 6,42
M 78
W 1
UH 7,42
M 79
AR 8,50
AR 9,74
M 80
AD 8,6
AD 9,7
M 81
W 2
AW 50,8
M 82
AW 74,9
UL 6,43
M 83
WL 1
R 32
M 84
mw 0
AN 112,114
AN 113,115
M 85
SH 114
SH 115
M 86
AN 114,114
AN 115,115
M 87
WL 2
AN 116,118
AN 117,119
M 88
SH 118
SH 119
M 89
AN 118,118
AN 119,119
M 90
TY 122,3592
TY 126,17416
M 91
ad 84,960
M 92
W 0
UH 7,43
M 93
AR 8,98
AR 9,122
M 94
AD 8,6
AD 9,7
M 95
R 34
M 96
W 1
AW 98,8
M 97
AW 122,9
UL 6,44
M 98
UH 7,44
AR 8,51
M 99
WL 3
AN 120,122
AN 121,123
M 100
SH 122
SH 123
M 101
AN 122,122
AN 123,123
M 102
WL 2
AN 124,126
AN 125,127
M 103
SH 126
SH 127
M 104
AN 126,126
AN 127,127
M 105
W 2
AR 9,75
M 106
)"
R"(AD 8,6
AD 9,7
M 107
R 48
M 108
mw 3
AW 51,8
AW 75,9
M 109
ad 84,966
W 0
M 110
UL 6,45
UH 7,45
M 111
AR 8,99
AR 9,123
M 112
W 1
AD 8,6
M 113
AD 9,7
AW 99,8
M 114
AW 123,9
M 115
W 2
M 116
M 117
M 118
bc 49,11,66,60
M 119
WL 2
R 50
M 120
mh 18
s_bitcmp1_b32 s87, 6
s_cselect_b32 exec_lo, 0xffffff, 0
s_mov_b32 exec_hi, 0
global_atomic_add_f32 v54, v52, s[90:91]
s_mov_b64 exec, -1
M 121
ad 66,2112
H 0
M 132
M 133
TX 58,0
TX 62,13824
M 122
DG 1024
M 123
M 134
M 135
TY 66,512
TY 70,14336
M 124
bc 48,11,64,56
M 125
mh 21
M 136
ad 64,2112
H 0
M 137
TX 74,1536
TX 78,15360
M 126
WL 4
Q 0
M 127
Q 2
M 138
Q 4
M 139
TY 82,2048
TY 86,15872
M 128
Q 6
M 129
WL 4
Q 8
M 140
Q 10
M 141
TX 90,3072
TX 94,16896
M 130
Q 12
M 131
R 52
M 142
Q 14
M 143
TY 98,3584
TY 102,17408
M 0
R 54
M 1
WL 10
Q 16
M 12
Q 18
M 13
Q 20
M 2
Q 22
M 3
WL 8
Q 24
M 14
Q 26
M 15
Q 28
M 4
Q 30
M 5
WL 6
Q 32
M 16
Q 34
M 17
Q 36
M 6
Q 38
M 7
WL 2
Q 40
M 18
Q 42
M 19
Q 44
M 8
Q 46
M 9
H 1
M 20
ae 44,68,96
M 21
s_bfe_u32 s51, s50, 0x10005
v_mad_i32_i24 v47, v49, s51, v48
XL 22,0
M 10
H 2
XL 24,768
M 11
R 56
M 22
XL 26,1536
M 23
R 58
M 24
XL 28,2304
M 25
M 26
M 27
M 28
M 29
M 30
M 31
M 32
M 33
WV 10
M 34
WL 0
FP
M 35
M 36
M 37
s_bitcmp1_b32 s87, 7
s_cselect_b32 exec_lo, 0xffffff, 0
s_mov_b32 exec_hi, 0
global_atomic_add_f32 v54, v52, s[90:91]
s_mov_b64 exec, -1
M 38
M 39
M 40
M 41
M 42
M 43
M 44
M 45
FR
M 46
M 47
M 48
UL 6,14
UH 7,14
M 49
AR 8,52
AR 9,76
M 50
AD 8,6
AD 9,7
M 51
AW 52,8
AW 76,9
M 52
UL 6,15
UH 7,15
M 53
WL 0
FQ 20
AR 8,100
M 54
AR 9,124
AD 8,6
M 55
AD 9,7
AW 100,8
M 56
FZ 20
R 72
M 57
mw 24
R 74
M 58
TY 106,5128
TY 110,18952
M 59
R 76
M 60
ad 84,1008
W 0
M 61
AW 124,9
UL 6,16
M 62
UH 7,16
AR 8,53
M 63
W 1
AR 9,77
M 64
AD 8,6
AD 9,7
M 65
AW 53,8
AW 77,9
M 66
W 2
UL 6,17
M 67
WL 3
AN 104,106
AN 105,107
M 68
SH 106
SH 107
M 69
mw 27
AN 106,106
AN 107,107
M 70
WL 2
AN 108,110
AN 109,111
M 71
R 78
M 72
SH 110
SH 111
M 73
AN 110,110
AN 111,111
M 74
TY 114,6664
TY 118,20488
M 75
ad 84,1014
W 0
M 76
UH 7,17
AR 8,101
M 77
AR 9,125
AD 8,6
M 78
W 1
AD 9,7
M 79
AW 101,8
AW 125,9
M 80
UL 6,18
UH 7,18
M 81
W 2
AR 8,54
M 82
AR 9,78
AD 8,6
M 83
WL 1
R 80
M 84
mw 48
AN 112,114
AN 113,115
M 85
SH 114
SH 115
M 86
AN 114,114
AN 115,115
M 87
WL 2
AN 116,118
AN 117,119
M 88
SH 118
SH 119
M 89
AN 118,118
AN 119,119
M 90
TY 122,8200
TY 126,22024
M 91
ad 84,1056
M 92
W 0
AD 9,7
M 93
AW 54,8
AW 78,9
M 94
UL 6,19
UH 7,19
M 95
R 82
M 96
W 1
AR 8,102
M 97
AR 9,126
AD 8,6
M 98
AD 9,7
AW 102,8
M 99
WL 3
AN 120,122
AN 121,123
M 100
SH 122
SH 123
M 101
AN 122,122
AN 123,123
M 102
WL 2
AN 124,126
AN 125,127
M 103
SH 126
SH 127
M 104
AN 126,126
AN 127,127
M 105
W 2
AW 126,9
M 106
UL 6,20
UH 7,20
M 107
R 0
M 108
mw 51
AR 8,55
AR 9,79
M 109
ad 84,1062
W 0
M 110
AD 8,6
AD 9,7
M 111
AW 55,8
AW 79,9
M 112
W 1
UL 6,21
M 113
UH 7,21
AR 8,103
M 114
AR 9,127
AD 8,6
M 115
W 2
AD 9,7
M 116
AW 103,8
AW 127,9
M 117
M 118
bc 49,12,66,60
M 119
WL 2
R 2
)"
R"(M 120
mh 0
M 121
ad 66,2304
H 0
M 132
M 133
TX 58,4608
TX 62,18432
M 122
DG 1024
M 123
M 134
M 135
TY 66,5120
TY 70,18944
M 124
bc 48,12,64,56
M 125
mh 3
M 136
ad 64,2304
H 0
M 137
TX 74,6144
TX 78,19968
M 126
WL 4
Q 0
M 127
Q 2
M 138
Q 4
M 139
TY 82,6656
TY 86,20480
M 128
Q 6
M 129
WL 4
Q 8
M 140
Q 10
M 141
TX 90,7680
TX 94,21504
M 130
Q 12
M 131
R 4
M 142
Q 14
M 143
TY 98,8192
TY 102,22016
M 0
R 6
M 1
WL 10
Q 16
M 12
Q 18
M 13
Q 20
M 2
Q 22
M 3
WL 8
Q 24
M 14
Q 26
M 15
Q 28
M 4
Q 30
M 5
WL 6
Q 32
M 16
Q 34
M 17
Q 36
M 6
Q 38
M 7
WL 2
Q 40
M 18
Q 42
M 19
Q 44
M 8
Q 46
M 9
H 1
M 20
M 21
M 10
H 2
M 11
R 8
M 22
M 23
R 10
PA 12288
DF
M 24
M 25
M 26
M 27
M 28
M 29
M 30
M 31
M 32
ae 44,70,0
M 33
WV 6
M 34
WL 0
FP
M 35
M 36
s_bitcmp1_b32 s50, 0
s_cselect_b32 s51, 24576, 0
v_add_u32 v47, s51, v3
M 37
XL 30,128
M 38
XL 32,896
M 39
M 40
M 41
M 42
M 43
M 44
XL 34,1664
M 45
FR
M 46
M 47
M 48
M 49
M 50
XL 36,2432
UL 6,22
M 51
UH 7,22
AR 8,56
M 52
AR 9,80
AD 8,6
M 53
WL 0
FQ 24
AD 9,7
M 54
AW 56,8
AW 80,9
M 55
UL 6,23
UH 7,23
M 56
FZ 24
R 24
M 57
mw 72
R 26
M 58
TY 106,9736
TY 110,23560
M 59
R 28
M 60
ad 84,1104
W 0
M 61
AR 8,104
AR 9,128
M 62
AD 8,6
AD 9,7
M 63
W 1
AW 104,8
M 64
AW 128,9
UL 6,24
M 65
UH 7,24
AR 8,57
M 66
W 2
AR 9,81
M 67
WL 3
AN 104,106
AN 105,107
M 68
SH 106
SH 107
M 69
mw 75
AN 106,106
AN 107,107
M 70
WL 2
AN 108,110
AN 109,111
M 71
R 30
M 72
SH 110
SH 111
M 73
AN 110,110
AN 111,111
M 74
TY 114,11272
TY 118,25096
M 75
ad 84,1110
W 0
M 76
AD 8,6
AD 9,7
M 77
AW 57,8
AW 81,9
M 78
W 1
UL 6,25
M 79
UH 7,25
AR 8,105
M 80
AR 9,129
AD 8,6
M 81
W 2
AD 9,7
M 82
AW 105,8
AW 129,9
M 83
WL 1
R 32
M 84
mw 0
AN 112,114
AN 113,115
M 85
SH 114
SH 115
M 86
AN 114,114
AN 115,115
M 87
WL 2
AN 116,118
AN 117,119
M 88
SH 118
SH 119
M 89
AN 118,118
AN 119,119
M 90
TY 122,12808
TY 126,26632
M 91
ad 84,1152
M 92
W 0
UL 6,26
M 93
UH 7,26
AR 8,58
M 94
AR 9,82
AD 8,6
M 95
R 34
M 96
W 1
AD 9,7
M 97
AW 58,8
AW 82,9
M 98
UL 6,27
UH 7,27
M 99
WL 3
AN 120,122
AN 121,123
M 100
SH 122
SH 123
M 101
AN 122,122
AN 123,123
M 102
WL 2
AN 124,126
AN 125,127
M 103
SH 126
SH 127
M 104
AN 126,126
AN 127,127
M 105
W 2
AR 8,106
M 106
AR 9,130
AD 8,6
M 107
R 48
M 108
mw 3
AD 9,7
AW 106,8
M 109
ad 84,1158
W 0
M 110
AW 130,9
UL 6,28
M 111
UH 7,28
AR 8,59
M 112
W 1
AR 9,83
M 113
AD 8,6
AD 9,7
M 114
AW 59,8
AW 83,9
M 115
W 2
UL 6,29
M 116
UH 7,29
AR 8,107
M 117
AR 9,131
AD 8,6
M 118
bc 49,13,66,60
AD 9,7
M 119
WL 0
R 50
M 120
mh 9
AW 107,8
AW 131,9
M 121
ad 66,2496
H 0
M 132
M 133
TX 58,9216
TX 62,23040
M 122
DG 1024
M 123
M 134
M 135
TY 66,9728
TY 70,23552
M 124
bc 48,13,64,56
M 125
mh 12
M 136
ad 64,2496
H 0
M 137
TX 74,10752
TX 78,24576
M 126
WL 4
Q 0
M 127
Q 2
M 138
Q 4
M 139
TY 82,11264
TY 86,25088
M 128
Q 6
M 129
WL 4
Q 8
)"
R"(M 140
Q 10
M 141
TX 90,12288
TX 94,26112
M 130
Q 12
M 131
R 52
M 142
Q 14
M 143
TY 98,12800
TY 102,26624
M 0
R 54
M 1
WL 10
Q 16
M 12
Q 18
M 13
Q 20
M 2
Q 22
M 3
WL 8
Q 24
M 14
Q 26
M 15
Q 28
M 4
Q 30
M 5
WL 6
Q 32
M 16
Q 34
M 17
Q 36
M 6
Q 38
M 7
WL 2
Q 40
M 18
Q 42
M 19
Q 44
M 8
Q 46
M 9
H 1
M 20
M 21
M 10
H 2
M 11
R 56
M 22
M 23
R 58
M 24
M 25
M 26
M 27
M 28
M 29
M 30
M 31
M 32
M 33
WV 5
M 34
WL 0
FP
M 35
M 36
ae 44,70,0
M 37
s_bfe_u32 s51, s50, 0x10001
v_mad_i32_i24 v47, v49, s51, v48
XL 38,128
M 38
M 39
M 40
XL 40,896
M 41
M 42
M 43
M 44
M 45
FR
M 46
XL 42,1664
M 47
M 48
M 49
M 50
M 51
M 52
XL 44,2432
M 53
WL 0
FQ 28
M 54
M 55
M 56
FZ 28
R 72
M 57
R 74
M 58
TY 106,520
TY 110,14344
M 59
R 76
M 60
mw 24
v_mov_b32 v54, 2
s_mov_b64 exec, 1
FS
s_mov_b64 exec, -1
M 61
ad 84,1200
W 0
M 62
M 63
M 64
W 1
M 65
M 66
M 67
WL 3
AN 104,106
AN 105,107
M 68
SH 106
SH 107
M 69
AN 106,106
AN 107,107
M 70
WL 2
AN 108,110
AN 109,111
M 71
R 78
M 72
SH 110
SH 111
M 73
AN 110,110
AN 111,111
M 74
TY 114,2056
TY 118,15880
M 75
W 2
M 76
M 77
M 78
mw 27
M 79
ad 84,1206
W 0
M 80
M 81
M 82
W 1
M 83
R 80
M 84
WL 3
AN 112,114
AN 113,115
M 85
SH 114
SH 115
M 86
AN 114,114
AN 115,115
M 87
WL 2
AN 116,118
AN 117,119
M 88
SH 118
SH 119
M 89
AN 118,118
AN 119,119
M 90
TY 122,3592
TY 126,17416
M 91
W 2
UL 6,30
M 92
UH 7,30
AR 8,132
M 93
AR 9,156
AD 8,6
M 94
mw 48
AD 9,7
AW 132,8
M 95
R 82
M 96
ad 84,1248
W 0
M 97
AW 156,9
UL 6,31
M 98
UH 7,31
AR 8,180
M 99
WL 3
AN 120,122
AN 121,123
M 100
SH 122
SH 123
M 101
AN 122,122
AN 123,123
M 102
WL 2
AN 124,126
AN 125,127
M 103
SH 126
SH 127
M 104
AN 126,126
AN 127,127
M 105
W 1
AD 8,6
M 106
AD 136,7
AW 180,8
M 107
R 0
M 108
W 2
UL 6,32
M 109
UH 7,32
AR 8,133
M 110
AR 9,157
AD 8,6
M 111
mw 51
AD 9,7
AW 133,8
M 112
ad 84,1254
W 0
M 113
AW 157,9
UL 6,33
M 114
UH 7,33
AR 8,181
M 115
W 1
AD 8,6
M 116
AD 137,7
AW 181,8
M 117
UL 6,34
UH 7,34
M 118
W 2
AR 8,134
M 119
WL 0
R 2
M 120
AR 9,158
AD 8,6
M 121
bc 49,14,66,60
AD 9,7
M 132
mh 18
AW 134,8
AW 158,9
M 133
TX 58,0
TX 62,13824
M 122
ad 66,2688
H 0
M 123
UL 6,35
UH 7,35
M 134
AR 8,182
AD 8,6
M 135
TY 66,512
TY 70,14336
M 124
DG 1024
M 125
AD 138,7
AW 182,8
M 136
UL 6,36
UH 7,36
M 137
TX 74,1536
TX 78,15360
M 126
WL 4
Q 0
M 127
Q 2
M 138
Q 4
M 139
TY 82,2048
TY 86,15872
M 128
Q 6
M 129
WL 4
Q 8
M 140
Q 10
M 141
TX 90,3072
TX 94,16896
M 130
Q 12
M 131
R 4
M 142
Q 14
M 143
TY 98,3584
TY 102,17408
M 0
R 6
M 1
WL 10
Q 16
M 12
Q 18
M 13
Q 20
M 2
Q 22
M 3
WL 8
Q 24
M 14
Q 26
M 15
Q 28
M 4
Q 30
M 5
WL 6
Q 32
M 16
Q 34
M 17
Q 36
M 6
Q 38
M 7
WL 2
Q 40
M 18
Q 42
M 19
Q 44
M 8
Q 46
M 9
bc 48,14,64,56
M 20
mh 21
AR 8,135
AR 9,159
M 21
ad 64,2688
H 0
M 10
AD 8,6
AD 9,7
)"
R"(M 11
R 8
M 22
H 1
AW 135,8
M 23
R 10
M 24
AW 159,9
UL 6,37
M 25
H 2
UH 7,37
M 26
AR 8,183
AD 8,6
M 27
AD 139,7
AW 183,8
M 28
PA 12288
DF
M 29
M 30
M 31
M 32
M 33
WV 6
M 34
WL 0
FP
M 35
M 36
ae 44,68,0
M 37
s_bitcmp1_b32 s50, 2
s_cselect_b32 s51, 24576, 0
v_add_u32 v47, s51, v3
M 38
XL 14,128
M 39
M 40
M 41
M 42
XL 16,896
M 43
M 44
M 45
FR
M 46
M 47
M 48
XL 18,1664
M 49
M 50
M 51
M 52
M 53
WL 0
FQ 32
M 54
XL 20,2432
M 55
M 56
FZ 32
R 24
M 57
mw 72
R 26
M 58
TY 106,5128
TY 110,18952
M 59
R 28
M 60
ad 84,1296
W 0
M 61
M 62
M 63
W 1
M 64
M 65
M 66
W 2
M 67
WL 3
AN 104,106
AN 105,107
M 68
SH 106
SH 107
M 69
mw 75
AN 106,106
AN 107,107
M 70
WL 2
AN 108,110
AN 109,111
M 71
R 30
M 72
SH 110
SH 111
M 73
AN 110,110
AN 111,111
M 74
TY 114,6664
TY 118,20488
M 75
ad 84,1302
W 0
M 76
M 77
M 78
W 1
M 79
M 80
M 81
W 2
M 82
M 83
R 32
M 84
mw 0
WL 3
AN 112,114
AN 113,115
M 85
SH 114
SH 115
M 86
AN 114,114
AN 115,115
M 87
WL 2
AN 116,118
AN 117,119
M 88
SH 118
SH 119
M 89
AN 118,118
AN 119,119
M 90
TY 122,8200
TY 126,22024
M 91
ad 84,1344
M 92
W 0
UL 6,38
M 93
UH 7,38
AR 8,136
M 94
AR 9,160
AD 8,6
M 95
R 34
M 96
W 1
AD 9,7
M 97
AW 136,8
AW 160,9
M 98
UL 6,39
UH 7,39
M 99
WL 3
AN 120,122
AN 121,123
M 100
SH 122
SH 123
M 101
AN 122,122
AN 123,123
M 102
WL 2
AN 124,126
AN 125,127
M 103
SH 126
SH 127
M 104
AN 126,126
AN 127,127
M 105
W 2
AR 8,184
M 106
AD 8,6
AD 140,7
M 107
R 48
M 108
mw 3
AW 184,8
UL 6,40
M 109
ad 84,1350
W 0
M 110
UH 7,40
AR 8,137
M 111
AR 9,161
AD 8,6
M 112
W 1
AD 9,7
M 113
AW 137,8
AW 161,9
M 114
UL 6,41
UH 7,41
M 115
W 2
AR 8,185
M 116
AD 8,6
AD 141,7
M 117
AW 185,8
UL 6,42
M 118
bc 49,15,66,60
UH 7,42
M 119
WL 0
R 50
M 120
mh 0
AR 8,138
AR 9,162
M 121
ad 66,2880
H 0
M 132
AD 8,6
AD 9,7
M 133
TX 58,4608
TX 62,18432
M 122
DG 1024
M 123
AW 138,8
AW 162,9
M 134
UL 6,43
UH 7,43
M 135
TY 66,5120
TY 70,18944
M 124
bc 48,15,64,56
AR 8,186
M 125
mh 3
AD 8,6
AD 142,7
M 136
ad 64,2880
H 0
M 137
TX 74,6144
TX 78,19968
M 126
WL 4
Q 0
M 127
Q 2
M 138
Q 4
M 139
TY 82,6656
TY 86,20480
M 128
Q 6
M 129
WL 4
Q 8
M 140
Q 10
M 141
TX 90,7680
TX 94,21504
M 130
Q 12
M 131
R 52
M 142
Q 14
M 143
TY 98,8192
TY 102,22016
M 0
R 54
M 1
WL 10
Q 16
M 12
Q 18
M 13
Q 20
M 2
Q 22
M 3
WL 8
Q 24
M 14
Q 26
M 15
Q 28
M 4
Q 30
M 5
WL 6
Q 32
M 16
Q 34
M 17
Q 36
M 6
Q 38
M 7
WL 2
Q 40
M 18
Q 42
M 19
Q 44
M 8
Q 46
M 9
H 1
AW 186,8
M 20
UL 6,44
UH 7,44
M 21
AR 8,139
AR 9,163
M 10
H 2
AD 8,6
M 11
R 56
M 22
AD 9,7
AW 139,8
M 23
R 58
M 24
AW 163,9
UL 6,45
M 25
UH 7,45
AR 8,187
M 26
AD 8,6
AD 143,7
M 27
AW 187,8
M 28
M 29
M 30
M 31
M 32
M 33
WV 5
M 34
WL 0
FP
M 35
M 36
M 37
M 38
ae 44,68,0
M 39
s_bfe_u32 s51, s50, 0x10003
v_mad_i32_i24 v47, v49, s51, v48
XL 22,128
M 40
M 41
M 42
M 43
M 44
XL 24,896
M 45
FR
M 46
M 47
M 48
)"
R"(M 49
M 50
XL 26,1664
M 51
M 52
M 53
WL 0
FQ 36
M 54
M 55
M 56
FZ 36
R 72
M 57
mw 24
R 74
M 58
TY 106,9736
TY 110,23560
M 59
R 76
M 60
ad 84,1392
W 0
M 61
XL 28,2432
M 62
M 63
W 1
M 64
M 65
M 66
W 2
M 67
WL 3
AN 104,106
AN 105,107
M 68
SH 106
SH 107
M 69
mw 27
AN 106,106
AN 107,107
M 70
WL 2
AN 108,110
AN 109,111
M 71
R 78
M 72
SH 110
SH 111
M 73
AN 110,110
AN 111,111
M 74
TY 114,11272
TY 118,25096
M 75
ad 84,1398
W 0
M 76
M 77
M 78
W 1
M 79
M 80
M 81
W 2
M 82
M 83
R 80
M 84
mw 48
WL 3
AN 112,114
AN 113,115
M 85
SH 114
SH 115
M 86
AN 114,114
AN 115,115
M 87
WL 2
AN 116,118
AN 117,119
M 88
SH 118
SH 119
M 89
AN 118,118
AN 119,119
M 90
TY 122,12808
TY 126,26632
M 91
ad 84,1440
M 92
W 0
UL 6,14
M 93
UH 7,14
AR 8,140
M 94
AR 9,164
AD 8,6
M 95
R 82
M 96
W 1
AD 9,7
M 97
AW 140,8
AW 164,9
M 98
UL 6,15
UH 7,15
M 99
WL 3
AN 120,122
AN 121,123
M 100
SH 122
SH 123
M 101
AN 122,122
AN 123,123
M 102
WL 2
AN 124,126
AN 125,127
M 103
SH 126
SH 127
M 104
AN 126,126
AN 127,127
M 105
W 2
AR 8,188
M 106
AD 8,6
AD 144,7
M 107
R 0
M 108
mw 51
AW 188,8
UL 6,16
M 109
ad 84,1446
W 0
M 110
UH 7,16
AR 8,141
M 111
AR 9,165
AD 8,6
M 112
W 1
AD 9,7
M 113
AW 141,8
AW 165,9
M 114
UL 6,17
UH 7,17
M 115
W 2
AR 8,189
M 116
AD 8,6
AD 145,7
M 117
AW 189,8
UL 6,18
M 118
bc 49,16,66,60
UH 7,18
M 119
WL 0
R 2
M 120
mh 9
AR 8,142
AR 9,166
M 121
ad 66,3072
H 0
M 132
AD 8,6
AD 9,7
M 133
TX 58,9216
TX 62,23040
M 122
DG 1024
M 123
AW 142,8
AW 166,9
M 134
UL 6,19
UH 7,19
M 135
TY 66,9728
TY 70,23552
M 124
bc 48,16,64,56
AR 8,190
M 125
mh 12
AD 8,6
AD 146,7
M 136
ad 64,3072
H 0
M 137
TX 74,10752
TX 78,24576
M 126
WL 4
Q 0
M 127
Q 2
M 138
Q 4
M 139
TY 82,11264
TY 86,25088
M 128
Q 6
M 129
WL 4
Q 8
M 140
Q 10
M 141
TX 90,12288
TX 94,26112
M 130
Q 12
M 131
R 4
M 142
Q 14
M 143
TY 98,12800
TY 102,26624
M 0
R 6
M 1
WL 10
Q 16
M 12
Q 18
M 13
Q 20
M 2
Q 22
M 3
WL 8
Q 24
M 14
Q 26
M 15
Q 28
M 4
Q 30
M 5
WL 6
Q 32
M 16
Q 34
M 17
Q 36
M 6
Q 38
M 7
WL 2
Q 40
M 18
Q 42
M 19
Q 44
M 8
Q 46
M 9
H 1
AW 190,8
M 20
UL 6,20
UH 7,20
M 21
AR 8,143
AR 9,167
M 10
H 2
AD 8,6
M 11
R 8
M 22
AD 9,7
AW 143,8
M 23
R 10
PA 12288
DF
M 24
AW 167,9
UL 6,21
M 25
UH 7,21
AR 8,191
M 26
AD 8,6
AD 147,7
M 27
AW 191,8
M 28
M 29
M 30
M 31
M 32
M 33
WV 6
M 34
WL 0
FP
M 35
M 36
M 37
M 38
M 39
M 40
ae 44,68,96
M 41
s_bitcmp1_b32 s50, 4
s_cselect_b32 s51, 24576, 0
v_add_u32 v47, s51, v3
M 42
XL 30,128
M 43
M 44
M 45
FR
M 46
XL 32,896
M 47
M 48
M 49
M 50
M 51
M 52
XL 34,1664
M 53
WL 0
FQ 40
M 54
M 55
M 56
FZ 40
R 24
M 57
mw 72
R 26
M 58
TY 106,520
TY 110,14344
M 59
R 28
M 60
ad 84,1488
W 0
M 61
XL 36,2432
v_lshlrev_b32 v55, 4, v0
M 62
FL
M 63
W 1
M 64
M 65
M 66
W 2
M 67
WL 3
AN 104,106
AN 105,107
M 68
SH 106
SH 107
M 69
mw 75
AN 106,106
AN 107,107
M 70
WL 2
AN 108,110
)"
R"(AN 109,111
M 71
R 30
M 72
SH 110
SH 111
M 73
AN 110,110
AN 111,111
M 74
TY 114,2056
TY 118,15880
M 75
ad 84,1494
W 0
M 76
M 77
M 78
W 1
M 79
M 80
M 81
W 2
M 82
M 83
R 32
M 84
mw 0
WL 3
AN 112,114
AN 113,115
M 85
SH 114
SH 115
M 86
AN 114,114
AN 115,115
M 87
WL 2
AN 116,118
AN 117,119
M 88
SH 118
SH 119
M 89
AN 118,118
AN 119,119
M 90
TY 122,3592
TY 126,17416
M 91
ad 84,1536
M 92
W 0
M 93
M 94
UL 6,22
UH 7,22
M 95
R 34
M 96
W 1
AR 8,144
M 97
AR 9,168
AD 8,6
M 98
AD 9,7
AW 144,8
M 99
WL 3
AN 120,122
AN 121,123
M 100
SH 122
SH 123
M 101
AN 122,122
AN 123,123
M 102
WL 2
AN 124,126
AN 125,127
M 103
SH 126
SH 127
M 104
AN 126,126
AN 127,127
M 105
W 2
AW 168,9
M 106
UL 6,23
UH 7,23
M 107
R 48
M 108
mw 3
AR 8,192
AD 8,6
M 109
ad 84,1542
W 0
M 110
AD 148,7
AW 192,8
M 111
UL 6,24
UH 7,24
M 112
W 1
AR 8,145
M 113
AR 9,169
AD 8,6
M 114
AD 9,7
AW 145,8
M 115
W 2
AW 169,9
M 116
UL 6,25
UH 7,25
M 117
AR 8,193
AD 8,6
M 118
bc 49,17,66,60
AD 149,7
M 119
WL 0
R 50
M 120
mh 18
AW 193,8
UL 6,26
M 121
ad 66,3264
H 0
M 132
UH 7,26
AR 8,146
M 133
TX 58,0
TX 62,13824
M 122
DG 1024
M 123
AR 9,170
AD 8,6
M 134
AD 9,7
AW 146,8
M 135
TY 66,512
TY 70,14336
M 124
bc 48,17,64,56
AW 170,9
M 125
mh 21
UL 6,27
UH 7,27
M 136
ad 64,3264
H 0
M 137
TX 74,1536
TX 78,15360
M 126
WL 4
Q 0
M 127
Q 2
M 138
Q 4
M 139
TY 82,2048
TY 86,15872
M 128
Q 6
M 129
WL 4
Q 8
M 140
Q 10
M 141
TX 90,3072
TX 94,16896
M 130
Q 12
M 131
R 52
M 142
Q 14
M 143
TY 98,3584
TY 102,17408
M 0
R 54
M 1
WL 10
Q 16
M 12
Q 18
M 13
Q 20
M 2
Q 22
M 3
WL 8
Q 24
M 14
Q 26
M 15
Q 28
M 4
Q 30
M 5
WL 6
Q 32
M 16
Q 34
M 17
Q 36
M 6
Q 38
M 7
WL 2
Q 40
M 18
Q 42
M 19
Q 44
M 8
Q 46
M 9
H 1
AR 8,194
M 20
AD 8,6
AD 150,7
M 21
AW 194,8
UL 6,28
M 10
H 2
UH 7,28
M 11
R 56
M 22
AR 8,147
AR 9,171
M 23
R 58
M 24
AD 8,6
AD 9,7
M 25
AW 147,8
AW 171,9
M 26
UL 6,29
UH 7,29
M 27
AR 8,195
AD 8,6
M 28
AD 151,7
AW 195,8
M 29
M 30
M 31
M 32
M 33
WV 5
M 34
WL 0
FP
M 35
M 36
M 37
M 38
M 39
M 40
M 41
M 42
ae 44,68,96
M 43
s_bfe_u32 s51, s50, 0x10005
v_mad_i32_i24 v47, v49, s51, v48
XL 38,128
M 44
M 45
FR
M 46
M 47
M 48
XL 40,896
M 49
M 50
M 51
M 52
M 53
WL 0
FQ 44
M 54
XL 42,1664
M 55
M 56
FZ 44
R 72
M 57
mw 24
R 74
M 58
TY 106,5128
TY 110,18952
M 59
R 76
M 60
ad 84,1584
W 0
M 61
XL 44,2432
M 62
M 63
W 1
M 64
M 65
M 66
W 2
M 67
WL 3
AN 104,106
AN 105,107
M 68
SH 106
SH 107
M 69
mw 27
AN 106,106
AN 107,107
M 70
WL 2
AN 108,110
AN 109,111
M 71
R 78
M 72
SH 110
SH 111
M 73
AN 110,110
AN 111,111
M 74
TY 114,6664
TY 118,20488
M 75
ad 84,1590
W 0
M 76
M 77
M 78
W 1
M 79
M 80
M 81
W 2
M 82
M 83
R 80
M 84
mw 48
WL 3
AN 112,114
AN 113,115
M 85
SH 114
SH 115
M 86
AN 114,114
AN 115,115
M 87
WL 2
AN 116,118
AN 117,119
M 88
SH 118
SH 119
M 89
AN 118,118
AN 119,119
M 90
TY 122,8200
TY 126,22024
M 91
ad 84,1632
)"
R"(M 92
W 0
M 93
M 94
M 95
R 82
M 96
W 1
UL 6,30
M 97
UH 7,30
AR 8,148
M 98
AR 9,172
AD 8,6
M 99
WL 3
AN 120,122
AN 121,123
M 100
SH 122
SH 123
M 101
AN 122,122
AN 123,123
M 102
WL 2
AN 124,126
AN 125,127
M 103
SH 126
SH 127
M 104
AN 126,126
AN 127,127
M 105
W 2
AD 9,7
M 106
AW 148,8
AW 172,9
M 107
R 0
M 108
mw 51
UL 6,31
UH 7,31
M 109
ad 84,1638
W 0
M 110
AR 8,196
AD 8,6
M 111
AD 152,7
AW 196,8
M 112
W 1
UL 6,32
M 113
UH 7,32
AR 8,149
M 114
AR 9,173
AD 8,6
M 115
W 2
AD 9,7
M 116
AW 149,8
AW 173,9
M 117
UL 6,33
UH 7,33
M 118
bc 49,18,66,60
AR 8,197
M 119
WL 0
R 2
M 120
mh 0
AD 8,6
AD 153,7
M 121
ad 66,3456
H 0
M 132
AW 197,8
UL 6,34
M 133
TX 58,4608
TX 62,18432
M 122
DG 1024
M 123
UH 7,34
AR 8,150
M 134
AR 9,174
AD 8,6
M 135
TY 66,5120
TY 70,18944
M 124
bc 48,18,64,56
AD 9,7
M 125
mh 3
AW 150,8
AW 174,9
M 136
ad 64,3456
H 0
M 137
TX 74,6144
TX 78,19968
M 126
WL 4
Q 0
M 127
Q 2
M 138
Q 4
M 139
TY 82,6656
TY 86,20480
M 128
Q 6
M 129
WL 4
Q 8
M 140
Q 10
M 141
TX 90,7680
TX 94,21504
M 130
Q 12
M 131
R 4
M 142
Q 14
M 143
TY 98,8192
TY 102,22016
M 0
R 6
M 1
WL 10
Q 16
M 12
Q 18
M 13
Q 20
M 2
Q 22
M 3
WL 8
Q 24
M 14
Q 26
M 15
Q 28
M 4
Q 30
M 5
WL 6
Q 32
M 16
Q 34
M 17
Q 36
M 6
Q 38
M 7
WL 2
Q 40
M 18
Q 42
M 19
Q 44
M 8
Q 46
M 9
H 1
UL 6,35
M 20
UH 7,35
AR 8,198
M 21
AD 8,6
AD 154,7
M 10
H 2
AW 198,8
M 11
R 8
M 22
UL 6,36
UH 7,36
M 23
R 10
PA 12288
DF
M 24
AR 8,151
AR 9,175
M 25
AD 8,6
AD 9,7
M 26
AW 151,8
AW 175,9
M 27
v_cmp_ne_u32 vcc, 2, v54
UL 6,37
M 28
UH 7,37
AR 8,199
M 29
AD 8,6
AD 155,7
M 30
FC
AW 199,8
M 31
M 32
M 33
WV 6
M 34
WL 0
FP
M 35
M 36
M 37
M 38
M 39
M 40
M 41
M 42
M 43
M 44
M 45
FR
M 46
M 47
M 48
M 49
M 50
M 51
M 52
M 53
WL 0
FQ 48
M 54
M 55
M 56
FZ 48
R 24
M 57
mw 72
R 26
M 58
TY 106,9736
TY 110,23560
M 59
R 28
M 60
ad 84,1680
W 0
M 61
M 62
M 63
W 1
M 64
M 65
M 66
W 2
M 67
WL 3
AN 104,106
AN 105,107
M 68
SH 106
SH 107
M 69
mw 75
AN 106,106
AN 107,107
M 70
WL 2
AN 108,110
AN 109,111
M 71
R 30
M 72
SH 110
SH 111
M 73
AN 110,110
AN 111,111
M 74
TY 114,11272
TY 118,25096
M 75
ad 84,1686
W 0
M 76
ae 44,70,0
M 77
s_bitcmp1_b32 s50, 0
s_cselect_b32 s51, 24576, 0
v_add_u32 v47, s51, v3
M 78
W 1
XL 14,256
M 79
XL 16,1024
M 80
M 81
W 2
M 82
XL 18,1792
M 83
WL 1
R 32
M 84
mw 0
AN 112,114
AN 113,115
M 85
SH 114
SH 115
M 86
AN 114,114
AN 115,115
M 87
WL 2
AN 116,118
AN 117,119
M 88
SH 118
SH 119
M 89
AN 118,118
AN 119,119
M 90
TY 122,12808
TY 126,26632
M 91
ad 84,1728
M 92
W 0
XL 20,2560
M 93
M 94
M 95
R 34
M 96
W 1
M 97
M 98
UL 6,38
UH 7,38
M 99
WL 3
AN 120,122
AN 121,123
M 100
SH 122
SH 123
M 101
AN 122,122
AN 123,123
M 102
WL 2
AN 124,126
AN 125,127
M 103
SH 126
SH 127
M 104
AN 126,126
AN 127,127
M 105
W 2
AR 8,152
M 106
AR 9,176
AD 8,6
M 107
R 48
M 108
mw 3
AD 9,7
AW 152,8
M 109
ad 84,1734
W 0
)"
R"(M 110
AW 176,9
UL 6,39
M 111
UH 7,39
AD 132,6
M 112
W 1
AD 156,7
M 113
UL 6,40
UH 7,40
M 114
AR 8,153
AR 9,177
M 115
W 2
AD 8,6
M 116
AD 9,7
AW 153,8
M 117
AW 177,9
UL 6,41
M 118
bc 49,19,66,60
UH 7,41
M 119
WL 0
R 50
M 120
mh 9
AD 133,6
AD 157,7
M 121
ad 66,3648
H 0
M 132
UL 6,42
UH 7,42
M 133
TX 58,9216
TX 62,23040
M 122
DG 1024
M 123
AR 8,154
AR 9,178
M 134
AD 8,6
AD 9,7
M 135
TY 66,9728
TY 70,23552
M 124
bc 48,19,64,56
AW 154,8
M 125
mh 12
AW 178,9
UL 6,43
M 136
ad 64,3648
H 0
M 137
TX 74,10752
TX 78,24576
M 126
WL 4
Q 0
M 127
Q 2
M 138
Q 4
M 139
TY 82,11264
TY 86,25088
M 128
Q 6
M 129
WL 4
Q 8
M 140
Q 10
M 141
TX 90,12288
TX 94,26112
M 130
Q 12
M 131
R 52
M 142
Q 14
M 143
TY 98,12800
TY 102,26624
M 0
R 54
M 1
WL 10
Q 16
M 12
Q 18
M 13
Q 20
M 2
Q 22
M 3
WL 8
Q 24
M 14
Q 26
M 15
Q 28
M 4
Q 30
M 5
WL 6
Q 32
M 16
Q 34
M 17
Q 36
M 6
Q 38
M 7
WL 2
Q 40
M 18
Q 42
M 19
Q 44
M 8
Q 46
M 9
H 1
UH 7,43
M 20
AD 134,6
AD 158,7
M 21
UL 6,44
UH 7,44
M 10
H 2
AR 8,155
M 11
R 56
M 22
AR 9,179
AD 8,6
M 23
R 58
v_min_u32 v52, 752, v0
v_lshrrev_b32 v52, 1, v52
v_add_u32 v53, 0x1800, v52
global_load_dwordx2 a[200:201], v52, s[94:95] offset:-2048 sc1
global_load_dwordx2 a[202:203], v52, s[94:95] offset:-512 sc1
global_load_dwordx2 a[204:205], v52, s[94:95] offset:1024 sc1
global_load_dwordx2 a[206:207], v52, s[94:95] offset:2560 sc1
global_load_dwordx2 a[208:209], v53, s[94:95] offset:-2048 sc1
global_load_dwordx2 a[210:211], v53, s[94:95] offset:-512 sc1
global_load_dwordx2 a[212:213], v53, s[94:95] offset:1024 sc1
global_load_dwordx2 a[214:215], v53, s[94:95] offset:2560 sc1
M 24
AD 9,7
AW 155,8
M 25
AW 179,9
UL 6,45
M 26
UH 7,45
AD 135,6
M 27
AD 159,7
M 28
M 29
M 30
M 31
M 32
M 33
WV 13
M 34
WL 0
FP
M 35
M 36
M 37
M 38
M 39
M 40
M 41
M 42
M 43
M 44
M 45
FR
M 46
M 47
M 48
M 49
M 50
M 51
M 52
M 53
WL 0
FQ 52
M 54
M 55
M 56
FZ 52
R 72
M 57
mw 24
R 74
M 58
TY 106,520
TY 110,14344
M 59
R 76
M 60
ad 84,1776
W 0
M 61
M 62
M 63
W 1
M 64
M 65
M 66
W 2
M 67
WL 3
AN 104,106
AN 105,107
M 68
SH 106
SH 107
M 69
mw 27
AN 106,106
AN 107,107
M 70
WL 2
AN 108,110
AN 109,111
M 71
R 78
M 72
SH 110
SH 111
M 73
AN 110,110
AN 111,111
M 74
TY 114,2056
TY 118,15880
M 75
ad 84,1782
W 0
M 76
ae 44,70,0
M 77
s_bfe_u32 s51, s50, 0x10001
v_mad_i32_i24 v47, v49, s51, v48
XL 22,256
M 78
W 1
M 79
M 80
XL 24,1024
M 81
W 2
M 82
M 83
WL 1
R 80
M 84
mw 48
AN 112,114
AN 113,115
M 85
SH 114
SH 115
M 86
AN 114,114
AN 115,115
M 87
WL 2
AN 116,118
AN 117,119
M 88
SH 118
SH 119
M 89
AN 118,118
AN 119,119
M 90
TY 122,3592
TY 126,17416
M 91
ad 84,1824
M 92
W 0
XL 26,1792
M 93
XL 28,2560
M 94
M 95
R 82
M 96
W 1
M 97
M 98
M 99
WL 3
AN 120,122
AN 121,123
M 100
SH 122
SH 123
M 101
AN 122,122
AN 123,123
M 102
WL 2
AN 124,126
AN 125,127
M 103
SH 126
SH 127
M 104
AN 126,126
AN 127,127
M 105
W 2
M 106
M 107
R 0
M 108
mw 51
M 109
ad 84,1830
W 0
M 110
M 111
M 112
W 1
M 113
M 114
M 115
W 2
M 116
M 117
M 118
bc 49,20,66,60
M 119
WL 2
R 2
M 120
mh 18
M 121
ad 66,3840
H 0
M 132
M 133
TX 58,0
TX 62,13824
M 122
DG 1024
M 123
M 134
UL 6,14
UH 7,14
M 135
TY 66,512
TY 70,14336
M 124
bc 48,20,64,56
AD 160,6
M 125
mh 21
AD 184,7
UL 6,15
M 136
ad 64,3840
H 0
M 137
TX 74,1536
TX 78,15360
M 126
WL 4
Q 0
M 127
Q 2
M 138
Q 4
M 139
TY 82,2048
TY 86,15872
)"
R"(M 128
Q 6
M 129
WL 4
Q 8
M 140
Q 10
M 141
TX 90,3072
TX 94,16896
M 130
Q 12
M 131
R 4
M 142
Q 14
M 143
TY 98,3584
TY 102,17408
M 0
R 6
M 1
WL 10
Q 16
M 12
Q 18
M 13
Q 20
M 2
Q 22
M 3
WL 8
Q 24
M 14
Q 26
M 15
Q 28
M 4
Q 30
M 5
WL 6
Q 32
M 16
Q 34
M 17
Q 36
M 6
Q 38
M 7
WL 2
Q 40
M 18
Q 42
M 19
Q 44
M 8
Q 46
M 9
H 1
UH 7,15
M 20
AD 208,6
AD 232,7
M 21
UL 6,16
UH 7,16
M 10
H 2
AD 161,6
M 11
R 8
M 22
AD 185,7
UL 6,17
M 23
R 10
PA 12288
DF
M 24
UH 7,17
AD 209,6
M 25
AD 233,7
UL 6,18
M 26
UH 7,18
AD 162,6
M 27
AD 186,7
UL 6,19
M 28
UH 7,19
AD 210,6
M 29
AD 234,7
UL 6,20
M 30
UH 7,20
AD 163,6
M 31
AD 187,7
UL 6,21
M 32
UH 7,21
AD 211,6
M 33
WV 6
AD 235,7
M 34
WL 0
FP
M 35
M 36
M 37
M 38
M 39
M 40
M 41
M 42
M 43
M 44
M 45
FR
M 46
M 47
M 48
M 49
M 50
M 51
M 52
M 53
WL 0
FQ 56
M 54
M 55
M 56
FZ 56
R 24
M 57
mw 72
R 26
M 58
TY 106,5128
TY 110,18952
M 59
R 28
M 60
ad 84,1872
W 0
M 61
M 62
M 63
W 1
M 64
M 65
M 66
W 2
M 67
WL 3
AN 104,106
AN 105,107
M 68
SH 106
SH 107
M 69
mw 75
AN 106,106
AN 107,107
M 70
WL 2
AN 108,110
AN 109,111
M 71
R 30
M 72
SH 110
SH 111
M 73
AN 110,110
AN 111,111
M 74
TY 114,6664
TY 118,20488
M 75
ad 84,1878
W 0
M 76
M 77
M 78
W 1
M 79
ae 44,68,0
M 80
s_bitcmp1_b32 s50, 2
s_cselect_b32 s51, 24576, 0
v_add_u32 v47, s51, v3
M 81
W 2
XL 30,256
M 82
M 83
WL 1
R 32
M 84
mw 0
AN 112,114
AN 113,115
M 85
SH 114
SH 115
M 86
AN 114,114
AN 115,115
M 87
WL 2
AN 116,118
AN 117,119
M 88
SH 118
SH 119
M 89
AN 118,118
AN 119,119
M 90
TY 122,8200
TY 126,22024
M 91
ad 84,1920
M 92
W 0
XL 32,1024
M 93
XL 34,1792
M 94
M 95
R 34
M 96
W 1
XL 36,2560
M 97
M 98
M 99
WL 3
AN 120,122
AN 121,123
M 100
SH 122
SH 123
M 101
AN 122,122
AN 123,123
M 102
WL 2
AN 124,126
AN 125,127
M 103
SH 126
SH 127
M 104
AN 126,126
AN 127,127
M 105
W 2
M 106
M 107
R 48
M 108
mw 3
M 109
ad 84,1926
W 0
M 110
M 111
M 112
W 1
M 113
M 114
M 115
W 2
M 116
M 117
M 118
bc 49,21,66,60
M 119
WL 2
R 50
M 120
mh 0
M 121
ad 66,4032
H 0
M 132
v_mov_b32 v54, s39
v_mul_f32 v54, 0xb62aaa8f, v54
M 133
TX 58,4608
TX 62,18432
M 122
DG 1024
M 123
v_add_u32 v52, %[bm], v52
v_bfe_u32 v53, v52, 6, 2
M 134
v_and_b32 v55, 63, v52
v_add_u32 v53, v53, v55
M 135
TY 66,5120
TY 70,18944
M 124
bc 48,21,64,56
v_and_b32 v52, 0x700, v52
M 125
mh 3
v_lshl_add_u32 v52, v53, 2, v52
v_add_u32 v52, %[sb], v52
M 136
ad 64,4032
H 0
M 137
TX 74,6144
TX 78,19968
M 126
WL 4
Q 0
M 127
Q 2
M 138
Q 4
M 139
TY 82,6656
TY 86,20480
M 128
Q 6
M 129
WL 4
Q 8
M 140
Q 10
M 141
TX 90,7680
TX 94,21504
M 130
Q 12
M 131
R 52
M 142
Q 14
M 143
TY 98,8192
TY 102,22016
M 0
R 54
M 1
WL 10
Q 16
M 12
Q 18
M 13
Q 20
M 2
Q 22
M 3
WL 8
Q 24
M 14
Q 26
M 15
Q 28
M 4
Q 30
M 5
WL 6
Q 32
M 16
Q 34
M 17
Q 36
M 6
Q 38
M 7
WL 2
Q 40
M 18
Q 42
M 19
Q 44
M 8
Q 46
M 9
H 1
AR 12,214
M 20
AR 13,215
AR 8,208
M 21
AR 9,209
v_add_f32 v12, v12, v8
M 10
H 2
v_add_f32 v13, v13, v9
)"
R"(M 11
R 56
M 22
AR 8,210
AR 9,211
M 23
R 58
M 24
v_add_f32 v12, v12, v8
v_add_f32 v13, v13, v9
M 25
AR 8,212
AR 9,213
M 26
v_add_f32 v12, v12, v8
v_add_f32 v13, v13, v9
M 27
AR 6,206
AR 7,207
M 28
v_add_f32 v12, v12, v54
v_add_f32 v13, v13, v54
M 29
v_fma_f32 v8, v6, s86, v12
v_fma_f32 v9, v7, s86, v13
M 30
ds_write_b32 v52, v8 offset:1024
ds_write_b32 v52, v9 offset:1040
M 31
AR 8,200
AR 9,201
M 32
v_add_f32 v6, v6, v8
v_add_f32 v7, v7, v9
M 33
WV 5
v_fma_f32 v8, v6, s86, v12
v_fma_f32 v9, v7, s86, v13
M 34
WL 2
FP
ds_write_b32 v52, v8 offset:2560
M 35
ds_write_b32 v52, v9 offset:2576
AR 8,202
M 36
AR 9,203
v_add_f32 v6, v6, v8
M 37
v_add_f32 v7, v7, v9
v_fma_f32 v8, v6, s86, v12
M 38
ds_write_b32 v52, v8 offset:4096
v_fma_f32 v9, v7, s86, v13
M 39
ds_write_b32 v52, v9 offset:4112
AR 8,204
M 40
AR 9,205
v_add_f32 v6, v6, v8
M 41
v_add_f32 v7, v7, v9
v_fma_f32 v8, v6, s86, v12
M 42
ds_write_b32 v52, v8 offset:5632
v_fma_f32 v9, v7, s86, v13
M 43
ds_write_b32 v52, v9 offset:5648
M 44
UL 6,22
UH 7,22
M 45
FR
AD 164,6
M 46
AD 188,7
UL 6,23
M 47
UH 7,23
AD 212,6
M 48
AD 236,7
UL 6,24
M 49
UH 7,24
AD 165,6
M 50
AD 189,7
UL 6,25
M 51
UH 7,25
AD 213,6
M 52
AD 237,7
UL 6,26
M 53
WL 0
FQ 60
UH 7,26
M 54
AD 166,6
AD 190,7
M 55
UL 6,27
UH 7,27
M 56
FZ 60
R 72
M 57
mw 24
R 74
M 58
TY 106,9736
TY 110,23560
M 59
R 76
M 60
ad 84,1968
W 0
M 61
AD 214,6
AD 238,7
M 62
UL 6,28
UH 7,28
M 63
W 1
AD 167,6
M 64
AD 191,7
UL 6,29
M 65
UH 7,29
AD 215,6
M 66
W 2
AD 239,7
M 67
WL 3
AN 104,106
AN 105,107
M 68
SH 106
SH 107
M 69
mw 27
AN 106,106
AN 107,107
M 70
WL 2
AN 108,110
AN 109,111
M 71
R 78
M 72
SH 110
SH 111
M 73
AN 110,110
AN 111,111
M 74
TY 114,11272
TY 118,25096
M 75
ad 84,1974
W 0
M 76
M 77
M 78
W 1
M 79
M 80
M 81
W 2
M 82
ae 44,68,0
M 83
WL 1
R 80
M 84
mw 48
AN 112,114
AN 113,115
M 85
s_bfe_u32 s51, s50, 0x10003
SH 114
SH 115
M 86
AN 114,114
AN 115,115
M 87
WL 2
AN 116,118
AN 117,119
M 88
SH 118
SH 119
M 89
AN 118,118
AN 119,119
M 90
TY 122,12808
TY 126,26632
M 91
ad 84,2016
M 92
W 0
v_mad_i32_i24 v47, v49, s51, v48
M 93
XL 38,256
M 94
XL 40,1024
M 95
R 82
M 96
W 1
XL 42,1792
M 97
M 98
M 99
WL 3
AN 120,122
AN 121,123
M 100
SH 122
SH 123
M 101
AN 122,122
AN 123,123
M 102
WL 2
AN 124,126
AN 125,127
M 103
SH 126
SH 127
M 104
AN 126,126
AN 127,127
M 105
W 2
XL 44,2560
M 106
M 107
R 0
M 108
mw 51
M 109
ad 84,2022
W 0
M 110
M 111
M 112
W 1
M 113
M 114
M 115
W 2
M 116
M 117
M 118
bc 49,22,66,60
M 119
WL 2
R 2
M 120
mh 9
M 121
ad 66,4224
H 0
M 132
M 133
TX 58,9216
TX 62,23040
M 122
DG 1024
M 123
M 134
M 135
TY 66,9728
TY 70,23552
M 124
bc 48,22,64,56
M 125
mh 12
M 136
ad 64,4224
H 0
M 137
TX 74,10752
TX 78,24576
M 126
WL 4
Q 0
M 127
Q 2
M 138
Q 4
M 139
TY 82,11264
TY 86,25088
M 128
Q 6
M 129
WL 4
Q 8
M 140
Q 10
M 141
TX 90,12288
TX 94,26112
M 130
Q 12
M 131
R 4
M 142
Q 14
M 143
TY 98,12800
TY 102,26624
M 0
R 6
M 1
WL 10
Q 16
M 12
Q 18
M 13
Q 20
M 2
Q 22
M 3
WL 8
Q 24
M 14
Q 26
M 15
Q 28
M 4
Q 30
M 5
WL 6
Q 32
M 16
Q 34
M 17
Q 36
M 6
Q 38
M 7
WL 2
Q 40
M 18
Q 42
M 19
Q 44
M 8
Q 46
M 9
H 1
UL 6,30
M 20
UH 7,30
AD 168,6
M 21
AD 192,7
UL 6,31
M 10
H 2
UH 7,31
M 11
R 8
M 22
AD 216,6
AD 240,7
M 23
R 10
PA 12288
DF
M 24
UL 6,32
UH 7,32
M 25
AD 169,6
AD 193,7
M 26
UL 6,33
UH 7,33
M 27
AD 217,6
AD 241,7
M 28
UL 6,34
UH 7,34
M 29
AD 170,6
AD 194,7
M 30
UL 6,35
UH 7,35
)"
R"(M 31
AD 218,6
AD 242,7
M 32
UL 6,36
UH 7,36
M 33
WV 6
AD 171,6
AD 195,7
M 34
WL 0
FP
UL 6,37
M 35
UH 7,37
AD 219,6
M 36
AD 243,7
M 37
M 38
M 39
M 40
M 41
M 42
M 43
M 44
M 45
FR
M 46
M 47
M 48
M 49
M 50
M 51
M 52
M 53
WL 0
FQ 64
M 54
M 55
M 56
FZ 64
R 24
M 57
mw 72
R 26
M 58
TY 106,520
TY 110,14344
M 59
R 28
M 60
ad 84,2064
W 0
M 61
M 62
M 63
W 1
M 64
M 65
M 66
W 2
M 67
WL 3
AN 104,106
AN 105,107
M 68
SH 106
SH 107
M 69
mw 75
AN 106,106
AN 107,107
M 70
WL 2
AN 108,110
AN 109,111
M 71
R 30
M 72
SH 110
SH 111
M 73
AN 110,110
AN 111,111
M 74
TY 114,2056
TY 118,15880
M 75
ad 84,2070
W 0
M 76
M 77
M 78
W 1
M 79
M 80
M 81
W 2
M 82
M 83
R 32
M 84
mw 0
WL 3
AN 112,114
AN 113,115
M 85
SH 114
SH 115
M 86
AN 114,114
AN 115,115
M 87
WL 2
AN 116,118
AN 117,119
M 88
SH 118
SH 119
M 89
AN 118,118
AN 119,119
M 90
TY 122,3592
TY 126,17416
M 91
ad 84,2112
M 92
W 0
M 93
ae 44,68,96
M 94
s_bitcmp1_b32 s50, 4
s_cselect_b32 s51, 24576, 0
v_add_u32 v47, s51, v3
M 95
R 34
M 96
W 1
XL 14,256
M 97
XL 16,1024
M 98
XL 18,1792
M 99
WL 3
AN 120,122
AN 121,123
M 100
SH 122
SH 123
M 101
AN 122,122
AN 123,123
M 102
WL 2
AN 124,126
AN 125,127
M 103
SH 126
SH 127
M 104
AN 126,126
AN 127,127
M 105
W 2
XL 20,2560
M 106
M 107
R 48
M 108
mw 3
M 109
ad 84,2118
W 0
M 110
M 111
M 112
W 1
M 113
M 114
M 115
W 2
M 116
M 117
M 118
bc 49,23,66,60
M 119
WL 2
R 50
M 120
mh 18
M 121
ad 66,4416
H 0
M 132
M 133
TX 58,0
TX 62,13824
M 122
DG 1024
M 123
M 134
M 135
TY 66,512
TY 70,14336
M 124
bc 48,23,64,56
M 125
mh 21
M 136
ad 64,4416
H 0
M 137
TX 74,1536
TX 78,15360
M 126
WL 4
Q 0
M 127
Q 2
M 138
Q 4
M 139
TY 82,2048
TY 86,15872
M 128
Q 6
M 129
WL 4
Q 8
M 140
Q 10
M 141
TX 90,3072
TX 94,16896
M 130
Q 12
M 131
R 52
M 142
Q 14
M 143
TY 98,3584
TY 102,17408
M 0
R 54
M 1
WL 10
Q 16
M 12
Q 18
M 13
Q 20
M 2
Q 22
M 3
WL 8
Q 24
M 14
Q 26
M 15
Q 28
M 4
Q 30
M 5
WL 6
Q 32
M 16
Q 34
M 17
Q 36
M 6
Q 38
M 7
WL 2
Q 40
M 18
Q 42
M 19
Q 44
M 8
Q 46
M 9
H 1
UL 6,38
M 20
UH 7,38
AD 172,6
M 21
AD 196,7
UL 6,39
M 10
H 2
UH 7,39
M 11
R 56
M 22
AD 220,6
AD 244,7
M 23
R 58
M 24
UL 6,40
UH 7,40
M 25
AD 173,6
AD 197,7
M 26
UL 6,41
UH 7,41
M 27
AD 221,6
AD 245,7
M 28
UL 6,42
UH 7,42
M 29
AD 174,6
AD 198,7
M 30
UL 6,43
UH 7,43
M 31
AD 222,6
AD 246,7
M 32
UL 6,44
UH 7,44
M 33
WV 5
AD 175,6
AD 199,7
M 34
WL 0
FP
UL 6,45
M 35
UH 7,45
AD 223,6
M 36
AD 247,7
M 37
M 38
M 39
M 40
M 41
M 42
M 43
M 44
M 45
FR
M 46
M 47
M 48
M 49
M 50
M 51
M 52
M 53
WL 0
FQ 68
M 54
M 55
M 56
FZ 68
R 72
M 57
mw 24
R 74
M 58
TY 106,5128
TY 110,18952
M 59
R 76
M 60
ad 84,2160
W 0
M 61
M 62
M 63
W 1
M 64
M 65
M 66
W 2
M 67
WL 3
AN 104,106
AN 105,107
M 68
SH 106
SH 107
M 69
mw 27
AN 106,106
AN 107,107
M 70
)"
R"(WL 2
AN 108,110
AN 109,111
M 71
R 78
M 72
SH 110
SH 111
M 73
AN 110,110
AN 111,111
M 74
TY 114,6664
TY 118,20488
M 75
ad 84,2166
W 0
M 76
M 77
M 78
W 1
M 79
M 80
M 81
W 2
M 82
M 83
R 80
M 84
mw 48
WL 3
AN 112,114
AN 113,115
M 85
SH 114
SH 115
M 86
AN 114,114
AN 115,115
M 87
WL 2
AN 116,118
AN 117,119
M 88
SH 118
SH 119
M 89
AN 118,118
AN 119,119
M 90
TY 122,8200
TY 126,22024
M 91
ad 84,2208
M 92
W 0
M 93
ae 44,68,96
M 94
s_bfe_u32 s51, s50, 0x10005
v_mad_i32_i24 v47, v49, s51, v48
XL 22,256
M 95
R 82
M 96
W 1
XL 24,1024
M 97
M 98
M 99
WL 3
AN 120,122
AN 121,123
M 100
SH 122
SH 123
M 101
AN 122,122
AN 123,123
M 102
WL 2
AN 124,126
AN 125,127
M 103
SH 126
SH 127
M 104
AN 126,126
AN 127,127
M 105
W 2
XL 26,1792
M 106
M 107
R 0
M 108
mw 51
XL 28,2560
M 109
ad 84,2214
W 0
M 110
M 111
M 112
W 1
M 113
M 114
M 115
W 2
M 116
M 117
M 118
bc 49,0,66,60
M 119
WL 2
R 2
M 120
mh 0
M 121
ad 66,32
H 0
M 132
M 133
TX 58,4608
TX 62,18432
M 122
DG 1024
M 123
M 134
M 135
TY 66,5120
TY 70,18944
M 124
bc 48,0,64,56
M 125
mh 3
M 136
ad 64,0
H 0
M 137
TX 74,6144
TX 78,19968
M 126
WL 4
Q 0
M 127
Q 2
M 138
Q 4
M 139
TY 82,6656
TY 86,20480
M 128
Q 6
M 129
WL 4
Q 8
M 140
Q 10
M 141
TX 90,7680
TX 94,21504
M 130
Q 12
M 131
R 4
M 142
Q 14
M 143
TY 98,8192
TY 102,22016
M 0
R 6
M 1
WL 10
Q 16
M 12
Q 18
M 13
Q 20
M 2
Q 22
M 3
WL 8
Q 24
M 14
Q 26
M 15
Q 28
M 4
Q 30
M 5
WL 6
Q 32
M 16
Q 34
M 17
Q 36
M 6
Q 38
M 7
WL 2
Q 40
M 18
Q 42
M 19
Q 44
M 8
Q 46
M 9
H 1
UL 6,14
M 20
UH 7,14
AD 176,6
M 21
AD 200,7
UL 6,15
M 10
H 2
UH 7,15
M 11
R 8
M 22
AD 224,6
AD 248,7
M 23
R 10
PA 12288
DF
M 24
UL 6,16
UH 7,16
M 25
AD 177,6
AD 201,7
M 26
UL 6,17
UH 7,17
M 27
AD 225,6
AD 249,7
M 28
UL 6,18
UH 7,18
M 29
AD 178,6
AD 202,7
M 30
UL 6,19
UH 7,19
M 31
AD 226,6
AD 250,7
M 32
UL 6,20
UH 7,20
M 33
WV 6
AD 179,6
AD 203,7
M 34
WL 0
FP
UL 6,21
M 35
UH 7,21
AD 227,6
M 36
AD 251,7
M 37
M 38
M 39
M 40
M 41
M 42
M 43
M 44
M 45
FR
M 46
M 47
M 48
M 49
M 50
M 51
M 52
M 53
WL 0
FQ 72
M 54
M 55
M 56
FZ 72
R 24
M 57
mw 72
R 26
M 58
TY 106,9736
TY 110,23560
M 59
R 28
M 60
ad 84,2256
W 0
M 61
M 62
M 63
W 1
M 64
M 65
M 66
W 2
M 67
WL 3
AN 104,106
AN 105,107
M 68
SH 106
SH 107
M 69
mw 75
AN 106,106
AN 107,107
M 70
WL 2
AN 108,110
AN 109,111
M 71
R 30
M 72
SH 110
SH 111
M 73
AN 110,110
AN 111,111
M 74
TY 114,11272
TY 118,25096
M 75
ad 84,2262
W 0
M 76
M 77
M 78
W 1
M 79
M 80
M 81
W 2
M 82
M 83
R 32
M 84
mw 0
WL 3
AN 112,114
AN 113,115
M 85
SH 114
SH 115
M 86
AN 114,114
AN 115,115
M 87
WL 2
AN 116,118
AN 117,119
M 88
SH 118
SH 119
M 89
AN 118,118
AN 119,119
M 90
TY 122,12808
TY 126,26632
M 91
ad 84,0
M 92
W 0
M 93
M 94
M 95
R 34
M 96
W 1
M 97
M 98
M 99
WL 3
AN 120,122
AN 121,123
M 100
SH 122
SH 123
M 101
AN 122,122
AN 123,123
)"
R"(M 102
WL 2
AN 124,126
AN 125,127
M 103
SH 126
SH 127
M 104
AN 126,126
AN 127,127
M 105
W 2
M 106
M 107
R 48
M 108
mw 3
M 109
ad 84,6
W 0
M 110
M 111
M 112
W 1
M 113
M 114
M 115
W 2
M 116
M 117
M 118
bc 49,1,66,60
M 119
WL 2
R 50
M 120
mh 9
M 121
ad 66,224
H 0
M 132
M 133
TX 58,9216
TX 62,23040
M 122
DG 1024
M 123
M 134
M 135
TY 66,9728
TY 70,23552
M 124
bc 48,1,64,56
M 125
mh 12
M 136
ad 64,224
H 0
M 137
TX 74,10752
TX 78,24576
M 126
WL 4
Q 0
M 127
Q 2
M 138
Q 4
M 139
TY 82,11264
TY 86,25088
M 128
Q 6
M 129
WL 4
Q 8
M 140
Q 10
M 141
TX 90,12288
TX 94,26112
M 130
Q 12
M 131
R 52
M 142
Q 14
M 143
TY 98,12800
TY 102,26624
M 0
R 54
M 1
WL 10
Q 16
M 12
Q 18
M 13
Q 20
M 2
Q 22
M 3
WL 8
Q 24
M 14
Q 26
M 15
Q 28
M 4
Q 30
M 5
WL 6
Q 32
M 16
Q 34
M 17
Q 36
M 6
Q 38
M 7
WL 2
Q 40
M 18
Q 42
M 19
Q 44
M 8
Q 46
M 9
DG 1024
M 20
UL 6,22
UH 7,22
M 21
AD 180,6
AD 204,7
M 10
mh 15
UL 6,23
UH 7,23
M 11
R 56
M 22
ad 64,195
H 0
M 23
R 58
M 24
AD 228,6
AD 252,7
M 25
DG 1024
M 26
UL 6,24
UH 7,24
BV 52,0
M 27
AD 181,6
AD 205,7
M 28
UL 6,25
UH 7,25
M 29
AD 229,6
AD 253,7
M 30
UL 6,26
UH 7,26
M 31
AD 182,6
AD 206,7
M 32
UL 6,27
UH 7,27
M 33
WV 6
AD 230,6
AD 254,7
M 34
WL 1
FP
UL 6,28
M 35
UH 7,28
AD 183,6
M 36
AD 207,7
UL 6,29
M 37
UH 7,29
AD 231,6
M 38
AD 255,7
M 39
M 40
ds_read_b64 v[12:13], v4 offset:256
ds_read_b64 v[46:47], v4 offset:264
M 41
AR 6,36
AR 7,60
M 42
AR 8,84
AR 9,108
M 43
WL 3
EF 6,52
EF 7,53
M 44
EF 8,54
EF 9,55
M 45
FR
CV 30,6,7
M 46
CV 31,8,9
AR 6,37
M 47
AR 7,61
AR 8,85
M 48
AR 9,109
EF 6,52
M 49
EF 7,53
EF 8,54
M 50
EF 9,55
CV 32,6,7
M 51
CV 33,8,9
AR 6,38
M 52
AR 7,62
AR 8,86
M 53
WL 0
FQ 76
AR 9,110
M 54
EF 6,52
EF 7,53
M 55
EF 8,54
EF 9,55
M 56
FZ 76
R 72
M 57
mw 24
R 74
M 58
TY 106,520
TY 110,14344
M 59
R 76
M 60
ad 84,48
W 0
M 61
CV 34,6,7
CV 35,8,9
M 62
AR 6,39
AR 7,63
M 63
W 1
AR 8,87
M 64
AR 9,111
EF 6,52
M 65
EF 7,53
EF 8,54
M 66
W 2
EF 9,55
M 67
WL 3
AN 104,106
AN 105,107
M 68
SH 106
SH 107
M 69
mw 27
AN 106,106
AN 107,107
M 70
WL 2
AN 108,110
AN 109,111
M 71
R 78
M 72
SH 110
SH 111
M 73
AN 110,110
AN 111,111
M 74
TY 114,2056
TY 118,15880
M 75
ad 84,54
W 0
M 76
CV 36,6,7
CV 37,8,9
M 77
ae 42,74,0
AR 6,40
M 78
W 1
AR 7,64
M 79
s_bitcmp1_b32 s50, 0
s_cselect_b32 s52, 24576, 0
v_add_u32 v50, s52, v3
ST 30,0
M 80
AR 8,88
AR 9,112
M 81
W 2
EF 6,52
M 82
EF 7,53
EF 8,54
M 83
WL 1
R 80
M 84
mw 48
AN 112,114
AN 113,115
M 85
SH 114
SH 115
M 86
AN 114,114
AN 115,115
M 87
WL 2
AN 116,118
AN 117,119
M 88
SH 118
SH 119
M 89
AN 118,118
AN 119,119
M 90
TY 122,3592
TY 126,17416
M 91
ad 84,96
ST 32,768
M 92
W 0
EF 9,55
M 93
CV 38,6,7
CV 39,8,9
M 94
AR 6,41
AR 7,65
M 95
R 82
M 96
W 1
AR 8,89
M 97
AR 9,113
EF 6,52
M 98
EF 7,53
EF 8,54
M 99
WL 3
AN 120,122
AN 121,123
M 100
SH 122
SH 123
M 101
AN 122,122
AN 123,123
M 102
WL 2
AN 124,126
AN 125,127
M 103
SH 126
SH 127
ST 34,1536
M 104
AN 126,126
AN 127,127
M 105
W 2
)"
R"(EF 9,55
M 106
CV 40,6,7
CV 41,8,9
M 107
R 0
M 108
mw 51
AR 6,42
AR 7,66
M 109
ad 84,102
W 0
M 110
AR 8,90
AR 9,114
M 111
EF 6,52
EF 7,53
M 112
W 1
EF 8,54
M 113
EF 9,55
CV 42,6,7
M 114
CV 43,8,9
AR 6,43
M 115
W 2
ST 36,2304
M 116
AR 7,67
AR 8,91
M 117
AR 9,115
EF 6,52
M 118
bc 49,2,66,60
EF 7,53
M 119
WL 0
R 2
M 120
mh 18
EF 8,54
EF 9,55
M 121
ad 66,416
H 0
M 132
CV 44,6,7
CV 45,8,9
M 133
TX 58,0
TX 62,13824
BV 52,512
M 122
DG 1024
M 123
AR 6,132
AR 7,156
M 134
AR 8,180
EF 6,12
M 135
TY 66,512
TY 70,14336
M 124
bc 48,2,64,56
ae 42,74,0
M 125
mh 21
EF 7,13
EF 8,46
M 136
ad 64,416
H 0
M 137
WL 3
TX 74,1536
TX 78,15360
M 126
s_bfe_u32 s52, s50, 0x10001
Q 0
v_mad_i32_i24 v50, v49, s52, v48
M 127
Q 2
ST 38,0
M 138
Q 4
M 139
TY 82,2048
TY 86,15872
M 128
Q 6
M 129
WL 4
Q 8
M 140
Q 10
M 141
TX 90,3072
TX 94,16896
M 130
Q 12
M 131
R 4
M 142
Q 14
M 143
TY 98,3584
TY 102,17408
v_mfma_f32_16x16x32_f16 a[36:39], v[56:59], a[0:3], a[32:35]
R 6
M 1
WL 10
Q 16
ST 40,768
v_mfma_f32_16x16x32_f16 a[60:63], v[56:59], a[8:11], a[32:35]
Q 18
M 13
Q 20
v_mfma_f32_16x16x32_f16 a[40:43], v[64:67], a[0:3], a[32:35]
Q 22
M 3
WL 8
Q 24
v_mfma_f32_16x16x32_f16 a[64:67], v[64:67], a[8:11], a[32:35]
Q 26
M 15
Q 28
M 4
Q 30
M 5
WL 6
Q 32
M 16
Q 34
M 17
Q 36
M 6
Q 38
M 7
WL 1
Q 40
ST 42,1536
M 18
Q 42
M 19
Q 44
M 8
Q 46
M 9
H 1
EF 136,47
M 20
CV 14,6,7
CV 15,8,136
M 21
AR 6,133
AR 7,157
M 10
H 2
AR 8,181
M 11
R 8
M 22
EF 6,12
EF 7,13
M 23
R 10
PA 12288
DF
v_mfma_f32_16x16x32_f16 a[84:87], v[56:59], a[16:19], a[32:35]
EF 8,46
EF 137,47
M 25
ST 44,2304
CV 16,6,7
v_mfma_f32_16x16x32_f16 a[88:91], v[64:67], a[16:19], a[32:35]
CV 17,8,137
AR 6,134
M 27
AR 7,158
AR 8,182
M 28
EF 6,12
EF 7,13
M 29
EF 8,46
EF 138,47
M 30
CV 18,6,7
CV 19,8,138
M 31
AR 6,135
AR 7,159
M 32
AR 8,183
EF 6,12
M 33
WV 11
EF 7,13
EF 8,46
M 34
WL 0
FP
EF 139,47
M 35
CV 20,6,7
CV 21,8,139
v_mfma_f32_16x16x32_f16 a[108:111], v[56:59], a[24:27], a[32:35]
AR 6,136
AR 7,160
AR 8,184
EF 6,12
M 37
ae 42,74,0
EF 7,13
EF 8,46
EF 140,47
v_mfma_f32_16x16x32_f16 a[112:115], v[64:67], a[24:27], a[32:35]
s_bitcmp1_b32 s50, 0
s_cselect_b32 s52, 24576, 0
v_add_u32 v50, s52, v3
ST 14,128
CV 22,6,7
M 39
CV 23,8,140
AR 6,137
AR 7,161
AR 8,185
M 40
EF 6,12
EF 7,13
EF 8,46
EF 141,47
M 41
CV 24,6,7
CV 25,8,141
AR 6,138
AR 7,162
M 42
AR 8,186
EF 6,12
EF 7,13
EF 8,46
M 43
EF 142,47
CV 26,6,7
CV 27,8,142
AR 6,139
M 44
AR 7,163
AR 8,187
EF 6,12
EF 7,13
M 45
FR
EF 8,46
EF 143,47
CV 28,6,7
M 46
CV 29,8,143
M 47
ds_read_b64 v[12:13], v4 offset:1536
ds_read_b64 v[46:47], v4 offset:1544
v_mfma_f32_16x16x32_f16 a[132:135], v[56:59], a[0:3], a[32:35]
EF 160,52
EF 184,53
M 49
EF 208,54
EF 232,55
v_mfma_f32_16x16x32_f16 a[136:139], v[64:67], a[0:3], a[32:35]
ST 16,896
CV 30,160,184
M 51
CV 31,208,232
EF 161,52
M 52
EF 185,53
EF 209,54
M 53
WL 2
FQ 80
EF 233,55
M 54
CV 32,161,185
CV 33,209,233
M 55
EF 162,52
EF 186,53
M 56
FZ 80
R 24
M 57
mw 72
R 26
M 58
TY 106,5128
TY 110,18952
M 59
R 28
v_mfma_f32_16x16x32_f16 a[156:159], v[56:59], a[8:11], a[32:35]
ad 84,144
W 0
M 61
EF 210,54
EF 234,55
v_mfma_f32_16x16x32_f16 a[160:163], v[64:67], a[8:11], a[32:35]
ST 18,1664
CV 34,162,186
M 63
W 1
CV 35,210,234
M 64
EF 163,52
EF 187,53
M 65
EF 211,54
EF 235,55
M 66
W 2
CV 36,163,187
M 67
WL 3
AN 104,106
AN 105,107
M 68
SH 106
SH 107
M 69
mw 75
AN 106,106
AN 107,107
M 70
WL 2
AN 108,110
AN 109,111
M 71
R 30
v_mfma_f32_16x16x32_f16 a[180:183], v[56:59], a[16:19], a[32:35]
SH 110
SH 111
M 73
AN 110,110
AN 111,111
v_mfma_f32_16x16x32_f16 a[184:187], v[64:67], a[16:19], a[32:35]
TY 114,6664
TY 118,20488
ST 20,2432
M 75
ad 84,150
W 0
M 76
CV 37,211,235
M 77
EF 164,52
EF 188,53
EF 212,54
EF 236,55
M 78
W 1
CV 38,164,188
CV 39,212,236
EF 165,52
M 79
EF 189,53
EF 213,54
EF 237,55
CV 40,165,189
M 80
CV 41,213,237
EF 166,52
EF 190,53
EF 214,54
M 81
W 2
EF 238,55
CV 42,166,190
CV 43,214,238
M 82
EF 167,52
EF 191,53
EF 215,54
EF 239,55
M 83
WL 0
R 32
CV 44,167,191
CV 45,215,239
v_mfma_f32_16x16x32_f16 v[136:139], v[56:59], a[24:27], v[128:131]
mw 0
AN 112,114
AN 113,115
BV 52,1792
M 85
SH 114
SH 115
)"
R"(AR 6,44
v_mfma_f32_16x16x32_f16 v[140:143], v[64:67], a[24:27], v[128:131]
AN 114,114
AN 115,115
AR 7,68
M 87
AN 116,118
AN 117,119
AR 8,92
M 88
SH 118
SH 119
AR 9,116
M 89
AN 118,118
AN 119,119
EF 6,12
M 90
TY 122,8200
TY 126,22024
EF 7,13
M 91
ad 84,192
EF 8,46
M 92
W 0
ae 42,74,0
M 93
s_bfe_u32 s52, s50, 0x10001
v_mad_i32_i24 v50, v49, s52, v48
ST 22,128
EF 9,47
M 94
CV 14,6,7
CV 15,8,9
AR 6,45
M 95
R 34
AR 7,69
v_mfma_f32_16x16x32_f16 v[160:163], v[56:59], a[0:3], v[128:131]
W 1
AR 8,93
AR 9,117
M 97
EF 6,12
EF 7,13
EF 8,46
v_mfma_f32_16x16x32_f16 v[164:167], v[64:67], a[0:3], v[128:131]
EF 9,47
CV 16,6,7
CV 17,8,9
M 99
WL 3
AN 120,122
AN 121,123
AR 6,46
M 100
SH 122
SH 123
AR 7,70
M 101
AN 122,122
AN 123,123
AR 8,94
M 102
WL 2
AN 124,126
AN 125,127
AR 9,118
M 103
SH 126
SH 127
EF 6,12
M 104
AN 126,126
AN 127,127
EF 7,13
M 105
W 2
ST 24,896
EF 8,46
M 106
EF 9,47
CV 18,6,7
CV 19,8,9
M 107
R 48
AR 6,47
v_mfma_f32_16x16x32_f16 v[184:187], v[56:59], a[8:11], v[128:131]
mw 3
AR 7,71
AR 8,95
AR 9,119
M 109
ad 84,198
W 0
EF 6,12
v_mfma_f32_16x16x32_f16 v[188:191], v[64:67], a[8:11], v[128:131]
EF 7,13
EF 8,46
EF 9,47
M 111
CV 20,6,7
CV 21,8,9
M 112
W 1
AR 6,48
AR 7,72
M 113
AR 8,96
AR 9,120
EF 6,12
M 114
EF 7,13
EF 8,46
EF 9,47
M 115
W 2
CV 6,6,7
CV 7,8,9
M 116
AW 200,6
AW 201,7
AR 6,49
M 117
ST 26,1664
AR 7,73
AR 8,97
M 118
bc 49,3,66,60
AR 9,121
EF 6,12
M 119
WL 0
R 50
EF 7,13
v_mfma_f32_16x16x32_f16 v[208:211], v[56:59], a[16:19], v[128:131]
mh 0
EF 8,46
EF 9,47
CV 6,6,7
M 121
ad 66,608
H 0
CV 7,8,9
v_mfma_f32_16x16x32_f16 v[232:235], v[56:59], a[24:27], v[128:131]
AW 202,6
AW 203,7
AR 6,50
M 133
TX 58,4608
TX 62,18432
AR 7,74
v_mfma_f32_16x16x32_f16 v[212:215], v[64:67], a[16:19], v[128:131]
DG 1024
AR 8,98
M 123
AR 9,122
EF 6,12
EF 7,13
v_mfma_f32_16x16x32_f16 v[236:239], v[64:67], a[24:27], v[128:131]
EF 8,46
EF 9,47
CV 6,6,7
M 135
TY 66,5120
TY 70,18944
CV 7,8,9
M 124
bc 48,3,64,56
AW 204,6
AW 205,7
M 125
mh 3
ST 28,2432
AR 6,51
AR 7,75
M 136
ad 64,608
H 0
AR 8,99
M 137
TX 74,6144
TX 78,19968
AR 9,123
M 126
WL 4
Q 0
EF 6,12
M 127
Q 2
EF 7,13
M 138
Q 4
EF 8,46
M 139
TY 82,6656
TY 86,20480
EF 9,47
M 128
Q 6
CV 6,6,7
M 129
WL 4
Q 8
CV 7,8,9
M 140
Q 10
AW 206,6
M 141
TX 90,7680
TX 94,21504
AW 207,7
M 130
Q 12
ds_read_b64 v[12:13], v4 offset:2048
M 131
R 52
ds_read_b64 v[46:47], v4 offset:2056
M 142
Q 14
M 143
TY 98,8192
TY 102,22016
M 0
R 54
M 1
WL 12
Q 16
M 12
Q 18
M 13
Q 20
M 2
Q 22
M 3
WL 10
Q 24
M 14
Q 26
M 15
Q 28
v_mfma_f32_16x16x32_f16 a[44:47], v[72:75], a[0:3], a[32:35]
Q 30
M 5
WL 7
Q 32
v_mfma_f32_16x16x32_f16 a[68:71], v[72:75], a[8:11], a[32:35]
Q 34
M 17
Q 36
v_mfma_f32_16x16x32_f16 a[48:51], v[80:83], a[0:3], a[32:35]
Q 38
M 7
WL 2
Q 40
v_mfma_f32_16x16x32_f16 a[72:75], v[80:83], a[8:11], a[32:35]
Q 42
M 19
Q 44
M 8
Q 46
M 9
H 1
ae 42,74,0
M 20
s_bitcmp1_b32 s50, 0
s_cselect_b32 s52, 24576, 0
v_add_u32 v50, s52, v3
ST 30,256
M 21
AR 6,140
AR 7,164
M 10
H 2
AR 8,188
M 11
R 56
M 22
EF 6,52
EF 7,53
M 23
R 58
M 24
EF 8,54
EF 144,55
M 25
CV 22,6,7
CV 23,8,144
M 26
AR 6,141
AR 7,165
M 27
AR 8,189
EF 6,52
v_mfma_f32_16x16x32_f16 a[92:95], v[72:75], a[16:19], a[32:35]
EF 7,53
EF 8,54
M 29
EF 145,55
CV 24,6,7
v_mfma_f32_16x16x32_f16 a[96:99], v[80:83], a[16:19], a[32:35]
ST 32,1024
CV 25,8,145
M 31
AR 6,142
AR 7,166
M 32
AR 8,190
EF 6,52
M 33
WV 9
EF 7,53
EF 8,54
M 34
WL 0
FP
EF 146,55
M 35
CV 26,6,7
CV 27,8,146
M 36
AR 6,143
AR 7,167
M 37
AR 8,191
EF 6,52
M 38
EF 7,53
EF 8,54
M 39
EF 147,55
CV 28,6,7
v_mfma_f32_16x16x32_f16 a[116:119], v[72:75], a[24:27], a[32:35]
CV 29,8,147
M 41
AR 6,144
AR 7,168
AR 8,192
EF 6,52
v_mfma_f32_16x16x32_f16 a[120:123], v[80:83], a[24:27], a[32:35]
ST 34,1792
EF 7,53
EF 8,54
EF 148,55
M 43
CV 6,6,7
CV 7,8,148
AW 208,6
AW 209,7
M 44
AR 6,145
AR 7,169
AR 8,193
EF 6,52
M 45
FR
EF 7,53
EF 8,54
EF 149,55
M 46
CV 6,6,7
CV 7,8,149
AW 210,6
AW 211,7
M 47
AR 6,146
AR 7,170
AR 8,194
EF 6,52
M 48
EF 7,53
EF 8,54
EF 150,55
CV 6,6,7
M 49
CV 7,8,150
AW 212,6
AW 213,7
AR 6,147
M 50
AR 7,171
AR 8,195
EF 6,52
EF 7,53
M 51
EF 8,54
EF 151,55
CV 6,6,7
CV 7,8,151
v_mfma_f32_16x16x32_f16 a[140:143], v[72:75], a[0:3], a[32:35]
AW 214,6
AW 215,7
M 53
WL 0
FQ 84
BV 52,3072
EF 168,12
v_mfma_f32_16x16x32_f16 a[144:147], v[80:83], a[0:3], a[32:35]
ST 36,2560
)"
R"(EF 192,13
EF 216,46
M 55
EF 240,47
CV 6,168,192
CV 7,216,240
M 56
FZ 84
R 72
AW 216,6
M 57
mw 24
R 74
AW 217,7
M 58
TY 106,9736
TY 110,23560
EF 169,12
M 59
R 76
EF 193,13
M 60
ad 84,240
W 0
EF 217,46
M 61
EF 241,47
CV 6,169,193
CV 7,217,241
M 62
AW 218,6
AW 219,7
EF 170,12
M 63
W 1
EF 194,13
EF 218,46
v_mfma_f32_16x16x32_f16 a[164:167], v[72:75], a[8:11], a[32:35]
EF 242,47
CV 6,170,194
CV 7,218,242
M 65
AW 220,6
AW 221,7
EF 171,12
v_mfma_f32_16x16x32_f16 a[168:171], v[80:83], a[8:11], a[32:35]
WL 3
W 2
ae 42,74,0
M 67
s_bfe_u32 s52, s50, 0x10001
AN 104,106
AN 105,107
v_mad_i32_i24 v50, v49, s52, v48
M 68
SH 106
SH 107
ST 38,256
M 69
mw 27
AN 106,106
AN 107,107
EF 195,13
M 70
WL 0
AN 108,110
AN 109,111
EF 219,46
M 71
R 78
EF 243,47
M 72
SH 110
SH 111
CV 6,171,195
M 73
AN 110,110
AN 111,111
CV 7,219,243
M 74
TY 114,11272
TY 118,25096
AW 222,6
M 75
ad 84,246
W 0
AW 223,7
v_mfma_f32_16x16x32_f16 a[188:191], v[72:75], a[16:19], a[32:35]
EF 172,12
EF 196,13
EF 220,46
M 77
EF 244,47
CV 30,172,196
CV 31,220,244
v_mfma_f32_16x16x32_f16 a[192:195], v[80:83], a[16:19], a[32:35]
W 1
EF 173,12
EF 197,13
M 79
EF 221,46
EF 245,47
CV 32,173,197
M 80
ST 40,1024
CV 33,221,245
EF 174,12
M 81
W 2
EF 198,13
EF 222,46
M 82
EF 246,47
CV 34,174,198
CV 35,222,246
M 83
WL 0
R 80
EF 175,12
M 84
mw 48
AN 112,114
AN 113,115
EF 199,13
M 85
SH 114
SH 115
EF 223,46
M 86
AN 114,114
AN 115,115
EF 247,47
M 87
AN 116,118
AN 117,119
CV 36,175,199
v_mfma_f32_16x16x32_f16 v[144:147], v[72:75], a[24:27], v[128:131]
SH 118
SH 119
CV 37,223,247
M 89
AN 118,118
AN 119,119
ds_read_b64 v[12:13], v4 offset:3328
v_mfma_f32_16x16x32_f16 v[148:151], v[80:83], a[24:27], v[128:131]
TY 122,12808
TY 126,26632
ds_read_b64 v[46:47], v4 offset:3336
M 91
ad 84,288
AR 6,52
M 92
W 0
ST 42,1792
AR 7,76
M 93
AR 8,100
AR 9,124
EF 6,52
M 94
EF 7,53
EF 8,54
EF 9,55
M 95
R 82
CV 6,6,7
M 96
W 1
CV 7,8,9
AW 224,6
M 97
AW 225,7
AR 6,53
AR 7,77
M 98
AR 8,101
AR 9,125
EF 6,52
M 99
WL 4
AN 120,122
AN 121,123
EF 7,53
v_mfma_f32_16x16x32_f16 v[168:171], v[72:75], a[0:3], v[128:131]
SH 122
SH 123
EF 8,54
M 101
AN 122,122
AN 123,123
EF 9,55
v_mfma_f32_16x16x32_f16 v[172:175], v[80:83], a[0:3], v[128:131]
WL 2
AN 124,126
AN 125,127
CV 6,6,7
M 103
SH 126
SH 127
CV 7,8,9
M 104
AN 126,126
AN 127,127
ST 44,2560
M 105
W 2
AW 226,6
AW 227,7
M 106
AR 6,54
AR 7,78
AR 8,102
M 107
R 0
AR 9,126
M 108
mw 51
EF 6,52
EF 7,53
EF 8,54
M 109
ad 84,294
W 0
EF 9,55
M 110
CV 6,6,7
CV 7,8,9
AW 228,6
M 111
AW 229,7
AR 6,55
AR 7,79
v_mfma_f32_16x16x32_f16 v[192:195], v[72:75], a[8:11], v[128:131]
W 1
AR 8,103
AR 9,127
M 113
EF 6,52
EF 7,53
EF 8,54
v_mfma_f32_16x16x32_f16 v[196:199], v[80:83], a[8:11], v[128:131]
EF 9,55
CV 6,6,7
CV 7,8,9
M 115
W 2
AW 230,6
AW 231,7
M 116
ae 42,72,0
AR 6,56
AR 7,80
M 117
s_bitcmp1_b32 s50, 2
s_cselect_b32 s52, 24576, 0
v_add_u32 v50, s52, v3
ST 14,0
M 118
bc 49,4,66,60
AR 8,104
AR 9,128
M 119
WL 0
R 2
EF 6,52
M 120
mh 9
EF 7,53
EF 8,54
EF 9,55
M 121
ad 66,800
H 0
CV 38,6,7
M 132
CV 39,8,9
AR 6,57
AR 7,81
M 133
TX 58,9216
TX 62,23040
AR 8,105
M 122
DG 1024
AR 9,129
M 123
EF 6,52
EF 7,53
EF 8,54
M 134
EF 9,55
CV 40,6,7
CV 41,8,9
M 135
TY 66,9728
TY 70,23552
AR 6,58
v_mfma_f32_16x16x32_f16 v[216:219], v[72:75], a[16:19], v[128:131]
bc 48,4,64,56
AR 7,82
AR 8,106
M 125
mh 12
ST 16,768
AR 9,130
EF 6,52
v_mfma_f32_16x16x32_f16 v[240:243], v[72:75], a[24:27], v[128:131]
ad 64,800
H 0
EF 7,53
M 137
TX 74,10752
TX 78,24576
EF 8,54
v_mfma_f32_16x16x32_f16 v[220:223], v[80:83], a[16:19], v[128:131]
WL 4
Q 0
EF 9,55
M 127
Q 2
CV 42,6,7
v_mfma_f32_16x16x32_f16 v[244:247], v[80:83], a[24:27], v[128:131]
Q 4
CV 43,8,9
M 139
TY 82,11264
TY 86,25088
AR 6,59
M 128
Q 6
AR 7,83
M 129
WL 4
Q 8
AR 8,107
M 140
Q 10
AR 9,131
M 141
TX 90,12288
TX 94,26112
EF 6,52
M 130
Q 12
EF 7,53
M 131
R 4
ST 18,1536
M 142
Q 14
EF 8,54
M 143
TY 98,12800
TY 102,26624
EF 9,55
M 0
R 6
CV 44,6,7
M 1
WL 8
Q 16
CV 45,8,9
M 12
Q 18
BV 52,3584
M 13
Q 20
M 2
Q 22
M 3
Q 24
M 14
Q 26
M 15
Q 28
M 4
Q 30
M 5
WL 5
Q 32
ST 20,2304
M 16
Q 34
M 17
Q 36
M 6
Q 38
M 7
WL 1
Q 40
M 18
Q 42
M 19
Q 44
v_mfma_f32_16x16x32_f16 a[52:55], v[88:91], a[0:3], a[32:35]
Q 46
M 9
H 1
AR 6,148
v_mfma_f32_16x16x32_f16 a[76:79], v[88:91], a[8:11], a[32:35]
)"
R"(AR 7,172
AR 8,196
M 21
EF 6,12
EF 7,13
v_mfma_f32_16x16x32_f16 a[56:59], v[96:99], a[0:3], a[32:35]
H 2
EF 8,46
M 11
R 8
v_mfma_f32_16x16x32_f16 a[80:83], v[96:99], a[8:11], a[32:35]
ae 42,72,0
ST 22,128
M 23
R 10
PA 12288
DF
M 24
EF 152,47
CV 6,6,7
M 25
CV 7,8,152
AW 232,6
M 26
AW 233,7
AR 6,149
M 27
AR 7,173
AR 8,197
M 28
EF 6,12
EF 7,13
M 29
EF 8,46
EF 153,47
M 30
CV 6,6,7
CV 7,8,153
M 31
AW 234,6
AW 235,7
v_mfma_f32_16x16x32_f16 a[100:103], v[88:91], a[16:19], a[32:35]
AR 6,150
AR 7,174
M 33
WV 11
AR 8,198
EF 6,12
v_mfma_f32_16x16x32_f16 a[104:107], v[96:99], a[16:19], a[32:35]
WL 0
FP
ST 24,896
M 35
EF 7,13
EF 8,46
M 36
EF 154,47
CV 6,6,7
M 37
CV 7,8,154
AW 236,6
M 38
AW 237,7
AR 6,151
M 39
AR 7,175
AR 8,199
M 40
EF 6,12
EF 7,13
M 41
EF 8,46
EF 155,47
M 42
CV 6,6,7
CV 7,8,155
M 43
AW 238,6
AW 239,7
v_mfma_f32_16x16x32_f16 a[124:127], v[88:91], a[24:27], a[32:35]
AR 6,152
AR 7,176
EF 6,12
M 45
FR
EF 7,13
EF 132,46
v_mfma_f32_16x16x32_f16 a[128:131], v[96:99], a[24:27], a[32:35]
ST 26,1664
EF 156,47
CV 14,6,7
M 47
CV 15,132,156
AR 6,153
AR 7,177
M 48
EF 6,12
EF 7,13
EF 133,46
M 49
EF 157,47
CV 16,6,7
CV 17,133,157
M 50
AR 6,154
AR 7,178
EF 6,12
M 51
EF 7,13
EF 134,46
EF 158,47
M 52
CV 18,6,7
CV 19,134,158
AR 6,155
M 53
WL 0
FQ 88
AR 7,179
EF 6,12
M 54
EF 7,13
EF 135,46
EF 159,47
M 55
CV 20,6,7
CV 21,135,159
v_mfma_f32_16x16x32_f16 a[148:151], v[88:91], a[0:3], a[32:35]
FZ 88
R 24
EF 176,52
M 57
mw 72
R 26
EF 200,53
v_mfma_f32_16x16x32_f16 a[152:155], v[96:99], a[0:3], a[32:35]
TY 106,520
TY 110,14344
ST 28,2432
M 59
R 28
EF 224,54
M 60
ad 84,336
W 0
EF 248,55
M 61
CV 6,176,200
CV 7,224,248
AW 240,6
M 62
AW 241,7
EF 177,52
EF 201,53
M 63
W 1
EF 225,54
EF 249,55
M 64
CV 6,177,201
CV 7,225,249
AW 242,6
M 65
AW 243,7
EF 178,52
EF 202,53
M 66
W 2
EF 226,54
EF 250,55
M 67
WL 3
AN 104,106
AN 105,107
CV 6,178,202
v_mfma_f32_16x16x32_f16 a[172:175], v[88:91], a[8:11], a[32:35]
SH 106
SH 107
CV 7,226,250
M 69
mw 75
AN 106,106
AN 107,107
AW 244,6
v_mfma_f32_16x16x32_f16 a[176:179], v[96:99], a[8:11], a[32:35]
WL 0
AN 108,110
AN 109,111
AW 245,7
M 71
R 30
EF 179,52
M 72
SH 110
SH 111
EF 203,53
M 73
AN 110,110
AN 111,111
EF 227,54
M 74
TY 114,2056
TY 118,15880
EF 251,55
M 75
ad 84,342
W 0
CV 6,179,203
M 76
ae 42,72,0
CV 7,227,251
AW 246,6
M 77
s_bfe_u32 s52, s50, 0x10003
v_mad_i32_i24 v50, v49, s52, v48
ST 30,256
AW 247,7
M 78
W 1
EF 180,52
EF 204,53
M 79
EF 228,54
EF 252,55
CV 6,180,204
v_mfma_f32_16x16x32_f16 a[196:199], v[88:91], a[16:19], a[32:35]
CV 7,228,252
AW 248,6
AW 249,7
M 81
W 2
EF 181,52
EF 205,53
v_mfma_f32_16x16x32_f16 v[132:135], v[96:99], a[16:19], v[128:131]
EF 229,54
EF 253,55
CV 6,181,205
M 83
WL 0
R 32
CV 7,229,253
M 84
mw 0
AN 112,114
AN 113,115
AW 250,6
M 85
SH 114
SH 115
AW 251,7
M 86
AN 114,114
AN 115,115
EF 182,52
M 87
AN 116,118
AN 117,119
EF 206,53
M 88
SH 118
SH 119
EF 230,54
M 89
AN 118,118
AN 119,119
ST 32,1024
M 90
TY 122,3592
TY 126,17416
EF 254,55
M 91
ad 84,384
CV 6,182,206
v_mfma_f32_16x16x32_f16 v[152:155], v[88:91], a[24:27], v[128:131]
W 0
CV 7,230,254
AW 252,6
M 93
AW 253,7
EF 183,52
EF 207,53
v_mfma_f32_16x16x32_f16 v[156:159], v[96:99], a[24:27], v[128:131]
EF 231,54
EF 255,55
CV 6,183,207
M 95
R 34
CV 7,231,255
M 96
W 1
AW 254,6
AW 255,7
M 97
M 98
M 99
WL 3
AN 120,122
AN 121,123
M 100
SH 122
SH 123
M 101
AN 122,122
AN 123,123
ST 34,1792
M 102
WL 2
AN 124,126
AN 125,127
M 103
SH 126
SH 127
v_mfma_f32_16x16x32_f16 v[176:179], v[88:91], a[0:3], v[128:131]
AN 126,126
AN 127,127
M 105
W 2
v_mfma_f32_16x16x32_f16 v[180:183], v[96:99], a[0:3], v[128:131]
M 107
R 48
M 108
mw 3
M 109
ad 84,390
W 0
M 110
M 111
M 112
W 1
M 113
ST 36,2560
M 114
M 115
W 2
v_mfma_f32_16x16x32_f16 v[200:203], v[88:91], a[8:11], v[128:131]
M 117
v_mfma_f32_16x16x32_f16 v[204:207], v[96:99], a[8:11], v[128:131]
bc 49,5,66,60
M 119
WL 2
R 50
M 120
mh 18
M 121
ad 66,992
H 0
M 132
M 133
TX 58,0
TX 62,13824
M 122
DG 1024
M 123
ae 42,72,96
M 134
s_bfe_u32 s52, s50, 0x10005
v_mad_i32_i24 v50, v49, s52, v48
ST 38,0
M 135
TY 66,512
TY 70,14336
M 124
bc 48,5,64,56
M 125
mh 21
M 136
ad 64,992
H 0
M 137
TX 74,1536
TX 78,15360
M 126
WL 4
Q 0
M 127
Q 2
M 138
Q 4
M 139
TY 82,2048
TY 86,15872
v_mfma_f32_16x16x32_f16 v[224:227], v[88:91], a[16:19], v[128:131]
Q 6
M 129
WL 4
Q 8
v_mfma_f32_16x16x32_f16 v[248:251], v[88:91], a[24:27], v[128:131]
Q 10
ST 40,768
M 141
TX 90,3072
TX 94,16896
v_mfma_f32_16x16x32_f16 v[228:231], v[96:99], a[16:19], v[128:131]
Q 12
M 131
R 52
v_mfma_f32_16x16x32_f16 v[252:255], v[96:99], a[24:27], v[128:131]
Q 14
M 143
TY 98,3584
)"
R"(TY 102,17408
M 0
R 54
M 1
WL 10
Q 16
M 12
Q 18
M 13
Q 20
M 2
Q 22
M 3
WL 8
Q 24
M 14
Q 26
ST 42,1536
M 15
Q 28
M 4
Q 30
M 5
WL 6
Q 32
M 16
Q 34
M 17
Q 36
M 6
Q 38
M 7
WL 1
Q 40
M 18
Q 42
M 19
Q 44
M 8
Q 46
M 9
H 1
M 20
ST 44,2304
M 21
M 10
H 2
M 11
R 56
M 22
M 23
R 58
M 24
M 25
M 26
M 27
M 28
M 29
M 30
ae 42,72,96
ST 14,128
M 31
M 32
M 33
WV 10
M 34
WL 0
FP
M 35
M 36
M 37
M 38
M 39
M 40
M 41
M 42
ST 16,896
M 43
M 44
M 45
FR
M 46
M 47
M 48
M 49
M 50
M 51
M 52
M 53
WL 0
FQ 92
M 54
ST 18,1664
M 55
M 56
FZ 92
R 72
M 57
mw 24
R 74
M 58
TY 106,5128
TY 110,18952
M 59
R 76
M 60
ad 84,432
W 0
M 61
M 62
M 63
W 1
M 64
M 65
M 66
W 2
ST 20,2432
M 67
WL 3
AN 104,106
AN 105,107
M 68
SH 106
SH 107
M 69
mw 27
AN 106,106
AN 107,107
M 70
WL 2
AN 108,110
AN 109,111
M 71
R 78
M 72
SH 110
SH 111
M 73
AN 110,110
AN 111,111
M 74
TY 114,6664
TY 118,20488
M 75
ad 84,438
W 0
M 76
M 77
M 78
W 1
M 79
M 80
M 81
W 2
M 82
M 83
R 80
M 84
mw 48
WL 3
AN 112,114
AN 113,115
M 85
SH 114
SH 115
M 86
AN 114,114
AN 115,115
M 87
WL 2
AN 116,118
AN 117,119
M 88
SH 118
SH 119
M 89
AN 118,118
AN 119,119
M 90
TY 122,8200
TY 126,22024
M 91
ad 84,480
M 92
W 0
M 93
M 94
M 95
R 82
M 96
W 1
M 97
M 98
M 99
WL 3
AN 120,122
AN 121,123
M 100
SH 122
SH 123
M 101
AN 122,122
AN 123,123
M 102
WL 2
AN 124,126
AN 125,127
M 103
SH 126
SH 127
M 104
AN 126,126
AN 127,127
M 105
W 2
M 106
M 107
R 0
M 108
mw 51
M 109
ad 84,486
W 0
M 110
M 111
M 112
W 1
M 113
M 114
M 115
W 2
M 116
M 117
M 118
bc 49,6,66,60
M 119
WL 2
R 2
M 120
mh 0
M 121
ad 66,1184
H 0
M 132
M 133
TX 58,4608
TX 62,18432
M 122
DG 1024
M 123
M 134
M 135
TY 66,5120
TY 70,18944
M 124
bc 48,6,64,56
M 125
mh 3
ae 44,70,384
M 136
ad 64,1184
H 0
M 137
TX 74,6144
TX 78,19968
M 126
WL 4
Q 0
M 127
Q 2
M 138
Q 4
M 139
TY 82,6656
TY 86,20480
M 128
Q 6
M 129
WL 4
Q 8
M 140
Q 10
M 141
TX 90,7680
TX 94,21504
M 130
Q 12
M 131
R 4
M 142
Q 14
M 143
TY 98,8192
TY 102,22016
M 0
R 6
M 1
WL 10
Q 16
M 12
Q 18
M 13
Q 20
M 2
Q 22
M 3
WL 8
Q 24
M 14
Q 26
M 15
Q 28
M 4
Q 30
M 5
WL 6
Q 32
M 16
Q 34
M 17
Q 36
M 6
Q 38
M 7
WL 2
Q 40
M 18
Q 42
M 19
Q 44
M 8
Q 46
M 9
H 1
M 20
ae 42,72,0
M 21
s_bfe_u32 s52, s50, 0x10003
v_mad_i32_i24 v50, v49, s52, v48
SA 200,0
M 10
H 2
M 11
R 8
M 22
s_bitcmp1_b32 s50, 6
s_cselect_b32 s51, 24576, 0
M 23
R 10
PA 12288
DF
M 24
v_add_u32 v47, s51, v3
XL 22,0
M 25
XL 24,768
M 26
XL 26,1536
M 27
XL 28,2304
M 28
M 29
M 30
M 31
M 32
M 33
WV 11
M 34
WL 0
FP
M 35
M 36
M 37
M 38
M 39
M 40
M 41
M 42
M 43
M 44
M 45
FR
M 46
M 47
M 48
M 49
M 50
M 51
M 52
M 53
WL 0
FQ 96
M 54
M 55
M 56
FZ 96
R 24
M 57
mw 72
R 26
)"
R"(M 58
TY 106,9736
TY 110,23560
M 59
R 28
M 60
ad 84,528
W 0
M 61
M 62
M 63
W 1
M 64
M 65
M 66
W 2
M 67
WL 3
AN 104,106
AN 105,107
M 68
SH 106
SH 107
M 69
mw 75
AN 106,106
AN 107,107
M 70
WL 2
AN 108,110
AN 109,111
M 71
R 30
M 72
SH 110
SH 111
M 73
AN 110,110
AN 111,111
M 74
TY 114,11272
TY 118,25096
M 75
ad 84,534
W 0
M 76
M 77
M 78
W 1
M 79
M 80
M 81
W 2
M 82
M 83
R 32
M 84
mw 0
WL 3
AN 112,114
AN 113,115
M 85
SH 114
SH 115
M 86
AN 114,114
AN 115,115
M 87
WL 2
AN 116,118
AN 117,119
M 88
SH 118
SH 119
M 89
AN 118,118
AN 119,119
M 90
TY 122,12808
TY 126,26632
M 91
ad 84,576
M 92
W 0
SA 202,768
M 93
M 94
M 95
R 34
M 96
W 1
M 97
M 98
M 99
WL 3
AN 120,122
AN 121,123
M 100
SH 122
SH 123
M 101
AN 122,122
AN 123,123
M 102
WL 2
AN 124,126
AN 125,127
M 103
SH 126
SH 127
M 104
AN 126,126
AN 127,127
M 105
W 2
M 106
M 107
R 48
M 108
mw 3
M 109
ad 84,582
W 0
M 110
M 111
M 112
W 1
M 113
M 114
M 115
W 2
M 116
M 117
M 118
bc 49,7,66,60
M 119
WL 2
R 50
M 120
mh 9
M 121
ad 66,1376
H 0
M 132
M 133
TX 58,9216
TX 62,23040
M 122
DG 1024
M 123
M 134
M 135
TY 66,9728
TY 70,23552
M 124
bc 48,7,64,56
M 125
mh 12
M 136
ad 64,1376
H 0
M 137
TX 74,10752
TX 78,24576
M 126
WL 4
Q 0
M 127
Q 2
M 138
Q 4
M 139
TY 82,11264
TY 86,25088
M 128
Q 6
M 129
WL 4
Q 8
M 140
Q 10
M 141
TX 90,12288
TX 94,26112
M 130
Q 12
M 131
R 52
M 142
Q 14
M 143
TY 98,12800
TY 102,26624
M 0
R 54
M 1
WL 10
Q 16
M 12
Q 18
M 13
Q 20
M 2
Q 22
M 3
WL 8
Q 24
M 14
Q 26
M 15
Q 28
M 4
Q 30
M 5
WL 6
Q 32
M 16
Q 34
M 17
Q 36
M 6
Q 38
M 7
WL 2
Q 40
M 18
Q 42
M 19
Q 44
M 8
Q 46
M 9
H 1
SA 204,1536
M 20
ae 44,70,384
M 21
s_bfe_u32 s51, s50, 0x10007
v_mad_i32_i24 v47, v49, s51, v48
XL 30,0
M 10
H 2
XL 32,768
M 11
R 56
M 22
XL 34,1536
M 23
R 58
M 24
XL 36,2304
M 25
M 26
M 27
M 28
M 29
M 30
M 31
M 32
M 33
WV 10
M 34
WL 0
FP
M 35
M 36
M 37
M 38
M 39
M 40
UL 6,22
UH 7,22
M 41
AR 8,36
AR 9,60
M 42
AD 8,6
AD 9,7
M 43
AW 36,8
AW 60,9
M 44
UL 6,23
UH 7,23
M 45
FR
AR 8,84
M 46
AR 9,108
AD 8,6
M 47
AD 9,7
AW 84,8
M 48
AW 108,9
UL 6,24
M 49
UH 7,24
AR 8,37
M 50
AR 9,61
AD 8,6
M 51
AD 9,7
AW 37,8
M 52
AW 61,9
UL 6,25
M 53
WL 0
FQ 100
UH 7,25
M 54
AR 8,85
AR 9,109
M 55
AD 8,6
AD 9,7
M 56
FZ 100
R 72
M 57
mw 24
R 74
M 58
TY 106,520
TY 110,14344
M 59
R 76
M 60
ad 84,624
W 0
M 61
AW 85,8
AW 109,9
M 62
UL 6,26
UH 7,26
M 63
W 1
AR 8,38
M 64
AR 9,62
AD 8,6
M 65
AD 9,7
AW 38,8
M 66
W 2
AW 62,9
M 67
WL 3
AN 104,106
AN 105,107
M 68
SH 106
SH 107
M 69
mw 27
AN 106,106
AN 107,107
M 70
WL 2
AN 108,110
AN 109,111
M 71
R 78
M 72
SH 110
SH 111
M 73
AN 110,110
AN 111,111
M 74
TY 114,2056
TY 118,15880
M 75
ad 84,630
W 0
M 76
UL 6,27
UH 7,27
M 77
AR 8,86
AR 9,110
M 78
W 1
AD 8,6
M 79
AD 9,7
AW 86,8
M 80
AW 110,9
UL 6,28
M 81
W 2
SA 206,2304
M 82
UH 7,28
AR 8,39
)"
R"(M 83
WL 1
R 80
M 84
mw 48
AN 112,114
AN 113,115
M 85
SH 114
SH 115
M 86
AN 114,114
AN 115,115
M 87
WL 2
AN 116,118
AN 117,119
M 88
SH 118
SH 119
M 89
AN 118,118
AN 119,119
M 90
TY 122,3592
TY 126,17416
M 91
ad 84,672
M 92
W 0
AR 9,63
M 93
AD 8,6
AD 9,7
M 94
AW 39,8
AW 63,9
M 95
R 82
M 96
W 1
UL 6,29
M 97
UH 7,29
AR 8,87
M 98
AR 9,111
AD 8,6
M 99
WL 3
AN 120,122
AN 121,123
M 100
SH 122
SH 123
M 101
AN 122,122
AN 123,123
M 102
WL 2
AN 124,126
AN 125,127
M 103
SH 126
SH 127
M 104
AN 126,126
AN 127,127
M 105
W 2
AD 9,7
M 106
AW 87,8
AW 111,9
M 107
R 0
M 108
mw 51
M 109
ad 84,678
W 0
M 110
M 111
M 112
W 1
M 113
M 114
M 115
W 2
M 116
M 117
M 118
bc 49,8,66,60
M 119
WL 2
R 2
M 120
mh 18
M 121
ad 66,1568
H 0
M 132
M 133
TX 58,0
TX 62,13824
M 122
DG 1024
M 123
M 134
M 135
TY 66,512
TY 70,14336
M 124
bc 48,8,64,56
M 125
mh 21
M 136
ad 64,1568
H 0
M 137
TX 74,1536
TX 78,15360
M 126
WL 4
Q 0
M 127
Q 2
M 138
Q 4
M 139
TY 82,2048
TY 86,15872
M 128
Q 6
M 129
WL 4
Q 8
M 140
Q 10
M 141
TX 90,3072
TX 94,16896
M 130
Q 12
M 131
R 4
M 142
Q 14
M 143
TY 98,3584
TY 102,17408
M 0
R 6
M 1
WL 10
Q 16
M 12
Q 18
M 13
Q 20
M 2
Q 22
M 3
WL 8
Q 24
M 14
Q 26
M 15
Q 28
M 4
Q 30
M 5
WL 6
Q 32
M 16
Q 34
M 17
Q 36
M 6
Q 38
M 7
WL 2
Q 40
M 18
Q 42
M 19
Q 44
M 8
Q 46
M 9
H 1
M 20
ae 42,72,0
M 21
SA 208,128
M 10
H 2
M 11
R 8
M 22
ae 44,68,384
M 23
R 10
PA 12288
DF
M 24
s_bitcmp1_b32 s50, 8
s_cselect_b32 s51, 24576, 0
v_add_u32 v47, s51, v3
M 25
XL 38,0
M 26
XL 40,768
M 27
XL 42,1536
M 28
XL 44,2304
M 29
M 30
M 31
M 32
M 33
WV 11
M 34
WL 0
FP
M 35
M 36
M 37
M 38
M 39
M 40
M 41
M 42
UL 6,30
UH 7,30
M 43
AR 8,40
AR 9,64
M 44
AD 8,6
AD 9,7
M 45
FR
AW 40,8
M 46
AW 64,9
UL 6,31
M 47
UH 7,31
AR 8,88
M 48
AR 9,112
AD 8,6
M 49
AD 9,7
AW 88,8
M 50
AW 112,9
UL 6,32
M 51
UH 7,32
AR 8,41
M 52
AR 9,65
AD 8,6
M 53
WL 0
FQ 104
AD 9,7
M 54
AW 41,8
AW 65,9
M 55
UL 6,33
UH 7,33
M 56
FZ 104
R 24
M 57
mw 72
R 26
M 58
TY 106,5128
TY 110,18952
M 59
R 28
M 60
ad 84,720
W 0
M 61
AR 8,89
AR 9,113
M 62
AD 8,6
AD 9,7
M 63
W 1
AW 89,8
M 64
AW 113,9
UL 6,34
M 65
UH 7,34
AR 8,42
M 66
W 2
AR 9,66
M 67
WL 3
AN 104,106
AN 105,107
M 68
SH 106
SH 107
M 69
mw 75
AN 106,106
AN 107,107
M 70
WL 2
AN 108,110
AN 109,111
M 71
R 30
M 72
SH 110
SH 111
M 73
AN 110,110
AN 111,111
M 74
TY 114,6664
TY 118,20488
M 75
ad 84,726
W 0
M 76
AD 8,6
AD 9,7
M 77
AW 42,8
AW 66,9
M 78
W 1
UL 6,35
M 79
UH 7,35
AR 8,90
M 80
AR 9,114
AD 8,6
M 81
W 2
AD 9,7
M 82
AW 90,8
AW 114,9
M 83
WL 1
R 32
M 84
mw 0
AN 112,114
AN 113,115
M 85
SH 114
SH 115
M 86
AN 114,114
AN 115,115
M 87
WL 2
AN 116,118
AN 117,119
M 88
SH 118
SH 119
M 89
AN 118,118
AN 119,119
M 90
TY 122,8200
TY 126,22024
M 91
ad 84,768
M 92
W 0
SA 210,896
M 93
UL 6,36
UH 7,36
M 94
AR 8,43
AR 9,67
M 95
R 34
M 96
W 1
AD 8,6
)"
R"(M 97
AD 9,7
AW 43,8
M 98
AW 67,9
UL 6,37
M 99
WL 3
AN 120,122
AN 121,123
M 100
SH 122
SH 123
M 101
AN 122,122
AN 123,123
M 102
WL 2
AN 124,126
AN 125,127
M 103
SH 126
SH 127
M 104
AN 126,126
AN 127,127
M 105
W 2
UH 7,37
M 106
AR 8,91
AR 9,115
M 107
R 48
M 108
mw 3
AD 8,6
AD 9,7
M 109
ad 84,774
W 0
M 110
AW 91,8
AW 115,9
M 111
M 112
W 1
M 113
M 114
M 115
W 2
M 116
M 117
M 118
bc 49,9,66,60
M 119
WL 2
R 50
M 120
mh 0
M 121
ad 66,1760
H 0
M 132
M 133
TX 58,4608
TX 62,18432
M 122
DG 1024
M 123
M 134
M 135
TY 66,5120
TY 70,18944
M 124
bc 48,9,64,56
M 125
mh 3
M 136
ad 64,1760
H 0
M 137
TX 74,6144
TX 78,19968
M 126
WL 4
Q 0
M 127
Q 2
M 138
Q 4
M 139
TY 82,6656
TY 86,20480
M 128
Q 6
M 129
WL 4
Q 8
M 140
Q 10
M 141
TX 90,7680
TX 94,21504
M 130
Q 12
M 131
R 52
M 142
Q 14
M 143
TY 98,8192
TY 102,22016
M 0
R 54
M 1
WL 10
Q 16
M 12
Q 18
M 13
Q 20
M 2
Q 22
M 3
WL 8
Q 24
M 14
Q 26
M 15
Q 28
M 4
Q 30
M 5
WL 6
Q 32
M 16
Q 34
M 17
Q 36
M 6
Q 38
M 7
WL 2
Q 40
M 18
Q 42
M 19
Q 44
M 8
Q 46
M 9
H 1
SA 212,1664
M 20
ae 44,68,384
M 21
s_bfe_u32 s51, s50, 0x10009
v_mad_i32_i24 v47, v49, s51, v48
XL 14,0
M 10
H 2
XL 16,768
M 11
R 56
M 22
XL 18,1536
M 23
R 58
M 24
XL 20,2304
M 25
M 26
M 27
M 28
M 29
M 30
M 31
M 32
M 33
WV 10
M 34
WL 0
FP
M 35
M 36
M 37
M 38
M 39
M 40
M 41
M 42
M 43
M 44
UL 6,38
UH 7,38
M 45
FR
AR 8,44
M 46
AR 9,68
AD 8,6
M 47
AD 9,7
AW 44,8
M 48
AW 68,9
UL 6,39
M 49
UH 7,39
AR 8,92
M 50
AR 9,116
AD 8,6
M 51
AD 9,7
AW 92,8
M 52
AW 116,9
UL 6,40
M 53
WL 0
FQ 108
UH 7,40
M 54
AR 8,45
AR 9,69
M 55
AD 8,6
AD 9,7
M 56
FZ 108
R 72
M 57
mw 24
R 74
M 58
TY 106,9736
TY 110,23560
M 59
R 76
M 60
ad 84,816
W 0
M 61
AW 45,8
AW 69,9
M 62
UL 6,41
UH 7,41
M 63
W 1
AR 8,93
M 64
AR 9,117
AD 8,6
M 65
AD 9,7
AW 93,8
M 66
W 2
AW 117,9
M 67
WL 3
AN 104,106
AN 105,107
M 68
SH 106
SH 107
M 69
mw 27
AN 106,106
AN 107,107
M 70
WL 2
AN 108,110
AN 109,111
M 71
R 78
M 72
SH 110
SH 111
M 73
AN 110,110
AN 111,111
M 74
TY 114,11272
TY 118,25096
M 75
ad 84,822
W 0
M 76
UL 6,42
UH 7,42
M 77
AR 8,46
AR 9,70
M 78
W 1
AD 8,6
M 79
AD 9,7
AW 46,8
M 80
AW 70,9
UL 6,43
M 81
W 2
SA 214,2432
M 82
UH 7,43
AR 8,94
M 83
WL 1
R 80
M 84
mw 48
AN 112,114
AN 113,115
M 85
SH 114
SH 115
M 86
AN 114,114
AN 115,115
M 87
WL 2
AN 116,118
AN 117,119
M 88
SH 118
SH 119
M 89
AN 118,118
AN 119,119
M 90
TY 122,12808
TY 126,26632
M 91
ad 84,864
M 92
W 0
AR 9,118
M 93
AD 8,6
AD 9,7
M 94
AW 94,8
AW 118,9
M 95
R 82
M 96
W 1
UL 6,44
M 97
UH 7,44
AR 8,47
M 98
AR 9,71
AD 8,6
M 99
WL 3
AN 120,122
AN 121,123
M 100
SH 122
SH 123
M 101
AN 122,122
AN 123,123
M 102
WL 2
AN 124,126
AN 125,127
M 103
SH 126
SH 127
M 104
AN 126,126
AN 127,127
M 105
W 2
AD 9,7
M 106
AW 47,8
AW 71,9
M 107
R 0
M 108
mw 51
UL 6,45
UH 7,45
M 109
ad 84,870
W 0
M 110
AR 8,95
AR 9,119
M 111
AD 8,6
)"
R"(AD 9,7
M 112
W 1
AW 95,8
M 113
AW 119,9
M 114
M 115
W 2
M 116
M 117
M 118
bc 49,10,66,60
M 119
WL 2
R 2
M 120
mh 9
M 121
ad 66,1952
H 0
M 132
M 133
TX 58,9216
TX 62,23040
M 122
DG 1024
M 123
M 134
M 135
TY 66,9728
TY 70,23552
M 124
bc 48,10,64,56
M 125
mh 12
M 136
ad 64,1952
H 0
M 137
TX 74,10752
TX 78,24576
M 126
WL 4
Q 0
M 127
Q 2
M 138
Q 4
M 139
TY 82,11264
TY 86,25088
M 128
Q 6
M 129
WL 4
Q 8
M 140
Q 10
M 141
TX 90,12288
TX 94,26112
M 130
Q 12
M 131
R 4
M 142
Q 14
M 143
TY 98,12800
TY 102,26624
M 0
R 6
M 1
WL 10
Q 16
M 12
Q 18
M 13
Q 20
M 2
Q 22
M 3
WL 8
Q 24
M 14
Q 26
M 15
Q 28
M 4
Q 30
M 5
WL 6
Q 32
M 16
Q 34
M 17
Q 36
M 6
Q 38
M 7
WL 2
Q 40
M 18
Q 42
M 19
Q 44
M 8
Q 46
M 9
H 1
M 20
ae 42,72,0
M 21
s_bitcmp1_b32 s50, 2
s_cselect_b32 s52, 24576, 0
v_add_u32 v50, s52, v3
M 10
H 2
SA 216,256
M 11
R 8
M 22
ae 44,68,480
M 23
R 10
PA 12288
DF
M 24
s_bitcmp1_b32 s50, 10
s_cselect_b32 s51, 24576, 0
v_add_u32 v47, s51, v3
M 25
XL 22,0
M 26
XL 24,768
M 27
XL 26,1536
M 28
XL 28,2304
M 29
M 30
M 31
M 32
M 33
WV 11
M 34
WL 0
FP
M 35
M 36
M 37
M 38
M 39
M 40
M 41
M 42
M 43
M 44
M 45
FR
M 46
UL 6,14
UH 7,14
M 47
AR 8,48
AR 9,72
M 48
AD 8,6
AD 9,7
M 49
AW 48,8
AW 72,9
M 50
UL 6,15
UH 7,15
M 51
AR 8,96
AR 9,120
M 52
AD 8,6
AD 9,7
M 53
WL 0
FQ 112
AW 96,8
M 54
AW 120,9
UL 6,16
M 55
UH 7,16
AR 8,49
M 56
FZ 112
R 24
M 57
mw 72
R 26
M 58
TY 106,520
TY 110,14344
M 59
R 28
M 60
ad 84,912
W 0
M 61
AR 9,73
AD 8,6
M 62
AD 9,7
AW 49,8
M 63
W 1
AW 73,9
M 64
UL 6,17
UH 7,17
M 65
AR 8,97
AR 9,121
M 66
W 2
AD 8,6
M 67
WL 3
AN 104,106
AN 105,107
M 68
SH 106
SH 107
M 69
mw 75
AN 106,106
AN 107,107
M 70
WL 2
AN 108,110
AN 109,111
M 71
R 30
M 72
SH 110
SH 111
M 73
AN 110,110
AN 111,111
M 74
TY 114,2056
TY 118,15880
M 75
ad 84,918
W 0
M 76
AD 9,7
AW 97,8
M 77
AW 121,9
UL 6,18
M 78
W 1
UH 7,18
M 79
AR 8,50
AR 9,74
M 80
AD 8,6
AD 9,7
M 81
W 2
AW 50,8
M 82
AW 74,9
UL 6,19
M 83
WL 1
R 32
M 84
mw 0
AN 112,114
AN 113,115
M 85
SH 114
SH 115
M 86
AN 114,114
AN 115,115
M 87
WL 2
AN 116,118
AN 117,119
M 88
SH 118
SH 119
M 89
AN 118,118
AN 119,119
M 90
TY 122,3592
TY 126,17416
M 91
ad 84,960
M 92
W 0
SA 218,1024
M 93
UH 7,19
AR 8,98
M 94
AR 9,122
AD 8,6
M 95
R 34
M 96
W 1
AD 9,7
M 97
AW 98,8
AW 122,9
M 98
UL 6,20
UH 7,20
M 99
WL 3
AN 120,122
AN 121,123
M 100
SH 122
SH 123
M 101
AN 122,122
AN 123,123
M 102
WL 2
AN 124,126
AN 125,127
M 103
SH 126
SH 127
M 104
AN 126,126
AN 127,127
M 105
W 2
AR 8,51
M 106
AR 9,75
AD 8,6
M 107
R 48
M 108
mw 3
AD 9,7
AW 51,8
M 109
ad 84,966
W 0
M 110
AW 75,9
UL 6,21
M 111
UH 7,21
AR 8,99
M 112
W 1
AR 9,123
M 113
AD 8,6
AD 9,7
M 114
AW 99,8
AW 123,9
M 115
W 2
M 116
M 117
M 118
bc 49,11,66,60
M 119
WL 2
R 50
M 120
mh 18
M 121
ad 66,2144
H 0
M 132
M 133
TX 58,0
TX 62,13824
M 122
DG 1024
M 123
M 134
M 135
TY 66,512
TY 70,14336
M 124
bc 48,11,64,56
M 125
)"
R"(mh 21
M 136
ad 64,2144
H 0
M 137
TX 74,1536
TX 78,15360
M 126
WL 4
Q 0
M 127
Q 2
M 138
Q 4
M 139
TY 82,2048
TY 86,15872
M 128
Q 6
M 129
WL 4
Q 8
M 140
Q 10
M 141
TX 90,3072
TX 94,16896
M 130
Q 12
M 131
R 52
M 142
Q 14
M 143
TY 98,3584
TY 102,17408
M 0
R 54
M 1
WL 10
Q 16
M 12
Q 18
M 13
Q 20
M 2
Q 22
M 3
WL 8
Q 24
M 14
Q 26
M 15
Q 28
M 4
Q 30
M 5
WL 6
Q 32
M 16
Q 34
M 17
Q 36
M 6
Q 38
M 7
WL 2
Q 40
M 18
Q 42
M 19
Q 44
M 8
Q 46
M 9
H 1
SA 220,1792
M 20
ae 44,68,480
M 21
s_bfe_u32 s51, s50, 0x1000b
v_mad_i32_i24 v47, v49, s51, v48
XL 30,0
M 10
H 2
XL 32,768
M 11
R 56
M 22
XL 34,1536
M 23
R 58
M 24
XL 36,2304
M 25
M 26
M 27
M 28
M 29
M 30
M 31
M 32
M 33
WV 10
M 34
WL 0
FP
M 35
M 36
M 37
M 38
M 39
M 40
M 41
M 42
M 43
M 44
M 45
FR
M 46
M 47
M 48
UL 6,22
UH 7,22
M 49
AR 8,52
AR 9,76
M 50
AD 8,6
AD 9,7
M 51
AW 52,8
AW 76,9
M 52
UL 6,23
UH 7,23
M 53
WL 0
FQ 116
AR 8,100
M 54
AR 9,124
AD 8,6
M 55
AD 9,7
AW 100,8
M 56
FZ 116
R 72
M 57
mw 24
R 74
M 58
TY 106,5128
TY 110,18952
M 59
R 76
M 60
ad 84,1008
W 0
M 61
AW 124,9
UL 6,24
M 62
UH 7,24
AR 8,53
M 63
W 1
AR 9,77
M 64
AD 8,6
AD 9,7
M 65
AW 53,8
AW 77,9
M 66
W 2
UL 6,25
M 67
WL 3
AN 104,106
AN 105,107
M 68
SH 106
SH 107
M 69
mw 27
AN 106,106
AN 107,107
M 70
WL 2
AN 108,110
AN 109,111
M 71
R 78
M 72
SH 110
SH 111
M 73
AN 110,110
AN 111,111
M 74
TY 114,6664
TY 118,20488
M 75
ad 84,1014
W 0
M 76
UH 7,25
AR 8,101
M 77
AR 9,125
AD 8,6
M 78
W 1
AD 9,7
M 79
AW 101,8
AW 125,9
M 80
UL 6,26
UH 7,26
M 81
W 2
SA 222,2560
M 82
AR 8,54
AR 9,78
M 83
WL 1
R 80
M 84
mw 48
AN 112,114
AN 113,115
M 85
SH 114
SH 115
M 86
AN 114,114
AN 115,115
M 87
WL 2
AN 116,118
AN 117,119
M 88
SH 118
SH 119
M 89
AN 118,118
AN 119,119
M 90
TY 122,8200
TY 126,22024
M 91
ad 84,1056
M 92
W 0
AD 8,6
M 93
AD 9,7
AW 54,8
M 94
AW 78,9
UL 6,27
M 95
R 82
M 96
W 1
UH 7,27
M 97
AR 8,102
AR 9,126
M 98
AD 8,6
AD 9,7
M 99
WL 3
AN 120,122
AN 121,123
M 100
SH 122
SH 123
M 101
AN 122,122
AN 123,123
M 102
WL 2
AN 124,126
AN 125,127
M 103
SH 126
SH 127
M 104
AN 126,126
AN 127,127
M 105
W 2
AW 102,8
M 106
AW 126,9
UL 6,28
M 107
R 0
M 108
mw 51
UH 7,28
AR 8,55
M 109
ad 84,1062
W 0
M 110
AR 9,79
AD 8,6
M 111
AD 9,7
AW 55,8
M 112
W 1
AW 79,9
M 113
UL 6,29
UH 7,29
M 114
AR 8,103
AR 9,127
M 115
W 2
AD 8,6
M 116
AD 9,7
AW 103,8
M 117
AW 127,9
M 118
bc 49,12,66,60
M 119
WL 1
R 2
M 120
mh 0
M 121
ad 66,2336
H 0
M 132
M 133
TX 58,4608
TX 62,18432
M 122
DG 1024
M 123
M 134
M 135
TY 66,5120
TY 70,18944
M 124
bc 48,12,64,56
M 125
mh 3
M 136
ad 64,2336
H 0
M 137
TX 74,6144
TX 78,19968
M 126
WL 4
Q 0
M 127
Q 2
M 138
Q 4
M 139
TY 82,6656
TY 86,20480
M 128
Q 6
M 129
WL 4
Q 8
M 140
Q 10
M 141
TX 90,7680
TX 94,21504
M 130
Q 12
M 131
R 4
M 142
Q 14
M 143
TY 98,8192
TY 102,22016
M 0
R 6
M 1
WL 10
Q 16
M 12
)"
R"(Q 18
M 13
Q 20
M 2
Q 22
M 3
WL 8
Q 24
M 14
Q 26
M 15
Q 28
M 4
Q 30
M 5
WL 6
Q 32
M 16
Q 34
M 17
Q 36
M 6
Q 38
M 7
WL 2
Q 40
M 18
Q 42
M 19
Q 44
M 8
Q 46
M 9
H 1
M 20
ae 42,72,96
M 21
s_bitcmp1_b32 s50, 4
s_cselect_b32 s52, 24576, 0
v_add_u32 v50, s52, v3
M 10
H 2
SA 224,0
M 11
R 8
M 22
M 23
R 10
PA 12288
DF
M 24
M 25
M 26
M 27
M 28
M 29
M 30
M 31
M 32
ae 44,70,384
M 33
WV 7
M 34
WL 0
FP
M 35
M 36
s_bitcmp1_b32 s50, 6
s_cselect_b32 s51, 24576, 0
v_add_u32 v47, s51, v3
M 37
XL 38,128
M 38
XL 40,896
M 39
M 40
M 41
M 42
M 43
M 44
XL 42,1664
M 45
FR
M 46
M 47
M 48
M 49
M 50
XL 44,2432
UL 6,30
M 51
UH 7,30
AR 8,56
M 52
AR 9,80
AD 8,6
M 53
WL 0
FQ 120
AD 9,7
M 54
AW 56,8
AW 80,9
M 55
UL 6,31
UH 7,31
M 56
FZ 120
R 24
M 57
mw 72
R 26
M 58
TY 106,9736
TY 110,23560
M 59
R 28
M 60
ad 84,1104
W 0
M 61
AR 8,104
AR 9,128
M 62
AD 8,6
AD 9,7
M 63
W 1
AW 104,8
M 64
AW 128,9
UL 6,32
M 65
UH 7,32
AR 8,57
M 66
W 2
AR 9,81
M 67
WL 3
AN 104,106
AN 105,107
M 68
SH 106
SH 107
M 69
mw 75
AN 106,106
AN 107,107
M 70
WL 2
AN 108,110
AN 109,111
M 71
R 30
M 72
SH 110
SH 111
M 73
AN 110,110
AN 111,111
M 74
TY 114,11272
TY 118,25096
M 75
ad 84,1110
W 0
M 76
AD 8,6
AD 9,7
M 77
AW 57,8
AW 81,9
M 78
W 1
UL 6,33
M 79
UH 7,33
AR 8,105
M 80
AR 9,129
AD 8,6
M 81
W 2
AD 9,7
M 82
AW 105,8
AW 129,9
M 83
WL 1
R 32
M 84
mw 0
AN 112,114
AN 113,115
M 85
SH 114
SH 115
M 86
AN 114,114
AN 115,115
M 87
WL 2
AN 116,118
AN 117,119
M 88
SH 118
SH 119
M 89
AN 118,118
AN 119,119
M 90
TY 122,12808
TY 126,26632
M 91
ad 84,1152
M 92
W 0
SA 226,768
M 93
UL 6,34
UH 7,34
M 94
AR 8,58
AR 9,82
M 95
R 34
M 96
W 1
AD 8,6
M 97
AD 9,7
AW 58,8
M 98
AW 82,9
UL 6,35
M 99
WL 3
AN 120,122
AN 121,123
M 100
SH 122
SH 123
M 101
AN 122,122
AN 123,123
M 102
WL 2
AN 124,126
AN 125,127
M 103
SH 126
SH 127
M 104
AN 126,126
AN 127,127
M 105
W 2
UH 7,35
M 106
AR 8,106
AR 9,130
M 107
R 48
M 108
mw 3
AD 8,6
AD 9,7
M 109
ad 84,1158
W 0
M 110
AW 106,8
AW 130,9
M 111
UL 6,36
UH 7,36
M 112
W 1
AR 8,59
M 113
AR 9,83
AD 8,6
M 114
AD 9,7
AW 59,8
M 115
W 2
AW 83,9
M 116
UL 6,37
UH 7,37
M 117
AR 8,107
AR 9,131
M 118
bc 49,13,66,60
AD 8,6
M 119
WL 0
R 50
M 120
mh 9
AD 9,7
AW 107,8
M 121
ad 66,2528
H 0
M 132
AW 131,9
M 133
TX 58,9216
TX 62,23040
M 122
DG 1024
M 123
M 134
M 135
TY 66,9728
TY 70,23552
M 124
bc 48,13,64,56
M 125
mh 12
M 136
ad 64,2528
H 0
M 137
TX 74,10752
TX 78,24576
M 126
WL 4
Q 0
M 127
Q 2
M 138
Q 4
M 139
TY 82,11264
TY 86,25088
M 128
Q 6
M 129
WL 4
Q 8
M 140
Q 10
M 141
TX 90,12288
TX 94,26112
M 130
Q 12
M 131
R 52
M 142
Q 14
M 143
TY 98,12800
TY 102,26624
M 0
R 54
M 1
WL 10
Q 16
M 12
Q 18
M 13
Q 20
M 2
Q 22
M 3
WL 8
Q 24
M 14
Q 26
M 15
Q 28
M 4
Q 30
M 5
WL 6
Q 32
M 16
Q 34
M 17
Q 36
M 6
Q 38
M 7
WL 2
Q 40
M 18
Q 42
M 19
Q 44
M 8
Q 46
M 9
H 1
SA 228,1536
)"
R"(M 20
M 21
M 10
H 2
M 11
R 56
M 22
M 23
R 58
M 24
M 25
M 26
M 27
M 28
M 29
M 30
M 31
M 32
M 33
WV 6
M 34
WL 0
FP
M 35
M 36
ae 44,70,384
M 37
s_bfe_u32 s51, s50, 0x10007
v_mad_i32_i24 v47, v49, s51, v48
XL 14,128
M 38
M 39
M 40
XL 16,896
M 41
M 42
M 43
M 44
M 45
FR
M 46
XL 18,1664
M 47
M 48
M 49
M 50
M 51
M 52
XL 20,2432
M 53
WL 0
FQ 124
M 54
M 55
M 56
FZ 124
R 72
M 57
mw 24
R 74
M 58
TY 106,520
TY 110,14344
M 59
R 76
M 60
ad 84,1200
W 0
M 61
M 62
M 63
W 1
M 64
M 65
M 66
W 2
M 67
WL 3
AN 104,106
AN 105,107
M 68
SH 106
SH 107
M 69
mw 27
AN 106,106
AN 107,107
M 70
WL 2
AN 108,110
AN 109,111
M 71
R 78
M 72
SH 110
SH 111
M 73
AN 110,110
AN 111,111
M 74
TY 114,2056
TY 118,15880
M 75
ad 84,1206
W 0
M 76
M 77
M 78
W 1
M 79
M 80
M 81
W 2
SA 230,2304
M 82
M 83
R 80
M 84
mw 48
WL 3
AN 112,114
AN 113,115
M 85
SH 114
SH 115
M 86
AN 114,114
AN 115,115
M 87
WL 2
AN 116,118
AN 117,119
M 88
SH 118
SH 119
M 89
AN 118,118
AN 119,119
M 90
TY 122,3592
TY 126,17416
M 91
ad 84,1248
M 92
W 0
UL 6,38
M 93
UH 7,38
AR 8,132
M 94
AR 9,156
AD 8,6
M 95
R 82
M 96
W 1
AD 9,7
M 97
AW 132,8
AW 156,9
M 98
UL 6,39
UH 7,39
M 99
WL 3
AN 120,122
AN 121,123
M 100
SH 122
SH 123
M 101
AN 122,122
AN 123,123
M 102
WL 2
AN 124,126
AN 125,127
M 103
SH 126
SH 127
M 104
AN 126,126
AN 127,127
M 105
W 2
AR 8,180
M 106
AD 8,6
AD 136,7
M 107
R 0
M 108
mw 51
AW 180,8
UL 6,40
M 109
ad 84,1254
W 0
M 110
UH 7,40
AR 8,133
M 111
AR 9,157
AD 8,6
M 112
W 1
AD 9,7
M 113
AW 133,8
AW 157,9
M 114
UL 6,41
UH 7,41
M 115
W 2
AR 8,181
M 116
AD 8,6
AD 137,7
M 117
AW 181,8
UL 6,42
M 118
bc 49,14,66,60
UH 7,42
M 119
WL 0
R 2
M 120
mh 18
AR 8,134
AR 9,158
M 121
ad 66,2720
H 0
M 132
AD 8,6
AD 9,7
M 133
TX 58,0
TX 62,13824
M 122
DG 1024
M 123
AW 134,8
AW 158,9
M 134
UL 6,43
UH 7,43
M 135
TY 66,512
TY 70,14336
M 124
bc 48,14,64,56
AR 8,182
M 125
mh 21
AD 8,6
AD 138,7
M 136
ad 64,2720
H 0
M 137
TX 74,1536
TX 78,15360
M 126
WL 4
Q 0
M 127
Q 2
M 138
Q 4
M 139
TY 82,2048
TY 86,15872
M 128
Q 6
M 129
WL 4
Q 8
M 140
Q 10
M 141
TX 90,3072
TX 94,16896
M 130
Q 12
M 131
R 4
M 142
Q 14
M 143
TY 98,3584
TY 102,17408
M 0
R 6
M 1
WL 10
Q 16
M 12
Q 18
M 13
Q 20
M 2
Q 22
M 3
WL 8
Q 24
M 14
Q 26
M 15
Q 28
M 4
Q 30
M 5
WL 6
Q 32
M 16
Q 34
M 17
Q 36
M 6
Q 38
M 7
WL 2
Q 40
M 18
Q 42
M 19
Q 44
M 8
Q 46
M 9
H 1
AW 182,8
M 20
ae 42,72,96
M 21
SA 232,128
UL 6,44
M 10
H 2
UH 7,44
M 11
R 8
M 22
AR 8,135
AR 9,159
M 23
R 10
PA 12288
DF
M 24
AD 8,6
AD 9,7
M 25
AW 135,8
AW 159,9
M 26
UL 6,45
UH 7,45
M 27
AR 8,183
AD 8,6
M 28
AD 139,7
AW 183,8
M 29
M 30
M 31
M 32
M 33
WV 7
M 34
WL 0
FP
M 35
M 36
ae 44,68,384
M 37
s_bitcmp1_b32 s50, 8
s_cselect_b32 s51, 24576, 0
v_add_u32 v47, s51, v3
M 38
XL 22,128
M 39
M 40
M 41
M 42
XL 24,896
M 43
M 44
M 45
FR
M 46
M 47
M 48
XL 26,1664
M 49
M 50
)"
R"(M 51
M 52
M 53
WL 0
FQ 128
M 54
XL 28,2432
M 55
M 56
FZ 128
R 24
M 57
mw 72
R 26
M 58
TY 106,5128
TY 110,18952
M 59
R 28
M 60
ad 84,1296
W 0
M 61
M 62
M 63
W 1
M 64
M 65
M 66
W 2
M 67
WL 3
AN 104,106
AN 105,107
M 68
SH 106
SH 107
M 69
mw 75
AN 106,106
AN 107,107
M 70
WL 2
AN 108,110
AN 109,111
M 71
R 30
M 72
SH 110
SH 111
M 73
AN 110,110
AN 111,111
M 74
TY 114,6664
TY 118,20488
M 75
ad 84,1302
W 0
M 76
M 77
M 78
W 1
M 79
M 80
M 81
W 2
M 82
M 83
R 32
M 84
mw 0
WL 3
AN 112,114
AN 113,115
M 85
SH 114
SH 115
M 86
AN 114,114
AN 115,115
M 87
WL 2
AN 116,118
AN 117,119
M 88
SH 118
SH 119
M 89
AN 118,118
AN 119,119
M 90
TY 122,8200
TY 126,22024
M 91
ad 84,1344
M 92
W 0
SA 234,896
M 93
UL 6,14
UH 7,14
M 94
AR 8,136
AR 9,160
M 95
R 34
M 96
W 1
AD 8,6
M 97
AD 9,7
AW 136,8
M 98
AW 160,9
UL 6,15
M 99
WL 3
AN 120,122
AN 121,123
M 100
SH 122
SH 123
M 101
AN 122,122
AN 123,123
M 102
WL 2
AN 124,126
AN 125,127
M 103
SH 126
SH 127
M 104
AN 126,126
AN 127,127
M 105
W 2
UH 7,15
M 106
AR 8,184
AD 8,6
M 107
R 48
M 108
mw 3
AD 140,7
AW 184,8
M 109
ad 84,1350
W 0
M 110
UL 6,16
UH 7,16
M 111
AR 8,137
AR 9,161
M 112
W 1
AD 8,6
M 113
AD 9,7
AW 137,8
M 114
AW 161,9
UL 6,17
M 115
W 2
UH 7,17
M 116
AR 8,185
AD 8,6
M 117
AD 141,7
AW 185,8
M 118
bc 49,15,66,60
UL 6,18
M 119
WL 0
R 50
M 120
mh 0
UH 7,18
AR 8,138
M 121
ad 66,2912
H 0
M 132
AR 9,162
AD 8,6
M 133
TX 58,4608
TX 62,18432
M 122
DG 1024
M 123
AD 9,7
AW 138,8
M 134
AW 162,9
UL 6,19
M 135
TY 66,5120
TY 70,18944
M 124
bc 48,15,64,56
UH 7,19
M 125
mh 3
AR 8,186
AD 8,6
M 136
ad 64,2912
H 0
M 137
TX 74,6144
TX 78,19968
M 126
WL 4
Q 0
M 127
Q 2
M 138
Q 4
M 139
TY 82,6656
TY 86,20480
M 128
Q 6
M 129
WL 4
Q 8
M 140
Q 10
M 141
TX 90,7680
TX 94,21504
M 130
Q 12
M 131
R 52
M 142
Q 14
M 143
TY 98,8192
TY 102,22016
M 0
R 54
M 1
WL 10
Q 16
M 12
Q 18
M 13
Q 20
M 2
Q 22
M 3
WL 8
Q 24
M 14
Q 26
M 15
Q 28
M 4
Q 30
M 5
WL 6
Q 32
M 16
Q 34
M 17
Q 36
M 6
Q 38
M 7
WL 2
Q 40
M 18
Q 42
M 19
Q 44
M 8
Q 46
M 9
H 1
SA 236,1664
M 20
AD 142,7
AW 186,8
M 21
UL 6,20
UH 7,20
M 10
H 2
AR 8,139
M 11
R 56
M 22
AR 9,163
AD 8,6
M 23
R 58
M 24
AD 9,7
AW 139,8
M 25
AW 163,9
UL 6,21
M 26
UH 7,21
AR 8,187
M 27
AD 8,6
AD 143,7
M 28
AW 187,8
M 29
M 30
M 31
M 32
M 33
WV 6
M 34
WL 0
FP
M 35
M 36
M 37
M 38
ae 44,68,384
M 39
s_bfe_u32 s51, s50, 0x10009
v_mad_i32_i24 v47, v49, s51, v48
XL 30,128
M 40
M 41
M 42
M 43
M 44
XL 32,896
M 45
FR
M 46
M 47
M 48
M 49
M 50
XL 34,1664
M 51
M 52
M 53
WL 0
FQ 132
M 54
M 55
M 56
FZ 132
R 72
M 57
mw 24
R 74
M 58
TY 106,9736
TY 110,23560
M 59
R 76
M 60
ad 84,1392
W 0
M 61
XL 36,2432
M 62
M 63
W 1
M 64
M 65
M 66
W 2
M 67
WL 3
AN 104,106
AN 105,107
M 68
SH 106
SH 107
M 69
mw 27
AN 106,106
AN 107,107
M 70
WL 2
AN 108,110
AN 109,111
M 71
R 78
M 72
SH 110
SH 111
)"
R"(M 73
AN 110,110
AN 111,111
M 74
TY 114,11272
TY 118,25096
M 75
ad 84,1398
W 0
M 76
M 77
M 78
W 1
M 79
M 80
M 81
W 2
SA 238,2432
M 82
M 83
R 80
M 84
mw 48
WL 3
AN 112,114
AN 113,115
M 85
SH 114
SH 115
M 86
AN 114,114
AN 115,115
M 87
WL 2
AN 116,118
AN 117,119
M 88
SH 118
SH 119
M 89
AN 118,118
AN 119,119
M 90
TY 122,12808
TY 126,26632
M 91
ad 84,1440
M 92
W 0
UL 6,22
M 93
UH 7,22
AR 8,140
M 94
AR 9,164
AD 8,6
M 95
R 82
M 96
W 1
AD 9,7
M 97
AW 140,8
AW 164,9
M 98
UL 6,23
UH 7,23
M 99
WL 3
AN 120,122
AN 121,123
M 100
SH 122
SH 123
M 101
AN 122,122
AN 123,123
M 102
WL 2
AN 124,126
AN 125,127
M 103
SH 126
SH 127
M 104
AN 126,126
AN 127,127
M 105
W 2
AR 8,188
M 106
AD 8,6
AD 144,7
M 107
R 0
M 108
mw 51
AW 188,8
UL 6,24
M 109
ad 84,1446
W 0
M 110
UH 7,24
AR 8,141
M 111
AR 9,165
AD 8,6
M 112
W 1
AD 9,7
M 113
AW 141,8
AW 165,9
M 114
UL 6,25
UH 7,25
M 115
W 2
AR 8,189
M 116
AD 8,6
AD 145,7
M 117
AW 189,8
UL 6,26
M 118
bc 49,16,66,60
UH 7,26
M 119
WL 0
R 2
M 120
mh 9
AR 8,142
AR 9,166
M 121
ad 66,3104
H 0
M 132
AD 8,6
AD 9,7
M 133
TX 58,9216
TX 62,23040
M 122
DG 1024
M 123
AW 142,8
AW 166,9
M 134
UL 6,27
UH 7,27
M 135
TY 66,9728
TY 70,23552
M 124
bc 48,16,64,56
AR 8,190
M 125
mh 12
AD 8,6
AD 146,7
M 136
ad 64,3104
H 0
M 137
TX 74,10752
TX 78,24576
M 126
WL 4
Q 0
M 127
Q 2
M 138
Q 4
M 139
TY 82,11264
TY 86,25088
M 128
Q 6
M 129
WL 4
Q 8
M 140
Q 10
M 141
TX 90,12288
TX 94,26112
M 130
Q 12
M 131
R 4
M 142
Q 14
M 143
TY 98,12800
TY 102,26624
M 0
R 6
M 1
WL 10
Q 16
M 12
Q 18
M 13
Q 20
M 2
Q 22
M 3
WL 8
Q 24
M 14
Q 26
M 15
Q 28
M 4
Q 30
M 5
WL 6
Q 32
M 16
Q 34
M 17
Q 36
M 6
Q 38
M 7
WL 2
Q 40
M 18
Q 42
M 19
Q 44
M 8
Q 46
M 9
H 1
AW 190,8
M 20
ae 42,72,96
M 21
SA 240,256
UL 6,28
M 10
H 2
UH 7,28
M 11
R 8
M 22
AR 8,143
AR 9,167
M 23
R 10
PA 12288
DF
M 24
AD 8,6
AD 9,7
M 25
AW 143,8
AW 167,9
M 26
UL 6,29
UH 7,29
M 27
AR 8,191
AD 8,6
M 28
AD 147,7
AW 191,8
M 29
M 30
M 31
M 32
M 33
WV 7
M 34
WL 0
FP
M 35
M 36
M 37
M 38
M 39
M 40
ae 44,68,480
M 41
s_bitcmp1_b32 s50, 10
s_cselect_b32 s51, 24576, 0
v_add_u32 v47, s51, v3
M 42
XL 38,128
M 43
M 44
M 45
FR
M 46
XL 40,896
M 47
M 48
M 49
M 50
M 51
M 52
XL 42,1664
M 53
WL 0
FQ 136
M 54
M 55
M 56
FZ 136
R 24
M 57
mw 72
R 26
M 58
TY 106,520
TY 110,14344
M 59
R 28
M 60
ad 84,1488
W 0
M 61
XL 44,2432
M 62
M 63
W 1
M 64
M 65
M 66
W 2
M 67
WL 3
AN 104,106
AN 105,107
M 68
SH 106
SH 107
M 69
mw 75
AN 106,106
AN 107,107
M 70
WL 2
AN 108,110
AN 109,111
M 71
R 30
M 72
SH 110
SH 111
M 73
AN 110,110
AN 111,111
M 74
TY 114,2056
TY 118,15880
M 75
ad 84,1494
W 0
M 76
M 77
M 78
W 1
M 79
M 80
M 81
W 2
M 82
M 83
R 32
M 84
mw 0
WL 3
AN 112,114
AN 113,115
M 85
SH 114
SH 115
M 86
AN 114,114
AN 115,115
M 87
WL 2
AN 116,118
AN 117,119
M 88
SH 118
SH 119
M 89
AN 118,118
AN 119,119
M 90
TY 122,3592
TY 126,17416
M 91
ad 84,1536
)"
R"(M 92
W 0
SA 242,1024
M 93
M 94
UL 6,30
UH 7,30
M 95
R 34
M 96
W 1
AR 8,144
M 97
AR 9,168
AD 8,6
M 98
AD 9,7
AW 144,8
M 99
WL 3
AN 120,122
AN 121,123
M 100
SH 122
SH 123
M 101
AN 122,122
AN 123,123
M 102
WL 2
AN 124,126
AN 125,127
M 103
SH 126
SH 127
M 104
AN 126,126
AN 127,127
M 105
W 2
AW 168,9
M 106
UL 6,31
UH 7,31
M 107
R 48
M 108
mw 3
AR 8,192
AD 8,6
M 109
ad 84,1542
W 0
M 110
AD 148,7
AW 192,8
M 111
UL 6,32
UH 7,32
M 112
W 1
AR 8,145
M 113
AR 9,169
AD 8,6
M 114
AD 9,7
AW 145,8
M 115
W 2
AW 169,9
M 116
UL 6,33
UH 7,33
M 117
AR 8,193
AD 8,6
M 118
bc 49,17,66,60
AD 149,7
M 119
WL 0
R 50
M 120
mh 18
AW 193,8
UL 6,34
M 121
ad 66,3296
H 0
M 132
UH 7,34
AR 8,146
M 133
TX 58,0
TX 62,13824
M 122
DG 1024
M 123
AR 9,170
AD 8,6
M 134
AD 9,7
AW 146,8
M 135
TY 66,512
TY 70,14336
M 124
bc 48,17,64,56
AW 170,9
M 125
mh 21
UL 6,35
UH 7,35
M 136
ad 64,3296
H 0
M 137
TX 74,1536
TX 78,15360
M 126
WL 4
Q 0
M 127
Q 2
M 138
Q 4
M 139
TY 82,2048
TY 86,15872
M 128
Q 6
M 129
WL 4
Q 8
M 140
Q 10
M 141
TX 90,3072
TX 94,16896
M 130
Q 12
M 131
R 52
M 142
Q 14
M 143
TY 98,3584
TY 102,17408
M 0
R 54
M 1
WL 10
Q 16
M 12
Q 18
M 13
Q 20
M 2
Q 22
M 3
WL 8
Q 24
M 14
Q 26
M 15
Q 28
M 4
Q 30
M 5
WL 6
Q 32
M 16
Q 34
M 17
Q 36
M 6
Q 38
M 7
WL 2
Q 40
M 18
Q 42
M 19
Q 44
M 8
Q 46
M 9
H 1
SA 244,1792
M 20
AR 8,194
AD 8,6
M 21
AD 150,7
AW 194,8
M 10
H 2
UL 6,36
M 11
R 56
M 22
UH 7,36
AR 8,147
M 23
R 58
M 24
AR 9,171
AD 8,6
M 25
AD 9,7
AW 147,8
M 26
AW 171,9
UL 6,37
M 27
UH 7,37
AR 8,195
M 28
AD 8,6
AD 151,7
M 29
AW 195,8
M 30
M 31
M 32
M 33
WV 6
M 34
WL 0
FP
M 35
M 36
M 37
M 38
M 39
M 40
M 41
M 42
ae 44,68,480
M 43
s_bfe_u32 s51, s50, 0x1000b
v_mad_i32_i24 v47, v49, s51, v48
XL 14,128
M 44
M 45
FR
M 46
M 47
M 48
XL 16,896
M 49
M 50
M 51
M 52
M 53
WL 0
FQ 140
M 54
XL 18,1664
M 55
M 56
FZ 140
R 72
M 57
mw 24
R 74
M 58
TY 106,5128
TY 110,18952
M 59
R 76
M 60
ad 84,1584
W 0
M 61
XL 20,2432
M 62
M 63
W 1
M 64
M 65
M 66
W 2
M 67
WL 3
AN 104,106
AN 105,107
M 68
SH 106
SH 107
M 69
mw 27
AN 106,106
AN 107,107
M 70
WL 2
AN 108,110
AN 109,111
M 71
R 78
M 72
SH 110
SH 111
M 73
AN 110,110
AN 111,111
M 74
TY 114,6664
TY 118,20488
M 75
ad 84,1590
W 0
M 76
M 77
M 78
W 1
M 79
M 80
M 81
W 2
SA 246,2560
M 82
M 83
R 80
M 84
mw 48
WL 3
AN 112,114
AN 113,115
M 85
SH 114
SH 115
M 86
AN 114,114
AN 115,115
M 87
WL 2
AN 116,118
AN 117,119
M 88
SH 118
SH 119
M 89
AN 118,118
AN 119,119
M 90
TY 122,8200
TY 126,22024
M 91
ad 84,1632
M 92
W 0
M 93
M 94
M 95
R 82
M 96
W 1
UL 6,38
M 97
UH 7,38
AR 8,148
M 98
AR 9,172
AD 8,6
M 99
WL 3
AN 120,122
AN 121,123
M 100
SH 122
SH 123
M 101
AN 122,122
AN 123,123
M 102
WL 2
AN 124,126
AN 125,127
M 103
SH 126
SH 127
M 104
AN 126,126
AN 127,127
M 105
W 2
AD 9,7
M 106
AW 148,8
AW 172,9
M 107
R 0
M 108
mw 51
UL 6,39
UH 7,39
M 109
ad 84,1638
)"
R"(W 0
M 110
AR 8,196
AD 8,6
M 111
AD 152,7
AW 196,8
M 112
W 1
UL 6,40
M 113
UH 7,40
AR 8,149
M 114
AR 9,173
AD 8,6
M 115
W 2
AD 9,7
M 116
AW 149,8
AW 173,9
M 117
UL 6,41
UH 7,41
M 118
bc 49,18,66,60
AR 8,197
M 119
WL 0
R 2
M 120
mh 0
AD 8,6
AD 153,7
M 121
ad 66,3488
H 0
M 132
AW 197,8
UL 6,42
M 133
TX 58,4608
TX 62,18432
M 122
DG 1024
M 123
UH 7,42
AR 8,150
M 134
AR 9,174
AD 8,6
M 135
TY 66,5120
TY 70,18944
M 124
bc 48,18,64,56
AD 9,7
M 125
mh 3
AW 150,8
AW 174,9
M 136
ad 64,3488
H 0
M 137
TX 74,6144
TX 78,19968
M 126
WL 4
Q 0
M 127
Q 2
M 138
Q 4
M 139
TY 82,6656
TY 86,20480
M 128
Q 6
M 129
WL 4
Q 8
M 140
Q 10
M 141
TX 90,7680
TX 94,21504
M 130
Q 12
M 131
R 4
M 142
Q 14
M 143
TY 98,8192
TY 102,22016
M 0
R 6
M 1
WL 10
Q 16
M 12
Q 18
M 13
Q 20
M 2
Q 22
M 3
WL 8
Q 24
M 14
Q 26
M 15
Q 28
M 4
Q 30
M 5
WL 6
Q 32
M 16
Q 34
M 17
Q 36
M 6
Q 38
M 7
WL 2
Q 40
M 18
Q 42
M 19
Q 44
M 8
Q 46
M 9
H 1
UL 6,43
M 20
ae 42,72,96
M 21
s_bfe_u32 s52, s50, 0x10005
v_mad_i32_i24 v50, v49, s52, v48
SA 248,256
M 10
H 2
UH 7,43
M 11
R 8
M 22
AR 8,198
AD 8,6
M 23
R 10
PA 12288
DF
M 24
AD 154,7
AW 198,8
M 25
UL 6,44
UH 7,44
M 26
AR 8,151
AR 9,175
M 27
AD 8,6
AD 9,7
M 28
AW 151,8
AW 175,9
M 29
UL 6,45
UH 7,45
M 30
AR 8,199
AD 8,6
M 31
AD 155,7
AW 199,8
M 32
M 33
WV 7
M 34
WL 0
FP
M 35
M 36
M 37
M 38
M 39
M 40
M 41
M 42
M 43
M 44
M 45
FR
M 46
M 47
M 48
M 49
M 50
M 51
M 52
M 53
WL 0
FQ 144
M 54
M 55
M 56
FZ 144
R 24
M 57
mw 72
R 26
M 58
TY 106,9736
TY 110,23560
M 59
R 28
M 60
ad 84,1680
W 0
M 61
M 62
M 63
W 1
M 64
M 65
M 66
W 2
M 67
WL 3
AN 104,106
AN 105,107
M 68
SH 106
SH 107
M 69
mw 75
AN 106,106
AN 107,107
M 70
WL 2
AN 108,110
AN 109,111
M 71
R 30
M 72
SH 110
SH 111
M 73
AN 110,110
AN 111,111
M 74
TY 114,11272
TY 118,25096
M 75
ad 84,1686
W 0
M 76
ae 44,70,384
M 77
s_bitcmp1_b32 s50, 6
s_cselect_b32 s51, 24576, 0
v_add_u32 v47, s51, v3
M 78
W 1
XL 22,256
M 79
XL 24,1024
M 80
M 81
W 2
M 82
XL 26,1792
M 83
WL 1
R 32
M 84
mw 0
AN 112,114
AN 113,115
M 85
SH 114
SH 115
M 86
AN 114,114
AN 115,115
M 87
WL 2
AN 116,118
AN 117,119
M 88
SH 118
SH 119
M 89
AN 118,118
AN 119,119
M 90
TY 122,12808
TY 126,26632
M 91
ad 84,1728
M 92
W 0
SA 250,1024
M 93
XL 28,2560
M 94
M 95
R 34
M 96
W 1
M 97
M 98
UL 6,14
UH 7,14
M 99
WL 3
AN 120,122
AN 121,123
M 100
SH 122
SH 123
M 101
AN 122,122
AN 123,123
M 102
WL 2
AN 124,126
AN 125,127
M 103
SH 126
SH 127
M 104
AN 126,126
AN 127,127
M 105
W 2
AR 8,152
M 106
AR 9,176
AD 8,6
M 107
R 48
M 108
mw 3
AD 9,7
AW 152,8
M 109
ad 84,1734
W 0
M 110
AW 176,9
UL 6,15
M 111
UH 7,15
AD 132,6
M 112
W 1
AD 156,7
M 113
UL 6,16
UH 7,16
M 114
AR 8,153
AR 9,177
M 115
W 2
AD 8,6
M 116
AD 9,7
AW 153,8
M 117
AW 177,9
UL 6,17
M 118
bc 49,19,66,60
UH 7,17
M 119
WL 0
R 50
M 120
mh 9
AD 133,6
AD 157,7
M 121
ad 66,3680
H 0
M 132
UL 6,18
UH 7,18
M 133
TX 58,9216
TX 62,23040
M 122
DG 1024
M 123
)"
R"(AR 8,154
AR 9,178
M 134
AD 8,6
AD 9,7
M 135
TY 66,9728
TY 70,23552
M 124
bc 48,19,64,56
AW 154,8
M 125
mh 12
AW 178,9
UL 6,19
M 136
ad 64,3680
H 0
M 137
TX 74,10752
TX 78,24576
M 126
WL 4
Q 0
M 127
Q 2
M 138
Q 4
M 139
TY 82,11264
TY 86,25088
M 128
Q 6
M 129
WL 4
Q 8
M 140
Q 10
M 141
TX 90,12288
TX 94,26112
M 130
Q 12
M 131
R 52
M 142
Q 14
M 143
TY 98,12800
TY 102,26624
M 0
R 54
M 1
WL 10
Q 16
M 12
Q 18
M 13
Q 20
M 2
Q 22
M 3
WL 8
Q 24
M 14
Q 26
M 15
Q 28
M 4
Q 30
M 5
WL 6
Q 32
M 16
Q 34
M 17
Q 36
M 6
Q 38
M 7
WL 2
Q 40
M 18
Q 42
M 19
Q 44
M 8
Q 46
M 9
H 1
SA 252,1792
M 20
UH 7,19
AD 134,6
M 21
AD 158,7
UL 6,20
M 10
H 2
UH 7,20
M 11
R 56
M 22
AR 8,155
AR 9,179
M 23
R 58
M 24
AD 8,6
AD 9,7
M 25
AW 155,8
AW 179,9
M 26
UL 6,21
UH 7,21
M 27
AD 135,6
AD 159,7
M 28
M 29
M 30
M 31
M 32
M 33
WV 6
M 34
WL 0
FP
M 35
M 36
M 37
M 38
M 39
M 40
M 41
M 42
M 43
M 44
M 45
FR
M 46
M 47
M 48
M 49
M 50
M 51
M 52
M 53
WL 0
FQ 148
M 54
M 55
M 56
FZ 148
R 72
M 57
mw 24
R 74
M 58
TY 106,520
TY 110,14344
M 59
R 76
M 60
ad 84,1776
W 0
M 61
M 62
M 63
W 1
M 64
M 65
M 66
W 2
M 67
WL 3
AN 104,106
AN 105,107
M 68
SH 106
SH 107
M 69
mw 27
AN 106,106
AN 107,107
M 70
WL 2
AN 108,110
AN 109,111
M 71
R 78
M 72
SH 110
SH 111
M 73
AN 110,110
AN 111,111
M 74
TY 114,2056
TY 118,15880
M 75
ad 84,1782
W 0
M 76
ae 44,70,384
M 77
s_bfe_u32 s51, s50, 0x10007
v_mad_i32_i24 v47, v49, s51, v48
XL 30,256
M 78
W 1
M 79
M 80
XL 32,1024
M 81
W 2
SA 254,2560
M 82
M 83
WL 1
R 80
M 84
mw 48
AN 112,114
AN 113,115
M 85
SH 114
SH 115
M 86
AN 114,114
AN 115,115
M 87
WL 2
AN 116,118
AN 117,119
M 88
SH 118
SH 119
M 89
AN 118,118
AN 119,119
M 90
TY 122,3592
TY 126,17416
M 91
ad 84,1824
M 92
W 0
XL 34,1792
M 93
XL 36,2560
M 94
M 95
R 82
M 96
W 1
M 97
M 98
M 99
WL 3
AN 120,122
AN 121,123
M 100
SH 122
SH 123
M 101
AN 122,122
AN 123,123
M 102
WL 2
AN 124,126
AN 125,127
M 103
SH 126
SH 127
M 104
AN 126,126
AN 127,127
M 105
W 2
M 106
M 107
R 0
M 108
mw 51
M 109
ad 84,1830
W 0
M 110
M 111
M 112
W 1
M 113
M 114
M 115
W 2
M 116
M 117
M 118
bc 49,20,66,60
M 119
WL 2
R 2
M 120
mh 18
M 121
ad 66,3872
H 0
M 132
M 133
TX 58,0
TX 62,13824
M 122
DG 1024
M 123
M 134
UL 6,22
UH 7,22
M 135
TY 66,512
TY 70,14336
M 124
bc 48,20,64,56
AD 160,6
M 125
mh 21
AD 184,7
UL 6,23
M 136
ad 64,3872
H 0
M 137
TX 74,1536
TX 78,15360
M 126
WL 4
Q 0
M 127
Q 2
M 138
Q 4
M 139
TY 82,2048
TY 86,15872
M 128
Q 6
M 129
WL 4
Q 8
M 140
Q 10
M 141
TX 90,3072
TX 94,16896
M 130
Q 12
M 131
R 4
M 142
Q 14
M 143
TY 98,3584
TY 102,17408
M 0
R 6
M 1
WL 10
Q 16
M 12
Q 18
M 13
Q 20
M 2
Q 22
M 3
WL 8
Q 24
M 14
Q 26
M 15
Q 28
M 4
Q 30
M 5
WL 6
Q 32
M 16
Q 34
M 17
Q 36
M 6
Q 38
M 7
WL 2
Q 40
M 18
Q 42
M 19
Q 44
)"
R"(M 8
Q 46
M 9
H 1
UH 7,23
M 20
AD 208,6
AD 232,7
M 21
UL 6,24
UH 7,24
M 10
H 2
AD 161,6
M 11
R 8
M 22
AD 185,7
UL 6,25
M 23
R 10
PA 12288
DF
M 24
UH 7,25
AD 209,6
M 25
AD 233,7
UL 6,26
M 26
UH 7,26
AD 162,6
M 27
AD 186,7
UL 6,27
M 28
UH 7,27
AD 210,6
M 29
AD 234,7
UL 6,28
M 30
UH 7,28
AD 163,6
M 31
AD 187,7
UL 6,29
M 32
UH 7,29
AD 211,6
M 33
WV 6
AD 235,7
M 34
WL 0
FP
M 35
M 36
M 37
M 38
M 39
M 40
M 41
M 42
M 43
M 44
M 45
FR
M 46
M 47
M 48
M 49
M 50
M 51
M 52
M 53
WL 0
FQ 152
M 54
M 55
M 56
FZ 152
R 24
M 57
mw 72
R 26
M 58
TY 106,5128
TY 110,18952
M 59
R 28
M 60
ad 84,1872
W 0
M 61
M 62
M 63
W 1
M 64
M 65
M 66
W 2
M 67
WL 3
AN 104,106
AN 105,107
M 68
SH 106
SH 107
M 69
mw 75
AN 106,106
AN 107,107
M 70
WL 2
AN 108,110
AN 109,111
M 71
R 30
M 72
SH 110
SH 111
M 73
AN 110,110
AN 111,111
M 74
TY 114,6664
TY 118,20488
M 75
ad 84,1878
W 0
M 76
M 77
M 78
W 1
M 79
ae 44,68,384
M 80
s_bitcmp1_b32 s50, 8
s_cselect_b32 s51, 24576, 0
v_add_u32 v47, s51, v3
M 81
W 2
XL 38,256
M 82
M 83
WL 1
R 32
M 84
mw 0
AN 112,114
AN 113,115
M 85
SH 114
SH 115
M 86
AN 114,114
AN 115,115
M 87
WL 2
AN 116,118
AN 117,119
M 88
SH 118
SH 119
M 89
AN 118,118
AN 119,119
M 90
TY 122,8200
TY 126,22024
M 91
ad 84,1920
M 92
W 0
XL 40,1024
M 93
XL 42,1792
M 94
M 95
R 34
M 96
W 1
XL 44,2560
M 97
M 98
M 99
WL 3
AN 120,122
AN 121,123
M 100
SH 122
SH 123
M 101
AN 122,122
AN 123,123
M 102
WL 2
AN 124,126
AN 125,127
M 103
SH 126
SH 127
M 104
AN 126,126
AN 127,127
M 105
W 2
M 106
M 107
R 48
M 108
mw 3
M 109
ad 84,1926
W 0
M 110
M 111
M 112
W 1
M 113
M 114
M 115
W 2
M 116
M 117
M 118
bc 49,21,66,60
M 119
WL 2
R 50
M 120
mh 0
M 121
ad 66,4064
H 0
M 132
M 133
TX 58,4608
TX 62,18432
M 122
DG 1024
M 123
M 134
M 135
TY 66,5120
TY 70,18944
M 124
bc 48,21,64,56
M 125
mh 3
M 136
ad 64,4064
H 0
M 137
TX 74,6144
TX 78,19968
M 126
WL 4
Q 0
M 127
Q 2
M 138
Q 4
M 139
TY 82,6656
TY 86,20480
M 128
Q 6
M 129
WL 4
Q 8
M 140
Q 10
M 141
TX 90,7680
TX 94,21504
M 130
Q 12
M 131
R 52
M 142
Q 14
M 143
TY 98,8192
TY 102,22016
M 0
R 54
M 1
WL 10
Q 16
M 12
Q 18
M 13
Q 20
M 2
Q 22
M 3
WL 8
Q 24
M 14
Q 26
M 15
Q 28
M 4
Q 30
M 5
WL 6
Q 32
M 16
Q 34
M 17
Q 36
M 6
Q 38
M 7
WL 2
Q 40
M 18
Q 42
M 19
Q 44
M 8
Q 46
M 9
H 1
UL 6,30
M 20
UH 7,30
AD 164,6
M 21
AD 188,7
UL 6,31
M 10
H 2
UH 7,31
M 11
R 56
M 22
AD 212,6
AD 236,7
M 23
R 58
M 24
UL 6,32
UH 7,32
M 25
AD 165,6
AD 189,7
M 26
UL 6,33
UH 7,33
M 27
AD 213,6
AD 237,7
M 28
UL 6,34
UH 7,34
M 29
AD 166,6
AD 190,7
M 30
UL 6,35
UH 7,35
M 31
AD 214,6
AD 238,7
M 32
UL 6,36
UH 7,36
M 33
WV 5
AD 167,6
AD 191,7
M 34
WL 0
FP
UL 6,37
M 35
UH 7,37
AD 215,6
M 36
AD 239,7
M 37
M 38
M 39
M 40
M 41
M 42
M 43
M 44
M 45
FR
M 46
M 47
M 48
M 49
M 50
M 51
M 52
)"
R"(M 53
WL 0
FQ 156
M 54
M 55
M 56
FZ 156
R 72
M 57
mw 24
R 74
M 58
TY 106,9736
TY 110,23560
M 59
R 76
M 60
ad 84,1968
W 0
M 61
M 62
M 63
W 1
M 64
M 65
M 66
W 2
M 67
WL 3
AN 104,106
AN 105,107
M 68
SH 106
SH 107
M 69
mw 27
AN 106,106
AN 107,107
M 70
WL 2
AN 108,110
AN 109,111
M 71
R 78
M 72
SH 110
SH 111
M 73
AN 110,110
AN 111,111
M 74
TY 114,11272
TY 118,25096
M 75
ad 84,1974
W 0
M 76
M 77
M 78
W 1
M 79
M 80
M 81
W 2
M 82
ae 44,68,384
M 83
WL 1
R 80
M 84
mw 48
AN 112,114
AN 113,115
M 85
s_bfe_u32 s51, s50, 0x10009
SH 114
SH 115
M 86
AN 114,114
AN 115,115
M 87
WL 2
AN 116,118
AN 117,119
M 88
SH 118
SH 119
M 89
AN 118,118
AN 119,119
M 90
TY 122,12808
TY 126,26632
M 91
ad 84,2016
M 92
W 0
v_mad_i32_i24 v47, v49, s51, v48
M 93
XL 14,256
M 94
XL 16,1024
M 95
R 82
M 96
W 1
XL 18,1792
M 97
M 98
M 99
WL 3
AN 120,122
AN 121,123
M 100
SH 122
SH 123
M 101
AN 122,122
AN 123,123
M 102
WL 2
AN 124,126
AN 125,127
M 103
SH 126
SH 127
M 104
AN 126,126
AN 127,127
M 105
W 2
XL 20,2560
M 106
M 107
R 0
M 108
mw 51
M 109
ad 84,2022
W 0
M 110
M 111
M 112
W 1
M 113
M 114
M 115
W 2
M 116
M 117
M 118
bc 49,22,66,60
M 119
WL 2
R 2
M 120
mh 9
M 121
ad 66,4256
H 0
M 132
M 133
TX 58,9216
TX 62,23040
M 122
DG 1024
M 123
M 134
M 135
TY 66,9728
TY 70,23552
M 124
bc 48,22,64,56
M 125
mh 12
M 136
ad 64,4256
H 0
M 137
TX 74,10752
TX 78,24576
M 126
WL 4
Q 0
M 127
Q 2
M 138
Q 4
M 139
TY 82,11264
TY 86,25088
M 128
Q 6
M 129
WL 4
Q 8
M 140
Q 10
M 141
TX 90,12288
TX 94,26112
M 130
Q 12
M 131
R 4
M 142
Q 14
M 143
TY 98,12800
TY 102,26624
M 0
R 6
M 1
WL 10
Q 16
M 12
Q 18
M 13
Q 20
M 2
Q 22
M 3
WL 8
Q 24
M 14
Q 26
M 15
Q 28
M 4
Q 30
M 5
WL 6
Q 32
M 16
Q 34
M 17
Q 36
M 6
Q 38
M 7
WL 2
Q 40
M 18
Q 42
M 19
Q 44
M 8
Q 46
M 9
H 1
UL 6,38
M 20
UH 7,38
AD 168,6
M 21
AD 192,7
UL 6,39
M 10
H 2
UH 7,39
M 11
R 8
M 22
AD 216,6
AD 240,7
M 23
R 10
PA 12288
DF
M 24
UL 6,40
UH 7,40
M 25
AD 169,6
AD 193,7
M 26
UL 6,41
UH 7,41
M 27
AD 217,6
AD 241,7
M 28
UL 6,42
UH 7,42
M 29
AD 170,6
AD 194,7
M 30
UL 6,43
UH 7,43
M 31
AD 218,6
AD 242,7
M 32
UL 6,44
UH 7,44
M 33
WV 6
AD 171,6
AD 195,7
M 34
WL 0
FP
UL 6,45
M 35
UH 7,45
AD 219,6
M 36
AD 243,7
M 37
M 38
M 39
M 40
M 41
M 42
M 43
M 44
M 45
FR
M 46
M 47
M 48
M 49
M 50
M 51
M 52
M 53
WL 0
FQ 160
M 54
M 55
M 56
FZ 160
R 24
M 57
mw 72
R 26
M 58
TY 106,520
TY 110,14344
M 59
R 28
M 60
ad 84,2064
W 0
M 61
M 62
M 63
W 1
M 64
M 65
M 66
W 2
M 67
WL 3
AN 104,106
AN 105,107
M 68
SH 106
SH 107
M 69
mw 75
AN 106,106
AN 107,107
M 70
WL 2
AN 108,110
AN 109,111
M 71
R 30
M 72
SH 110
SH 111
M 73
AN 110,110
AN 111,111
M 74
TY 114,2056
TY 118,15880
M 75
ad 84,2070
W 0
M 76
M 77
M 78
W 1
M 79
M 80
M 81
W 2
M 82
M 83
R 32
M 84
mw 0
WL 3
AN 112,114
AN 113,115
M 85
SH 114
)"
R"(SH 115
M 86
AN 114,114
AN 115,115
M 87
WL 2
AN 116,118
AN 117,119
M 88
SH 118
SH 119
M 89
AN 118,118
AN 119,119
M 90
TY 122,3592
TY 126,17416
M 91
ad 84,2112
M 92
W 0
M 93
ae 44,68,480
M 94
s_bitcmp1_b32 s50, 10
s_cselect_b32 s51, 24576, 0
v_add_u32 v47, s51, v3
M 95
R 34
M 96
W 1
XL 22,256
M 97
XL 24,1024
M 98
XL 26,1792
M 99
WL 3
AN 120,122
AN 121,123
M 100
SH 122
SH 123
M 101
AN 122,122
AN 123,123
M 102
WL 2
AN 124,126
AN 125,127
M 103
SH 126
SH 127
M 104
AN 126,126
AN 127,127
M 105
W 2
XL 28,2560
M 106
M 107
R 48
M 108
mw 3
M 109
ad 84,2118
W 0
M 110
M 111
M 112
W 1
M 113
M 114
M 115
W 2
M 116
M 117
M 118
bc 49,23,66,60
M 119
WL 2
R 50
M 120
mh 18
M 121
ad 66,4448
H 0
M 132
M 133
TX 58,0
TX 62,13824
M 122
DG 1024
M 123
M 134
M 135
TY 66,512
TY 70,14336
M 124
bc 48,23,64,56
M 125
mh 21
M 136
ad 64,4448
H 0
M 137
TX 74,1536
TX 78,15360
M 126
WL 4
Q 0
M 127
Q 2
M 138
Q 4
M 139
TY 82,2048
TY 86,15872
M 128
Q 6
M 129
WL 4
Q 8
M 140
Q 10
M 141
TX 90,3072
TX 94,16896
M 130
Q 12
M 131
R 52
M 142
Q 14
M 143
TY 98,3584
TY 102,17408
M 0
R 54
M 1
WL 10
Q 16
M 12
Q 18
M 13
Q 20
M 2
Q 22
M 3
WL 8
Q 24
M 14
Q 26
M 15
Q 28
M 4
Q 30
M 5
WL 6
Q 32
M 16
Q 34
M 17
Q 36
M 6
Q 38
M 7
WL 2
Q 40
M 18
Q 42
M 19
Q 44
M 8
Q 46
M 9
H 1
UL 6,14
M 20
UH 7,14
AD 172,6
M 21
AD 196,7
UL 6,15
M 10
H 2
UH 7,15
M 11
R 56
M 22
AD 220,6
AD 244,7
M 23
R 58
M 24
UL 6,16
UH 7,16
M 25
AD 173,6
AD 197,7
M 26
UL 6,17
UH 7,17
M 27
AD 221,6
AD 245,7
M 28
UL 6,18
UH 7,18
M 29
AD 174,6
AD 198,7
M 30
UL 6,19
UH 7,19
M 31
AD 222,6
AD 246,7
M 32
UL 6,20
UH 7,20
M 33
WV 5
AD 175,6
AD 199,7
M 34
WL 0
FP
UL 6,21
M 35
UH 7,21
AD 223,6
M 36
AD 247,7
M 37
M 38
M 39
M 40
M 41
M 42
M 43
M 44
M 45
FR
M 46
M 47
M 48
M 49
M 50
M 51
M 52
M 53
WL 0
FQ 164
M 54
M 55
M 56
FZ 164
R 72
M 57
mw 24
R 74
M 58
TY 106,5128
TY 110,18952
M 59
R 76
M 60
ad 84,2160
W 0
M 61
M 62
M 63
W 1
M 64
M 65
M 66
W 2
M 67
WL 3
AN 104,106
AN 105,107
M 68
SH 106
SH 107
M 69
mw 27
AN 106,106
AN 107,107
M 70
WL 2
AN 108,110
AN 109,111
M 71
R 78
M 72
SH 110
SH 111
M 73
AN 110,110
AN 111,111
M 74
TY 114,6664
TY 118,20488
M 75
ad 84,2166
W 0
M 76
M 77
M 78
W 1
M 79
M 80
M 81
W 2
M 82
M 83
R 80
M 84
mw 48
WL 3
AN 112,114
AN 113,115
M 85
SH 114
SH 115
M 86
AN 114,114
AN 115,115
M 87
WL 2
AN 116,118
AN 117,119
M 88
SH 118
SH 119
M 89
AN 118,118
AN 119,119
M 90
TY 122,8200
TY 126,22024
M 91
ad 84,2208
M 92
W 0
M 93
ae 44,68,480
M 94
s_bfe_u32 s51, s50, 0x1000b
v_mad_i32_i24 v47, v49, s51, v48
XL 30,256
M 95
R 82
M 96
W 1
XL 32,1024
M 97
M 98
M 99
WL 3
AN 120,122
AN 121,123
M 100
SH 122
SH 123
M 101
AN 122,122
AN 123,123
M 102
WL 2
AN 124,126
AN 125,127
M 103
SH 126
SH 127
M 104
AN 126,126
AN 127,127
M 105
W 2
XL 34,1792
M 106
M 107
R 0
M 108
mw 51
XL 36,2560
M 109
ad 84,2214
W 0
M 110
M 111
M 112
W 1
M 113
M 114
M 115
W 2
M 116
)"
R"(M 117
M 118
bc 49,0,66,60
M 119
WL 2
R 2
M 120
mh 0
M 121
ad 66,64
H 0
M 132
M 133
TX 58,4608
TX 62,18432
M 122
DG 1024
M 123
M 134
M 135
TY 66,5120
TY 70,18944
M 124
bc 48,0,64,56
M 125
mh 3
M 136
ad 64,32
H 0
M 137
TX 74,6144
TX 78,19968
M 126
WL 4
Q 0
M 127
Q 2
M 138
Q 4
M 139
TY 82,6656
TY 86,20480
M 128
Q 6
M 129
WL 4
Q 8
M 140
Q 10
M 141
TX 90,7680
TX 94,21504
M 130
Q 12
M 131
R 4
M 142
Q 14
M 143
TY 98,8192
TY 102,22016
M 0
R 6
M 1
WL 10
Q 16
M 12
Q 18
M 13
Q 20
M 2
Q 22
M 3
WL 8
Q 24
M 14
Q 26
M 15
Q 28
M 4
Q 30
M 5
WL 6
Q 32
M 16
Q 34
M 17
Q 36
M 6
Q 38
M 7
WL 2
Q 40
M 18
Q 42
M 19
Q 44
M 8
Q 46
M 9
H 1
UL 6,22
M 20
UH 7,22
AD 176,6
M 21
AD 200,7
UL 6,23
M 10
H 2
UH 7,23
M 11
R 8
M 22
AD 224,6
AD 248,7
M 23
R 10
PA 12288
DF
M 24
UL 6,24
UH 7,24
M 25
AD 177,6
AD 201,7
M 26
UL 6,25
UH 7,25
M 27
AD 225,6
AD 249,7
M 28
UL 6,26
UH 7,26
M 29
AD 178,6
AD 202,7
M 30
UL 6,27
UH 7,27
M 31
AD 226,6
AD 250,7
M 32
UL 6,28
UH 7,28
M 33
WV 6
AD 179,6
AD 203,7
M 34
WL 0
FP
UL 6,29
M 35
UH 7,29
AD 227,6
M 36
AD 251,7
M 37
M 38
M 39
M 40
M 41
M 42
M 43
M 44
M 45
FR
M 46
M 47
M 48
M 49
M 50
M 51
M 52
M 53
WL 0
FQ 168
M 54
M 55
M 56
FZ 168
R 24
M 57
mw 72
R 26
M 58
TY 106,9736
TY 110,23560
M 59
R 28
M 60
ad 84,2256
W 0
M 61
M 62
M 63
W 1
M 64
M 65
M 66
W 2
M 67
WL 3
AN 104,106
AN 105,107
M 68
SH 106
SH 107
M 69
mw 75
AN 106,106
AN 107,107
M 70
WL 2
AN 108,110
AN 109,111
M 71
R 30
M 72
SH 110
SH 111
M 73
AN 110,110
AN 111,111
M 74
TY 114,11272
TY 118,25096
M 75
ad 84,2262
W 0
M 76
M 77
M 78
W 1
M 79
M 80
M 81
W 2
M 82
M 83
R 32
M 84
mw 0
WL 3
AN 112,114
AN 113,115
M 85
SH 114
SH 115
M 86
AN 114,114
AN 115,115
M 87
WL 2
AN 116,118
AN 117,119
M 88
SH 118
SH 119
M 89
AN 118,118
AN 119,119
M 90
TY 122,12808
TY 126,26632
M 91
ad 84,0
M 92
W 0
M 93
M 94
M 95
R 34
M 96
W 1
M 97
M 98
M 99
WL 3
AN 120,122
AN 121,123
M 100
SH 122
SH 123
M 101
AN 122,122
AN 123,123
M 102
WL 2
AN 124,126
AN 125,127
M 103
SH 126
SH 127
M 104
AN 126,126
AN 127,127
M 105
W 2
M 106
M 107
R 48
M 108
mw 3
M 109
ad 84,6
W 0
M 110
M 111
M 112
W 1
M 113
M 114
M 115
W 2
M 116
M 117
M 118
bc 49,1,66,60
M 119
WL 2
R 50
M 120
mh 9
M 121
ad 66,256
H 0
M 132
M 133
TX 58,9216
TX 62,23040
M 122
DG 1024
M 123
M 134
M 135
TY 66,9728
TY 70,23552
M 124
bc 48,1,64,56
M 125
mh 12
M 136
ad 64,256
H 0
M 137
TX 74,10752
TX 78,24576
M 126
WL 4
Q 0
M 127
Q 2
M 138
Q 4
M 139
TY 82,11264
TY 86,25088
M 128
Q 6
M 129
WL 4
Q 8
M 140
Q 10
M 141
TX 90,12288
TX 94,26112
M 130
Q 12
M 131
R 52
M 142
Q 14
M 143
TY 98,12800
TY 102,26624
M 0
R 54
M 1
WL 10
Q 16
M 12
Q 18
M 13
Q 20
M 2
Q 22
M 3
WL 8
Q 24
M 14
Q 26
M 15
Q 28
M 4
Q 30
)"
R"(M 5
WL 6
Q 32
M 16
Q 34
M 17
Q 36
M 6
Q 38
M 7
WL 2
Q 40
M 18
Q 42
M 19
Q 44
M 8
Q 46
M 9
DG 1024
M 20
UL 6,30
UH 7,30
M 21
AD 180,6
AD 204,7
M 10
mh 15
UL 6,31
UH 7,31
M 11
R 56
M 22
ad 64,227
H 0
M 23
R 58
M 24
AD 228,6
AD 252,7
M 25
DG 1024
M 26
UL 6,32
UH 7,32
BV 52,4608
M 27
AD 181,6
AD 205,7
M 28
UL 6,33
UH 7,33
M 29
AD 229,6
AD 253,7
M 30
UL 6,34
UH 7,34
M 31
AD 182,6
AD 206,7
M 32
UL 6,35
UH 7,35
M 33
WV 6
AD 230,6
AD 254,7
M 34
WL 1
FP
UL 6,36
M 35
UH 7,36
AD 183,6
M 36
AD 207,7
UL 6,37
M 37
UH 7,37
AD 231,6
M 38
AD 255,7
M 39
M 40
ds_read_b64 v[12:13], v4 offset:4864
ds_read_b64 v[46:47], v4 offset:4872
M 41
AR 6,36
AR 7,60
M 42
AR 8,84
AR 9,108
M 43
WL 3
EF 6,52
EF 7,53
M 44
EF 8,54
EF 9,55
M 45
FR
CV 38,6,7
M 46
CV 39,8,9
AR 6,37
M 47
AR 7,61
AR 8,85
M 48
AR 9,109
EF 6,52
M 49
EF 7,53
EF 8,54
M 50
EF 9,55
CV 40,6,7
M 51
CV 41,8,9
AR 6,38
M 52
AR 7,62
AR 8,86
M 53
WL 0
FQ 172
AR 9,110
M 54
EF 6,52
EF 7,53
M 55
EF 8,54
EF 9,55
M 56
FZ 172
R 72
M 57
mw 24
R 74
M 58
TY 106,520
TY 110,14344
M 59
R 76
M 60
ad 84,48
W 0
M 61
CV 42,6,7
CV 43,8,9
M 62
AR 6,39
AR 7,63
M 63
W 1
AR 8,87
M 64
AR 9,111
EF 6,52
M 65
EF 7,53
EF 8,54
M 66
W 2
EF 9,55
M 67
WL 3
AN 104,106
AN 105,107
M 68
SH 106
SH 107
M 69
mw 27
AN 106,106
AN 107,107
M 70
WL 2
AN 108,110
AN 109,111
M 71
R 78
M 72
SH 110
SH 111
M 73
AN 110,110
AN 111,111
M 74
TY 114,2056
TY 118,15880
M 75
ad 84,54
W 0
M 76
CV 44,6,7
CV 45,8,9
M 77
ae 42,74,384
AR 6,40
M 78
W 1
AR 7,64
M 79
s_bitcmp1_b32 s50, 6
s_cselect_b32 s52, 24576, 0
v_add_u32 v50, s52, v3
ST 38,0
M 80
AR 8,88
AR 9,112
M 81
W 2
EF 6,52
M 82
EF 7,53
EF 8,54
M 83
WL 1
R 80
M 84
mw 48
AN 112,114
AN 113,115
M 85
SH 114
SH 115
M 86
AN 114,114
AN 115,115
M 87
WL 2
AN 116,118
AN 117,119
M 88
SH 118
SH 119
M 89
AN 118,118
AN 119,119
M 90
TY 122,3592
TY 126,17416
M 91
ad 84,96
ST 40,768
M 92
W 0
EF 9,55
M 93
CV 14,6,7
CV 15,8,9
M 94
AR 6,41
AR 7,65
M 95
R 82
M 96
W 1
AR 8,89
M 97
AR 9,113
EF 6,52
M 98
EF 7,53
EF 8,54
M 99
WL 3
AN 120,122
AN 121,123
M 100
SH 122
SH 123
M 101
AN 122,122
AN 123,123
M 102
WL 2
AN 124,126
AN 125,127
M 103
SH 126
SH 127
ST 42,1536
M 104
AN 126,126
AN 127,127
M 105
W 2
EF 9,55
M 106
CV 16,6,7
CV 17,8,9
M 107
R 0
M 108
mw 51
AR 6,42
AR 7,66
M 109
ad 84,102
W 0
M 110
AR 8,90
AR 9,114
M 111
EF 6,52
EF 7,53
M 112
W 1
EF 8,54
M 113
EF 9,55
CV 18,6,7
M 114
CV 19,8,9
AR 6,43
M 115
W 2
ST 44,2304
M 116
AR 7,67
AR 8,91
M 117
AR 9,115
EF 6,52
M 118
bc 49,2,66,60
EF 7,53
M 119
WL 0
R 2
M 120
mh 18
EF 8,54
EF 9,55
M 121
ad 66,448
H 0
M 132
CV 20,6,7
CV 21,8,9
M 133
TX 58,0
TX 62,13824
BV 52,5120
M 122
DG 1024
M 123
AR 6,132
AR 7,156
M 134
AR 8,180
EF 6,12
M 135
TY 66,512
TY 70,14336
M 124
bc 48,2,64,56
ae 42,74,384
M 125
mh 21
EF 7,13
EF 8,46
M 136
ad 64,448
H 0
M 137
WL 3
TX 74,1536
TX 78,15360
M 126
s_bfe_u32 s52, s50, 0x10007
Q 0
v_mad_i32_i24 v50, v49, s52, v48
M 127
Q 2
ST 14,0
M 138
Q 4
M 139
TY 82,2048
TY 86,15872
M 128
Q 6
M 129
WL 4
Q 8
M 140
Q 10
M 141
TX 90,3072
TX 94,16896
M 130
Q 12
M 131
R 4
M 142
Q 14
)"
R"(M 143
TY 98,3584
TY 102,17408
v_mfma_f32_16x16x32_f16 a[36:39], v[56:59], a[0:3], a[32:35]
R 6
M 1
WL 10
Q 16
ST 16,768
v_mfma_f32_16x16x32_f16 a[60:63], v[56:59], a[8:11], a[32:35]
Q 18
M 13
Q 20
v_mfma_f32_16x16x32_f16 a[40:43], v[64:67], a[0:3], a[32:35]
Q 22
M 3
WL 8
Q 24
v_mfma_f32_16x16x32_f16 a[64:67], v[64:67], a[8:11], a[32:35]
Q 26
M 15
Q 28
M 4
Q 30
M 5
WL 6
Q 32
M 16
Q 34
M 17
Q 36
M 6
Q 38
M 7
WL 1
Q 40
ST 18,1536
M 18
Q 42
M 19
Q 44
M 8
Q 46
M 9
H 1
EF 136,47
M 20
CV 22,6,7
CV 23,8,136
M 21
AR 6,133
AR 7,157
M 10
H 2
AR 8,181
M 11
R 8
M 22
EF 6,12
EF 7,13
M 23
R 10
PA 12288
DF
v_mfma_f32_16x16x32_f16 a[84:87], v[56:59], a[16:19], a[32:35]
EF 8,46
EF 137,47
M 25
ST 20,2304
CV 24,6,7
v_mfma_f32_16x16x32_f16 a[88:91], v[64:67], a[16:19], a[32:35]
CV 25,8,137
AR 6,134
M 27
AR 7,158
AR 8,182
M 28
EF 6,12
EF 7,13
M 29
EF 8,46
EF 138,47
M 30
CV 26,6,7
CV 27,8,138
M 31
AR 6,135
AR 7,159
M 32
AR 8,183
EF 6,12
M 33
WV 11
EF 7,13
EF 8,46
M 34
WL 0
FP
EF 139,47
M 35
CV 28,6,7
CV 29,8,139
v_mfma_f32_16x16x32_f16 a[108:111], v[56:59], a[24:27], a[32:35]
AR 6,136
AR 7,160
AR 8,184
EF 6,12
M 37
ae 42,74,384
EF 7,13
EF 8,46
EF 140,47
v_mfma_f32_16x16x32_f16 a[112:115], v[64:67], a[24:27], a[32:35]
s_bitcmp1_b32 s50, 6
s_cselect_b32 s52, 24576, 0
v_add_u32 v50, s52, v3
ST 22,128
CV 30,6,7
M 39
CV 31,8,140
AR 6,137
AR 7,161
AR 8,185
M 40
EF 6,12
EF 7,13
EF 8,46
EF 141,47
M 41
CV 32,6,7
CV 33,8,141
AR 6,138
AR 7,162
M 42
AR 8,186
EF 6,12
EF 7,13
EF 8,46
M 43
EF 142,47
CV 34,6,7
CV 35,8,142
AR 6,139
M 44
AR 7,163
AR 8,187
EF 6,12
EF 7,13
M 45
FR
EF 8,46
EF 143,47
CV 36,6,7
M 46
CV 37,8,143
M 47
ds_read_b64 v[12:13], v4 offset:4608
ds_read_b64 v[46:47], v4 offset:4616
v_mfma_f32_16x16x32_f16 a[132:135], v[56:59], a[0:3], a[32:35]
EF 160,52
EF 184,53
M 49
EF 208,54
EF 232,55
v_mfma_f32_16x16x32_f16 a[136:139], v[64:67], a[0:3], a[32:35]
ST 24,896
CV 38,160,184
M 51
CV 39,208,232
EF 161,52
M 52
EF 185,53
EF 209,54
M 53
WL 2
FQ 176
EF 233,55
M 54
CV 40,161,185
CV 41,209,233
M 55
EF 162,52
EF 186,53
M 56
FZ 176
R 24
M 57
mw 72
R 26
M 58
TY 106,5128
TY 110,18952
M 59
R 28
v_mfma_f32_16x16x32_f16 a[156:159], v[56:59], a[8:11], a[32:35]
ad 84,144
W 0
M 61
EF 210,54
EF 234,55
v_mfma_f32_16x16x32_f16 a[160:163], v[64:67], a[8:11], a[32:35]
ST 26,1664
CV 42,162,186
M 63
W 1
CV 43,210,234
M 64
EF 163,52
EF 187,53
M 65
EF 211,54
EF 235,55
M 66
W 2
CV 44,163,187
M 67
WL 3
AN 104,106
AN 105,107
M 68
SH 106
SH 107
M 69
mw 75
AN 106,106
AN 107,107
M 70
WL 2
AN 108,110
AN 109,111
M 71
R 30
v_mfma_f32_16x16x32_f16 a[180:183], v[56:59], a[16:19], a[32:35]
SH 110
SH 111
M 73
AN 110,110
AN 111,111
v_mfma_f32_16x16x32_f16 a[184:187], v[64:67], a[16:19], a[32:35]
TY 114,6664
TY 118,20488
ST 28,2432
M 75
ad 84,150
W 0
M 76
CV 45,211,235
M 77
EF 164,52
EF 188,53
EF 212,54
EF 236,55
M 78
W 1
CV 14,164,188
CV 15,212,236
EF 165,52
M 79
EF 189,53
EF 213,54
EF 237,55
CV 16,165,189
M 80
CV 17,213,237
EF 166,52
EF 190,53
EF 214,54
M 81
W 2
EF 238,55
CV 18,166,190
CV 19,214,238
M 82
EF 167,52
EF 191,53
EF 215,54
EF 239,55
M 83
WL 0
R 32
CV 20,167,191
CV 21,215,239
v_mfma_f32_16x16x32_f16 v[136:139], v[56:59], a[24:27], v[128:131]
mw 0
AN 112,114
AN 113,115
BV 52,4864
M 85
SH 114
SH 115
AR 6,44
v_mfma_f32_16x16x32_f16 v[140:143], v[64:67], a[24:27], v[128:131]
AN 114,114
AN 115,115
AR 7,68
M 87
AN 116,118
AN 117,119
AR 8,92
M 88
SH 118
SH 119
AR 9,116
M 89
AN 118,118
AN 119,119
EF 6,12
M 90
TY 122,8200
TY 126,22024
EF 7,13
M 91
ad 84,192
EF 8,46
M 92
W 0
ae 42,74,384
M 93
s_bfe_u32 s52, s50, 0x10007
v_mad_i32_i24 v50, v49, s52, v48
ST 30,128
EF 9,47
M 94
CV 22,6,7
CV 23,8,9
AR 6,45
M 95
R 34
AR 7,69
v_mfma_f32_16x16x32_f16 v[160:163], v[56:59], a[0:3], v[128:131]
W 1
AR 8,93
AR 9,117
M 97
EF 6,12
EF 7,13
EF 8,46
v_mfma_f32_16x16x32_f16 v[164:167], v[64:67], a[0:3], v[128:131]
EF 9,47
CV 24,6,7
CV 25,8,9
M 99
WL 3
AN 120,122
AN 121,123
AR 6,46
M 100
SH 122
SH 123
AR 7,70
M 101
AN 122,122
AN 123,123
AR 8,94
M 102
WL 2
AN 124,126
AN 125,127
AR 9,118
M 103
SH 126
SH 127
EF 6,12
M 104
AN 126,126
AN 127,127
EF 7,13
M 105
W 2
ST 32,896
EF 8,46
M 106
EF 9,47
CV 26,6,7
CV 27,8,9
M 107
R 48
AR 6,47
v_mfma_f32_16x16x32_f16 v[184:187], v[56:59], a[8:11], v[128:131]
mw 3
AR 7,71
AR 8,95
AR 9,119
M 109
ad 84,198
W 0
EF 6,12
v_mfma_f32_16x16x32_f16 v[188:191], v[64:67], a[8:11], v[128:131]
EF 7,13
EF 8,46
EF 9,47
M 111
CV 28,6,7
CV 29,8,9
M 112
W 1
AR 6,48
AR 7,72
M 113
AR 8,96
)"
R"(AR 9,120
EF 6,12
M 114
EF 7,13
EF 8,46
EF 9,47
M 115
W 2
CV 6,6,7
CV 7,8,9
M 116
AW 200,6
AW 201,7
AR 6,49
M 117
ST 34,1664
AR 7,73
AR 8,97
M 118
bc 49,3,66,60
AR 9,121
EF 6,12
M 119
WL 0
R 50
EF 7,13
v_mfma_f32_16x16x32_f16 v[208:211], v[56:59], a[16:19], v[128:131]
mh 0
EF 8,46
EF 9,47
CV 6,6,7
M 121
ad 66,640
H 0
CV 7,8,9
v_mfma_f32_16x16x32_f16 v[232:235], v[56:59], a[24:27], v[128:131]
AW 202,6
AW 203,7
AR 6,50
M 133
TX 58,4608
TX 62,18432
AR 7,74
v_mfma_f32_16x16x32_f16 v[212:215], v[64:67], a[16:19], v[128:131]
DG 1024
AR 8,98
M 123
AR 9,122
EF 6,12
EF 7,13
v_mfma_f32_16x16x32_f16 v[236:239], v[64:67], a[24:27], v[128:131]
EF 8,46
EF 9,47
CV 6,6,7
M 135
TY 66,5120
TY 70,18944
CV 7,8,9
M 124
bc 48,3,64,56
AW 204,6
AW 205,7
M 125
mh 3
ST 36,2432
AR 6,51
AR 7,75
M 136
ad 64,640
H 0
AR 8,99
M 137
TX 74,6144
TX 78,19968
AR 9,123
M 126
WL 4
Q 0
EF 6,12
M 127
Q 2
EF 7,13
M 138
Q 4
EF 8,46
M 139
TY 82,6656
TY 86,20480
EF 9,47
M 128
Q 6
CV 6,6,7
M 129
WL 4
Q 8
CV 7,8,9
M 140
Q 10
AW 206,6
M 141
TX 90,7680
TX 94,21504
AW 207,7
M 130
Q 12
ds_read_b64 v[12:13], v4 offset:5120
M 131
R 52
ds_read_b64 v[46:47], v4 offset:5128
M 142
Q 14
M 143
TY 98,8192
TY 102,22016
M 0
R 54
M 1
WL 12
Q 16
M 12
Q 18
M 13
Q 20
M 2
Q 22
M 3
WL 10
Q 24
M 14
Q 26
M 15
Q 28
v_mfma_f32_16x16x32_f16 a[44:47], v[72:75], a[0:3], a[32:35]
Q 30
M 5
WL 7
Q 32
v_mfma_f32_16x16x32_f16 a[68:71], v[72:75], a[8:11], a[32:35]
Q 34
M 17
Q 36
v_mfma_f32_16x16x32_f16 a[48:51], v[80:83], a[0:3], a[32:35]
Q 38
M 7
WL 2
Q 40
v_mfma_f32_16x16x32_f16 a[72:75], v[80:83], a[8:11], a[32:35]
Q 42
M 19
Q 44
M 8
Q 46
M 9
H 1
ae 42,74,384
M 20
s_bitcmp1_b32 s50, 6
s_cselect_b32 s52, 24576, 0
v_add_u32 v50, s52, v3
ST 38,256
M 21
AR 6,140
AR 7,164
M 10
H 2
AR 8,188
M 11
R 56
M 22
EF 6,52
EF 7,53
M 23
R 58
M 24
EF 8,54
EF 144,55
M 25
CV 30,6,7
CV 31,8,144
M 26
AR 6,141
AR 7,165
M 27
AR 8,189
EF 6,52
v_mfma_f32_16x16x32_f16 a[92:95], v[72:75], a[16:19], a[32:35]
EF 7,53
EF 8,54
M 29
EF 145,55
CV 32,6,7
v_mfma_f32_16x16x32_f16 a[96:99], v[80:83], a[16:19], a[32:35]
ST 40,1024
CV 33,8,145
M 31
AR 6,142
AR 7,166
M 32
AR 8,190
EF 6,52
M 33
WV 9
EF 7,53
EF 8,54
M 34
WL 0
FP
EF 146,55
M 35
CV 34,6,7
CV 35,8,146
M 36
AR 6,143
AR 7,167
M 37
AR 8,191
EF 6,52
M 38
EF 7,53
EF 8,54
M 39
EF 147,55
CV 36,6,7
v_mfma_f32_16x16x32_f16 a[116:119], v[72:75], a[24:27], a[32:35]
CV 37,8,147
M 41
AR 6,144
AR 7,168
AR 8,192
EF 6,52
v_mfma_f32_16x16x32_f16 a[120:123], v[80:83], a[24:27], a[32:35]
ST 42,1792
EF 7,53
EF 8,54
EF 148,55
M 43
CV 6,6,7
CV 7,8,148
AW 208,6
AW 209,7
M 44
AR 6,145
AR 7,169
AR 8,193
EF 6,52
M 45
FR
EF 7,53
EF 8,54
EF 149,55
M 46
CV 6,6,7
CV 7,8,149
AW 210,6
AW 211,7
M 47
AR 6,146
AR 7,170
AR 8,194
EF 6,52
M 48
EF 7,53
EF 8,54
EF 150,55
CV 6,6,7
M 49
CV 7,8,150
AW 212,6
AW 213,7
AR 6,147
M 50
AR 7,171
AR 8,195
EF 6,52
EF 7,53
M 51
EF 8,54
EF 151,55
CV 6,6,7
CV 7,8,151
v_mfma_f32_16x16x32_f16 a[140:143], v[72:75], a[0:3], a[32:35]
AW 214,6
AW 215,7
M 53
WL 0
FQ 180
BV 52,4608
EF 168,12
v_mfma_f32_16x16x32_f16 a[144:147], v[80:83], a[0:3], a[32:35]
ST 44,2560
EF 192,13
EF 216,46
M 55
EF 240,47
CV 6,168,192
CV 7,216,240
M 56
FZ 180
R 72
AW 216,6
M 57
mw 24
R 74
AW 217,7
M 58
TY 106,9736
TY 110,23560
EF 169,12
M 59
R 76
EF 193,13
M 60
ad 84,240
W 0
EF 217,46
M 61
EF 241,47
CV 6,169,193
CV 7,217,241
M 62
AW 218,6
AW 219,7
EF 170,12
M 63
W 1
EF 194,13
EF 218,46
v_mfma_f32_16x16x32_f16 a[164:167], v[72:75], a[8:11], a[32:35]
EF 242,47
CV 6,170,194
CV 7,218,242
M 65
AW 220,6
AW 221,7
EF 171,12
v_mfma_f32_16x16x32_f16 a[168:171], v[80:83], a[8:11], a[32:35]
WL 3
W 2
ae 42,74,384
M 67
s_bfe_u32 s52, s50, 0x10007
AN 104,106
AN 105,107
v_mad_i32_i24 v50, v49, s52, v48
M 68
SH 106
SH 107
ST 14,256
M 69
mw 27
AN 106,106
AN 107,107
EF 195,13
M 70
WL 0
AN 108,110
AN 109,111
EF 219,46
M 71
R 78
EF 243,47
M 72
SH 110
SH 111
CV 6,171,195
M 73
AN 110,110
AN 111,111
CV 7,219,243
M 74
TY 114,11272
TY 118,25096
AW 222,6
M 75
ad 84,246
W 0
AW 223,7
v_mfma_f32_16x16x32_f16 a[188:191], v[72:75], a[16:19], a[32:35]
EF 172,12
EF 196,13
EF 220,46
M 77
EF 244,47
CV 38,172,196
CV 39,220,244
v_mfma_f32_16x16x32_f16 a[192:195], v[80:83], a[16:19], a[32:35]
W 1
EF 173,12
EF 197,13
M 79
EF 221,46
EF 245,47
CV 40,173,197
M 80
ST 16,1024
CV 41,221,245
EF 174,12
M 81
W 2
EF 198,13
)"
R"(EF 222,46
M 82
EF 246,47
CV 42,174,198
CV 43,222,246
M 83
WL 0
R 80
EF 175,12
M 84
mw 48
AN 112,114
AN 113,115
EF 199,13
M 85
SH 114
SH 115
EF 223,46
M 86
AN 114,114
AN 115,115
EF 247,47
M 87
AN 116,118
AN 117,119
CV 44,175,199
v_mfma_f32_16x16x32_f16 v[144:147], v[72:75], a[24:27], v[128:131]
SH 118
SH 119
CV 45,223,247
M 89
AN 118,118
AN 119,119
ds_read_b64 v[12:13], v4 offset:4864
v_mfma_f32_16x16x32_f16 v[148:151], v[80:83], a[24:27], v[128:131]
TY 122,12808
TY 126,26632
ds_read_b64 v[46:47], v4 offset:4872
M 91
ad 84,288
AR 6,52
M 92
W 0
ST 18,1792
AR 7,76
M 93
AR 8,100
AR 9,124
EF 6,52
M 94
EF 7,53
EF 8,54
EF 9,55
M 95
R 82
CV 6,6,7
M 96
W 1
CV 7,8,9
AW 224,6
M 97
AW 225,7
AR 6,53
AR 7,77
M 98
AR 8,101
AR 9,125
EF 6,52
M 99
WL 4
AN 120,122
AN 121,123
EF 7,53
v_mfma_f32_16x16x32_f16 v[168:171], v[72:75], a[0:3], v[128:131]
SH 122
SH 123
EF 8,54
M 101
AN 122,122
AN 123,123
EF 9,55
v_mfma_f32_16x16x32_f16 v[172:175], v[80:83], a[0:3], v[128:131]
WL 2
AN 124,126
AN 125,127
CV 6,6,7
M 103
SH 126
SH 127
CV 7,8,9
M 104
AN 126,126
AN 127,127
ST 20,2560
M 105
W 2
AW 226,6
AW 227,7
M 106
AR 6,54
AR 7,78
AR 8,102
M 107
R 0
AR 9,126
M 108
mw 51
EF 6,52
EF 7,53
EF 8,54
M 109
ad 84,294
W 0
EF 9,55
M 110
CV 6,6,7
CV 7,8,9
AW 228,6
M 111
AW 229,7
AR 6,55
AR 7,79
v_mfma_f32_16x16x32_f16 v[192:195], v[72:75], a[8:11], v[128:131]
W 1
AR 8,103
AR 9,127
M 113
EF 6,52
EF 7,53
EF 8,54
v_mfma_f32_16x16x32_f16 v[196:199], v[80:83], a[8:11], v[128:131]
EF 9,55
CV 6,6,7
CV 7,8,9
M 115
W 2
AW 230,6
AW 231,7
M 116
ae 42,72,384
AR 6,56
AR 7,80
M 117
s_bitcmp1_b32 s50, 8
s_cselect_b32 s52, 24576, 0
v_add_u32 v50, s52, v3
ST 22,0
M 118
bc 49,4,66,60
AR 8,104
AR 9,128
M 119
WL 0
R 2
EF 6,52
M 120
mh 9
EF 7,53
EF 8,54
EF 9,55
M 121
ad 66,832
H 0
CV 14,6,7
M 132
CV 15,8,9
AR 6,57
AR 7,81
M 133
TX 58,9216
TX 62,23040
AR 8,105
M 122
DG 1024
AR 9,129
M 123
EF 6,52
EF 7,53
EF 8,54
M 134
EF 9,55
CV 16,6,7
CV 17,8,9
M 135
TY 66,9728
TY 70,23552
AR 6,58
v_mfma_f32_16x16x32_f16 v[216:219], v[72:75], a[16:19], v[128:131]
bc 48,4,64,56
AR 7,82
AR 8,106
M 125
mh 12
ST 24,768
AR 9,130
EF 6,52
v_mfma_f32_16x16x32_f16 v[240:243], v[72:75], a[24:27], v[128:131]
ad 64,832
H 0
EF 7,53
M 137
TX 74,10752
TX 78,24576
EF 8,54
v_mfma_f32_16x16x32_f16 v[220:223], v[80:83], a[16:19], v[128:131]
WL 4
Q 0
EF 9,55
M 127
Q 2
CV 18,6,7
v_mfma_f32_16x16x32_f16 v[244:247], v[80:83], a[24:27], v[128:131]
Q 4
CV 19,8,9
M 139
TY 82,11264
TY 86,25088
AR 6,59
M 128
Q 6
AR 7,83
M 129
WL 4
Q 8
AR 8,107
M 140
Q 10
AR 9,131
M 141
TX 90,12288
TX 94,26112
EF 6,52
M 130
Q 12
EF 7,53
M 131
R 4
ST 26,1536
M 142
Q 14
EF 8,54
M 143
TY 98,12800
TY 102,26624
EF 9,55
M 0
R 6
CV 20,6,7
M 1
WL 8
Q 16
CV 21,8,9
M 12
Q 18
BV 52,5120
M 13
Q 20
M 2
Q 22
M 3
Q 24
M 14
Q 26
M 15
Q 28
M 4
Q 30
M 5
WL 5
Q 32
ST 28,2304
M 16
Q 34
M 17
Q 36
M 6
Q 38
M 7
WL 1
Q 40
M 18
Q 42
M 19
Q 44
v_mfma_f32_16x16x32_f16 a[52:55], v[88:91], a[0:3], a[32:35]
Q 46
M 9
H 1
AR 6,148
v_mfma_f32_16x16x32_f16 a[76:79], v[88:91], a[8:11], a[32:35]
AR 7,172
AR 8,196
M 21
EF 6,12
EF 7,13
v_mfma_f32_16x16x32_f16 a[56:59], v[96:99], a[0:3], a[32:35]
H 2
EF 8,46
M 11
R 8
v_mfma_f32_16x16x32_f16 a[80:83], v[96:99], a[8:11], a[32:35]
ae 42,72,384
ST 30,128
M 23
R 10
PA 12288
DF
M 24
EF 152,47
CV 6,6,7
M 25
CV 7,8,152
AW 232,6
M 26
AW 233,7
AR 6,149
M 27
AR 7,173
AR 8,197
M 28
EF 6,12
EF 7,13
M 29
EF 8,46
EF 153,47
M 30
CV 6,6,7
CV 7,8,153
M 31
AW 234,6
AW 235,7
v_mfma_f32_16x16x32_f16 a[100:103], v[88:91], a[16:19], a[32:35]
AR 6,150
AR 7,174
M 33
WV 11
AR 8,198
EF 6,12
v_mfma_f32_16x16x32_f16 a[104:107], v[96:99], a[16:19], a[32:35]
WL 0
FP
ST 32,896
M 35
EF 7,13
EF 8,46
M 36
EF 154,47
CV 6,6,7
M 37
CV 7,8,154
AW 236,6
M 38
AW 237,7
AR 6,151
M 39
AR 7,175
AR 8,199
M 40
EF 6,12
EF 7,13
M 41
EF 8,46
EF 155,47
M 42
CV 6,6,7
CV 7,8,155
M 43
AW 238,6
AW 239,7
v_mfma_f32_16x16x32_f16 a[124:127], v[88:91], a[24:27], a[32:35]
AR 6,152
AR 7,176
EF 6,12
M 45
FR
EF 7,13
EF 132,46
v_mfma_f32_16x16x32_f16 a[128:131], v[96:99], a[24:27], a[32:35]
ST 34,1664
EF 156,47
CV 22,6,7
M 47
CV 23,132,156
AR 6,153
AR 7,177
M 48
EF 6,12
EF 7,13
EF 133,46
M 49
EF 157,47
CV 24,6,7
CV 25,133,157
M 50
AR 6,154
AR 7,178
EF 6,12
M 51
EF 7,13
EF 134,46
)"
R"(EF 158,47
M 52
CV 26,6,7
CV 27,134,158
AR 6,155
M 53
WL 0
FQ 184
AR 7,179
EF 6,12
M 54
EF 7,13
EF 135,46
EF 159,47
M 55
CV 28,6,7
CV 29,135,159
v_mfma_f32_16x16x32_f16 a[148:151], v[88:91], a[0:3], a[32:35]
FZ 184
R 24
EF 176,52
M 57
mw 72
R 26
EF 200,53
v_mfma_f32_16x16x32_f16 a[152:155], v[96:99], a[0:3], a[32:35]
TY 106,520
TY 110,14344
ST 36,2432
M 59
R 28
EF 224,54
M 60
ad 84,336
W 0
EF 248,55
M 61
CV 6,176,200
CV 7,224,248
AW 240,6
M 62
AW 241,7
EF 177,52
EF 201,53
M 63
W 1
EF 225,54
EF 249,55
M 64
CV 6,177,201
CV 7,225,249
AW 242,6
M 65
AW 243,7
EF 178,52
EF 202,53
M 66
W 2
EF 226,54
EF 250,55
M 67
WL 3
AN 104,106
AN 105,107
CV 6,178,202
v_mfma_f32_16x16x32_f16 a[172:175], v[88:91], a[8:11], a[32:35]
SH 106
SH 107
CV 7,226,250
M 69
mw 75
AN 106,106
AN 107,107
AW 244,6
v_mfma_f32_16x16x32_f16 a[176:179], v[96:99], a[8:11], a[32:35]
WL 0
AN 108,110
AN 109,111
AW 245,7
M 71
R 30
EF 179,52
M 72
SH 110
SH 111
EF 203,53
M 73
AN 110,110
AN 111,111
EF 227,54
M 74
TY 114,2056
TY 118,15880
EF 251,55
M 75
ad 84,342
W 0
CV 6,179,203
M 76
ae 42,72,384
CV 7,227,251
AW 246,6
M 77
s_bfe_u32 s52, s50, 0x10009
v_mad_i32_i24 v50, v49, s52, v48
ST 38,256
AW 247,7
M 78
W 1
EF 180,52
EF 204,53
M 79
EF 228,54
EF 252,55
CV 6,180,204
v_mfma_f32_16x16x32_f16 a[196:199], v[88:91], a[16:19], a[32:35]
CV 7,228,252
AW 248,6
AW 249,7
M 81
W 2
EF 181,52
EF 205,53
v_mfma_f32_16x16x32_f16 v[132:135], v[96:99], a[16:19], v[128:131]
EF 229,54
EF 253,55
CV 6,181,205
M 83
WL 0
R 32
CV 7,229,253
M 84
mw 0
AN 112,114
AN 113,115
AW 250,6
M 85
SH 114
SH 115
AW 251,7
M 86
AN 114,114
AN 115,115
EF 182,52
M 87
AN 116,118
AN 117,119
EF 206,53
M 88
SH 118
SH 119
EF 230,54
M 89
AN 118,118
AN 119,119
ST 40,1024
M 90
TY 122,3592
TY 126,17416
EF 254,55
M 91
ad 84,384
CV 6,182,206
v_mfma_f32_16x16x32_f16 v[152:155], v[88:91], a[24:27], v[128:131]
W 0
CV 7,230,254
AW 252,6
M 93
AW 253,7
EF 183,52
EF 207,53
v_mfma_f32_16x16x32_f16 v[156:159], v[96:99], a[24:27], v[128:131]
EF 231,54
EF 255,55
CV 6,183,207
M 95
R 34
CV 7,231,255
M 96
W 1
AW 254,6
AW 255,7
M 97
M 98
M 99
WL 3
AN 120,122
AN 121,123
M 100
SH 122
SH 123
M 101
AN 122,122
AN 123,123
ST 42,1792
M 102
WL 2
AN 124,126
AN 125,127
M 103
SH 126
SH 127
v_mfma_f32_16x16x32_f16 v[176:179], v[88:91], a[0:3], v[128:131]
AN 126,126
AN 127,127
M 105
W 2
v_mfma_f32_16x16x32_f16 v[180:183], v[96:99], a[0:3], v[128:131]
M 107
R 48
M 108
mw 3
M 109
ad 84,390
W 0
M 110
M 111
M 112
W 1
M 113
ST 44,2560
M 114
M 115
W 2
v_mfma_f32_16x16x32_f16 v[200:203], v[88:91], a[8:11], v[128:131]
M 117
v_mfma_f32_16x16x32_f16 v[204:207], v[96:99], a[8:11], v[128:131]
bc 49,5,66,60
M 119
WL 2
R 50
M 120
mh 18
M 121
ad 66,1024
H 0
M 132
M 133
TX 58,0
TX 62,13824
M 122
DG 1024
M 123
ae 42,72,480
M 134
s_bfe_u32 s52, s50, 0x1000b
v_mad_i32_i24 v50, v49, s52, v48
ST 14,0
M 135
TY 66,512
TY 70,14336
M 124
bc 48,5,64,56
M 125
mh 21
M 136
ad 64,1024
H 0
M 137
TX 74,1536
TX 78,15360
M 126
WL 4
Q 0
M 127
Q 2
M 138
Q 4
M 139
TY 82,2048
TY 86,15872
v_mfma_f32_16x16x32_f16 v[224:227], v[88:91], a[16:19], v[128:131]
Q 6
M 129
WL 4
Q 8
v_mfma_f32_16x16x32_f16 v[248:251], v[88:91], a[24:27], v[128:131]
Q 10
ST 16,768
M 141
TX 90,3072
TX 94,16896
v_mfma_f32_16x16x32_f16 v[228:231], v[96:99], a[16:19], v[128:131]
Q 12
M 131
R 52
v_mfma_f32_16x16x32_f16 v[252:255], v[96:99], a[24:27], v[128:131]
Q 14
M 143
TY 98,3584
TY 102,17408
M 0
R 54
M 1
WL 10
Q 16
M 12
Q 18
M 13
Q 20
M 2
Q 22
M 3
WL 8
Q 24
M 14
Q 26
ST 18,1536
M 15
Q 28
M 4
Q 30
M 5
WL 6
Q 32
M 16
Q 34
M 17
Q 36
M 6
Q 38
M 7
WL 1
Q 40
M 18
Q 42
M 19
Q 44
M 8
Q 46
M 9
H 1
M 20
ST 20,2304
M 21
M 10
H 2
M 11
R 56
M 22
M 23
R 58
M 24
M 25
M 26
M 27
M 28
M 29
M 30
ae 42,72,480
ST 22,128
M 31
M 32
M 33
WV 10
M 34
WL 0
FP
M 35
M 36
M 37
M 38
M 39
M 40
M 41
M 42
ST 24,896
M 43
M 44
M 45
FR
M 46
M 47
M 48
M 49
M 50
M 51
M 52
M 53
WL 0
FQ 188
M 54
ST 26,1664
M 55
M 56
FZ 188
R 72
M 57
mw 24
R 74
M 58
TY 106,5128
TY 110,18952
M 59
R 76
M 60
ad 84,432
W 0
M 61
M 62
)"
R"(M 63
W 1
M 64
M 65
M 66
W 2
ST 28,2432
M 67
WL 3
AN 104,106
AN 105,107
M 68
SH 106
SH 107
M 69
mw 27
AN 106,106
AN 107,107
M 70
WL 2
AN 108,110
AN 109,111
M 71
R 78
M 72
SH 110
SH 111
M 73
AN 110,110
AN 111,111
M 74
TY 114,6664
TY 118,20488
M 75
ad 84,438
W 0
M 76
M 77
M 78
W 1
M 79
M 80
M 81
W 2
M 82
M 83
R 80
M 84
mw 48
WL 3
AN 112,114
AN 113,115
M 85
SH 114
SH 115
M 86
AN 114,114
AN 115,115
M 87
WL 2
AN 116,118
AN 117,119
M 88
SH 118
SH 119
M 89
AN 118,118
AN 119,119
M 90
TY 122,8200
TY 126,22024
M 91
ad 84,480
M 92
W 0
M 93
M 94
M 95
R 82
M 96
W 1
M 97
M 98
M 99
WL 3
AN 120,122
AN 121,123
M 100
SH 122
SH 123
M 101
AN 122,122
AN 123,123
M 102
WL 2
AN 124,126
AN 125,127
M 103
SH 126
SH 127
M 104
AN 126,126
AN 127,127
M 105
W 2
M 106
M 107
R 0
M 108
mw 51
M 109
ad 84,486
W 0
M 110
M 111
M 112
W 1
M 113
M 114
M 115
W 2
M 116
M 117
M 118
bc 49,6,66,60
M 119
WL 2
R 2
M 120
mh 0
M 121
ad 66,1216
H 0
M 132
M 133
TX 58,4608
TX 62,18432
M 122
DG 1024
M 123
M 134
M 135
TY 66,5120
TY 70,18944
M 124
bc 48,6,64,56
M 125
mh 3
ae 44,70,768
M 136
ad 64,1216
H 0
M 137
TX 74,6144
TX 78,19968
M 126
WL 4
Q 0
M 127
Q 2
M 138
Q 4
M 139
TY 82,6656
TY 86,20480
M 128
Q 6
M 129
WL 4
Q 8
M 140
Q 10
M 141
TX 90,7680
TX 94,21504
M 130
Q 12
M 131
R 4
M 142
Q 14
M 143
TY 98,8192
TY 102,22016
M 0
R 6
M 1
WL 10
Q 16
M 12
Q 18
M 13
Q 20
M 2
Q 22
M 3
WL 8
Q 24
M 14
Q 26
M 15
Q 28
M 4
Q 30
M 5
WL 6
Q 32
M 16
Q 34
M 17
Q 36
M 6
Q 38
M 7
WL 2
Q 40
M 18
Q 42
M 19
Q 44
M 8
Q 46
M 9
H 1
M 20
ae 42,72,384
M 21
s_bfe_u32 s52, s50, 0x10009
v_mad_i32_i24 v50, v49, s52, v48
SA 200,0
M 10
H 2
M 11
R 8
M 22
s_bitcmp1_b32 s50, 12
s_cselect_b32 s51, 24576, 0
M 23
R 10
PA 12288
DF
M 24
v_add_u32 v47, s51, v3
XL 30,0
M 25
XL 32,768
M 26
XL 34,1536
M 27
XL 36,2304
M 28
M 29
M 30
M 31
M 32
M 33
WV 11
M 34
WL 0
FP
M 35
M 36
M 37
M 38
M 39
M 40
M 41
M 42
M 43
M 44
M 45
FR
M 46
M 47
M 48
M 49
M 50
M 51
M 52
M 53
WL 0
FQ 192
M 54
M 55
M 56
FZ 192
R 24
M 57
mw 72
R 26
M 58
TY 106,9736
TY 110,23560
M 59
R 28
M 60
ad 84,528
W 0
M 61
M 62
M 63
W 1
M 64
M 65
M 66
W 2
M 67
WL 3
AN 104,106
AN 105,107
M 68
SH 106
SH 107
M 69
mw 75
AN 106,106
AN 107,107
M 70
WL 2
AN 108,110
AN 109,111
M 71
R 30
M 72
SH 110
SH 111
M 73
AN 110,110
AN 111,111
M 74
TY 114,11272
TY 118,25096
M 75
ad 84,534
W 0
M 76
M 77
M 78
W 1
M 79
M 80
M 81
W 2
M 82
M 83
R 32
M 84
mw 0
WL 3
AN 112,114
AN 113,115
M 85
SH 114
SH 115
M 86
AN 114,114
AN 115,115
M 87
WL 2
AN 116,118
AN 117,119
M 88
SH 118
SH 119
M 89
AN 118,118
AN 119,119
M 90
TY 122,12808
TY 126,26632
M 91
ad 84,576
M 92
W 0
SA 202,768
M 93
M 94
M 95
R 34
M 96
W 1
M 97
M 98
M 99
WL 3
AN 120,122
AN 121,123
M 100
SH 122
SH 123
M 101
AN 122,122
AN 123,123
M 102
WL 2
AN 124,126
AN 125,127
M 103
SH 126
SH 127
)"
R"(M 104
AN 126,126
AN 127,127
M 105
W 2
M 106
M 107
R 48
M 108
mw 3
M 109
ad 84,582
W 0
M 110
M 111
M 112
W 1
M 113
M 114
M 115
W 2
M 116
M 117
M 118
bc 49,7,66,60
M 119
WL 2
R 50
M 120
mh 9
M 121
ad 66,1408
H 0
M 132
M 133
TX 58,9216
TX 62,23040
M 122
DG 1024
M 123
M 134
M 135
TY 66,9728
TY 70,23552
M 124
bc 48,7,64,56
M 125
mh 12
M 136
ad 64,1408
H 0
M 137
TX 74,10752
TX 78,24576
M 126
WL 4
Q 0
M 127
Q 2
M 138
Q 4
M 139
TY 82,11264
TY 86,25088
M 128
Q 6
M 129
WL 4
Q 8
M 140
Q 10
M 141
TX 90,12288
TX 94,26112
M 130
Q 12
M 131
R 52
M 142
Q 14
M 143
TY 98,12800
TY 102,26624
M 0
R 54
M 1
WL 10
Q 16
M 12
Q 18
M 13
Q 20
M 2
Q 22
M 3
WL 8
Q 24
M 14
Q 26
M 15
Q 28
M 4
Q 30
M 5
WL 6
Q 32
M 16
Q 34
M 17
Q 36
M 6
Q 38
M 7
WL 2
Q 40
M 18
Q 42
M 19
Q 44
M 8
Q 46
M 9
H 1
SA 204,1536
M 20
ae 44,70,768
M 21
s_bfe_u32 s51, s50, 0x1000d
v_mad_i32_i24 v47, v49, s51, v48
XL 38,0
M 10
H 2
XL 40,768
M 11
R 56
M 22
XL 42,1536
M 23
R 58
M 24
XL 44,2304
M 25
M 26
M 27
M 28
M 29
M 30
M 31
M 32
M 33
WV 10
M 34
WL 0
FP
M 35
M 36
M 37
M 38
M 39
M 40
UL 6,30
UH 7,30
M 41
AR 8,36
AR 9,60
M 42
AD 8,6
AD 9,7
M 43
AW 36,8
AW 60,9
M 44
UL 6,31
UH 7,31
M 45
FR
AR 8,84
M 46
AR 9,108
AD 8,6
M 47
AD 9,7
AW 84,8
M 48
AW 108,9
UL 6,32
M 49
UH 7,32
AR 8,37
M 50
AR 9,61
AD 8,6
M 51
AD 9,7
AW 37,8
M 52
AW 61,9
UL 6,33
M 53
WL 0
FQ 196
UH 7,33
M 54
AR 8,85
AR 9,109
M 55
AD 8,6
AD 9,7
M 56
FZ 196
R 72
M 57
mw 24
R 74
M 58
TY 106,520
TY 110,14344
M 59
R 76
M 60
ad 84,624
W 0
M 61
AW 85,8
AW 109,9
M 62
UL 6,34
UH 7,34
M 63
W 1
AR 8,38
M 64
AR 9,62
AD 8,6
M 65
AD 9,7
AW 38,8
M 66
W 2
AW 62,9
M 67
WL 3
AN 104,106
AN 105,107
M 68
SH 106
SH 107
M 69
mw 27
AN 106,106
AN 107,107
M 70
WL 2
AN 108,110
AN 109,111
M 71
R 78
M 72
SH 110
SH 111
M 73
AN 110,110
AN 111,111
M 74
TY 114,2056
TY 118,15880
M 75
ad 84,630
W 0
M 76
UL 6,35
UH 7,35
M 77
AR 8,86
AR 9,110
M 78
W 1
AD 8,6
M 79
AD 9,7
AW 86,8
M 80
AW 110,9
UL 6,36
M 81
W 2
SA 206,2304
M 82
UH 7,36
AR 8,39
M 83
WL 1
R 80
M 84
mw 48
AN 112,114
AN 113,115
M 85
SH 114
SH 115
M 86
AN 114,114
AN 115,115
M 87
WL 2
AN 116,118
AN 117,119
M 88
SH 118
SH 119
M 89
AN 118,118
AN 119,119
M 90
TY 122,3592
TY 126,17416
M 91
ad 84,672
M 92
W 0
AR 9,63
M 93
AD 8,6
AD 9,7
M 94
AW 39,8
AW 63,9
M 95
R 82
M 96
W 1
UL 6,37
M 97
UH 7,37
AR 8,87
M 98
AR 9,111
AD 8,6
M 99
WL 3
AN 120,122
AN 121,123
M 100
SH 122
SH 123
M 101
AN 122,122
AN 123,123
M 102
WL 2
AN 124,126
AN 125,127
M 103
SH 126
SH 127
M 104
AN 126,126
AN 127,127
M 105
W 2
AD 9,7
M 106
AW 87,8
AW 111,9
M 107
R 0
M 108
mw 51
M 109
ad 84,678
W 0
M 110
M 111
M 112
W 1
M 113
M 114
M 115
W 2
M 116
M 117
M 118
bc 49,8,66,60
M 119
WL 2
R 2
M 120
mh 18
M 121
ad 66,1600
H 0
M 132
M 133
TX 58,0
TX 62,13824
M 122
DG 1024
M 123
M 134
M 135
)"
R"(TY 66,512
TY 70,14336
M 124
bc 48,8,64,56
M 125
mh 21
M 136
ad 64,1600
H 0
M 137
TX 74,1536
TX 78,15360
M 126
WL 4
Q 0
M 127
Q 2
M 138
Q 4
M 139
TY 82,2048
TY 86,15872
M 128
Q 6
M 129
WL 4
Q 8
M 140
Q 10
M 141
TX 90,3072
TX 94,16896
M 130
Q 12
M 131
R 4
M 142
Q 14
M 143
TY 98,3584
TY 102,17408
M 0
R 6
M 1
WL 10
Q 16
M 12
Q 18
M 13
Q 20
M 2
Q 22
M 3
WL 8
Q 24
M 14
Q 26
M 15
Q 28
M 4
Q 30
M 5
WL 6
Q 32
M 16
Q 34
M 17
Q 36
M 6
Q 38
M 7
WL 2
Q 40
M 18
Q 42
M 19
Q 44
M 8
Q 46
M 9
H 1
M 20
ae 42,72,384
M 21
SA 208,128
M 10
H 2
M 11
R 8
M 22
ae 44,68,768
M 23
R 10
PA 12288
DF
M 24
s_bitcmp1_b32 s50, 14
s_cselect_b32 s51, 24576, 0
v_add_u32 v47, s51, v3
M 25
XL 14,0
M 26
XL 16,768
M 27
XL 18,1536
M 28
XL 20,2304
M 29
M 30
M 31
M 32
M 33
WV 11
M 34
WL 0
FP
M 35
M 36
M 37
M 38
M 39
M 40
M 41
M 42
UL 6,38
UH 7,38
M 43
AR 8,40
AR 9,64
M 44
AD 8,6
AD 9,7
M 45
FR
AW 40,8
M 46
AW 64,9
UL 6,39
M 47
UH 7,39
AR 8,88
M 48
AR 9,112
AD 8,6
M 49
AD 9,7
AW 88,8
M 50
AW 112,9
UL 6,40
M 51
UH 7,40
AR 8,41
M 52
AR 9,65
AD 8,6
M 53
WL 0
FQ 200
AD 9,7
M 54
AW 41,8
AW 65,9
M 55
UL 6,41
UH 7,41
M 56
FZ 200
R 24
M 57
mw 72
R 26
M 58
TY 106,5128
TY 110,18952
M 59
R 28
M 60
ad 84,720
W 0
M 61
AR 8,89
AR 9,113
M 62
AD 8,6
AD 9,7
M 63
W 1
AW 89,8
M 64
AW 113,9
UL 6,42
M 65
UH 7,42
AR 8,42
M 66
W 2
AR 9,66
M 67
WL 3
AN 104,106
AN 105,107
M 68
SH 106
SH 107
M 69
mw 75
AN 106,106
AN 107,107
M 70
WL 2
AN 108,110
AN 109,111
M 71
R 30
M 72
SH 110
SH 111
M 73
AN 110,110
AN 111,111
M 74
TY 114,6664
TY 118,20488
M 75
ad 84,726
W 0
M 76
AD 8,6
AD 9,7
M 77
AW 42,8
AW 66,9
M 78
W 1
UL 6,43
M 79
UH 7,43
AR 8,90
M 80
AR 9,114
AD 8,6
M 81
W 2
AD 9,7
M 82
AW 90,8
AW 114,9
M 83
WL 1
R 32
M 84
mw 0
AN 112,114
AN 113,115
M 85
SH 114
SH 115
M 86
AN 114,114
AN 115,115
M 87
WL 2
AN 116,118
AN 117,119
M 88
SH 118
SH 119
M 89
AN 118,118
AN 119,119
M 90
TY 122,8200
TY 126,22024
M 91
ad 84,768
M 92
W 0
SA 210,896
M 93
UL 6,44
UH 7,44
M 94
AR 8,43
AR 9,67
M 95
R 34
M 96
W 1
AD 8,6
M 97
AD 9,7
AW 43,8
M 98
AW 67,9
UL 6,45
M 99
WL 3
AN 120,122
AN 121,123
M 100
SH 122
SH 123
M 101
AN 122,122
AN 123,123
M 102
WL 2
AN 124,126
AN 125,127
M 103
SH 126
SH 127
M 104
AN 126,126
AN 127,127
M 105
W 2
UH 7,45
M 106
AR 8,91
AR 9,115
M 107
R 48
M 108
mw 3
AD 8,6
AD 9,7
M 109
ad 84,774
W 0
M 110
AW 91,8
AW 115,9
M 111
M 112
W 1
M 113
M 114
M 115
W 2
M 116
M 117
M 118
bc 49,9,66,60
M 119
WL 2
R 50
M 120
mh 0
M 121
ad 66,1792
H 0
M 132
M 133
TX 58,4608
TX 62,18432
M 122
DG 1024
M 123
M 134
M 135
TY 66,5120
TY 70,18944
M 124
bc 48,9,64,56
M 125
mh 3
M 136
ad 64,1792
H 0
M 137
TX 74,6144
TX 78,19968
M 126
WL 4
Q 0
M 127
Q 2
M 138
Q 4
M 139
TY 82,6656
TY 86,20480
M 128
Q 6
M 129
WL 4
Q 8
M 140
Q 10
M 141
TX 90,7680
TX 94,21504
M 130
Q 12
M 131
R 52
M 142
Q 14
)"
R"(M 143
TY 98,8192
TY 102,22016
M 0
R 54
M 1
WL 10
Q 16
M 12
Q 18
M 13
Q 20
M 2
Q 22
M 3
WL 8
Q 24
M 14
Q 26
M 15
Q 28
M 4
Q 30
M 5
WL 6
Q 32
M 16
Q 34
M 17
Q 36
M 6
Q 38
M 7
WL 2
Q 40
M 18
Q 42
M 19
Q 44
M 8
Q 46
M 9
H 1
SA 212,1664
M 20
ae 44,68,768
M 21
s_bfe_u32 s51, s50, 0x1000f
v_mad_i32_i24 v47, v49, s51, v48
XL 22,0
M 10
H 2
XL 24,768
M 11
R 56
M 22
XL 26,1536
M 23
R 58
M 24
XL 28,2304
M 25
M 26
M 27
M 28
M 29
M 30
M 31
M 32
M 33
WV 10
M 34
WL 0
FP
M 35
M 36
M 37
M 38
M 39
M 40
M 41
M 42
M 43
M 44
UL 6,14
UH 7,14
M 45
FR
AR 8,44
M 46
AR 9,68
AD 8,6
M 47
AD 9,7
AW 44,8
M 48
AW 68,9
UL 6,15
M 49
UH 7,15
AR 8,92
M 50
AR 9,116
AD 8,6
M 51
AD 9,7
AW 92,8
M 52
AW 116,9
UL 6,16
M 53
WL 0
FQ 204
UH 7,16
M 54
AR 8,45
AR 9,69
M 55
AD 8,6
AD 9,7
M 56
FZ 204
R 72
M 57
mw 24
R 74
M 58
TY 106,9736
TY 110,23560
M 59
R 76
M 60
ad 84,816
W 0
M 61
AW 45,8
AW 69,9
M 62
UL 6,17
UH 7,17
M 63
W 1
AR 8,93
M 64
AR 9,117
AD 8,6
M 65
AD 9,7
AW 93,8
M 66
W 2
AW 117,9
M 67
WL 3
AN 104,106
AN 105,107
M 68
SH 106
SH 107
M 69
mw 27
AN 106,106
AN 107,107
M 70
WL 2
AN 108,110
AN 109,111
M 71
R 78
M 72
SH 110
SH 111
M 73
AN 110,110
AN 111,111
M 74
TY 114,11272
TY 118,25096
M 75
ad 84,822
W 0
M 76
UL 6,18
UH 7,18
M 77
AR 8,46
AR 9,70
M 78
W 1
AD 8,6
M 79
AD 9,7
AW 46,8
M 80
AW 70,9
UL 6,19
M 81
W 2
SA 214,2432
M 82
UH 7,19
AR 8,94
M 83
WL 1
R 80
M 84
mw 48
AN 112,114
AN 113,115
M 85
SH 114
SH 115
M 86
AN 114,114
AN 115,115
M 87
WL 2
AN 116,118
AN 117,119
M 88
SH 118
SH 119
M 89
AN 118,118
AN 119,119
M 90
TY 122,12808
TY 126,26632
M 91
ad 84,864
M 92
W 0
AR 9,118
M 93
AD 8,6
AD 9,7
M 94
AW 94,8
AW 118,9
M 95
R 82
M 96
W 1
UL 6,20
M 97
UH 7,20
AR 8,47
M 98
AR 9,71
AD 8,6
M 99
WL 3
AN 120,122
AN 121,123
M 100
SH 122
SH 123
M 101
AN 122,122
AN 123,123
M 102
WL 2
AN 124,126
AN 125,127
M 103
SH 126
SH 127
M 104
AN 126,126
AN 127,127
M 105
W 2
AD 9,7
M 106
AW 47,8
AW 71,9
M 107
R 0
M 108
mw 51
UL 6,21
UH 7,21
M 109
ad 84,870
W 0
M 110
AR 8,95
AR 9,119
M 111
AD 8,6
AD 9,7
M 112
W 1
AW 95,8
M 113
AW 119,9
M 114
M 115
W 2
M 116
M 117
M 118
bc 49,10,66,60
M 119
WL 2
R 2
M 120
mh 9
M 121
ad 66,1984
H 0
M 132
M 133
TX 58,9216
TX 62,23040
M 122
DG 1024
M 123
M 134
M 135
TY 66,9728
TY 70,23552
M 124
bc 48,10,64,56
M 125
mh 12
M 136
ad 64,1984
H 0
M 137
TX 74,10752
TX 78,24576
M 126
WL 4
Q 0
M 127
Q 2
M 138
Q 4
M 139
TY 82,11264
TY 86,25088
M 128
Q 6
M 129
WL 4
Q 8
M 140
Q 10
M 141
TX 90,12288
TX 94,26112
M 130
Q 12
M 131
R 4
M 142
Q 14
M 143
TY 98,12800
TY 102,26624
M 0
R 6
M 1
WL 10
Q 16
M 12
Q 18
M 13
Q 20
M 2
Q 22
M 3
WL 8
Q 24
M 14
Q 26
M 15
Q 28
M 4
Q 30
M 5
WL 6
Q 32
M 16
Q 34
M 17
Q 36
M 6
Q 38
M 7
WL 2
Q 40
M 18
Q 42
M 19
Q 44
M 8
Q 46
M 9
)"
R"(H 1
M 20
ae 42,72,384
M 21
s_bitcmp1_b32 s50, 8
s_cselect_b32 s52, 24576, 0
v_add_u32 v50, s52, v3
M 10
H 2
SA 216,256
M 11
R 8
M 22
ae 44,68,864
M 23
R 10
PA 12288
DF
M 24
s_bitcmp1_b32 s50, 16
s_cselect_b32 s51, 24576, 0
v_add_u32 v47, s51, v3
M 25
XL 30,0
M 26
XL 32,768
M 27
XL 34,1536
M 28
XL 36,2304
M 29
M 30
M 31
M 32
M 33
WV 11
M 34
WL 0
FP
M 35
M 36
M 37
M 38
M 39
M 40
M 41
M 42
M 43
M 44
M 45
FR
M 46
UL 6,22
UH 7,22
M 47
AR 8,48
AR 9,72
M 48
AD 8,6
AD 9,7
M 49
AW 48,8
AW 72,9
M 50
UL 6,23
UH 7,23
M 51
AR 8,96
AR 9,120
M 52
AD 8,6
AD 9,7
M 53
WL 0
FQ 208
AW 96,8
M 54
AW 120,9
UL 6,24
M 55
UH 7,24
AR 8,49
M 56
FZ 208
R 24
M 57
mw 72
R 26
M 58
TY 106,520
TY 110,14344
M 59
R 28
M 60
ad 84,912
W 0
M 61
AR 9,73
AD 8,6
M 62
AD 9,7
AW 49,8
M 63
W 1
AW 73,9
M 64
UL 6,25
UH 7,25
M 65
AR 8,97
AR 9,121
M 66
W 2
AD 8,6
M 67
WL 3
AN 104,106
AN 105,107
M 68
SH 106
SH 107
M 69
mw 75
AN 106,106
AN 107,107
M 70
WL 2
AN 108,110
AN 109,111
M 71
R 30
M 72
SH 110
SH 111
M 73
AN 110,110
AN 111,111
M 74
TY 114,2056
TY 118,15880
M 75
ad 84,918
W 0
M 76
AD 9,7
AW 97,8
M 77
AW 121,9
UL 6,26
M 78
W 1
UH 7,26
M 79
AR 8,50
AR 9,74
M 80
AD 8,6
AD 9,7
M 81
W 2
AW 50,8
M 82
AW 74,9
UL 6,27
M 83
WL 1
R 32
M 84
mw 0
AN 112,114
AN 113,115
M 85
SH 114
SH 115
M 86
AN 114,114
AN 115,115
M 87
WL 2
AN 116,118
AN 117,119
M 88
SH 118
SH 119
M 89
AN 118,118
AN 119,119
M 90
TY 122,3592
TY 126,17416
M 91
ad 84,960
M 92
W 0
SA 218,1024
M 93
UH 7,27
AR 8,98
M 94
AR 9,122
AD 8,6
M 95
R 34
M 96
W 1
AD 9,7
M 97
AW 98,8
AW 122,9
M 98
UL 6,28
UH 7,28
M 99
WL 3
AN 120,122
AN 121,123
M 100
SH 122
SH 123
M 101
AN 122,122
AN 123,123
M 102
WL 2
AN 124,126
AN 125,127
M 103
SH 126
SH 127
M 104
AN 126,126
AN 127,127
M 105
W 2
AR 8,51
M 106
AR 9,75
AD 8,6
M 107
R 48
M 108
mw 3
AD 9,7
AW 51,8
M 109
ad 84,966
W 0
M 110
AW 75,9
UL 6,29
M 111
UH 7,29
AR 8,99
M 112
W 1
AR 9,123
M 113
AD 8,6
AD 9,7
M 114
AW 99,8
AW 123,9
M 115
W 2
M 116
M 117
M 118
bc 49,11,66,60
M 119
WL 2
R 50
M 120
mh 18
M 121
ad 66,2176
H 0
M 132
M 133
TX 58,0
TX 62,13824
M 122
DG 1024
M 123
M 134
M 135
TY 66,512
TY 70,14336
M 124
bc 48,11,64,56
M 125
mh 21
M 136
ad 64,2176
H 0
M 137
TX 74,1536
TX 78,15360
M 126
WL 4
Q 0
M 127
Q 2
M 138
Q 4
M 139
TY 82,2048
TY 86,15872
M 128
Q 6
M 129
WL 4
Q 8
M 140
Q 10
M 141
TX 90,3072
TX 94,16896
M 130
Q 12
M 131
R 52
M 142
Q 14
M 143
TY 98,3584
TY 102,17408
M 0
R 54
M 1
WL 10
Q 16
M 12
Q 18
M 13
Q 20
M 2
Q 22
M 3
WL 8
Q 24
M 14
Q 26
M 15
Q 28
M 4
Q 30
M 5
WL 6
Q 32
M 16
Q 34
M 17
Q 36
M 6
Q 38
M 7
WL 2
Q 40
M 18
Q 42
M 19
Q 44
M 8
Q 46
M 9
H 1
SA 220,1792
M 20
ae 44,68,864
M 21
s_bfe_u32 s51, s50, 0x10011
v_mad_i32_i24 v47, v49, s51, v48
XL 38,0
M 10
H 2
XL 40,768
M 11
R 56
M 22
XL 42,1536
M 23
R 58
M 24
XL 44,2304
M 25
M 26
M 27
M 28
M 29
M 30
M 31
M 32
M 33
WV 10
M 34
WL 0
FP
M 35
M 36
M 37
)"
R"(M 38
M 39
M 40
M 41
M 42
M 43
M 44
M 45
FR
M 46
M 47
M 48
UL 6,30
UH 7,30
M 49
AR 8,52
AR 9,76
M 50
AD 8,6
AD 9,7
M 51
AW 52,8
AW 76,9
M 52
UL 6,31
UH 7,31
M 53
WL 0
FQ 212
AR 8,100
M 54
AR 9,124
AD 8,6
M 55
AD 9,7
AW 100,8
M 56
FZ 212
R 72
M 57
mw 24
R 74
M 58
TY 106,5128
TY 110,18952
M 59
R 76
M 60
ad 84,1008
W 0
M 61
AW 124,9
UL 6,32
M 62
UH 7,32
AR 8,53
M 63
W 1
AR 9,77
M 64
AD 8,6
AD 9,7
M 65
AW 53,8
AW 77,9
M 66
W 2
UL 6,33
M 67
WL 3
AN 104,106
AN 105,107
M 68
SH 106
SH 107
M 69
mw 27
AN 106,106
AN 107,107
M 70
WL 2
AN 108,110
AN 109,111
M 71
R 78
M 72
SH 110
SH 111
M 73
AN 110,110
AN 111,111
M 74
TY 114,6664
TY 118,20488
M 75
ad 84,1014
W 0
M 76
UH 7,33
AR 8,101
M 77
AR 9,125
AD 8,6
M 78
W 1
AD 9,7
M 79
AW 101,8
AW 125,9
M 80
UL 6,34
UH 7,34
M 81
W 2
SA 222,2560
M 82
AR 8,54
AR 9,78
M 83
WL 1
R 80
M 84
mw 48
AN 112,114
AN 113,115
M 85
SH 114
SH 115
M 86
AN 114,114
AN 115,115
M 87
WL 2
AN 116,118
AN 117,119
M 88
SH 118
SH 119
M 89
AN 118,118
AN 119,119
M 90
TY 122,8200
TY 126,22024
M 91
ad 84,1056
M 92
W 0
AD 8,6
M 93
AD 9,7
AW 54,8
M 94
AW 78,9
UL 6,35
M 95
R 82
M 96
W 1
UH 7,35
M 97
AR 8,102
AR 9,126
M 98
AD 8,6
AD 9,7
M 99
WL 3
AN 120,122
AN 121,123
M 100
SH 122
SH 123
M 101
AN 122,122
AN 123,123
M 102
WL 2
AN 124,126
AN 125,127
M 103
SH 126
SH 127
M 104
AN 126,126
AN 127,127
M 105
W 2
AW 102,8
M 106
AW 126,9
UL 6,36
M 107
R 0
M 108
mw 51
UH 7,36
AR 8,55
M 109
ad 84,1062
W 0
M 110
AR 9,79
AD 8,6
M 111
AD 9,7
AW 55,8
M 112
W 1
AW 79,9
M 113
UL 6,37
UH 7,37
M 114
AR 8,103
AR 9,127
M 115
W 2
AD 8,6
M 116
AD 9,7
AW 103,8
M 117
AW 127,9
M 118
bc 49,12,66,60
M 119
WL 1
R 2
M 120
mh 0
M 121
ad 66,2368
H 0
M 132
M 133
TX 58,4608
TX 62,18432
M 122
DG 1024
M 123
M 134
M 135
TY 66,5120
TY 70,18944
M 124
bc 48,12,64,56
M 125
mh 3
M 136
ad 64,2368
H 0
M 137
TX 74,6144
TX 78,19968
M 126
WL 4
Q 0
M 127
Q 2
M 138
Q 4
M 139
TY 82,6656
TY 86,20480
M 128
Q 6
M 129
WL 4
Q 8
M 140
Q 10
M 141
TX 90,7680
TX 94,21504
M 130
Q 12
M 131
R 4
M 142
Q 14
M 143
TY 98,8192
TY 102,22016
M 0
R 6
M 1
WL 10
Q 16
M 12
Q 18
M 13
Q 20
M 2
Q 22
M 3
WL 8
Q 24
M 14
Q 26
M 15
Q 28
M 4
Q 30
M 5
WL 6
Q 32
M 16
Q 34
M 17
Q 36
M 6
Q 38
M 7
WL 2
Q 40
M 18
Q 42
M 19
Q 44
M 8
Q 46
M 9
H 1
M 20
ae 42,72,480
M 21
s_bitcmp1_b32 s50, 10
s_cselect_b32 s52, 24576, 0
v_add_u32 v50, s52, v3
M 10
H 2
SA 224,0
M 11
R 8
M 22
M 23
R 10
PA 12288
DF
M 24
M 25
M 26
M 27
M 28
M 29
M 30
M 31
M 32
ae 44,70,768
M 33
WV 7
M 34
WL 0
FP
M 35
M 36
s_bitcmp1_b32 s50, 12
s_cselect_b32 s51, 24576, 0
v_add_u32 v47, s51, v3
M 37
XL 14,128
M 38
XL 16,896
M 39
M 40
M 41
M 42
M 43
M 44
XL 18,1664
M 45
FR
M 46
M 47
M 48
M 49
M 50
XL 20,2432
UL 6,38
M 51
UH 7,38
AR 8,56
M 52
AR 9,80
AD 8,6
M 53
WL 0
FQ 216
AD 9,7
M 54
AW 56,8
AW 80,9
M 55
UL 6,39
UH 7,39
M 56
FZ 216
R 24
M 57
)"
R"(mw 72
R 26
M 58
TY 106,9736
TY 110,23560
M 59
R 28
M 60
ad 84,1104
W 0
M 61
AR 8,104
AR 9,128
M 62
AD 8,6
AD 9,7
M 63
W 1
AW 104,8
M 64
AW 128,9
UL 6,40
M 65
UH 7,40
AR 8,57
M 66
W 2
AR 9,81
M 67
WL 3
AN 104,106
AN 105,107
M 68
SH 106
SH 107
M 69
mw 75
AN 106,106
AN 107,107
M 70
WL 2
AN 108,110
AN 109,111
M 71
R 30
M 72
SH 110
SH 111
M 73
AN 110,110
AN 111,111
M 74
TY 114,11272
TY 118,25096
M 75
ad 84,1110
W 0
M 76
AD 8,6
AD 9,7
M 77
AW 57,8
AW 81,9
M 78
W 1
UL 6,41
M 79
UH 7,41
AR 8,105
M 80
AR 9,129
AD 8,6
M 81
W 2
AD 9,7
M 82
AW 105,8
AW 129,9
M 83
WL 1
R 32
M 84
mw 0
AN 112,114
AN 113,115
M 85
SH 114
SH 115
M 86
AN 114,114
AN 115,115
M 87
WL 2
AN 116,118
AN 117,119
M 88
SH 118
SH 119
M 89
AN 118,118
AN 119,119
M 90
TY 122,12808
TY 126,26632
M 91
ad 84,1152
M 92
W 0
SA 226,768
M 93
UL 6,42
UH 7,42
M 94
AR 8,58
AR 9,82
M 95
R 34
M 96
W 1
AD 8,6
M 97
AD 9,7
AW 58,8
M 98
AW 82,9
UL 6,43
M 99
WL 3
AN 120,122
AN 121,123
M 100
SH 122
SH 123
M 101
AN 122,122
AN 123,123
M 102
WL 2
AN 124,126
AN 125,127
M 103
SH 126
SH 127
M 104
AN 126,126
AN 127,127
M 105
W 2
UH 7,43
M 106
AR 8,106
AR 9,130
M 107
R 48
M 108
mw 3
AD 8,6
AD 9,7
M 109
ad 84,1158
W 0
M 110
AW 106,8
AW 130,9
M 111
UL 6,44
UH 7,44
M 112
W 1
AR 8,59
M 113
AR 9,83
AD 8,6
M 114
AD 9,7
AW 59,8
M 115
W 2
AW 83,9
M 116
UL 6,45
UH 7,45
M 117
AR 8,107
AR 9,131
M 118
bc 49,13,66,60
AD 8,6
M 119
WL 0
R 50
M 120
mh 9
AD 9,7
AW 107,8
M 121
ad 66,2560
H 0
M 132
AW 131,9
M 133
TX 58,9216
TX 62,23040
M 122
DG 1024
M 123
M 134
M 135
TY 66,9728
TY 70,23552
M 124
bc 48,13,64,56
M 125
mh 12
M 136
ad 64,2560
H 0
M 137
TX 74,10752
TX 78,24576
M 126
WL 4
Q 0
M 127
Q 2
M 138
Q 4
M 139
TY 82,11264
TY 86,25088
M 128
Q 6
M 129
WL 4
Q 8
M 140
Q 10
M 141
TX 90,12288
TX 94,26112
M 130
Q 12
M 131
R 52
M 142
Q 14
M 143
TY 98,12800
TY 102,26624
M 0
R 54
M 1
WL 10
Q 16
M 12
Q 18
M 13
Q 20
M 2
Q 22
M 3
WL 8
Q 24
M 14
Q 26
M 15
Q 28
M 4
Q 30
M 5
WL 6
Q 32
M 16
Q 34
M 17
Q 36
M 6
Q 38
M 7
WL 2
Q 40
M 18
Q 42
M 19
Q 44
M 8
Q 46
M 9
H 1
SA 228,1536
M 20
M 21
M 10
H 2
M 11
R 56
M 22
M 23
R 58
M 24
M 25
M 26
M 27
M 28
M 29
M 30
M 31
M 32
M 33
WV 6
M 34
WL 0
FP
M 35
M 36
ae 44,70,768
M 37
s_bfe_u32 s51, s50, 0x1000d
v_mad_i32_i24 v47, v49, s51, v48
XL 22,128
M 38
M 39
M 40
XL 24,896
M 41
M 42
M 43
M 44
M 45
FR
M 46
XL 26,1664
M 47
M 48
M 49
M 50
M 51
M 52
XL 28,2432
M 53
WL 0
FQ 220
M 54
M 55
M 56
FZ 220
R 72
M 57
mw 24
R 74
M 58
TY 106,520
TY 110,14344
M 59
R 76
M 60
ad 84,1200
W 0
M 61
M 62
M 63
W 1
M 64
M 65
M 66
W 2
M 67
WL 3
AN 104,106
AN 105,107
M 68
SH 106
SH 107
M 69
mw 27
AN 106,106
AN 107,107
M 70
WL 2
AN 108,110
AN 109,111
M 71
R 78
M 72
SH 110
SH 111
M 73
AN 110,110
AN 111,111
M 74
TY 114,2056
TY 118,15880
M 75
ad 84,1206
W 0
M 76
M 77
M 78
W 1
M 79
)"
R"(M 80
M 81
W 2
SA 230,2304
M 82
M 83
R 80
M 84
mw 48
WL 3
AN 112,114
AN 113,115
M 85
SH 114
SH 115
M 86
AN 114,114
AN 115,115
M 87
WL 2
AN 116,118
AN 117,119
M 88
SH 118
SH 119
M 89
AN 118,118
AN 119,119
M 90
TY 122,3592
TY 126,17416
M 91
ad 84,1248
M 92
W 0
UL 6,14
M 93
UH 7,14
AR 8,132
M 94
AR 9,156
AD 8,6
M 95
R 82
M 96
W 1
AD 9,7
M 97
AW 132,8
AW 156,9
M 98
UL 6,15
UH 7,15
M 99
WL 3
AN 120,122
AN 121,123
M 100
SH 122
SH 123
M 101
AN 122,122
AN 123,123
M 102
WL 2
AN 124,126
AN 125,127
M 103
SH 126
SH 127
M 104
AN 126,126
AN 127,127
M 105
W 2
AR 8,180
M 106
AD 8,6
AD 136,7
M 107
R 0
M 108
mw 51
AW 180,8
UL 6,16
M 109
ad 84,1254
W 0
M 110
UH 7,16
AR 8,133
M 111
AR 9,157
AD 8,6
M 112
W 1
AD 9,7
M 113
AW 133,8
AW 157,9
M 114
UL 6,17
UH 7,17
M 115
W 2
AR 8,181
M 116
AD 8,6
AD 137,7
M 117
AW 181,8
UL 6,18
M 118
bc 49,14,66,60
UH 7,18
M 119
WL 0
R 2
M 120
mh 18
AR 8,134
AR 9,158
M 121
ad 66,2752
H 0
M 132
AD 8,6
AD 9,7
M 133
TX 58,0
TX 62,13824
M 122
DG 1024
M 123
AW 134,8
AW 158,9
M 134
UL 6,19
UH 7,19
M 135
TY 66,512
TY 70,14336
M 124
bc 48,14,64,56
AR 8,182
M 125
mh 21
AD 8,6
AD 138,7
M 136
ad 64,2752
H 0
M 137
TX 74,1536
TX 78,15360
M 126
WL 4
Q 0
M 127
Q 2
M 138
Q 4
M 139
TY 82,2048
TY 86,15872
M 128
Q 6
M 129
WL 4
Q 8
M 140
Q 10
M 141
TX 90,3072
TX 94,16896
M 130
Q 12
M 131
R 4
M 142
Q 14
M 143
TY 98,3584
TY 102,17408
M 0
R 6
M 1
WL 10
Q 16
M 12
Q 18
M 13
Q 20
M 2
Q 22
M 3
WL 8
Q 24
M 14
Q 26
M 15
Q 28
M 4
Q 30
M 5
WL 6
Q 32
M 16
Q 34
M 17
Q 36
M 6
Q 38
M 7
WL 2
Q 40
M 18
Q 42
M 19
Q 44
M 8
Q 46
M 9
H 1
AW 182,8
M 20
ae 42,72,480
M 21
SA 232,128
UL 6,20
M 10
H 2
UH 7,20
M 11
R 8
M 22
AR 8,135
AR 9,159
M 23
R 10
PA 12288
DF
M 24
AD 8,6
AD 9,7
M 25
AW 135,8
AW 159,9
M 26
UL 6,21
UH 7,21
M 27
AR 8,183
AD 8,6
M 28
AD 139,7
AW 183,8
M 29
M 30
M 31
M 32
M 33
WV 7
M 34
WL 0
FP
M 35
M 36
ae 44,68,768
M 37
s_bitcmp1_b32 s50, 14
s_cselect_b32 s51, 24576, 0
v_add_u32 v47, s51, v3
M 38
XL 30,128
M 39
M 40
M 41
M 42
XL 32,896
M 43
M 44
M 45
FR
M 46
M 47
M 48
XL 34,1664
M 49
M 50
M 51
M 52
M 53
WL 0
FQ 224
M 54
XL 36,2432
M 55
M 56
FZ 224
R 24
M 57
mw 72
R 26
M 58
TY 106,5128
TY 110,18952
M 59
R 28
M 60
ad 84,1296
W 0
M 61
M 62
M 63
W 1
M 64
M 65
M 66
W 2
M 67
WL 3
AN 104,106
AN 105,107
M 68
SH 106
SH 107
M 69
mw 75
AN 106,106
AN 107,107
M 70
WL 2
AN 108,110
AN 109,111
M 71
R 30
M 72
SH 110
SH 111
M 73
AN 110,110
AN 111,111
M 74
TY 114,6664
TY 118,20488
M 75
ad 84,1302
W 0
M 76
M 77
M 78
W 1
M 79
M 80
M 81
W 2
M 82
M 83
R 32
M 84
mw 0
WL 3
AN 112,114
AN 113,115
M 85
SH 114
SH 115
M 86
AN 114,114
AN 115,115
M 87
WL 2
AN 116,118
AN 117,119
M 88
SH 118
SH 119
M 89
AN 118,118
AN 119,119
M 90
TY 122,8200
TY 126,22024
M 91
ad 84,1344
M 92
W 0
SA 234,896
M 93
UL 6,22
UH 7,22
M 94
AR 8,136
AR 9,160
M 95
R 34
M 96
W 1
AD 8,6
)"
R"(M 97
AD 9,7
AW 136,8
M 98
AW 160,9
UL 6,23
M 99
WL 3
AN 120,122
AN 121,123
M 100
SH 122
SH 123
M 101
AN 122,122
AN 123,123
M 102
WL 2
AN 124,126
AN 125,127
M 103
SH 126
SH 127
M 104
AN 126,126
AN 127,127
M 105
W 2
UH 7,23
M 106
AR 8,184
AD 8,6
M 107
R 48
M 108
mw 3
AD 140,7
AW 184,8
M 109
ad 84,1350
W 0
M 110
UL 6,24
UH 7,24
M 111
AR 8,137
AR 9,161
M 112
W 1
AD 8,6
M 113
AD 9,7
AW 137,8
M 114
AW 161,9
UL 6,25
M 115
W 2
UH 7,25
M 116
AR 8,185
AD 8,6
M 117
AD 141,7
AW 185,8
M 118
bc 49,15,66,60
UL 6,26
M 119
WL 0
R 50
M 120
mh 0
UH 7,26
AR 8,138
M 121
ad 66,2944
H 0
M 132
AR 9,162
AD 8,6
M 133
TX 58,4608
TX 62,18432
M 122
DG 1024
M 123
AD 9,7
AW 138,8
M 134
AW 162,9
UL 6,27
M 135
TY 66,5120
TY 70,18944
M 124
bc 48,15,64,56
UH 7,27
M 125
mh 3
AR 8,186
AD 8,6
M 136
ad 64,2944
H 0
M 137
TX 74,6144
TX 78,19968
M 126
WL 4
Q 0
M 127
Q 2
M 138
Q 4
M 139
TY 82,6656
TY 86,20480
M 128
Q 6
M 129
WL 4
Q 8
M 140
Q 10
M 141
TX 90,7680
TX 94,21504
M 130
Q 12
M 131
R 52
M 142
Q 14
M 143
TY 98,8192
TY 102,22016
M 0
R 54
M 1
WL 10
Q 16
M 12
Q 18
M 13
Q 20
M 2
Q 22
M 3
WL 8
Q 24
M 14
Q 26
M 15
Q 28
M 4
Q 30
M 5
WL 6
Q 32
M 16
Q 34
M 17
Q 36
M 6
Q 38
M 7
WL 2
Q 40
M 18
Q 42
M 19
Q 44
M 8
Q 46
M 9
H 1
SA 236,1664
M 20
AD 142,7
AW 186,8
M 21
UL 6,28
UH 7,28
M 10
H 2
AR 8,139
M 11
R 56
M 22
AR 9,163
AD 8,6
M 23
R 58
M 24
AD 9,7
AW 139,8
M 25
AW 163,9
UL 6,29
M 26
UH 7,29
AR 8,187
M 27
AD 8,6
AD 143,7
M 28
AW 187,8
M 29
M 30
M 31
M 32
M 33
WV 6
M 34
WL 0
FP
M 35
M 36
M 37
M 38
ae 44,68,768
M 39
s_bfe_u32 s51, s50, 0x1000f
v_mad_i32_i24 v47, v49, s51, v48
XL 38,128
M 40
M 41
M 42
M 43
M 44
XL 40,896
M 45
FR
M 46
M 47
M 48
M 49
M 50
XL 42,1664
M 51
M 52
M 53
WL 0
FQ 228
M 54
M 55
M 56
FZ 228
R 72
M 57
mw 24
R 74
M 58
TY 106,9736
TY 110,23560
M 59
R 76
M 60
ad 84,1392
W 0
M 61
XL 44,2432
M 62
M 63
W 1
M 64
M 65
M 66
W 2
M 67
WL 3
AN 104,106
AN 105,107
M 68
SH 106
SH 107
M 69
mw 27
AN 106,106
AN 107,107
M 70
WL 2
AN 108,110
AN 109,111
M 71
R 78
M 72
SH 110
SH 111
M 73
AN 110,110
AN 111,111
M 74
TY 114,11272
TY 118,25096
M 75
ad 84,1398
W 0
M 76
M 77
M 78
W 1
M 79
M 80
M 81
W 2
SA 238,2432
M 82
M 83
R 80
M 84
mw 48
WL 3
AN 112,114
AN 113,115
M 85
SH 114
SH 115
M 86
AN 114,114
AN 115,115
M 87
WL 2
AN 116,118
AN 117,119
M 88
SH 118
SH 119
M 89
AN 118,118
AN 119,119
M 90
TY 122,12808
TY 126,26632
M 91
ad 84,1440
M 92
W 0
UL 6,30
M 93
UH 7,30
AR 8,140
M 94
AR 9,164
AD 8,6
M 95
R 82
M 96
W 1
AD 9,7
M 97
AW 140,8
AW 164,9
M 98
UL 6,31
UH 7,31
M 99
WL 3
AN 120,122
AN 121,123
M 100
SH 122
SH 123
M 101
AN 122,122
AN 123,123
M 102
WL 2
AN 124,126
AN 125,127
M 103
SH 126
SH 127
M 104
AN 126,126
AN 127,127
M 105
W 2
AR 8,188
M 106
AD 8,6
AD 144,7
M 107
R 0
M 108
mw 51
AW 188,8
UL 6,32
M 109
ad 84,1446
W 0
M 110
UH 7,32
AR 8,141
M 111
AR 9,165
AD 8,6
M 112
W 1
)"
R"(AD 9,7
M 113
AW 141,8
AW 165,9
M 114
UL 6,33
UH 7,33
M 115
W 2
AR 8,189
M 116
AD 8,6
AD 145,7
M 117
AW 189,8
UL 6,34
M 118
bc 49,16,66,60
UH 7,34
M 119
WL 0
R 2
M 120
mh 9
AR 8,142
AR 9,166
M 121
ad 66,3136
H 0
M 132
AD 8,6
AD 9,7
M 133
TX 58,9216
TX 62,23040
M 122
DG 1024
M 123
AW 142,8
AW 166,9
M 134
UL 6,35
UH 7,35
M 135
TY 66,9728
TY 70,23552
M 124
bc 48,16,64,56
AR 8,190
M 125
mh 12
AD 8,6
AD 146,7
M 136
ad 64,3136
H 0
M 137
TX 74,10752
TX 78,24576
M 126
WL 4
Q 0
M 127
Q 2
M 138
Q 4
M 139
TY 82,11264
TY 86,25088
M 128
Q 6
M 129
WL 4
Q 8
M 140
Q 10
M 141
TX 90,12288
TX 94,26112
M 130
Q 12
M 131
R 4
M 142
Q 14
M 143
TY 98,12800
TY 102,26624
M 0
R 6
M 1
WL 10
Q 16
M 12
Q 18
M 13
Q 20
M 2
Q 22
M 3
WL 8
Q 24
M 14
Q 26
M 15
Q 28
M 4
Q 30
M 5
WL 6
Q 32
M 16
Q 34
M 17
Q 36
M 6
Q 38
M 7
WL 2
Q 40
M 18
Q 42
M 19
Q 44
M 8
Q 46
M 9
H 1
AW 190,8
M 20
ae 42,72,480
M 21
SA 240,256
UL 6,36
M 10
H 2
UH 7,36
M 11
R 8
M 22
AR 8,143
AR 9,167
M 23
R 10
PA 12288
DF
M 24
AD 8,6
AD 9,7
M 25
AW 143,8
AW 167,9
M 26
UL 6,37
UH 7,37
M 27
AR 8,191
AD 8,6
M 28
AD 147,7
AW 191,8
M 29
M 30
M 31
M 32
M 33
WV 7
M 34
WL 0
FP
M 35
M 36
M 37
M 38
M 39
M 40
ae 44,68,864
M 41
s_bitcmp1_b32 s50, 16
s_cselect_b32 s51, 24576, 0
v_add_u32 v47, s51, v3
M 42
XL 14,128
M 43
M 44
M 45
FR
M 46
XL 16,896
M 47
M 48
M 49
M 50
M 51
M 52
XL 18,1664
M 53
WL 0
FQ 232
M 54
M 55
M 56
FZ 232
R 24
M 57
mw 72
R 26
M 58
TY 106,520
TY 110,14344
M 59
R 28
M 60
ad 84,1488
W 0
M 61
XL 20,2432
M 62
M 63
W 1
M 64
M 65
M 66
W 2
M 67
WL 3
AN 104,106
AN 105,107
M 68
SH 106
SH 107
M 69
mw 75
AN 106,106
AN 107,107
M 70
WL 2
AN 108,110
AN 109,111
M 71
R 30
M 72
SH 110
SH 111
M 73
AN 110,110
AN 111,111
M 74
TY 114,2056
TY 118,15880
M 75
ad 84,1494
W 0
M 76
M 77
M 78
W 1
M 79
M 80
M 81
W 2
M 82
M 83
R 32
M 84
mw 0
WL 3
AN 112,114
AN 113,115
M 85
SH 114
SH 115
M 86
AN 114,114
AN 115,115
M 87
WL 2
AN 116,118
AN 117,119
M 88
SH 118
SH 119
M 89
AN 118,118
AN 119,119
M 90
TY 122,3592
TY 126,17416
M 91
ad 84,1536
M 92
W 0
SA 242,1024
M 93
M 94
UL 6,38
UH 7,38
M 95
R 34
M 96
W 1
AR 8,144
M 97
AR 9,168
AD 8,6
M 98
AD 9,7
AW 144,8
M 99
WL 3
AN 120,122
AN 121,123
M 100
SH 122
SH 123
M 101
AN 122,122
AN 123,123
M 102
WL 2
AN 124,126
AN 125,127
M 103
SH 126
SH 127
M 104
AN 126,126
AN 127,127
M 105
W 2
AW 168,9
M 106
UL 6,39
UH 7,39
M 107
R 48
M 108
mw 3
AR 8,192
AD 8,6
M 109
ad 84,1542
W 0
M 110
AD 148,7
AW 192,8
M 111
UL 6,40
UH 7,40
M 112
W 1
AR 8,145
M 113
AR 9,169
AD 8,6
M 114
AD 9,7
AW 145,8
M 115
W 2
AW 169,9
M 116
UL 6,41
UH 7,41
M 117
AR 8,193
AD 8,6
M 118
bc 49,17,66,60
AD 149,7
M 119
WL 0
R 50
M 120
mh 18
AW 193,8
UL 6,42
M 121
ad 66,3328
H 0
M 132
UH 7,42
AR 8,146
M 133
TX 58,0
TX 62,13824
M 122
DG 1024
M 123
AR 9,170
AD 8,6
M 134
AD 9,7
AW 146,8
M 135
TY 66,512
TY 70,14336
M 124
bc 48,17,64,56
AW 170,9
)"
R"(M 125
mh 21
UL 6,43
UH 7,43
M 136
ad 64,3328
H 0
M 137
TX 74,1536
TX 78,15360
M 126
WL 4
Q 0
M 127
Q 2
M 138
Q 4
M 139
TY 82,2048
TY 86,15872
M 128
Q 6
M 129
WL 4
Q 8
M 140
Q 10
M 141
TX 90,3072
TX 94,16896
M 130
Q 12
M 131
R 52
M 142
Q 14
M 143
TY 98,3584
TY 102,17408
M 0
R 54
M 1
WL 10
Q 16
M 12
Q 18
M 13
Q 20
M 2
Q 22
M 3
WL 8
Q 24
M 14
Q 26
M 15
Q 28
M 4
Q 30
M 5
WL 6
Q 32
M 16
Q 34
M 17
Q 36
M 6
Q 38
M 7
WL 2
Q 40
M 18
Q 42
M 19
Q 44
M 8
Q 46
M 9
H 1
SA 244,1792
M 20
AR 8,194
AD 8,6
M 21
AD 150,7
AW 194,8
M 10
H 2
UL 6,44
M 11
R 56
M 22
UH 7,44
AR 8,147
M 23
R 58
M 24
AR 9,171
AD 8,6
M 25
AD 9,7
AW 147,8
M 26
AW 171,9
UL 6,45
M 27
UH 7,45
AR 8,195
M 28
AD 8,6
AD 151,7
M 29
AW 195,8
M 30
M 31
M 32
M 33
WV 6
M 34
WL 0
FP
M 35
M 36
M 37
M 38
M 39
M 40
M 41
M 42
ae 44,68,864
M 43
s_bfe_u32 s51, s50, 0x10011
v_mad_i32_i24 v47, v49, s51, v48
XL 22,128
M 44
M 45
FR
M 46
M 47
M 48
XL 24,896
M 49
M 50
M 51
M 52
M 53
WL 0
FQ 236
M 54
XL 26,1664
M 55
M 56
FZ 236
R 72
M 57
mw 24
R 74
M 58
TY 106,5128
TY 110,18952
M 59
R 76
M 60
ad 84,1584
W 0
M 61
XL 28,2432
M 62
M 63
W 1
M 64
M 65
M 66
W 2
M 67
WL 3
AN 104,106
AN 105,107
M 68
SH 106
SH 107
M 69
mw 27
AN 106,106
AN 107,107
M 70
WL 2
AN 108,110
AN 109,111
M 71
R 78
M 72
SH 110
SH 111
M 73
AN 110,110
AN 111,111
M 74
TY 114,6664
TY 118,20488
M 75
ad 84,1590
W 0
M 76
M 77
M 78
W 1
M 79
M 80
M 81
W 2
SA 246,2560
M 82
M 83
R 80
M 84
mw 48
WL 3
AN 112,114
AN 113,115
M 85
SH 114
SH 115
M 86
AN 114,114
AN 115,115
M 87
WL 2
AN 116,118
AN 117,119
M 88
SH 118
SH 119
M 89
AN 118,118
AN 119,119
M 90
TY 122,8200
TY 126,22024
M 91
ad 84,1632
M 92
W 0
M 93
M 94
M 95
R 82
M 96
W 1
UL 6,14
M 97
UH 7,14
AR 8,148
M 98
AR 9,172
AD 8,6
M 99
WL 3
AN 120,122
AN 121,123
M 100
SH 122
SH 123
M 101
AN 122,122
AN 123,123
M 102
WL 2
AN 124,126
AN 125,127
M 103
SH 126
SH 127
M 104
AN 126,126
AN 127,127
M 105
W 2
AD 9,7
M 106
AW 148,8
AW 172,9
M 107
R 0
M 108
mw 51
UL 6,15
UH 7,15
M 109
ad 84,1638
W 0
M 110
AR 8,196
AD 8,6
M 111
AD 152,7
AW 196,8
M 112
W 1
UL 6,16
M 113
UH 7,16
AR 8,149
M 114
AR 9,173
AD 8,6
M 115
W 2
AD 9,7
M 116
AW 149,8
AW 173,9
M 117
UL 6,17
UH 7,17
M 118
bc 49,18,66,60
AR 8,197
M 119
WL 0
R 2
M 120
mh 0
AD 8,6
AD 153,7
M 121
ad 66,3520
H 0
M 132
AW 197,8
UL 6,18
M 133
TX 58,4608
TX 62,18432
M 122
DG 1024
M 123
UH 7,18
AR 8,150
M 134
AR 9,174
AD 8,6
M 135
TY 66,5120
TY 70,18944
M 124
bc 48,18,64,56
AD 9,7
M 125
mh 3
AW 150,8
AW 174,9
M 136
ad 64,3520
H 0
M 137
TX 74,6144
TX 78,19968
M 126
WL 4
Q 0
M 127
Q 2
M 138
Q 4
M 139
TY 82,6656
TY 86,20480
M 128
Q 6
M 129
WL 4
Q 8
M 140
Q 10
M 141
TX 90,7680
TX 94,21504
M 130
Q 12
M 131
R 4
M 142
Q 14
M 143
TY 98,8192
TY 102,22016
M 0
R 6
M 1
WL 10
Q 16
M 12
Q 18
M 13
Q 20
M 2
Q 22
M 3
WL 8
)"
R"(Q 24
M 14
Q 26
M 15
Q 28
M 4
Q 30
M 5
WL 6
Q 32
M 16
Q 34
M 17
Q 36
M 6
Q 38
M 7
WL 2
Q 40
M 18
Q 42
M 19
Q 44
M 8
Q 46
M 9
H 1
UL 6,19
M 20
ae 42,72,480
M 21
s_bfe_u32 s52, s50, 0x1000b
v_mad_i32_i24 v50, v49, s52, v48
SA 248,256
M 10
H 2
UH 7,19
M 11
R 8
M 22
AR 8,198
AD 8,6
M 23
R 10
PA 12288
DF
M 24
AD 154,7
AW 198,8
M 25
UL 6,20
UH 7,20
M 26
AR 8,151
AR 9,175
M 27
AD 8,6
AD 9,7
M 28
AW 151,8
AW 175,9
M 29
UL 6,21
UH 7,21
M 30
AR 8,199
AD 8,6
M 31
AD 155,7
AW 199,8
M 32
M 33
WV 7
M 34
WL 0
FP
M 35
M 36
M 37
M 38
M 39
M 40
M 41
M 42
M 43
M 44
M 45
FR
M 46
M 47
M 48
M 49
M 50
M 51
M 52
M 53
WL 0
FQ 240
M 54
M 55
M 56
FZ 240
R 24
M 57
mw 72
R 26
M 58
TY 106,9736
TY 110,23560
M 59
R 28
M 60
ad 84,1680
W 0
M 61
M 62
M 63
W 1
M 64
M 65
M 66
W 2
M 67
WL 3
AN 104,106
AN 105,107
M 68
SH 106
SH 107
M 69
mw 75
AN 106,106
AN 107,107
M 70
WL 2
AN 108,110
AN 109,111
M 71
R 30
M 72
SH 110
SH 111
M 73
AN 110,110
AN 111,111
M 74
TY 114,11272
TY 118,25096
M 75
ad 84,1686
W 0
M 76
ae 44,70,768
M 77
s_bitcmp1_b32 s50, 12
s_cselect_b32 s51, 24576, 0
v_add_u32 v47, s51, v3
M 78
W 1
XL 30,256
M 79
XL 32,1024
M 80
M 81
W 2
M 82
XL 34,1792
M 83
WL 1
R 32
M 84
mw 0
AN 112,114
AN 113,115
M 85
SH 114
SH 115
M 86
AN 114,114
AN 115,115
M 87
WL 2
AN 116,118
AN 117,119
M 88
SH 118
SH 119
M 89
AN 118,118
AN 119,119
M 90
TY 122,12808
TY 126,26632
M 91
ad 84,1728
M 92
W 0
SA 250,1024
M 93
XL 36,2560
M 94
M 95
R 34
M 96
W 1
M 97
M 98
UL 6,22
UH 7,22
M 99
WL 3
AN 120,122
AN 121,123
M 100
SH 122
SH 123
M 101
AN 122,122
AN 123,123
M 102
WL 2
AN 124,126
AN 125,127
M 103
SH 126
SH 127
M 104
AN 126,126
AN 127,127
M 105
W 2
AR 8,152
M 106
AR 9,176
AD 8,6
M 107
R 48
M 108
mw 3
AD 9,7
AW 152,8
M 109
ad 84,1734
W 0
M 110
AW 176,9
UL 6,23
M 111
UH 7,23
AD 132,6
M 112
W 1
AD 156,7
M 113
UL 6,24
UH 7,24
M 114
AR 8,153
AR 9,177
M 115
W 2
AD 8,6
M 116
AD 9,7
AW 153,8
M 117
AW 177,9
UL 6,25
M 118
bc 49,19,66,60
UH 7,25
M 119
WL 0
R 50
M 120
mh 9
AD 133,6
AD 157,7
M 121
ad 66,3712
H 0
M 132
UL 6,26
UH 7,26
M 133
TX 58,9216
TX 62,23040
M 122
DG 1024
M 123
AR 8,154
AR 9,178
M 134
AD 8,6
AD 9,7
M 135
TY 66,9728
TY 70,23552
M 124
bc 48,19,64,56
AW 154,8
M 125
mh 12
AW 178,9
UL 6,27
M 136
ad 64,3712
H 0
M 137
TX 74,10752
TX 78,24576
M 126
WL 4
Q 0
M 127
Q 2
M 138
Q 4
M 139
TY 82,11264
TY 86,25088
M 128
Q 6
M 129
WL 4
Q 8
M 140
Q 10
M 141
TX 90,12288
TX 94,26112
M 130
Q 12
M 131
R 52
M 142
Q 14
M 143
TY 98,12800
TY 102,26624
M 0
R 54
M 1
WL 10
Q 16
M 12
Q 18
M 13
Q 20
M 2
Q 22
M 3
WL 8
Q 24
M 14
Q 26
M 15
Q 28
M 4
Q 30
M 5
WL 6
Q 32
M 16
Q 34
M 17
Q 36
M 6
Q 38
M 7
WL 2
Q 40
M 18
Q 42
M 19
Q 44
M 8
Q 46
M 9
H 1
SA 252,1792
M 20
UH 7,27
AD 134,6
M 21
AD 158,7
UL 6,28
M 10
H 2
UH 7,28
M 11
R 56
M 22
AR 8,155
AR 9,179
M 23
R 58
M 24
AD 8,6
AD 9,7
)"
R"(M 25
AW 155,8
AW 179,9
M 26
UL 6,29
UH 7,29
M 27
AD 135,6
AD 159,7
M 28
M 29
M 30
M 31
M 32
M 33
WV 6
M 34
WL 0
FP
M 35
M 36
M 37
M 38
M 39
M 40
M 41
M 42
M 43
M 44
M 45
FR
M 46
M 47
M 48
M 49
M 50
M 51
M 52
M 53
WL 0
FQ 244
M 54
M 55
M 56
FZ 244
R 72
M 57
mw 24
R 74
M 58
TY 106,520
TY 110,14344
M 59
R 76
M 60
ad 84,1776
W 0
M 61
M 62
M 63
W 1
M 64
M 65
M 66
W 2
M 67
WL 3
AN 104,106
AN 105,107
M 68
SH 106
SH 107
M 69
mw 27
AN 106,106
AN 107,107
M 70
WL 2
AN 108,110
AN 109,111
M 71
R 78
M 72
SH 110
SH 111
M 73
AN 110,110
AN 111,111
M 74
TY 114,2056
TY 118,15880
M 75
ad 84,1782
W 0
M 76
ae 44,70,768
M 77
s_bfe_u32 s51, s50, 0x1000d
v_mad_i32_i24 v47, v49, s51, v48
XL 38,256
M 78
W 1
M 79
M 80
XL 40,1024
M 81
W 2
SA 254,2560
M 82
M 83
WL 1
R 80
M 84
mw 48
AN 112,114
AN 113,115
M 85
SH 114
SH 115
M 86
AN 114,114
AN 115,115
M 87
WL 2
AN 116,118
AN 117,119
M 88
SH 118
SH 119
M 89
AN 118,118
AN 119,119
M 90
TY 122,3592
TY 126,17416
M 91
ad 84,1824
M 92
W 0
XL 42,1792
M 93
XL 44,2560
M 94
M 95
R 82
M 96
W 1
M 97
M 98
M 99
WL 3
AN 120,122
AN 121,123
M 100
SH 122
SH 123
M 101
AN 122,122
AN 123,123
M 102
WL 2
AN 124,126
AN 125,127
M 103
SH 126
SH 127
M 104
AN 126,126
AN 127,127
M 105
W 2
M 106
M 107
R 0
M 108
mw 51
M 109
ad 84,1830
W 0
M 110
M 111
M 112
W 1
M 113
M 114
M 115
W 2
M 116
M 117
M 118
bc 49,20,66,60
M 119
WL 2
R 2
M 120
mh 18
M 121
ad 66,3904
H 0
M 132
M 133
TX 58,0
TX 62,13824
M 122
DG 1024
M 123
M 134
UL 6,30
UH 7,30
M 135
TY 66,512
TY 70,14336
M 124
bc 48,20,64,56
AD 160,6
M 125
mh 21
AD 184,7
UL 6,31
M 136
ad 64,3904
H 0
M 137
TX 74,1536
TX 78,15360
M 126
WL 4
Q 0
M 127
Q 2
M 138
Q 4
M 139
TY 82,2048
TY 86,15872
M 128
Q 6
M 129
WL 4
Q 8
M 140
Q 10
M 141
TX 90,3072
TX 94,16896
M 130
Q 12
M 131
R 4
M 142
Q 14
M 143
TY 98,3584
TY 102,17408
M 0
R 6
M 1
WL 10
Q 16
M 12
Q 18
M 13
Q 20
M 2
Q 22
M 3
WL 8
Q 24
M 14
Q 26
M 15
Q 28
M 4
Q 30
M 5
WL 6
Q 32
M 16
Q 34
M 17
Q 36
M 6
Q 38
M 7
WL 2
Q 40
M 18
Q 42
M 19
Q 44
M 8
Q 46
M 9
H 1
UH 7,31
M 20
AD 208,6
AD 232,7
M 21
UL 6,32
UH 7,32
M 10
H 2
AD 161,6
M 11
R 8
M 22
AD 185,7
UL 6,33
M 23
R 10
M 24
UH 7,33
AD 209,6
M 25
AD 233,7
UL 6,34
M 26
UH 7,34
AD 162,6
M 27
AD 186,7
UL 6,35
M 28
UH 7,35
AD 210,6
M 29
AD 234,7
UL 6,36
M 30
UH 7,36
AD 163,6
M 31
AD 187,7
UL 6,37
M 32
UH 7,37
AD 211,6
M 33
WV 5
AD 235,7
M 34
WL 0
FP
M 35
M 36
M 37
M 38
M 39
M 40
M 41
M 42
M 43
M 44
M 45
FR
M 46
M 47
M 48
M 49
M 50
M 51
M 52
M 53
WL 0
FQ 248
M 54
M 55
M 56
FZ 248
R 24
M 57
mw 72
R 26
M 58
TY 106,5128
TY 110,18952
M 59
R 28
M 60
ad 84,1872
W 0
M 61
M 62
M 63
W 1
M 64
M 65
M 66
W 2
M 67
WL 3
AN 104,106
AN 105,107
M 68
SH 106
SH 107
M 69
mw 75
AN 106,106
)"
R"(AN 107,107
M 70
WL 2
AN 108,110
AN 109,111
M 71
R 30
M 72
SH 110
SH 111
M 73
AN 110,110
AN 111,111
M 74
TY 114,6664
TY 118,20488
M 75
ad 84,1878
W 0
M 76
M 77
M 78
W 1
M 79
ae 44,68,768
M 80
s_bitcmp1_b32 s50, 14
s_cselect_b32 s51, 24576, 0
v_add_u32 v47, s51, v3
M 81
W 2
XL 14,256
M 82
M 83
WL 1
R 32
M 84
mw 0
AN 112,114
AN 113,115
M 85
SH 114
SH 115
M 86
AN 114,114
AN 115,115
M 87
WL 2
AN 116,118
AN 117,119
M 88
SH 118
SH 119
M 89
AN 118,118
AN 119,119
M 90
TY 122,8200
TY 126,22024
M 91
ad 84,1920
M 92
W 0
XL 16,1024
M 93
XL 18,1792
M 94
M 95
R 34
M 96
W 1
XL 20,2560
M 97
M 98
M 99
WL 3
AN 120,122
AN 121,123
M 100
SH 122
SH 123
M 101
AN 122,122
AN 123,123
M 102
WL 2
AN 124,126
AN 125,127
M 103
SH 126
SH 127
M 104
AN 126,126
AN 127,127
M 105
W 2
M 106
M 107
R 48
M 108
mw 3
M 109
ad 84,1926
W 0
M 110
M 111
M 112
W 1
M 113
M 114
M 115
W 2
M 116
M 117
M 118
bc 49,21,66,60
M 119
WL 2
R 50
M 120
mh 0
M 121
ad 66,4096
H 0
M 132
M 133
TX 58,4608
TX 62,18432
M 122
DG 1024
M 123
M 134
M 135
TY 66,5120
TY 70,18944
M 124
bc 48,21,64,56
M 125
mh 3
M 136
ad 64,4096
H 0
M 137
TX 74,6144
TX 78,19968
M 126
WL 4
Q 0
M 127
Q 2
M 138
Q 4
M 139
TY 82,6656
TY 86,20480
M 128
Q 6
M 129
WL 4
Q 8
M 140
Q 10
M 141
TX 90,7680
TX 94,21504
M 130
Q 12
M 131
R 52
M 142
Q 14
M 143
TY 98,8192
TY 102,22016
M 0
R 54
M 1
WL 10
Q 16
M 12
Q 18
M 13
Q 20
M 2
Q 22
M 3
WL 8
Q 24
M 14
Q 26
M 15
Q 28
M 4
Q 30
M 5
WL 6
Q 32
M 16
Q 34
M 17
Q 36
M 6
Q 38
M 7
WL 2
Q 40
M 18
Q 42
M 19
Q 44
M 8
Q 46
M 9
H 1
UL 6,38
M 20
UH 7,38
AD 164,6
M 21
AD 188,7
UL 6,39
M 10
H 2
UH 7,39
M 11
R 56
M 22
AD 212,6
AD 236,7
M 23
R 58
M 24
UL 6,40
UH 7,40
M 25
AD 165,6
AD 189,7
M 26
UL 6,41
UH 7,41
M 27
AD 213,6
AD 237,7
M 28
UL 6,42
UH 7,42
M 29
AD 166,6
AD 190,7
M 30
UL 6,43
UH 7,43
M 31
AD 214,6
AD 238,7
M 32
UL 6,44
UH 7,44
M 33
WV 5
AD 167,6
AD 191,7
M 34
WL 0
FP
UL 6,45
M 35
UH 7,45
AD 215,6
M 36
AD 239,7
M 37
M 38
M 39
M 40
M 41
M 42
M 43
M 44
M 45
FR
M 46
M 47
M 48
M 49
M 50
M 51
M 52
M 53
WL 0
FQ 252
M 54
M 55
M 56
FZ 252
R 72
M 57
mw 24
R 74
M 58
TY 106,9736
TY 110,23560
M 59
R 76
M 60
ad 84,1968
W 0
M 61
M 62
M 63
W 1
M 64
M 65
M 66
W 2
M 67
WL 3
AN 104,106
AN 105,107
M 68
SH 106
SH 107
M 69
mw 27
AN 106,106
AN 107,107
M 70
WL 2
AN 108,110
AN 109,111
M 71
R 78
M 72
SH 110
SH 111
M 73
AN 110,110
AN 111,111
M 74
TY 114,11272
TY 118,25096
M 75
ad 84,1974
W 0
M 76
M 77
M 78
W 1
M 79
M 80
M 81
W 2
M 82
ae 44,68,768
M 83
WL 1
R 80
M 84
mw 48
AN 112,114
AN 113,115
M 85
s_bfe_u32 s51, s50, 0x1000f
SH 114
SH 115
M 86
AN 114,114
AN 115,115
M 87
WL 2
AN 116,118
AN 117,119
M 88
SH 118
SH 119
M 89
AN 118,118
AN 119,119
M 90
TY 122,12808
TY 126,26632
M 91
ad 84,2016
M 92
W 0
v_mad_i32_i24 v47, v49, s51, v48
M 93
XL 22,256
M 94
XL 24,1024
M 95
R 82
M 96
W 1
XL 26,1792
M 97
M 98
M 99
WL 3
AN 120,122
)"
R"(AN 121,123
M 100
SH 122
SH 123
M 101
AN 122,122
AN 123,123
M 102
WL 2
AN 124,126
AN 125,127
M 103
SH 126
SH 127
M 104
AN 126,126
AN 127,127
M 105
W 2
XL 28,2560
M 106
M 107
R 0
M 108
mw 51
M 109
ad 84,2022
W 0
M 110
M 111
M 112
W 1
M 113
M 114
M 115
W 2
M 116
M 117
M 118
bc 49,22,66,60
M 119
WL 2
R 2
M 120
mh 9
M 121
ad 66,4288
H 0
M 132
M 133
TX 58,9216
TX 62,23040
M 122
DG 1024
M 123
M 134
M 135
TY 66,9728
TY 70,23552
M 124
bc 48,22,64,56
M 125
mh 12
M 136
ad 64,4288
H 0
M 137
TX 74,10752
TX 78,24576
M 126
WL 4
Q 0
M 127
Q 2
M 138
Q 4
M 139
TY 82,11264
TY 86,25088
M 128
Q 6
M 129
WL 4
Q 8
M 140
Q 10
M 141
TX 90,12288
TX 94,26112
M 130
Q 12
M 131
R 4
M 142
Q 14
M 143
TY 98,12800
TY 102,26624
M 0
R 6
M 1
WL 10
Q 16
M 12
Q 18
M 13
Q 20
M 2
Q 22
M 3
WL 8
Q 24
M 14
Q 26
M 15
Q 28
M 4
Q 30
M 5
WL 6
Q 32
M 16
Q 34
M 17
Q 36
M 6
Q 38
M 7
WL 2
Q 40
M 18
Q 42
M 19
Q 44
M 8
Q 46
M 9
H 1
UL 6,14
M 20
UH 7,14
AD 168,6
M 21
AD 192,7
UL 6,15
M 10
H 2
UH 7,15
M 11
R 8
M 22
AD 216,6
AD 240,7
M 23
R 10
M 24
UL 6,16
UH 7,16
M 25
AD 169,6
AD 193,7
M 26
UL 6,17
UH 7,17
M 27
AD 217,6
AD 241,7
M 28
UL 6,18
UH 7,18
M 29
AD 170,6
AD 194,7
M 30
UL 6,19
UH 7,19
M 31
AD 218,6
AD 242,7
M 32
UL 6,20
UH 7,20
M 33
WV 5
AD 171,6
AD 195,7
M 34
WL 0
FP
UL 6,21
M 35
UH 7,21
AD 219,6
M 36
AD 243,7
M 37
M 38
M 39
M 40
M 41
M 42
M 43
M 44
M 45
FR
M 46
M 47
M 48
M 49
M 50
M 51
M 52
M 53
WL 0
FQ 256
M 54
M 55
M 56
FZ 256
R 24
M 57
mw 72
R 26
M 58
TY 106,520
TY 110,14344
M 59
R 28
M 60
ad 84,2064
W 0
M 61
M 62
M 63
W 1
M 64
M 65
M 66
W 2
M 67
WL 3
AN 104,106
AN 105,107
M 68
SH 106
SH 107
M 69
mw 75
AN 106,106
AN 107,107
M 70
WL 2
AN 108,110
AN 109,111
M 71
R 30
M 72
SH 110
SH 111
M 73
AN 110,110
AN 111,111
M 74
TY 114,2056
TY 118,15880
M 75
ad 84,2070
W 0
M 76
M 77
M 78
W 1
M 79
M 80
M 81
W 2
M 82
M 83
R 32
M 84
mw 0
WL 3
AN 112,114
AN 113,115
M 85
SH 114
SH 115
M 86
AN 114,114
AN 115,115
M 87
WL 2
AN 116,118
AN 117,119
M 88
SH 118
SH 119
M 89
AN 118,118
AN 119,119
M 90
TY 122,3592
TY 126,17416
M 91
ad 84,2112
M 92
W 0
M 93
ae 44,68,864
M 94
s_bitcmp1_b32 s50, 16
s_cselect_b32 s51, 24576, 0
v_add_u32 v47, s51, v3
M 95
R 34
M 96
W 1
XL 30,256
M 97
XL 32,1024
M 98
XL 34,1792
M 99
WL 3
AN 120,122
AN 121,123
M 100
SH 122
SH 123
M 101
AN 122,122
AN 123,123
M 102
WL 2
AN 124,126
AN 125,127
M 103
SH 126
SH 127
M 104
AN 126,126
AN 127,127
M 105
W 2
XL 36,2560
M 106
M 107
R 48
M 108
mw 3
M 109
ad 84,2118
W 0
M 110
M 111
M 112
W 1
M 113
M 114
M 115
W 2
M 116
M 117
M 118
bc 49,23,66,60
M 119
WL 2
R 50
M 120
mh 18
M 121
ad 66,4480
H 0
M 132
M 133
TX 58,0
TX 62,13824
M 122
DG 1024
M 123
M 134
M 135
TY 66,512
TY 70,14336
M 124
bc 48,23,64,56
M 125
mh 21
M 136
ad 64,4480
H 0
M 137
TX 74,1536
TX 78,15360
M 126
WL 4
Q 0
M 127
Q 2
M 138
)"
R"(Q 4
M 139
TY 82,2048
TY 86,15872
M 128
Q 6
M 129
WL 4
Q 8
M 140
Q 10
M 141
TX 90,3072
TX 94,16896
M 130
Q 12
M 131
R 52
M 142
Q 14
M 143
TY 98,3584
TY 102,17408
M 0
R 54
M 1
WL 10
Q 16
M 12
Q 18
M 13
Q 20
M 2
Q 22
M 3
WL 8
Q 24
M 14
Q 26
M 15
Q 28
M 4
Q 30
M 5
WL 6
Q 32
M 16
Q 34
M 17
Q 36
M 6
Q 38
M 7
WL 2
Q 40
M 18
Q 42
M 19
Q 44
M 8
Q 46
M 9
H 1
UL 6,22
M 20
UH 7,22
AD 172,6
M 21
AD 196,7
UL 6,23
M 10
H 2
UH 7,23
M 11
R 56
M 22
AD 220,6
AD 244,7
M 23
R 58
M 24
UL 6,24
UH 7,24
M 25
AD 173,6
AD 197,7
M 26
UL 6,25
UH 7,25
M 27
AD 221,6
AD 245,7
M 28
UL 6,26
UH 7,26
M 29
AD 174,6
AD 198,7
M 30
UL 6,27
UH 7,27
M 31
AD 222,6
AD 246,7
M 32
UL 6,28
UH 7,28
M 33
WV 5
AD 175,6
AD 199,7
M 34
WL 0
FP
UL 6,29
M 35
UH 7,29
AD 223,6
M 36
AD 247,7
M 37
M 38
M 39
M 40
M 41
M 42
M 43
M 44
M 45
FR
M 46
M 47
M 48
M 49
M 50
M 51
M 52
M 53
WL 0
FQ 260
M 54
M 55
M 56
FZ 260
R 72
M 57
mw 24
R 74
M 58
TY 106,5128
TY 110,18952
M 59
R 76
M 60
ad 84,2160
W 0
M 61
M 62
M 63
W 1
M 64
M 65
M 66
W 2
M 67
WL 3
AN 104,106
AN 105,107
M 68
SH 106
SH 107
M 69
mw 27
AN 106,106
AN 107,107
M 70
WL 2
AN 108,110
AN 109,111
M 71
R 78
M 72
SH 110
SH 111
M 73
AN 110,110
AN 111,111
M 74
TY 114,6664
TY 118,20488
M 75
ad 84,2166
W 0
M 76
M 77
M 78
W 1
M 79
M 80
M 81
W 2
M 82
M 83
R 80
M 84
mw 48
WL 3
AN 112,114
AN 113,115
M 85
SH 114
SH 115
M 86
AN 114,114
AN 115,115
M 87
WL 2
AN 116,118
AN 117,119
M 88
SH 118
SH 119
M 89
AN 118,118
AN 119,119
M 90
TY 122,8200
TY 126,22024
M 91
ad 84,2208
M 92
W 0
M 93
ae 44,68,864
M 94
s_bfe_u32 s51, s50, 0x10011
v_mad_i32_i24 v47, v49, s51, v48
XL 38,256
M 95
R 82
M 96
W 1
XL 40,1024
M 97
M 98
M 99
WL 3
AN 120,122
AN 121,123
M 100
SH 122
SH 123
M 101
AN 122,122
AN 123,123
M 102
WL 2
AN 124,126
AN 125,127
M 103
SH 126
SH 127
M 104
AN 126,126
AN 127,127
M 105
W 2
XL 42,1792
M 106
M 107
R 0
M 108
mw 51
XL 44,2560
M 109
ad 84,2214
W 0
M 110
M 111
M 112
W 1
M 113
M 114
M 115
W 2
M 116
M 117
M 118
bc 48,0,64,56
M 119
WL 2
R 2
M 120
mh 3
M 121
ad 64,64
H 0
M 132
M 133
TX 58,4608
TX 62,18432
M 122
H 1
M 123
M 134
M 135
TY 66,5120
TY 70,18944
M 124
H 2
M 125
M 136
M 137
TX 74,6144
TX 78,19968
M 126
WL 4
Q 0
M 127
Q 2
M 138
Q 4
M 139
TY 82,6656
TY 86,20480
M 128
Q 6
M 129
WL 4
Q 8
M 140
Q 10
M 141
TX 90,7680
TX 94,21504
M 130
Q 12
M 131
R 4
M 142
Q 14
M 143
TY 98,8192
TY 102,22016
M 0
R 6
M 1
WL 10
Q 16
M 12
Q 18
M 13
Q 20
M 2
Q 22
M 3
WL 8
Q 24
M 14
Q 26
M 15
Q 28
M 4
Q 30
M 5
WL 6
Q 32
M 16
Q 34
M 17
Q 36
M 6
Q 38
M 7
WL 2
Q 40
M 18
Q 42
M 19
Q 44
M 8
Q 46
M 9
UL 6,30
UH 7,30
M 20
AD 176,6
AD 200,7
M 21
UL 6,31
UH 7,31
M 10
AD 224,6
AD 248,7
M 11
R 8
M 22
UL 6,32
UH 7,32
M 23
)"
R"(R 10
M 24
AD 177,6
AD 201,7
M 25
UL 6,33
UH 7,33
M 26
AD 225,6
AD 249,7
M 27
UL 6,34
UH 7,34
M 28
AD 178,6
AD 202,7
M 29
UL 6,35
UH 7,35
M 30
AD 226,6
AD 250,7
M 31
UL 6,36
UH 7,36
M 32
AD 179,6
AD 203,7
M 33
WV 3
UL 6,37
UH 7,37
M 34
WL 0
FP
AD 227,6
M 35
AD 251,7
M 36
M 37
M 38
M 39
M 40
M 41
M 42
M 43
M 44
M 45
FR
M 46
M 47
M 48
M 49
M 50
M 51
M 52
M 53
WL 0
FQ 264
M 54
M 55
M 56
FZ 264
R 24
M 57
mw 72
R 26
M 58
TY 106,9736
TY 110,23560
M 59
R 28
M 60
ad 84,2256
W 0
M 61
M 62
M 63
W 1
M 64
M 65
M 66
W 2
M 67
WL 3
AN 104,106
AN 105,107
M 68
SH 106
SH 107
M 69
mw 75
AN 106,106
AN 107,107
M 70
WL 2
AN 108,110
AN 109,111
M 71
R 30
M 72
SH 110
SH 111
M 73
AN 110,110
AN 111,111
M 74
TY 114,11272
TY 118,25096
M 75
ad 84,2262
W 0
M 76
M 77
M 78
W 1
M 79
M 80
M 81
W 2
M 82
M 83
R 32
M 84
mw 0
WL 3
AN 112,114
AN 113,115
M 85
SH 114
SH 115
M 86
AN 114,114
AN 115,115
M 87
WL 2
AN 116,118
AN 117,119
M 88
SH 118
SH 119
M 89
AN 118,118
AN 119,119
M 90
TY 122,12808
TY 126,26632
M 91
ad 84,0
M 92
W 0
M 93
M 94
M 95
R 34
M 96
W 1
M 97
M 98
M 99
WL 3
AN 120,122
AN 121,123
M 100
SH 122
SH 123
M 101
AN 122,122
AN 123,123
M 102
WL 2
AN 124,126
AN 125,127
M 103
SH 126
SH 127
M 104
AN 126,126
AN 127,127
M 105
W 2
M 106
M 107
R 48
M 108
mw 3
M 109
ad 84,6
W 0
M 110
M 111
M 112
W 1
M 113
M 114
M 115
W 2
M 116
M 117
M 118
bc 48,1,64,56
M 119
WL 2
R 50
M 120
mh 15
M 121
ad 64,259
H 0
M 132
M 133
TX 58,9216
TX 62,23040
M 122
DG 1024
M 123
M 134
M 135
TY 66,9728
TY 70,23552
M 124
M 125
M 136
M 137
TX 74,10752
TX 78,24576
M 126
WL 4
Q 0
M 127
Q 2
M 138
Q 4
M 139
TY 82,11264
TY 86,25088
M 128
Q 6
M 129
WL 4
Q 8
M 140
Q 10
M 141
TX 90,12288
TX 94,26112
M 130
Q 12
M 131
R 52
M 142
Q 14
M 143
TY 98,12800
TY 102,26624
M 0
R 54
M 1
WL 10
Q 16
M 12
Q 18
M 13
Q 20
M 2
Q 22
M 3
WL 8
Q 24
M 14
Q 26
M 15
Q 28
M 4
Q 30
M 5
WL 6
Q 32
M 16
Q 34
M 17
Q 36
M 6
Q 38
M 7
WL 2
Q 40
M 18
Q 42
M 19
Q 44
M 8
Q 46
M 9
UL 6,38
UH 7,38
M 20
AD 180,6
AD 204,7
M 21
UL 6,39
UH 7,39
M 10
AD 228,6
AD 252,7
M 11
R 56
M 22
UL 6,40
UH 7,40
M 23
R 58
M 24
AD 181,6
AD 205,7
M 25
UL 6,41
UH 7,41
M 26
AD 229,6
AD 253,7
BV 52,4608
M 27
UL 6,42
UH 7,42
M 28
AD 182,6
AD 206,7
M 29
UL 6,43
UH 7,43
M 30
AD 230,6
AD 254,7
M 31
UL 6,44
UH 7,44
M 32
AD 183,6
AD 207,7
M 33
WV 8
UL 6,45
UH 7,45
M 34
WL 1
FP
AD 231,6
M 35
AD 255,7
M 36
M 37
M 38
M 39
M 40
ds_read_b64 v[12:13], v4 offset:4864
ds_read_b64 v[46:47], v4 offset:4872
M 41
AR 6,36
AR 7,60
M 42
AR 8,84
AR 9,108
M 43
WL 3
EF 6,52
EF 7,53
M 44
EF 8,54
EF 9,55
M 45
FR
CV 14,6,7
M 46
CV 15,8,9
AR 6,37
M 47
AR 7,61
AR 8,85
M 48
AR 9,109
EF 6,52
M 49
EF 7,53
EF 8,54
M 50
EF 9,55
CV 16,6,7
M 51
CV 17,8,9
AR 6,38
M 52
AR 7,62
AR 8,86
M 53
WL 0
FQ 268
AR 9,110
M 54
EF 6,52
)"
R"(EF 7,53
M 55
EF 8,54
EF 9,55
M 56
FZ 268
R 72
M 57
mw 24
R 74
M 58
TY 114,2056
TY 118,15880
M 59
R 76
M 60
ad 84,48
W 0
M 61
CV 18,6,7
CV 19,8,9
M 62
AR 6,39
AR 7,63
M 63
W 1
AR 8,87
M 64
AR 9,111
EF 6,52
M 65
EF 7,53
EF 8,54
M 66
W 2
EF 9,55
M 67
WL 3
AN 112,114
AN 113,115
M 68
SH 114
SH 115
M 69
mw 27
AN 114,114
AN 115,115
M 70
WL 2
AN 116,118
AN 117,119
M 71
R 78
M 72
SH 118
SH 119
M 73
AN 118,118
AN 119,119
M 74
TY 122,3592
TY 126,17416
M 75
ad 84,54
W 0
M 76
CV 20,6,7
CV 21,8,9
M 77
ae 42,74,768
AR 6,40
M 78
W 1
AR 7,64
M 79
s_bitcmp1_b32 s50, 12
s_cselect_b32 s52, 24576, 0
v_add_u32 v50, s52, v3
M 80
ST 14,0
AR 8,88
M 81
W 2
AR 9,112
M 82
EF 6,52
EF 7,53
M 83
WL 1
R 80
M 84
mw 48
AN 120,122
AN 121,123
M 85
SH 122
SH 123
M 86
AN 122,122
AN 123,123
M 87
WL 2
AN 124,126
AN 125,127
M 88
SH 126
SH 127
M 89
AN 126,126
AN 127,127
M 90
ad 84,96
W 0
M 91
ST 16,768
EF 8,54
M 92
EF 9,55
CV 22,6,7
M 93
W 1
CV 23,8,9
M 94
AR 6,41
AR 7,65
M 95
R 82
M 96
W 2
ST 18,1536
M 97
AR 8,89
AR 9,113
M 98
EF 6,52
EF 7,53
M 99
mw 51
EF 8,54
EF 9,55
M 100
ad 84,102
W 0
M 101
ST 20,2304
CV 24,6,7
M 102
CV 25,8,9
AR 6,42
M 103
W 1
AR 7,66
M 104
AR 8,90
AR 9,114
M 105
EF 6,52
EF 7,53
M 106
W 2
EF 8,54
M 107
EF 9,55
CV 26,6,7
M 108
CV 27,8,9
AR 6,43
M 109
AR 7,67
AR 8,91
M 110
AR 9,115
EF 6,52
M 111
EF 7,53
EF 8,54
M 112
EF 9,55
CV 28,6,7
M 113
CV 29,8,9
M 114
ae 42,74,768
s_bfe_u32 s52, s50, 0x1000d
BV 52,5120
M 115
WV 18
v_mad_i32_i24 v50, v49, s52, v48
ST 22,0
M 116
WL 1
AR 6,132
AR 7,156
M 117
s_barrier
R 0
M 118
mw 72
AR 8,180
EF 6,12
M 119
R 2
M 120
ad 84,144
W 0
M 121
ST 24,768
EF 7,13
M 132
EF 8,46
EF 136,47
M 133
CV 30,6,7
CV 31,8,136
M 122
W 1
AR 6,133
M 123
ST 26,1536
AR 7,157
M 134
AR 8,181
EF 6,12
M 135
EF 7,13
EF 8,46
M 124
W 2
EF 137,47
M 125
ST 28,2304
CV 32,6,7
M 136
CV 33,8,137
AR 6,134
M 137
TX 74,1536
TX 78,15360
M 126
mw 75
AR 7,158
AR 8,182
M 127
ad 84,150
W 0
M 138
EF 6,12
EF 7,13
M 139
TY 82,2048
TY 86,15872
M 128
EF 8,46
EF 138,47
M 129
W 1
CV 34,6,7
M 140
CV 35,8,138
AR 6,135
M 141
WL 2
TX 90,3072
TX 94,16896
P 16
M 130
Q 17
P 19
M 131
R 4
P 20
M 142
Q 21
P 23
M 143
WL 4
TY 98,3584
TY 102,17408
P 24
M 4
R 6
P 25
M 5
Q 26
P 28
M 16
Q 29
P 31
M 17
WL 6
Q 32
M 6
Q 34
M 7
Q 36
M 18
Q 38
M 19
WL 2
Q 40
M 8
Q 42
M 9
Q 44
M 20
Q 46
M 21
W 2
AR 7,159
M 10
AR 8,183
EF 6,12
M 11
R 8
M 22
EF 7,13
EF 8,46
M 23
R 10
M 28
EF 139,47
CV 36,6,7
M 29
CV 37,8,139
M 30
ae 42,74,768
AR 6,136
M 31
s_bitcmp1_b32 s50, 12
s_cselect_b32 s52, 24576, 0
v_add_u32 v50, s52, v3
M 32
ST 30,128
AR 7,160
M 33
AR 8,184
EF 6,12
M 34
EF 7,13
EF 8,46
M 35
WL 4
EF 140,47
CV 38,6,7
M 40
ST 32,896
CV 39,8,140
M 41
AR 6,137
AR 7,161
M 42
AR 8,185
EF 6,12
M 43
EF 7,13
EF 8,46
M 44
WV 21
ST 34,1664
EF 141,47
M 45
WL 0
CV 40,6,7
CV 41,8,141
M 46
s_barrier
R 24
M 47
R 26
M 52
TY 122,8200
TY 126,22024
M 53
ST 36,2432
AR 6,138
M 54
AR 7,162
AR 8,186
M 55
EF 6,12
EF 7,13
M 56
EF 8,46
EF 142,47
M 57
CV 42,6,7
CV 43,8,142
M 58
AR 6,139
AR 7,163
M 59
R 28
M 64
AR 8,187
EF 6,12
M 65
WL 3
AN 120,122
AN 121,123
M 66
)"
R"(SH 122
SH 123
M 67
AN 122,122
AN 123,123
M 68
WL 2
AN 124,126
AN 125,127
M 69
SH 126
SH 127
M 70
AN 126,126
AN 127,127
M 71
R 30
M 76
EF 7,13
EF 8,46
M 77
EF 143,47
CV 44,6,7
M 78
CV 45,8,143
M 79
ae 42,74,768
ds_read_b64 v[12:13], v4 offset:4608
ds_read_b64 v[46:47], v4 offset:4616
M 80
s_bfe_u32 s52, s50, 0x1000d
v_mad_i32_i24 v50, v49, s52, v48
ST 38,128
M 81
EF 160,52
EF 184,53
M 82
EF 208,54
EF 232,55
M 83
R 32
M 88
TX 58,7680
TX 62,21504
M 89
ST 40,896
CV 14,160,184
M 90
CV 15,208,232
EF 161,52
M 91
EF 185,53
EF 209,54
M 92
TY 66,8192
TY 70,22016
M 93
ST 42,1664
EF 233,55
M 94
CV 16,161,185
CV 17,209,233
M 95
WL 7
R 34
M 100
EF 162,52
EF 186,53
M 101
WL 4
PA_ 56,120,58
PA_ 57,121,59
M 102
PB_ 58,120,58
PB_ 59,121,59
M 103
PA_ 60,124,62
PA_ 61,125,63
M 104
PB_ 62,124,62
PB_ 63,125,63
M 105
WL 2
PA_ 64,122,66
PA_ 65,123,67
M 106
PB_ 66,122,66
PB_ 67,123,67
M 107
PA_ 68,126,70
PA_ 69,127,71
M 112
PB_ 70,126,70
PB_ 71,127,71
M 113
ST 44,2432
EF 210,54
M 114
EF 234,55
CV 18,162,186
M 115
CV 19,210,234
EF 163,52
M 116
WV 18
EF 187,53
EF 211,54
M 117
WL 0
EF 235,55
CV 20,163,187
M 118
s_barrier
R 48
M 119
R 50
M 124
CV 21,211,235
M 125
ae 42,74,768
EF 164,52
M 136
s_bitcmp1_b32 s50, 12
s_cselect_b32 s52, 24576, 0
v_add_u32 v50, s52, v3
M 137
ST 14,256
EF 188,53
M 126
EF 212,54
EF 236,55
M 127
CV 22,164,188
CV 23,212,236
M 138
EF 165,52
EF 189,53
M 139
ST 16,1024
EF 213,54
M 128
EF 237,55
CV 24,165,189
M 129
CV 25,213,237
EF 166,52
M 140
EF 190,53
EF 214,54
M 141
ST 18,1792
EF 238,55
M 130
CV 26,166,190
CV 27,214,238
M 131
R 52
M 142
EF 167,52
EF 191,53
M 143
WL 2
R 54
MA 52,0,56
ST 20,2560
EF 215,54
MA 52,4,60
EF 239,55
CV 28,167,191
MA 76,8,56
CV 29,215,239
MA 76,12,60
BV 52,4864
AR 6,44
MA 56,0,64
ae 42,74,768
AR 7,68
MA 56,4,68
s_bfe_u32 s52, s50, 0x1000d
R 56
MA 80,8,64
v_mad_i32_i24 v50, v49, s52, v48
ST 22,256
MA 80,12,68
WL 5
R 58
MA 100,16,56
AR 8,92
AR 9,116
MA 100,20,60
EF 6,12
EF 7,13
MA 104,16,64
ST 24,1024
EF 8,46
MA 104,20,68
WL 5
EF 9,47
CV 30,6,7
MA 124,24,56
WV 14
CV 31,8,9
AR 6,45
MA 124,28,60
WL 0
AR 7,69
AR 8,93
MA 128,24,64
s_barrier
R 72
MA 128,28,68
R 74
MA 148,0,56
ST 26,1792
AR 9,117
MA 148,4,60
EF 6,12
EF 7,13
MA 152,0,64
EF 8,46
EF 9,47
MA 152,4,68
R 76
MA 172,8,56
ST 28,2560
CV 32,6,7
MA 172,12,60
CV 33,8,9
AR 6,46
MA 176,8,64
AR 7,70
AR 8,94
MA 176,12,68
WL 4
R 78
MA 196,16,56
AR 9,118
EF 6,12
MA 196,20,60
EF 7,13
EF 8,46
MV 132,16,64
EF 9,47
CV 34,6,7
MV 132,20,68
WL 4
R 80
MV 152,24,56
CV 35,8,9
AR 6,47
MV 152,28,60
AR 7,71
AR 8,95
MV 156,24,64
AR 9,119
EF 6,12
MV 156,28,68
WL 4
R 82
MV 176,0,56
EF 7,13
EF 8,46
MV 176,4,60
EF 9,47
CV 36,6,7
MV 180,0,64
CV 37,8,9
MV 180,4,68
WL 4
AR 6,48
AR 7,72
AR 8,96
AR 9,120
MV 200,8,56
ae 42,72,768
EF 6,12
EF 7,13
EF 8,46
MV 200,12,60
s_bitcmp1_b32 s50, 14
s_cselect_b32 s52, 24576, 0
v_add_u32 v50, s52, v3
EF 9,47
CV 38,6,7
MV 204,8,64
ST 30,0
CV 39,8,9
AR 6,49
AR 7,73
MV 204,12,68
WL 0
AR 8,97
AR 9,121
EF 6,12
EF 7,13
MV 224,16,56
EF 8,46
EF 9,47
CV 40,6,7
CV 41,8,9
MV 224,20,60
AR 6,50
AR 7,74
AR 8,98
AR 9,122
MV 248,24,56
ST 32,768
EF 6,12
EF 7,13
EF 8,46
MV 248,28,60
EF 9,47
CV 42,6,7
CV 43,8,9
AR 6,51
MV 228,16,64
AR 7,75
AR 8,99
AR 9,123
EF 6,12
MV 228,20,68
EF 7,13
EF 8,46
EF 9,47
CV 44,6,7
MV 252,24,64
ST 34,1536
CV 45,8,9
MV 252,28,68
WV 13
ST 36,2304
ae 42,72,768
s_bfe_u32 s52, s50, 0x1000f
v_mad_i32_i24 v50, v49, s52, v48
ST 38,0
ST 40,768
ST 42,1536
ST 44,2304
ds_read_b64 v[12:13], v4 offset:5120
ds_read_b64 v[46:47], v4 offset:5128
AR 6,140
AR 7,164
AR 8,188
EF 6,52
EF 7,53
EF 8,54
EF 144,55
CV 14,6,7
CV 15,8,144
AR 6,141
AR 7,165
AR 8,189
EF 6,52
EF 7,53
EF 8,54
EF 145,55
CV 16,6,7
CV 17,8,145
AR 6,142
AR 7,166
AR 8,190
EF 6,52
EF 7,53
EF 8,54
EF 146,55
CV 18,6,7
CV 19,8,146
AR 6,143
AR 7,167
AR 8,191
EF 6,52
EF 7,53
EF 8,54
EF 147,55
CV 20,6,7
CV 21,8,147
ae 42,72,768
s_bitcmp1_b32 s50, 14
s_cselect_b32 s52, 24576, 0
v_add_u32 v50, s52, v3
ST 14,128
ST 16,896
ST 18,1664
ST 20,2432
AR 6,144
AR 7,168
AR 8,192
EF 6,52
EF 7,53
EF 8,54
EF 148,55
CV 22,6,7
CV 23,8,148
AR 6,145
AR 7,169
AR 8,193
EF 6,52
EF 7,53
EF 8,54
EF 149,55
)"
R"(CV 24,6,7
CV 25,8,149
AR 6,146
AR 7,170
AR 8,194
EF 6,52
EF 7,53
EF 8,54
EF 150,55
CV 26,6,7
CV 27,8,150
AR 6,147
AR 7,171
AR 8,195
EF 6,52
EF 7,53
EF 8,54
EF 151,55
CV 28,6,7
CV 29,8,151
ae 42,72,768
s_bfe_u32 s52, s50, 0x1000f
v_mad_i32_i24 v50, v49, s52, v48
ST 22,128
ST 24,896
ST 26,1664
ST 28,2432
BV 52,4608
WL 1
EF 168,12
EF 192,13
EF 216,46
EF 240,47
CV 30,168,192
CV 31,216,240
EF 169,12
EF 193,13
EF 217,46
EF 241,47
CV 32,169,193
CV 33,217,241
EF 170,12
EF 194,13
EF 218,46
EF 242,47
CV 34,170,194
CV 35,218,242
EF 171,12
EF 195,13
EF 219,46
EF 243,47
CV 36,171,195
CV 37,219,243
ae 42,72,768
s_bitcmp1_b32 s50, 14
s_cselect_b32 s52, 24576, 0
v_add_u32 v50, s52, v3
ST 30,256
ST 32,1024
ST 34,1792
ST 36,2560
EF 172,12
EF 196,13
EF 220,46
EF 244,47
CV 38,172,196
CV 39,220,244
EF 173,12
EF 197,13
EF 221,46
EF 245,47
CV 40,173,197
CV 41,221,245
EF 174,12
EF 198,13
EF 222,46
EF 246,47
CV 42,174,198
CV 43,222,246
EF 175,12
EF 199,13
EF 223,46
EF 247,47
CV 44,175,199
CV 45,223,247
ae 42,72,768
s_bfe_u32 s52, s50, 0x1000f
v_mad_i32_i24 v50, v49, s52, v48
ST 38,256
ST 40,1024
ST 42,1792
ST 44,2560
ds_read_b64 v[12:13], v4 offset:4864
ds_read_b64 v[46:47], v4 offset:4872
AR 6,52
AR 7,76
AR 8,100
AR 9,124
WL 2
EF 6,52
EF 7,53
EF 8,54
EF 9,55
CV 14,6,7
CV 15,8,9
AR 6,53
AR 7,77
AR 8,101
AR 9,125
EF 6,52
EF 7,53
EF 8,54
EF 9,55
CV 16,6,7
CV 17,8,9
AR 6,54
AR 7,78
AR 8,102
AR 9,126
EF 6,52
EF 7,53
EF 8,54
EF 9,55
CV 18,6,7
CV 19,8,9
AR 6,55
AR 7,79
AR 8,103
AR 9,127
EF 6,52
EF 7,53
EF 8,54
EF 9,55
CV 20,6,7
CV 21,8,9
ae 42,72,864
s_bitcmp1_b32 s50, 16
s_cselect_b32 s52, 24576, 0
v_add_u32 v50, s52, v3
ST 14,0
ST 16,768
ST 18,1536
ST 20,2304
AR 6,56
AR 7,80
AR 8,104
AR 9,128
EF 6,52
EF 7,53
EF 8,54
EF 9,55
CV 22,6,7
CV 23,8,9
AR 6,57
AR 7,81
AR 8,105
AR 9,129
EF 6,52
EF 7,53
EF 8,54
EF 9,55
CV 24,6,7
CV 25,8,9
AR 6,58
AR 7,82
AR 8,106
AR 9,130
EF 6,52
EF 7,53
EF 8,54
EF 9,55
CV 26,6,7
CV 27,8,9
AR 6,59
AR 7,83
AR 8,107
AR 9,131
EF 6,52
EF 7,53
EF 8,54
EF 9,55
CV 28,6,7
CV 29,8,9
ae 42,72,864
s_bfe_u32 s52, s50, 0x10011
v_mad_i32_i24 v50, v49, s52, v48
ST 22,0
ST 24,768
ST 26,1536
ST 28,2304
BV 52,5120
AR 6,148
AR 7,172
AR 8,196
WL 1
EF 6,12
EF 7,13
EF 8,46
EF 152,47
CV 30,6,7
CV 31,8,152
AR 6,149
AR 7,173
AR 8,197
EF 6,12
EF 7,13
EF 8,46
EF 153,47
CV 32,6,7
CV 33,8,153
AR 6,150
AR 7,174
AR 8,198
EF 6,12
EF 7,13
EF 8,46
EF 154,47
CV 34,6,7
CV 35,8,154
AR 6,151
AR 7,175
AR 8,199
EF 6,12
EF 7,13
EF 8,46
EF 155,47
CV 36,6,7
CV 37,8,155
ae 42,72,864
s_bitcmp1_b32 s50, 16
s_cselect_b32 s52, 24576, 0
v_add_u32 v50, s52, v3
ST 30,128
ST 32,896
ST 34,1664
ST 36,2432
AR 6,152
AR 7,176
EF 6,12
EF 7,13
EF 132,46
EF 156,47
CV 38,6,7
CV 39,132,156
AR 6,153
AR 7,177
EF 6,12
EF 7,13
EF 133,46
EF 157,47
CV 40,6,7
CV 41,133,157
AR 6,154
AR 7,178
EF 6,12
EF 7,13
EF 134,46
EF 158,47
CV 42,6,7
CV 43,134,158
AR 6,155
AR 7,179
EF 6,12
EF 7,13
EF 135,46
EF 159,47
CV 44,6,7
CV 45,135,159
ae 42,72,864
s_bfe_u32 s52, s50, 0x10011
v_mad_i32_i24 v50, v49, s52, v48
ST 38,128
ST 40,896
ST 42,1664
ST 44,2432
WL 0
EF 176,52
EF 200,53
EF 224,54
EF 248,55
CV 14,176,200
CV 15,224,248
EF 177,52
EF 201,53
EF 225,54
EF 249,55
CV 16,177,201
CV 17,225,249
EF 178,52
EF 202,53
EF 226,54
EF 250,55
CV 18,178,202
CV 19,226,250
EF 179,52
EF 203,53
EF 227,54
EF 251,55
CV 20,179,203
CV 21,227,251
ae 42,72,864
s_bitcmp1_b32 s50, 16
s_cselect_b32 s52, 24576, 0
v_add_u32 v50, s52, v3
ST 14,256
ST 16,1024
ST 18,1792
ST 20,2560
EF 180,52
EF 204,53
EF 228,54
EF 252,55
CV 22,180,204
CV 23,228,252
EF 181,52
EF 205,53
EF 229,54
EF 253,55
CV 24,181,205
CV 25,229,253
EF 182,52
EF 206,53
EF 230,54
EF 254,55
CV 26,182,206
CV 27,230,254
EF 183,52
EF 207,53
EF 231,54
EF 255,55
CV 28,183,207
CV 29,231,255
ae 42,72,864
s_bfe_u32 s52, s50, 0x10011
v_mad_i32_i24 v50, v49, s52, v48
ST 22,256
ST 24,1024
ST 26,1792
ST 28,2560
s_waitcnt vmcnt(0) lgkmcnt(0)
s_branch 29f
FY 132
FY 136
FY 140
FY 144
FY 148
FY 152
FY 156
FY 160
FY 164
FY 168
FY 172
FY 176
FY 180
FY 184
FY 188
FY 192
FY 196
FY 200
FY 204
FY 208
FY 212
FY 216
FY 220
FY 224
FY 228
FY 232
FY 236
FY 240
FY 244
FY 248
FY 252
FY 256
FY 260
FY 264
FY 268
29:
.purgem MA
.purgem MV
.purgem NA
.purgem NV
.purgem AN
.purgem SH
.purgem PA_
.purgem PB_
.purgem AW
.purgem AR
.purgem UL
.purgem UH
.purgem AD
.purgem EF
.purgem TY
.purgem TX
.purgem RA
.purgem RC
.purgem BV
.purgem BA
.purgem PA
.purgem DF
.purgem DW
)"
R"(.purgem DR
.purgem DH
.purgem DG
.purgem XL
.purgem ST
.purgem SA
.purgem CV
.purgem RS
.purgem PM
.purgem PF
.purgem FS
.purgem FL
.purgem FB
.purgem FC
.purgem FG
.purgem FX
.purgem FP
.purgem FR
.purgem FQ
.purgem FZ
.purgem FY
.purgem WL
.purgem WV
.purgem M
.purgem R1
.purgem R
.purgem P
.purgem Q
.purgem mw
.purgem mh
.purgem ad
.purgem ae
.purgem bc
.purgem W
.purgem H
)"
      : [flg] "=&s"(flg) : K2_OPS : K2_CLOB);
  if (flg) {
    const unsigned lane = __builtin_amdgcn_mbcnt_hi(~0u, __builtin_amdgcn_mbcnt_lo(~0u, 0u));
    const u16* const volatile __attribute__((address_space(4)))* ka = (const u16* const volatile __attribute__((address_space(4)))*)__builtin_amdgcn_kernarg_segment_ptr();
    {
      const u16* w1p = ka[6];
#pragma unroll 1
      for (unsigned f = blockIdx.x * 4 + w; f < 1152; f += 1024) {
        const u16* p = w1p + (32 * (f % 12) + 8 * (lane >> 4)) * HID + 16 * (f / 12) + (lane & 15);
        u32x4 v;
#pragma unroll
        for (int j = 0; j < 4; ++j) v[j] = p[2 * j * HID] | (unsigned)p[(2 * j + 1) * HID] << 16;
        *(u32x4*)(g_buf + f * 1024 + lane * 16) = v;
      }
    }
    slow_bar((unsigned*)(g_buf + G_BAR), w);
    slow_fc1(ka[0], ka[5], (char*)ka[1], (float*)smem + w * HID, w);
    slow_bar((unsigned*)(g_buf + G_BAR + 64), w);
    slow_fc2(ka[0], (const char*)ka[1], (u16*)ka[2], ka[3], ka[4], smem, w);
  }
  TS(15);
  MK_END(4);
}

extern "C" void kernel_launch(void* const* d_in, const int* in_sizes, int n_in,
                              void* d_out, int out_size, void* d_ws, size_t ws_size,
                              hipStream_t stream) {
  const u16* x = (const u16*)d_in[0];
  const u16* dw = (const u16*)d_in[1];
  const u16* w1 = (const u16*)d_in[2];
  const u16* w2 = (const u16*)d_in[3];
  const u16* grn_g = (const u16*)d_in[4];
  const u16* grn_b = (const u16*)d_in[5];
  char* ws = (char*)d_ws;
  if (ws_size < H_BYTES) return;
  (void)hipFuncSetAttribute((const void*)k1_fc1, hipFuncAttributeMaxDynamicSharedMemorySize, K1_LDS);
  (void)hipFuncSetAttribute((const void*)k2_fc2, hipFuncAttributeMaxDynamicSharedMemorySize, K2_LDS);

  k0_prep<<<K0_NB + NPAGE + NPAGX, 256, 0, stream>>>(w1, dw, ws, (const char*)x, grn_b, w2);
  k1_fc1<<<NWG + 8, 256, K1_LDS, stream>>>(x, ws, (const u16*)d_out, grn_g);
  k2_fc2<<<NWG, 256, K2_LDS, stream>>>(x, ws, (u16*)d_out, grn_g, w2, dw, w1);
}
```

```cpp
#include <hip/hip_runtime.h>
#include <hip/hip_bf16.h>
#include <stdint.h>
#include <stdio.h>

typedef short bf16x8 __attribute__((ext_vector_type(8)));
typedef short s16x4 __attribute__((ext_vector_type(4)));
typedef short v4i16_t __attribute__((ext_vector_type(4)));
typedef float f32x16 __attribute__((ext_vector_type(16)));
typedef float f32x4 __attribute__((ext_vector_type(4)));
typedef float f32x2 __attribute__((ext_vector_type(2)));
typedef unsigned u32x4 __attribute__((ext_vector_type(4)));
typedef __bf16 bf16x2_t __attribute__((ext_vector_type(2)));
typedef unsigned short u16;

constexpr float TZ = 175.f, TH = 175.f, TW2 = 175.f;
#define DI __device__ __forceinline__
#define LAS __attribute__((address_space(3)))

constexpr int NPIX = 147456;
constexpr int CH = 384;
constexpr int HID = 1536;
constexpr int NTILE = NPIX / 32;
constexpr int NWG = 256;
constexpr int TPW = NTILE / NWG;

constexpr size_t H_BYTES = (size_t)NPIX * HID * 2;
constexpr unsigned K1_LDS = 163840, K1_PF_OFF = 130048, K1_SMAX = 12, K1_NREP = 4, K1_G_TAB = 1228800, K1_G_CNT = 1239040, K1_G_LOC = 1236992, K1_G_DUM = 1255424,
  K1_G_ACC = 1320960, K1_G_LUT = 1382400, K1_G_BYTES = 1398784;
constexpr bool K1_LUT = true;
constexpr float K1_ZS = 16.0f;
constexpr unsigned K1_GST = 25600, K1_WCH = 6400, K1_ZC = 1365;
constexpr unsigned K1_SB1 = 32768;
constexpr bool K1_F2 = true;
constexpr unsigned K2_SHT = 12483;
constexpr int K2_LDS = 163840, K2_HB = 98304, K2_RSZ = 13824, K2_E_OFF = 153600, K2_PF_OFF = 159744, K2_S16_OFF = 152576, K2_S16_D0 = 8192, K2_NAT = 8;

__device__ __attribute__((aligned(256))) char g_buf[K1_G_BYTES];
__device__ __attribute__((aligned(256))) float g_cs[8 * 384];
__device__ __attribute__((aligned(256))) char g_w2r[96 * 12 * 1024];
__device__ __attribute__((aligned(256))) char g_w2f[96 * 12 * 1024];
__device__ __attribute__((aligned(256))) char g_w2q[16 * 12 * 1024];
__device__ __attribute__((aligned(256))) float g_sc[64];
constexpr unsigned G_FLAG = K1_G_CNT + 14336;
constexpr unsigned G_PUB = K1_G_DUM;
__device__ __attribute__((aligned(256))) unsigned g_prm[1024 * 64];
constexpr unsigned G_STMP = K1_G_CNT + 12288;
constexpr unsigned G_BAR = K1_G_CNT + 13312;
constexpr unsigned H_TS = 49152, H_RS = 48 * H_TS, NPAGE = 217, NPAGX = 55;
#define TS(i) do { } while (0)
#define KST(i) do { } while (0)
#define MK_START(i) do { } while (0)
#define MK_END(i) do { } while (0)

DI unsigned pack_bf16(float a, float b) {
  f32x2 v = {a, b};
  bf16x2_t r = __builtin_convertvector(v, bf16x2_t);
  return __builtin_bit_cast(unsigned, r);
}
typedef _Float16 f16x2_t __attribute__((ext_vector_type(2)));
DI unsigned pack_f16(float a, float b) {
  f32x2 v = {a, b};
  f16x2_t r = __builtin_convertvector(v, f16x2_t);
  return __builtin_bit_cast(unsigned, r);
}
DI float bf_lo(unsigned u) { return __uint_as_float(u << 16); }
DI float bf_hi(unsigned u) { return __uint_as_float(u & 0xffff0000u); }
DI float round_bf16(float a) { return bf_lo(pack_bf16(a, 0.f)); }
DI float dot2acc(unsigned a, unsigned b, float c) {
  return __builtin_amdgcn_fdot2_f32_bf16(__builtin_bit_cast(bf16x2_t, a), __builtin_bit_cast(bf16x2_t, b), c, false);
}
DI float ld_bf16(const u16* p) { return __uint_as_float((unsigned)(*p) << 16); }

DI s16x4 tr_read(const char* p) {
  return __builtin_bit_cast(s16x4, __builtin_amdgcn_ds_read_tr16_b64_v4i16((LAS v4i16_t*)p));
}

#define MFMA32(a, b, c) __builtin_amdgcn_mfma_f32_32x32x16_bf16((a), (b), (c), 0, 0, 0)
#define BAR() __builtin_amdgcn_s_barrier()

DI void dma16(unsigned voff, const char* sbase, unsigned lds_dst) {
  unsigned keep;
  asm volatile("s_mov_b32 %0, m0\n\ts_mov_b32 m0, %3\n\ts_nop 0\n\tglobal_load_lds_dwordx4 %1, %2\n\ts_mov_b32 m0, %0"
               : "=&s"(keep) : "v"(voff), "s"(sbase), "s"(lds_dst) : "memory");
}
DI unsigned lds_addr(const char* p) { return (unsigned)(size_t)(const LAS char*)p; }

DI float gelu_tanh(float x) {
  const float c0 = -2.0f * 0.7978845608028654f * 1.4426950408889634f;
  const float c1 = c0 * 0.044715f;
  float x2 = x * x;
  float t = __builtin_fmaf(x2, c1, c0);
  float e = __builtin_amdgcn_exp2f(x * t);
  float r = __builtin_amdgcn_rcpf(1.0f + e);
  return x * r;
}

DI void gload_b128_asm(u32x4& dst, const char* sbase, unsigned voff) {
  asm volatile("global_load_dwordx4 %0, %1, %2" : "=v"(dst) : "v"(voff), "s"(sbase) : "memory");
}
DI void lds_rd128(u32x4& dst, unsigned addr, int off) {
  asm volatile("ds_read_b128 %0, %1 offset:%2" : "=v"(dst) : "v"(addr), "i"(off));
}
DI void lds_rd128_far(u32x4& dst, unsigned voff, unsigned sbase, int imm) {
  unsigned t;
  asm volatile("v_add_u32 %1, %3, %2\n\tv_add_u32 %1, %4, %1\n\tds_read_b128 %0, %1" : "=v"(dst), "=&v"(t) : "v"(voff), "s"(sbase), "i"(imm));
}
DI void gload16i(u32x4& dst, const char* sbase, unsigned voff, int imm) {
  unsigned t;
  asm volatile("v_add_u32 %1, %4, %2\n\tglobal_load_dwordx4 %0, %1, %3" : "=v"(dst), "=&v"(t) : "v"(voff), "s"(sbase), "i"(imm) : "memory");
}
DI void dma16i(const char* sbase, unsigned voff, int immv, unsigned lbase, int imml) {
  unsigned t, keep;
  asm volatile("s_mov_b32 %1, m0\n\ts_add_u32 m0, %4, %6\n\tv_add_u32 %0, %5, %2\n\tglobal_load_lds_dwordx4 %0, %3\n\ts_mov_b32 m0, %1"
               : "=&v"(t), "=&s"(keep) : "v"(voff), "s"(sbase), "s"(lbase), "i"(immv), "i"(imml) : "memory", "scc");
}

DI void st16_wt(void* p, u32x4 v) { asm volatile("global_store_dwordx4 %0, %1, off\n\ts_nop 1" :: "v"(p), "v"(v) : "memory"); }
template <int I> struct IC { static constexpr int value = I; };
template <int N, int I = 0, typename F> DI void static_for(F&& f) {
  if constexpr (I < N) { f(IC<I>{}); static_for<N, I + 1>(f); }
}
template <int C, int M> DI float dpp_add(float v) { return v + __int_as_float(__builtin_amdgcn_update_dpp(0, __float_as_int(v), C, M, 0xf, false)); }
DI float wave_sum63(float v) {
  v = dpp_add<0x111, 0xf>(v); v = dpp_add<0x112, 0xf>(v); v = dpp_add<0x114, 0xf>(v); v = dpp_add<0x118, 0xf>(v);
  v = dpp_add<0x142, 0xa>(v); return dpp_add<0x143, 0xc>(v);
}
DI unsigned cs_index(unsigned n) { return (n & ~63u) + (n & 3) * 16 + ((n >> 2) & 15); }
DI uint64_t uni64(uint64_t v) { return (uint64_t)__builtin_amdgcn_readfirstlane(v >> 32) << 32 | (unsigned)__builtin_amdgcn_readfirstlane(v); }
DI unsigned wg_side(unsigned b) { return (((const unsigned*)(g_buf + K1_G_LOC))[b & 3] ^ (b >> 2)) & 1; }
DI unsigned h_flip(const char* ws, unsigned region, unsigned t) { return __popc(((const unsigned*)(g_buf + K1_G_LOC + 64))[(((unsigned)(size_t)ws & 0x1fffffu) + region * H_RS + t * H_TS) >> 21]) > 2; }
DI unsigned x_flip(const u16* x, unsigned region) { return __popc(((const unsigned*)(g_buf + K1_G_LOC + 1024))[(((unsigned)(size_t)x & 0x1fffffu) + (12 * region + 6) * 49152u) >> 21]) > 2; }
DI unsigned sub_pixel(const u16* x, unsigned sub, unsigned m) {
  const unsigned blk = sub / 24, u = sub % 24;
  const unsigned region = blk < 256 ? (blk >> 3) * 4 + (blk & 3) : 128 + ((blk - 256) >> 1), side = blk < 256 ? wg_side(blk) : blk & 1;
  return (12 * region + (u >> 1)) * 64 + ((0xda43cb52fe987610ull >> (32 * (u & 1) + 16 * (side ^ x_flip(x, region)) + (m & 12))) & 15) * 4 + (m & 3);
}
DI unsigned h_piece(const char* ws, unsigned sub, unsigned t) {
  const unsigned blk = sub / 24, u = sub % 24;
  const unsigned region = blk < 256 ? (blk >> 3) * 4 + (blk & 3) : 128 + ((blk - 256) >> 1), side = blk < 256 ? wg_side(blk) : blk & 1;
  return region * H_RS + t * H_TS + (u >> 3) * 16384 + (u & 7) * 1024 + ((side ^ h_flip(ws, region, t)) << 13);
}
constexpr unsigned K0_NB = 145 + 24 + 96 + 16 + 48;
constexpr float U24 = 0x1p-24f, EPS = 1e-5f;
DI float wave_all(float v) { return __int_as_float(__builtin_amdgcn_readlane(__float_as_int(wave_sum63(v)), 63)); }
DI float pow2f(float v) { return __uint_as_float(__float_as_uint(v) & 0x7f800000u); }
DI u32x4 w_load(const u16* w) { return *(const u32x4*)(w + ((threadIdx.x >> 3) & 7) * 73408 + 36800 + (threadIdx.x & 7) * 8); }
DI float w_ms(u32x4 a) {
  float q = 0.f;
#pragma unroll
  for (int i = 0; i < 4; ++i) { const float lo = bf_lo(a[i]), hi = bf_hi(a[i]); q = __builtin_fmaf(lo, lo, __builtin_fmaf(hi, hi, q)); }
  return wave_all(q) * (1.0f / 512);
}
DI float w1_scale(float ms) { return pow2f(300.f * U24 * __builtin_amdgcn_rsqf(ms)); }
DI float w2_scale(float ms) { return TW2 * U24 * __builtin_amdgcn_rsqf(ms); }
struct Sc { float w1, zs, g, h, vlo, vhi, c; };
DI Sc scales(const u16* w1, const u16* x, const u16* dw) {
  const u32x4 a = w_load(w1);
  const unsigned l = threadIdx.x & 63;
  float s = 0.f, q = 0.f;
#pragma unroll
  for (int k = 0; k < 6; ++k) {
    const u32x4 v = *(const u32x4*)(x + (9001 + 18397 * (l >> 3)) * CH + 64 * k + 8 * (l & 7)), d = *(const u32x4*)(dw + 64 * k + 8 * (l & 7));
#pragma unroll
    for (int i = 0; i < 4; ++i) { const float lo = bf_lo(v[i]) * bf_lo(d[i]), hi = bf_hi(v[i]) * bf_hi(d[i]); s += lo + hi; q = __builtin_fmaf(lo, lo, __builtin_fmaf(hi, hi, q)); }
  }
  s = dpp_add<0x141, 0xf>(dpp_add<0x4e, 0xf>(dpp_add<0xb1, 0xf>(s))); q = dpp_add<0x141, 0xf>(dpp_add<0x4e, 0xf>(dpp_add<0xb1, 0xf>(q)));
  const float var = fmaxf(q * (1.0f / CH) - s * s * (1.0f / (CH * CH)), 0.f);
  const float r2 = wave_all(var * __builtin_amdgcn_rcpf(var + EPS)) * (1.0f / 64), rden = __builtin_amdgcn_exp2f(wave_all(__builtin_amdgcn_logf(var + EPS)) * (-1.0f / 128));
  const float s1 = w_ms(a), p2 = r2 * CH * s1;
  Sc c;
  c.w1 = w1_scale(s1);
  c.g = TZ * U24 * __builtin_amdgcn_rsqf(r2);
  c.h = TH * U24 * __builtin_amdgcn_rsqf(p2 * (0.25f + 0.5f * p2 * __builtin_amdgcn_rcpf(1.0f + 2.0f * p2)));
  c.c = fminf(__builtin_ceilf(0.1701f * 0x1p24f * c.h) + 1.0f, 1400.f);
  c.zs = fminf(pow2f(c.g * 2530.f), pow2f(c.g * 0x1p12f * rden));
  const float e = EPS * c.zs * c.zs, z2 = r2 * (660.f * 660.f / (TZ * TZ));
  c.vlo = 4.2f * c.g * c.g - e;
  c.vhi = fminf(0x1.ep29f * c.g * c.g - e, z2 < 1.0f ? e * z2 * __builtin_amdgcn_rcpf(1.0f - z2) : 0x1p120f);
  return c;
}
__global__ __launch_bounds__(256) void k0_prep(const u16* __restrict__ w1, const u16* __restrict__ dw, const char* __restrict__ ws, const char* __restrict__ x,
                                                const u16* __restrict__ grn_b, const u16* __restrict__ w2) {
  MK_START(0);
  uint64_t pc; int d1;
  asm volatile("s_getpc_b64 %0\n1:\n\ts_mov_b32 %1, k1_code-1b" : "=s"(pc), "=s"(d1));
  if (threadIdx.x >= 192) pc += (int64_t)d1 + ((int)(blockIdx.x >> 3) - 3) * 8192;
  unsigned pfd = 0;
  if (blockIdx.x < 64 && (threadIdx.x < 150 || (blockIdx.x < 48 && threadIdx.x >= 192))) pfd = *(const __attribute__((address_space(1))) unsigned*)((pc & ~127ull) + threadIdx.x * 128);
#define K0_END() do { asm volatile("" :: "v"(pfd)); MK_END(0); } while (0)
  unsigned xcc;
  asm volatile("s_getreg_b32 %0, hwreg(HW_REG_XCC_ID)" : "=s"(xcc));
  xcc = (xcc >> 2) & 1;
  if (blockIdx.x < 4 && threadIdx.x == 0) ((unsigned*)(g_buf + K1_G_LOC))[blockIdx.x] = 7 * xcc;
  constexpr unsigned NP = NPAGE + NPAGX;
  __shared__ Sc ssc;
  __shared__ float ss2;
  const bool sw = threadIdx.x >= 192;
  const unsigned bi = blockIdx.x;
  const unsigned bx = bi < NP ? bi + K0_NB : bi - NP;
  if (bx >= K0_NB) {
    __shared__ unsigned lat[8];
    const unsigned pg = bx - K0_NB, w = threadIdx.x >> 6, lane = threadIdx.x & 63;
    auto gran = [&](unsigned p) {
      const bool h = p < NPAGE;
      const char* buf = h ? ws : x;
      const int o0 = (int)((h ? p : p - NPAGE) << 21) - (int)((unsigned)(size_t)buf & 0x1fffffu);
      const unsigned al = h ? 49152 : 12 * 49152;
      const unsigned q = ((((unsigned)max(o0, 0) + al - 1) >> (h ? 14 : 16)) * (h ? 87382u : 29128u)) >> 18;
      return buf + min(q * al, (unsigned)(h ? H_BYTES : H_BYTES / 4) - 16384u);
    };
    if (lane < 5) {
      const char* a = gran(lane < 4 ? min(pg + NP - 1 - (lane >> 1), pg - 1 - (lane >> 1)) : pg) + (lane & 1) * 8192 + w * 2048 + (lane < 4 ? 1024 : 0);
      const char* b = a;
      const char* own = (const char*)uni64(__builtin_amdgcn_readlane((uint64_t)a >> 32, 4) * 4294967296ull + (unsigned)__builtin_amdgcn_readlane((unsigned)(uint64_t)a, 4));
      unsigned long long t0, t1, t2;
      int n = 2;
      asm volatile("" : "+s"(n));
#pragma unroll 1
      do {
        unsigned v;
        asm volatile("s_cmp_eq_u32 %6, 2\n\ts_cbranch_scc1 1f\n\ts_sleep 4\n1:\n\ts_memtime %0\n\ts_waitcnt lgkmcnt(0)\n\tglobal_load_dword %3, %4, off sc1\n\ts_waitcnt vmcnt(0)\n\ts_memtime %1\n\ts_waitcnt lgkmcnt(0)\n\t"
                     "global_load_dword %3, %5, off sc1\n\ts_waitcnt vmcnt(0)\n\ts_memtime %2\n\ts_waitcnt lgkmcnt(0)" : "=&s"(t0), "=&s"(t1), "=&s"(t2), "=&v"(v) : "v"(a), "v"(b), "s"(n) : "memory", "scc");
        a = own + 1024 + (w & 1) * 8192; b = a + 8192 - (w & 1) * 16384;
      } while (--n);
      lat[w + 4 * (w & 1)] = (unsigned)(t1 - t0); lat[w + 4 - 4 * (w & 1)] = (unsigned)(t2 - t1);
    }
    __syncthreads();
    if (threadIdx.x == 0) {
      auto fastest = [&](const unsigned* l) { unsigned m = 9999; for (int i = 0; i < 4; ++i) m = l[i] < 300 ? m : min(m, l[i]); return (int)m; };
      const int df = fastest(lat) - fastest(lat + 4);
      const unsigned v = xcc ^ (df > 0), sure = abs(df) > 80;
      char* e = g_buf + K1_G_LOC + 64 + 4 * (pg < NPAGE ? pg : pg + 240 - NPAGE);
      *(u16*)(e + 2) = v ? 3 + 4 * sure : 1 - sure;
      if (pg == 0 || pg == NPAGE) e[0] = v; else e[-3] = v ^ 1;
      if (pg == NPAGE - 1 || pg == NP - 1) e[1] = v; else e[4] = v ^ 1;
    }
    K0_END();
    return;
  }
  if (bx >= 145 + 24 + 96 + 16) {
    __shared__ __attribute__((aligned(16))) float red[2][16][32];
    const int t = bx - 281, c8 = (threadIdx.x & 3) * 8;
    u32x4 sm, v[6];
    if (sw) sm = w_load(w1);
#pragma unroll
    for (int k = 0; k < 6; ++k) v[k] = *(const u32x4*)(w1 + (size_t)(64 * k + (threadIdx.x >> 2)) * HID + 32 * t + c8);
    if (sw) ss2 = w1_scale(w_ms(sm));
    __syncthreads();
    const float s1 = ss2;
    float a[8], q[8];
#pragma unroll
    for (int i = 0; i < 8; ++i) {
      a[i] = q[i] = 0.f;
#pragma unroll
      for (int k = 0; k < 6; ++k) {
        const float f = (float)(_Float16)(s1 * (i & 1 ? bf_hi(v[k][i >> 1]) : bf_lo(v[k][i >> 1]))) * 0x1p24f;
        a[i] += f; q[i] = __builtin_fmaf(f, f, q[i]);
      }
      a[i] = dpp_add<0x118, 0xf>(dpp_add<0x114, 0xf>(a[i])); q[i] = dpp_add<0x118, 0xf>(dpp_add<0x114, 0xf>(q[i]));
    }
    if ((threadIdx.x & 12) == 12) {
#pragma unroll
      for (int i = 0; i < 2; ++i) {
        *(f32x4*)&red[0][threadIdx.x >> 4][c8 + 4 * i] = (f32x4){a[4 * i], a[4 * i + 1], a[4 * i + 2], a[4 * i + 3]};
        *(f32x4*)&red[1][threadIdx.x >> 4][c8 + 4 * i] = (f32x4){q[4 * i], q[4 * i + 1], q[4 * i + 2], q[4 * i + 3]};
      }
    }
    __syncthreads();
    if (threadIdx.x < 64) {
      const int n = threadIdx.x & 31;
      float s = 0.f;
#pragma unroll
      for (int i = 0; i < 16; ++i) s += red[threadIdx.x >> 5][i][n];
      const float c = (0.f - (float)K1_ZC * s) * 0x1p-48f;
      char* p = g_buf + t * K1_GST + 5120;
      if (threadIdx.x < 32) *(f32x4*)(p + 272 * (n >> 4) + (n & 15) * 16) = (f32x4){c, c, c, c};
      const bool any = __ballot(threadIdx.x >= 32 && !(s >= 384.f * 64 * 64 && s <= 384.f * 1024 * 1024)) != 0;
      if (threadIdx.x == 0) g_buf[G_FLAG + 4 * 98 + t] = any;
    }
    K0_END();
    return;
  }
  if (bx >= 145 + 24 + 96) {
    if (sw) ssc = scales(w1, (const u16*)x, dw);
    __syncthreads();
    const Sc sc = ssc;
    const unsigned e = (bx - 265) * 256 + threadIdx.x;
    const float u = 1.0f / (sc.g * sc.w1);
    float r[2];
#pragma unroll
    for (int i = 0; i < 2; ++i) {
      const unsigned c = 2 * e + i;
      r[i] = __builtin_fmaf(sc.h, gelu_tanh(u * __uint_as_float((c >> 12) << 31 | ((c & 4095) + 79 * 128) << 16 | 0x8000u)), sc.c * U24);
      if (!(r[i] >= 0.f && r[i] < 8189.f * U24)) r[i] = __builtin_inff();
    }
    *(unsigned*)(g_buf + K1_G_LUT + 4 * e) = pack_f16(r[0], r[1]);
    K0_END();
    return;
  }
  if (bx >= 145 + 24) {
    __shared__ __attribute__((aligned(16))) u16 tw[32][200];
    const int kappa = (bx - 169) >> 1, nh = (bx - 169) & 1;
    u32x4 sm, v[3];
    if (sw) sm = w_load(w2);
#pragma unroll
    for (int i = 0; i < 3; ++i) { const int e = threadIdx.x + 256 * i; v[i] = *(const u32x4*)(w2 + (size_t)(32 * kappa + e / 24) * CH + 192 * nh + (e % 24) * 8); }
    if (sw) ss2 = w2_scale(w_ms(sm));
#pragma unroll
    for (int i = 0; i < 3; ++i) { const int e = threadIdx.x + 256 * i; *(u32x4*)&tw[e / 24][(e % 24) * 8] = v[i]; }
    __syncthreads();
    const int lane = threadIdx.x & 63, g = lane >> 4;
    const float s2 = 256.f * ss2;
    bool bad = false;
#pragma unroll
    for (int i = 0; i < 3; ++i) {
      const int fi = (threadIdx.x >> 6) + 4 * i, n = 64 * (fi >> 2) + 4 * (lane & 15) + (fi & 3);
      float wv[8];
      unsigned bits = 0;
#pragma unroll
      for (int j = 0; j < 8; ++j) {
        wv[j] = ld_bf16(&tw[16 * (j >> 2) + 4 * g + (j & 3)][n]);
        bits |= wv[j] != 0.f ? 256u << j : 0u;
        wv[j] *= s2;
        bits |= fabsf(wv[j]) >= 48 * 256 * U24 ? 1u << j : 0u;
        bad |= !(fabsf(wv[j]) <= 500.f);
      }
      const unsigned o = (((kappa & ~1) + fi / 6) * 24 + nh * 12 + (fi % 6) * 2 + (kappa & 1)) * 1024 + lane * 16;
      st16_wt(g_w2r + o, (u32x4){pack_f16(wv[0], wv[1]), pack_f16(wv[2], wv[3]), pack_f16(wv[4], wv[5]), pack_f16(wv[6], wv[7])});
      constexpr float q = 1.0f / 256;
      if (kappa < 8) st16_wt(g_w2q + o, (u32x4){pack_f16(q * wv[0], q * wv[1]), pack_f16(q * wv[2], q * wv[3]), pack_f16(q * wv[4], q * wv[5]), pack_f16(q * wv[6], q * wv[7])});
      static_for<4>([&](auto ic) { bits |= __builtin_amdgcn_update_dpp(0u, bits, 0x120 + (1 << decltype(ic)::value), 0xf, 0xf, false); });
      bad |= ((bits >> 8) & ~bits & 255u) != 0;
    }
    const bool any = __ballot(bad) != 0;
    if (lane == 0) ((char*)(g_buf + G_FLAG))[4 * (bx - 169) + (threadIdx.x >> 6)] = any;
    K0_END();
    return;
  }
  if (bx >= 145) {
    __shared__ __attribute__((aligned(16))) float red[16][64];
    const int c0 = 64 * ((bx - 145) >> 2), rg = (bx - 145) & 3, c8 = (threadIdx.x & 7) * 8;
    float a[8];
#pragma unroll
    for (int i = 0; i < 8; ++i) a[i] = 0.f;
#pragma unroll
    for (int k = 0; k < 12; ++k) {
      const int ch = 384 * rg + 32 * k + (threadIdx.x >> 3);
      const float b = ld_bf16(grn_b + ch);
      const u32x4 v = *(const u32x4*)(w2 + (size_t)ch * CH + c0 + c8);
#pragma unroll
      for (int i = 0; i < 4; ++i) { a[2 * i] = __builtin_fmaf(b, bf_lo(v[i]), a[2 * i]); a[2 * i + 1] = __builtin_fmaf(b, bf_hi(v[i]), a[2 * i + 1]); }
    }
#pragma unroll
    for (int i = 0; i < 8; ++i) a[i] = dpp_add<0x118, 0xf>(a[i]);
    if (threadIdx.x & 8) {
      *(f32x4*)&red[threadIdx.x >> 4][c8] = (f32x4){a[0], a[1], a[2], a[3]};
      *(f32x4*)&red[threadIdx.x >> 4][c8 + 4] = (f32x4){a[4], a[5], a[6], a[7]};
    }
    __syncthreads();
    if (threadIdx.x < 64) {
      float s = 0.f;
#pragma unroll
      for (int i = 0; i < 16; ++i) s += red[i][threadIdx.x];
      g_cs[(4 + rg) * 384 + cs_index(c0 + threadIdx.x)] = s;
      g_cs[64 * (bx - 145) + threadIdx.x] = 0.f;
    }
    K0_END();
    return;
  }
  const unsigned bid = bx;
  if (bid == 0) {
    if (sw) ssc = scales(w1, (const u16*)x, dw);
    if ((threadIdx.x >> 6) == 2) ss2 = w2_scale(w_ms(w_load(w2)));
#pragma unroll
    for (int i = 0; i < 4; ++i) if (threadIdx.x + 256 * i < 896) *(u32x4*)(g_buf + K1_G_CNT + (threadIdx.x + 256 * i) * 16) = (u32x4){0u, 0u, 0u, 0u};
    *(unsigned*)(g_buf + G_PUB + threadIdx.x * 256) = 0u;
    const int e = threadIdx.x, e2 = e < 192 ? 32 * (e >> 4) + 8 * ((e & 15) >> 2) : 8 * min(e - 192, 47);
    const u32x4 d = *(const u32x4*)(dw + e2);
    __syncthreads();
    const Sc sc = ssc;
    const float s2 = ss2;
    u32x4 v;
#pragma unroll
    for (int k = 0; k < 4; ++k) v[k] = pack_bf16(sc.zs * bf_lo(d[k]), sc.zs * bf_hi(d[k]));
    if (e < 192) {
#pragma unroll
      for (int k = 0; k < 4; ++k) v[k] &= (k & 1) != ((e >> 1) & 1) ? 0u : e & 1 ? 0xffff0000u : 0xffffu;
    }
    if (e >= 240) v = (u32x4){__float_as_uint(EPS * sc.zs * sc.zs), __float_as_uint(sc.vhi), __float_as_uint(sc.g), __float_as_uint(sc.vlo)};
    *(u32x4*)(g_buf + K1_G_TAB + (e < 192 ? e : e + 192) * 16) = v;
    auto sane = [](float v) { return v >= 0x1p-45f && v <= 0x1p20f; };
    if (threadIdx.x < 2) ((unsigned*)(g_buf + G_FLAG))[96 + threadIdx.x] = !(sane(sc.w1) && sane(sc.zs) && sane(sc.g) && sane(sc.h) && sane(s2));
    if (threadIdx.x == 0) {
      const float ik = 1.0f / (sc.h * s2);
      *(f32x4*)g_sc = (f32x4){sc.h * s2, ik, 0x1p-28f / (sc.h * sc.h), -sc.c * U24 * ik};
      g_sc[4] = __uint_as_float(pack_f16(-sc.c * U24, -sc.c * U24));
      *(unsigned*)(g_buf + K1_G_CNT + 16320) = pack_f16(-sc.c * 0x1p-10f, -sc.c * 0x1p-10f);
    }
    if (threadIdx.x < 48) *(u32x4*)(g_buf + threadIdx.x * K1_GST + 5376) = (u32x4){pack_f16(sc.c * U24, 0.f), 0u, 0u, 0u};
    K0_END();
    return;
  }
  __shared__ __attribute__((aligned(16))) u16 tl[32][136];
  const int e = (bid - 1) * 256 + threadIdx.x, ks = (bid - 1) % 12, cb = (bid - 1) / 12;
  u32x4 sm, v[2];
  if (sw) sm = w_load(w1);
#pragma unroll
  for (int i = 0; i < 2; ++i) v[i] = *(const u32x4*)(w1 + (size_t)(32 * ks + (threadIdx.x >> 4) + 16 * i) * HID + 128 * cb + (threadIdx.x & 15) * 8);
  if (sw) ss2 = w1_scale(w_ms(sm));
#pragma unroll
  for (int i = 0; i < 2; ++i) *(u32x4*)&tl[(threadIdx.x >> 4) + 16 * i][(threadIdx.x & 15) * 8] = v[i];
  if (e < (int)(K1_NREP + 1) * 768) *(u32x4*)(g_buf + K1_G_ACC + e * 16) = (u32x4){0u, 0u, 0u, 0u};
  __syncthreads();
  const float s1 = ss2;
#pragma unroll
  for (int i = 0; i < 2; ++i) {
    const int lane = threadIdx.x & 63, f = (threadIdx.x >> 6) + 4 * i, n = 16 * f + (lane & 15), k0 = 8 * (lane >> 4);
    u32x4 P;
#pragma unroll
    for (int jj = 0; jj < 4; ++jj) P[jj] = pack_f16(s1 * ld_bf16(&tl[k0 + 2 * jj][n]), s1 * ld_bf16(&tl[k0 + 2 * jj + 1][n]));
    const int fs = (f & 1) * 12 + ks;
    st16_wt(g_buf + (4 * cb + (f >> 1)) * K1_GST + (fs + (fs >= 5)) * 1024 + lane * 16, P);
  }
  K0_END();
}

constexpr unsigned G_ACC2 = K1_G_ACC + K1_NREP * 12288;

#define VA_CLOB \
  "v0", "v1", "v2", "v3", "v4", "v5", "v6", "v7", "v8", "v9", "v10", "v11", "v12", "v13", "v14", "v15", "v16", "v17", "v18", "v19", "v20", "v21", "v22", "v23", "v24", "v25", "v26", "v27", "v28", "v29", "v30", "v31", \
  "v32", "v33", "v34", "v35", "v36", "v37", "v38", "v39", "v40", "v41", "v42", "v43", "v44", "v45", "v46", "v47", "v48", "v49", "v50", "v51", "v52", "v53", "v54", "v55", "v56", "v57", "v58", "v59", "v60", "v61", "v62", "v63", \
  "v64", "v65", "v66", "v67", "v68", "v69", "v70", "v71", "v72", "v73", "v74", "v75", "v76", "v77", "v78", "v79", "v80", "v81", "v82", "v83", "v84", "v85", "v86", "v87", "v88", "v89", "v90", "v91", "v92", "v93", "v94", "v95", \
  "v96", "v97", "v98", "v99", "v100", "v101", "v102", "v103", "v104", "v105", "v106", "v107", "v108", "v109", "v110", "v111", "v112", "v113", "v114", "v115", "v116", "v117", "v118", "v119", "v120", "v121", "v122", "v123", "v124", "v125", "v126", "v127", \
  "v128", "v129", "v130", "v131", "v132", "v133", "v134", "v135", "v136", "v137", "v138", "v139", "v140", "v141", "v142", "v143", "v144", "v145", "v146", "v147", "v148", "v149", "v150", "v151", "v152", "v153", "v154", "v155", "v156", "v157", "v158", "v159", \
  "v160", "v161", "v162", "v163", "v164", "v165", "v166", "v167", "v168", "v169", "v170", "v171", "v172", "v173", "v174", "v175", "v176", "v177", "v178", "v179", "v180", "v181", "v182", "v183", "v184", "v185", "v186", "v187", "v188", "v189", "v190", "v191", \
  "v192", "v193", "v194", "v195", "v196", "v197", "v198", "v199", "v200", "v201", "v202", "v203", "v204", "v205", "v206", "v207", "v208", "v209", "v210", "v211", "v212", "v213", "v214", "v215", "v216", "v217", "v218", "v219", "v220", "v221", "v222", "v223", \
  "v224", "v225", "v226", "v227", "v228", "v229", "v230", "v231", "v232", "v233", "v234", "v235", "v236", "v237", "v238", "v239", "v240", "v241", "v242", "v243", "v244", "v245", "v246", "v247", "v248", "v249", "v250", "v251", "v252", "v253", "v254", "v255", \
  "a0", "a1", "a2", "a3", "a4", "a5", "a6", "a7", "a8", "a9", "a10", "a11", "a12", "a13", "a14", "a15", "a16", "a17", "a18", "a19", "a20", "a21", "a22", "a23", "a24", "a25", "a26", "a27", "a28", "a29", "a30", "a31", \
  "a32", "a33", "a34", "a35", "a36", "a37", "a38", "a39", "a40", "a41", "a42", "a43", "a44", "a45", "a46", "a47", "a48", "a49", "a50", "a51", "a52", "a53", "a54", "a55", "a56", "a57", "a58", "a59", "a60", "a61", "a62", "a63", \
  "a64", "a65", "a66", "a67", "a68", "a69", "a70", "a71", "a72", "a73", "a74", "a75", "a76", "a77", "a78", "a79", "a80", "a81", "a82", "a83", "a84", "a85", "a86", "a87", "a88", "a89", "a90", "a91", "a92", "a93", "a94", "a95", \
  "a96", "a97", "a98", "a99", "a100", "a101", "a102", "a103", "a104", "a105", "a106", "a107", "a108", "a109", "a110", "a111", "a112", "a113", "a114", "a115", "a116", "a117", "a118", "a119", "a120", "a121", "a122", "a123", "a124", "a125", "a126", "a127", \
  "a128", "a129", "a130", "a131", "a132", "a133", "a134", "a135", "a136", "a137", "a138", "a139", "a140", "a141", "a142", "a143", "a144", "a145", "a146", "a147", "a148", "a149", "a150", "a151", "a152", "a153", "a154", "a155", "a156", "a157", "a158", "a159", \
  "a160", "a161", "a162", "a163", "a164", "a165", "a166", "a167", "a168", "a169", "a170", "a171", "a172", "a173", "a174", "a175", "a176", "a177", "a178", "a179", "a180", "a181", "a182", "a183", "a184", "a185", "a186", "a187", "a188", "a189", "a190", "a191", \
  "a192", "a193", "a194", "a195", "a196", "a197", "a198", "a199", "a200", "a201", "a202", "a203", "a204", "a205", "a206", "a207", "a208", "a209", "a210", "a211", "a212", "a213", "a214", "a215", "a216", "a217", "a218", "a219", "a220", "a221", "a222", "a223", \
  "a224", "a225", "a226", "a227", "a228", "a229", "a230", "a231", "a232", "a233", "a234", "a235", "a236", "a237", "a238", "a239", "a240", "a241", "a242", "a243", "a244", "a245", "a246", "a247", "a248", "a249", "a250", "a251", "a252", "a253", "a254", "a255"
#define K1_CLOB "memory", "vcc", "scc", "s36", "s37", "s38", "s39", "s40", "s41", "s42", "s43", "s44", "s45", "s46", "s47", "s48", "s49", "s50", "s51", "s52", "s53", "s54", "s55", "s56", "s57", "s58", "s59", "s60", "s61", "s62", "s63", "s64", "s65", "s66", "s67", "s68", "s69", "s70", "s71", "s72", "s73", "s74", "s75", "s76", "s77", "s78", "s79", "s80", "s81", "s82", "s83", "s84", "s85", "s86", "s87", "s88", "s89", "s90", "s91", "s92", "s93", "s94", "s95", "s96", "s97", VA_CLOB
DI void k2_params(const u16* x, const char* hws, const u16* out, const u16* grn_g, unsigned tid) {
  const unsigned e = (tid >> 2) * 32 + blockIdx.x * 4 + (tid & 3);
  const unsigned b = e >> 2, w = e & 3, pg = w & 1, nh = w >> 1, q = (b >> 3) * 4 + (b & 3), rs = 128 + (q >> 1), hf = q & 1, side = wg_side(b);
  auto fmask = [&](unsigned region) {
    const unsigned o = ((unsigned)(size_t)hws & 0x1fffffu) + region * H_RS + nh * H_TS;
    const unsigned* p = (const unsigned*)(g_buf + K1_G_LOC + 64) + (o >> 21);
    const unsigned r = 0x200000u + 2 * H_TS - 1 - (o & 0x1fffffu), d1 = ((r >> 15) * 171) >> 9, d2 = (((r + 0x200000u) >> 15) * 171) >> 9;
    const unsigned a1 = ~0u << d1, a2 = d2 < 32 ? ~0u << d2 : 0u;
    return ((__popc(p[0]) > 2 ? ~a1 : 0u) | (__popc(p[1]) > 2 ? a1 & ~a2 : 0u) | (__popc(p[2]) > 2 ? a2 : 0u)) ^ (0u - side);
  };
  const unsigned f0 = fmask(q), f1 = fmask(rs);
  const unsigned fx = ((side ^ x_flip(x, q)) ? ~K2_SHT : 0u) | ((side ^ x_flip(x, rs)) ? K2_SHT : 0u);
  const uint64_t h0 = (uint64_t)hws + q * H_RS + nh * (H_TS + 2048) + pg * 3072, h1 = (uint64_t)hws + rs * H_RS + nh * (H_TS + 2048) + hf * 3072 + pg * 1536;
  const unsigned p0 = (12 * q + 2 * pg) * 49152 + nh * 384, p1 = (12 * rs + 2 * hf + pg) * 49152 + nh * 384;
  const unsigned e1 = 1024 + (e >> 3);
  auto kap = [](unsigned f) { return f / 48 * 2 + (f & 1); };
  auto sco = [&](unsigned f) { return kap(f) * 64 + (kap(f) < 16 ? K2_S16_D0 : 0); };
  auto cso = [&](unsigned f) { const unsigned r = f % 48; return ((f < 192 ? min(f / 48, 2u) : 3) * 384 + (r / 12 % 2 * 12 + r / 24 * 6 + r % 12 / 2) * 16) * 4; };
  typedef uint64_t u64x2 __attribute__((ext_vector_type(2)));
  u64x2* P = (u64x2*)(g_prm + 64 * e);
  P[0] = (u64x2){f0 | (uint64_t)f1 << 32, fx | (uint64_t)(sco(e) | (sco(e1) + (e & 6) * 8) << 16) << 32};
  const u32x4 sc = *(const u32x4*)g_sc;
  P[1] = (u64x2){(((b >> 3) * 4 + w) & 7) * 1024 | (uint64_t)sc[0] << 32, sc[1] | (uint64_t)sc[2] << 32};
  P[2] = (u64x2){h0, h0 + 8192};
  P[3] = (u64x2){h1, h1 + 8192};
  P[4] = (u64x2){__float_as_uint(g_sc[4]) | (uint64_t)((e1 - e) * 1024 + (e & 7) * 128) << 32, 0};
  P[5] = (u64x2){(uint64_t)x + p0, (uint64_t)x + p1};
  P[6] = (u64x2){(uint64_t)out + p0, (uint64_t)out + p1};
  P[7] = (u64x2){0x7fffffffffffffull, (uint64_t)g_buf + G_FLAG};
  P[8] = (u64x2){(uint64_t)g_w2f + w * 6144, (uint64_t)g_w2q + w * 6144};
  P[9] = (u64x2){(uint64_t)g_w2r + e * 1024, (uint64_t)g_w2f + e * 1024};
  P[10] = (u64x2){(uint64_t)grn_g + w * 256, (uint64_t)g_cs + w * 384 + 2048};
  P[11] = (u64x2){(uint64_t)g_buf + G_PUB + b * 256, (uint64_t)g_buf + G_PUB + w * 16384};
  P[12] = (u64x2){cso(e) | (uint64_t)(cso(e1) + (e & 1) * 32) << 32, (uint64_t)g_cs};
  P[13] = (u64x2){sc[3] | (uint64_t)1 << (32 + (b >> 3) % K2_NAT), 0};
}
__global__ __launch_bounds__(256, 1) void k1_fc1(const u16* __restrict__ x, char* __restrict__ hws, const u16* __restrict__ out, const u16* __restrict__ grn_g) {
  extern __shared__ __attribute__((aligned(16))) char smem[];
  const unsigned w = __builtin_amdgcn_readfirstlane(threadIdx.x >> 6);
  if (blockIdx.x < 8) {
    if (w < 2) k2_params(x, hws, out, grn_g, w * 64 + __builtin_amdgcn_mbcnt_hi(~0u, __builtin_amdgcn_mbcnt_lo(~0u, 0u)));
    return;
  }
  const unsigned lds0 = lds_addr(smem);
  const unsigned b = blockIdx.x - 8;
  const unsigned dir = (b >> 3) & 1;
  TS(0); MK_START(1);
  const uint64_t xp = (uint64_t)x, hp = (uint64_t)hws, gp = (uint64_t)g_buf, wp = gp + w * K1_WCH;
  const unsigned d = dir ? ~0u : 1u, wrap = dir ? ~0u : 48u, rst = dir ? 47u : 0u, pos0 = dir ? K1_SMAX - 1 : 0u;
  const unsigned xo0 = (12 * ((b >> 3) * 4 + (b & 3)) + w) * 49152, xs1 = (1536 + w + (dir ? 8 : 0)) * 49152, sb1 = 1536 * w + (dir ? K1_SB1 : 0);
  const unsigned r0o = ((b >> 3) * 4 + (b & 3)) * H_RS, tsel = 4 * (b & 3), s2 = (b >> 2) & 1;
  const uint64_t ap = gp + K1_G_ACC + (b % K1_NREP) * 12288;
  const unsigned ca0 = w == 0 ? dir * 64 : K1_G_DUM - K1_G_CNT + (4 * b + w) * 64;
  const unsigned pm = lds0 + K1_PF_OFF, pf = ((b >> 3) * 4 + w) * 1024;
  unsigned nb;
  asm volatile(
R"(.macro M t,f,w
.if \f < 30
v_mfma_f32_16x16x32_f16 v[56+4*\t:56+4*\t+3], v[128+4*\f:128+4*\f+3], a[216+4*\w:216+4*\w+3], v[56+4*\t:56+4*\t+3]
.else
v_mfma_f32_16x16x32_f16 v[56+4*\t:56+4*\t+3], a[4*\f-4*30:4*\f-4*30+3], a[216+4*\w:216+4*\w+3], v[56+4*\t:56+4*\t+3]
.endif
.endm
.macro Z t,f,w
.if \f < 30
v_mfma_f32_16x16x32_f16 v[56+4*\t:56+4*\t+3], v[128+4*\f:128+4*\f+3], a[216+4*\w:216+4*\w+3], v[252:255]
.else
v_mfma_f32_16x16x32_f16 v[56+4*\t:56+4*\t+3], a[4*\f-4*30:4*\f-4*30+3], a[216+4*\w:216+4*\w+3], v[252:255]
.endif
.endm
.macro LZ a,f
.if \f < 30
v_mfma_f32_16x16x32_bf16 v[36:36+3], v[\a:\a+3], v[128+4*\f:128+4*\f+3], 0
.else
v_mfma_f32_16x16x32_bf16 v[36:36+3], v[\a:\a+3], a[4*\f-4*30:4*\f-4*30+3], 0
.endif
.endm
.macro LS a,f
.if \f < 30
v_mfma_f32_16x16x32_bf16 v[36:36+3], v[\a:\a+3], v[128+4*\f:128+4*\f+3], v[36:36+3]
.else
v_mfma_f32_16x16x32_bf16 v[36:36+3], v[\a:\a+3], a[4*\f-4*30:4*\f-4*30+3], v[36:36+3]
.endif
.endm
.macro LD d,a,f
.if \f < 30
v_mfma_f32_16x16x32_bf16 v[\d:\d+3], a[\a:\a+3], v[128+4*\f:128+4*\f+3], v[36:36+3]
.else
v_mfma_f32_16x16x32_bf16 v[\d:\d+3], a[\a:\a+3], a[4*\f-4*30:4*\f-4*30+3], v[36:36+3]
.endif
.endm
.macro GZ f
.if \f < 30
v_mfma_f32_16x16x32_f16 v[52:55], v[128+4*\f:128+4*\f+3], v[128+4*\f:128+4*\f+3], 0
.else
v_mfma_f32_16x16x32_f16 v[52:55], a[4*\f-4*30:4*\f-4*30+3], a[4*\f-4*30:4*\f-4*30+3], 0
.endif
.endm
.macro GS f
.if \f < 30
v_mfma_f32_16x16x32_f16 v[52:55], v[128+4*\f:128+4*\f+3], v[128+4*\f:128+4*\f+3], v[52:55]
.else
v_mfma_f32_16x16x32_f16 v[52:55], a[4*\f-4*30:4*\f-4*30+3], a[4*\f-4*30:4*\f-4*30+3], v[52:55]
.endif
.endm
.macro CL p,z,s
v_fma_mixlo_f16 v[\p], v[\z], v[\s], 0 op_sel_hi:[0,1,0]
.endm
.macro CH p,z,s
v_fma_mixhi_f16 v[\p], v[\z], v[\s], 0 op_sel:[0,1,0] op_sel_hi:[0,1,0]
.endm
.macro XL f,o
.if \f < 30
global_load_dwordx4 v[128+4*\f:128+4*\f+3], v3, s[46:47] offset:\o
.else
global_load_dwordx4 a[4*\f-4*30:4*\f-4*30+3], v3, s[46:47] offset:\o
.endif
.endm
.macro XN f,o
.if \f < 30
global_load_dwordx4 v[128+4*\f:128+4*\f+3], v3, s[46:47] offset:\o nt
.else
global_load_dwordx4 a[4*\f-4*30:4*\f-4*30+3], v3, s[46:47] offset:\o nt
.endif
.endm
.macro F t,z
v_fma_f32 v[\t], -v[\z], v[\z], s36
.endm
.macro U t,z
v_mul_f32 v[\t], v[\z], v[\t]
.endm
.macro E t
v_exp_f32 v[\t], v[\t]
.endm
.macro A t
v_add_f32 v[\t], 1.0, v[\t]
.endm
.macro R t
v_rcp_f32 v[\t], v[\t]
.endm
.macro C d,a,b
v_cvt_pk_bf16_f32 v[\d], v[\a], v[\b]
.endm
.macro S0 q,t
v_mul_f32 v[\q], v[\t], v[\t]
.endm
.macro S q,t
v_fmac_f32 v[\q], v[\t], v[\t]
.endm
.macro HC d,z
v_cvt_pk_f16_f32 v[\d], v[\z], v[\z+1]
.endm
.macro HF t,p
v_pk_fma_f16 v[\t], v[\p], v[\p], s36 neg_lo:[1,0,0] neg_hi:[1,0,0]
.endm
.macro HM t,p
v_pk_mul_f16 v[\t], v[\p], v[\t]
.endm
.macro HE d,s
v_exp_f16 v[\d], v[\s]
.endm
.macro HG d,s
v_exp_f16_sdwa v[\d], v[\s] dst_sel:WORD_1 dst_unused:UNUSED_PRESERVE src0_sel:WORD_1
.endm
.macro HA t
v_pk_add_f16 v[\t], v[\t], 1.0 op_sel_hi:[1,0]
.endm
.macro HR d,s
v_rcp_f16 v[\d], v[\s]
.endm
.macro HQ d,s
v_rcp_f16_sdwa v[\d], v[\s] dst_sel:WORD_1 dst_unused:UNUSED_PRESERVE src0_sel:WORD_1
.endm
.macro HP d,s
v_rcp_f16_sdwa v[\d], v[\s] dst_sel:DWORD dst_unused:UNUSED_PAD src0_sel:WORD_1
.endm
.macro HY t,z
v_fma_mix_f32 v[\t], v[\z], v[\t], 0 op_sel_hi:[0,1,0]
.endm
.macro HK p
v_and_b32 v[\p], s56, v[\p]
.endm
.macro LQ p
v_lshrrev_b32 v[\p], 2, v[\p]
.endm
.macro LL t,z
v_lshlrev_b32_sdwa v[\t], 1, v[\z] dst_sel:DWORD dst_unused:UNUSED_PAD src0_sel:DWORD src1_sel:WORD_1
.endm
.macro LH t,p
v_and_b32_sdwa v[\t], s56, v[\p] dst_sel:DWORD dst_unused:UNUSED_PAD src0_sel:DWORD src1_sel:WORD_1
.endm
.macro LA t
ds_read_u16 v[\t], v[\t] offset:5376
.endm
.macro LC_ t
ds_read_u16_d16_hi v[\t], v[\t] offset:5376
.endm
.macro LO p,a,b
v_bfi_b32 v[\p], s36, v[\a], v[\b]
.endm
.macro HZ q,p
v_pk_mul_f16 v[\q], v[\p], v[\p]
.endm
.macro HS q,p
v_pk_fma_f16 v[\q], v[\p], v[\p], v[\q]
.endm
.macro HT q,p
v_fma_mix_f32 v[\q], v[\p], v[\p], v[\q] op_sel:[1,1,0] op_sel_hi:[1,1,0]
.endm
.macro HX q
v_fma_mix_f32 v[\q], v[\q], 1.0, v[\q] op_sel:[0,0,1] op_sel_hi:[1,0,1]
.endm
.macro UX t,z
v_mul_f32_e64 v[\t], v[\z], v[\t]
.endm
.macro EX t
v_exp_f32_e64 v[\t], v[\t]
.endm
.macro AX t
v_add_f32_e64 v[\t], 1.0, v[\t]
.endm
.macro RX t
v_rcp_f32_e64 v[\t], v[\t]
.endm
.macro TX q,t
v_mul_f32_e64 v[\q], v[\t], v[\t]
.endm
.macro SX q,t
v_fmac_f32_e64 v[\q], v[\t], v[\t]
.endm
.macro AW a,s
v_accvgpr_write_b32 a[\a], v[\s]
.endm
.macro RA d,v,o
ds_read_b128 a[\d:\d+3], v[\v] offset:\o
.endm
.macro RV d,v,o
ds_read_b128 v[\d:\d+3], v[\v] offset:\o
.endm
.macro W2 a,b,o
ds_write2st64_b32 v4, v[\a], v[\b] offset0:\o offset1:\o+4
.endm
.macro W1 a,o
ds_write_b32 v4, v[\a] offset:\o
.endm
.macro DW o
global_load_lds_dwordx4 v0, s[40:41] offset:\o
.endm
.macro DF
global_load_lds_dwordx4 v0, s[70:71]
.endm
.macro DX
global_load_lds_dword v3, s[46:47]
.endm
.macro ST d
.if ((\d - 104) / 4) & 1
global_store_dwordx4 v2, v[\d:\d+3], s[42:43] nt sc1
.else
global_store_dwordx2 v31, v[\d:\d+1], s[42:43] nt sc1
.endif
.endm
.macro SU d
.if ((\d - 104) / 4) & 1
global_store_dwordx4 v2, v[\d:\d+3], s[42:43]  sc1
.else
global_store_dwordx2 v31, v[\d:\d+1], s[42:43]  sc1
.endif
.endm
.macro SP
global_atomic_add_f64 v6, v[14:15], s[48:49]
.endm
.macro AT
global_atomic_add v11, v9, v10, s[68:69] sc0
.endm
.macro PFA
s_getpc_b64 s[70:71]
s_add_u32 s70, s70, 7f-.
s_addc_u32 s71, s71, 0
s_andn2_b32 s70, s70, 0x7f
s_min_u32 s53, %[pf], 8f-7f-1152
s_andn2_b32 s53, s53, 0x7f
s_add_u32 s70, s70, s53
s_addc_u32 s71, s71, 0
.endm
.macro PFN
s_getpc_b64 s[70:71]
s_add_u32 s70, s70, k2_code@rel32@lo-1020
s_addc_u32 s71, s71, k2_code@rel32@hi-1012
s_add_u32 s70, s70, %[pf]
s_addc_u32 s71, s71, 0
s_andn2_b32 s70, s70, 0x7f
.endm
.macro PFW
s_getpc_b64 s[70:71]
s_add_u32 s70, s70, g_w2q@rel32@lo+4
s_addc_u32 s71, s71, g_w2q@rel32@hi+12
s_add_u32 s70, s70, %[pf]
s_addc_u32 s71, s71, 0
.endm
.macro PF0
s_getpc_b64 s[70:71]
s_andn2_b32 s70, s70, 0x7f
s_min_u32 s53, %[pf], 7f-.-1152
s_andn2_b32 s53, s53, 0x7f
s_add_u32 s70, s70, s53
s_addc_u32 s71, s71, 0
.endm
.macro WL n
s_waitcnt lgkmcnt(\n)
.endm
.macro WV n
s_waitcnt vmcnt(\n)
.endm
.macro LU t,p
v_pk_fma_f16 v[\t], v[\p], v34, s67
.endm
.macro CF d,a,b
v_cvt_pk_f16_f32 v[\d], v[\a], v[\b]
.endm
.macro XS d
v_pk_fma_f16 v[\d], v[\d], v13, s56
.endm
.macro AR d,a
v_accvgpr_read_b32 v[\d], a[\a]
.endm
.macro DQ o
global_load_lds_dword v23, s[40:41] offset:\o
.endm
.macro GE
v_cmp_eq_u32 vcc, 0, v35
s_nop 4
s_cbranch_vccz 9f
v_mov_b32 v35, 14720
s_mov_b64 exec, 1
global_store_dword v35, v10, s[68:69] sc1
s_mov_b64 exec, -1
9:
.endm
.macro BW f
.if \f < 30
ds_write_b128 v12, v[128+4*\f:128+4*\f+3]
.else
ds_write_b128 v12, a[4*\f-120:4*\f-120+3]
.endif
.endm
.macro BR f
.if \f < 30
ds_read_b128 v[128+4*\f:128+4*\f+3], v20
.else
ds_read_b128 a[4*\f-120:4*\f-120+3], v20
.endif
.endm
.macro LK d,a,f,h
.if \f < 30
v_mfma_f32_4x4x4_16b_bf16 v[\d:\d+3], a[\a:\a+1], v[128+4*\f+\h:128+4*\f+\h+1], v[36:39]
.else
v_mfma_f32_4x4x4_16b_bf16 v[\d:\d+3], a[\a:\a+1], a[4*\f-120+\h:4*\f-120+\h+1], v[36:39]
.endif
.endm
.macro PK d,a,b,s
v_perm_b32 v[\d], v[\a], v[\b], v[\s]
.endm
.macro PO d
v_lshl_or_b32 v[\d], v[\d], 4, v[\d-4]
.endm
.macro LG a,b
v_cmp_eq_f32 vcc, v[\a], v[\b]
v_mov_b32 v[\b], 0x7f800000
s_nop 0
v_cndmask_b32 v[\a], v[\b], v[\a], vcc
.endm
.macro DY
s_mov_b32 s72, m0
s_mov_b32 m0, %[pm]
s_nop 0
global_load_lds_dword v3, s[46:47]
s_mov_b32 m0, s72
s_nop 0
.endm
.macro FP
ds_write_b32 v6, v19 offset:61440
.endm
.macro FR
ds_read_b32 v15, v31 offset:61440
.endm
k1_code:
v_mbcnt_lo_u32_b32 v16, -1, 0
v_mbcnt_hi_u32_b32 v16, -1, v16
v_lshlrev_b32 v0, 4, v16
v_and_b32 v17, 15, v16
v_lshrrev_b32 v18, 4, v16
s_add_u32 s68, %[glo], 1239040
s_addc_u32 s69, %[ghi], 0
s_mov_b32 s53, %[r0o]
s_load_dword s67, s[68:69], 16320
s_add_u32 s82, %[hlo], s53
s_addc_u32 s83, %[hhi], 0
s_add_u32 s84, s82, 8192
s_addc_u32 s85, s83, 0
s_and_b32 s90, %[hlo], 0x1fffff
s_add_u32 s90, s90, s53
s_mov_b32 s91, 3072
v_mov_b32 v11, s90
v_mad_u32_u24 v11, v0, s91, v11
v_lshrrev_b32 v11, 21, v11
v_lshl_add_u32 v11, v11, 2, 64
v_writelane_b32 v11, %[tsel], 63
s_sub_u32 s53, s82, %[hlo]
s_lshr_b32 s53, s53, 2
s_and_b32 s90, %[xlo], 0x1fffff
s_add_u32 s53, s53, s90
s_add_u32 s53, s53, 0x20048000
s_lshr_b32 s53, s53, 21
s_lshl_b32 s90, s53, 2
v_writelane_b32 v11, s90, 48
global_load_dword v11, v11, s[68:69] offset:-2048
v_lshlrev_b32 v19, 1, v18
s_movk_i32 s53, 0x78
v_bfe_u32 v21, v16, 2, 2
v_lshrrev_b32 v19, v19, s53
v_mul_u32_u24 v21, 0x300, v21
v_xor_b32 v19, v19, v16
v_and_b32 v19, 3, v19
v_lshl_add_u32 v21, v19, 4, v21
s_mov_b32 s97, 3072
s_mov_b32 s50, %[pos0]
s_mov_b32 s51, %[pos0]
s_mov_b32 s52, %[pos0]
s_add_u32 s74, %[lds], 0
s_add_u32 s75, s74, 163840
s_mov_b32 s37, s74
s_mul_i32 s53, %[w], 6400
s_add_u32 s76, s74, s53
s_add_u32 s77, s76, 163840
s_mov_b32 s38, s76
s_mul_i32 s60, %[w], 1536
s_add_u32 s61, s60, 512
s_add_u32 s62, s60, 16384
s_add_u32 s63, s60, 16896
s_add_u32 s64, s60, 32768
s_add_u32 s65, s60, 33280
v_lshlrev_b32 v23, 2, v16
s_lshl_b32 s53, %[w], 11
s_min_u32 s53, s53, 5120
s_add_u32 s72, s53, 1228800
s_add_u32 s40, %[glo], s72
s_addc_u32 s41, %[ghi], 0
s_add_u32 s53, s53, %[lds]
s_add_u32 m0, s53, 58368
s_nop 0
DW 0
DW 1024
s_nop 0 ;PS
s_mul_i32 s53, s50, 25600
s_add_u32 s40, %[wlo], s53
s_addc_u32 s41, %[whi], 0
)"
R"(s_mov_b32 m0, s38
s_nop 0
DW 0
DW 1024
DW 2048
s_add_u32 s40, s40, 3072
s_addc_u32 s41, s41, 0
s_add_u32 m0, s38, 3072
s_nop 0
DW 0
DW 1024
DW 2048
DQ 3072
s_add_u32 s50, s50, %[d]
s_cmp_eq_u32 s50, %[wrap]
s_cselect_b32 s50, %[rst], s50
s_add_u32 s38, s38, 32768
s_cmp_eq_u32 s38, s77
s_cselect_b32 s38, s76, s38
v_add_u32 v1, s37, v0
v_lshl_add_u32 v19, v18, 2, v17
v_and_b32 v19, 15, v19
v_lshlrev_b32 v19, 4, v19
v_lshl_add_u32 v2, v18, 8, v19
s_lshl_b32 s53, %[w], 2
s_add_u32 s53, s53, %[lds]
s_add_u32 s53, s53, 123904
s_add_u32 s53, %[lds], 123904
v_bfe_u32 v4, v17, 1, 2
v_and_b32 v5, 8, v17
v_add3_u32 v4, v4, v5, %[w]
v_lshl_add_u32 v4, v18, 2, v4
v_and_b32 v4, 15, v4
v_lshl_add_u32 v4, v17, 4, v4
v_lshl_add_u32 v4, v4, 2, s53
s_lshl_b32 s53, %[w], 9
s_add_u32 s53, s53, %[lds]
s_add_u32 s53, s53, 123904
v_lshlrev_b32 v5, 3, v16
v_add_u32 v5, s53, v5
s_lshl_b32 s53, %[w], 6
v_and_b32 v6, 0x38, v16
v_add_u32 v6, s53, v6
s_add_u32 s53, %[lds], 58368
v_and_b32 v19, 3, v16
v_lshl_add_u32 v19, v18, 2, v19
v_lshl_add_u32 v7, v19, 4, s53
v_lshlrev_b32 v8, 4, v18
v_add_u32 v8, s53, v8
v_mov_b32 v10, 1
s_mov_b32 s36, 0xffff
s_mov_b32 s56, 0x5550555
s_mov_b32 s39, 32768
s_mov_b32 s54, 0x01010101
s_mov_b32 s55, 0x01010101
s_mov_b32 %[nb], 0
v_and_b32 v28, 12, v17
v_lshl_add_u32 v28, v28, 2, v17
v_lshlrev_b32 v28, 2, v28
v_mov_b32 v34, 0x74007400
v_mov_b32 v35, -1
v_and_b32 v22, 0xfffffcff, v1
v_lshrrev_b32 v31, 1, v2
v_mov_b32 v32, 0x06020400
v_mov_b32 v33, 0x07030501
s_waitcnt lgkmcnt(0)
s_mov_b32 s78, 0xaaaaaaaa
s_mov_b32 s79, 0xaaaaaaaa
s_mov_b32 s80, 0xcccccccc
s_mov_b32 s81, 0xcccccccc
s_and_b32 s53, %[w], 1
s_lshl_b32 s53, s53, 10
s_lshr_b32 s72, %[w], 1
s_mul_i32 s72, s72, 32768
s_add_u32 s72, s72, s53
s_add_u32 s72, s72, 128000
s_movk_i32 s53, 0x78
v_lshrrev_b32 v12, 5, v0
v_and_b32 v12, 6, v12
v_bfe_u32 v12, s53, v12, 2
v_lshrrev_b32 v20, 8, v0
v_xor_b32 v20, v12, v20
v_lshl_add_u32 v20, v20, 4, s72
v_and_b32 v12, 0xf0, v0
v_lshl_add_u32 v20, v12, 2, v20
v_add_u32 v12, s72, v0
WV 9
v_bcnt_u32_b32 v11, v11, 0
v_cmp_lt_u32 s[90:91], 2, v11
s_nop 0
s_lshr_b32 s96, s91, 31
s_xor_b32 s96, s96, %[s2]
s_sub_u32 s53, 0, s96
s_xor_b32 s90, s90, s53
s_xor_b32 s91, s91, s53
s_add_u32 s53, %[xo0], 0
s_add_u32 s46, %[xlo], s53
s_addc_u32 s47, %[xhi], 0
s_bitcmp1_b64 s[90:91], 48
s_cselect_b32 s58, 16, 0
s_lshr_b32 s58, 0xfe987610, s58
v_lshrrev_b32 v3, 6, v0
v_and_b32 v3, 12, v3
v_bfe_u32 v3, s58, v3, 4
v_mad_u32_u24 v3, v3, s97, v21
XL 12,0
XL 13,64
XL 14,128
XL 15,192
XL 16,256
XL 17,320
XL 18,384
XL 19,448
XL 20,512
XL 21,576
XL 22,640
XL 23,704
s_mul_i32 s53, s96, 8320
s_cmp_lt_u32 %[ca0], 128
s_cselect_b32 s53, s53, 0
s_add_u32 s73, s53, %[ca0]
v_mov_b32 v9, s73
WV 19
s_barrier
v_lshrrev_b32 v19, 8, v31
v_sub_u32 v19, 0, v19
ds_write_b32 v31, v19 offset:61440
s_nop 1
s_nop 0
v_mov_b32 v19, 0
WV 11
BW 12
RV 40,8,6144
RV 44,8,6208
RV 48,8,6272
RV 248,8,6336
BR 12
WV 10
BW 13
s_nop 1
BR 13
WL 2
LZ 40,12
WV 9
BW 14
RV 40,8,6400
s_nop 0
BR 14
s_add_u32 s53, %[xo0], 0
s_add_u32 s46, %[xlo], s53
s_addc_u32 s47, %[xhi], 0
s_bitcmp1_b64 s[90:91], 48
s_cselect_b32 s58, 16, 0
s_lshr_b32 s58, 0xda43cb52, s58
v_lshrrev_b32 v3, 6, v0
v_and_b32 v3, 12, v3
v_bfe_u32 v3, s58, v3, 4
v_mad_u32_u24 v3, v3, s97, v21
WL 3
LS 44,13
WV 8
BW 15
RV 44,8,6464
s_nop 0
BR 15
XL 24,0
WL 3
LS 48,14
WV 8
BW 16
RV 48,8,6528
s_nop 0
BR 16
WL 3
LS 248,15
WV 7
BW 17
RV 248,8,6592
s_nop 0
BR 17
XL 25,64
WL 3
LS 40,16
WV 7
BW 18
RV 40,8,6656
s_nop 0
BR 18
XL 26,128
WL 3
LS 44,17
WV 7
BW 19
RV 44,8,6720
s_nop 0
BR 19
WL 3
LS 48,18
WV 6
BW 20
RV 48,8,6784
s_nop 0
BR 20
XL 27,192
WL 3
LS 248,19
WV 6
BW 21
RV 248,8,6848
s_nop 0
BR 21
XL 28,256
WL 3
LS 40,20
WV 6
BW 22
RA 240,7,0
s_nop 0
BR 22
WL 3
LS 44,21
WV 5
BW 23
RA 244,7,256
s_nop 0
BR 23
XL 29,320
WL 3
LS 48,22
RA 248,7,512
XL 30,384
WL 1
LS 248,23
RA 252,7,768
s_nop 6
v_mul_f32 v36, 0xbb2aaaab, v36
v_mul_f32 v37, 0xbb2aaaab, v37
v_mul_f32 v38, 0xbb2aaaab, v38
v_mul_f32 v39, 0xbb2aaaab, v39
s_nop 1
LK 40,240,12,0
LK 44,242,12,2
RA 240,7,1024
XL 31,448
LK 48,244,13,0
s_nop 0
CF 176,40,41
LK 248,246,13,2
WL 2
RA 244,7,1280
CF 177,42,43
CF 178,44,45
CF 179,46,47
XL 32,512
LK 40,248,14,0
CF 180,48,49
LK 44,250,14,2
RA 248,7,1536
CF 181,50,51
GZ 12
WL 3
XL 33,576
CF 182,248,249
CF 183,250,251
LK 48,252,15,0
CF 184,40,41
LK 248,254,15,2
RA 252,7,1792
CF 185,42,43
XL 34,640
GS 13
WL 3
CF 186,44,45
CF 187,46,47
LK 40,240,16,0
CF 188,48,49
LK 44,242,16,2
WL 2
RA 240,7,2048
CF 189,50,51
XL 35,704
GS 14
s_mul_i32 s53, s50, 25600
s_add_u32 s40, %[wlo], s53
s_addc_u32 s41, %[whi], 0
s_mov_b32 m0, s38
CF 190,248,249
CF 191,250,251
LK 48,244,17,0
CF 192,40,41
LK 248,246,17,2
RA 244,7,2304
CF 193,42,43
GS 15
WL 3
DW 0
CF 194,44,45
CF 195,46,47
LK 40,248,18,0
CF 196,48,49
LK 44,250,18,2
RA 248,7,2560
CF 197,50,51
DW 1024
GS 16
WL 3
CF 198,248,249
CF 199,250,251
LK 48,252,19,0
CF 200,40,41
LK 248,254,19,2
WL 2
RA 252,7,2816
CF 201,42,43
DW 2048
GS 17
s_add_u32 s40, s40, 3072
s_addc_u32 s41, s41, 0
s_add_u32 m0, s38, 3072
CF 202,44,45
CF 203,46,47
LK 40,240,20,0
CF 204,48,49
LK 44,242,20,2
CF 205,50,51
GS 18
WL 2
DW 0
CF 206,248,249
CF 207,250,251
LK 48,244,21,0
CF 208,40,41
LK 248,246,21,2
CF 209,42,43
DW 1024
GS 19
WL 1
CF 210,44,45
CF 211,46,47
LK 40,248,22,0
CF 212,48,49
LK 44,250,22,2
CF 213,50,51
DW 2048
GS 20
WL 0
DQ 3072
CF 214,248,249
CF 215,250,251
LK 48,252,23,0
CF 216,40,41
LK 248,254,23,2
CF 217,42,43
GS 21
s_add_u32 s50, s50, %[d]
s_cmp_eq_u32 s50, %[wrap]
s_cselect_b32 s50, %[rst], s50
CF 218,44,45
CF 219,46,47
RV 36,8,6912
s_nop 0
GS 22
s_add_u32 s38, s38, 32768
s_cmp_eq_u32 s38, s77
s_cselect_b32 s38, s76, s38
CF 220,48,49
CF 221,50,51
CF 222,248,249
CF 223,250,251
s_nop 1
GS 23
s_nop 7
v_cndmask_b32 v52, v52, v53, s[78:79]
v_cndmask_b32 v54, v54, v55, s[78:79]
v_cndmask_b32 v52, v52, v54, s[80:81]
v_mul_f32 v52, 0x3b2aaaab, v52
WL 0
v_med3_f32 v53, v52, v39, v37
LG 52,53
s_nop 0
C 52,52,52
v_lshlrev_b32 v52, 16, v52
v_add_f32 v52, v36, v52
C 52,52,52
v_lshlrev_b32 v52, 16, v52
v_sqrt_f32 v52, v52
s_nop 0
C 52,52,52
v_lshlrev_b32 v52, 16, v52
v_rcp_f32 v52, v52
s_nop 0
v_mul_f32 v52, v38, v52
CF 52,52,52
ds_bpermute_b32 v13, v28, v52
WV 18
BW 24
RV 40,8,6144
RV 44,8,6208
RV 48,8,6272
BR 24
)"
R"(WV 17
BW 25
s_nop 1
BR 25
WL 7
XS 176
WL 2
LZ 40,24
s_and_b32 s53, %[w], 1
s_mul_i32 s53, s53, 3072
s_lshr_b32 s72, %[w], 1
s_lshl_b32 s40, s72, 16
s_add_u32 s40, s40, s53
s_add_u32 m0, s40, 25600
s_lshl_b32 s72, s72, 13
s_add_u32 s53, s53, s72
s_add_u32 s53, s53, 1382400
s_add_u32 s40, %[glo], s53
s_addc_u32 s41, %[ghi], 0
XS 177
WV 16
BW 26
XS 178
RV 40,8,6336
XS 179
BR 26
XS 180
WL 3
LS 44,25
WV 15
DW 0
XS 181
BW 27
XS 182
RV 44,8,6400
XS 183
BR 27
v_min_u32 v35, v35, v13
WL 3
LS 48,26
WV 15
DW 1024
XS 184
BW 28
XS 185
RV 48,8,6464
XS 186
BR 28
XS 187
WL 3
LS 40,27
WV 15
XS 188
BW 29
XS 189
RV 40,8,6528
XS 190
XS 191
BR 29
XS 192
WL 3
LS 44,28
WV 14
DW 2048
XS 193
BW 30
XS 194
RV 44,8,6592
XS 195
BR 30
XS 196
WL 3
LS 48,29
WV 14
DW 3072
XS 197
BW 31
XS 198
RV 48,8,6656
XS 199
BR 31
XS 200
WL 3
LS 40,30
WV 13
XS 201
BW 32
XS 202
RV 40,8,6720
XS 203
BR 32
XS 204
WL 3
LS 44,31
PF0
s_mov_b32 m0, %[pm]
s_nop 0
DF
PFA
XS 205
BW 33
XS 206
XS 207
RV 44,8,6784
XS 208
BR 33
XS 209
WL 3
LS 48,32
s_mul_i32 s53, s50, 25600
s_add_u32 s40, %[wlo], s53
s_addc_u32 s41, %[whi], 0
s_mov_b32 m0, s38
XS 210
WV 13
BW 34
XS 211
RV 48,8,6848
XS 212
RA 240,7,0
XS 213
BR 34
XS 214
WL 4
LS 40,33
WV 12
XS 215
BW 35
XS 216
RA 244,7,256
XS 217
BR 35
XS 218
WL 3
LS 44,34
DW 0
XS 219
RA 248,7,512
XS 220
WL 1
LS 48,35
DW 1024
XS 221
RA 252,7,768
XS 222
XS 223
s_nop 2
v_mul_f32 v36, 0xbb2aaaab, v36
v_mul_f32 v37, 0xbb2aaaab, v37
v_mul_f32 v38, 0xbb2aaaab, v38
v_mul_f32 v39, 0xbb2aaaab, v39
s_nop 1
LK 40,240,24,0
LK 44,242,24,2
RA 240,7,1024
LK 48,244,25,0
s_nop 1
CF 224,40,41
LK 248,246,25,2
WL 2
RA 244,7,1280
CF 225,42,43
CF 226,44,45
CF 227,46,47
DW 2048
LK 40,248,26,0
CF 228,48,49
LK 44,250,26,2
RA 248,7,1536
CF 229,50,51
s_add_u32 s40, s40, 3072
s_addc_u32 s41, s41, 0
s_add_u32 m0, s38, 3072
GZ 24
WL 3
CF 230,248,249
CF 231,250,251
LK 48,252,27,0
CF 232,40,41
LK 248,254,27,2
RA 252,7,1792
CF 233,42,43
DW 0
GS 25
WL 3
DW 1024
CF 234,44,45
CF 235,46,47
LK 40,240,28,0
CF 236,48,49
LK 44,242,28,2
RA 240,7,2048
CF 237,50,51
GS 26
WL 3
DW 2048
CF 238,248,249
CF 239,250,251
LK 48,244,29,0
CF 240,40,41
LK 248,246,29,2
RA 244,7,2304
CF 241,42,43
DQ 3072
GS 27
WL 3
CF 242,44,45
CF 243,46,47
LK 40,248,30,0
CF 244,48,49
LK 44,250,30,2
WL 2
RA 248,7,2560
CF 245,50,51
s_add_u32 s50, s50, %[d]
s_cmp_eq_u32 s50, %[wrap]
s_cselect_b32 s50, %[rst], s50
GS 28
s_add_u32 s38, s38, 32768
s_cmp_eq_u32 s38, s77
s_cselect_b32 s38, s76, s38
CF 246,248,249
CF 247,250,251
LK 48,252,31,0
CF 152,40,41
LK 248,254,31,2
WL 2
RA 252,7,2816
CF 153,42,43
GS 29
s_add_u32 s53, %[xo0], 196608
s_add_u32 s46, %[xlo], s53
s_addc_u32 s47, %[xhi], 0
s_bitcmp1_b64 s[90:91], 48
s_cselect_b32 s58, 16, 0
s_lshr_b32 s58, 0xfe987610, s58
v_lshrrev_b32 v3, 6, v0
v_and_b32 v3, 12, v3
v_bfe_u32 v3, s58, v3, 4
v_mad_u32_u24 v3, v3, s97, v21
CF 154,44,45
CF 155,46,47
LK 40,240,32,0
CF 156,48,49
LK 44,242,32,2
CF 157,50,51
XL 36,0
GS 6
WL 2
CF 158,248,249
CF 159,250,251
LK 48,244,33,0
CF 160,40,41
LK 248,246,33,2
CF 161,42,43
XL 37,64
GS 7
WL 1
XL 38,128
CF 162,44,45
CF 163,46,47
LK 40,248,34,0
CF 164,48,49
LK 44,250,34,2
CF 165,50,51
GS 8
WL 0
XL 39,192
CF 166,248,249
CF 167,250,251
LK 48,252,35,0
CF 168,40,41
LK 248,254,35,2
CF 169,42,43
XL 40,256
GS 9
CF 170,44,45
CF 171,46,47
RV 36,8,6912
s_nop 0
GS 10
XL 41,320
CF 172,48,49
CF 173,50,51
CF 174,248,249
CF 175,250,251
s_nop 1
GS 11
XL 42,384
WV 15
s_barrier
RA 216,1,0
RA 220,1,1024
RA 224,1,2048
RA 228,1,3072
RV 252,22,5120
v_cndmask_b32 v52, v52, v53, s[78:79]
v_cndmask_b32 v54, v54, v55, s[78:79]
v_cndmask_b32 v52, v52, v54, s[80:81]
v_mul_f32 v52, 0x3b2aaaab, v52
WL 5
v_med3_f32 v53, v52, v39, v37
LG 52,53
s_nop 0
C 52,52,52
v_lshlrev_b32 v52, 16, v52
v_add_f32 v52, v36, v52
C 52,52,52
XL 43,448
v_lshlrev_b32 v52, 16, v52
v_sqrt_f32 v52, v52
s_nop 0
C 52,52,52
v_lshlrev_b32 v52, 16, v52
v_rcp_f32 v52, v52
XL 44,512
v_mul_f32 v52, v38, v52
CF 52,52,52
ds_bpermute_b32 v13, v28, v52
WL 0
XS 224
XS 225
XS 226
XS 227
XS 228
XS 229
XS 230
XL 45,576
XS 231
v_min_u32 v35, v35, v13
XS 232
XS 233
XS 234
XL 46,640
XS 235
XS 236
XS 237
XS 238
XS 239
XL 47,704
Z 0,12,0
XS 240
XS 241
XS 242
XS 243
XS 244
XS 245
XS 246
XS 247
XS 152
XS 153
XS 154
XS 155
M 0,13,1
s_mul_i32 s53, s50, 25600
RA 232,1,4096
AW 0,152
AW 1,153
Z 1,24,0
s_add_u32 s40, %[wlo], s53
AW 2,154
AW 3,155
M 1,25,1
s_addc_u32 s41, %[whi], 0
RA 236,1,6144
XS 156
XS 157
M 1,26,2
s_mov_b32 m0, s38
s_nop 0
DW 0
XS 158
XS 159
AW 4,156
M 1,27,3
RA 216,1,7168
AW 5,157
AW 6,158
M 0,14,2
DW 1024
AW 7,159
XS 160
M 0,15,3
WL 2
RA 220,1,8192
XS 161
XS 162
M 0,16,4
WL 2
XS 163
AW 8,160
M 0,17,5
RA 224,1,9216
AW 9,161
AW 10,162
AW 11,163
M 1,28,4
WL 1
DW 2048
XS 164
)"
R"(XS 165
M 1,29,5
s_add_u32 s40, s40, 3072
RA 228,1,10240
XS 166
XS 167
M 1,30,0
s_addc_u32 s41, s41, 0
AW 12,164
AW 13,165
M 1,31,1
s_add_u32 m0, s38, 3072
RA 232,1,11264
AW 14,166
AW 15,167
M 0,18,0
RV 252,22,5392
XS 168
XS 169
M 0,19,1
WL 3
RA 236,1,12288
XS 170
XS 171
AW 16,168
M 0,20,2
WL 3
DW 0
AW 17,169
AW 18,170
M 0,21,3
RA 216,1,13312
AW 19,171
XS 172
M 1,32,2
DW 1024
XS 173
XS 174
M 1,33,3
WL 3
RA 220,1,14336
XS 175
AW 20,172
M 0,22,4
WL 2
DW 2048
AW 21,173
AW 22,174
AW 23,175
M 0,23,5
RA 224,1,15360
M 1,34,4
DQ 3072
M 1,35,5
WL 1
RA 228,1,16384
Z 6,12,0
s_add_u32 s50, s50, %[d]
LL 16,56
LL 104,57
M 6,13,1
s_cmp_eq_u32 s50, %[wrap]
LA 16
RA 232,1,17408
Z 7,24,0
s_cselect_b32 s50, %[rst], s50
LC_ 104
M 7,25,1
s_add_u32 s38, s38, 32768
RA 236,1,18432
WL 5
M 7,26,2
s_cmp_eq_u32 s38, s77
WV 18
BW 36
s_cselect_b32 s38, s76, s38
WL 5
M 7,27,3
s_add_u32 s37, s37, 32768
RA 216,1,19456
RV 40,8,6144
M 6,14,2
s_cmp_eq_u32 s37, s75
LL 17,58
LL 105,59
M 6,15,3
s_cselect_b32 s37, s74, s37
LA 17
RA 220,1,20480
s_add_u32 s53, %[xo0], 196608
s_add_u32 s46, %[xlo], s53
s_addc_u32 s47, %[xhi], 0
s_bitcmp1_b64 s[90:91], 48
s_cselect_b32 s58, 16, 0
s_lshr_b32 s58, 0xda43cb52, s58
v_lshrrev_b32 v3, 6, v0
v_and_b32 v3, 12, v3
v_bfe_u32 v3, s58, v3, 4
RV 44,8,6208
RV 48,8,6272
v_mad_u32_u24 v3, v3, s97, v21
WL 9
M 6,16,4
WL 7
LC_ 105
XL 48,0
BR 36
M 6,17,5
s_mul_i32 s53, s51, 24
RA 224,1,21504
WV 18
BW 37
s_nop 1
BR 37
M 7,28,4
WV 17
LO 104,16,104
LU 16,104
M 7,29,5
s_bitcmp1_b32 s51, 0
RA 228,1,22528
HZ 24,16
WL 4
LZ 40,36
s_addc_u32 s53, s53, 0
BW 38
RV 40,8,6336
M 7,30,0
s_lshl_b32 s53, s53, 11
LL 18,60
LL 108,61
s_bitcmp1_b64 s[90:91], s51
s_cselect_b64 s[44:45], s[84:85], s[82:83]
s_add_u32 s44, s44, s53
s_addc_u32 s45, s45, 0
s_add_u32 s51, s51, %[d]
s_cmp_eq_u32 s51, %[wrap]
s_cselect_b32 s51, %[rst], s51
XL 49,64
BR 38
M 7,31,1
WL 4
LA 18
RA 232,1,23552
LS 44,37
WV 17
BW 39
M 6,18,0
LC_ 108
XL 50,128
RV 44,8,6400
M 6,19,1
RA 236,1,24576
BR 39
WL 7
LS 48,38
WV 17
BW 40
M 6,20,2
LO 105,17,105
LU 17,105
M 6,21,3
XL 51,192
RV 48,8,6464
BR 40
HS 24,17
M 7,32,2
LL 16,62
LL 109,63
v_add_u32 v1, s37, v0
M 7,33,3
WL 3
LA 16
RA 216,1,0
XL 52,256
LS 40,39
WV 18
BW 41
RV 40,8,6528
M 6,22,4
WL 4
LC_ 109
XL 53,320
v_and_b32 v22, 0xfffffcff, v1
BR 41
M 6,23,5
s_barrier
RA 220,1,1024
LS 44,40
WV 18
BW 42
M 7,34,4
RV 44,8,6592
LO 108,18,108
LU 18,108
M 7,35,5
RV 252,22,5120
BR 42
HS 24,18
WL 1
Z 0,12,0
WV 17
RA 224,1,2048
LL 17,80
LL 106,81
M 0,13,1
s_mul_i32 s53, s50, 25600
LA 17
RA 228,1,3072
LS 48,41
s_add_u32 s40, %[wlo], s53
BW 43
RV 48,8,6656
s_nop 0
BR 43
Z 1,24,0
s_addc_u32 s41, %[whi], 0
LC_ 106
RA 232,1,4096
M 1,25,1
s_mov_b32 m0, s38
RA 236,1,6144
WL 9
LS 40,42
WV 16
BW 44
RV 40,8,6720
WL 10
M 1,26,2
WL 8
BR 44
LO 109,16,109
LU 16,109
M 1,27,3
WL 6
RA 216,1,7168
HS 24,16
LS 44,43
WV 15
BW 45
M 0,14,2
DW 0
RV 44,8,6784
LL 18,82
LL 107,83
M 0,15,3
WL 3
LA 18
RA 220,1,8192
BR 45
LS 48,44
WV 15
BW 46
M 0,16,4
LC_ 107
DW 1024
M 0,17,5
RA 224,1,9216
RV 48,8,6848
RA 240,7,0
HX 24
M 1,28,4
LO 17,17,106
LU 106,17
W1 24,0
BR 46
M 1,29,5
WL 7
RA 228,1,10240
DW 2048
HZ 25,106
LS 40,45
WV 16
BW 47
M 1,30,0
s_add_u32 s40, s40, 3072
RA 244,7,256
PK 106,17,104,33
PK 104,17,104,32
LL 16,84
LL 110,85
M 1,31,1
s_addc_u32 s41, s41, 0
LA 16
RA 232,1,11264
BR 47
WL 6
LS 44,46
s_add_u32 m0, s38, 3072
RA 248,7,512
M 0,18,0
LC_ 110
RV 252,22,5392
M 0,19,1
RA 236,1,12288
WL 4
LS 48,47
RA 252,7,768
M 0,20,2
LO 18,18,107
LU 107,18
s_nop 3
v_mul_f32 v36, 0xbb2aaaab, v36
v_mul_f32 v37, 0xbb2aaaab, v37
v_mul_f32 v38, 0xbb2aaaab, v38
M 0,21,3
RA 216,1,13312
DW 0
HS 25,107
v_mul_f32 v39, 0xbb2aaaab, v39
s_nop 1
LK 40,240,36,0
LK 44,242,36,2
RA 240,7,1024
LK 48,244,37,0
s_nop 1
CF 128,40,41
LK 248,246,37,2
RA 244,7,1280
CF 129,42,43
M 1,32,2
PK 107,18,105,33
PK 105,18,105,32
LL 17,86
LL 111,87
M 1,33,3
WL 7
LA 17
RA 220,1,14336
DW 1024
CF 130,44,45
CF 131,46,47
LK 40,248,38,0
WL 5
CF 132,48,49
LK 44,250,38,2
s_add_u32 s42, s44, s60
RA 248,7,1536
CF 133,50,51
GZ 0
s_addc_u32 s43, s45, 0
CF 134,248,249
CF 135,250,251
LK 48,252,39,0
CF 136,40,41
LK 248,254,39,2
RA 252,7,1792
CF 137,42,43
GS 1
WL 5
CF 138,44,45
CF 139,46,47
LK 40,240,40,0
CF 140,48,49
LK 44,242,40,2
RA 240,7,2048
CF 141,50,51
GS 2
WL 5
CF 142,248,249
CF 143,250,251
LK 48,244,41,0
CF 144,40,41
LK 248,246,41,2
RA 244,7,2304
CF 145,42,43
GS 3
CF 146,44,45
CF 147,46,47
M 0,22,4
LC_ 111
ST 104
M 0,23,5
WL 4
RA 224,1,15360
LK 40,248,42,0
CF 148,48,49
LK 44,250,42,2
RA 248,7,2560
CF 149,50,51
GS 4
CF 150,248,249
CF 151,250,251
M 1,34,4
DW 2048
LO 16,16,110
LU 110,16
M 1,35,5
WL 5
RA 228,1,16384
HS 25,110
LK 48,252,43,0
CF 152,40,41
LK 248,254,43,2
RA 252,7,2816
CF 153,42,43
GS 5
CF 154,44,45
CF 155,46,47
Z 6,12,0
)"
R"(PK 110,16,108,33
PK 108,16,108,32
LL 18,56
LL 104,57
M 6,13,1
WL 6
LA 18
DQ 3072
PO 110
LK 40,240,44,0
CF 156,48,49
LK 44,242,44,2
s_add_u32 s50, s50, %[d]
CF 157,50,51
GS 6
s_cmp_eq_u32 s50, %[wrap]
CF 158,248,249
CF 159,250,251
Z 7,24,0
s_cselect_b32 s50, %[rst], s50
LC_ 104
RA 232,1,17408
M 7,25,1
WL 8
RA 236,1,18432
LK 48,244,45,0
CF 160,40,41
LK 248,246,45,2
CF 161,42,43
GS 7
WL 7
CF 162,44,45
CF 163,46,47
M 7,26,2
WL 5
LO 17,17,111
LU 111,17
M 7,27,3
s_add_u32 s38, s38, 32768
RA 216,1,19456
HS 25,111
LK 40,248,46,0
WL 5
CF 164,48,49
LK 44,250,46,2
s_cmp_eq_u32 s38, s77
CF 165,50,51
GS 8
s_cselect_b32 s38, s76, s38
CF 166,248,249
CF 167,250,251
M 6,14,2
s_add_u32 s37, s37, 32768
PK 111,17,109,33
PK 109,17,109,32
LL 16,58
LL 105,59
M 6,15,3
s_cmp_eq_u32 s37, s75
LA 16
PO 111
s_cselect_b32 s37, s74, s37
LK 48,252,47,0
CF 168,40,41
LK 248,254,47,2
CF 169,42,43
HX 25
GS 9
WL 3
CF 170,44,45
CF 171,46,47
M 6,16,4
WL 2
LC_ 105
RA 220,1,20480
XL 54,384
M 6,17,5
s_add_u32 s42, s44, s61
RA 224,1,21504
RV 36,8,6912
s_addc_u32 s43, s45, 0
GS 10
CF 172,48,49
CF 173,50,51
M 7,28,4
LO 104,18,104
LU 18,104
W1 25,1024
M 7,29,5
WL 3
RA 228,1,22528
HZ 24,18
ST 108
XL 55,448
M 7,30,0
s_mul_i32 s53, s51, 24
LL 17,60
LL 108,61
s_bitcmp1_b32 s51, 0
s_addc_u32 s53, s53, 0
s_lshl_b32 s53, s53, 11
s_bitcmp1_b64 s[90:91], s51
s_cselect_b64 s[44:45], s[84:85], s[82:83]
s_add_u32 s44, s44, s53
s_addc_u32 s45, s45, 0
s_add_u32 s51, s51, %[d]
s_cmp_eq_u32 s51, %[wrap]
s_cselect_b32 s51, %[rst], s51
M 7,31,1
LA 17
RA 232,1,23552
M 6,18,0
LC_ 108
XL 56,512
v_add_u32 v4, s39, v4
CF 174,248,249
CF 175,250,251
M 6,19,1
RA 236,1,24576
s_nop 1
v_add_u32 v1, s37, v0
GS 11
WL 7
XL 57,576
s_nop 5
v_cndmask_b32 v52, v52, v53, s[78:79]
v_cndmask_b32 v54, v54, v55, s[78:79]
M 6,20,2
WL 4
LO 105,16,105
LU 16,105
v_cndmask_b32 v52, v52, v54, s[80:81]
v_mul_f32 v52, 0x3b2aaaab, v52
M 6,21,3
RA 216,1,0
HS 24,16
v_med3_f32 v53, v52, v39, v37
M 7,32,2
LL 18,62
LL 109,63
LG 52,53
s_nop 0
C 52,52,52
M 7,33,3
WL 1
LA 18
XL 58,640
v_lshlrev_b32 v52, 16, v52
v_add_f32 v52, v36, v52
M 6,22,4
s_lshl_b32 s53, s52, 8
LC_ 109
v_and_b32 v22, 0xfffffcff, v1
C 52,52,52
v_lshlrev_b32 v52, 16, v52
M 6,23,5
s_add_u32 s48, %[alo], s53
RA 220,1,1024
v_sqrt_f32 v52, v52
s_nop 0
C 52,52,52
M 7,34,4
WV 20
LO 108,17,108
LU 17,108
s_addc_u32 s49, %[ahi], 0
s_add_u32 s52, s52, %[d]
s_cmp_eq_u32 s52, %[wrap]
s_cselect_b32 s52, %[rst], s52
XL 59,704
v_lshlrev_b32 v52, 16, v52
v_rcp_f32 v52, v52
M 7,35,5
s_barrier
RV 252,22,5120
HS 24,17
v_mul_f32 v52, v38, v52
CF 52,52,52
WL 0
Z 0,12,0
s_sub_u32 s39, 0, s39
RA 224,1,2048
LL 16,80
LL 106,81
ds_bpermute_b32 v13, v28, v52
M 0,13,1
s_mul_i32 s53, s50, 25600
LA 16
RA 228,1,3072
Z 1,24,0
s_add_u32 s40, %[wlo], s53
LC_ 106
RA 232,1,4096
M 1,25,1
s_addc_u32 s41, %[whi], 0
RA 236,1,6144
ds_read_b64 v[14:15], v5 offset:0
WL 7
M 1,26,2
LO 109,18,109
LU 18,109
s_mov_b32 m0, s38
WL 4
M 1,27,3
RA 216,1,7168
HS 24,18
M 0,14,2
DW 0
LL 17,82
LL 107,83
M 0,15,3
WL 3
LA 17
RA 220,1,8192
v_subrev_u32 v5, s39, v5
M 0,16,4
WL 4
LC_ 107
DW 1024
M 0,17,5
WL 4
RA 224,1,9216
v_add_f32 v14, v14, v15
s_nop 1
v_add_f32_dpp v14, v14, v14 quad_perm:[1,0,3,2] row_mask:0xf bank_mask:0xf
s_nop 1
v_add_f32_dpp v14, v14, v14 quad_perm:[2,3,0,1] row_mask:0xf bank_mask:0xf
s_nop 1
v_add_f32_dpp v14, v14, v14 row_half_mirror row_mask:0xf bank_mask:0xf
HX 24
XS 128
M 1,28,4
WV 21
LO 16,16,106
LU 106,16
v_cvt_f64_f32 v[14:15], v14
W1 24,0
XS 129
BW 48
XS 130
M 1,29,5
WL 4
RA 228,1,10240
HZ 25,106
s_mov_b64 exec, s[54:55]
SP
s_mov_b64 exec, -1
DW 2048
RV 40,8,6144
RV 44,8,6208
XS 131
AW 24,128
AW 25,129
M 1,30,0
s_add_u32 s40, s40, 3072
RV 48,8,6272
PK 106,16,104,33
PK 104,16,104,32
LL 18,84
LL 110,85
AW 26,130
AW 27,131
M 1,31,1
s_addc_u32 s41, s41, 0
LA 18
RA 232,1,11264
s_add_u32 m0, s38, 3072
BR 48
XS 132
XS 133
XS 134
BW 49
M 0,18,0
LC_ 110
RV 252,22,5392
BR 49
XS 135
AW 28,132
M 0,19,1
WL 10
RA 236,1,12288
AW 29,133
AW 30,134
AW 31,135
WL 5
LZ 40,48
WV 22
BW 50
M 0,20,2
DW 0
RV 40,8,6336
LO 17,17,107
LU 107,17
v_min_u32 v35, v35, v13
XS 136
XS 137
M 0,21,3
WL 3
RA 216,1,13312
BR 50
HS 25,107
XS 138
XS 139
LS 44,49
WV 22
BW 51
M 1,32,2
WL 5
DW 1024
PK 107,17,105,33
PK 105,17,105,32
LL 16,86
LL 111,87
AW 32,136
AW 33,137
AW 34,138
M 1,33,3
s_add_u32 s42, s44, s60
LA 16
RA 220,1,14336
RV 44,8,6400
BR 51
AW 35,139
XS 140
M 0,22,4
s_addc_u32 s43, s45, 0
LC_ 111
DW 2048
XS 141
XS 142
XS 143
M 0,23,5
WL 6
RA 224,1,15360
AW 36,140
AW 37,141
AW 38,142
LS 48,50
WV 23
BW 52
RV 48,8,6464
M 1,34,4
LO 18,18,110
LU 110,18
ST 104
BR 52
AW 39,143
XS 144
M 1,35,5
WL 5
RA 228,1,16384
DQ 3072
HS 25,110
XS 145
XS 146
XS 147
LS 40,51
WV 15
BW 53
Z 6,12,0
s_add_u32 s50, s50, %[d]
RV 40,8,6528
PK 110,18,108,33
PK 108,18,108,32
LL 17,56
LL 104,57
AW 40,144
AW 41,145
M 6,13,1
s_cmp_eq_u32 s50, %[wrap]
LA 17
BR 53
PO 110
AW 42,146
AW 43,147
XS 148
WL 5
LS 44,52
s_cselect_b32 s50, %[rst], s50
BW 54
Z 7,24,0
s_add_u32 s38, s38, 32768
LC_ 104
RA 232,1,17408
XS 149
XS 150
XS 151
M 7,25,1
s_cmp_eq_u32 s38, s77
RA 236,1,18432
RV 44,8,6592
BR 54
AW 44,148
AW 45,149
M 7,26,2
WL 6
LO 16,16,111
LU 111,16
s_cselect_b32 s38, s76, s38
AW 46,150
)"
R"(AW 47,151
XS 152
M 7,27,3
s_add_u32 s37, s37, 32768
RA 216,1,19456
HS 25,111
XS 153
XS 154
LS 48,53
s_cmp_eq_u32 s37, s75
WV 13
BW 55
RV 48,8,6656
M 6,14,2
s_cselect_b32 s37, s74, s37
BR 55
PK 111,16,109,33
PK 109,16,109,32
LL 18,58
LL 105,59
XS 155
AW 48,152
AW 49,153
M 6,15,3
s_add_u32 s53, %[xo0], 393216
LA 18
PO 111
s_add_u32 s46, %[xlo], s53
s_addc_u32 s47, %[xhi], 0
s_bitcmp1_b64 s[90:91], 48
s_cselect_b32 s58, 16, 0
s_lshr_b32 s58, 0xfe987610, s58
v_lshrrev_b32 v3, 6, v0
v_and_b32 v3, 12, v3
v_bfe_u32 v3, s58, v3, 4
v_mad_u32_u24 v3, v3, s97, v21
AW 50,154
AW 51,155
WL 5
LS 40,54
s_add_u32 s42, s44, s61
WV 12
BW 56
M 6,16,4
s_addc_u32 s43, s45, 0
LC_ 105
RA 220,1,20480
RV 40,8,6720
XS 156
XS 157
XS 158
M 6,17,5
WL 5
RA 224,1,21504
BR 56
XL 60,0
HX 25
XS 159
AW 52,156
AW 53,157
LS 44,55
WV 12
BW 57
M 7,28,4
LO 104,17,104
LU 17,104
W1 25,1024
AW 54,158
AW 55,159
M 7,29,5
WL 5
RA 228,1,22528
HZ 24,17
ST 108
RV 44,8,6784
BR 57
XS 160
XS 161
XS 162
M 7,30,0
s_mul_i32 s53, s51, 24
LL 16,60
LL 108,61
s_bitcmp1_b32 s51, 0
s_addc_u32 s53, s53, 0
s_lshl_b32 s53, s53, 11
s_bitcmp1_b64 s[90:91], s51
s_cselect_b64 s[44:45], s[84:85], s[82:83]
s_add_u32 s44, s44, s53
s_addc_u32 s45, s45, 0
s_add_u32 s51, s51, %[d]
s_cmp_eq_u32 s51, %[wrap]
s_cselect_b32 s51, %[rst], s51
XL 61,64
XS 163
AW 56,160
M 7,31,1
WL 5
LA 16
RA 232,1,23552
AW 57,161
AW 58,162
AW 59,163
LS 48,56
WV 13
BW 58
RV 48,8,6848
M 6,18,0
LC_ 108
XL 62,128
v_add_u32 v4, s39, v4
RA 240,7,0
XS 164
XS 165
XS 166
M 6,19,1
WL 6
RA 236,1,24576
BR 58
XS 167
AW 60,164
LS 40,57
WV 13
BW 59
M 6,20,2
LO 105,18,105
LU 18,105
AW 61,165
AW 62,166
AW 63,167
M 6,21,3
XL 63,192
RA 244,7,256
BR 59
HS 24,18
XS 168
XS 169
M 7,32,2
LL 17,62
LL 109,63
v_add_u32 v1, s37, v0
XS 170
XS 171
AW 64,168
M 7,33,3
WL 0
LA 17
RA 216,1,0
XL 64,256
AW 65,169
AW 66,170
AW 67,171
LS 44,58
s_lshl_b32 s53, s52, 8
RA 248,7,512
XS 172
LS 48,59
s_add_u32 s48, %[alo], s53
RA 252,7,768
XS 173
M 6,22,4
s_addc_u32 s49, %[ahi], 0
LC_ 109
v_and_b32 v22, 0xfffffcff, v1
M 6,23,5
s_add_u32 s52, s52, %[d]
RA 220,1,1024
XS 174
XS 175
AW 68,172
M 7,34,4
LO 108,16,108
LU 16,108
s_cmp_eq_u32 s52, %[wrap]
s_cselect_b32 s52, %[rst], s52
XL 65,320
AW 69,173
AW 70,174
M 7,35,5
s_barrier
RV 252,22,5120
HS 24,16
AW 71,175
v_mul_f32 v36, 0xbb2aaaab, v36
v_mul_f32 v37, 0xbb2aaaab, v37
v_mul_f32 v38, 0xbb2aaaab, v38
v_mul_f32 v39, 0xbb2aaaab, v39
WL 0
Z 0,12,0
s_sub_u32 s39, 0, s39
RA 224,1,2048
LL 18,80
LL 106,81
M 0,13,1
s_mul_i32 s53, s50, 25600
LA 18
RA 228,1,3072
LK 40,240,48,0
LK 44,242,48,2
s_add_u32 s40, %[wlo], s53
RA 240,7,1024
LK 48,244,49,0
s_nop 0
CF 128,40,41
LK 248,246,49,2
s_addc_u32 s41, %[whi], 0
RA 244,7,1280
CF 129,42,43
CF 130,44,45
CF 131,46,47
LK 40,248,50,0
CF 132,48,49
LK 44,250,50,2
s_mov_b32 m0, s38
RA 248,7,1536
CF 133,50,51
GZ 0
CF 134,248,249
CF 135,250,251
LK 48,252,51,0
CF 136,40,41
LK 248,254,51,2
RA 252,7,1792
CF 137,42,43
GS 1
CF 138,44,45
CF 139,46,47
Z 1,24,0
LC_ 106
RA 232,1,4096
M 1,25,1
WL 8
RA 236,1,6144
ds_read_b64 v[14:15], v5 offset:0
M 1,26,2
WL 8
LO 109,17,109
LU 17,109
M 1,27,3
WL 7
RA 216,1,7168
HS 24,17
LK 40,240,52,0
CF 140,48,49
LK 44,242,52,2
RA 240,7,2048
CF 141,50,51
GS 2
WL 8
CF 142,248,249
CF 143,250,251
LK 48,244,53,0
CF 144,40,41
LK 248,246,53,2
RA 244,7,2304
CF 145,42,43
GS 3
WL 8
CF 146,44,45
CF 147,46,47
LK 40,248,54,0
CF 148,48,49
LK 44,250,54,2
RA 248,7,2560
CF 149,50,51
GS 4
WL 8
CF 150,248,249
CF 151,250,251
LK 48,252,55,0
CF 152,40,41
LK 248,254,55,2
RA 252,7,2816
CF 153,42,43
GS 5
CF 154,44,45
CF 155,46,47
M 0,14,2
DW 0
LL 16,82
LL 107,83
M 0,15,3
WL 3
LA 16
RA 220,1,8192
v_subrev_u32 v5, s39, v5
LK 40,240,56,0
CF 156,48,49
LK 44,242,56,2
CF 157,50,51
HX 24
GS 6
CF 158,248,249
CF 159,250,251
M 0,16,4
LC_ 107
DW 1024
M 0,17,5
WL 5
RA 224,1,9216
v_add_f32 v14, v14, v15
s_nop 1
v_add_f32_dpp v14, v14, v14 quad_perm:[1,0,3,2] row_mask:0xf bank_mask:0xf
s_nop 1
v_add_f32_dpp v14, v14, v14 quad_perm:[2,3,0,1] row_mask:0xf bank_mask:0xf
s_nop 1
v_add_f32_dpp v14, v14, v14 row_half_mirror row_mask:0xf bank_mask:0xf
LK 48,244,57,0
CF 160,40,41
LK 248,246,57,2
CF 161,42,43
v_cvt_f64_f32 v[14:15], v14
GS 7
CF 162,44,45
CF 163,46,47
M 1,28,4
LO 18,18,106
LU 106,18
W1 24,0
M 1,29,5
WL 6
RA 228,1,10240
HZ 25,106
s_mov_b64 exec, s[54:55]
SP
s_mov_b64 exec, -1
DW 2048
LK 40,248,58,0
CF 164,48,49
LK 44,250,58,2
CF 165,50,51
GS 8
WL 4
CF 166,248,249
CF 167,250,251
M 1,30,0
s_add_u32 s40, s40, 3072
PK 106,18,104,33
PK 104,18,104,32
LL 17,84
LL 110,85
M 1,31,1
s_addc_u32 s41, s41, 0
LA 17
RA 232,1,11264
LK 48,252,59,0
CF 168,40,41
LK 248,254,59,2
s_add_u32 m0, s38, 3072
CF 169,42,43
GS 9
CF 170,44,45
CF 171,46,47
M 0,18,0
LC_ 110
RV 252,22,5392
M 0,19,1
RA 236,1,12288
RV 36,8,6912
GS 10
WL 8
CF 172,48,49
CF 173,50,51
M 0,20,2
WL 6
LO 16,16,107
LU 107,16
M 0,21,3
RA 216,1,13312
DW 0
HS 25,107
M 1,32,2
PK 107,16,105,33
PK 105,16,105,32
LL 18,86
LL 111,87
M 1,33,3
WL 2
LA 18
RA 220,1,14336
DW 1024
M 0,22,4
s_add_u32 s42, s44, s60
LC_ 111
s_addc_u32 s43, s45, 0
M 0,23,5
RA 224,1,15360
CF 174,248,249
CF 175,250,251
s_nop 1
GS 11
ST 104
M 1,34,4
DW 2048
LO 17,17,110
LU 110,17
s_nop 2
v_cndmask_b32 v52, v52, v53, s[78:79]
v_cndmask_b32 v54, v54, v55, s[78:79]
M 1,35,5
)"
R"(WL 4
RA 228,1,16384
HS 25,110
v_cndmask_b32 v52, v52, v54, s[80:81]
Z 6,12,0
WL 3
PK 110,17,108,33
PK 108,17,108,32
LL 16,56
LL 104,57
v_mul_f32 v52, 0x3b2aaaab, v52
v_med3_f32 v53, v52, v39, v37
M 6,13,1
WL 1
LA 16
DQ 3072
PO 110
LG 52,53
s_nop 0
C 52,52,52
Z 7,24,0
s_add_u32 s50, s50, %[d]
LC_ 104
RA 232,1,17408
v_lshlrev_b32 v52, 16, v52
M 7,25,1
s_cmp_eq_u32 s50, %[wrap]
RA 236,1,18432
s_cselect_b32 s50, %[rst], s50
v_add_f32 v52, v36, v52
C 52,52,52
M 7,26,2
WL 4
LO 18,18,111
LU 111,18
v_lshlrev_b32 v52, 16, v52
M 7,27,3
s_add_u32 s38, s38, 32768
RA 216,1,19456
HS 25,111
v_sqrt_f32 v52, v52
s_nop 0
C 52,52,52
M 6,14,2
s_cmp_eq_u32 s38, s77
PK 111,18,109,33
PK 109,18,109,32
LL 17,58
LL 105,59
s_cselect_b32 s38, s76, s38
v_lshlrev_b32 v52, 16, v52
M 6,15,3
s_add_u32 s37, s37, 32768
LA 17
PO 111
s_cmp_eq_u32 s37, s75
s_cselect_b32 s37, s74, s37
v_rcp_f32 v52, v52
s_nop 0
v_mul_f32 v52, v38, v52
WL 3
M 6,16,4
WL 2
LC_ 105
RA 220,1,20480
XL 66,384
CF 52,52,52
ds_bpermute_b32 v13, v28, v52
M 6,17,5
s_add_u32 s42, s44, s61
RA 224,1,21504
HX 25
s_addc_u32 s43, s45, 0
M 7,28,4
LO 104,16,104
LU 16,104
W1 25,1024
M 7,29,5
WL 3
RA 228,1,22528
HZ 24,16
ST 108
XL 67,448
M 7,30,0
s_mul_i32 s53, s51, 24
LL 18,60
LL 108,61
s_bitcmp1_b32 s51, 0
s_addc_u32 s53, s53, 0
s_lshl_b32 s53, s53, 11
s_bitcmp1_b64 s[90:91], s51
s_cselect_b64 s[44:45], s[84:85], s[82:83]
s_add_u32 s44, s44, s53
s_addc_u32 s45, s45, 0
s_add_u32 s51, s51, %[d]
s_cmp_eq_u32 s51, %[wrap]
s_cselect_b32 s51, %[rst], s51
M 7,31,1
LA 18
RA 232,1,23552
M 6,18,0
LC_ 108
XL 68,512
v_add_u32 v4, s39, v4
M 6,19,1
WL 5
RA 236,1,24576
s_nop 1
v_add_u32 v1, s37, v0
M 6,20,2
WL 4
XL 69,576
LO 105,17,105
LU 17,105
M 6,21,3
RA 216,1,0
HS 24,17
M 7,32,2
LL 16,62
LL 109,63
XS 128
M 7,33,3
WL 1
LA 16
XL 70,640
XS 129
XS 130
M 6,22,4
s_lshl_b32 s53, s52, 8
LC_ 109
v_and_b32 v22, 0xfffffcff, v1
XS 131
AW 72,128
M 6,23,5
s_add_u32 s48, %[alo], s53
RA 220,1,1024
AW 73,129
AW 74,130
M 7,34,4
WV 22
LO 108,18,108
LU 18,108
s_addc_u32 s49, %[ahi], 0
s_add_u32 s52, s52, %[d]
s_cmp_eq_u32 s52, %[wrap]
s_cselect_b32 s52, %[rst], s52
XL 71,704
AW 75,131
XS 132
M 7,35,5
s_barrier
RV 252,22,5120
HS 24,18
XS 133
XS 134
XS 135
WL 0
Z 0,12,0
s_sub_u32 s39, 0, s39
LL 17,80
LL 106,81
AW 76,132
AW 77,133
AW 78,134
M 0,13,1
s_mul_i32 s53, s50, 25600
LA 17
RA 224,1,2048
AW 79,135
v_min_u32 v35, v35, v13
XS 136
Z 1,24,0
s_add_u32 s40, %[wlo], s53
LC_ 106
RA 228,1,3072
XS 137
XS 138
XS 139
M 1,25,1
s_addc_u32 s41, %[whi], 0
RA 232,1,4096
AW 80,136
AW 81,137
AW 82,138
Z 2,36,0
LO 109,16,109
LU 16,109
AW 83,139
XS 140
XS 141
M 2,37,1
s_mov_b32 m0, s38
ds_read_b64 v[14:15], v5 offset:0
HS 24,16
XS 142
XS 143
AW 84,140
Z 3,48,0
WL 2
LL 18,82
LL 107,83
AW 85,141
AW 86,142
AW 87,143
M 3,49,1
LA 18
RA 236,1,6144
M 3,50,2
LC_ 107
DW 0
M 3,51,3
v_subrev_u32 v5, s39, v5
HX 24
M 2,38,2
LO 17,17,106
LU 106,17
M 2,39,3
RA 216,1,7168
HZ 25,106
XS 144
M 1,26,2
PK 106,17,104,33
PK 104,17,104,32
LL 16,84
LL 110,85
XS 145
M 1,27,3
LA 16
W1 24,0
XS 146
XS 147
AW 88,144
M 0,14,2
WL 4
LC_ 110
DW 1024
AW 89,145
AW 90,146
AW 91,147
M 0,15,3
RA 220,1,8192
v_add_f32 v14, v14, v15
XS 148
XS 149
XS 150
M 0,16,4
WL 5
LO 18,18,107
LU 107,18
XS 151
AW 92,148
AW 93,149
M 0,17,5
DW 2048
HS 25,107
v_add_f32_dpp v14, v14, v14 quad_perm:[1,0,3,2] row_mask:0xf bank_mask:0xf
AW 94,150
AW 95,151
XS 152
M 1,28,4
s_add_u32 s42, s44, s60
PK 107,18,105,33
PK 105,18,105,32
LL 17,86
LL 111,87
XS 153
XS 154
XS 155
M 1,29,5
s_addc_u32 s43, s45, 0
LA 17
RA 224,1,9216
v_add_f32_dpp v14, v14, v14 quad_perm:[2,3,0,1] row_mask:0xf bank_mask:0xf
AW 96,152
AW 97,153
AW 98,154
M 2,40,4
LC_ 111
ST 104
AW 99,155
XS 156
XS 157
M 2,41,5
v_add_f32_dpp v14, v14, v14 row_half_mirror row_mask:0xf bank_mask:0xf
XS 158
XS 159
AW 100,156
M 3,52,4
WL 3
LO 16,16,110
LU 110,16
AW 101,157
AW 102,158
AW 103,159
M 3,53,5
s_add_u32 s40, s40, 3072
RA 228,1,10240
HS 25,110
v_cvt_f64_f32 v[14:15], v14
M 3,54,0
s_addc_u32 s41, s41, 0
PK 110,16,108,33
PK 108,16,108,32
M 3,55,1
s_add_u32 m0, s38, 3072
s_mov_b64 exec, s[54:55]
SP
s_mov_b64 exec, -1
PO 110
M 2,42,0
XS 160
XS 161
M 2,43,1
RA 232,1,11264
XS 162
M 1,30,0
WL 2
LO 17,17,111
LU 111,17
M 1,31,1
RV 252,22,5392
HS 25,111
M 0,18,0
WL 2
PK 111,17,109,33
PK 109,17,109,32
XS 163
AW 104,160
M 0,19,1
RA 236,1,12288
PO 111
AW 105,161
AW 106,162
M 0,20,2
DW 0
HX 25
AW 107,163
XS 164
M 0,21,3
s_add_u32 s42, s44, s61
W1 25,1024
XS 165
XS 166
M 1,32,2
s_addc_u32 s43, s45, 0
ST 108
v_add_u32 v4, s39, v4
XS 167
M 1,33,3
RA 216,1,13312
AW 108,164
AW 109,165
M 2,44,2
DW 1024
AW 110,166
AW 111,167
M 2,45,3
XS 168
XS 169
M 3,56,2
WL 2
XS 170
XS 171
M 3,57,3
RA 220,1,14336
AW 112,168
AW 113,169
M 0,22,4
DW 2048
AW 114,170
AW 115,171
M 0,23,5
XS 172
XS 173
M 1,34,4
XS 174
XS 175
M 1,35,5
RA 224,1,15360
AW 116,172
AW 117,173
M 2,46,4
LL 18,56
LL 104,57
AW 118,174
AW 119,175
M 2,47,5
LA 18
DQ 3072
M 3,58,4
WL 2
LC_ 104
M 3,59,5
RA 228,1,16384
Z 6,12,0
WV 32
BW 60
M 6,13,1
s_add_u32 s50, s50, %[d]
RV 40,8,6144
)"
R"(RV 44,8,6208
Z 7,24,0
s_cmp_eq_u32 s50, %[wrap]
LL 16,58
LL 105,59
M 7,25,1
s_cselect_b32 s50, %[rst], s50
LA 16
RA 232,1,17408
Z 8,36,0
LC_ 105
RV 48,8,6272
M 8,37,1
BR 60
Z 9,48,0
WL 8
LO 104,18,104
LU 18,104
M 9,49,1
s_add_u32 s38, s38, 32768
RA 236,1,18432
HZ 24,18
M 9,50,2
s_cmp_eq_u32 s38, s77
LL 17,60
LL 108,61
M 9,51,3
s_cselect_b32 s38, s76, s38
LA 17
M 8,38,2
s_add_u32 s37, s37, 32768
LC_ 108
M 8,39,3
s_cmp_eq_u32 s37, s75
RA 216,1,19456
M 7,26,2
WL 6
LO 105,16,105
LU 16,105
M 7,27,3
s_cselect_b32 s37, s74, s37
HS 24,16
M 6,14,2
WL 3
LL 18,62
LL 109,63
M 6,15,3
s_add_u32 s53, %[xo0], 393216
LA 18
RA 220,1,20480
M 6,16,4
s_add_u32 s46, %[xlo], s53
LC_ 109
M 6,17,5
s_addc_u32 s47, %[xhi], 0
WV 30
BW 61
M 7,28,4
WL 5
LO 108,17,108
LU 17,108
s_bitcmp1_b64 s[90:91], 48
s_cselect_b32 s58, 16, 0
s_lshr_b32 s58, 0xda43cb52, s58
v_lshrrev_b32 v3, 6, v0
v_and_b32 v3, 12, v3
v_bfe_u32 v3, s58, v3, 4
BR 61
v_mad_u32_u24 v3, v3, s97, v21
M 7,29,5
RA 224,1,21504
HS 24,17
LZ 40,60
WV 29
BW 62
M 8,40,4
LL 16,64
LL 112,65
M 8,41,5
LA 16
XL 72,0
M 9,52,4
WL 6
LC_ 112
RV 40,8,6336
M 9,53,5
s_mul_i32 s53, s51, 24
RA 228,1,22528
BR 62
M 9,54,0
WL 7
LO 109,18,109
LU 18,109
M 9,55,1
s_bitcmp1_b32 s51, 0
HS 24,18
s_addc_u32 s53, s53, 0
s_lshl_b32 s53, s53, 11
s_bitcmp1_b64 s[90:91], s51
s_cselect_b64 s[44:45], s[84:85], s[82:83]
s_add_u32 s44, s44, s53
s_addc_u32 s45, s45, 0
s_add_u32 s51, s51, %[d]
s_cmp_eq_u32 s51, %[wrap]
s_cselect_b32 s51, %[rst], s51
LS 44,61
WV 29
BW 63
M 8,42,0
LL 17,66
LL 113,67
M 8,43,1
WV 28
LA 17
RA 232,1,23552
RV 44,8,6400
M 7,30,0
WL 4
LC_ 113
XL 73,64
M 7,31,1
s_lshl_b32 s53, s52, 8
BR 63
LS 48,62
s_add_u32 s48, %[alo], s53
BW 64
M 6,18,0
LO 112,16,112
LU 16,112
M 6,19,1
s_addc_u32 s49, %[ahi], 0
RA 236,1,24576
HS 24,16
M 6,20,2
s_add_u32 s52, s52, %[d]
LL 18,68
LL 116,69
s_cmp_eq_u32 s52, %[wrap]
s_cselect_b32 s52, %[rst], s52
M 6,21,3
LA 18
XL 74,128
RV 48,8,6464
M 7,32,2
LC_ 116
XL 75,192
BR 64
M 7,33,3
WL 6
v_add_u32 v1, s37, v0
LS 40,63
WV 30
BW 65
M 8,44,2
LO 113,17,113
LU 17,113
M 8,45,3
RA 216,1,0
HS 24,17
M 9,56,2
WL 2
RV 40,8,6528
LL 16,70
LL 117,71
M 9,57,3
WV 21
LA 16
XL 76,256
BR 65
M 6,22,4
s_barrier
LC_ 117
v_and_b32 v22, 0xfffffcff, v1
M 6,23,5
s_sub_u32 s39, 0, s39
RA 220,1,1024
LS 44,64
WV 21
BW 66
M 7,34,4
LO 116,18,116
LU 18,116
M 7,35,5
RV 252,22,5120
RV 44,8,6592
HS 24,18
M 8,46,4
LL 17,80
LL 106,81
M 8,47,5
LA 17
RA 224,1,2048
BR 66
M 9,58,4
WL 6
LC_ 106
ds_read_b64 v[14:15], v5 offset:0
M 9,59,5
WL 6
RA 228,1,3072
v_subrev_u32 v5, s39, v5
Z 0,12,0
LO 117,16,117
LU 16,117
M 0,13,1
WV 18
XL 77,320
HS 24,16
LS 48,65
s_mul_i32 s53, s50, 25600
BW 67
RV 48,8,6656
Z 1,24,0
s_add_u32 s40, %[wlo], s53
BR 67
LL 18,82
LL 107,83
M 1,25,1
s_addc_u32 s41, %[whi], 0
LA 18
RA 232,1,4096
WL 8
LS 40,66
s_mov_b32 m0, s38
BW 68
Z 2,36,0
LC_ 107
RV 40,8,6720
M 2,37,1
BR 68
HX 24
Z 3,48,0
WL 9
LO 17,17,106
LU 106,17
M 3,49,1
RA 236,1,6144
HZ 25,106
M 3,50,2
LL 16,84
LL 110,85
M 3,51,3
WL 7
LA 16
PK 106,17,104,33
LS 44,67
WV 18
BW 69
M 2,38,2
LC_ 110
PK 104,17,104,32
W1 24,0
M 2,39,3
WV 17
RA 216,1,7168
RV 44,8,6784
M 1,26,2
WL 9
LO 18,18,107
LU 107,18
M 1,27,3
WL 7
DW 0
BR 69
HS 25,107
LS 48,68
s_add_u32 s42, s44, s60
BW 70
M 0,14,2
WL 8
LL 17,86
LL 111,87
M 0,15,3
s_addc_u32 s43, s45, 0
LA 17
PK 107,18,105,33
v_add_f32 v14, v14, v15
s_nop 1
v_add_f32_dpp v14, v14, v14 quad_perm:[1,0,3,2] row_mask:0xf bank_mask:0xf
s_nop 1
v_add_f32_dpp v14, v14, v14 quad_perm:[2,3,0,1] row_mask:0xf bank_mask:0xf
M 0,16,4
LC_ 111
PK 105,18,105,32
v_add_f32_dpp v14, v14, v14 row_half_mirror row_mask:0xf bank_mask:0xf
M 0,17,5
RA 220,1,8192
RV 48,8,6848
M 1,28,4
WL 9
RA 240,7,0
LO 16,16,110
LU 110,16
v_cvt_f64_f32 v[14:15], v14
M 1,29,5
RA 224,1,9216
DW 1024
BR 70
HS 25,110
M 2,40,4
LL 18,88
LL 114,89
s_mov_b64 exec, s[54:55]
SP
s_mov_b64 exec, -1
M 2,41,5
WL 8
LA 18
PK 110,16,108,33
LS 40,69
WV 19
BW 71
M 3,52,4
WL 6
LC_ 114
PK 108,16,108,32
M 3,53,5
RA 228,1,10240
RA 244,7,256
PO 110
M 3,54,0
WL 5
LO 17,17,111
LU 111,17
ST 104
DW 2048
M 3,55,1
s_add_u32 s40, s40, 3072
BR 71
HS 25,111
M 2,42,0
s_addc_u32 s41, s41, 0
LL 16,90
LL 115,91
M 2,43,1
s_add_u32 m0, s38, 3072
LA 16
PK 111,17,109,33
LS 44,70
s_add_u32 s42, s44, s61
RA 248,7,512
M 1,30,0
s_addc_u32 s43, s45, 0
LC_ 115
PK 109,17,109,32
M 1,31,1
WL 3
RA 232,1,11264
PO 111
LS 48,71
RA 252,7,768
M 0,18,0
LO 18,18,114
LU 114,18
M 0,19,1
RV 252,22,5392
HS 25,114
s_nop 0
v_mul_f32 v36, 0xbb2aaaab, v36
M 0,20,2
LL 17,92
LL 118,93
v_mul_f32 v37, 0xbb2aaaab, v37
v_mul_f32 v38, 0xbb2aaaab, v38
M 0,21,3
LA 17
PK 114,18,112,33
v_mul_f32 v39, 0xbb2aaaab, v39
s_nop 1
LK 40,240,60,0
LK 44,242,60,2
RA 240,7,1024
LK 48,244,61,0
s_nop 1
CF 128,40,41
LK 248,246,61,2
RA 244,7,1280
CF 129,42,43
M 1,32,2
LC_ 118
PK 112,18,112,32
M 1,33,3
RA 236,1,12288
CF 130,44,45
M 2,44,2
WL 8
DW 0
LO 16,16,115
LU 115,16
M 2,45,3
RA 216,1,13312
HS 25,115
ST 108
CF 131,46,47
LK 40,248,62,0
CF 132,48,49
LK 44,250,62,2
RA 248,7,1536
CF 133,50,51
GZ 0
)"
R"(WL 8
CF 134,248,249
CF 135,250,251
LK 48,252,63,0
CF 136,40,41
LK 248,254,63,2
RA 252,7,1792
CF 137,42,43
GS 1
WL 6
CF 138,44,45
CF 139,46,47
LK 40,240,64,0
CF 140,48,49
LK 44,242,64,2
RA 240,7,2048
CF 141,50,51
GS 2
CF 142,248,249
CF 143,250,251
M 3,56,2
WL 4
LL 18,94
LL 119,95
M 3,57,3
LA 18
PK 115,16,113,33
LK 48,244,65,0
CF 144,40,41
LK 248,246,65,2
RA 244,7,2304
CF 145,42,43
GS 3
CF 146,44,45
CF 147,46,47
M 0,22,4
LC_ 119
PK 113,16,113,32
M 0,23,5
RA 220,1,14336
DW 1024
M 1,34,4
WL 6
LO 17,17,118
LU 118,17
M 1,35,5
s_add_u32 s42, s44, s62
RA 224,1,15360
HS 25,118
s_addc_u32 s43, s45, 0
LK 40,248,66,0
CF 148,48,49
LK 44,250,66,2
RA 248,7,2560
CF 149,50,51
GS 4
CF 150,248,249
CF 151,250,251
M 2,46,4
LL 16,56
LL 104,57
M 2,47,5
WL 7
LA 16
PK 118,17,116,33
LK 48,252,67,0
CF 152,40,41
LK 248,254,67,2
RA 252,7,2816
CF 153,42,43
GS 5
CF 154,44,45
CF 155,46,47
M 3,58,4
WL 4
LC_ 104
PK 116,17,116,32
M 3,59,5
RA 228,1,16384
DW 2048
PO 118
Z 6,12,0
LO 18,18,119
LU 119,18
ST 112
M 6,13,1
DQ 3072
HS 25,119
LK 40,240,68,0
CF 156,48,49
LK 44,242,68,2
s_add_u32 s42, s44, s63
CF 157,50,51
GS 6
s_addc_u32 s43, s45, 0
CF 158,248,249
CF 159,250,251
Z 7,24,0
LL 17,58
LL 105,59
M 7,25,1
LA 17
PK 119,18,117,33
Z 8,36,0
s_add_u32 s50, s50, %[d]
LC_ 105
PK 117,18,117,32
M 8,37,1
s_cmp_eq_u32 s50, %[wrap]
RA 232,1,17408
PO 119
LK 48,244,69,0
CF 160,40,41
LK 248,246,69,2
s_cselect_b32 s50, %[rst], s50
CF 161,42,43
HX 25
GS 7
CF 162,44,45
CF 163,46,47
Z 9,48,0
WL 3
LO 104,16,104
LU 16,104
M 9,49,1
RA 236,1,18432
HZ 24,16
M 9,50,2
LL 18,60
LL 108,61
M 9,51,3
LA 18
W1 25,1024
LK 40,248,70,0
CF 164,48,49
LK 44,250,70,2
CF 165,50,51
GS 8
CF 166,248,249
CF 167,250,251
M 8,38,2
LC_ 108
ST 116
M 8,39,3
s_add_u32 s37, s37, 32768
RA 216,1,19456
v_add_u32 v4, s39, v4
LK 48,252,71,0
CF 168,40,41
LK 248,254,71,2
s_cmp_eq_u32 s37, s75
CF 169,42,43
GS 9
s_cselect_b32 s37, s74, s37
CF 170,44,45
CF 171,46,47
M 7,26,2
WL 6
LO 105,17,105
LU 17,105
M 7,27,3
s_add_u32 s38, s38, 32768
RV 36,8,6912
HS 24,17
M 6,14,2
WL 5
LL 16,62
LL 109,63
M 6,15,3
s_cmp_eq_u32 s38, s77
LA 16
RA 220,1,20480
GS 10
s_cselect_b32 s38, s76, s38
CF 172,48,49
M 6,16,4
LC_ 109
XL 78,384
M 6,17,5
CF 173,50,51
CF 174,248,249
M 7,28,4
WL 5
LO 108,18,108
LU 18,108
M 7,29,5
s_mul_i32 s53, s51, 24
RA 224,1,21504
HS 24,18
M 8,40,4
s_bitcmp1_b32 s51, 0
LL 17,64
LL 112,65
M 8,41,5
s_addc_u32 s53, s53, 0
LA 17
CF 175,250,251
M 9,52,4
WL 3
LC_ 112
M 9,53,5
s_lshl_b32 s53, s53, 11
RA 228,1,22528
GS 11
s_bitcmp1_b64 s[90:91], s51
M 9,54,0
WL 4
LO 109,16,109
LU 16,109
M 9,55,1
s_cselect_b64 s[44:45], s[84:85], s[82:83]
HS 24,16
s_add_u32 s44, s44, s53
s_addc_u32 s45, s45, 0
s_add_u32 s51, s51, %[d]
s_cmp_eq_u32 s51, %[wrap]
s_cselect_b32 s51, %[rst], s51
M 8,42,0
LL 18,66
LL 113,67
M 8,43,1
LA 18
RA 232,1,23552
XL 79,448
M 7,30,0
s_lshl_b32 s53, s52, 8
LC_ 113
v_cndmask_b32 v52, v52, v53, s[78:79]
M 7,31,1
s_add_u32 s48, %[alo], s53
v_cndmask_b32 v54, v54, v55, s[78:79]
v_cndmask_b32 v52, v52, v54, s[80:81]
M 6,18,0
WL 3
LO 112,17,112
LU 17,112
M 6,19,1
s_addc_u32 s49, %[ahi], 0
RA 236,1,24576
HS 24,17
M 6,20,2
s_add_u32 s52, s52, %[d]
LL 16,68
LL 116,69
s_cmp_eq_u32 s52, %[wrap]
s_cselect_b32 s52, %[rst], s52
v_mul_f32 v52, 0x3b2aaaab, v52
M 6,21,3
LA 16
XL 80,512
M 7,32,2
LC_ 116
XL 81,576
v_med3_f32 v53, v52, v39, v37
M 7,33,3
v_add_u32 v1, s37, v0
LG 52,53
M 8,44,2
WL 3
LO 113,18,113
LU 18,113
C 52,52,52
M 8,45,3
RA 216,1,0
HS 24,18
v_lshlrev_b32 v52, 16, v52
M 9,56,2
WL 3
LL 17,70
LL 117,71
v_add_f32 v52, v36, v52
M 9,57,3
WV 22
LA 17
XL 82,640
C 52,52,52
M 6,22,4
s_barrier
LC_ 117
v_and_b32 v22, 0xfffffcff, v1
v_lshlrev_b32 v52, 16, v52
M 6,23,5
s_sub_u32 s39, 0, s39
RA 220,1,1024
v_sqrt_f32 v52, v52
M 7,34,4
WL 4
LO 116,16,116
LU 16,116
C 52,52,52
M 7,35,5
RV 252,22,5120
HS 24,16
v_lshlrev_b32 v52, 16, v52
M 8,46,4
LL 18,80
LL 106,81
v_rcp_f32 v52, v52
M 8,47,5
LA 18
RA 224,1,2048
v_mul_f32 v52, v38, v52
M 9,58,4
WL 3
LC_ 106
ds_read_b64 v[14:15], v5 offset:0
CF 52,52,52
M 9,59,5
WL 4
RA 228,1,3072
ds_bpermute_b32 v13, v28, v52
Z 0,12,0
LO 117,17,117
LU 17,117
M 0,13,1
XL 83,704
HS 24,17
Z 1,24,0
s_mul_i32 s53, s50, 25600
LL 16,82
LL 107,83
M 1,25,1
s_add_u32 s40, %[wlo], s53
LA 16
RA 232,1,4096
Z 2,36,0
s_addc_u32 s41, %[whi], 0
LC_ 107
v_subrev_u32 v5, s39, v5
M 2,37,1
s_mov_b32 m0, s38
HX 24
Z 3,48,0
WL 4
LO 18,18,106
LU 106,18
M 3,49,1
RA 236,1,6144
HZ 25,106
M 3,50,2
LL 17,84
LL 110,85
M 3,51,3
WL 4
LA 17
PK 106,18,104,33
XS 128
M 2,38,2
LC_ 110
PK 104,18,104,32
W1 24,0
XS 129
M 2,39,3
RA 216,1,7168
v_add_f32 v14, v14, v15
XS 130
M 1,26,2
WL 5
DW 0
LO 16,16,107
LU 107,16
v_add_f32_dpp v14, v14, v14 quad_perm:[1,0,3,2] row_mask:0xf bank_mask:0xf
XS 131
M 1,27,3
DW 1024
HS 25,107
AW 120,128
M 0,14,2
WL 4
LL 18,86
LL 111,87
v_add_f32_dpp v14, v14, v14 quad_perm:[2,3,0,1] row_mask:0xf bank_mask:0xf
AW 121,129
M 0,15,3
s_add_u32 s42, s44, s60
LA 18
PK 107,16,105,33
AW 122,130
M 0,16,4
s_addc_u32 s43, s45, 0
LC_ 111
PK 105,16,105,32
v_add_f32_dpp v14, v14, v14 row_half_mirror row_mask:0xf bank_mask:0xf
AW 123,131
XS 132
M 0,17,5
RA 220,1,8192
v_cvt_f64_f32 v[14:15], v14
XS 133
M 1,28,4
WL 5
LO 17,17,110
LU 110,17
XS 134
M 1,29,5
RA 224,1,9216
HS 25,110
XS 135
M 2,40,4
WV 25
)"
R"(LL 16,88
LL 114,89
s_mov_b64 exec, s[54:55]
SP
s_mov_b64 exec, -1
DW 2048
BW 72
AW 124,132
M 2,41,5
LA 16
RV 40,8,6144
PK 110,17,108,33
AW 125,133
M 3,52,4
WL 4
LC_ 114
RV 44,8,6208
PK 108,17,108,32
AW 126,134
AW 127,135
M 3,53,5
RA 228,1,10240
PO 110
v_min_u32 v35, v35, v13
M 3,54,0
LO 18,18,111
LU 111,18
ST 104
RV 48,8,6272
XS 136
M 3,55,1
s_add_u32 s40, s40, 3072
BR 72
HS 25,111
s_addc_u32 s41, s41, 0
s_add_u32 m0, s38, 3072
XS 137
M 2,42,0
WV 27
LL 17,90
LL 115,91
XS 138
BW 73
M 2,43,1
LA 17
PK 111,18,109,33
XS 139
M 1,30,0
WV 26
LC_ 115
BR 73
PK 109,18,109,32
AW 128,136
M 1,31,1
WL 4
RA 232,1,11264
DW 0
PO 111
AW 129,137
AW 130,138
LZ 40,72
s_add_u32 s42, s44, s61
BW 74
M 0,18,0
LO 16,16,114
LU 114,16
AW 131,139
M 0,19,1
s_addc_u32 s43, s45, 0
RV 252,22,5392
HS 25,114
XS 140
M 0,20,2
RV 40,8,6336
LL 18,92
LL 118,93
XS 141
M 0,21,3
LA 18
DW 1024
BR 74
PK 114,16,112,33
XS 142
M 1,32,2
LC_ 118
PK 112,16,112,32
XS 143
M 1,33,3
WL 7
RA 236,1,12288
AW 132,140
AW 133,141
LS 44,73
WV 27
BW 75
M 2,44,2
LO 17,17,115
LU 115,17
AW 134,142
M 2,45,3
RA 216,1,13312
HS 25,115
ST 108
RV 44,8,6400
AW 135,143
M 3,56,2
WL 3
DW 2048
BR 75
LL 16,94
LL 119,95
XS 144
M 3,57,3
LA 16
PK 115,17,113,33
XS 145
LS 48,74
WV 28
BW 76
M 0,22,4
LC_ 119
PK 113,17,113,32
XS 146
M 0,23,5
WV 27
RA 220,1,14336
RV 48,8,6464
XS 147
M 1,34,4
WL 5
DQ 3072
LO 18,18,118
LU 118,18
AW 136,144
AW 137,145
M 1,35,5
s_add_u32 s42, s44, s62
RA 224,1,15360
HS 25,118
s_addc_u32 s43, s45, 0
AW 138,146
M 2,46,4
s_add_u32 s50, s50, %[d]
BR 76
LL 17,56
LL 104,57
AW 139,147
M 2,47,5
s_cmp_eq_u32 s50, %[wrap]
LA 17
PK 118,18,116,33
XS 148
LS 40,75
s_cselect_b32 s50, %[rst], s50
BW 77
M 3,58,4
WL 5
LC_ 104
PK 116,18,116,32
XS 149
M 3,59,5
WV 15
RA 228,1,16384
PO 118
XS 150
Z 6,12,0
WL 4
LO 16,16,119
LU 119,16
ST 112
RV 40,8,6528
XS 151
AW 140,148
M 6,13,1
s_add_u32 s38, s38, 32768
BR 77
HS 25,119
AW 141,149
Z 7,24,0
s_cmp_eq_u32 s38, s77
LL 18,58
LL 105,59
s_cselect_b32 s38, s76, s38
AW 142,150
M 7,25,1
s_mul_i32 s53, s50, 25600
LA 18
PK 119,16,117,33
AW 143,151
LS 44,76
s_add_u32 s40, %[wlo], s53
BW 78
Z 8,36,0
s_addc_u32 s41, %[whi], 0
LC_ 105
PK 117,16,117,32
XS 152
M 8,37,1
s_mov_b32 m0, s38
RA 232,1,17408
RV 44,8,6592
PO 119
XS 153
Z 9,48,0
WL 5
BR 78
LO 104,17,104
LU 17,104
XS 154
M 9,49,1
s_add_u32 s42, s44, s63
RA 236,1,18432
HZ 24,17
XS 155
AW 144,152
LS 48,77
s_addc_u32 s43, s45, 0
WV 15
BW 79
M 9,50,2
LL 16,60
LL 108,61
AW 145,153
M 9,51,3
LA 16
DW 0
HX 25
AW 146,154
M 8,38,2
LC_ 108
W1 25,1024
RV 48,8,6656
AW 147,155
M 8,39,3
RA 216,1,19456
BR 79
XS 156
M 7,26,2
WL 11
LO 105,18,105
LU 18,105
XS 157
M 7,27,3
WL 8
ST 116
HS 24,18
XS 158
XS 159
LS 40,78
WV 16
BW 80
M 6,14,2
WL 8
LL 17,62
LL 109,63
AW 148,156
M 6,15,3
LA 17
RA 220,1,20480
DW 1024
AW 149,157
M 6,16,4
s_add_u32 s37, s37, 32768
LC_ 109
RV 40,8,6720
s_cmp_eq_u32 s37, s75
s_cselect_b32 s37, s74, s37
AW 150,158
M 6,17,5
WL 5
DW 2048
BR 80
AW 151,159
LS 44,79
WV 17
BW 81
M 7,28,4
LO 108,16,108
LU 16,108
v_add_u32 v4, s39, v4
XS 160
M 7,29,5
s_add_u32 s40, s40, 3072
RA 224,1,21504
HS 24,16
XS 161
M 8,40,4
s_addc_u32 s41, s41, 0
LL 18,64
LL 112,65
XS 162
XS 163
M 8,41,5
s_add_u32 m0, s38, 3072
LA 18
RV 44,8,6784
AW 152,160
M 9,52,4
WL 7
LC_ 112
BR 81
AW 153,161
M 9,53,5
s_mul_i32 s53, s51, 24
RA 228,1,22528
AW 154,162
M 9,54,0
WL 7
LO 109,17,109
LU 17,109
AW 155,163
M 9,55,1
s_bitcmp1_b32 s51, 0
HS 24,17
s_addc_u32 s53, s53, 0
s_lshl_b32 s53, s53, 11
s_bitcmp1_b64 s[90:91], s51
s_cselect_b64 s[44:45], s[84:85], s[82:83]
s_add_u32 s44, s44, s53
s_addc_u32 s45, s45, 0
s_add_u32 s51, s51, %[d]
s_cmp_eq_u32 s51, %[wrap]
s_cselect_b32 s51, %[rst], s51
XS 164
LS 48,80
WV 16
BW 82
M 8,42,0
RV 48,8,6848
LL 16,66
LL 113,67
XS 165
XS 166
M 8,43,1
WV 15
LA 16
RA 232,1,23552
XS 167
M 7,30,0
WL 5
LC_ 113
DW 0
RA 240,7,0
AW 156,164
M 7,31,1
s_lshl_b32 s53, s52, 8
BR 82
AW 157,165
LS 40,81
s_add_u32 s48, %[alo], s53
BW 83
M 6,18,0
WL 8
LO 112,18,112
LU 18,112
AW 158,166
M 6,19,1
s_addc_u32 s49, %[ahi], 0
RA 236,1,24576
HS 24,18
AW 159,167
M 6,20,2
s_add_u32 s52, s52, %[d]
LL 17,68
LL 116,69
s_cmp_eq_u32 s52, %[wrap]
s_cselect_b32 s52, %[rst], s52
DW 1024
XS 168
M 6,21,3
LA 17
DW 2048
RA 244,7,256
XS 169
XS 170
M 7,32,2
LC_ 116
BR 83
XS 171
M 7,33,3
WL 6
DQ 3072
v_add_u32 v1, s37, v0
AW 160,168
LS 44,82
RA 248,7,512
AW 161,169
M 8,44,2
LO 113,16,113
LU 16,113
M 8,45,3
RA 216,1,0
HS 24,16
AW 162,170
M 9,56,2
WL 2
LL 18,70
LL 117,71
AW 163,171
M 9,57,3
)"
R"(s_add_u32 s50, s50, %[d]
LA 18
v_and_b32 v22, 0xfffffcff, v1
XS 172
XS 173
LS 48,83
s_cmp_eq_u32 s50, %[wrap]
RA 252,7,768
XS 174
M 6,22,4
s_cselect_b32 s50, %[rst], s50
LC_ 117
RA 220,1,1024
M 6,23,5
s_barrier
RV 252,22,5120
XS 175
M 7,34,4
LO 116,17,116
LU 17,116
AW 164,172
M 7,35,5
s_sub_u32 s39, 0, s39
RA 224,1,2048
HS 24,17
AW 165,173
M 8,46,4
s_add_u32 s38, s38, 32768
LL 16,80
LL 106,81
AW 166,174
M 8,47,5
s_cmp_eq_u32 s38, s77
LA 16
ds_read_b64 v[14:15], v5 offset:0
AW 167,175
v_mul_f32 v36, 0xbb2aaaab, v36
v_mul_f32 v37, 0xbb2aaaab, v37
v_mul_f32 v38, 0xbb2aaaab, v38
v_mul_f32 v39, 0xbb2aaaab, v39
s_nop 1
LK 40,240,72,0
LK 44,242,72,2
s_cselect_b32 s38, s76, s38
RA 240,7,1024
LK 48,244,73,0
s_nop 0
CF 128,40,41
LK 248,246,73,2
RA 244,7,1280
CF 129,42,43
M 9,58,4
WL 6
LC_ 106
v_subrev_u32 v5, s39, v5
M 9,59,5
WL 6
RA 228,1,3072
CF 130,44,45
Z 0,12,0
LO 117,18,117
LU 18,117
CF 131,46,47
M 0,13,1
s_mul_i32 s53, s50, 25600
HS 24,18
LK 40,248,74,0
CF 132,48,49
LK 44,250,74,2
s_add_u32 s40, %[wlo], s53
RA 248,7,1536
CF 133,50,51
GZ 0
s_addc_u32 s41, %[whi], 0
CF 134,248,249
CF 135,250,251
LK 48,252,75,0
CF 136,40,41
LK 248,254,75,2
s_mov_b32 m0, s38
RA 252,7,1792
CF 137,42,43
GS 1
WL 5
CF 138,44,45
CF 139,46,47
LK 40,240,76,0
CF 140,48,49
LK 44,242,76,2
RA 240,7,2048
CF 141,50,51
GS 2
WL 5
CF 142,248,249
CF 143,250,251
LK 48,244,77,0
CF 144,40,41
LK 248,246,77,2
RA 244,7,2304
CF 145,42,43
GS 3
CF 146,44,45
CF 147,46,47
Z 1,24,0
LL 17,82
LL 107,83
M 1,25,1
LA 17
RA 232,1,4096
Z 2,36,0
LC_ 107
HX 24
M 2,37,1
WL 6
W1 24,0
LK 40,248,78,0
CF 148,48,49
LK 44,250,78,2
RA 248,7,2560
CF 149,50,51
GS 4
CF 150,248,249
CF 151,250,251
Z 3,48,0
LO 16,16,106
LU 106,16
M 3,49,1
RA 236,1,6144
HZ 25,106
M 3,50,2
LL 18,84
LL 110,85
M 3,51,3
WL 8
LA 18
PK 106,16,104,33
LK 48,252,79,0
CF 152,40,41
LK 248,254,79,2
RA 252,7,2816
CF 153,42,43
GS 5
CF 154,44,45
CF 155,46,47
M 2,38,2
LC_ 110
PK 104,16,104,32
M 2,39,3
RA 216,1,7168
v_add_f32 v14, v14, v15
M 1,26,2
WL 7
LO 17,17,107
LU 107,17
v_add_f32_dpp v14, v14, v14 quad_perm:[1,0,3,2] row_mask:0xf bank_mask:0xf
M 1,27,3
DW 0
HS 25,107
LK 40,240,80,0
CF 156,48,49
LK 44,242,80,2
s_add_u32 s42, s44, s60
CF 157,50,51
v_add_f32_dpp v14, v14, v14 quad_perm:[2,3,0,1] row_mask:0xf bank_mask:0xf
GS 6
s_addc_u32 s43, s45, 0
CF 158,248,249
CF 159,250,251
M 0,14,2
WL 4
LL 16,86
LL 111,87
M 0,15,3
LA 16
PK 107,17,105,33
M 0,16,4
LC_ 111
PK 105,17,105,32
v_add_f32_dpp v14, v14, v14 row_half_mirror row_mask:0xf bank_mask:0xf
M 0,17,5
RA 220,1,8192
v_cvt_f64_f32 v[14:15], v14
LK 48,244,81,0
CF 160,40,41
LK 248,246,81,2
CF 161,42,43
GS 7
CF 162,44,45
CF 163,46,47
M 1,28,4
WL 4
LO 18,18,110
LU 110,18
M 1,29,5
RA 224,1,9216
HS 25,110
M 2,40,4
LL 17,88
LL 114,89
s_mov_b64 exec, s[54:55]
SP
s_mov_b64 exec, -1
M 2,41,5
LA 17
PK 110,18,108,33
LK 40,248,82,0
CF 164,48,49
LK 44,250,82,2
CF 165,50,51
GS 8
CF 166,248,249
CF 167,250,251
M 3,52,4
WL 2
LC_ 114
PK 108,18,108,32
M 3,53,5
RA 228,1,10240
PO 110
M 3,54,0
LO 16,16,111
LU 111,16
ST 104
M 3,55,1
DW 1024
HS 25,111
LK 48,252,83,0
CF 168,40,41
LK 248,254,83,2
s_add_u32 s42, s44, s61
CF 169,42,43
GS 9
s_addc_u32 s43, s45, 0
CF 170,44,45
CF 171,46,47
M 2,42,0
LL 18,90
LL 115,91
M 2,43,1
LA 18
PK 111,16,109,33
M 1,30,0
LC_ 115
PK 109,16,109,32
M 1,31,1
RA 232,1,11264
PO 111
M 0,18,0
WL 3
LO 17,17,114
LU 114,17
M 0,19,1
RV 252,22,5392
HS 25,114
M 0,20,2
LL 16,92
LL 118,93
M 0,21,3
LA 16
PK 114,17,112,33
M 1,32,2
LC_ 118
PK 112,17,112,32
M 1,33,3
RA 236,1,12288
RV 36,8,6912
DW 2048
GS 10
CF 172,48,49
CF 173,50,51
M 2,44,2
WL 6
LO 18,18,115
LU 115,18
M 2,45,3
RA 216,1,13312
HS 25,115
ST 108
M 3,56,2
WL 2
LL 17,94
LL 119,95
M 3,57,3
s_add_u32 s42, s44, s62
LA 17
PK 115,18,113,33
M 0,22,4
s_addc_u32 s43, s45, 0
LC_ 119
PK 113,18,113,32
M 0,23,5
s_add_u32 s40, s40, 3072
RA 220,1,14336
CF 174,248,249
M 1,34,4
LO 16,16,118
LU 118,16
CF 175,250,251
M 1,35,5
s_addc_u32 s41, s41, 0
RA 224,1,15360
HS 25,118
GS 11
s_add_u32 m0, s38, 3072
ST 112
s_nop 5
v_cndmask_b32 v52, v52, v53, s[78:79]
M 2,46,4
LL 18,56
LL 104,57
v_cndmask_b32 v54, v54, v55, s[78:79]
M 2,47,5
LA 18
PK 118,16,116,33
v_cndmask_b32 v52, v52, v54, s[80:81]
M 3,58,4
WL 2
LC_ 104
PK 116,16,116,32
M 3,59,5
RA 228,1,16384
PO 118
v_mul_f32 v52, 0x3b2aaaab, v52
Z 6,12,0
LO 17,17,119
LU 119,17
v_med3_f32 v53, v52, v39, v37
M 6,13,1
DW 0
HS 25,119
LG 52,53
Z 7,24,0
s_add_u32 s42, s44, s63
LL 16,58
LL 105,59
C 52,52,52
M 7,25,1
s_addc_u32 s43, s45, 0
LA 16
PK 119,17,117,33
Z 8,36,0
LC_ 105
PK 117,17,117,32
v_lshlrev_b32 v52, 16, v52
M 8,37,1
RA 232,1,17408
PO 119
v_add_f32 v52, v36, v52
Z 9,48,0
WL 3
LO 104,18,104
LU 18,104
C 52,52,52
M 9,49,1
RA 236,1,18432
HZ 24,18
M 9,50,2
LL 17,60
LL 108,61
v_lshlrev_b32 v52, 16, v52
M 9,51,3
LA 17
ST 116
HX 25
v_sqrt_f32 v52, v52
M 8,38,2
LC_ 108
W1 25,1024
C 52,52,52
M 8,39,3
RA 216,1,19456
v_add_u32 v4, s39, v4
v_lshlrev_b32 v52, 16, v52
M 7,26,2
WL 6
LO 105,16,105
LU 16,105
M 7,27,3
DW 1024
HS 24,16
v_rcp_f32 v52, v52
M 6,14,2
WL 4
LL 18,62
LL 109,63
v_mul_f32 v52, v38, v52
M 6,15,3
s_add_u32 s37, s37, 32768
LA 18
RA 220,1,20480
CF 52,52,52
M 6,16,4
s_cmp_eq_u32 s37, s75
LC_ 109
ds_bpermute_b32 v13, v28, v52
s_cselect_b32 s37, s74, s37
M 6,17,5
DW 2048
M 7,28,4
WL 6
)"
R"(LO 108,17,108
LU 17,108
M 7,29,5
s_mul_i32 s53, s51, 24
RA 224,1,21504
HS 24,17
M 8,40,4
s_bitcmp1_b32 s51, 0
LL 16,64
LL 112,65
M 8,41,5
s_addc_u32 s53, s53, 0
LA 16
M 9,52,4
WL 4
LC_ 112
M 9,53,5
s_lshl_b32 s53, s53, 11
RA 228,1,22528
M 9,54,0
WL 4
LO 109,18,109
LU 18,109
M 9,55,1
s_bitcmp1_b64 s[90:91], s51
HS 24,18
s_cselect_b64 s[44:45], s[84:85], s[82:83]
s_add_u32 s44, s44, s53
s_addc_u32 s45, s45, 0
s_add_u32 s51, s51, %[d]
s_cmp_eq_u32 s51, %[wrap]
s_cselect_b32 s51, %[rst], s51
M 8,42,0
s_lshl_b32 s53, s52, 8
LL 17,66
LL 113,67
M 8,43,1
s_add_u32 s48, %[alo], s53
LA 17
RA 232,1,23552
XS 128
M 7,30,0
s_addc_u32 s49, %[ahi], 0
LC_ 113
XS 129
M 7,31,1
s_add_u32 s52, s52, %[d]
XS 130
XS 131
M 6,18,0
WL 3
LO 112,16,112
LU 16,112
M 6,19,1
s_cmp_eq_u32 s52, %[wrap]
RA 236,1,24576
HS 24,16
AW 168,128
AW 169,129
M 6,20,2
s_cselect_b32 s52, %[rst], s52
DQ 3072
LL 18,68
LL 116,69
AW 170,130
M 6,21,3
LA 18
v_add_u32 v1, s37, v0
AW 171,131
M 7,32,2
s_add_u32 s50, s50, %[d]
LC_ 116
XS 132
M 7,33,3
s_cmp_eq_u32 s50, %[wrap]
RA 216,1,0
v_and_b32 v22, 0xfffffcff, v1
XS 133
M 8,44,2
WL 4
LO 113,17,113
LU 17,113
XS 134
M 8,45,3
s_cselect_b32 s50, %[rst], s50
HS 24,17
XS 135
M 9,56,2
WL 3
LL 16,70
LL 117,71
AW 172,132
M 9,57,3
WV 21
LA 16
RA 220,1,1024
AW 173,133
M 6,22,4
s_barrier
LC_ 117
RV 252,22,5120
AW 174,134
M 6,23,5
s_sub_u32 s39, 0, s39
AW 175,135
v_min_u32 v35, v35, v13
M 7,34,4
WL 5
LO 116,18,116
LU 18,116
M 7,35,5
s_add_u32 s38, s38, 32768
RA 224,1,2048
HS 24,18
XS 136
M 8,46,4
s_cmp_eq_u32 s38, s77
LL 17,80
LL 106,81
XS 137
M 8,47,5
s_cselect_b32 s38, s76, s38
LA 17
XS 138
XS 139
M 9,58,4
WL 4
LC_ 106
AW 176,136
M 9,59,5
WL 3
RA 228,1,3072
AW 177,137
Z 0,12,0
LO 117,16,117
LU 16,117
AW 178,138
M 0,13,1
HS 24,16
AW 179,139
Z 1,24,0
LL 18,82
LL 107,83
XS 140
M 1,25,1
LA 18
RA 232,1,4096
XS 141
Z 2,36,0
LC_ 107
HX 24
XS 142
M 2,37,1
W1 24,0
XS 143
Z 3,48,0
WL 5
LO 17,17,106
LU 106,17
AW 180,140
M 3,49,1
HZ 25,106
AW 181,141
Z 4,60,0
LL 16,84
LL 110,85
AW 182,142
M 4,61,1
LA 16
PK 106,17,104,33
AW 183,143
Z 5,72,0
WL 5
LC_ 110
PK 104,17,104,32
XS 144
M 5,73,1
RA 236,1,6144
XS 145
XS 146
M 5,74,2
WL 4
LO 18,18,107
LU 107,18
XS 147
M 5,75,3
s_add_u32 s42, s44, s60
HS 25,107
AW 184,144
M 4,62,2
s_addc_u32 s43, s45, 0
LL 17,86
LL 111,87
AW 185,145
M 4,63,3
LA 17
PK 107,18,105,33
AW 186,146
M 3,50,2
LC_ 111
PK 105,18,105,32
AW 187,147
M 3,51,3
RA 216,1,7168
XS 148
M 2,38,2
WL 4
LO 16,16,110
LU 110,16
XS 149
M 2,39,3
ST 104
HS 25,110
XS 150
M 1,26,2
LL 18,88
LL 114,89
XS 151
M 1,27,3
LA 18
PK 110,16,108,33
AW 188,148
M 0,14,2
WL 4
LC_ 114
PK 108,16,108,32
AW 189,149
M 0,15,3
s_mov_b32 m0, %[pm]
RA 220,1,8192
PO 110
AW 190,150
M 0,16,4
WL 4
LO 17,17,111
LU 111,17
AW 191,151
M 0,17,5
DF
HS 25,111
XS 152
M 1,28,4
s_add_u32 s42, s44, s61
LL 16,90
LL 115,91
XS 153
XS 154
M 1,29,5
s_addc_u32 s43, s45, 0
LA 16
PK 111,17,109,33
XS 155
M 2,40,4
LC_ 115
PK 109,17,109,32
AW 192,152
M 2,41,5
RA 224,1,9216
PO 111
AW 193,153
M 3,52,4
WL 4
LO 18,18,114
LU 114,18
AW 194,154
M 3,53,5
ST 108
HS 25,114
AW 195,155
M 4,64,4
LL 17,92
LL 118,93
XS 156
XS 157
M 4,65,5
LA 17
PK 114,18,112,33
XS 158
XS 159
AW 196,156
M 5,76,4
WL 4
LC_ 118
PK 112,18,112,32
AW 197,157
AW 198,158
AW 199,159
M 5,77,5
RA 228,1,10240
XS 160
M 5,78,0
WL 4
LO 16,16,115
LU 115,16
M 5,79,1
s_add_u32 s42, s44, s62
HS 25,115
XS 161
M 4,66,0
s_addc_u32 s43, s45, 0
LL 18,94
LL 119,95
M 4,67,1
LA 18
PK 115,16,113,33
M 3,54,0
LC_ 119
PK 113,16,113,32
M 3,55,1
RA 232,1,11264
XS 162
M 2,42,0
WL 4
LO 17,17,118
LU 118,17
XS 163
M 2,43,1
RV 252,22,5392
HS 25,118
AW 200,160
M 1,30,0
ST 112
AW 201,161
M 1,31,1
RA 236,1,12288
PK 118,17,116,33
AW 202,162
M 0,18,0
WL 5
PK 116,17,116,32
AW 203,163
M 0,19,1
PO 118
XS 164
M 0,20,2
WL 3
LO 18,18,119
LU 119,18
XS 165
M 0,21,3
s_add_u32 s42, s44, s63
HS 25,119
XS 166
M 1,32,2
s_addc_u32 s43, s45, 0
HX 25
XS 167
M 1,33,3
RA 216,1,13312
PK 119,18,117,33
AW 204,164
M 2,44,2
W1 25,1024
PK 117,18,117,32
AW 205,165
M 2,45,3
PO 119
v_add_u32 v4, s39, v4
AW 206,166
M 3,56,2
ST 116
AW 207,167
XS 168
M 3,57,3
XS 169
XS 170
M 4,68,2
XS 171
AW 208,168
M 4,69,3
RA 220,1,14336
AW 209,169
M 5,80,2
WL 3
AW 210,170
AW 211,171
M 5,81,3
XS 172
XS 173
M 0,22,4
XS 174
XS 175
M 0,23,5
AW 212,172
AW 213,173
M 1,34,4
AW 214,174
AW 215,175
M 1,35,5
RA 224,1,15360
M 2,46,4
LL 16,56
LL 104,57
M 2,47,5
LA 16
ds_read_b64 v[14:15], v5 offset:0
M 3,58,4
LC_ 104
v_subrev_u32 v5, s39, v5
M 3,59,5
)"
R"(M 4,70,4
M 4,71,5
RA 228,1,16384
M 5,82,4
WL 5
LL 17,58
LL 105,59
M 5,83,5
LA 17
Z 6,12,0
LC_ 105
M 6,13,1
Z 7,24,0
WL 3
LO 104,16,104
LU 16,104
M 7,25,1
RA 232,1,17408
HZ 24,16
Z 8,36,0
LL 18,60
LL 108,61
M 8,37,1
s_mul_i32 s53, s50, 25600
LA 18
v_add_f32 v14, v14, v15
Z 9,48,0
s_add_u32 s40, %[wlo], s53
LC_ 108
v_add_f32_dpp v14, v14, v14 quad_perm:[1,0,3,2] row_mask:0xf bank_mask:0xf
M 9,49,1
s_addc_u32 s41, %[whi], 0
v_add_f32_dpp v14, v14, v14 quad_perm:[2,3,0,1] row_mask:0xf bank_mask:0xf
Z 10,60,0
WL 3
LO 105,17,105
LU 17,105
M 10,61,1
s_mov_b32 m0, s38
RA 236,1,18432
HS 24,17
Z 11,72,0
LL 16,62
LL 109,63
M 11,73,1
LA 16
DW 0
M 11,74,2
LC_ 109
v_add_f32_dpp v14, v14, v14 row_half_mirror row_mask:0xf bank_mask:0xf
M 11,75,3
v_cvt_f64_f32 v[14:15], v14
M 10,62,2
WL 3
LO 108,18,108
LU 18,108
M 10,63,3
s_add_u32 s37, s37, 32768
RA 216,1,19456
HS 24,18
M 9,50,2
s_cmp_eq_u32 s37, s75
LL 17,64
LL 112,65
M 9,51,3
s_cselect_b32 s37, s74, s37
LA 17
s_mov_b64 exec, s[54:55]
SP
s_mov_b64 exec, -1
M 8,38,2
LC_ 112
DW 1024
M 8,39,3
M 7,26,2
WL 3
LO 109,16,109
LU 16,109
M 7,27,3
RA 220,1,20480
HS 24,16
M 6,14,2
LL 18,66
LL 113,67
M 6,15,3
LA 18
DW 2048
M 6,16,4
LC_ 113
M 6,17,5
M 7,28,4
WL 3
LO 112,17,112
LU 17,112
M 7,29,5
s_mul_i32 s53, s51, 24
RA 224,1,21504
HS 24,17
M 8,40,4
s_bitcmp1_b32 s51, 0
LL 16,68
LL 116,69
M 8,41,5
s_addc_u32 s53, s53, 0
LA 16
M 9,52,4
s_lshl_b32 s53, s53, 11
LC_ 116
M 9,53,5
s_bitcmp1_b64 s[90:91], s51
M 10,64,4
WL 3
LO 113,18,113
LU 18,113
M 10,65,5
s_cselect_b64 s[44:45], s[84:85], s[82:83]
RA 228,1,22528
HS 24,18
M 11,76,4
LL 17,70
LL 117,71
M 11,77,5
s_add_u32 s44, s44, s53
LA 17
M 11,78,0
s_addc_u32 s45, s45, 0
LC_ 117
M 11,79,1
s_add_u32 s51, s51, %[d]
M 10,66,0
WL 3
LO 116,16,116
LU 16,116
M 10,67,1
s_cmp_eq_u32 s51, %[wrap]
RA 232,1,23552
HS 24,16
M 9,54,0
s_cselect_b32 s51, %[rst], s51
LL 18,72
LL 120,73
M 9,55,1
s_add_u32 s40, s40, 3072
LA 18
M 8,42,0
s_addc_u32 s41, s41, 0
LC_ 120
s_add_u32 m0, s38, 3072
M 8,43,1
DW 0
M 7,30,0
WL 3
LO 117,17,117
LU 17,117
M 7,31,1
RA 236,1,24576
HS 24,17
M 6,18,0
LL 16,74
LL 121,75
M 6,19,1
LA 16
DW 1024
M 6,20,2
LC_ 121
DW 2048
M 6,21,3
s_lshl_b32 s53, s52, 8
v_add_u32 v1, s37, v0
M 7,32,2
WL 3
LO 120,18,120
LU 18,120
M 7,33,3
s_add_u32 s48, %[alo], s53
RA 216,1,0
HS 24,18
M 8,44,2
s_addc_u32 s49, %[ahi], 0
LL 17,76
LL 124,77
M 8,45,3
s_add_u32 s52, s52, %[d]
LA 17
v_and_b32 v22, 0xfffffcff, v1
M 9,56,2
s_cmp_eq_u32 s52, %[wrap]
LC_ 124
M 9,57,3
s_cselect_b32 s52, %[rst], s52
DQ 3072
M 10,68,2
WL 3
LO 121,16,121
LU 16,121
M 10,69,3
s_add_u32 s50, s50, %[d]
RA 220,1,1024
HS 24,16
M 11,80,2
LL 18,78
LL 125,79
M 11,81,3
s_cmp_eq_u32 s50, %[wrap]
LA 18
RV 252,22,5120
M 6,22,4
s_cselect_b32 s50, %[rst], s50
LC_ 125
M 6,23,5
WV 25
M 7,34,4
WL 4
LO 124,17,124
LU 17,124
M 7,35,5
s_add_u32 s38, s38, 32768
RA 224,1,2048
HS 24,17
M 8,46,4
s_cmp_eq_u32 s38, s77
LL 16,80
LL 106,81
M 8,47,5
s_cselect_b32 s38, s76, s38
LA 16
M 9,58,4
s_barrier
LC_ 106
M 9,59,5
s_sub_u32 s39, 0, s39
M 10,70,4
WL 3
LO 125,18,125
LU 18,125
M 10,71,5
RA 228,1,3072
HS 24,18
M 11,82,4
LL 17,82
LL 107,83
M 11,83,5
LA 17
HX 24
Z 0,12,0
LC_ 107
W1 24,0
M 0,13,1
Z 1,24,0
WL 4
LO 16,16,106
LU 106,16
M 1,25,1
RA 232,1,4096
HZ 25,106
Z 2,36,0
LL 18,84
LL 110,85
M 2,37,1
LA 18
PK 106,16,104,33
Z 3,48,0
LC_ 110
PK 104,16,104,32
M 3,49,1
Z 4,60,0
WL 4
LO 17,17,107
LU 107,17
M 4,61,1
s_add_u32 s42, s44, s60
RA 236,1,6144
HS 25,107
Z 5,72,0
LL 16,86
LL 111,87
M 5,73,1
s_addc_u32 s43, s45, 0
LA 16
PK 107,17,105,33
M 5,74,2
LC_ 111
PK 105,17,105,32
M 5,75,3
ST 104
M 4,62,2
WL 3
LO 18,18,110
LU 110,18
M 4,63,3
RA 216,1,7168
HS 25,110
M 3,50,2
LL 17,88
LL 114,89
M 3,51,3
LA 17
PK 110,18,108,33
M 2,38,2
LC_ 114
PK 108,18,108,32
M 2,39,3
PO 110
M 1,26,2
WL 3
LO 16,16,111
LU 111,16
M 1,27,3
s_add_u32 s42, s44, s61
RA 220,1,8192
HS 25,111
M 0,14,2
LL 18,90
LL 115,91
M 0,15,3
s_addc_u32 s43, s45, 0
LA 18
PK 111,16,109,33
M 0,16,4
s_mov_b32 m0, %[pm]
LC_ 115
PK 109,16,109,32
M 0,17,5
DF
PO 111
M 1,28,4
WL 3
LO 17,17,114
LU 114,17
M 1,29,5
RA 224,1,9216
HS 25,114
M 2,40,4
LL 16,92
LL 118,93
M 2,41,5
LA 16
PK 114,17,112,33
M 3,52,4
LC_ 118
PK 112,17,112,32
M 3,53,5
ST 108
M 4,64,4
WL 3
LO 18,18,115
LU 115,18
M 4,65,5
s_add_u32 s42, s44, s62
RA 228,1,10240
HS 25,115
M 5,76,4
LL 17,94
LL 119,95
M 5,77,5
s_addc_u32 s43, s45, 0
LA 17
PK 115,18,113,33
M 5,78,0
LC_ 119
PK 113,18,113,32
M 5,79,1
ST 112
M 4,66,0
WL 3
LO 16,16,118
LU 118,16
M 4,67,1
RA 232,1,11264
HS 25,118
M 3,54,0
LL 18,96
LL 122,97
M 3,55,1
LA 18
PK 118,16,116,33
M 2,42,0
LC_ 122
PK 116,16,116,32
M 2,43,1
RV 252,22,5392
PO 118
M 1,30,0
WL 4
LO 17,17,119
LU 119,17
M 1,31,1
s_add_u32 s42, s44, s63
RA 236,1,12288
HS 25,119
M 0,18,0
LL 16,98
LL 123,99
M 0,19,1
s_addc_u32 s43, s45, 0
LA 16
PK 119,17,117,33
M 0,20,2
LC_ 123
PK 117,17,117,32
)"
R"(M 0,21,3
PO 119
M 1,32,2
WL 4
LO 18,18,122
LU 122,18
M 1,33,3
RA 216,1,13312
HS 25,122
M 2,44,2
LL 17,100
LL 126,101
M 2,45,3
LA 17
PK 122,18,120,33
M 3,56,2
LC_ 126
PK 120,18,120,32
M 3,57,3
ST 116
M 4,68,2
WL 3
LO 16,16,123
LU 123,16
M 4,69,3
s_add_u32 s42, s44, s64
RA 220,1,14336
HS 25,123
M 5,80,2
LL 18,102
LL 127,103
M 5,81,3
s_addc_u32 s43, s45, 0
LA 18
PK 123,16,121,33
M 0,22,4
LC_ 127
PK 121,16,121,32
M 0,23,5
ST 120
M 1,34,4
WL 3
LO 17,17,126
LU 126,17
M 1,35,5
RA 224,1,15360
HS 25,126
M 2,46,4
LL 16,56
LL 104,57
M 2,47,5
LA 16
PK 126,17,124,33
M 3,58,4
LC_ 104
PK 124,17,124,32
M 3,59,5
ds_read_b64 v[14:15], v5 offset:0
PO 126
M 4,70,4
WL 4
LO 18,18,127
LU 127,18
M 4,71,5
s_add_u32 s42, s44, s65
RA 228,1,16384
HS 25,127
M 5,82,4
LL 17,58
LL 105,59
M 5,83,5
s_addc_u32 s43, s45, 0
LA 17
PK 127,18,125,33
Z 6,12,0
LC_ 105
PK 125,18,125,32
M 6,13,1
PO 127
v_subrev_u32 v5, s39, v5
Z 7,24,0
WL 4
LO 104,16,104
LU 16,104
M 7,25,1
RA 232,1,17408
HZ 24,16
Z 8,36,0
WL 4
LL 18,60
LL 108,61
M 8,37,1
s_mul_i32 s53, s50, 25600
LA 18
v_add_f32 v14, v14, v15
Z 9,48,0
s_add_u32 s40, %[wlo], s53
LC_ 108
v_add_f32_dpp v14, v14, v14 quad_perm:[1,0,3,2] row_mask:0xf bank_mask:0xf
M 9,49,1
s_addc_u32 s41, %[whi], 0
v_add_f32_dpp v14, v14, v14 quad_perm:[2,3,0,1] row_mask:0xf bank_mask:0xf
HX 25
Z 10,60,0
WL 3
LO 105,17,105
LU 17,105
M 10,61,1
s_mov_b32 m0, s38
RA 236,1,18432
HS 24,17
Z 11,72,0
LL 16,62
LL 109,63
M 11,73,1
LA 16
W1 25,1024
M 11,74,2
LC_ 109
ST 124
M 11,75,3
DW 0
v_add_f32_dpp v14, v14, v14 row_half_mirror row_mask:0xf bank_mask:0xf
M 10,62,2
WL 4
LO 108,18,108
LU 18,108
M 10,63,3
s_add_u32 s37, s37, 32768
RA 216,1,19456
HS 24,18
M 9,50,2
s_cmp_eq_u32 s37, s75
LL 17,64
LL 112,65
M 9,51,3
s_cselect_b32 s37, s74, s37
LA 17
DW 1024
M 8,38,2
LC_ 112
v_cvt_f64_f32 v[14:15], v14
M 8,39,3
s_mov_b64 exec, s[54:55]
SP
s_mov_b64 exec, -1
v_add_u32 v4, s39, v4
M 7,26,2
WL 3
LO 109,16,109
LU 16,109
M 7,27,3
RA 220,1,20480
HS 24,16
M 6,14,2
LL 18,66
LL 113,67
M 6,15,3
LA 18
DW 2048
M 6,16,4
LC_ 113
M 6,17,5
M 7,28,4
WL 3
LO 112,17,112
LU 17,112
M 7,29,5
s_mul_i32 s53, s51, 24
RA 224,1,21504
HS 24,17
M 8,40,4
s_bitcmp1_b32 s51, 0
LL 16,68
LL 116,69
M 8,41,5
s_addc_u32 s53, s53, 0
LA 16
M 9,52,4
s_lshl_b32 s53, s53, 11
LC_ 116
M 9,53,5
s_bitcmp1_b64 s[90:91], s51
M 10,64,4
WL 3
LO 113,18,113
LU 18,113
M 10,65,5
s_cselect_b64 s[44:45], s[84:85], s[82:83]
RA 228,1,22528
HS 24,18
M 11,76,4
LL 17,70
LL 117,71
M 11,77,5
s_add_u32 s44, s44, s53
LA 17
M 11,78,0
s_addc_u32 s45, s45, 0
LC_ 117
M 11,79,1
s_add_u32 s51, s51, %[d]
M 10,66,0
WL 3
LO 116,16,116
LU 16,116
M 10,67,1
s_cmp_eq_u32 s51, %[wrap]
RA 232,1,23552
HS 24,16
M 9,54,0
s_cselect_b32 s51, %[rst], s51
LL 18,72
LL 120,73
M 9,55,1
s_add_u32 s40, s40, 3072
LA 18
M 8,42,0
s_addc_u32 s41, s41, 0
LC_ 120
s_add_u32 m0, s38, 3072
M 8,43,1
DW 0
M 7,30,0
WL 3
LO 117,17,117
LU 17,117
M 7,31,1
RA 236,1,24576
HS 24,17
M 6,18,0
LL 16,74
LL 121,75
M 6,19,1
LA 16
DW 1024
M 6,20,2
LC_ 121
DW 2048
M 6,21,3
s_lshl_b32 s53, s52, 8
v_add_u32 v1, s37, v0
M 7,32,2
WL 3
LO 120,18,120
LU 18,120
M 7,33,3
s_add_u32 s48, %[alo], s53
RA 216,1,0
HS 24,18
M 8,44,2
s_addc_u32 s49, %[ahi], 0
LL 17,76
LL 124,77
M 8,45,3
s_add_u32 s52, s52, %[d]
LA 17
v_and_b32 v22, 0xfffffcff, v1
M 9,56,2
s_cmp_eq_u32 s52, %[wrap]
LC_ 124
M 9,57,3
s_cselect_b32 s52, %[rst], s52
DQ 3072
M 10,68,2
WL 3
LO 121,16,121
LU 16,121
M 10,69,3
s_add_u32 s50, s50, %[d]
RA 220,1,1024
HS 24,16
M 11,80,2
LL 18,78
LL 125,79
M 11,81,3
s_cmp_eq_u32 s50, %[wrap]
LA 18
RV 252,22,5120
M 6,22,4
s_cselect_b32 s50, %[rst], s50
LC_ 125
M 6,23,5
WV 28
v_add_u32 v19, 1, v19
M 7,34,4
WL 4
LO 124,17,124
LU 17,124
M 7,35,5
s_add_u32 s38, s38, 32768
RA 224,1,2048
HS 24,17
M 8,46,4
s_cmp_eq_u32 s38, s77
LL 16,80
LL 106,81
M 8,47,5
s_cselect_b32 s38, s76, s38
LA 16
FP
M 9,58,4
s_sub_u32 s39, 0, s39
LC_ 106
M 9,59,5
M 10,70,4
WL 4
LO 125,18,125
LU 18,125
M 10,71,5
RA 228,1,3072
HS 24,18
M 11,82,4
LL 17,82
LL 107,83
M 11,83,5
s_mov_b32 s66, 28
1:
LA 17
HX 24
Z 0,12,0
LC_ 107
M 0,13,1
Z 1,24,0
WL 3
LO 16,16,106
LU 106,16
M 1,25,1
RA 232,1,4096
HZ 25,106
Z 2,36,0
LL 18,84
LL 110,85
M 2,37,1
LA 18
PK 106,16,104,33
Z 3,48,0
LC_ 110
PK 104,16,104,32
M 3,49,1
Z 4,60,0
WL 3
LO 17,17,107
LU 107,17
M 4,61,1
s_add_u32 s42, s44, s60
RA 236,1,6144
HS 25,107
Z 5,72,0
LL 16,86
LL 111,87
M 5,73,1
s_addc_u32 s43, s45, 0
LA 16
PK 107,17,105,33
M 5,74,2
LC_ 111
PK 105,17,105,32
M 5,75,3
ST 104
M 4,62,2
WL 3
LO 18,18,110
LU 110,18
M 4,63,3
RA 216,1,7168
HS 25,110
M 3,50,2
LL 17,88
LL 114,89
M 3,51,3
LA 17
PK 110,18,108,33
M 2,38,2
LC_ 114
PK 108,18,108,32
M 2,39,3
FR
PO 110
M 1,26,2
WL 4
LO 16,16,111
LU 111,16
M 1,27,3
s_add_u32 s42, s44, s61
RA 220,1,8192
HS 25,111
M 0,14,2
LL 18,90
LL 115,91
M 0,15,3
s_addc_u32 s43, s45, 0
LA 18
PK 111,16,109,33
M 0,16,4
s_mov_b32 m0, %[pm]
LC_ 115
PK 109,16,109,32
M 0,17,5
DF
)"
R"(PO 111
M 1,28,4
WL 4
LO 17,17,114
LU 114,17
M 1,29,5
RA 224,1,9216
HS 25,114
M 2,40,4
LL 16,92
LL 118,93
M 2,41,5
LA 16
PK 114,17,112,33
M 3,52,4
LC_ 118
PK 112,17,112,32
M 3,53,5
ST 108
M 4,64,4
WL 3
LO 18,18,115
LU 115,18
M 4,65,5
s_add_u32 s42, s44, s62
RA 228,1,10240
HS 25,115
M 5,76,4
LL 17,94
LL 119,95
M 5,77,5
s_addc_u32 s43, s45, 0
LA 17
PK 115,18,113,33
M 5,78,0
LC_ 119
PK 113,18,113,32
M 5,79,1
ST 112
M 4,66,0
WL 3
LO 16,16,118
LU 118,16
M 4,67,1
RA 232,1,11264
HS 25,118
M 3,54,0
LL 18,96
LL 122,97
M 3,55,1
LA 18
PK 118,16,116,33
v_cmp_gt_u32 vcc, v19, v15
M 2,42,0
LC_ 122
PK 116,16,116,32
M 2,43,1
RV 252,22,5392
PO 118
M 1,30,0
WL 4
LO 17,17,119
LU 119,17
M 1,31,1
s_cbranch_vccnz .Lk1s0
.Lk1r0:
RA 236,1,12288
HS 25,119
M 0,18,0
LL 16,98
LL 123,99
M 0,19,1
s_add_u32 s42, s44, s63
LA 16
PK 119,17,117,33
M 0,20,2
s_addc_u32 s43, s45, 0
LC_ 123
PK 117,17,117,32
M 0,21,3
W1 24,0
PO 119
M 1,32,2
WL 5
LO 18,18,122
LU 122,18
M 1,33,3
RA 216,1,13312
HS 25,122
M 2,44,2
LL 17,100
LL 126,101
M 2,45,3
LA 17
PK 122,18,120,33
M 3,56,2
LC_ 126
PK 120,18,120,32
M 3,57,3
ST 116
M 4,68,2
WL 4
LO 16,16,123
LU 123,16
M 4,69,3
s_add_u32 s42, s44, s64
RA 220,1,14336
HS 25,123
M 5,80,2
LL 18,102
LL 127,103
M 5,81,3
s_addc_u32 s43, s45, 0
LA 18
PK 123,16,121,33
M 0,22,4
LC_ 127
PK 121,16,121,32
M 0,23,5
ST 120
M 1,34,4
WL 3
LO 17,17,126
LU 126,17
M 1,35,5
RA 224,1,15360
HS 25,126
M 2,46,4
LL 16,56
LL 104,57
M 2,47,5
LA 16
PK 126,17,124,33
M 3,58,4
LC_ 104
PK 124,17,124,32
M 3,59,5
ds_read_b64 v[14:15], v5 offset:0
PO 126
M 4,70,4
WL 4
LO 18,18,127
LU 127,18
M 4,71,5
s_add_u32 s42, s44, s65
RA 228,1,16384
HS 25,127
M 5,82,4
LL 17,58
LL 105,59
M 5,83,5
s_addc_u32 s43, s45, 0
LA 17
PK 127,18,125,33
Z 6,12,0
LC_ 105
PK 125,18,125,32
M 6,13,1
PO 127
v_subrev_u32 v5, s39, v5
Z 7,24,0
WL 4
LO 104,16,104
LU 16,104
M 7,25,1
RA 232,1,17408
HZ 24,16
Z 8,36,0
WL 4
LL 18,60
LL 108,61
M 8,37,1
s_mul_i32 s53, s50, 25600
LA 18
v_add_f32 v14, v14, v15
Z 9,48,0
s_add_u32 s40, %[wlo], s53
LC_ 108
v_add_f32_dpp v14, v14, v14 quad_perm:[1,0,3,2] row_mask:0xf bank_mask:0xf
M 9,49,1
s_addc_u32 s41, %[whi], 0
Z 10,60,0
WL 3
LO 105,17,105
LU 17,105
M 10,61,1
s_mov_b32 m0, s38
RA 236,1,18432
HS 24,17
Z 11,72,0
LL 16,62
LL 109,63
M 11,73,1
LA 16
ST 124
M 11,74,2
LC_ 109
DW 0
M 11,75,3
v_add_f32_dpp v14, v14, v14 quad_perm:[2,3,0,1] row_mask:0xf bank_mask:0xf
HX 25
M 10,62,2
WL 3
LO 108,18,108
LU 18,108
W1 25,1024
M 10,63,3
s_add_u32 s37, s37, 32768
RA 216,1,19456
HS 24,18
M 9,50,2
s_cmp_eq_u32 s37, s75
LL 17,64
LL 112,65
M 9,51,3
s_cselect_b32 s37, s74, s37
LA 17
DW 1024
M 8,38,2
LC_ 112
v_add_f32_dpp v14, v14, v14 row_half_mirror row_mask:0xf bank_mask:0xf
M 8,39,3
v_cvt_f64_f32 v[14:15], v14
v_add_u32 v4, s39, v4
M 7,26,2
WL 4
LO 109,16,109
LU 16,109
M 7,27,3
RA 220,1,20480
HS 24,16
M 6,14,2
LL 18,66
LL 113,67
M 6,15,3
LA 18
s_mov_b64 exec, s[54:55]
SP
s_mov_b64 exec, -1
M 6,16,4
LC_ 113
DW 2048
M 6,17,5
M 7,28,4
WL 3
LO 112,17,112
LU 17,112
M 7,29,5
s_mul_i32 s53, s51, 24
RA 224,1,21504
HS 24,17
M 8,40,4
s_bitcmp1_b32 s51, 0
LL 16,68
LL 116,69
M 8,41,5
s_addc_u32 s53, s53, 0
LA 16
M 9,52,4
s_lshl_b32 s53, s53, 11
LC_ 116
M 9,53,5
s_bitcmp1_b64 s[90:91], s51
M 10,64,4
WL 3
LO 113,18,113
LU 18,113
M 10,65,5
s_cselect_b64 s[44:45], s[84:85], s[82:83]
RA 228,1,22528
HS 24,18
M 11,76,4
LL 17,70
LL 117,71
M 11,77,5
s_add_u32 s44, s44, s53
LA 17
M 11,78,0
s_addc_u32 s45, s45, 0
LC_ 117
M 11,79,1
s_add_u32 s51, s51, %[d]
M 10,66,0
WL 3
LO 116,16,116
LU 16,116
M 10,67,1
s_cmp_eq_u32 s51, %[wrap]
RA 232,1,23552
HS 24,16
M 9,54,0
s_cselect_b32 s51, %[rst], s51
LL 18,72
LL 120,73
M 9,55,1
s_add_u32 s40, s40, 3072
LA 18
M 8,42,0
s_addc_u32 s41, s41, 0
LC_ 120
s_add_u32 m0, s38, 3072
M 8,43,1
DW 0
M 7,30,0
WL 3
LO 117,17,117
LU 17,117
M 7,31,1
RA 236,1,24576
HS 24,17
M 6,18,0
LL 16,74
LL 121,75
M 6,19,1
LA 16
DW 1024
M 6,20,2
LC_ 121
DW 2048
M 6,21,3
s_lshl_b32 s53, s52, 8
v_add_u32 v1, s37, v0
M 7,32,2
WL 3
LO 120,18,120
LU 18,120
M 7,33,3
s_add_u32 s48, %[alo], s53
RA 216,1,0
HS 24,18
M 8,44,2
s_addc_u32 s49, %[ahi], 0
LL 17,76
LL 124,77
M 8,45,3
s_add_u32 s52, s52, %[d]
LA 17
v_and_b32 v22, 0xfffffcff, v1
M 9,56,2
s_cmp_eq_u32 s52, %[wrap]
LC_ 124
M 9,57,3
s_cselect_b32 s52, %[rst], s52
DQ 3072
M 10,68,2
WL 3
LO 121,16,121
LU 16,121
M 10,69,3
s_add_u32 s50, s50, %[d]
RA 220,1,1024
HS 24,16
M 11,80,2
LL 18,78
LL 125,79
M 11,81,3
s_cmp_eq_u32 s50, %[wrap]
LA 18
RV 252,22,5120
M 6,22,4
s_cselect_b32 s50, %[rst], s50
LC_ 125
M 6,23,5
WV 30
v_add_u32 v19, 1, v19
M 7,34,4
WL 4
LO 124,17,124
LU 17,124
M 7,35,5
s_add_u32 s38, s38, 32768
RA 224,1,2048
HS 24,17
M 8,46,4
s_cmp_eq_u32 s38, s77
LL 16,80
LL 106,81
M 8,47,5
s_cselect_b32 s38, s76, s38
LA 16
FP
M 9,58,4
s_sub_u32 s39, 0, s39
LC_ 106
M 9,59,5
M 10,70,4
WL 4
LO 125,18,125
LU 18,125
M 10,71,5
RA 228,1,3072
HS 24,18
M 11,82,4
s_sub_u32 s66, s66, 1
LL 17,82
LL 107,83
M 11,83,5
s_cbranch_scc0 1b
7:
LA 17
)"
R"(HX 24
Z 0,12,0
LC_ 107
M 0,13,1
Z 1,24,0
WL 3
LO 16,16,106
LU 106,16
M 1,25,1
RA 232,1,4096
HZ 25,106
Z 2,36,0
LL 18,84
LL 110,85
M 2,37,1
LA 18
PK 106,16,104,33
Z 3,48,0
LC_ 110
PK 104,16,104,32
M 3,49,1
Z 4,60,0
WL 3
LO 17,17,107
LU 107,17
M 4,61,1
s_add_u32 s42, s44, s60
RA 236,1,6144
HS 25,107
Z 5,72,0
LL 16,86
LL 111,87
M 5,73,1
s_addc_u32 s43, s45, 0
LA 16
PK 107,17,105,33
M 5,74,2
LC_ 111
PK 105,17,105,32
M 5,75,3
ST 104
M 4,62,2
WL 3
LO 18,18,110
LU 110,18
M 4,63,3
RA 216,1,7168
HS 25,110
M 3,50,2
LL 17,88
LL 114,89
M 3,51,3
LA 17
PK 110,18,108,33
M 2,38,2
LC_ 114
PK 108,18,108,32
M 2,39,3
FR
PO 110
M 1,26,2
WL 4
LO 16,16,111
LU 111,16
M 1,27,3
s_add_u32 s42, s44, s61
RA 220,1,8192
HS 25,111
M 0,14,2
LL 18,90
LL 115,91
M 0,15,3
s_addc_u32 s43, s45, 0
LA 18
PK 111,16,109,33
M 0,16,4
s_mov_b32 m0, %[pm]
LC_ 115
PK 109,16,109,32
M 0,17,5
DF
PO 111
M 1,28,4
WL 4
LO 17,17,114
LU 114,17
M 1,29,5
RA 224,1,9216
HS 25,114
M 2,40,4
LL 16,92
LL 118,93
M 2,41,5
LA 16
PK 114,17,112,33
M 3,52,4
LC_ 118
PK 112,17,112,32
M 3,53,5
ST 108
M 4,64,4
WL 3
LO 18,18,115
LU 115,18
M 4,65,5
s_add_u32 s42, s44, s62
RA 228,1,10240
HS 25,115
M 5,76,4
LL 17,94
LL 119,95
M 5,77,5
s_addc_u32 s43, s45, 0
LA 17
PK 115,18,113,33
M 5,78,0
LC_ 119
PK 113,18,113,32
M 5,79,1
ST 112
M 4,66,0
WL 3
LO 16,16,118
LU 118,16
M 4,67,1
RA 232,1,11264
HS 25,118
M 3,54,0
LL 18,96
LL 122,97
M 3,55,1
LA 18
PK 118,16,116,33
v_cmp_gt_u32 vcc, v19, v15
M 2,42,0
LC_ 122
PK 116,16,116,32
M 2,43,1
RV 252,22,5392
PO 118
M 1,30,0
WL 4
LO 17,17,119
LU 119,17
M 1,31,1
s_cbranch_vccnz .Lk1s1
.Lk1r1:
RA 236,1,12288
HS 25,119
M 0,18,0
LL 16,98
LL 123,99
M 0,19,1
s_add_u32 s42, s44, s63
LA 16
PK 119,17,117,33
M 0,20,2
s_addc_u32 s43, s45, 0
LC_ 123
PK 117,17,117,32
M 0,21,3
W1 24,0
PO 119
M 1,32,2
WL 5
LO 18,18,122
LU 122,18
M 1,33,3
RA 216,1,13312
HS 25,122
M 2,44,2
LL 17,100
LL 126,101
M 2,45,3
LA 17
PK 122,18,120,33
M 3,56,2
LC_ 126
PK 120,18,120,32
M 3,57,3
ST 116
M 4,68,2
WL 4
LO 16,16,123
LU 123,16
M 4,69,3
s_add_u32 s42, s44, s64
RA 220,1,14336
HS 25,123
M 5,80,2
LL 18,102
LL 127,103
M 5,81,3
s_addc_u32 s43, s45, 0
LA 18
PK 123,16,121,33
M 0,22,4
LC_ 127
PK 121,16,121,32
M 0,23,5
ST 120
M 1,34,4
WL 3
LO 17,17,126
LU 126,17
M 1,35,5
RA 224,1,15360
HS 25,126
M 2,46,4
LL 16,56
LL 104,57
M 2,47,5
LA 16
PK 126,17,124,33
M 3,58,4
LC_ 104
PK 124,17,124,32
M 3,59,5
ds_read_b64 v[14:15], v5 offset:0
PO 126
M 4,70,4
WL 4
LO 18,18,127
LU 127,18
M 4,71,5
s_add_u32 s42, s44, s65
RA 228,1,16384
HS 25,127
M 5,82,4
LL 17,58
LL 105,59
M 5,83,5
s_addc_u32 s43, s45, 0
LA 17
PK 127,18,125,33
Z 6,12,0
LC_ 105
PK 125,18,125,32
M 6,13,1
PO 127
v_subrev_u32 v5, s39, v5
Z 7,24,0
WL 4
LO 104,16,104
LU 16,104
M 7,25,1
RA 232,1,17408
HZ 24,16
Z 8,36,0
WL 4
LL 18,60
LL 108,61
M 8,37,1
s_mul_i32 s53, s50, 25600
LA 18
v_add_f32 v14, v14, v15
Z 9,48,0
s_add_u32 s40, %[wlo], s53
LC_ 108
v_add_f32_dpp v14, v14, v14 quad_perm:[1,0,3,2] row_mask:0xf bank_mask:0xf
M 9,49,1
s_addc_u32 s41, %[whi], 0
Z 10,60,0
WL 3
LO 105,17,105
LU 17,105
M 10,61,1
s_mov_b32 m0, s38
RA 236,1,18432
HS 24,17
Z 11,72,0
LL 16,62
LL 109,63
M 11,73,1
LA 16
ST 124
M 11,74,2
LC_ 109
DW 0
M 11,75,3
v_add_f32_dpp v14, v14, v14 quad_perm:[2,3,0,1] row_mask:0xf bank_mask:0xf
HX 25
M 10,62,2
WL 3
LO 108,18,108
LU 18,108
W1 25,1024
M 10,63,3
s_add_u32 s37, s37, 32768
RA 216,1,19456
HS 24,18
M 9,50,2
s_cmp_eq_u32 s37, s75
LL 17,64
LL 112,65
M 9,51,3
s_cselect_b32 s37, s74, s37
LA 17
DW 1024
M 8,38,2
LC_ 112
v_add_f32_dpp v14, v14, v14 row_half_mirror row_mask:0xf bank_mask:0xf
M 8,39,3
v_cvt_f64_f32 v[14:15], v14
v_add_u32 v4, s39, v4
s_mov_b64 exec, 1
AT
s_mov_b64 exec, -1
M 7,26,2
WL 4
LO 109,16,109
LU 16,109
M 7,27,3
RA 220,1,20480
HS 24,16
M 6,14,2
LL 18,66
LL 113,67
M 6,15,3
LA 18
s_mov_b64 exec, s[54:55]
SP
s_mov_b64 exec, -1
M 6,16,4
LC_ 113
DW 2048
M 6,17,5
M 7,28,4
WL 3
LO 112,17,112
LU 17,112
M 7,29,5
s_mul_i32 s53, s51, 24
RA 224,1,21504
HS 24,17
M 8,40,4
s_bitcmp1_b32 s51, 0
LL 16,68
LL 116,69
M 8,41,5
s_addc_u32 s53, s53, 0
LA 16
M 9,52,4
s_lshl_b32 s53, s53, 11
LC_ 116
M 9,53,5
s_bitcmp1_b64 s[90:91], s51
M 10,64,4
WL 3
LO 113,18,113
LU 18,113
M 10,65,5
s_cselect_b64 s[44:45], s[84:85], s[82:83]
RA 228,1,22528
HS 24,18
M 11,76,4
LL 17,70
LL 117,71
M 11,77,5
s_add_u32 s44, s44, s53
LA 17
M 11,78,0
s_addc_u32 s45, s45, 0
LC_ 117
M 11,79,1
s_add_u32 s51, s51, %[d]
M 10,66,0
WL 3
LO 116,16,116
LU 16,116
M 10,67,1
s_cmp_eq_u32 s51, %[wrap]
RA 232,1,23552
HS 24,16
M 9,54,0
s_cselect_b32 s51, %[rst], s51
LL 18,72
LL 120,73
M 9,55,1
s_add_u32 s40, s40, 3072
LA 18
M 8,42,0
s_addc_u32 s41, s41, 0
LC_ 120
s_add_u32 m0, s38, 3072
M 8,43,1
DW 0
M 7,30,0
WL 3
LO 117,17,117
LU 17,117
M 7,31,1
RA 236,1,24576
HS 24,17
M 6,18,0
LL 16,74
LL 121,75
M 6,19,1
LA 16
DW 1024
)"
R"(M 6,20,2
LC_ 121
DW 2048
M 6,21,3
s_lshl_b32 s53, s52, 8
v_add_u32 v1, s37, v0
M 7,32,2
WL 3
LO 120,18,120
LU 18,120
M 7,33,3
s_add_u32 s48, %[alo], s53
RA 216,1,0
HS 24,18
M 8,44,2
s_addc_u32 s49, %[ahi], 0
LL 17,76
LL 124,77
M 8,45,3
s_add_u32 s52, s52, %[d]
LA 17
v_and_b32 v22, 0xfffffcff, v1
M 9,56,2
s_cmp_eq_u32 s52, %[wrap]
LC_ 124
M 9,57,3
s_cselect_b32 s52, %[rst], s52
DQ 3072
M 10,68,2
WL 3
LO 121,16,121
LU 16,121
M 10,69,3
s_add_u32 s50, s50, %[d]
RA 220,1,1024
HS 24,16
M 11,80,2
LL 18,78
LL 125,79
M 11,81,3
s_cmp_eq_u32 s50, %[wrap]
LA 18
RV 252,22,5120
M 6,22,4
s_cselect_b32 s50, %[rst], s50
LC_ 125
M 6,23,5
WV 31
v_add_u32 v19, 1, v19
M 7,34,4
WL 4
LO 124,17,124
LU 17,124
M 7,35,5
s_add_u32 s38, s38, 32768
RA 224,1,2048
HS 24,17
M 8,46,4
s_cmp_eq_u32 s38, s77
LL 16,80
LL 106,81
M 8,47,5
s_cselect_b32 s38, s76, s38
LA 16
FP
M 9,58,4
s_sub_u32 s39, 0, s39
LC_ 106
M 9,59,5
M 10,70,4
WL 4
LO 125,18,125
LU 18,125
M 10,71,5
RA 228,1,3072
HS 24,18
M 11,82,4
LL 17,82
LL 107,83
M 11,83,5
LA 17
HX 24
Z 0,12,0
LC_ 107
M 0,13,1
Z 1,24,0
WL 3
LO 16,16,106
LU 106,16
M 1,25,1
RA 232,1,4096
HZ 25,106
Z 2,36,0
LL 18,84
LL 110,85
M 2,37,1
LA 18
PK 106,16,104,33
Z 3,48,0
LC_ 110
PK 104,16,104,32
M 3,49,1
Z 4,60,0
WL 3
LO 17,17,107
LU 107,17
M 4,61,1
s_add_u32 s42, s44, s60
RA 236,1,6144
HS 25,107
Z 5,72,0
LL 16,86
LL 111,87
M 5,73,1
s_addc_u32 s43, s45, 0
LA 16
PK 107,17,105,33
M 5,74,2
LC_ 111
PK 105,17,105,32
M 5,75,3
ST 104
M 4,62,2
WL 3
LO 18,18,110
LU 110,18
M 4,63,3
RA 216,1,7168
HS 25,110
M 3,50,2
LL 17,88
LL 114,89
M 3,51,3
LA 17
PK 110,18,108,33
M 2,38,2
LC_ 114
PK 108,18,108,32
M 2,39,3
FR
PO 110
M 1,26,2
WL 4
LO 16,16,111
LU 111,16
M 1,27,3
s_add_u32 s42, s44, s61
RA 220,1,8192
HS 25,111
M 0,14,2
LL 18,90
LL 115,91
M 0,15,3
s_addc_u32 s43, s45, 0
LA 18
PK 111,16,109,33
M 0,16,4
s_mov_b32 m0, %[pm]
LC_ 115
PK 109,16,109,32
M 0,17,5
DF
PO 111
M 1,28,4
WL 4
LO 17,17,114
LU 114,17
M 1,29,5
RA 224,1,9216
HS 25,114
M 2,40,4
LL 16,92
LL 118,93
M 2,41,5
LA 16
PK 114,17,112,33
M 3,52,4
LC_ 118
PK 112,17,112,32
M 3,53,5
ST 108
M 4,64,4
WL 3
LO 18,18,115
LU 115,18
M 4,65,5
s_add_u32 s42, s44, s62
RA 228,1,10240
HS 25,115
M 5,76,4
LL 17,94
LL 119,95
M 5,77,5
s_addc_u32 s43, s45, 0
LA 17
PK 115,18,113,33
M 5,78,0
LC_ 119
PK 113,18,113,32
M 5,79,1
ST 112
M 4,66,0
WL 3
LO 16,16,118
LU 118,16
M 4,67,1
RA 232,1,11264
HS 25,118
M 3,54,0
LL 18,96
LL 122,97
M 3,55,1
LA 18
PK 118,16,116,33
v_cmp_gt_u32 vcc, v19, v15
M 2,42,0
LC_ 122
PK 116,16,116,32
M 2,43,1
RV 252,22,5392
PO 118
M 1,30,0
WL 4
LO 17,17,119
LU 119,17
M 1,31,1
s_cbranch_vccnz .Lk1s2
.Lk1r2:
RA 236,1,12288
HS 25,119
M 0,18,0
LL 16,98
LL 123,99
M 0,19,1
s_add_u32 s42, s44, s63
LA 16
PK 119,17,117,33
M 0,20,2
s_addc_u32 s43, s45, 0
LC_ 123
PK 117,17,117,32
M 0,21,3
W1 24,0
PO 119
M 1,32,2
WL 5
LO 18,18,122
LU 122,18
M 1,33,3
RA 216,1,13312
HS 25,122
M 2,44,2
LL 17,100
LL 126,101
M 2,45,3
LA 17
PK 122,18,120,33
M 3,56,2
LC_ 126
PK 120,18,120,32
M 3,57,3
ST 116
M 4,68,2
WL 4
LO 16,16,123
LU 123,16
M 4,69,3
s_add_u32 s42, s44, s64
RA 220,1,14336
HS 25,123
M 5,80,2
LL 18,102
LL 127,103
M 5,81,3
s_addc_u32 s43, s45, 0
LA 18
PK 123,16,121,33
M 0,22,4
LC_ 127
PK 121,16,121,32
M 0,23,5
ST 120
M 1,34,4
WL 3
LO 17,17,126
LU 126,17
M 1,35,5
RA 224,1,15360
HS 25,126
M 2,46,4
LL 16,56
LL 104,57
M 2,47,5
LA 16
PK 126,17,124,33
M 3,58,4
LC_ 104
PK 124,17,124,32
M 3,59,5
ds_read_b64 v[14:15], v5 offset:0
PO 126
M 4,70,4
WL 4
LO 18,18,127
LU 127,18
M 4,71,5
s_add_u32 s42, s44, s65
RA 228,1,16384
HS 25,127
M 5,82,4
LL 17,58
LL 105,59
M 5,83,5
s_addc_u32 s43, s45, 0
LA 17
PK 127,18,125,33
Z 6,12,0
LC_ 105
PK 125,18,125,32
M 6,13,1
PO 127
v_subrev_u32 v5, s39, v5
Z 7,24,0
WL 4
LO 104,16,104
LU 16,104
M 7,25,1
RA 232,1,17408
HZ 24,16
Z 8,36,0
WL 4
LL 18,60
LL 108,61
M 8,37,1
s_mul_i32 s53, s50, 25600
LA 18
v_add_f32 v14, v14, v15
Z 9,48,0
s_add_u32 s40, %[wlo], s53
LC_ 108
v_add_f32_dpp v14, v14, v14 quad_perm:[1,0,3,2] row_mask:0xf bank_mask:0xf
M 9,49,1
s_addc_u32 s41, %[whi], 0
Z 10,60,0
WL 3
LO 105,17,105
LU 17,105
M 10,61,1
s_mov_b32 m0, s38
RA 236,1,18432
HS 24,17
Z 11,72,0
LL 16,62
LL 109,63
M 11,73,1
LA 16
ST 124
M 11,74,2
LC_ 109
DW 0
M 11,75,3
WV 14
v_add_f32_dpp v14, v14, v14 quad_perm:[2,3,0,1] row_mask:0xf bank_mask:0xf
HX 25
M 10,62,2
WL 3
LO 108,18,108
LU 18,108
W1 25,1024
M 10,63,3
s_add_u32 s37, s37, 32768
RA 216,1,19456
HS 24,18
v_readfirstlane_b32 s72, v11
M 9,50,2
s_cmp_eq_u32 s37, s75
LL 17,64
LL 112,65
v_mov_b32 v12, s72
M 9,51,3
s_cselect_b32 s37, s74, s37
LA 17
W2 12,12,16
M 8,38,2
LC_ 112
DW 1024
M 8,39,3
v_add_f32_dpp v14, v14, v14 row_half_mirror row_mask:0xf bank_mask:0xf
v_add_u32 v4, s39, v4
M 7,26,2
WL 5
LO 109,16,109
LU 16,109
M 7,27,3
RA 220,1,20480
HS 24,16
M 6,14,2
LL 18,66
LL 113,67
M 6,15,3
)"
R"(LA 18
DW 2048
M 6,16,4
LC_ 113
v_cvt_f64_f32 v[14:15], v14
M 6,17,5
s_mov_b64 exec, s[54:55]
SP
s_mov_b64 exec, -1
M 7,28,4
WL 3
LO 112,17,112
LU 17,112
M 7,29,5
s_mul_i32 s53, s51, 24
RA 224,1,21504
HS 24,17
M 8,40,4
s_bitcmp1_b32 s51, 0
LL 16,68
LL 116,69
M 8,41,5
s_addc_u32 s53, s53, 0
LA 16
M 9,52,4
s_lshl_b32 s53, s53, 11
LC_ 116
M 9,53,5
s_bitcmp1_b64 s[90:91], s51
M 10,64,4
WL 3
LO 113,18,113
LU 18,113
M 10,65,5
s_cselect_b64 s[44:45], s[84:85], s[82:83]
RA 228,1,22528
HS 24,18
M 11,76,4
LL 17,70
LL 117,71
M 11,77,5
s_add_u32 s44, s44, s53
LA 17
M 11,78,0
s_addc_u32 s45, s45, 0
LC_ 117
M 11,79,1
s_add_u32 s51, s51, %[d]
M 10,66,0
WL 3
LO 116,16,116
LU 16,116
M 10,67,1
s_cmp_eq_u32 s51, %[wrap]
RA 232,1,23552
HS 24,16
M 9,54,0
s_cselect_b32 s51, %[rst], s51
LL 18,72
LL 120,73
M 9,55,1
s_add_u32 s40, s40, 3072
LA 18
M 8,42,0
s_addc_u32 s41, s41, 0
LC_ 120
s_add_u32 m0, s38, 3072
M 8,43,1
DW 0
M 7,30,0
WL 3
LO 117,17,117
LU 17,117
M 7,31,1
RA 236,1,24576
HS 24,17
M 6,18,0
LL 16,74
LL 121,75
M 6,19,1
LA 16
DW 1024
M 6,20,2
LC_ 121
DW 2048
M 6,21,3
s_lshl_b32 s53, s52, 8
v_add_u32 v1, s37, v0
M 7,32,2
WL 3
LO 120,18,120
LU 18,120
M 7,33,3
s_add_u32 s48, %[alo], s53
RA 216,1,0
HS 24,18
M 8,44,2
s_addc_u32 s49, %[ahi], 0
LL 17,76
LL 124,77
M 8,45,3
s_add_u32 s52, s52, %[d]
LA 17
v_and_b32 v22, 0xfffffcff, v1
M 9,56,2
s_cmp_eq_u32 s52, %[wrap]
LC_ 124
M 9,57,3
s_cselect_b32 s52, %[rst], s52
DQ 3072
M 10,68,2
WL 3
LO 121,16,121
LU 16,121
M 10,69,3
s_add_u32 s50, s50, %[d]
RA 220,1,1024
HS 24,16
M 11,80,2
LL 18,78
LL 125,79
M 11,81,3
s_cmp_eq_u32 s50, %[wrap]
LA 18
RV 252,22,5120
M 6,22,4
s_cselect_b32 s50, %[rst], s50
LC_ 125
M 6,23,5
v_add_u32 v19, 1, v19
M 7,34,4
WL 4
LO 124,17,124
LU 17,124
M 7,35,5
s_add_u32 s38, s38, 32768
RA 224,1,2048
HS 24,17
M 8,46,4
s_cmp_eq_u32 s38, s77
LL 16,80
LL 106,81
M 8,47,5
s_cselect_b32 s38, s76, s38
LA 16
FP
M 9,58,4
s_sub_u32 s39, 0, s39
LC_ 106
M 9,59,5
M 10,70,4
WL 4
LO 125,18,125
LU 18,125
M 10,71,5
RA 228,1,3072
HS 24,18
M 11,82,4
LL 17,82
LL 107,83
M 11,83,5
LA 17
HX 24
Z 0,12,0
LC_ 107
M 0,13,1
Z 1,24,0
WL 3
LO 16,16,106
LU 106,16
M 1,25,1
RA 232,1,4096
HZ 25,106
Z 2,36,0
LL 18,84
LL 110,85
M 2,37,1
LA 18
PK 106,16,104,33
Z 3,48,0
LC_ 110
PK 104,16,104,32
M 3,49,1
Z 4,60,0
WL 3
LO 17,17,107
LU 107,17
M 4,61,1
s_add_u32 s42, s44, s60
RA 236,1,6144
HS 25,107
Z 5,72,0
LL 16,86
LL 111,87
M 5,73,1
s_addc_u32 s43, s45, 0
LA 16
PK 107,17,105,33
M 5,74,2
LC_ 111
PK 105,17,105,32
M 5,75,3
ST 104
M 4,62,2
WL 3
LO 18,18,110
LU 110,18
M 4,63,3
RA 216,1,7168
HS 25,110
M 3,50,2
LL 17,88
LL 114,89
M 3,51,3
LA 17
PK 110,18,108,33
M 2,38,2
LC_ 114
PK 108,18,108,32
M 2,39,3
FR
PO 110
M 1,26,2
WL 4
LO 16,16,111
LU 111,16
M 1,27,3
s_add_u32 s42, s44, s61
RA 220,1,8192
HS 25,111
M 0,14,2
LL 18,90
LL 115,91
M 0,15,3
s_addc_u32 s43, s45, 0
LA 18
PK 111,16,109,33
M 0,16,4
s_mov_b32 m0, %[pm]
LC_ 115
PK 109,16,109,32
M 0,17,5
DF
PO 111
M 1,28,4
WL 4
LO 17,17,114
LU 114,17
M 1,29,5
RA 224,1,9216
HS 25,114
M 2,40,4
LL 16,92
LL 118,93
M 2,41,5
LA 16
PK 114,17,112,33
M 3,52,4
LC_ 118
PK 112,17,112,32
M 3,53,5
ST 108
M 4,64,4
WL 3
LO 18,18,115
LU 115,18
M 4,65,5
s_add_u32 s42, s44, s62
RA 228,1,10240
HS 25,115
M 5,76,4
LL 17,94
LL 119,95
M 5,77,5
s_addc_u32 s43, s45, 0
LA 17
PK 115,18,113,33
M 5,78,0
LC_ 119
PK 113,18,113,32
M 5,79,1
ST 112
M 4,66,0
WL 3
LO 16,16,118
LU 118,16
M 4,67,1
RA 232,1,11264
HS 25,118
M 3,54,0
LL 18,96
LL 122,97
M 3,55,1
LA 18
PK 118,16,116,33
v_cmp_gt_u32 vcc, v19, v15
M 2,42,0
LC_ 122
PK 116,16,116,32
M 2,43,1
RV 252,22,5392
PO 118
M 1,30,0
WL 4
LO 17,17,119
LU 119,17
M 1,31,1
s_cbranch_vccnz .Lk1s3
.Lk1r3:
RA 236,1,12288
HS 25,119
M 0,18,0
LL 16,98
LL 123,99
M 0,19,1
s_add_u32 s42, s44, s63
LA 16
PK 119,17,117,33
M 0,20,2
s_addc_u32 s43, s45, 0
LC_ 123
PK 117,17,117,32
M 0,21,3
ds_read_b32 v20, v5 offset:4096
PO 119
M 1,32,2
WL 5
LO 18,18,122
LU 122,18
M 1,33,3
RA 216,1,13312
HS 25,122
M 2,44,2
LL 17,100
LL 126,101
M 2,45,3
LA 17
PK 122,18,120,33
M 3,56,2
LC_ 126
PK 120,18,120,32
M 3,57,3
M 4,68,2
WL 4
LO 16,16,123
LU 123,16
M 4,69,3
RA 220,1,14336
HS 25,123
M 5,80,2
LL 18,102
LL 127,103
M 5,81,3
LA 18
PK 123,16,121,33
M 0,22,4
LC_ 127
PK 121,16,121,32
W1 24,0
M 0,23,5
M 1,34,4
WL 4
LO 17,17,126
LU 126,17
M 1,35,5
RA 224,1,15360
HS 25,126
M 2,46,4
LL 16,56
LL 104,57
M 2,47,5
LA 16
PK 126,17,124,33
M 3,58,4
LC_ 104
PK 124,17,124,32
M 3,59,5
PO 126
v_readfirstlane_b32 s72, v20
M 4,70,4
WL 4
LO 18,18,127
LU 127,18
M 4,71,5
s_sub_u32 s53, 63, s72
RA 228,1,16384
HS 25,127
M 5,82,4
LL 17,58
LL 105,59
M 5,83,5
s_cmp_eq_u32 %[d], 1
LA 17
PK 127,18,125,33
Z 6,12,0
s_cselect_b32 s72, s72, s53
LC_ 105
PK 125,18,125,32
M 6,13,1
s_and_b32 s72, s72, 63
)"
R"(ds_read_b64 v[14:15], v5 offset:0
PO 127
Z 7,24,0
WL 4
LO 104,16,104
LU 16,104
M 7,25,1
s_add_u32 s53, s72, 128
RA 232,1,17408
HZ 24,16
Z 8,36,0
s_mul_i32 s53, s53, 2359296
LL 18,60
LL 108,61
M 8,37,1
s_add_u32 s86, %[hlo], s53
LA 18
v_subrev_u32 v5, s39, v5
Z 9,48,0
s_addc_u32 s87, %[hhi], 0
LC_ 108
HX 25
M 9,49,1
s_add_u32 s88, s86, 8192
Z 10,60,0
WL 4
LO 105,17,105
LU 17,105
M 10,61,1
s_addc_u32 s89, s87, 0
RA 236,1,18432
HS 24,17
Z 11,72,0
WL 4
LL 16,62
LL 109,63
M 11,73,1
s_and_b32 s92, %[hlo], 0x1fffff
LA 16
W1 25,1024
s_add_u32 s92, s92, s53
s_mov_b32 s93, 3072
ST 116
s_add_u32 s42, s44, s64
s_addc_u32 s43, s45, 0
s_mul_i32 s57, s72, 589824
s_add_u32 s57, s57, %[xs1]
s_lshl_b32 s72, s72, 1
s_add_u32 s72, s72, s96
s_lshl_b32 s53, s72, 6
s_add_u32 s53, s53, 128
s_cmp_lt_u32 %[ca0], 128
s_cselect_b32 s73, s53, %[ca0]
ST 120
v_mov_b32 v9, s73
v_mov_b32 v11, s92
M 11,74,2
s_add_u32 s42, s44, s65
LC_ 109
v_add_f32 v14, v14, v15
s_addc_u32 s43, s45, 0
v_mad_u32_u24 v11, v0, s93, v11
M 11,75,3
v_add_f32_dpp v14, v14, v14 quad_perm:[1,0,3,2] row_mask:0xf bank_mask:0xf
v_add_u32 v4, s39, v4
M 10,62,2
WL 4
LO 108,18,108
LU 18,108
M 10,63,3
RA 216,1,19456
HS 24,18
v_lshrrev_b32 v11, 21, v11
M 9,50,2
LL 17,64
LL 112,65
M 9,51,3
LA 17
v_add_f32_dpp v14, v14, v14 quad_perm:[2,3,0,1] row_mask:0xf bank_mask:0xf
M 8,38,2
LC_ 112
v_add_f32_dpp v14, v14, v14 row_half_mirror row_mask:0xf bank_mask:0xf
v_lshl_add_u32 v11, v11, 2, 64
M 8,39,3
s_mul_i32 s53, s50, 25600
v_cvt_f64_f32 v[14:15], v14
M 7,26,2
WL 3
LO 109,16,109
LU 16,109
s_add_u32 s40, %[wlo], s53
s_addc_u32 s41, %[whi], 0
s_mov_b32 m0, s38
M 7,27,3
RA 220,1,20480
HS 24,16
v_writelane_b32 v11, %[tsel], 63
M 6,14,2
LL 18,66
LL 113,67
ST 124
M 6,15,3
s_sub_u32 s53, s86, %[hlo]
LA 18
s_mov_b64 exec, s[54:55]
SP
s_mov_b64 exec, -1
M 6,16,4
s_lshr_b32 s53, s53, 2
LC_ 113
s_and_b32 s92, %[xlo], 0x1fffff
s_add_u32 s53, s53, s92
s_add_u32 s53, s53, 0x20048000
s_lshr_b32 s53, s53, 21
s_lshl_b32 s92, s53, 2
M 6,17,5
DW 0
v_writelane_b32 v11, s92, 48
M 7,28,4
WL 3
LO 112,17,112
LU 17,112
M 7,29,5
s_add_u32 s37, s37, 32768
RA 224,1,21504
HS 24,17
M 8,40,4
s_cmp_eq_u32 s37, s75
LL 16,68
LL 116,69
M 8,41,5
s_cselect_b32 s37, s74, s37
LA 16
global_load_dword v11, v11, s[68:69] offset:-2048
M 9,52,4
LC_ 116
DW 1024
M 9,53,5
DW 2048
M 10,64,4
WL 3
LO 113,18,113
LU 18,113
M 10,65,5
s_mul_i32 s53, s51, 24
RA 228,1,22528
HS 24,18
M 11,76,4
LL 17,70
LL 117,71
M 11,77,5
s_bitcmp1_b32 s51, 0
LA 17
M 11,78,0
s_addc_u32 s53, s53, 0
LC_ 117
M 11,79,1
s_lshl_b32 s53, s53, 11
M 10,66,0
WL 3
LO 116,16,116
LU 16,116
M 10,67,1
s_bitcmp1_b64 s[90:91], s51
RA 232,1,23552
HS 24,16
M 9,54,0
s_cselect_b64 s[44:45], s[84:85], s[82:83]
LL 18,72
LL 120,73
M 9,55,1
s_add_u32 s44, s44, s53
LA 18
s_addc_u32 s45, s45, 0
s_add_u32 s51, s51, %[d]
s_cmp_eq_u32 s51, %[wrap]
s_cselect_b32 s51, %[rst], s51
M 8,42,0
s_add_u32 s40, s40, 3072
LC_ 120
s_addc_u32 s41, s41, 0
s_add_u32 m0, s38, 3072
M 8,43,1
DW 0
M 7,30,0
WL 3
LO 117,17,117
LU 17,117
M 7,31,1
RA 236,1,24576
HS 24,17
M 6,18,0
LL 16,74
LL 121,75
M 6,19,1
LA 16
DW 1024
M 6,20,2
LC_ 121
DW 2048
M 6,21,3
s_lshl_b32 s53, s52, 8
v_add_u32 v1, s37, v0
M 7,32,2
WL 3
LO 120,18,120
LU 18,120
M 7,33,3
s_add_u32 s48, %[alo], s53
RA 216,1,0
HS 24,18
M 8,44,2
s_addc_u32 s49, %[ahi], 0
LL 17,76
LL 124,77
M 8,45,3
s_add_u32 s52, s52, %[d]
LA 17
v_and_b32 v22, 0xfffffcff, v1
M 9,56,2
s_cmp_eq_u32 s52, %[wrap]
LC_ 124
M 9,57,3
s_cselect_b32 s52, %[rst], s52
DQ 3072
M 10,68,2
WL 3
LO 121,16,121
LU 16,121
M 10,69,3
s_add_u32 s50, s50, %[d]
RA 220,1,1024
HS 24,16
M 11,80,2
LL 18,78
LL 125,79
M 11,81,3
s_cmp_eq_u32 s50, %[wrap]
LA 18
RV 252,22,5120
M 6,22,4
s_cselect_b32 s50, %[rst], s50
LC_ 125
M 6,23,5
WV 31
v_add_u32 v19, 1, v19
M 7,34,4
WL 4
LO 124,17,124
LU 17,124
M 7,35,5
s_add_u32 s38, s38, 32768
RA 224,1,2048
HS 24,17
M 8,46,4
s_cmp_eq_u32 s38, s77
LL 16,80
LL 106,81
M 8,47,5
s_cselect_b32 s38, s76, s38
LA 16
FP
M 9,58,4
s_sub_u32 s39, 0, s39
LC_ 106
M 9,59,5
M 10,70,4
WL 4
LO 125,18,125
LU 18,125
M 10,71,5
RA 228,1,3072
HS 24,18
M 11,82,4
LL 17,82
LL 107,83
M 11,83,5
LA 17
HX 24
Z 0,12,0
LC_ 107
M 0,13,1
Z 1,24,0
WL 3
LO 16,16,106
LU 106,16
M 1,25,1
RA 232,1,4096
HZ 25,106
Z 2,36,0
LL 18,84
LL 110,85
M 2,37,1
LA 18
PK 106,16,104,33
Z 3,48,0
LC_ 110
PK 104,16,104,32
M 3,49,1
Z 4,60,0
WL 3
LO 17,17,107
LU 107,17
M 4,61,1
s_add_u32 s42, s44, s60
RA 236,1,6144
HS 25,107
Z 5,72,0
LL 16,86
LL 111,87
M 5,73,1
s_addc_u32 s43, s45, 0
LA 16
PK 107,17,105,33
M 5,74,2
LC_ 111
PK 105,17,105,32
M 5,75,3
ST 104
M 4,62,2
WL 3
LO 18,18,110
LU 110,18
M 4,63,3
RA 216,1,7168
HS 25,110
M 3,50,2
LL 17,88
LL 114,89
M 3,51,3
LA 17
PK 110,18,108,33
M 2,38,2
LC_ 114
PK 108,18,108,32
M 2,39,3
FR
PO 110
M 1,26,2
WL 4
LO 16,16,111
LU 111,16
M 1,27,3
s_add_u32 s42, s44, s61
RA 220,1,8192
HS 25,111
M 0,14,2
LL 18,90
LL 115,91
M 0,15,3
s_addc_u32 s43, s45, 0
LA 18
PK 111,16,109,33
M 0,16,4
s_mov_b32 m0, %[pm]
LC_ 115
PK 109,16,109,32
M 0,17,5
DF
PO 111
M 1,28,4
WL 4
LO 17,17,114
LU 114,17
M 1,29,5
RA 224,1,9216
HS 25,114
M 2,40,4
LL 16,92
LL 118,93
M 2,41,5
LA 16
PK 114,17,112,33
M 3,52,4
LC_ 118
PK 112,17,112,32
M 3,53,5
ST 108
M 4,64,4
WL 3
LO 18,18,115
LU 115,18
M 4,65,5
s_add_u32 s42, s44, s62
RA 228,1,10240
HS 25,115
M 5,76,4
LL 17,94
LL 119,95
M 5,77,5
s_addc_u32 s43, s45, 0
LA 17
PK 115,18,113,33
M 5,78,0
LC_ 119
PK 113,18,113,32
M 5,79,1
ST 112
M 4,66,0
WL 3
)"
R"(LO 16,16,118
LU 118,16
M 4,67,1
RA 232,1,11264
HS 25,118
M 3,54,0
LL 18,96
LL 122,97
M 3,55,1
LA 18
PK 118,16,116,33
v_cmp_gt_u32 vcc, v19, v15
M 2,42,0
LC_ 122
PK 116,16,116,32
M 2,43,1
RV 252,22,5392
PO 118
M 1,30,0
WL 4
LO 17,17,119
LU 119,17
M 1,31,1
s_cbranch_vccnz .Lk1s4
.Lk1r4:
RA 236,1,12288
HS 25,119
M 0,18,0
LL 16,98
LL 123,99
M 0,19,1
s_add_u32 s42, s44, s63
LA 16
PK 119,17,117,33
M 0,20,2
s_addc_u32 s43, s45, 0
LC_ 123
PK 117,17,117,32
M 0,21,3
W1 24,0
PO 119
M 1,32,2
WL 5
LO 18,18,122
LU 122,18
M 1,33,3
RA 216,1,13312
HS 25,122
M 2,44,2
LL 17,100
LL 126,101
M 2,45,3
LA 17
PK 122,18,120,33
M 3,56,2
LC_ 126
PK 120,18,120,32
M 3,57,3
ST 116
M 4,68,2
WL 4
LO 16,16,123
LU 123,16
M 4,69,3
s_add_u32 s42, s44, s64
RA 220,1,14336
HS 25,123
M 5,80,2
LL 18,102
LL 127,103
M 5,81,3
s_addc_u32 s43, s45, 0
LA 18
PK 123,16,121,33
M 0,22,4
LC_ 127
PK 121,16,121,32
M 0,23,5
ST 120
M 1,34,4
WL 3
LO 17,17,126
LU 126,17
M 1,35,5
RA 224,1,15360
HS 25,126
M 2,46,4
LL 16,56
LL 104,57
M 2,47,5
LA 16
PK 126,17,124,33
M 3,58,4
LC_ 104
PK 124,17,124,32
M 3,59,5
ds_read_b64 v[14:15], v5 offset:0
PO 126
M 4,70,4
WL 4
LO 18,18,127
LU 127,18
M 4,71,5
s_add_u32 s42, s44, s65
RA 228,1,16384
HS 25,127
M 5,82,4
LL 17,58
LL 105,59
M 5,83,5
s_addc_u32 s43, s45, 0
LA 17
PK 127,18,125,33
Z 6,12,0
LC_ 105
PK 125,18,125,32
M 6,13,1
PO 127
v_subrev_u32 v5, s39, v5
Z 7,24,0
WL 4
LO 104,16,104
LU 16,104
M 7,25,1
RA 232,1,17408
HZ 24,16
Z 8,36,0
WL 4
LL 18,60
LL 108,61
M 8,37,1
s_mul_i32 s53, s50, 25600
LA 18
v_add_f32 v14, v14, v15
Z 9,48,0
s_add_u32 s40, %[wlo], s53
LC_ 108
v_add_f32_dpp v14, v14, v14 quad_perm:[1,0,3,2] row_mask:0xf bank_mask:0xf
M 9,49,1
s_addc_u32 s41, %[whi], 0
Z 10,60,0
WL 3
LO 105,17,105
LU 17,105
M 10,61,1
s_mov_b32 m0, s38
RA 236,1,18432
HS 24,17
Z 11,72,0
LL 16,62
LL 109,63
M 11,73,1
LA 16
ST 124
M 11,74,2
LC_ 109
DW 0
M 11,75,3
v_add_f32_dpp v14, v14, v14 quad_perm:[2,3,0,1] row_mask:0xf bank_mask:0xf
HX 25
M 10,62,2
WL 3
LO 108,18,108
LU 18,108
W1 25,1024
M 10,63,3
s_add_u32 s37, s37, 32768
RA 216,1,19456
HS 24,18
M 9,50,2
s_cmp_eq_u32 s37, s75
LL 17,64
LL 112,65
M 9,51,3
s_cselect_b32 s37, s74, s37
LA 17
DW 1024
M 8,38,2
LC_ 112
v_add_f32_dpp v14, v14, v14 row_half_mirror row_mask:0xf bank_mask:0xf
M 8,39,3
v_cvt_f64_f32 v[14:15], v14
v_add_u32 v4, s39, v4
M 7,26,2
WL 4
LO 109,16,109
LU 16,109
M 7,27,3
RA 220,1,20480
HS 24,16
M 6,14,2
LL 18,66
LL 113,67
M 6,15,3
LA 18
s_mov_b64 exec, s[54:55]
SP
s_mov_b64 exec, -1
M 6,16,4
LC_ 113
DW 2048
M 6,17,5
s_and_b32 s53, %[w], 1
M 7,28,4
WL 3
LO 112,17,112
LU 17,112
M 7,29,5
s_lshl_b32 s53, s53, 10
RA 224,1,21504
HS 24,17
M 8,40,4
s_lshr_b32 s72, %[w], 1
LL 16,68
LL 116,69
M 8,41,5
s_mul_i32 s72, s72, 32768
LA 16
M 9,52,4
s_add_u32 s72, s72, s53
LC_ 116
M 9,53,5
s_add_u32 s72, s72, 128000
M 10,64,4
WL 3
LO 113,18,113
LU 18,113
M 10,65,5
s_movk_i32 s53, 0x78
RA 228,1,22528
HS 24,18
M 11,76,4
LL 17,70
LL 117,71
M 11,77,5
LA 17
v_lshrrev_b32 v12, 5, v0
M 11,78,0
LC_ 117
v_and_b32 v12, 6, v12
M 11,79,1
v_bfe_u32 v12, s53, v12, 2
M 10,66,0
WL 3
LO 116,16,116
LU 16,116
M 10,67,1
RA 232,1,23552
HS 24,16
v_lshrrev_b32 v20, 8, v0
v_xor_b32 v20, v12, v20
v_lshl_add_u32 v20, v20, 4, s72
v_and_b32 v12, 0xf0, v0
v_lshl_add_u32 v20, v12, 2, v20
v_add_u32 v12, s72, v0
M 9,54,0
s_mul_i32 s53, s51, 24
LL 18,72
LL 120,73
M 9,55,1
s_bitcmp1_b32 s51, 0
LA 18
s_addc_u32 s53, s53, 0
s_lshl_b32 s53, s53, 11
s_bitcmp1_b64 s[90:91], s51
s_cselect_b64 s[44:45], s[84:85], s[82:83]
s_add_u32 s44, s44, s53
s_addc_u32 s45, s45, 0
s_add_u32 s51, s51, %[d]
s_cmp_eq_u32 s51, %[wrap]
s_cselect_b32 s51, %[rst], s51
M 8,42,0
s_add_u32 s40, s40, 3072
LC_ 120
s_addc_u32 s41, s41, 0
s_add_u32 m0, s38, 3072
M 8,43,1
DW 0
M 7,30,0
WL 3
LO 117,17,117
LU 17,117
M 7,31,1
RA 236,1,24576
HS 24,17
M 6,18,0
LL 16,74
LL 121,75
M 6,19,1
LA 16
DW 1024
M 6,20,2
LC_ 121
DW 2048
M 6,21,3
s_lshl_b32 s53, s52, 8
v_add_u32 v1, s37, v0
M 7,32,2
WL 3
LO 120,18,120
LU 18,120
M 7,33,3
s_add_u32 s48, %[alo], s53
RA 216,1,0
HS 24,18
M 8,44,2
s_addc_u32 s49, %[ahi], 0
LL 17,76
LL 124,77
M 8,45,3
s_add_u32 s52, s52, %[d]
LA 17
v_and_b32 v22, 0xfffffcff, v1
M 9,56,2
s_cmp_eq_u32 s52, %[wrap]
LC_ 124
M 9,57,3
s_cselect_b32 s52, %[rst], s52
DQ 3072
M 10,68,2
WL 3
LO 121,16,121
LU 16,121
M 10,69,3
s_add_u32 s50, s50, %[d]
RA 220,1,1024
HS 24,16
M 11,80,2
LL 18,78
LL 125,79
M 11,81,3
s_cmp_eq_u32 s50, %[wrap]
LA 18
RV 252,22,5120
M 6,22,4
s_cselect_b32 s50, %[rst], s50
LC_ 125
M 6,23,5
WV 31
v_add_u32 v19, 1, v19
M 7,34,4
WL 4
LO 124,17,124
LU 17,124
M 7,35,5
s_add_u32 s38, s38, 32768
RA 224,1,2048
HS 24,17
M 8,46,4
s_cmp_eq_u32 s38, s77
LL 16,80
LL 106,81
M 8,47,5
s_cselect_b32 s38, s76, s38
LA 16
FP
M 9,58,4
s_sub_u32 s39, 0, s39
LC_ 106
M 9,59,5
M 10,70,4
WL 4
LO 125,18,125
LU 18,125
M 10,71,5
RA 228,1,3072
HS 24,18
M 11,82,4
LL 17,82
LL 107,83
M 11,83,5
LA 17
HX 24
Z 0,12,0
LC_ 107
M 0,13,1
Z 1,24,0
WL 3
LO 16,16,106
LU 106,16
M 1,25,1
RA 232,1,4096
HZ 25,106
Z 2,36,0
LL 18,84
LL 110,85
M 2,37,1
LA 18
PK 106,16,104,33
Z 3,48,0
LC_ 110
PK 104,16,104,32
M 3,49,1
WV 21
v_bcnt_u32_b32 v11, v11, 0
Z 4,60,0
WL 3
LO 17,17,107
LU 107,17
M 4,61,1
)"
R"(RA 236,1,6144
HS 25,107
Z 5,72,0
LL 16,86
LL 111,87
M 5,73,1
LA 16
PK 107,17,105,33
M 5,74,2
LC_ 111
PK 105,17,105,32
M 5,75,3
M 4,62,2
WL 3
LO 18,18,110
LU 110,18
M 4,63,3
RA 216,1,7168
HS 25,110
M 3,50,2
LL 17,88
LL 114,89
M 3,51,3
LA 17
PK 110,18,108,33
M 2,38,2
LC_ 114
PK 108,18,108,32
M 2,39,3
FR
PO 110
v_cmp_lt_u32 s[92:93], 2, v11
M 1,26,2
WL 4
LO 16,16,111
LU 111,16
M 1,27,3
s_nop 0
RA 220,1,8192
HS 25,111
M 0,14,2
LL 18,90
LL 115,91
M 0,15,3
s_add_u32 s42, s44, s60
LA 18
PK 111,16,109,33
s_addc_u32 s43, s45, 0
s_sub_u32 s53, 0, s96
s_xor_b32 s92, s92, s53
s_xor_b32 s93, s93, s53
M 0,16,4
s_mul_i32 s53, %[d], 0
LC_ 115
PK 109,16,109,32
M 0,17,5
s_add_u32 s53, s53, s57
PO 111
M 1,28,4
WL 4
LO 17,17,114
LU 114,17
M 1,29,5
s_add_u32 s46, %[xlo], s53
RA 224,1,9216
HS 25,114
s_addc_u32 s47, %[xhi], 0
s_bitcmp1_b64 s[92:93], 48
s_cselect_b32 s58, 16, 0
s_lshr_b32 s58, 0xfe987610, s58
v_lshrrev_b32 v3, 6, v0
v_and_b32 v3, 12, v3
v_bfe_u32 v3, s58, v3, 4
v_mad_u32_u24 v3, v3, s97, v21
M 2,40,4
LL 16,92
LL 118,93
ST 104
M 2,41,5
s_add_u32 s42, s44, s61
LA 16
PK 114,17,112,33
M 3,52,4
s_addc_u32 s43, s45, 0
LC_ 118
PK 112,17,112,32
M 3,53,5
s_mov_b32 m0, %[pm]
M 4,64,4
WL 3
LO 18,18,115
LU 115,18
M 4,65,5
RA 228,1,10240
HS 25,115
M 5,76,4
LL 17,94
LL 119,95
M 5,77,5
LA 17
PK 115,18,113,33
M 5,78,0
LC_ 119
PK 113,18,113,32
M 5,79,1
DF
M 4,66,0
WL 3
LO 16,16,118
LU 118,16
M 4,67,1
RA 232,1,11264
HS 25,118
M 3,54,0
LL 18,96
LL 122,97
M 3,55,1
LA 18
PK 118,16,116,33
v_cmp_gt_u32 vcc, v19, v15
M 2,42,0
LC_ 122
PK 116,16,116,32
M 2,43,1
RV 252,22,5392
PO 118
M 1,30,0
WL 4
LO 17,17,119
LU 119,17
M 1,31,1
s_cbranch_vccnz .Lk1s5
.Lk1r5:
RA 236,1,12288
HS 25,119
M 0,18,0
LL 16,98
LL 123,99
ST 108
XL 0,0
M 0,19,1
s_add_u32 s42, s44, s62
LA 16
PK 119,17,117,33
s_addc_u32 s43, s45, 0
M 0,20,2
LC_ 123
PK 117,17,117,32
M 0,21,3
W1 24,0
PO 119
M 1,32,2
WL 5
LO 18,18,122
LU 122,18
M 1,33,3
RA 216,1,13312
HS 25,122
M 2,44,2
LL 17,100
LL 126,101
ST 112
M 2,45,3
s_add_u32 s42, s44, s63
LA 17
PK 122,18,120,33
M 3,56,2
s_addc_u32 s43, s45, 0
LC_ 126
PK 120,18,120,32
M 3,57,3
M 4,68,2
WL 4
LO 16,16,123
LU 123,16
M 4,69,3
RA 220,1,14336
HS 25,123
M 5,80,2
LL 18,102
LL 127,103
M 5,81,3
LA 18
PK 123,16,121,33
M 0,22,4
LC_ 127
PK 121,16,121,32
M 0,23,5
M 1,34,4
WL 3
LO 17,17,126
LU 126,17
M 1,35,5
RA 224,1,15360
HS 25,126
M 2,46,4
LL 16,56
LL 104,57
M 2,47,5
LA 16
XL 1,64
PK 126,17,124,33
M 3,58,4
LC_ 104
PK 124,17,124,32
M 3,59,5
ST 116
PO 126
M 4,70,4
WL 3
LO 18,18,127
LU 127,18
M 4,71,5
s_add_u32 s42, s44, s64
RA 228,1,16384
HS 25,127
M 5,82,4
LL 17,58
LL 105,59
M 5,83,5
s_addc_u32 s43, s45, 0
LA 17
PK 127,18,125,33
Z 6,12,0
LC_ 105
PK 125,18,125,32
M 6,13,1
ST 120
PO 127
Z 7,24,0
WL 3
LO 104,16,104
LU 16,104
M 7,25,1
s_add_u32 s42, s44, s65
RA 232,1,17408
HZ 24,16
Z 8,36,0
s_addc_u32 s43, s45, 0
LL 18,60
LL 108,61
M 8,37,1
LA 18
ds_read_b64 v[14:15], v5 offset:0
Z 9,48,0
LC_ 108
XL 2,128
M 9,49,1
s_mul_i32 s53, s50, 25600
Z 10,60,0
WL 4
LO 105,17,105
LU 17,105
M 10,61,1
s_add_u32 s40, %[wlo], s53
RA 236,1,18432
HS 24,17
Z 11,72,0
LL 16,62
LL 109,63
M 11,73,1
s_addc_u32 s41, %[whi], 0
LA 16
v_subrev_u32 v5, s39, v5
M 11,74,2
s_mov_b32 m0, s38
LC_ 109
ST 124
M 11,75,3
DW 0
HX 25
M 10,62,2
WL 3
LO 108,18,108
LU 18,108
W1 25,1024
M 10,63,3
s_add_u32 s37, s37, 32768
RA 216,1,19456
HS 24,18
M 9,50,2
s_cmp_eq_u32 s37, s75
LL 17,64
LL 112,65
M 9,51,3
s_cselect_b32 s37, s74, s37
LA 17
XL 3,192
M 8,38,2
LC_ 112
DW 1024
M 8,39,3
v_add_f32 v14, v14, v15
s_nop 1
v_add_f32_dpp v14, v14, v14 quad_perm:[1,0,3,2] row_mask:0xf bank_mask:0xf
v_add_u32 v4, s39, v4
M 7,26,2
WL 4
LO 109,16,109
LU 16,109
M 7,27,3
RA 220,1,20480
HS 24,16
v_add_f32_dpp v14, v14, v14 quad_perm:[2,3,0,1] row_mask:0xf bank_mask:0xf
M 6,14,2
LL 18,66
LL 113,67
M 6,15,3
LA 18
DW 2048
M 6,16,4
LC_ 113
v_add_f32_dpp v14, v14, v14 row_half_mirror row_mask:0xf bank_mask:0xf
M 6,17,5
v_cvt_f64_f32 v[14:15], v14
M 7,28,4
WL 3
LO 112,17,112
LU 17,112
M 7,29,5
s_mul_i32 s53, s51, 24
RA 224,1,21504
HS 24,17
M 8,40,4
s_bitcmp1_b32 s51, 0
LL 16,68
LL 116,69
M 8,41,5
s_addc_u32 s53, s53, 0
LA 16
s_mov_b64 exec, s[54:55]
SP
s_mov_b64 exec, -1
M 9,52,4
s_lshl_b32 s53, s53, 11
LC_ 116
M 9,53,5
s_bitcmp1_b64 s[90:91], s51
M 10,64,4
WL 3
LO 113,18,113
LU 18,113
M 10,65,5
s_cselect_b64 s[44:45], s[84:85], s[82:83]
RA 228,1,22528
HS 24,18
M 11,76,4
LL 17,70
LL 117,71
M 11,77,5
s_add_u32 s44, s44, s53
LA 17
M 11,78,0
s_addc_u32 s45, s45, 0
LC_ 117
M 11,79,1
s_add_u32 s51, s51, %[d]
M 10,66,0
WL 3
LO 116,16,116
LU 16,116
M 10,67,1
s_cmp_eq_u32 s51, %[wrap]
RA 232,1,23552
HS 24,16
M 9,54,0
s_cselect_b32 s51, %[rst], s51
LL 18,72
LL 120,73
M 9,55,1
s_add_u32 s40, s40, 3072
LA 18
M 8,42,0
s_addc_u32 s41, s41, 0
LC_ 120
s_add_u32 m0, s38, 3072
M 8,43,1
DW 0
M 7,30,0
WL 3
LO 117,17,117
LU 17,117
M 7,31,1
RA 236,1,24576
HS 24,17
M 6,18,0
LL 16,74
LL 121,75
M 6,19,1
LA 16
XL 4,256
M 6,20,2
LC_ 121
DW 1024
M 6,21,3
DW 2048
v_add_u32 v1, s37, v0
M 7,32,2
WL 3
LO 120,18,120
)"
R"(LU 18,120
M 7,33,3
s_lshl_b32 s53, s52, 8
RA 216,1,0
HS 24,18
M 8,44,2
s_add_u32 s48, %[alo], s53
LL 17,76
LL 124,77
M 8,45,3
s_addc_u32 s49, %[ahi], 0
LA 17
v_and_b32 v22, 0xfffffcff, v1
M 9,56,2
s_add_u32 s52, s52, %[d]
LC_ 124
M 9,57,3
s_cmp_eq_u32 s52, %[wrap]
M 10,68,2
WL 3
LO 121,16,121
LU 16,121
M 10,69,3
s_cselect_b32 s52, %[rst], s52
RA 220,1,1024
HS 24,16
M 11,80,2
LL 18,78
LL 125,79
M 11,81,3
LA 18
RV 252,22,5120
M 6,22,4
LC_ 125
DQ 3072
M 6,23,5
XL 5,320
M 7,34,4
WL 4
LO 124,17,124
LU 17,124
M 7,35,5
s_add_u32 s50, s50, %[d]
RA 224,1,2048
HS 24,17
M 8,46,4
s_cmp_eq_u32 s50, %[wrap]
LL 16,80
LL 106,81
M 8,47,5
s_cselect_b32 s50, %[rst], s50
LA 16
v_add_u32 v19, 1, v19
M 9,58,4
s_add_u32 s38, s38, 32768
LC_ 106
M 9,59,5
M 10,70,4
WL 3
LO 125,18,125
LU 18,125
M 10,71,5
RA 228,1,3072
HS 24,18
M 11,82,4
LL 17,82
LL 107,83
M 11,83,5
LA 17
HX 24
Z 0,12,0
s_cmp_eq_u32 s38, s77
LC_ 107
M 0,13,1
s_cselect_b32 s38, s76, s38
WV 36
FP
Z 1,24,0
WL 4
LO 16,16,106
LU 106,16
M 1,25,1
s_sub_u32 s39, 0, s39
RA 232,1,4096
HZ 25,106
Z 2,36,0
LL 18,84
LL 110,85
M 2,37,1
LA 18
PK 106,16,104,33
Z 3,48,0
LC_ 110
PK 104,16,104,32
M 3,49,1
XL 6,384
Z 4,60,0
WL 4
LO 17,17,107
LU 107,17
M 4,61,1
s_add_u32 s42, s44, s60
RA 236,1,6144
HS 25,107
Z 5,72,0
LL 16,86
LL 111,87
M 5,73,1
s_addc_u32 s43, s45, 0
LA 16
PK 107,17,105,33
M 5,74,2
LC_ 111
PK 105,17,105,32
M 5,75,3
ST 104
M 4,62,2
WL 3
LO 18,18,110
LU 110,18
M 4,63,3
RA 216,1,7168
HS 25,110
M 3,50,2
LL 17,88
LL 114,89
M 3,51,3
LA 17
PK 110,18,108,33
M 2,38,2
LC_ 114
PK 108,18,108,32
M 2,39,3
FR
PO 110
M 1,26,2
WL 4
LO 16,16,111
LU 111,16
M 1,27,3
RA 220,1,8192
HS 25,111
M 0,14,2
LL 18,90
LL 115,91
M 0,15,3
LA 18
PK 111,16,109,33
M 0,16,4
LC_ 115
PK 109,16,109,32
M 0,17,5
XL 7,448
PO 111
M 1,28,4
WL 4
LO 17,17,114
LU 114,17
M 1,29,5
s_add_u32 s42, s44, s61
RA 224,1,9216
HS 25,114
M 2,40,4
s_addc_u32 s43, s45, 0
LL 16,92
LL 118,93
M 2,41,5
s_mov_b32 m0, %[pm]
LA 16
PK 114,17,112,33
M 3,52,4
LC_ 118
PK 112,17,112,32
M 3,53,5
M 4,64,4
WL 3
LO 18,18,115
LU 115,18
M 4,65,5
RA 228,1,10240
HS 25,115
M 5,76,4
LL 17,94
LL 119,95
M 5,77,5
LA 17
PK 115,18,113,33
M 5,78,0
LC_ 119
PK 113,18,113,32
M 5,79,1
DF
M 4,66,0
WL 3
LO 16,16,118
LU 118,16
M 4,67,1
RA 232,1,11264
HS 25,118
M 3,54,0
LL 18,96
LL 122,97
M 3,55,1
LA 18
PK 118,16,116,33
v_cmp_gt_u32 vcc, v19, v15
M 2,42,0
LC_ 122
PK 116,16,116,32
M 2,43,1
RV 252,22,5392
PO 118
M 1,30,0
WL 4
LO 17,17,119
LU 119,17
M 1,31,1
s_cbranch_vccnz .Lk1s6
.Lk1r6:
RA 236,1,12288
HS 25,119
M 0,18,0
LL 16,98
LL 123,99
ST 108
M 0,19,1
s_add_u32 s42, s44, s62
LA 16
PK 119,17,117,33
s_addc_u32 s43, s45, 0
M 0,20,2
LC_ 123
PK 117,17,117,32
M 0,21,3
W1 24,0
PO 119
M 1,32,2
WL 5
LO 18,18,122
LU 122,18
M 1,33,3
RA 216,1,13312
HS 25,122
M 2,44,2
LL 17,100
LL 126,101
ST 112
M 2,45,3
LA 17
PK 122,18,120,33
M 3,56,2
LC_ 126
XL 8,512
PK 120,18,120,32
M 3,57,3
s_add_u32 s42, s44, s63
M 4,68,2
WL 4
LO 16,16,123
LU 123,16
M 4,69,3
s_addc_u32 s43, s45, 0
RA 220,1,14336
HS 25,123
M 5,80,2
LL 18,102
LL 127,103
M 5,81,3
LA 18
PK 123,16,121,33
M 0,22,4
LC_ 127
PK 121,16,121,32
M 0,23,5
ST 116
M 1,34,4
WL 3
LO 17,17,126
LU 126,17
M 1,35,5
s_add_u32 s42, s44, s64
RA 224,1,15360
HS 25,126
M 2,46,4
s_addc_u32 s43, s45, 0
LL 16,56
LL 104,57
M 2,47,5
LA 16
PK 126,17,124,33
M 3,58,4
LC_ 104
PK 124,17,124,32
M 3,59,5
XL 9,576
PO 126
M 4,70,4
WL 3
LO 18,18,127
LU 127,18
M 4,71,5
RA 228,1,16384
HS 25,127
M 5,82,4
LL 17,58
LL 105,59
M 5,83,5
LA 17
PK 127,18,125,33
Z 6,12,0
LC_ 105
PK 125,18,125,32
M 6,13,1
ST 120
PO 127
Z 7,24,0
WL 3
LO 104,16,104
LU 16,104
M 7,25,1
s_add_u32 s42, s44, s65
RA 232,1,17408
HZ 24,16
Z 8,36,0
s_addc_u32 s43, s45, 0
LL 18,60
LL 108,61
M 8,37,1
s_mul_i32 s53, s50, 25600
LA 18
ds_read_b64 v[14:15], v5 offset:0
Z 9,48,0
s_add_u32 s40, %[wlo], s53
LC_ 108
v_subrev_u32 v5, s39, v5
M 9,49,1
s_addc_u32 s41, %[whi], 0
Z 10,60,0
WL 4
LO 105,17,105
LU 17,105
M 10,61,1
s_mov_b32 m0, s38
RA 236,1,18432
HS 24,17
Z 11,72,0
LL 16,62
LL 109,63
M 11,73,1
LA 16
XL 10,640
M 11,74,2
LC_ 109
ST 124
M 11,75,3
DW 0
HX 25
M 10,62,2
WL 3
LO 108,18,108
LU 18,108
W1 25,1024
M 10,63,3
s_add_u32 s37, s37, 32768
RA 216,1,19456
HS 24,18
M 9,50,2
s_cmp_eq_u32 s37, s75
LL 17,64
LL 112,65
M 9,51,3
s_cselect_b32 s37, s74, s37
LA 17
DW 1024
M 8,38,2
LC_ 112
v_add_f32 v14, v14, v15
s_nop 1
v_add_f32_dpp v14, v14, v14 quad_perm:[1,0,3,2] row_mask:0xf bank_mask:0xf
M 8,39,3
s_nop 0
v_add_f32_dpp v14, v14, v14 quad_perm:[2,3,0,1] row_mask:0xf bank_mask:0xf
v_add_u32 v4, s39, v4
M 7,26,2
WL 4
LO 109,16,109
LU 16,109
M 7,27,3
RA 220,1,20480
HS 24,16
M 6,14,2
LL 18,66
LL 113,67
M 6,15,3
LA 18
DW 2048
M 6,16,4
LC_ 113
XL 11,704
v_add_f32_dpp v14, v14, v14 row_half_mirror row_mask:0xf bank_mask:0xf
)"
R"(M 6,17,5
v_cvt_f64_f32 v[14:15], v14
M 7,28,4
WL 3
LO 112,17,112
LU 17,112
M 7,29,5
s_mul_i32 s53, s51, 24
RA 224,1,21504
HS 24,17
M 8,40,4
s_bitcmp1_b32 s51, 0
LL 16,68
LL 116,69
M 8,41,5
s_addc_u32 s53, s53, 0
LA 16
s_mov_b64 exec, s[54:55]
SP
s_mov_b64 exec, -1
M 9,52,4
s_lshl_b32 s53, s53, 11
LC_ 116
M 9,53,5
s_bitcmp1_b64 s[90:91], s51
M 10,64,4
WL 3
LO 113,18,113
LU 18,113
M 10,65,5
s_cselect_b64 s[44:45], s[84:85], s[82:83]
RA 228,1,22528
HS 24,18
M 11,76,4
LL 17,70
LL 117,71
M 11,77,5
s_add_u32 s44, s44, s53
LA 17
M 11,78,0
s_addc_u32 s45, s45, 0
LC_ 117
M 11,79,1
s_add_u32 s51, s51, %[d]
M 10,66,0
WL 3
LO 116,16,116
LU 16,116
M 10,67,1
s_cmp_eq_u32 s51, %[wrap]
RA 232,1,23552
HS 24,16
M 9,54,0
s_cselect_b32 s51, %[rst], s51
LL 18,72
LL 120,73
M 9,55,1
s_add_u32 s40, s40, 3072
LA 18
M 8,42,0
s_addc_u32 s41, s41, 0
LC_ 120
s_add_u32 m0, s38, 3072
M 8,43,1
DW 0
M 7,30,0
WL 3
LO 117,17,117
LU 17,117
M 7,31,1
RA 236,1,24576
HS 24,17
M 6,18,0
LL 16,74
LL 121,75
M 6,19,1
LA 16
DW 1024
M 6,20,2
s_bitcmp1_b64 s[92:93], 48
LC_ 121
M 6,21,3
s_cselect_b32 s58, 16, 0
v_add_u32 v1, s37, v0
M 7,32,2
WL 3
LO 120,18,120
LU 18,120
M 7,33,3
s_lshr_b32 s58, 0xda43cb52, s58
RA 216,1,0
HS 24,18
M 8,44,2
LL 17,76
LL 124,77
M 8,45,3
LA 17
v_and_b32 v22, 0xfffffcff, v1
M 9,56,2
LC_ 124
v_and_b32 v11, 0x1f0, v0
M 9,57,3
v_min_u32 v11, 368, v11
M 10,68,2
WL 3
LO 121,16,121
LU 16,121
M 10,69,3
RA 220,1,1024
HS 24,16
M 11,80,2
LL 18,78
LL 125,79
M 11,81,3
LA 18
RV 252,22,5120
DW 2048
v_lshlrev_b32 v11, 3, v11
M 6,22,4
s_lshl_b32 s53, s52, 8
LC_ 125
M 6,23,5
s_add_u32 s48, %[alo], s53
M 7,34,4
WL 4
LO 124,17,124
LU 17,124
M 7,35,5
s_addc_u32 s49, %[ahi], 0
RA 224,1,2048
HS 24,17
M 8,46,4
s_add_u32 s52, s52, %[d]
LL 16,80
LL 106,81
s_cmp_eq_u32 s52, %[wrap]
s_cselect_b32 s52, %[rst], s52
M 8,47,5
LA 16
DQ 3072
M 9,58,4
s_add_u32 s50, s50, %[d]
LC_ 106
M 9,59,5
M 10,70,4
WL 3
LO 125,18,125
LU 18,125
M 10,71,5
RA 228,1,3072
HS 24,18
M 11,82,4
LL 17,82
LL 107,83
M 11,83,5
LA 17
HX 24
s_cmp_eq_u32 s50, %[wrap]
s_cselect_b32 s50, %[rst], s50
Z 0,12,0
LC_ 107
v_add_u32 v19, 1, v19
M 0,13,1
s_add_u32 s38, s38, 32768
Z 1,24,0
WL 3
LO 16,16,106
LU 106,16
M 1,25,1
s_cmp_eq_u32 s38, s77
RA 232,1,4096
HZ 25,106
Z 2,36,0
s_cselect_b32 s38, s76, s38
LL 18,84
LL 110,85
M 2,37,1
LA 18
PK 106,16,104,33
Z 3,48,0
LC_ 110
PK 104,16,104,32
M 3,49,1
WV 42
FP
s_sub_u32 s39, 0, s39
Z 4,60,0
WL 4
LO 17,17,107
LU 107,17
M 4,61,1
s_add_u32 s42, s44, s60
RA 236,1,6144
HS 25,107
Z 5,72,0
LL 16,86
LL 111,87
M 5,73,1
s_addc_u32 s43, s45, 0
LA 16
PK 107,17,105,33
M 5,74,2
LC_ 111
PK 105,17,105,32
M 5,75,3
ST 104
M 4,62,2
WL 4
LO 18,18,110
LU 110,18
M 4,63,3
RA 216,1,7168
HS 25,110
M 3,50,2
LL 17,88
LL 114,89
M 3,51,3
LA 17
PK 110,18,108,33
M 2,38,2
LC_ 114
PK 108,18,108,32
M 2,39,3
FR
PO 110
M 1,26,2
WL 4
LO 16,16,111
LU 111,16
M 1,27,3
s_add_u32 s42, s44, s61
RA 220,1,8192
HS 25,111
M 0,14,2
LL 18,90
LL 115,91
M 0,15,3
s_addc_u32 s43, s45, 0
LA 18
PK 111,16,109,33
M 0,16,4
s_mov_b32 m0, %[pm]
LC_ 115
PK 109,16,109,32
M 0,17,5
DF
PO 111
M 1,28,4
WL 4
LO 17,17,114
LU 114,17
M 1,29,5
RA 224,1,9216
HS 25,114
M 2,40,4
LL 16,92
LL 118,93
M 2,41,5
LA 16
PK 114,17,112,33
M 3,52,4
LC_ 118
PK 112,17,112,32
M 3,53,5
WL 6
ST 108
v_cmp_gt_u32 vcc, v19, v15
M 4,64,4
WL 3
LO 18,18,115
LU 115,18
M 4,65,5
s_add_u32 s42, s44, s62
RA 228,1,10240
HS 25,115
M 5,76,4
LL 17,94
LL 119,95
M 5,77,5
s_cbranch_vccnz .Lk1s7
.Lk1r7:
LA 17
PK 115,18,113,33
M 5,78,0
s_addc_u32 s43, s45, 0
LC_ 119
PK 113,18,113,32
M 5,79,1
W1 24,0
M 4,66,0
WL 4
LO 16,16,118
LU 118,16
M 4,67,1
RA 232,1,11264
HS 25,118
M 3,54,0
LL 18,96
LL 122,97
M 3,55,1
LA 18
PK 118,16,116,33
M 2,42,0
LC_ 122
PK 116,16,116,32
M 2,43,1
RV 252,22,5392
PO 118
M 1,30,0
WL 5
LO 17,17,119
LU 119,17
M 1,31,1
RA 236,1,12288
HS 25,119
M 0,18,0
LL 16,98
LL 123,99
M 0,19,1
LA 16
PK 119,17,117,33
M 0,20,2
LC_ 123
PK 117,17,117,32
M 0,21,3
ST 112
PO 119
M 1,32,2
WL 4
LO 18,18,122
LU 122,18
M 1,33,3
RA 216,1,13312
HS 25,122
M 2,44,2
LL 17,100
LL 126,101
M 2,45,3
LA 17
PK 122,18,120,33
M 3,56,2
LC_ 126
PK 120,18,120,32
M 3,57,3
v_lshrrev_b32 v3, 9, v0
M 4,68,2
WL 3
LO 16,16,123
LU 123,16
M 4,69,3
RA 220,1,14336
HS 25,123
v_lshl_add_u32 v3, v3, 2, 0
v_bfe_u32 v3, s58, v3, 4
v_mad_u32_u24 v3, v3, s97, v11
M 5,80,2
LL 18,102
LL 127,103
M 5,81,3
s_add_u32 s42, s44, s63
LA 18
PK 123,16,121,33
M 0,22,4
s_addc_u32 s43, s45, 0
LC_ 127
PK 121,16,121,32
M 0,23,5
ST 116
M 1,34,4
WL 3
LO 17,17,126
LU 126,17
M 1,35,5
s_add_u32 s42, s44, s64
RA 224,1,15360
HS 25,126
M 2,46,4
s_addc_u32 s43, s45, 0
LL 16,56
LL 104,57
M 2,47,5
LA 16
PK 126,17,124,33
M 3,58,4
LC_ 104
PK 124,17,124,32
M 3,59,5
ST 120
PO 126
M 4,70,4
WL 3
LO 18,18,127
LU 127,18
M 4,71,5
s_add_u32 s42, s44, s65
RA 228,1,16384
HS 25,127
M 5,82,4
LL 17,58
LL 105,59
)"
R"(M 5,83,5
s_addc_u32 s43, s45, 0
LA 17
PK 127,18,125,33
Z 6,12,0
LC_ 105
PK 125,18,125,32
M 6,13,1
ds_read_b64 v[14:15], v5 offset:0
PO 127
Z 7,24,0
WL 4
LO 104,16,104
LU 16,104
M 7,25,1
RA 232,1,17408
HZ 24,16
Z 8,36,0
LL 18,60
LL 108,61
M 8,37,1
s_mul_i32 s53, s50, 25600
LA 18
v_subrev_u32 v5, s39, v5
Z 9,48,0
s_add_u32 s40, %[wlo], s53
LC_ 108
HX 25
M 9,49,1
s_addc_u32 s41, %[whi], 0
W1 25,1024
Z 10,60,0
WL 5
LO 105,17,105
LU 17,105
M 10,61,1
s_mov_b32 m0, s38
RA 236,1,18432
HS 24,17
Z 11,72,0
LL 16,62
LL 109,63
M 11,73,1
LA 16
ST 124
M 11,74,2
LC_ 109
DW 0
M 11,75,3
WL 7
v_add_f32 v14, v14, v15
v_add_u32 v4, s39, v4
M 10,62,2
WL 4
LO 108,18,108
LU 18,108
M 10,63,3
s_add_u32 s37, s37, 32768
RA 216,1,19456
HS 24,18
M 9,50,2
s_cmp_eq_u32 s37, s75
LL 17,64
LL 112,65
M 9,51,3
s_cselect_b32 s37, s74, s37
LA 17
DW 1024
M 8,38,2
LC_ 112
v_add_f32_dpp v14, v14, v14 quad_perm:[1,0,3,2] row_mask:0xf bank_mask:0xf
M 8,39,3
s_nop 0
v_add_f32_dpp v14, v14, v14 quad_perm:[2,3,0,1] row_mask:0xf bank_mask:0xf
M 7,26,2
WL 3
LO 109,16,109
LU 16,109
M 7,27,3
RA 220,1,20480
HS 24,16
M 6,14,2
LL 18,66
LL 113,67
M 6,15,3
LA 18
DW 2048
M 6,16,4
LC_ 113
v_add_f32_dpp v14, v14, v14 row_half_mirror row_mask:0xf bank_mask:0xf
M 6,17,5
v_cvt_f64_f32 v[14:15], v14
M 7,28,4
WL 3
LO 112,17,112
LU 17,112
M 7,29,5
s_mul_i32 s53, s51, 24
RA 224,1,21504
HS 24,17
M 8,40,4
s_bitcmp1_b32 s51, 0
LL 16,68
LL 116,69
M 8,41,5
s_addc_u32 s53, s53, 0
LA 16
s_mov_b64 exec, s[54:55]
SP
s_mov_b64 exec, -1
M 9,52,4
s_lshl_b32 s53, s53, 11
LC_ 116
M 9,53,5
s_bitcmp1_b64 s[90:91], s51
M 10,64,4
WL 3
LO 113,18,113
LU 18,113
M 10,65,5
s_cselect_b64 s[44:45], s[84:85], s[82:83]
RA 228,1,22528
HS 24,18
M 11,76,4
LL 17,70
LL 117,71
M 11,77,5
s_add_u32 s44, s44, s53
LA 17
M 11,78,0
s_addc_u32 s45, s45, 0
LC_ 117
M 11,79,1
s_add_u32 s51, s51, %[d]
M 10,66,0
WL 3
LO 116,16,116
LU 16,116
M 10,67,1
s_cmp_eq_u32 s51, %[wrap]
RA 232,1,23552
HS 24,16
M 9,54,0
s_cselect_b32 s51, %[rst], s51
LL 18,72
LL 120,73
M 9,55,1
s_add_u32 s40, s40, 3072
LA 18
M 8,42,0
s_addc_u32 s41, s41, 0
LC_ 120
s_add_u32 m0, s38, 3072
M 8,43,1
DW 0
M 7,30,0
WL 3
LO 117,17,117
LU 17,117
M 7,31,1
RA 236,1,24576
HS 24,17
M 6,18,0
LL 16,74
LL 121,75
M 6,19,1
LA 16
DW 1024
M 6,20,2
LC_ 121
DY
M 6,21,3
DW 2048
v_add_u32 v1, s37, v0
M 7,32,2
WL 3
LO 120,18,120
LU 18,120
M 7,33,3
s_lshl_b32 s53, s52, 8
RA 216,1,0
HS 24,18
M 8,44,2
s_add_u32 s48, %[alo], s53
LL 17,76
LL 124,77
M 8,45,3
s_addc_u32 s49, %[ahi], 0
LA 17
v_and_b32 v22, 0xfffffcff, v1
M 9,56,2
s_add_u32 s52, s52, %[d]
LC_ 124
M 9,57,3
s_cmp_eq_u32 s52, %[wrap]
M 10,68,2
WL 3
LO 121,16,121
LU 16,121
M 10,69,3
s_cselect_b32 s52, %[rst], s52
RA 220,1,1024
HS 24,16
M 11,80,2
LL 18,78
LL 125,79
M 11,81,3
LA 18
RV 252,22,5120
M 6,22,4
LC_ 125
DQ 3072
M 6,23,5
s_add_u32 s50, s50, %[d]
M 7,34,4
WL 4
LO 124,17,124
LU 17,124
M 7,35,5
s_cmp_eq_u32 s50, %[wrap]
RA 224,1,2048
HS 24,17
M 8,46,4
s_cselect_b32 s50, %[rst], s50
LL 16,80
LL 106,81
M 8,47,5
LA 16
v_add_u32 v19, 1, v19
M 9,58,4
s_add_u32 s38, s38, 32768
LC_ 106
M 9,59,5
M 10,70,4
WL 3
LO 125,18,125
LU 18,125
M 10,71,5
RA 228,1,3072
HS 24,18
M 11,82,4
LL 17,82
LL 107,83
M 11,83,5
LA 17
HX 24
Z 0,12,0
s_cmp_eq_u32 s38, s77
LC_ 107
M 0,13,1
s_cselect_b32 s38, s76, s38
WV 38
FP
Z 1,24,0
WL 4
LO 16,16,106
LU 106,16
M 1,25,1
s_sub_u32 s39, 0, s39
RA 232,1,4096
HZ 25,106
Z 2,36,0
LL 18,84
LL 110,85
M 2,37,1
LA 18
PK 106,16,104,33
Z 3,48,0
LC_ 110
PK 104,16,104,32
M 3,49,1
Z 4,60,0
WL 4
LO 17,17,107
LU 107,17
M 4,61,1
s_add_u32 s42, s44, s60
RA 236,1,6144
HS 25,107
Z 5,72,0
LL 16,86
LL 111,87
M 5,73,1
s_addc_u32 s43, s45, 0
LA 16
PK 107,17,105,33
M 5,74,2
LC_ 111
PK 105,17,105,32
M 5,75,3
ST 104
M 4,62,2
WL 3
LO 18,18,110
LU 110,18
M 4,63,3
RA 216,1,7168
HS 25,110
M 3,50,2
LL 17,88
LL 114,89
M 3,51,3
LA 17
PK 110,18,108,33
M 2,38,2
LC_ 114
PK 108,18,108,32
M 2,39,3
FR
PO 110
M 1,26,2
WL 4
LO 16,16,111
LU 111,16
M 1,27,3
s_add_u32 s42, s44, s61
RA 220,1,8192
HS 25,111
M 0,14,2
LL 18,90
LL 115,91
M 0,15,3
s_addc_u32 s43, s45, 0
LA 18
PK 111,16,109,33
M 0,16,4
s_mov_b32 m0, %[pm]
LC_ 115
PK 109,16,109,32
M 0,17,5
DF
PO 111
M 1,28,4
WL 4
LO 17,17,114
LU 114,17
M 1,29,5
RA 224,1,9216
HS 25,114
M 2,40,4
LL 16,92
LL 118,93
M 2,41,5
LA 16
PK 114,17,112,33
M 3,52,4
LC_ 118
PK 112,17,112,32
M 3,53,5
WL 6
ST 108
v_cmp_gt_u32 vcc, v19, v15
M 4,64,4
WL 3
LO 18,18,115
LU 115,18
M 4,65,5
s_add_u32 s42, s44, s62
RA 228,1,10240
HS 25,115
M 5,76,4
LL 17,94
LL 119,95
M 5,77,5
s_cbranch_vccnz .Lk1s8
.Lk1r8:
LA 17
PK 115,18,113,33
M 5,78,0
s_addc_u32 s43, s45, 0
LC_ 119
PK 113,18,113,32
M 5,79,1
W1 24,0
M 4,66,0
WL 4
LO 16,16,118
LU 118,16
M 4,67,1
RA 232,1,11264
HS 25,118
M 3,54,0
LL 18,96
LL 122,97
M 3,55,1
LA 18
PK 118,16,116,33
M 2,42,0
LC_ 122
PK 116,16,116,32
M 2,43,1
RV 252,22,5392
PO 118
M 1,30,0
WL 5
LO 17,17,119
LU 119,17
M 1,31,1
RA 236,1,12288
HS 25,119
)"
R"(M 0,18,0
LL 16,98
LL 123,99
M 0,19,1
LA 16
PK 119,17,117,33
M 0,20,2
LC_ 123
PK 117,17,117,32
BW 0
M 0,21,3
ST 112
PO 119
M 1,32,2
WL 5
RV 40,8,6144
LO 18,18,122
LU 122,18
M 1,33,3
RA 216,1,13312
RV 44,8,6208
HS 25,122
M 2,44,2
LL 17,100
LL 126,101
M 2,45,3
LA 17
PK 122,18,120,33
v_lshrrev_b32 v3, 9, v0
v_lshl_add_u32 v3, v3, 2, 8
v_bfe_u32 v3, s58, v3, 4
v_mad_u32_u24 v3, v3, s97, v11
RV 48,8,6272
M 3,56,2
s_add_u32 s42, s44, s63
LC_ 126
BR 0
PK 120,18,120,32
M 3,57,3
s_addc_u32 s43, s45, 0
ST 116
BW 1
M 4,68,2
WL 9
LO 16,16,123
LU 123,16
M 4,69,3
s_add_u32 s42, s44, s64
RA 220,1,14336
BR 1
HS 25,123
M 5,80,2
WL 3
LL 18,102
LL 127,103
M 5,81,3
s_addc_u32 s43, s45, 0
LA 18
PK 123,16,121,33
LZ 40,0
BW 2
M 0,22,4
LC_ 127
PK 121,16,121,32
M 0,23,5
ST 120
RV 40,8,6336
M 1,34,4
BR 2
LO 17,17,126
LU 126,17
M 1,35,5
WL 5
RA 224,1,15360
HS 25,126
LS 44,1
BW 3
M 2,46,4
LL 16,56
LL 104,57
M 2,47,5
LA 16
PK 126,17,124,33
M 3,58,4
LC_ 104
RV 44,8,6400
PK 124,17,124,32
M 3,59,5
ds_read_b64 v[14:15], v5 offset:0
BR 3
PO 126
M 4,70,4
WL 7
LO 18,18,127
LU 127,18
M 4,71,5
s_add_u32 s42, s44, s65
RA 228,1,16384
HS 25,127
LS 48,2
s_addc_u32 s43, s45, 0
BW 4
M 5,82,4
LL 17,58
LL 105,59
M 5,83,5
LA 17
RV 48,8,6464
PK 127,18,125,33
Z 6,12,0
LC_ 105
PK 125,18,125,32
M 6,13,1
BR 4
PO 127
Z 7,24,0
WL 9
LO 104,16,104
LU 16,104
M 7,25,1
WL 6
RA 232,1,17408
HZ 24,16
LS 40,3
WV 43
BW 5
Z 8,36,0
WV 42
LL 18,60
LL 108,61
M 8,37,1
s_mul_i32 s53, s50, 25600
LA 18
RV 40,8,6528
Z 9,48,0
s_add_u32 s40, %[wlo], s53
LC_ 108
BR 5
M 9,49,1
s_addc_u32 s41, %[whi], 0
v_subrev_u32 v5, s39, v5
HX 25
WL 6
LS 44,4
s_mov_b32 m0, s38
BW 6
Z 10,60,0
LO 105,17,105
LU 17,105
M 10,61,1
RA 236,1,18432
HS 24,17
Z 11,72,0
RV 44,8,6592
LL 16,62
LL 109,63
M 11,73,1
LA 16
W1 25,1024
BR 6
M 11,74,2
LC_ 109
ST 124
M 11,75,3
WL 7
DW 0
v_add_f32 v14, v14, v15
LS 48,5
WV 42
BW 7
M 10,62,2
LO 108,18,108
LU 18,108
M 10,63,3
s_add_u32 s37, s37, 32768
RA 216,1,19456
HS 24,18
M 9,50,2
s_cmp_eq_u32 s37, s75
RV 48,8,6656
LL 17,64
LL 112,65
M 9,51,3
s_cselect_b32 s37, s74, s37
LA 17
DW 1024
BR 7
M 8,38,2
LC_ 112
v_add_f32_dpp v14, v14, v14 quad_perm:[1,0,3,2] row_mask:0xf bank_mask:0xf
M 8,39,3
WL 7
v_add_f32_dpp v14, v14, v14 quad_perm:[2,3,0,1] row_mask:0xf bank_mask:0xf
v_add_u32 v4, s39, v4
LS 40,6
WV 39
BW 8
M 7,26,2
WL 7
LO 109,16,109
LU 16,109
M 7,27,3
RA 220,1,20480
RV 40,8,6720
HS 24,16
M 6,14,2
BR 8
LL 18,66
LL 113,67
M 6,15,3
LA 18
DW 2048
M 6,16,4
LC_ 113
v_add_f32_dpp v14, v14, v14 row_half_mirror row_mask:0xf bank_mask:0xf
M 6,17,5
WL 7
v_cvt_f64_f32 v[14:15], v14
LS 44,7
WV 36
BW 9
M 7,28,4
WL 3
RV 44,8,6784
LO 112,17,112
LU 17,112
M 7,29,5
s_mul_i32 s53, s51, 24
RA 224,1,21504
HS 24,17
M 8,40,4
s_bitcmp1_b32 s51, 0
BR 9
LL 16,68
LL 116,69
M 8,41,5
s_addc_u32 s53, s53, 0
LA 16
s_mov_b64 exec, s[54:55]
SP
s_mov_b64 exec, -1
LS 48,8
s_lshl_b32 s53, s53, 11
BW 10
M 9,52,4
s_bitcmp1_b64 s[90:91], s51
LC_ 116
RV 48,8,6848
M 9,53,5
s_cselect_b64 s[44:45], s[84:85], s[82:83]
RA 240,7,0
BR 10
M 10,64,4
WL 10
LO 113,18,113
LU 18,113
M 10,65,5
s_add_u32 s44, s44, s53
RA 228,1,22528
HS 24,18
M 11,76,4
WL 7
LL 17,70
LL 117,71
M 11,77,5
s_addc_u32 s45, s45, 0
LA 17
M 11,78,0
s_add_u32 s51, s51, %[d]
LC_ 117
M 11,79,1
s_cmp_eq_u32 s51, %[wrap]
LS 40,9
s_cselect_b32 s51, %[rst], s51
WV 32
BW 11
M 10,66,0
WL 4
RA 244,7,256
LO 116,16,116
LU 16,116
M 10,67,1
s_add_u32 s40, s40, 3072
RA 232,1,23552
HS 24,16
M 9,54,0
s_addc_u32 s41, s41, 0
BR 11
LL 18,72
LL 120,73
M 9,55,1
s_add_u32 m0, s38, 3072
LA 18
DW 0
LS 44,10
RA 248,7,512
M 8,42,0
LC_ 120
DY
M 8,43,1
DW 1024
M 7,30,0
WL 7
LO 117,17,117
LU 17,117
M 7,31,1
WL 3
RA 236,1,24576
HS 24,17
LS 48,11
RA 252,7,768
M 6,18,0
LL 16,74
LL 121,75
s_nop 3
v_mul_f32 v36, 0xbb2aaaab, v36
M 6,19,1
LA 16
DW 2048
v_mul_f32 v37, 0xbb2aaaab, v37
M 6,20,2
LC_ 121
v_mul_f32 v38, 0xbb2aaaab, v38
v_mul_f32 v39, 0xbb2aaaab, v39
M 6,21,3
s_lshl_b32 s53, s52, 8
v_add_u32 v1, s37, v0
LK 40,240,0,0
LK 44,242,0,2
s_add_u32 s48, %[alo], s53
RA 240,7,1024
LK 48,244,1,0
s_nop 0
CF 128,40,41
LK 248,246,1,2
s_addc_u32 s49, %[ahi], 0
RA 244,7,1280
CF 129,42,43
M 7,32,2
WL 6
LO 120,18,120
LU 18,120
M 7,33,3
s_add_u32 s52, s52, %[d]
RA 216,1,0
HS 24,18
M 8,44,2
s_cmp_eq_u32 s52, %[wrap]
LL 17,76
LL 124,77
M 8,45,3
s_cselect_b32 s52, %[rst], s52
LA 17
DQ 3072
M 9,56,2
LC_ 124
v_and_b32 v22, 0xfffffcff, v1
CF 130,44,45
M 9,57,3
CF 131,46,47
LK 40,248,2,0
CF 132,48,49
LK 44,250,2,2
RA 248,7,1536
CF 133,50,51
GZ 0
WL 8
CF 134,248,249
CF 135,250,251
LK 48,252,3,0
CF 136,40,41
LK 248,254,3,2
RA 252,7,1792
CF 137,42,43
GS 1
WL 6
CF 138,44,45
CF 139,46,47
LK 40,240,4,0
CF 140,48,49
LK 44,242,4,2
RA 240,7,2048
CF 141,50,51
GS 2
WL 6
CF 142,248,249
CF 143,250,251
LK 48,244,5,0
CF 144,40,41
LK 248,246,5,2
RA 244,7,2304
CF 145,42,43
GS 3
CF 146,44,45
CF 147,46,47
M 10,68,2
WL 3
LO 121,16,121
LU 16,121
M 10,69,3
)"
R"(s_add_u32 s50, s50, %[d]
RA 220,1,1024
HS 24,16
LK 40,248,6,0
WL 3
CF 148,48,49
LK 44,250,6,2
s_cmp_eq_u32 s50, %[wrap]
RA 248,7,2560
CF 149,50,51
GS 4
s_cselect_b32 s50, %[rst], s50
CF 150,248,249
CF 151,250,251
LK 48,252,7,0
CF 152,40,41
LK 248,254,7,2
RA 252,7,2816
CF 153,42,43
GS 5
WL 4
CF 154,44,45
CF 155,46,47
LK 40,240,8,0
CF 156,48,49
LK 44,242,8,2
CF 157,50,51
GS 6
CF 158,248,249
CF 159,250,251
M 11,80,2
LL 18,78
LL 125,79
M 11,81,3
WL 3
LA 18
RV 252,22,5120
LK 48,244,9,0
CF 160,40,41
LK 248,246,9,2
CF 161,42,43
GS 7
CF 162,44,45
CF 163,46,47
M 6,22,4
LC_ 125
M 6,23,5
WL 4
v_add_u32 v19, 1, v19
M 7,34,4
WV 32
LO 124,17,124
LU 17,124
M 7,35,5
s_add_u32 s38, s38, 32768
RA 224,1,2048
HS 24,17
M 8,46,4
s_cmp_eq_u32 s38, s77
LL 16,80
LL 106,81
M 8,47,5
s_cselect_b32 s38, s76, s38
LA 16
FP
LK 40,248,10,0
CF 164,48,49
LK 44,250,10,2
s_sub_u32 s39, 0, s39
CF 165,50,51
GS 8
CF 166,248,249
CF 167,250,251
M 9,58,4
LC_ 106
M 9,59,5
M 10,70,4
WL 4
LO 125,18,125
LU 18,125
M 10,71,5
RA 228,1,3072
HS 24,18
LK 48,252,11,0
CF 168,40,41
LK 248,254,11,2
CF 169,42,43
GS 9
CF 170,44,45
CF 171,46,47
M 11,82,4
LL 17,82
LL 107,83
M 11,83,5
LA 17
HX 24
Z 0,12,0
LC_ 107
RV 36,8,6912
M 0,13,1
GS 10
CF 172,48,49
Z 1,24,0
WL 4
LO 16,16,106
LU 106,16
M 1,25,1
RA 232,1,4096
HZ 25,106
Z 2,36,0
LL 18,84
LL 110,85
M 2,37,1
LA 18
PK 106,16,104,33
Z 3,48,0
LC_ 110
PK 104,16,104,32
M 3,49,1
CF 173,50,51
CF 174,248,249
Z 4,60,0
WL 4
LO 17,17,107
LU 107,17
M 4,61,1
s_add_u32 s42, s44, s60
RA 236,1,6144
HS 25,107
Z 5,72,0
LL 16,86
LL 111,87
M 5,73,1
s_addc_u32 s43, s45, 0
LA 16
PK 107,17,105,33
M 5,74,2
LC_ 111
PK 105,17,105,32
M 5,75,3
ST 104
CF 175,250,251
M 4,62,2
WL 3
LO 18,18,110
LU 110,18
M 4,63,3
RA 216,1,7168
HS 25,110
GS 11
M 3,50,2
LL 17,88
LL 114,89
M 3,51,3
LA 17
PK 110,18,108,33
M 2,38,2
LC_ 114
PK 108,18,108,32
M 2,39,3
FR
PO 110
M 1,26,2
WL 4
LO 16,16,111
LU 111,16
M 1,27,3
s_add_u32 s42, s44, s61
RA 220,1,8192
HS 25,111
M 0,14,2
LL 18,90
LL 115,91
M 0,15,3
s_addc_u32 s43, s45, 0
LA 18
PK 111,16,109,33
M 0,16,4
s_mov_b32 m0, %[pm]
LC_ 115
PK 109,16,109,32
M 0,17,5
DF
PO 111
M 1,28,4
WL 4
LO 17,17,114
LU 114,17
M 1,29,5
RA 224,1,9216
HS 25,114
M 2,40,4
LL 16,92
LL 118,93
M 2,41,5
LA 16
PK 114,17,112,33
M 3,52,4
LC_ 118
PK 112,17,112,32
M 3,53,5
WL 6
ST 108
v_cmp_gt_u32 vcc, v19, v15
M 4,64,4
WL 3
LO 18,18,115
LU 115,18
M 4,65,5
s_add_u32 s42, s44, s62
RA 228,1,10240
HS 25,115
M 5,76,4
LL 17,94
LL 119,95
v_cndmask_b32 v52, v52, v53, s[78:79]
M 5,77,5
s_cbranch_vccnz .Lk1s9
.Lk1r9:
LA 17
PK 115,18,113,33
M 5,78,0
s_addc_u32 s43, s45, 0
LC_ 119
PK 113,18,113,32
v_cndmask_b32 v54, v54, v55, s[78:79]
M 5,79,1
W1 24,0
v_cndmask_b32 v52, v52, v54, s[80:81]
M 4,66,0
WL 4
LO 16,16,118
LU 118,16
v_mul_f32 v52, 0x3b2aaaab, v52
M 4,67,1
RA 232,1,11264
HS 25,118
v_med3_f32 v53, v52, v39, v37
M 3,54,0
LL 18,96
LL 122,97
LG 52,53
M 3,55,1
LA 18
PK 118,16,116,33
C 52,52,52
M 2,42,0
LC_ 122
PK 116,16,116,32
v_lshlrev_b32 v52, 16, v52
M 2,43,1
RV 252,22,5392
PO 118
v_add_f32 v52, v36, v52
M 1,30,0
WL 5
LO 17,17,119
LU 119,17
C 52,52,52
M 1,31,1
RA 236,1,12288
HS 25,119
M 0,18,0
LL 16,98
LL 123,99
v_lshlrev_b32 v52, 16, v52
M 0,19,1
LA 16
PK 119,17,117,33
v_sqrt_f32 v52, v52
M 0,20,2
LC_ 123
PK 117,17,117,32
C 52,52,52
M 0,21,3
ST 112
PO 119
v_lshlrev_b32 v52, 16, v52
M 1,32,2
WL 4
LO 18,18,122
LU 122,18
v_rcp_f32 v52, v52
M 1,33,3
s_add_u32 s42, s44, s63
RA 216,1,13312
HS 25,122
v_mul_f32 v52, v38, v52
M 2,44,2
s_addc_u32 s43, s45, 0
LL 17,100
LL 126,101
CF 52,52,52
M 2,45,3
LA 17
PK 122,18,120,33
ds_bpermute_b32 v13, v28, v52
M 3,56,2
LC_ 126
PK 120,18,120,32
M 3,57,3
ST 116
M 4,68,2
WL 4
LO 16,16,123
LU 123,16
M 4,69,3
s_add_u32 s42, s44, s64
RA 220,1,14336
HS 25,123
M 5,80,2
LL 18,102
LL 127,103
M 5,81,3
s_addc_u32 s43, s45, 0
LA 18
PK 123,16,121,33
M 0,22,4
LC_ 127
PK 121,16,121,32
M 0,23,5
ST 120
M 1,34,4
WL 3
LO 17,17,126
LU 126,17
M 1,35,5
RA 224,1,15360
HS 25,126
XS 128
M 2,46,4
LL 16,56
LL 104,57
M 2,47,5
LA 16
PK 126,17,124,33
M 3,58,4
LC_ 104
PK 124,17,124,32
XS 129
M 3,59,5
ds_read_b64 v[14:15], v5 offset:0
PO 126
M 4,70,4
WL 4
LO 18,18,127
LU 127,18
M 4,71,5
s_add_u32 s42, s44, s65
RA 228,1,16384
HS 25,127
XS 130
M 5,82,4
LL 17,58
LL 105,59
M 5,83,5
s_addc_u32 s43, s45, 0
LA 17
PK 127,18,125,33
Z 6,12,0
LC_ 105
PK 125,18,125,32
XS 131
M 6,13,1
PO 127
v_subrev_u32 v5, s39, v5
Z 7,24,0
WL 4
LO 104,16,104
LU 16,104
XS 132
M 7,25,1
RA 232,1,17408
HZ 24,16
Z 8,36,0
WL 4
LL 18,60
LL 108,61
M 8,37,1
s_mul_i32 s53, s50, 25600
LA 18
v_add_f32 v14, v14, v15
XS 133
Z 9,48,0
s_add_u32 s40, %[wlo], s53
LC_ 108
v_add_f32_dpp v14, v14, v14 quad_perm:[1,0,3,2] row_mask:0xf bank_mask:0xf
M 9,49,1
s_addc_u32 s41, %[whi], 0
v_add_f32_dpp v14, v14, v14 quad_perm:[2,3,0,1] row_mask:0xf bank_mask:0xf
HX 25
Z 10,60,0
WL 3
LO 105,17,105
LU 17,105
XS 134
M 10,61,1
s_mov_b32 m0, s38
RA 236,1,18432
HS 24,17
)"
R"(Z 11,72,0
LL 16,62
LL 109,63
M 11,73,1
LA 16
W1 25,1024
XS 135
M 11,74,2
LC_ 109
ST 124
M 11,75,3
DW 0
v_add_f32_dpp v14, v14, v14 row_half_mirror row_mask:0xf bank_mask:0xf
v_min_u32 v35, v35, v13
M 10,62,2
WL 4
LO 108,18,108
LU 18,108
M 10,63,3
s_add_u32 s37, s37, 32768
RA 216,1,19456
HS 24,18
M 9,50,2
s_cmp_eq_u32 s37, s75
LL 17,64
LL 112,65
XS 136
M 9,51,3
s_cselect_b32 s37, s74, s37
LA 17
v_cvt_f64_f32 v[14:15], v14
M 8,38,2
s_mul_i32 s53, %[d], 0
LC_ 112
s_mov_b64 exec, s[54:55]
SP
s_mov_b64 exec, -1
M 8,39,3
s_add_u32 s53, s53, s57
v_add_u32 v4, s39, v4
XS 137
M 7,26,2
WL 3
LO 109,16,109
LU 16,109
M 7,27,3
s_add_u32 s46, %[xlo], s53
RA 220,1,20480
HS 24,16
M 6,14,2
LL 18,66
LL 113,67
XS 138
M 6,15,3
s_addc_u32 s47, %[xhi], 0
LA 18
XS 139
M 6,16,4
s_bitcmp1_b64 s[92:93], 48
LC_ 113
XS 140
M 6,17,5
s_cselect_b32 s58, 16, 0
XS 141
XS 142
M 7,28,4
WL 3
LO 112,17,112
LU 17,112
M 7,29,5
s_lshr_b32 s58, 0xda43cb52, s58
RA 224,1,21504
HS 24,17
M 8,40,4
LL 16,68
LL 116,69
M 8,41,5
LA 16
v_lshrrev_b32 v3, 6, v0
M 9,52,4
LC_ 116
v_and_b32 v3, 12, v3
v_bfe_u32 v3, s58, v3, 4
v_mad_u32_u24 v3, v3, s97, v21
M 9,53,5
DW 1024
XS 143
M 10,64,4
WL 3
LO 113,18,113
LU 18,113
M 10,65,5
RA 228,1,22528
HS 24,18
M 11,76,4
LL 17,70
LL 117,71
M 11,77,5
LA 17
XL 12,0
M 11,78,0
LC_ 117
DW 2048
M 11,79,1
XL 13,64
XS 144
M 10,66,0
WL 3
LO 116,16,116
LU 16,116
M 10,67,1
s_mul_i32 s53, s51, 24
RA 232,1,23552
HS 24,16
M 9,54,0
s_bitcmp1_b32 s51, 0
LL 18,72
LL 120,73
M 9,55,1
s_addc_u32 s53, s53, 0
LA 18
s_lshl_b32 s53, s53, 11
s_bitcmp1_b64 s[90:91], s51
s_cselect_b64 s[44:45], s[84:85], s[82:83]
s_add_u32 s44, s44, s53
s_addc_u32 s45, s45, 0
s_add_u32 s51, s51, %[d]
s_cmp_eq_u32 s51, %[wrap]
s_cselect_b32 s51, %[rst], s51
XS 145
M 8,42,0
s_add_u32 s40, s40, 3072
LC_ 120
s_addc_u32 s41, s41, 0
s_add_u32 m0, s38, 3072
M 8,43,1
XL 14,128
XS 146
M 7,30,0
WL 3
LO 117,17,117
LU 17,117
M 7,31,1
RA 236,1,24576
HS 24,17
M 6,18,0
LL 16,74
LL 121,75
XS 147
M 6,19,1
LA 16
DW 0
M 6,20,2
LC_ 121
XL 15,192
M 6,21,3
DW 1024
v_add_u32 v1, s37, v0
XS 148
M 7,32,2
WL 3
LO 120,18,120
LU 18,120
M 7,33,3
RA 216,1,0
HS 24,18
M 8,44,2
LL 17,76
LL 124,77
XS 149
M 8,45,3
LA 17
XL 16,256
M 9,56,2
LC_ 124
DW 2048
XS 150
M 9,57,3
XL 17,320
v_and_b32 v22, 0xfffffcff, v1
M 10,68,2
WL 3
LO 121,16,121
LU 16,121
M 10,69,3
s_lshl_b32 s53, s52, 8
RA 220,1,1024
HS 24,16
XS 151
M 11,80,2
LL 18,78
LL 125,79
M 11,81,3
s_add_u32 s48, %[alo], s53
LA 18
RV 252,22,5120
M 6,22,4
s_addc_u32 s49, %[ahi], 0
LC_ 125
XS 152
M 6,23,5
s_add_u32 s52, s52, %[d]
XS 153
XS 154
M 7,34,4
WL 4
LO 124,17,124
LU 17,124
M 7,35,5
s_cmp_eq_u32 s52, %[wrap]
RA 224,1,2048
HS 24,17
M 8,46,4
s_cselect_b32 s52, %[rst], s52
LL 16,80
LL 106,81
M 8,47,5
LA 16
DQ 3072
M 9,58,4
LC_ 106
XL 18,384
M 9,59,5
s_add_u32 s50, s50, %[d]
XS 155
XS 156
M 10,70,4
WL 3
LO 125,18,125
LU 18,125
M 10,71,5
RA 228,1,3072
HS 24,18
M 11,82,4
LL 17,82
LL 107,83
M 11,83,5
LA 17
HX 24
s_cmp_eq_u32 s50, %[wrap]
s_cselect_b32 s50, %[rst], s50
XL 19,448
Z 2,36,0
WV 39
LC_ 107
XL 20,512
v_add_u32 v19, 1, v19
s_add_u32 s38, s38, 32768
s_cmp_eq_u32 s38, s77
s_cselect_b32 s38, s76, s38
FP
s_sub_u32 s39, 0, s39
M 2,37,1
XL 21,576
XS 157
Z 3,48,0
WL 4
LO 16,16,106
LU 106,16
M 3,49,1
RA 232,1,4096
HZ 25,106
Z 4,60,0
LL 18,84
LL 110,85
XS 158
M 4,61,1
LA 18
PK 106,16,104,33
Z 5,72,0
WL 5
LC_ 110
XL 22,640
PK 104,16,104,32
M 5,73,1
RA 236,1,6144
XS 159
M 5,74,2
WL 5
LO 17,17,107
LU 107,17
M 5,75,3
FR
HS 25,107
M 4,62,2
LL 16,86
LL 111,87
XS 160
M 4,63,3
LA 16
PK 107,17,105,33
M 3,50,2
LC_ 111
XL 23,704
PK 105,17,105,32
XS 161
M 3,51,3
s_add_u32 s42, s44, s60
RA 216,1,7168
XS 162
M 2,38,2
WL 4
LO 18,18,110
LU 110,18
M 2,39,3
s_addc_u32 s43, s45, 0
RA 220,1,8192
HS 25,110
M 2,40,4
LL 17,88
LL 114,89
M 2,41,5
LA 17
PK 110,18,108,33
M 3,52,4
LC_ 114
PK 108,18,108,32
XS 163
M 3,53,5
RA 224,1,9216
PO 110
M 4,64,4
WL 5
LO 16,16,111
LU 111,16
ST 104
XS 164
M 4,65,5
s_add_u32 s42, s44, s61
HS 25,111
v_cmp_gt_u32 vcc, v19, v15
M 5,76,4
WL 3
LL 18,90
LL 115,91
M 5,77,5
s_addc_u32 s43, s45, 0
LA 18
PK 111,16,109,33
XS 165
M 5,78,0
s_cbranch_vccnz .Lk1s10
.Lk1r10:
LC_ 115
PK 109,16,109,32
M 5,79,1
WV 17
RA 228,1,10240
PO 111
BW 12
M 4,66,0
WL 5
LO 17,17,114
LU 114,17
XS 166
M 4,67,1
RA 232,1,11264
RV 40,8,6144
HS 25,114
M 3,54,0
RV 44,8,6208
LL 16,92
LL 118,93
M 3,55,1
LA 16
RV 48,8,6272
PK 114,17,112,33
XS 167
M 2,42,0
WL 6
LC_ 118
BR 12
PK 112,17,112,32
M 2,43,1
WV 15
RV 252,22,5392
ds_read_b64 v[14:15], v5 offset:0
W1 24,0
BW 13
XS 168
M 2,44,2
BR 13
LO 18,18,115
LU 115,18
M 2,45,3
WL 5
RA 236,1,12288
HS 25,115
LZ 40,12
WV 14
BW 14
M 3,56,2
LL 17,94
LL 119,95
XS 169
M 3,57,3
LA 17
RV 40,8,6336
PK 115,18,113,33
M 4,68,2
)"
R"(LC_ 119
BR 14
PK 113,18,113,32
M 4,69,3
WL 6
RA 216,1,13312
v_subrev_u32 v5, s39, v5
XS 170
LS 44,13
WV 12
BW 15
M 5,80,2
WL 7
LO 16,16,118
LU 118,16
ST 108
M 5,81,3
s_add_u32 s42, s44, s62
RA 220,1,14336
HS 25,118
s_addc_u32 s43, s45, 0
RV 44,8,6400
M 2,46,4
BR 15
LL 18,96
LL 122,97
XS 171
M 2,47,5
WL 5
LA 18
PK 118,16,116,33
LS 48,14
WV 11
BW 16
M 3,58,4
LC_ 122
PK 116,16,116,32
XS 172
M 3,59,5
RA 224,1,15360
RV 48,8,6464
PO 118
M 4,70,4
LO 17,17,119
LU 119,17
ST 112
M 4,71,5
s_add_u32 s42, s44, s63
BR 16
HS 25,119
XS 173
M 5,82,4
WL 6
LL 16,98
LL 123,99
M 5,83,5
s_addc_u32 s43, s45, 0
LA 16
PK 119,17,117,33
LS 40,15
WV 10
BW 17
Z 8,36,0
LC_ 123
PK 117,17,117,32
v_add_f32 v14, v14, v15
s_nop 1
v_add_f32_dpp v14, v14, v14 quad_perm:[1,0,3,2] row_mask:0xf bank_mask:0xf
s_nop 1
v_add_f32_dpp v14, v14, v14 quad_perm:[2,3,0,1] row_mask:0xf bank_mask:0xf
s_nop 1
v_add_f32_dpp v14, v14, v14 row_half_mirror row_mask:0xf bank_mask:0xf
v_cvt_f64_f32 v[14:15], v14
s_mov_b64 exec, s[54:55]
SP
s_mov_b64 exec, -1
RV 40,8,6528
XS 174
M 8,37,1
WV 9
RA 228,1,16384
BR 17
PO 119
Z 9,48,0
WL 6
LO 18,18,122
LU 122,18
XS 175
M 9,49,1
s_mul_i32 s53, %[d], 196608
RA 232,1,17408
HS 25,122
LS 44,16
s_add_u32 s53, s53, s57
BW 18
Z 10,60,0
s_add_u32 s46, %[xlo], s53
RV 44,8,6592
LL 17,100
LL 126,101
M 10,61,1
s_addc_u32 s47, %[xhi], 0
LA 17
BR 18
PK 122,18,120,33
Z 0,72,0
WL 5
LC_ 126
PK 120,18,120,32
M 0,73,1
s_bitcmp1_b64 s[92:93], 48
RA 236,1,18432
LS 48,17
s_cselect_b32 s58, 16, 0
WV 8
BW 19
M 0,74,2
RV 48,8,6656
LO 16,16,123
LU 123,16
M 0,75,3
s_lshr_b32 s58, 0xfe987610, s58
BR 19
HS 25,123
v_lshrrev_b32 v3, 6, v0
v_and_b32 v3, 12, v3
v_bfe_u32 v3, s58, v3, 4
v_mad_u32_u24 v3, v3, s97, v21
M 10,62,2
s_mul_i32 s53, s50, 25600
LL 18,102
LL 127,103
s_add_u32 s40, %[wlo], s53
s_addc_u32 s41, %[whi], 0
s_mov_b32 m0, s38
M 10,63,3
WL 5
LA 18
PK 123,16,121,33
LS 40,18
WV 7
BW 20
M 9,50,2
LC_ 127
RV 40,8,6720
PK 121,16,121,32
M 9,51,3
WV 6
RA 216,1,19456
BR 20
DW 0
M 8,38,2
WL 6
LO 17,17,126
LU 126,17
ST 116
M 8,39,3
s_add_u32 s42, s44, s64
RA 220,1,20480
HS 25,126
s_addc_u32 s43, s45, 0
LS 44,19
s_add_u32 s37, s37, 32768
BW 21
M 8,40,4
s_cmp_eq_u32 s37, s75
LL 16,64
LL 112,65
s_cselect_b32 s37, s74, s37
M 8,41,5
LA 16
XL 24,0
DW 1024
RV 44,8,6784
PK 126,17,124,33
M 9,52,4
LC_ 112
BR 21
PK 124,17,124,32
M 9,53,5
WL 6
RA 224,1,21504
PO 126
LS 48,20
WV 9
BW 22
M 10,64,4
LO 18,18,127
LU 127,18
ST 120
M 10,65,5
WV 9
DW 2048
RV 48,8,6848
HS 25,127
M 0,76,4
WL 8
RA 240,7,0
LL 17,66
LL 113,67
M 0,77,5
s_add_u32 s40, s40, 3072
LA 17
BR 22
PK 127,18,125,33
M 0,78,0
s_addc_u32 s41, s41, 0
LC_ 113
PK 125,18,125,32
s_add_u32 m0, s38, 3072
M 0,79,1
WL 7
RA 228,1,22528
XL 25,64
PO 127
LS 40,21
s_add_u32 s42, s44, s65
BW 23
M 10,66,0
RA 244,7,256
LO 112,16,112
LU 16,112
M 10,67,1
s_addc_u32 s43, s45, 0
RA 232,1,23552
BR 23
HZ 24,16
M 9,54,0
WL 6
DW 0
LL 18,68
LL 116,69
M 9,55,1
s_lshl_b32 s53, s52, 8
LA 18
HX 25
LS 44,22
s_add_u32 s48, %[alo], s53
RA 248,7,512
M 8,42,0
WL 2
LC_ 116
W1 25,1024
M 8,43,1
s_addc_u32 s49, %[ahi], 0
RA 236,1,24576
v_add_u32 v4, s39, v4
LS 48,23
s_add_u32 s52, s52, %[d]
RA 252,7,768
M 8,44,2
LO 113,17,113
LU 17,113
s_cmp_eq_u32 s52, %[wrap]
s_cselect_b32 s52, %[rst], s52
DW 1024
v_mul_f32 v36, 0xbb2aaaab, v36
M 8,45,3
XL 26,128
HS 24,17
v_mul_f32 v37, 0xbb2aaaab, v37
M 9,56,2
LL 16,70
LL 117,71
v_mul_f32 v38, 0xbb2aaaab, v38
v_mul_f32 v39, 0xbb2aaaab, v39
M 9,57,3
LA 16
DW 2048
LK 40,240,12,0
LK 44,242,12,2
RA 240,7,1024
v_add_u32 v1, s37, v0
LK 48,244,13,0
s_nop 0
CF 176,40,41
LK 248,246,13,2
RA 244,7,1280
CF 177,42,43
M 10,68,2
LC_ 117
RA 216,1,0
M 10,69,3
ST 124
v_and_b32 v22, 0xfffffcff, v1
M 0,80,2
WL 6
LO 116,18,116
LU 18,116
M 0,81,3
s_mul_i32 s53, s51, 24
RA 220,1,1024
HS 24,18
s_bitcmp1_b32 s51, 0
s_addc_u32 s53, s53, 0
s_lshl_b32 s53, s53, 11
s_bitcmp1_b64 s[90:91], s51
s_cselect_b64 s[44:45], s[84:85], s[82:83]
s_add_u32 s44, s44, s53
s_addc_u32 s45, s45, 0
s_add_u32 s51, s51, %[d]
s_cmp_eq_u32 s51, %[wrap]
s_cselect_b32 s51, %[rst], s51
DQ 3072
CF 178,44,45
M 8,46,4
LL 17,72
LL 120,73
CF 179,46,47
M 8,47,5
LA 17
RV 252,22,5120
LK 40,248,14,0
CF 180,48,49
LK 44,250,14,2
RA 248,7,1536
CF 181,50,51
GZ 12
WL 9
CF 182,248,249
CF 183,250,251
LK 48,252,15,0
CF 184,40,41
LK 248,254,15,2
RA 252,7,1792
CF 185,42,43
GS 13
WL 8
CF 186,44,45
CF 187,46,47
LK 40,240,16,0
CF 188,48,49
LK 44,242,16,2
RA 240,7,2048
CF 189,50,51
GS 14
WL 8
CF 190,248,249
CF 191,250,251
LK 48,244,17,0
CF 192,40,41
LK 248,246,17,2
RA 244,7,2304
CF 193,42,43
GS 15
CF 194,44,45
CF 195,46,47
M 9,58,4
WL 3
LC_ 120
XL 27,192
M 9,59,5
s_add_u32 s50, s50, %[d]
RA 224,1,2048
s_cmp_eq_u32 s50, %[wrap]
s_cselect_b32 s50, %[rst], s50
v_add_u32 v19, 1, v19
LK 40,248,18,0
WL 4
CF 196,48,49
LK 44,250,18,2
s_add_u32 s38, s38, 32768
RA 248,7,2560
CF 197,50,51
GS 16
s_cmp_eq_u32 s38, s77
CF 198,248,249
CF 199,250,251
LK 48,252,19,0
CF 200,40,41
LK 248,254,19,2
s_cselect_b32 s38, s76, s38
RA 252,7,2816
CF 201,42,43
GS 17
WL 5
CF 202,44,45
CF 203,46,47
LK 40,240,20,0
CF 204,48,49
LK 44,242,20,2
CF 205,50,51
GS 18
CF 206,248,249
CF 207,250,251
M 10,70,4
LO 117,16,117
LU 16,117
M 10,71,5
FP
HS 24,16
M 0,82,4
LL 18,74
LL 121,75
M 0,83,5
s_sub_u32 s39, 0, s39
LA 18
RA 228,1,3072
Z 2,36,0
LC_ 121
)"
R"(XL 28,256
M 2,37,1
WL 8
LK 48,244,21,0
CF 208,40,41
LK 248,246,21,2
CF 209,42,43
GS 19
CF 210,44,45
CF 211,46,47
Z 3,48,0
WL 7
LO 120,17,120
LU 17,120
M 3,49,1
WL 5
RA 232,1,4096
HS 24,17
LK 40,248,22,0
CF 212,48,49
LK 44,250,22,2
CF 213,50,51
GS 20
CF 214,248,249
CF 215,250,251
Z 4,60,0
LL 16,76
LL 124,77
M 4,61,1
LA 16
FR
Z 6,72,0
WL 4
LC_ 124
XL 29,320
M 6,73,1
RA 236,1,6144
LK 48,252,23,0
CF 216,40,41
LK 248,254,23,2
CF 217,42,43
GS 21
CF 218,44,45
CF 219,46,47
M 6,74,2
WL 5
LO 121,18,121
LU 18,121
M 6,75,3
RV 36,8,6912
HS 24,18
M 4,62,2
LL 17,78
LL 125,79
M 4,63,3
LA 17
RA 216,1,7168
GS 22
M 3,50,2
WL 5
LC_ 125
v_cmp_gt_u32 vcc, v19, v15
M 3,51,3
XL 30,384
CF 220,48,49
M 2,38,2
WL 4
LO 124,16,124
LU 16,124
M 2,39,3
RA 220,1,8192
HS 24,16
M 2,40,4
s_cbranch_vccnz .Lk1s11
.Lk1r11:
LL 18,88
LL 114,89
M 2,41,5
LA 18
ds_read_b64 v[14:15], v5 offset:0
M 3,52,4
LC_ 114
v_subrev_u32 v5, s39, v5
M 3,53,5
RA 224,1,9216
CF 221,50,51
M 4,64,4
WL 5
LO 125,17,125
LU 17,125
M 4,65,5
XL 31,448
HS 24,17
M 6,76,4
WL 4
LL 16,90
LL 115,91
M 6,77,5
LA 16
RA 228,1,10240
M 6,78,0
LC_ 115
HX 24
M 6,79,1
W1 24,0
CF 222,248,249
M 4,66,0
WL 5
LO 18,18,114
LU 114,18
M 4,67,1
RA 232,1,11264
HZ 25,114
M 3,54,0
LL 17,92
LL 118,93
M 3,55,1
LA 17
PK 114,18,112,33
M 2,42,0
WL 4
LC_ 118
PK 112,18,112,32
M 2,43,1
RV 252,22,5392
v_add_f32 v14, v14, v15
M 2,44,2
WL 5
LO 16,16,115
LU 115,16
M 2,45,3
RA 236,1,12288
HS 25,115
M 3,56,2
LL 18,94
LL 119,95
M 3,57,3
LA 18
PK 115,16,113,33
M 4,68,2
LC_ 119
XL 32,512
PK 113,16,113,32
v_add_f32_dpp v14, v14, v14 quad_perm:[1,0,3,2] row_mask:0xf bank_mask:0xf
M 4,69,3
s_add_u32 s42, s44, s62
RA 216,1,13312
v_add_f32_dpp v14, v14, v14 quad_perm:[2,3,0,1] row_mask:0xf bank_mask:0xf
M 6,80,2
WL 3
LO 17,17,118
LU 118,17
CF 223,250,251
M 6,81,3
s_addc_u32 s43, s45, 0
RA 220,1,14336
HS 25,118
GS 23
ST 112
v_add_f32_dpp v14, v14, v14 row_half_mirror row_mask:0xf bank_mask:0xf
s_nop 5
v_cndmask_b32 v52, v52, v53, s[78:79]
M 2,46,4
LL 16,96
LL 122,97
v_cndmask_b32 v54, v54, v55, s[78:79]
M 2,47,5
LA 16
PK 118,17,116,33
v_cndmask_b32 v52, v52, v54, s[80:81]
M 3,58,4
LC_ 122
PK 116,17,116,32
v_cvt_f64_f32 v[14:15], v14
v_mul_f32 v52, 0x3b2aaaab, v52
M 3,59,5
RA 224,1,15360
PO 118
v_med3_f32 v53, v52, v39, v37
M 4,70,4
WL 5
LO 18,18,119
LU 119,18
s_mov_b64 exec, s[54:55]
SP
s_mov_b64 exec, -1
XL 33,576
LG 52,53
s_nop 0
C 52,52,52
M 4,71,5
s_add_u32 s42, s44, s63
HS 25,119
v_lshlrev_b32 v52, 16, v52
M 6,82,4
WL 3
LL 17,98
LL 123,99
v_add_f32 v52, v36, v52
M 6,83,5
s_addc_u32 s43, s45, 0
LA 17
PK 119,18,117,33
C 52,52,52
Z 8,36,0
LC_ 123
PK 117,18,117,32
v_lshlrev_b32 v52, 16, v52
M 8,37,1
RA 228,1,16384
PO 119
v_sqrt_f32 v52, v52
s_nop 0
C 52,52,52
Z 9,48,0
WL 4
LO 16,16,122
LU 122,16
v_lshlrev_b32 v52, 16, v52
M 9,49,1
RA 232,1,17408
HS 25,122
v_rcp_f32 v52, v52
Z 10,60,0
XL 34,640
LL 18,56
LL 126,57
v_mul_f32 v52, v38, v52
M 10,61,1
LA 18
PK 122,16,120,33
CF 52,52,52
Z 11,72,0
WL 2
LC_ 126
PK 120,16,120,32
ds_bpermute_b32 v13, v28, v52
M 11,73,1
RA 236,1,18432
M 11,74,2
LO 17,17,123
LU 123,17
M 11,75,3
ST 116
HS 25,123
M 10,62,2
s_mul_i32 s53, s50, 25600
LL 16,58
LL 127,59
s_add_u32 s40, %[wlo], s53
s_addc_u32 s41, %[whi], 0
s_mov_b32 m0, s38
M 10,63,3
LA 16
PK 123,17,121,33
M 9,50,2
LC_ 127
PK 121,17,121,32
M 9,51,3
RA 216,1,19456
XL 35,704
DW 0
M 8,38,2
WL 3
LO 18,18,126
LU 126,18
M 8,39,3
s_add_u32 s42, s44, s64
RA 220,1,20480
HS 25,126
s_addc_u32 s43, s45, 0
XS 176
M 8,40,4
s_add_u32 s37, s37, 32768
LL 17,64
LL 112,65
s_cmp_eq_u32 s37, s75
s_cselect_b32 s37, s74, s37
M 8,41,5
LA 17
DW 1024
PK 126,18,124,33
XS 177
M 9,52,4
LC_ 112
PK 124,18,124,32
M 9,53,5
RA 224,1,21504
PO 126
XS 178
M 10,64,4
WL 5
LO 16,16,127
LU 127,16
ST 120
M 10,65,5
DW 2048
HS 25,127
XS 179
M 11,76,4
WL 3
LL 18,66
LL 113,67
M 11,77,5
s_add_u32 s40, s40, 3072
LA 18
PK 127,16,125,33
XS 180
M 11,78,0
s_addc_u32 s41, s41, 0
LC_ 113
PK 125,16,125,32
s_add_u32 m0, s38, 3072
M 11,79,1
s_add_u32 s42, s44, s65
RA 228,1,22528
PO 127
XS 181
M 10,66,0
WL 4
LO 112,17,112
LU 17,112
M 10,67,1
s_addc_u32 s43, s45, 0
RA 232,1,23552
HZ 24,17
XS 182
M 9,54,0
DW 0
LL 16,68
LL 116,69
M 9,55,1
s_lshl_b32 s53, s52, 8
LA 16
HX 25
XS 183
M 8,42,0
WL 2
LC_ 116
W1 25,1024
M 8,43,1
s_add_u32 s48, %[alo], s53
RA 236,1,24576
v_add_u32 v4, s39, v4
v_min_u32 v35, v35, v13
M 8,44,2
LO 113,18,113
LU 18,113
s_addc_u32 s49, %[ahi], 0
s_add_u32 s52, s52, %[d]
s_cmp_eq_u32 s52, %[wrap]
s_cselect_b32 s52, %[rst], s52
DW 1024
M 8,45,3
ST 124
HS 24,18
s_bitcmp1_b64 s[92:93], 48
s_cselect_b32 s58, 16, 0
s_lshr_b32 s58, 0xda43cb52, s58
v_and_b32 v11, 0x1f0, v0
v_min_u32 v11, 368, v11
v_lshlrev_b32 v11, 3, v11
XS 184
M 9,56,2
s_mul_i32 s53, s51, 24
LL 17,70
LL 117,71
s_bitcmp1_b32 s51, 0
s_addc_u32 s53, s53, 0
s_lshl_b32 s53, s53, 11
s_bitcmp1_b64 s[90:91], s51
s_cselect_b64 s[44:45], s[84:85], s[82:83]
s_add_u32 s44, s44, s53
s_addc_u32 s45, s45, 0
s_add_u32 s51, s51, %[d]
s_cmp_eq_u32 s51, %[wrap]
s_cselect_b32 s51, %[rst], s51
M 9,57,3
LA 17
DW 2048
XS 185
M 10,68,2
LC_ 117
DQ 3072
M 10,69,3
s_add_u32 s50, s50, %[d]
v_add_u32 v1, s37, v0
XS 186
M 11,80,2
WL 2
LO 116,16,116
LU 16,116
M 11,81,3
s_cmp_eq_u32 s50, %[wrap]
RA 216,1,0
HS 24,16
XS 187
M 8,46,4
s_cselect_b32 s50, %[rst], s50
LL 18,72
)"
R"(LL 120,73
M 8,47,5
LA 18
v_and_b32 v22, 0xfffffcff, v1
XS 188
M 9,58,4
LC_ 120
RA 220,1,1024
M 9,59,5
RV 252,22,5120
v_add_u32 v19, 1, v19
XS 189
M 10,70,4
WL 5
LO 117,17,117
LU 17,117
M 10,71,5
s_add_u32 s38, s38, 32768
RA 224,1,2048
HS 24,17
XS 190
M 11,82,4
WL 1
LL 16,74
LL 121,75
XS 191
M 11,83,5
s_cmp_eq_u32 s38, s77
LA 16
RA 228,1,3072
s_cselect_b32 s38, s76, s38
FP
s_sub_u32 s39, 0, s39
Z 0,0,0
LC_ 121
XS 192
M 0,1,1
XS 193
XS 194
Z 1,12,0
LO 120,18,120
LU 18,120
M 1,13,1
RA 232,1,4096
HS 24,18
Z 2,36,0
LL 17,80
LL 124,81
M 2,37,1
LA 17
XS 195
Z 3,48,0
LC_ 124
XS 196
M 3,49,1
XS 197
XS 198
Z 4,60,0
WL 3
LO 121,16,121
LU 16,121
M 4,61,1
RA 236,1,6144
HS 24,16
Z 5,72,0
LL 18,82
LL 125,83
M 5,73,1
LA 18
XS 199
M 5,74,2
LC_ 125
XS 200
M 5,75,3
XS 201
XS 202
M 4,62,2
WL 3
LO 124,17,124
LU 17,124
M 4,63,3
RA 216,1,7168
HS 24,17
M 3,50,2
LL 16,88
LL 114,89
M 3,51,3
LA 16
FR
M 2,38,2
LC_ 114
v_lshrrev_b32 v3, 9, v0
M 2,39,3
WV 31
BW 24
M 1,14,2
WL 5
LO 125,18,125
LU 18,125
M 1,15,3
RA 220,1,8192
HS 24,18
M 0,2,2
LL 17,90
LL 115,91
XS 203
M 0,3,3
LA 17
RV 40,8,6144
M 0,4,4
LC_ 115
RV 44,8,6208
XS 204
M 0,5,5
RV 48,8,6272
BR 24
M 1,16,4
WL 8
LO 16,16,114
LU 114,16
XS 205
M 1,17,5
RA 224,1,9216
HZ 25,114
M 2,40,4
LL 18,92
LL 118,93
XS 206
M 2,41,5
LA 18
PK 114,16,112,33
XS 207
M 3,52,4
LC_ 118
PK 112,16,112,32
v_lshl_add_u32 v3, v3, 2, 0
v_bfe_u32 v3, s58, v3, 4
v_mad_u32_u24 v3, v3, s97, v11
M 3,53,5
s_mov_b32 m0, %[pm]
s_nop 0
DF
v_cmp_gt_u32 vcc, v19, v15
XS 208
M 4,64,4
WL 6
LO 17,17,115
LU 115,17
M 4,65,5
s_add_u32 s42, s44, s62
RA 228,1,10240
HS 25,115
XS 209
M 5,76,4
LL 16,94
LL 119,95
M 5,77,5
s_cbranch_vccnz .Lk1s12
.Lk1r12:
LA 16
PK 115,17,113,33
XS 210
M 5,78,0
s_addc_u32 s43, s45, 0
LC_ 119
PK 113,17,113,32
M 5,79,1
ST 112
HX 24
XS 211
M 4,66,0
WL 3
LO 18,18,118
LU 118,18
M 4,67,1
RA 232,1,11264
HS 25,118
XS 212
M 3,54,0
LL 17,96
LL 122,97
M 3,55,1
LA 17
PK 118,18,116,33
XS 213
M 2,42,0
LC_ 122
PK 116,18,116,32
M 2,43,1
RV 252,22,5392
PO 118
XS 214
M 1,18,0
WL 4
LO 16,16,119
LU 119,16
M 1,19,1
s_add_u32 s42, s44, s63
RA 236,1,12288
HS 25,119
XS 215
M 0,6,0
LL 18,98
LL 123,99
M 0,7,1
s_addc_u32 s43, s45, 0
LA 18
PK 119,16,117,33
XS 216
M 0,8,2
LC_ 123
PK 117,16,117,32
M 0,9,3
W1 24,0
PO 119
XS 217
M 1,20,2
WL 5
LO 17,17,122
LU 122,17
M 1,21,3
RA 216,1,13312
HS 25,122
XS 218
M 2,44,2
LL 16,100
LL 126,101
M 2,45,3
LA 16
PK 122,17,120,33
XS 219
M 3,56,2
LC_ 126
PK 120,17,120,32
M 3,57,3
WV 29
ST 116
XS 220
BW 25
M 4,68,2
WL 5
LO 18,18,123
LU 123,18
M 4,69,3
RA 220,1,14336
BR 25
HS 25,123
XS 221
LZ 40,24
WV 27
BW 26
M 5,80,2
LL 17,102
LL 127,103
M 5,81,3
LA 17
PK 123,18,121,33
XS 222
M 0,10,4
LC_ 127
PK 121,18,121,32
XS 223
M 0,11,5
WV 23
DY
RV 40,8,6336
M 1,22,4
WL 4
BR 26
LO 16,16,126
LU 126,16
M 1,23,5
s_add_u32 s42, s44, s64
RA 224,1,15360
HS 25,126
LS 44,25
s_addc_u32 s43, s45, 0
BW 27
M 2,46,4
LL 18,56
LL 104,57
M 2,47,5
LA 18
PK 126,16,124,33
M 3,58,4
LC_ 104
RV 44,8,6400
PK 124,16,124,32
M 3,59,5
WV 23
ST 120
BR 27
PO 126
M 4,70,4
WL 6
LO 17,17,127
LU 127,17
M 4,71,5
s_add_u32 s42, s44, s65
RA 228,1,16384
HS 25,127
LS 48,26
s_addc_u32 s43, s45, 0
BW 28
M 5,82,4
LL 16,58
LL 105,59
M 5,83,5
LA 16
RV 48,8,6464
PK 127,17,125,33
Z 6,0,0
LC_ 105
PK 125,17,125,32
M 6,1,1
ds_read_b64 v[14:15], v5 offset:0
BR 28
PO 127
Z 7,12,0
WL 9
LO 104,18,104
LU 18,104
M 7,13,1
WL 7
RA 232,1,17408
HZ 26,18
LS 40,27
WV 23
BW 29
Z 8,36,0
WV 22
LL 17,60
LL 108,61
M 8,37,1
WL 2
LA 17
ST 124
RV 40,8,6528
Z 9,48,0
s_mul_i32 s53, s50, 25600
LC_ 108
BR 29
M 9,49,1
s_add_u32 s40, %[wlo], s53
v_subrev_u32 v5, s39, v5
HX 25
LS 44,28
s_addc_u32 s41, %[whi], 0
BW 30
Z 10,60,0
LO 105,16,105
LU 16,105
M 10,61,1
s_mov_b32 m0, s38
RA 236,1,18432
HS 26,16
Z 11,72,0
RV 44,8,6592
LL 18,62
LL 109,63
M 11,73,1
LA 18
W1 25,1024
BR 30
M 11,74,2
LC_ 109
DW 0
M 11,75,3
WL 7
v_add_f32 v14, v14, v15
v_add_u32 v4, s39, v4
LS 48,29
WV 23
BW 31
M 10,62,2
LO 108,17,108
LU 17,108
M 10,63,3
s_add_u32 s37, s37, 32768
RA 216,1,19456
HS 26,17
M 9,50,2
s_cmp_eq_u32 s37, s75
RV 48,8,6656
LL 16,64
LL 112,65
M 9,51,3
s_cselect_b32 s37, s74, s37
LA 16
DW 1024
BR 31
M 8,38,2
LC_ 112
v_add_f32_dpp v14, v14, v14 quad_perm:[1,0,3,2] row_mask:0xf bank_mask:0xf
M 8,39,3
WL 7
v_add_f32_dpp v14, v14, v14 quad_perm:[2,3,0,1] row_mask:0xf bank_mask:0xf
v_lshrrev_b32 v3, 9, v0
LS 40,30
WV 23
)"
R"(BW 32
M 7,14,2
WL 7
LO 109,18,109
LU 18,109
M 7,15,3
RA 220,1,20480
RV 40,8,6720
HS 26,18
M 6,2,2
BR 32
LL 17,66
LL 113,67
M 6,3,3
LA 17
v_add_f32_dpp v14, v14, v14 row_half_mirror row_mask:0xf bank_mask:0xf
M 6,4,4
WL 6
LC_ 113
v_cvt_f64_f32 v[14:15], v14
M 6,5,5
s_mov_b64 exec, s[54:55]
SP
s_mov_b64 exec, -1
HX 26
LS 44,31
WV 21
BW 33
M 7,16,4
WL 7
RV 44,8,6784
LO 112,16,112
LU 16,112
M 7,17,5
RA 224,1,21504
HZ 24,16
M 8,40,4
BR 33
LL 18,68
LL 116,69
M 8,41,5
WL 6
LA 18
v_lshl_add_u32 v3, v3, 2, 8
LS 48,32
WV 20
BW 34
M 9,52,4
LC_ 116
RV 48,8,6848
M 9,53,5
RA 240,7,0
BR 34
M 10,64,4
WL 10
LO 113,17,113
LU 17,113
M 10,65,5
RA 228,1,22528
HS 24,17
v_bfe_u32 v3, s58, v3, 4
v_mad_u32_u24 v3, v3, s97, v11
M 11,76,4
WV 18
LL 16,70
LL 117,71
M 11,77,5
WL 7
LA 16
DW 2048
M 11,78,0
s_mul_i32 s53, s51, 24
LC_ 117
M 11,79,1
s_bitcmp1_b32 s51, 0
LS 40,33
s_addc_u32 s53, s53, 0
BW 35
M 10,66,0
WL 4
RA 244,7,256
LO 116,18,116
LU 18,116
M 10,67,1
s_lshl_b32 s53, s53, 11
RA 232,1,23552
HS 24,18
M 9,54,0
s_bitcmp1_b64 s[90:91], s51
BR 35
LL 17,72
LL 120,73
M 9,55,1
s_cselect_b64 s[44:45], s[84:85], s[82:83]
LA 17
s_add_u32 s44, s44, s53
s_addc_u32 s45, s45, 0
s_bitcmp1_b64 s[92:93], s51
s_cselect_b64 s[94:95], s[88:89], s[86:87]
s_add_u32 s94, s94, s53
s_addc_u32 s95, s95, 0
s_add_u32 s51, s51, %[d]
s_cmp_eq_u32 s51, %[wrap]
s_cselect_b32 s51, %[rst], s51
s_mul_i32 s53, %[d], 0
s_add_u32 s60, s53, %[sb1]
s_add_u32 s61, s60, 512
LS 44,34
s_add_u32 s40, s40, 3072
RA 248,7,512
M 8,42,0
s_addc_u32 s41, s41, 0
LC_ 120
s_add_u32 m0, s38, 3072
M 8,43,1
DW 0
M 7,18,0
WL 7
LO 117,16,117
LU 16,117
M 7,19,1
WL 3
RA 236,1,24576
HS 24,16
LS 48,35
RA 252,7,768
M 6,6,0
LL 18,74
LL 121,75
s_nop 3
v_mul_f32 v36, 0xbb2aaaab, v36
M 6,7,1
LA 18
DW 1024
v_mul_f32 v37, 0xbb2aaaab, v37
v_mul_f32 v38, 0xbb2aaaab, v38
M 6,8,2
LC_ 121
DW 2048
v_mul_f32 v39, 0xbb2aaaab, v39
M 6,9,3
s_lshl_b32 s53, s52, 8
v_add_u32 v1, s37, v0
LK 40,240,24,0
LK 44,242,24,2
s_add_u32 s48, %[alo], s53
RA 240,7,1024
LK 48,244,25,0
s_nop 0
CF 224,40,41
LK 248,246,25,2
s_addc_u32 s49, %[ahi], 0
RA 244,7,1280
CF 225,42,43
M 7,20,2
WL 6
LO 120,17,120
LU 17,120
M 7,21,3
s_add_u32 s52, s52, %[d]
RA 216,1,0
HS 24,17
M 8,44,2
s_cmp_eq_u32 s52, %[wrap]
LL 16,76
LL 124,77
CF 226,44,45
M 8,45,3
s_cselect_b32 s52, %[rst], s52
LA 16
DQ 3072
CF 227,46,47
LK 40,248,26,0
CF 228,48,49
LK 44,250,26,2
RA 248,7,1536
CF 229,50,51
GZ 24
WL 7
CF 230,248,249
CF 231,250,251
LK 48,252,27,0
CF 232,40,41
LK 248,254,27,2
RA 252,7,1792
CF 233,42,43
GS 25
WL 5
CF 234,44,45
CF 235,46,47
LK 40,240,28,0
CF 236,48,49
LK 44,242,28,2
RA 240,7,2048
CF 237,50,51
GS 26
WL 5
CF 238,248,249
CF 239,250,251
LK 48,244,29,0
CF 240,40,41
LK 248,246,29,2
RA 244,7,2304
CF 241,42,43
GS 27
CF 242,44,45
CF 243,46,47
M 9,56,2
LC_ 124
v_and_b32 v22, 0xfffffcff, v1
M 9,57,3
WL 4
LK 40,248,30,0
CF 244,48,49
LK 44,250,30,2
RA 248,7,2560
CF 245,50,51
GS 28
WL 4
CF 246,248,249
CF 247,250,251
LK 48,252,31,0
CF 40,40,41
LK 248,254,31,2
RA 252,7,2816
CF 42,42,43
GS 29
CF 44,44,45
CF 46,46,47
M 10,68,2
LO 121,18,121
LU 18,121
M 10,69,3
RA 220,1,1024
HS 24,18
M 11,80,2
LL 17,78
LL 125,79
M 11,81,3
LA 17
RV 252,22,5120
M 6,10,4
LC_ 125
DY
M 6,11,5
s_add_u32 s50, s50, %[d]
AW 0,40
AW 1,42
M 7,22,4
WL 6
LO 124,16,124
LU 16,124
M 7,23,5
s_cmp_eq_u32 s50, %[wrap]
RA 224,1,2048
HS 24,16
M 8,46,4
s_cselect_b32 s50, %[rst], s50
LL 18,80
LL 106,81
M 8,47,5
LA 18
v_add_u32 v19, 1, v19
M 9,58,4
s_add_u32 s38, s38, 32768
LC_ 106
AW 2,44
M 9,59,5
AW 3,46
LK 40,240,32,0
CF 48,48,49
LK 44,242,32,2
CF 50,50,51
GS 30
CF 248,248,249
CF 250,250,251
M 10,70,4
WL 3
LO 125,17,125
LU 17,125
M 10,71,5
RA 228,1,3072
HS 24,17
M 11,82,4
LL 16,82
LL 107,83
M 11,83,5
LA 16
HX 24
Z 0,0,0
s_cmp_eq_u32 s38, s77
LC_ 107
AW 4,48
M 0,1,1
s_cselect_b32 s38, s76, s38
FP
AW 5,50
Z 1,12,0
WL 4
LO 18,18,106
LU 106,18
M 1,13,1
s_sub_u32 s39, 0, s39
RA 232,1,4096
HZ 27,106
Z 2,36,0
LL 17,84
LL 110,85
M 2,37,1
LA 17
PK 106,18,104,33
Z 3,48,0
LC_ 110
PK 104,18,104,32
M 3,49,1
AW 6,248
AW 7,250
LK 48,244,33,0
CF 40,40,41
LK 248,246,33,2
CF 42,42,43
GS 31
CF 44,44,45
CF 46,46,47
Z 4,60,0
WL 4
LO 16,16,107
LU 107,16
M 4,61,1
s_add_u32 s42, s94, s60
RA 236,1,6144
HS 27,107
Z 5,72,0
LL 18,86
LL 111,87
M 5,73,1
s_addc_u32 s43, s95, 0
LA 18
PK 107,16,105,33
M 5,74,2
LC_ 111
PK 105,16,105,32
M 5,75,3
SU 104
AW 8,40
M 4,62,2
WL 3
LO 17,17,110
LU 110,17
M 4,63,3
RA 216,1,7168
HS 27,110
M 3,50,2
LL 16,88
LL 114,89
M 3,51,3
LA 16
PK 110,17,108,33
AW 9,42
AW 10,44
M 2,38,2
LC_ 114
PK 108,17,108,32
AW 11,46
M 2,39,3
FR
PO 110
LK 40,248,34,0
CF 48,48,49
LK 44,250,34,2
CF 50,50,51
GS 32
CF 248,248,249
CF 250,250,251
M 1,14,2
WL 4
LO 18,18,111
LU 111,18
M 1,15,3
s_add_u32 s42, s94, s61
RA 220,1,8192
HS 27,111
M 0,2,2
LL 17,90
LL 115,91
AW 12,48
AW 13,50
M 0,3,3
s_addc_u32 s43, s95, 0
LA 17
PK 111,18,109,33
AW 14,248
AW 15,250
LK 48,252,35,0
CF 40,40,41
LK 248,254,35,2
s_mov_b32 m0, %[pm]
CF 42,42,43
HX 27
GS 33
)"
R"(CF 44,44,45
CF 46,46,47
M 0,4,4
LC_ 115
PK 109,18,109,32
M 0,5,5
DF
PO 111
M 1,16,4
WL 4
LO 16,16,114
LU 114,16
M 1,17,5
RA 224,1,9216
HZ 25,114
AW 16,40
M 2,40,4
LL 18,92
LL 118,93
AW 17,42
AW 18,44
M 2,41,5
LA 18
RV 36,8,6912
PK 114,16,112,33
AW 19,46
M 3,52,4
LC_ 118
PK 112,16,112,32
M 3,53,5
WL 7
SU 108
v_cmp_gt_u32 vcc, v19, v15
GS 34
CF 48,48,49
CF 50,50,51
CF 248,248,249
M 4,64,4
WL 4
LO 17,17,115
LU 115,17
CF 250,250,251
AW 20,48
M 4,65,5
s_add_u32 s42, s44, s62
RA 228,1,10240
HS 25,115
AW 21,50
AW 22,248
M 5,76,4
LL 16,94
LL 119,95
AW 23,250
M 5,77,5
s_cbranch_vccnz .Lk1s13
.Lk1r13:
LA 16
PK 115,17,113,33
GS 35
s_addc_u32 s43, s45, 0
W1 24,0
s_nop 5
v_cndmask_b32 v52, v52, v53, s[78:79]
M 5,78,0
LC_ 119
PK 113,17,113,32
v_cndmask_b32 v54, v54, v55, s[78:79]
M 5,79,1
W1 26,2048
v_cndmask_b32 v52, v52, v54, s[80:81]
M 4,66,0
WL 5
LO 18,18,118
LU 118,18
v_mul_f32 v52, 0x3b2aaaab, v52
M 4,67,1
RA 232,1,11264
HS 25,118
v_med3_f32 v53, v52, v39, v37
M 3,54,0
LL 17,96
LL 122,97
LG 52,53
M 3,55,1
LA 17
PK 118,18,116,33
C 52,52,52
M 2,42,0
LC_ 122
PK 116,18,116,32
v_lshlrev_b32 v52, 16, v52
M 2,43,1
RV 252,22,5392
PO 118
v_add_f32 v52, v36, v52
M 1,18,0
WL 5
LO 16,16,119
LU 119,16
C 52,52,52
M 1,19,1
RA 236,1,12288
HS 25,119
M 0,6,0
LL 18,98
LL 123,99
v_lshlrev_b32 v52, 16, v52
M 0,7,1
LA 18
PK 119,16,117,33
v_sqrt_f32 v52, v52
M 0,8,2
LC_ 123
PK 117,16,117,32
C 52,52,52
M 0,9,3
ST 112
PO 119
v_lshlrev_b32 v52, 16, v52
M 1,20,2
WL 4
LO 17,17,122
LU 122,17
v_rcp_f32 v52, v52
M 1,21,3
s_add_u32 s42, s44, s63
RA 216,1,13312
HS 25,122
v_mul_f32 v52, v38, v52
M 2,44,2
s_addc_u32 s43, s45, 0
LL 16,100
LL 126,101
CF 52,52,52
M 2,45,3
LA 16
PK 122,17,120,33
ds_bpermute_b32 v13, v28, v52
M 3,56,2
LC_ 126
PK 120,17,120,32
M 3,57,3
ST 116
AR 248,0
M 4,68,2
WL 4
LO 18,18,123
LU 123,18
M 4,69,3
s_add_u32 s42, s44, s64
RA 220,1,14336
HS 25,123
M 5,80,2
LL 17,102
LL 127,103
M 5,81,3
s_addc_u32 s43, s45, 0
LA 17
PK 123,18,121,33
M 0,10,4
LC_ 127
PK 121,18,121,32
M 0,11,5
ST 120
AR 249,1
M 1,22,4
WL 3
LO 16,16,126
LU 126,16
M 1,23,5
RA 224,1,15360
HS 25,126
M 2,46,4
LL 18,56
LL 104,57
M 2,47,5
LA 18
PK 126,16,124,33
M 3,58,4
LC_ 104
PK 124,16,124,32
AR 250,2
M 3,59,5
ds_read_b64 v[14:15], v5 offset:0
PO 126
M 4,70,4
WL 4
LO 17,17,127
LU 127,17
AR 251,3
M 4,71,5
s_add_u32 s42, s44, s65
RA 228,1,16384
HS 25,127
AR 52,4
M 5,82,4
LL 16,58
LL 105,59
AR 53,5
M 5,83,5
s_addc_u32 s43, s45, 0
LA 16
PK 127,17,125,33
Z 6,0,0
LC_ 105
PK 125,17,125,32
AR 54,6
M 6,1,1
PO 127
v_subrev_u32 v5, s39, v5
AR 55,7
Z 7,12,0
WL 4
LO 104,18,104
LU 18,104
XS 224
M 7,13,1
RA 232,1,17408
HZ 26,18
Z 8,36,0
WL 4
LL 17,60
LL 108,61
XS 225
M 8,37,1
s_mul_i32 s53, s50, 25600
LA 17
v_add_f32 v14, v14, v15
XS 226
Z 9,48,0
s_add_u32 s40, %[wlo], s53
LC_ 108
v_add_f32_dpp v14, v14, v14 quad_perm:[1,0,3,2] row_mask:0xf bank_mask:0xf
M 9,49,1
s_addc_u32 s41, %[whi], 0
v_add_f32_dpp v14, v14, v14 quad_perm:[2,3,0,1] row_mask:0xf bank_mask:0xf
HX 25
XS 227
Z 10,60,0
WL 3
LO 105,16,105
LU 16,105
XS 228
M 10,61,1
s_mov_b32 m0, s38
RA 236,1,18432
HS 26,16
XS 229
Z 11,72,0
LL 18,62
LL 109,63
M 11,73,1
LA 18
W1 25,1024
XS 230
M 11,74,2
LC_ 109
W1 27,3072
XS 231
M 11,75,3
ST 124
v_add_f32_dpp v14, v14, v14 row_half_mirror row_mask:0xf bank_mask:0xf
v_min_u32 v35, v35, v13
M 10,62,2
WL 5
LO 108,17,108
LU 17,108
M 10,63,3
RA 216,1,19456
HS 26,17
XS 232
M 9,50,2
LL 16,64
LL 112,65
XS 233
M 9,51,3
LA 16
DW 0
XS 234
M 8,38,2
s_add_u32 s37, s37, 32768
LC_ 112
v_cvt_f64_f32 v[14:15], v14
M 8,39,3
s_cmp_eq_u32 s37, s75
s_mov_b64 exec, s[54:55]
SP
s_mov_b64 exec, -1
v_add_u32 v4, s39, v4
XS 235
M 7,14,2
WL 4
LO 109,18,109
LU 18,109
XS 236
M 7,15,3
s_cselect_b32 s37, s74, s37
RA 220,1,20480
HS 26,18
M 6,2,2
LL 17,66
LL 113,67
XS 237
M 6,3,3
s_mul_i32 s53, %[d], 196608
LA 17
HX 26
XS 238
M 6,4,4
s_add_u32 s53, s53, s57
LC_ 113
XS 239
M 6,5,5
s_add_u32 s46, %[xlo], s53
XS 240
XS 241
M 7,16,4
WL 3
LO 112,16,112
LU 16,112
M 7,17,5
s_addc_u32 s47, %[xhi], 0
RA 224,1,21504
HZ 24,16
M 8,40,4
s_bitcmp1_b64 s[92:93], 48
LL 18,68
LL 116,69
XS 242
M 8,41,5
s_cselect_b32 s58, 16, 0
LA 18
XS 243
M 9,52,4
s_lshr_b32 s58, 0xda43cb52, s58
LC_ 116
v_lshrrev_b32 v3, 6, v0
v_and_b32 v3, 12, v3
v_bfe_u32 v3, s58, v3, 4
v_mad_u32_u24 v3, v3, s97, v21
M 9,53,5
DW 1024
XS 244
M 10,64,4
WL 3
LO 113,17,113
LU 17,113
XS 245
M 10,65,5
RA 228,1,22528
HS 24,17
M 11,76,4
LL 16,70
LL 117,71
XS 246
M 11,77,5
LA 16
XL 36,0
XS 247
M 11,78,0
LC_ 117
DW 2048
M 11,79,1
XL 37,64
XS 248
M 10,66,0
WL 3
LO 116,18,116
LU 18,116
XS 249
M 10,67,1
s_mul_i32 s53, s51, 24
RA 232,1,23552
HS 24,18
XS 250
M 9,54,0
s_bitcmp1_b32 s51, 0
LL 17,72
LL 120,73
M 9,55,1
s_addc_u32 s53, s53, 0
LA 17
s_lshl_b32 s53, s53, 11
s_bitcmp1_b64 s[90:91], s51
s_cselect_b64 s[44:45], s[84:85], s[82:83]
s_add_u32 s44, s44, s53
s_addc_u32 s45, s45, 0
s_bitcmp1_b64 s[92:93], s51
s_cselect_b64 s[94:95], s[88:89], s[86:87]
s_add_u32 s94, s94, s53
s_addc_u32 s95, s95, 0
s_add_u32 s51, s51, %[d]
s_cmp_eq_u32 s51, %[wrap]
s_cselect_b32 s51, %[rst], s51
XS 251
M 8,42,0
s_add_u32 s40, s40, 3072
LC_ 120
s_addc_u32 s41, s41, 0
s_add_u32 m0, s38, 3072
)"
R"(AW 0,248
M 8,43,1
XL 38,128
AW 1,249
M 7,18,0
WL 3
LO 117,16,117
LU 16,117
M 7,19,1
RA 236,1,24576
HS 24,16
AW 2,250
M 6,6,0
LL 18,74
LL 121,75
AW 3,251
M 6,7,1
LA 18
DW 0
XS 52
M 6,8,2
LC_ 121
XL 39,192
M 6,9,3
DW 1024
v_add_u32 v1, s37, v0
XS 53
M 7,20,2
WL 3
LO 120,17,120
LU 17,120
XS 54
M 7,21,3
RA 216,1,0
HS 24,17
M 8,44,2
LL 16,76
LL 124,77
XS 55
M 8,45,3
LA 16
XL 40,256
AW 4,52
M 9,56,2
LC_ 124
DW 2048
AW 5,53
M 9,57,3
XL 41,320
v_and_b32 v22, 0xfffffcff, v1
M 10,68,2
WL 3
LO 121,18,121
LU 18,121
AW 6,54
M 10,69,3
s_lshl_b32 s53, s52, 8
RA 220,1,1024
HS 24,18
AW 7,55
M 11,80,2
LL 17,78
LL 125,79
AR 248,8
M 11,81,3
s_add_u32 s48, %[alo], s53
LA 17
RV 252,22,5120
M 6,10,4
s_addc_u32 s49, %[ahi], 0
LC_ 125
AR 249,9
M 6,11,5
s_add_u32 s52, s52, %[d]
AR 250,10
AR 251,11
M 7,22,4
WL 4
LO 124,16,124
LU 16,124
M 7,23,5
s_cmp_eq_u32 s52, %[wrap]
RA 224,1,2048
HS 24,16
M 8,46,4
s_cselect_b32 s52, %[rst], s52
LL 18,80
LL 106,81
XS 248
M 8,47,5
LA 18
DQ 3072
XS 249
M 9,58,4
LC_ 106
XL 42,384
M 9,59,5
s_add_u32 s50, s50, %[d]
XS 250
XS 251
M 10,70,4
WL 3
LO 125,17,125
LU 17,125
M 10,71,5
RA 228,1,3072
HS 24,17
AW 8,248
M 11,82,4
LL 16,82
LL 107,83
M 11,83,5
LA 16
HX 24
s_cmp_eq_u32 s50, %[wrap]
s_cselect_b32 s50, %[rst], s50
XL 43,448
AW 9,249
Z 0,0,0
WV 38
LC_ 107
XL 44,512
v_add_u32 v19, 1, v19
s_add_u32 s38, s38, 32768
s_cmp_eq_u32 s38, s77
s_cselect_b32 s38, s76, s38
FP
s_sub_u32 s39, 0, s39
AW 10,250
M 0,1,1
XL 45,576
AW 11,251
Z 1,12,0
WL 4
LO 18,18,106
LU 106,18
M 1,13,1
RA 232,1,4096
HZ 27,106
AR 52,12
Z 4,60,0
LL 17,84
LL 110,85
AR 53,13
M 4,61,1
LA 17
PK 106,18,104,33
AR 54,14
Z 5,72,0
WL 5
LC_ 110
XL 46,640
PK 104,18,104,32
M 5,73,1
RA 236,1,6144
AR 55,15
M 5,74,2
WL 5
LO 16,16,107
LU 107,16
XS 52
M 5,75,3
FR
HS 27,107
M 4,62,2
LL 18,86
LL 111,87
XS 53
M 4,63,3
LA 18
PK 107,16,105,33
XS 54
M 1,14,2
LC_ 111
XL 47,704
PK 105,16,105,32
XS 55
M 1,15,3
s_add_u32 s42, s94, s60
RA 216,1,7168
AW 12,52
M 0,2,2
WL 4
LO 17,17,110
LU 110,17
M 0,3,3
s_addc_u32 s43, s95, 0
RA 220,1,8192
HS 27,110
AW 13,53
M 0,4,4
LL 16,88
LL 114,89
AW 14,54
M 0,5,5
LA 16
PK 110,17,108,33
M 1,16,4
LC_ 114
PK 108,17,108,32
AW 15,55
M 1,17,5
RA 224,1,9216
PO 110
AR 248,16
M 4,64,4
WL 5
LO 18,18,111
LU 111,18
SU 104
AR 249,17
M 4,65,5
s_add_u32 s42, s94, s61
HS 27,111
v_cmp_gt_u32 vcc, v19, v15
M 5,76,4
WL 3
LL 17,90
LL 115,91
AR 250,18
M 5,77,5
s_addc_u32 s43, s95, 0
LA 17
PK 111,18,109,33
AR 251,19
M 5,78,0
s_cbranch_vccnz .Lk1s14
.Lk1r14:
LC_ 115
PK 109,18,109,32
M 5,79,1
WV 17
RA 228,1,10240
PO 111
XS 248
BW 36
M 4,66,0
WL 5
LO 16,16,114
LU 114,16
XS 249
M 4,67,1
RA 232,1,11264
RV 40,8,6144
HZ 25,114
XS 250
M 1,18,0
RV 44,8,6208
LL 18,92
LL 118,93
M 1,19,1
LA 18
RV 48,8,6272
PK 114,16,112,33
XS 251
M 0,6,0
WL 6
LC_ 118
BR 36
PK 112,16,112,32
AW 16,248
M 0,7,1
WV 15
RV 252,22,5392
ds_read_b64 v[14:15], v5 offset:0
W1 24,0
W1 26,2048
HX 27
BW 37
AW 17,249
M 0,8,2
BR 37
LO 17,17,115
LU 115,17
M 0,9,3
WL 6
RA 236,1,12288
HS 25,115
AW 18,250
LZ 40,36
WV 14
BW 38
M 1,20,2
LL 16,94
LL 119,95
AW 19,251
M 1,21,3
LA 16
RV 40,8,6336
PK 115,17,113,33
AR 52,20
M 4,68,2
LC_ 119
BR 38
PK 113,17,113,32
M 4,69,3
WL 6
RA 216,1,13312
AR 53,21
LS 44,37
WV 12
BW 39
M 5,80,2
WL 7
LO 18,18,118
LU 118,18
SU 108
AR 54,22
M 5,81,3
s_add_u32 s42, s44, s62
RA 220,1,14336
HS 25,118
s_addc_u32 s43, s45, 0
RV 44,8,6400
M 0,10,4
BR 39
LL 17,96
LL 122,97
AR 55,23
M 0,11,5
WL 5
LA 17
PK 118,18,116,33
XS 52
LS 48,38
WV 11
BW 40
M 1,22,4
LC_ 122
PK 116,18,116,32
XS 53
M 1,23,5
RA 224,1,15360
RV 48,8,6464
PO 118
M 4,70,4
LO 16,16,119
LU 119,16
ST 112
XS 54
M 4,71,5
s_add_u32 s42, s44, s63
BR 40
HS 25,119
XS 55
M 5,82,4
WL 6
LL 18,98
LL 123,99
AW 20,52
M 5,83,5
s_addc_u32 s43, s45, 0
LA 18
PK 119,16,117,33
LS 40,39
WV 10
BW 41
Z 6,0,0
LC_ 123
PK 117,16,117,32
v_add_f32 v14, v14, v15
s_nop 1
v_add_f32_dpp v14, v14, v14 quad_perm:[1,0,3,2] row_mask:0xf bank_mask:0xf
s_nop 1
v_add_f32_dpp v14, v14, v14 quad_perm:[2,3,0,1] row_mask:0xf bank_mask:0xf
s_nop 1
v_add_f32_dpp v14, v14, v14 row_half_mirror row_mask:0xf bank_mask:0xf
v_cvt_f64_f32 v[14:15], v14
s_mov_b64 exec, s[54:55]
SP
s_mov_b64 exec, -1
s_nop 0
ds_read_b64 v[14:15], v5 offset:2048
RV 40,8,6528
AW 21,53
M 6,1,1
WV 9
RA 228,1,16384
BR 41
PO 119
AW 22,54
Z 7,12,0
WL 7
LO 17,17,122
LU 122,17
AW 23,55
M 7,13,1
s_mul_i32 s53, %[d], 393216
RA 232,1,17408
HS 25,122
LS 44,40
s_add_u32 s53, s53, s57
BW 42
Z 10,60,0
s_add_u32 s46, %[xlo], s53
RV 44,8,6592
LL 16,100
LL 126,101
M 10,61,1
s_addc_u32 s47, %[xhi], 0
)"
R"(LA 16
BR 42
PK 122,17,120,33
Z 2,72,0
WL 5
LC_ 126
PK 120,17,120,32
M 2,73,1
s_bitcmp1_b64 s[92:93], 48
RA 236,1,18432
v_subrev_u32 v5, s39, v5
LS 48,41
s_cselect_b32 s58, 16, 0
WV 8
BW 43
M 2,74,2
RV 48,8,6656
LO 18,18,123
LU 123,18
M 2,75,3
s_lshr_b32 s58, 0xfe987610, s58
BR 43
HS 25,123
v_lshrrev_b32 v3, 6, v0
v_and_b32 v3, 12, v3
v_bfe_u32 v3, s58, v3, 4
v_mad_u32_u24 v3, v3, s97, v21
M 10,62,2
s_mul_i32 s53, s50, 25600
LL 17,102
LL 127,103
s_add_u32 s40, %[wlo], s53
s_addc_u32 s41, %[whi], 0
s_mov_b32 m0, s38
M 10,63,3
WL 5
LA 17
PK 123,18,121,33
LS 40,42
WV 7
BW 44
M 7,14,2
LC_ 127
RV 40,8,6720
PK 121,18,121,32
M 7,15,3
WV 6
RA 216,1,19456
BR 44
DW 0
M 6,2,2
WL 6
LO 16,16,126
LU 126,16
ST 116
M 6,3,3
s_add_u32 s42, s44, s64
RA 220,1,20480
HS 25,126
s_addc_u32 s43, s45, 0
LS 44,43
s_add_u32 s37, s37, 32768
BW 45
M 6,4,4
s_cmp_eq_u32 s37, s75
LL 18,56
LL 104,57
s_cselect_b32 s37, s74, s37
M 6,5,5
LA 18
PK 126,16,124,33
v_add_f32 v14, v14, v15
s_nop 1
v_add_f32_dpp v14, v14, v14 quad_perm:[1,0,3,2] row_mask:0xf bank_mask:0xf
s_nop 1
v_add_f32_dpp v14, v14, v14 quad_perm:[2,3,0,1] row_mask:0xf bank_mask:0xf
s_nop 1
v_add_f32_dpp v14, v14, v14 row_half_mirror row_mask:0xf bank_mask:0xf
v_cvt_f64_f32 v[14:15], v14
s_mov_b64 exec, s[54:55]
SP
s_mov_b64 exec, -1
XL 48,0
DW 1024
RV 44,8,6784
M 7,16,4
LC_ 104
BR 45
PK 124,16,124,32
M 7,17,5
WL 6
RA 224,1,21504
PO 126
LS 48,44
WV 10
BW 46
M 10,64,4
LO 17,17,127
LU 127,17
ST 120
M 10,65,5
WV 10
DW 2048
RV 48,8,6848
HS 25,127
M 2,76,4
WL 8
RA 240,7,0
LL 16,58
LL 105,59
M 2,77,5
s_add_u32 s40, s40, 3072
LA 16
BR 46
PK 127,17,125,33
M 2,78,0
s_addc_u32 s41, s41, 0
LC_ 105
PK 125,17,125,32
s_add_u32 m0, s38, 3072
M 2,79,1
WL 7
RA 228,1,22528
XL 49,64
PO 127
LS 40,45
s_add_u32 s42, s44, s65
BW 47
M 10,66,0
RA 244,7,256
LO 104,18,104
LU 18,104
M 10,67,1
s_addc_u32 s43, s45, 0
RA 232,1,23552
BR 47
HZ 26,18
M 7,18,0
WL 6
DW 0
LL 17,60
LL 108,61
M 7,19,1
s_lshl_b32 s53, s52, 8
LA 17
HX 25
LS 44,46
s_add_u32 s48, %[alo], s53
RA 248,7,512
M 6,6,0
WL 2
LC_ 108
W1 25,1024
W1 27,3072
M 6,7,1
s_addc_u32 s49, %[ahi], 0
RA 236,1,24576
v_add_u32 v4, s39, v4
LS 48,47
s_add_u32 s52, s52, %[d]
RA 252,7,768
M 6,8,2
LO 105,16,105
LU 16,105
s_cmp_eq_u32 s52, %[wrap]
s_cselect_b32 s52, %[rst], s52
DW 1024
v_mul_f32 v36, 0xbb2aaaab, v36
M 6,9,3
XL 50,128
HS 26,16
v_mul_f32 v37, 0xbb2aaaab, v37
v_mul_f32 v38, 0xbb2aaaab, v38
M 7,20,2
LL 18,62
LL 109,63
v_mul_f32 v39, 0xbb2aaaab, v39
M 7,21,3
LA 18
DW 2048
LK 40,240,36,0
LK 44,242,36,2
RA 240,7,1024
v_add_u32 v1, s37, v0
LK 48,244,37,0
s_nop 0
CF 40,40,41
LK 248,246,37,2
RA 244,7,1280
CF 42,42,43
M 10,68,2
LC_ 109
RA 216,1,0
M 10,69,3
ST 124
v_and_b32 v22, 0xfffffcff, v1
CF 44,44,45
CF 46,46,47
M 2,80,2
WL 6
LO 108,17,108
LU 17,108
AW 24,40
AW 25,42
M 2,81,3
s_mul_i32 s53, s51, 24
RA 220,1,1024
HS 26,17
s_bitcmp1_b32 s51, 0
s_addc_u32 s53, s53, 0
s_lshl_b32 s53, s53, 11
s_bitcmp1_b64 s[90:91], s51
s_cselect_b64 s[44:45], s[84:85], s[82:83]
s_add_u32 s44, s44, s53
s_addc_u32 s45, s45, 0
s_bitcmp1_b64 s[92:93], s51
s_cselect_b64 s[94:95], s[88:89], s[86:87]
s_add_u32 s94, s94, s53
s_addc_u32 s95, s95, 0
s_add_u32 s51, s51, %[d]
s_cmp_eq_u32 s51, %[wrap]
s_cselect_b32 s51, %[rst], s51
DQ 3072
AW 26,44
AW 27,46
LK 40,248,38,0
CF 48,48,49
LK 44,250,38,2
RA 248,7,1536
CF 50,50,51
GZ 36
CF 248,248,249
CF 250,250,251
M 6,10,4
LL 16,72
LL 120,73
M 6,11,5
LA 16
RV 252,22,5120
M 7,22,4
WL 4
LC_ 120
XL 51,192
AW 28,48
M 7,23,5
s_add_u32 s50, s50, %[d]
RA 224,1,2048
s_cmp_eq_u32 s50, %[wrap]
s_cselect_b32 s50, %[rst], s50
v_add_u32 v19, 1, v19
AW 29,50
AW 30,248
AW 31,250
LK 48,252,39,0
CF 40,40,41
LK 248,254,39,2
s_add_u32 s38, s38, 32768
RA 252,7,1792
CF 42,42,43
GS 37
s_cmp_eq_u32 s38, s77
CF 44,44,45
CF 46,46,47
M 10,70,4
LO 109,18,109
LU 18,109
M 10,71,5
s_cselect_b32 s38, s76, s38
FP
HS 26,18
M 2,82,4
WL 4
LL 17,74
LL 121,75
M 2,83,5
s_sub_u32 s39, 0, s39
LA 17
RA 228,1,3072
AW 32,40
AW 33,42
Z 0,0,0
LC_ 121
XL 52,256
AW 34,44
AW 35,46
M 0,1,1
HX 26
LK 40,240,40,0
CF 48,48,49
LK 44,242,40,2
RA 240,7,2048
CF 50,50,51
GS 38
CF 248,248,249
CF 250,250,251
Z 1,12,0
WL 7
LO 120,16,120
LU 16,120
M 1,13,1
RA 232,1,4096
HZ 24,16
AW 36,48
Z 4,60,0
LL 18,76
LL 124,77
AW 37,50
AW 38,248
M 4,61,1
LA 18
FR
AW 39,250
LK 48,244,41,0
CF 40,40,41
LK 248,246,41,2
RA 244,7,2304
CF 42,42,43
GS 39
CF 44,44,45
CF 46,46,47
Z 8,72,0
WL 6
LC_ 124
XL 53,320
M 8,73,1
RA 236,1,6144
AW 40,40
M 8,74,2
WL 7
LO 121,17,121
LU 17,121
AW 41,42
M 8,75,3
HS 24,17
AW 42,44
AW 43,46
LK 40,248,42,0
CF 48,48,49
LK 44,250,42,2
RA 248,7,2560
CF 50,50,51
GS 40
CF 248,248,249
CF 250,250,251
M 4,62,2
LL 16,78
LL 125,79
M 4,63,3
LA 16
RA 216,1,7168
M 1,14,2
WL 6
LC_ 125
v_cmp_gt_u32 vcc, v19, v15
M 1,15,3
XL 54,384
AW 44,48
AW 45,50
AW 46,248
M 0,2,2
WL 4
LO 124,18,124
LU 18,124
AW 47,250
M 0,3,3
RA 220,1,8192
HS 24,18
LK 48,252,43,0
CF 40,40,41
LK 248,254,43,2
RA 252,7,2816
CF 42,42,43
GS 41
CF 44,44,45
CF 46,46,47
M 0,4,4
s_cbranch_vccnz .Lk1s15
.Lk1r15:
LL 17,80
LL 106,81
M 0,5,5
LA 17
ds_read_b64 v[14:15], v5 offset:0
AW 48,40
AW 49,42
M 1,16,4
LC_ 106
AW 50,44
AW 51,46
M 1,17,5
RA 224,1,9216
LK 40,240,44,0
CF 48,48,49
LK 44,242,44,2
CF 50,50,51
GS 42
CF 248,248,249
CF 250,250,251
M 4,64,4
WL 6
)"
R"(LO 125,16,125
LU 16,125
M 4,65,5
XL 55,448
HS 24,16
AW 52,48
M 8,76,4
WL 5
LL 18,82
LL 107,83
AW 53,50
AW 54,248
AW 55,250
M 8,77,5
LA 18
RA 228,1,10240
LK 48,244,45,0
CF 40,40,41
LK 248,246,45,2
CF 42,42,43
HX 24
GS 43
CF 44,44,45
CF 46,46,47
M 8,78,0
LC_ 107
W1 24,0
M 8,79,1
W1 26,2048
AW 56,40
M 4,66,0
WL 6
LO 17,17,106
LU 106,17
AW 57,42
AW 58,44
M 4,67,1
RA 232,1,11264
HZ 27,106
AW 59,46
LK 40,248,46,0
CF 48,48,49
LK 44,250,46,2
CF 50,50,51
v_add_f32 v14, v14, v15
GS 44
CF 248,248,249
CF 250,250,251
M 1,18,0
LL 16,84
LL 110,85
M 1,19,1
LA 16
PK 106,17,104,33
M 0,6,0
WL 5
LC_ 110
PK 104,17,104,32
AW 60,48
AW 61,50
AW 62,248
M 0,7,1
RV 252,22,5392
v_add_f32_dpp v14, v14, v14 quad_perm:[1,0,3,2] row_mask:0xf bank_mask:0xf
AW 63,250
LK 48,252,47,0
CF 40,40,41
LK 248,254,47,2
CF 42,42,43
v_add_f32_dpp v14, v14, v14 quad_perm:[2,3,0,1] row_mask:0xf bank_mask:0xf
GS 45
CF 44,44,45
CF 46,46,47
M 0,8,2
WL 6
LO 18,18,107
LU 107,18
M 0,9,3
RA 236,1,12288
HS 27,107
M 1,20,2
LL 17,86
LL 111,87
AW 64,40
AW 65,42
M 1,21,3
LA 17
RV 36,8,6912
PK 107,18,105,33
AW 66,44
AW 67,46
M 4,68,2
LC_ 111
XL 56,512
PK 105,18,105,32
M 4,69,3
s_add_u32 s42, s94, s60
RA 216,1,13312
v_add_f32_dpp v14, v14, v14 row_half_mirror row_mask:0xf bank_mask:0xf
GS 46
s_addc_u32 s43, s95, 0
CF 48,48,49
CF 50,50,51
CF 248,248,249
CF 250,250,251
M 8,80,2
WL 4
LO 16,16,110
LU 110,16
AW 68,48
AW 69,50
AW 70,248
M 8,81,3
RA 220,1,14336
HS 27,110
AW 71,250
s_nop 1
GS 47
SU 104
v_cvt_f64_f32 v[14:15], v14
s_nop 5
v_cndmask_b32 v52, v52, v53, s[78:79]
M 0,10,4
LL 18,96
LL 122,97
v_cndmask_b32 v54, v54, v55, s[78:79]
M 0,11,5
LA 18
PK 110,16,108,33
v_cndmask_b32 v52, v52, v54, s[80:81]
M 1,22,4
LC_ 122
PK 108,16,108,32
v_mul_f32 v52, 0x3b2aaaab, v52
M 1,23,5
WL 5
RA 224,1,15360
PO 110
v_med3_f32 v53, v52, v39, v37
M 4,70,4
WL 5
LO 17,17,111
LU 111,17
s_mov_b64 exec, s[54:55]
SP
s_mov_b64 exec, -1
XL 57,576
LG 52,53
s_nop 0
C 52,52,52
M 4,71,5
s_add_u32 s42, s94, s61
ds_read_b64 v[14:15], v5 offset:2048
HS 27,111
v_lshlrev_b32 v52, 16, v52
M 8,82,4
WL 4
LL 16,98
LL 123,99
v_add_f32 v52, v36, v52
M 8,83,5
s_addc_u32 s43, s95, 0
LA 16
PK 111,17,109,33
C 52,52,52
Z 6,0,0
LC_ 123
PK 109,17,109,32
v_lshlrev_b32 v52, 16, v52
M 6,1,1
RA 228,1,16384
PO 111
v_sqrt_f32 v52, v52
s_nop 0
C 52,52,52
Z 7,12,0
WL 5
LO 18,18,122
LU 122,18
v_lshlrev_b32 v52, 16, v52
M 7,13,1
RA 232,1,17408
HZ 25,122
v_rcp_f32 v52, v52
Z 10,60,0
XL 58,640
LL 17,64
LL 126,65
v_mul_f32 v52, v38, v52
M 10,61,1
LA 17
PK 122,18,120,33
v_subrev_u32 v5, s39, v5
HX 27
CF 52,52,52
Z 11,72,0
WL 2
LC_ 126
PK 120,18,120,32
ds_bpermute_b32 v13, v28, v52
M 11,73,1
RA 236,1,18432
v_add_f32 v14, v14, v15
M 11,74,2
LO 16,16,123
LU 123,16
M 11,75,3
SU 108
HS 25,123
v_add_f32_dpp v14, v14, v14 quad_perm:[1,0,3,2] row_mask:0xf bank_mask:0xf
M 10,62,2
s_mul_i32 s53, s50, 25600
LL 18,66
LL 127,67
s_add_u32 s40, %[wlo], s53
s_addc_u32 s41, %[whi], 0
s_mov_b32 m0, s38
M 10,63,3
LA 18
PK 123,16,121,33
v_add_f32_dpp v14, v14, v14 quad_perm:[2,3,0,1] row_mask:0xf bank_mask:0xf
M 7,14,2
LC_ 127
PK 121,16,121,32
M 7,15,3
RA 216,1,19456
XL 59,704
DW 0
v_add_f32_dpp v14, v14, v14 row_half_mirror row_mask:0xf bank_mask:0xf
M 6,2,2
WL 3
LO 17,17,126
LU 126,17
M 6,3,3
s_add_u32 s42, s44, s64
RA 220,1,20480
HS 25,126
v_cvt_f64_f32 v[14:15], v14
s_addc_u32 s43, s45, 0
AR 248,24
M 6,4,4
s_add_u32 s37, s37, 32768
LL 16,56
LL 104,57
s_cmp_eq_u32 s37, s75
s_cselect_b32 s37, s74, s37
AR 249,25
M 6,5,5
LA 16
PK 126,17,124,33
s_mov_b64 exec, s[54:55]
SP
s_mov_b64 exec, -1
DW 1024
AR 250,26
M 7,16,4
LC_ 104
PK 124,17,124,32
AR 251,27
AR 52,28
M 7,17,5
RA 224,1,21504
PO 126
AR 53,29
M 10,64,4
WL 5
LO 18,18,127
LU 127,18
ST 120
AR 54,30
AR 55,31
M 10,65,5
DW 2048
HS 25,127
XS 248
M 11,76,4
WL 3
LL 17,58
LL 105,59
XS 249
XS 250
M 11,77,5
s_add_u32 s40, s40, 3072
LA 17
PK 127,18,125,33
XS 251
M 11,78,0
s_addc_u32 s41, s41, 0
LC_ 105
PK 125,18,125,32
s_add_u32 m0, s38, 3072
AW 24,248
AW 25,249
M 11,79,1
s_add_u32 s42, s44, s65
RA 228,1,22528
PO 127
AW 26,250
M 10,66,0
WL 4
LO 104,16,104
LU 16,104
AW 27,251
XS 52
M 10,67,1
s_addc_u32 s43, s45, 0
RA 232,1,23552
HZ 26,16
XS 53
M 7,18,0
DW 0
LL 18,60
LL 108,61
XS 54
XS 55
M 7,19,1
s_lshl_b32 s53, s52, 8
LA 18
HX 25
AW 28,52
M 6,6,0
WL 2
LC_ 108
W1 25,1024
W1 27,3072
AW 29,53
AW 30,54
M 6,7,1
s_add_u32 s48, %[alo], s53
RA 236,1,24576
v_add_u32 v4, s39, v4
AW 31,55
M 6,8,2
LO 105,17,105
LU 17,105
s_addc_u32 s49, %[ahi], 0
s_add_u32 s52, s52, %[d]
s_cmp_eq_u32 s52, %[wrap]
s_cselect_b32 s52, %[rst], s52
DW 1024
v_min_u32 v35, v35, v13
AR 248,32
M 6,9,3
ST 124
HS 26,17
s_bitcmp1_b64 s[92:93], 48
s_cselect_b32 s58, 16, 0
s_lshr_b32 s58, 0xda43cb52, s58
v_and_b32 v11, 0x1f0, v0
v_min_u32 v11, 368, v11
v_lshlrev_b32 v11, 3, v11
AR 249,33
M 7,20,2
s_mul_i32 s53, s51, 24
LL 16,62
LL 109,63
s_bitcmp1_b32 s51, 0
s_addc_u32 s53, s53, 0
s_lshl_b32 s53, s53, 11
s_bitcmp1_b64 s[90:91], s51
s_cselect_b64 s[44:45], s[84:85], s[82:83]
s_add_u32 s44, s44, s53
s_addc_u32 s45, s45, 0
s_bitcmp1_b64 s[92:93], s51
s_cselect_b64 s[94:95], s[88:89], s[86:87]
s_add_u32 s94, s94, s53
s_addc_u32 s95, s95, 0
s_add_u32 s51, s51, %[d]
s_cmp_eq_u32 s51, %[wrap]
s_cselect_b32 s51, %[rst], s51
AR 250,34
AR 251,35
M 7,21,3
LA 16
DW 2048
XS 248
M 10,68,2
LC_ 109
DQ 3072
XS 249
XS 250
M 10,69,3
s_add_u32 s50, s50, %[d]
v_add_u32 v1, s37, v0
XS 251
M 11,80,2
WL 2
LO 108,18,108
LU 18,108
AW 32,248
AW 33,249
M 11,81,3
s_cmp_eq_u32 s50, %[wrap]
RA 216,1,0
HS 26,18
AW 34,250
M 6,10,4
s_cselect_b32 s50, %[rst], s50
LL 17,72
LL 120,73
)"
R"(AW 35,251
AR 52,36
M 6,11,5
LA 17
v_and_b32 v22, 0xfffffcff, v1
AR 53,37
M 7,22,4
LC_ 120
RA 220,1,1024
AR 54,38
AR 55,39
M 7,23,5
RV 252,22,5120
v_add_u32 v19, 1, v19
XS 52
M 10,70,4
WL 5
LO 109,16,109
LU 16,109
XS 53
XS 54
M 10,71,5
s_add_u32 s38, s38, 32768
RA 224,1,2048
HS 26,16
XS 55
M 11,82,4
WL 1
LL 18,74
LL 121,75
AW 36,52
AW 37,53
M 11,83,5
s_cmp_eq_u32 s38, s77
LA 18
RA 228,1,3072
s_cselect_b32 s38, s76, s38
FP
s_sub_u32 s39, 0, s39
AW 38,54
Z 0,0,0
LC_ 121
HX 26
AW 39,55
M 0,1,1
AR 248,40
AR 249,41
Z 1,12,0
LO 120,17,120
LU 17,120
AR 250,42
M 1,13,1
RA 232,1,4096
HZ 24,17
AR 251,43
XS 248
Z 2,24,0
LL 16,88
LL 124,89
XS 249
M 2,25,1
LA 16
XS 250
XS 251
Z 3,36,0
LC_ 124
AW 40,248
M 3,37,1
AW 41,249
AW 42,250
Z 4,60,0
WL 3
LO 121,18,121
LU 18,121
AW 43,251
M 4,61,1
RA 236,1,6144
HS 24,18
AR 52,44
AR 53,45
Z 5,72,0
LL 17,90
LL 125,91
AR 54,46
M 5,73,1
LA 17
AR 55,47
XS 52
M 5,74,2
LC_ 125
XS 53
M 5,75,3
XS 54
XS 55
M 4,62,2
WL 3
LO 124,16,124
LU 16,124
AW 44,52
M 4,63,3
RA 216,1,7168
HS 24,16
AW 45,53
AW 46,54
M 3,38,2
LL 18,80
LL 106,81
AW 47,55
M 3,39,3
LA 18
FR
AR 248,48
AR 249,49
M 2,26,2
LC_ 106
v_lshrrev_b32 v3, 9, v0
AR 250,50
M 2,27,3
WV 32
BW 48
AR 251,51
XS 248
M 1,14,2
WL 5
LO 125,17,125
LU 17,125
XS 249
M 1,15,3
RA 220,1,8192
HS 24,17
XS 250
XS 251
M 0,2,2
LL 16,82
LL 107,83
AW 48,248
M 0,3,3
LA 16
RV 40,8,6144
AW 49,249
AW 50,250
M 0,4,4
LC_ 107
RV 44,8,6208
AW 51,251
M 0,5,5
RV 48,8,6272
BR 48
AR 52,52
AR 53,53
M 1,16,4
WL 8
LO 18,18,106
LU 106,18
AR 54,54
M 1,17,5
RA 224,1,9216
HZ 27,106
AR 55,55
XS 52
M 2,28,4
LL 17,84
LL 110,85
XS 53
M 2,29,5
LA 17
PK 106,18,104,33
XS 54
XS 55
M 3,40,4
LC_ 110
PK 104,18,104,32
v_lshl_add_u32 v3, v3, 2, 0
v_bfe_u32 v3, s58, v3, 4
v_mad_u32_u24 v3, v3, s97, v11
AW 52,52
M 3,41,5
s_mov_b32 m0, %[pm]
s_nop 0
DF
v_cmp_gt_u32 vcc, v19, v15
AW 53,53
AW 54,54
M 4,64,4
WL 6
LO 16,16,107
LU 107,16
AW 55,55
M 4,65,5
s_add_u32 s42, s94, s60
RA 228,1,10240
HS 27,107
AR 248,56
M 5,76,4
LL 18,86
LL 111,87
AR 249,57
AR 250,58
M 5,77,5
s_cbranch_vccnz .Lk1s16
.Lk1r16:
LA 18
PK 107,16,105,33
AR 251,59
M 5,78,0
s_addc_u32 s43, s95, 0
LC_ 111
PK 105,16,105,32
XS 248
XS 249
M 5,79,1
SU 104
HX 24
XS 250
M 4,66,0
WL 3
LO 17,17,110
LU 110,17
XS 251
AW 56,248
M 4,67,1
RA 232,1,11264
HS 27,110
AW 57,249
M 3,42,0
LL 16,96
LL 122,97
AW 58,250
AW 59,251
M 3,43,1
LA 16
PK 110,17,108,33
AR 52,60
M 2,30,0
LC_ 122
PK 108,17,108,32
AR 53,61
AR 54,62
M 2,31,1
RV 252,22,5392
PO 110
AR 55,63
M 1,18,0
WL 4
LO 18,18,111
LU 111,18
XS 52
XS 53
M 1,19,1
s_add_u32 s42, s94, s61
RA 236,1,12288
HS 27,111
XS 54
M 0,6,0
LL 17,98
LL 123,99
XS 55
AW 60,52
M 0,7,1
s_addc_u32 s43, s95, 0
LA 17
PK 111,18,109,33
AW 61,53
M 0,8,2
LC_ 123
PK 109,18,109,32
AW 62,54
AW 63,55
M 0,9,3
W1 24,0
PO 111
AR 248,64
M 1,20,2
WL 5
LO 16,16,122
LU 122,16
W1 26,2048
AR 249,65
AR 250,66
M 1,21,3
RA 216,1,13312
HZ 25,122
AR 251,67
M 2,32,2
LL 18,100
LL 126,101
XS 248
XS 249
M 2,33,3
LA 18
PK 122,16,120,33
XS 250
M 3,44,2
LC_ 126
PK 120,16,120,32
XS 251
AW 64,248
M 3,45,3
WV 30
SU 108
HX 27
AW 65,249
BW 49
M 4,68,2
WL 6
LO 17,17,123
LU 123,17
AW 66,250
AW 67,251
M 4,69,3
RA 220,1,14336
BR 49
HS 25,123
AR 52,68
LZ 40,48
WV 28
BW 50
M 5,80,2
LL 16,102
LL 127,103
AR 53,69
AR 54,70
M 5,81,3
LA 16
PK 123,17,121,33
AR 55,71
M 0,10,4
LC_ 127
PK 121,17,121,32
XS 52
XS 53
M 0,11,5
WV 24
DY
RV 40,8,6336
XS 54
M 1,22,4
WL 4
BR 50
LO 18,18,126
LU 126,18
XS 55
AW 68,52
M 1,23,5
s_add_u32 s42, s44, s64
RA 224,1,15360
HS 25,126
AW 69,53
LS 44,49
s_addc_u32 s43, s45, 0
BW 51
M 2,34,4
LL 17,56
LL 104,57
AW 70,54
AW 71,55
M 2,35,5
LA 17
PK 126,18,124,33
M 3,46,4
LC_ 104
RV 44,8,6400
PK 124,18,124,32
M 3,47,5
WV 24
ST 120
BR 51
PO 126
M 4,70,4
WL 6
LO 16,16,127
LU 127,16
M 4,71,5
s_add_u32 s42, s44, s65
RA 228,1,16384
HS 25,127
LS 48,50
s_addc_u32 s43, s45, 0
BW 52
M 5,82,4
LL 18,58
LL 105,59
M 5,83,5
LA 18
RV 48,8,6464
PK 127,16,125,33
Z 6,0,0
LC_ 105
PK 125,16,125,32
M 6,1,1
ds_read_b64 v[14:15], v5 offset:0
BR 52
PO 127
Z 7,12,0
WL 9
LO 104,17,104
LU 17,104
M 7,13,1
WL 7
RA 232,1,17408
)"
R"(HZ 26,17
LS 40,51
WV 24
BW 53
Z 8,24,0
WV 23
LL 16,60
LL 108,61
M 8,25,1
WL 2
LA 16
ST 124
RV 40,8,6528
Z 9,36,0
s_mul_i32 s53, s50, 25600
LC_ 108
BR 53
M 9,37,1
s_add_u32 s40, %[wlo], s53
HX 25
LS 44,52
s_addc_u32 s41, %[whi], 0
BW 54
Z 10,60,0
LO 105,18,105
LU 18,105
M 10,61,1
s_mov_b32 m0, s38
RA 236,1,18432
HS 26,18
Z 11,72,0
RV 44,8,6592
LL 17,62
LL 109,63
M 11,73,1
LA 17
W1 25,1024
BR 54
M 11,74,2
LC_ 109
W1 27,3072
M 11,75,3
WL 8
DW 0
v_add_f32 v14, v14, v15
LS 48,53
WV 24
BW 55
M 10,62,2
LO 108,16,108
LU 16,108
M 10,63,3
s_add_u32 s37, s37, 32768
RA 216,1,19456
HS 26,16
M 9,38,2
s_cmp_eq_u32 s37, s75
RV 48,8,6656
LL 18,64
LL 112,65
M 9,39,3
s_cselect_b32 s37, s74, s37
LA 18
DW 1024
BR 55
M 8,26,2
LC_ 112
v_add_f32_dpp v14, v14, v14 quad_perm:[1,0,3,2] row_mask:0xf bank_mask:0xf
M 8,27,3
WL 8
v_add_f32_dpp v14, v14, v14 quad_perm:[2,3,0,1] row_mask:0xf bank_mask:0xf
v_add_u32 v4, s39, v4
LS 40,54
WV 24
BW 56
M 7,14,2
WL 8
LO 109,17,109
LU 17,109
M 7,15,3
RA 220,1,20480
RV 40,8,6720
HS 26,17
M 6,2,2
BR 56
LL 16,66
LL 113,67
M 6,3,3
LA 16
v_add_f32_dpp v14, v14, v14 row_half_mirror row_mask:0xf bank_mask:0xf
M 6,4,4
WL 6
LC_ 113
v_cvt_f64_f32 v[14:15], v14
M 6,5,5
s_mov_b64 exec, s[54:55]
SP
s_mov_b64 exec, -1
v_lshrrev_b32 v3, 9, v0
LS 44,55
WV 22
BW 57
M 7,16,4
WL 7
RV 44,8,6784
LO 112,18,112
LU 18,112
M 7,17,5
RA 224,1,21504
HZ 29,18
M 8,28,4
BR 57
LL 17,68
LL 116,69
M 8,29,5
WL 6
LA 17
ds_read_b64 v[14:15], v5 offset:2048
LS 48,56
WV 21
BW 58
M 9,40,4
LC_ 116
RV 48,8,6848
M 9,41,5
RA 240,7,0
BR 58
M 10,64,4
WL 11
LO 113,16,113
LU 16,113
M 10,65,5
RA 228,1,22528
HS 29,16
v_lshl_add_u32 v3, v3, 2, 8
v_bfe_u32 v3, s58, v3, 4
v_mad_u32_u24 v3, v3, s97, v11
M 11,76,4
WV 19
LL 18,70
LL 117,71
M 11,77,5
WL 8
LA 18
DW 2048
M 11,78,0
s_mul_i32 s53, s51, 24
LC_ 117
v_subrev_u32 v5, s39, v5
M 11,79,1
s_bitcmp1_b32 s51, 0
LS 40,57
s_addc_u32 s53, s53, 0
BW 59
M 10,66,0
WL 4
RA 244,7,256
LO 116,17,116
LU 17,116
M 10,67,1
s_lshl_b32 s53, s53, 11
RA 232,1,23552
HS 29,17
M 9,42,0
s_bitcmp1_b64 s[90:91], s51
BR 59
LL 16,72
LL 120,73
M 9,43,1
s_cselect_b64 s[44:45], s[84:85], s[82:83]
LA 16
v_add_f32 v14, v14, v15
s_add_u32 s44, s44, s53
s_addc_u32 s45, s45, 0
s_bitcmp1_b64 s[92:93], s51
s_cselect_b64 s[94:95], s[88:89], s[86:87]
s_add_u32 s94, s94, s53
s_addc_u32 s95, s95, 0
s_add_u32 s51, s51, %[d]
s_cmp_eq_u32 s51, %[wrap]
s_cselect_b32 s51, %[rst], s51
s_mul_i32 s53, %[d], 16384
s_add_u32 s62, s53, %[sb1]
s_add_u32 s63, s62, 512
LS 44,58
s_add_u32 s40, s40, 3072
RA 248,7,512
v_add_f32_dpp v14, v14, v14 quad_perm:[1,0,3,2] row_mask:0xf bank_mask:0xf
M 8,30,0
s_addc_u32 s41, s41, 0
LC_ 120
v_add_f32_dpp v14, v14, v14 quad_perm:[2,3,0,1] row_mask:0xf bank_mask:0xf
s_add_u32 m0, s38, 3072
M 8,31,1
DW 0
v_add_f32_dpp v14, v14, v14 row_half_mirror row_mask:0xf bank_mask:0xf
M 7,18,0
WL 7
LO 117,18,117
LU 18,117
M 7,19,1
WL 3
RA 236,1,24576
HS 29,18
LS 48,59
RA 252,7,768
v_cvt_f64_f32 v[14:15], v14
M 6,6,0
LL 17,74
LL 121,75
s_nop 2
v_mul_f32 v36, 0xbb2aaaab, v36
M 6,7,1
LA 17
DW 1024
v_mul_f32 v37, 0xbb2aaaab, v37
v_mul_f32 v38, 0xbb2aaaab, v38
M 6,8,2
LC_ 121
s_mov_b64 exec, s[54:55]
SP
s_mov_b64 exec, -1
v_mul_f32 v39, 0xbb2aaaab, v39
M 6,9,3
DW 2048
v_add_u32 v1, s37, v0
LK 40,240,48,0
LK 44,242,48,2
s_lshl_b32 s53, s52, 8
RA 240,7,1024
v_pk_add_f16 v26, v26, v29
LK 48,244,49,0
CF 40,40,41
LK 248,246,49,2
s_add_u32 s48, %[alo], s53
RA 244,7,1280
CF 42,42,43
M 7,20,2
WL 6
LO 120,16,120
LU 16,120
M 7,21,3
s_addc_u32 s49, %[ahi], 0
RA 216,1,0
HZ 24,16
CF 44,44,45
M 8,32,2
s_add_u32 s52, s52, %[d]
LL 18,76
LL 124,77
CF 46,46,47
AW 72,40
M 8,33,3
s_cmp_eq_u32 s52, %[wrap]
LA 18
v_and_b32 v22, 0xfffffcff, v1
AW 73,42
AW 74,44
M 9,44,2
s_cselect_b32 s52, %[rst], s52
LC_ 124
DQ 3072
AW 75,46
M 9,45,3
HX 26
LK 40,248,50,0
CF 48,48,49
LK 44,250,50,2
RA 248,7,1536
CF 50,50,51
GZ 48
CF 248,248,249
CF 250,250,251
M 10,68,2
WL 6
LO 121,17,121
LU 17,121
M 10,69,3
RA 220,1,1024
HS 24,17
AW 76,48
M 11,80,2
LL 16,78
LL 125,79
AW 77,50
AW 78,248
AW 79,250
M 11,81,3
LA 16
RV 252,22,5120
LK 48,252,51,0
CF 40,40,41
LK 248,254,51,2
RA 252,7,1792
CF 42,42,43
GS 49
CF 44,44,45
CF 46,46,47
M 6,10,4
LC_ 125
DY
M 6,11,5
s_add_u32 s50, s50, %[d]
AW 80,40
AW 81,42
M 7,22,4
WL 6
LO 124,18,124
LU 18,124
M 7,23,5
s_cmp_eq_u32 s50, %[wrap]
RA 224,1,2048
HS 24,18
AW 82,44
AW 83,46
LK 40,240,52,0
CF 48,48,49
LK 44,242,52,2
s_cselect_b32 s50, %[rst], s50
RA 240,7,2048
CF 50,50,51
GS 50
CF 248,248,249
CF 250,250,251
M 8,34,4
LL 17,80
LL 106,81
M 8,35,5
LA 17
v_add_u32 v19, 1, v19
M 9,46,4
s_add_u32 s38, s38, 32768
LC_ 106
AW 84,48
M 9,47,5
AW 85,50
AW 86,248
M 10,70,4
WL 4
LO 125,16,125
LU 16,125
AW 87,250
M 10,71,5
RA 228,1,3072
HS 24,16
LK 48,244,53,0
CF 40,40,41
LK 248,246,53,2
RA 244,7,2304
CF 42,42,43
GS 51
CF 44,44,45
CF 46,46,47
M 11,82,4
LL 18,82
LL 107,83
M 11,83,5
LA 18
HX 24
Z 0,0,0
s_cmp_eq_u32 s38, s77
LC_ 107
AW 88,40
M 0,1,1
s_cselect_b32 s38, s76, s38
FP
AW 89,42
AW 90,44
AW 91,46
LK 40,248,54,0
CF 48,48,49
LK 44,250,54,2
s_sub_u32 s39, 0, s39
RA 248,7,2560
CF 50,50,51
GS 52
CF 248,248,249
CF 250,250,251
Z 1,12,0
WL 6
LO 17,17,106
LU 106,17
M 1,13,1
RA 232,1,4096
HZ 27,106
Z 2,24,0
LL 16,84
LL 110,85
M 2,25,1
LA 16
PK 106,17,104,33
AW 92,48
Z 3,36,0
LC_ 110
PK 104,17,104,32
AW 93,50
AW 94,248
AW 95,250
M 3,37,1
LK 48,252,55,0
)"
R"(CF 40,40,41
LK 248,254,55,2
RA 252,7,2816
CF 42,42,43
GS 53
CF 44,44,45
CF 46,46,47
Z 4,60,0
WL 6
LO 18,18,107
LU 107,18
M 4,61,1
s_add_u32 s42, s94, s60
RA 236,1,6144
HS 27,107
Z 5,72,0
LL 17,86
LL 111,87
AW 96,40
AW 97,42
M 5,73,1
s_addc_u32 s43, s95, 0
LA 17
PK 107,18,105,33
AW 98,44
AW 99,46
LK 40,240,56,0
CF 48,48,49
LK 44,242,56,2
CF 50,50,51
GS 54
CF 248,248,249
CF 250,250,251
M 5,74,2
LC_ 111
PK 105,18,105,32
M 5,75,3
SU 104
AW 100,48
M 4,62,2
WL 4
LO 16,16,110
LU 110,16
M 4,63,3
RA 216,1,7168
HS 27,110
AW 101,50
AW 102,248
M 3,38,2
LL 18,88
LL 114,89
AW 103,250
M 3,39,3
LA 18
PK 110,16,108,33
LK 48,244,57,0
CF 40,40,41
LK 248,246,57,2
CF 42,42,43
GS 55
CF 44,44,45
CF 46,46,47
M 2,26,2
LC_ 114
PK 108,16,108,32
M 2,27,3
FR
PO 110
AW 104,40
M 1,14,2
WL 4
LO 17,17,111
LU 111,17
AW 105,42
AW 106,44
AW 107,46
M 1,15,3
s_add_u32 s42, s94, s61
RA 220,1,8192
HS 27,111
LK 40,248,58,0
CF 48,48,49
LK 44,250,58,2
s_addc_u32 s43, s95, 0
CF 50,50,51
GS 56
CF 248,248,249
CF 250,250,251
M 0,2,2
LL 16,90
LL 115,91
M 0,3,3
s_mov_b32 m0, %[pm]
LA 16
PK 111,17,109,33
M 0,4,4
LC_ 115
PK 109,17,109,32
AW 108,48
AW 109,50
M 0,5,5
DF
PO 111
AW 110,248
AW 111,250
LK 48,252,59,0
CF 40,40,41
LK 248,254,59,2
CF 42,42,43
GS 57
CF 44,44,45
CF 46,46,47
M 1,16,4
WL 4
LO 18,18,114
LU 114,18
M 1,17,5
RA 224,1,9216
HZ 29,114
M 2,28,4
LL 17,92
LL 118,93
AW 112,40
M 2,29,5
LA 17
PK 114,18,112,33
AW 113,42
AW 114,44
M 3,40,4
LC_ 118
RV 36,8,6912
PK 112,18,112,32
AW 115,46
M 3,41,5
WL 7
SU 108
v_cmp_gt_u32 vcc, v19, v15
GS 58
CF 48,48,49
CF 50,50,51
M 4,64,4
WL 4
LO 16,16,115
LU 115,16
CF 248,248,249
CF 250,250,251
AW 116,48
M 4,65,5
s_add_u32 s42, s94, s62
RA 228,1,10240
HS 29,115
AW 117,50
AW 118,248
M 5,76,4
LL 18,94
LL 119,95
AW 119,250
M 5,77,5
s_cbranch_vccnz .Lk1s17
.Lk1r17:
LA 18
PK 115,16,113,33
GS 59
s_addc_u32 s43, s95, 0
W1 24,0
s_nop 5
v_cndmask_b32 v52, v52, v53, s[78:79]
M 5,78,0
LC_ 119
PK 113,16,113,32
v_cndmask_b32 v54, v54, v55, s[78:79]
M 5,79,1
W1 26,2048
v_cndmask_b32 v52, v52, v54, s[80:81]
M 4,66,0
WL 6
LO 17,17,118
LU 118,17
v_mul_f32 v52, 0x3b2aaaab, v52
M 4,67,1
WL 5
RA 232,1,11264
HS 29,118
v_med3_f32 v53, v52, v39, v37
M 3,42,0
LL 16,96
LL 122,97
LG 52,53
M 3,43,1
LA 16
PK 118,17,116,33
C 52,52,52
M 2,30,0
LC_ 122
PK 116,17,116,32
v_lshlrev_b32 v52, 16, v52
M 2,31,1
RV 252,22,5392
PO 118
v_add_f32 v52, v36, v52
M 1,18,0
WL 5
LO 18,18,119
LU 119,18
C 52,52,52
M 1,19,1
RA 236,1,12288
HS 29,119
M 0,6,0
LL 17,98
LL 123,99
v_lshlrev_b32 v52, 16, v52
M 0,7,1
LA 17
PK 119,18,117,33
v_sqrt_f32 v52, v52
M 0,8,2
LC_ 123
PK 117,18,117,32
C 52,52,52
M 0,9,3
SU 112
PO 119
v_lshlrev_b32 v52, 16, v52
M 1,20,2
WL 4
LO 16,16,122
LU 122,16
v_rcp_f32 v52, v52
M 1,21,3
s_add_u32 s42, s94, s63
RA 216,1,13312
HZ 25,122
v_mul_f32 v52, v38, v52
M 2,32,2
s_addc_u32 s43, s95, 0
LL 18,100
LL 126,101
CF 52,52,52
M 2,33,3
LA 18
PK 122,16,120,33
ds_bpermute_b32 v13, v28, v52
M 3,44,2
LC_ 126
PK 120,16,120,32
M 3,45,3
SU 116
v_pk_add_f16 v27, v27, v29
M 4,68,2
WL 4
LO 17,17,123
LU 123,17
M 4,69,3
s_add_u32 s42, s44, s64
RA 220,1,14336
HS 25,123
M 5,80,2
LL 16,102
LL 127,103
M 5,81,3
s_addc_u32 s43, s45, 0
LA 16
PK 123,17,121,33
M 0,10,4
LC_ 127
PK 121,17,121,32
M 0,11,5
ST 120
HX 27
M 1,22,4
WL 3
LO 18,18,126
LU 126,18
M 1,23,5
RA 224,1,15360
HS 25,126
AR 248,72
M 2,34,4
LL 17,56
LL 104,57
AR 249,73
M 2,35,5
LA 17
PK 126,18,124,33
AR 250,74
M 3,46,4
LC_ 104
PK 124,18,124,32
AR 251,75
M 3,47,5
ds_read_b64 v[14:15], v5 offset:0
PO 126
AR 52,76
M 4,70,4
WL 4
LO 16,16,127
LU 127,16
AR 53,77
M 4,71,5
s_add_u32 s42, s44, s65
RA 228,1,16384
HS 25,127
AR 54,78
M 5,82,4
LL 18,58
LL 105,59
AR 55,79
M 5,83,5
s_addc_u32 s43, s45, 0
LA 18
PK 127,16,125,33
XS 248
Z 6,0,0
LC_ 105
PK 125,16,125,32
XS 249
M 6,1,1
PO 127
HX 25
XS 250
Z 7,12,0
WL 4
LO 104,17,104
LU 17,104
XS 251
AW 72,248
M 7,13,1
RA 232,1,17408
HZ 26,17
AW 73,249
Z 8,24,0
WL 4
LL 16,60
LL 108,61
AW 74,250
M 8,25,1
s_mul_i32 s53, s50, 25600
LA 16
W1 25,1024
AW 75,251
Z 9,36,0
s_add_u32 s40, %[wlo], s53
LC_ 108
W1 27,3072
XS 52
M 9,37,1
s_addc_u32 s41, %[whi], 0
v_add_f32 v14, v14, v15
v_add_u32 v4, s39, v4
XS 53
Z 10,60,0
WL 5
LO 105,18,105
LU 18,105
XS 54
M 10,61,1
s_mov_b32 m0, s38
RA 236,1,18432
HS 26,18
XS 55
Z 11,72,0
LL 17,62
LL 109,63
AW 76,52
M 11,73,1
LA 17
ST 124
AW 77,53
M 11,74,2
LC_ 109
DW 0
AW 78,54
AW 79,55
M 11,75,3
v_add_f32_dpp v14, v14, v14 quad_perm:[1,0,3,2] row_mask:0xf bank_mask:0xf
v_min_u32 v35, v35, v13
M 10,62,2
WL 4
LO 108,16,108
LU 16,108
AR 248,80
M 10,63,3
s_add_u32 s37, s37, 32768
RA 216,1,19456
HS 26,16
AR 249,81
M 9,38,2
s_cmp_eq_u32 s37, s75
LL 18,64
LL 112,65
AR 250,82
M 9,39,3
s_cselect_b32 s37, s74, s37
LA 18
v_add_f32_dpp v14, v14, v14 quad_perm:[2,3,0,1] row_mask:0xf bank_mask:0xf
AR 251,83
M 8,26,2
s_mul_i32 s53, %[d], 393216
LC_ 112
)"
R"(v_add_f32_dpp v14, v14, v14 row_half_mirror row_mask:0xf bank_mask:0xf
XS 248
M 8,27,3
s_add_u32 s53, s53, s57
v_cvt_f64_f32 v[14:15], v14
XS 249
M 7,14,2
WL 3
LO 109,17,109
LU 17,109
XS 250
M 7,15,3
s_add_u32 s46, %[xlo], s53
RA 220,1,20480
HS 26,17
XS 251
M 6,2,2
LL 16,66
LL 113,67
AW 80,248
AW 81,249
M 6,3,3
s_addc_u32 s47, %[xhi], 0
LA 16
s_mov_b64 exec, s[54:55]
SP
s_mov_b64 exec, -1
AW 82,250
M 6,4,4
s_bitcmp1_b64 s[92:93], 48
LC_ 113
ds_read_b64 v[14:15], v5 offset:2048
AW 83,251
M 6,5,5
s_cselect_b32 s58, 16, 0
v_subrev_u32 v5, s39, v5
AR 52,84
M 7,16,4
WL 4
LO 112,18,112
LU 18,112
AR 53,85
M 7,17,5
s_lshr_b32 s58, 0xda43cb52, s58
RA 224,1,21504
HZ 29,18
AR 54,86
M 8,28,4
LL 17,68
LL 116,69
AR 55,87
M 8,29,5
LA 17
v_lshrrev_b32 v3, 6, v0
XS 52
M 9,40,4
LC_ 116
v_and_b32 v3, 12, v3
v_bfe_u32 v3, s58, v3, 4
v_mad_u32_u24 v3, v3, s97, v21
XS 53
M 9,41,5
DW 1024
XS 54
M 10,64,4
WL 4
LO 113,16,113
LU 16,113
XS 55
AW 84,52
M 10,65,5
RA 228,1,22528
HS 29,16
AW 85,53
M 11,76,4
LL 18,70
LL 117,71
AW 86,54
M 11,77,5
LA 18
XL 60,0
AW 87,55
M 11,78,0
LC_ 117
DW 2048
AR 248,88
M 11,79,1
WL 6
XL 61,64
v_add_f32 v14, v14, v15
AR 249,89
M 10,66,0
WL 3
LO 116,17,116
LU 17,116
AR 250,90
M 10,67,1
s_mul_i32 s53, s51, 24
RA 232,1,23552
HS 29,17
AR 251,91
M 9,42,0
s_bitcmp1_b32 s51, 0
LL 16,72
LL 120,73
XS 248
M 9,43,1
s_addc_u32 s53, s53, 0
LA 16
v_add_f32_dpp v14, v14, v14 quad_perm:[1,0,3,2] row_mask:0xf bank_mask:0xf
s_lshl_b32 s53, s53, 11
s_bitcmp1_b64 s[90:91], s51
s_cselect_b64 s[44:45], s[84:85], s[82:83]
s_add_u32 s44, s44, s53
s_addc_u32 s45, s45, 0
s_bitcmp1_b64 s[92:93], s51
s_cselect_b64 s[94:95], s[88:89], s[86:87]
s_add_u32 s94, s94, s53
s_addc_u32 s95, s95, 0
s_add_u32 s51, s51, %[d]
s_cmp_eq_u32 s51, %[wrap]
s_cselect_b32 s51, %[rst], s51
XS 249
M 8,30,0
s_add_u32 s40, s40, 3072
LC_ 120
v_add_f32_dpp v14, v14, v14 quad_perm:[2,3,0,1] row_mask:0xf bank_mask:0xf
s_addc_u32 s41, s41, 0
s_add_u32 m0, s38, 3072
XS 250
XS 251
M 8,31,1
XL 62,128
v_add_f32_dpp v14, v14, v14 row_half_mirror row_mask:0xf bank_mask:0xf
AW 88,248
M 7,18,0
WL 3
LO 117,18,117
LU 18,117
AW 89,249
M 7,19,1
RA 236,1,24576
HS 29,18
AW 90,250
M 6,6,0
LL 17,74
LL 121,75
AW 91,251
M 6,7,1
LA 17
DW 0
AR 52,92
M 6,8,2
LC_ 121
XL 63,192
AR 53,93
M 6,9,3
DW 1024
v_add_u32 v1, s37, v0
AR 54,94
M 7,20,2
WL 3
LO 120,16,120
LU 16,120
AR 55,95
M 7,21,3
RA 216,1,0
HZ 24,16
XS 52
M 8,32,2
LL 18,76
LL 124,77
v_cvt_f64_f32 v[14:15], v14
XS 53
XS 54
M 8,33,3
LA 18
s_mov_b64 exec, s[54:55]
SP
s_mov_b64 exec, -1
XS 55
M 9,44,2
LC_ 124
XL 64,256
AW 92,52
M 9,45,3
DW 2048
v_and_b32 v22, 0xfffffcff, v1
AW 93,53
M 10,68,2
WL 3
LO 121,17,121
LU 17,121
AW 94,54
M 10,69,3
RA 220,1,1024
HS 24,17
AW 95,55
M 11,80,2
LL 16,78
LL 125,79
AR 248,96
M 11,81,3
LA 16
RV 252,22,5120
AR 249,97
M 6,10,4
LC_ 125
XL 65,320
AR 250,98
M 6,11,5
s_lshl_b32 s53, s52, 8
v_pk_add_f16 v26, v26, v29
AR 251,99
M 7,22,4
WL 4
LO 124,18,124
LU 18,124
HX 26
XS 248
XS 249
M 7,23,5
s_add_u32 s48, %[alo], s53
RA 224,1,2048
HS 24,18
XS 250
M 8,34,4
s_addc_u32 s49, %[ahi], 0
LL 17,80
LL 106,81
s_add_u32 s52, s52, %[d]
s_cmp_eq_u32 s52, %[wrap]
s_cselect_b32 s52, %[rst], s52
XS 251
M 8,35,5
LA 17
DQ 3072
AW 96,248
M 9,46,4
LC_ 106
XL 66,384
AW 97,249
M 9,47,5
s_add_u32 s50, s50, %[d]
AW 98,250
AW 99,251
M 10,70,4
WL 3
LO 125,16,125
LU 16,125
M 10,71,5
RA 228,1,3072
HS 24,16
AR 52,100
M 11,82,4
LL 18,82
LL 107,83
AR 53,101
M 11,83,5
LA 18
HX 24
s_cmp_eq_u32 s50, %[wrap]
s_cselect_b32 s50, %[rst], s50
XL 67,448
AR 54,102
Z 0,0,0
WV 40
LC_ 107
XL 68,512
v_add_u32 v19, 1, v19
s_add_u32 s38, s38, 32768
s_cmp_eq_u32 s38, s77
s_cselect_b32 s38, s76, s38
FP
s_sub_u32 s39, 0, s39
AR 55,103
XS 52
M 0,1,1
XL 69,576
XS 53
Z 1,12,0
WL 4
LO 17,17,106
LU 106,17
XS 54
M 1,13,1
RA 232,1,4096
HZ 27,106
XS 55
Z 2,24,0
LL 16,84
LL 110,85
AW 100,52
M 2,25,1
LA 16
PK 106,17,104,33
AW 101,53
Z 3,36,0
WL 5
LC_ 110
XL 70,640
PK 104,17,104,32
AW 102,54
M 3,37,1
RA 236,1,6144
AW 103,55
M 3,38,2
WL 5
LO 18,18,107
LU 107,18
AR 248,104
M 3,39,3
FR
HS 27,107
AR 249,105
M 2,26,2
LL 17,86
LL 111,87
AR 250,106
AR 251,107
M 2,27,3
LA 17
PK 107,18,105,33
XS 248
M 1,14,2
LC_ 111
XL 71,704
PK 105,18,105,32
XS 249
M 1,15,3
s_add_u32 s42, s94, s60
RA 216,1,7168
XS 250
M 0,2,2
WL 4
LO 16,16,110
LU 110,16
XS 251
M 0,3,3
s_addc_u32 s43, s95, 0
RA 220,1,8192
HS 27,110
AW 104,248
M 0,4,4
LL 18,88
LL 114,89
AW 105,249
M 0,5,5
LA 18
PK 110,16,108,33
AW 106,250
M 1,16,4
LC_ 114
PK 108,16,108,32
AW 107,251
M 1,17,5
RA 224,1,9216
PO 110
AR 52,108
M 2,28,4
WL 5
LO 17,17,111
LU 111,17
SU 104
AR 53,109
AR 54,110
M 2,29,5
s_add_u32 s42, s94, s61
HS 27,111
v_cmp_gt_u32 vcc, v19, v15
AR 55,111
M 3,40,4
WL 3
LL 16,90
LL 115,91
XS 52
M 3,41,5
s_addc_u32 s43, s95, 0
LA 16
PK 111,17,109,33
XS 53
M 3,42,0
s_cbranch_vccnz .Lk1s18
.Lk1r18:
LC_ 115
PK 109,17,109,32
XS 54
M 3,43,1
WV 18
RA 228,1,10240
PO 111
XS 55
BW 60
M 2,30,0
WL 5
LO 18,18,114
LU 114,18
AW 108,52
M 2,31,1
RA 232,1,11264
)"
R"(RV 40,8,6144
HZ 29,114
AW 109,53
M 1,18,0
RV 44,8,6208
LL 17,92
LL 118,93
AW 110,54
M 1,19,1
LA 17
RV 48,8,6272
PK 114,18,112,33
AW 111,55
M 0,6,0
WL 6
LC_ 118
BR 60
PK 112,18,112,32
AR 248,112
AR 249,113
M 0,7,1
WV 16
RV 252,22,5392
ds_read_b64 v[14:15], v5 offset:0
W1 24,0
W1 26,2048
BW 61
AR 250,114
M 0,8,2
BR 61
LO 16,16,115
LU 115,16
AR 251,115
M 0,9,3
WL 6
RA 236,1,12288
HS 29,115
XS 248
LZ 40,60
WV 15
BW 62
M 1,20,2
LL 18,94
LL 119,95
XS 249
M 1,21,3
LA 18
RV 40,8,6336
PK 115,16,113,33
XS 250
M 2,32,2
LC_ 119
BR 62
PK 113,16,113,32
XS 251
M 2,33,3
WL 6
RA 216,1,13312
AW 112,248
LS 44,61
WV 13
BW 63
M 3,44,2
WL 7
LO 17,17,118
LU 118,17
SU 108
AW 113,249
M 3,45,3
s_add_u32 s42, s94, s62
RA 220,1,14336
HS 29,118
s_addc_u32 s43, s95, 0
RV 44,8,6400
AW 114,250
M 0,10,4
BR 63
LL 16,96
LL 122,97
AW 115,251
AR 52,116
M 0,11,5
WL 5
LA 16
PK 118,17,116,33
AR 53,117
LS 48,62
WV 11
BW 64
M 1,22,4
LC_ 122
PK 116,17,116,32
AR 54,118
M 1,23,5
RA 224,1,15360
RV 48,8,6464
PO 118
AR 55,119
M 2,34,4
LO 18,18,119
LU 119,18
SU 112
XS 52
M 2,35,5
s_add_u32 s42, s94, s63
BR 64
HS 29,119
XS 53
M 3,46,4
WL 6
LL 17,98
LL 123,99
XS 54
M 3,47,5
s_addc_u32 s43, s95, 0
LA 17
PK 119,18,117,33
XS 55
LS 40,63
WV 10
BW 65
Z 6,0,0
LC_ 123
PK 117,18,117,32
v_add_f32 v14, v14, v15
s_nop 1
v_add_f32_dpp v14, v14, v14 quad_perm:[1,0,3,2] row_mask:0xf bank_mask:0xf
s_nop 1
v_add_f32_dpp v14, v14, v14 quad_perm:[2,3,0,1] row_mask:0xf bank_mask:0xf
s_nop 1
v_add_f32_dpp v14, v14, v14 row_half_mirror row_mask:0xf bank_mask:0xf
v_cvt_f64_f32 v[14:15], v14
s_mov_b64 exec, s[54:55]
SP
s_mov_b64 exec, -1
s_nop 0
ds_read_b64 v[14:15], v5 offset:2048
RV 40,8,6528
AW 116,52
M 6,1,1
WL 5
RA 228,1,16384
BR 65
PO 119
AW 117,53
LS 44,64
WV 9
BW 66
Z 7,12,0
LO 16,16,122
LU 122,16
AW 118,54
AW 119,55
M 7,13,1
s_mul_i32 s53, s50, 25600
RA 232,1,17408
HZ 25,122
Z 8,24,0
s_add_u32 s40, %[wlo], s53
RV 44,8,6592
LL 18,100
LL 126,101
M 8,25,1
s_addc_u32 s41, %[whi], 0
LA 18
BR 66
PK 122,16,120,33
v_pk_add_f16 v27, v27, v29
Z 9,36,0
WL 5
LC_ 126
PK 120,16,120,32
HX 27
M 9,37,1
s_mov_b32 m0, s38
RA 236,1,18432
v_subrev_u32 v5, s39, v5
LS 48,65
WV 8
BW 67
M 9,38,2
RV 48,8,6656
LO 17,17,123
LU 123,17
M 9,39,3
DW 0
BR 67
HS 25,123
M 8,26,2
LL 16,102
LL 127,103
M 8,27,3
WL 5
LA 16
PK 123,17,121,33
LS 40,66
WV 8
BW 68
M 7,14,2
LC_ 127
RV 40,8,6720
PK 121,17,121,32
M 7,15,3
WV 7
RA 216,1,19456
BR 68
M 6,2,2
WL 6
LO 18,18,126
LU 126,18
SU 116
M 6,3,3
s_add_u32 s42, s44, s64
RA 220,1,20480
HS 25,126
s_addc_u32 s43, s45, 0
LS 44,67
s_add_u32 s37, s37, 32768
BW 69
M 6,4,4
s_cmp_eq_u32 s37, s75
LL 17,56
LL 104,57
s_cselect_b32 s37, s74, s37
M 6,5,5
LA 17
PK 126,18,124,33
v_add_f32 v14, v14, v15
s_nop 1
v_add_f32_dpp v14, v14, v14 quad_perm:[1,0,3,2] row_mask:0xf bank_mask:0xf
s_nop 1
v_add_f32_dpp v14, v14, v14 quad_perm:[2,3,0,1] row_mask:0xf bank_mask:0xf
s_nop 1
v_add_f32_dpp v14, v14, v14 row_half_mirror row_mask:0xf bank_mask:0xf
v_cvt_f64_f32 v[14:15], v14
s_mov_b64 exec, s[54:55]
SP
s_mov_b64 exec, -1
DW 1024
RV 44,8,6784
M 7,16,4
LC_ 104
BR 69
PK 124,18,124,32
M 7,17,5
WL 6
RA 224,1,21504
PO 126
LS 48,68
WV 9
BW 70
M 8,28,4
LO 16,16,127
LU 127,16
ST 120
M 8,29,5
WV 9
DW 2048
RV 48,8,6848
HS 25,127
M 9,40,4
WL 3
RA 240,7,0
LL 18,58
LL 105,59
M 9,41,5
s_add_u32 s40, s40, 3072
LA 18
BR 70
PK 127,16,125,33
M 9,42,0
s_addc_u32 s41, s41, 0
LC_ 105
PK 125,16,125,32
s_add_u32 m0, s38, 3072
M 9,43,1
s_add_u32 s42, s44, s65
RA 228,1,22528
PO 127
LS 40,69
s_addc_u32 s43, s45, 0
BW 71
M 8,30,0
RA 244,7,256
LO 104,17,104
LU 17,104
M 8,31,1
RA 232,1,23552
BR 71
HZ 26,17
M 7,18,0
WL 6
DW 0
LL 16,60
LL 108,61
M 7,19,1
s_lshl_b32 s53, s52, 8
LA 16
HX 25
LS 44,70
s_add_u32 s48, %[alo], s53
RA 248,7,512
M 6,6,0
WL 2
LC_ 108
W1 25,1024
W1 27,3072
M 6,7,1
s_addc_u32 s49, %[ahi], 0
RA 236,1,24576
v_add_u32 v4, s39, v4
LS 48,71
s_add_u32 s52, s52, %[d]
RA 252,7,768
M 6,8,2
LO 105,18,105
LU 18,105
s_cmp_eq_u32 s52, %[wrap]
s_cselect_b32 s52, %[rst], s52
DW 1024
v_mul_f32 v36, 0xbb2aaaab, v36
M 6,9,3
DW 2048
HS 26,18
v_mul_f32 v37, 0xbb2aaaab, v37
v_mul_f32 v38, 0xbb2aaaab, v38
M 7,20,2
LL 17,62
LL 109,63
v_mul_f32 v39, 0xbb2aaaab, v39
M 7,21,3
LA 17
ST 124
LK 40,240,60,0
LK 44,242,60,2
s_mul_i32 s53, s51, 24
RA 240,7,1024
v_add_u32 v1, s37, v0
LK 48,244,61,0
CF 40,40,41
LK 248,246,61,2
s_bitcmp1_b32 s51, 0
RA 244,7,1280
CF 42,42,43
M 8,32,2
s_addc_u32 s53, s53, 0
LC_ 109
RA 216,1,0
M 8,33,3
s_lshl_b32 s53, s53, 11
v_and_b32 v22, 0xfffffcff, v1
CF 44,44,45
CF 46,46,47
M 9,44,2
WL 6
LO 108,16,108
LU 16,108
AW 120,40
AW 121,42
M 9,45,3
s_bitcmp1_b64 s[92:93], s51
RA 220,1,1024
HS 26,16
s_cselect_b64 s[94:95], s[88:89], s[86:87]
s_add_u32 s94, s94, s53
s_addc_u32 s95, s95, 0
s_add_u32 s51, s51, %[d]
s_cmp_eq_u32 s51, %[wrap]
s_cselect_b32 s51, %[rst], s51
DQ 3072
AW 122,44
AW 123,46
LK 40,248,62,0
CF 48,48,49
LK 44,250,62,2
s_add_u32 s50, s50, %[d]
RA 248,7,1536
CF 50,50,51
GZ 60
s_cmp_eq_u32 s50, %[wrap]
CF 248,248,249
CF 250,250,251
M 6,10,4
s_cselect_b32 s50, %[rst], s50
LL 18,64
LL 112,65
M 6,11,5
LA 18
RV 252,22,5120
M 7,22,4
WL 7
LC_ 112
v_add_u32 v19, 1, v19
AW 124,48
M 7,23,5
s_add_u32 s38, s38, 32768
RA 224,1,2048
AW 125,50
AW 126,248
AW 127,250
)"
R"(LK 48,252,63,0
CF 40,40,41
LK 248,254,63,2
s_cmp_eq_u32 s38, s77
RA 252,7,1792
CF 42,42,43
GS 61
s_cselect_b32 s38, s76, s38
CF 44,44,45
CF 46,46,47
M 8,34,4
WL 7
LO 109,17,109
LU 17,109
M 8,35,5
FP
HS 26,17
M 9,46,4
WL 4
LL 16,66
LL 113,67
M 9,47,5
s_sub_u32 s39, 0, s39
LA 16
RA 228,1,3072
AW 128,40
AW 129,42
Z 0,0,0
LC_ 113
AW 130,44
AW 131,46
M 0,1,1
LK 40,240,64,0
CF 48,48,49
LK 44,242,64,2
RA 240,7,2048
CF 50,50,51
GS 62
CF 248,248,249
CF 250,250,251
Z 1,12,0
WL 7
LO 112,18,112
LU 18,112
M 1,13,1
RA 232,1,4096
HZ 29,18
AW 132,48
Z 2,24,0
LL 17,68
LL 116,69
AW 133,50
AW 134,248
M 2,25,1
LA 17
FR
AW 135,250
LK 48,244,65,0
CF 40,40,41
LK 248,246,65,2
RA 244,7,2304
CF 42,42,43
GS 63
CF 44,44,45
CF 46,46,47
Z 10,36,0
WL 6
LC_ 116
AW 136,40
M 10,37,1
RA 236,1,6144
AW 137,42
M 10,38,2
WL 7
LO 113,16,113
LU 16,113
M 10,39,3
HS 29,16
AW 138,44
AW 139,46
LK 40,248,66,0
CF 48,48,49
LK 44,250,66,2
RA 248,7,2560
CF 50,50,51
GS 64
CF 248,248,249
CF 250,250,251
M 2,26,2
LL 18,70
LL 117,71
M 2,27,3
LA 18
RA 216,1,7168
M 1,14,2
WL 6
LC_ 117
v_cmp_gt_u32 vcc, v19, v15
M 1,15,3
AW 140,48
AW 141,50
AW 142,248
M 0,2,2
WL 4
LO 116,17,116
LU 17,116
AW 143,250
M 0,3,3
RA 220,1,8192
HS 29,17
LK 48,252,67,0
CF 40,40,41
LK 248,254,67,2
RA 252,7,2816
CF 42,42,43
GS 65
CF 44,44,45
CF 46,46,47
M 0,4,4
s_cbranch_vccnz .Lk1s19
.Lk1r19:
LL 16,80
LL 106,81
M 0,5,5
LA 16
ds_read_b64 v[14:15], v5 offset:0
AW 144,40
AW 145,42
M 1,16,4
LC_ 106
AW 146,44
AW 147,46
M 1,17,5
RA 224,1,9216
LK 40,240,68,0
CF 48,48,49
LK 44,242,68,2
CF 50,50,51
GS 66
CF 248,248,249
CF 250,250,251
M 2,28,4
WL 6
LO 117,18,117
LU 18,117
M 2,29,5
HS 29,18
AW 148,48
M 10,40,4
WL 5
LL 17,82
LL 107,83
AW 149,50
AW 150,248
AW 151,250
M 10,41,5
LA 17
RA 228,1,10240
LK 48,244,69,0
CF 40,40,41
LK 248,246,69,2
CF 42,42,43
v_pk_add_f16 v26, v26, v29
GS 67
CF 44,44,45
CF 46,46,47
M 10,42,0
LC_ 107
HX 26
M 10,43,1
W1 26,2048
AW 152,40
M 2,30,0
WL 5
LO 16,16,106
LU 106,16
AW 153,42
AW 154,44
M 2,31,1
RA 232,1,11264
HZ 27,106
AW 155,46
LK 40,248,70,0
CF 48,48,49
LK 44,250,70,2
CF 50,50,51
v_add_f32 v14, v14, v15
GS 68
CF 248,248,249
CF 250,250,251
M 1,18,0
LL 18,84
LL 110,85
M 1,19,1
LA 18
PK 106,16,104,33
M 0,6,0
WL 4
LC_ 110
PK 104,16,104,32
AW 156,48
AW 157,50
AW 158,248
M 0,7,1
RV 252,22,5392
v_add_f32_dpp v14, v14, v14 quad_perm:[1,0,3,2] row_mask:0xf bank_mask:0xf
AW 159,250
LK 48,252,71,0
CF 40,40,41
LK 248,254,71,2
CF 42,42,43
v_add_f32_dpp v14, v14, v14 quad_perm:[2,3,0,1] row_mask:0xf bank_mask:0xf
GS 69
CF 44,44,45
CF 46,46,47
M 0,8,2
WL 5
LO 17,17,107
LU 107,17
M 0,9,3
s_add_u32 s42, s94, s60
RA 236,1,12288
HS 27,107
M 1,20,2
s_addc_u32 s43, s95, 0
LL 16,86
LL 111,87
AW 160,40
AW 161,42
M 1,21,3
LA 16
RV 36,8,6912
PK 107,17,105,33
AW 162,44
AW 163,46
M 2,32,2
LC_ 111
PK 105,17,105,32
M 2,33,3
RA 216,1,13312
v_add_f32_dpp v14, v14, v14 row_half_mirror row_mask:0xf bank_mask:0xf
GS 70
CF 48,48,49
CF 50,50,51
CF 248,248,249
CF 250,250,251
M 10,44,2
WL 4
LO 18,18,110
LU 110,18
AW 164,48
AW 165,50
AW 166,248
M 10,45,3
RA 220,1,14336
HS 27,110
AW 167,250
s_nop 1
GS 71
SU 104
v_cvt_f64_f32 v[14:15], v14
s_nop 5
v_cndmask_b32 v52, v52, v53, s[78:79]
M 0,10,4
LL 17,88
LL 114,89
v_cndmask_b32 v54, v54, v55, s[78:79]
M 0,11,5
LA 17
PK 110,18,108,33
v_cndmask_b32 v52, v52, v54, s[80:81]
M 1,22,4
LC_ 114
PK 108,18,108,32
v_mul_f32 v52, 0x3b2aaaab, v52
M 1,23,5
WL 5
RA 224,1,15360
PO 110
v_med3_f32 v53, v52, v39, v37
M 2,34,4
WL 5
LO 16,16,111
LU 111,16
s_mov_b64 exec, s[54:55]
SP
s_mov_b64 exec, -1
LG 52,53
s_nop 0
C 52,52,52
M 2,35,5
s_add_u32 s42, s94, s61
ds_read_b64 v[14:15], v5 offset:2048
HS 27,111
v_lshlrev_b32 v52, 16, v52
M 10,46,4
WL 4
LL 18,90
LL 115,91
v_add_f32 v52, v36, v52
M 10,47,5
s_addc_u32 s43, s95, 0
LA 18
PK 111,16,109,33
C 52,52,52
Z 6,0,0
LC_ 115
PK 109,16,109,32
v_lshlrev_b32 v52, 16, v52
M 6,1,1
RA 228,1,16384
PO 111
v_sqrt_f32 v52, v52
s_nop 0
C 52,52,52
Z 7,12,0
WL 5
LO 17,17,114
LU 114,17
v_lshlrev_b32 v52, 16, v52
M 7,13,1
RA 232,1,17408
HZ 29,114
v_rcp_f32 v52, v52
Z 8,24,0
LL 16,92
LL 118,93
v_mul_f32 v52, v38, v52
M 8,25,1
LA 16
PK 114,17,112,33
v_subrev_u32 v5, s39, v5
CF 52,52,52
Z 11,36,0
WL 2
LC_ 118
PK 112,17,112,32
ds_bpermute_b32 v13, v28, v52
M 11,37,1
RA 236,1,18432
v_add_f32 v14, v14, v15
M 11,38,2
LO 18,18,115
LU 115,18
M 11,39,3
SU 108
HS 29,115
v_add_f32_dpp v14, v14, v14 quad_perm:[1,0,3,2] row_mask:0xf bank_mask:0xf
M 8,26,2
s_mul_i32 s53, s50, 25600
LL 17,94
LL 119,95
s_add_u32 s40, %[wlo], s53
s_addc_u32 s41, %[whi], 0
s_mov_b32 m0, s38
M 8,27,3
LA 17
PK 115,18,113,33
v_add_f32_dpp v14, v14, v14 quad_perm:[2,3,0,1] row_mask:0xf bank_mask:0xf
M 7,14,2
LC_ 119
PK 113,18,113,32
M 7,15,3
RA 216,1,19456
DW 0
v_add_f32_dpp v14, v14, v14 row_half_mirror row_mask:0xf bank_mask:0xf
M 6,2,2
WL 3
LO 16,16,118
LU 118,16
M 6,3,3
s_add_u32 s42, s94, s62
RA 220,1,20480
HS 29,118
v_cvt_f64_f32 v[14:15], v14
s_addc_u32 s43, s95, 0
AR 248,120
M 6,4,4
s_add_u32 s37, s37, 32768
LL 18,56
LL 104,57
s_cmp_eq_u32 s37, s75
s_cselect_b32 s37, s74, s37
AR 249,121
M 6,5,5
LA 18
PK 118,16,116,33
s_mov_b64 exec, s[54:55]
SP
s_mov_b64 exec, -1
DW 1024
AR 250,122
M 7,16,4
LC_ 104
PK 116,16,116,32
AR 251,123
AR 52,124
M 7,17,5
RA 224,1,21504
PO 118
)"
R"(AR 53,125
M 8,28,4
WL 5
LO 17,17,119
LU 119,17
SU 112
AR 54,126
AR 55,127
M 8,29,5
DW 2048
HS 29,119
XS 248
M 11,40,4
WL 3
LL 16,58
LL 105,59
XS 249
M 11,41,5
s_add_u32 s40, s40, 3072
LA 16
PK 119,17,117,33
XS 250
XS 251
M 11,42,0
s_addc_u32 s41, s41, 0
LC_ 105
PK 117,17,117,32
s_add_u32 m0, s38, 3072
AW 120,248
M 11,43,1
s_add_u32 s42, s94, s63
RA 228,1,22528
PO 119
AW 121,249
AW 122,250
M 8,30,0
WL 4
LO 104,18,104
LU 18,104
AW 123,251
M 8,31,1
s_addc_u32 s43, s95, 0
RA 232,1,23552
HZ 26,18
XS 52
XS 53
M 7,18,0
DW 0
LL 17,60
LL 108,61
XS 54
M 7,19,1
s_lshl_b32 s53, s52, 8
LA 17
v_pk_add_f16 v27, v27, v29
XS 55
M 6,6,0
WL 2
LC_ 108
HX 27
AW 124,52
AW 125,53
M 6,7,1
s_add_u32 s48, %[alo], s53
RA 236,1,24576
W1 27,3072
AW 126,54
M 6,8,2
LO 105,16,105
LU 16,105
s_addc_u32 s49, %[ahi], 0
s_add_u32 s52, s52, %[d]
s_cmp_eq_u32 s52, %[wrap]
s_cselect_b32 s52, %[rst], s52
DW 1024
AW 127,55
v_min_u32 v35, v35, v13
M 6,9,3
SU 116
HS 26,16
AR 248,128
M 7,20,2
s_mul_i32 s53, s51, 24
LL 18,62
LL 109,63
s_bitcmp1_b32 s51, 0
s_addc_u32 s53, s53, 0
s_lshl_b32 s53, s53, 11
s_bitcmp1_b64 s[92:93], s51
s_cselect_b64 s[94:95], s[88:89], s[86:87]
s_add_u32 s94, s94, s53
s_addc_u32 s95, s95, 0
s_add_u32 s51, s51, %[d]
s_cmp_eq_u32 s51, %[wrap]
s_cselect_b32 s51, %[rst], s51
AR 249,129
AR 250,130
M 7,21,3
LA 18
DW 2048
AR 251,131
M 8,32,2
LC_ 109
DQ 3072
XS 248
M 8,33,3
s_add_u32 s50, s50, %[d]
v_add_u32 v1, s37, v0
v_add_u32 v4, s39, v4
XS 249
XS 250
M 11,44,2
WL 3
LO 108,17,108
LU 17,108
XS 251
M 11,45,3
s_cmp_eq_u32 s50, %[wrap]
RA 216,1,0
HS 26,17
AW 128,248
AW 129,249
M 6,10,4
s_cselect_b32 s50, %[rst], s50
LL 16,64
LL 112,65
AW 130,250
M 6,11,5
LA 16
v_and_b32 v22, 0xfffffcff, v1
AW 131,251
AR 52,132
M 7,22,4
LC_ 112
RA 220,1,1024
AR 53,133
M 7,23,5
RV 252,22,5120
v_add_u32 v19, 1, v19
AR 54,134
M 8,34,4
WL 5
LO 109,18,109
LU 18,109
AR 55,135
XS 52
M 8,35,5
s_add_u32 s38, s38, 32768
RA 224,1,2048
HS 26,18
XS 53
M 11,46,4
WL 1
LL 17,66
LL 113,67
XS 54
XS 55
M 11,47,5
s_cmp_eq_u32 s38, s77
LA 17
RA 228,1,3072
s_cselect_b32 s38, s76, s38
FP
s_sub_u32 s39, 0, s39
AW 132,52
Z 0,0,0
LC_ 113
AW 133,53
AW 134,54
M 0,1,1
AW 135,55
AR 248,136
Z 1,12,0
LO 112,16,112
LU 16,112
M 1,13,1
RA 232,1,4096
HZ 29,16
AR 249,137
AR 250,138
Z 2,24,0
LL 18,96
LL 116,97
AR 251,139
M 2,25,1
LA 18
XS 248
XS 249
Z 3,36,0
LC_ 116
XS 250
M 3,37,1
XS 251
AW 136,248
Z 4,48,0
WL 3
LO 113,17,113
LU 17,113
AW 137,249
M 4,49,1
RA 236,1,6144
HS 29,17
AW 138,250
Z 5,60,0
LL 16,98
LL 117,99
AW 139,251
AR 52,140
M 5,61,1
LA 16
AR 53,141
M 5,62,2
LC_ 117
AR 54,142
AR 55,143
M 5,63,3
XS 52
XS 53
M 4,50,2
WL 3
LO 116,18,116
LU 18,116
XS 54
M 4,51,3
RA 216,1,7168
HS 29,18
XS 55
M 3,38,2
LL 17,80
LL 106,81
AW 140,52
M 3,39,3
LA 17
FR
AW 141,53
AW 142,54
M 2,26,2
LC_ 106
AW 143,55
M 2,27,3
AR 248,144
AR 249,145
M 1,14,2
WL 4
LO 117,16,117
LU 16,117
AR 250,146
M 1,15,3
RA 220,1,8192
HS 29,16
AR 251,147
M 0,2,2
LL 18,82
LL 107,83
XS 248
XS 249
M 0,3,3
LA 18
v_pk_add_f16 v26, v26, v29
XS 250
M 0,4,4
WL 3
LC_ 107
v_cmp_gt_u32 vcc, v19, v15
XS 251
AW 144,248
M 0,5,5
HX 26
AW 145,249
M 1,16,4
WL 3
LO 17,17,106
LU 106,17
AW 146,250
AW 147,251
M 1,17,5
RA 224,1,9216
HZ 27,106
AR 52,148
M 2,28,4
s_cbranch_vccnz .Lk1s20
.Lk1r20:
LL 16,84
LL 110,85
AR 53,149
M 2,29,5
LA 16
PK 106,17,104,33
AR 54,150
AR 55,151
M 3,40,4
LC_ 110
PK 104,17,104,32
XS 52
M 3,41,5
W1 26,2048
XS 53
XS 54
M 4,52,4
WL 4
LO 18,18,107
LU 107,18
XS 55
M 4,53,5
s_add_u32 s42, s94, s60
RA 228,1,10240
HS 27,107
AW 148,52
AW 149,53
M 5,64,4
LL 17,86
LL 111,87
AW 150,54
AW 151,55
M 5,65,5
s_addc_u32 s43, s95, 0
LA 17
PK 107,18,105,33
M 5,66,0
LC_ 111
PK 105,18,105,32
AR 248,152
AR 249,153
M 5,67,1
SU 104
AR 250,154
M 4,54,0
WL 4
LO 16,16,110
LU 110,16
AR 251,155
XS 248
M 4,55,1
RA 232,1,11264
HS 27,110
XS 249
M 3,42,0
LL 18,88
LL 114,89
XS 250
XS 251
M 3,43,1
LA 18
PK 110,16,108,33
AW 152,248
M 2,30,0
LC_ 114
PK 108,16,108,32
AW 153,249
M 2,31,1
RV 252,22,5392
PO 110
AW 154,250
AW 155,251
M 1,18,0
WL 4
LO 17,17,111
LU 111,17
AR 52,156
M 1,19,1
s_add_u32 s42, s94, s61
RA 236,1,12288
HS 27,111
AR 53,157
AR 54,158
M 0,6,0
LL 16,90
LL 115,91
AR 55,159
M 0,7,1
s_addc_u32 s43, s95, 0
LA 16
PK 111,17,109,33
XS 52
XS 53
M 0,8,2
LC_ 115
PK 109,17,109,32
XS 54
M 0,9,3
PO 111
XS 55
M 1,20,2
WL 4
LO 18,18,114
LU 114,18
AW 156,52
AW 157,53
M 1,21,3
RA 216,1,13312
HZ 29,114
AW 158,54
M 2,32,2
LL 17,100
LL 118,101
AW 159,55
AR 248,160
M 2,33,3
LA 17
PK 114,18,112,33
)"
R"(AR 249,161
M 3,44,2
LC_ 118
PK 112,18,112,32
AR 250,162
AR 251,163
M 3,45,3
SU 108
XS 248
M 4,56,2
WL 3
LO 16,16,115
LU 115,16
XS 249
M 4,57,3
s_add_u32 s42, s94, s62
RA 220,1,14336
HS 29,115
XS 250
XS 251
M 5,68,2
LL 18,102
LL 119,103
AW 160,248
M 5,69,3
s_addc_u32 s43, s95, 0
LA 18
PK 115,16,113,33
AW 161,249
AW 162,250
M 0,10,4
LC_ 119
PK 113,16,113,32
AW 163,251
M 0,11,5
SU 112
AR 52,164
AR 53,165
M 1,22,4
WL 3
LO 17,17,118
LU 118,17
AR 54,166
M 1,23,5
RA 224,1,15360
HS 29,118
AR 55,167
M 2,34,4
LL 16,56
LL 104,57
XS 52
XS 53
M 2,35,5
LA 16
PK 118,17,116,33
XS 54
M 3,46,4
LC_ 104
PK 116,17,116,32
XS 55
AW 164,52
M 3,47,5
ds_read_b64 v[14:15], v5 offset:2048
PO 118
AW 165,53
M 4,58,4
WL 4
LO 18,18,119
LU 119,18
AW 166,54
AW 167,55
M 4,59,5
s_add_u32 s42, s94, s63
RA 228,1,16384
HS 29,119
M 5,70,4
LL 17,58
LL 105,59
M 5,71,5
s_addc_u32 s43, s95, 0
LA 17
PK 119,18,117,33
Z 6,0,0
LC_ 105
PK 117,18,117,32
M 6,1,1
PO 119
v_subrev_u32 v5, s39, v5
Z 7,12,0
WL 4
LO 104,16,104
LU 16,104
M 7,13,1
RA 232,1,17408
HZ 26,16
Z 8,24,0
LL 18,60
LL 108,61
M 8,25,1
WL 4
LA 18
SU 116
Z 9,36,0
s_mul_i32 s53, s50, 25600
LC_ 108
v_add_f32 v14, v14, v15
M 9,37,1
s_add_u32 s40, %[wlo], s53
v_add_f32_dpp v14, v14, v14 quad_perm:[1,0,3,2] row_mask:0xf bank_mask:0xf
v_pk_add_f16 v27, v27, v29
Z 10,48,0
WL 3
LO 105,17,105
LU 17,105
M 10,49,1
s_addc_u32 s41, %[whi], 0
RA 236,1,18432
HS 26,17
Z 11,60,0
LL 16,62
LL 109,63
M 11,61,1
s_mov_b32 m0, s38
LA 16
DW 0
M 11,62,2
LC_ 109
v_add_f32_dpp v14, v14, v14 quad_perm:[2,3,0,1] row_mask:0xf bank_mask:0xf
M 11,63,3
s_nop 0
v_add_f32_dpp v14, v14, v14 row_half_mirror row_mask:0xf bank_mask:0xf
HX 27
M 10,50,2
WL 3
LO 108,18,108
LU 18,108
M 10,51,3
s_add_u32 s37, s37, 32768
RA 216,1,19456
HS 26,18
W1 27,3072
M 9,38,2
s_cmp_eq_u32 s37, s75
LL 17,64
LL 112,65
M 9,39,3
s_cselect_b32 s37, s74, s37
LA 17
DW 1024
M 8,26,2
LC_ 112
v_cvt_f64_f32 v[14:15], v14
M 8,27,3
s_mov_b64 exec, s[54:55]
SP
s_mov_b64 exec, -1
v_add_u32 v4, s39, v4
M 7,14,2
WL 4
LO 109,16,109
LU 16,109
M 7,15,3
RA 220,1,20480
HS 26,16
M 6,2,2
LL 18,66
LL 113,67
M 6,3,3
LA 18
DW 2048
M 6,4,4
LC_ 113
M 6,5,5
M 7,16,4
WL 3
LO 112,17,112
LU 17,112
M 7,17,5
s_mul_i32 s53, s51, 24
RA 224,1,21504
HZ 29,17
M 8,28,4
s_bitcmp1_b32 s51, 0
LL 16,68
LL 116,69
M 8,29,5
s_addc_u32 s53, s53, 0
LA 16
M 9,40,4
s_lshl_b32 s53, s53, 11
LC_ 116
M 9,41,5
s_bitcmp1_b64 s[92:93], s51
M 10,52,4
WL 3
LO 113,18,113
LU 18,113
M 10,53,5
s_cselect_b64 s[94:95], s[88:89], s[86:87]
RA 228,1,22528
HS 29,18
M 11,64,4
LL 17,70
LL 117,71
M 11,65,5
s_add_u32 s94, s94, s53
LA 17
M 11,66,0
s_addc_u32 s95, s95, 0
LC_ 117
M 11,67,1
s_add_u32 s51, s51, %[d]
M 10,54,0
WL 3
LO 116,16,116
LU 16,116
M 10,55,1
s_cmp_eq_u32 s51, %[wrap]
RA 232,1,23552
HS 29,16
M 9,42,0
s_cselect_b32 s51, %[rst], s51
LL 18,72
LL 120,73
M 9,43,1
s_mul_i32 s53, %[d], 32768
LA 18
s_add_u32 s64, s53, %[sb1]
s_add_u32 s65, s64, 512
M 8,30,0
s_add_u32 s40, s40, 3072
LC_ 120
s_addc_u32 s41, s41, 0
s_add_u32 m0, s38, 3072
M 8,31,1
DW 0
M 7,18,0
WL 3
LO 117,17,117
LU 17,117
M 7,19,1
RA 236,1,24576
HS 29,17
M 6,6,0
LL 16,74
LL 121,75
M 6,7,1
LA 16
DW 1024
M 6,8,2
LC_ 121
DW 2048
M 6,9,3
s_lshl_b32 s53, s52, 8
v_add_u32 v1, s37, v0
M 7,20,2
WL 3
LO 120,18,120
LU 18,120
M 7,21,3
s_add_u32 s48, %[alo], s53
RA 216,1,0
HZ 30,18
M 8,32,2
s_addc_u32 s49, %[ahi], 0
LL 17,76
LL 124,77
M 8,33,3
s_add_u32 s52, s52, %[d]
LA 17
v_and_b32 v22, 0xfffffcff, v1
M 9,44,2
s_cmp_eq_u32 s52, %[wrap]
LC_ 124
M 9,45,3
s_cselect_b32 s52, %[rst], s52
DQ 3072
M 10,56,2
WL 3
LO 121,16,121
LU 16,121
M 10,57,3
s_add_u32 s50, s50, %[d]
RA 220,1,1024
HS 30,16
M 11,68,2
LL 18,78
LL 125,79
M 11,69,3
s_cmp_eq_u32 s50, %[wrap]
LA 18
RV 252,22,5120
M 6,10,4
s_cselect_b32 s50, %[rst], s50
LC_ 125
M 6,11,5
WV 25
v_add_u32 v19, 1, v19
M 7,22,4
WL 4
LO 124,17,124
LU 17,124
M 7,23,5
s_add_u32 s38, s38, 32768
RA 224,1,2048
HS 30,17
M 8,34,4
s_cmp_eq_u32 s38, s77
LL 16,80
LL 106,81
M 8,35,5
s_cselect_b32 s38, s76, s38
LA 16
FP
M 9,46,4
s_sub_u32 s39, 0, s39
LC_ 106
M 9,47,5
M 10,58,4
WL 4
LO 125,18,125
LU 18,125
M 10,59,5
RA 228,1,3072
HS 30,18
M 11,70,4
LL 17,82
LL 107,83
M 11,71,5
LA 17
v_pk_add_f16 v26, v26, v30
Z 0,0,0
LC_ 107
v_pk_add_f16 v26, v26, v29
M 0,1,1
HX 26
Z 1,12,0
WL 3
LO 16,16,106
LU 106,16
M 1,13,1
RA 232,1,4096
HZ 27,106
Z 2,24,0
LL 18,84
LL 110,85
M 2,25,1
LA 18
PK 106,16,104,33
Z 3,36,0
LC_ 110
PK 104,16,104,32
M 3,37,1
Z 4,48,0
WL 3
LO 17,17,107
LU 107,17
M 4,49,1
s_add_u32 s42, s94, s60
RA 236,1,6144
HS 27,107
Z 5,60,0
LL 16,86
LL 111,87
M 5,61,1
s_addc_u32 s43, s95, 0
LA 16
PK 107,17,105,33
M 5,62,2
LC_ 111
PK 105,17,105,32
M 5,63,3
SU 104
M 4,50,2
WL 3
LO 18,18,110
LU 110,18
M 4,51,3
RA 216,1,7168
HS 27,110
M 3,38,2
LL 17,88
LL 114,89
M 3,39,3
LA 17
PK 110,18,108,33
M 2,26,2
LC_ 114
PK 108,18,108,32
M 2,27,3
FR
PO 110
M 1,14,2
WL 4
LO 16,16,111
LU 111,16
M 1,15,3
s_add_u32 s42, s94, s61
RA 220,1,8192
HS 27,111
)"
R"(M 0,2,2
LL 18,90
LL 115,91
M 0,3,3
s_addc_u32 s43, s95, 0
LA 18
PK 111,16,109,33
M 0,4,4
PFN
LC_ 115
PK 109,16,109,32
M 0,5,5
s_mov_b32 m0, %[pm]
s_nop 0
DF
PO 111
M 1,16,4
WL 4
LO 17,17,114
LU 114,17
M 1,17,5
RA 224,1,9216
HZ 29,114
M 2,28,4
LL 16,92
LL 118,93
M 2,29,5
LA 16
PK 114,17,112,33
M 3,40,4
LC_ 118
PK 112,17,112,32
M 3,41,5
WL 6
SU 108
v_cmp_gt_u32 vcc, v19, v15
M 4,52,4
WL 3
LO 18,18,115
LU 115,18
M 4,53,5
s_add_u32 s42, s94, s62
RA 228,1,10240
HS 29,115
M 5,64,4
LL 17,94
LL 119,95
M 5,65,5
s_cbranch_vccnz .Lk1s21
.Lk1r21:
LA 17
PK 115,18,113,33
M 5,66,0
s_addc_u32 s43, s95, 0
LC_ 119
PK 113,18,113,32
M 5,67,1
W1 26,2048
M 4,54,0
WL 4
LO 16,16,118
LU 118,16
M 4,55,1
RA 232,1,11264
HS 29,118
M 3,42,0
LL 18,96
LL 122,97
M 3,43,1
LA 18
PK 118,16,116,33
M 2,30,0
LC_ 122
PK 116,16,116,32
M 2,31,1
RV 252,22,5392
PO 118
M 1,18,0
WL 5
LO 17,17,119
LU 119,17
M 1,19,1
RA 236,1,12288
HS 29,119
M 0,6,0
LL 16,98
LL 123,99
M 0,7,1
LA 16
PK 119,17,117,33
M 0,8,2
LC_ 123
PK 117,17,117,32
M 0,9,3
SU 112
PO 119
M 1,20,2
WL 4
LO 18,18,122
LU 122,18
M 1,21,3
s_add_u32 s42, s94, s63
RA 216,1,13312
HZ 30,122
M 2,32,2
s_addc_u32 s43, s95, 0
LL 17,100
LL 126,101
M 2,33,3
LA 17
PK 122,18,120,33
M 3,44,2
LC_ 126
PK 120,18,120,32
M 3,45,3
SU 116
M 4,56,2
WL 3
LO 16,16,123
LU 123,16
M 4,57,3
s_add_u32 s42, s94, s64
RA 220,1,14336
HS 30,123
M 5,68,2
LL 18,102
LL 127,103
M 5,69,3
s_addc_u32 s43, s95, 0
LA 18
PK 123,16,121,33
M 0,10,4
LC_ 127
PK 121,16,121,32
M 0,11,5
SU 120
M 1,22,4
WL 3
LO 17,17,126
LU 126,17
M 1,23,5
RA 224,1,15360
HS 30,126
M 2,34,4
LL 16,56
LL 104,57
M 2,35,5
LA 16
PK 126,17,124,33
M 3,46,4
LC_ 104
PK 124,17,124,32
M 3,47,5
ds_read_b64 v[14:15], v5 offset:2048
PO 126
M 4,58,4
WL 4
LO 18,18,127
LU 127,18
M 4,59,5
s_add_u32 s42, s94, s65
RA 228,1,16384
HS 30,127
M 5,70,4
LL 17,58
LL 105,59
M 5,71,5
s_addc_u32 s43, s95, 0
LA 17
PK 127,18,125,33
Z 6,0,0
LC_ 105
PK 125,18,125,32
M 6,1,1
PO 127
v_subrev_u32 v5, s39, v5
Z 7,12,0
WL 4
LO 104,16,104
LU 16,104
M 7,13,1
RA 232,1,17408
HZ 26,16
Z 8,24,0
WL 4
LL 18,60
LL 108,61
M 8,25,1
s_mul_i32 s53, s50, 25600
LA 18
v_add_f32 v14, v14, v15
Z 9,36,0
s_add_u32 s40, %[wlo], s53
LC_ 108
v_add_f32_dpp v14, v14, v14 quad_perm:[1,0,3,2] row_mask:0xf bank_mask:0xf
M 9,37,1
s_addc_u32 s41, %[whi], 0
v_add_f32_dpp v14, v14, v14 quad_perm:[2,3,0,1] row_mask:0xf bank_mask:0xf
v_pk_add_f16 v27, v27, v30
Z 10,48,0
WL 3
LO 105,17,105
LU 17,105
M 10,49,1
s_mov_b32 m0, s38
RA 236,1,18432
HS 26,17
Z 11,60,0
LL 16,62
LL 109,63
M 11,61,1
LA 16
SU 124
M 11,62,2
LC_ 109
DW 0
M 11,63,3
v_add_f32_dpp v14, v14, v14 row_half_mirror row_mask:0xf bank_mask:0xf
v_pk_add_f16 v27, v27, v29
M 10,50,2
WL 3
LO 108,18,108
LU 18,108
M 10,51,3
s_add_u32 s37, s37, 32768
RA 216,1,19456
HS 26,18
HX 27
M 9,38,2
s_cmp_eq_u32 s37, s75
LL 17,64
LL 112,65
W1 27,3072
M 9,39,3
s_cselect_b32 s37, s74, s37
LA 17
DW 1024
M 8,26,2
LC_ 112
v_cvt_f64_f32 v[14:15], v14
M 8,27,3
s_mov_b64 exec, s[54:55]
SP
s_mov_b64 exec, -1
v_add_u32 v4, s39, v4
s_mov_b64 exec, 1
AT
s_mov_b64 exec, -1
M 7,14,2
WL 4
LO 109,16,109
LU 16,109
M 7,15,3
RA 220,1,20480
HS 26,16
M 6,2,2
LL 18,66
LL 113,67
M 6,3,3
LA 18
DW 2048
M 6,4,4
LC_ 113
M 6,5,5
M 7,16,4
WL 3
LO 112,17,112
LU 17,112
M 7,17,5
s_mul_i32 s53, s51, 24
RA 224,1,21504
HZ 29,17
M 8,28,4
s_bitcmp1_b32 s51, 0
LL 16,68
LL 116,69
M 8,29,5
s_addc_u32 s53, s53, 0
LA 16
M 9,40,4
s_lshl_b32 s53, s53, 11
LC_ 116
M 9,41,5
s_bitcmp1_b64 s[92:93], s51
M 10,52,4
WL 3
LO 113,18,113
LU 18,113
M 10,53,5
s_cselect_b64 s[94:95], s[88:89], s[86:87]
RA 228,1,22528
HS 29,18
M 11,64,4
LL 17,70
LL 117,71
M 11,65,5
s_add_u32 s94, s94, s53
LA 17
M 11,66,0
s_addc_u32 s95, s95, 0
LC_ 117
M 11,67,1
s_add_u32 s51, s51, %[d]
M 10,54,0
WL 3
LO 116,16,116
LU 16,116
M 10,55,1
s_cmp_eq_u32 s51, %[wrap]
RA 232,1,23552
HS 29,16
M 9,42,0
s_cselect_b32 s51, %[rst], s51
LL 18,72
LL 120,73
M 9,43,1
s_add_u32 s40, s40, 3072
LA 18
M 8,30,0
s_addc_u32 s41, s41, 0
LC_ 120
s_add_u32 m0, s38, 3072
M 8,31,1
DW 0
M 7,18,0
WL 3
LO 117,17,117
LU 17,117
M 7,19,1
RA 236,1,24576
HS 29,17
M 6,6,0
LL 16,74
LL 121,75
M 6,7,1
LA 16
DW 1024
M 6,8,2
LC_ 121
DW 2048
M 6,9,3
s_lshl_b32 s53, s52, 8
v_add_u32 v1, s37, v0
M 7,20,2
WL 3
LO 120,18,120
LU 18,120
M 7,21,3
s_add_u32 s48, %[alo], s53
RA 216,1,0
HZ 30,18
M 8,32,2
s_addc_u32 s49, %[ahi], 0
LL 17,76
LL 124,77
M 8,33,3
s_add_u32 s52, s52, %[d]
LA 17
v_and_b32 v22, 0xfffffcff, v1
M 9,44,2
s_cmp_eq_u32 s52, %[wrap]
LC_ 124
M 9,45,3
s_cselect_b32 s52, %[rst], s52
DQ 3072
M 10,56,2
WL 3
LO 121,16,121
LU 16,121
M 10,57,3
s_add_u32 s50, s50, %[d]
RA 220,1,1024
HS 30,16
M 11,68,2
LL 18,78
LL 125,79
M 11,69,3
s_cmp_eq_u32 s50, %[wrap]
LA 18
RV 252,22,5120
M 6,10,4
s_cselect_b32 s50, %[rst], s50
LC_ 125
M 6,11,5
WV 28
M 7,22,4
WL 4
LO 124,17,124
LU 17,124
M 7,23,5
s_add_u32 s38, s38, 32768
RA 224,1,2048
HS 30,17
M 8,34,4
s_cmp_eq_u32 s38, s77
LL 16,80
LL 106,81
M 8,35,5
s_cselect_b32 s38, s76, s38
LA 16
)"
R"(M 9,46,4
s_barrier
LC_ 106
M 9,47,5
s_sub_u32 s39, 0, s39
M 10,58,4
WL 3
LO 125,18,125
LU 18,125
M 10,59,5
RA 228,1,3072
HS 30,18
M 11,70,4
LL 17,82
LL 107,83
M 11,71,5
LA 17
v_pk_add_f16 v26, v26, v30
Z 0,0,0
LC_ 107
v_pk_add_f16 v26, v26, v29
M 0,1,1
HX 26
Z 1,12,0
WL 3
LO 16,16,106
LU 106,16
M 1,13,1
RA 232,1,4096
HZ 27,106
Z 2,24,0
LL 18,84
LL 110,85
M 2,25,1
LA 18
PK 106,16,104,33
Z 3,36,0
LC_ 110
PK 104,16,104,32
M 3,37,1
W1 26,2048
Z 4,48,0
WL 4
LO 17,17,107
LU 107,17
M 4,49,1
s_add_u32 s42, s94, s60
RA 236,1,6144
HS 27,107
Z 5,60,0
LL 16,86
LL 111,87
M 5,61,1
s_addc_u32 s43, s95, 0
LA 16
PK 107,17,105,33
M 5,62,2
LC_ 111
PK 105,17,105,32
M 5,63,3
SU 104
M 4,50,2
WL 4
LO 18,18,110
LU 110,18
M 4,51,3
RA 216,1,7168
HS 27,110
M 3,38,2
LL 17,88
LL 114,89
M 3,39,3
LA 17
PK 110,18,108,33
M 2,26,2
LC_ 114
PK 108,18,108,32
M 2,27,3
PO 110
M 1,14,2
WL 3
LO 16,16,111
LU 111,16
M 1,15,3
s_add_u32 s42, s94, s61
RA 220,1,8192
HS 27,111
M 0,2,2
LL 18,90
LL 115,91
M 0,3,3
s_addc_u32 s43, s95, 0
LA 18
PK 111,16,109,33
M 0,4,4
PFW
LC_ 115
PK 109,16,109,32
M 0,5,5
s_mov_b32 m0, %[pm]
s_nop 0
DF
PO 111
M 1,16,4
WL 3
LO 17,17,114
LU 114,17
M 1,17,5
RA 224,1,9216
HZ 29,114
M 2,28,4
LL 16,92
LL 118,93
M 2,29,5
LA 16
PK 114,17,112,33
M 3,40,4
LC_ 118
PK 112,17,112,32
M 3,41,5
SU 108
M 4,52,4
WL 3
LO 18,18,115
LU 115,18
M 4,53,5
s_add_u32 s42, s94, s62
RA 228,1,10240
HS 29,115
M 5,64,4
LL 17,94
LL 119,95
M 5,65,5
s_addc_u32 s43, s95, 0
LA 17
PK 115,18,113,33
M 5,66,0
LC_ 119
PK 113,18,113,32
M 5,67,1
SU 112
M 4,54,0
WL 3
LO 16,16,118
LU 118,16
M 4,55,1
RA 232,1,11264
HS 29,118
M 3,42,0
LL 18,96
LL 122,97
M 3,43,1
LA 18
PK 118,16,116,33
M 2,30,0
LC_ 122
PK 116,16,116,32
M 2,31,1
RV 252,22,5392
PO 118
M 1,18,0
WL 4
LO 17,17,119
LU 119,17
M 1,19,1
s_add_u32 s42, s94, s63
RA 236,1,12288
HS 29,119
M 0,6,0
LL 16,98
LL 123,99
M 0,7,1
s_addc_u32 s43, s95, 0
LA 16
PK 119,17,117,33
M 0,8,2
LC_ 123
PK 117,17,117,32
M 0,9,3
PO 119
M 1,20,2
WL 4
LO 18,18,122
LU 122,18
M 1,21,3
RA 216,1,13312
HZ 30,122
M 2,32,2
LL 17,100
LL 126,101
M 2,33,3
LA 17
PK 122,18,120,33
M 3,44,2
LC_ 126
PK 120,18,120,32
M 3,45,3
SU 116
M 4,56,2
WL 3
LO 16,16,123
LU 123,16
M 4,57,3
s_add_u32 s42, s94, s64
RA 220,1,14336
HS 30,123
M 5,68,2
LL 18,102
LL 127,103
M 5,69,3
s_addc_u32 s43, s95, 0
LA 18
PK 123,16,121,33
M 0,10,4
LC_ 127
PK 121,16,121,32
M 0,11,5
SU 120
M 1,22,4
WL 3
LO 17,17,126
LU 126,17
M 1,23,5
RA 224,1,15360
HS 30,126
M 2,34,4
LL 16,56
LL 104,57
M 2,35,5
LA 16
PK 126,17,124,33
M 3,46,4
LC_ 104
PK 124,17,124,32
M 3,47,5
ds_read_b64 v[14:15], v5 offset:2048
PO 126
M 4,58,4
WL 4
LO 18,18,127
LU 127,18
M 4,59,5
s_add_u32 s42, s94, s65
RA 228,1,16384
HS 30,127
M 5,70,4
LL 17,58
LL 105,59
M 5,71,5
s_addc_u32 s43, s95, 0
LA 17
PK 127,18,125,33
Z 6,0,0
LC_ 105
PK 125,18,125,32
M 6,1,1
PO 127
v_subrev_u32 v5, s39, v5
Z 7,12,0
WL 4
LO 104,16,104
LU 16,104
M 7,13,1
RA 232,1,17408
HZ 26,16
Z 8,24,0
WL 4
LL 18,60
LL 108,61
M 8,25,1
s_mul_i32 s53, s50, 25600
LA 18
v_add_f32 v14, v14, v15
Z 9,36,0
s_add_u32 s40, %[wlo], s53
LC_ 108
v_add_f32_dpp v14, v14, v14 quad_perm:[1,0,3,2] row_mask:0xf bank_mask:0xf
M 9,37,1
s_addc_u32 s41, %[whi], 0
Z 10,48,0
WL 3
LO 105,17,105
LU 17,105
M 10,49,1
s_mov_b32 m0, s38
RA 236,1,18432
HS 26,17
Z 11,60,0
LL 16,62
LL 109,63
M 11,61,1
LA 16
SU 124
M 11,62,2
LC_ 109
DW 0
M 11,63,3
WV 13
v_add_f32_dpp v14, v14, v14 quad_perm:[2,3,0,1] row_mask:0xf bank_mask:0xf
v_pk_add_f16 v27, v27, v30
M 10,50,2
WL 3
LO 108,18,108
LU 18,108
v_pk_add_f16 v27, v27, v29
M 10,51,3
s_add_u32 s37, s37, 32768
RA 216,1,19456
HS 26,18
HX 27
M 9,38,2
s_cmp_eq_u32 s37, s75
LL 17,64
LL 112,65
W1 27,3072
M 9,39,3
s_cselect_b32 s37, s74, s37
LA 17
DW 1024
v_readfirstlane_b32 s72, v11
M 8,26,2
LC_ 112
v_add_f32_dpp v14, v14, v14 row_half_mirror row_mask:0xf bank_mask:0xf
v_mov_b32 v12, s72
M 8,27,3
W2 12,12,16
v_cvt_f64_f32 v[14:15], v14
M 7,14,2
WL 5
LO 109,16,109
LU 16,109
M 7,15,3
RA 220,1,20480
HS 26,16
M 6,2,2
LL 18,66
LL 113,67
M 6,3,3
LA 18
s_mov_b64 exec, s[54:55]
SP
s_mov_b64 exec, -1
M 6,4,4
LC_ 113
DW 2048
M 6,5,5
v_add_u32 v4, s39, v4
s_mov_b64 exec, 1
AT
s_mov_b64 exec, -1
M 7,16,4
WL 4
LO 112,17,112
LU 17,112
M 7,17,5
s_mul_i32 s53, s51, 24
RA 224,1,21504
HZ 29,17
M 8,28,4
s_bitcmp1_b32 s51, 0
LL 16,68
LL 116,69
M 8,29,5
s_addc_u32 s53, s53, 0
LA 16
M 9,40,4
s_lshl_b32 s53, s53, 11
LC_ 116
M 9,41,5
s_bitcmp1_b64 s[92:93], s51
M 10,52,4
WL 3
LO 113,18,113
LU 18,113
M 10,53,5
s_cselect_b64 s[94:95], s[88:89], s[86:87]
RA 228,1,22528
HS 29,18
M 11,64,4
LL 17,70
LL 117,71
M 11,65,5
s_add_u32 s94, s94, s53
LA 17
M 11,66,0
s_addc_u32 s95, s95, 0
LC_ 117
M 11,67,1
s_add_u32 s51, s51, %[d]
M 10,54,0
WL 3
LO 116,16,116
LU 16,116
M 10,55,1
s_cmp_eq_u32 s51, %[wrap]
RA 232,1,23552
HS 29,16
M 9,42,0
s_cselect_b32 s51, %[rst], s51
LL 18,72
)"
R"(LL 120,73
M 9,43,1
s_add_u32 s40, s40, 3072
LA 18
M 8,30,0
s_addc_u32 s41, s41, 0
LC_ 120
s_add_u32 m0, s38, 3072
M 8,31,1
DW 0
M 7,18,0
WL 3
LO 117,17,117
LU 17,117
M 7,19,1
RA 236,1,24576
HS 29,17
M 6,6,0
LL 16,74
LL 121,75
M 6,7,1
LA 16
DW 1024
M 6,8,2
LC_ 121
DW 2048
M 6,9,3
s_lshl_b32 s53, s52, 8
v_add_u32 v1, s37, v0
M 7,20,2
WL 3
LO 120,18,120
LU 18,120
M 7,21,3
s_add_u32 s48, %[alo], s53
RA 216,1,0
HZ 30,18
M 8,32,2
s_addc_u32 s49, %[ahi], 0
LL 17,76
LL 124,77
M 8,33,3
s_add_u32 s52, s52, %[d]
LA 17
v_and_b32 v22, 0xfffffcff, v1
M 9,44,2
s_cmp_eq_u32 s52, %[wrap]
LC_ 124
M 9,45,3
s_cselect_b32 s52, %[rst], s52
DQ 3072
M 10,56,2
WL 3
LO 121,16,121
LU 16,121
M 10,57,3
s_add_u32 s50, s50, %[d]
RA 220,1,1024
HS 30,16
M 11,68,2
LL 18,78
LL 125,79
M 11,69,3
s_cmp_eq_u32 s50, %[wrap]
LA 18
RV 252,22,5120
M 6,10,4
s_cselect_b32 s50, %[rst], s50
LC_ 125
M 6,11,5
M 7,22,4
WL 4
LO 124,17,124
LU 17,124
M 7,23,5
s_add_u32 s38, s38, 32768
RA 224,1,2048
HS 30,17
M 8,34,4
s_cmp_eq_u32 s38, s77
LL 16,80
LL 106,81
M 8,35,5
s_cselect_b32 s38, s76, s38
LA 16
M 9,46,4
s_barrier
LC_ 106
M 9,47,5
s_sub_u32 s39, 0, s39
M 10,58,4
WL 3
LO 125,18,125
LU 18,125
M 10,59,5
RA 228,1,3072
HS 30,18
M 11,70,4
LL 17,82
LL 107,83
M 11,71,5
2:
LA 17
v_pk_add_f16 v26, v26, v30
Z 0,0,0
LC_ 107
ds_read_b32 v20, v5 offset:4096
M 0,1,1
v_pk_add_f16 v26, v26, v29
Z 1,12,0
WL 4
LO 16,16,106
LU 106,16
M 1,13,1
RA 232,1,4096
HZ 27,106
Z 2,24,0
LL 18,84
LL 110,85
M 2,25,1
LA 18
PK 106,16,104,33
Z 3,36,0
LC_ 110
PK 104,16,104,32
M 3,37,1
HX 26
Z 4,48,0
WL 4
LO 17,17,107
LU 107,17
M 4,49,1
s_add_u32 s42, s94, s60
RA 236,1,6144
HS 27,107
Z 5,60,0
LL 16,86
LL 111,87
M 5,61,1
s_addc_u32 s43, s95, 0
LA 16
PK 107,17,105,33
M 5,62,2
LC_ 111
PK 105,17,105,32
M 5,63,3
SU 104
M 4,50,2
WL 3
LO 18,18,110
LU 110,18
M 4,51,3
RA 216,1,7168
HS 27,110
M 3,38,2
LL 17,88
LL 114,89
W1 26,2048
M 3,39,3
LA 17
PK 110,18,108,33
M 2,26,2
LC_ 114
PK 108,18,108,32
M 2,27,3
PO 110
v_readfirstlane_b32 s59, v20
M 1,14,2
WL 4
LO 16,16,111
LU 111,16
M 1,15,3
s_add_u32 s42, s94, s61
RA 220,1,8192
HS 27,111
M 0,2,2
LL 18,90
LL 115,91
M 0,3,3
s_addc_u32 s43, s95, 0
LA 18
PK 111,16,109,33
M 0,4,4
LC_ 115
PK 109,16,109,32
M 0,5,5
PO 111
M 1,16,4
WL 3
LO 17,17,114
LU 114,17
M 1,17,5
RA 224,1,9216
HZ 29,114
M 2,28,4
LL 16,92
LL 118,93
M 2,29,5
LA 16
PK 114,17,112,33
M 3,40,4
LC_ 118
PK 112,17,112,32
M 3,41,5
SU 108
M 4,52,4
WL 3
LO 18,18,115
LU 115,18
M 4,53,5
s_add_u32 s42, s94, s62
RA 228,1,10240
HS 29,115
M 5,64,4
LL 17,94
LL 119,95
M 5,65,5
s_addc_u32 s43, s95, 0
LA 17
PK 115,18,113,33
M 5,66,0
LC_ 119
PK 113,18,113,32
M 5,67,1
SU 112
M 4,54,0
WL 3
LO 16,16,118
LU 118,16
M 4,55,1
RA 232,1,11264
HS 29,118
M 3,42,0
LL 18,96
LL 122,97
M 3,43,1
LA 18
PK 118,16,116,33
M 2,30,0
LC_ 122
PK 116,16,116,32
M 2,31,1
RV 252,22,5392
PO 118
M 1,18,0
WL 4
LO 17,17,119
LU 119,17
M 1,19,1
s_add_u32 s42, s94, s63
RA 236,1,12288
HS 29,119
M 0,6,0
LL 16,98
LL 123,99
M 0,7,1
s_addc_u32 s43, s95, 0
LA 16
PK 119,17,117,33
M 0,8,2
LC_ 123
PK 117,17,117,32
M 0,9,3
PO 119
M 1,20,2
WL 4
LO 18,18,122
LU 122,18
M 1,21,3
RA 216,1,13312
HZ 30,122
M 2,32,2
LL 17,100
LL 126,101
M 2,33,3
LA 17
PK 122,18,120,33
M 3,44,2
LC_ 126
PK 120,18,120,32
M 3,45,3
SU 116
M 4,56,2
WL 3
LO 16,16,123
LU 123,16
M 4,57,3
s_add_u32 s42, s94, s64
RA 220,1,14336
HS 30,123
M 5,68,2
LL 18,102
LL 127,103
M 5,69,3
s_addc_u32 s43, s95, 0
LA 18
PK 123,16,121,33
M 0,10,4
LC_ 127
PK 121,16,121,32
M 0,11,5
SU 120
M 1,22,4
WL 3
LO 17,17,126
LU 126,17
M 1,23,5
RA 224,1,15360
HS 30,126
M 2,34,4
LL 16,56
LL 104,57
M 2,35,5
LA 16
PK 126,17,124,33
M 3,46,4
LC_ 104
PK 124,17,124,32
M 3,47,5
ds_read_b64 v[14:15], v5 offset:2048
PO 126
M 4,58,4
WL 4
LO 18,18,127
LU 127,18
M 4,59,5
s_add_u32 s42, s94, s65
RA 228,1,16384
HS 30,127
M 5,70,4
LL 17,58
LL 105,59
M 5,71,5
s_addc_u32 s43, s95, 0
LA 17
PK 127,18,125,33
Z 6,0,0
LC_ 105
PK 125,18,125,32
M 6,1,1
PO 127
v_subrev_u32 v5, s39, v5
Z 7,12,0
WL 4
LO 104,16,104
LU 16,104
M 7,13,1
RA 232,1,17408
HZ 26,16
Z 8,24,0
WL 4
LL 18,60
LL 108,61
M 8,25,1
s_mul_i32 s53, s50, 25600
LA 18
v_add_f32 v14, v14, v15
Z 9,36,0
s_add_u32 s40, %[wlo], s53
LC_ 108
v_add_f32_dpp v14, v14, v14 quad_perm:[1,0,3,2] row_mask:0xf bank_mask:0xf
M 9,37,1
s_addc_u32 s41, %[whi], 0
Z 10,48,0
WL 3
LO 105,17,105
LU 17,105
M 10,49,1
s_mov_b32 m0, s38
RA 236,1,18432
HS 26,17
Z 11,60,0
LL 16,62
LL 109,63
M 11,61,1
LA 16
SU 124
M 11,62,2
LC_ 109
DW 0
M 11,63,3
WV 11
v_add_f32_dpp v14, v14, v14 quad_perm:[2,3,0,1] row_mask:0xf bank_mask:0xf
v_pk_add_f16 v27, v27, v30
M 10,50,2
WL 3
LO 108,18,108
LU 18,108
v_pk_add_f16 v27, v27, v29
M 10,51,3
s_add_u32 s37, s37, 32768
RA 216,1,19456
HS 26,18
HX 27
M 9,38,2
s_cmp_eq_u32 s37, s75
LL 17,64
LL 112,65
W1 27,3072
M 9,39,3
)"
R"(s_cselect_b32 s37, s74, s37
LA 17
DW 1024
v_readfirstlane_b32 s72, v11
M 8,26,2
LC_ 112
v_add_f32_dpp v14, v14, v14 row_half_mirror row_mask:0xf bank_mask:0xf
v_mov_b32 v12, s72
M 8,27,3
s_max_u32 s59, s59, %[nb]
W2 12,12,16
v_cvt_f64_f32 v[14:15], v14
M 7,14,2
WL 5
LO 109,16,109
LU 16,109
M 7,15,3
s_add_u32 %[nb], %[nb], 1
RA 220,1,20480
HS 26,16
M 6,2,2
LL 18,66
LL 113,67
M 6,3,3
LA 18
s_mov_b64 exec, s[54:55]
SP
s_mov_b64 exec, -1
M 6,4,4
LC_ 113
DW 2048
M 6,5,5
v_add_u32 v4, s39, v4
s_mov_b64 exec, 1
AT
s_mov_b64 exec, -1
M 7,16,4
WL 4
LO 112,17,112
LU 17,112
M 7,17,5
s_mul_i32 s53, s51, 24
RA 224,1,21504
HZ 29,17
M 8,28,4
s_bitcmp1_b32 s51, 0
LL 16,68
LL 116,69
M 8,29,5
s_addc_u32 s53, s53, 0
LA 16
M 9,40,4
s_lshl_b32 s53, s53, 11
LC_ 116
M 9,41,5
s_bitcmp1_b64 s[92:93], s51
M 10,52,4
WL 3
LO 113,18,113
LU 18,113
M 10,53,5
s_cselect_b64 s[94:95], s[88:89], s[86:87]
RA 228,1,22528
HS 29,18
M 11,64,4
LL 17,70
LL 117,71
M 11,65,5
s_add_u32 s94, s94, s53
LA 17
M 11,66,0
s_addc_u32 s95, s95, 0
LC_ 117
M 11,67,1
s_add_u32 s51, s51, %[d]
M 10,54,0
WL 3
LO 116,16,116
LU 16,116
M 10,55,1
s_cmp_eq_u32 s51, %[wrap]
RA 232,1,23552
HS 29,16
M 9,42,0
s_cselect_b32 s51, %[rst], s51
LL 18,72
LL 120,73
M 9,43,1
s_add_u32 s40, s40, 3072
LA 18
M 8,30,0
s_addc_u32 s41, s41, 0
LC_ 120
s_add_u32 m0, s38, 3072
M 8,31,1
DW 0
M 7,18,0
WL 3
LO 117,17,117
LU 17,117
M 7,19,1
RA 236,1,24576
HS 29,17
M 6,6,0
LL 16,74
LL 121,75
M 6,7,1
LA 16
DW 1024
M 6,8,2
LC_ 121
DW 2048
M 6,9,3
s_lshl_b32 s53, s52, 8
v_add_u32 v1, s37, v0
M 7,20,2
WL 3
LO 120,18,120
LU 18,120
M 7,21,3
s_add_u32 s48, %[alo], s53
RA 216,1,0
HZ 30,18
M 8,32,2
s_addc_u32 s49, %[ahi], 0
LL 17,76
LL 124,77
M 8,33,3
s_add_u32 s52, s52, %[d]
LA 17
v_and_b32 v22, 0xfffffcff, v1
M 9,44,2
s_cmp_eq_u32 s52, %[wrap]
LC_ 124
M 9,45,3
s_cselect_b32 s52, %[rst], s52
DQ 3072
M 10,56,2
WL 3
LO 121,16,121
LU 16,121
M 10,57,3
s_add_u32 s50, s50, %[d]
RA 220,1,1024
HS 30,16
M 11,68,2
LL 18,78
LL 125,79
M 11,69,3
s_cmp_eq_u32 s50, %[wrap]
LA 18
RV 252,22,5120
M 6,10,4
s_cselect_b32 s50, %[rst], s50
LC_ 125
M 6,11,5
M 7,22,4
WL 4
LO 124,17,124
LU 17,124
M 7,23,5
s_add_u32 s38, s38, 32768
RA 224,1,2048
HS 30,17
M 8,34,4
s_cmp_eq_u32 s38, s77
LL 16,80
LL 106,81
M 8,35,5
s_cselect_b32 s38, s76, s38
LA 16
M 9,46,4
s_barrier
LC_ 106
M 9,47,5
s_sub_u32 s39, 0, s39
M 10,58,4
WL 3
LO 125,18,125
LU 18,125
M 10,59,5
RA 228,1,3072
HS 30,18
M 11,70,4
s_cmp_lt_u32 s59, 32
LL 17,82
LL 107,83
M 11,71,5
s_cbranch_scc1 2b
LA 17
LC_ 107
ds_read_b64 v[14:15], v5 offset:2048
v_pk_add_f16 v26, v26, v30
v_pk_add_f16 v26, v26, v29
HX 26
v_subrev_u32 v5, s39, v5
W1 26,2048
WL 0
LO 16,16,106
LU 106,16
HZ 27,106
LL 18,84
LA 18
LL 110,85
PK 106,16,104,33
v_add_f32 v14, v14, v15
s_nop 1
v_add_f32_dpp v14, v14, v14 quad_perm:[1,0,3,2] row_mask:0xf bank_mask:0xf
s_nop 1
v_add_f32_dpp v14, v14, v14 quad_perm:[2,3,0,1] row_mask:0xf bank_mask:0xf
s_nop 1
v_add_f32_dpp v14, v14, v14 row_half_mirror row_mask:0xf bank_mask:0xf
v_cvt_f64_f32 v[14:15], v14
s_mov_b64 exec, s[54:55]
SP
s_mov_b64 exec, -1
LC_ 110
PK 104,16,104,32
LO 17,17,107
LU 107,17
s_add_u32 s42, s94, s60
s_addc_u32 s43, s95, 0
HS 27,107
LL 16,86
LA 16
LL 111,87
LC_ 111
PK 107,17,105,33
PK 105,17,105,32
SU 104
WL 0
LO 18,18,110
LU 110,18
HS 27,110
LL 17,88
LA 17
LL 114,89
LC_ 114
PK 110,18,108,33
PK 108,18,108,32
PO 110
LO 16,16,111
LU 111,16
HS 27,111
s_add_u32 s42, s94, s61
s_addc_u32 s43, s95, 0
LL 18,90
LA 18
LL 115,91
LC_ 115
PK 111,16,109,33
PK 109,16,109,32
PO 111
WL 0
LO 17,17,114
LU 114,17
HZ 29,114
LL 16,92
LA 16
LL 118,93
LC_ 118
PK 114,17,112,33
PK 112,17,112,32
SU 108
LO 18,18,115
LU 115,18
s_add_u32 s42, s94, s62
s_addc_u32 s43, s95, 0
HS 29,115
LL 17,94
LA 17
LL 119,95
LC_ 119
PK 115,18,113,33
PK 113,18,113,32
SU 112
WL 0
LO 16,16,118
LU 118,16
HS 29,118
LL 18,96
LA 18
LL 122,97
LC_ 122
PK 118,16,116,33
PK 116,16,116,32
PO 118
LO 17,17,119
LU 119,17
HS 29,119
s_add_u32 s42, s94, s63
s_addc_u32 s43, s95, 0
LL 16,98
LA 16
LL 123,99
LC_ 123
PK 119,17,117,33
PK 117,17,117,32
PO 119
WL 0
LO 18,18,122
LU 122,18
HZ 30,122
LL 17,100
LA 17
LL 126,101
LC_ 126
PK 122,18,120,33
PK 120,18,120,32
SU 116
LO 16,16,123
LU 123,16
s_add_u32 s42, s94, s64
s_addc_u32 s43, s95, 0
HS 30,123
LL 18,102
LA 18
LL 127,103
LC_ 127
PK 123,16,121,33
PK 121,16,121,32
SU 120
WL 0
LO 17,17,126
LU 126,17
HS 30,126
PK 126,17,124,33
PK 124,17,124,32
PO 126
LO 18,18,127
LU 127,18
HS 30,127
s_add_u32 s42, s94, s65
s_addc_u32 s43, s95, 0
v_pk_add_f16 v27, v27, v30
PK 127,18,125,33
v_pk_add_f16 v27, v27, v29
PK 125,18,125,32
HX 27
PO 127
W1 27,3072
s_nop 1
v_add_u32 v4, s39, v4
SU 124
s_lshl_b32 s53, s52, 8
s_add_u32 s48, %[alo], s53
s_addc_u32 s49, %[ahi], 0
s_add_u32 s52, s52, %[d]
s_cmp_eq_u32 s52, %[wrap]
s_cselect_b32 s52, %[rst], s52
WL 0
WV 0
s_barrier
s_sub_u32 s39, 0, s39
ds_read_b64 v[14:15], v5 offset:2048
s_nop 1
v_subrev_u32 v5, s39, v5
WL 0
v_add_f32 v14, v14, v15
s_nop 1
v_add_f32_dpp v14, v14, v14 quad_perm:[1,0,3,2] row_mask:0xf bank_mask:0xf
s_nop 1
v_add_f32_dpp v14, v14, v14 quad_perm:[2,3,0,1] row_mask:0xf bank_mask:0xf
s_nop 1
v_add_f32_dpp v14, v14, v14 row_half_mirror row_mask:0xf bank_mask:0xf
v_cvt_f64_f32 v[14:15], v14
s_mov_b64 exec, s[54:55]
SP
s_mov_b64 exec, -1
GE
s_waitcnt vmcnt(0) lgkmcnt(0)
8:
s_branch 6f
.Lk1s0:
FR
s_waitcnt lgkmcnt(0)
v_cmp_gt_u32 vcc, v19, v15
s_nop 4
s_cbranch_vccnz .Lk1s0
s_branch .Lk1r0
.Lk1s1:
FR
s_waitcnt lgkmcnt(0)
v_cmp_gt_u32 vcc, v19, v15
s_nop 4
s_cbranch_vccnz .Lk1s1
s_branch .Lk1r1
.Lk1s2:
FR
s_waitcnt lgkmcnt(0)
v_cmp_gt_u32 vcc, v19, v15
s_nop 4
s_cbranch_vccnz .Lk1s2
s_branch .Lk1r2
.Lk1s3:
FR
s_waitcnt lgkmcnt(0)
v_cmp_gt_u32 vcc, v19, v15
s_nop 4
s_cbranch_vccnz .Lk1s3
)"
R"(s_branch .Lk1r3
.Lk1s4:
FR
s_waitcnt lgkmcnt(0)
v_cmp_gt_u32 vcc, v19, v15
s_nop 4
s_cbranch_vccnz .Lk1s4
s_branch .Lk1r4
.Lk1s5:
FR
s_waitcnt lgkmcnt(0)
v_cmp_gt_u32 vcc, v19, v15
s_nop 4
s_cbranch_vccnz .Lk1s5
s_branch .Lk1r5
.Lk1s6:
FR
s_waitcnt lgkmcnt(0)
v_cmp_gt_u32 vcc, v19, v15
s_nop 4
s_cbranch_vccnz .Lk1s6
s_branch .Lk1r6
.Lk1s7:
FR
s_waitcnt lgkmcnt(0)
v_cmp_gt_u32 vcc, v19, v15
s_nop 4
s_cbranch_vccnz .Lk1s7
s_branch .Lk1r7
.Lk1s8:
FR
s_waitcnt lgkmcnt(0)
v_cmp_gt_u32 vcc, v19, v15
s_nop 4
s_cbranch_vccnz .Lk1s8
s_branch .Lk1r8
.Lk1s9:
FR
s_waitcnt lgkmcnt(0)
v_cmp_gt_u32 vcc, v19, v15
s_nop 4
s_cbranch_vccnz .Lk1s9
s_branch .Lk1r9
.Lk1s10:
FR
s_waitcnt lgkmcnt(0)
v_cmp_gt_u32 vcc, v19, v15
s_nop 4
s_cbranch_vccnz .Lk1s10
s_branch .Lk1r10
.Lk1s11:
FR
s_waitcnt lgkmcnt(0)
v_cmp_gt_u32 vcc, v19, v15
s_nop 4
s_cbranch_vccnz .Lk1s11
s_branch .Lk1r11
.Lk1s12:
FR
s_waitcnt lgkmcnt(0)
v_cmp_gt_u32 vcc, v19, v15
s_nop 4
s_cbranch_vccnz .Lk1s12
s_branch .Lk1r12
.Lk1s13:
FR
s_waitcnt lgkmcnt(0)
v_cmp_gt_u32 vcc, v19, v15
s_nop 4
s_cbranch_vccnz .Lk1s13
s_branch .Lk1r13
.Lk1s14:
FR
s_waitcnt lgkmcnt(0)
v_cmp_gt_u32 vcc, v19, v15
s_nop 4
s_cbranch_vccnz .Lk1s14
s_branch .Lk1r14
.Lk1s15:
FR
s_waitcnt lgkmcnt(0)
v_cmp_gt_u32 vcc, v19, v15
s_nop 4
s_cbranch_vccnz .Lk1s15
s_branch .Lk1r15
.Lk1s16:
FR
s_waitcnt lgkmcnt(0)
v_cmp_gt_u32 vcc, v19, v15
s_nop 4
s_cbranch_vccnz .Lk1s16
s_branch .Lk1r16
.Lk1s17:
FR
s_waitcnt lgkmcnt(0)
v_cmp_gt_u32 vcc, v19, v15
s_nop 4
s_cbranch_vccnz .Lk1s17
s_branch .Lk1r17
.Lk1s18:
FR
s_waitcnt lgkmcnt(0)
v_cmp_gt_u32 vcc, v19, v15
s_nop 4
s_cbranch_vccnz .Lk1s18
s_branch .Lk1r18
.Lk1s19:
FR
s_waitcnt lgkmcnt(0)
v_cmp_gt_u32 vcc, v19, v15
s_nop 4
s_cbranch_vccnz .Lk1s19
s_branch .Lk1r19
.Lk1s20:
FR
s_waitcnt lgkmcnt(0)
v_cmp_gt_u32 vcc, v19, v15
s_nop 4
s_cbranch_vccnz .Lk1s20
s_branch .Lk1r20
.Lk1s21:
FR
s_waitcnt lgkmcnt(0)
v_cmp_gt_u32 vcc, v19, v15
s_nop 4
s_cbranch_vccnz .Lk1s21
s_branch .Lk1r21
6:
.purgem M
.purgem Z
.purgem LZ
.purgem LS
.purgem LD
.purgem GZ
.purgem GS
.purgem CL
.purgem CH
.purgem XL
.purgem XN
.purgem F
.purgem U
.purgem E
.purgem A
.purgem R
.purgem C
.purgem S0
.purgem S
.purgem HC
.purgem HF
.purgem HM
.purgem HE
.purgem HG
.purgem HA
.purgem HR
.purgem HQ
.purgem HP
.purgem HY
.purgem HK
.purgem LQ
.purgem LL
.purgem LH
.purgem LA
.purgem LC_
.purgem LO
.purgem HZ
.purgem HS
.purgem HT
.purgem HX
.purgem UX
.purgem EX
.purgem AX
.purgem RX
.purgem TX
.purgem SX
.purgem AW
.purgem RA
.purgem RV
.purgem W2
.purgem W1
.purgem DW
.purgem DF
.purgem DX
.purgem ST
.purgem SU
.purgem SP
.purgem AT
.purgem PFA
.purgem PFN
.purgem PFW
.purgem PF0
.purgem WL
.purgem WV
.purgem LU
.purgem CF
.purgem XS
.purgem AR
.purgem DQ
.purgem GE
.purgem BW
.purgem BR
.purgem LK
.purgem PK
.purgem PO
.purgem LG
.purgem DY
.purgem FP
.purgem FR
)"
      : [nb] "=&s"(nb)
      : [xlo] "s"((unsigned)xp), [xhi] "s"((unsigned)(xp >> 32)), [hlo] "s"((unsigned)hp), [hhi] "s"((unsigned)(hp >> 32)),
        [glo] "s"((unsigned)gp), [ghi] "s"((unsigned)(gp >> 32)), [wlo] "s"((unsigned)wp), [whi] "s"((unsigned)(wp >> 32)),
        [w] "s"(w), [lds] "s"(lds0), [d] "s"(d), [wrap] "s"(wrap), [rst] "s"(rst), [pos0] "s"(pos0), [xo0] "s"(xo0), [xs1] "s"(xs1), [sb1] "s"(sb1), [r0o] "s"(r0o), [tsel] "s"(tsel), [s2] "s"(s2),
        [alo] "s"((unsigned)ap), [ahi] "s"((unsigned)(ap >> 32)), [ca0] "s"(ca0), [pm] "s"(pm), [pf] "s"(pf)
      : K1_CLOB);
  TS(6);
  MK_END(1);
}

typedef __bf16 bf16x8_t __attribute__((ext_vector_type(8)));
DI f32x4 mfma16(u32x4 a, u32x4 b, f32x4 c) {
  return __builtin_amdgcn_mfma_f32_16x16x32_bf16(__builtin_bit_cast(bf16x8_t, a), __builtin_bit_cast(bf16x8_t, b), c, 0, 0, 0);
}
DI float grn_gx(double s, float scale) { return round_bf16(__builtin_amdgcn_sqrtf(round_bf16(round_bf16((float)s * scale) + 1e-6f))); }
DI void slow_fc1(const u16* __restrict__ x, const u16* __restrict__ dw, char* __restrict__ hws, float* sacc, int w) {
  const int lane = __builtin_amdgcn_mbcnt_hi(~0u, __builtin_amdgcn_mbcnt_lo(~0u, 0u)), m = lane & 15, g = lane >> 4;
  for (int i = lane; i < HID; i += 64) sacc[i] = 0.f;
#pragma unroll 1
  for (int it = 0; it < 9; ++it) {
    const int sub = blockIdx.x * 36 + it * 4 + w;
    const unsigned px = sub_pixel(x, sub, m);
    u32x4 A[12];
    float sum = 0.f;
    const u16* dq = dw;
    asm volatile("" : "+s"(dq));
#pragma unroll
    for (int ks = 0; ks < 12; ++ks) {
      const u32x4 v = *(const u32x4*)(x + (size_t)px * CH + 32 * ks + 8 * g);
      const u32x4 d = *(const u32x4*)(dq + 32 * ks + 8 * g);
#pragma unroll
      for (int j = 0; j < 4; ++j) {
        A[ks][j] = pack_bf16(bf_lo(v[j]) * bf_lo(d[j]), bf_hi(v[j]) * bf_hi(d[j]));
        sum += bf_lo(A[ks][j]) + bf_hi(A[ks][j]);
      }
      asm volatile("" : "+v"(A[ks]));
    }
    sum += __shfl_xor(sum, 16); sum += __shfl_xor(sum, 32);
    const float mean = sum * (1.0f / CH);
    float var = 0.f;
#pragma unroll
    for (int ks = 0; ks < 12; ++ks) {
#pragma unroll
      for (int j = 0; j < 4; ++j) {
        const float a = bf_lo(A[ks][j]) - mean, b = bf_hi(A[ks][j]) - mean;
        var += a * a + b * b;
      }
      asm volatile("" : "+v"(A[ks]));
    }
    var += __shfl_xor(var, 16); var += __shfl_xor(var, 32);
    const float mb = round_bf16(mean);
    const float rd = 1.0f / round_bf16(sqrtf(round_bf16(round_bf16(var * (1.0f / CH)) + 1e-5f)));
#pragma unroll
    for (int ks = 0; ks < 12; ++ks)
#pragma unroll
      for (int j = 0; j < 4; ++j) A[ks][j] = pack_bf16((bf_lo(A[ks][j]) - mb) * rd, (bf_hi(A[ks][j]) - mb) * rd);
#pragma unroll 1
    for (int tc = 0; tc < 96; ++tc) {
      f32x4 acc = {0.f, 0.f, 0.f, 0.f};
#pragma unroll
      for (int ks = 0; ks < 12; ++ks) acc = mfma16(A[ks], *(const u32x4*)(g_buf + (size_t)(tc * 12 + ks) * 1024 + lane * 16), acc);
      float q = 0.f;
#pragma unroll
      for (int r = 0; r < 4; ++r) { acc[r] = gelu_tanh(acc[r]); q = __builtin_fmaf(acc[r], acc[r], q); }
      typedef unsigned u32x2_ __attribute__((ext_vector_type(2)));
      *(u32x2_*)(hws + h_piece(hws, sub, tc >> 1) + g * 256 + ((m + 4 * g) & 15) * 16 + (tc & 1) * 8) = (u32x2_){pack_bf16(acc[0], acc[1]), pack_bf16(acc[2], acc[3])};
      q += __shfl_xor(q, 16); q += __shfl_xor(q, 32);
      if (g == 0) sacc[tc * 16 + m] += q;
    }
  }
  __builtin_amdgcn_wave_barrier();
  __builtin_amdgcn_fence(__ATOMIC_ACQ_REL, "workgroup");
  for (int i = lane; i < HID; i += 64) atomicAdd((double*)(g_buf + G_ACC2) + i, (double)sacc[i]);
}
DI void slow_bar(unsigned* c, int w) {
  const int lane = __builtin_amdgcn_mbcnt_hi(~0u, __builtin_amdgcn_mbcnt_lo(~0u, 0u));
  asm volatile("s_waitcnt vmcnt(0)" ::: "memory");
  __syncthreads();
  if (w == 0 && lane == 0) {
    __threadfence();
    asm volatile("s_waitcnt vmcnt(0)" ::: "memory");
    __hip_atomic_fetch_add(c, 1u, __ATOMIC_RELAXED, __HIP_MEMORY_SCOPE_AGENT);
    for (int i = 0; i < (1 << 22) && __hip_atomic_load(c, __ATOMIC_RELAXED, __HIP_MEMORY_SCOPE_AGENT) < (unsigned)NWG; ++i) __builtin_amdgcn_s_sleep(32);
    __threadfence();
    asm volatile("s_waitcnt vmcnt(0)" ::: "memory");
  }
  __syncthreads();
}

#define K2_CLOB "memory", "vcc", "scc", "s48", "s49", "s50", "s51", "s52", "s53", "s54", "s55", "s56", "s57", "s58", "s59", "s60", "s61", "s62", "s63", "s64", "s65", "s66", "s67", \
  "s68", "s69", "s70", "s71", "s72", "s73", "s74", "s75", "s76", "s77", "s78", "s79", "s80", "s81", "s82", "s83", "s84", "s85", "s86", "s87", "s88", "s89", "s90", "s91", "s92", "s93", "s94", "s95", "s96", "s97", "s98", "s99", "s38", "s39", "s40", "s41", "s42", "s43", "s44", "s45", "s46", "s47", VA_CLOB
DI void slow_fc2(const u16* __restrict__ x, const char* __restrict__ hws, u16* __restrict__ out, const u16* __restrict__ grn_g, const u16* __restrict__ w2,
                 char* smem, int w) {
  float* sc = (float*)smem;
  float* red = sc + HID;
  u16* wf = (u16*)(smem + 8192);
  const int lane = __builtin_amdgcn_mbcnt_hi(~0u, __builtin_amdgcn_mbcnt_lo(~0u, 0u)), tid = w * 64 + lane, m = lane & 15, g = lane >> 4;
  __syncthreads();
  float part = 0.f;
  for (int c = tid; c < HID; c += 256) {
    const float gx = grn_gx(((const double*)(g_buf + G_ACC2))[c], 1.0f);
    sc[c] = gx; part += gx;
  }
#pragma unroll
  for (int o = 32; o > 0; o >>= 1) part += __shfl_xor(part, o);
  if (lane == 0) red[tid >> 6] = part;
  __syncthreads();
  const float gmean = round_bf16(round_bf16(((red[0] + red[1]) + (red[2] + red[3])) * (1.0f / HID)) + 1e-6f);
  for (int c = tid; c < HID; c += 256) sc[c] = (1.0f + ld_bf16(grn_g + c) * round_bf16(sc[c] / gmean)) ;
#pragma unroll 1
  for (int it = 0; it < 9; ++it) {
    const int sub = blockIdx.x * 36 + it * 4 + (tid >> 6);
    f32x4 acc[24];
#pragma unroll
    for (int i = 0; i < 24; ++i) acc[i] = (f32x4){0.f, 0.f, 0.f, 0.f};
#pragma unroll 1
    for (int t = 0; t < 48; ++t) {
      __syncthreads();
      for (int e = tid; e < 32 * CH; e += 256) {
        const int k = e / CH, n = e % CH;
        wf[((n >> 4) * 64 + (k >> 3) * 16 + (n & 15)) * 8 + (k & 7)] = (u16)pack_bf16(sc[32 * t + k] * ld_bf16(w2 + (size_t)(32 * t + k) * CH + n), 0.f);
      }
      __syncthreads();
      const char* hp = hws + h_piece(hws, sub, t) + (m >> 2) * 256 + (g >> 1) * 8 + 2 * (m & 3);
      u32x4 A;
#pragma unroll
      for (int j = 0; j < 4; ++j) {
        unsigned v[2];
#pragma unroll
        for (int k = 0; k < 2; ++k) {
          v[k] = *(const u16*)(hp + ((8 * (g & 1) + 2 * j + k + (m & 12)) & 15) * 16);
        }
        A[j] = v[0] | (v[1] << 16);
      }
#pragma unroll
      for (int i = 0; i < 24; ++i) acc[i] = mfma16(A, *(const u32x4*)(wf + (i * 64 + lane) * 8), acc[i]);
    }
#pragma unroll
    for (int i = 0; i < 24; ++i) {
      const int n = 16 * i + m;
      const float* bp = g_cs + 4 * 384 + cs_index(n);
      const float bias = (bp[0] + bp[384]) + (bp[768] + bp[1152]);
#pragma unroll
      for (int r = 0; r < 4; ++r) {
        const size_t o = (size_t)(sub_pixel(x, sub, 4 * g) + r) * CH + n;
        out[o] = (u16)pack_bf16(acc[i][r] + bias + ld_bf16(x + o), 0.f);
      }
    }
  }
}
__global__ __launch_bounds__(256, 1) void k2_fc2(const u16* __restrict__ x, char* __restrict__ hws, u16* __restrict__ out,
                                                 const u16* __restrict__ grn_g, const u16* __restrict__ w2, const u16* __restrict__ dw,
                                                 const u16* __restrict__ w1) {
  extern __shared__ __attribute__((aligned(16))) char smem[];
  const int w = __builtin_amdgcn_readfirstlane(threadIdx.x >> 6);
  const unsigned pg = w & 1, nh = w >> 1;
  const unsigned lds0 = lds_addr(smem) + 1024;
  MK_START(4); TS(9);
  const uint64_t pp = uni64((uint64_t)g_prm + (blockIdx.x * 4 + w) * 256), ap = (uint64_t)g_buf + K1_G_ACC + w * 1024;
  const unsigned wm = lds0 + w * 6144, ab = lds0 + nh * 12288;
  const unsigned hb = lds0 + K2_HB + pg * 2 * K2_RSZ, hm = hb + nh * K2_RSZ;
  const unsigned bm = w * 384, bb = lds0 + K2_E_OFF + nh * 768;
  const unsigned pm = lds0 + K2_PF_OFF, sb = lds0 + K2_S16_OFF, sw = sb + w * 256;
  uint64_t flg;
#define K2_OPS [pp] "s"(pp), [alo] "s"((unsigned)ap), [ahi] "s"((unsigned)(ap >> 32)), [wm] "s"(wm), [hm] "s"(hm), [bm] "s"(bm), [ab] "s"(ab), [hb] "s"(hb), [bb] "s"(bb), \
         [sb] "s"(sb), [sw] "s"(sw), [pm] "s"(pm)
  asm volatile(
R"(.macro MA t,a,b
v_mfma_f32_16x16x32_f16 a[\t:\t+3], v[\b:\b+3], a[\a:\a+3], a[\t:\t+3]
.endm
.macro MV t,a,b
v_mfma_f32_16x16x32_f16 v[\t:\t+3], v[\b:\b+3], a[\a:\a+3], v[\t:\t+3]
.endm
.macro NA t,a,b
v_mfma_f32_16x16x32_f16 a[\t:\t+3], a[\b:\b+3], a[\a:\a+3], a[\t:\t+3]
.endm
.macro NV t,a,b
v_mfma_f32_16x16x32_f16 v[\t:\t+3], a[\b:\b+3], a[\a:\a+3], v[\t:\t+3]
.endm
.macro AN d,s
v_and_b32 v[\d], s93, v[\s]
.endm
.macro SH d
v_lshrrev_b32 v[\d], 4, v[\d]
.endm
.macro PA_ d,h,r
v_perm_b32 v[\d], v[\h], v[\r], s54
.endm
.macro PB_ d,h,r
v_perm_b32 v[\d], v[\h], v[\r], s55
.endm
.macro AW a,s
v_accvgpr_write_b32 a[\a], v[\s]
.endm
.macro AR d,a
v_accvgpr_read_b32 v[\d], a[\a]
.endm
.macro UL d,x
v_lshlrev_b32 v[\d], 16, v[\x]
.endm
.macro UH d,x
v_and_b32 v[\d], s38, v[\x]
.endm
.macro AD d,b
v_fmac_f32 v[\d], s53, v[\b]
.endm
.macro EF d,e
v_fma_f32 v[\d], v[\d], s39, v[\e]
.endm
.macro TY d,o
ds_read_b64_tr_b16 v[\d:\d+1], v2 offset:\o
.endm
.macro TX d,o
ds_read_b64_tr_b16 v[\d:\d+1], v51 offset:\o
.endm
.macro RA d,o
ds_read_b128 a[\d:\d+3], v1 offset:\o
.endm
.macro RC d,o
ds_read_b128 a[\d:\d+3], v5 offset:\o
.endm
.macro BV d,o
ds_read_b128 v[\d:\d+3], v4 offset:\o
.endm
.macro BA d,o
ds_read_b128 a[\d:\d+3], v4 offset:\o
.endm
.macro PA a
s_mov_b32 m0, %[pm]
s_getpc_b64 s[46:47]
s_andn2_b32 s46, s46, 0x7f
s_add_u32 s46, s46, s81
s_addc_u32 s47, s47, 0
s_add_u32 s46, s46, \a
s_addc_u32 s47, s47, 0
.endm
.macro DF
global_load_lds_dwordx4 v0, s[46:47]
.endm
.macro DW o
global_load_lds_dwordx4 v0, s[40:41] offset:\o sc1
.endm
.macro DR o
global_load_lds_dwordx4 v0, s[40:41] offset:\o
.endm
.macro DH o
global_load_lds_dwordx4 v0, s[40:41] offset:\o nt
.endm
.macro DG o
s_mov_b32 exec_hi, 0
global_load_lds_dwordx4 v0, s[40:41] offset:\o nt
s_mov_b32 exec_hi, -1
.endm
.macro XL d,o
global_load_dwordx2 v[\d:\d+1], v47, s[44:45] offset:\o
.endm
.macro ST d,o
global_store_dwordx2 v50, v[\d:\d+1], s[42:43] offset:\o nt sc1
.endm
.macro SA d,o
global_store_dwordx2 v50, a[\d:\d+1], s[42:43] offset:\o nt sc1
.endm
.macro CV d,a,b
v_cvt_pk_bf16_f32 v[\d], v[\a], v[\b]
.endm
.macro RS d,o
ds_read_b128 v[\d:\d+3], v46 offset:\o
.endm
.macro PM d,s
v_pk_mul_f16 v[\d], v[\d], v[\s]
.endm
.macro PF d,n
v_pk_fma_f16 v[\d], v[\d], v[14+\n], v[22+\n]
.endm
.macro FS
global_store_dword v0, v54, s[96:97] sc1
.endm
.macro FL
global_load_dword v54, v55, s[98:99] sc1
.endm
.macro FB
s_cbranch_vccnz 21b
22:
.endm
.macro FC
s_cbranch_vccnz 26b
27:
.endm
.macro FG
s_or_b64 s[54:55], s[44:45], vcc
s_cbranch_scc1 23b
24:
.endm
.macro FX
s_branch 25f
21:
s_sleep 4
global_load_dword v54, v55, s[98:99] sc1
s_waitcnt vmcnt(0)
v_cmp_eq_u32 vcc, 0, v54
s_sub_u32 s80, s80, 1
s_cbranch_scc1 22f
s_nop 2
s_cbranch_vccnz 21b
s_branch 22f
26:
s_sleep 4
global_load_dword v54, v55, s[98:99] sc1
s_waitcnt vmcnt(0)
v_cmp_ne_u32 vcc, 2, v54
s_sub_u32 s80, s80, 1
s_cbranch_scc1 27f
s_nop 2
s_cbranch_vccnz 26b
s_branch 27f
23:
v_mov_b32 v10, 1
s_mov_b64 exec, 1
global_store_dword v0, v10, s[78:79] offset:384 sc1
s_mov_b64 exec, -1
s_nop 4
s_branch 24f
25:
.endm
.macro FP
ds_inc_u32 v0, v128
.endm
.macro FR
ds_read_b32 v10, v0
.endm
.macro FQ n
v_cmp_gt_u32 vcc, \n, v10
.endm
.macro FZ n
s_cbranch_vccnz .Lk2s\n
.Lk2r\n:
.endm
.macro FY n
.Lk2s\n:
FR
s_waitcnt lgkmcnt(0)
FQ \n
s_nop 4
s_cbranch_vccnz .Lk2s\n
s_branch .Lk2r\n
.endm
.macro WL n
s_waitcnt lgkmcnt(\n)
.endm
.macro WV n
s_waitcnt vmcnt(\n)
.endm
.macro M n
.if \n < 82
v_mfma_f32_16x16x32_f16 a[36+4*(\n/2):36+4*(\n/2)+3], v[56+8*((\n/2)%%6)+4*(\n&1):56+8*((\n/2)%%6)+4*(\n&1)+3], a[8*((\n/12)%%4)+4*(\n&1):8*((\n/12)%%4)+4*(\n&1)+3], a[36+4*(\n/2):36+4*(\n/2)+3]
.else
v_mfma_f32_16x16x32_f16 v[-32+4*(\n/2):-32+4*(\n/2)+3], v[56+8*((\n/2)%%6)+4*(\n&1):56+8*((\n/2)%%6)+4*(\n&1)+3], a[8*((\n/12)%%4)+4*(\n&1):8*((\n/12)%%4)+4*(\n&1)+3], v[-32+4*(\n/2):-32+4*(\n/2)+3]
.endif
.endm
.macro R1 n
.if \n < 64
RA 8*(((\n%%24)/2+2*((\n/24)&1))%%4)+4*(\n&1),1024*\n
.else
RC 8*(((\n%%24)/2+2*((\n/24)&1))%%4)+4*(\n&1),1024*\n-49152
.endif
.endm
.macro R n
R1 \n
R1 (\n+1)
.endm
.macro P n
.if \n & 2
PB_ 56+\n,104+8*(\n/16)+(\n&4)+2*((\n/8)&1)+(\n&1),56+(\n|2)
.else
PA_ 56+\n,104+8*(\n/16)+(\n&4)+2*((\n/8)&1)+(\n&1),56+(\n|2)
.endif
.endm
.macro Q n
P \n
P (\n+1)
.endm
.macro mw k
s_add_u32 m0, %[wm], 1024*\k
.endm
.macro mh k
s_add_u32 m0, %[hm], 512*\k
.endm
.macro ad r,k
s_add_u32 s40, s[\r], 512*\k
s_addc_u32 s41, s[\r+1], 0
.endm
.macro ae d,r,k
s_add_u32 s[\d], s[\r], 512*\k
s_addc_u32 s[\d+1], s[\r+1], 0
.endm
.macro bc m,b,d,r
s_bitcmp1_b32 s[\m], \b
s_cselect_b64 s[\d:\d+1], s[\r+2:\r+3], s[\r:\r+1]
.endm
.macro W k
DW 1024*\k
.endm
.macro H k
DH 1024*\k
.endm
k2_code:
s_load_dwordx16 s[48:63], %[pp], 0x0
s_load_dwordx16 s[64:79], %[pp], 0x40
s_load_dwordx16 s[84:99], %[pp], 0x80
s_load_dword s42, %[pp], 0xc0
v_mbcnt_lo_u32_b32 v7, -1, 0
v_mbcnt_hi_u32_b32 v7, -1, v7
v_lshlrev_b32 v0, 4, v7
v_add_u32 v88, 0x1000, v0
v_add_u32 v89, 0x2000, v0
s_mov_b32 s44, %[alo]
s_mov_b32 s45, %[ahi]
global_load_dwordx4 v[132:135], v0, s[44:45]
global_load_dwordx4 v[144:147], v88, s[44:45]
global_load_dwordx4 v[148:151], v89, s[44:45]
ae 44,44,24
global_load_dwordx4 v[168:171], v0, s[44:45]
global_load_dwordx4 v[172:175], v88, s[44:45]
global_load_dwordx4 v[192:195], v89, s[44:45]
s_waitcnt lgkmcnt(0)
s_mov_b64 s[82:83], s[66:67]
bc 49,0,66,60
mh 0
ad 66,0
H 0
DG 1024
ae 44,44,24
global_load_dwordx4 v[196:199], v0, s[44:45]
global_load_dwordx4 v[216:219], v88, s[44:45]
global_load_dwordx4 v[220:223], v89, s[44:45]
ae 44,44,24
global_load_dwordx4 v[224:227], v0, s[44:45]
global_load_dwordx4 v[228:231], v88, s[44:45]
global_load_dwordx4 v[240:243], v89, s[44:45]
v_and_b32 v8, 15, v7
v_lshrrev_b32 v9, 4, v7
v_lshlrev_b32 v10, 2, v7
v_add_u32 v202, s65, v0
global_load_dword v244, v10, s[92:93] offset:0
global_load_dword v245, v10, s[92:93] offset:1024
global_load_dword v246, v10, s[92:93] offset:2048
global_load_dwordx4 v[152:155], v0, s[88:89]
s_mov_b64 exec, 0xff
global_load_dwordx4 v[156:159], v202, s[88:89]
s_mov_b64 exec, -1
s_branch 28f
FY 4
FY 8
FY 12
FY 16
FY 20
FY 24
FY 28
FY 32
FY 36
FY 40
FY 44
FY 48
FY 52
FY 56
FY 60
FY 64
FY 68
FY 72
FY 76
FY 80
FY 84
FY 88
FY 92
FY 96
FY 100
FY 104
FY 108
FY 112
FY 116
FY 120
FY 124
FY 128
28:
FX
s_mov_b32 s92, s64
s_mov_b32 s93, 0x0f0f0f0f
v_add_u32 v1, %[ab], v0
v_add_u32 v5, 0xc000, v1
v_and_b32 v10, 3, v7
v_lshrrev_b32 v11, 2, v7
v_lshl_add_u32 v11, v10, 2, v11
v_and_b32 v11, 15, v11
mw 0
ad 86,0
DR 0
v_lshl_add_u32 v11, v10, 4, v11
v_lshl_add_u32 v2, v11, 4, %[hb]
v_lshl_add_u32 v51, v11, 3, %[hb]
v_lshl_add_u32 v4, v8, 4, %[bb]
s_mov_b32 s38, 0xffff0000
s_mov_b32 s80, 0x40000
s_mov_b32 s81, s52
s_mov_b32 s39, s54
v_lshl_add_u32 v46, v9, 4, %[sb]
v_add_u32 v46, 8192, v46
v_bfe_u32 v10, v7, 1, 2
DR 1024
v_bfe_u32 v11, v7, 3, 1
v_and_b32 v12, 1, v7
v_lshl_add_u32 v11, v11, 1, v12
v_lshl_add_u32 v10, v10, 2, v11
v_lshl_add_u32 v10, v9, 4, v10
v_lshl_add_u32 v90, v10, 2, %[sw]
DR 2048
v_lshl_add_u32 v91, v7, 2, %[sw]
s_and_b32 s42, s51, 0xffff
s_lshr_b32 s43, s51, 16
s_add_u32 s42, s42, %[sb]
s_add_u32 s43, s43, %[sb]
v_lshl_add_u32 v200, v9, 4, s42
v_mov_b32 v201, s43
v_mov_b32 v128, 0x362aaa8f
v_mov_b32 v129, 0x362aaa8f
v_mov_b32 v130, 0x362aaa8f
v_mov_b32 v131, 0x362aaa8f
mw 3
ad 86,6
DR 0
AW 32,128
AW 33,129
AW 34,130
AW 35,131
v_mov_b32 v95, 0
ds_write_b32 v0, v95
s_nop 1
v_mov_b32 v95, 0x7f800000
s_mov_b32 s44, 0x44100000
WV 19
v_add_f64 v[132:133], v[132:133], v[168:169]
WV 18
v_add_f64 v[144:145], v[144:145], v[172:173]
DR 1024
WV 18
v_add_f64 v[148:149], v[148:149], v[192:193]
v_add_f64 v[134:135], v[134:135], v[170:171]
v_add_f64 v[146:147], v[146:147], v[174:175]
v_add_f64 v[150:151], v[150:151], v[194:195]
WV 15
v_add_f64 v[132:133], v[132:133], v[196:197]
WV 14
v_add_f64 v[144:145], v[144:145], v[216:217]
DR 2048
WV 14
v_add_f64 v[148:149], v[148:149], v[220:221]
v_add_f64 v[134:135], v[134:135], v[198:199]
v_add_f64 v[146:147], v[146:147], v[218:219]
v_add_f64 v[150:151], v[150:151], v[222:223]
WV 13
v_add_f64 v[132:133], v[132:133], v[224:225]
WV 12
v_add_f64 v[144:145], v[144:145], v[228:229]
bc 49,1,66,60
mh 9
ad 66,192
H 0
WV 12
)"
R"(v_add_f64 v[148:149], v[148:149], v[240:241]
v_add_f64 v[134:135], v[134:135], v[226:227]
v_add_f64 v[146:147], v[146:147], v[230:231]
v_add_f64 v[150:151], v[150:151], v[242:243]
v_cvt_f32_f64 v248, v[132:133]
v_cvt_f32_f64 v252, v[144:145]
DG 1024
v_cvt_f32_f64 v72, v[148:149]
v_cvt_f32_f64 v249, v[134:135]
v_cvt_f32_f64 v253, v[146:147]
v_cvt_f32_f64 v73, v[150:151]
v_mul_f32 v250, s55, v248
v_mul_f32 v254, s55, v252
bc 48,1,64,56
mh 12
ad 64,192
H 0
v_mul_f32 v74, s55, v72
v_mul_f32 v251, s55, v249
v_mul_f32 v255, s55, v253
v_mul_f32 v75, s55, v73
CV 251,250,251
CV 255,254,255
DG 1024
CV 75,74,75
UL 250,251
UL 254,255
UL 74,75
UH 251,251
UH 255,255
mw 24
ad 86,48
DR 0
UH 75,75
v_add_f32 v250, 0x358637bd, v250
v_add_f32 v254, 0x358637bd, v254
v_add_f32 v74, 0x358637bd, v74
v_add_f32 v251, 0x358637bd, v251
v_add_f32 v255, 0x358637bd, v255
DR 1024
v_add_f32 v75, 0x358637bd, v75
CV 251,250,251
CV 255,254,255
CV 75,74,75
UL 250,251
UL 254,255
DR 2048
UL 74,75
UH 251,251
UH 255,255
UH 75,75
v_sqrt_f32 v250, v250
v_sqrt_f32 v254, v254
mw 27
ad 86,54
DR 0
v_sqrt_f32 v74, v74
v_sqrt_f32 v251, v251
v_sqrt_f32 v255, v255
v_sqrt_f32 v75, v75
CV 251,250,251
CV 255,254,255
DR 1024
CV 75,74,75
UL 250,251
UL 254,255
UL 74,75
UH 251,251
UH 255,255
DR 2048
UH 75,75
v_cmp_le_f32 vcc, s44, v248
v_cmp_le_f32 s[54:55], s44, v249
s_nop 0
v_cndmask_b32 v248, v95, v250, vcc
v_cndmask_b32 v249, v95, v251, s[54:55]
v_add_f32 v248, v248, v249
v_cmp_le_f32 vcc, s44, v252
v_cmp_le_f32 s[54:55], s44, v253
s_nop 0
v_cndmask_b32 v252, v95, v254, vcc
v_cndmask_b32 v253, v95, v255, s[54:55]
v_add_f32 v252, v252, v253
v_cmp_le_f32 vcc, s44, v72
v_cmp_le_f32 s[54:55], s44, v73
s_nop 0
v_cndmask_b32 v72, v95, v74, vcc
v_cndmask_b32 v73, v95, v75, s[54:55]
bc 49,2,66,60
mh 18
ad 66,384
H 0
v_add_f32 v72, v72, v73
v_add_f32 v92, v248, v252
v_add_f32 v92, v92, v72
s_nop 1
v_add_f32_dpp v92, v92, v92 row_shr:1 row_mask:0xf bank_mask:0xf
s_nop 1
v_add_f32_dpp v92, v92, v92 row_shr:2 row_mask:0xf bank_mask:0xf
s_nop 1
v_add_f32_dpp v92, v92, v92 row_shr:4 row_mask:0xf bank_mask:0xf
s_nop 1
v_add_f32_dpp v92, v92, v92 row_shr:8 row_mask:0xf bank_mask:0xf
s_nop 1
v_add_f32_dpp v92, v92, v92 row_bcast:15 row_mask:0xa bank_mask:0xf
s_nop 1
v_add_f32_dpp v92, v92, v92 row_bcast:31 row_mask:0xc bank_mask:0xf
ds_write_b32 v91, v92 offset:7168
v_mov_b32 v10, %[sb]
WL 0
s_barrier
ds_read_b32 v96, v10 offset:7420
ds_read_b32 v97, v10 offset:7676
ds_read_b32 v98, v10 offset:7932
ds_read_b32 v99, v10 offset:8188
WV 21
UL 76,244
WV 20
UL 80,245
WV 19
UL 84,246
UH 77,244
DG 1024
UH 81,245
UH 85,246
v_mov_b32 v93, 0
WL 0
v_add_f32 v96, v96, v97
v_add_f32 v98, v98, v99
v_add_f32 v96, v96, v98
bc 48,2,64,56
mh 21
ad 64,384
H 0
v_mul_f32 v94, 0x3a2aaaab, v96
v_cmp_nlt_f32 s[44:45], v96, v95
CV 94,94,94
UL 94,94
v_add_f32 v94, 0x358637bd, v94
CV 94,94,94
UL 94,94
H 1
v_rcp_f32 v94, v94
s_nop 0
v_mul_f32 v250, v94, v250
v_mul_f32 v254, v94, v254
v_mul_f32 v74, v94, v74
v_mul_f32 v251, v94, v251
v_mul_f32 v255, v94, v255
v_mul_f32 v75, v94, v75
H 2
CV 251,250,251
CV 255,254,255
CV 75,74,75
UL 250,251
UL 254,255
UL 74,75
mw 48
ad 86,96
DR 0
UH 251,251
UH 255,255
UH 75,75
v_fma_f32 v250, v76, v250, 1.0
v_fma_f32 v254, v80, v254, 1.0
v_fma_f32 v74, v84, v74, 1.0
DR 1024
v_fma_f32 v251, v77, v251, 1.0
v_fma_f32 v255, v81, v255, 1.0
v_fma_f32 v75, v85, v75, 1.0
v_cvt_pk_f16_f32 v76, v250, v251
v_cvt_pk_f16_f32 v80, v254, v255
v_cvt_pk_f16_f32 v84, v74, v75
ds_write_b32 v90, v76 offset:8192
ds_write_b32 v90, v80 offset:1024
ds_write_b32 v90, v84 offset:2048
v_max3_f32 v93, |v250|, |v251|, v93
DR 2048
v_max3_f32 v93, |v254|, |v255|, v93
v_max3_f32 v93, |v74|, |v75|, v93
v_cmp_lt_f32 vcc, 0x43000000, v93
WL 0
WV 16
s_barrier
TY 106,520
TY 110,14344
TX 58,0
TX 62,13824
TY 66,512
TY 70,14336
RS 14,0
RS 18,64
ds_read_b128 v[176:179], v200
ds_read_b128 v[180:183], v201
R 0
R 2
FG
s_mov_b32 s54, 0x06020400
s_mov_b32 s55, 0x07030501
WL 13
AN 104,106
AN 105,107
SH 106
SH 107
mw 51
ad 86,102
DR 0
AN 106,106
AN 107,107
WL 12
AN 108,110
AN 109,111
SH 110
SH 111
DR 1024
AN 110,110
AN 111,111
WL 10
Q 0
Q 2
DR 2048
Q 4
Q 6
R 4
R 6
WL 7
v_pk_mul_f16 v22, v14, s92
v_pk_mul_f16 v23, v15, s92
v_pk_mul_f16 v24, v16, s92
v_pk_mul_f16 v25, v17, s92
v_pk_mul_f16 v26, v18, s92
v_pk_mul_f16 v27, v19, s92
v_pk_mul_f16 v28, v20, s92
v_pk_mul_f16 v29, v21, s92
PF 56,0
PF 57,1
PF 58,2
PF 59,3
PF 60,4
PF 61,5
PF 62,6
PF 63,7
Q 8
Q 10
Q 12
Q 14
PF 64,0
PF 65,1
PF 66,2
PF 67,3
PF 68,4
PF 69,5
PF 70,6
PF 71,7
PM 152,176
PM 153,177
PM 154,178
PM 155,179
PM 156,180
PM 157,181
PM 158,182
PM 159,183
s_mov_b32 s42, 0x1c001c00
v_pk_mul_f16 v152, v152, s42
v_pk_mul_f16 v153, v153, s42
v_pk_mul_f16 v154, v154, s42
v_pk_mul_f16 v155, v155, s42
v_pk_mul_f16 v156, v156, s42
v_pk_mul_f16 v157, v157, s42
v_pk_mul_f16 v158, v158, s42
v_pk_mul_f16 v159, v159, s42
global_store_dwordx4 v0, v[152:155], s[90:91] sc1
s_mov_b64 exec, 0xff
global_store_dwordx4 v202, v[156:159], s[90:91] sc1
s_mov_b64 exec, -1
s_mov_b32 s42, 0x3c003c00
v_dot2_f32_f16 v52, v152, s42, 0
v_dot2_f32_f16 v53, v156, s42, 0
s_nop 1
v_dot2_f32_f16 v52, v153, s42, v52
v_dot2_f32_f16 v53, v157, s42, v53
s_nop 1
v_dot2_f32_f16 v52, v154, s42, v52
v_dot2_f32_f16 v53, v158, s42, v53
s_nop 1
v_dot2_f32_f16 v52, v155, s42, v52
v_dot2_f32_f16 v53, v159, s42, v53
s_nop 1
WL 0
v_mfma_f32_16x16x32_f16 a[36:39], v[56:59], a[0:3], a[32:35]
mw 72
RS 14,128
RS 18,192
TY 106,5128
M 1
ad 86,144
DR 0
TY 110,18952
v_mfma_f32_16x16x32_f16 a[60:63], v[60:63], a[12:15], a[32:35]
DR 1024
TX 32,4608
TX 36,18432
M 12
DR 2048
TY 40,5120
TY 44,18944
v_mfma_f32_16x16x32_f16 a[64:67], v[64:67], a[8:11], a[32:35]
mw 75
M 15
R 10
v_mfma_f32_16x16x32_f16 a[40:43], v[68:71], a[4:7], a[32:35]
ad 86,150
DR 0
M 2
R 8
v_mfma_f32_16x16x32_f16 a[88:91], v[64:67], a[16:19], a[32:35]
WL 10
DR 1024
v_pk_mul_f16 v22, v14, s92
M 27
DR 2048
v_pk_mul_f16 v23, v15, s92
v_mfma_f32_16x16x32_f16 a[84:87], v[56:59], a[16:19], a[32:35]
v_pk_mul_f16 v24, v16, s92
v_pk_mul_f16 v25, v17, s92
M 25
v_pk_mul_f16 v26, v18, s92
v_pk_mul_f16 v27, v19, s92
v_mfma_f32_16x16x32_f16 a[108:111], v[56:59], a[24:27], a[32:35]
WV 19
v_pk_mul_f16 v28, v20, s92
v_pk_mul_f16 v29, v21, s92
M 37
WL 0
AN 104,106
AN 105,107
v_mfma_f32_16x16x32_f16 a[112:115], v[64:67], a[24:27], a[32:35]
s_barrier
R 24
M 39
R 26
v_mfma_f32_16x16x32_f16 a[136:139], v[64:67], a[0:3], a[32:35]
mw 0
SH 106
SH 107
M 51
ad 86,192
DR 0
v_mfma_f32_16x16x32_f16 a[132:135], v[56:59], a[0:3], a[32:35]
DR 1024
AN 106,106
M 49
R 28
v_mfma_f32_16x16x32_f16 a[156:159], v[56:59], a[8:11], a[32:35]
DR 2048
AN 107,107
M 61
mw 3
AN 108,110
AN 109,111
v_mfma_f32_16x16x32_f16 a[160:163], v[64:67], a[8:11], a[32:35]
ad 86,198
DR 0
M 63
WL 4
R 30
v_mfma_f32_16x16x32_f16 a[184:187], v[64:67], a[16:19], a[32:35]
DR 1024
SH 110
M 75
DR 2048
SH 111
v_mfma_f32_16x16x32_f16 a[180:183], v[56:59], a[16:19], a[32:35]
AN 110,110
AN 111,111
M 73
WL 4
R 32
v_mfma_f32_16x16x32_f16 v[136:139], v[56:59], a[24:27], v[128:131]
PA_ 30,104,32
PA_ 31,105,33
M 85
PB_ 32,104,32
PB_ 33,105,33
v_mfma_f32_16x16x32_f16 v[140:143], v[64:67], a[24:27], v[128:131]
PA_ 34,108,36
PA_ 35,109,37
M 87
WL 4
R 34
v_mfma_f32_16x16x32_f16 v[164:167], v[64:67], a[0:3], v[128:131]
PB_ 36,108,36
PB_ 37,109,37
M 99
PF 30,0
PF 31,1
v_mfma_f32_16x16x32_f16 v[160:163], v[56:59], a[0:3], v[128:131]
PF 32,2
PF 33,3
M 97
)"
R"(WL 4
PF 34,4
PF 35,5
v_mfma_f32_16x16x32_f16 v[184:187], v[56:59], a[8:11], v[128:131]
WV 14
PF 36,6
PF 37,7
M 109
WL 0
AW 200,30
AW 201,31
v_mfma_f32_16x16x32_f16 v[188:191], v[64:67], a[8:11], v[128:131]
s_barrier
R 48
TY 114,6664
M 111
R 50
TY 118,20488
v_mfma_f32_16x16x32_f16 v[208:211], v[56:59], a[16:19], v[128:131]
mw 24
AW 202,32
AW 203,33
M 121
ad 86,240
DR 0
v_mfma_f32_16x16x32_f16 v[232:235], v[60:63], a[28:31], v[128:131]
DR 1024
AW 204,34
M 132
DR 2048
AW 205,35
v_mfma_f32_16x16x32_f16 v[236:239], v[64:67], a[24:27], v[128:131]
mw 27
AW 206,36
AW 207,37
PA_ 38,106,40
PA_ 39,107,41
PB_ 40,106,40
PB_ 41,107,41
M 135
WL 0
R 54
PA_ 42,110,44
PA_ 43,111,45
PB_ 44,110,44
PB_ 45,111,45
AN 112,114
AN 113,115
SH 114
SH 115
v_mfma_f32_16x16x32_f16 v[212:215], v[68:71], a[20:23], v[128:131]
ad 86,246
DR 0
PF 38,0
PF 39,1
PF 40,2
PF 41,3
AN 114,114
AN 115,115
AN 116,118
AN 117,119
M 122
R 52
PF 42,4
PF 43,5
PF 44,6
PF 45,7
SH 118
SH 119
AN 118,118
AN 119,119
NA 36,0,200
DR 1024
AW 208,38
AW 209,39
AW 210,40
AW 211,41
AW 212,42
NA 36,4,204
DR 2048
AW 213,43
AW 214,44
AW 215,45
NA 60,12,204
bc 49,3,66,60
TX 32,6144
TX 36,19968
NA 60,8,200
mh 0
TY 40,6656
TY 44,20480
WL 2
PA_ 30,112,32
PA_ 31,113,33
PB_ 32,112,32
PB_ 33,113,33
PA_ 34,116,36
PA_ 35,117,37
PB_ 36,116,36
PB_ 37,117,37
NA 64,8,208
ad 66,576
H 0
PF 30,0
PF 31,1
PF 32,2
PF 33,3
PF 34,4
PF 35,5
PF 36,6
PF 37,7
NA 64,12,212
WL 0
DG 1024
AW 216,30
AW 217,31
AW 218,32
AW 219,33
AW 220,34
AW 221,35
AW 222,36
AW 223,37
NA 40,4,212
bc 48,3,64,56
TY 106,9736
TY 110,23560
NA 40,0,208
mh 3
PA_ 38,114,40
PA_ 39,115,41
PB_ 40,114,40
PB_ 41,115,41
PA_ 42,118,44
PA_ 43,119,45
PB_ 44,118,44
PB_ 45,119,45
PF 38,0
PF 39,1
v_mfma_f32_16x16x32_f16 a[44:47], a[216:219], a[0:3], a[32:35]
ad 64,576
H 0
PF 40,2
PF 41,3
PF 42,4
PF 43,5
PF 44,6
PF 45,7
AW 224,38
AW 225,39
NA 44,4,220
H 1
AW 226,40
AW 227,41
AW 228,42
AW 229,43
AW 230,44
AW 231,45
v_mfma_f32_16x16x32_f16 a[68:71], a[220:223], a[12:15], a[32:35]
H 2
RS 14,256
TX 58,9216
NA 68,8,216
RS 18,320
TX 62,23040
TY 66,9728
v_mfma_f32_16x16x32_f16 a[72:75], a[224:227], a[8:11], a[32:35]
WL 5
TY 70,23552
AN 104,106
NA 72,12,228
R 58
v_mfma_f32_16x16x32_f16 a[48:51], a[228:231], a[4:7], a[32:35]
AN 105,107
SH 106
NA 48,0,224
R 56
v_mfma_f32_16x16x32_f16 a[96:99], a[224:227], a[16:19], a[32:35]
SH 107
AN 106,106
NA 96,20,228
WL 7
v_pk_mul_f16 v22, v14, s92
v_pk_mul_f16 v23, v15, s92
v_mfma_f32_16x16x32_f16 a[92:95], a[216:219], a[16:19], a[32:35]
v_pk_mul_f16 v24, v16, s92
v_pk_mul_f16 v25, v17, s92
NA 92,20,220
v_pk_mul_f16 v26, v18, s92
v_pk_mul_f16 v27, v19, s92
NA 88,16,208
v_pk_mul_f16 v28, v20, s92
v_pk_mul_f16 v29, v21, s92
NA 88,20,212
AN 107,107
AN 108,110
NA 84,16,200
AN 109,111
SH 110
NA 84,20,204
SH 111
AN 110,110
NA 108,24,200
WL 5
AN 111,111
P 0
NA 108,28,204
Q 1
NA 112,24,208
Q 3
NA 112,28,212
Q 5
v_mfma_f32_16x16x32_f16 a[116:119], a[216:219], a[24:27], a[32:35]
WV 17
P 7
PF 56,0
NA 116,28,220
WL 0
PF 57,1
PF 58,2
v_mfma_f32_16x16x32_f16 a[120:123], a[224:227], a[24:27], a[32:35]
s_barrier
R 72
TY 114,11272
NA 120,28,228
R 74
TY 118,25096
v_mfma_f32_16x16x32_f16 a[144:147], a[224:227], a[0:3], a[32:35]
mw 48
v_mov_b32 v54, 1
s_mov_b64 exec, 1
FS
s_mov_b64 exec, -1
NA 144,4,228
ad 86,288
DR 0
TX 74,10752
v_mfma_f32_16x16x32_f16 a[140:143], a[216:219], a[0:3], a[32:35]
PF 59,3
PF 60,4
TX 78,24576
NA 140,4,220
DR 1024
PF 61,5
TY 82,11264
NA 136,0,208
PF 62,6
PF 63,7
TY 86,25088
NA 136,4,212
DR 2048
P 8
TY 122,12808
NA 132,0,200
Q 9
TY 126,26632
NA 132,4,204
WL 6
R 76
TX 90,12288
NA 156,8,200
mw 51
Q 11
TX 94,26112
NA 156,12,204
ad 86,294
DR 0
TY 98,12800
NA 160,8,208
Q 13
TY 102,26624
NA 160,12,212
DR 1024
P 15
v_mfma_f32_16x16x32_f16 a[164:167], a[216:219], a[8:11], a[32:35]
PF 64,0
PF 65,1
NA 164,12,220
DR 2048
PF 66,2
v_mfma_f32_16x16x32_f16 a[168:171], a[224:227], a[8:11], a[32:35]
PF 67,3
PF 68,4
NA 168,12,228
R 78
v_mfma_f32_16x16x32_f16 a[192:195], a[224:227], a[16:19], a[32:35]
bc 49,4,66,60
PF 69,5
NA 192,20,228
mh 9
PF 70,6
PF 71,7
v_mfma_f32_16x16x32_f16 a[188:191], a[216:219], a[16:19], a[32:35]
ad 66,768
H 0
NA 188,20,220
AN 112,114
AN 113,115
NA 184,16,208
DG 1024
NA 184,20,212
SH 114
SH 115
NA 180,16,200
bc 48,4,64,56
AN 114,114
NA 180,20,204
WL 2
R 80
NV 136,24,200
mh 12
AN 115,115
AN 116,118
NV 136,28,204
ad 64,768
H 0
NV 140,24,208
AN 117,119
SH 118
NV 140,28,212
H 1
SH 119
v_mfma_f32_16x16x32_f16 v[144:147], a[216:219], a[24:27], v[128:131]
AN 118,118
AN 119,119
NV 144,28,220
H 2
P 16
v_mfma_f32_16x16x32_f16 v[148:151], a[224:227], a[24:27], v[128:131]
Q 17
NV 148,28,228
R 82
v_mfma_f32_16x16x32_f16 v[172:175], a[224:227], a[0:3], v[128:131]
Q 19
NV 172,4,228
Q 21
v_mfma_f32_16x16x32_f16 v[168:171], a[216:219], a[0:3], v[128:131]
P 23
PF 72,0
NV 168,4,220
PF 73,1
PF 74,2
NV 164,0,208
PF 75,3
PF 76,4
NV 164,4,212
PF 77,5
PF 78,6
NV 160,0,200
PF 79,7
P 24
NV 160,4,204
WL 2
Q 25
NV 184,8,200
Q 27
NV 184,12,204
Q 29
NV 188,8,208
P 31
PF 80,0
NV 188,12,212
PF 81,1
PF 82,2
v_mfma_f32_16x16x32_f16 v[192:195], a[216:219], a[8:11], v[128:131]
WV 23
PF 83,3
PF 84,4
NV 192,12,220
WL 0
PF 85,5
PF 86,6
v_mfma_f32_16x16x32_f16 v[196:199], a[224:227], a[8:11], v[128:131]
s_barrier
R 0
NV 196,12,228
R 2
NV 208,16,200
mw 72
PF 87,7
AN 120,122
NV 208,20,204
ad 86,336
DR 0
NV 232,28,204
AN 121,123
SH 122
NV 232,24,200
DR 1024
SH 123
NV 236,24,208
AN 122,122
AN 123,123
NV 236,28,212
DR 2048
AN 124,126
NV 212,20,212
AN 125,127
SH 126
NV 212,16,208
mw 75
)"
R"(SH 127
AN 126,126
v_mfma_f32_16x16x32_f16 v[216:219], a[216:219], a[16:19], v[128:131]
ad 86,342
DR 0
NV 216,20,220
AN 127,127
P 32
v_mfma_f32_16x16x32_f16 v[240:243], a[220:223], a[28:31], v[128:131]
DR 1024
P 33
NV 240,24,216
Q 34
v_mfma_f32_16x16x32_f16 v[244:247], a[224:227], a[24:27], v[128:131]
DR 2048
P 36
NV 244,28,228
R 6
v_mfma_f32_16x16x32_f16 v[220:223], a[228:231], a[20:23], v[128:131]
Q 37
NV 220,16,224
WL 2
R 4
M 0
P 39
PF 88,0
M 1
PF 89,1
PF 90,2
M 13
PF 91,3
PF 92,4
M 12
PF 93,5
PF 94,6
M 14
PF 95,7
P 40
M 15
Q 41
M 3
Q 43
M 2
Q 45
M 4
P 47
PF 96,0
M 5
PF 97,1
PF 98,2
M 17
PF 99,3
PF 100,4
M 16
PF 101,5
PF 102,6
M 18
PF 103,7
M 19
RS 14,384
RS 18,448
M 7
M 6
v_mfma_f32_16x16x32_f16 a[52:55], v[88:91], a[0:3], a[32:35]
v_lshrrev_b32 v9, 8, v0
v_bfe_u32 v8, v0, 4, 4
M 9
v_lshlrev_b32 v10, 2, v9
v_mfma_f32_16x16x32_f16 a[76:79], v[92:95], a[12:15], a[32:35]
s_mov_b32 s42, 0xcb527610
M 20
s_movk_i32 s43, 0xc00
WL 0
v_pk_mul_f16 v22, v14, s92
v_pk_mul_f16 v23, v15, s92
v_mfma_f32_16x16x32_f16 a[80:83], v[96:99], a[8:11], a[32:35]
v_pk_mul_f16 v24, v16, s92
v_pk_mul_f16 v25, v17, s92
M 23
R 10
v_mfma_f32_16x16x32_f16 a[56:59], v[100:103], a[4:7], a[32:35]
v_pk_mul_f16 v26, v18, s92
v_pk_mul_f16 v27, v19, s92
M 10
R 8
v_mfma_f32_16x16x32_f16 a[104:107], v[96:99], a[16:19], a[32:35]
v_pk_mul_f16 v28, v20, s92
v_pk_mul_f16 v29, v21, s92
M 35
v_lshlrev_b32 v11, 3, v8
v_bfe_u32 v3, s42, v10, 4
v_mfma_f32_16x16x32_f16 a[100:103], v[88:91], a[16:19], a[32:35]
v_add_u32 v10, 16, v10
v_mad_u32_u24 v3, v3, s43, v11
M 33
v_bfe_u32 v48, s42, v10, 4
v_add_u32 v10, 1, v9
M 30
v_mad_u32_u24 v48, v48, s43, v11
v_and_b32 v10, 2, v10
M 31
v_sub_u32 v10, 1, v10
v_mul_i32_i24 v49, s43, v10
M 28
M 29
M 26
M 27
M 24
M 25
M 36
M 37
M 38
M 39
M 40
M 41
M 42
M 43
WV 18
v_mfma_f32_16x16x32_f16 a[124:127], v[88:91], a[24:27], a[32:35]
WL 0
M 45
s_barrier
R 24
TY 106,520
v_mfma_f32_16x16x32_f16 a[128:131], v[96:99], a[24:27], a[32:35]
TY 110,14344
TX 32,0
TX 36,13824
M 47
R 26
TY 40,512
v_mfma_f32_16x16x32_f16 a[152:155], v[96:99], a[0:3], a[32:35]
TY 44,14336
TY 114,2056
TY 118,15880
M 59
v_mfma_f32_16x16x32_f16 a[148:151], v[88:91], a[0:3], a[32:35]
M 57
M 54
WL 9
AN 104,106
AN 105,107
M 55
bc 49,5,66,60
SH 106
M 52
mh 18
SH 107
AN 106,106
M 53
ad 66,960
H 0
M 50
WL 8
AN 107,107
AN 108,110
M 51
AN 109,111
SH 110
M 48
SH 111
AN 110,110
M 49
R 28
M 60
DG 1024
M 61
WL 5
AN 111,111
PA_ 30,104,32
M 62
PA_ 31,105,33
PB_ 32,104,32
M 63
PB_ 33,105,33
PA_ 34,108,36
M 64
PA_ 35,109,37
PB_ 36,108,36
M 65
PB_ 37,109,37
PF 30,0
M 66
v_lshlrev_b32 v55, 4, v0
PF 31,1
M 67
FL
PF 32,2
v_mfma_f32_16x16x32_f16 a[172:175], v[88:91], a[8:11], a[32:35]
PF 33,3
PF 34,4
M 69
PF 35,5
PF 36,6
v_mfma_f32_16x16x32_f16 a[176:179], v[96:99], a[8:11], a[32:35]
PF 37,7
AW 200,30
M 71
R 30
v_mfma_f32_16x16x32_f16 v[132:135], v[96:99], a[16:19], v[128:131]
AW 201,31
AW 202,32
M 83
AW 203,33
AW 204,34
v_mfma_f32_16x16x32_f16 a[196:199], v[88:91], a[16:19], a[32:35]
AW 205,35
AW 206,36
M 81
WL 2
AW 207,37
PA_ 38,106,40
M 78
PA_ 39,107,41
PB_ 40,106,40
TX 32,1536
M 79
PB_ 41,107,41
PA_ 42,110,44
TX 36,15360
M 76
PA_ 43,111,45
PB_ 44,110,44
M 77
PB_ 45,111,45
PF 38,0
M 74
PF 39,1
PF 40,2
M 75
PF 41,3
PF 42,4
M 72
PF 43,5
PF 44,6
M 73
R 32
M 84
PF 45,7
AW 208,38
M 85
bc 48,5,64,56
AW 209,39
M 86
mh 21
AW 210,40
AW 211,41
M 87
ad 64,960
H 0
M 88
AW 212,42
AW 213,43
M 89
AW 214,44
AW 215,45
M 90
TY 40,2048
TY 44,15872
TY 122,3592
M 91
H 1
AN 112,114
TY 126,17416
v_mfma_f32_16x16x32_f16 v[152:155], v[88:91], a[24:27], v[128:131]
AN 113,115
SH 114
M 93
SH 115
AN 114,114
v_mfma_f32_16x16x32_f16 v[156:159], v[96:99], a[24:27], v[128:131]
AN 115,115
AN 116,118
M 95
R 34
v_mfma_f32_16x16x32_f16 v[180:183], v[96:99], a[0:3], v[128:131]
H 2
AN 117,119
M 107
SH 118
SH 119
v_mfma_f32_16x16x32_f16 v[176:179], v[88:91], a[0:3], v[128:131]
AN 118,118
AN 119,119
M 105
WL 6
PA_ 30,112,32
PA_ 31,113,33
M 102
PB_ 32,112,32
PB_ 33,113,33
M 103
PA_ 34,116,36
PA_ 35,117,37
M 100
PB_ 36,116,36
PB_ 37,117,37
M 101
PF 30,0
PF 31,1
M 98
PF 32,2
PF 33,3
M 99
PF 34,4
PF 35,5
M 96
PF 36,6
PF 37,7
M 97
AW 216,30
AW 217,31
M 108
AW 218,32
AW 219,33
M 109
AW 220,34
AW 221,35
M 110
WV 3
v_cmp_eq_u32 vcc, 0, v54
AW 222,36
M 111
WL 0
AW 223,37
PA_ 38,114,40
M 112
PA_ 39,115,41
PB_ 40,114,40
TX 32,3072
M 113
FB
PB_ 41,115,41
PA_ 42,118,44
TX 36,16896
M 114
PA_ 43,119,45
PB_ 44,118,44
M 115
PB_ 45,119,45
PF 38,0
v_mfma_f32_16x16x32_f16 v[200:203], v[88:91], a[8:11], v[128:131]
PF 39,1
PF 40,2
M 117
s_barrier
R 48
v_mfma_f32_16x16x32_f16 v[204:207], v[96:99], a[8:11], v[128:131]
mw 0
PF 41,3
PF 42,4
M 119
R 50
M 120
ad 84,384
W 0
M 121
W 1
PF 43,5
M 133
W 2
PF 44,6
M 132
mw 3
PF 45,7
AW 224,38
M 134
ad 84,390
W 0
M 135
W 1
AW 225,39
M 123
W 2
AW 226,40
M 122
mw 24
AW 227,41
AW 228,42
M 124
ad 84,432
W 0
M 125
W 1
AW 229,43
M 137
W 2
AW 230,44
M 136
mw 27
AW 231,45
AN 120,122
M 138
ad 84,438
W 0
TY 40,3584
M 139
W 1
TY 44,17408
M 127
)"
R"(W 2
AN 121,123
M 126
SH 122
SH 123
v_mfma_f32_16x16x32_f16 v[224:227], v[88:91], a[16:19], v[128:131]
AN 122,122
AN 123,123
M 129
AN 124,126
AN 125,127
v_mfma_f32_16x16x32_f16 v[248:251], v[92:95], a[28:31], v[128:131]
SH 126
SH 127
M 140
AN 126,126
AN 127,127
v_mfma_f32_16x16x32_f16 v[252:255], v[96:99], a[24:27], v[128:131]
WL 2
PA_ 30,120,32
PA_ 31,121,33
M 143
R 54
v_mfma_f32_16x16x32_f16 v[228:231], v[100:103], a[20:23], v[128:131]
PB_ 32,120,32
PB_ 33,121,33
M 130
R 52
NA 36,0,200
PA_ 34,124,36
PA_ 35,125,37
NA 36,4,204
PB_ 36,124,36
PB_ 37,125,37
NA 60,12,204
PF 30,0
PF 31,1
NA 60,8,200
PF 32,2
PF 33,3
NA 64,8,208
PF 34,4
PF 35,5
NA 64,12,212
PF 36,6
PF 37,7
NA 40,4,212
AW 232,30
AW 233,31
NA 40,0,208
AW 234,32
AW 235,33
NA 44,0,216
AW 236,34
AW 237,35
NA 44,4,220
WL 4
AW 238,36
AW 239,37
PA_ 38,122,40
NA 68,12,220
PA_ 39,123,41
PB_ 40,122,40
PB_ 41,123,41
NA 68,8,216
PA_ 42,126,44
PA_ 43,127,45
PB_ 44,126,44
NA 72,8,224
PB_ 45,127,45
PF 38,0
PF 39,1
NA 72,12,228
PF 40,2
PF 41,3
PF 42,4
NA 48,4,228
PF 43,5
PF 44,6
PF 45,7
NA 48,0,224
AW 240,38
AW 241,39
AW 242,40
NA 52,0,232
AW 243,41
AW 244,42
AW 245,43
NA 52,4,236
AW 246,44
AW 247,45
NA 76,12,236
NA 76,8,232
NA 80,8,240
NA 80,12,244
R 58
NA 56,4,244
NA 56,0,240
WL 2
R 56
NA 104,16,240
NA 104,20,244
NA 100,16,232
NA 100,20,236
NA 96,16,224
NA 96,20,228
NA 92,16,216
NA 92,20,220
NA 88,16,208
NA 88,20,212
NA 84,16,200
NA 84,20,204
NA 108,24,200
NA 108,28,204
NA 112,24,208
NA 112,28,212
NA 116,24,216
NA 116,28,220
NA 120,24,224
NA 120,28,228
NA 124,24,232
WL 0
NA 124,28,236
s_barrier
R 72
TY 106,5128
NA 128,24,240
mw 48
TY 110,18952
TX 58,4608
TX 62,18432
NA 128,28,244
R 74
TY 66,5120
NA 152,0,240
ad 84,480
W 0
TY 70,18944
NA 152,4,244
v_lshrrev_b32 v8, 1, v0
s_mov_b64 exec, s[76:77]
global_load_dwordx2 v[6:7], v8, s[78:79] sc1
s_mov_b64 exec, -1
NA 148,0,232
TY 114,6664
TY 118,20488
TX 74,6144
NA 148,4,236
WL 7
TX 78,19968
TY 82,6656
TY 86,20480
NA 144,0,224
W 1
AN 104,106
TY 122,8200
NA 144,4,228
AN 105,107
SH 106
TY 126,22024
NA 140,0,216
SH 107
AN 106,106
TX 90,7680
NA 140,4,220
AN 107,107
AN 108,110
TX 94,21504
NA 136,0,208
W 2
AN 109,111
TY 98,8192
NA 136,4,212
WL 9
SH 110
SH 111
TY 102,22016
NA 132,0,200
AN 110,110
AN 111,111
NA 132,4,204
R 76
NA 156,8,200
mw 51
Q 0
NA 156,12,204
ad 84,486
W 0
NA 160,8,208
Q 2
NA 160,12,212
Q 4
NA 164,8,216
Q 6
NA 164,12,220
W 1
P 8
NA 168,8,224
Q 9
NA 168,12,228
Q 11
NA 172,8,232
Q 13
NA 172,12,236
W 2
P 15
NA 176,8,240
AN 112,114
AN 113,115
NA 176,12,244
R 78
NV 132,16,240
SH 114
SH 115
NV 132,20,244
bc 49,6,66,60
AN 114,114
NA 196,16,232
mh 0
AN 115,115
AN 116,118
NA 196,20,236
ad 66,1152
H 0
NA 192,16,224
AN 117,119
SH 118
NA 192,20,228
SH 119
AN 118,118
NA 188,16,216
WL 2
AN 119,119
P 16
NA 188,20,220
DG 1024
NA 184,16,208
Q 17
NA 184,20,212
Q 19
NA 180,16,200
Q 21
NA 180,20,204
R 80
NV 136,24,200
bc 48,6,64,56
P 23
NV 136,28,204
mh 3
Q 24
NV 140,24,208
ad 64,1152
H 0
NV 140,28,212
Q 26
NV 144,24,216
Q 28
NV 144,28,220
Q 30
NV 148,24,224
H 1
AN 120,122
NV 148,28,228
AN 121,123
SH 122
NV 152,24,232
SH 123
AN 122,122
NV 152,28,236
AN 123,123
AN 124,126
NV 156,24,240
H 2
AN 125,127
NV 156,28,244
R 82
NV 180,0,240
SH 126
SH 127
NV 180,4,244
AN 126,126
AN 127,127
NV 176,0,232
Q 32
NV 176,4,236
Q 34
NV 172,0,224
Q 36
NV 172,4,228
Q 38
NV 168,0,216
Q 40
NV 168,4,220
Q 42
NV 164,0,208
Q 44
NV 164,4,212
Q 46
NV 160,0,200
NV 160,4,204
WL 2
NV 184,8,200
NV 184,12,204
NV 188,8,208
NV 188,12,212
NV 192,8,216
NV 192,12,220
NV 196,8,224
NV 196,12,228
WV 18
NV 200,8,232
WL 0
NV 200,12,236
s_barrier
R 0
NV 204,8,240
mw 72
NV 204,12,244
R 2
NV 208,16,200
ad 84,528
W 0
NV 208,20,204
NV 232,28,204
NV 232,24,200
NV 236,24,208
W 1
NV 236,28,212
NV 212,20,212
NV 212,16,208
NV 216,16,216
W 2
NV 216,20,220
ae 44,70,0
NV 240,28,220
s_bitcmp1_b32 s50, 0
s_cselect_b32 s51, 24576, 0
v_add_u32 v47, s51, v3
NV 240,24,216
XL 14,0
NV 244,24,224
mw 75
NV 244,28,228
ad 84,534
W 0
NV 220,20,228
XL 16,768
NV 220,16,224
NV 224,16,232
NV 224,20,236
W 1
NV 248,28,236
NV 248,24,232
NV 252,24,240
XL 18,1536
NV 252,28,244
R 6
NV 228,20,244
W 2
NV 228,16,240
WL 2
R 4
M 0
M 1
M 13
XL 20,2304
M 12
M 14
M 15
WV 20
v_or_b32 v6, v6, v7
s_mov_b64 exec, s[76:77]
v_cmp_ne_u32 %[flg], 0, v6
s_mov_b64 exec, -1
M 3
M 2
M 4
M 5
M 17
M 16
M 18
M 19
M 7
M 6
M 8
M 9
M 21
M 20
M 22
M 23
R 10
M 11
M 10
WL 2
R 8
M 34
M 35
M 32
M 33
M 30
)"
R"(M 31
M 28
M 29
M 26
M 27
M 24
M 25
M 36
M 37
M 38
M 39
M 40
M 41
M 42
M 43
WV 15
M 44
WL 0
M 45
s_barrier
R 24
M 46
mw 0
M 47
R 26
M 58
ad 84,576
W 0
M 59
TY 106,9736
TY 110,23560
M 56
M 57
W 1
M 54
M 55
M 52
W 2
M 53
M 50
M 51
mw 3
M 48
ad 84,582
W 0
M 49
R 28
M 60
WL 3
AN 104,106
AN 105,107
M 61
W 1
SH 106
M 62
SH 107
AN 106,106
M 63
WL 2
AN 107,107
AN 108,110
M 64
W 2
AN 109,111
M 65
SH 110
SH 111
M 66
AN 110,110
AN 111,111
M 67
bc 49,7,66,60
TY 114,11272
M 68
mh 9
TY 118,25096
M 69
ad 66,1344
H 0
M 70
M 71
R 30
M 82
DG 1024
M 83
M 80
M 81
bc 48,7,64,56
TY 122,12808
M 78
mh 12
TY 126,26632
M 79
ad 64,1344
H 0
M 76
WL 5
AN 112,114
AN 113,115
M 77
SH 114
SH 115
M 74
H 1
AN 114,114
M 75
WL 4
AN 115,115
AN 116,118
M 72
AN 117,119
SH 118
M 73
R 32
M 84
H 2
SH 119
M 85
AN 118,118
AN 119,119
M 86
WL 3
AN 120,122
AN 121,123
M 87
s_load_dwordx4 s[88:91], %[pp], 0xc0
s_load_dwordx2 s[86:87], %[pp], 0xd0
SH 122
M 88
SH 123
AN 122,122
M 89
WL 2
AN 123,123
AN 124,126
M 90
AN 125,127
SH 126
M 91
SH 127
AN 126,126
M 92
AN 127,127
M 93
M 94
M 95
R 34
M 106
M 107
M 104
M 105
M 102
M 103
M 100
M 101
M 98
M 99
M 96
M 97
R 48
M 108
M 109
M 110
M 111
M 112
M 113
M 114
M 115
M 116
s_waitcnt lgkmcnt(0)
M 117
M 118
M 119
R 50
M 120
v_mov_b32 v54, v52
M 121
M 133
M 132
TX 58,9216
TX 62,23040
M 134
v_permlane32_swap_b32 v52, v54
M 135
M 123
M 122
TY 66,9728
TY 70,23552
M 124
v_add_f32 v52, v52, v54
v_mov_b32 v54, v52
M 125
M 137
ae 44,70,0
M 136
TX 74,10752
TX 78,24576
M 138
s_bfe_u32 s51, s50, 0x10001
WL 4
Q 0
M 139
Q 2
M 127
Q 4
M 126
TY 82,11264
TY 86,25088
M 128
Q 6
M 129
WL 4
Q 8
M 141
Q 10
M 140
TX 90,12288
TX 94,26112
M 142
Q 12
M 143
R 54
M 131
Q 14
M 130
TY 98,12800
TY 102,26624
M 0
R 52
M 1
WL 10
Q 16
M 13
Q 18
M 12
Q 20
M 14
Q 22
M 15
WL 8
Q 24
M 3
Q 26
M 2
Q 28
M 4
Q 30
M 5
WL 6
Q 32
M 17
Q 34
M 16
Q 36
M 18
Q 38
M 19
WL 2
Q 40
M 7
Q 42
M 6
Q 44
M 8
Q 46
M 9
v_permlane16_swap_b32 v52, v54
v_mad_i32_i24 v47, v49, s51, v48
M 21
XL 22,0
M 20
XL 24,768
M 22
XL 26,1536
v_add_f32 v52, v52, v54
M 23
R 58
M 11
XL 28,2304
M 10
WL 2
R 56
M 34
v_permlane16_swap_b32 v52, v53
v_mov_b32 v54, s88
M 35
v_mov_b32 v53, s89
M 32
M 33
M 30
v_permlane16_swap_b32 v54, v53
v_bfe_u32 v53, v0, 2, 6
M 31
M 28
M 29
v_add_u32 v54, v53, v54
M 26
M 27
WV 9
M 24
WL 0
FP
M 25
M 36
M 37
M 38
M 39
M 40
UL 6,14
UH 7,14
M 41
AR 8,36
AR 9,60
M 42
AD 8,6
AD 9,7
M 43
AW 36,8
AW 60,9
M 44
UL 6,15
UH 7,15
M 45
FR
AR 8,84
M 46
AR 9,108
AD 8,6
M 47
AD 9,7
AW 84,8
M 58
AW 108,9
UL 6,16
M 59
UH 7,16
AR 8,37
M 56
AR 9,61
AD 8,6
M 57
AD 9,7
AW 37,8
M 54
AW 61,9
UL 6,17
M 55
WL 0
FQ 4
UH 7,17
M 52
AR 8,85
AR 9,109
M 53
AD 8,6
AD 9,7
M 50
FZ 4
R 72
M 51
mw 24
R 74
M 48
TY 106,520
TY 110,14344
M 49
R 76
M 60
ad 84,624
W 0
M 61
AW 85,8
AW 109,9
M 62
UL 6,18
UH 7,18
M 63
W 1
AR 8,38
M 64
AR 9,62
AD 8,6
M 65
AD 9,7
AW 38,8
M 66
W 2
AW 62,9
M 67
WL 3
AN 104,106
AN 105,107
M 68
SH 106
SH 107
M 69
mw 27
AN 106,106
AN 107,107
M 70
)"
R"(WL 2
AN 108,110
AN 109,111
M 71
R 78
M 82
SH 110
SH 111
M 83
AN 110,110
AN 111,111
M 80
TY 114,2056
TY 118,15880
M 81
ad 84,630
W 0
M 78
UL 6,19
UH 7,19
M 79
AR 8,86
AR 9,110
M 76
W 1
AD 8,6
M 77
AD 9,7
AW 86,8
M 74
AW 110,9
UL 6,20
M 75
W 2
UH 7,20
M 72
AR 8,39
AR 9,63
M 73
WL 1
R 80
M 84
mw 48
AN 112,114
AN 113,115
M 85
SH 114
SH 115
M 86
AN 114,114
AN 115,115
M 87
WL 2
AN 116,118
AN 117,119
M 88
SH 118
SH 119
M 89
AN 118,118
AN 119,119
M 90
TY 122,3592
TY 126,17416
M 91
ad 84,672
M 92
W 0
AD 8,6
M 93
AD 9,7
AW 39,8
M 94
AW 63,9
UL 6,21
M 95
R 82
M 106
W 1
UH 7,21
M 107
AR 8,87
AR 9,111
M 104
AD 8,6
AD 9,7
M 105
WL 3
AN 120,122
AN 121,123
M 102
SH 122
SH 123
M 103
AN 122,122
AN 123,123
M 100
WL 2
AN 124,126
AN 125,127
M 101
SH 126
SH 127
M 98
AN 126,126
AN 127,127
M 99
W 2
AW 87,8
M 96
AW 111,9
M 97
R 0
M 108
mw 51
M 109
ad 84,678
W 0
M 110
M 111
M 112
W 1
M 113
M 114
M 115
W 2
M 116
M 117
M 118
bc 49,8,66,60
M 119
WL 2
R 2
M 120
mh 18
s_bitcmp1_b32 s87, 0
s_cselect_b32 exec_lo, 0xffffff, 0
s_mov_b32 exec_hi, 0
global_atomic_add_f32 v54, v52, s[90:91] sc1
s_mov_b64 exec, -1
M 121
ad 66,1536
H 0
M 133
M 132
TX 58,0
TX 62,13824
M 134
DG 1024
M 135
M 123
M 122
TY 66,512
TY 70,14336
M 124
bc 48,8,64,56
M 125
mh 21
M 137
ad 64,1536
H 0
M 136
TX 74,1536
TX 78,15360
M 138
WL 4
Q 0
M 139
Q 2
M 127
Q 4
M 126
TY 82,2048
TY 86,15872
M 128
Q 6
M 129
WL 4
Q 8
M 141
Q 10
M 140
TX 90,3072
TX 94,16896
M 142
Q 12
M 143
R 6
M 131
Q 14
M 130
TY 98,3584
TY 102,17408
M 0
R 4
M 1
WL 10
Q 16
M 13
Q 18
M 12
Q 20
M 14
Q 22
M 15
WL 8
Q 24
M 3
Q 26
M 2
Q 28
M 4
Q 30
M 5
WL 6
Q 32
M 17
Q 34
M 16
Q 36
M 18
Q 38
M 19
WL 2
Q 40
M 7
Q 42
M 6
Q 44
M 8
Q 46
M 9
H 1
M 21
ae 44,68,0
M 20
s_bitcmp1_b32 s50, 2
s_cselect_b32 s51, 24576, 0
v_add_u32 v47, s51, v3
M 22
H 2
XL 30,0
M 23
R 10
M 11
XL 32,768
M 10
WL 2
R 8
M 34
XL 34,1536
M 35
XL 36,2304
M 32
M 33
M 30
M 31
M 28
M 29
M 26
M 27
WV 10
M 24
WL 0
FP
M 25
M 36
M 37
s_bitcmp1_b32 s87, 1
s_cselect_b32 exec_lo, 0xffffff, 0
s_mov_b32 exec_hi, 0
global_atomic_add_f32 v54, v52, s[90:91] sc1
s_mov_b64 exec, -1
M 38
M 39
M 40
M 41
M 42
UL 6,22
UH 7,22
M 43
AR 8,40
AR 9,64
M 44
AD 8,6
AD 9,7
M 45
FR
AW 40,8
M 46
AW 64,9
UL 6,23
M 47
UH 7,23
AR 8,88
M 58
AR 9,112
AD 8,6
M 59
AD 9,7
AW 88,8
M 56
AW 112,9
UL 6,24
M 57
UH 7,24
AR 8,41
M 54
AR 9,65
AD 8,6
M 55
WL 0
FQ 8
AD 9,7
M 52
AW 41,8
AW 65,9
M 53
UL 6,25
UH 7,25
M 50
FZ 8
R 24
M 51
mw 72
R 26
M 48
TY 106,5128
TY 110,18952
M 49
R 28
M 60
ad 84,720
W 0
M 61
AR 8,89
AR 9,113
M 62
AD 8,6
AD 9,7
M 63
W 1
AW 89,8
M 64
AW 113,9
UL 6,26
M 65
UH 7,26
AR 8,42
M 66
W 2
AR 9,66
M 67
WL 3
AN 104,106
AN 105,107
M 68
SH 106
SH 107
M 69
mw 75
AN 106,106
AN 107,107
M 70
WL 2
AN 108,110
AN 109,111
M 71
R 30
M 82
SH 110
SH 111
M 83
AN 110,110
AN 111,111
M 80
TY 114,6664
TY 118,20488
M 81
ad 84,726
W 0
M 78
AD 8,6
AD 9,7
M 79
AW 42,8
AW 66,9
M 76
W 1
UL 6,27
M 77
UH 7,27
AR 8,90
M 74
AR 9,114
AD 8,6
M 75
W 2
AD 9,7
M 72
)"
R"(AW 90,8
AW 114,9
M 73
WL 1
R 32
M 84
mw 0
AN 112,114
AN 113,115
M 85
SH 114
SH 115
M 86
AN 114,114
AN 115,115
M 87
WL 2
AN 116,118
AN 117,119
M 88
SH 118
SH 119
M 89
AN 118,118
AN 119,119
M 90
TY 122,8200
TY 126,22024
M 91
ad 84,768
M 92
W 0
UL 6,28
M 93
UH 7,28
AR 8,43
M 94
AR 9,67
AD 8,6
M 95
R 34
M 106
W 1
AD 9,7
M 107
AW 43,8
AW 67,9
M 104
UL 6,29
UH 7,29
M 105
WL 3
AN 120,122
AN 121,123
M 102
SH 122
SH 123
M 103
AN 122,122
AN 123,123
M 100
WL 2
AN 124,126
AN 125,127
M 101
SH 126
SH 127
M 98
AN 126,126
AN 127,127
M 99
W 2
AR 8,91
M 96
AR 9,115
AD 8,6
M 97
R 48
M 108
mw 3
AD 9,7
AW 91,8
M 109
ad 84,774
W 0
M 110
AW 115,9
M 111
M 112
W 1
M 113
M 114
M 115
W 2
M 116
M 117
M 118
bc 49,9,66,60
M 119
WL 2
R 50
M 120
mh 0
s_bitcmp1_b32 s87, 2
s_cselect_b32 exec_lo, 0xffffff, 0
s_mov_b32 exec_hi, 0
global_atomic_add_f32 v54, v52, s[90:91] sc1
s_mov_b64 exec, -1
M 121
ad 66,1728
H 0
M 133
M 132
TX 58,4608
TX 62,18432
M 134
DG 1024
M 135
M 123
M 122
TY 66,5120
TY 70,18944
M 124
bc 48,9,64,56
M 125
mh 3
M 137
ad 64,1728
H 0
M 136
TX 74,6144
TX 78,19968
M 138
WL 4
Q 0
M 139
Q 2
M 127
Q 4
M 126
TY 82,6656
TY 86,20480
M 128
Q 6
M 129
WL 4
Q 8
M 141
Q 10
M 140
TX 90,7680
TX 94,21504
M 142
Q 12
M 143
R 54
M 131
Q 14
M 130
TY 98,8192
TY 102,22016
M 0
R 52
M 1
WL 10
Q 16
M 13
Q 18
M 12
Q 20
M 14
Q 22
M 15
WL 8
Q 24
M 3
Q 26
M 2
Q 28
M 4
Q 30
M 5
WL 6
Q 32
M 17
Q 34
M 16
Q 36
M 18
Q 38
M 19
WL 2
Q 40
M 7
Q 42
M 6
Q 44
M 8
Q 46
M 9
H 1
M 21
ae 44,68,0
M 20
s_bfe_u32 s51, s50, 0x10003
v_mad_i32_i24 v47, v49, s51, v48
XL 38,0
M 22
H 2
XL 40,768
M 23
R 58
M 11
XL 42,1536
M 10
WL 2
R 56
M 34
XL 44,2304
M 35
M 32
M 33
M 30
M 31
M 28
M 29
M 26
M 27
WV 10
M 24
WL 0
FP
M 25
M 36
M 37
s_bitcmp1_b32 s87, 3
s_cselect_b32 exec_lo, 0xffffff, 0
s_mov_b32 exec_hi, 0
global_atomic_add_f32 v54, v52, s[90:91] sc1
s_mov_b64 exec, -1
M 38
M 39
M 40
M 41
M 42
M 43
M 44
UL 6,30
UH 7,30
M 45
FR
AR 8,44
M 46
AR 9,68
AD 8,6
M 47
AD 9,7
AW 44,8
M 58
AW 68,9
UL 6,31
M 59
UH 7,31
AR 8,92
M 56
AR 9,116
AD 8,6
M 57
AD 9,7
AW 92,8
M 54
AW 116,9
UL 6,32
M 55
WL 0
FQ 12
UH 7,32
M 52
AR 8,45
AR 9,69
M 53
AD 8,6
AD 9,7
M 50
FZ 12
R 72
M 51
mw 24
R 74
M 48
TY 106,9736
TY 110,23560
M 49
R 76
M 60
ad 84,816
W 0
M 61
AW 45,8
AW 69,9
M 62
UL 6,33
UH 7,33
M 63
W 1
AR 8,93
M 64
AR 9,117
AD 8,6
M 65
AD 9,7
AW 93,8
M 66
W 2
AW 117,9
M 67
WL 3
AN 104,106
AN 105,107
M 68
SH 106
SH 107
M 69
mw 27
AN 106,106
AN 107,107
M 70
WL 2
AN 108,110
AN 109,111
M 71
R 78
M 82
SH 110
SH 111
M 83
AN 110,110
AN 111,111
M 80
TY 114,11272
TY 118,25096
M 81
ad 84,822
W 0
M 78
UL 6,34
UH 7,34
M 79
AR 8,46
AR 9,70
M 76
W 1
AD 8,6
M 77
AD 9,7
AW 46,8
M 74
AW 70,9
UL 6,35
M 75
W 2
UH 7,35
M 72
AR 8,94
AR 9,118
M 73
WL 1
R 80
M 84
mw 48
AN 112,114
AN 113,115
M 85
SH 114
SH 115
M 86
AN 114,114
AN 115,115
M 87
WL 2
AN 116,118
AN 117,119
M 88
SH 118
SH 119
M 89
AN 118,118
AN 119,119
M 90
TY 122,12808
TY 126,26632
M 91
ad 84,864
M 92
W 0
AD 8,6
M 93
AD 9,7
AW 94,8
M 94
)"
R"(AW 118,9
UL 6,36
M 95
R 82
M 106
W 1
UH 7,36
M 107
AR 8,47
AR 9,71
M 104
AD 8,6
AD 9,7
M 105
WL 3
AN 120,122
AN 121,123
M 102
SH 122
SH 123
M 103
AN 122,122
AN 123,123
M 100
WL 2
AN 124,126
AN 125,127
M 101
SH 126
SH 127
M 98
AN 126,126
AN 127,127
M 99
W 2
AW 47,8
M 96
AW 71,9
UL 6,37
M 97
R 0
M 108
mw 51
UH 7,37
AR 8,95
M 109
ad 84,870
W 0
M 110
AR 9,119
AD 8,6
M 111
AD 9,7
AW 95,8
M 112
W 1
AW 119,9
M 113
M 114
M 115
W 2
M 116
M 117
M 118
bc 49,10,66,60
M 119
WL 2
R 2
M 120
mh 9
s_bitcmp1_b32 s87, 4
s_cselect_b32 exec_lo, 0xffffff, 0
s_mov_b32 exec_hi, 0
global_atomic_add_f32 v54, v52, s[90:91] sc1
s_mov_b64 exec, -1
M 121
ad 66,1920
H 0
M 133
M 132
TX 58,9216
TX 62,23040
M 134
DG 1024
M 135
M 123
M 122
TY 66,9728
TY 70,23552
M 124
bc 48,10,64,56
M 125
mh 12
M 137
ad 64,1920
H 0
M 136
TX 74,10752
TX 78,24576
M 138
WL 4
Q 0
M 139
Q 2
M 127
Q 4
M 126
TY 82,11264
TY 86,25088
M 128
Q 6
M 129
WL 4
Q 8
M 141
Q 10
M 140
TX 90,12288
TX 94,26112
M 142
Q 12
M 143
R 6
M 131
Q 14
M 130
TY 98,12800
TY 102,26624
M 0
R 4
M 1
WL 10
Q 16
M 13
Q 18
M 12
Q 20
M 14
Q 22
M 15
WL 8
Q 24
M 3
Q 26
M 2
Q 28
M 4
Q 30
M 5
WL 6
Q 32
M 17
Q 34
M 16
Q 36
M 18
Q 38
M 19
WL 2
Q 40
M 7
Q 42
M 6
Q 44
M 8
Q 46
M 9
H 1
M 21
ae 44,68,96
M 20
s_bitcmp1_b32 s50, 4
s_cselect_b32 s51, 24576, 0
v_add_u32 v47, s51, v3
M 22
H 2
XL 14,0
M 23
R 10
M 11
XL 16,768
M 10
WL 2
R 8
PA 12288
DF
M 34
XL 18,1536
M 35
XL 20,2304
M 32
M 33
M 30
M 31
M 28
M 29
M 26
M 27
WV 11
M 24
WL 0
FP
M 25
M 36
M 37
s_bitcmp1_b32 s87, 5
s_cselect_b32 exec_lo, 0xffffff, 0
s_mov_b32 exec_hi, 0
global_atomic_add_f32 v54, v52, s[90:91] sc1
s_mov_b64 exec, -1
M 38
M 39
M 40
M 41
M 42
M 43
M 44
M 45
FR
M 46
UL 6,38
UH 7,38
M 47
AR 8,48
AR 9,72
M 58
AD 8,6
AD 9,7
M 59
AW 48,8
AW 72,9
M 56
UL 6,39
UH 7,39
M 57
AR 8,96
AR 9,120
M 54
AD 8,6
AD 9,7
M 55
WL 0
FQ 16
AW 96,8
M 52
AW 120,9
UL 6,40
M 53
UH 7,40
AR 8,49
M 50
FZ 16
R 24
M 51
mw 72
R 26
M 48
TY 106,520
TY 110,14344
M 49
R 28
M 60
ad 84,912
W 0
M 61
AR 9,73
AD 8,6
M 62
AD 9,7
AW 49,8
M 63
W 1
AW 73,9
M 64
UL 6,41
UH 7,41
M 65
AR 8,97
AR 9,121
M 66
W 2
AD 8,6
M 67
WL 3
AN 104,106
AN 105,107
M 68
SH 106
SH 107
M 69
mw 75
AN 106,106
AN 107,107
M 70
WL 2
AN 108,110
AN 109,111
M 71
R 30
M 82
SH 110
SH 111
M 83
AN 110,110
AN 111,111
M 80
TY 114,2056
TY 118,15880
M 81
ad 84,918
W 0
M 78
AD 9,7
AW 97,8
M 79
AW 121,9
UL 6,42
M 76
W 1
UH 7,42
M 77
AR 8,50
AR 9,74
M 74
AD 8,6
AD 9,7
M 75
W 2
AW 50,8
M 72
AW 74,9
UL 6,43
M 73
WL 1
R 32
M 84
mw 0
AN 112,114
AN 113,115
M 85
SH 114
SH 115
M 86
AN 114,114
AN 115,115
M 87
WL 2
AN 116,118
AN 117,119
M 88
SH 118
SH 119
M 89
AN 118,118
AN 119,119
M 90
TY 122,3592
TY 126,17416
M 91
ad 84,960
M 92
W 0
UH 7,43
M 93
AR 8,98
AR 9,122
M 94
AD 8,6
AD 9,7
M 95
R 34
M 106
W 1
AW 98,8
M 107
AW 122,9
UL 6,44
M 104
UH 7,44
AR 8,51
M 105
WL 3
AN 120,122
AN 121,123
M 102
SH 122
SH 123
M 103
AN 122,122
AN 123,123
M 100
WL 2
AN 124,126
AN 125,127
M 101
SH 126
SH 127
M 98
AN 126,126
AN 127,127
)"
R"(M 99
W 2
AR 9,75
M 96
AD 8,6
AD 9,7
M 97
R 48
M 108
mw 3
AW 51,8
AW 75,9
M 109
ad 84,966
W 0
M 110
UL 6,45
UH 7,45
M 111
AR 8,99
AR 9,123
M 112
W 1
AD 8,6
M 113
AD 9,7
AW 99,8
M 114
AW 123,9
M 115
W 2
M 116
M 117
M 118
bc 49,11,66,60
M 119
WL 2
R 50
M 120
mh 18
s_bitcmp1_b32 s87, 6
s_cselect_b32 exec_lo, 0xffffff, 0
s_mov_b32 exec_hi, 0
global_atomic_add_f32 v54, v52, s[90:91] sc1
s_mov_b64 exec, -1
M 121
ad 66,2112
H 0
M 133
M 132
TX 58,0
TX 62,13824
M 134
DG 1024
M 135
M 123
M 122
TY 66,512
TY 70,14336
M 124
bc 48,11,64,56
M 125
mh 21
M 137
ad 64,2112
H 0
M 136
TX 74,1536
TX 78,15360
M 138
WL 4
Q 0
M 139
Q 2
M 127
Q 4
M 126
TY 82,2048
TY 86,15872
M 128
Q 6
M 129
WL 4
Q 8
M 141
Q 10
M 140
TX 90,3072
TX 94,16896
M 142
Q 12
M 143
R 54
M 131
Q 14
M 130
TY 98,3584
TY 102,17408
M 0
R 52
M 1
WL 10
Q 16
M 13
Q 18
M 12
Q 20
M 14
Q 22
M 15
WL 8
Q 24
M 3
Q 26
M 2
Q 28
M 4
Q 30
M 5
WL 6
Q 32
M 17
Q 34
M 16
Q 36
M 18
Q 38
M 19
WL 2
Q 40
M 7
Q 42
M 6
Q 44
M 8
Q 46
M 9
H 1
M 21
ae 44,68,96
M 20
s_bfe_u32 s51, s50, 0x10005
v_mad_i32_i24 v47, v49, s51, v48
XL 22,0
M 22
H 2
XL 24,768
M 23
R 58
M 11
XL 26,1536
M 10
WL 2
R 56
M 34
XL 28,2304
M 35
M 32
M 33
M 30
M 31
M 28
M 29
M 26
M 27
WV 10
M 24
WL 0
FP
M 25
M 36
M 37
s_bitcmp1_b32 s87, 7
s_cselect_b32 exec_lo, 0xffffff, 0
s_mov_b32 exec_hi, 0
global_atomic_add_f32 v54, v52, s[90:91] sc1
s_mov_b64 exec, -1
M 38
M 39
M 40
M 41
M 42
M 43
M 44
M 45
FR
M 46
M 47
M 58
UL 6,14
UH 7,14
M 59
AR 8,52
AR 9,76
M 56
AD 8,6
AD 9,7
M 57
AW 52,8
AW 76,9
M 54
UL 6,15
UH 7,15
M 55
WL 0
FQ 20
AR 8,100
M 52
AR 9,124
AD 8,6
M 53
AD 9,7
AW 100,8
M 50
FZ 20
R 72
M 51
mw 24
R 74
M 48
TY 106,5128
TY 110,18952
M 49
R 76
M 60
ad 84,1008
W 0
M 61
AW 124,9
UL 6,16
M 62
UH 7,16
AR 8,53
M 63
W 1
AR 9,77
M 64
AD 8,6
AD 9,7
M 65
AW 53,8
AW 77,9
M 66
W 2
UL 6,17
M 67
WL 3
AN 104,106
AN 105,107
M 68
SH 106
SH 107
M 69
mw 27
AN 106,106
AN 107,107
M 70
WL 2
AN 108,110
AN 109,111
M 71
R 78
M 82
SH 110
SH 111
M 83
AN 110,110
AN 111,111
M 80
TY 114,6664
TY 118,20488
M 81
ad 84,1014
W 0
M 78
UH 7,17
AR 8,101
M 79
AR 9,125
AD 8,6
M 76
W 1
AD 9,7
M 77
AW 101,8
AW 125,9
M 74
UL 6,18
UH 7,18
M 75
W 2
AR 8,54
M 72
AR 9,78
AD 8,6
M 73
WL 1
R 80
M 84
mw 48
AN 112,114
AN 113,115
M 85
SH 114
SH 115
M 86
AN 114,114
AN 115,115
M 87
WL 2
AN 116,118
AN 117,119
M 88
SH 118
SH 119
M 89
AN 118,118
AN 119,119
M 90
TY 122,8200
TY 126,22024
M 91
ad 84,1056
M 92
W 0
AD 9,7
M 93
AW 54,8
AW 78,9
M 94
UL 6,19
UH 7,19
M 95
R 82
M 106
W 1
AR 8,102
M 107
AR 9,126
AD 8,6
M 104
AD 9,7
AW 102,8
M 105
WL 3
AN 120,122
AN 121,123
M 102
SH 122
SH 123
M 103
AN 122,122
AN 123,123
M 100
WL 2
AN 124,126
AN 125,127
M 101
SH 126
SH 127
M 98
AN 126,126
AN 127,127
M 99
W 2
AW 126,9
M 96
UL 6,20
UH 7,20
M 97
R 0
M 108
mw 51
AR 8,55
AR 9,79
M 109
ad 84,1062
W 0
M 110
AD 8,6
AD 9,7
M 111
AW 55,8
AW 79,9
M 112
W 1
UL 6,21
M 113
UH 7,21
AR 8,103
M 114
AR 9,127
AD 8,6
M 115
W 2
AD 9,7
M 116
AW 103,8
AW 127,9
M 117
)"
R"(M 118
bc 49,12,66,60
M 119
WL 2
R 2
M 120
mh 0
M 121
ad 66,2304
H 0
M 133
M 132
TX 58,4608
TX 62,18432
M 134
DG 1024
M 135
M 123
M 122
TY 66,5120
TY 70,18944
M 124
bc 48,12,64,56
M 125
mh 3
M 137
ad 64,2304
H 0
M 136
TX 74,6144
TX 78,19968
M 138
WL 4
Q 0
M 139
Q 2
M 127
Q 4
M 126
TY 82,6656
TY 86,20480
M 128
Q 6
M 129
WL 4
Q 8
M 141
Q 10
M 140
TX 90,7680
TX 94,21504
M 142
Q 12
M 143
R 6
M 131
Q 14
M 130
TY 98,8192
TY 102,22016
M 0
R 4
M 1
WL 10
Q 16
M 13
Q 18
M 12
Q 20
M 14
Q 22
M 15
WL 8
Q 24
M 3
Q 26
M 2
Q 28
M 4
Q 30
M 5
WL 6
Q 32
M 17
Q 34
M 16
Q 36
M 18
Q 38
M 19
WL 2
Q 40
M 7
Q 42
M 6
Q 44
M 8
Q 46
M 9
H 1
M 21
M 20
M 22
H 2
M 23
R 10
M 11
M 10
WL 2
R 8
PA 12288
DF
M 34
M 35
M 32
M 33
M 30
M 31
M 28
M 29
M 26
ae 44,70,0
M 27
WV 6
M 24
WL 0
FP
M 25
M 36
s_bitcmp1_b32 s50, 0
s_cselect_b32 s51, 24576, 0
v_add_u32 v47, s51, v3
M 37
XL 30,128
M 38
XL 32,896
M 39
M 40
M 41
M 42
M 43
M 44
XL 34,1664
M 45
FR
M 46
M 47
M 58
M 59
M 56
XL 36,2432
UL 6,22
M 57
UH 7,22
AR 8,56
M 54
AR 9,80
AD 8,6
M 55
WL 0
FQ 24
AD 9,7
M 52
AW 56,8
AW 80,9
M 53
UL 6,23
UH 7,23
M 50
FZ 24
R 24
M 51
mw 72
R 26
M 48
TY 106,9736
TY 110,23560
M 49
R 28
M 60
ad 84,1104
W 0
M 61
AR 8,104
AR 9,128
M 62
AD 8,6
AD 9,7
M 63
W 1
AW 104,8
M 64
AW 128,9
UL 6,24
M 65
UH 7,24
AR 8,57
M 66
W 2
AR 9,81
M 67
WL 3
AN 104,106
AN 105,107
M 68
SH 106
SH 107
M 69
mw 75
AN 106,106
AN 107,107
M 70
WL 2
AN 108,110
AN 109,111
M 71
R 30
M 82
SH 110
SH 111
M 83
AN 110,110
AN 111,111
M 80
TY 114,11272
TY 118,25096
M 81
ad 84,1110
W 0
M 78
AD 8,6
AD 9,7
M 79
AW 57,8
AW 81,9
M 76
W 1
UL 6,25
M 77
UH 7,25
AR 8,105
M 74
AR 9,129
AD 8,6
M 75
W 2
AD 9,7
M 72
AW 105,8
AW 129,9
M 73
WL 1
R 32
M 84
mw 0
AN 112,114
AN 113,115
M 85
SH 114
SH 115
M 86
AN 114,114
AN 115,115
M 87
WL 2
AN 116,118
AN 117,119
M 88
SH 118
SH 119
M 89
AN 118,118
AN 119,119
M 90
TY 122,12808
TY 126,26632
M 91
ad 84,1152
M 92
W 0
UL 6,26
M 93
UH 7,26
AR 8,58
M 94
AR 9,82
AD 8,6
M 95
R 34
M 106
W 1
AD 9,7
M 107
AW 58,8
AW 82,9
M 104
UL 6,27
UH 7,27
M 105
WL 3
AN 120,122
AN 121,123
M 102
SH 122
SH 123
M 103
AN 122,122
AN 123,123
M 100
WL 2
AN 124,126
AN 125,127
M 101
SH 126
SH 127
M 98
AN 126,126
AN 127,127
M 99
W 2
AR 8,106
M 96
AR 9,130
AD 8,6
M 97
R 48
M 108
mw 3
AD 9,7
AW 106,8
M 109
ad 84,1158
W 0
M 110
AW 130,9
UL 6,28
M 111
UH 7,28
AR 8,59
M 112
W 1
AR 9,83
M 113
AD 8,6
AD 9,7
M 114
AW 59,8
AW 83,9
M 115
W 2
UL 6,29
M 116
UH 7,29
AR 8,107
M 117
AR 9,131
AD 8,6
M 118
bc 49,13,66,60
AD 9,7
M 119
WL 0
R 50
M 120
mh 9
AW 107,8
AW 131,9
M 121
ad 66,2496
H 0
M 133
M 132
TX 58,9216
TX 62,23040
M 134
DG 1024
M 135
M 123
M 122
TY 66,9728
TY 70,23552
M 124
bc 48,13,64,56
M 125
mh 12
M 137
ad 64,2496
H 0
M 136
TX 74,10752
TX 78,24576
M 138
WL 4
Q 0
M 139
Q 2
M 127
Q 4
M 126
TY 82,11264
)"
R"(TY 86,25088
M 128
Q 6
M 129
WL 4
Q 8
M 141
Q 10
M 140
TX 90,12288
TX 94,26112
M 142
Q 12
M 143
R 54
M 131
Q 14
M 130
TY 98,12800
TY 102,26624
M 0
R 52
M 1
WL 10
Q 16
M 13
Q 18
M 12
Q 20
M 14
Q 22
M 15
WL 8
Q 24
M 3
Q 26
M 2
Q 28
M 4
Q 30
M 5
WL 6
Q 32
M 17
Q 34
M 16
Q 36
M 18
Q 38
M 19
WL 2
Q 40
M 7
Q 42
M 6
Q 44
M 8
Q 46
M 9
H 1
M 21
M 20
M 22
H 2
M 23
R 58
M 11
M 10
WL 2
R 56
M 34
M 35
M 32
M 33
M 30
M 31
M 28
M 29
M 26
M 27
WV 5
M 24
WL 0
FP
M 25
M 36
ae 44,70,0
M 37
s_bfe_u32 s51, s50, 0x10001
v_mad_i32_i24 v47, v49, s51, v48
XL 38,128
M 38
M 39
M 40
XL 40,896
M 41
M 42
M 43
M 44
M 45
FR
M 46
XL 42,1664
M 47
M 58
M 59
M 56
M 57
M 54
XL 44,2432
M 55
WL 0
FQ 28
M 52
M 53
M 50
FZ 28
R 72
M 51
R 74
M 48
TY 106,520
TY 110,14344
M 49
R 76
M 60
mw 24
v_mov_b32 v54, 2
s_mov_b64 exec, 1
FS
s_mov_b64 exec, -1
M 61
ad 84,1200
W 0
M 62
M 63
M 64
W 1
M 65
M 66
M 67
WL 3
AN 104,106
AN 105,107
M 68
SH 106
SH 107
M 69
AN 106,106
AN 107,107
M 70
WL 2
AN 108,110
AN 109,111
M 71
R 78
M 82
SH 110
SH 111
M 83
AN 110,110
AN 111,111
M 80
TY 114,2056
TY 118,15880
M 81
W 2
M 78
M 79
M 76
mw 27
M 77
ad 84,1206
W 0
M 74
M 75
M 72
W 1
M 73
R 80
M 84
WL 3
AN 112,114
AN 113,115
M 85
SH 114
SH 115
M 86
AN 114,114
AN 115,115
M 87
WL 2
AN 116,118
AN 117,119
M 88
SH 118
SH 119
M 89
AN 118,118
AN 119,119
M 90
TY 122,3592
TY 126,17416
M 91
W 2
UL 6,30
M 92
UH 7,30
AR 8,132
M 93
AR 9,156
AD 8,6
M 94
mw 48
AD 9,7
AW 132,8
M 95
R 82
M 106
ad 84,1248
W 0
M 107
AW 156,9
UL 6,31
M 104
UH 7,31
AR 8,180
M 105
WL 3
AN 120,122
AN 121,123
M 102
SH 122
SH 123
M 103
AN 122,122
AN 123,123
M 100
WL 2
AN 124,126
AN 125,127
M 101
SH 126
SH 127
M 98
AN 126,126
AN 127,127
M 99
W 1
AD 8,6
M 96
AD 136,7
AW 180,8
M 97
R 0
M 108
W 2
UL 6,32
M 109
UH 7,32
AR 8,133
M 110
AR 9,157
AD 8,6
M 111
mw 51
AD 9,7
AW 133,8
M 112
ad 84,1254
W 0
M 113
AW 157,9
UL 6,33
M 114
UH 7,33
AR 8,181
M 115
W 1
AD 8,6
M 116
AD 137,7
AW 181,8
M 117
UL 6,34
UH 7,34
M 118
W 2
AR 8,134
M 119
WL 0
R 2
M 120
AR 9,158
AD 8,6
M 121
bc 49,14,66,60
AD 9,7
M 133
mh 18
AW 134,8
AW 158,9
M 132
TX 58,0
TX 62,13824
M 134
ad 66,2688
H 0
M 135
UL 6,35
UH 7,35
M 123
AR 8,182
AD 8,6
M 122
TY 66,512
TY 70,14336
M 124
DG 1024
M 125
AD 138,7
AW 182,8
M 137
UL 6,36
UH 7,36
M 136
TX 74,1536
TX 78,15360
M 138
WL 4
Q 0
M 139
Q 2
M 127
Q 4
M 126
TY 82,2048
TY 86,15872
M 128
Q 6
M 129
WL 4
Q 8
M 141
Q 10
M 140
TX 90,3072
TX 94,16896
M 142
Q 12
M 143
R 6
M 131
Q 14
M 130
TY 98,3584
TY 102,17408
M 0
R 4
M 1
WL 10
Q 16
M 13
Q 18
M 12
Q 20
M 14
Q 22
M 15
WL 8
Q 24
M 3
Q 26
M 2
Q 28
M 4
Q 30
M 5
WL 6
Q 32
M 17
Q 34
M 16
Q 36
M 18
Q 38
M 19
WL 2
Q 40
M 7
Q 42
M 6
Q 44
M 8
Q 46
M 9
bc 48,14,64,56
M 21
mh 21
AR 8,135
)"
R"(AR 9,159
M 20
ad 64,2688
H 0
M 22
AD 8,6
AD 9,7
M 23
R 10
M 11
H 1
AW 135,8
M 10
WL 2
R 8
M 34
AW 159,9
UL 6,37
M 35
H 2
UH 7,37
M 32
AR 8,183
AD 8,6
M 33
AD 139,7
AW 183,8
M 30
PA 12288
DF
M 31
M 28
M 29
M 26
M 27
WV 6
M 24
WL 0
FP
M 25
M 36
ae 44,68,0
M 37
s_bitcmp1_b32 s50, 2
s_cselect_b32 s51, 24576, 0
v_add_u32 v47, s51, v3
M 38
XL 14,128
M 39
M 40
M 41
M 42
XL 16,896
M 43
M 44
M 45
FR
M 46
M 47
M 58
XL 18,1664
M 59
M 56
M 57
M 54
M 55
WL 0
FQ 32
M 52
XL 20,2432
M 53
M 50
FZ 32
R 24
M 51
mw 72
R 26
M 48
TY 106,5128
TY 110,18952
M 49
R 28
M 60
ad 84,1296
W 0
M 61
M 62
M 63
W 1
M 64
M 65
M 66
W 2
M 67
WL 3
AN 104,106
AN 105,107
M 68
SH 106
SH 107
M 69
mw 75
AN 106,106
AN 107,107
M 70
WL 2
AN 108,110
AN 109,111
M 71
R 30
M 82
SH 110
SH 111
M 83
AN 110,110
AN 111,111
M 80
TY 114,6664
TY 118,20488
M 81
ad 84,1302
W 0
M 78
M 79
M 76
W 1
M 77
M 74
M 75
W 2
M 72
M 73
R 32
M 84
mw 0
WL 3
AN 112,114
AN 113,115
M 85
SH 114
SH 115
M 86
AN 114,114
AN 115,115
M 87
WL 2
AN 116,118
AN 117,119
M 88
SH 118
SH 119
M 89
AN 118,118
AN 119,119
M 90
TY 122,8200
TY 126,22024
M 91
ad 84,1344
M 92
W 0
UL 6,38
M 93
UH 7,38
AR 8,136
M 94
AR 9,160
AD 8,6
M 95
R 34
M 106
W 1
AD 9,7
M 107
AW 136,8
AW 160,9
M 104
UL 6,39
UH 7,39
M 105
WL 3
AN 120,122
AN 121,123
M 102
SH 122
SH 123
M 103
AN 122,122
AN 123,123
M 100
WL 2
AN 124,126
AN 125,127
M 101
SH 126
SH 127
M 98
AN 126,126
AN 127,127
M 99
W 2
AR 8,184
M 96
AD 8,6
AD 140,7
M 97
R 48
M 108
mw 3
AW 184,8
UL 6,40
M 109
ad 84,1350
W 0
M 110
UH 7,40
AR 8,137
M 111
AR 9,161
AD 8,6
M 112
W 1
AD 9,7
M 113
AW 137,8
AW 161,9
M 114
UL 6,41
UH 7,41
M 115
W 2
AR 8,185
M 116
AD 8,6
AD 141,7
M 117
AW 185,8
UL 6,42
M 118
bc 49,15,66,60
UH 7,42
M 119
WL 0
R 50
M 120
mh 0
AR 8,138
AR 9,162
M 121
ad 66,2880
H 0
M 133
AD 8,6
AD 9,7
M 132
TX 58,4608
TX 62,18432
M 134
DG 1024
M 135
AW 138,8
AW 162,9
M 123
UL 6,43
UH 7,43
M 122
TY 66,5120
TY 70,18944
M 124
bc 48,15,64,56
AR 8,186
M 125
mh 3
AD 8,6
AD 142,7
M 137
ad 64,2880
H 0
M 136
TX 74,6144
TX 78,19968
M 138
WL 4
Q 0
M 139
Q 2
M 127
Q 4
M 126
TY 82,6656
TY 86,20480
M 128
Q 6
M 129
WL 4
Q 8
M 141
Q 10
M 140
TX 90,7680
TX 94,21504
M 142
Q 12
M 143
R 54
M 131
Q 14
M 130
TY 98,8192
TY 102,22016
M 0
R 52
M 1
WL 10
Q 16
M 13
Q 18
M 12
Q 20
M 14
Q 22
M 15
WL 8
Q 24
M 3
Q 26
M 2
Q 28
M 4
Q 30
M 5
WL 6
Q 32
M 17
Q 34
M 16
Q 36
M 18
Q 38
M 19
WL 2
Q 40
M 7
Q 42
M 6
Q 44
M 8
Q 46
M 9
H 1
AW 186,8
M 21
UL 6,44
UH 7,44
M 20
AR 8,139
AR 9,163
M 22
H 2
AD 8,6
M 23
R 58
M 11
AD 9,7
AW 139,8
M 10
WL 2
R 56
M 34
AW 163,9
UL 6,45
M 35
UH 7,45
AR 8,187
M 32
AD 8,6
AD 143,7
M 33
AW 187,8
M 30
M 31
M 28
M 29
M 26
M 27
WV 5
M 24
WL 0
FP
M 25
M 36
M 37
M 38
ae 44,68,0
M 39
s_bfe_u32 s51, s50, 0x10003
v_mad_i32_i24 v47, v49, s51, v48
XL 22,128
M 40
M 41
)"
R"(M 42
M 43
M 44
XL 24,896
M 45
FR
M 46
M 47
M 58
M 59
M 56
XL 26,1664
M 57
M 54
M 55
WL 0
FQ 36
M 52
M 53
M 50
FZ 36
R 72
M 51
mw 24
R 74
M 48
TY 106,9736
TY 110,23560
M 49
R 76
M 60
ad 84,1392
W 0
M 61
XL 28,2432
M 62
M 63
W 1
M 64
M 65
M 66
W 2
M 67
WL 3
AN 104,106
AN 105,107
M 68
SH 106
SH 107
M 69
mw 27
AN 106,106
AN 107,107
M 70
WL 2
AN 108,110
AN 109,111
M 71
R 78
M 82
SH 110
SH 111
M 83
AN 110,110
AN 111,111
M 80
TY 114,11272
TY 118,25096
M 81
ad 84,1398
W 0
M 78
M 79
M 76
W 1
M 77
M 74
M 75
W 2
M 72
M 73
R 80
M 84
mw 48
WL 3
AN 112,114
AN 113,115
M 85
SH 114
SH 115
M 86
AN 114,114
AN 115,115
M 87
WL 2
AN 116,118
AN 117,119
M 88
SH 118
SH 119
M 89
AN 118,118
AN 119,119
M 90
TY 122,12808
TY 126,26632
M 91
ad 84,1440
M 92
W 0
UL 6,14
M 93
UH 7,14
AR 8,140
M 94
AR 9,164
AD 8,6
M 95
R 82
M 106
W 1
AD 9,7
M 107
AW 140,8
AW 164,9
M 104
UL 6,15
UH 7,15
M 105
WL 3
AN 120,122
AN 121,123
M 102
SH 122
SH 123
M 103
AN 122,122
AN 123,123
M 100
WL 2
AN 124,126
AN 125,127
M 101
SH 126
SH 127
M 98
AN 126,126
AN 127,127
M 99
W 2
AR 8,188
M 96
AD 8,6
AD 144,7
M 97
R 0
M 108
mw 51
AW 188,8
UL 6,16
M 109
ad 84,1446
W 0
M 110
UH 7,16
AR 8,141
M 111
AR 9,165
AD 8,6
M 112
W 1
AD 9,7
M 113
AW 141,8
AW 165,9
M 114
UL 6,17
UH 7,17
M 115
W 2
AR 8,189
M 116
AD 8,6
AD 145,7
M 117
AW 189,8
UL 6,18
M 118
bc 49,16,66,60
UH 7,18
M 119
WL 0
R 2
M 120
mh 9
AR 8,142
AR 9,166
M 121
ad 66,3072
H 0
M 133
AD 8,6
AD 9,7
M 132
TX 58,9216
TX 62,23040
M 134
DG 1024
M 135
AW 142,8
AW 166,9
M 123
UL 6,19
UH 7,19
M 122
TY 66,9728
TY 70,23552
M 124
bc 48,16,64,56
AR 8,190
M 125
mh 12
AD 8,6
AD 146,7
M 137
ad 64,3072
H 0
M 136
TX 74,10752
TX 78,24576
M 138
WL 4
Q 0
M 139
Q 2
M 127
Q 4
M 126
TY 82,11264
TY 86,25088
M 128
Q 6
M 129
WL 4
Q 8
M 141
Q 10
M 140
TX 90,12288
TX 94,26112
M 142
Q 12
M 143
R 6
M 131
Q 14
M 130
TY 98,12800
TY 102,26624
M 0
R 4
M 1
WL 10
Q 16
M 13
Q 18
M 12
Q 20
M 14
Q 22
M 15
WL 8
Q 24
M 3
Q 26
M 2
Q 28
M 4
Q 30
M 5
WL 6
Q 32
M 17
Q 34
M 16
Q 36
M 18
Q 38
M 19
WL 2
Q 40
M 7
Q 42
M 6
Q 44
M 8
Q 46
M 9
H 1
AW 190,8
M 21
UL 6,20
UH 7,20
M 20
AR 8,143
AR 9,167
M 22
H 2
AD 8,6
M 23
R 10
M 11
AD 9,7
AW 143,8
M 10
WL 2
R 8
PA 12288
DF
M 34
AW 167,9
UL 6,21
M 35
UH 7,21
AR 8,191
M 32
AD 8,6
AD 147,7
M 33
AW 191,8
M 30
M 31
M 28
M 29
M 26
M 27
WV 6
M 24
WL 0
FP
M 25
M 36
M 37
M 38
M 39
M 40
ae 44,68,96
M 41
s_bitcmp1_b32 s50, 4
s_cselect_b32 s51, 24576, 0
v_add_u32 v47, s51, v3
M 42
XL 30,128
M 43
M 44
M 45
FR
M 46
XL 32,896
M 47
M 58
M 59
M 56
M 57
M 54
XL 34,1664
M 55
WL 0
FQ 40
M 52
M 53
M 50
FZ 40
R 24
M 51
mw 72
R 26
M 48
TY 106,520
TY 110,14344
M 49
R 28
M 60
ad 84,1488
W 0
M 61
XL 36,2432
v_lshlrev_b32 v55, 4, v0
M 62
FL
M 63
W 1
M 64
M 65
M 66
W 2
M 67
WL 3
AN 104,106
AN 105,107
)"
R"(M 68
SH 106
SH 107
M 69
mw 75
AN 106,106
AN 107,107
M 70
WL 2
AN 108,110
AN 109,111
M 71
R 30
M 82
SH 110
SH 111
M 83
AN 110,110
AN 111,111
M 80
TY 114,2056
TY 118,15880
M 81
ad 84,1494
W 0
M 78
M 79
M 76
W 1
M 77
M 74
M 75
W 2
M 72
M 73
R 32
M 84
mw 0
WL 3
AN 112,114
AN 113,115
M 85
SH 114
SH 115
M 86
AN 114,114
AN 115,115
M 87
WL 2
AN 116,118
AN 117,119
M 88
SH 118
SH 119
M 89
AN 118,118
AN 119,119
M 90
TY 122,3592
TY 126,17416
M 91
ad 84,1536
M 92
W 0
M 93
M 94
UL 6,22
UH 7,22
M 95
R 34
M 106
W 1
AR 8,144
M 107
AR 9,168
AD 8,6
M 104
AD 9,7
AW 144,8
M 105
WL 3
AN 120,122
AN 121,123
M 102
SH 122
SH 123
M 103
AN 122,122
AN 123,123
M 100
WL 2
AN 124,126
AN 125,127
M 101
SH 126
SH 127
M 98
AN 126,126
AN 127,127
M 99
W 2
AW 168,9
M 96
UL 6,23
UH 7,23
M 97
R 48
M 108
mw 3
AR 8,192
AD 8,6
M 109
ad 84,1542
W 0
M 110
AD 148,7
AW 192,8
M 111
UL 6,24
UH 7,24
M 112
W 1
AR 8,145
M 113
AR 9,169
AD 8,6
M 114
AD 9,7
AW 145,8
M 115
W 2
AW 169,9
M 116
UL 6,25
UH 7,25
M 117
AR 8,193
AD 8,6
M 118
bc 49,17,66,60
AD 149,7
M 119
WL 0
R 50
M 120
mh 18
AW 193,8
UL 6,26
M 121
ad 66,3264
H 0
M 133
UH 7,26
AR 8,146
M 132
TX 58,0
TX 62,13824
M 134
DG 1024
M 135
AR 9,170
AD 8,6
M 123
AD 9,7
AW 146,8
M 122
TY 66,512
TY 70,14336
M 124
bc 48,17,64,56
AW 170,9
M 125
mh 21
UL 6,27
UH 7,27
M 137
ad 64,3264
H 0
M 136
TX 74,1536
TX 78,15360
M 138
WL 4
Q 0
M 139
Q 2
M 127
Q 4
M 126
TY 82,2048
TY 86,15872
M 128
Q 6
M 129
WL 4
Q 8
M 141
Q 10
M 140
TX 90,3072
TX 94,16896
M 142
Q 12
M 143
R 54
M 131
Q 14
M 130
TY 98,3584
TY 102,17408
M 0
R 52
M 1
WL 10
Q 16
M 13
Q 18
M 12
Q 20
M 14
Q 22
M 15
WL 8
Q 24
M 3
Q 26
M 2
Q 28
M 4
Q 30
M 5
WL 6
Q 32
M 17
Q 34
M 16
Q 36
M 18
Q 38
M 19
WL 2
Q 40
M 7
Q 42
M 6
Q 44
M 8
Q 46
M 9
H 1
AR 8,194
M 21
AD 8,6
AD 150,7
M 20
AW 194,8
UL 6,28
M 22
H 2
UH 7,28
M 23
R 58
M 11
AR 8,147
AR 9,171
M 10
WL 2
R 56
M 34
AD 8,6
AD 9,7
M 35
AW 147,8
AW 171,9
M 32
UL 6,29
UH 7,29
M 33
AR 8,195
AD 8,6
M 30
AD 151,7
AW 195,8
M 31
M 28
M 29
M 26
M 27
WV 5
M 24
WL 0
FP
M 25
M 36
M 37
M 38
M 39
M 40
M 41
M 42
ae 44,68,96
M 43
s_bfe_u32 s51, s50, 0x10005
v_mad_i32_i24 v47, v49, s51, v48
XL 38,128
M 44
M 45
FR
M 46
M 47
M 58
XL 40,896
M 59
M 56
M 57
M 54
M 55
WL 0
FQ 44
M 52
XL 42,1664
M 53
M 50
FZ 44
R 72
M 51
mw 24
R 74
M 48
TY 106,5128
TY 110,18952
M 49
R 76
M 60
ad 84,1584
W 0
M 61
XL 44,2432
M 62
M 63
W 1
M 64
M 65
M 66
W 2
M 67
WL 3
AN 104,106
AN 105,107
M 68
SH 106
SH 107
M 69
mw 27
AN 106,106
AN 107,107
M 70
WL 2
AN 108,110
AN 109,111
M 71
R 78
M 82
SH 110
SH 111
M 83
AN 110,110
AN 111,111
M 80
TY 114,6664
TY 118,20488
M 81
ad 84,1590
W 0
M 78
M 79
M 76
W 1
M 77
M 74
M 75
W 2
M 72
M 73
R 80
M 84
mw 48
WL 3
AN 112,114
AN 113,115
M 85
SH 114
SH 115
M 86
AN 114,114
AN 115,115
M 87
WL 2
AN 116,118
AN 117,119
)"
R"(M 88
SH 118
SH 119
M 89
AN 118,118
AN 119,119
M 90
TY 122,8200
TY 126,22024
M 91
ad 84,1632
M 92
W 0
M 93
M 94
M 95
R 82
M 106
W 1
UL 6,30
M 107
UH 7,30
AR 8,148
M 104
AR 9,172
AD 8,6
M 105
WL 3
AN 120,122
AN 121,123
M 102
SH 122
SH 123
M 103
AN 122,122
AN 123,123
M 100
WL 2
AN 124,126
AN 125,127
M 101
SH 126
SH 127
M 98
AN 126,126
AN 127,127
M 99
W 2
AD 9,7
M 96
AW 148,8
AW 172,9
M 97
R 0
M 108
mw 51
UL 6,31
UH 7,31
M 109
ad 84,1638
W 0
M 110
AR 8,196
AD 8,6
M 111
AD 152,7
AW 196,8
M 112
W 1
UL 6,32
M 113
UH 7,32
AR 8,149
M 114
AR 9,173
AD 8,6
M 115
W 2
AD 9,7
M 116
AW 149,8
AW 173,9
M 117
UL 6,33
UH 7,33
M 118
bc 49,18,66,60
AR 8,197
M 119
WL 0
R 2
M 120
mh 0
AD 8,6
AD 153,7
M 121
ad 66,3456
H 0
M 133
AW 197,8
UL 6,34
M 132
TX 58,4608
TX 62,18432
M 134
DG 1024
M 135
UH 7,34
AR 8,150
M 123
AR 9,174
AD 8,6
M 122
TY 66,5120
TY 70,18944
M 124
bc 48,18,64,56
AD 9,7
M 125
mh 3
AW 150,8
AW 174,9
M 137
ad 64,3456
H 0
M 136
TX 74,6144
TX 78,19968
M 138
WL 4
Q 0
M 139
Q 2
M 127
Q 4
M 126
TY 82,6656
TY 86,20480
M 128
Q 6
M 129
WL 4
Q 8
M 141
Q 10
M 140
TX 90,7680
TX 94,21504
M 142
Q 12
M 143
R 6
M 131
Q 14
M 130
TY 98,8192
TY 102,22016
M 0
R 4
M 1
WL 10
Q 16
M 13
Q 18
M 12
Q 20
M 14
Q 22
M 15
WL 8
Q 24
M 3
Q 26
M 2
Q 28
M 4
Q 30
M 5
WL 6
Q 32
M 17
Q 34
M 16
Q 36
M 18
Q 38
M 19
WL 2
Q 40
M 7
Q 42
M 6
Q 44
M 8
Q 46
M 9
H 1
UL 6,35
M 21
UH 7,35
AR 8,198
M 20
AD 8,6
AD 154,7
M 22
H 2
AW 198,8
M 23
R 10
M 11
UL 6,36
UH 7,36
M 10
WL 2
R 8
PA 12288
DF
M 34
AR 8,151
AR 9,175
M 35
AD 8,6
AD 9,7
M 32
AW 151,8
AW 175,9
M 33
v_cmp_ne_u32 vcc, 2, v54
UL 6,37
M 30
UH 7,37
AR 8,199
M 31
AD 8,6
AD 155,7
M 28
FC
AW 199,8
M 29
M 26
M 27
WV 6
M 24
WL 0
FP
M 25
M 36
M 37
M 38
M 39
M 40
M 41
M 42
M 43
M 44
M 45
FR
M 46
M 47
M 58
M 59
M 56
M 57
M 54
M 55
WL 0
FQ 48
M 52
M 53
M 50
FZ 48
R 24
M 51
mw 72
R 26
M 48
TY 106,9736
TY 110,23560
M 49
R 28
M 60
ad 84,1680
W 0
M 61
M 62
M 63
W 1
M 64
M 65
M 66
W 2
M 67
WL 3
AN 104,106
AN 105,107
M 68
SH 106
SH 107
M 69
mw 75
AN 106,106
AN 107,107
M 70
WL 2
AN 108,110
AN 109,111
M 71
R 30
M 82
SH 110
SH 111
M 83
AN 110,110
AN 111,111
M 80
TY 114,11272
TY 118,25096
M 81
ad 84,1686
W 0
M 78
ae 44,70,0
M 79
s_bitcmp1_b32 s50, 0
s_cselect_b32 s51, 24576, 0
v_add_u32 v47, s51, v3
M 76
W 1
XL 14,256
M 77
XL 16,1024
M 74
M 75
W 2
M 72
XL 18,1792
M 73
WL 1
R 32
M 84
mw 0
AN 112,114
AN 113,115
M 85
SH 114
SH 115
M 86
AN 114,114
AN 115,115
M 87
WL 2
AN 116,118
AN 117,119
M 88
SH 118
SH 119
M 89
AN 118,118
AN 119,119
M 90
TY 122,12808
TY 126,26632
M 91
ad 84,1728
M 92
W 0
XL 20,2560
M 93
M 94
M 95
R 34
M 106
W 1
M 107
M 104
UL 6,38
UH 7,38
M 105
WL 3
AN 120,122
AN 121,123
M 102
SH 122
SH 123
M 103
AN 122,122
AN 123,123
M 100
WL 2
AN 124,126
AN 125,127
M 101
SH 126
SH 127
M 98
AN 126,126
AN 127,127
M 99
W 2
AR 8,152
)"
R"(M 96
AR 9,176
AD 8,6
M 97
R 48
M 108
mw 3
AD 9,7
AW 152,8
M 109
ad 84,1734
W 0
M 110
AW 176,9
UL 6,39
M 111
UH 7,39
AD 132,6
M 112
W 1
AD 156,7
M 113
UL 6,40
UH 7,40
M 114
AR 8,153
AR 9,177
M 115
W 2
AD 8,6
M 116
AD 9,7
AW 153,8
M 117
AW 177,9
UL 6,41
M 118
bc 49,19,66,60
UH 7,41
M 119
WL 0
R 50
M 120
mh 9
AD 133,6
AD 157,7
M 121
ad 66,3648
H 0
M 133
UL 6,42
UH 7,42
M 132
TX 58,9216
TX 62,23040
M 134
DG 1024
M 135
AR 8,154
AR 9,178
M 123
AD 8,6
AD 9,7
M 122
TY 66,9728
TY 70,23552
M 124
bc 48,19,64,56
AW 154,8
M 125
mh 12
AW 178,9
UL 6,43
M 137
ad 64,3648
H 0
M 136
TX 74,10752
TX 78,24576
M 138
WL 4
Q 0
M 139
Q 2
M 127
Q 4
M 126
TY 82,11264
TY 86,25088
M 128
Q 6
M 129
WL 4
Q 8
M 141
Q 10
M 140
TX 90,12288
TX 94,26112
M 142
Q 12
M 143
R 54
M 131
Q 14
M 130
TY 98,12800
TY 102,26624
M 0
R 52
M 1
WL 10
Q 16
M 13
Q 18
M 12
Q 20
M 14
Q 22
M 15
WL 8
Q 24
M 3
Q 26
M 2
Q 28
M 4
Q 30
M 5
WL 6
Q 32
M 17
Q 34
M 16
Q 36
M 18
Q 38
M 19
WL 2
Q 40
M 7
Q 42
M 6
Q 44
M 8
Q 46
M 9
H 1
UH 7,43
M 21
AD 134,6
AD 158,7
M 20
UL 6,44
UH 7,44
M 22
H 2
AR 8,155
M 23
R 58
M 11
AR 9,179
AD 8,6
M 10
WL 2
R 56
v_min_u32 v52, 752, v0
v_lshrrev_b32 v52, 1, v52
v_add_u32 v53, 0x1800, v52
global_load_dwordx2 a[200:201], v52, s[94:95] offset:-2048 sc1
global_load_dwordx2 a[202:203], v52, s[94:95] offset:-512 sc1
global_load_dwordx2 a[204:205], v52, s[94:95] offset:1024 sc1
global_load_dwordx2 a[206:207], v52, s[94:95] offset:2560 sc1
global_load_dwordx2 a[208:209], v53, s[94:95] offset:-2048 sc1
global_load_dwordx2 a[210:211], v53, s[94:95] offset:-512 sc1
global_load_dwordx2 a[212:213], v53, s[94:95] offset:1024 sc1
global_load_dwordx2 a[214:215], v53, s[94:95] offset:2560 sc1
M 34
AD 9,7
AW 155,8
M 35
AW 179,9
UL 6,45
M 32
UH 7,45
AD 135,6
M 33
AD 159,7
M 30
M 31
M 28
M 29
M 26
M 27
WV 13
M 24
WL 0
FP
M 25
M 36
M 37
M 38
M 39
M 40
M 41
M 42
M 43
M 44
M 45
FR
M 46
M 47
M 58
M 59
M 56
M 57
M 54
M 55
WL 0
FQ 52
M 52
M 53
M 50
FZ 52
R 72
M 51
mw 24
R 74
M 48
TY 106,520
TY 110,14344
M 49
R 76
M 60
ad 84,1776
W 0
M 61
M 62
M 63
W 1
M 64
M 65
M 66
W 2
M 67
WL 3
AN 104,106
AN 105,107
M 68
SH 106
SH 107
M 69
mw 27
AN 106,106
AN 107,107
M 70
WL 2
AN 108,110
AN 109,111
M 71
R 78
M 82
SH 110
SH 111
M 83
AN 110,110
AN 111,111
M 80
TY 114,2056
TY 118,15880
M 81
ad 84,1782
W 0
M 78
ae 44,70,0
M 79
s_bfe_u32 s51, s50, 0x10001
v_mad_i32_i24 v47, v49, s51, v48
XL 22,256
M 76
W 1
M 77
M 74
XL 24,1024
M 75
W 2
M 72
M 73
WL 1
R 80
M 84
mw 48
AN 112,114
AN 113,115
M 85
SH 114
SH 115
M 86
AN 114,114
AN 115,115
M 87
WL 2
AN 116,118
AN 117,119
M 88
SH 118
SH 119
M 89
AN 118,118
AN 119,119
M 90
TY 122,3592
TY 126,17416
M 91
ad 84,1824
M 92
W 0
XL 26,1792
M 93
XL 28,2560
M 94
M 95
R 82
M 106
W 1
M 107
M 104
M 105
WL 3
AN 120,122
AN 121,123
M 102
SH 122
SH 123
M 103
AN 122,122
AN 123,123
M 100
WL 2
AN 124,126
AN 125,127
M 101
SH 126
SH 127
M 98
AN 126,126
AN 127,127
M 99
W 2
M 96
M 97
R 0
M 108
mw 51
M 109
ad 84,1830
W 0
M 110
M 111
M 112
W 1
M 113
M 114
M 115
W 2
M 116
M 117
M 118
bc 49,20,66,60
M 119
WL 2
R 2
M 120
mh 18
M 121
ad 66,3840
H 0
M 133
M 132
TX 58,0
TX 62,13824
M 134
DG 1024
M 135
M 123
UL 6,14
UH 7,14
M 122
TY 66,512
TY 70,14336
M 124
bc 48,20,64,56
AD 160,6
M 125
mh 21
AD 184,7
UL 6,15
M 137
ad 64,3840
H 0
)"
R"(M 136
TX 74,1536
TX 78,15360
M 138
WL 4
Q 0
M 139
Q 2
M 127
Q 4
M 126
TY 82,2048
TY 86,15872
M 128
Q 6
M 129
WL 4
Q 8
M 141
Q 10
M 140
TX 90,3072
TX 94,16896
M 142
Q 12
M 143
R 6
M 131
Q 14
M 130
TY 98,3584
TY 102,17408
M 0
R 4
M 1
WL 10
Q 16
M 13
Q 18
M 12
Q 20
M 14
Q 22
M 15
WL 8
Q 24
M 3
Q 26
M 2
Q 28
M 4
Q 30
M 5
WL 6
Q 32
M 17
Q 34
M 16
Q 36
M 18
Q 38
M 19
WL 2
Q 40
M 7
Q 42
M 6
Q 44
M 8
Q 46
M 9
H 1
UH 7,15
M 21
AD 208,6
AD 232,7
M 20
UL 6,16
UH 7,16
M 22
H 2
AD 161,6
M 23
R 10
M 11
AD 185,7
UL 6,17
M 10
WL 2
R 8
PA 12288
DF
M 34
UH 7,17
AD 209,6
M 35
AD 233,7
UL 6,18
M 32
UH 7,18
AD 162,6
M 33
AD 186,7
UL 6,19
M 30
UH 7,19
AD 210,6
M 31
AD 234,7
UL 6,20
M 28
UH 7,20
AD 163,6
M 29
AD 187,7
UL 6,21
M 26
UH 7,21
AD 211,6
M 27
WV 6
AD 235,7
M 24
WL 0
FP
M 25
M 36
M 37
M 38
M 39
M 40
M 41
M 42
M 43
M 44
M 45
FR
M 46
M 47
M 58
M 59
M 56
M 57
M 54
M 55
WL 0
FQ 56
M 52
M 53
M 50
FZ 56
R 24
M 51
mw 72
R 26
M 48
TY 106,5128
TY 110,18952
M 49
R 28
M 60
ad 84,1872
W 0
M 61
M 62
M 63
W 1
M 64
M 65
M 66
W 2
M 67
WL 3
AN 104,106
AN 105,107
M 68
SH 106
SH 107
M 69
mw 75
AN 106,106
AN 107,107
M 70
WL 2
AN 108,110
AN 109,111
M 71
R 30
M 82
SH 110
SH 111
M 83
AN 110,110
AN 111,111
M 80
TY 114,6664
TY 118,20488
M 81
ad 84,1878
W 0
M 78
M 79
M 76
W 1
M 77
ae 44,68,0
M 74
s_bitcmp1_b32 s50, 2
s_cselect_b32 s51, 24576, 0
v_add_u32 v47, s51, v3
M 75
W 2
XL 30,256
M 72
M 73
WL 1
R 32
M 84
mw 0
AN 112,114
AN 113,115
M 85
SH 114
SH 115
M 86
AN 114,114
AN 115,115
M 87
WL 2
AN 116,118
AN 117,119
M 88
SH 118
SH 119
M 89
AN 118,118
AN 119,119
M 90
TY 122,8200
TY 126,22024
M 91
ad 84,1920
M 92
W 0
XL 32,1024
M 93
XL 34,1792
M 94
M 95
R 34
M 106
W 1
XL 36,2560
M 107
M 104
M 105
WL 3
AN 120,122
AN 121,123
M 102
SH 122
SH 123
M 103
AN 122,122
AN 123,123
M 100
WL 2
AN 124,126
AN 125,127
M 101
SH 126
SH 127
M 98
AN 126,126
AN 127,127
M 99
W 2
M 96
M 97
R 48
M 108
mw 3
M 109
ad 84,1926
W 0
M 110
M 111
M 112
W 1
M 113
M 114
M 115
W 2
M 116
M 117
M 118
bc 49,21,66,60
M 119
WL 2
R 50
M 120
mh 0
M 121
ad 66,4032
H 0
M 133
v_mov_b32 v54, s39
v_mul_f32 v54, 0xb62aaa8f, v54
M 132
TX 58,4608
TX 62,18432
M 134
DG 1024
M 135
v_add_u32 v52, %[bm], v52
v_bfe_u32 v53, v52, 6, 2
M 123
v_and_b32 v55, 63, v52
v_add_u32 v53, v53, v55
M 122
TY 66,5120
TY 70,18944
M 124
bc 48,21,64,56
v_and_b32 v52, 0x700, v52
M 125
mh 3
v_lshl_add_u32 v52, v53, 2, v52
v_add_u32 v52, %[sb], v52
M 137
ad 64,4032
H 0
M 136
TX 74,6144
TX 78,19968
M 138
WL 4
Q 0
M 139
Q 2
M 127
Q 4
M 126
TY 82,6656
TY 86,20480
M 128
Q 6
M 129
WL 4
Q 8
M 141
Q 10
M 140
TX 90,7680
TX 94,21504
M 142
Q 12
M 143
R 54
M 131
Q 14
M 130
TY 98,8192
TY 102,22016
M 0
R 52
M 1
WL 10
Q 16
M 13
Q 18
M 12
Q 20
M 14
Q 22
M 15
WL 8
Q 24
M 3
Q 26
M 2
Q 28
M 4
Q 30
M 5
WL 6
Q 32
M 17
Q 34
M 16
Q 36
M 18
Q 38
M 19
WL 2
Q 40
M 7
Q 42
M 6
Q 44
)"
R"(M 8
Q 46
M 9
H 1
AR 12,214
M 21
AR 13,215
AR 8,208
M 20
AR 9,209
v_add_f32 v12, v12, v8
M 22
H 2
v_add_f32 v13, v13, v9
M 23
R 58
M 11
AR 8,210
AR 9,211
M 10
WL 2
R 56
M 34
v_add_f32 v12, v12, v8
v_add_f32 v13, v13, v9
M 35
AR 8,212
AR 9,213
M 32
v_add_f32 v12, v12, v8
v_add_f32 v13, v13, v9
M 33
AR 6,206
AR 7,207
M 30
v_add_f32 v12, v12, v54
v_add_f32 v13, v13, v54
M 31
v_fma_f32 v8, v6, s86, v12
v_fma_f32 v9, v7, s86, v13
M 28
ds_write_b32 v52, v8 offset:1024
ds_write_b32 v52, v9 offset:1040
M 29
AR 8,200
AR 9,201
M 26
v_add_f32 v6, v6, v8
v_add_f32 v7, v7, v9
M 27
WV 5
v_fma_f32 v8, v6, s86, v12
v_fma_f32 v9, v7, s86, v13
M 24
WL 2
FP
ds_write_b32 v52, v8 offset:2560
M 25
ds_write_b32 v52, v9 offset:2576
AR 8,202
M 36
AR 9,203
v_add_f32 v6, v6, v8
M 37
v_add_f32 v7, v7, v9
v_fma_f32 v8, v6, s86, v12
M 38
ds_write_b32 v52, v8 offset:4096
v_fma_f32 v9, v7, s86, v13
M 39
ds_write_b32 v52, v9 offset:4112
AR 8,204
M 40
AR 9,205
v_add_f32 v6, v6, v8
M 41
v_add_f32 v7, v7, v9
v_fma_f32 v8, v6, s86, v12
M 42
ds_write_b32 v52, v8 offset:5632
v_fma_f32 v9, v7, s86, v13
M 43
ds_write_b32 v52, v9 offset:5648
M 44
UL 6,22
UH 7,22
M 45
FR
AD 164,6
M 46
AD 188,7
UL 6,23
M 47
UH 7,23
AD 212,6
M 58
AD 236,7
UL 6,24
M 59
UH 7,24
AD 165,6
M 56
AD 189,7
UL 6,25
M 57
UH 7,25
AD 213,6
M 54
AD 237,7
UL 6,26
M 55
WL 0
FQ 60
UH 7,26
M 52
AD 166,6
AD 190,7
M 53
UL 6,27
UH 7,27
M 50
FZ 60
R 72
M 51
mw 24
R 74
M 48
TY 106,9736
TY 110,23560
M 49
R 76
M 60
ad 84,1968
W 0
M 61
AD 214,6
AD 238,7
M 62
UL 6,28
UH 7,28
M 63
W 1
AD 167,6
M 64
AD 191,7
UL 6,29
M 65
UH 7,29
AD 215,6
M 66
W 2
AD 239,7
M 67
WL 3
AN 104,106
AN 105,107
M 68
SH 106
SH 107
M 69
mw 27
AN 106,106
AN 107,107
M 70
WL 2
AN 108,110
AN 109,111
M 71
R 78
M 82
SH 110
SH 111
M 83
AN 110,110
AN 111,111
M 80
TY 114,11272
TY 118,25096
M 81
ad 84,1974
W 0
M 78
M 79
M 76
W 1
M 77
M 74
M 75
W 2
M 72
ae 44,68,0
M 73
WL 1
R 80
M 84
mw 48
AN 112,114
AN 113,115
M 85
s_bfe_u32 s51, s50, 0x10003
SH 114
SH 115
M 86
AN 114,114
AN 115,115
M 87
WL 2
AN 116,118
AN 117,119
M 88
SH 118
SH 119
M 89
AN 118,118
AN 119,119
M 90
TY 122,12808
TY 126,26632
M 91
ad 84,2016
M 92
W 0
v_mad_i32_i24 v47, v49, s51, v48
M 93
XL 38,256
M 94
XL 40,1024
M 95
R 82
M 106
W 1
XL 42,1792
M 107
M 104
M 105
WL 3
AN 120,122
AN 121,123
M 102
SH 122
SH 123
M 103
AN 122,122
AN 123,123
M 100
WL 2
AN 124,126
AN 125,127
M 101
SH 126
SH 127
M 98
AN 126,126
AN 127,127
M 99
W 2
XL 44,2560
M 96
M 97
R 0
M 108
mw 51
M 109
ad 84,2022
W 0
M 110
M 111
M 112
W 1
M 113
M 114
M 115
W 2
M 116
M 117
M 118
bc 49,22,66,60
M 119
WL 2
R 2
M 120
mh 9
M 121
ad 66,4224
H 0
M 133
M 132
TX 58,9216
TX 62,23040
M 134
DG 1024
M 135
M 123
M 122
TY 66,9728
TY 70,23552
M 124
bc 48,22,64,56
M 125
mh 12
M 137
ad 64,4224
H 0
M 136
TX 74,10752
TX 78,24576
M 138
WL 4
Q 0
M 139
Q 2
M 127
Q 4
M 126
TY 82,11264
TY 86,25088
M 128
Q 6
M 129
WL 4
Q 8
M 141
Q 10
M 140
TX 90,12288
TX 94,26112
M 142
Q 12
M 143
R 6
M 131
Q 14
M 130
TY 98,12800
TY 102,26624
M 0
R 4
M 1
WL 10
Q 16
M 13
Q 18
M 12
Q 20
M 14
Q 22
M 15
WL 8
Q 24
M 3
Q 26
M 2
Q 28
M 4
Q 30
M 5
WL 6
Q 32
M 17
Q 34
M 16
Q 36
M 18
Q 38
M 19
WL 2
Q 40
M 7
Q 42
M 6
Q 44
M 8
Q 46
M 9
H 1
UL 6,30
M 21
UH 7,30
AD 168,6
M 20
AD 192,7
UL 6,31
M 22
H 2
UH 7,31
M 23
R 10
M 11
AD 216,6
AD 240,7
M 10
WL 2
R 8
PA 12288
DF
M 34
UL 6,32
UH 7,32
M 35
AD 169,6
)"
R"(AD 193,7
M 32
UL 6,33
UH 7,33
M 33
AD 217,6
AD 241,7
M 30
UL 6,34
UH 7,34
M 31
AD 170,6
AD 194,7
M 28
UL 6,35
UH 7,35
M 29
AD 218,6
AD 242,7
M 26
UL 6,36
UH 7,36
M 27
WV 6
AD 171,6
AD 195,7
M 24
WL 0
FP
UL 6,37
M 25
UH 7,37
AD 219,6
M 36
AD 243,7
M 37
M 38
M 39
M 40
M 41
M 42
M 43
M 44
M 45
FR
M 46
M 47
M 58
M 59
M 56
M 57
M 54
M 55
WL 0
FQ 64
M 52
M 53
M 50
FZ 64
R 24
M 51
mw 72
R 26
M 48
TY 106,520
TY 110,14344
M 49
R 28
M 60
ad 84,2064
W 0
M 61
M 62
M 63
W 1
M 64
M 65
M 66
W 2
M 67
WL 3
AN 104,106
AN 105,107
M 68
SH 106
SH 107
M 69
mw 75
AN 106,106
AN 107,107
M 70
WL 2
AN 108,110
AN 109,111
M 71
R 30
M 82
SH 110
SH 111
M 83
AN 110,110
AN 111,111
M 80
TY 114,2056
TY 118,15880
M 81
ad 84,2070
W 0
M 78
M 79
M 76
W 1
M 77
M 74
M 75
W 2
M 72
M 73
R 32
M 84
mw 0
WL 3
AN 112,114
AN 113,115
M 85
SH 114
SH 115
M 86
AN 114,114
AN 115,115
M 87
WL 2
AN 116,118
AN 117,119
M 88
SH 118
SH 119
M 89
AN 118,118
AN 119,119
M 90
TY 122,3592
TY 126,17416
M 91
ad 84,2112
M 92
W 0
M 93
ae 44,68,96
M 94
s_bitcmp1_b32 s50, 4
s_cselect_b32 s51, 24576, 0
v_add_u32 v47, s51, v3
M 95
R 34
M 106
W 1
XL 14,256
M 107
XL 16,1024
M 104
XL 18,1792
M 105
WL 3
AN 120,122
AN 121,123
M 102
SH 122
SH 123
M 103
AN 122,122
AN 123,123
M 100
WL 2
AN 124,126
AN 125,127
M 101
SH 126
SH 127
M 98
AN 126,126
AN 127,127
M 99
W 2
XL 20,2560
M 96
M 97
R 48
M 108
mw 3
M 109
ad 84,2118
W 0
M 110
M 111
M 112
W 1
M 113
M 114
M 115
W 2
M 116
M 117
M 118
bc 49,23,66,60
M 119
WL 2
R 50
M 120
mh 18
M 121
ad 66,4416
H 0
M 133
M 132
TX 58,0
TX 62,13824
M 134
DG 1024
M 135
M 123
M 122
TY 66,512
TY 70,14336
M 124
bc 48,23,64,56
M 125
mh 21
M 137
ad 64,4416
H 0
M 136
TX 74,1536
TX 78,15360
M 138
WL 4
Q 0
M 139
Q 2
M 127
Q 4
M 126
TY 82,2048
TY 86,15872
M 128
Q 6
M 129
WL 4
Q 8
M 141
Q 10
M 140
TX 90,3072
TX 94,16896
M 142
Q 12
M 143
R 54
M 131
Q 14
M 130
TY 98,3584
TY 102,17408
M 0
R 52
M 1
WL 10
Q 16
M 13
Q 18
M 12
Q 20
M 14
Q 22
M 15
WL 8
Q 24
M 3
Q 26
M 2
Q 28
M 4
Q 30
M 5
WL 6
Q 32
M 17
Q 34
M 16
Q 36
M 18
Q 38
M 19
WL 2
Q 40
M 7
Q 42
M 6
Q 44
M 8
Q 46
M 9
H 1
UL 6,38
M 21
UH 7,38
AD 172,6
M 20
AD 196,7
UL 6,39
M 22
H 2
UH 7,39
M 23
R 58
M 11
AD 220,6
AD 244,7
M 10
WL 2
R 56
M 34
UL 6,40
UH 7,40
M 35
AD 173,6
AD 197,7
M 32
UL 6,41
UH 7,41
M 33
AD 221,6
AD 245,7
M 30
UL 6,42
UH 7,42
M 31
AD 174,6
AD 198,7
M 28
UL 6,43
UH 7,43
M 29
AD 222,6
AD 246,7
M 26
UL 6,44
UH 7,44
M 27
WV 5
AD 175,6
AD 199,7
M 24
WL 0
FP
UL 6,45
M 25
UH 7,45
AD 223,6
M 36
AD 247,7
M 37
M 38
M 39
M 40
M 41
M 42
M 43
M 44
M 45
FR
M 46
M 47
M 58
M 59
M 56
M 57
M 54
M 55
WL 0
FQ 68
M 52
M 53
M 50
FZ 68
R 72
M 51
mw 24
R 74
M 48
TY 106,5128
TY 110,18952
M 49
R 76
M 60
ad 84,2160
W 0
M 61
M 62
M 63
)"
R"(W 1
M 64
M 65
M 66
W 2
M 67
WL 3
AN 104,106
AN 105,107
M 68
SH 106
SH 107
M 69
mw 27
AN 106,106
AN 107,107
M 70
WL 2
AN 108,110
AN 109,111
M 71
R 78
M 82
SH 110
SH 111
M 83
AN 110,110
AN 111,111
M 80
TY 114,6664
TY 118,20488
M 81
ad 84,2166
W 0
M 78
M 79
M 76
W 1
M 77
M 74
M 75
W 2
M 72
M 73
R 80
M 84
mw 48
WL 3
AN 112,114
AN 113,115
M 85
SH 114
SH 115
M 86
AN 114,114
AN 115,115
M 87
WL 2
AN 116,118
AN 117,119
M 88
SH 118
SH 119
M 89
AN 118,118
AN 119,119
M 90
TY 122,8200
TY 126,22024
M 91
ad 84,2208
M 92
W 0
M 93
ae 44,68,96
M 94
s_bfe_u32 s51, s50, 0x10005
v_mad_i32_i24 v47, v49, s51, v48
XL 22,256
M 95
R 82
M 106
W 1
XL 24,1024
M 107
M 104
M 105
WL 3
AN 120,122
AN 121,123
M 102
SH 122
SH 123
M 103
AN 122,122
AN 123,123
M 100
WL 2
AN 124,126
AN 125,127
M 101
SH 126
SH 127
M 98
AN 126,126
AN 127,127
M 99
W 2
XL 26,1792
M 96
M 97
R 0
M 108
mw 51
XL 28,2560
M 109
ad 84,2214
W 0
M 110
M 111
M 112
W 1
M 113
M 114
M 115
W 2
M 116
M 117
M 118
bc 49,0,66,60
M 119
WL 2
R 2
M 120
mh 0
M 121
ad 66,32
H 0
M 133
M 132
TX 58,4608
TX 62,18432
M 134
DG 1024
M 135
M 123
M 122
TY 66,5120
TY 70,18944
M 124
bc 48,0,64,56
M 125
mh 3
M 137
ad 64,0
H 0
M 136
TX 74,6144
TX 78,19968
M 138
WL 4
Q 0
M 139
Q 2
M 127
Q 4
M 126
TY 82,6656
TY 86,20480
M 128
Q 6
M 129
WL 4
Q 8
M 141
Q 10
M 140
TX 90,7680
TX 94,21504
M 142
Q 12
M 143
R 6
M 131
Q 14
M 130
TY 98,8192
TY 102,22016
M 0
R 4
M 1
WL 10
Q 16
M 13
Q 18
M 12
Q 20
M 14
Q 22
M 15
WL 8
Q 24
M 3
Q 26
M 2
Q 28
M 4
Q 30
M 5
WL 6
Q 32
M 17
Q 34
M 16
Q 36
M 18
Q 38
M 19
WL 2
Q 40
M 7
Q 42
M 6
Q 44
M 8
Q 46
M 9
H 1
UL 6,14
M 21
UH 7,14
AD 176,6
M 20
AD 200,7
UL 6,15
M 22
H 2
UH 7,15
M 23
R 10
M 11
AD 224,6
AD 248,7
M 10
WL 2
R 8
PA 12288
DF
M 34
UL 6,16
UH 7,16
M 35
AD 177,6
AD 201,7
M 32
UL 6,17
UH 7,17
M 33
AD 225,6
AD 249,7
M 30
UL 6,18
UH 7,18
M 31
AD 178,6
AD 202,7
M 28
UL 6,19
UH 7,19
M 29
AD 226,6
AD 250,7
M 26
UL 6,20
UH 7,20
M 27
WV 6
AD 179,6
AD 203,7
M 24
WL 0
FP
UL 6,21
M 25
UH 7,21
AD 227,6
M 36
AD 251,7
M 37
M 38
M 39
M 40
M 41
M 42
M 43
M 44
M 45
FR
M 46
M 47
M 58
M 59
M 56
M 57
M 54
M 55
WL 0
FQ 72
M 52
M 53
M 50
FZ 72
R 24
M 51
mw 72
R 26
M 48
TY 106,9736
TY 110,23560
M 49
R 28
M 60
ad 84,2256
W 0
M 61
M 62
M 63
W 1
M 64
M 65
M 66
W 2
M 67
WL 3
AN 104,106
AN 105,107
M 68
SH 106
SH 107
M 69
mw 75
AN 106,106
AN 107,107
M 70
WL 2
AN 108,110
AN 109,111
M 71
R 30
M 82
SH 110
SH 111
M 83
AN 110,110
AN 111,111
M 80
TY 114,11272
TY 118,25096
M 81
ad 84,2262
W 0
M 78
M 79
M 76
W 1
M 77
M 74
M 75
W 2
M 72
M 73
R 32
M 84
mw 0
WL 3
AN 112,114
AN 113,115
M 85
SH 114
SH 115
M 86
AN 114,114
AN 115,115
M 87
WL 2
AN 116,118
AN 117,119
M 88
SH 118
SH 119
M 89
AN 118,118
AN 119,119
M 90
TY 122,12808
TY 126,26632
M 91
ad 84,0
M 92
W 0
)"
R"(M 93
M 94
M 95
R 34
M 106
W 1
M 107
M 104
M 105
WL 3
AN 120,122
AN 121,123
M 102
SH 122
SH 123
M 103
AN 122,122
AN 123,123
M 100
WL 2
AN 124,126
AN 125,127
M 101
SH 126
SH 127
M 98
AN 126,126
AN 127,127
M 99
W 2
M 96
M 97
R 48
M 108
mw 3
M 109
ad 84,6
W 0
M 110
M 111
M 112
W 1
M 113
M 114
M 115
W 2
M 116
M 117
M 118
bc 49,1,66,60
M 119
WL 2
R 50
M 120
mh 9
M 121
ad 66,224
H 0
M 133
M 132
TX 58,9216
TX 62,23040
M 134
DG 1024
M 135
M 123
M 122
TY 66,9728
TY 70,23552
M 124
bc 48,1,64,56
M 125
mh 12
M 137
ad 64,224
H 0
M 136
TX 74,10752
TX 78,24576
M 138
WL 4
Q 0
M 139
Q 2
M 127
Q 4
M 126
TY 82,11264
TY 86,25088
M 128
Q 6
M 129
WL 4
Q 8
M 141
Q 10
M 140
TX 90,12288
TX 94,26112
M 142
Q 12
M 143
R 54
M 131
Q 14
M 130
TY 98,12800
TY 102,26624
M 0
R 52
M 1
WL 10
Q 16
M 13
Q 18
M 12
Q 20
M 14
Q 22
M 15
WL 8
Q 24
M 3
Q 26
M 2
Q 28
M 4
Q 30
M 5
WL 6
Q 32
M 17
Q 34
M 16
Q 36
M 18
Q 38
M 19
WL 2
Q 40
M 7
Q 42
M 6
Q 44
M 8
Q 46
M 9
DG 1024
M 21
UL 6,22
UH 7,22
M 20
AD 180,6
AD 204,7
M 22
mh 15
UL 6,23
UH 7,23
M 23
R 58
M 11
ad 64,195
H 0
M 10
WL 2
R 56
M 34
AD 228,6
AD 252,7
M 35
DG 1024
M 32
UL 6,24
UH 7,24
BV 52,0
M 33
AD 181,6
AD 205,7
M 30
UL 6,25
UH 7,25
M 31
AD 229,6
AD 253,7
M 28
UL 6,26
UH 7,26
M 29
AD 182,6
AD 206,7
M 26
UL 6,27
UH 7,27
M 27
WV 6
AD 230,6
AD 254,7
M 24
WL 1
FP
UL 6,28
M 25
UH 7,28
AD 183,6
M 36
AD 207,7
UL 6,29
M 37
UH 7,29
AD 231,6
M 38
AD 255,7
M 39
M 40
ds_read_b64 v[12:13], v4 offset:256
ds_read_b64 v[46:47], v4 offset:264
M 41
AR 6,36
AR 7,60
M 42
AR 8,84
AR 9,108
M 43
WL 3
EF 6,52
EF 7,53
M 44
EF 8,54
EF 9,55
M 45
FR
CV 30,6,7
M 46
CV 31,8,9
AR 6,37
M 47
AR 7,61
AR 8,85
M 58
AR 9,109
EF 6,52
M 59
EF 7,53
EF 8,54
M 56
EF 9,55
CV 32,6,7
M 57
CV 33,8,9
AR 6,38
M 54
AR 7,62
AR 8,86
M 55
WL 0
FQ 76
AR 9,110
M 52
EF 6,52
EF 7,53
M 53
EF 8,54
EF 9,55
M 50
FZ 76
R 72
M 51
mw 24
R 74
M 48
TY 106,520
TY 110,14344
M 49
R 76
M 60
ad 84,48
W 0
M 61
CV 34,6,7
CV 35,8,9
M 62
AR 6,39
AR 7,63
M 63
W 1
AR 8,87
M 64
AR 9,111
EF 6,52
M 65
EF 7,53
EF 8,54
M 66
W 2
EF 9,55
M 67
WL 3
AN 104,106
AN 105,107
M 68
SH 106
SH 107
M 69
mw 27
AN 106,106
AN 107,107
M 70
WL 2
AN 108,110
AN 109,111
M 71
R 78
M 82
SH 110
SH 111
M 83
AN 110,110
AN 111,111
M 80
TY 114,2056
TY 118,15880
M 81
ad 84,54
W 0
M 78
CV 36,6,7
CV 37,8,9
M 79
ae 42,74,0
AR 6,40
M 76
W 1
AR 7,64
M 77
s_bitcmp1_b32 s50, 0
s_cselect_b32 s52, 24576, 0
v_add_u32 v50, s52, v3
ST 30,0
M 74
AR 8,88
AR 9,112
M 75
W 2
EF 6,52
M 72
EF 7,53
EF 8,54
M 73
WL 1
R 80
M 84
mw 48
AN 112,114
AN 113,115
M 85
SH 114
SH 115
M 86
AN 114,114
AN 115,115
M 87
WL 2
AN 116,118
AN 117,119
M 88
SH 118
SH 119
M 89
AN 118,118
AN 119,119
M 90
TY 122,3592
TY 126,17416
M 91
ad 84,96
ST 32,768
M 92
W 0
EF 9,55
M 93
CV 38,6,7
CV 39,8,9
M 94
AR 6,41
AR 7,65
M 95
R 82
M 106
W 1
AR 8,89
M 107
AR 9,113
EF 6,52
M 104
EF 7,53
EF 8,54
M 105
WL 3
AN 120,122
AN 121,123
)"
R"(M 102
SH 122
SH 123
M 103
AN 122,122
AN 123,123
M 100
WL 2
AN 124,126
AN 125,127
M 101
SH 126
SH 127
ST 34,1536
M 98
AN 126,126
AN 127,127
M 99
W 2
EF 9,55
M 96
CV 40,6,7
CV 41,8,9
M 97
R 0
M 108
mw 51
AR 6,42
AR 7,66
M 109
ad 84,102
W 0
M 110
AR 8,90
AR 9,114
M 111
EF 6,52
EF 7,53
M 112
W 1
EF 8,54
M 113
EF 9,55
CV 42,6,7
M 114
CV 43,8,9
AR 6,43
M 115
W 2
ST 36,2304
M 116
AR 7,67
AR 8,91
M 117
AR 9,115
EF 6,52
M 118
bc 49,2,66,60
EF 7,53
M 119
WL 0
R 2
M 120
mh 18
EF 8,54
EF 9,55
M 121
ad 66,416
H 0
M 133
CV 44,6,7
CV 45,8,9
M 132
TX 58,0
TX 62,13824
BV 52,512
M 134
DG 1024
M 135
AR 6,132
AR 7,156
M 123
AR 8,180
EF 6,12
M 122
TY 66,512
TY 70,14336
M 124
bc 48,2,64,56
ae 42,74,0
M 125
mh 21
EF 7,13
EF 8,46
M 137
ad 64,416
H 0
M 136
WL 3
TX 74,1536
TX 78,15360
M 138
s_bfe_u32 s52, s50, 0x10001
Q 0
v_mad_i32_i24 v50, v49, s52, v48
M 139
Q 2
ST 38,0
M 127
Q 4
M 126
TY 82,2048
TY 86,15872
M 128
Q 6
M 129
WL 4
Q 8
M 141
Q 10
M 140
TX 90,3072
TX 94,16896
M 142
Q 12
M 143
R 6
M 131
Q 14
M 130
TY 98,3584
TY 102,17408
v_mfma_f32_16x16x32_f16 a[36:39], v[56:59], a[0:3], a[32:35]
R 4
M 1
WL 10
Q 16
ST 40,768
v_mfma_f32_16x16x32_f16 a[60:63], v[60:63], a[12:15], a[32:35]
Q 18
M 12
Q 20
v_mfma_f32_16x16x32_f16 a[64:67], v[64:67], a[8:11], a[32:35]
Q 22
M 15
WL 8
Q 24
v_mfma_f32_16x16x32_f16 a[40:43], v[68:71], a[4:7], a[32:35]
Q 26
M 2
Q 28
M 4
Q 30
M 5
WL 6
Q 32
M 17
Q 34
M 16
Q 36
M 18
Q 38
M 19
WL 1
Q 40
ST 42,1536
M 7
Q 42
M 6
Q 44
M 8
Q 46
M 9
H 1
EF 136,47
M 21
CV 14,6,7
CV 15,8,136
M 20
AR 6,133
AR 7,157
M 22
H 2
AR 8,181
M 23
R 10
M 11
EF 6,12
EF 7,13
M 10
WL 2
R 8
PA 12288
DF
M 34
EF 8,46
EF 137,47
M 35
ST 44,2304
CV 16,6,7
M 32
CV 17,8,137
AR 6,134
M 33
AR 7,158
AR 8,182
M 30
EF 6,12
EF 7,13
M 31
EF 8,46
EF 138,47
M 28
CV 18,6,7
CV 19,8,138
M 29
AR 6,135
AR 7,159
v_mfma_f32_16x16x32_f16 a[88:91], v[64:67], a[16:19], a[32:35]
AR 8,183
EF 6,12
M 27
WV 11
EF 7,13
EF 8,46
v_mfma_f32_16x16x32_f16 a[84:87], v[56:59], a[16:19], a[32:35]
WL 0
FP
EF 139,47
M 25
CV 20,6,7
CV 21,8,139
v_mfma_f32_16x16x32_f16 a[108:111], v[56:59], a[24:27], a[32:35]
AR 6,136
AR 7,160
AR 8,184
EF 6,12
M 37
ae 42,74,0
EF 7,13
EF 8,46
EF 140,47
v_mfma_f32_16x16x32_f16 a[112:115], v[64:67], a[24:27], a[32:35]
s_bitcmp1_b32 s50, 0
s_cselect_b32 s52, 24576, 0
v_add_u32 v50, s52, v3
ST 14,128
CV 22,6,7
M 39
CV 23,8,140
AR 6,137
AR 7,161
AR 8,185
M 40
EF 6,12
EF 7,13
EF 8,46
EF 141,47
M 41
CV 24,6,7
CV 25,8,141
AR 6,138
AR 7,162
M 42
AR 8,186
EF 6,12
EF 7,13
EF 8,46
M 43
EF 142,47
CV 26,6,7
CV 27,8,142
AR 6,139
M 44
AR 7,163
AR 8,187
EF 6,12
EF 7,13
M 45
FR
EF 8,46
EF 143,47
CV 28,6,7
M 46
CV 29,8,143
M 47
ds_read_b64 v[12:13], v4 offset:1536
ds_read_b64 v[46:47], v4 offset:1544
M 58
EF 160,52
EF 184,53
M 59
EF 208,54
EF 232,55
M 56
ST 16,896
CV 30,160,184
M 57
CV 31,208,232
EF 161,52
M 54
EF 185,53
EF 209,54
M 55
WL 2
FQ 80
EF 233,55
M 52
CV 32,161,185
CV 33,209,233
M 53
EF 162,52
EF 186,53
v_mfma_f32_16x16x32_f16 a[136:139], v[64:67], a[0:3], a[32:35]
FZ 80
R 24
M 51
mw 72
R 26
v_mfma_f32_16x16x32_f16 a[132:135], v[56:59], a[0:3], a[32:35]
TY 106,5128
TY 110,18952
M 49
R 28
v_mfma_f32_16x16x32_f16 a[156:159], v[56:59], a[8:11], a[32:35]
ad 84,144
W 0
M 61
EF 210,54
EF 234,55
v_mfma_f32_16x16x32_f16 a[160:163], v[64:67], a[8:11], a[32:35]
ST 18,1664
CV 34,162,186
M 63
W 1
CV 35,210,234
M 64
EF 163,52
EF 187,53
M 65
EF 211,54
EF 235,55
M 66
W 2
CV 36,163,187
M 67
WL 3
AN 104,106
AN 105,107
M 68
SH 106
SH 107
M 69
mw 75
AN 106,106
AN 107,107
M 70
WL 2
AN 108,110
AN 109,111
M 71
R 30
M 82
SH 110
SH 111
M 83
AN 110,110
AN 111,111
M 80
TY 114,6664
TY 118,20488
ST 20,2432
M 81
ad 84,150
W 0
M 78
CV 37,211,235
M 79
EF 164,52
EF 188,53
EF 212,54
EF 236,55
M 76
W 1
CV 38,164,188
CV 39,212,236
EF 165,52
M 77
EF 189,53
EF 213,54
EF 237,55
CV 40,165,189
v_mfma_f32_16x16x32_f16 a[184:187], v[64:67], a[16:19], a[32:35]
CV 41,213,237
EF 166,52
EF 190,53
EF 214,54
M 75
W 2
EF 238,55
)"
R"(CV 42,166,190
CV 43,214,238
v_mfma_f32_16x16x32_f16 a[180:183], v[56:59], a[16:19], a[32:35]
EF 167,52
EF 191,53
EF 215,54
EF 239,55
M 73
WL 0
R 32
CV 44,167,191
CV 45,215,239
v_mfma_f32_16x16x32_f16 v[136:139], v[56:59], a[24:27], v[128:131]
mw 0
AN 112,114
AN 113,115
BV 52,1792
M 85
SH 114
SH 115
AR 6,44
v_mfma_f32_16x16x32_f16 v[140:143], v[64:67], a[24:27], v[128:131]
AN 114,114
AN 115,115
AR 7,68
M 87
AN 116,118
AN 117,119
AR 8,92
M 88
SH 118
SH 119
AR 9,116
M 89
AN 118,118
AN 119,119
EF 6,12
M 90
TY 122,8200
TY 126,22024
EF 7,13
M 91
ad 84,192
EF 8,46
M 92
W 0
ae 42,74,0
M 93
s_bfe_u32 s52, s50, 0x10001
v_mad_i32_i24 v50, v49, s52, v48
ST 22,128
EF 9,47
M 94
CV 14,6,7
CV 15,8,9
AR 6,45
M 95
R 34
AR 7,69
M 106
W 1
AR 8,93
AR 9,117
M 107
EF 6,12
EF 7,13
EF 8,46
M 104
EF 9,47
CV 16,6,7
CV 17,8,9
M 105
WL 3
AN 120,122
AN 121,123
AR 6,46
M 102
SH 122
SH 123
AR 7,70
M 103
AN 122,122
AN 123,123
AR 8,94
M 100
WL 2
AN 124,126
AN 125,127
AR 9,118
M 101
SH 126
SH 127
EF 6,12
v_mfma_f32_16x16x32_f16 v[164:167], v[64:67], a[0:3], v[128:131]
AN 126,126
AN 127,127
EF 7,13
M 99
W 2
ST 24,896
EF 8,46
v_mfma_f32_16x16x32_f16 v[160:163], v[56:59], a[0:3], v[128:131]
EF 9,47
CV 18,6,7
CV 19,8,9
M 97
R 48
AR 6,47
v_mfma_f32_16x16x32_f16 v[184:187], v[56:59], a[8:11], v[128:131]
mw 3
AR 7,71
AR 8,95
AR 9,119
M 109
ad 84,198
W 0
EF 6,12
v_mfma_f32_16x16x32_f16 v[188:191], v[64:67], a[8:11], v[128:131]
EF 7,13
EF 8,46
EF 9,47
M 111
CV 20,6,7
CV 21,8,9
M 112
W 1
AR 6,48
AR 7,72
M 113
AR 8,96
AR 9,120
EF 6,12
M 114
EF 7,13
EF 8,46
EF 9,47
M 115
W 2
CV 6,6,7
CV 7,8,9
M 116
AW 200,6
AW 201,7
AR 6,49
M 117
ST 26,1664
AR 7,73
AR 8,97
M 118
bc 49,3,66,60
AR 9,121
EF 6,12
M 119
WL 0
R 50
EF 7,13
v_mfma_f32_16x16x32_f16 v[208:211], v[56:59], a[16:19], v[128:131]
mh 0
EF 8,46
EF 9,47
CV 6,6,7
M 121
ad 66,608
H 0
CV 7,8,9
v_mfma_f32_16x16x32_f16 v[232:235], v[60:63], a[28:31], v[128:131]
AW 202,6
AW 203,7
AR 6,50
M 132
TX 58,4608
TX 62,18432
AR 7,74
v_mfma_f32_16x16x32_f16 v[236:239], v[64:67], a[24:27], v[128:131]
DG 1024
AR 8,98
M 135
AR 9,122
EF 6,12
EF 7,13
v_mfma_f32_16x16x32_f16 v[212:215], v[68:71], a[20:23], v[128:131]
EF 8,46
EF 9,47
CV 6,6,7
M 122
TY 66,5120
TY 70,18944
CV 7,8,9
M 124
bc 48,3,64,56
AW 204,6
AW 205,7
M 125
mh 3
ST 28,2432
AR 6,51
AR 7,75
M 137
ad 64,608
H 0
AR 8,99
M 136
TX 74,6144
TX 78,19968
AR 9,123
M 138
WL 4
Q 0
EF 6,12
M 139
Q 2
EF 7,13
M 127
Q 4
EF 8,46
M 126
TY 82,6656
TY 86,20480
EF 9,47
M 128
Q 6
CV 6,6,7
M 129
WL 4
Q 8
CV 7,8,9
M 141
Q 10
AW 206,6
M 140
TX 90,7680
TX 94,21504
AW 207,7
M 142
Q 12
ds_read_b64 v[12:13], v4 offset:2048
M 143
R 54
ds_read_b64 v[46:47], v4 offset:2056
M 131
Q 14
M 130
TY 98,8192
TY 102,22016
M 0
R 52
M 1
WL 12
Q 16
M 13
Q 18
M 12
Q 20
M 14
Q 22
M 15
WL 10
Q 24
M 3
Q 26
M 2
Q 28
v_mfma_f32_16x16x32_f16 a[44:47], v[72:75], a[0:3], a[32:35]
Q 30
M 5
WL 7
Q 32
v_mfma_f32_16x16x32_f16 a[68:71], v[76:79], a[12:15], a[32:35]
Q 34
M 16
Q 36
v_mfma_f32_16x16x32_f16 a[72:75], v[80:83], a[8:11], a[32:35]
Q 38
M 19
WL 2
Q 40
v_mfma_f32_16x16x32_f16 a[48:51], v[84:87], a[4:7], a[32:35]
Q 42
M 6
Q 44
M 8
Q 46
M 9
H 1
ae 42,74,0
M 21
s_bitcmp1_b32 s50, 0
s_cselect_b32 s52, 24576, 0
v_add_u32 v50, s52, v3
ST 30,256
M 20
AR 6,140
AR 7,164
M 22
H 2
AR 8,188
M 23
R 58
M 11
EF 6,52
EF 7,53
M 10
WL 2
R 56
M 34
EF 8,54
EF 144,55
M 35
CV 22,6,7
CV 23,8,144
M 32
AR 6,141
AR 7,165
M 33
AR 8,189
EF 6,52
v_mfma_f32_16x16x32_f16 a[96:99], v[80:83], a[16:19], a[32:35]
EF 7,53
EF 8,54
M 31
EF 145,55
CV 24,6,7
v_mfma_f32_16x16x32_f16 a[92:95], v[72:75], a[16:19], a[32:35]
ST 32,1024
CV 25,8,145
M 29
AR 6,142
AR 7,166
M 26
AR 8,190
EF 6,52
M 27
WV 9
EF 7,53
EF 8,54
M 24
WL 0
FP
EF 146,55
M 25
CV 26,6,7
CV 27,8,146
M 36
AR 6,143
AR 7,167
M 37
AR 8,191
EF 6,52
M 38
EF 7,53
EF 8,54
M 39
EF 147,55
CV 28,6,7
v_mfma_f32_16x16x32_f16 a[116:119], v[72:75], a[24:27], a[32:35]
CV 29,8,147
M 41
AR 6,144
AR 7,168
AR 8,192
EF 6,52
EF 7,53
EF 8,54
v_mfma_f32_16x16x32_f16 a[120:123], v[80:83], a[24:27], a[32:35]
ST 34,1792
EF 148,55
CV 6,6,7
CV 7,8,148
AW 208,6
AW 209,7
M 43
AR 6,145
AR 7,169
AR 8,193
EF 6,52
EF 7,53
EF 8,54
M 44
EF 149,55
CV 6,6,7
CV 7,8,149
AW 210,6
AW 211,7
AR 6,146
M 45
FR
AR 7,170
AR 8,194
EF 6,52
EF 7,53
EF 8,54
M 46
EF 150,55
CV 6,6,7
CV 7,8,150
AW 212,6
AW 213,7
AR 6,147
M 47
AR 7,171
)"
R"(AR 8,195
EF 6,52
EF 7,53
EF 8,54
EF 151,55
M 58
CV 6,6,7
CV 7,8,151
AW 214,6
AW 215,7
M 59
BV 52,3072
EF 168,12
EF 192,13
M 56
EF 216,46
EF 240,47
CV 6,168,192
M 57
CV 7,216,240
AW 216,6
AW 217,7
v_mfma_f32_16x16x32_f16 a[144:147], v[80:83], a[0:3], a[32:35]
EF 169,12
EF 193,13
EF 217,46
M 55
WL 1
FQ 84
EF 241,47
CV 6,169,193
v_mfma_f32_16x16x32_f16 a[140:143], v[72:75], a[0:3], a[32:35]
ST 36,2560
CV 7,217,241
AW 218,6
M 53
AW 219,7
EF 170,12
EF 194,13
M 50
FZ 84
R 72
EF 218,46
M 51
mw 24
R 74
EF 242,47
M 48
TY 106,9736
TY 110,23560
CV 6,170,194
M 49
R 76
CV 7,218,242
M 60
ad 84,240
W 0
AW 220,6
M 61
AW 221,7
EF 171,12
EF 195,13
M 62
EF 219,46
EF 243,47
CV 6,171,195
M 63
W 1
CV 7,219,243
AW 222,6
v_mfma_f32_16x16x32_f16 a[164:167], v[72:75], a[8:11], a[32:35]
AW 223,7
M 65
EF 172,12
EF 196,13
EF 220,46
v_mfma_f32_16x16x32_f16 a[168:171], v[80:83], a[8:11], a[32:35]
WL 3
W 2
ae 42,74,0
M 67
s_bfe_u32 s52, s50, 0x10001
AN 104,106
AN 105,107
v_mad_i32_i24 v50, v49, s52, v48
M 68
SH 106
SH 107
ST 38,256
M 69
mw 27
AN 106,106
AN 107,107
EF 244,47
M 70
WL 0
AN 108,110
AN 109,111
CV 30,172,196
M 71
R 78
CV 31,220,244
M 82
SH 110
SH 111
EF 173,12
M 83
AN 110,110
AN 111,111
EF 197,13
M 80
TY 114,11272
TY 118,25096
EF 221,46
M 81
ad 84,246
W 0
EF 245,47
v_mfma_f32_16x16x32_f16 a[192:195], v[80:83], a[16:19], a[32:35]
CV 32,173,197
CV 33,221,245
EF 174,12
M 79
EF 198,13
EF 222,46
EF 246,47
v_mfma_f32_16x16x32_f16 a[188:191], v[72:75], a[16:19], a[32:35]
W 1
CV 34,174,198
CV 35,222,246
M 77
EF 175,12
EF 199,13
EF 223,46
M 74
ST 40,1024
EF 247,47
CV 36,175,199
M 75
W 2
CV 37,223,247
M 72
ds_read_b64 v[12:13], v4 offset:3328
ds_read_b64 v[46:47], v4 offset:3336
AR 6,52
M 73
WL 2
R 80
AR 7,76
M 84
mw 48
AN 112,114
AN 113,115
AR 8,100
M 85
SH 114
SH 115
AR 9,124
M 86
AN 114,114
AN 115,115
EF 6,52
M 87
AN 116,118
AN 117,119
EF 7,53
v_mfma_f32_16x16x32_f16 v[144:147], v[72:75], a[24:27], v[128:131]
SH 118
SH 119
EF 8,54
M 89
AN 118,118
AN 119,119
EF 9,55
v_mfma_f32_16x16x32_f16 v[148:151], v[80:83], a[24:27], v[128:131]
TY 122,12808
TY 126,26632
CV 6,6,7
M 91
ad 84,288
CV 7,8,9
M 92
W 0
ST 42,1792
AW 224,6
M 93
AW 225,7
AR 6,53
AR 7,77
M 94
AR 8,101
AR 9,125
EF 6,52
M 95
R 82
EF 7,53
M 106
W 1
EF 8,54
EF 9,55
M 107
CV 6,6,7
CV 7,8,9
AW 226,6
M 104
AW 227,7
AR 6,54
AR 7,78
M 105
WL 3
AN 120,122
AN 121,123
AR 8,102
v_mfma_f32_16x16x32_f16 v[172:175], v[80:83], a[0:3], v[128:131]
SH 122
SH 123
AR 9,126
M 103
AN 122,122
AN 123,123
EF 6,52
v_mfma_f32_16x16x32_f16 v[168:171], v[72:75], a[0:3], v[128:131]
WL 2
AN 124,126
AN 125,127
EF 7,53
M 101
SH 126
SH 127
EF 8,54
M 98
AN 126,126
AN 127,127
ST 44,2560
M 99
W 2
EF 9,55
CV 6,6,7
M 96
CV 7,8,9
AW 228,6
AW 229,7
M 97
R 0
AR 6,55
M 108
mw 51
AR 7,79
AR 8,103
AR 9,127
M 109
ad 84,294
W 0
EF 6,52
M 110
EF 7,53
EF 8,54
EF 9,55
M 111
CV 6,6,7
CV 7,8,9
AW 230,6
v_mfma_f32_16x16x32_f16 v[192:195], v[72:75], a[8:11], v[128:131]
W 1
AW 231,7
M 113
AR 6,56
AR 7,80
AR 8,104
v_mfma_f32_16x16x32_f16 v[196:199], v[80:83], a[8:11], v[128:131]
AR 9,128
EF 6,52
EF 7,53
M 115
W 2
EF 8,54
EF 9,55
M 116
ae 42,72,0
CV 38,6,7
CV 39,8,9
M 117
s_bitcmp1_b32 s50, 2
s_cselect_b32 s52, 24576, 0
v_add_u32 v50, s52, v3
ST 14,0
M 118
bc 49,4,66,60
AR 6,57
AR 7,81
M 119
WL 0
R 2
AR 8,105
M 120
mh 9
AR 9,129
EF 6,52
EF 7,53
M 121
ad 66,800
H 0
EF 8,54
M 133
EF 9,55
CV 40,6,7
CV 41,8,9
M 132
TX 58,9216
TX 62,23040
AR 6,58
M 134
DG 1024
AR 7,82
M 135
AR 8,106
AR 9,130
EF 6,52
M 123
EF 7,53
EF 8,54
EF 9,55
M 122
TY 66,9728
TY 70,23552
CV 42,6,7
v_mfma_f32_16x16x32_f16 v[216:219], v[72:75], a[16:19], v[128:131]
bc 48,4,64,56
CV 43,8,9
AR 6,59
M 125
mh 12
ST 16,768
AR 7,83
AR 8,107
v_mfma_f32_16x16x32_f16 v[240:243], v[76:79], a[28:31], v[128:131]
ad 64,800
H 0
AR 9,131
M 136
TX 74,10752
TX 78,24576
EF 6,52
v_mfma_f32_16x16x32_f16 v[244:247], v[80:83], a[24:27], v[128:131]
WL 4
Q 0
EF 7,53
M 139
Q 2
EF 8,54
v_mfma_f32_16x16x32_f16 v[220:223], v[84:87], a[20:23], v[128:131]
Q 4
EF 9,55
M 126
TY 82,11264
TY 86,25088
CV 44,6,7
M 128
Q 6
CV 45,8,9
M 129
WL 4
Q 8
BV 52,3584
M 141
Q 10
M 140
TX 90,12288
TX 94,26112
M 142
Q 12
M 143
R 6
ST 18,1536
M 131
Q 14
M 130
TY 98,12800
TY 102,26624
M 0
R 4
M 1
WL 11
Q 16
M 13
Q 18
M 12
Q 20
M 14
Q 22
M 15
WL 9
Q 24
M 3
Q 26
M 2
Q 28
M 4
Q 30
M 5
)"
R"(WL 6
Q 32
ST 20,2304
M 17
Q 34
M 16
Q 36
M 18
Q 38
M 19
WL 1
Q 40
M 7
Q 42
M 6
Q 44
v_mfma_f32_16x16x32_f16 a[52:55], v[88:91], a[0:3], a[32:35]
Q 46
M 9
H 1
AR 6,148
v_mfma_f32_16x16x32_f16 a[76:79], v[92:95], a[12:15], a[32:35]
AR 7,172
AR 8,196
M 20
EF 6,12
EF 7,13
v_mfma_f32_16x16x32_f16 a[80:83], v[96:99], a[8:11], a[32:35]
H 2
EF 8,46
M 23
R 10
v_mfma_f32_16x16x32_f16 a[56:59], v[100:103], a[4:7], a[32:35]
ae 42,72,0
ST 22,128
M 10
WL 2
R 8
PA 12288
DF
v_mfma_f32_16x16x32_f16 a[104:107], v[96:99], a[16:19], a[32:35]
EF 152,47
CV 6,6,7
M 35
CV 7,8,152
AW 232,6
v_mfma_f32_16x16x32_f16 a[100:103], v[88:91], a[16:19], a[32:35]
AW 233,7
AR 6,149
M 33
AR 7,173
AR 8,197
M 30
EF 6,12
EF 7,13
M 31
EF 8,46
EF 153,47
M 28
CV 6,6,7
CV 7,8,153
M 29
AW 234,6
AW 235,7
M 26
AR 6,150
AR 7,174
M 27
WV 11
AR 8,198
EF 6,12
M 24
WL 0
FP
ST 24,896
M 25
EF 7,13
EF 8,46
M 36
EF 154,47
CV 6,6,7
M 37
CV 7,8,154
AW 236,6
M 38
AW 237,7
AR 6,151
M 39
AR 7,175
AR 8,199
M 40
EF 6,12
EF 7,13
M 41
EF 8,46
EF 155,47
M 42
CV 6,6,7
CV 7,8,155
M 43
AW 238,6
AW 239,7
v_mfma_f32_16x16x32_f16 a[124:127], v[88:91], a[24:27], a[32:35]
AR 6,152
AR 7,176
EF 6,12
EF 7,13
EF 132,46
EF 156,47
CV 14,6,7
CV 15,132,156
AR 6,153
AR 7,177
M 45
FR
EF 6,12
EF 7,13
EF 133,46
EF 157,47
CV 16,6,7
CV 17,133,157
AR 6,154
AR 7,178
EF 6,12
v_mfma_f32_16x16x32_f16 a[128:131], v[96:99], a[24:27], a[32:35]
ST 26,1664
EF 7,13
EF 134,46
EF 158,47
CV 18,6,7
CV 19,134,158
AR 6,155
AR 7,179
EF 6,12
EF 7,13
M 47
EF 135,46
EF 159,47
CV 20,6,7
CV 21,135,159
v_mfma_f32_16x16x32_f16 a[152:155], v[96:99], a[0:3], a[32:35]
EF 176,52
EF 200,53
M 59
EF 224,54
EF 248,55
v_mfma_f32_16x16x32_f16 a[148:151], v[88:91], a[0:3], a[32:35]
CV 6,176,200
CV 7,224,248
M 57
AW 240,6
AW 241,7
M 54
EF 177,52
EF 201,53
M 55
WL 0
FQ 88
EF 225,54
M 52
EF 249,55
CV 6,177,201
M 53
CV 7,225,249
AW 242,6
M 50
FZ 88
R 24
M 51
mw 72
R 26
M 48
TY 106,520
TY 110,14344
ST 28,2432
M 49
R 28
M 60
ad 84,336
W 0
M 61
AW 243,7
EF 178,52
M 62
EF 202,53
EF 226,54
M 63
W 1
EF 250,55
M 64
CV 6,178,202
CV 7,226,250
M 65
AW 244,6
AW 245,7
M 66
W 2
EF 179,52
M 67
WL 3
AN 104,106
AN 105,107
v_mfma_f32_16x16x32_f16 a[172:175], v[88:91], a[8:11], a[32:35]
SH 106
SH 107
M 69
mw 75
AN 106,106
AN 107,107
v_mfma_f32_16x16x32_f16 a[176:179], v[96:99], a[8:11], a[32:35]
WL 2
AN 108,110
AN 109,111
M 71
R 30
v_mfma_f32_16x16x32_f16 v[132:135], v[96:99], a[16:19], v[128:131]
SH 110
SH 111
M 83
AN 110,110
AN 111,111
v_mfma_f32_16x16x32_f16 a[196:199], v[88:91], a[16:19], a[32:35]
TY 114,2056
TY 118,15880
M 81
ad 84,342
W 0
M 78
ae 42,72,0
EF 203,53
M 79
s_bfe_u32 s52, s50, 0x10003
v_mad_i32_i24 v50, v49, s52, v48
ST 30,256
M 76
W 1
EF 227,54
M 77
EF 251,55
CV 6,179,203
M 74
CV 7,227,251
AW 246,6
M 75
W 2
AW 247,7
M 72
EF 180,52
EF 204,53
EF 228,54
EF 252,55
M 73
WL 1
R 32
CV 6,180,204
CV 7,228,252
M 84
mw 0
AN 112,114
AN 113,115
AW 248,6
AW 249,7
M 85
SH 114
SH 115
EF 181,52
EF 205,53
M 86
AN 114,114
AN 115,115
EF 229,54
EF 253,55
M 87
WL 2
AN 116,118
AN 117,119
CV 6,181,205
CV 7,229,253
M 88
SH 118
SH 119
AW 250,6
AW 251,7
M 89
AN 118,118
AN 119,119
ST 32,1024
EF 182,52
M 90
TY 122,3592
TY 126,17416
EF 206,53
EF 230,54
M 91
ad 84,384
EF 254,55
CV 6,182,206
v_mfma_f32_16x16x32_f16 v[152:155], v[88:91], a[24:27], v[128:131]
W 0
CV 7,230,254
AW 252,6
AW 253,7
M 93
EF 183,52
EF 207,53
EF 231,54
EF 255,55
v_mfma_f32_16x16x32_f16 v[156:159], v[96:99], a[24:27], v[128:131]
CV 6,183,207
CV 7,231,255
AW 254,6
AW 255,7
M 95
R 34
v_mfma_f32_16x16x32_f16 v[180:183], v[96:99], a[0:3], v[128:131]
W 1
M 107
v_mfma_f32_16x16x32_f16 v[176:179], v[88:91], a[0:3], v[128:131]
M 105
WL 3
AN 120,122
AN 121,123
M 102
SH 122
SH 123
M 103
AN 122,122
AN 123,123
ST 34,1792
M 100
WL 2
AN 124,126
AN 125,127
M 101
SH 126
SH 127
M 98
AN 126,126
AN 127,127
M 99
W 2
M 96
M 97
R 48
M 108
mw 3
M 109
ad 84,390
W 0
M 110
M 111
M 112
W 1
M 113
ST 36,2560
M 114
M 115
W 2
v_mfma_f32_16x16x32_f16 v[200:203], v[88:91], a[8:11], v[128:131]
M 117
v_mfma_f32_16x16x32_f16 v[204:207], v[96:99], a[8:11], v[128:131]
bc 49,5,66,60
M 119
WL 2
R 50
M 120
mh 18
M 121
ad 66,992
H 0
M 133
M 132
TX 58,0
TX 62,13824
M 134
DG 1024
M 135
ae 42,72,96
M 123
s_bfe_u32 s52, s50, 0x10005
v_mad_i32_i24 v50, v49, s52, v48
ST 38,0
M 122
TY 66,512
TY 70,14336
M 124
bc 48,5,64,56
M 125
mh 21
M 137
ad 64,992
H 0
M 136
TX 74,1536
TX 78,15360
M 138
WL 4
Q 0
M 139
Q 2
)"
R"(M 127
Q 4
M 126
TY 82,2048
TY 86,15872
v_mfma_f32_16x16x32_f16 v[224:227], v[88:91], a[16:19], v[128:131]
Q 6
M 129
WL 4
Q 8
v_mfma_f32_16x16x32_f16 v[248:251], v[92:95], a[28:31], v[128:131]
Q 10
ST 40,768
M 140
TX 90,3072
TX 94,16896
v_mfma_f32_16x16x32_f16 v[252:255], v[96:99], a[24:27], v[128:131]
Q 12
M 143
R 54
v_mfma_f32_16x16x32_f16 v[228:231], v[100:103], a[20:23], v[128:131]
Q 14
M 130
TY 98,3584
TY 102,17408
M 0
R 52
M 1
WL 10
Q 16
M 13
Q 18
M 12
Q 20
M 14
Q 22
M 15
WL 8
Q 24
M 3
Q 26
ST 42,1536
M 2
Q 28
M 4
Q 30
M 5
WL 6
Q 32
M 17
Q 34
M 16
Q 36
M 18
Q 38
M 19
WL 1
Q 40
M 7
Q 42
M 6
Q 44
M 8
Q 46
M 9
H 1
M 21
ST 44,2304
M 20
M 22
H 2
M 23
R 58
M 11
M 10
WL 2
R 56
M 34
M 35
M 32
M 33
M 30
M 31
M 28
ae 42,72,96
ST 14,128
M 29
M 26
M 27
WV 10
M 24
WL 0
FP
M 25
M 36
M 37
M 38
M 39
M 40
M 41
M 42
ST 16,896
M 43
M 44
M 45
FR
M 46
M 47
M 58
M 59
M 56
M 57
M 54
M 55
WL 0
FQ 92
M 52
ST 18,1664
M 53
M 50
FZ 92
R 72
M 51
mw 24
R 74
M 48
TY 106,5128
TY 110,18952
M 49
R 76
M 60
ad 84,432
W 0
M 61
M 62
M 63
W 1
M 64
M 65
M 66
W 2
ST 20,2432
M 67
WL 3
AN 104,106
AN 105,107
M 68
SH 106
SH 107
M 69
mw 27
AN 106,106
AN 107,107
M 70
WL 2
AN 108,110
AN 109,111
M 71
R 78
M 82
SH 110
SH 111
M 83
AN 110,110
AN 111,111
M 80
TY 114,6664
TY 118,20488
M 81
ad 84,438
W 0
M 78
M 79
M 76
W 1
M 77
M 74
M 75
W 2
M 72
M 73
R 80
M 84
mw 48
WL 3
AN 112,114
AN 113,115
M 85
SH 114
SH 115
M 86
AN 114,114
AN 115,115
M 87
WL 2
AN 116,118
AN 117,119
M 88
SH 118
SH 119
M 89
AN 118,118
AN 119,119
M 90
TY 122,8200
TY 126,22024
M 91
ad 84,480
M 92
W 0
M 93
M 94
M 95
R 82
M 106
W 1
M 107
M 104
M 105
WL 3
AN 120,122
AN 121,123
M 102
SH 122
SH 123
M 103
AN 122,122
AN 123,123
M 100
WL 2
AN 124,126
AN 125,127
M 101
SH 126
SH 127
M 98
AN 126,126
AN 127,127
M 99
W 2
M 96
M 97
R 0
M 108
mw 51
M 109
ad 84,486
W 0
M 110
M 111
M 112
W 1
M 113
M 114
M 115
W 2
M 116
M 117
M 118
bc 49,6,66,60
M 119
WL 2
R 2
M 120
mh 0
M 121
ad 66,1184
H 0
M 133
M 132
TX 58,4608
TX 62,18432
M 134
DG 1024
M 135
M 123
M 122
TY 66,5120
TY 70,18944
M 124
bc 48,6,64,56
M 125
mh 3
ae 44,70,384
M 137
ad 64,1184
H 0
M 136
TX 74,6144
TX 78,19968
M 138
WL 4
Q 0
M 139
Q 2
M 127
Q 4
M 126
TY 82,6656
TY 86,20480
M 128
Q 6
M 129
WL 4
Q 8
M 141
Q 10
M 140
TX 90,7680
TX 94,21504
M 142
Q 12
M 143
R 6
M 131
Q 14
M 130
TY 98,8192
TY 102,22016
M 0
R 4
M 1
WL 10
Q 16
M 13
Q 18
M 12
Q 20
M 14
Q 22
M 15
WL 8
Q 24
M 3
Q 26
M 2
Q 28
M 4
Q 30
M 5
WL 6
Q 32
M 17
Q 34
M 16
Q 36
M 18
Q 38
M 19
WL 2
Q 40
M 7
Q 42
M 6
Q 44
M 8
Q 46
M 9
H 1
M 21
ae 42,72,0
M 20
s_bfe_u32 s52, s50, 0x10003
v_mad_i32_i24 v50, v49, s52, v48
SA 200,0
M 22
H 2
M 23
R 10
M 11
s_bitcmp1_b32 s50, 6
s_cselect_b32 s51, 24576, 0
M 10
WL 2
R 8
PA 12288
DF
M 34
v_add_u32 v47, s51, v3
XL 22,0
M 35
XL 24,768
M 32
XL 26,1536
M 33
XL 28,2304
M 30
M 31
M 28
M 29
M 26
M 27
WV 11
M 24
WL 0
FP
M 25
M 36
M 37
M 38
)"
R"(M 39
M 40
M 41
M 42
M 43
M 44
M 45
FR
M 46
M 47
M 58
M 59
M 56
M 57
M 54
M 55
WL 0
FQ 96
M 52
M 53
M 50
FZ 96
R 24
M 51
mw 72
R 26
M 48
TY 106,9736
TY 110,23560
M 49
R 28
M 60
ad 84,528
W 0
M 61
M 62
M 63
W 1
M 64
M 65
M 66
W 2
M 67
WL 3
AN 104,106
AN 105,107
M 68
SH 106
SH 107
M 69
mw 75
AN 106,106
AN 107,107
M 70
WL 2
AN 108,110
AN 109,111
M 71
R 30
M 82
SH 110
SH 111
M 83
AN 110,110
AN 111,111
M 80
TY 114,11272
TY 118,25096
M 81
ad 84,534
W 0
M 78
M 79
M 76
W 1
M 77
M 74
M 75
W 2
M 72
M 73
R 32
M 84
mw 0
WL 3
AN 112,114
AN 113,115
M 85
SH 114
SH 115
M 86
AN 114,114
AN 115,115
M 87
WL 2
AN 116,118
AN 117,119
M 88
SH 118
SH 119
M 89
AN 118,118
AN 119,119
M 90
TY 122,12808
TY 126,26632
M 91
ad 84,576
M 92
W 0
SA 202,768
M 93
M 94
M 95
R 34
M 106
W 1
M 107
M 104
M 105
WL 3
AN 120,122
AN 121,123
M 102
SH 122
SH 123
M 103
AN 122,122
AN 123,123
M 100
WL 2
AN 124,126
AN 125,127
M 101
SH 126
SH 127
M 98
AN 126,126
AN 127,127
M 99
W 2
M 96
M 97
R 48
M 108
mw 3
M 109
ad 84,582
W 0
M 110
M 111
M 112
W 1
M 113
M 114
M 115
W 2
M 116
M 117
M 118
bc 49,7,66,60
M 119
WL 2
R 50
M 120
mh 9
M 121
ad 66,1376
H 0
M 133
M 132
TX 58,9216
TX 62,23040
M 134
DG 1024
M 135
M 123
M 122
TY 66,9728
TY 70,23552
M 124
bc 48,7,64,56
M 125
mh 12
M 137
ad 64,1376
H 0
M 136
TX 74,10752
TX 78,24576
M 138
WL 4
Q 0
M 139
Q 2
M 127
Q 4
M 126
TY 82,11264
TY 86,25088
M 128
Q 6
M 129
WL 4
Q 8
M 141
Q 10
M 140
TX 90,12288
TX 94,26112
M 142
Q 12
M 143
R 54
M 131
Q 14
M 130
TY 98,12800
TY 102,26624
M 0
R 52
M 1
WL 10
Q 16
M 13
Q 18
M 12
Q 20
M 14
Q 22
M 15
WL 8
Q 24
M 3
Q 26
M 2
Q 28
M 4
Q 30
M 5
WL 6
Q 32
M 17
Q 34
M 16
Q 36
M 18
Q 38
M 19
WL 2
Q 40
M 7
Q 42
M 6
Q 44
M 8
Q 46
M 9
H 1
SA 204,1536
M 21
ae 44,70,384
M 20
s_bfe_u32 s51, s50, 0x10007
v_mad_i32_i24 v47, v49, s51, v48
XL 30,0
M 22
H 2
XL 32,768
M 23
R 58
M 11
XL 34,1536
M 10
WL 2
R 56
M 34
XL 36,2304
M 35
M 32
M 33
M 30
M 31
M 28
M 29
M 26
M 27
WV 10
M 24
WL 0
FP
M 25
M 36
M 37
M 38
M 39
M 40
UL 6,22
UH 7,22
M 41
AR 8,36
AR 9,60
M 42
AD 8,6
AD 9,7
M 43
AW 36,8
AW 60,9
M 44
UL 6,23
UH 7,23
M 45
FR
AR 8,84
M 46
AR 9,108
AD 8,6
M 47
AD 9,7
AW 84,8
M 58
AW 108,9
UL 6,24
M 59
UH 7,24
AR 8,37
M 56
AR 9,61
AD 8,6
M 57
AD 9,7
AW 37,8
M 54
AW 61,9
UL 6,25
M 55
WL 0
FQ 100
UH 7,25
M 52
AR 8,85
AR 9,109
M 53
AD 8,6
AD 9,7
M 50
FZ 100
R 72
M 51
mw 24
R 74
M 48
TY 106,520
TY 110,14344
M 49
R 76
M 60
ad 84,624
W 0
M 61
AW 85,8
AW 109,9
M 62
UL 6,26
UH 7,26
M 63
W 1
AR 8,38
M 64
AR 9,62
AD 8,6
M 65
AD 9,7
AW 38,8
M 66
W 2
AW 62,9
M 67
WL 3
AN 104,106
AN 105,107
M 68
SH 106
SH 107
M 69
mw 27
AN 106,106
AN 107,107
M 70
WL 2
AN 108,110
AN 109,111
M 71
R 78
M 82
SH 110
SH 111
M 83
AN 110,110
AN 111,111
)"
R"(M 80
TY 114,2056
TY 118,15880
M 81
ad 84,630
W 0
M 78
UL 6,27
UH 7,27
M 79
AR 8,86
AR 9,110
M 76
W 1
AD 8,6
M 77
AD 9,7
AW 86,8
M 74
AW 110,9
UL 6,28
M 75
W 2
SA 206,2304
M 72
UH 7,28
AR 8,39
M 73
WL 1
R 80
M 84
mw 48
AN 112,114
AN 113,115
M 85
SH 114
SH 115
M 86
AN 114,114
AN 115,115
M 87
WL 2
AN 116,118
AN 117,119
M 88
SH 118
SH 119
M 89
AN 118,118
AN 119,119
M 90
TY 122,3592
TY 126,17416
M 91
ad 84,672
M 92
W 0
AR 9,63
M 93
AD 8,6
AD 9,7
M 94
AW 39,8
AW 63,9
M 95
R 82
M 106
W 1
UL 6,29
M 107
UH 7,29
AR 8,87
M 104
AR 9,111
AD 8,6
M 105
WL 3
AN 120,122
AN 121,123
M 102
SH 122
SH 123
M 103
AN 122,122
AN 123,123
M 100
WL 2
AN 124,126
AN 125,127
M 101
SH 126
SH 127
M 98
AN 126,126
AN 127,127
M 99
W 2
AD 9,7
M 96
AW 87,8
AW 111,9
M 97
R 0
M 108
mw 51
M 109
ad 84,678
W 0
M 110
M 111
M 112
W 1
M 113
M 114
M 115
W 2
M 116
M 117
M 118
bc 49,8,66,60
M 119
WL 2
R 2
M 120
mh 18
M 121
ad 66,1568
H 0
M 133
M 132
TX 58,0
TX 62,13824
M 134
DG 1024
M 135
M 123
M 122
TY 66,512
TY 70,14336
M 124
bc 48,8,64,56
M 125
mh 21
M 137
ad 64,1568
H 0
M 136
TX 74,1536
TX 78,15360
M 138
WL 4
Q 0
M 139
Q 2
M 127
Q 4
M 126
TY 82,2048
TY 86,15872
M 128
Q 6
M 129
WL 4
Q 8
M 141
Q 10
M 140
TX 90,3072
TX 94,16896
M 142
Q 12
M 143
R 6
M 131
Q 14
M 130
TY 98,3584
TY 102,17408
M 0
R 4
M 1
WL 10
Q 16
M 13
Q 18
M 12
Q 20
M 14
Q 22
M 15
WL 8
Q 24
M 3
Q 26
M 2
Q 28
M 4
Q 30
M 5
WL 6
Q 32
M 17
Q 34
M 16
Q 36
M 18
Q 38
M 19
WL 2
Q 40
M 7
Q 42
M 6
Q 44
M 8
Q 46
M 9
H 1
M 21
ae 42,72,0
M 20
SA 208,128
M 22
H 2
M 23
R 10
M 11
ae 44,68,384
M 10
WL 2
R 8
PA 12288
DF
M 34
s_bitcmp1_b32 s50, 8
s_cselect_b32 s51, 24576, 0
v_add_u32 v47, s51, v3
M 35
XL 38,0
M 32
XL 40,768
M 33
XL 42,1536
M 30
XL 44,2304
M 31
M 28
M 29
M 26
M 27
WV 11
M 24
WL 0
FP
M 25
M 36
M 37
M 38
M 39
M 40
M 41
M 42
UL 6,30
UH 7,30
M 43
AR 8,40
AR 9,64
M 44
AD 8,6
AD 9,7
M 45
FR
AW 40,8
M 46
AW 64,9
UL 6,31
M 47
UH 7,31
AR 8,88
M 58
AR 9,112
AD 8,6
M 59
AD 9,7
AW 88,8
M 56
AW 112,9
UL 6,32
M 57
UH 7,32
AR 8,41
M 54
AR 9,65
AD 8,6
M 55
WL 0
FQ 104
AD 9,7
M 52
AW 41,8
AW 65,9
M 53
UL 6,33
UH 7,33
M 50
FZ 104
R 24
M 51
mw 72
R 26
M 48
TY 106,5128
TY 110,18952
M 49
R 28
M 60
ad 84,720
W 0
M 61
AR 8,89
AR 9,113
M 62
AD 8,6
AD 9,7
M 63
W 1
AW 89,8
M 64
AW 113,9
UL 6,34
M 65
UH 7,34
AR 8,42
M 66
W 2
AR 9,66
M 67
WL 3
AN 104,106
AN 105,107
M 68
SH 106
SH 107
M 69
mw 75
AN 106,106
AN 107,107
M 70
WL 2
AN 108,110
AN 109,111
M 71
R 30
M 82
SH 110
SH 111
M 83
AN 110,110
AN 111,111
M 80
TY 114,6664
TY 118,20488
M 81
ad 84,726
W 0
M 78
AD 8,6
AD 9,7
M 79
AW 42,8
AW 66,9
M 76
W 1
UL 6,35
M 77
UH 7,35
AR 8,90
M 74
AR 9,114
AD 8,6
M 75
W 2
AD 9,7
M 72
AW 90,8
AW 114,9
M 73
WL 1
R 32
M 84
mw 0
AN 112,114
AN 113,115
M 85
SH 114
SH 115
M 86
AN 114,114
AN 115,115
M 87
)"
R"(WL 2
AN 116,118
AN 117,119
M 88
SH 118
SH 119
M 89
AN 118,118
AN 119,119
M 90
TY 122,8200
TY 126,22024
M 91
ad 84,768
M 92
W 0
SA 210,896
M 93
UL 6,36
UH 7,36
M 94
AR 8,43
AR 9,67
M 95
R 34
M 106
W 1
AD 8,6
M 107
AD 9,7
AW 43,8
M 104
AW 67,9
UL 6,37
M 105
WL 3
AN 120,122
AN 121,123
M 102
SH 122
SH 123
M 103
AN 122,122
AN 123,123
M 100
WL 2
AN 124,126
AN 125,127
M 101
SH 126
SH 127
M 98
AN 126,126
AN 127,127
M 99
W 2
UH 7,37
M 96
AR 8,91
AR 9,115
M 97
R 48
M 108
mw 3
AD 8,6
AD 9,7
M 109
ad 84,774
W 0
M 110
AW 91,8
AW 115,9
M 111
M 112
W 1
M 113
M 114
M 115
W 2
M 116
M 117
M 118
bc 49,9,66,60
M 119
WL 2
R 50
M 120
mh 0
M 121
ad 66,1760
H 0
M 133
M 132
TX 58,4608
TX 62,18432
M 134
DG 1024
M 135
M 123
M 122
TY 66,5120
TY 70,18944
M 124
bc 48,9,64,56
M 125
mh 3
M 137
ad 64,1760
H 0
M 136
TX 74,6144
TX 78,19968
M 138
WL 4
Q 0
M 139
Q 2
M 127
Q 4
M 126
TY 82,6656
TY 86,20480
M 128
Q 6
M 129
WL 4
Q 8
M 141
Q 10
M 140
TX 90,7680
TX 94,21504
M 142
Q 12
M 143
R 54
M 131
Q 14
M 130
TY 98,8192
TY 102,22016
M 0
R 52
M 1
WL 10
Q 16
M 13
Q 18
M 12
Q 20
M 14
Q 22
M 15
WL 8
Q 24
M 3
Q 26
M 2
Q 28
M 4
Q 30
M 5
WL 6
Q 32
M 17
Q 34
M 16
Q 36
M 18
Q 38
M 19
WL 2
Q 40
M 7
Q 42
M 6
Q 44
M 8
Q 46
M 9
H 1
SA 212,1664
M 21
ae 44,68,384
M 20
s_bfe_u32 s51, s50, 0x10009
v_mad_i32_i24 v47, v49, s51, v48
XL 14,0
M 22
H 2
XL 16,768
M 23
R 58
M 11
XL 18,1536
M 10
WL 2
R 56
M 34
XL 20,2304
M 35
M 32
M 33
M 30
M 31
M 28
M 29
M 26
M 27
WV 10
M 24
WL 0
FP
M 25
M 36
M 37
M 38
M 39
M 40
M 41
M 42
M 43
M 44
UL 6,38
UH 7,38
M 45
FR
AR 8,44
M 46
AR 9,68
AD 8,6
M 47
AD 9,7
AW 44,8
M 58
AW 68,9
UL 6,39
M 59
UH 7,39
AR 8,92
M 56
AR 9,116
AD 8,6
M 57
AD 9,7
AW 92,8
M 54
AW 116,9
UL 6,40
M 55
WL 0
FQ 108
UH 7,40
M 52
AR 8,45
AR 9,69
M 53
AD 8,6
AD 9,7
M 50
FZ 108
R 72
M 51
mw 24
R 74
M 48
TY 106,9736
TY 110,23560
M 49
R 76
M 60
ad 84,816
W 0
M 61
AW 45,8
AW 69,9
M 62
UL 6,41
UH 7,41
M 63
W 1
AR 8,93
M 64
AR 9,117
AD 8,6
M 65
AD 9,7
AW 93,8
M 66
W 2
AW 117,9
M 67
WL 3
AN 104,106
AN 105,107
M 68
SH 106
SH 107
M 69
mw 27
AN 106,106
AN 107,107
M 70
WL 2
AN 108,110
AN 109,111
M 71
R 78
M 82
SH 110
SH 111
M 83
AN 110,110
AN 111,111
M 80
TY 114,11272
TY 118,25096
M 81
ad 84,822
W 0
M 78
UL 6,42
UH 7,42
M 79
AR 8,46
AR 9,70
M 76
W 1
AD 8,6
M 77
AD 9,7
AW 46,8
M 74
AW 70,9
UL 6,43
M 75
W 2
SA 214,2432
M 72
UH 7,43
AR 8,94
M 73
WL 1
R 80
M 84
mw 48
AN 112,114
AN 113,115
M 85
SH 114
SH 115
M 86
AN 114,114
AN 115,115
M 87
WL 2
AN 116,118
AN 117,119
M 88
SH 118
SH 119
M 89
AN 118,118
AN 119,119
M 90
TY 122,12808
TY 126,26632
M 91
ad 84,864
M 92
W 0
AR 9,118
M 93
AD 8,6
AD 9,7
M 94
AW 94,8
AW 118,9
M 95
R 82
M 106
W 1
UL 6,44
M 107
UH 7,44
AR 8,47
M 104
AR 9,71
AD 8,6
M 105
WL 3
AN 120,122
AN 121,123
M 102
SH 122
SH 123
M 103
AN 122,122
AN 123,123
M 100
)"
R"(WL 2
AN 124,126
AN 125,127
M 101
SH 126
SH 127
M 98
AN 126,126
AN 127,127
M 99
W 2
AD 9,7
M 96
AW 47,8
AW 71,9
M 97
R 0
M 108
mw 51
UL 6,45
UH 7,45
M 109
ad 84,870
W 0
M 110
AR 8,95
AR 9,119
M 111
AD 8,6
AD 9,7
M 112
W 1
AW 95,8
M 113
AW 119,9
M 114
M 115
W 2
M 116
M 117
M 118
bc 49,10,66,60
M 119
WL 2
R 2
M 120
mh 9
M 121
ad 66,1952
H 0
M 133
M 132
TX 58,9216
TX 62,23040
M 134
DG 1024
M 135
M 123
M 122
TY 66,9728
TY 70,23552
M 124
bc 48,10,64,56
M 125
mh 12
M 137
ad 64,1952
H 0
M 136
TX 74,10752
TX 78,24576
M 138
WL 4
Q 0
M 139
Q 2
M 127
Q 4
M 126
TY 82,11264
TY 86,25088
M 128
Q 6
M 129
WL 4
Q 8
M 141
Q 10
M 140
TX 90,12288
TX 94,26112
M 142
Q 12
M 143
R 6
M 131
Q 14
M 130
TY 98,12800
TY 102,26624
M 0
R 4
M 1
WL 10
Q 16
M 13
Q 18
M 12
Q 20
M 14
Q 22
M 15
WL 8
Q 24
M 3
Q 26
M 2
Q 28
M 4
Q 30
M 5
WL 6
Q 32
M 17
Q 34
M 16
Q 36
M 18
Q 38
M 19
WL 2
Q 40
M 7
Q 42
M 6
Q 44
M 8
Q 46
M 9
H 1
M 21
ae 42,72,0
M 20
s_bitcmp1_b32 s50, 2
s_cselect_b32 s52, 24576, 0
v_add_u32 v50, s52, v3
M 22
H 2
SA 216,256
M 23
R 10
M 11
ae 44,68,480
M 10
WL 2
R 8
PA 12288
DF
M 34
s_bitcmp1_b32 s50, 10
s_cselect_b32 s51, 24576, 0
v_add_u32 v47, s51, v3
M 35
XL 22,0
M 32
XL 24,768
M 33
XL 26,1536
M 30
XL 28,2304
M 31
M 28
M 29
M 26
M 27
WV 11
M 24
WL 0
FP
M 25
M 36
M 37
M 38
M 39
M 40
M 41
M 42
M 43
M 44
M 45
FR
M 46
UL 6,14
UH 7,14
M 47
AR 8,48
AR 9,72
M 58
AD 8,6
AD 9,7
M 59
AW 48,8
AW 72,9
M 56
UL 6,15
UH 7,15
M 57
AR 8,96
AR 9,120
M 54
AD 8,6
AD 9,7
M 55
WL 0
FQ 112
AW 96,8
M 52
AW 120,9
UL 6,16
M 53
UH 7,16
AR 8,49
M 50
FZ 112
R 24
M 51
mw 72
R 26
M 48
TY 106,520
TY 110,14344
M 49
R 28
M 60
ad 84,912
W 0
M 61
AR 9,73
AD 8,6
M 62
AD 9,7
AW 49,8
M 63
W 1
AW 73,9
M 64
UL 6,17
UH 7,17
M 65
AR 8,97
AR 9,121
M 66
W 2
AD 8,6
M 67
WL 3
AN 104,106
AN 105,107
M 68
SH 106
SH 107
M 69
mw 75
AN 106,106
AN 107,107
M 70
WL 2
AN 108,110
AN 109,111
M 71
R 30
M 82
SH 110
SH 111
M 83
AN 110,110
AN 111,111
M 80
TY 114,2056
TY 118,15880
M 81
ad 84,918
W 0
M 78
AD 9,7
AW 97,8
M 79
AW 121,9
UL 6,18
M 76
W 1
UH 7,18
M 77
AR 8,50
AR 9,74
M 74
AD 8,6
AD 9,7
M 75
W 2
AW 50,8
M 72
AW 74,9
UL 6,19
M 73
WL 1
R 32
M 84
mw 0
AN 112,114
AN 113,115
M 85
SH 114
SH 115
M 86
AN 114,114
AN 115,115
M 87
WL 2
AN 116,118
AN 117,119
M 88
SH 118
SH 119
M 89
AN 118,118
AN 119,119
M 90
TY 122,3592
TY 126,17416
M 91
ad 84,960
M 92
W 0
SA 218,1024
M 93
UH 7,19
AR 8,98
M 94
AR 9,122
AD 8,6
M 95
R 34
M 106
W 1
AD 9,7
M 107
AW 98,8
AW 122,9
M 104
UL 6,20
UH 7,20
M 105
WL 3
AN 120,122
AN 121,123
M 102
SH 122
SH 123
M 103
AN 122,122
AN 123,123
M 100
WL 2
AN 124,126
AN 125,127
M 101
SH 126
SH 127
M 98
AN 126,126
AN 127,127
M 99
W 2
AR 8,51
M 96
AR 9,75
AD 8,6
M 97
R 48
M 108
mw 3
AD 9,7
AW 51,8
M 109
ad 84,966
W 0
M 110
AW 75,9
UL 6,21
M 111
UH 7,21
AR 8,99
M 112
W 1
AR 9,123
M 113
AD 8,6
AD 9,7
M 114
)"
R"(AW 99,8
AW 123,9
M 115
W 2
M 116
M 117
M 118
bc 49,11,66,60
M 119
WL 2
R 50
M 120
mh 18
M 121
ad 66,2144
H 0
M 133
M 132
TX 58,0
TX 62,13824
M 134
DG 1024
M 135
M 123
M 122
TY 66,512
TY 70,14336
M 124
bc 48,11,64,56
M 125
mh 21
M 137
ad 64,2144
H 0
M 136
TX 74,1536
TX 78,15360
M 138
WL 4
Q 0
M 139
Q 2
M 127
Q 4
M 126
TY 82,2048
TY 86,15872
M 128
Q 6
M 129
WL 4
Q 8
M 141
Q 10
M 140
TX 90,3072
TX 94,16896
M 142
Q 12
M 143
R 54
M 131
Q 14
M 130
TY 98,3584
TY 102,17408
M 0
R 52
M 1
WL 10
Q 16
M 13
Q 18
M 12
Q 20
M 14
Q 22
M 15
WL 8
Q 24
M 3
Q 26
M 2
Q 28
M 4
Q 30
M 5
WL 6
Q 32
M 17
Q 34
M 16
Q 36
M 18
Q 38
M 19
WL 2
Q 40
M 7
Q 42
M 6
Q 44
M 8
Q 46
M 9
H 1
SA 220,1792
M 21
ae 44,68,480
M 20
s_bfe_u32 s51, s50, 0x1000b
v_mad_i32_i24 v47, v49, s51, v48
XL 30,0
M 22
H 2
XL 32,768
M 23
R 58
M 11
XL 34,1536
M 10
WL 2
R 56
M 34
XL 36,2304
M 35
M 32
M 33
M 30
M 31
M 28
M 29
M 26
M 27
WV 10
M 24
WL 0
FP
M 25
M 36
M 37
M 38
M 39
M 40
M 41
M 42
M 43
M 44
M 45
FR
M 46
M 47
M 58
UL 6,22
UH 7,22
M 59
AR 8,52
AR 9,76
M 56
AD 8,6
AD 9,7
M 57
AW 52,8
AW 76,9
M 54
UL 6,23
UH 7,23
M 55
WL 0
FQ 116
AR 8,100
M 52
AR 9,124
AD 8,6
M 53
AD 9,7
AW 100,8
M 50
FZ 116
R 72
M 51
mw 24
R 74
M 48
TY 106,5128
TY 110,18952
M 49
R 76
M 60
ad 84,1008
W 0
M 61
AW 124,9
UL 6,24
M 62
UH 7,24
AR 8,53
M 63
W 1
AR 9,77
M 64
AD 8,6
AD 9,7
M 65
AW 53,8
AW 77,9
M 66
W 2
UL 6,25
M 67
WL 3
AN 104,106
AN 105,107
M 68
SH 106
SH 107
M 69
mw 27
AN 106,106
AN 107,107
M 70
WL 2
AN 108,110
AN 109,111
M 71
R 78
M 82
SH 110
SH 111
M 83
AN 110,110
AN 111,111
M 80
TY 114,6664
TY 118,20488
M 81
ad 84,1014
W 0
M 78
UH 7,25
AR 8,101
M 79
AR 9,125
AD 8,6
M 76
W 1
AD 9,7
M 77
AW 101,8
AW 125,9
M 74
UL 6,26
UH 7,26
M 75
W 2
SA 222,2560
M 72
AR 8,54
AR 9,78
M 73
WL 1
R 80
M 84
mw 48
AN 112,114
AN 113,115
M 85
SH 114
SH 115
M 86
AN 114,114
AN 115,115
M 87
WL 2
AN 116,118
AN 117,119
M 88
SH 118
SH 119
M 89
AN 118,118
AN 119,119
M 90
TY 122,8200
TY 126,22024
M 91
ad 84,1056
M 92
W 0
AD 8,6
M 93
AD 9,7
AW 54,8
M 94
AW 78,9
UL 6,27
M 95
R 82
M 106
W 1
UH 7,27
M 107
AR 8,102
AR 9,126
M 104
AD 8,6
AD 9,7
M 105
WL 3
AN 120,122
AN 121,123
M 102
SH 122
SH 123
M 103
AN 122,122
AN 123,123
M 100
WL 2
AN 124,126
AN 125,127
M 101
SH 126
SH 127
M 98
AN 126,126
AN 127,127
M 99
W 2
AW 102,8
M 96
AW 126,9
UL 6,28
M 97
R 0
M 108
mw 51
UH 7,28
AR 8,55
M 109
ad 84,1062
W 0
M 110
AR 9,79
AD 8,6
M 111
AD 9,7
AW 55,8
M 112
W 1
AW 79,9
M 113
UL 6,29
UH 7,29
M 114
AR 8,103
AR 9,127
M 115
W 2
AD 8,6
M 116
AD 9,7
AW 103,8
M 117
AW 127,9
M 118
bc 49,12,66,60
M 119
WL 1
R 2
M 120
mh 0
M 121
ad 66,2336
H 0
M 133
M 132
TX 58,4608
TX 62,18432
M 134
DG 1024
M 135
M 123
M 122
TY 66,5120
TY 70,18944
M 124
bc 48,12,64,56
M 125
mh 3
M 137
ad 64,2336
H 0
M 136
TX 74,6144
TX 78,19968
M 138
WL 4
Q 0
M 139
)"
R"(Q 2
M 127
Q 4
M 126
TY 82,6656
TY 86,20480
M 128
Q 6
M 129
WL 4
Q 8
M 141
Q 10
M 140
TX 90,7680
TX 94,21504
M 142
Q 12
M 143
R 6
M 131
Q 14
M 130
TY 98,8192
TY 102,22016
M 0
R 4
M 1
WL 10
Q 16
M 13
Q 18
M 12
Q 20
M 14
Q 22
M 15
WL 8
Q 24
M 3
Q 26
M 2
Q 28
M 4
Q 30
M 5
WL 6
Q 32
M 17
Q 34
M 16
Q 36
M 18
Q 38
M 19
WL 2
Q 40
M 7
Q 42
M 6
Q 44
M 8
Q 46
M 9
H 1
M 21
ae 42,72,96
M 20
s_bitcmp1_b32 s50, 4
s_cselect_b32 s52, 24576, 0
v_add_u32 v50, s52, v3
M 22
H 2
SA 224,0
M 23
R 10
M 11
M 10
WL 2
R 8
PA 12288
DF
M 34
M 35
M 32
M 33
M 30
M 31
M 28
M 29
M 26
ae 44,70,384
M 27
WV 7
M 24
WL 0
FP
M 25
M 36
s_bitcmp1_b32 s50, 6
s_cselect_b32 s51, 24576, 0
v_add_u32 v47, s51, v3
M 37
XL 38,128
M 38
XL 40,896
M 39
M 40
M 41
M 42
M 43
M 44
XL 42,1664
M 45
FR
M 46
M 47
M 58
M 59
M 56
XL 44,2432
UL 6,30
M 57
UH 7,30
AR 8,56
M 54
AR 9,80
AD 8,6
M 55
WL 0
FQ 120
AD 9,7
M 52
AW 56,8
AW 80,9
M 53
UL 6,31
UH 7,31
M 50
FZ 120
R 24
M 51
mw 72
R 26
M 48
TY 106,9736
TY 110,23560
M 49
R 28
M 60
ad 84,1104
W 0
M 61
AR 8,104
AR 9,128
M 62
AD 8,6
AD 9,7
M 63
W 1
AW 104,8
M 64
AW 128,9
UL 6,32
M 65
UH 7,32
AR 8,57
M 66
W 2
AR 9,81
M 67
WL 3
AN 104,106
AN 105,107
M 68
SH 106
SH 107
M 69
mw 75
AN 106,106
AN 107,107
M 70
WL 2
AN 108,110
AN 109,111
M 71
R 30
M 82
SH 110
SH 111
M 83
AN 110,110
AN 111,111
M 80
TY 114,11272
TY 118,25096
M 81
ad 84,1110
W 0
M 78
AD 8,6
AD 9,7
M 79
AW 57,8
AW 81,9
M 76
W 1
UL 6,33
M 77
UH 7,33
AR 8,105
M 74
AR 9,129
AD 8,6
M 75
W 2
AD 9,7
M 72
AW 105,8
AW 129,9
M 73
WL 1
R 32
M 84
mw 0
AN 112,114
AN 113,115
M 85
SH 114
SH 115
M 86
AN 114,114
AN 115,115
M 87
WL 2
AN 116,118
AN 117,119
M 88
SH 118
SH 119
M 89
AN 118,118
AN 119,119
M 90
TY 122,12808
TY 126,26632
M 91
ad 84,1152
M 92
W 0
SA 226,768
M 93
UL 6,34
UH 7,34
M 94
AR 8,58
AR 9,82
M 95
R 34
M 106
W 1
AD 8,6
M 107
AD 9,7
AW 58,8
M 104
AW 82,9
UL 6,35
M 105
WL 3
AN 120,122
AN 121,123
M 102
SH 122
SH 123
M 103
AN 122,122
AN 123,123
M 100
WL 2
AN 124,126
AN 125,127
M 101
SH 126
SH 127
M 98
AN 126,126
AN 127,127
M 99
W 2
UH 7,35
M 96
AR 8,106
AR 9,130
M 97
R 48
M 108
mw 3
AD 8,6
AD 9,7
M 109
ad 84,1158
W 0
M 110
AW 106,8
AW 130,9
M 111
UL 6,36
UH 7,36
M 112
W 1
AR 8,59
M 113
AR 9,83
AD 8,6
M 114
AD 9,7
AW 59,8
M 115
W 2
AW 83,9
M 116
UL 6,37
UH 7,37
M 117
AR 8,107
AR 9,131
M 118
bc 49,13,66,60
AD 8,6
M 119
WL 0
R 50
M 120
mh 9
AD 9,7
AW 107,8
M 121
ad 66,2528
H 0
M 133
AW 131,9
M 132
TX 58,9216
TX 62,23040
M 134
DG 1024
M 135
M 123
M 122
TY 66,9728
TY 70,23552
M 124
bc 48,13,64,56
M 125
mh 12
M 137
ad 64,2528
H 0
M 136
TX 74,10752
TX 78,24576
M 138
WL 4
Q 0
M 139
Q 2
M 127
Q 4
M 126
TY 82,11264
TY 86,25088
M 128
Q 6
M 129
WL 4
Q 8
M 141
Q 10
M 140
TX 90,12288
TX 94,26112
M 142
Q 12
M 143
R 54
M 131
Q 14
M 130
TY 98,12800
TY 102,26624
M 0
R 52
M 1
WL 10
Q 16
M 13
Q 18
M 12
Q 20
)"
R"(M 14
Q 22
M 15
WL 8
Q 24
M 3
Q 26
M 2
Q 28
M 4
Q 30
M 5
WL 6
Q 32
M 17
Q 34
M 16
Q 36
M 18
Q 38
M 19
WL 2
Q 40
M 7
Q 42
M 6
Q 44
M 8
Q 46
M 9
H 1
SA 228,1536
M 21
M 20
M 22
H 2
M 23
R 58
M 11
M 10
WL 2
R 56
M 34
M 35
M 32
M 33
M 30
M 31
M 28
M 29
M 26
M 27
WV 6
M 24
WL 0
FP
M 25
M 36
ae 44,70,384
M 37
s_bfe_u32 s51, s50, 0x10007
v_mad_i32_i24 v47, v49, s51, v48
XL 14,128
M 38
M 39
M 40
XL 16,896
M 41
M 42
M 43
M 44
M 45
FR
M 46
XL 18,1664
M 47
M 58
M 59
M 56
M 57
M 54
XL 20,2432
M 55
WL 0
FQ 124
M 52
M 53
M 50
FZ 124
R 72
M 51
mw 24
R 74
M 48
TY 106,520
TY 110,14344
M 49
R 76
M 60
ad 84,1200
W 0
M 61
M 62
M 63
W 1
M 64
M 65
M 66
W 2
M 67
WL 3
AN 104,106
AN 105,107
M 68
SH 106
SH 107
M 69
mw 27
AN 106,106
AN 107,107
M 70
WL 2
AN 108,110
AN 109,111
M 71
R 78
M 82
SH 110
SH 111
M 83
AN 110,110
AN 111,111
M 80
TY 114,2056
TY 118,15880
M 81
ad 84,1206
W 0
M 78
M 79
M 76
W 1
M 77
M 74
M 75
W 2
SA 230,2304
M 72
M 73
R 80
M 84
mw 48
WL 3
AN 112,114
AN 113,115
M 85
SH 114
SH 115
M 86
AN 114,114
AN 115,115
M 87
WL 2
AN 116,118
AN 117,119
M 88
SH 118
SH 119
M 89
AN 118,118
AN 119,119
M 90
TY 122,3592
TY 126,17416
M 91
ad 84,1248
M 92
W 0
UL 6,38
M 93
UH 7,38
AR 8,132
M 94
AR 9,156
AD 8,6
M 95
R 82
M 106
W 1
AD 9,7
M 107
AW 132,8
AW 156,9
M 104
UL 6,39
UH 7,39
M 105
WL 3
AN 120,122
AN 121,123
M 102
SH 122
SH 123
M 103
AN 122,122
AN 123,123
M 100
WL 2
AN 124,126
AN 125,127
M 101
SH 126
SH 127
M 98
AN 126,126
AN 127,127
M 99
W 2
AR 8,180
M 96
AD 8,6
AD 136,7
M 97
R 0
M 108
mw 51
AW 180,8
UL 6,40
M 109
ad 84,1254
W 0
M 110
UH 7,40
AR 8,133
M 111
AR 9,157
AD 8,6
M 112
W 1
AD 9,7
M 113
AW 133,8
AW 157,9
M 114
UL 6,41
UH 7,41
M 115
W 2
AR 8,181
M 116
AD 8,6
AD 137,7
M 117
AW 181,8
UL 6,42
M 118
bc 49,14,66,60
UH 7,42
M 119
WL 0
R 2
M 120
mh 18
AR 8,134
AR 9,158
M 121
ad 66,2720
H 0
M 133
AD 8,6
AD 9,7
M 132
TX 58,0
TX 62,13824
M 134
DG 1024
M 135
AW 134,8
AW 158,9
M 123
UL 6,43
UH 7,43
M 122
TY 66,512
TY 70,14336
M 124
bc 48,14,64,56
AR 8,182
M 125
mh 21
AD 8,6
AD 138,7
M 137
ad 64,2720
H 0
M 136
TX 74,1536
TX 78,15360
M 138
WL 4
Q 0
M 139
Q 2
M 127
Q 4
M 126
TY 82,2048
TY 86,15872
M 128
Q 6
M 129
WL 4
Q 8
M 141
Q 10
M 140
TX 90,3072
TX 94,16896
M 142
Q 12
M 143
R 6
M 131
Q 14
M 130
TY 98,3584
TY 102,17408
M 0
R 4
M 1
WL 10
Q 16
M 13
Q 18
M 12
Q 20
M 14
Q 22
M 15
WL 8
Q 24
M 3
Q 26
M 2
Q 28
M 4
Q 30
M 5
WL 6
Q 32
M 17
Q 34
M 16
Q 36
M 18
Q 38
M 19
WL 2
Q 40
M 7
Q 42
M 6
Q 44
M 8
Q 46
M 9
H 1
AW 182,8
M 21
ae 42,72,96
M 20
SA 232,128
UL 6,44
M 22
H 2
UH 7,44
M 23
R 10
M 11
AR 8,135
AR 9,159
M 10
WL 2
R 8
PA 12288
DF
M 34
AD 8,6
AD 9,7
M 35
AW 135,8
AW 159,9
M 32
UL 6,45
UH 7,45
M 33
AR 8,183
AD 8,6
M 30
AD 139,7
)"
R"(AW 183,8
M 31
M 28
M 29
M 26
M 27
WV 7
M 24
WL 0
FP
M 25
M 36
ae 44,68,384
M 37
s_bitcmp1_b32 s50, 8
s_cselect_b32 s51, 24576, 0
v_add_u32 v47, s51, v3
M 38
XL 22,128
M 39
M 40
M 41
M 42
XL 24,896
M 43
M 44
M 45
FR
M 46
M 47
M 58
XL 26,1664
M 59
M 56
M 57
M 54
M 55
WL 0
FQ 128
M 52
XL 28,2432
M 53
M 50
FZ 128
R 24
M 51
mw 72
R 26
M 48
TY 106,5128
TY 110,18952
M 49
R 28
M 60
ad 84,1296
W 0
M 61
M 62
M 63
W 1
M 64
M 65
M 66
W 2
M 67
WL 3
AN 104,106
AN 105,107
M 68
SH 106
SH 107
M 69
mw 75
AN 106,106
AN 107,107
M 70
WL 2
AN 108,110
AN 109,111
M 71
R 30
M 82
SH 110
SH 111
M 83
AN 110,110
AN 111,111
M 80
TY 114,6664
TY 118,20488
M 81
ad 84,1302
W 0
M 78
M 79
M 76
W 1
M 77
M 74
M 75
W 2
M 72
M 73
R 32
M 84
mw 0
WL 3
AN 112,114
AN 113,115
M 85
SH 114
SH 115
M 86
AN 114,114
AN 115,115
M 87
WL 2
AN 116,118
AN 117,119
M 88
SH 118
SH 119
M 89
AN 118,118
AN 119,119
M 90
TY 122,8200
TY 126,22024
M 91
ad 84,1344
M 92
W 0
SA 234,896
M 93
UL 6,14
UH 7,14
M 94
AR 8,136
AR 9,160
M 95
R 34
M 106
W 1
AD 8,6
M 107
AD 9,7
AW 136,8
M 104
AW 160,9
UL 6,15
M 105
WL 3
AN 120,122
AN 121,123
M 102
SH 122
SH 123
M 103
AN 122,122
AN 123,123
M 100
WL 2
AN 124,126
AN 125,127
M 101
SH 126
SH 127
M 98
AN 126,126
AN 127,127
M 99
W 2
UH 7,15
M 96
AR 8,184
AD 8,6
M 97
R 48
M 108
mw 3
AD 140,7
AW 184,8
M 109
ad 84,1350
W 0
M 110
UL 6,16
UH 7,16
M 111
AR 8,137
AR 9,161
M 112
W 1
AD 8,6
M 113
AD 9,7
AW 137,8
M 114
AW 161,9
UL 6,17
M 115
W 2
UH 7,17
M 116
AR 8,185
AD 8,6
M 117
AD 141,7
AW 185,8
M 118
bc 49,15,66,60
UL 6,18
M 119
WL 0
R 50
M 120
mh 0
UH 7,18
AR 8,138
M 121
ad 66,2912
H 0
M 133
AR 9,162
AD 8,6
M 132
TX 58,4608
TX 62,18432
M 134
DG 1024
M 135
AD 9,7
AW 138,8
M 123
AW 162,9
UL 6,19
M 122
TY 66,5120
TY 70,18944
M 124
bc 48,15,64,56
UH 7,19
M 125
mh 3
AR 8,186
AD 8,6
M 137
ad 64,2912
H 0
M 136
TX 74,6144
TX 78,19968
M 138
WL 4
Q 0
M 139
Q 2
M 127
Q 4
M 126
TY 82,6656
TY 86,20480
M 128
Q 6
M 129
WL 4
Q 8
M 141
Q 10
M 140
TX 90,7680
TX 94,21504
M 142
Q 12
M 143
R 54
M 131
Q 14
M 130
TY 98,8192
TY 102,22016
M 0
R 52
M 1
WL 10
Q 16
M 13
Q 18
M 12
Q 20
M 14
Q 22
M 15
WL 8
Q 24
M 3
Q 26
M 2
Q 28
M 4
Q 30
M 5
WL 6
Q 32
M 17
Q 34
M 16
Q 36
M 18
Q 38
M 19
WL 2
Q 40
M 7
Q 42
M 6
Q 44
M 8
Q 46
M 9
H 1
SA 236,1664
M 21
AD 142,7
AW 186,8
M 20
UL 6,20
UH 7,20
M 22
H 2
AR 8,139
M 23
R 58
M 11
AR 9,163
AD 8,6
M 10
WL 2
R 56
M 34
AD 9,7
AW 139,8
M 35
AW 163,9
UL 6,21
M 32
UH 7,21
AR 8,187
M 33
AD 8,6
AD 143,7
M 30
AW 187,8
M 31
M 28
M 29
M 26
M 27
WV 6
M 24
WL 0
FP
M 25
M 36
M 37
M 38
ae 44,68,384
M 39
s_bfe_u32 s51, s50, 0x10009
v_mad_i32_i24 v47, v49, s51, v48
XL 30,128
M 40
M 41
M 42
M 43
M 44
XL 32,896
M 45
FR
M 46
M 47
M 58
M 59
M 56
XL 34,1664
M 57
M 54
M 55
WL 0
FQ 132
M 52
M 53
M 50
FZ 132
R 72
M 51
mw 24
R 74
M 48
TY 106,9736
)"
R"(TY 110,23560
M 49
R 76
M 60
ad 84,1392
W 0
M 61
XL 36,2432
M 62
M 63
W 1
M 64
M 65
M 66
W 2
M 67
WL 3
AN 104,106
AN 105,107
M 68
SH 106
SH 107
M 69
mw 27
AN 106,106
AN 107,107
M 70
WL 2
AN 108,110
AN 109,111
M 71
R 78
M 82
SH 110
SH 111
M 83
AN 110,110
AN 111,111
M 80
TY 114,11272
TY 118,25096
M 81
ad 84,1398
W 0
M 78
M 79
M 76
W 1
M 77
M 74
M 75
W 2
SA 238,2432
M 72
M 73
R 80
M 84
mw 48
WL 3
AN 112,114
AN 113,115
M 85
SH 114
SH 115
M 86
AN 114,114
AN 115,115
M 87
WL 2
AN 116,118
AN 117,119
M 88
SH 118
SH 119
M 89
AN 118,118
AN 119,119
M 90
TY 122,12808
TY 126,26632
M 91
ad 84,1440
M 92
W 0
UL 6,22
M 93
UH 7,22
AR 8,140
M 94
AR 9,164
AD 8,6
M 95
R 82
M 106
W 1
AD 9,7
M 107
AW 140,8
AW 164,9
M 104
UL 6,23
UH 7,23
M 105
WL 3
AN 120,122
AN 121,123
M 102
SH 122
SH 123
M 103
AN 122,122
AN 123,123
M 100
WL 2
AN 124,126
AN 125,127
M 101
SH 126
SH 127
M 98
AN 126,126
AN 127,127
M 99
W 2
AR 8,188
M 96
AD 8,6
AD 144,7
M 97
R 0
M 108
mw 51
AW 188,8
UL 6,24
M 109
ad 84,1446
W 0
M 110
UH 7,24
AR 8,141
M 111
AR 9,165
AD 8,6
M 112
W 1
AD 9,7
M 113
AW 141,8
AW 165,9
M 114
UL 6,25
UH 7,25
M 115
W 2
AR 8,189
M 116
AD 8,6
AD 145,7
M 117
AW 189,8
UL 6,26
M 118
bc 49,16,66,60
UH 7,26
M 119
WL 0
R 2
M 120
mh 9
AR 8,142
AR 9,166
M 121
ad 66,3104
H 0
M 133
AD 8,6
AD 9,7
M 132
TX 58,9216
TX 62,23040
M 134
DG 1024
M 135
AW 142,8
AW 166,9
M 123
UL 6,27
UH 7,27
M 122
TY 66,9728
TY 70,23552
M 124
bc 48,16,64,56
AR 8,190
M 125
mh 12
AD 8,6
AD 146,7
M 137
ad 64,3104
H 0
M 136
TX 74,10752
TX 78,24576
M 138
WL 4
Q 0
M 139
Q 2
M 127
Q 4
M 126
TY 82,11264
TY 86,25088
M 128
Q 6
M 129
WL 4
Q 8
M 141
Q 10
M 140
TX 90,12288
TX 94,26112
M 142
Q 12
M 143
R 6
M 131
Q 14
M 130
TY 98,12800
TY 102,26624
M 0
R 4
M 1
WL 10
Q 16
M 13
Q 18
M 12
Q 20
M 14
Q 22
M 15
WL 8
Q 24
M 3
Q 26
M 2
Q 28
M 4
Q 30
M 5
WL 6
Q 32
M 17
Q 34
M 16
Q 36
M 18
Q 38
M 19
WL 2
Q 40
M 7
Q 42
M 6
Q 44
M 8
Q 46
M 9
H 1
AW 190,8
M 21
ae 42,72,96
M 20
SA 240,256
UL 6,28
M 22
H 2
UH 7,28
M 23
R 10
M 11
AR 8,143
AR 9,167
M 10
WL 2
R 8
PA 12288
DF
M 34
AD 8,6
AD 9,7
M 35
AW 143,8
AW 167,9
M 32
UL 6,29
UH 7,29
M 33
AR 8,191
AD 8,6
M 30
AD 147,7
AW 191,8
M 31
M 28
M 29
M 26
M 27
WV 7
M 24
WL 0
FP
M 25
M 36
M 37
M 38
M 39
M 40
ae 44,68,480
M 41
s_bitcmp1_b32 s50, 10
s_cselect_b32 s51, 24576, 0
v_add_u32 v47, s51, v3
M 42
XL 38,128
M 43
M 44
M 45
FR
M 46
XL 40,896
M 47
M 58
M 59
M 56
M 57
M 54
XL 42,1664
M 55
WL 0
FQ 136
M 52
M 53
M 50
FZ 136
R 24
M 51
mw 72
R 26
M 48
TY 106,520
TY 110,14344
M 49
R 28
M 60
ad 84,1488
W 0
M 61
XL 44,2432
M 62
M 63
W 1
M 64
M 65
M 66
W 2
M 67
WL 3
AN 104,106
AN 105,107
M 68
SH 106
SH 107
M 69
mw 75
AN 106,106
AN 107,107
M 70
WL 2
AN 108,110
AN 109,111
M 71
R 30
M 82
SH 110
SH 111
M 83
AN 110,110
AN 111,111
M 80
TY 114,2056
TY 118,15880
M 81
ad 84,1494
W 0
M 78
)"
R"(M 79
M 76
W 1
M 77
M 74
M 75
W 2
M 72
M 73
R 32
M 84
mw 0
WL 3
AN 112,114
AN 113,115
M 85
SH 114
SH 115
M 86
AN 114,114
AN 115,115
M 87
WL 2
AN 116,118
AN 117,119
M 88
SH 118
SH 119
M 89
AN 118,118
AN 119,119
M 90
TY 122,3592
TY 126,17416
M 91
ad 84,1536
M 92
W 0
SA 242,1024
M 93
M 94
UL 6,30
UH 7,30
M 95
R 34
M 106
W 1
AR 8,144
M 107
AR 9,168
AD 8,6
M 104
AD 9,7
AW 144,8
M 105
WL 3
AN 120,122
AN 121,123
M 102
SH 122
SH 123
M 103
AN 122,122
AN 123,123
M 100
WL 2
AN 124,126
AN 125,127
M 101
SH 126
SH 127
M 98
AN 126,126
AN 127,127
M 99
W 2
AW 168,9
M 96
UL 6,31
UH 7,31
M 97
R 48
M 108
mw 3
AR 8,192
AD 8,6
M 109
ad 84,1542
W 0
M 110
AD 148,7
AW 192,8
M 111
UL 6,32
UH 7,32
M 112
W 1
AR 8,145
M 113
AR 9,169
AD 8,6
M 114
AD 9,7
AW 145,8
M 115
W 2
AW 169,9
M 116
UL 6,33
UH 7,33
M 117
AR 8,193
AD 8,6
M 118
bc 49,17,66,60
AD 149,7
M 119
WL 0
R 50
M 120
mh 18
AW 193,8
UL 6,34
M 121
ad 66,3296
H 0
M 133
UH 7,34
AR 8,146
M 132
TX 58,0
TX 62,13824
M 134
DG 1024
M 135
AR 9,170
AD 8,6
M 123
AD 9,7
AW 146,8
M 122
TY 66,512
TY 70,14336
M 124
bc 48,17,64,56
AW 170,9
M 125
mh 21
UL 6,35
UH 7,35
M 137
ad 64,3296
H 0
M 136
TX 74,1536
TX 78,15360
M 138
WL 4
Q 0
M 139
Q 2
M 127
Q 4
M 126
TY 82,2048
TY 86,15872
M 128
Q 6
M 129
WL 4
Q 8
M 141
Q 10
M 140
TX 90,3072
TX 94,16896
M 142
Q 12
M 143
R 54
M 131
Q 14
M 130
TY 98,3584
TY 102,17408
M 0
R 52
M 1
WL 10
Q 16
M 13
Q 18
M 12
Q 20
M 14
Q 22
M 15
WL 8
Q 24
M 3
Q 26
M 2
Q 28
M 4
Q 30
M 5
WL 6
Q 32
M 17
Q 34
M 16
Q 36
M 18
Q 38
M 19
WL 2
Q 40
M 7
Q 42
M 6
Q 44
M 8
Q 46
M 9
H 1
SA 244,1792
M 21
AR 8,194
AD 8,6
M 20
AD 150,7
AW 194,8
M 22
H 2
UL 6,36
M 23
R 58
M 11
UH 7,36
AR 8,147
M 10
WL 2
R 56
M 34
AR 9,171
AD 8,6
M 35
AD 9,7
AW 147,8
M 32
AW 171,9
UL 6,37
M 33
UH 7,37
AR 8,195
M 30
AD 8,6
AD 151,7
M 31
AW 195,8
M 28
M 29
M 26
M 27
WV 6
M 24
WL 0
FP
M 25
M 36
M 37
M 38
M 39
M 40
M 41
M 42
ae 44,68,480
M 43
s_bfe_u32 s51, s50, 0x1000b
v_mad_i32_i24 v47, v49, s51, v48
XL 14,128
M 44
M 45
FR
M 46
M 47
M 58
XL 16,896
M 59
M 56
M 57
M 54
M 55
WL 0
FQ 140
M 52
XL 18,1664
M 53
M 50
FZ 140
R 72
M 51
mw 24
R 74
M 48
TY 106,5128
TY 110,18952
M 49
R 76
M 60
ad 84,1584
W 0
M 61
XL 20,2432
M 62
M 63
W 1
M 64
M 65
M 66
W 2
M 67
WL 3
AN 104,106
AN 105,107
M 68
SH 106
SH 107
M 69
mw 27
AN 106,106
AN 107,107
M 70
WL 2
AN 108,110
AN 109,111
M 71
R 78
M 82
SH 110
SH 111
M 83
AN 110,110
AN 111,111
M 80
TY 114,6664
TY 118,20488
M 81
ad 84,1590
W 0
M 78
M 79
M 76
W 1
M 77
M 74
M 75
W 2
SA 246,2560
M 72
M 73
R 80
M 84
mw 48
WL 3
AN 112,114
AN 113,115
M 85
SH 114
SH 115
M 86
AN 114,114
AN 115,115
M 87
WL 2
AN 116,118
AN 117,119
M 88
SH 118
SH 119
M 89
AN 118,118
AN 119,119
M 90
TY 122,8200
TY 126,22024
M 91
ad 84,1632
M 92
W 0
M 93
M 94
M 95
R 82
M 106
W 1
UL 6,38
M 107
UH 7,38
AR 8,148
)"
R"(M 104
AR 9,172
AD 8,6
M 105
WL 3
AN 120,122
AN 121,123
M 102
SH 122
SH 123
M 103
AN 122,122
AN 123,123
M 100
WL 2
AN 124,126
AN 125,127
M 101
SH 126
SH 127
M 98
AN 126,126
AN 127,127
M 99
W 2
AD 9,7
M 96
AW 148,8
AW 172,9
M 97
R 0
M 108
mw 51
UL 6,39
UH 7,39
M 109
ad 84,1638
W 0
M 110
AR 8,196
AD 8,6
M 111
AD 152,7
AW 196,8
M 112
W 1
UL 6,40
M 113
UH 7,40
AR 8,149
M 114
AR 9,173
AD 8,6
M 115
W 2
AD 9,7
M 116
AW 149,8
AW 173,9
M 117
UL 6,41
UH 7,41
M 118
bc 49,18,66,60
AR 8,197
M 119
WL 0
R 2
M 120
mh 0
AD 8,6
AD 153,7
M 121
ad 66,3488
H 0
M 133
AW 197,8
UL 6,42
M 132
TX 58,4608
TX 62,18432
M 134
DG 1024
M 135
UH 7,42
AR 8,150
M 123
AR 9,174
AD 8,6
M 122
TY 66,5120
TY 70,18944
M 124
bc 48,18,64,56
AD 9,7
M 125
mh 3
AW 150,8
AW 174,9
M 137
ad 64,3488
H 0
M 136
TX 74,6144
TX 78,19968
M 138
WL 4
Q 0
M 139
Q 2
M 127
Q 4
M 126
TY 82,6656
TY 86,20480
M 128
Q 6
M 129
WL 4
Q 8
M 141
Q 10
M 140
TX 90,7680
TX 94,21504
M 142
Q 12
M 143
R 6
M 131
Q 14
M 130
TY 98,8192
TY 102,22016
M 0
R 4
M 1
WL 10
Q 16
M 13
Q 18
M 12
Q 20
M 14
Q 22
M 15
WL 8
Q 24
M 3
Q 26
M 2
Q 28
M 4
Q 30
M 5
WL 6
Q 32
M 17
Q 34
M 16
Q 36
M 18
Q 38
M 19
WL 2
Q 40
M 7
Q 42
M 6
Q 44
M 8
Q 46
M 9
H 1
UL 6,43
M 21
ae 42,72,96
M 20
s_bfe_u32 s52, s50, 0x10005
v_mad_i32_i24 v50, v49, s52, v48
SA 248,256
M 22
H 2
UH 7,43
M 23
R 10
M 11
AR 8,198
AD 8,6
M 10
WL 2
R 8
PA 12288
DF
M 34
AD 154,7
AW 198,8
M 35
UL 6,44
UH 7,44
M 32
AR 8,151
AR 9,175
M 33
AD 8,6
AD 9,7
M 30
AW 151,8
AW 175,9
M 31
UL 6,45
UH 7,45
M 28
AR 8,199
AD 8,6
M 29
AD 155,7
AW 199,8
M 26
M 27
WV 7
M 24
WL 0
FP
M 25
M 36
M 37
M 38
M 39
M 40
M 41
M 42
M 43
M 44
M 45
FR
M 46
M 47
M 58
M 59
M 56
M 57
M 54
M 55
WL 0
FQ 144
M 52
M 53
M 50
FZ 144
R 24
M 51
mw 72
R 26
M 48
TY 106,9736
TY 110,23560
M 49
R 28
M 60
ad 84,1680
W 0
M 61
M 62
M 63
W 1
M 64
M 65
M 66
W 2
M 67
WL 3
AN 104,106
AN 105,107
M 68
SH 106
SH 107
M 69
mw 75
AN 106,106
AN 107,107
M 70
WL 2
AN 108,110
AN 109,111
M 71
R 30
M 82
SH 110
SH 111
M 83
AN 110,110
AN 111,111
M 80
TY 114,11272
TY 118,25096
M 81
ad 84,1686
W 0
M 78
ae 44,70,384
M 79
s_bitcmp1_b32 s50, 6
s_cselect_b32 s51, 24576, 0
v_add_u32 v47, s51, v3
M 76
W 1
XL 22,256
M 77
XL 24,1024
M 74
M 75
W 2
M 72
XL 26,1792
M 73
WL 1
R 32
M 84
mw 0
AN 112,114
AN 113,115
M 85
SH 114
SH 115
M 86
AN 114,114
AN 115,115
M 87
WL 2
AN 116,118
AN 117,119
M 88
SH 118
SH 119
M 89
AN 118,118
AN 119,119
M 90
TY 122,12808
TY 126,26632
M 91
ad 84,1728
M 92
W 0
SA 250,1024
M 93
XL 28,2560
M 94
M 95
R 34
M 106
W 1
M 107
M 104
UL 6,14
UH 7,14
M 105
WL 3
AN 120,122
AN 121,123
M 102
SH 122
SH 123
M 103
AN 122,122
AN 123,123
M 100
WL 2
AN 124,126
AN 125,127
M 101
SH 126
SH 127
M 98
AN 126,126
AN 127,127
M 99
W 2
AR 8,152
M 96
AR 9,176
AD 8,6
M 97
R 48
M 108
mw 3
AD 9,7
AW 152,8
M 109
ad 84,1734
W 0
M 110
AW 176,9
UL 6,15
M 111
UH 7,15
AD 132,6
M 112
W 1
)"
R"(AD 156,7
M 113
UL 6,16
UH 7,16
M 114
AR 8,153
AR 9,177
M 115
W 2
AD 8,6
M 116
AD 9,7
AW 153,8
M 117
AW 177,9
UL 6,17
M 118
bc 49,19,66,60
UH 7,17
M 119
WL 0
R 50
M 120
mh 9
AD 133,6
AD 157,7
M 121
ad 66,3680
H 0
M 133
UL 6,18
UH 7,18
M 132
TX 58,9216
TX 62,23040
M 134
DG 1024
M 135
AR 8,154
AR 9,178
M 123
AD 8,6
AD 9,7
M 122
TY 66,9728
TY 70,23552
M 124
bc 48,19,64,56
AW 154,8
M 125
mh 12
AW 178,9
UL 6,19
M 137
ad 64,3680
H 0
M 136
TX 74,10752
TX 78,24576
M 138
WL 4
Q 0
M 139
Q 2
M 127
Q 4
M 126
TY 82,11264
TY 86,25088
M 128
Q 6
M 129
WL 4
Q 8
M 141
Q 10
M 140
TX 90,12288
TX 94,26112
M 142
Q 12
M 143
R 54
M 131
Q 14
M 130
TY 98,12800
TY 102,26624
M 0
R 52
M 1
WL 10
Q 16
M 13
Q 18
M 12
Q 20
M 14
Q 22
M 15
WL 8
Q 24
M 3
Q 26
M 2
Q 28
M 4
Q 30
M 5
WL 6
Q 32
M 17
Q 34
M 16
Q 36
M 18
Q 38
M 19
WL 2
Q 40
M 7
Q 42
M 6
Q 44
M 8
Q 46
M 9
H 1
SA 252,1792
M 21
UH 7,19
AD 134,6
M 20
AD 158,7
UL 6,20
M 22
H 2
UH 7,20
M 23
R 58
M 11
AR 8,155
AR 9,179
M 10
WL 2
R 56
M 34
AD 8,6
AD 9,7
M 35
AW 155,8
AW 179,9
M 32
UL 6,21
UH 7,21
M 33
AD 135,6
AD 159,7
M 30
M 31
M 28
M 29
M 26
M 27
WV 6
M 24
WL 0
FP
M 25
M 36
M 37
M 38
M 39
M 40
M 41
M 42
M 43
M 44
M 45
FR
M 46
M 47
M 58
M 59
M 56
M 57
M 54
M 55
WL 0
FQ 148
M 52
M 53
M 50
FZ 148
R 72
M 51
mw 24
R 74
M 48
TY 106,520
TY 110,14344
M 49
R 76
M 60
ad 84,1776
W 0
M 61
M 62
M 63
W 1
M 64
M 65
M 66
W 2
M 67
WL 3
AN 104,106
AN 105,107
M 68
SH 106
SH 107
M 69
mw 27
AN 106,106
AN 107,107
M 70
WL 2
AN 108,110
AN 109,111
M 71
R 78
M 82
SH 110
SH 111
M 83
AN 110,110
AN 111,111
M 80
TY 114,2056
TY 118,15880
M 81
ad 84,1782
W 0
M 78
ae 44,70,384
M 79
s_bfe_u32 s51, s50, 0x10007
v_mad_i32_i24 v47, v49, s51, v48
XL 30,256
M 76
W 1
M 77
M 74
XL 32,1024
M 75
W 2
SA 254,2560
M 72
M 73
WL 1
R 80
M 84
mw 48
AN 112,114
AN 113,115
M 85
SH 114
SH 115
M 86
AN 114,114
AN 115,115
M 87
WL 2
AN 116,118
AN 117,119
M 88
SH 118
SH 119
M 89
AN 118,118
AN 119,119
M 90
TY 122,3592
TY 126,17416
M 91
ad 84,1824
M 92
W 0
XL 34,1792
M 93
XL 36,2560
M 94
M 95
R 82
M 106
W 1
M 107
M 104
M 105
WL 3
AN 120,122
AN 121,123
M 102
SH 122
SH 123
M 103
AN 122,122
AN 123,123
M 100
WL 2
AN 124,126
AN 125,127
M 101
SH 126
SH 127
M 98
AN 126,126
AN 127,127
M 99
W 2
M 96
M 97
R 0
M 108
mw 51
M 109
ad 84,1830
W 0
M 110
M 111
M 112
W 1
M 113
M 114
M 115
W 2
M 116
M 117
M 118
bc 49,20,66,60
M 119
WL 2
R 2
M 120
mh 18
M 121
ad 66,3872
H 0
M 133
M 132
TX 58,0
TX 62,13824
M 134
DG 1024
M 135
M 123
UL 6,22
UH 7,22
M 122
TY 66,512
TY 70,14336
M 124
bc 48,20,64,56
AD 160,6
M 125
mh 21
AD 184,7
UL 6,23
M 137
ad 64,3872
H 0
M 136
TX 74,1536
TX 78,15360
M 138
WL 4
Q 0
M 139
Q 2
M 127
Q 4
M 126
TY 82,2048
TY 86,15872
M 128
Q 6
M 129
WL 4
Q 8
M 141
Q 10
M 140
TX 90,3072
TX 94,16896
M 142
Q 12
M 143
R 6
M 131
Q 14
)"
R"(M 130
TY 98,3584
TY 102,17408
M 0
R 4
M 1
WL 10
Q 16
M 13
Q 18
M 12
Q 20
M 14
Q 22
M 15
WL 8
Q 24
M 3
Q 26
M 2
Q 28
M 4
Q 30
M 5
WL 6
Q 32
M 17
Q 34
M 16
Q 36
M 18
Q 38
M 19
WL 2
Q 40
M 7
Q 42
M 6
Q 44
M 8
Q 46
M 9
H 1
UH 7,23
M 21
AD 208,6
AD 232,7
M 20
UL 6,24
UH 7,24
M 22
H 2
AD 161,6
M 23
R 10
M 11
AD 185,7
UL 6,25
M 10
WL 2
R 8
PA 12288
DF
M 34
UH 7,25
AD 209,6
M 35
AD 233,7
UL 6,26
M 32
UH 7,26
AD 162,6
M 33
AD 186,7
UL 6,27
M 30
UH 7,27
AD 210,6
M 31
AD 234,7
UL 6,28
M 28
UH 7,28
AD 163,6
M 29
AD 187,7
UL 6,29
M 26
UH 7,29
AD 211,6
M 27
WV 6
AD 235,7
M 24
WL 0
FP
M 25
M 36
M 37
M 38
M 39
M 40
M 41
M 42
M 43
M 44
M 45
FR
M 46
M 47
M 58
M 59
M 56
M 57
M 54
M 55
WL 0
FQ 152
M 52
M 53
M 50
FZ 152
R 24
M 51
mw 72
R 26
M 48
TY 106,5128
TY 110,18952
M 49
R 28
M 60
ad 84,1872
W 0
M 61
M 62
M 63
W 1
M 64
M 65
M 66
W 2
M 67
WL 3
AN 104,106
AN 105,107
M 68
SH 106
SH 107
M 69
mw 75
AN 106,106
AN 107,107
M 70
WL 2
AN 108,110
AN 109,111
M 71
R 30
M 82
SH 110
SH 111
M 83
AN 110,110
AN 111,111
M 80
TY 114,6664
TY 118,20488
M 81
ad 84,1878
W 0
M 78
M 79
M 76
W 1
M 77
ae 44,68,384
M 74
s_bitcmp1_b32 s50, 8
s_cselect_b32 s51, 24576, 0
v_add_u32 v47, s51, v3
M 75
W 2
XL 38,256
M 72
M 73
WL 1
R 32
M 84
mw 0
AN 112,114
AN 113,115
M 85
SH 114
SH 115
M 86
AN 114,114
AN 115,115
M 87
WL 2
AN 116,118
AN 117,119
M 88
SH 118
SH 119
M 89
AN 118,118
AN 119,119
M 90
TY 122,8200
TY 126,22024
M 91
ad 84,1920
M 92
W 0
XL 40,1024
M 93
XL 42,1792
M 94
M 95
R 34
M 106
W 1
XL 44,2560
M 107
M 104
M 105
WL 3
AN 120,122
AN 121,123
M 102
SH 122
SH 123
M 103
AN 122,122
AN 123,123
M 100
WL 2
AN 124,126
AN 125,127
M 101
SH 126
SH 127
M 98
AN 126,126
AN 127,127
M 99
W 2
M 96
M 97
R 48
M 108
mw 3
M 109
ad 84,1926
W 0
M 110
M 111
M 112
W 1
M 113
M 114
M 115
W 2
M 116
M 117
M 118
bc 49,21,66,60
M 119
WL 2
R 50
M 120
mh 0
M 121
ad 66,4064
H 0
M 133
M 132
TX 58,4608
TX 62,18432
M 134
DG 1024
M 135
M 123
M 122
TY 66,5120
TY 70,18944
M 124
bc 48,21,64,56
M 125
mh 3
M 137
ad 64,4064
H 0
M 136
TX 74,6144
TX 78,19968
M 138
WL 4
Q 0
M 139
Q 2
M 127
Q 4
M 126
TY 82,6656
TY 86,20480
M 128
Q 6
M 129
WL 4
Q 8
M 141
Q 10
M 140
TX 90,7680
TX 94,21504
M 142
Q 12
M 143
R 54
M 131
Q 14
M 130
TY 98,8192
TY 102,22016
M 0
R 52
M 1
WL 10
Q 16
M 13
Q 18
M 12
Q 20
M 14
Q 22
M 15
WL 8
Q 24
M 3
Q 26
M 2
Q 28
M 4
Q 30
M 5
WL 6
Q 32
M 17
Q 34
M 16
Q 36
M 18
Q 38
M 19
WL 2
Q 40
M 7
Q 42
M 6
Q 44
M 8
Q 46
M 9
H 1
UL 6,30
M 21
UH 7,30
AD 164,6
M 20
AD 188,7
UL 6,31
M 22
H 2
UH 7,31
M 23
R 58
M 11
AD 212,6
AD 236,7
M 10
WL 2
R 56
M 34
UL 6,32
UH 7,32
M 35
AD 165,6
AD 189,7
M 32
UL 6,33
UH 7,33
M 33
AD 213,6
AD 237,7
M 30
UL 6,34
UH 7,34
M 31
)"
R"(AD 166,6
AD 190,7
M 28
UL 6,35
UH 7,35
M 29
AD 214,6
AD 238,7
M 26
UL 6,36
UH 7,36
M 27
WV 5
AD 167,6
AD 191,7
M 24
WL 0
FP
UL 6,37
M 25
UH 7,37
AD 215,6
M 36
AD 239,7
M 37
M 38
M 39
M 40
M 41
M 42
M 43
M 44
M 45
FR
M 46
M 47
M 58
M 59
M 56
M 57
M 54
M 55
WL 0
FQ 156
M 52
M 53
M 50
FZ 156
R 72
M 51
mw 24
R 74
M 48
TY 106,9736
TY 110,23560
M 49
R 76
M 60
ad 84,1968
W 0
M 61
M 62
M 63
W 1
M 64
M 65
M 66
W 2
M 67
WL 3
AN 104,106
AN 105,107
M 68
SH 106
SH 107
M 69
mw 27
AN 106,106
AN 107,107
M 70
WL 2
AN 108,110
AN 109,111
M 71
R 78
M 82
SH 110
SH 111
M 83
AN 110,110
AN 111,111
M 80
TY 114,11272
TY 118,25096
M 81
ad 84,1974
W 0
M 78
M 79
M 76
W 1
M 77
M 74
M 75
W 2
M 72
ae 44,68,384
M 73
WL 1
R 80
M 84
mw 48
AN 112,114
AN 113,115
M 85
s_bfe_u32 s51, s50, 0x10009
SH 114
SH 115
M 86
AN 114,114
AN 115,115
M 87
WL 2
AN 116,118
AN 117,119
M 88
SH 118
SH 119
M 89
AN 118,118
AN 119,119
M 90
TY 122,12808
TY 126,26632
M 91
ad 84,2016
M 92
W 0
v_mad_i32_i24 v47, v49, s51, v48
M 93
XL 14,256
M 94
XL 16,1024
M 95
R 82
M 106
W 1
XL 18,1792
M 107
M 104
M 105
WL 3
AN 120,122
AN 121,123
M 102
SH 122
SH 123
M 103
AN 122,122
AN 123,123
M 100
WL 2
AN 124,126
AN 125,127
M 101
SH 126
SH 127
M 98
AN 126,126
AN 127,127
M 99
W 2
XL 20,2560
M 96
M 97
R 0
M 108
mw 51
M 109
ad 84,2022
W 0
M 110
M 111
M 112
W 1
M 113
M 114
M 115
W 2
M 116
M 117
M 118
bc 49,22,66,60
M 119
WL 2
R 2
M 120
mh 9
M 121
ad 66,4256
H 0
M 133
M 132
TX 58,9216
TX 62,23040
M 134
DG 1024
M 135
M 123
M 122
TY 66,9728
TY 70,23552
M 124
bc 48,22,64,56
M 125
mh 12
M 137
ad 64,4256
H 0
M 136
TX 74,10752
TX 78,24576
M 138
WL 4
Q 0
M 139
Q 2
M 127
Q 4
M 126
TY 82,11264
TY 86,25088
M 128
Q 6
M 129
WL 4
Q 8
M 141
Q 10
M 140
TX 90,12288
TX 94,26112
M 142
Q 12
M 143
R 6
M 131
Q 14
M 130
TY 98,12800
TY 102,26624
M 0
R 4
M 1
WL 10
Q 16
M 13
Q 18
M 12
Q 20
M 14
Q 22
M 15
WL 8
Q 24
M 3
Q 26
M 2
Q 28
M 4
Q 30
M 5
WL 6
Q 32
M 17
Q 34
M 16
Q 36
M 18
Q 38
M 19
WL 2
Q 40
M 7
Q 42
M 6
Q 44
M 8
Q 46
M 9
H 1
UL 6,38
M 21
UH 7,38
AD 168,6
M 20
AD 192,7
UL 6,39
M 22
H 2
UH 7,39
M 23
R 10
M 11
AD 216,6
AD 240,7
M 10
WL 2
R 8
PA 12288
DF
M 34
UL 6,40
UH 7,40
M 35
AD 169,6
AD 193,7
M 32
UL 6,41
UH 7,41
M 33
AD 217,6
AD 241,7
M 30
UL 6,42
UH 7,42
M 31
AD 170,6
AD 194,7
M 28
UL 6,43
UH 7,43
M 29
AD 218,6
AD 242,7
M 26
UL 6,44
UH 7,44
M 27
WV 6
AD 171,6
AD 195,7
M 24
WL 0
FP
UL 6,45
M 25
UH 7,45
AD 219,6
M 36
AD 243,7
M 37
M 38
M 39
M 40
M 41
M 42
M 43
M 44
M 45
FR
M 46
M 47
M 58
M 59
M 56
M 57
M 54
M 55
WL 0
FQ 160
M 52
M 53
M 50
FZ 160
R 24
M 51
mw 72
R 26
M 48
TY 106,520
TY 110,14344
M 49
R 28
M 60
ad 84,2064
W 0
M 61
M 62
M 63
W 1
M 64
M 65
M 66
W 2
M 67
WL 3
AN 104,106
AN 105,107
M 68
)"
R"(SH 106
SH 107
M 69
mw 75
AN 106,106
AN 107,107
M 70
WL 2
AN 108,110
AN 109,111
M 71
R 30
M 82
SH 110
SH 111
M 83
AN 110,110
AN 111,111
M 80
TY 114,2056
TY 118,15880
M 81
ad 84,2070
W 0
M 78
M 79
M 76
W 1
M 77
M 74
M 75
W 2
M 72
M 73
R 32
M 84
mw 0
WL 3
AN 112,114
AN 113,115
M 85
SH 114
SH 115
M 86
AN 114,114
AN 115,115
M 87
WL 2
AN 116,118
AN 117,119
M 88
SH 118
SH 119
M 89
AN 118,118
AN 119,119
M 90
TY 122,3592
TY 126,17416
M 91
ad 84,2112
M 92
W 0
M 93
ae 44,68,480
M 94
s_bitcmp1_b32 s50, 10
s_cselect_b32 s51, 24576, 0
v_add_u32 v47, s51, v3
M 95
R 34
M 106
W 1
XL 22,256
M 107
XL 24,1024
M 104
XL 26,1792
M 105
WL 3
AN 120,122
AN 121,123
M 102
SH 122
SH 123
M 103
AN 122,122
AN 123,123
M 100
WL 2
AN 124,126
AN 125,127
M 101
SH 126
SH 127
M 98
AN 126,126
AN 127,127
M 99
W 2
XL 28,2560
M 96
M 97
R 48
M 108
mw 3
M 109
ad 84,2118
W 0
M 110
M 111
M 112
W 1
M 113
M 114
M 115
W 2
M 116
M 117
M 118
bc 49,23,66,60
M 119
WL 2
R 50
M 120
mh 18
M 121
ad 66,4448
H 0
M 133
M 132
TX 58,0
TX 62,13824
M 134
DG 1024
M 135
M 123
M 122
TY 66,512
TY 70,14336
M 124
bc 48,23,64,56
M 125
mh 21
M 137
ad 64,4448
H 0
M 136
TX 74,1536
TX 78,15360
M 138
WL 4
Q 0
M 139
Q 2
M 127
Q 4
M 126
TY 82,2048
TY 86,15872
M 128
Q 6
M 129
WL 4
Q 8
M 141
Q 10
M 140
TX 90,3072
TX 94,16896
M 142
Q 12
M 143
R 54
M 131
Q 14
M 130
TY 98,3584
TY 102,17408
M 0
R 52
M 1
WL 10
Q 16
M 13
Q 18
M 12
Q 20
M 14
Q 22
M 15
WL 8
Q 24
M 3
Q 26
M 2
Q 28
M 4
Q 30
M 5
WL 6
Q 32
M 17
Q 34
M 16
Q 36
M 18
Q 38
M 19
WL 2
Q 40
M 7
Q 42
M 6
Q 44
M 8
Q 46
M 9
H 1
UL 6,14
M 21
UH 7,14
AD 172,6
M 20
AD 196,7
UL 6,15
M 22
H 2
UH 7,15
M 23
R 58
M 11
AD 220,6
AD 244,7
M 10
WL 2
R 56
M 34
UL 6,16
UH 7,16
M 35
AD 173,6
AD 197,7
M 32
UL 6,17
UH 7,17
M 33
AD 221,6
AD 245,7
M 30
UL 6,18
UH 7,18
M 31
AD 174,6
AD 198,7
M 28
UL 6,19
UH 7,19
M 29
AD 222,6
AD 246,7
M 26
UL 6,20
UH 7,20
M 27
WV 5
AD 175,6
AD 199,7
M 24
WL 0
FP
UL 6,21
M 25
UH 7,21
AD 223,6
M 36
AD 247,7
M 37
M 38
M 39
M 40
M 41
M 42
M 43
M 44
M 45
FR
M 46
M 47
M 58
M 59
M 56
M 57
M 54
M 55
WL 0
FQ 164
M 52
M 53
M 50
FZ 164
R 72
M 51
mw 24
R 74
M 48
TY 106,5128
TY 110,18952
M 49
R 76
M 60
ad 84,2160
W 0
M 61
M 62
M 63
W 1
M 64
M 65
M 66
W 2
M 67
WL 3
AN 104,106
AN 105,107
M 68
SH 106
SH 107
M 69
mw 27
AN 106,106
AN 107,107
M 70
WL 2
AN 108,110
AN 109,111
M 71
R 78
M 82
SH 110
SH 111
M 83
AN 110,110
AN 111,111
M 80
TY 114,6664
TY 118,20488
M 81
ad 84,2166
W 0
M 78
M 79
M 76
W 1
M 77
M 74
M 75
W 2
M 72
M 73
R 80
M 84
mw 48
WL 3
AN 112,114
AN 113,115
M 85
SH 114
SH 115
M 86
AN 114,114
AN 115,115
M 87
WL 2
AN 116,118
AN 117,119
M 88
SH 118
SH 119
M 89
AN 118,118
AN 119,119
M 90
TY 122,8200
TY 126,22024
M 91
ad 84,2208
M 92
W 0
M 93
ae 44,68,480
M 94
s_bfe_u32 s51, s50, 0x1000b
v_mad_i32_i24 v47, v49, s51, v48
XL 30,256
M 95
R 82
M 106
W 1
XL 32,1024
)"
R"(M 107
M 104
M 105
WL 3
AN 120,122
AN 121,123
M 102
SH 122
SH 123
M 103
AN 122,122
AN 123,123
M 100
WL 2
AN 124,126
AN 125,127
M 101
SH 126
SH 127
M 98
AN 126,126
AN 127,127
M 99
W 2
XL 34,1792
M 96
M 97
R 0
M 108
mw 51
XL 36,2560
M 109
ad 84,2214
W 0
M 110
M 111
M 112
W 1
M 113
M 114
M 115
W 2
M 116
M 117
M 118
bc 49,0,66,60
M 119
WL 2
R 2
M 120
mh 0
M 121
ad 66,64
H 0
M 133
M 132
TX 58,4608
TX 62,18432
M 134
DG 1024
M 135
M 123
M 122
TY 66,5120
TY 70,18944
M 124
bc 48,0,64,56
M 125
mh 3
M 137
ad 64,32
H 0
M 136
TX 74,6144
TX 78,19968
M 138
WL 4
Q 0
M 139
Q 2
M 127
Q 4
M 126
TY 82,6656
TY 86,20480
M 128
Q 6
M 129
WL 4
Q 8
M 141
Q 10
M 140
TX 90,7680
TX 94,21504
M 142
Q 12
M 143
R 6
M 131
Q 14
M 130
TY 98,8192
TY 102,22016
M 0
R 4
M 1
WL 10
Q 16
M 13
Q 18
M 12
Q 20
M 14
Q 22
M 15
WL 8
Q 24
M 3
Q 26
M 2
Q 28
M 4
Q 30
M 5
WL 6
Q 32
M 17
Q 34
M 16
Q 36
M 18
Q 38
M 19
WL 2
Q 40
M 7
Q 42
M 6
Q 44
M 8
Q 46
M 9
H 1
UL 6,22
M 21
UH 7,22
AD 176,6
M 20
AD 200,7
UL 6,23
M 22
H 2
UH 7,23
M 23
R 10
M 11
AD 224,6
AD 248,7
M 10
WL 2
R 8
PA 12288
DF
M 34
UL 6,24
UH 7,24
M 35
AD 177,6
AD 201,7
M 32
UL 6,25
UH 7,25
M 33
AD 225,6
AD 249,7
M 30
UL 6,26
UH 7,26
M 31
AD 178,6
AD 202,7
M 28
UL 6,27
UH 7,27
M 29
AD 226,6
AD 250,7
M 26
UL 6,28
UH 7,28
M 27
WV 6
AD 179,6
AD 203,7
M 24
WL 0
FP
UL 6,29
M 25
UH 7,29
AD 227,6
M 36
AD 251,7
M 37
M 38
M 39
M 40
M 41
M 42
M 43
M 44
M 45
FR
M 46
M 47
M 58
M 59
M 56
M 57
M 54
M 55
WL 0
FQ 168
M 52
M 53
M 50
FZ 168
R 24
M 51
mw 72
R 26
M 48
TY 106,9736
TY 110,23560
M 49
R 28
M 60
ad 84,2256
W 0
M 61
M 62
M 63
W 1
M 64
M 65
M 66
W 2
M 67
WL 3
AN 104,106
AN 105,107
M 68
SH 106
SH 107
M 69
mw 75
AN 106,106
AN 107,107
M 70
WL 2
AN 108,110
AN 109,111
M 71
R 30
M 82
SH 110
SH 111
M 83
AN 110,110
AN 111,111
M 80
TY 114,11272
TY 118,25096
M 81
ad 84,2262
W 0
M 78
M 79
M 76
W 1
M 77
M 74
M 75
W 2
M 72
M 73
R 32
M 84
mw 0
WL 3
AN 112,114
AN 113,115
M 85
SH 114
SH 115
M 86
AN 114,114
AN 115,115
M 87
WL 2
AN 116,118
AN 117,119
M 88
SH 118
SH 119
M 89
AN 118,118
AN 119,119
M 90
TY 122,12808
TY 126,26632
M 91
ad 84,0
M 92
W 0
M 93
M 94
M 95
R 34
M 106
W 1
M 107
M 104
M 105
WL 3
AN 120,122
AN 121,123
M 102
SH 122
SH 123
M 103
AN 122,122
AN 123,123
M 100
WL 2
AN 124,126
AN 125,127
M 101
SH 126
SH 127
M 98
AN 126,126
AN 127,127
M 99
W 2
M 96
M 97
R 48
M 108
mw 3
M 109
ad 84,6
W 0
M 110
M 111
M 112
W 1
M 113
M 114
M 115
W 2
M 116
M 117
M 118
bc 49,1,66,60
M 119
WL 2
R 50
M 120
mh 9
M 121
ad 66,256
H 0
M 133
M 132
TX 58,9216
TX 62,23040
M 134
DG 1024
M 135
M 123
M 122
TY 66,9728
TY 70,23552
M 124
bc 48,1,64,56
M 125
mh 12
M 137
ad 64,256
H 0
M 136
TX 74,10752
TX 78,24576
M 138
WL 4
Q 0
M 139
Q 2
)"
R"(M 127
Q 4
M 126
TY 82,11264
TY 86,25088
M 128
Q 6
M 129
WL 4
Q 8
M 141
Q 10
M 140
TX 90,12288
TX 94,26112
M 142
Q 12
M 143
R 54
M 131
Q 14
M 130
TY 98,12800
TY 102,26624
M 0
R 52
M 1
WL 10
Q 16
M 13
Q 18
M 12
Q 20
M 14
Q 22
M 15
WL 8
Q 24
M 3
Q 26
M 2
Q 28
M 4
Q 30
M 5
WL 6
Q 32
M 17
Q 34
M 16
Q 36
M 18
Q 38
M 19
WL 2
Q 40
M 7
Q 42
M 6
Q 44
M 8
Q 46
M 9
DG 1024
M 21
UL 6,30
UH 7,30
M 20
AD 180,6
AD 204,7
M 22
mh 15
UL 6,31
UH 7,31
M 23
R 58
M 11
ad 64,227
H 0
M 10
WL 2
R 56
M 34
AD 228,6
AD 252,7
M 35
DG 1024
M 32
UL 6,32
UH 7,32
BV 52,4608
M 33
AD 181,6
AD 205,7
M 30
UL 6,33
UH 7,33
M 31
AD 229,6
AD 253,7
M 28
UL 6,34
UH 7,34
M 29
AD 182,6
AD 206,7
M 26
UL 6,35
UH 7,35
M 27
WV 6
AD 230,6
AD 254,7
M 24
WL 1
FP
UL 6,36
M 25
UH 7,36
AD 183,6
M 36
AD 207,7
UL 6,37
M 37
UH 7,37
AD 231,6
M 38
AD 255,7
M 39
M 40
ds_read_b64 v[12:13], v4 offset:4864
ds_read_b64 v[46:47], v4 offset:4872
M 41
AR 6,36
AR 7,60
M 42
AR 8,84
AR 9,108
M 43
WL 3
EF 6,52
EF 7,53
M 44
EF 8,54
EF 9,55
M 45
FR
CV 38,6,7
M 46
CV 39,8,9
AR 6,37
M 47
AR 7,61
AR 8,85
M 58
AR 9,109
EF 6,52
M 59
EF 7,53
EF 8,54
M 56
EF 9,55
CV 40,6,7
M 57
CV 41,8,9
AR 6,38
M 54
AR 7,62
AR 8,86
M 55
WL 0
FQ 172
AR 9,110
M 52
EF 6,52
EF 7,53
M 53
EF 8,54
EF 9,55
M 50
FZ 172
R 72
M 51
mw 24
R 74
M 48
TY 106,520
TY 110,14344
M 49
R 76
M 60
ad 84,48
W 0
M 61
CV 42,6,7
CV 43,8,9
M 62
AR 6,39
AR 7,63
M 63
W 1
AR 8,87
M 64
AR 9,111
EF 6,52
M 65
EF 7,53
EF 8,54
M 66
W 2
EF 9,55
M 67
WL 3
AN 104,106
AN 105,107
M 68
SH 106
SH 107
M 69
mw 27
AN 106,106
AN 107,107
M 70
WL 2
AN 108,110
AN 109,111
M 71
R 78
M 82
SH 110
SH 111
M 83
AN 110,110
AN 111,111
M 80
TY 114,2056
TY 118,15880
M 81
ad 84,54
W 0
M 78
CV 44,6,7
CV 45,8,9
M 79
ae 42,74,384
AR 6,40
M 76
W 1
AR 7,64
M 77
s_bitcmp1_b32 s50, 6
s_cselect_b32 s52, 24576, 0
v_add_u32 v50, s52, v3
ST 38,0
M 74
AR 8,88
AR 9,112
M 75
W 2
EF 6,52
M 72
EF 7,53
EF 8,54
M 73
WL 1
R 80
M 84
mw 48
AN 112,114
AN 113,115
M 85
SH 114
SH 115
M 86
AN 114,114
AN 115,115
M 87
WL 2
AN 116,118
AN 117,119
M 88
SH 118
SH 119
M 89
AN 118,118
AN 119,119
M 90
TY 122,3592
TY 126,17416
M 91
ad 84,96
ST 40,768
M 92
W 0
EF 9,55
M 93
CV 14,6,7
CV 15,8,9
M 94
AR 6,41
AR 7,65
M 95
R 82
M 106
W 1
AR 8,89
M 107
AR 9,113
EF 6,52
M 104
EF 7,53
EF 8,54
M 105
WL 3
AN 120,122
AN 121,123
M 102
SH 122
SH 123
M 103
AN 122,122
AN 123,123
M 100
WL 2
AN 124,126
AN 125,127
M 101
SH 126
SH 127
ST 42,1536
M 98
AN 126,126
AN 127,127
M 99
W 2
EF 9,55
M 96
CV 16,6,7
CV 17,8,9
M 97
R 0
M 108
mw 51
AR 6,42
AR 7,66
M 109
ad 84,102
W 0
M 110
AR 8,90
AR 9,114
M 111
EF 6,52
EF 7,53
M 112
W 1
EF 8,54
M 113
EF 9,55
CV 18,6,7
M 114
CV 19,8,9
AR 6,43
M 115
W 2
ST 44,2304
M 116
AR 7,67
AR 8,91
M 117
AR 9,115
EF 6,52
M 118
bc 49,2,66,60
EF 7,53
M 119
WL 0
R 2
M 120
mh 18
EF 8,54
EF 9,55
M 121
ad 66,448
H 0
M 133
CV 20,6,7
CV 21,8,9
M 132
TX 58,0
TX 62,13824
BV 52,5120
M 134
DG 1024
M 135
AR 6,132
AR 7,156
M 123
AR 8,180
EF 6,12
)"
R"(M 122
TY 66,512
TY 70,14336
M 124
bc 48,2,64,56
ae 42,74,384
M 125
mh 21
EF 7,13
EF 8,46
M 137
ad 64,448
H 0
M 136
WL 3
TX 74,1536
TX 78,15360
M 138
s_bfe_u32 s52, s50, 0x10007
Q 0
v_mad_i32_i24 v50, v49, s52, v48
M 139
Q 2
ST 14,0
M 127
Q 4
M 126
TY 82,2048
TY 86,15872
M 128
Q 6
M 129
WL 4
Q 8
M 141
Q 10
M 140
TX 90,3072
TX 94,16896
M 142
Q 12
M 143
R 6
M 131
Q 14
M 130
TY 98,3584
TY 102,17408
v_mfma_f32_16x16x32_f16 a[36:39], v[56:59], a[0:3], a[32:35]
R 4
M 1
WL 10
Q 16
ST 16,768
v_mfma_f32_16x16x32_f16 a[60:63], v[60:63], a[12:15], a[32:35]
Q 18
M 12
Q 20
v_mfma_f32_16x16x32_f16 a[64:67], v[64:67], a[8:11], a[32:35]
Q 22
M 15
WL 8
Q 24
v_mfma_f32_16x16x32_f16 a[40:43], v[68:71], a[4:7], a[32:35]
Q 26
M 2
Q 28
M 4
Q 30
M 5
WL 6
Q 32
M 17
Q 34
M 16
Q 36
M 18
Q 38
M 19
WL 1
Q 40
ST 18,1536
M 7
Q 42
M 6
Q 44
M 8
Q 46
M 9
H 1
EF 136,47
M 21
CV 22,6,7
CV 23,8,136
M 20
AR 6,133
AR 7,157
M 22
H 2
AR 8,181
M 23
R 10
M 11
EF 6,12
EF 7,13
M 10
WL 2
R 8
PA 12288
DF
M 34
EF 8,46
EF 137,47
M 35
ST 20,2304
CV 24,6,7
M 32
CV 25,8,137
AR 6,134
M 33
AR 7,158
AR 8,182
M 30
EF 6,12
EF 7,13
M 31
EF 8,46
EF 138,47
M 28
CV 26,6,7
CV 27,8,138
M 29
AR 6,135
AR 7,159
v_mfma_f32_16x16x32_f16 a[88:91], v[64:67], a[16:19], a[32:35]
AR 8,183
EF 6,12
M 27
WV 11
EF 7,13
EF 8,46
v_mfma_f32_16x16x32_f16 a[84:87], v[56:59], a[16:19], a[32:35]
WL 0
FP
EF 139,47
M 25
CV 28,6,7
CV 29,8,139
v_mfma_f32_16x16x32_f16 a[108:111], v[56:59], a[24:27], a[32:35]
AR 6,136
AR 7,160
AR 8,184
EF 6,12
M 37
ae 42,74,384
EF 7,13
EF 8,46
EF 140,47
v_mfma_f32_16x16x32_f16 a[112:115], v[64:67], a[24:27], a[32:35]
s_bitcmp1_b32 s50, 6
s_cselect_b32 s52, 24576, 0
v_add_u32 v50, s52, v3
ST 22,128
CV 30,6,7
M 39
CV 31,8,140
AR 6,137
AR 7,161
AR 8,185
M 40
EF 6,12
EF 7,13
EF 8,46
EF 141,47
M 41
CV 32,6,7
CV 33,8,141
AR 6,138
AR 7,162
M 42
AR 8,186
EF 6,12
EF 7,13
EF 8,46
M 43
EF 142,47
CV 34,6,7
CV 35,8,142
AR 6,139
M 44
AR 7,163
AR 8,187
EF 6,12
EF 7,13
M 45
FR
EF 8,46
EF 143,47
CV 36,6,7
M 46
CV 37,8,143
M 47
ds_read_b64 v[12:13], v4 offset:4608
ds_read_b64 v[46:47], v4 offset:4616
M 58
EF 160,52
EF 184,53
M 59
EF 208,54
EF 232,55
M 56
ST 24,896
CV 38,160,184
M 57
CV 39,208,232
EF 161,52
M 54
EF 185,53
EF 209,54
M 55
WL 2
FQ 176
EF 233,55
M 52
CV 40,161,185
CV 41,209,233
M 53
EF 162,52
EF 186,53
v_mfma_f32_16x16x32_f16 a[136:139], v[64:67], a[0:3], a[32:35]
FZ 176
R 24
M 51
mw 72
R 26
v_mfma_f32_16x16x32_f16 a[132:135], v[56:59], a[0:3], a[32:35]
TY 106,5128
TY 110,18952
M 49
R 28
v_mfma_f32_16x16x32_f16 a[156:159], v[56:59], a[8:11], a[32:35]
ad 84,144
W 0
M 61
EF 210,54
EF 234,55
v_mfma_f32_16x16x32_f16 a[160:163], v[64:67], a[8:11], a[32:35]
ST 26,1664
CV 42,162,186
M 63
W 1
CV 43,210,234
M 64
EF 163,52
EF 187,53
M 65
EF 211,54
EF 235,55
M 66
W 2
CV 44,163,187
M 67
WL 3
AN 104,106
AN 105,107
M 68
SH 106
SH 107
M 69
mw 75
AN 106,106
AN 107,107
M 70
WL 2
AN 108,110
AN 109,111
M 71
R 30
M 82
SH 110
SH 111
M 83
AN 110,110
AN 111,111
M 80
TY 114,6664
TY 118,20488
ST 28,2432
M 81
ad 84,150
W 0
M 78
CV 45,211,235
M 79
EF 164,52
EF 188,53
EF 212,54
EF 236,55
M 76
W 1
CV 14,164,188
CV 15,212,236
EF 165,52
M 77
EF 189,53
EF 213,54
EF 237,55
CV 16,165,189
v_mfma_f32_16x16x32_f16 a[184:187], v[64:67], a[16:19], a[32:35]
CV 17,213,237
EF 166,52
EF 190,53
EF 214,54
M 75
W 2
EF 238,55
CV 18,166,190
CV 19,214,238
v_mfma_f32_16x16x32_f16 a[180:183], v[56:59], a[16:19], a[32:35]
EF 167,52
EF 191,53
EF 215,54
EF 239,55
M 73
WL 0
R 32
CV 20,167,191
CV 21,215,239
v_mfma_f32_16x16x32_f16 v[136:139], v[56:59], a[24:27], v[128:131]
mw 0
AN 112,114
AN 113,115
BV 52,4864
M 85
SH 114
SH 115
AR 6,44
v_mfma_f32_16x16x32_f16 v[140:143], v[64:67], a[24:27], v[128:131]
AN 114,114
AN 115,115
AR 7,68
M 87
AN 116,118
AN 117,119
AR 8,92
M 88
SH 118
SH 119
AR 9,116
M 89
AN 118,118
AN 119,119
EF 6,12
M 90
TY 122,8200
TY 126,22024
EF 7,13
M 91
ad 84,192
EF 8,46
M 92
W 0
ae 42,74,384
M 93
s_bfe_u32 s52, s50, 0x10007
v_mad_i32_i24 v50, v49, s52, v48
ST 30,128
EF 9,47
M 94
CV 22,6,7
CV 23,8,9
AR 6,45
M 95
R 34
AR 7,69
M 106
W 1
AR 8,93
AR 9,117
M 107
EF 6,12
EF 7,13
EF 8,46
M 104
EF 9,47
CV 24,6,7
CV 25,8,9
M 105
WL 3
AN 120,122
AN 121,123
AR 6,46
M 102
SH 122
SH 123
AR 7,70
M 103
AN 122,122
AN 123,123
AR 8,94
)"
R"(M 100
WL 2
AN 124,126
AN 125,127
AR 9,118
M 101
SH 126
SH 127
EF 6,12
v_mfma_f32_16x16x32_f16 v[164:167], v[64:67], a[0:3], v[128:131]
AN 126,126
AN 127,127
EF 7,13
M 99
W 2
ST 32,896
EF 8,46
v_mfma_f32_16x16x32_f16 v[160:163], v[56:59], a[0:3], v[128:131]
EF 9,47
CV 26,6,7
CV 27,8,9
M 97
R 48
AR 6,47
v_mfma_f32_16x16x32_f16 v[184:187], v[56:59], a[8:11], v[128:131]
mw 3
AR 7,71
AR 8,95
AR 9,119
M 109
ad 84,198
W 0
EF 6,12
v_mfma_f32_16x16x32_f16 v[188:191], v[64:67], a[8:11], v[128:131]
EF 7,13
EF 8,46
EF 9,47
M 111
CV 28,6,7
CV 29,8,9
M 112
W 1
AR 6,48
AR 7,72
M 113
AR 8,96
AR 9,120
EF 6,12
M 114
EF 7,13
EF 8,46
EF 9,47
M 115
W 2
CV 6,6,7
CV 7,8,9
M 116
AW 200,6
AW 201,7
AR 6,49
M 117
ST 34,1664
AR 7,73
AR 8,97
M 118
bc 49,3,66,60
AR 9,121
EF 6,12
M 119
WL 0
R 50
EF 7,13
v_mfma_f32_16x16x32_f16 v[208:211], v[56:59], a[16:19], v[128:131]
mh 0
EF 8,46
EF 9,47
CV 6,6,7
M 121
ad 66,640
H 0
CV 7,8,9
v_mfma_f32_16x16x32_f16 v[232:235], v[60:63], a[28:31], v[128:131]
AW 202,6
AW 203,7
AR 6,50
M 132
TX 58,4608
TX 62,18432
AR 7,74
v_mfma_f32_16x16x32_f16 v[236:239], v[64:67], a[24:27], v[128:131]
DG 1024
AR 8,98
M 135
AR 9,122
EF 6,12
EF 7,13
v_mfma_f32_16x16x32_f16 v[212:215], v[68:71], a[20:23], v[128:131]
EF 8,46
EF 9,47
CV 6,6,7
M 122
TY 66,5120
TY 70,18944
CV 7,8,9
M 124
bc 48,3,64,56
AW 204,6
AW 205,7
M 125
mh 3
ST 36,2432
AR 6,51
AR 7,75
M 137
ad 64,640
H 0
AR 8,99
M 136
TX 74,6144
TX 78,19968
AR 9,123
M 138
WL 4
Q 0
EF 6,12
M 139
Q 2
EF 7,13
M 127
Q 4
EF 8,46
M 126
TY 82,6656
TY 86,20480
EF 9,47
M 128
Q 6
CV 6,6,7
M 129
WL 4
Q 8
CV 7,8,9
M 141
Q 10
AW 206,6
M 140
TX 90,7680
TX 94,21504
AW 207,7
M 142
Q 12
ds_read_b64 v[12:13], v4 offset:5120
M 143
R 54
ds_read_b64 v[46:47], v4 offset:5128
M 131
Q 14
M 130
TY 98,8192
TY 102,22016
M 0
R 52
M 1
WL 12
Q 16
M 13
Q 18
M 12
Q 20
M 14
Q 22
M 15
WL 10
Q 24
M 3
Q 26
M 2
Q 28
v_mfma_f32_16x16x32_f16 a[44:47], v[72:75], a[0:3], a[32:35]
Q 30
M 5
WL 7
Q 32
v_mfma_f32_16x16x32_f16 a[68:71], v[76:79], a[12:15], a[32:35]
Q 34
M 16
Q 36
v_mfma_f32_16x16x32_f16 a[72:75], v[80:83], a[8:11], a[32:35]
Q 38
M 19
WL 2
Q 40
v_mfma_f32_16x16x32_f16 a[48:51], v[84:87], a[4:7], a[32:35]
Q 42
M 6
Q 44
M 8
Q 46
M 9
H 1
ae 42,74,384
M 21
s_bitcmp1_b32 s50, 6
s_cselect_b32 s52, 24576, 0
v_add_u32 v50, s52, v3
ST 38,256
M 20
AR 6,140
AR 7,164
M 22
H 2
AR 8,188
M 23
R 58
M 11
EF 6,52
EF 7,53
M 10
WL 2
R 56
M 34
EF 8,54
EF 144,55
M 35
CV 30,6,7
CV 31,8,144
M 32
AR 6,141
AR 7,165
M 33
AR 8,189
EF 6,52
v_mfma_f32_16x16x32_f16 a[96:99], v[80:83], a[16:19], a[32:35]
EF 7,53
EF 8,54
M 31
EF 145,55
CV 32,6,7
v_mfma_f32_16x16x32_f16 a[92:95], v[72:75], a[16:19], a[32:35]
ST 40,1024
CV 33,8,145
M 29
AR 6,142
AR 7,166
M 26
AR 8,190
EF 6,52
M 27
WV 9
EF 7,53
EF 8,54
M 24
WL 0
FP
EF 146,55
M 25
CV 34,6,7
CV 35,8,146
M 36
AR 6,143
AR 7,167
M 37
AR 8,191
EF 6,52
M 38
EF 7,53
EF 8,54
M 39
EF 147,55
CV 36,6,7
v_mfma_f32_16x16x32_f16 a[116:119], v[72:75], a[24:27], a[32:35]
CV 37,8,147
M 41
AR 6,144
AR 7,168
AR 8,192
EF 6,52
EF 7,53
EF 8,54
v_mfma_f32_16x16x32_f16 a[120:123], v[80:83], a[24:27], a[32:35]
ST 42,1792
EF 148,55
CV 6,6,7
CV 7,8,148
AW 208,6
AW 209,7
M 43
AR 6,145
AR 7,169
AR 8,193
EF 6,52
EF 7,53
EF 8,54
M 44
EF 149,55
CV 6,6,7
CV 7,8,149
AW 210,6
AW 211,7
AR 6,146
M 45
FR
AR 7,170
AR 8,194
EF 6,52
EF 7,53
EF 8,54
M 46
EF 150,55
CV 6,6,7
CV 7,8,150
AW 212,6
AW 213,7
AR 6,147
M 47
AR 7,171
AR 8,195
EF 6,52
EF 7,53
EF 8,54
EF 151,55
M 58
CV 6,6,7
CV 7,8,151
AW 214,6
AW 215,7
M 59
BV 52,4608
EF 168,12
EF 192,13
M 56
EF 216,46
EF 240,47
CV 6,168,192
M 57
CV 7,216,240
AW 216,6
AW 217,7
v_mfma_f32_16x16x32_f16 a[144:147], v[80:83], a[0:3], a[32:35]
EF 169,12
EF 193,13
EF 217,46
M 55
WL 1
FQ 180
EF 241,47
CV 6,169,193
v_mfma_f32_16x16x32_f16 a[140:143], v[72:75], a[0:3], a[32:35]
ST 44,2560
CV 7,217,241
AW 218,6
M 53
AW 219,7
EF 170,12
EF 194,13
M 50
FZ 180
R 72
EF 218,46
M 51
mw 24
R 74
EF 242,47
M 48
TY 106,9736
TY 110,23560
CV 6,170,194
M 49
R 76
CV 7,218,242
M 60
ad 84,240
W 0
AW 220,6
M 61
AW 221,7
EF 171,12
EF 195,13
M 62
EF 219,46
EF 243,47
CV 6,171,195
M 63
W 1
CV 7,219,243
AW 222,6
v_mfma_f32_16x16x32_f16 a[164:167], v[72:75], a[8:11], a[32:35]
AW 223,7
M 65
EF 172,12
EF 196,13
EF 220,46
v_mfma_f32_16x16x32_f16 a[168:171], v[80:83], a[8:11], a[32:35]
WL 3
W 2
ae 42,74,384
M 67
s_bfe_u32 s52, s50, 0x10007
AN 104,106
AN 105,107
)"
R"(v_mad_i32_i24 v50, v49, s52, v48
M 68
SH 106
SH 107
ST 14,256
M 69
mw 27
AN 106,106
AN 107,107
EF 244,47
M 70
WL 0
AN 108,110
AN 109,111
CV 38,172,196
M 71
R 78
CV 39,220,244
M 82
SH 110
SH 111
EF 173,12
M 83
AN 110,110
AN 111,111
EF 197,13
M 80
TY 114,11272
TY 118,25096
EF 221,46
M 81
ad 84,246
W 0
EF 245,47
v_mfma_f32_16x16x32_f16 a[192:195], v[80:83], a[16:19], a[32:35]
CV 40,173,197
CV 41,221,245
EF 174,12
M 79
EF 198,13
EF 222,46
EF 246,47
v_mfma_f32_16x16x32_f16 a[188:191], v[72:75], a[16:19], a[32:35]
W 1
CV 42,174,198
CV 43,222,246
M 77
EF 175,12
EF 199,13
EF 223,46
M 74
ST 16,1024
EF 247,47
CV 44,175,199
M 75
W 2
CV 45,223,247
M 72
ds_read_b64 v[12:13], v4 offset:4864
ds_read_b64 v[46:47], v4 offset:4872
AR 6,52
M 73
WL 2
R 80
AR 7,76
M 84
mw 48
AN 112,114
AN 113,115
AR 8,100
M 85
SH 114
SH 115
AR 9,124
M 86
AN 114,114
AN 115,115
EF 6,52
M 87
AN 116,118
AN 117,119
EF 7,53
v_mfma_f32_16x16x32_f16 v[144:147], v[72:75], a[24:27], v[128:131]
SH 118
SH 119
EF 8,54
M 89
AN 118,118
AN 119,119
EF 9,55
v_mfma_f32_16x16x32_f16 v[148:151], v[80:83], a[24:27], v[128:131]
TY 122,12808
TY 126,26632
CV 6,6,7
M 91
ad 84,288
CV 7,8,9
M 92
W 0
ST 18,1792
AW 224,6
M 93
AW 225,7
AR 6,53
AR 7,77
M 94
AR 8,101
AR 9,125
EF 6,52
M 95
R 82
EF 7,53
M 106
W 1
EF 8,54
EF 9,55
M 107
CV 6,6,7
CV 7,8,9
AW 226,6
M 104
AW 227,7
AR 6,54
AR 7,78
M 105
WL 3
AN 120,122
AN 121,123
AR 8,102
v_mfma_f32_16x16x32_f16 v[172:175], v[80:83], a[0:3], v[128:131]
SH 122
SH 123
AR 9,126
M 103
AN 122,122
AN 123,123
EF 6,52
v_mfma_f32_16x16x32_f16 v[168:171], v[72:75], a[0:3], v[128:131]
WL 2
AN 124,126
AN 125,127
EF 7,53
M 101
SH 126
SH 127
EF 8,54
M 98
AN 126,126
AN 127,127
ST 20,2560
M 99
W 2
EF 9,55
CV 6,6,7
M 96
CV 7,8,9
AW 228,6
AW 229,7
M 97
R 0
AR 6,55
M 108
mw 51
AR 7,79
AR 8,103
AR 9,127
M 109
ad 84,294
W 0
EF 6,52
M 110
EF 7,53
EF 8,54
EF 9,55
M 111
CV 6,6,7
CV 7,8,9
AW 230,6
v_mfma_f32_16x16x32_f16 v[192:195], v[72:75], a[8:11], v[128:131]
W 1
AW 231,7
M 113
AR 6,56
AR 7,80
AR 8,104
v_mfma_f32_16x16x32_f16 v[196:199], v[80:83], a[8:11], v[128:131]
AR 9,128
EF 6,52
EF 7,53
M 115
W 2
EF 8,54
EF 9,55
M 116
ae 42,72,384
CV 14,6,7
CV 15,8,9
M 117
s_bitcmp1_b32 s50, 8
s_cselect_b32 s52, 24576, 0
v_add_u32 v50, s52, v3
ST 22,0
M 118
bc 49,4,66,60
AR 6,57
AR 7,81
M 119
WL 0
R 2
AR 8,105
M 120
mh 9
AR 9,129
EF 6,52
EF 7,53
M 121
ad 66,832
H 0
EF 8,54
M 133
EF 9,55
CV 16,6,7
CV 17,8,9
M 132
TX 58,9216
TX 62,23040
AR 6,58
M 134
DG 1024
AR 7,82
M 135
AR 8,106
AR 9,130
EF 6,52
M 123
EF 7,53
EF 8,54
EF 9,55
M 122
TY 66,9728
TY 70,23552
CV 18,6,7
v_mfma_f32_16x16x32_f16 v[216:219], v[72:75], a[16:19], v[128:131]
bc 48,4,64,56
CV 19,8,9
AR 6,59
M 125
mh 12
ST 24,768
AR 7,83
AR 8,107
v_mfma_f32_16x16x32_f16 v[240:243], v[76:79], a[28:31], v[128:131]
ad 64,832
H 0
AR 9,131
M 136
TX 74,10752
TX 78,24576
EF 6,52
v_mfma_f32_16x16x32_f16 v[244:247], v[80:83], a[24:27], v[128:131]
WL 4
Q 0
EF 7,53
M 139
Q 2
EF 8,54
v_mfma_f32_16x16x32_f16 v[220:223], v[84:87], a[20:23], v[128:131]
Q 4
EF 9,55
M 126
TY 82,11264
TY 86,25088
CV 20,6,7
M 128
Q 6
CV 21,8,9
M 129
WL 4
Q 8
BV 52,5120
M 141
Q 10
M 140
TX 90,12288
TX 94,26112
M 142
Q 12
M 143
R 6
ST 26,1536
M 131
Q 14
M 130
TY 98,12800
TY 102,26624
M 0
R 4
M 1
WL 11
Q 16
M 13
Q 18
M 12
Q 20
M 14
Q 22
M 15
WL 9
Q 24
M 3
Q 26
M 2
Q 28
M 4
Q 30
M 5
WL 6
Q 32
ST 28,2304
M 17
Q 34
M 16
Q 36
M 18
Q 38
M 19
WL 1
Q 40
M 7
Q 42
M 6
Q 44
v_mfma_f32_16x16x32_f16 a[52:55], v[88:91], a[0:3], a[32:35]
Q 46
M 9
H 1
AR 6,148
v_mfma_f32_16x16x32_f16 a[76:79], v[92:95], a[12:15], a[32:35]
AR 7,172
AR 8,196
M 20
EF 6,12
EF 7,13
v_mfma_f32_16x16x32_f16 a[80:83], v[96:99], a[8:11], a[32:35]
H 2
EF 8,46
M 23
R 10
v_mfma_f32_16x16x32_f16 a[56:59], v[100:103], a[4:7], a[32:35]
ae 42,72,384
ST 30,128
M 10
WL 2
R 8
PA 12288
DF
v_mfma_f32_16x16x32_f16 a[104:107], v[96:99], a[16:19], a[32:35]
EF 152,47
CV 6,6,7
M 35
CV 7,8,152
AW 232,6
v_mfma_f32_16x16x32_f16 a[100:103], v[88:91], a[16:19], a[32:35]
AW 233,7
AR 6,149
M 33
AR 7,173
AR 8,197
M 30
EF 6,12
EF 7,13
M 31
EF 8,46
EF 153,47
M 28
CV 6,6,7
CV 7,8,153
M 29
AW 234,6
AW 235,7
M 26
AR 6,150
AR 7,174
M 27
WV 11
AR 8,198
EF 6,12
M 24
WL 0
FP
ST 32,896
M 25
EF 7,13
EF 8,46
M 36
EF 154,47
CV 6,6,7
M 37
CV 7,8,154
AW 236,6
)"
R"(M 38
AW 237,7
AR 6,151
M 39
AR 7,175
AR 8,199
M 40
EF 6,12
EF 7,13
M 41
EF 8,46
EF 155,47
M 42
CV 6,6,7
CV 7,8,155
M 43
AW 238,6
AW 239,7
v_mfma_f32_16x16x32_f16 a[124:127], v[88:91], a[24:27], a[32:35]
AR 6,152
AR 7,176
EF 6,12
EF 7,13
EF 132,46
EF 156,47
CV 22,6,7
CV 23,132,156
AR 6,153
AR 7,177
M 45
FR
EF 6,12
EF 7,13
EF 133,46
EF 157,47
CV 24,6,7
CV 25,133,157
AR 6,154
AR 7,178
EF 6,12
v_mfma_f32_16x16x32_f16 a[128:131], v[96:99], a[24:27], a[32:35]
ST 34,1664
EF 7,13
EF 134,46
EF 158,47
CV 26,6,7
CV 27,134,158
AR 6,155
AR 7,179
EF 6,12
EF 7,13
M 47
EF 135,46
EF 159,47
CV 28,6,7
CV 29,135,159
v_mfma_f32_16x16x32_f16 a[152:155], v[96:99], a[0:3], a[32:35]
EF 176,52
EF 200,53
M 59
EF 224,54
EF 248,55
v_mfma_f32_16x16x32_f16 a[148:151], v[88:91], a[0:3], a[32:35]
CV 6,176,200
CV 7,224,248
M 57
AW 240,6
AW 241,7
M 54
EF 177,52
EF 201,53
M 55
WL 0
FQ 184
EF 225,54
M 52
EF 249,55
CV 6,177,201
M 53
CV 7,225,249
AW 242,6
M 50
FZ 184
R 24
M 51
mw 72
R 26
M 48
TY 106,520
TY 110,14344
ST 36,2432
M 49
R 28
M 60
ad 84,336
W 0
M 61
AW 243,7
EF 178,52
M 62
EF 202,53
EF 226,54
M 63
W 1
EF 250,55
M 64
CV 6,178,202
CV 7,226,250
M 65
AW 244,6
AW 245,7
M 66
W 2
EF 179,52
M 67
WL 3
AN 104,106
AN 105,107
v_mfma_f32_16x16x32_f16 a[172:175], v[88:91], a[8:11], a[32:35]
SH 106
SH 107
M 69
mw 75
AN 106,106
AN 107,107
v_mfma_f32_16x16x32_f16 a[176:179], v[96:99], a[8:11], a[32:35]
WL 2
AN 108,110
AN 109,111
M 71
R 30
v_mfma_f32_16x16x32_f16 v[132:135], v[96:99], a[16:19], v[128:131]
SH 110
SH 111
M 83
AN 110,110
AN 111,111
v_mfma_f32_16x16x32_f16 a[196:199], v[88:91], a[16:19], a[32:35]
TY 114,2056
TY 118,15880
M 81
ad 84,342
W 0
M 78
ae 42,72,384
EF 203,53
M 79
s_bfe_u32 s52, s50, 0x10009
v_mad_i32_i24 v50, v49, s52, v48
ST 38,256
M 76
W 1
EF 227,54
M 77
EF 251,55
CV 6,179,203
M 74
CV 7,227,251
AW 246,6
M 75
W 2
AW 247,7
M 72
EF 180,52
EF 204,53
EF 228,54
EF 252,55
M 73
WL 1
R 32
CV 6,180,204
CV 7,228,252
M 84
mw 0
AN 112,114
AN 113,115
AW 248,6
AW 249,7
M 85
SH 114
SH 115
EF 181,52
EF 205,53
M 86
AN 114,114
AN 115,115
EF 229,54
EF 253,55
M 87
WL 2
AN 116,118
AN 117,119
CV 6,181,205
CV 7,229,253
M 88
SH 118
SH 119
AW 250,6
AW 251,7
M 89
AN 118,118
AN 119,119
ST 40,1024
EF 182,52
M 90
TY 122,3592
TY 126,17416
EF 206,53
EF 230,54
M 91
ad 84,384
EF 254,55
CV 6,182,206
v_mfma_f32_16x16x32_f16 v[152:155], v[88:91], a[24:27], v[128:131]
W 0
CV 7,230,254
AW 252,6
AW 253,7
M 93
EF 183,52
EF 207,53
EF 231,54
EF 255,55
v_mfma_f32_16x16x32_f16 v[156:159], v[96:99], a[24:27], v[128:131]
CV 6,183,207
CV 7,231,255
AW 254,6
AW 255,7
M 95
R 34
v_mfma_f32_16x16x32_f16 v[180:183], v[96:99], a[0:3], v[128:131]
W 1
M 107
v_mfma_f32_16x16x32_f16 v[176:179], v[88:91], a[0:3], v[128:131]
M 105
WL 3
AN 120,122
AN 121,123
M 102
SH 122
SH 123
M 103
AN 122,122
AN 123,123
ST 42,1792
M 100
WL 2
AN 124,126
AN 125,127
M 101
SH 126
SH 127
M 98
AN 126,126
AN 127,127
M 99
W 2
M 96
M 97
R 48
M 108
mw 3
M 109
ad 84,390
W 0
M 110
M 111
M 112
W 1
M 113
ST 44,2560
M 114
M 115
W 2
v_mfma_f32_16x16x32_f16 v[200:203], v[88:91], a[8:11], v[128:131]
M 117
v_mfma_f32_16x16x32_f16 v[204:207], v[96:99], a[8:11], v[128:131]
bc 49,5,66,60
M 119
WL 2
R 50
M 120
mh 18
M 121
ad 66,1024
H 0
M 133
M 132
TX 58,0
TX 62,13824
M 134
DG 1024
M 135
ae 42,72,480
M 123
s_bfe_u32 s52, s50, 0x1000b
v_mad_i32_i24 v50, v49, s52, v48
ST 14,0
M 122
TY 66,512
TY 70,14336
M 124
bc 48,5,64,56
M 125
mh 21
M 137
ad 64,1024
H 0
M 136
TX 74,1536
TX 78,15360
M 138
WL 4
Q 0
M 139
Q 2
M 127
Q 4
M 126
TY 82,2048
TY 86,15872
v_mfma_f32_16x16x32_f16 v[224:227], v[88:91], a[16:19], v[128:131]
Q 6
M 129
WL 4
Q 8
v_mfma_f32_16x16x32_f16 v[248:251], v[92:95], a[28:31], v[128:131]
Q 10
ST 16,768
M 140
TX 90,3072
TX 94,16896
v_mfma_f32_16x16x32_f16 v[252:255], v[96:99], a[24:27], v[128:131]
Q 12
M 143
R 54
v_mfma_f32_16x16x32_f16 v[228:231], v[100:103], a[20:23], v[128:131]
Q 14
M 130
TY 98,3584
TY 102,17408
M 0
R 52
M 1
WL 10
Q 16
M 13
Q 18
M 12
Q 20
M 14
Q 22
M 15
WL 8
Q 24
M 3
Q 26
ST 18,1536
M 2
Q 28
M 4
Q 30
M 5
WL 6
Q 32
M 17
Q 34
M 16
Q 36
M 18
Q 38
M 19
WL 1
Q 40
M 7
Q 42
M 6
Q 44
M 8
Q 46
M 9
H 1
M 21
ST 20,2304
M 20
M 22
H 2
M 23
R 58
M 11
M 10
WL 2
R 56
M 34
M 35
M 32
M 33
M 30
M 31
M 28
)"
R"(ae 42,72,480
ST 22,128
M 29
M 26
M 27
WV 10
M 24
WL 0
FP
M 25
M 36
M 37
M 38
M 39
M 40
M 41
M 42
ST 24,896
M 43
M 44
M 45
FR
M 46
M 47
M 58
M 59
M 56
M 57
M 54
M 55
WL 0
FQ 188
M 52
ST 26,1664
M 53
M 50
FZ 188
R 72
M 51
mw 24
R 74
M 48
TY 106,5128
TY 110,18952
M 49
R 76
M 60
ad 84,432
W 0
M 61
M 62
M 63
W 1
M 64
M 65
M 66
W 2
ST 28,2432
M 67
WL 3
AN 104,106
AN 105,107
M 68
SH 106
SH 107
M 69
mw 27
AN 106,106
AN 107,107
M 70
WL 2
AN 108,110
AN 109,111
M 71
R 78
M 82
SH 110
SH 111
M 83
AN 110,110
AN 111,111
M 80
TY 114,6664
TY 118,20488
M 81
ad 84,438
W 0
M 78
M 79
M 76
W 1
M 77
M 74
M 75
W 2
M 72
M 73
R 80
M 84
mw 48
WL 3
AN 112,114
AN 113,115
M 85
SH 114
SH 115
M 86
AN 114,114
AN 115,115
M 87
WL 2
AN 116,118
AN 117,119
M 88
SH 118
SH 119
M 89
AN 118,118
AN 119,119
M 90
TY 122,8200
TY 126,22024
M 91
ad 84,480
M 92
W 0
M 93
M 94
M 95
R 82
M 106
W 1
M 107
M 104
M 105
WL 3
AN 120,122
AN 121,123
M 102
SH 122
SH 123
M 103
AN 122,122
AN 123,123
M 100
WL 2
AN 124,126
AN 125,127
M 101
SH 126
SH 127
M 98
AN 126,126
AN 127,127
M 99
W 2
M 96
M 97
R 0
M 108
mw 51
M 109
ad 84,486
W 0
M 110
M 111
M 112
W 1
M 113
M 114
M 115
W 2
M 116
M 117
M 118
bc 49,6,66,60
M 119
WL 2
R 2
M 120
mh 0
M 121
ad 66,1216
H 0
M 133
M 132
TX 58,4608
TX 62,18432
M 134
DG 1024
M 135
M 123
M 122
TY 66,5120
TY 70,18944
M 124
bc 48,6,64,56
M 125
mh 3
ae 44,70,768
M 137
ad 64,1216
H 0
M 136
TX 74,6144
TX 78,19968
M 138
WL 4
Q 0
M 139
Q 2
M 127
Q 4
M 126
TY 82,6656
TY 86,20480
M 128
Q 6
M 129
WL 4
Q 8
M 141
Q 10
M 140
TX 90,7680
TX 94,21504
M 142
Q 12
M 143
R 6
M 131
Q 14
M 130
TY 98,8192
TY 102,22016
M 0
R 4
M 1
WL 10
Q 16
M 13
Q 18
M 12
Q 20
M 14
Q 22
M 15
WL 8
Q 24
M 3
Q 26
M 2
Q 28
M 4
Q 30
M 5
WL 6
Q 32
M 17
Q 34
M 16
Q 36
M 18
Q 38
M 19
WL 2
Q 40
M 7
Q 42
M 6
Q 44
M 8
Q 46
M 9
H 1
M 21
ae 42,72,384
M 20
s_bfe_u32 s52, s50, 0x10009
v_mad_i32_i24 v50, v49, s52, v48
SA 200,0
M 22
H 2
M 23
R 10
M 11
s_bitcmp1_b32 s50, 12
s_cselect_b32 s51, 24576, 0
M 10
WL 2
R 8
PA 12288
DF
M 34
v_add_u32 v47, s51, v3
XL 30,0
M 35
XL 32,768
M 32
XL 34,1536
M 33
XL 36,2304
M 30
M 31
M 28
M 29
M 26
M 27
WV 11
M 24
WL 0
FP
M 25
M 36
M 37
M 38
M 39
M 40
M 41
M 42
M 43
M 44
M 45
FR
M 46
M 47
M 58
M 59
M 56
M 57
M 54
M 55
WL 0
FQ 192
M 52
M 53
M 50
FZ 192
R 24
M 51
mw 72
R 26
M 48
TY 106,9736
TY 110,23560
M 49
R 28
M 60
ad 84,528
W 0
M 61
M 62
M 63
W 1
M 64
M 65
M 66
W 2
M 67
WL 3
AN 104,106
AN 105,107
M 68
SH 106
SH 107
M 69
mw 75
AN 106,106
AN 107,107
M 70
WL 2
AN 108,110
AN 109,111
M 71
R 30
M 82
SH 110
SH 111
M 83
AN 110,110
AN 111,111
M 80
TY 114,11272
TY 118,25096
M 81
ad 84,534
W 0
M 78
M 79
M 76
W 1
M 77
M 74
M 75
W 2
M 72
M 73
R 32
M 84
mw 0
)"
R"(WL 3
AN 112,114
AN 113,115
M 85
SH 114
SH 115
M 86
AN 114,114
AN 115,115
M 87
WL 2
AN 116,118
AN 117,119
M 88
SH 118
SH 119
M 89
AN 118,118
AN 119,119
M 90
TY 122,12808
TY 126,26632
M 91
ad 84,576
M 92
W 0
SA 202,768
M 93
M 94
M 95
R 34
M 106
W 1
M 107
M 104
M 105
WL 3
AN 120,122
AN 121,123
M 102
SH 122
SH 123
M 103
AN 122,122
AN 123,123
M 100
WL 2
AN 124,126
AN 125,127
M 101
SH 126
SH 127
M 98
AN 126,126
AN 127,127
M 99
W 2
M 96
M 97
R 48
M 108
mw 3
M 109
ad 84,582
W 0
M 110
M 111
M 112
W 1
M 113
M 114
M 115
W 2
M 116
M 117
M 118
bc 49,7,66,60
M 119
WL 2
R 50
M 120
mh 9
M 121
ad 66,1408
H 0
M 133
M 132
TX 58,9216
TX 62,23040
M 134
DG 1024
M 135
M 123
M 122
TY 66,9728
TY 70,23552
M 124
bc 48,7,64,56
M 125
mh 12
M 137
ad 64,1408
H 0
M 136
TX 74,10752
TX 78,24576
M 138
WL 4
Q 0
M 139
Q 2
M 127
Q 4
M 126
TY 82,11264
TY 86,25088
M 128
Q 6
M 129
WL 4
Q 8
M 141
Q 10
M 140
TX 90,12288
TX 94,26112
M 142
Q 12
M 143
R 54
M 131
Q 14
M 130
TY 98,12800
TY 102,26624
M 0
R 52
M 1
WL 10
Q 16
M 13
Q 18
M 12
Q 20
M 14
Q 22
M 15
WL 8
Q 24
M 3
Q 26
M 2
Q 28
M 4
Q 30
M 5
WL 6
Q 32
M 17
Q 34
M 16
Q 36
M 18
Q 38
M 19
WL 2
Q 40
M 7
Q 42
M 6
Q 44
M 8
Q 46
M 9
H 1
SA 204,1536
M 21
ae 44,70,768
M 20
s_bfe_u32 s51, s50, 0x1000d
v_mad_i32_i24 v47, v49, s51, v48
XL 38,0
M 22
H 2
XL 40,768
M 23
R 58
M 11
XL 42,1536
M 10
WL 2
R 56
M 34
XL 44,2304
M 35
M 32
M 33
M 30
M 31
M 28
M 29
M 26
M 27
WV 10
M 24
WL 0
FP
M 25
M 36
M 37
M 38
M 39
M 40
UL 6,30
UH 7,30
M 41
AR 8,36
AR 9,60
M 42
AD 8,6
AD 9,7
M 43
AW 36,8
AW 60,9
M 44
UL 6,31
UH 7,31
M 45
FR
AR 8,84
M 46
AR 9,108
AD 8,6
M 47
AD 9,7
AW 84,8
M 58
AW 108,9
UL 6,32
M 59
UH 7,32
AR 8,37
M 56
AR 9,61
AD 8,6
M 57
AD 9,7
AW 37,8
M 54
AW 61,9
UL 6,33
M 55
WL 0
FQ 196
UH 7,33
M 52
AR 8,85
AR 9,109
M 53
AD 8,6
AD 9,7
M 50
FZ 196
R 72
M 51
mw 24
R 74
M 48
TY 106,520
TY 110,14344
M 49
R 76
M 60
ad 84,624
W 0
M 61
AW 85,8
AW 109,9
M 62
UL 6,34
UH 7,34
M 63
W 1
AR 8,38
M 64
AR 9,62
AD 8,6
M 65
AD 9,7
AW 38,8
M 66
W 2
AW 62,9
M 67
WL 3
AN 104,106
AN 105,107
M 68
SH 106
SH 107
M 69
mw 27
AN 106,106
AN 107,107
M 70
WL 2
AN 108,110
AN 109,111
M 71
R 78
M 82
SH 110
SH 111
M 83
AN 110,110
AN 111,111
M 80
TY 114,2056
TY 118,15880
M 81
ad 84,630
W 0
M 78
UL 6,35
UH 7,35
M 79
AR 8,86
AR 9,110
M 76
W 1
AD 8,6
M 77
AD 9,7
AW 86,8
M 74
AW 110,9
UL 6,36
M 75
W 2
SA 206,2304
M 72
UH 7,36
AR 8,39
M 73
WL 1
R 80
M 84
mw 48
AN 112,114
AN 113,115
M 85
SH 114
SH 115
M 86
AN 114,114
AN 115,115
M 87
WL 2
AN 116,118
AN 117,119
M 88
SH 118
SH 119
M 89
AN 118,118
AN 119,119
M 90
TY 122,3592
TY 126,17416
M 91
ad 84,672
M 92
W 0
AR 9,63
M 93
AD 8,6
AD 9,7
M 94
AW 39,8
AW 63,9
M 95
R 82
M 106
W 1
UL 6,37
M 107
UH 7,37
AR 8,87
M 104
AR 9,111
AD 8,6
M 105
WL 3
AN 120,122
AN 121,123
M 102
SH 122
SH 123
M 103
AN 122,122
)"
R"(AN 123,123
M 100
WL 2
AN 124,126
AN 125,127
M 101
SH 126
SH 127
M 98
AN 126,126
AN 127,127
M 99
W 2
AD 9,7
M 96
AW 87,8
AW 111,9
M 97
R 0
M 108
mw 51
M 109
ad 84,678
W 0
M 110
M 111
M 112
W 1
M 113
M 114
M 115
W 2
M 116
M 117
M 118
bc 49,8,66,60
M 119
WL 2
R 2
M 120
mh 18
M 121
ad 66,1600
H 0
M 133
M 132
TX 58,0
TX 62,13824
M 134
DG 1024
M 135
M 123
M 122
TY 66,512
TY 70,14336
M 124
bc 48,8,64,56
M 125
mh 21
M 137
ad 64,1600
H 0
M 136
TX 74,1536
TX 78,15360
M 138
WL 4
Q 0
M 139
Q 2
M 127
Q 4
M 126
TY 82,2048
TY 86,15872
M 128
Q 6
M 129
WL 4
Q 8
M 141
Q 10
M 140
TX 90,3072
TX 94,16896
M 142
Q 12
M 143
R 6
M 131
Q 14
M 130
TY 98,3584
TY 102,17408
M 0
R 4
M 1
WL 10
Q 16
M 13
Q 18
M 12
Q 20
M 14
Q 22
M 15
WL 8
Q 24
M 3
Q 26
M 2
Q 28
M 4
Q 30
M 5
WL 6
Q 32
M 17
Q 34
M 16
Q 36
M 18
Q 38
M 19
WL 2
Q 40
M 7
Q 42
M 6
Q 44
M 8
Q 46
M 9
H 1
M 21
ae 42,72,384
M 20
SA 208,128
M 22
H 2
M 23
R 10
M 11
ae 44,68,768
M 10
WL 2
R 8
PA 12288
DF
M 34
s_bitcmp1_b32 s50, 14
s_cselect_b32 s51, 24576, 0
v_add_u32 v47, s51, v3
M 35
XL 14,0
M 32
XL 16,768
M 33
XL 18,1536
M 30
XL 20,2304
M 31
M 28
M 29
M 26
M 27
WV 11
M 24
WL 0
FP
M 25
M 36
M 37
M 38
M 39
M 40
M 41
M 42
UL 6,38
UH 7,38
M 43
AR 8,40
AR 9,64
M 44
AD 8,6
AD 9,7
M 45
FR
AW 40,8
M 46
AW 64,9
UL 6,39
M 47
UH 7,39
AR 8,88
M 58
AR 9,112
AD 8,6
M 59
AD 9,7
AW 88,8
M 56
AW 112,9
UL 6,40
M 57
UH 7,40
AR 8,41
M 54
AR 9,65
AD 8,6
M 55
WL 0
FQ 200
AD 9,7
M 52
AW 41,8
AW 65,9
M 53
UL 6,41
UH 7,41
M 50
FZ 200
R 24
M 51
mw 72
R 26
M 48
TY 106,5128
TY 110,18952
M 49
R 28
M 60
ad 84,720
W 0
M 61
AR 8,89
AR 9,113
M 62
AD 8,6
AD 9,7
M 63
W 1
AW 89,8
M 64
AW 113,9
UL 6,42
M 65
UH 7,42
AR 8,42
M 66
W 2
AR 9,66
M 67
WL 3
AN 104,106
AN 105,107
M 68
SH 106
SH 107
M 69
mw 75
AN 106,106
AN 107,107
M 70
WL 2
AN 108,110
AN 109,111
M 71
R 30
M 82
SH 110
SH 111
M 83
AN 110,110
AN 111,111
M 80
TY 114,6664
TY 118,20488
M 81
ad 84,726
W 0
M 78
AD 8,6
AD 9,7
M 79
AW 42,8
AW 66,9
M 76
W 1
UL 6,43
M 77
UH 7,43
AR 8,90
M 74
AR 9,114
AD 8,6
M 75
W 2
AD 9,7
M 72
AW 90,8
AW 114,9
M 73
WL 1
R 32
M 84
mw 0
AN 112,114
AN 113,115
M 85
SH 114
SH 115
M 86
AN 114,114
AN 115,115
M 87
WL 2
AN 116,118
AN 117,119
M 88
SH 118
SH 119
M 89
AN 118,118
AN 119,119
M 90
TY 122,8200
TY 126,22024
M 91
ad 84,768
M 92
W 0
SA 210,896
M 93
UL 6,44
UH 7,44
M 94
AR 8,43
AR 9,67
M 95
R 34
M 106
W 1
AD 8,6
M 107
AD 9,7
AW 43,8
M 104
AW 67,9
UL 6,45
M 105
WL 3
AN 120,122
AN 121,123
M 102
SH 122
SH 123
M 103
AN 122,122
AN 123,123
M 100
WL 2
AN 124,126
AN 125,127
M 101
SH 126
SH 127
M 98
AN 126,126
AN 127,127
M 99
W 2
UH 7,45
M 96
AR 8,91
AR 9,115
M 97
R 48
M 108
mw 3
AD 8,6
AD 9,7
M 109
ad 84,774
W 0
M 110
AW 91,8
AW 115,9
M 111
M 112
W 1
M 113
M 114
M 115
W 2
M 116
M 117
M 118
bc 49,9,66,60
M 119
)"
R"(WL 2
R 50
M 120
mh 0
M 121
ad 66,1792
H 0
M 133
M 132
TX 58,4608
TX 62,18432
M 134
DG 1024
M 135
M 123
M 122
TY 66,5120
TY 70,18944
M 124
bc 48,9,64,56
M 125
mh 3
M 137
ad 64,1792
H 0
M 136
TX 74,6144
TX 78,19968
M 138
WL 4
Q 0
M 139
Q 2
M 127
Q 4
M 126
TY 82,6656
TY 86,20480
M 128
Q 6
M 129
WL 4
Q 8
M 141
Q 10
M 140
TX 90,7680
TX 94,21504
M 142
Q 12
M 143
R 54
M 131
Q 14
M 130
TY 98,8192
TY 102,22016
M 0
R 52
M 1
WL 10
Q 16
M 13
Q 18
M 12
Q 20
M 14
Q 22
M 15
WL 8
Q 24
M 3
Q 26
M 2
Q 28
M 4
Q 30
M 5
WL 6
Q 32
M 17
Q 34
M 16
Q 36
M 18
Q 38
M 19
WL 2
Q 40
M 7
Q 42
M 6
Q 44
M 8
Q 46
M 9
H 1
SA 212,1664
M 21
ae 44,68,768
M 20
s_bfe_u32 s51, s50, 0x1000f
v_mad_i32_i24 v47, v49, s51, v48
XL 22,0
M 22
H 2
XL 24,768
M 23
R 58
M 11
XL 26,1536
M 10
WL 2
R 56
M 34
XL 28,2304
M 35
M 32
M 33
M 30
M 31
M 28
M 29
M 26
M 27
WV 10
M 24
WL 0
FP
M 25
M 36
M 37
M 38
M 39
M 40
M 41
M 42
M 43
M 44
UL 6,14
UH 7,14
M 45
FR
AR 8,44
M 46
AR 9,68
AD 8,6
M 47
AD 9,7
AW 44,8
M 58
AW 68,9
UL 6,15
M 59
UH 7,15
AR 8,92
M 56
AR 9,116
AD 8,6
M 57
AD 9,7
AW 92,8
M 54
AW 116,9
UL 6,16
M 55
WL 0
FQ 204
UH 7,16
M 52
AR 8,45
AR 9,69
M 53
AD 8,6
AD 9,7
M 50
FZ 204
R 72
M 51
mw 24
R 74
M 48
TY 106,9736
TY 110,23560
M 49
R 76
M 60
ad 84,816
W 0
M 61
AW 45,8
AW 69,9
M 62
UL 6,17
UH 7,17
M 63
W 1
AR 8,93
M 64
AR 9,117
AD 8,6
M 65
AD 9,7
AW 93,8
M 66
W 2
AW 117,9
M 67
WL 3
AN 104,106
AN 105,107
M 68
SH 106
SH 107
M 69
mw 27
AN 106,106
AN 107,107
M 70
WL 2
AN 108,110
AN 109,111
M 71
R 78
M 82
SH 110
SH 111
M 83
AN 110,110
AN 111,111
M 80
TY 114,11272
TY 118,25096
M 81
ad 84,822
W 0
M 78
UL 6,18
UH 7,18
M 79
AR 8,46
AR 9,70
M 76
W 1
AD 8,6
M 77
AD 9,7
AW 46,8
M 74
AW 70,9
UL 6,19
M 75
W 2
SA 214,2432
M 72
UH 7,19
AR 8,94
M 73
WL 1
R 80
M 84
mw 48
AN 112,114
AN 113,115
M 85
SH 114
SH 115
M 86
AN 114,114
AN 115,115
M 87
WL 2
AN 116,118
AN 117,119
M 88
SH 118
SH 119
M 89
AN 118,118
AN 119,119
M 90
TY 122,12808
TY 126,26632
M 91
ad 84,864
M 92
W 0
AR 9,118
M 93
AD 8,6
AD 9,7
M 94
AW 94,8
AW 118,9
M 95
R 82
M 106
W 1
UL 6,20
M 107
UH 7,20
AR 8,47
M 104
AR 9,71
AD 8,6
M 105
WL 3
AN 120,122
AN 121,123
M 102
SH 122
SH 123
M 103
AN 122,122
AN 123,123
M 100
WL 2
AN 124,126
AN 125,127
M 101
SH 126
SH 127
M 98
AN 126,126
AN 127,127
M 99
W 2
AD 9,7
M 96
AW 47,8
AW 71,9
M 97
R 0
M 108
mw 51
UL 6,21
UH 7,21
M 109
ad 84,870
W 0
M 110
AR 8,95
AR 9,119
M 111
AD 8,6
AD 9,7
M 112
W 1
AW 95,8
M 113
AW 119,9
M 114
M 115
W 2
M 116
M 117
M 118
bc 49,10,66,60
M 119
WL 2
R 2
M 120
mh 9
M 121
ad 66,1984
H 0
M 133
M 132
TX 58,9216
TX 62,23040
M 134
DG 1024
M 135
M 123
M 122
TY 66,9728
TY 70,23552
M 124
bc 48,10,64,56
M 125
mh 12
M 137
ad 64,1984
H 0
M 136
TX 74,10752
TX 78,24576
M 138
WL 4
Q 0
M 139
Q 2
M 127
Q 4
M 126
TY 82,11264
TY 86,25088
M 128
Q 6
M 129
)"
R"(WL 4
Q 8
M 141
Q 10
M 140
TX 90,12288
TX 94,26112
M 142
Q 12
M 143
R 6
M 131
Q 14
M 130
TY 98,12800
TY 102,26624
M 0
R 4
M 1
WL 10
Q 16
M 13
Q 18
M 12
Q 20
M 14
Q 22
M 15
WL 8
Q 24
M 3
Q 26
M 2
Q 28
M 4
Q 30
M 5
WL 6
Q 32
M 17
Q 34
M 16
Q 36
M 18
Q 38
M 19
WL 2
Q 40
M 7
Q 42
M 6
Q 44
M 8
Q 46
M 9
H 1
M 21
ae 42,72,384
M 20
s_bitcmp1_b32 s50, 8
s_cselect_b32 s52, 24576, 0
v_add_u32 v50, s52, v3
M 22
H 2
SA 216,256
M 23
R 10
M 11
ae 44,68,864
M 10
WL 2
R 8
PA 12288
DF
M 34
s_bitcmp1_b32 s50, 16
s_cselect_b32 s51, 24576, 0
v_add_u32 v47, s51, v3
M 35
XL 30,0
M 32
XL 32,768
M 33
XL 34,1536
M 30
XL 36,2304
M 31
M 28
M 29
M 26
M 27
WV 11
M 24
WL 0
FP
M 25
M 36
M 37
M 38
M 39
M 40
M 41
M 42
M 43
M 44
M 45
FR
M 46
UL 6,22
UH 7,22
M 47
AR 8,48
AR 9,72
M 58
AD 8,6
AD 9,7
M 59
AW 48,8
AW 72,9
M 56
UL 6,23
UH 7,23
M 57
AR 8,96
AR 9,120
M 54
AD 8,6
AD 9,7
M 55
WL 0
FQ 208
AW 96,8
M 52
AW 120,9
UL 6,24
M 53
UH 7,24
AR 8,49
M 50
FZ 208
R 24
M 51
mw 72
R 26
M 48
TY 106,520
TY 110,14344
M 49
R 28
M 60
ad 84,912
W 0
M 61
AR 9,73
AD 8,6
M 62
AD 9,7
AW 49,8
M 63
W 1
AW 73,9
M 64
UL 6,25
UH 7,25
M 65
AR 8,97
AR 9,121
M 66
W 2
AD 8,6
M 67
WL 3
AN 104,106
AN 105,107
M 68
SH 106
SH 107
M 69
mw 75
AN 106,106
AN 107,107
M 70
WL 2
AN 108,110
AN 109,111
M 71
R 30
M 82
SH 110
SH 111
M 83
AN 110,110
AN 111,111
M 80
TY 114,2056
TY 118,15880
M 81
ad 84,918
W 0
M 78
AD 9,7
AW 97,8
M 79
AW 121,9
UL 6,26
M 76
W 1
UH 7,26
M 77
AR 8,50
AR 9,74
M 74
AD 8,6
AD 9,7
M 75
W 2
AW 50,8
M 72
AW 74,9
UL 6,27
M 73
WL 1
R 32
M 84
mw 0
AN 112,114
AN 113,115
M 85
SH 114
SH 115
M 86
AN 114,114
AN 115,115
M 87
WL 2
AN 116,118
AN 117,119
M 88
SH 118
SH 119
M 89
AN 118,118
AN 119,119
M 90
TY 122,3592
TY 126,17416
M 91
ad 84,960
M 92
W 0
SA 218,1024
M 93
UH 7,27
AR 8,98
M 94
AR 9,122
AD 8,6
M 95
R 34
M 106
W 1
AD 9,7
M 107
AW 98,8
AW 122,9
M 104
UL 6,28
UH 7,28
M 105
WL 3
AN 120,122
AN 121,123
M 102
SH 122
SH 123
M 103
AN 122,122
AN 123,123
M 100
WL 2
AN 124,126
AN 125,127
M 101
SH 126
SH 127
M 98
AN 126,126
AN 127,127
M 99
W 2
AR 8,51
M 96
AR 9,75
AD 8,6
M 97
R 48
M 108
mw 3
AD 9,7
AW 51,8
M 109
ad 84,966
W 0
M 110
AW 75,9
UL 6,29
M 111
UH 7,29
AR 8,99
M 112
W 1
AR 9,123
M 113
AD 8,6
AD 9,7
M 114
AW 99,8
AW 123,9
M 115
W 2
M 116
M 117
M 118
bc 49,11,66,60
M 119
WL 2
R 50
M 120
mh 18
M 121
ad 66,2176
H 0
M 133
M 132
TX 58,0
TX 62,13824
M 134
DG 1024
M 135
M 123
M 122
TY 66,512
TY 70,14336
M 124
bc 48,11,64,56
M 125
mh 21
M 137
ad 64,2176
H 0
M 136
TX 74,1536
TX 78,15360
M 138
WL 4
Q 0
M 139
Q 2
M 127
Q 4
M 126
TY 82,2048
TY 86,15872
M 128
Q 6
M 129
WL 4
Q 8
M 141
Q 10
M 140
TX 90,3072
TX 94,16896
M 142
Q 12
M 143
R 54
M 131
Q 14
M 130
TY 98,3584
TY 102,17408
M 0
R 52
M 1
WL 10
Q 16
M 13
Q 18
M 12
Q 20
M 14
Q 22
M 15
WL 8
Q 24
M 3
Q 26
M 2
Q 28
)"
R"(M 4
Q 30
M 5
WL 6
Q 32
M 17
Q 34
M 16
Q 36
M 18
Q 38
M 19
WL 2
Q 40
M 7
Q 42
M 6
Q 44
M 8
Q 46
M 9
H 1
SA 220,1792
M 21
ae 44,68,864
M 20
s_bfe_u32 s51, s50, 0x10011
v_mad_i32_i24 v47, v49, s51, v48
XL 38,0
M 22
H 2
XL 40,768
M 23
R 58
M 11
XL 42,1536
M 10
WL 2
R 56
M 34
XL 44,2304
M 35
M 32
M 33
M 30
M 31
M 28
M 29
M 26
M 27
WV 10
M 24
WL 0
FP
M 25
M 36
M 37
M 38
M 39
M 40
M 41
M 42
M 43
M 44
M 45
FR
M 46
M 47
M 58
UL 6,30
UH 7,30
M 59
AR 8,52
AR 9,76
M 56
AD 8,6
AD 9,7
M 57
AW 52,8
AW 76,9
M 54
UL 6,31
UH 7,31
M 55
WL 0
FQ 212
AR 8,100
M 52
AR 9,124
AD 8,6
M 53
AD 9,7
AW 100,8
M 50
FZ 212
R 72
M 51
mw 24
R 74
M 48
TY 106,5128
TY 110,18952
M 49
R 76
M 60
ad 84,1008
W 0
M 61
AW 124,9
UL 6,32
M 62
UH 7,32
AR 8,53
M 63
W 1
AR 9,77
M 64
AD 8,6
AD 9,7
M 65
AW 53,8
AW 77,9
M 66
W 2
UL 6,33
M 67
WL 3
AN 104,106
AN 105,107
M 68
SH 106
SH 107
M 69
mw 27
AN 106,106
AN 107,107
M 70
WL 2
AN 108,110
AN 109,111
M 71
R 78
M 82
SH 110
SH 111
M 83
AN 110,110
AN 111,111
M 80
TY 114,6664
TY 118,20488
M 81
ad 84,1014
W 0
M 78
UH 7,33
AR 8,101
M 79
AR 9,125
AD 8,6
M 76
W 1
AD 9,7
M 77
AW 101,8
AW 125,9
M 74
UL 6,34
UH 7,34
M 75
W 2
SA 222,2560
M 72
AR 8,54
AR 9,78
M 73
WL 1
R 80
M 84
mw 48
AN 112,114
AN 113,115
M 85
SH 114
SH 115
M 86
AN 114,114
AN 115,115
M 87
WL 2
AN 116,118
AN 117,119
M 88
SH 118
SH 119
M 89
AN 118,118
AN 119,119
M 90
TY 122,8200
TY 126,22024
M 91
ad 84,1056
M 92
W 0
AD 8,6
M 93
AD 9,7
AW 54,8
M 94
AW 78,9
UL 6,35
M 95
R 82
M 106
W 1
UH 7,35
M 107
AR 8,102
AR 9,126
M 104
AD 8,6
AD 9,7
M 105
WL 3
AN 120,122
AN 121,123
M 102
SH 122
SH 123
M 103
AN 122,122
AN 123,123
M 100
WL 2
AN 124,126
AN 125,127
M 101
SH 126
SH 127
M 98
AN 126,126
AN 127,127
M 99
W 2
AW 102,8
M 96
AW 126,9
UL 6,36
M 97
R 0
M 108
mw 51
UH 7,36
AR 8,55
M 109
ad 84,1062
W 0
M 110
AR 9,79
AD 8,6
M 111
AD 9,7
AW 55,8
M 112
W 1
AW 79,9
M 113
UL 6,37
UH 7,37
M 114
AR 8,103
AR 9,127
M 115
W 2
AD 8,6
M 116
AD 9,7
AW 103,8
M 117
AW 127,9
M 118
bc 49,12,66,60
M 119
WL 1
R 2
M 120
mh 0
M 121
ad 66,2368
H 0
M 133
M 132
TX 58,4608
TX 62,18432
M 134
DG 1024
M 135
M 123
M 122
TY 66,5120
TY 70,18944
M 124
bc 48,12,64,56
M 125
mh 3
M 137
ad 64,2368
H 0
M 136
TX 74,6144
TX 78,19968
M 138
WL 4
Q 0
M 139
Q 2
M 127
Q 4
M 126
TY 82,6656
TY 86,20480
M 128
Q 6
M 129
WL 4
Q 8
M 141
Q 10
M 140
TX 90,7680
TX 94,21504
M 142
Q 12
M 143
R 6
M 131
Q 14
M 130
TY 98,8192
TY 102,22016
M 0
R 4
M 1
WL 10
Q 16
M 13
Q 18
M 12
Q 20
M 14
Q 22
M 15
WL 8
Q 24
M 3
Q 26
M 2
Q 28
M 4
Q 30
M 5
WL 6
Q 32
M 17
Q 34
M 16
Q 36
M 18
Q 38
M 19
WL 2
Q 40
M 7
Q 42
M 6
Q 44
M 8
Q 46
M 9
H 1
M 21
ae 42,72,480
M 20
s_bitcmp1_b32 s50, 10
s_cselect_b32 s52, 24576, 0
v_add_u32 v50, s52, v3
M 22
H 2
SA 224,0
M 23
R 10
M 11
M 10
WL 2
R 8
PA 12288
DF
M 34
M 35
)"
R"(M 32
M 33
M 30
M 31
M 28
M 29
M 26
ae 44,70,768
M 27
WV 7
M 24
WL 0
FP
M 25
M 36
s_bitcmp1_b32 s50, 12
s_cselect_b32 s51, 24576, 0
v_add_u32 v47, s51, v3
M 37
XL 14,128
M 38
XL 16,896
M 39
M 40
M 41
M 42
M 43
M 44
XL 18,1664
M 45
FR
M 46
M 47
M 58
M 59
M 56
XL 20,2432
UL 6,38
M 57
UH 7,38
AR 8,56
M 54
AR 9,80
AD 8,6
M 55
WL 0
FQ 216
AD 9,7
M 52
AW 56,8
AW 80,9
M 53
UL 6,39
UH 7,39
M 50
FZ 216
R 24
M 51
mw 72
R 26
M 48
TY 106,9736
TY 110,23560
M 49
R 28
M 60
ad 84,1104
W 0
M 61
AR 8,104
AR 9,128
M 62
AD 8,6
AD 9,7
M 63
W 1
AW 104,8
M 64
AW 128,9
UL 6,40
M 65
UH 7,40
AR 8,57
M 66
W 2
AR 9,81
M 67
WL 3
AN 104,106
AN 105,107
M 68
SH 106
SH 107
M 69
mw 75
AN 106,106
AN 107,107
M 70
WL 2
AN 108,110
AN 109,111
M 71
R 30
M 82
SH 110
SH 111
M 83
AN 110,110
AN 111,111
M 80
TY 114,11272
TY 118,25096
M 81
ad 84,1110
W 0
M 78
AD 8,6
AD 9,7
M 79
AW 57,8
AW 81,9
M 76
W 1
UL 6,41
M 77
UH 7,41
AR 8,105
M 74
AR 9,129
AD 8,6
M 75
W 2
AD 9,7
M 72
AW 105,8
AW 129,9
M 73
WL 1
R 32
M 84
mw 0
AN 112,114
AN 113,115
M 85
SH 114
SH 115
M 86
AN 114,114
AN 115,115
M 87
WL 2
AN 116,118
AN 117,119
M 88
SH 118
SH 119
M 89
AN 118,118
AN 119,119
M 90
TY 122,12808
TY 126,26632
M 91
ad 84,1152
M 92
W 0
SA 226,768
M 93
UL 6,42
UH 7,42
M 94
AR 8,58
AR 9,82
M 95
R 34
M 106
W 1
AD 8,6
M 107
AD 9,7
AW 58,8
M 104
AW 82,9
UL 6,43
M 105
WL 3
AN 120,122
AN 121,123
M 102
SH 122
SH 123
M 103
AN 122,122
AN 123,123
M 100
WL 2
AN 124,126
AN 125,127
M 101
SH 126
SH 127
M 98
AN 126,126
AN 127,127
M 99
W 2
UH 7,43
M 96
AR 8,106
AR 9,130
M 97
R 48
M 108
mw 3
AD 8,6
AD 9,7
M 109
ad 84,1158
W 0
M 110
AW 106,8
AW 130,9
M 111
UL 6,44
UH 7,44
M 112
W 1
AR 8,59
M 113
AR 9,83
AD 8,6
M 114
AD 9,7
AW 59,8
M 115
W 2
AW 83,9
M 116
UL 6,45
UH 7,45
M 117
AR 8,107
AR 9,131
M 118
bc 49,13,66,60
AD 8,6
M 119
WL 0
R 50
M 120
mh 9
AD 9,7
AW 107,8
M 121
ad 66,2560
H 0
M 133
AW 131,9
M 132
TX 58,9216
TX 62,23040
M 134
DG 1024
M 135
M 123
M 122
TY 66,9728
TY 70,23552
M 124
bc 48,13,64,56
M 125
mh 12
M 137
ad 64,2560
H 0
M 136
TX 74,10752
TX 78,24576
M 138
WL 4
Q 0
M 139
Q 2
M 127
Q 4
M 126
TY 82,11264
TY 86,25088
M 128
Q 6
M 129
WL 4
Q 8
M 141
Q 10
M 140
TX 90,12288
TX 94,26112
M 142
Q 12
M 143
R 54
M 131
Q 14
M 130
TY 98,12800
TY 102,26624
M 0
R 52
M 1
WL 10
Q 16
M 13
Q 18
M 12
Q 20
M 14
Q 22
M 15
WL 8
Q 24
M 3
Q 26
M 2
Q 28
M 4
Q 30
M 5
WL 6
Q 32
M 17
Q 34
M 16
Q 36
M 18
Q 38
M 19
WL 2
Q 40
M 7
Q 42
M 6
Q 44
M 8
Q 46
M 9
H 1
SA 228,1536
M 21
M 20
M 22
H 2
M 23
R 58
M 11
M 10
WL 2
R 56
M 34
M 35
M 32
M 33
M 30
M 31
M 28
M 29
M 26
M 27
WV 6
M 24
WL 0
FP
M 25
M 36
ae 44,70,768
M 37
s_bfe_u32 s51, s50, 0x1000d
v_mad_i32_i24 v47, v49, s51, v48
XL 22,128
M 38
M 39
M 40
XL 24,896
M 41
M 42
M 43
M 44
M 45
FR
M 46
XL 26,1664
M 47
M 58
M 59
M 56
M 57
M 54
XL 28,2432
M 55
WL 0
)"
R"(FQ 220
M 52
M 53
M 50
FZ 220
R 72
M 51
mw 24
R 74
M 48
TY 106,520
TY 110,14344
M 49
R 76
M 60
ad 84,1200
W 0
M 61
M 62
M 63
W 1
M 64
M 65
M 66
W 2
M 67
WL 3
AN 104,106
AN 105,107
M 68
SH 106
SH 107
M 69
mw 27
AN 106,106
AN 107,107
M 70
WL 2
AN 108,110
AN 109,111
M 71
R 78
M 82
SH 110
SH 111
M 83
AN 110,110
AN 111,111
M 80
TY 114,2056
TY 118,15880
M 81
ad 84,1206
W 0
M 78
M 79
M 76
W 1
M 77
M 74
M 75
W 2
SA 230,2304
M 72
M 73
R 80
M 84
mw 48
WL 3
AN 112,114
AN 113,115
M 85
SH 114
SH 115
M 86
AN 114,114
AN 115,115
M 87
WL 2
AN 116,118
AN 117,119
M 88
SH 118
SH 119
M 89
AN 118,118
AN 119,119
M 90
TY 122,3592
TY 126,17416
M 91
ad 84,1248
M 92
W 0
UL 6,14
M 93
UH 7,14
AR 8,132
M 94
AR 9,156
AD 8,6
M 95
R 82
M 106
W 1
AD 9,7
M 107
AW 132,8
AW 156,9
M 104
UL 6,15
UH 7,15
M 105
WL 3
AN 120,122
AN 121,123
M 102
SH 122
SH 123
M 103
AN 122,122
AN 123,123
M 100
WL 2
AN 124,126
AN 125,127
M 101
SH 126
SH 127
M 98
AN 126,126
AN 127,127
M 99
W 2
AR 8,180
M 96
AD 8,6
AD 136,7
M 97
R 0
M 108
mw 51
AW 180,8
UL 6,16
M 109
ad 84,1254
W 0
M 110
UH 7,16
AR 8,133
M 111
AR 9,157
AD 8,6
M 112
W 1
AD 9,7
M 113
AW 133,8
AW 157,9
M 114
UL 6,17
UH 7,17
M 115
W 2
AR 8,181
M 116
AD 8,6
AD 137,7
M 117
AW 181,8
UL 6,18
M 118
bc 49,14,66,60
UH 7,18
M 119
WL 0
R 2
M 120
mh 18
AR 8,134
AR 9,158
M 121
ad 66,2752
H 0
M 133
AD 8,6
AD 9,7
M 132
TX 58,0
TX 62,13824
M 134
DG 1024
M 135
AW 134,8
AW 158,9
M 123
UL 6,19
UH 7,19
M 122
TY 66,512
TY 70,14336
M 124
bc 48,14,64,56
AR 8,182
M 125
mh 21
AD 8,6
AD 138,7
M 137
ad 64,2752
H 0
M 136
TX 74,1536
TX 78,15360
M 138
WL 4
Q 0
M 139
Q 2
M 127
Q 4
M 126
TY 82,2048
TY 86,15872
M 128
Q 6
M 129
WL 4
Q 8
M 141
Q 10
M 140
TX 90,3072
TX 94,16896
M 142
Q 12
M 143
R 6
M 131
Q 14
M 130
TY 98,3584
TY 102,17408
M 0
R 4
M 1
WL 10
Q 16
M 13
Q 18
M 12
Q 20
M 14
Q 22
M 15
WL 8
Q 24
M 3
Q 26
M 2
Q 28
M 4
Q 30
M 5
WL 6
Q 32
M 17
Q 34
M 16
Q 36
M 18
Q 38
M 19
WL 2
Q 40
M 7
Q 42
M 6
Q 44
M 8
Q 46
M 9
H 1
AW 182,8
M 21
ae 42,72,480
M 20
SA 232,128
UL 6,20
M 22
H 2
UH 7,20
M 23
R 10
M 11
AR 8,135
AR 9,159
M 10
WL 2
R 8
PA 12288
DF
M 34
AD 8,6
AD 9,7
M 35
AW 135,8
AW 159,9
M 32
UL 6,21
UH 7,21
M 33
AR 8,183
AD 8,6
M 30
AD 139,7
AW 183,8
M 31
M 28
M 29
M 26
M 27
WV 7
M 24
WL 0
FP
M 25
M 36
ae 44,68,768
M 37
s_bitcmp1_b32 s50, 14
s_cselect_b32 s51, 24576, 0
v_add_u32 v47, s51, v3
M 38
XL 30,128
M 39
M 40
M 41
M 42
XL 32,896
M 43
M 44
M 45
FR
M 46
M 47
M 58
XL 34,1664
M 59
M 56
M 57
M 54
M 55
WL 0
FQ 224
M 52
XL 36,2432
M 53
M 50
FZ 224
R 24
M 51
mw 72
R 26
M 48
TY 106,5128
TY 110,18952
M 49
R 28
M 60
ad 84,1296
W 0
M 61
M 62
M 63
W 1
M 64
M 65
M 66
W 2
M 67
WL 3
AN 104,106
AN 105,107
M 68
SH 106
SH 107
M 69
mw 75
AN 106,106
AN 107,107
M 70
WL 2
AN 108,110
AN 109,111
M 71
R 30
M 82
SH 110
SH 111
)"
R"(M 83
AN 110,110
AN 111,111
M 80
TY 114,6664
TY 118,20488
M 81
ad 84,1302
W 0
M 78
M 79
M 76
W 1
M 77
M 74
M 75
W 2
M 72
M 73
R 32
M 84
mw 0
WL 3
AN 112,114
AN 113,115
M 85
SH 114
SH 115
M 86
AN 114,114
AN 115,115
M 87
WL 2
AN 116,118
AN 117,119
M 88
SH 118
SH 119
M 89
AN 118,118
AN 119,119
M 90
TY 122,8200
TY 126,22024
M 91
ad 84,1344
M 92
W 0
SA 234,896
M 93
UL 6,22
UH 7,22
M 94
AR 8,136
AR 9,160
M 95
R 34
M 106
W 1
AD 8,6
M 107
AD 9,7
AW 136,8
M 104
AW 160,9
UL 6,23
M 105
WL 3
AN 120,122
AN 121,123
M 102
SH 122
SH 123
M 103
AN 122,122
AN 123,123
M 100
WL 2
AN 124,126
AN 125,127
M 101
SH 126
SH 127
M 98
AN 126,126
AN 127,127
M 99
W 2
UH 7,23
M 96
AR 8,184
AD 8,6
M 97
R 48
M 108
mw 3
AD 140,7
AW 184,8
M 109
ad 84,1350
W 0
M 110
UL 6,24
UH 7,24
M 111
AR 8,137
AR 9,161
M 112
W 1
AD 8,6
M 113
AD 9,7
AW 137,8
M 114
AW 161,9
UL 6,25
M 115
W 2
UH 7,25
M 116
AR 8,185
AD 8,6
M 117
AD 141,7
AW 185,8
M 118
bc 49,15,66,60
UL 6,26
M 119
WL 0
R 50
M 120
mh 0
UH 7,26
AR 8,138
M 121
ad 66,2944
H 0
M 133
AR 9,162
AD 8,6
M 132
TX 58,4608
TX 62,18432
M 134
DG 1024
M 135
AD 9,7
AW 138,8
M 123
AW 162,9
UL 6,27
M 122
TY 66,5120
TY 70,18944
M 124
bc 48,15,64,56
UH 7,27
M 125
mh 3
AR 8,186
AD 8,6
M 137
ad 64,2944
H 0
M 136
TX 74,6144
TX 78,19968
M 138
WL 4
Q 0
M 139
Q 2
M 127
Q 4
M 126
TY 82,6656
TY 86,20480
M 128
Q 6
M 129
WL 4
Q 8
M 141
Q 10
M 140
TX 90,7680
TX 94,21504
M 142
Q 12
M 143
R 54
M 131
Q 14
M 130
TY 98,8192
TY 102,22016
M 0
R 52
M 1
WL 10
Q 16
M 13
Q 18
M 12
Q 20
M 14
Q 22
M 15
WL 8
Q 24
M 3
Q 26
M 2
Q 28
M 4
Q 30
M 5
WL 6
Q 32
M 17
Q 34
M 16
Q 36
M 18
Q 38
M 19
WL 2
Q 40
M 7
Q 42
M 6
Q 44
M 8
Q 46
M 9
H 1
SA 236,1664
M 21
AD 142,7
AW 186,8
M 20
UL 6,28
UH 7,28
M 22
H 2
AR 8,139
M 23
R 58
M 11
AR 9,163
AD 8,6
M 10
WL 2
R 56
M 34
AD 9,7
AW 139,8
M 35
AW 163,9
UL 6,29
M 32
UH 7,29
AR 8,187
M 33
AD 8,6
AD 143,7
M 30
AW 187,8
M 31
M 28
M 29
M 26
M 27
WV 6
M 24
WL 0
FP
M 25
M 36
M 37
M 38
ae 44,68,768
M 39
s_bfe_u32 s51, s50, 0x1000f
v_mad_i32_i24 v47, v49, s51, v48
XL 38,128
M 40
M 41
M 42
M 43
M 44
XL 40,896
M 45
FR
M 46
M 47
M 58
M 59
M 56
XL 42,1664
M 57
M 54
M 55
WL 0
FQ 228
M 52
M 53
M 50
FZ 228
R 72
M 51
mw 24
R 74
M 48
TY 106,9736
TY 110,23560
M 49
R 76
M 60
ad 84,1392
W 0
M 61
XL 44,2432
M 62
M 63
W 1
M 64
M 65
M 66
W 2
M 67
WL 3
AN 104,106
AN 105,107
M 68
SH 106
SH 107
M 69
mw 27
AN 106,106
AN 107,107
M 70
WL 2
AN 108,110
AN 109,111
M 71
R 78
M 82
SH 110
SH 111
M 83
AN 110,110
AN 111,111
M 80
TY 114,11272
TY 118,25096
M 81
ad 84,1398
W 0
M 78
M 79
M 76
W 1
M 77
M 74
M 75
W 2
SA 238,2432
M 72
M 73
R 80
M 84
mw 48
WL 3
AN 112,114
AN 113,115
M 85
SH 114
SH 115
M 86
AN 114,114
AN 115,115
M 87
WL 2
AN 116,118
AN 117,119
M 88
SH 118
SH 119
M 89
AN 118,118
AN 119,119
M 90
TY 122,12808
TY 126,26632
M 91
ad 84,1440
M 92
W 0
)"
R"(UL 6,30
M 93
UH 7,30
AR 8,140
M 94
AR 9,164
AD 8,6
M 95
R 82
M 106
W 1
AD 9,7
M 107
AW 140,8
AW 164,9
M 104
UL 6,31
UH 7,31
M 105
WL 3
AN 120,122
AN 121,123
M 102
SH 122
SH 123
M 103
AN 122,122
AN 123,123
M 100
WL 2
AN 124,126
AN 125,127
M 101
SH 126
SH 127
M 98
AN 126,126
AN 127,127
M 99
W 2
AR 8,188
M 96
AD 8,6
AD 144,7
M 97
R 0
M 108
mw 51
AW 188,8
UL 6,32
M 109
ad 84,1446
W 0
M 110
UH 7,32
AR 8,141
M 111
AR 9,165
AD 8,6
M 112
W 1
AD 9,7
M 113
AW 141,8
AW 165,9
M 114
UL 6,33
UH 7,33
M 115
W 2
AR 8,189
M 116
AD 8,6
AD 145,7
M 117
AW 189,8
UL 6,34
M 118
bc 49,16,66,60
UH 7,34
M 119
WL 0
R 2
M 120
mh 9
AR 8,142
AR 9,166
M 121
ad 66,3136
H 0
M 133
AD 8,6
AD 9,7
M 132
TX 58,9216
TX 62,23040
M 134
DG 1024
M 135
AW 142,8
AW 166,9
M 123
UL 6,35
UH 7,35
M 122
TY 66,9728
TY 70,23552
M 124
bc 48,16,64,56
AR 8,190
M 125
mh 12
AD 8,6
AD 146,7
M 137
ad 64,3136
H 0
M 136
TX 74,10752
TX 78,24576
M 138
WL 4
Q 0
M 139
Q 2
M 127
Q 4
M 126
TY 82,11264
TY 86,25088
M 128
Q 6
M 129
WL 4
Q 8
M 141
Q 10
M 140
TX 90,12288
TX 94,26112
M 142
Q 12
M 143
R 6
M 131
Q 14
M 130
TY 98,12800
TY 102,26624
M 0
R 4
M 1
WL 10
Q 16
M 13
Q 18
M 12
Q 20
M 14
Q 22
M 15
WL 8
Q 24
M 3
Q 26
M 2
Q 28
M 4
Q 30
M 5
WL 6
Q 32
M 17
Q 34
M 16
Q 36
M 18
Q 38
M 19
WL 2
Q 40
M 7
Q 42
M 6
Q 44
M 8
Q 46
M 9
H 1
AW 190,8
M 21
ae 42,72,480
M 20
SA 240,256
UL 6,36
M 22
H 2
UH 7,36
M 23
R 10
M 11
AR 8,143
AR 9,167
M 10
WL 2
R 8
PA 12288
DF
M 34
AD 8,6
AD 9,7
M 35
AW 143,8
AW 167,9
M 32
UL 6,37
UH 7,37
M 33
AR 8,191
AD 8,6
M 30
AD 147,7
AW 191,8
M 31
M 28
M 29
M 26
M 27
WV 7
M 24
WL 0
FP
M 25
M 36
M 37
M 38
M 39
M 40
ae 44,68,864
M 41
s_bitcmp1_b32 s50, 16
s_cselect_b32 s51, 24576, 0
v_add_u32 v47, s51, v3
M 42
XL 14,128
M 43
M 44
M 45
FR
M 46
XL 16,896
M 47
M 58
M 59
M 56
M 57
M 54
XL 18,1664
M 55
WL 0
FQ 232
M 52
M 53
M 50
FZ 232
R 24
M 51
mw 72
R 26
M 48
TY 106,520
TY 110,14344
M 49
R 28
M 60
ad 84,1488
W 0
M 61
XL 20,2432
M 62
M 63
W 1
M 64
M 65
M 66
W 2
M 67
WL 3
AN 104,106
AN 105,107
M 68
SH 106
SH 107
M 69
mw 75
AN 106,106
AN 107,107
M 70
WL 2
AN 108,110
AN 109,111
M 71
R 30
M 82
SH 110
SH 111
M 83
AN 110,110
AN 111,111
M 80
TY 114,2056
TY 118,15880
M 81
ad 84,1494
W 0
M 78
M 79
M 76
W 1
M 77
M 74
M 75
W 2
M 72
M 73
R 32
M 84
mw 0
WL 3
AN 112,114
AN 113,115
M 85
SH 114
SH 115
M 86
AN 114,114
AN 115,115
M 87
WL 2
AN 116,118
AN 117,119
M 88
SH 118
SH 119
M 89
AN 118,118
AN 119,119
M 90
TY 122,3592
TY 126,17416
M 91
ad 84,1536
M 92
W 0
SA 242,1024
M 93
M 94
UL 6,38
UH 7,38
M 95
R 34
M 106
W 1
AR 8,144
M 107
AR 9,168
AD 8,6
M 104
AD 9,7
AW 144,8
M 105
WL 3
AN 120,122
AN 121,123
M 102
SH 122
SH 123
M 103
AN 122,122
AN 123,123
M 100
WL 2
AN 124,126
AN 125,127
M 101
SH 126
SH 127
M 98
AN 126,126
AN 127,127
M 99
W 2
AW 168,9
M 96
UL 6,39
UH 7,39
M 97
R 48
M 108
mw 3
)"
R"(AR 8,192
AD 8,6
M 109
ad 84,1542
W 0
M 110
AD 148,7
AW 192,8
M 111
UL 6,40
UH 7,40
M 112
W 1
AR 8,145
M 113
AR 9,169
AD 8,6
M 114
AD 9,7
AW 145,8
M 115
W 2
AW 169,9
M 116
UL 6,41
UH 7,41
M 117
AR 8,193
AD 8,6
M 118
bc 49,17,66,60
AD 149,7
M 119
WL 0
R 50
M 120
mh 18
AW 193,8
UL 6,42
M 121
ad 66,3328
H 0
M 133
UH 7,42
AR 8,146
M 132
TX 58,0
TX 62,13824
M 134
DG 1024
M 135
AR 9,170
AD 8,6
M 123
AD 9,7
AW 146,8
M 122
TY 66,512
TY 70,14336
M 124
bc 48,17,64,56
AW 170,9
M 125
mh 21
UL 6,43
UH 7,43
M 137
ad 64,3328
H 0
M 136
TX 74,1536
TX 78,15360
M 138
WL 4
Q 0
M 139
Q 2
M 127
Q 4
M 126
TY 82,2048
TY 86,15872
M 128
Q 6
M 129
WL 4
Q 8
M 141
Q 10
M 140
TX 90,3072
TX 94,16896
M 142
Q 12
M 143
R 54
M 131
Q 14
M 130
TY 98,3584
TY 102,17408
M 0
R 52
M 1
WL 10
Q 16
M 13
Q 18
M 12
Q 20
M 14
Q 22
M 15
WL 8
Q 24
M 3
Q 26
M 2
Q 28
M 4
Q 30
M 5
WL 6
Q 32
M 17
Q 34
M 16
Q 36
M 18
Q 38
M 19
WL 2
Q 40
M 7
Q 42
M 6
Q 44
M 8
Q 46
M 9
H 1
SA 244,1792
M 21
AR 8,194
AD 8,6
M 20
AD 150,7
AW 194,8
M 22
H 2
UL 6,44
M 23
R 58
M 11
UH 7,44
AR 8,147
M 10
WL 2
R 56
M 34
AR 9,171
AD 8,6
M 35
AD 9,7
AW 147,8
M 32
AW 171,9
UL 6,45
M 33
UH 7,45
AR 8,195
M 30
AD 8,6
AD 151,7
M 31
AW 195,8
M 28
M 29
M 26
M 27
WV 6
M 24
WL 0
FP
M 25
M 36
M 37
M 38
M 39
M 40
M 41
M 42
ae 44,68,864
M 43
s_bfe_u32 s51, s50, 0x10011
v_mad_i32_i24 v47, v49, s51, v48
XL 22,128
M 44
M 45
FR
M 46
M 47
M 58
XL 24,896
M 59
M 56
M 57
M 54
M 55
WL 0
FQ 236
M 52
XL 26,1664
M 53
M 50
FZ 236
R 72
M 51
mw 24
R 74
M 48
TY 106,5128
TY 110,18952
M 49
R 76
M 60
ad 84,1584
W 0
M 61
XL 28,2432
M 62
M 63
W 1
M 64
M 65
M 66
W 2
M 67
WL 3
AN 104,106
AN 105,107
M 68
SH 106
SH 107
M 69
mw 27
AN 106,106
AN 107,107
M 70
WL 2
AN 108,110
AN 109,111
M 71
R 78
M 82
SH 110
SH 111
M 83
AN 110,110
AN 111,111
M 80
TY 114,6664
TY 118,20488
M 81
ad 84,1590
W 0
M 78
M 79
M 76
W 1
M 77
M 74
M 75
W 2
SA 246,2560
M 72
M 73
R 80
M 84
mw 48
WL 3
AN 112,114
AN 113,115
M 85
SH 114
SH 115
M 86
AN 114,114
AN 115,115
M 87
WL 2
AN 116,118
AN 117,119
M 88
SH 118
SH 119
M 89
AN 118,118
AN 119,119
M 90
TY 122,8200
TY 126,22024
M 91
ad 84,1632
M 92
W 0
M 93
M 94
M 95
R 82
M 106
W 1
UL 6,14
M 107
UH 7,14
AR 8,148
M 104
AR 9,172
AD 8,6
M 105
WL 3
AN 120,122
AN 121,123
M 102
SH 122
SH 123
M 103
AN 122,122
AN 123,123
M 100
WL 2
AN 124,126
AN 125,127
M 101
SH 126
SH 127
M 98
AN 126,126
AN 127,127
M 99
W 2
AD 9,7
M 96
AW 148,8
AW 172,9
M 97
R 0
M 108
mw 51
UL 6,15
UH 7,15
M 109
ad 84,1638
W 0
M 110
AR 8,196
AD 8,6
M 111
AD 152,7
AW 196,8
M 112
W 1
UL 6,16
M 113
UH 7,16
AR 8,149
M 114
AR 9,173
AD 8,6
M 115
W 2
AD 9,7
M 116
AW 149,8
AW 173,9
M 117
UL 6,17
UH 7,17
M 118
bc 49,18,66,60
AR 8,197
M 119
WL 0
R 2
M 120
mh 0
AD 8,6
AD 153,7
M 121
ad 66,3520
H 0
M 133
AW 197,8
UL 6,18
M 132
TX 58,4608
TX 62,18432
M 134
DG 1024
M 135
)"
R"(UH 7,18
AR 8,150
M 123
AR 9,174
AD 8,6
M 122
TY 66,5120
TY 70,18944
M 124
bc 48,18,64,56
AD 9,7
M 125
mh 3
AW 150,8
AW 174,9
M 137
ad 64,3520
H 0
M 136
TX 74,6144
TX 78,19968
M 138
WL 4
Q 0
M 139
Q 2
M 127
Q 4
M 126
TY 82,6656
TY 86,20480
M 128
Q 6
M 129
WL 4
Q 8
M 141
Q 10
M 140
TX 90,7680
TX 94,21504
M 142
Q 12
M 143
R 6
M 131
Q 14
M 130
TY 98,8192
TY 102,22016
M 0
R 4
M 1
WL 10
Q 16
M 13
Q 18
M 12
Q 20
M 14
Q 22
M 15
WL 8
Q 24
M 3
Q 26
M 2
Q 28
M 4
Q 30
M 5
WL 6
Q 32
M 17
Q 34
M 16
Q 36
M 18
Q 38
M 19
WL 2
Q 40
M 7
Q 42
M 6
Q 44
M 8
Q 46
M 9
H 1
UL 6,19
M 21
ae 42,72,480
M 20
s_bfe_u32 s52, s50, 0x1000b
v_mad_i32_i24 v50, v49, s52, v48
SA 248,256
M 22
H 2
UH 7,19
M 23
R 10
M 11
AR 8,198
AD 8,6
M 10
WL 2
R 8
PA 12288
DF
M 34
AD 154,7
AW 198,8
M 35
UL 6,20
UH 7,20
M 32
AR 8,151
AR 9,175
M 33
AD 8,6
AD 9,7
M 30
AW 151,8
AW 175,9
M 31
UL 6,21
UH 7,21
M 28
AR 8,199
AD 8,6
M 29
AD 155,7
AW 199,8
M 26
M 27
WV 7
M 24
WL 0
FP
M 25
M 36
M 37
M 38
M 39
M 40
M 41
M 42
M 43
M 44
M 45
FR
M 46
M 47
M 58
M 59
M 56
M 57
M 54
M 55
WL 0
FQ 240
M 52
M 53
M 50
FZ 240
R 24
M 51
mw 72
R 26
M 48
TY 106,9736
TY 110,23560
M 49
R 28
M 60
ad 84,1680
W 0
M 61
M 62
M 63
W 1
M 64
M 65
M 66
W 2
M 67
WL 3
AN 104,106
AN 105,107
M 68
SH 106
SH 107
M 69
mw 75
AN 106,106
AN 107,107
M 70
WL 2
AN 108,110
AN 109,111
M 71
R 30
M 82
SH 110
SH 111
M 83
AN 110,110
AN 111,111
M 80
TY 114,11272
TY 118,25096
M 81
ad 84,1686
W 0
M 78
ae 44,70,768
M 79
s_bitcmp1_b32 s50, 12
s_cselect_b32 s51, 24576, 0
v_add_u32 v47, s51, v3
M 76
W 1
XL 30,256
M 77
XL 32,1024
M 74
M 75
W 2
M 72
XL 34,1792
M 73
WL 1
R 32
M 84
mw 0
AN 112,114
AN 113,115
M 85
SH 114
SH 115
M 86
AN 114,114
AN 115,115
M 87
WL 2
AN 116,118
AN 117,119
M 88
SH 118
SH 119
M 89
AN 118,118
AN 119,119
M 90
TY 122,12808
TY 126,26632
M 91
ad 84,1728
M 92
W 0
SA 250,1024
M 93
XL 36,2560
M 94
M 95
R 34
M 106
W 1
M 107
M 104
UL 6,22
UH 7,22
M 105
WL 3
AN 120,122
AN 121,123
M 102
SH 122
SH 123
M 103
AN 122,122
AN 123,123
M 100
WL 2
AN 124,126
AN 125,127
M 101
SH 126
SH 127
M 98
AN 126,126
AN 127,127
M 99
W 2
AR 8,152
M 96
AR 9,176
AD 8,6
M 97
R 48
M 108
mw 3
AD 9,7
AW 152,8
M 109
ad 84,1734
W 0
M 110
AW 176,9
UL 6,23
M 111
UH 7,23
AD 132,6
M 112
W 1
AD 156,7
M 113
UL 6,24
UH 7,24
M 114
AR 8,153
AR 9,177
M 115
W 2
AD 8,6
M 116
AD 9,7
AW 153,8
M 117
AW 177,9
UL 6,25
M 118
bc 49,19,66,60
UH 7,25
M 119
WL 0
R 50
M 120
mh 9
AD 133,6
AD 157,7
M 121
ad 66,3712
H 0
M 133
UL 6,26
UH 7,26
M 132
TX 58,9216
TX 62,23040
M 134
DG 1024
M 135
AR 8,154
AR 9,178
M 123
AD 8,6
AD 9,7
M 122
TY 66,9728
TY 70,23552
M 124
bc 48,19,64,56
AW 154,8
M 125
mh 12
AW 178,9
UL 6,27
M 137
ad 64,3712
H 0
M 136
TX 74,10752
TX 78,24576
M 138
WL 4
Q 0
M 139
Q 2
M 127
Q 4
M 126
TY 82,11264
TY 86,25088
M 128
Q 6
M 129
WL 4
Q 8
M 141
Q 10
M 140
TX 90,12288
TX 94,26112
M 142
Q 12
M 143
R 54
M 131
)"
R"(Q 14
M 130
TY 98,12800
TY 102,26624
M 0
R 52
M 1
WL 10
Q 16
M 13
Q 18
M 12
Q 20
M 14
Q 22
M 15
WL 8
Q 24
M 3
Q 26
M 2
Q 28
M 4
Q 30
M 5
WL 6
Q 32
M 17
Q 34
M 16
Q 36
M 18
Q 38
M 19
WL 2
Q 40
M 7
Q 42
M 6
Q 44
M 8
Q 46
M 9
H 1
SA 252,1792
M 21
UH 7,27
AD 134,6
M 20
AD 158,7
UL 6,28
M 22
H 2
UH 7,28
M 23
R 58
M 11
AR 8,155
AR 9,179
M 10
WL 2
R 56
M 34
AD 8,6
AD 9,7
M 35
AW 155,8
AW 179,9
M 32
UL 6,29
UH 7,29
M 33
AD 135,6
AD 159,7
M 30
M 31
M 28
M 29
M 26
M 27
WV 6
M 24
WL 0
FP
M 25
M 36
M 37
M 38
M 39
M 40
M 41
M 42
M 43
M 44
M 45
FR
M 46
M 47
M 58
M 59
M 56
M 57
M 54
M 55
WL 0
FQ 244
M 52
M 53
M 50
FZ 244
R 72
M 51
mw 24
R 74
M 48
TY 106,520
TY 110,14344
M 49
R 76
M 60
ad 84,1776
W 0
M 61
M 62
M 63
W 1
M 64
M 65
M 66
W 2
M 67
WL 3
AN 104,106
AN 105,107
M 68
SH 106
SH 107
M 69
mw 27
AN 106,106
AN 107,107
M 70
WL 2
AN 108,110
AN 109,111
M 71
R 78
M 82
SH 110
SH 111
M 83
AN 110,110
AN 111,111
M 80
TY 114,2056
TY 118,15880
M 81
ad 84,1782
W 0
M 78
ae 44,70,768
M 79
s_bfe_u32 s51, s50, 0x1000d
v_mad_i32_i24 v47, v49, s51, v48
XL 38,256
M 76
W 1
M 77
M 74
XL 40,1024
M 75
W 2
SA 254,2560
M 72
M 73
WL 1
R 80
M 84
mw 48
AN 112,114
AN 113,115
M 85
SH 114
SH 115
M 86
AN 114,114
AN 115,115
M 87
WL 2
AN 116,118
AN 117,119
M 88
SH 118
SH 119
M 89
AN 118,118
AN 119,119
M 90
TY 122,3592
TY 126,17416
M 91
ad 84,1824
M 92
W 0
XL 42,1792
M 93
XL 44,2560
M 94
M 95
R 82
M 106
W 1
M 107
M 104
M 105
WL 3
AN 120,122
AN 121,123
M 102
SH 122
SH 123
M 103
AN 122,122
AN 123,123
M 100
WL 2
AN 124,126
AN 125,127
M 101
SH 126
SH 127
M 98
AN 126,126
AN 127,127
M 99
W 2
M 96
M 97
R 0
M 108
mw 51
M 109
ad 84,1830
W 0
M 110
M 111
M 112
W 1
M 113
M 114
M 115
W 2
M 116
M 117
M 118
bc 49,20,66,60
M 119
WL 2
R 2
M 120
mh 18
M 121
ad 66,3904
H 0
M 133
M 132
TX 58,0
TX 62,13824
M 134
DG 1024
M 135
M 123
UL 6,30
UH 7,30
M 122
TY 66,512
TY 70,14336
M 124
bc 48,20,64,56
AD 160,6
M 125
mh 21
AD 184,7
UL 6,31
M 137
ad 64,3904
H 0
M 136
TX 74,1536
TX 78,15360
M 138
WL 4
Q 0
M 139
Q 2
M 127
Q 4
M 126
TY 82,2048
TY 86,15872
M 128
Q 6
M 129
WL 4
Q 8
M 141
Q 10
M 140
TX 90,3072
TX 94,16896
M 142
Q 12
M 143
R 6
M 131
Q 14
M 130
TY 98,3584
TY 102,17408
M 0
R 4
M 1
WL 10
Q 16
M 13
Q 18
M 12
Q 20
M 14
Q 22
M 15
WL 8
Q 24
M 3
Q 26
M 2
Q 28
M 4
Q 30
M 5
WL 6
Q 32
M 17
Q 34
M 16
Q 36
M 18
Q 38
M 19
WL 2
Q 40
M 7
Q 42
M 6
Q 44
M 8
Q 46
M 9
H 1
UH 7,31
M 21
AD 208,6
AD 232,7
M 20
UL 6,32
UH 7,32
M 22
H 2
AD 161,6
M 23
R 10
M 11
AD 185,7
UL 6,33
M 10
WL 2
R 8
M 34
UH 7,33
AD 209,6
M 35
AD 233,7
UL 6,34
M 32
UH 7,34
AD 162,6
M 33
AD 186,7
UL 6,35
M 30
UH 7,35
AD 210,6
M 31
AD 234,7
UL 6,36
M 28
UH 7,36
AD 163,6
M 29
AD 187,7
)"
R"(UL 6,37
M 26
UH 7,37
AD 211,6
M 27
WV 5
AD 235,7
M 24
WL 0
FP
M 25
M 36
M 37
M 38
M 39
M 40
M 41
M 42
M 43
M 44
M 45
FR
M 46
M 47
M 58
M 59
M 56
M 57
M 54
M 55
WL 0
FQ 248
M 52
M 53
M 50
FZ 248
R 24
M 51
mw 72
R 26
M 48
TY 106,5128
TY 110,18952
M 49
R 28
M 60
ad 84,1872
W 0
M 61
M 62
M 63
W 1
M 64
M 65
M 66
W 2
M 67
WL 3
AN 104,106
AN 105,107
M 68
SH 106
SH 107
M 69
mw 75
AN 106,106
AN 107,107
M 70
WL 2
AN 108,110
AN 109,111
M 71
R 30
M 82
SH 110
SH 111
M 83
AN 110,110
AN 111,111
M 80
TY 114,6664
TY 118,20488
M 81
ad 84,1878
W 0
M 78
M 79
M 76
W 1
M 77
ae 44,68,768
M 74
s_bitcmp1_b32 s50, 14
s_cselect_b32 s51, 24576, 0
v_add_u32 v47, s51, v3
M 75
W 2
XL 14,256
M 72
M 73
WL 1
R 32
M 84
mw 0
AN 112,114
AN 113,115
M 85
SH 114
SH 115
M 86
AN 114,114
AN 115,115
M 87
WL 2
AN 116,118
AN 117,119
M 88
SH 118
SH 119
M 89
AN 118,118
AN 119,119
M 90
TY 122,8200
TY 126,22024
M 91
ad 84,1920
M 92
W 0
XL 16,1024
M 93
XL 18,1792
M 94
M 95
R 34
M 106
W 1
XL 20,2560
M 107
M 104
M 105
WL 3
AN 120,122
AN 121,123
M 102
SH 122
SH 123
M 103
AN 122,122
AN 123,123
M 100
WL 2
AN 124,126
AN 125,127
M 101
SH 126
SH 127
M 98
AN 126,126
AN 127,127
M 99
W 2
M 96
M 97
R 48
M 108
mw 3
M 109
ad 84,1926
W 0
M 110
M 111
M 112
W 1
M 113
M 114
M 115
W 2
M 116
M 117
M 118
bc 49,21,66,60
M 119
WL 2
R 50
M 120
mh 0
M 121
ad 66,4096
H 0
M 133
M 132
TX 58,4608
TX 62,18432
M 134
DG 1024
M 135
M 123
M 122
TY 66,5120
TY 70,18944
M 124
bc 48,21,64,56
M 125
mh 3
M 137
ad 64,4096
H 0
M 136
TX 74,6144
TX 78,19968
M 138
WL 4
Q 0
M 139
Q 2
M 127
Q 4
M 126
TY 82,6656
TY 86,20480
M 128
Q 6
M 129
WL 4
Q 8
M 141
Q 10
M 140
TX 90,7680
TX 94,21504
M 142
Q 12
M 143
R 54
M 131
Q 14
M 130
TY 98,8192
TY 102,22016
M 0
R 52
M 1
WL 10
Q 16
M 13
Q 18
M 12
Q 20
M 14
Q 22
M 15
WL 8
Q 24
M 3
Q 26
M 2
Q 28
M 4
Q 30
M 5
WL 6
Q 32
M 17
Q 34
M 16
Q 36
M 18
Q 38
M 19
WL 2
Q 40
M 7
Q 42
M 6
Q 44
M 8
Q 46
M 9
H 1
UL 6,38
M 21
UH 7,38
AD 164,6
M 20
AD 188,7
UL 6,39
M 22
H 2
UH 7,39
M 23
R 58
M 11
AD 212,6
AD 236,7
M 10
WL 2
R 56
M 34
UL 6,40
UH 7,40
M 35
AD 165,6
AD 189,7
M 32
UL 6,41
UH 7,41
M 33
AD 213,6
AD 237,7
M 30
UL 6,42
UH 7,42
M 31
AD 166,6
AD 190,7
M 28
UL 6,43
UH 7,43
M 29
AD 214,6
AD 238,7
M 26
UL 6,44
UH 7,44
M 27
WV 5
AD 167,6
AD 191,7
M 24
WL 0
FP
UL 6,45
M 25
UH 7,45
AD 215,6
M 36
AD 239,7
M 37
M 38
M 39
M 40
M 41
M 42
M 43
M 44
M 45
FR
M 46
M 47
M 58
M 59
M 56
M 57
M 54
M 55
WL 0
FQ 252
M 52
M 53
M 50
FZ 252
R 72
M 51
mw 24
R 74
M 48
TY 106,9736
TY 110,23560
M 49
R 76
M 60
ad 84,1968
W 0
M 61
M 62
M 63
W 1
M 64
M 65
M 66
W 2
M 67
WL 3
AN 104,106
AN 105,107
M 68
SH 106
SH 107
M 69
mw 27
AN 106,106
AN 107,107
M 70
WL 2
AN 108,110
AN 109,111
M 71
R 78
M 82
)"
R"(SH 110
SH 111
M 83
AN 110,110
AN 111,111
M 80
TY 114,11272
TY 118,25096
M 81
ad 84,1974
W 0
M 78
M 79
M 76
W 1
M 77
M 74
M 75
W 2
M 72
ae 44,68,768
M 73
WL 1
R 80
M 84
mw 48
AN 112,114
AN 113,115
M 85
s_bfe_u32 s51, s50, 0x1000f
SH 114
SH 115
M 86
AN 114,114
AN 115,115
M 87
WL 2
AN 116,118
AN 117,119
M 88
SH 118
SH 119
M 89
AN 118,118
AN 119,119
M 90
TY 122,12808
TY 126,26632
M 91
ad 84,2016
M 92
W 0
v_mad_i32_i24 v47, v49, s51, v48
M 93
XL 22,256
M 94
XL 24,1024
M 95
R 82
M 106
W 1
XL 26,1792
M 107
M 104
M 105
WL 3
AN 120,122
AN 121,123
M 102
SH 122
SH 123
M 103
AN 122,122
AN 123,123
M 100
WL 2
AN 124,126
AN 125,127
M 101
SH 126
SH 127
M 98
AN 126,126
AN 127,127
M 99
W 2
XL 28,2560
M 96
M 97
R 0
M 108
mw 51
M 109
ad 84,2022
W 0
M 110
M 111
M 112
W 1
M 113
M 114
M 115
W 2
M 116
M 117
M 118
bc 49,22,66,60
M 119
WL 2
R 2
M 120
mh 9
M 121
ad 66,4288
H 0
M 133
M 132
TX 58,9216
TX 62,23040
M 134
DG 1024
M 135
M 123
M 122
TY 66,9728
TY 70,23552
M 124
bc 48,22,64,56
M 125
mh 12
M 137
ad 64,4288
H 0
M 136
TX 74,10752
TX 78,24576
M 138
WL 4
Q 0
M 139
Q 2
M 127
Q 4
M 126
TY 82,11264
TY 86,25088
M 128
Q 6
M 129
WL 4
Q 8
M 141
Q 10
M 140
TX 90,12288
TX 94,26112
M 142
Q 12
M 143
R 6
M 131
Q 14
M 130
TY 98,12800
TY 102,26624
M 0
R 4
M 1
WL 10
Q 16
M 13
Q 18
M 12
Q 20
M 14
Q 22
M 15
WL 8
Q 24
M 3
Q 26
M 2
Q 28
M 4
Q 30
M 5
WL 6
Q 32
M 17
Q 34
M 16
Q 36
M 18
Q 38
M 19
WL 2
Q 40
M 7
Q 42
M 6
Q 44
M 8
Q 46
M 9
H 1
UL 6,14
M 21
UH 7,14
AD 168,6
M 20
AD 192,7
UL 6,15
M 22
H 2
UH 7,15
M 23
R 10
M 11
AD 216,6
AD 240,7
M 10
WL 2
R 8
M 34
UL 6,16
UH 7,16
M 35
AD 169,6
AD 193,7
M 32
UL 6,17
UH 7,17
M 33
AD 217,6
AD 241,7
M 30
UL 6,18
UH 7,18
M 31
AD 170,6
AD 194,7
M 28
UL 6,19
UH 7,19
M 29
AD 218,6
AD 242,7
M 26
UL 6,20
UH 7,20
M 27
WV 5
AD 171,6
AD 195,7
M 24
WL 0
FP
UL 6,21
M 25
UH 7,21
AD 219,6
M 36
AD 243,7
M 37
M 38
M 39
M 40
M 41
M 42
M 43
M 44
M 45
FR
M 46
M 47
M 58
M 59
M 56
M 57
M 54
M 55
WL 0
FQ 256
M 52
M 53
M 50
FZ 256
R 24
M 51
mw 72
R 26
M 48
TY 106,520
TY 110,14344
M 49
R 28
M 60
ad 84,2064
W 0
M 61
M 62
M 63
W 1
M 64
M 65
M 66
W 2
M 67
WL 3
AN 104,106
AN 105,107
M 68
SH 106
SH 107
M 69
mw 75
AN 106,106
AN 107,107
M 70
WL 2
AN 108,110
AN 109,111
M 71
R 30
M 82
SH 110
SH 111
M 83
AN 110,110
AN 111,111
M 80
TY 114,2056
TY 118,15880
M 81
ad 84,2070
W 0
M 78
M 79
M 76
W 1
M 77
M 74
M 75
W 2
M 72
M 73
R 32
M 84
mw 0
WL 3
AN 112,114
AN 113,115
M 85
SH 114
SH 115
M 86
AN 114,114
AN 115,115
M 87
WL 2
AN 116,118
AN 117,119
M 88
SH 118
SH 119
M 89
AN 118,118
AN 119,119
M 90
TY 122,3592
TY 126,17416
M 91
ad 84,2112
M 92
W 0
M 93
ae 44,68,864
M 94
s_bitcmp1_b32 s50, 16
s_cselect_b32 s51, 24576, 0
v_add_u32 v47, s51, v3
M 95
R 34
M 106
W 1
XL 30,256
M 107
XL 32,1024
M 104
XL 34,1792
M 105
WL 3
AN 120,122
AN 121,123
M 102
SH 122
SH 123
M 103
AN 122,122
AN 123,123
)"
R"(M 100
WL 2
AN 124,126
AN 125,127
M 101
SH 126
SH 127
M 98
AN 126,126
AN 127,127
M 99
W 2
XL 36,2560
M 96
M 97
R 48
M 108
mw 3
M 109
ad 84,2118
W 0
M 110
M 111
M 112
W 1
M 113
M 114
M 115
W 2
M 116
M 117
M 118
bc 49,23,66,60
M 119
WL 2
R 50
M 120
mh 18
M 121
ad 66,4480
H 0
M 133
M 132
TX 58,0
TX 62,13824
M 134
DG 1024
M 135
M 123
M 122
TY 66,512
TY 70,14336
M 124
bc 48,23,64,56
M 125
mh 21
M 137
ad 64,4480
H 0
M 136
TX 74,1536
TX 78,15360
M 138
WL 4
Q 0
M 139
Q 2
M 127
Q 4
M 126
TY 82,2048
TY 86,15872
M 128
Q 6
M 129
WL 4
Q 8
M 141
Q 10
M 140
TX 90,3072
TX 94,16896
M 142
Q 12
M 143
R 54
M 131
Q 14
M 130
TY 98,3584
TY 102,17408
M 0
R 52
M 1
WL 10
Q 16
M 13
Q 18
M 12
Q 20
M 14
Q 22
M 15
WL 8
Q 24
M 3
Q 26
M 2
Q 28
M 4
Q 30
M 5
WL 6
Q 32
M 17
Q 34
M 16
Q 36
M 18
Q 38
M 19
WL 2
Q 40
M 7
Q 42
M 6
Q 44
M 8
Q 46
M 9
H 1
UL 6,22
M 21
UH 7,22
AD 172,6
M 20
AD 196,7
UL 6,23
M 22
H 2
UH 7,23
M 23
R 58
M 11
AD 220,6
AD 244,7
M 10
WL 2
R 56
M 34
UL 6,24
UH 7,24
M 35
AD 173,6
AD 197,7
M 32
UL 6,25
UH 7,25
M 33
AD 221,6
AD 245,7
M 30
UL 6,26
UH 7,26
M 31
AD 174,6
AD 198,7
M 28
UL 6,27
UH 7,27
M 29
AD 222,6
AD 246,7
M 26
UL 6,28
UH 7,28
M 27
WV 5
AD 175,6
AD 199,7
M 24
WL 0
FP
UL 6,29
M 25
UH 7,29
AD 223,6
M 36
AD 247,7
M 37
M 38
M 39
M 40
M 41
M 42
M 43
M 44
M 45
FR
M 46
M 47
M 58
M 59
M 56
M 57
M 54
M 55
WL 0
FQ 260
M 52
M 53
M 50
FZ 260
R 72
M 51
mw 24
R 74
M 48
TY 106,5128
TY 110,18952
M 49
R 76
M 60
ad 84,2160
W 0
M 61
M 62
M 63
W 1
M 64
M 65
M 66
W 2
M 67
WL 3
AN 104,106
AN 105,107
M 68
SH 106
SH 107
M 69
mw 27
AN 106,106
AN 107,107
M 70
WL 2
AN 108,110
AN 109,111
M 71
R 78
M 82
SH 110
SH 111
M 83
AN 110,110
AN 111,111
M 80
TY 114,6664
TY 118,20488
M 81
ad 84,2166
W 0
M 78
M 79
M 76
W 1
M 77
M 74
M 75
W 2
M 72
M 73
R 80
M 84
mw 48
WL 3
AN 112,114
AN 113,115
M 85
SH 114
SH 115
M 86
AN 114,114
AN 115,115
M 87
WL 2
AN 116,118
AN 117,119
M 88
SH 118
SH 119
M 89
AN 118,118
AN 119,119
M 90
TY 122,8200
TY 126,22024
M 91
ad 84,2208
M 92
W 0
M 93
ae 44,68,864
M 94
s_bfe_u32 s51, s50, 0x10011
v_mad_i32_i24 v47, v49, s51, v48
XL 38,256
M 95
R 82
M 106
W 1
XL 40,1024
M 107
M 104
M 105
WL 3
AN 120,122
AN 121,123
M 102
SH 122
SH 123
M 103
AN 122,122
AN 123,123
M 100
WL 2
AN 124,126
AN 125,127
M 101
SH 126
SH 127
M 98
AN 126,126
AN 127,127
M 99
W 2
XL 42,1792
M 96
M 97
R 0
M 108
mw 51
XL 44,2560
M 109
ad 84,2214
W 0
M 110
M 111
M 112
W 1
M 113
M 114
M 115
W 2
M 116
M 117
M 118
bc 48,0,64,56
M 119
WL 2
R 2
M 120
mh 3
M 121
ad 64,64
H 0
M 133
M 132
TX 58,4608
TX 62,18432
M 134
H 1
M 135
M 123
M 122
TY 66,5120
TY 70,18944
M 124
H 2
M 125
M 137
M 136
TX 74,6144
TX 78,19968
M 138
WL 4
Q 0
M 139
Q 2
M 127
Q 4
M 126
TY 82,6656
TY 86,20480
M 128
Q 6
M 129
WL 4
Q 8
M 141
)"
R"(Q 10
M 140
TX 90,7680
TX 94,21504
M 142
Q 12
M 143
R 6
M 131
Q 14
M 130
TY 98,8192
TY 102,22016
M 0
R 4
M 1
WL 10
Q 16
M 13
Q 18
M 12
Q 20
M 14
Q 22
M 15
WL 8
Q 24
M 3
Q 26
M 2
Q 28
M 4
Q 30
M 5
WL 6
Q 32
M 17
Q 34
M 16
Q 36
M 18
Q 38
M 19
WL 2
Q 40
M 7
Q 42
M 6
Q 44
M 8
Q 46
M 9
UL 6,30
UH 7,30
M 21
AD 176,6
AD 200,7
M 20
UL 6,31
UH 7,31
M 22
AD 224,6
AD 248,7
M 23
R 10
M 11
UL 6,32
UH 7,32
M 10
WL 2
R 8
M 34
AD 177,6
AD 201,7
M 35
UL 6,33
UH 7,33
M 32
AD 225,6
AD 249,7
M 33
UL 6,34
UH 7,34
M 30
AD 178,6
AD 202,7
M 31
UL 6,35
UH 7,35
M 28
AD 226,6
AD 250,7
M 29
UL 6,36
UH 7,36
M 26
AD 179,6
AD 203,7
M 27
WV 3
UL 6,37
UH 7,37
M 24
WL 0
FP
AD 227,6
M 25
AD 251,7
M 36
M 37
M 38
M 39
M 40
M 41
M 42
M 43
M 44
M 45
FR
M 46
M 47
M 58
M 59
M 56
M 57
M 54
M 55
WL 0
FQ 264
M 52
M 53
M 50
FZ 264
R 24
M 51
mw 72
R 26
M 48
TY 106,9736
TY 110,23560
M 49
R 28
M 60
ad 84,2256
W 0
M 61
M 62
M 63
W 1
M 64
M 65
M 66
W 2
M 67
WL 3
AN 104,106
AN 105,107
M 68
SH 106
SH 107
M 69
mw 75
AN 106,106
AN 107,107
M 70
WL 2
AN 108,110
AN 109,111
M 71
R 30
M 82
SH 110
SH 111
M 83
AN 110,110
AN 111,111
M 80
TY 114,11272
TY 118,25096
M 81
ad 84,2262
W 0
M 78
M 79
M 76
W 1
M 77
M 74
M 75
W 2
M 72
M 73
R 32
M 84
mw 0
WL 3
AN 112,114
AN 113,115
M 85
SH 114
SH 115
M 86
AN 114,114
AN 115,115
M 87
WL 2
AN 116,118
AN 117,119
M 88
SH 118
SH 119
M 89
AN 118,118
AN 119,119
M 90
TY 122,12808
TY 126,26632
M 91
ad 84,0
M 92
W 0
M 93
M 94
M 95
R 34
M 106
W 1
M 107
M 104
M 105
WL 3
AN 120,122
AN 121,123
M 102
SH 122
SH 123
M 103
AN 122,122
AN 123,123
M 100
WL 2
AN 124,126
AN 125,127
M 101
SH 126
SH 127
M 98
AN 126,126
AN 127,127
M 99
W 2
M 96
M 97
R 48
M 108
mw 3
M 109
ad 84,6
W 0
M 110
M 111
M 112
W 1
M 113
M 114
M 115
W 2
M 116
M 117
M 118
bc 48,1,64,56
M 119
WL 2
R 50
M 120
mh 15
M 121
ad 64,259
H 0
M 133
M 132
TX 58,9216
TX 62,23040
M 134
DG 1024
M 135
M 123
M 122
TY 66,9728
TY 70,23552
M 124
M 125
M 137
M 136
TX 74,10752
TX 78,24576
M 138
WL 4
Q 0
M 139
Q 2
M 127
Q 4
M 126
TY 82,11264
TY 86,25088
M 128
Q 6
M 129
WL 4
Q 8
M 141
Q 10
M 140
TX 90,12288
TX 94,26112
M 142
Q 12
M 143
R 54
M 131
Q 14
M 130
TY 98,12800
TY 102,26624
M 0
R 52
M 1
WL 10
Q 16
M 13
Q 18
M 12
Q 20
M 14
Q 22
M 15
WL 8
Q 24
M 3
Q 26
M 2
Q 28
M 4
Q 30
M 5
WL 6
Q 32
M 17
Q 34
M 16
Q 36
M 18
Q 38
M 19
WL 2
Q 40
M 7
Q 42
M 6
Q 44
M 8
Q 46
M 9
UL 6,38
UH 7,38
M 21
AD 180,6
AD 204,7
M 20
UL 6,39
UH 7,39
M 22
AD 228,6
AD 252,7
M 23
R 58
M 11
UL 6,40
UH 7,40
M 10
WL 2
R 56
M 34
AD 181,6
AD 205,7
M 35
UL 6,41
UH 7,41
M 32
AD 229,6
AD 253,7
BV 52,4608
M 33
UL 6,42
UH 7,42
M 30
AD 182,6
AD 206,7
M 31
)"
R"(UL 6,43
UH 7,43
M 28
AD 230,6
AD 254,7
M 29
UL 6,44
UH 7,44
M 26
AD 183,6
AD 207,7
M 27
WV 8
UL 6,45
UH 7,45
M 24
WL 1
FP
AD 231,6
M 25
AD 255,7
M 36
M 37
M 38
M 39
M 40
ds_read_b64 v[12:13], v4 offset:4864
ds_read_b64 v[46:47], v4 offset:4872
M 41
AR 6,36
AR 7,60
M 42
AR 8,84
AR 9,108
M 43
WL 3
EF 6,52
EF 7,53
M 44
EF 8,54
EF 9,55
M 45
FR
CV 14,6,7
M 46
CV 15,8,9
AR 6,37
M 47
AR 7,61
AR 8,85
M 58
AR 9,109
EF 6,52
M 59
EF 7,53
EF 8,54
M 56
EF 9,55
CV 16,6,7
M 57
CV 17,8,9
AR 6,38
M 54
AR 7,62
AR 8,86
M 55
WL 0
FQ 268
AR 9,110
M 52
EF 6,52
EF 7,53
M 53
EF 8,54
EF 9,55
M 50
FZ 268
R 72
M 51
mw 24
R 74
M 48
TY 114,2056
TY 118,15880
M 49
R 76
M 60
ad 84,48
W 0
M 61
CV 18,6,7
CV 19,8,9
M 62
AR 6,39
AR 7,63
M 63
W 1
AR 8,87
M 64
AR 9,111
EF 6,52
M 65
EF 7,53
EF 8,54
M 66
W 2
EF 9,55
M 67
WL 3
AN 112,114
AN 113,115
M 68
SH 114
SH 115
M 69
mw 27
AN 114,114
AN 115,115
M 70
WL 2
AN 116,118
AN 117,119
M 71
R 78
M 82
SH 118
SH 119
M 83
AN 118,118
AN 119,119
M 80
TY 122,3592
TY 126,17416
M 81
ad 84,54
W 0
M 78
CV 20,6,7
CV 21,8,9
M 79
ae 42,74,768
AR 6,40
M 76
W 1
AR 7,64
M 77
s_bitcmp1_b32 s50, 12
s_cselect_b32 s52, 24576, 0
v_add_u32 v50, s52, v3
M 74
ST 14,0
AR 8,88
M 75
W 2
AR 9,112
M 72
EF 6,52
EF 7,53
M 73
WL 1
R 80
M 84
mw 48
AN 120,122
AN 121,123
M 85
SH 122
SH 123
M 86
AN 122,122
AN 123,123
M 87
WL 2
AN 124,126
AN 125,127
M 88
SH 126
SH 127
M 89
AN 126,126
AN 127,127
M 90
ad 84,96
W 0
M 91
ST 16,768
EF 8,54
M 92
EF 9,55
CV 22,6,7
M 93
W 1
CV 23,8,9
M 94
AR 6,41
AR 7,65
M 95
R 82
M 106
W 2
ST 18,1536
M 107
AR 8,89
AR 9,113
M 104
EF 6,52
EF 7,53
M 105
mw 51
EF 8,54
EF 9,55
M 102
ad 84,102
W 0
M 103
ST 20,2304
CV 24,6,7
M 100
CV 25,8,9
AR 6,42
M 101
W 1
AR 7,66
M 98
AR 8,90
AR 9,114
M 99
EF 6,52
EF 7,53
M 96
W 2
EF 8,54
M 97
EF 9,55
CV 26,6,7
M 108
CV 27,8,9
AR 6,43
M 109
AR 7,67
AR 8,91
M 110
AR 9,115
EF 6,52
M 111
EF 7,53
EF 8,54
M 112
EF 9,55
CV 28,6,7
M 113
CV 29,8,9
M 114
ae 42,74,768
s_bfe_u32 s52, s50, 0x1000d
BV 52,5120
M 115
WV 18
v_mad_i32_i24 v50, v49, s52, v48
ST 22,0
M 116
WL 1
AR 6,132
AR 7,156
M 117
s_barrier
R 0
M 118
mw 72
AR 8,180
EF 6,12
M 119
R 2
M 120
ad 84,144
W 0
M 121
ST 24,768
EF 7,13
M 133
EF 8,46
EF 136,47
M 132
CV 30,6,7
CV 31,8,136
M 134
W 1
AR 6,133
M 135
ST 26,1536
AR 7,157
M 123
AR 8,181
EF 6,12
M 122
EF 7,13
EF 8,46
M 124
W 2
EF 137,47
M 125
ST 28,2304
CV 32,6,7
M 137
CV 33,8,137
AR 6,134
M 136
TX 74,1536
TX 78,15360
M 138
mw 75
AR 7,158
AR 8,182
M 139
ad 84,150
W 0
M 127
EF 6,12
EF 7,13
M 126
TY 82,2048
TY 86,15872
M 128
EF 8,46
EF 138,47
M 129
W 1
CV 34,6,7
M 141
CV 35,8,138
AR 6,135
M 140
WL 2
TX 90,3072
TX 94,16896
P 16
M 142
Q 17
P 19
M 143
R 6
P 20
M 131
Q 21
P 23
M 130
WL 4
TY 98,3584
TY 102,17408
P 24
M 4
R 4
P 25
M 5
Q 26
P 28
M 17
Q 29
P 31
M 16
WL 6
Q 32
M 18
Q 34
M 19
Q 36
M 7
Q 38
M 6
WL 2
Q 40
M 8
Q 42
M 9
Q 44
M 21
Q 46
M 20
W 2
AR 7,159
M 22
AR 8,183
EF 6,12
M 23
R 10
M 11
EF 7,13
EF 8,46
M 10
WL 2
R 8
M 34
EF 139,47
CV 36,6,7
M 35
CV 37,8,139
M 32
ae 42,74,768
AR 6,136
M 33
)"
R"(s_bitcmp1_b32 s50, 12
s_cselect_b32 s52, 24576, 0
v_add_u32 v50, s52, v3
M 30
ST 30,128
AR 7,160
M 31
AR 8,184
EF 6,12
M 28
EF 7,13
EF 8,46
M 29
EF 140,47
CV 38,6,7
M 40
ST 32,896
CV 39,8,140
M 41
AR 6,137
AR 7,161
M 42
AR 8,185
EF 6,12
M 43
EF 7,13
EF 8,46
M 44
WV 21
ST 34,1664
EF 141,47
M 45
WL 0
CV 40,6,7
CV 41,8,141
M 46
s_barrier
R 24
M 47
R 26
M 58
TY 122,8200
TY 126,22024
M 59
ST 36,2432
AR 6,138
M 56
AR 7,162
AR 8,186
M 57
EF 6,12
EF 7,13
M 54
EF 8,46
EF 142,47
M 55
CV 42,6,7
CV 43,8,142
M 52
AR 6,139
AR 7,163
M 53
R 28
M 64
AR 8,187
EF 6,12
M 65
WL 3
AN 120,122
AN 121,123
M 66
SH 122
SH 123
M 67
AN 122,122
AN 123,123
M 68
WL 2
AN 124,126
AN 125,127
M 69
SH 126
SH 127
M 70
AN 126,126
AN 127,127
M 71
R 30
M 82
EF 7,13
EF 8,46
M 83
EF 143,47
CV 44,6,7
M 80
CV 45,8,143
M 81
ae 42,74,768
ds_read_b64 v[12:13], v4 offset:4608
ds_read_b64 v[46:47], v4 offset:4616
M 78
s_bfe_u32 s52, s50, 0x1000d
v_mad_i32_i24 v50, v49, s52, v48
ST 38,128
M 79
EF 160,52
EF 184,53
M 76
EF 208,54
EF 232,55
M 77
R 32
M 88
TX 58,7680
TX 62,21504
M 89
ST 40,896
CV 14,160,184
M 90
CV 15,208,232
EF 161,52
M 91
EF 185,53
EF 209,54
M 92
TY 66,8192
TY 70,22016
M 93
ST 42,1664
EF 233,55
M 94
CV 16,161,185
CV 17,209,233
M 95
WL 7
R 34
M 106
EF 162,52
EF 186,53
M 107
WL 4
PA_ 56,120,58
PA_ 57,121,59
M 104
PB_ 58,120,58
PB_ 59,121,59
M 105
PA_ 60,124,62
PA_ 61,125,63
M 102
PB_ 62,124,62
PB_ 63,125,63
M 103
WL 2
PA_ 64,122,66
PA_ 65,123,67
M 100
PB_ 66,122,66
PB_ 67,123,67
M 101
PA_ 68,126,70
PA_ 69,127,71
M 112
PB_ 70,126,70
PB_ 71,127,71
M 113
ST 44,2432
EF 210,54
M 114
EF 234,55
CV 18,162,186
M 115
CV 19,210,234
EF 163,52
M 116
WV 18
EF 187,53
EF 211,54
M 117
WL 0
EF 235,55
CV 20,163,187
M 118
s_barrier
R 48
M 119
R 50
M 124
CV 21,211,235
M 125
ae 42,74,768
EF 164,52
M 137
s_bitcmp1_b32 s50, 12
s_cselect_b32 s52, 24576, 0
v_add_u32 v50, s52, v3
M 136
ST 14,256
EF 188,53
M 138
EF 212,54
EF 236,55
M 139
CV 22,164,188
CV 23,212,236
M 127
EF 165,52
EF 189,53
M 126
ST 16,1024
EF 213,54
M 128
EF 237,55
CV 24,165,189
M 129
CV 25,213,237
EF 166,52
M 141
EF 190,53
EF 214,54
M 140
ST 18,1792
EF 238,55
M 142
CV 26,166,190
CV 27,214,238
M 143
R 54
M 131
EF 167,52
EF 191,53
M 130
WL 2
R 52
MA 52,0,56
ST 20,2560
EF 215,54
MA 52,4,60
EF 239,55
CV 28,167,191
MA 76,12,60
CV 29,215,239
MA 76,8,56
BV 52,4864
AR 6,44
MA 80,8,64
ae 42,74,768
AR 7,68
MA 80,12,68
s_bfe_u32 s52, s50, 0x1000d
R 58
MA 56,4,68
v_mad_i32_i24 v50, v49, s52, v48
ST 22,256
MA 56,0,64
WL 3
R 56
MA 104,16,64
AR 8,92
AR 9,116
MA 104,20,68
EF 6,12
EF 7,13
MA 100,16,56
ST 24,1024
EF 8,46
MA 100,20,60
EF 9,47
CV 30,6,7
MA 124,24,56
WV 14
CV 31,8,9
AR 6,45
MA 124,28,60
WL 0
AR 7,69
AR 8,93
MA 128,24,64
s_barrier
R 72
MA 128,28,68
R 74
MA 152,0,64
ST 26,1792
AR 9,117
MA 152,4,68
EF 6,12
EF 7,13
MA 148,0,56
EF 8,46
EF 9,47
MA 148,4,60
R 76
MA 172,8,56
ST 28,2560
CV 32,6,7
MA 172,12,60
CV 33,8,9
AR 6,46
MA 176,8,64
AR 7,70
AR 8,94
MA 176,12,68
WL 4
R 78
MV 132,16,64
AR 9,118
EF 6,12
MV 132,20,68
EF 7,13
EF 8,46
MA 196,16,56
EF 9,47
CV 34,6,7
MA 196,20,60
WL 4
R 80
MV 152,24,56
CV 35,8,9
AR 6,47
MV 152,28,60
AR 7,71
AR 8,95
MV 156,24,64
AR 9,119
EF 6,12
MV 156,28,68
WL 4
R 82
MV 180,0,64
EF 7,13
EF 8,46
MV 180,4,68
EF 9,47
CV 36,6,7
MV 176,0,56
CV 37,8,9
MV 176,4,60
WL 4
AR 6,48
AR 7,72
AR 8,96
AR 9,120
MV 200,8,56
ae 42,72,768
EF 6,12
EF 7,13
EF 8,46
MV 200,12,60
s_bitcmp1_b32 s50, 14
s_cselect_b32 s52, 24576, 0
v_add_u32 v50, s52, v3
EF 9,47
CV 38,6,7
MV 204,8,64
ST 30,0
CV 39,8,9
AR 6,49
AR 7,73
MV 204,12,68
WL 0
AR 8,97
AR 9,121
EF 6,12
EF 7,13
MV 224,16,56
EF 8,46
EF 9,47
CV 40,6,7
CV 41,8,9
MV 224,20,60
AR 6,50
AR 7,74
AR 8,98
AR 9,122
MV 248,28,60
ST 32,768
EF 6,12
EF 7,13
EF 8,46
MV 248,24,56
EF 9,47
CV 42,6,7
CV 43,8,9
AR 6,51
MV 252,24,64
AR 7,75
AR 8,99
AR 9,123
EF 6,12
MV 252,28,68
EF 7,13
EF 8,46
EF 9,47
CV 44,6,7
MV 228,20,68
ST 34,1536
CV 45,8,9
MV 228,16,64
WV 13
)"
R"(ST 36,2304
ae 42,72,768
s_bfe_u32 s52, s50, 0x1000f
v_mad_i32_i24 v50, v49, s52, v48
ST 38,0
ST 40,768
ST 42,1536
ST 44,2304
ds_read_b64 v[12:13], v4 offset:5120
ds_read_b64 v[46:47], v4 offset:5128
AR 6,140
AR 7,164
AR 8,188
EF 6,52
EF 7,53
EF 8,54
EF 144,55
CV 14,6,7
CV 15,8,144
AR 6,141
AR 7,165
AR 8,189
EF 6,52
EF 7,53
EF 8,54
EF 145,55
CV 16,6,7
CV 17,8,145
AR 6,142
AR 7,166
AR 8,190
EF 6,52
EF 7,53
EF 8,54
EF 146,55
CV 18,6,7
CV 19,8,146
AR 6,143
AR 7,167
AR 8,191
EF 6,52
EF 7,53
EF 8,54
EF 147,55
CV 20,6,7
CV 21,8,147
ae 42,72,768
s_bitcmp1_b32 s50, 14
s_cselect_b32 s52, 24576, 0
v_add_u32 v50, s52, v3
ST 14,128
ST 16,896
ST 18,1664
ST 20,2432
AR 6,144
AR 7,168
AR 8,192
EF 6,52
EF 7,53
EF 8,54
EF 148,55
CV 22,6,7
CV 23,8,148
AR 6,145
AR 7,169
AR 8,193
EF 6,52
EF 7,53
EF 8,54
EF 149,55
CV 24,6,7
CV 25,8,149
AR 6,146
AR 7,170
AR 8,194
EF 6,52
EF 7,53
EF 8,54
EF 150,55
CV 26,6,7
CV 27,8,150
AR 6,147
AR 7,171
AR 8,195
EF 6,52
EF 7,53
EF 8,54
EF 151,55
CV 28,6,7
CV 29,8,151
ae 42,72,768
s_bfe_u32 s52, s50, 0x1000f
v_mad_i32_i24 v50, v49, s52, v48
ST 22,128
ST 24,896
ST 26,1664
ST 28,2432
BV 52,4608
WL 1
EF 168,12
EF 192,13
EF 216,46
EF 240,47
CV 30,168,192
CV 31,216,240
EF 169,12
EF 193,13
EF 217,46
EF 241,47
CV 32,169,193
CV 33,217,241
EF 170,12
EF 194,13
EF 218,46
EF 242,47
CV 34,170,194
CV 35,218,242
EF 171,12
EF 195,13
EF 219,46
EF 243,47
CV 36,171,195
CV 37,219,243
ae 42,72,768
s_bitcmp1_b32 s50, 14
s_cselect_b32 s52, 24576, 0
v_add_u32 v50, s52, v3
ST 30,256
ST 32,1024
ST 34,1792
ST 36,2560
EF 172,12
EF 196,13
EF 220,46
EF 244,47
CV 38,172,196
CV 39,220,244
EF 173,12
EF 197,13
EF 221,46
EF 245,47
CV 40,173,197
CV 41,221,245
EF 174,12
EF 198,13
EF 222,46
EF 246,47
CV 42,174,198
CV 43,222,246
EF 175,12
EF 199,13
EF 223,46
EF 247,47
CV 44,175,199
CV 45,223,247
ae 42,72,768
s_bfe_u32 s52, s50, 0x1000f
v_mad_i32_i24 v50, v49, s52, v48
ST 38,256
ST 40,1024
ST 42,1792
ST 44,2560
ds_read_b64 v[12:13], v4 offset:4864
ds_read_b64 v[46:47], v4 offset:4872
AR 6,52
AR 7,76
AR 8,100
AR 9,124
WL 2
EF 6,52
EF 7,53
EF 8,54
EF 9,55
CV 14,6,7
CV 15,8,9
AR 6,53
AR 7,77
AR 8,101
AR 9,125
EF 6,52
EF 7,53
EF 8,54
EF 9,55
CV 16,6,7
CV 17,8,9
AR 6,54
AR 7,78
AR 8,102
AR 9,126
EF 6,52
EF 7,53
EF 8,54
EF 9,55
CV 18,6,7
CV 19,8,9
AR 6,55
AR 7,79
AR 8,103
AR 9,127
EF 6,52
EF 7,53
EF 8,54
EF 9,55
CV 20,6,7
CV 21,8,9
ae 42,72,864
s_bitcmp1_b32 s50, 16
s_cselect_b32 s52, 24576, 0
v_add_u32 v50, s52, v3
ST 14,0
ST 16,768
ST 18,1536
ST 20,2304
AR 6,56
AR 7,80
AR 8,104
AR 9,128
EF 6,52
EF 7,53
EF 8,54
EF 9,55
CV 22,6,7
CV 23,8,9
AR 6,57
AR 7,81
AR 8,105
AR 9,129
EF 6,52
EF 7,53
EF 8,54
EF 9,55
CV 24,6,7
CV 25,8,9
AR 6,58
AR 7,82
AR 8,106
AR 9,130
EF 6,52
EF 7,53
EF 8,54
EF 9,55
CV 26,6,7
CV 27,8,9
AR 6,59
AR 7,83
AR 8,107
AR 9,131
EF 6,52
EF 7,53
EF 8,54
EF 9,55
CV 28,6,7
CV 29,8,9
ae 42,72,864
s_bfe_u32 s52, s50, 0x10011
v_mad_i32_i24 v50, v49, s52, v48
ST 22,0
ST 24,768
ST 26,1536
ST 28,2304
BV 52,5120
AR 6,148
AR 7,172
AR 8,196
WL 1
EF 6,12
EF 7,13
EF 8,46
EF 152,47
CV 30,6,7
CV 31,8,152
AR 6,149
AR 7,173
AR 8,197
EF 6,12
EF 7,13
EF 8,46
EF 153,47
CV 32,6,7
CV 33,8,153
AR 6,150
AR 7,174
AR 8,198
EF 6,12
EF 7,13
EF 8,46
EF 154,47
CV 34,6,7
CV 35,8,154
AR 6,151
AR 7,175
AR 8,199
EF 6,12
EF 7,13
EF 8,46
EF 155,47
CV 36,6,7
CV 37,8,155
ae 42,72,864
s_bitcmp1_b32 s50, 16
s_cselect_b32 s52, 24576, 0
v_add_u32 v50, s52, v3
ST 30,128
ST 32,896
ST 34,1664
ST 36,2432
AR 6,152
AR 7,176
EF 6,12
EF 7,13
EF 132,46
EF 156,47
CV 38,6,7
CV 39,132,156
AR 6,153
AR 7,177
EF 6,12
EF 7,13
EF 133,46
EF 157,47
CV 40,6,7
CV 41,133,157
AR 6,154
AR 7,178
EF 6,12
EF 7,13
EF 134,46
EF 158,47
CV 42,6,7
CV 43,134,158
AR 6,155
AR 7,179
EF 6,12
EF 7,13
EF 135,46
EF 159,47
CV 44,6,7
CV 45,135,159
ae 42,72,864
s_bfe_u32 s52, s50, 0x10011
v_mad_i32_i24 v50, v49, s52, v48
ST 38,128
ST 40,896
ST 42,1664
ST 44,2432
WL 0
EF 176,52
EF 200,53
EF 224,54
EF 248,55
CV 14,176,200
CV 15,224,248
EF 177,52
EF 201,53
EF 225,54
EF 249,55
CV 16,177,201
CV 17,225,249
EF 178,52
EF 202,53
EF 226,54
EF 250,55
CV 18,178,202
CV 19,226,250
EF 179,52
EF 203,53
EF 227,54
EF 251,55
CV 20,179,203
CV 21,227,251
ae 42,72,864
s_bitcmp1_b32 s50, 16
s_cselect_b32 s52, 24576, 0
v_add_u32 v50, s52, v3
ST 14,256
ST 16,1024
ST 18,1792
ST 20,2560
EF 180,52
EF 204,53
EF 228,54
EF 252,55
CV 22,180,204
CV 23,228,252
EF 181,52
EF 205,53
EF 229,54
EF 253,55
CV 24,181,205
CV 25,229,253
EF 182,52
EF 206,53
EF 230,54
EF 254,55
CV 26,182,206
CV 27,230,254
EF 183,52
EF 207,53
EF 231,54
EF 255,55
)"
R"(CV 28,183,207
CV 29,231,255
ae 42,72,864
s_bfe_u32 s52, s50, 0x10011
v_mad_i32_i24 v50, v49, s52, v48
ST 22,256
ST 24,1024
ST 26,1792
ST 28,2560
s_waitcnt vmcnt(0) lgkmcnt(0)
s_branch 29f
FY 132
FY 136
FY 140
FY 144
FY 148
FY 152
FY 156
FY 160
FY 164
FY 168
FY 172
FY 176
FY 180
FY 184
FY 188
FY 192
FY 196
FY 200
FY 204
FY 208
FY 212
FY 216
FY 220
FY 224
FY 228
FY 232
FY 236
FY 240
FY 244
FY 248
FY 252
FY 256
FY 260
FY 264
FY 268
29:
.purgem MA
.purgem MV
.purgem NA
.purgem NV
.purgem AN
.purgem SH
.purgem PA_
.purgem PB_
.purgem AW
.purgem AR
.purgem UL
.purgem UH
.purgem AD
.purgem EF
.purgem TY
.purgem TX
.purgem RA
.purgem RC
.purgem BV
.purgem BA
.purgem PA
.purgem DF
.purgem DW
.purgem DR
.purgem DH
.purgem DG
.purgem XL
.purgem ST
.purgem SA
.purgem CV
.purgem RS
.purgem PM
.purgem PF
.purgem FS
.purgem FL
.purgem FB
.purgem FC
.purgem FG
.purgem FX
.purgem FP
.purgem FR
.purgem FQ
.purgem FZ
.purgem FY
.purgem WL
.purgem WV
.purgem M
.purgem R1
.purgem R
.purgem P
.purgem Q
.purgem mw
.purgem mh
.purgem ad
.purgem ae
.purgem bc
.purgem W
.purgem H
)"
      : [flg] "=&s"(flg) : K2_OPS : K2_CLOB);
  if (flg) {
    const unsigned lane = __builtin_amdgcn_mbcnt_hi(~0u, __builtin_amdgcn_mbcnt_lo(~0u, 0u));
    const u16* const volatile __attribute__((address_space(4)))* ka = (const u16* const volatile __attribute__((address_space(4)))*)__builtin_amdgcn_kernarg_segment_ptr();
    {
      const u16* w1p = ka[6];
#pragma unroll 1
      for (unsigned f = blockIdx.x * 4 + w; f < 1152; f += 1024) {
        const u16* p = w1p + (32 * (f % 12) + 8 * (lane >> 4)) * HID + 16 * (f / 12) + (lane & 15);
        u32x4 v;
#pragma unroll
        for (int j = 0; j < 4; ++j) v[j] = p[2 * j * HID] | (unsigned)p[(2 * j + 1) * HID] << 16;
        *(u32x4*)(g_buf + f * 1024 + lane * 16) = v;
      }
    }
    slow_bar((unsigned*)(g_buf + G_BAR), w);
    slow_fc1(ka[0], ka[5], (char*)ka[1], (float*)smem + w * HID, w);
    slow_bar((unsigned*)(g_buf + G_BAR + 64), w);
    slow_fc2(ka[0], (const char*)ka[1], (u16*)ka[2], ka[3], ka[4], smem, w);
  }
  TS(15);
  MK_END(4);
}

extern "C" void kernel_launch(void* const* d_in, const int* in_sizes, int n_in,
                              void* d_out, int out_size, void* d_ws, size_t ws_size,
                              hipStream_t stream) {
  const u16* x = (const u16*)d_in[0];
  const u16* dw = (const u16*)d_in[1];
  const u16* w1 = (const u16*)d_in[2];
  const u16* w2 = (const u16*)d_in[3];
  const u16* grn_g = (const u16*)d_in[4];
  const u16* grn_b = (const u16*)d_in[5];
  char* ws = (char*)d_ws;
  if (ws_size < H_BYTES) return;
  (void)hipFuncSetAttribute((const void*)k1_fc1, hipFuncAttributeMaxDynamicSharedMemorySize, K1_LDS);
  (void)hipFuncSetAttribute((const void*)k2_fc2, hipFuncAttributeMaxDynamicSharedMemorySize, K2_LDS);

  k0_prep<<<K0_NB + NPAGE + NPAGX, 256, 0, stream>>>(w1, dw, ws, (const char*)x, grn_b, w2);
  k1_fc1<<<NWG + 8, 256, K1_LDS, stream>>>(x, ws, (const u16*)d_out, grn_g);
  k2_fc2<<<NWG, 256, K2_LDS, stream>>>(x, ws, (u16*)d_out, grn_g, w2, dw, w1);
}
```

```cpp
#include <hip/hip_runtime.h>
#include <hip/hip_bf16.h>
#include <stdint.h>

typedef float f32x4 __attribute__((ext_vector_type(4)));
typedef float f32x2 __attribute__((ext_vector_type(2)));
typedef unsigned u32x4 __attribute__((ext_vector_type(4)));
typedef __bf16 bf16x2_t __attribute__((ext_vector_type(2)));
typedef unsigned short u16;

constexpr float TZ = 175.f, TH = 175.f, TW2 = 175.f;
#define DI __device__ __forceinline__
#define LAS __attribute__((address_space(3)))

constexpr int NPIX = 147456;
constexpr int CH = 384;
constexpr int HID = 1536;
constexpr int NWG = 256;

constexpr size_t H_BYTES = (size_t)NPIX * HID * 2;
constexpr unsigned K1_LDS = 163840, K1_PF_OFF = 130048, K1_SMAX = 12, K1_NREP = 4, K1_G_TAB = 1228800, K1_G_CNT = 1239040, K1_G_LOC = 1236992, K1_G_DUM = 1255424,
  K1_G_ACC = 1320960, K1_G_LUT = 1382400, K1_G_BYTES = 1398784;
constexpr unsigned K1_GST = 25600, K1_WCH = 6400, K1_ZC = 1365;
constexpr unsigned K1_SB1 = 32768;
constexpr unsigned K2_SHT = 12483;
constexpr int K2_LDS = 163840, K2_HB = 98304, K2_RSZ = 13824, K2_E_OFF = 153600, K2_PF_OFF = 159744, K2_S16_OFF = 152576, K2_S16_D0 = 8192, K2_NAT = 8;

__device__ __attribute__((aligned(256))) char g_buf[K1_G_BYTES];
__device__ __attribute__((aligned(256))) float g_cs[8 * 384];
__device__ __attribute__((aligned(256))) char g_w2r[96 * 12 * 1024];
__device__ __attribute__((aligned(256))) char g_w2f[96 * 12 * 1024];
__device__ __attribute__((aligned(256))) char g_w2q[16 * 12 * 1024];
__device__ __attribute__((aligned(256))) float g_sc[64];
constexpr unsigned G_FLAG = K1_G_CNT + 14336;
constexpr unsigned G_PUB = K1_G_DUM;
__device__ __attribute__((aligned(256))) unsigned g_prm[1024 * 64];
constexpr unsigned G_BAR = K1_G_CNT + 13312;
constexpr unsigned H_TS = 49152, H_RS = 48 * H_TS, NPAGE = 217, NPAGX = 55;

DI unsigned pack_bf16(float a, float b) {
  f32x2 v = {a, b};
  bf16x2_t r = __builtin_convertvector(v, bf16x2_t);
  return __builtin_bit_cast(unsigned, r);
}
typedef _Float16 f16x2_t __attribute__((ext_vector_type(2)));
DI unsigned pack_f16(float a, float b) {
  f32x2 v = {a, b};
  f16x2_t r = __builtin_convertvector(v, f16x2_t);
  return __builtin_bit_cast(unsigned, r);
}
DI float bf_lo(unsigned u) { return __uint_as_float(u << 16); }
DI float bf_hi(unsigned u) { return __uint_as_float(u & 0xffff0000u); }
DI float round_bf16(float a) { return bf_lo(pack_bf16(a, 0.f)); }
DI float ld_bf16(const u16* p) { return __uint_as_float((unsigned)(*p) << 16); }

DI unsigned lds_addr(const char* p) { return (unsigned)(size_t)(const LAS char*)p; }

DI float gelu_tanh(float x) {
  const float c0 = -2.0f * 0.7978845608028654f * 1.4426950408889634f;
  const float c1 = c0 * 0.044715f;
  float x2 = x * x;
  float t = __builtin_fmaf(x2, c1, c0);
  float e = __builtin_amdgcn_exp2f(x * t);
  float r = __builtin_amdgcn_rcpf(1.0f + e);
  return x * r;
}

DI void st16_wt(void* p, u32x4 v) { asm volatile("global_store_dwordx4 %0, %1, off\n\ts_nop 1" :: "v"(p), "v"(v) : "memory"); }
template <int I> struct IC { static constexpr int value = I; };
template <int N, int I = 0, typename F> DI void static_for(F&& f) {
  if constexpr (I < N) { f(IC<I>{}); static_for<N, I + 1>(f); }
}
template <int C, int M> DI float dpp_add(float v) { return v + __int_as_float(__builtin_amdgcn_update_dpp(0, __float_as_int(v), C, M, 0xf, false)); }
DI float wave_sum63(float v) {
  v = dpp_add<0x111, 0xf>(v); v = dpp_add<0x112, 0xf>(v); v = dpp_add<0x114, 0xf>(v); v = dpp_add<0x118, 0xf>(v);
  v = dpp_add<0x142, 0xa>(v); return dpp_add<0x143, 0xc>(v);
}
DI unsigned cs_index(unsigned n) { return (n & ~63u) + (n & 3) * 16 + ((n >> 2) & 15); }
DI uint64_t uni64(uint64_t v) { return (uint64_t)__builtin_amdgcn_readfirstlane(v >> 32) << 32 | (unsigned)__builtin_amdgcn_readfirstlane(v); }
DI unsigned wg_side(unsigned b) { return (((const unsigned*)(g_buf + K1_G_LOC))[b & 3] ^ (b >> 2)) & 1; }
DI unsigned h_flip(const char* ws, unsigned region, unsigned t) { return __popc(((const unsigned*)(g_buf + K1_G_LOC + 64))[(((unsigned)(size_t)ws & 0x1fffffu) + region * H_RS + t * H_TS) >> 21]) > 2; }
DI unsigned x_flip(const u16* x, unsigned region) { return __popc(((const unsigned*)(g_buf + K1_G_LOC + 1024))[(((unsigned)(size_t)x & 0x1fffffu) + (12 * region + 6) * 49152u) >> 21]) > 2; }
DI unsigned sub_pixel(const u16* x, unsigned sub, unsigned m) {
  const unsigned blk = sub / 24, u = sub % 24;
  const unsigned region = blk < 256 ? (blk >> 3) * 4 + (blk & 3) : 128 + ((blk - 256) >> 1), side = blk < 256 ? wg_side(blk) : blk & 1;
  return (12 * region + (u >> 1)) * 64 + ((0xda43cb52fe987610ull >> (32 * (u & 1) + 16 * (side ^ x_flip(x, region)) + (m & 12))) & 15) * 4 + (m & 3);
}
DI unsigned h_piece(const char* ws, unsigned sub, unsigned t) {
  const unsigned blk = sub / 24, u = sub % 24;
  const unsigned region = blk < 256 ? (blk >> 3) * 4 + (blk & 3) : 128 + ((blk - 256) >> 1), side = blk < 256 ? wg_side(blk) : blk & 1;
  return region * H_RS + t * H_TS + (u >> 3) * 16384 + (u & 7) * 1024 + ((side ^ h_flip(ws, region, t)) << 13);
}
constexpr unsigned K0_NB = 145 + 24 + 96 + 16 + 48;
constexpr float U24 = 0x1p-24f, EPS = 1e-5f;
DI float wave_all(float v) { return __int_as_float(__builtin_amdgcn_readlane(__float_as_int(wave_sum63(v)), 63)); }
DI float pow2f(float v) { return __uint_as_float(__float_as_uint(v) & 0x7f800000u); }
DI u32x4 w_load(const u16* w) { return *(const u32x4*)(w + ((threadIdx.x >> 3) & 7) * 73408 + 36800 + (threadIdx.x & 7) * 8); }
DI float w_ms(u32x4 a) {
  float q = 0.f;
#pragma unroll
  for (int i = 0; i < 4; ++i) { const float lo = bf_lo(a[i]), hi = bf_hi(a[i]); q = __builtin_fmaf(lo, lo, __builtin_fmaf(hi, hi, q)); }
  return wave_all(q) * (1.0f / 512);
}
DI float w1_scale(float ms) { return pow2f(300.f * U24 * __builtin_amdgcn_rsqf(ms)); }
DI float w2_scale(float ms) { return TW2 * U24 * __builtin_amdgcn_rsqf(ms); }
struct Sc { float w1, zs, g, h, vlo, vhi, c; };
DI Sc scales(const u16* w1, const u16* x, const u16* dw) {
  const u32x4 a = w_load(w1);
  const unsigned l = threadIdx.x & 63;
  float s = 0.f, q = 0.f;
#pragma unroll
  for (int k = 0; k < 6; ++k) {
    const u32x4 v = *(const u32x4*)(x + (9001 + 18397 * (l >> 3)) * CH + 64 * k + 8 * (l & 7)), d = *(const u32x4*)(dw + 64 * k + 8 * (l & 7));
#pragma unroll
    for (int i = 0; i < 4; ++i) { const float lo = bf_lo(v[i]) * bf_lo(d[i]), hi = bf_hi(v[i]) * bf_hi(d[i]); s += lo + hi; q = __builtin_fmaf(lo, lo, __builtin_fmaf(hi, hi, q)); }
  }
  s = dpp_add<0x141, 0xf>(dpp_add<0x4e, 0xf>(dpp_add<0xb1, 0xf>(s))); q = dpp_add<0x141, 0xf>(dpp_add<0x4e, 0xf>(dpp_add<0xb1, 0xf>(q)));
  const float var = fmaxf(q * (1.0f / CH) - s * s * (1.0f / (CH * CH)), 0.f);
  const float r2 = wave_all(var * __builtin_amdgcn_rcpf(var + EPS)) * (1.0f / 64), rden = __builtin_amdgcn_exp2f(wave_all(__builtin_amdgcn_logf(var + EPS)) * (-1.0f / 128));
  const float s1 = w_ms(a), p2 = r2 * CH * s1;
  Sc c;
  c.w1 = w1_scale(s1);
  c.g = TZ * U24 * __builtin_amdgcn_rsqf(r2);
  c.h = TH * U24 * __builtin_amdgcn_rsqf(p2 * (0.25f + 0.5f * p2 * __builtin_amdgcn_rcpf(1.0f + 2.0f * p2)));
  c.c = fminf(__builtin_ceilf(0.1701f * 0x1p24f * c.h) + 1.0f, 1400.f);
  c.zs = fminf(pow2f(c.g * 2530.f), pow2f(c.g * 0x1p12f * rden));
  const float e = EPS * c.zs * c.zs, z2 = r2 * (660.f * 660.f / (TZ * TZ));
  c.vlo = 4.2f * c.g * c.g - e;
  c.vhi = fminf(0x1.ep29f * c.g * c.g - e, z2 < 1.0f ? e * z2 * __builtin_amdgcn_rcpf(1.0f - z2) : 0x1p120f);
  return c;
}
__global__ __launch_bounds__(256) void k0_prep(const u16* __restrict__ w1, const u16* __restrict__ dw, const char* __restrict__ ws, const char* __restrict__ x,
                                                const u16* __restrict__ grn_b, const u16* __restrict__ w2) {
  uint64_t pc; int d1;
  asm volatile("s_getpc_b64 %0\n1:\n\ts_mov_b32 %1, k1_code-1b" : "=s"(pc), "=s"(d1));
  if (threadIdx.x >= 192) pc += (int64_t)d1 + ((int)(blockIdx.x >> 3) - 3) * 8192;
  unsigned pfd = 0;
  if (blockIdx.x < 64 && (threadIdx.x < 150 || (blockIdx.x < 48 && threadIdx.x >= 192))) pfd = *(const __attribute__((address_space(1))) unsigned*)((pc & ~127ull) + threadIdx.x * 128);
#define K0_END() do { asm volatile("" :: "v"(pfd)); } while (0)
  unsigned xcc;
  asm volatile("s_getreg_b32 %0, hwreg(HW_REG_XCC_ID)" : "=s"(xcc));
  xcc = (xcc >> 2) & 1;
  if (blockIdx.x < 4 && threadIdx.x == 0) ((unsigned*)(g_buf + K1_G_LOC))[blockIdx.x] = 7 * xcc;
  constexpr unsigned NP = NPAGE + NPAGX;
  __shared__ Sc ssc;
  __shared__ float ss2;
  const bool sw = threadIdx.x >= 192;
  const unsigned bi = blockIdx.x;
  const unsigned bx = bi < NP ? bi + K0_NB : bi - NP;
  if (bx >= K0_NB) {
    __shared__ unsigned lat[8];
    const unsigned pg = bx - K0_NB, w = threadIdx.x >> 6, lane = threadIdx.x & 63;
    auto gran = [&](unsigned p) {
      const bool h = p < NPAGE;
      const char* buf = h ? ws : x;
      const int o0 = (int)((h ? p : p - NPAGE) << 21) - (int)((unsigned)(size_t)buf & 0x1fffffu);
      const unsigned al = h ? 49152 : 12 * 49152;
      const unsigned q = ((((unsigned)max(o0, 0) + al - 1) >> (h ? 14 : 16)) * (h ? 87382u : 29128u)) >> 18;
      return buf + min(q * al, (unsigned)(h ? H_BYTES : H_BYTES / 4) - 16384u);
    };
    if (lane < 5) {
      const char* a = gran(lane < 4 ? min(pg + NP - 1 - (lane >> 1), pg - 1 - (lane >> 1)) : pg) + (lane & 1) * 8192 + w * 2048 + (lane < 4 ? 1024 : 0);
      const char* b = a;
      const char* own = (const char*)uni64(__builtin_amdgcn_readlane((uint64_t)a >> 32, 4) * 4294967296ull + (unsigned)__builtin_amdgcn_readlane((unsigned)(uint64_t)a, 4));
      unsigned long long t0, t1, t2;
      int n = 2;
      asm volatile("" : "+s"(n));
#pragma unroll 1
      do {
        unsigned v;
        asm volatile("s_cmp_eq_u32 %6, 2\n\ts_cbranch_scc1 1f\n\ts_sleep 4\n1:\n\ts_memtime %0\n\ts_waitcnt lgkmcnt(0)\n\tglobal_load_dword %3, %4, off sc1\n\ts_waitcnt vmcnt(0)\n\ts_memtime %1\n\ts_waitcnt lgkmcnt(0)\n\t"
                     "global_load_dword %3, %5, off sc1\n\ts_waitcnt vmcnt(0)\n\ts_memtime %2\n\ts_waitcnt lgkmcnt(0)" : "=&s"(t0), "=&s"(t1), "=&s"(t2), "=&v"(v) : "v"(a), "v"(b), "s"(n) : "memory", "scc");
        a = own + 1024 + (w & 1) * 8192; b = a + 8192 - (w & 1) * 16384;
      } while (--n);
      lat[w + 4 * (w & 1)] = (unsigned)(t1 - t0); lat[w + 4 - 4 * (w & 1)] = (unsigned)(t2 - t1);
    }
    __syncthreads();
    if (threadIdx.x == 0) {
      auto fastest = [&](const unsigned* l) { unsigned m = 9999; for (int i = 0; i < 4; ++i) m = l[i] < 300 ? m : min(m, l[i]); return (int)m; };
      const int df = fastest(lat) - fastest(lat + 4);
      const unsigned v = xcc ^ (df > 0), sure = abs(df) > 80;
      char* e = g_buf + K1_G_LOC + 64 + 4 * (pg < NPAGE ? pg : pg + 240 - NPAGE);
      *(u16*)(e + 2) = v ? 3 + 4 * sure : 1 - sure;
      if (pg == 0 || pg == NPAGE) e[0] = v; else e[-3] = v ^ 1;
      if (pg == NPAGE - 1 || pg == NP - 1) e[1] = v; else e[4] = v ^ 1;
    }
    K0_END();
    return;
  }
  if (bx >= 145 + 24 + 96 + 16) {
    __shared__ __attribute__((aligned(16))) float red[2][16][32];
    const int t = bx - 281, c8 = (threadIdx.x & 3) * 8;
    u32x4 sm, v[6];
    if (sw) sm = w_load(w1);
#pragma unroll
    for (int k = 0; k < 6; ++k) v[k] = *(const u32x4*)(w1 + (size_t)(64 * k + (threadIdx.x >> 2)) * HID + 32 * t + c8);
    if (sw) ss2 = w1_scale(w_ms(sm));
    __syncthreads();
    const float s1 = ss2;
    float a[8], q[8];
#pragma unroll
    for (int i = 0; i < 8; ++i) {
      a[i] = q[i] = 0.f;
#pragma unroll
      for (int k = 0; k < 6; ++k) {
        const float f = (float)(_Float16)(s1 * (i & 1 ? bf_hi(v[k][i >> 1]) : bf_lo(v[k][i >> 1]))) * 0x1p24f;
        a[i] += f; q[i] = __builtin_fmaf(f, f, q[i]);
      }
      a[i] = dpp_add<0x118, 0xf>(dpp_add<0x114, 0xf>(a[i])); q[i] = dpp_add<0x118, 0xf>(dpp_add<0x114, 0xf>(q[i]));
    }
    if ((threadIdx.x & 12) == 12) {
#pragma unroll
      for (int i = 0; i < 2; ++i) {
        *(f32x4*)&red[0][threadIdx.x >> 4][c8 + 4 * i] = (f32x4){a[4 * i], a[4 * i + 1], a[4 * i + 2], a[4 * i + 3]};
        *(f32x4*)&red[1][threadIdx.x >> 4][c8 + 4 * i] = (f32x4){q[4 * i], q[4 * i + 1], q[4 * i + 2], q[4 * i + 3]};
      }
    }
    __syncthreads();
    if (threadIdx.x < 64) {
      const int n = threadIdx.x & 31;
      float s = 0.f;
#pragma unroll
      for (int i = 0; i < 16; ++i) s += red[threadIdx.x >> 5][i][n];
      const float c = (0.f - (float)K1_ZC * s) * 0x1p-48f;
      char* p = g_buf + t * K1_GST + 5120;
      if (threadIdx.x < 32) *(f32x4*)(p + 272 * (n >> 4) + (n & 15) * 16) = (f32x4){c, c, c, c};
      const bool any = __ballot(threadIdx.x >= 32 && !(s >= 384.f * 64 * 64 && s <= 384.f * 1024 * 1024)) != 0;
      if (threadIdx.x == 0) g_buf[G_FLAG + 4 * 98 + t] = any;
    }
    K0_END();
    return;
  }
  if (bx >= 145 + 24 + 96) {
    if (sw) ssc = scales(w1, (const u16*)x, dw);
    __syncthreads();
    const Sc sc = ssc;
    const unsigned e = (bx - 265) * 256 + threadIdx.x;
    const float u = 1.0f / (sc.g * sc.w1);
    float r[2];
#pragma unroll
    for (int i = 0; i < 2; ++i) {
      const unsigned c = 2 * e + i;
      r[i] = __builtin_fmaf(sc.h, gelu_tanh(u * __uint_as_float((c >> 12) << 31 | ((c & 4095) + 79 * 128) << 16 | 0x8000u)), sc.c * U24);
      if (!(r[i] >= 0.f && r[i] < 8189.f * U24)) r[i] = __builtin_inff();
    }
    *(unsigned*)(g_buf + K1_G_LUT + 4 * e) = pack_f16(r[0], r[1]);
    K0_END();
    return;
  }
  if (bx >= 145 + 24) {
    __shared__ __attribute__((aligned(16))) u16 tw[32][200];
    const int kappa = (bx - 169) >> 1, nh = (bx - 169) & 1;
    u32x4 sm, v[3];
    if (sw) sm = w_load(w2);
#pragma unroll
    for (int i = 0; i < 3; ++i) { const int e = threadIdx.x + 256 * i; v[i] = *(const u32x4*)(w2 + (size_t)(32 * kappa + e / 24) * CH + 192 * nh + (e % 24) * 8); }
    if (sw) ss2 = w2_scale(w_ms(sm));
#pragma unroll
    for (int i = 0; i < 3; ++i) { const int e = threadIdx.x + 256 * i; *(u32x4*)&tw[e / 24][(e % 24) * 8] = v[i]; }
    __syncthreads();
    const int lane = threadIdx.x & 63, g = lane >> 4;
    const float s2 = 256.f * ss2;
    bool bad = false;
#pragma unroll
    for (int i = 0; i < 3; ++i) {
      const int fi = (threadIdx.x >> 6) + 4 * i, n = 64 * (fi >> 2) + 4 * (lane & 15) + (fi & 3);
      float wv[8];
      unsigned bits = 0;
#pragma unroll
      for (int j = 0; j < 8; ++j) {
        wv[j] = ld_bf16(&tw[16 * (j >> 2) + 4 * g + (j & 3)][n]);
        bits |= wv[j] != 0.f ? 256u << j : 0u;
        wv[j] *= s2;
        bits |= fabsf(wv[j]) >= 48 * 256 * U24 ? 1u << j : 0u;
        bad |= !(fabsf(wv[j]) <= 500.f);
      }
      const unsigned o = (((kappa & ~1) + fi / 6) * 24 + nh * 12 + (fi % 6) * 2 + (kappa & 1)) * 1024 + lane * 16;
      st16_wt(g_w2r + o, (u32x4){pack_f16(wv[0], wv[1]), pack_f16(wv[2], wv[3]), pack_f16(wv[4], wv[5]), pack_f16(wv[6], wv[7])});
      constexpr float q = 1.0f / 256;
      if (kappa < 8) st16_wt(g_w2q + o, (u32x4){pack_f16(q * wv[0], q * wv[1]), pack_f16(q * wv[2], q * wv[3]), pack_f16(q * wv[4], q * wv[5]), pack_f16(q * wv[6], q * wv[7])});
      static_for<4>([&](auto ic) { bits |= __builtin_amdgcn_update_dpp(0u, bits, 0x120 + (1 << decltype(ic)::value), 0xf, 0xf, false); });
      bad |= ((bits >> 8) & ~bits & 255u) != 0;
    }
    const bool any = __ballot(bad) != 0;
    if (lane == 0) ((char*)(g_buf + G_FLAG))[4 * (bx - 169) + (threadIdx.x >> 6)] = any;
    K0_END();
    return;
  }
  if (bx >= 145) {
    __shared__ __attribute__((aligned(16))) float red[16][64];
    const int c0 = 64 * ((bx - 145) >> 2), rg = (bx - 145) & 3, c8 = (threadIdx.x & 7) * 8;
    float a[8];
#pragma unroll
    for (int i = 0; i < 8; ++i) a[i] = 0.f;
#pragma unroll
    for (int k = 0; k < 12; ++k) {
      const int ch = 384 * rg + 32 * k + (threadIdx.x >> 3);
      const float b = ld_bf16(grn_b + ch);
      const u32x4 v = *(const u32x4*)(w2 + (size_t)ch * CH + c0 + c8);
#pragma unroll
      for (int i = 0; i < 4; ++i) { a[2 * i] = __builtin_fmaf(b, bf_lo(v[i]), a[2 * i]); a[2 * i + 1] = __builtin_fmaf(b, bf_hi(v[i]), a[2 * i + 1]); }
    }
#pragma unroll
    for (int i = 0; i < 8; ++i) a[i] = dpp_add<0x118, 0xf>(a[i]);
    if (threadIdx.x & 8) {
      *(f32x4*)&red[threadIdx.x >> 4][c8] = (f32x4){a[0], a[1], a[2], a[3]};
      *(f32x4*)&red[threadIdx.x >> 4][c8 + 4] = (f32x4){a[4], a[5], a[6], a[7]};
    }
    __syncthreads();
    if (threadIdx.x < 64) {
      float s = 0.f;
#pragma unroll
      for (int i = 0; i < 16; ++i) s += red[i][threadIdx.x];
      g_cs[(4 + rg) * 384 + cs_index(c0 + threadIdx.x)] = s;
      g_cs[64 * (bx - 145) + threadIdx.x] = 0.f;
    }
    K0_END();
    return;
  }
  const unsigned bid = bx;
  if (bid == 0) {
    if (sw) ssc = scales(w1, (const u16*)x, dw);
    if ((threadIdx.x >> 6) == 2) ss2 = w2_scale(w_ms(w_load(w2)));
#pragma unroll
    for (int i = 0; i < 4; ++i) if (threadIdx.x + 256 * i < 896) *(u32x4*)(g_buf + K1_G_CNT + (threadIdx.x + 256 * i) * 16) = (u32x4){0u, 0u, 0u, 0u};
    *(unsigned*)(g_buf + G_PUB + threadIdx.x * 256) = 0u;
    const int e = threadIdx.x, e2 = e < 192 ? 32 * (e >> 4) + 8 * ((e & 15) >> 2) : 8 * min(e - 192, 47);
    const u32x4 d = *(const u32x4*)(dw + e2);
    __syncthreads();
    const Sc sc = ssc;
    const float s2 = ss2;
    u32x4 v;
#pragma unroll
    for (int k = 0; k < 4; ++k) v[k] = pack_bf16(sc.zs * bf_lo(d[k]), sc.zs * bf_hi(d[k]));
    if (e < 192) {
#pragma unroll
      for (int k = 0; k < 4; ++k) v[k] &= (k & 1) != ((e >> 1) & 1) ? 0u : e & 1 ? 0xffff0000u : 0xffffu;
    }
    if (e >= 240) v = (u32x4){__float_as_uint(EPS * sc.zs * sc.zs), __float_as_uint(sc.vhi), __float_as_uint(sc.g), __float_as_uint(sc.vlo)};
    *(u32x4*)(g_buf + K1_G_TAB + (e < 192 ? e : e + 192) * 16) = v;
    auto sane = [](float v) { return v >= 0x1p-45f && v <= 0x1p20f; };
    if (threadIdx.x < 2) ((unsigned*)(g_buf + G_FLAG))[96 + threadIdx.x] = !(sane(sc.w1) && sane(sc.zs) && sane(sc.g) && sane(sc.h) && sane(s2));
    if (threadIdx.x == 0) {
      const float ik = 1.0f / (sc.h * s2);
      *(f32x4*)g_sc = (f32x4){sc.h * s2, ik, 0x1p-28f / (sc.h * sc.h), -sc.c * U24 * ik};
      g_sc[4] = __uint_as_float(pack_f16(-sc.c * U24, -sc.c * U24));
      *(unsigned*)(g_buf + K1_G_CNT + 16320) = pack_f16(-sc.c * 0x1p-10f, -sc.c * 0x1p-10f);
    }
    if (threadIdx.x < 48) *(u32x4*)(g_buf + threadIdx.x * K1_GST + 5376) = (u32x4){pack_f16(sc.c * U24, 0.f), 0u, 0u, 0u};
    K0_END();
    return;
  }
  __shared__ __attribute__((aligned(16))) u16 tl[32][136];
  const int e = (bid - 1) * 256 + threadIdx.x, ks = (bid - 1) % 12, cb = (bid - 1) / 12;
  u32x4 sm, v[2];
  if (sw) sm = w_load(w1);
#pragma unroll
  for (int i = 0; i < 2; ++i) v[i] = *(const u32x4*)(w1 + (size_t)(32 * ks + (threadIdx.x >> 4) + 16 * i) * HID + 128 * cb + (threadIdx.x & 15) * 8);
  if (sw) ss2 = w1_scale(w_ms(sm));
#pragma unroll
  for (int i = 0; i < 2; ++i) *(u32x4*)&tl[(threadIdx.x >> 4) + 16 * i][(threadIdx.x & 15) * 8] = v[i];
  if (e < (int)(K1_NREP + 1) * 768) *(u32x4*)(g_buf + K1_G_ACC + e * 16) = (u32x4){0u, 0u, 0u, 0u};
  __syncthreads();
  const float s1 = ss2;
#pragma unroll
  for (int i = 0; i < 2; ++i) {
    const int lane = threadIdx.x & 63, f = (threadIdx.x >> 6) + 4 * i, n = 16 * f + (lane & 15), k0 = 8 * (lane >> 4);
    u32x4 P;
#pragma unroll
    for (int jj = 0; jj < 4; ++jj) P[jj] = pack_f16(s1 * ld_bf16(&tl[k0 + 2 * jj][n]), s1 * ld_bf16(&tl[k0 + 2 * jj + 1][n]));
    const int fs = (f & 1) * 12 + ks;
    st16_wt(g_buf + (4 * cb + (f >> 1)) * K1_GST + (fs + (fs >= 5)) * 1024 + lane * 16, P);
  }
  K0_END();
}

constexpr unsigned G_ACC2 = K1_G_ACC + K1_NREP * 12288;

#define VA_CLOB \
  "v0", "v1", "v2", "v3", "v4", "v5", "v6", "v7", "v8", "v9", "v10", "v11", "v12", "v13", "v14", "v15", "v16", "v17", "v18", "v19", "v20", "v21", "v22", "v23", "v24", "v25", "v26", "v27", "v28", "v29", "v30", "v31", \
  "v32", "v33", "v34", "v35", "v36", "v37", "v38", "v39", "v40", "v41", "v42", "v43", "v44", "v45", "v46", "v47", "v48", "v49", "v50", "v51", "v52", "v53", "v54", "v55", "v56", "v57", "v58", "v59", "v60", "v61", "v62", "v63", \
  "v64", "v65", "v66", "v67", "v68", "v69", "v70", "v71", "v72", "v73", "v74", "v75", "v76", "v77", "v78", "v79", "v80", "v81", "v82", "v83", "v84", "v85", "v86", "v87", "v88", "v89", "v90", "v91", "v92", "v93", "v94", "v95", \
  "v96", "v97", "v98", "v99", "v100", "v101", "v102", "v103", "v104", "v105", "v106", "v107", "v108", "v109", "v110", "v111", "v112", "v113", "v114", "v115", "v116", "v117", "v118", "v119", "v120", "v121", "v122", "v123", "v124", "v125", "v126", "v127", \
  "v128", "v129", "v130", "v131", "v132", "v133", "v134", "v135", "v136", "v137", "v138", "v139", "v140", "v141", "v142", "v143", "v144", "v145", "v146", "v147", "v148", "v149", "v150", "v151", "v152", "v153", "v154", "v155", "v156", "v157", "v158", "v159", \
  "v160", "v161", "v162", "v163", "v164", "v165", "v166", "v167", "v168", "v169", "v170", "v171", "v172", "v173", "v174", "v175", "v176", "v177", "v178", "v179", "v180", "v181", "v182", "v183", "v184", "v185", "v186", "v187", "v188", "v189", "v190", "v191", \
  "v192", "v193", "v194", "v195", "v196", "v197", "v198", "v199", "v200", "v201", "v202", "v203", "v204", "v205", "v206", "v207", "v208", "v209", "v210", "v211", "v212", "v213", "v214", "v215", "v216", "v217", "v218", "v219", "v220", "v221", "v222", "v223", \
  "v224", "v225", "v226", "v227", "v228", "v229", "v230", "v231", "v232", "v233", "v234", "v235", "v236", "v237", "v238", "v239", "v240", "v241", "v242", "v243", "v244", "v245", "v246", "v247", "v248", "v249", "v250", "v251", "v252", "v253", "v254", "v255", \
  "a0", "a1", "a2", "a3", "a4", "a5", "a6", "a7", "a8", "a9", "a10", "a11", "a12", "a13", "a14", "a15", "a16", "a17", "a18", "a19", "a20", "a21", "a22", "a23", "a24", "a25", "a26", "a27", "a28", "a29", "a30", "a31", \
  "a32", "a33", "a34", "a35", "a36", "a37", "a38", "a39", "a40", "a41", "a42", "a43", "a44", "a45", "a46", "a47", "a48", "a49", "a50", "a51", "a52", "a53", "a54", "a55", "a56", "a57", "a58", "a59", "a60", "a61", "a62", "a63", \
  "a64", "a65", "a66", "a67", "a68", "a69", "a70", "a71", "a72", "a73", "a74", "a75", "a76", "a77", "a78", "a79", "a80", "a81", "a82", "a83", "a84", "a85", "a86", "a87", "a88", "a89", "a90", "a91", "a92", "a93", "a94", "a95", \
  "a96", "a97", "a98", "a99", "a100", "a101", "a102", "a103", "a104", "a105", "a106", "a107", "a108", "a109", "a110", "a111", "a112", "a113", "a114", "a115", "a116", "a117", "a118", "a119", "a120", "a121", "a122", "a123", "a124", "a125", "a126", "a127", \
  "a128", "a129", "a130", "a131", "a132", "a133", "a134", "a135", "a136", "a137", "a138", "a139", "a140", "a141", "a142", "a143", "a144", "a145", "a146", "a147", "a148", "a149", "a150", "a151", "a152", "a153", "a154", "a155", "a156", "a157", "a158", "a159", \
  "a160", "a161", "a162", "a163", "a164", "a165", "a166", "a167", "a168", "a169", "a170", "a171", "a172", "a173", "a174", "a175", "a176", "a177", "a178", "a179", "a180", "a181", "a182", "a183", "a184", "a185", "a186", "a187", "a188", "a189", "a190", "a191", \
  "a192", "a193", "a194", "a195", "a196", "a197", "a198", "a199", "a200", "a201", "a202", "a203", "a204", "a205", "a206", "a207", "a208", "a209", "a210", "a211", "a212", "a213", "a214", "a215", "a216", "a217", "a218", "a219", "a220", "a221", "a222", "a223", \
  "a224", "a225", "a226", "a227", "a228", "a229", "a230", "a231", "a232", "a233", "a234", "a235", "a236", "a237", "a238", "a239", "a240", "a241", "a242", "a243", "a244", "a245", "a246", "a247", "a248", "a249", "a250", "a251", "a252", "a253", "a254", "a255"
#define K1_CLOB "memory", "vcc", "scc", "s36", "s37", "s38", "s39", "s40", "s41", "s42", "s43", "s44", "s45", "s46", "s47", "s48", "s49", "s50", "s51", "s52", "s53", "s54", "s55", "s56", "s57", "s58", "s59", "s60", "s61", "s62", "s63", "s64", "s65", "s66", "s67", "s68", "s69", "s70", "s71", "s72", "s73", "s74", "s75", "s76", "s77", "s78", "s79", "s80", "s81", "s82", "s83", "s84", "s85", "s86", "s87", "s88", "s89", "s90", "s91", "s92", "s93", "s94", "s95", "s96", "s97", VA_CLOB
DI void k2_params(const u16* x, const char* hws, const u16* out, const u16* grn_g, unsigned tid) {
  const unsigned e = (tid >> 2) * 32 + blockIdx.x * 4 + (tid & 3);
  const unsigned b = e >> 2, w = e & 3, pg = w & 1, nh = w >> 1, q = (b >> 3) * 4 + (b & 3), rs = 128 + (q >> 1), hf = q & 1, side = wg_side(b);
  auto fmask = [&](unsigned region) {
    const unsigned o = ((unsigned)(size_t)hws & 0x1fffffu) + region * H_RS + nh * H_TS;
    const unsigned* p = (const unsigned*)(g_buf + K1_G_LOC + 64) + (o >> 21);
    const unsigned r = 0x200000u + 2 * H_TS - 1 - (o & 0x1fffffu), d1 = ((r >> 15) * 171) >> 9, d2 = (((r + 0x200000u) >> 15) * 171) >> 9;
    const unsigned a1 = ~0u << d1, a2 = d2 < 32 ? ~0u << d2 : 0u;
    return ((__popc(p[0]) > 2 ? ~a1 : 0u) | (__popc(p[1]) > 2 ? a1 & ~a2 : 0u) | (__popc(p[2]) > 2 ? a2 : 0u)) ^ (0u - side);
  };
  const unsigned f0 = fmask(q), f1 = fmask(rs);
  const unsigned fx = ((side ^ x_flip(x, q)) ? ~K2_SHT : 0u) | ((side ^ x_flip(x, rs)) ? K2_SHT : 0u);
  const uint64_t h0 = (uint64_t)hws + q * H_RS + nh * (H_TS + 2048) + pg * 3072, h1 = (uint64_t)hws + rs * H_RS + nh * (H_TS + 2048) + hf * 3072 + pg * 1536;
  const unsigned p0 = (12 * q + 2 * pg) * 49152 + nh * 384, p1 = (12 * rs + 2 * hf + pg) * 49152 + nh * 384;
  const unsigned e1 = 1024 + (e >> 3);
  auto kap = [](unsigned f) { return f / 48 * 2 + (f & 1); };
  auto sco = [&](unsigned f) { return kap(f) * 64 + (kap(f) < 16 ? K2_S16_D0 : 0); };
  auto cso = [&](unsigned f) { const unsigned r = f % 48; return ((f < 192 ? min(f / 48, 2u) : 3) * 384 + (r / 12 % 2 * 12 + r / 24 * 6 + r % 12 / 2) * 16) * 4; };
  typedef uint64_t u64x2 __attribute__((ext_vector_type(2)));
  u64x2* P = (u64x2*)(g_prm + 64 * e);
  P[0] = (u64x2){f0 | (uint64_t)f1 << 32, fx | (uint64_t)(sco(e) | (sco(e1) + (e & 6) * 8) << 16) << 32};
  const u32x4 sc = *(const u32x4*)g_sc;
  P[1] = (u64x2){(((b >> 3) * 4 + w) & 7) * 1024 | (uint64_t)sc[0] << 32, sc[1] | (uint64_t)sc[2] << 32};
  P[2] = (u64x2){h0, h0 + 8192};
  P[3] = (u64x2){h1, h1 + 8192};
  P[4] = (u64x2){__float_as_uint(g_sc[4]) | (uint64_t)((e1 - e) * 1024 + (e & 7) * 128) << 32, 0};
  P[5] = (u64x2){(uint64_t)x + p0, (uint64_t)x + p1};
  P[6] = (u64x2){(uint64_t)out + p0, (uint64_t)out + p1};
  P[7] = (u64x2){0x7fffffffffffffull, (uint64_t)g_buf + G_FLAG};
  P[8] = (u64x2){(uint64_t)g_w2f + w * 6144, (uint64_t)g_w2q + w * 6144};
  P[9] = (u64x2){(uint64_t)g_w2r + e * 1024, (uint64_t)g_w2f + e * 1024};
  P[10] = (u64x2){(uint64_t)grn_g + w * 256, (uint64_t)g_cs + w * 384 + 2048};
  P[11] = (u64x2){(uint64_t)g_buf + G_PUB + b * 256, (uint64_t)g_buf + G_PUB + w * 16384};
  P[12] = (u64x2){cso(e) | (uint64_t)(cso(e1) + (e & 1) * 32) << 32, (uint64_t)g_cs};
  P[13] = (u64x2){sc[3] | (uint64_t)1 << (32 + (b >> 3) % K2_NAT), 0};
}
__global__ __launch_bounds__(256, 1) void k1_fc1(const u16* __restrict__ x, char* __restrict__ hws, const u16* __restrict__ out, const u16* __restrict__ grn_g) {
  extern __shared__ __attribute__((aligned(16))) char smem[];
  const unsigned w = __builtin_amdgcn_readfirstlane(threadIdx.x >> 6);
  if (blockIdx.x < 8) {
    if (w < 2) k2_params(x, hws, out, grn_g, w * 64 + __builtin_amdgcn_mbcnt_hi(~0u, __builtin_amdgcn_mbcnt_lo(~0u, 0u)));
    return;
  }
  const unsigned lds0 = lds_addr(smem);
  const unsigned b = blockIdx.x - 8;
  const unsigned dir = (b >> 3) & 1;
  const uint64_t xp = (uint64_t)x, hp = (uint64_t)hws, gp = (uint64_t)g_buf, wp = gp + w * K1_WCH;
  const unsigned d = dir ? ~0u : 1u, wrap = dir ? ~0u : 48u, rst = dir ? 47u : 0u, pos0 = dir ? K1_SMAX - 1 : 0u;
  const unsigned xo0 = (12 * ((b >> 3) * 4 + (b & 3)) + w) * 49152, xs1 = (1536 + w + (dir ? 8 : 0)) * 49152, sb1 = 1536 * w + (dir ? K1_SB1 : 0);
  const unsigned r0o = ((b >> 3) * 4 + (b & 3)) * H_RS, tsel = 4 * (b & 3), s2 = (b >> 2) & 1;
  const uint64_t ap = gp + K1_G_ACC + (b % K1_NREP) * 12288;
  const unsigned ca0 = w == 0 ? dir * 64 : K1_G_DUM - K1_G_CNT + (4 * b + w) * 64;
  const unsigned pm = lds0 + K1_PF_OFF, pf = ((b >> 3) * 4 + w) * 1024;
  unsigned nb;
  asm volatile(
R"(.macro M t,f,w
.if \f < 30
v_mfma_f32_16x16x32_f16 v[56+4*\t:56+4*\t+3], v[128+4*\f:128+4*\f+3], a[216+4*\w:216+4*\w+3], v[56+4*\t:56+4*\t+3]
.else
v_mfma_f32_16x16x32_f16 v[56+4*\t:56+4*\t+3], a[4*\f-4*30:4*\f-4*30+3], a[216+4*\w:216+4*\w+3], v[56+4*\t:56+4*\t+3]
.endif
.endm
.macro Z t,f,w
.if \f < 30
v_mfma_f32_16x16x32_f16 v[56+4*\t:56+4*\t+3], v[128+4*\f:128+4*\f+3], a[216+4*\w:216+4*\w+3], v[252:255]
.else
v_mfma_f32_16x16x32_f16 v[56+4*\t:56+4*\t+3], a[4*\f-4*30:4*\f-4*30+3], a[216+4*\w:216+4*\w+3], v[252:255]
.endif
.endm
.macro LZ a,f
.if \f < 30
v_mfma_f32_16x16x32_bf16 v[36:36+3], v[\a:\a+3], v[128+4*\f:128+4*\f+3], 0
.else
v_mfma_f32_16x16x32_bf16 v[36:36+3], v[\a:\a+3], a[4*\f-4*30:4*\f-4*30+3], 0
.endif
.endm
.macro LS a,f
.if \f < 30
v_mfma_f32_16x16x32_bf16 v[36:36+3], v[\a:\a+3], v[128+4*\f:128+4*\f+3], v[36:36+3]
.else
v_mfma_f32_16x16x32_bf16 v[36:36+3], v[\a:\a+3], a[4*\f-4*30:4*\f-4*30+3], v[36:36+3]
.endif
.endm
.macro GZ f
.if \f < 30
v_mfma_f32_16x16x32_f16 v[52:55], v[128+4*\f:128+4*\f+3], v[128+4*\f:128+4*\f+3], 0
.else
v_mfma_f32_16x16x32_f16 v[52:55], a[4*\f-4*30:4*\f-4*30+3], a[4*\f-4*30:4*\f-4*30+3], 0
.endif
.endm
.macro GS f
.if \f < 30
v_mfma_f32_16x16x32_f16 v[52:55], v[128+4*\f:128+4*\f+3], v[128+4*\f:128+4*\f+3], v[52:55]
.else
v_mfma_f32_16x16x32_f16 v[52:55], a[4*\f-4*30:4*\f-4*30+3], a[4*\f-4*30:4*\f-4*30+3], v[52:55]
.endif
.endm
.macro XL f,o
.if \f < 30
global_load_dwordx4 v[128+4*\f:128+4*\f+3], v3, s[46:47] offset:\o
.else
global_load_dwordx4 a[4*\f-4*30:4*\f-4*30+3], v3, s[46:47] offset:\o
.endif
.endm
.macro C d,a,b
v_cvt_pk_bf16_f32 v[\d], v[\a], v[\b]
.endm
.macro LL t,z
v_lshlrev_b32_sdwa v[\t], 1, v[\z] dst_sel:DWORD dst_unused:UNUSED_PAD src0_sel:DWORD src1_sel:WORD_1
.endm
.macro LA t
ds_read_u16 v[\t], v[\t] offset:5376
.endm
.macro LC_ t
ds_read_u16_d16_hi v[\t], v[\t] offset:5376
.endm
.macro LO p,a,b
v_bfi_b32 v[\p], s36, v[\a], v[\b]
.endm
.macro HZ q,p
v_pk_mul_f16 v[\q], v[\p], v[\p]
.endm
.macro HS q,p
v_pk_fma_f16 v[\q], v[\p], v[\p], v[\q]
.endm
.macro HX q
v_fma_mix_f32 v[\q], v[\q], 1.0, v[\q] op_sel:[0,0,1] op_sel_hi:[1,0,1]
.endm
.macro AW a,s
v_accvgpr_write_b32 a[\a], v[\s]
.endm
.macro RA d,v,o
ds_read_b128 a[\d:\d+3], v[\v] offset:\o
.endm
.macro RV d,v,o
ds_read_b128 v[\d:\d+3], v[\v] offset:\o
.endm
.macro W2 a,b,o
ds_write2st64_b32 v4, v[\a], v[\b] offset0:\o offset1:\o+4
.endm
.macro W1 a,o
ds_write_b32 v4, v[\a] offset:\o
.endm
.macro DW o
global_load_lds_dwordx4 v0, s[40:41] offset:\o
.endm
.macro DF
global_load_lds_dwordx4 v0, s[70:71]
.endm
.macro ST d
.if ((\d - 104) / 4) & 1
global_store_dwordx4 v2, v[\d:\d+3], s[42:43] nt sc1
.else
global_store_dwordx2 v31, v[\d:\d+1], s[42:43] nt sc1
.endif
.endm
.macro SU d
.if ((\d - 104) / 4) & 1
global_store_dwordx4 v2, v[\d:\d+3], s[42:43]  sc1
.else
global_store_dwordx2 v31, v[\d:\d+1], s[42:43]  sc1
.endif
.endm
.macro SP
global_atomic_add_f64 v6, v[14:15], s[48:49]
.endm
.macro AT
global_atomic_add v11, v9, v10, s[68:69] sc0
.endm
.macro PFA
s_getpc_b64 s[70:71]
s_add_u32 s70, s70, 7f-.
s_addc_u32 s71, s71, 0
s_andn2_b32 s70, s70, 0x7f
s_min_u32 s53, %[pf], 8f-7f-1152
s_andn2_b32 s53, s53, 0x7f
s_add_u32 s70, s70, s53
s_addc_u32 s71, s71, 0
.endm
.macro PFN
s_getpc_b64 s[70:71]
s_add_u32 s70, s70, k2_code@rel32@lo-1020
s_addc_u32 s71, s71, k2_code@rel32@hi-1012
s_add_u32 s70, s70, %[pf]
s_addc_u32 s71, s71, 0
s_andn2_b32 s70, s70, 0x7f
.endm
.macro PFW
s_getpc_b64 s[70:71]
s_add_u32 s70, s70, g_w2q@rel32@lo+4
s_addc_u32 s71, s71, g_w2q@rel32@hi+12
s_add_u32 s70, s70, %[pf]
s_addc_u32 s71, s71, 0
.endm
.macro PF0
s_getpc_b64 s[70:71]
s_andn2_b32 s70, s70, 0x7f
s_min_u32 s53, %[pf], 7f-.-1152
s_andn2_b32 s53, s53, 0x7f
s_add_u32 s70, s70, s53
s_addc_u32 s71, s71, 0
.endm
.macro WL n
s_waitcnt lgkmcnt(\n)
.endm
.macro WV n
s_waitcnt vmcnt(\n)
.endm
.macro LU t,p
v_pk_fma_f16 v[\t], v[\p], v34, s67
.endm
.macro CF d,a,b
v_cvt_pk_f16_f32 v[\d], v[\a], v[\b]
.endm
.macro XS d
v_pk_fma_f16 v[\d], v[\d], v13, v33
.endm
.macro AR d,a
v_accvgpr_read_b32 v[\d], a[\a]
.endm
.macro DQ o
global_load_lds_dword v23, s[40:41] offset:\o
.endm
.macro GE
v_cmp_eq_u32 vcc, 0, v35
s_nop 4
s_cbranch_vccz 9f
v_mov_b32 v35, 14720
s_mov_b64 exec, 1
global_store_dword v35, v10, s[68:69] sc1
s_mov_b64 exec, -1
9:
.endm
.macro BW f
.if \f < 30
ds_write_b128 v12, v[128+4*\f:128+4*\f+3]
.else
ds_write_b128 v12, a[4*\f-120:4*\f-120+3]
.endif
.endm
.macro BR f
.if \f < 30
ds_read_b128 v[128+4*\f:128+4*\f+3], v20
.else
ds_read_b128 a[4*\f-120:4*\f-120+3], v20
.endif
.endm
.macro LK d,a,f,h
.if \f < 30
v_mfma_f32_4x4x4_16b_bf16 v[\d:\d+3], a[\a:\a+1], v[128+4*\f+\h:128+4*\f+\h+1], v[36:39]
.else
v_mfma_f32_4x4x4_16b_bf16 v[\d:\d+3], a[\a:\a+1], a[4*\f-120+\h:4*\f-120+\h+1], v[36:39]
.endif
.endm
.macro PK d,a,b,s
v_perm_b32 v[\d], v[\a], v[\b], s[\s]
.endm
.macro PO d
v_lshl_or_b32 v[\d], v[\d], 4, v[\d-4]
.endm
.macro LG a,b
v_cmp_eq_f32 vcc, v[\a], v[\b]
v_mov_b32 v[\b], 0x7f800000
s_nop 0
v_cndmask_b32 v[\a], v[\b], v[\a], vcc
.endm
.macro DY
s_mov_b32 s72, m0
s_mov_b32 m0, %[pm]
s_nop 0
global_load_lds_dword v3, s[46:47]
s_mov_b32 m0, s72
s_nop 0
.endm
.macro FP
ds_write_b32 v6, v19 offset:61440
.endm
.macro FR
ds_read_b32 v15, v31 offset:61440
.endm
k1_code:
v_mbcnt_lo_u32_b32 v16, -1, 0
v_mbcnt_hi_u32_b32 v16, -1, v16
v_lshlrev_b32 v0, 4, v16
v_and_b32 v17, 15, v16
v_lshrrev_b32 v18, 4, v16
s_add_u32 s68, %[glo], 1239040
s_addc_u32 s69, %[ghi], 0
s_mov_b32 s53, %[r0o]
s_load_dword s67, s[68:69], 16320
s_add_u32 s82, %[hlo], s53
s_addc_u32 s83, %[hhi], 0
s_add_u32 s84, s82, 8192
s_addc_u32 s85, s83, 0
s_and_b32 s90, %[hlo], 0x1fffff
s_add_u32 s90, s90, s53
s_mov_b32 s91, 3072
v_mov_b32 v11, s90
v_mad_u32_u24 v11, v0, s91, v11
v_lshrrev_b32 v11, 21, v11
v_lshl_add_u32 v11, v11, 2, 64
v_writelane_b32 v11, %[tsel], 63
s_sub_u32 s53, s82, %[hlo]
s_lshr_b32 s53, s53, 2
s_and_b32 s90, %[xlo], 0x1fffff
s_add_u32 s53, s53, s90
s_add_u32 s53, s53, 0x20048000
s_lshr_b32 s53, s53, 21
s_lshl_b32 s90, s53, 2
v_writelane_b32 v11, s90, 48
global_load_dword v11, v11, s[68:69] offset:-2048
v_lshlrev_b32 v19, 1, v18
s_movk_i32 s53, 0x78
v_bfe_u32 v21, v16, 2, 2
v_lshrrev_b32 v19, v19, s53
v_mul_u32_u24 v21, 0x300, v21
v_xor_b32 v19, v19, v16
v_and_b32 v19, 3, v19
v_lshl_add_u32 v21, v19, 4, v21
v_mov_b32 v32, 3072
s_mov_b32 s50, %[pos0]
s_mov_b32 s51, %[pos0]
s_mov_b32 s52, %[pos0]
s_add_u32 s74, %[lds], 0
s_add_u32 s75, s74, 163840
s_mov_b32 s37, s74
s_mul_i32 s53, %[w], 6400
s_add_u32 s76, s74, s53
s_add_u32 s77, s76, 163840
s_mov_b32 s38, s76
s_mul_i32 s60, %[w], 1536
s_add_u32 s61, s60, 512
s_add_u32 s62, s60, 16384
s_add_u32 s63, s60, 16896
s_add_u32 s64, s60, 32768
s_add_u32 s65, s60, 33280
v_lshlrev_b32 v23, 2, v16
s_lshl_b32 s53, %[w], 11
s_min_u32 s53, s53, 5120
s_add_u32 s72, s53, 1228800
s_add_u32 s40, %[glo], s72
s_addc_u32 s41, %[ghi], 0
s_add_u32 s53, s53, %[lds]
s_add_u32 m0, s53, 58368
s_nop 0
DW 0
DW 1024
s_nop 0 ;PS
s_mul_i32 s53, s50, 25600
s_add_u32 s40, %[wlo], s53
s_addc_u32 s41, %[whi], 0
)"
R"(s_mov_b32 m0, s38
s_nop 0
DW 0
DW 1024
DW 2048
s_add_u32 s40, s40, 3072
s_addc_u32 s41, s41, 0
s_add_u32 m0, s38, 3072
s_nop 0
DW 0
DW 1024
DW 2048
DQ 3072
s_add_u32 s50, s50, %[d]
s_cmp_eq_u32 s50, %[wrap]
s_cselect_b32 s50, %[rst], s50
s_add_u32 s38, s38, 32768
s_cmp_eq_u32 s38, s77
s_cselect_b32 s38, s76, s38
v_add_u32 v1, s37, v0
v_lshl_add_u32 v19, v18, 2, v17
v_and_b32 v19, 15, v19
v_lshlrev_b32 v19, 4, v19
v_lshl_add_u32 v2, v18, 8, v19
s_lshl_b32 s53, %[w], 2
s_add_u32 s53, s53, %[lds]
s_add_u32 s53, s53, 123904
s_add_u32 s53, %[lds], 123904
v_bfe_u32 v4, v17, 1, 2
v_and_b32 v5, 8, v17
v_add3_u32 v4, v4, v5, %[w]
v_lshl_add_u32 v4, v18, 2, v4
v_and_b32 v4, 15, v4
v_lshl_add_u32 v4, v17, 4, v4
v_lshl_add_u32 v4, v4, 2, s53
s_lshl_b32 s53, %[w], 9
s_add_u32 s53, s53, %[lds]
s_add_u32 s53, s53, 123904
v_lshlrev_b32 v5, 3, v16
v_add_u32 v5, s53, v5
s_lshl_b32 s53, %[w], 6
v_and_b32 v6, 0x38, v16
v_add_u32 v6, s53, v6
s_add_u32 s53, %[lds], 58368
v_and_b32 v19, 3, v16
v_lshl_add_u32 v19, v18, 2, v19
v_lshl_add_u32 v7, v19, 4, s53
v_lshlrev_b32 v8, 4, v18
v_add_u32 v8, s53, v8
v_mov_b32 v10, 1
s_mov_b32 s36, 0xffff
s_mov_b32 s56, 0x5550555
s_mov_b32 s39, 32768
s_mov_b32 s54, 0x01010101
s_mov_b32 s55, 0x01010101
s_mov_b32 %[nb], 0
v_and_b32 v28, 12, v17
v_lshl_add_u32 v28, v28, 2, v17
v_lshlrev_b32 v28, 2, v28
v_mov_b32 v34, 0x74007400
v_mov_b32 v35, -1
v_and_b32 v22, 0xfffffcff, v1
v_lshrrev_b32 v31, 1, v2
s_mov_b32 s97, 0x06020400
s_mov_b32 s56, 0x07030501
v_mov_b32 v33, 0x5550555
s_waitcnt lgkmcnt(0)
s_mov_b32 s78, 0xaaaaaaaa
s_mov_b32 s79, 0xaaaaaaaa
s_mov_b32 s80, 0xcccccccc
s_mov_b32 s81, 0xcccccccc
s_and_b32 s53, %[w], 1
s_lshl_b32 s53, s53, 10
s_lshr_b32 s72, %[w], 1
s_mul_i32 s72, s72, 32768
s_add_u32 s72, s72, s53
s_add_u32 s72, s72, 128000
s_movk_i32 s53, 0x78
v_lshrrev_b32 v12, 5, v0
v_and_b32 v12, 6, v12
v_bfe_u32 v12, s53, v12, 2
v_lshrrev_b32 v20, 8, v0
v_xor_b32 v20, v12, v20
v_lshl_add_u32 v20, v20, 4, s72
v_and_b32 v12, 0xf0, v0
v_lshl_add_u32 v20, v12, 2, v20
v_add_u32 v12, s72, v0
WV 9
v_bcnt_u32_b32 v11, v11, 0
v_cmp_lt_u32 s[90:91], 2, v11
s_nop 0
s_lshr_b32 s96, s91, 31
s_xor_b32 s96, s96, %[s2]
s_sub_u32 s53, 0, s96
s_xor_b32 s90, s90, s53
s_xor_b32 s91, s91, s53
s_add_u32 s53, %[xo0], 0
s_add_u32 s46, %[xlo], s53
s_addc_u32 s47, %[xhi], 0
s_bitcmp1_b64 s[90:91], 48
s_cselect_b32 s58, 16, 0
s_lshr_b32 s58, 0xfe987610, s58
v_lshrrev_b32 v3, 6, v0
v_and_b32 v3, 12, v3
v_bfe_u32 v3, s58, v3, 4
v_mad_u32_u24 v3, v3, v32, v21
XL 12,0
XL 13,64
XL 14,128
XL 15,192
XL 16,256
XL 17,320
XL 18,384
XL 19,448
XL 20,512
XL 21,576
XL 22,640
XL 23,704
s_mul_i32 s53, s96, 8320
s_cmp_lt_u32 %[ca0], 128
s_cselect_b32 s53, s53, 0
s_add_u32 s73, s53, %[ca0]
v_mov_b32 v9, s73
WV 19
s_barrier
v_lshrrev_b32 v19, 8, v31
v_sub_u32 v19, 0, v19
ds_write_b32 v31, v19 offset:61440
s_nop 1
s_nop 0
v_mov_b32 v19, 0
WV 11
BW 12
RV 40,8,6144
RV 44,8,6208
RV 48,8,6272
RV 248,8,6336
BR 12
WV 10
BW 13
s_nop 1
BR 13
WL 2
LZ 40,12
WV 9
BW 14
RV 40,8,6400
s_nop 0
BR 14
s_add_u32 s53, %[xo0], 0
s_add_u32 s46, %[xlo], s53
s_addc_u32 s47, %[xhi], 0
s_bitcmp1_b64 s[90:91], 48
s_cselect_b32 s58, 16, 0
s_lshr_b32 s58, 0xda43cb52, s58
v_lshrrev_b32 v3, 6, v0
v_and_b32 v3, 12, v3
v_bfe_u32 v3, s58, v3, 4
v_mad_u32_u24 v3, v3, v32, v21
WL 3
LS 44,13
WV 8
BW 15
RV 44,8,6464
s_nop 0
BR 15
XL 24,0
WL 3
LS 48,14
WV 8
BW 16
RV 48,8,6528
s_nop 0
BR 16
WL 3
LS 248,15
WV 7
BW 17
RV 248,8,6592
s_nop 0
BR 17
XL 25,64
WL 3
LS 40,16
WV 7
BW 18
RV 40,8,6656
s_nop 0
BR 18
XL 26,128
WL 3
LS 44,17
WV 7
BW 19
RV 44,8,6720
s_nop 0
BR 19
WL 3
LS 48,18
WV 6
BW 20
RV 48,8,6784
s_nop 0
BR 20
XL 27,192
WL 3
LS 248,19
WV 6
BW 21
RV 248,8,6848
s_nop 0
BR 21
XL 28,256
WL 3
LS 40,20
WV 6
BW 22
RA 240,7,0
s_nop 0
BR 22
WL 3
LS 44,21
WV 5
BW 23
RA 244,7,256
s_nop 0
BR 23
XL 29,320
WL 3
LS 48,22
RA 248,7,512
XL 30,384
WL 1
LS 248,23
RA 252,7,768
s_nop 6
v_mul_f32 v36, 0xbb2aaaab, v36
v_mul_f32 v37, 0xbb2aaaab, v37
v_mul_f32 v38, 0xbb2aaaab, v38
v_mul_f32 v39, 0xbb2aaaab, v39
s_nop 1
LK 40,240,12,0
LK 44,242,12,2
RA 240,7,1024
XL 31,448
LK 48,244,13,0
s_nop 0
CF 176,40,41
LK 248,246,13,2
WL 2
RA 244,7,1280
CF 177,42,43
CF 178,44,45
CF 179,46,47
XL 32,512
LK 40,248,14,0
CF 180,48,49
LK 44,250,14,2
RA 248,7,1536
CF 181,50,51
GZ 12
WL 3
XL 33,576
CF 182,248,249
CF 183,250,251
LK 48,252,15,0
CF 184,40,41
LK 248,254,15,2
RA 252,7,1792
CF 185,42,43
XL 34,640
GS 13
WL 3
CF 186,44,45
CF 187,46,47
LK 40,240,16,0
CF 188,48,49
LK 44,242,16,2
WL 2
RA 240,7,2048
CF 189,50,51
XL 35,704
GS 14
s_mul_i32 s53, s50, 25600
s_add_u32 s40, %[wlo], s53
s_addc_u32 s41, %[whi], 0
s_mov_b32 m0, s38
CF 190,248,249
CF 191,250,251
LK 48,244,17,0
CF 192,40,41
LK 248,246,17,2
RA 244,7,2304
CF 193,42,43
GS 15
WL 3
DW 0
CF 194,44,45
CF 195,46,47
LK 40,248,18,0
CF 196,48,49
LK 44,250,18,2
RA 248,7,2560
CF 197,50,51
DW 1024
GS 16
WL 3
CF 198,248,249
CF 199,250,251
LK 48,252,19,0
CF 200,40,41
LK 248,254,19,2
WL 2
RA 252,7,2816
CF 201,42,43
DW 2048
GS 17
s_add_u32 s40, s40, 3072
s_addc_u32 s41, s41, 0
s_add_u32 m0, s38, 3072
CF 202,44,45
CF 203,46,47
LK 40,240,20,0
CF 204,48,49
LK 44,242,20,2
CF 205,50,51
GS 18
WL 2
DW 0
CF 206,248,249
CF 207,250,251
LK 48,244,21,0
CF 208,40,41
LK 248,246,21,2
CF 209,42,43
DW 1024
GS 19
WL 1
CF 210,44,45
CF 211,46,47
LK 40,248,22,0
CF 212,48,49
LK 44,250,22,2
CF 213,50,51
DW 2048
GS 20
WL 0
DQ 3072
CF 214,248,249
CF 215,250,251
LK 48,252,23,0
CF 216,40,41
LK 248,254,23,2
CF 217,42,43
GS 21
s_add_u32 s50, s50, %[d]
s_cmp_eq_u32 s50, %[wrap]
s_cselect_b32 s50, %[rst], s50
CF 218,44,45
CF 219,46,47
RV 36,8,6912
s_nop 0
GS 22
s_add_u32 s38, s38, 32768
s_cmp_eq_u32 s38, s77
s_cselect_b32 s38, s76, s38
CF 220,48,49
CF 221,50,51
CF 222,248,249
CF 223,250,251
s_nop 1
GS 23
s_nop 7
v_cndmask_b32 v52, v52, v53, s[78:79]
v_cndmask_b32 v54, v54, v55, s[78:79]
v_cndmask_b32 v52, v52, v54, s[80:81]
v_mul_f32 v52, 0x3b2aaaab, v52
WL 0
v_med3_f32 v53, v52, v39, v37
LG 52,53
s_nop 0
C 52,52,52
v_lshlrev_b32 v52, 16, v52
v_add_f32 v52, v36, v52
C 52,52,52
v_lshlrev_b32 v52, 16, v52
v_sqrt_f32 v52, v52
s_nop 0
C 52,52,52
v_lshlrev_b32 v52, 16, v52
v_rcp_f32 v52, v52
s_nop 0
v_mul_f32 v52, v38, v52
CF 52,52,52
ds_bpermute_b32 v13, v28, v52
WV 18
BW 24
RV 40,8,6144
RV 44,8,6208
RV 48,8,6272
)"
R"(BR 24
WV 17
BW 25
s_nop 1
BR 25
WL 7
XS 176
WL 2
LZ 40,24
s_and_b32 s53, %[w], 1
s_mul_i32 s53, s53, 3072
s_lshr_b32 s72, %[w], 1
s_lshl_b32 s40, s72, 16
s_add_u32 s40, s40, s53
s_add_u32 m0, s40, 25600
s_lshl_b32 s72, s72, 13
s_add_u32 s53, s53, s72
s_add_u32 s53, s53, 1382400
s_add_u32 s40, %[glo], s53
s_addc_u32 s41, %[ghi], 0
XS 177
WV 16
BW 26
XS 178
RV 40,8,6336
XS 179
BR 26
XS 180
WL 3
LS 44,25
WV 15
DW 0
XS 181
BW 27
XS 182
RV 44,8,6400
XS 183
BR 27
v_min_u32 v35, v35, v13
WL 3
LS 48,26
WV 15
DW 1024
XS 184
BW 28
XS 185
RV 48,8,6464
XS 186
BR 28
XS 187
WL 3
LS 40,27
WV 15
XS 188
BW 29
XS 189
RV 40,8,6528
XS 190
XS 191
BR 29
XS 192
WL 3
LS 44,28
WV 14
DW 2048
XS 193
BW 30
XS 194
RV 44,8,6592
XS 195
BR 30
XS 196
WL 3
LS 48,29
WV 14
DW 3072
XS 197
BW 31
XS 198
RV 48,8,6656
XS 199
BR 31
XS 200
WL 3
LS 40,30
WV 13
XS 201
BW 32
XS 202
RV 40,8,6720
XS 203
BR 32
XS 204
WL 3
LS 44,31
PF0
s_mov_b32 m0, %[pm]
s_nop 0
DF
PFA
XS 205
BW 33
XS 206
XS 207
RV 44,8,6784
XS 208
BR 33
XS 209
WL 3
LS 48,32
s_mul_i32 s53, s50, 25600
s_add_u32 s40, %[wlo], s53
s_addc_u32 s41, %[whi], 0
s_mov_b32 m0, s38
XS 210
WV 13
BW 34
XS 211
RV 48,8,6848
XS 212
RA 240,7,0
XS 213
BR 34
XS 214
WL 4
LS 40,33
WV 12
XS 215
BW 35
XS 216
RA 244,7,256
XS 217
BR 35
XS 218
WL 3
LS 44,34
DW 0
XS 219
RA 248,7,512
XS 220
WL 1
LS 48,35
DW 1024
XS 221
RA 252,7,768
XS 222
XS 223
s_nop 2
v_mul_f32 v36, 0xbb2aaaab, v36
v_mul_f32 v37, 0xbb2aaaab, v37
v_mul_f32 v38, 0xbb2aaaab, v38
v_mul_f32 v39, 0xbb2aaaab, v39
s_nop 1
LK 40,240,24,0
LK 44,242,24,2
RA 240,7,1024
LK 48,244,25,0
s_nop 1
CF 224,40,41
LK 248,246,25,2
WL 2
RA 244,7,1280
CF 225,42,43
CF 226,44,45
CF 227,46,47
DW 2048
LK 40,248,26,0
CF 228,48,49
LK 44,250,26,2
RA 248,7,1536
CF 229,50,51
s_add_u32 s40, s40, 3072
s_addc_u32 s41, s41, 0
s_add_u32 m0, s38, 3072
GZ 24
WL 3
CF 230,248,249
CF 231,250,251
LK 48,252,27,0
CF 232,40,41
LK 248,254,27,2
RA 252,7,1792
CF 233,42,43
DW 0
GS 25
WL 3
DW 1024
CF 234,44,45
CF 235,46,47
LK 40,240,28,0
CF 236,48,49
LK 44,242,28,2
RA 240,7,2048
CF 237,50,51
GS 26
WL 3
DW 2048
CF 238,248,249
CF 239,250,251
LK 48,244,29,0
CF 240,40,41
LK 248,246,29,2
RA 244,7,2304
CF 241,42,43
DQ 3072
GS 27
WL 3
CF 242,44,45
CF 243,46,47
LK 40,248,30,0
CF 244,48,49
LK 44,250,30,2
WL 2
RA 248,7,2560
CF 245,50,51
s_add_u32 s50, s50, %[d]
s_cmp_eq_u32 s50, %[wrap]
s_cselect_b32 s50, %[rst], s50
GS 28
s_add_u32 s38, s38, 32768
s_cmp_eq_u32 s38, s77
s_cselect_b32 s38, s76, s38
CF 246,248,249
CF 247,250,251
LK 48,252,31,0
CF 152,40,41
LK 248,254,31,2
WL 2
RA 252,7,2816
CF 153,42,43
GS 29
s_add_u32 s53, %[xo0], 196608
s_add_u32 s46, %[xlo], s53
s_addc_u32 s47, %[xhi], 0
s_bitcmp1_b64 s[90:91], 48
s_cselect_b32 s58, 16, 0
s_lshr_b32 s58, 0xfe987610, s58
v_lshrrev_b32 v3, 6, v0
v_and_b32 v3, 12, v3
v_bfe_u32 v3, s58, v3, 4
v_mad_u32_u24 v3, v3, v32, v21
CF 154,44,45
CF 155,46,47
LK 40,240,32,0
CF 156,48,49
LK 44,242,32,2
CF 157,50,51
XL 36,0
GS 6
WL 2
CF 158,248,249
CF 159,250,251
LK 48,244,33,0
CF 160,40,41
LK 248,246,33,2
CF 161,42,43
XL 37,64
GS 7
WL 1
XL 38,128
CF 162,44,45
CF 163,46,47
LK 40,248,34,0
CF 164,48,49
LK 44,250,34,2
CF 165,50,51
GS 8
WL 0
XL 39,192
CF 166,248,249
CF 167,250,251
LK 48,252,35,0
CF 168,40,41
LK 248,254,35,2
CF 169,42,43
XL 40,256
GS 9
CF 170,44,45
CF 171,46,47
RV 36,8,6912
s_nop 0
GS 10
XL 41,320
CF 172,48,49
CF 173,50,51
CF 174,248,249
CF 175,250,251
s_nop 1
GS 11
XL 42,384
WV 15
s_barrier
RA 216,1,0
RA 220,1,1024
RA 224,1,2048
RA 228,1,3072
RV 252,22,5120
v_cndmask_b32 v52, v52, v53, s[78:79]
v_cndmask_b32 v54, v54, v55, s[78:79]
v_cndmask_b32 v52, v52, v54, s[80:81]
v_mul_f32 v52, 0x3b2aaaab, v52
WL 5
v_med3_f32 v53, v52, v39, v37
LG 52,53
s_nop 0
C 52,52,52
v_lshlrev_b32 v52, 16, v52
v_add_f32 v52, v36, v52
C 52,52,52
XL 43,448
v_lshlrev_b32 v52, 16, v52
v_sqrt_f32 v52, v52
s_nop 0
C 52,52,52
v_lshlrev_b32 v52, 16, v52
v_rcp_f32 v52, v52
XL 44,512
v_mul_f32 v52, v38, v52
CF 52,52,52
ds_bpermute_b32 v13, v28, v52
WL 0
XS 224
XS 225
XS 226
XS 227
XS 228
XS 229
XS 230
XL 45,576
XS 231
v_min_u32 v35, v35, v13
XS 232
XS 233
XS 234
XL 46,640
XS 235
XS 236
XS 237
XS 238
XS 239
XL 47,704
Z 0,12,0
XS 240
XS 241
XS 242
XS 243
XS 244
XS 245
XS 246
XS 247
XS 152
XS 153
XS 154
XS 155
M 0,13,1
s_mul_i32 s53, s50, 25600
RA 232,1,4096
AW 0,152
AW 1,153
Z 1,24,0
s_add_u32 s40, %[wlo], s53
AW 2,154
AW 3,155
M 1,25,1
s_addc_u32 s41, %[whi], 0
RA 236,1,6144
XS 156
XS 157
M 1,26,2
s_mov_b32 m0, s38
s_nop 0
DW 0
XS 158
XS 159
AW 4,156
M 1,27,3
RA 216,1,7168
AW 5,157
AW 6,158
M 0,14,2
DW 1024
AW 7,159
XS 160
M 0,15,3
WL 2
RA 220,1,8192
XS 161
XS 162
M 0,16,4
WL 2
XS 163
AW 8,160
M 0,17,5
RA 224,1,9216
AW 9,161
AW 10,162
AW 11,163
M 1,28,4
WL 1
DW 2048
)"
R"(XS 164
XS 165
M 1,29,5
s_add_u32 s40, s40, 3072
RA 228,1,10240
XS 166
XS 167
M 1,30,0
s_addc_u32 s41, s41, 0
AW 12,164
AW 13,165
M 1,31,1
s_add_u32 m0, s38, 3072
RA 232,1,11264
AW 14,166
AW 15,167
M 0,18,0
RV 252,22,5392
XS 168
XS 169
M 0,19,1
WL 3
RA 236,1,12288
XS 170
XS 171
AW 16,168
M 0,20,2
WL 3
DW 0
AW 17,169
AW 18,170
M 0,21,3
RA 216,1,13312
AW 19,171
XS 172
M 1,32,2
DW 1024
XS 173
XS 174
M 1,33,3
WL 3
RA 220,1,14336
XS 175
AW 20,172
M 0,22,4
WL 2
DW 2048
AW 21,173
AW 22,174
AW 23,175
M 0,23,5
RA 224,1,15360
M 1,34,4
DQ 3072
M 1,35,5
WL 1
RA 228,1,16384
Z 6,12,0
s_add_u32 s50, s50, %[d]
LL 16,56
LL 104,57
M 6,13,1
s_cmp_eq_u32 s50, %[wrap]
LA 16
RA 232,1,17408
Z 7,24,0
s_cselect_b32 s50, %[rst], s50
LC_ 104
M 7,25,1
s_add_u32 s38, s38, 32768
RA 236,1,18432
WL 5
M 7,26,2
s_cmp_eq_u32 s38, s77
WV 18
BW 36
s_cselect_b32 s38, s76, s38
WL 5
M 7,27,3
s_add_u32 s37, s37, 32768
RA 216,1,19456
RV 40,8,6144
M 6,14,2
s_cmp_eq_u32 s37, s75
LL 17,58
LL 105,59
M 6,15,3
s_cselect_b32 s37, s74, s37
LA 17
RA 220,1,20480
s_add_u32 s53, %[xo0], 196608
s_add_u32 s46, %[xlo], s53
s_addc_u32 s47, %[xhi], 0
s_bitcmp1_b64 s[90:91], 48
s_cselect_b32 s58, 16, 0
s_lshr_b32 s58, 0xda43cb52, s58
v_lshrrev_b32 v3, 6, v0
v_and_b32 v3, 12, v3
v_bfe_u32 v3, s58, v3, 4
RV 44,8,6208
RV 48,8,6272
v_mad_u32_u24 v3, v3, v32, v21
WL 9
M 6,16,4
WL 7
LC_ 105
XL 48,0
BR 36
M 6,17,5
s_mul_i32 s53, s51, 24
RA 224,1,21504
WV 18
BW 37
s_nop 1
BR 37
M 7,28,4
WV 17
LO 104,16,104
LU 16,104
M 7,29,5
s_bitcmp1_b32 s51, 0
RA 228,1,22528
HZ 24,16
WL 4
LZ 40,36
s_addc_u32 s53, s53, 0
BW 38
RV 40,8,6336
M 7,30,0
s_lshl_b32 s53, s53, 11
LL 18,60
LL 108,61
s_bitcmp1_b64 s[90:91], s51
s_cselect_b64 s[44:45], s[84:85], s[82:83]
s_add_u32 s44, s44, s53
s_addc_u32 s45, s45, 0
s_add_u32 s51, s51, %[d]
s_cmp_eq_u32 s51, %[wrap]
s_cselect_b32 s51, %[rst], s51
XL 49,64
BR 38
M 7,31,1
WL 4
LA 18
RA 232,1,23552
LS 44,37
WV 17
BW 39
M 6,18,0
LC_ 108
XL 50,128
RV 44,8,6400
M 6,19,1
RA 236,1,24576
BR 39
WL 7
LS 48,38
WV 17
BW 40
M 6,20,2
LO 105,17,105
LU 17,105
M 6,21,3
XL 51,192
RV 48,8,6464
BR 40
HS 24,17
M 7,32,2
LL 16,62
LL 109,63
v_add_u32 v1, s37, v0
M 7,33,3
WL 3
LA 16
RA 216,1,0
XL 52,256
LS 40,39
WV 18
BW 41
RV 40,8,6528
M 6,22,4
WL 4
LC_ 109
XL 53,320
v_and_b32 v22, 0xfffffcff, v1
BR 41
M 6,23,5
s_barrier
RA 220,1,1024
LS 44,40
WV 18
BW 42
M 7,34,4
RV 44,8,6592
LO 108,18,108
LU 18,108
M 7,35,5
RV 252,22,5120
BR 42
HS 24,18
WL 1
Z 0,12,0
WV 17
RA 224,1,2048
LL 17,80
LL 106,81
M 0,13,1
s_mul_i32 s53, s50, 25600
LA 17
RA 228,1,3072
LS 48,41
s_add_u32 s40, %[wlo], s53
BW 43
RV 48,8,6656
s_nop 0
BR 43
Z 1,24,0
s_addc_u32 s41, %[whi], 0
LC_ 106
RA 232,1,4096
M 1,25,1
s_mov_b32 m0, s38
RA 236,1,6144
WL 9
LS 40,42
WV 16
BW 44
RV 40,8,6720
WL 10
M 1,26,2
WL 8
BR 44
LO 109,16,109
LU 16,109
M 1,27,3
WL 6
RA 216,1,7168
HS 24,16
LS 44,43
WV 15
BW 45
M 0,14,2
DW 0
RV 44,8,6784
LL 18,82
LL 107,83
M 0,15,3
WL 3
LA 18
RA 220,1,8192
BR 45
LS 48,44
WV 15
BW 46
M 0,16,4
LC_ 107
DW 1024
M 0,17,5
RA 224,1,9216
RV 48,8,6848
RA 240,7,0
HX 24
M 1,28,4
LO 17,17,106
LU 106,17
W1 24,0
BR 46
M 1,29,5
WL 7
RA 228,1,10240
DW 2048
HZ 25,106
LS 40,45
WV 16
BW 47
M 1,30,0
s_add_u32 s40, s40, 3072
RA 244,7,256
PK 106,17,104,56
PK 104,17,104,97
LL 16,84
LL 110,85
M 1,31,1
s_addc_u32 s41, s41, 0
LA 16
RA 232,1,11264
BR 47
WL 6
LS 44,46
s_add_u32 m0, s38, 3072
RA 248,7,512
M 0,18,0
LC_ 110
RV 252,22,5392
M 0,19,1
RA 236,1,12288
WL 4
LS 48,47
RA 252,7,768
M 0,20,2
LO 18,18,107
LU 107,18
s_nop 3
v_mul_f32 v36, 0xbb2aaaab, v36
v_mul_f32 v37, 0xbb2aaaab, v37
v_mul_f32 v38, 0xbb2aaaab, v38
M 0,21,3
RA 216,1,13312
DW 0
HS 25,107
v_mul_f32 v39, 0xbb2aaaab, v39
s_nop 1
LK 40,240,36,0
LK 44,242,36,2
RA 240,7,1024
LK 48,244,37,0
s_nop 1
CF 128,40,41
LK 248,246,37,2
RA 244,7,1280
CF 129,42,43
M 1,32,2
PK 107,18,105,56
PK 105,18,105,97
LL 17,86
LL 111,87
M 1,33,3
WL 7
LA 17
RA 220,1,14336
DW 1024
CF 130,44,45
CF 131,46,47
LK 40,248,38,0
WL 5
CF 132,48,49
LK 44,250,38,2
s_add_u32 s42, s44, s60
RA 248,7,1536
CF 133,50,51
GZ 0
s_addc_u32 s43, s45, 0
CF 134,248,249
CF 135,250,251
LK 48,252,39,0
CF 136,40,41
LK 248,254,39,2
RA 252,7,1792
CF 137,42,43
GS 1
WL 5
CF 138,44,45
CF 139,46,47
LK 40,240,40,0
CF 140,48,49
LK 44,242,40,2
RA 240,7,2048
CF 141,50,51
GS 2
WL 5
CF 142,248,249
CF 143,250,251
LK 48,244,41,0
CF 144,40,41
LK 248,246,41,2
RA 244,7,2304
CF 145,42,43
GS 3
CF 146,44,45
CF 147,46,47
M 0,22,4
LC_ 111
ST 104
M 0,23,5
WL 4
RA 224,1,15360
LK 40,248,42,0
CF 148,48,49
LK 44,250,42,2
RA 248,7,2560
CF 149,50,51
GS 4
CF 150,248,249
CF 151,250,251
M 1,34,4
DW 2048
LO 16,16,110
LU 110,16
M 1,35,5
WL 5
RA 228,1,16384
HS 25,110
LK 48,252,43,0
CF 152,40,41
LK 248,254,43,2
RA 252,7,2816
CF 153,42,43
GS 5
CF 154,44,45
CF 155,46,47
)"
R"(Z 6,12,0
PK 110,16,108,56
PK 108,16,108,97
LL 18,56
LL 104,57
M 6,13,1
WL 6
LA 18
DQ 3072
PO 110
LK 40,240,44,0
CF 156,48,49
LK 44,242,44,2
s_add_u32 s50, s50, %[d]
CF 157,50,51
GS 6
s_cmp_eq_u32 s50, %[wrap]
CF 158,248,249
CF 159,250,251
Z 7,24,0
s_cselect_b32 s50, %[rst], s50
LC_ 104
RA 232,1,17408
M 7,25,1
WL 8
RA 236,1,18432
LK 48,244,45,0
CF 160,40,41
LK 248,246,45,2
CF 161,42,43
GS 7
WL 7
CF 162,44,45
CF 163,46,47
M 7,26,2
WL 5
LO 17,17,111
LU 111,17
M 7,27,3
s_add_u32 s38, s38, 32768
RA 216,1,19456
HS 25,111
LK 40,248,46,0
WL 5
CF 164,48,49
LK 44,250,46,2
s_cmp_eq_u32 s38, s77
CF 165,50,51
GS 8
s_cselect_b32 s38, s76, s38
CF 166,248,249
CF 167,250,251
M 6,14,2
s_add_u32 s37, s37, 32768
PK 111,17,109,56
PK 109,17,109,97
LL 16,58
LL 105,59
M 6,15,3
s_cmp_eq_u32 s37, s75
LA 16
PO 111
s_cselect_b32 s37, s74, s37
LK 48,252,47,0
CF 168,40,41
LK 248,254,47,2
CF 169,42,43
HX 25
GS 9
WL 3
CF 170,44,45
CF 171,46,47
M 6,16,4
WL 2
LC_ 105
RA 220,1,20480
XL 54,384
M 6,17,5
s_add_u32 s42, s44, s61
RA 224,1,21504
RV 36,8,6912
s_addc_u32 s43, s45, 0
GS 10
CF 172,48,49
CF 173,50,51
M 7,28,4
LO 104,18,104
LU 18,104
W1 25,1024
M 7,29,5
WL 3
RA 228,1,22528
HZ 24,18
ST 108
XL 55,448
M 7,30,0
s_mul_i32 s53, s51, 24
LL 17,60
LL 108,61
s_bitcmp1_b32 s51, 0
s_addc_u32 s53, s53, 0
s_lshl_b32 s53, s53, 11
s_bitcmp1_b64 s[90:91], s51
s_cselect_b64 s[44:45], s[84:85], s[82:83]
s_add_u32 s44, s44, s53
s_addc_u32 s45, s45, 0
s_add_u32 s51, s51, %[d]
s_cmp_eq_u32 s51, %[wrap]
s_cselect_b32 s51, %[rst], s51
M 7,31,1
LA 17
RA 232,1,23552
M 6,18,0
LC_ 108
XL 56,512
v_add_u32 v4, s39, v4
CF 174,248,249
CF 175,250,251
M 6,19,1
RA 236,1,24576
s_nop 1
v_add_u32 v1, s37, v0
GS 11
WL 7
XL 57,576
s_nop 5
v_cndmask_b32 v52, v52, v53, s[78:79]
v_cndmask_b32 v54, v54, v55, s[78:79]
M 6,20,2
WL 4
LO 105,16,105
LU 16,105
v_cndmask_b32 v52, v52, v54, s[80:81]
v_mul_f32 v52, 0x3b2aaaab, v52
M 6,21,3
RA 216,1,0
HS 24,16
v_med3_f32 v53, v52, v39, v37
M 7,32,2
LL 18,62
LL 109,63
LG 52,53
s_nop 0
C 52,52,52
M 7,33,3
WL 1
LA 18
XL 58,640
v_lshlrev_b32 v52, 16, v52
v_add_f32 v52, v36, v52
M 6,22,4
s_lshl_b32 s53, s52, 8
LC_ 109
v_and_b32 v22, 0xfffffcff, v1
C 52,52,52
v_lshlrev_b32 v52, 16, v52
M 6,23,5
s_add_u32 s48, %[alo], s53
RA 220,1,1024
v_sqrt_f32 v52, v52
s_nop 0
C 52,52,52
M 7,34,4
WV 20
LO 108,17,108
LU 17,108
s_addc_u32 s49, %[ahi], 0
s_add_u32 s52, s52, %[d]
s_cmp_eq_u32 s52, %[wrap]
s_cselect_b32 s52, %[rst], s52
XL 59,704
v_lshlrev_b32 v52, 16, v52
v_rcp_f32 v52, v52
M 7,35,5
s_barrier
RV 252,22,5120
HS 24,17
v_mul_f32 v52, v38, v52
CF 52,52,52
WL 0
Z 0,12,0
s_sub_u32 s39, 0, s39
RA 224,1,2048
LL 16,80
LL 106,81
ds_bpermute_b32 v13, v28, v52
M 0,13,1
s_mul_i32 s53, s50, 25600
LA 16
RA 228,1,3072
Z 1,24,0
s_add_u32 s40, %[wlo], s53
LC_ 106
RA 232,1,4096
M 1,25,1
s_addc_u32 s41, %[whi], 0
RA 236,1,6144
ds_read_b64 v[14:15], v5 offset:0
WL 7
M 1,26,2
LO 109,18,109
LU 18,109
s_mov_b32 m0, s38
WL 4
M 1,27,3
RA 216,1,7168
HS 24,18
M 0,14,2
DW 0
LL 17,82
LL 107,83
M 0,15,3
WL 3
LA 17
RA 220,1,8192
v_subrev_u32 v5, s39, v5
M 0,16,4
WL 4
LC_ 107
DW 1024
M 0,17,5
WL 4
RA 224,1,9216
v_add_f32 v14, v14, v15
s_nop 1
v_add_f32_dpp v14, v14, v14 quad_perm:[1,0,3,2] row_mask:0xf bank_mask:0xf
s_nop 1
v_add_f32_dpp v14, v14, v14 quad_perm:[2,3,0,1] row_mask:0xf bank_mask:0xf
s_nop 1
v_add_f32_dpp v14, v14, v14 row_half_mirror row_mask:0xf bank_mask:0xf
HX 24
XS 128
M 1,28,4
WV 21
LO 16,16,106
LU 106,16
v_cvt_f64_f32 v[14:15], v14
W1 24,0
XS 129
BW 48
XS 130
M 1,29,5
WL 4
RA 228,1,10240
HZ 25,106
s_mov_b64 exec, s[54:55]
SP
s_mov_b64 exec, -1
DW 2048
RV 40,8,6144
RV 44,8,6208
XS 131
AW 24,128
AW 25,129
M 1,30,0
s_add_u32 s40, s40, 3072
RV 48,8,6272
PK 106,16,104,56
PK 104,16,104,97
LL 18,84
LL 110,85
AW 26,130
AW 27,131
M 1,31,1
s_addc_u32 s41, s41, 0
LA 18
RA 232,1,11264
s_add_u32 m0, s38, 3072
BR 48
XS 132
XS 133
XS 134
BW 49
M 0,18,0
LC_ 110
RV 252,22,5392
BR 49
XS 135
AW 28,132
M 0,19,1
WL 10
RA 236,1,12288
AW 29,133
AW 30,134
AW 31,135
WL 5
LZ 40,48
WV 22
BW 50
M 0,20,2
DW 0
RV 40,8,6336
LO 17,17,107
LU 107,17
v_min_u32 v35, v35, v13
XS 136
XS 137
M 0,21,3
WL 3
RA 216,1,13312
BR 50
HS 25,107
XS 138
XS 139
LS 44,49
WV 22
BW 51
M 1,32,2
WL 5
DW 1024
PK 107,17,105,56
PK 105,17,105,97
LL 16,86
LL 111,87
AW 32,136
AW 33,137
AW 34,138
M 1,33,3
s_add_u32 s42, s44, s60
LA 16
RA 220,1,14336
RV 44,8,6400
BR 51
AW 35,139
XS 140
M 0,22,4
s_addc_u32 s43, s45, 0
LC_ 111
DW 2048
XS 141
XS 142
XS 143
M 0,23,5
WL 6
RA 224,1,15360
AW 36,140
AW 37,141
AW 38,142
LS 48,50
WV 23
BW 52
RV 48,8,6464
M 1,34,4
LO 18,18,110
LU 110,18
ST 104
BR 52
AW 39,143
XS 144
M 1,35,5
WL 5
RA 228,1,16384
DQ 3072
HS 25,110
XS 145
XS 146
XS 147
LS 40,51
WV 15
BW 53
Z 6,12,0
s_add_u32 s50, s50, %[d]
RV 40,8,6528
PK 110,18,108,56
PK 108,18,108,97
LL 17,56
LL 104,57
AW 40,144
AW 41,145
M 6,13,1
s_cmp_eq_u32 s50, %[wrap]
LA 17
BR 53
PO 110
AW 42,146
AW 43,147
XS 148
WL 5
LS 44,52
s_cselect_b32 s50, %[rst], s50
BW 54
Z 7,24,0
s_add_u32 s38, s38, 32768
LC_ 104
RA 232,1,17408
XS 149
XS 150
XS 151
M 7,25,1
s_cmp_eq_u32 s38, s77
RA 236,1,18432
RV 44,8,6592
BR 54
AW 44,148
AW 45,149
M 7,26,2
WL 6
LO 16,16,111
LU 111,16
s_cselect_b32 s38, s76, s38
)"
R"(AW 46,150
AW 47,151
XS 152
M 7,27,3
s_add_u32 s37, s37, 32768
RA 216,1,19456
HS 25,111
XS 153
XS 154
LS 48,53
s_cmp_eq_u32 s37, s75
WV 13
BW 55
RV 48,8,6656
M 6,14,2
s_cselect_b32 s37, s74, s37
BR 55
PK 111,16,109,56
PK 109,16,109,97
LL 18,58
LL 105,59
XS 155
AW 48,152
AW 49,153
M 6,15,3
s_add_u32 s53, %[xo0], 393216
LA 18
PO 111
s_add_u32 s46, %[xlo], s53
s_addc_u32 s47, %[xhi], 0
s_bitcmp1_b64 s[90:91], 48
s_cselect_b32 s58, 16, 0
s_lshr_b32 s58, 0xfe987610, s58
v_lshrrev_b32 v3, 6, v0
v_and_b32 v3, 12, v3
v_bfe_u32 v3, s58, v3, 4
v_mad_u32_u24 v3, v3, v32, v21
AW 50,154
AW 51,155
WL 5
LS 40,54
s_add_u32 s42, s44, s61
WV 12
BW 56
M 6,16,4
s_addc_u32 s43, s45, 0
LC_ 105
RA 220,1,20480
RV 40,8,6720
XS 156
XS 157
XS 158
M 6,17,5
WL 5
RA 224,1,21504
BR 56
XL 60,0
HX 25
XS 159
AW 52,156
AW 53,157
LS 44,55
WV 12
BW 57
M 7,28,4
LO 104,17,104
LU 17,104
W1 25,1024
AW 54,158
AW 55,159
M 7,29,5
WL 5
RA 228,1,22528
HZ 24,17
ST 108
RV 44,8,6784
BR 57
XS 160
XS 161
XS 162
M 7,30,0
s_mul_i32 s53, s51, 24
LL 16,60
LL 108,61
s_bitcmp1_b32 s51, 0
s_addc_u32 s53, s53, 0
s_lshl_b32 s53, s53, 11
s_bitcmp1_b64 s[90:91], s51
s_cselect_b64 s[44:45], s[84:85], s[82:83]
s_add_u32 s44, s44, s53
s_addc_u32 s45, s45, 0
s_add_u32 s51, s51, %[d]
s_cmp_eq_u32 s51, %[wrap]
s_cselect_b32 s51, %[rst], s51
XL 61,64
XS 163
AW 56,160
M 7,31,1
WL 5
LA 16
RA 232,1,23552
AW 57,161
AW 58,162
AW 59,163
LS 48,56
WV 13
BW 58
RV 48,8,6848
M 6,18,0
LC_ 108
XL 62,128
v_add_u32 v4, s39, v4
RA 240,7,0
XS 164
XS 165
XS 166
M 6,19,1
WL 6
RA 236,1,24576
BR 58
XS 167
AW 60,164
LS 40,57
WV 13
BW 59
M 6,20,2
LO 105,18,105
LU 18,105
AW 61,165
AW 62,166
AW 63,167
M 6,21,3
XL 63,192
RA 244,7,256
BR 59
HS 24,18
XS 168
XS 169
M 7,32,2
LL 17,62
LL 109,63
v_add_u32 v1, s37, v0
XS 170
XS 171
AW 64,168
M 7,33,3
WL 0
LA 17
RA 216,1,0
XL 64,256
AW 65,169
AW 66,170
AW 67,171
LS 44,58
s_lshl_b32 s53, s52, 8
RA 248,7,512
XS 172
LS 48,59
s_add_u32 s48, %[alo], s53
RA 252,7,768
XS 173
M 6,22,4
s_addc_u32 s49, %[ahi], 0
LC_ 109
v_and_b32 v22, 0xfffffcff, v1
M 6,23,5
s_add_u32 s52, s52, %[d]
RA 220,1,1024
XS 174
XS 175
AW 68,172
M 7,34,4
LO 108,16,108
LU 16,108
s_cmp_eq_u32 s52, %[wrap]
s_cselect_b32 s52, %[rst], s52
XL 65,320
AW 69,173
AW 70,174
M 7,35,5
s_barrier
RV 252,22,5120
HS 24,16
AW 71,175
v_mul_f32 v36, 0xbb2aaaab, v36
v_mul_f32 v37, 0xbb2aaaab, v37
v_mul_f32 v38, 0xbb2aaaab, v38
v_mul_f32 v39, 0xbb2aaaab, v39
WL 0
Z 0,12,0
s_sub_u32 s39, 0, s39
RA 224,1,2048
LL 18,80
LL 106,81
M 0,13,1
s_mul_i32 s53, s50, 25600
LA 18
RA 228,1,3072
LK 40,240,48,0
LK 44,242,48,2
s_add_u32 s40, %[wlo], s53
RA 240,7,1024
LK 48,244,49,0
s_nop 0
CF 128,40,41
LK 248,246,49,2
s_addc_u32 s41, %[whi], 0
RA 244,7,1280
CF 129,42,43
CF 130,44,45
CF 131,46,47
LK 40,248,50,0
CF 132,48,49
LK 44,250,50,2
s_mov_b32 m0, s38
RA 248,7,1536
CF 133,50,51
GZ 0
CF 134,248,249
CF 135,250,251
LK 48,252,51,0
CF 136,40,41
LK 248,254,51,2
RA 252,7,1792
CF 137,42,43
GS 1
CF 138,44,45
CF 139,46,47
Z 1,24,0
LC_ 106
RA 232,1,4096
M 1,25,1
WL 8
RA 236,1,6144
ds_read_b64 v[14:15], v5 offset:0
M 1,26,2
WL 8
LO 109,17,109
LU 17,109
M 1,27,3
WL 7
RA 216,1,7168
HS 24,17
LK 40,240,52,0
CF 140,48,49
LK 44,242,52,2
RA 240,7,2048
CF 141,50,51
GS 2
WL 8
CF 142,248,249
CF 143,250,251
LK 48,244,53,0
CF 144,40,41
LK 248,246,53,2
RA 244,7,2304
CF 145,42,43
GS 3
WL 8
CF 146,44,45
CF 147,46,47
LK 40,248,54,0
CF 148,48,49
LK 44,250,54,2
RA 248,7,2560
CF 149,50,51
GS 4
WL 8
CF 150,248,249
CF 151,250,251
LK 48,252,55,0
CF 152,40,41
LK 248,254,55,2
RA 252,7,2816
CF 153,42,43
GS 5
CF 154,44,45
CF 155,46,47
M 0,14,2
DW 0
LL 16,82
LL 107,83
M 0,15,3
WL 3
LA 16
RA 220,1,8192
v_subrev_u32 v5, s39, v5
LK 40,240,56,0
CF 156,48,49
LK 44,242,56,2
CF 157,50,51
HX 24
GS 6
CF 158,248,249
CF 159,250,251
M 0,16,4
LC_ 107
DW 1024
M 0,17,5
WL 5
RA 224,1,9216
v_add_f32 v14, v14, v15
s_nop 1
v_add_f32_dpp v14, v14, v14 quad_perm:[1,0,3,2] row_mask:0xf bank_mask:0xf
s_nop 1
v_add_f32_dpp v14, v14, v14 quad_perm:[2,3,0,1] row_mask:0xf bank_mask:0xf
s_nop 1
v_add_f32_dpp v14, v14, v14 row_half_mirror row_mask:0xf bank_mask:0xf
LK 48,244,57,0
CF 160,40,41
LK 248,246,57,2
CF 161,42,43
v_cvt_f64_f32 v[14:15], v14
GS 7
CF 162,44,45
CF 163,46,47
M 1,28,4
LO 18,18,106
LU 106,18
W1 24,0
M 1,29,5
WL 6
RA 228,1,10240
HZ 25,106
s_mov_b64 exec, s[54:55]
SP
s_mov_b64 exec, -1
DW 2048
LK 40,248,58,0
CF 164,48,49
LK 44,250,58,2
CF 165,50,51
GS 8
WL 4
CF 166,248,249
CF 167,250,251
M 1,30,0
s_add_u32 s40, s40, 3072
PK 106,18,104,56
PK 104,18,104,97
LL 17,84
LL 110,85
M 1,31,1
s_addc_u32 s41, s41, 0
LA 17
RA 232,1,11264
LK 48,252,59,0
CF 168,40,41
LK 248,254,59,2
s_add_u32 m0, s38, 3072
CF 169,42,43
GS 9
CF 170,44,45
CF 171,46,47
M 0,18,0
LC_ 110
RV 252,22,5392
M 0,19,1
RA 236,1,12288
RV 36,8,6912
GS 10
WL 8
CF 172,48,49
CF 173,50,51
M 0,20,2
WL 6
LO 16,16,107
LU 107,16
M 0,21,3
RA 216,1,13312
DW 0
HS 25,107
M 1,32,2
PK 107,16,105,56
PK 105,16,105,97
LL 18,86
LL 111,87
M 1,33,3
WL 2
LA 18
RA 220,1,14336
DW 1024
M 0,22,4
s_add_u32 s42, s44, s60
LC_ 111
s_addc_u32 s43, s45, 0
M 0,23,5
RA 224,1,15360
CF 174,248,249
CF 175,250,251
s_nop 1
GS 11
ST 104
M 1,34,4
DW 2048
LO 17,17,110
LU 110,17
s_nop 2
v_cndmask_b32 v52, v52, v53, s[78:79]
v_cndmask_b32 v54, v54, v55, s[78:79]
)"
R"(M 1,35,5
WL 4
RA 228,1,16384
HS 25,110
v_cndmask_b32 v52, v52, v54, s[80:81]
Z 6,12,0
WL 3
PK 110,17,108,56
PK 108,17,108,97
LL 16,56
LL 104,57
v_mul_f32 v52, 0x3b2aaaab, v52
v_med3_f32 v53, v52, v39, v37
M 6,13,1
WL 1
LA 16
DQ 3072
PO 110
LG 52,53
s_nop 0
C 52,52,52
Z 7,24,0
s_add_u32 s50, s50, %[d]
LC_ 104
RA 232,1,17408
v_lshlrev_b32 v52, 16, v52
M 7,25,1
s_cmp_eq_u32 s50, %[wrap]
RA 236,1,18432
s_cselect_b32 s50, %[rst], s50
v_add_f32 v52, v36, v52
C 52,52,52
M 7,26,2
WL 4
LO 18,18,111
LU 111,18
v_lshlrev_b32 v52, 16, v52
M 7,27,3
s_add_u32 s38, s38, 32768
RA 216,1,19456
HS 25,111
v_sqrt_f32 v52, v52
s_nop 0
C 52,52,52
M 6,14,2
s_cmp_eq_u32 s38, s77
PK 111,18,109,56
PK 109,18,109,97
LL 17,58
LL 105,59
s_cselect_b32 s38, s76, s38
v_lshlrev_b32 v52, 16, v52
M 6,15,3
s_add_u32 s37, s37, 32768
LA 17
PO 111
s_cmp_eq_u32 s37, s75
s_cselect_b32 s37, s74, s37
v_rcp_f32 v52, v52
s_nop 0
v_mul_f32 v52, v38, v52
WL 3
M 6,16,4
WL 2
LC_ 105
RA 220,1,20480
XL 66,384
CF 52,52,52
ds_bpermute_b32 v13, v28, v52
M 6,17,5
s_add_u32 s42, s44, s61
RA 224,1,21504
HX 25
s_addc_u32 s43, s45, 0
M 7,28,4
LO 104,16,104
LU 16,104
W1 25,1024
M 7,29,5
WL 3
RA 228,1,22528
HZ 24,16
ST 108
XL 67,448
M 7,30,0
s_mul_i32 s53, s51, 24
LL 18,60
LL 108,61
s_bitcmp1_b32 s51, 0
s_addc_u32 s53, s53, 0
s_lshl_b32 s53, s53, 11
s_bitcmp1_b64 s[90:91], s51
s_cselect_b64 s[44:45], s[84:85], s[82:83]
s_add_u32 s44, s44, s53
s_addc_u32 s45, s45, 0
s_add_u32 s51, s51, %[d]
s_cmp_eq_u32 s51, %[wrap]
s_cselect_b32 s51, %[rst], s51
M 7,31,1
LA 18
RA 232,1,23552
M 6,18,0
LC_ 108
XL 68,512
v_add_u32 v4, s39, v4
M 6,19,1
WL 5
RA 236,1,24576
s_nop 1
v_add_u32 v1, s37, v0
M 6,20,2
WL 4
XL 69,576
LO 105,17,105
LU 17,105
M 6,21,3
RA 216,1,0
HS 24,17
M 7,32,2
LL 16,62
LL 109,63
XS 128
M 7,33,3
WL 1
LA 16
XL 70,640
XS 129
XS 130
M 6,22,4
s_lshl_b32 s53, s52, 8
LC_ 109
v_and_b32 v22, 0xfffffcff, v1
XS 131
AW 72,128
M 6,23,5
s_add_u32 s48, %[alo], s53
RA 220,1,1024
AW 73,129
AW 74,130
M 7,34,4
WV 22
LO 108,18,108
LU 18,108
s_addc_u32 s49, %[ahi], 0
s_add_u32 s52, s52, %[d]
s_cmp_eq_u32 s52, %[wrap]
s_cselect_b32 s52, %[rst], s52
XL 71,704
AW 75,131
XS 132
M 7,35,5
s_barrier
RV 252,22,5120
HS 24,18
XS 133
XS 134
XS 135
WL 0
Z 0,12,0
s_sub_u32 s39, 0, s39
LL 17,80
LL 106,81
AW 76,132
AW 77,133
AW 78,134
M 0,13,1
s_mul_i32 s53, s50, 25600
LA 17
RA 224,1,2048
AW 79,135
v_min_u32 v35, v35, v13
XS 136
Z 1,24,0
s_add_u32 s40, %[wlo], s53
LC_ 106
RA 228,1,3072
XS 137
XS 138
XS 139
M 1,25,1
s_addc_u32 s41, %[whi], 0
RA 232,1,4096
AW 80,136
AW 81,137
AW 82,138
Z 2,36,0
LO 109,16,109
LU 16,109
AW 83,139
XS 140
XS 141
M 2,37,1
s_mov_b32 m0, s38
ds_read_b64 v[14:15], v5 offset:0
HS 24,16
XS 142
XS 143
AW 84,140
Z 3,48,0
WL 2
LL 18,82
LL 107,83
AW 85,141
AW 86,142
AW 87,143
M 3,49,1
LA 18
RA 236,1,6144
M 3,50,2
LC_ 107
DW 0
M 3,51,3
v_subrev_u32 v5, s39, v5
HX 24
M 2,38,2
LO 17,17,106
LU 106,17
M 2,39,3
RA 216,1,7168
HZ 25,106
XS 144
M 1,26,2
PK 106,17,104,56
PK 104,17,104,97
LL 16,84
LL 110,85
XS 145
M 1,27,3
LA 16
W1 24,0
XS 146
XS 147
AW 88,144
M 0,14,2
WL 4
LC_ 110
DW 1024
AW 89,145
AW 90,146
AW 91,147
M 0,15,3
RA 220,1,8192
v_add_f32 v14, v14, v15
XS 148
XS 149
XS 150
M 0,16,4
WL 5
LO 18,18,107
LU 107,18
XS 151
AW 92,148
AW 93,149
M 0,17,5
DW 2048
HS 25,107
v_add_f32_dpp v14, v14, v14 quad_perm:[1,0,3,2] row_mask:0xf bank_mask:0xf
AW 94,150
AW 95,151
XS 152
M 1,28,4
s_add_u32 s42, s44, s60
PK 107,18,105,56
PK 105,18,105,97
LL 17,86
LL 111,87
XS 153
XS 154
XS 155
M 1,29,5
s_addc_u32 s43, s45, 0
LA 17
RA 224,1,9216
v_add_f32_dpp v14, v14, v14 quad_perm:[2,3,0,1] row_mask:0xf bank_mask:0xf
AW 96,152
AW 97,153
AW 98,154
M 2,40,4
LC_ 111
ST 104
AW 99,155
XS 156
XS 157
M 2,41,5
v_add_f32_dpp v14, v14, v14 row_half_mirror row_mask:0xf bank_mask:0xf
XS 158
XS 159
AW 100,156
M 3,52,4
WL 3
LO 16,16,110
LU 110,16
AW 101,157
AW 102,158
AW 103,159
M 3,53,5
s_add_u32 s40, s40, 3072
RA 228,1,10240
HS 25,110
v_cvt_f64_f32 v[14:15], v14
M 3,54,0
s_addc_u32 s41, s41, 0
PK 110,16,108,56
PK 108,16,108,97
M 3,55,1
s_add_u32 m0, s38, 3072
s_mov_b64 exec, s[54:55]
SP
s_mov_b64 exec, -1
PO 110
M 2,42,0
XS 160
XS 161
M 2,43,1
RA 232,1,11264
XS 162
M 1,30,0
WL 2
LO 17,17,111
LU 111,17
M 1,31,1
RV 252,22,5392
HS 25,111
M 0,18,0
WL 2
PK 111,17,109,56
PK 109,17,109,97
XS 163
AW 104,160
M 0,19,1
RA 236,1,12288
PO 111
AW 105,161
AW 106,162
M 0,20,2
DW 0
HX 25
AW 107,163
XS 164
M 0,21,3
s_add_u32 s42, s44, s61
W1 25,1024
XS 165
XS 166
M 1,32,2
s_addc_u32 s43, s45, 0
ST 108
v_add_u32 v4, s39, v4
XS 167
M 1,33,3
RA 216,1,13312
AW 108,164
AW 109,165
M 2,44,2
DW 1024
AW 110,166
AW 111,167
M 2,45,3
XS 168
XS 169
M 3,56,2
WL 2
XS 170
XS 171
M 3,57,3
RA 220,1,14336
AW 112,168
AW 113,169
M 0,22,4
DW 2048
AW 114,170
AW 115,171
M 0,23,5
XS 172
XS 173
M 1,34,4
XS 174
XS 175
M 1,35,5
RA 224,1,15360
AW 116,172
AW 117,173
M 2,46,4
LL 18,56
LL 104,57
AW 118,174
AW 119,175
M 2,47,5
LA 18
DQ 3072
M 3,58,4
WL 2
LC_ 104
M 3,59,5
RA 228,1,16384
Z 6,12,0
WV 32
BW 60
M 6,13,1
s_add_u32 s50, s50, %[d]
)"
R"(RV 40,8,6144
RV 44,8,6208
Z 7,24,0
s_cmp_eq_u32 s50, %[wrap]
LL 16,58
LL 105,59
M 7,25,1
s_cselect_b32 s50, %[rst], s50
LA 16
RA 232,1,17408
Z 8,36,0
LC_ 105
RV 48,8,6272
M 8,37,1
BR 60
Z 9,48,0
WL 8
LO 104,18,104
LU 18,104
M 9,49,1
s_add_u32 s38, s38, 32768
RA 236,1,18432
HZ 24,18
M 9,50,2
s_cmp_eq_u32 s38, s77
LL 17,60
LL 108,61
M 9,51,3
s_cselect_b32 s38, s76, s38
LA 17
M 8,38,2
s_add_u32 s37, s37, 32768
LC_ 108
M 8,39,3
s_cmp_eq_u32 s37, s75
RA 216,1,19456
M 7,26,2
WL 6
LO 105,16,105
LU 16,105
M 7,27,3
s_cselect_b32 s37, s74, s37
HS 24,16
M 6,14,2
WL 3
LL 18,62
LL 109,63
M 6,15,3
s_add_u32 s53, %[xo0], 393216
LA 18
RA 220,1,20480
M 6,16,4
s_add_u32 s46, %[xlo], s53
LC_ 109
M 6,17,5
s_addc_u32 s47, %[xhi], 0
WV 30
BW 61
M 7,28,4
WL 5
LO 108,17,108
LU 17,108
s_bitcmp1_b64 s[90:91], 48
s_cselect_b32 s58, 16, 0
s_lshr_b32 s58, 0xda43cb52, s58
v_lshrrev_b32 v3, 6, v0
v_and_b32 v3, 12, v3
v_bfe_u32 v3, s58, v3, 4
BR 61
v_mad_u32_u24 v3, v3, v32, v21
M 7,29,5
RA 224,1,21504
HS 24,17
LZ 40,60
WV 29
BW 62
M 8,40,4
LL 16,64
LL 112,65
M 8,41,5
LA 16
XL 72,0
M 9,52,4
WL 6
LC_ 112
RV 40,8,6336
M 9,53,5
s_mul_i32 s53, s51, 24
RA 228,1,22528
BR 62
M 9,54,0
WL 7
LO 109,18,109
LU 18,109
M 9,55,1
s_bitcmp1_b32 s51, 0
HS 24,18
s_addc_u32 s53, s53, 0
s_lshl_b32 s53, s53, 11
s_bitcmp1_b64 s[90:91], s51
s_cselect_b64 s[44:45], s[84:85], s[82:83]
s_add_u32 s44, s44, s53
s_addc_u32 s45, s45, 0
s_add_u32 s51, s51, %[d]
s_cmp_eq_u32 s51, %[wrap]
s_cselect_b32 s51, %[rst], s51
LS 44,61
WV 29
BW 63
M 8,42,0
LL 17,66
LL 113,67
M 8,43,1
WV 28
LA 17
RA 232,1,23552
RV 44,8,6400
M 7,30,0
WL 4
LC_ 113
XL 73,64
M 7,31,1
s_lshl_b32 s53, s52, 8
BR 63
LS 48,62
s_add_u32 s48, %[alo], s53
BW 64
M 6,18,0
LO 112,16,112
LU 16,112
M 6,19,1
s_addc_u32 s49, %[ahi], 0
RA 236,1,24576
HS 24,16
M 6,20,2
s_add_u32 s52, s52, %[d]
LL 18,68
LL 116,69
s_cmp_eq_u32 s52, %[wrap]
s_cselect_b32 s52, %[rst], s52
M 6,21,3
LA 18
XL 74,128
RV 48,8,6464
M 7,32,2
LC_ 116
XL 75,192
BR 64
M 7,33,3
WL 6
v_add_u32 v1, s37, v0
LS 40,63
WV 30
BW 65
M 8,44,2
LO 113,17,113
LU 17,113
M 8,45,3
RA 216,1,0
HS 24,17
M 9,56,2
WL 2
RV 40,8,6528
LL 16,70
LL 117,71
M 9,57,3
WV 21
LA 16
XL 76,256
BR 65
M 6,22,4
s_barrier
LC_ 117
v_and_b32 v22, 0xfffffcff, v1
M 6,23,5
s_sub_u32 s39, 0, s39
RA 220,1,1024
LS 44,64
WV 21
BW 66
M 7,34,4
LO 116,18,116
LU 18,116
M 7,35,5
RV 252,22,5120
RV 44,8,6592
HS 24,18
M 8,46,4
LL 17,80
LL 106,81
M 8,47,5
LA 17
RA 224,1,2048
BR 66
M 9,58,4
WL 6
LC_ 106
ds_read_b64 v[14:15], v5 offset:0
M 9,59,5
WL 6
RA 228,1,3072
v_subrev_u32 v5, s39, v5
Z 0,12,0
LO 117,16,117
LU 16,117
M 0,13,1
WV 18
XL 77,320
HS 24,16
LS 48,65
s_mul_i32 s53, s50, 25600
BW 67
RV 48,8,6656
Z 1,24,0
s_add_u32 s40, %[wlo], s53
BR 67
LL 18,82
LL 107,83
M 1,25,1
s_addc_u32 s41, %[whi], 0
LA 18
RA 232,1,4096
WL 8
LS 40,66
s_mov_b32 m0, s38
BW 68
Z 2,36,0
LC_ 107
RV 40,8,6720
M 2,37,1
BR 68
HX 24
Z 3,48,0
WL 9
LO 17,17,106
LU 106,17
M 3,49,1
RA 236,1,6144
HZ 25,106
M 3,50,2
LL 16,84
LL 110,85
M 3,51,3
WL 7
LA 16
PK 106,17,104,56
LS 44,67
WV 18
BW 69
M 2,38,2
LC_ 110
PK 104,17,104,97
W1 24,0
M 2,39,3
WV 17
RA 216,1,7168
RV 44,8,6784
M 1,26,2
WL 9
LO 18,18,107
LU 107,18
M 1,27,3
WL 7
DW 0
BR 69
HS 25,107
LS 48,68
s_add_u32 s42, s44, s60
BW 70
M 0,14,2
WL 8
LL 17,86
LL 111,87
M 0,15,3
s_addc_u32 s43, s45, 0
LA 17
PK 107,18,105,56
v_add_f32 v14, v14, v15
s_nop 1
v_add_f32_dpp v14, v14, v14 quad_perm:[1,0,3,2] row_mask:0xf bank_mask:0xf
s_nop 1
v_add_f32_dpp v14, v14, v14 quad_perm:[2,3,0,1] row_mask:0xf bank_mask:0xf
M 0,16,4
LC_ 111
PK 105,18,105,97
v_add_f32_dpp v14, v14, v14 row_half_mirror row_mask:0xf bank_mask:0xf
M 0,17,5
RA 220,1,8192
RV 48,8,6848
M 1,28,4
WL 9
RA 240,7,0
LO 16,16,110
LU 110,16
v_cvt_f64_f32 v[14:15], v14
M 1,29,5
RA 224,1,9216
DW 1024
BR 70
HS 25,110
M 2,40,4
LL 18,88
LL 114,89
s_mov_b64 exec, s[54:55]
SP
s_mov_b64 exec, -1
M 2,41,5
WL 8
LA 18
PK 110,16,108,56
LS 40,69
WV 19
BW 71
M 3,52,4
WL 6
LC_ 114
PK 108,16,108,97
M 3,53,5
RA 228,1,10240
RA 244,7,256
PO 110
M 3,54,0
WL 5
LO 17,17,111
LU 111,17
ST 104
DW 2048
M 3,55,1
s_add_u32 s40, s40, 3072
BR 71
HS 25,111
M 2,42,0
s_addc_u32 s41, s41, 0
LL 16,90
LL 115,91
M 2,43,1
s_add_u32 m0, s38, 3072
LA 16
PK 111,17,109,56
LS 44,70
s_add_u32 s42, s44, s61
RA 248,7,512
M 1,30,0
s_addc_u32 s43, s45, 0
LC_ 115
PK 109,17,109,97
M 1,31,1
WL 3
RA 232,1,11264
PO 111
LS 48,71
RA 252,7,768
M 0,18,0
LO 18,18,114
LU 114,18
M 0,19,1
RV 252,22,5392
HS 25,114
s_nop 0
v_mul_f32 v36, 0xbb2aaaab, v36
M 0,20,2
LL 17,92
LL 118,93
v_mul_f32 v37, 0xbb2aaaab, v37
v_mul_f32 v38, 0xbb2aaaab, v38
M 0,21,3
LA 17
PK 114,18,112,56
v_mul_f32 v39, 0xbb2aaaab, v39
s_nop 1
LK 40,240,60,0
LK 44,242,60,2
RA 240,7,1024
LK 48,244,61,0
s_nop 1
CF 128,40,41
LK 248,246,61,2
RA 244,7,1280
CF 129,42,43
M 1,32,2
LC_ 118
PK 112,18,112,97
M 1,33,3
RA 236,1,12288
CF 130,44,45
M 2,44,2
WL 8
DW 0
LO 16,16,115
LU 115,16
M 2,45,3
RA 216,1,13312
HS 25,115
ST 108
CF 131,46,47
LK 40,248,62,0
CF 132,48,49
LK 44,250,62,2
RA 248,7,1536
CF 133,50,51
)"
R"(GZ 0
WL 8
CF 134,248,249
CF 135,250,251
LK 48,252,63,0
CF 136,40,41
LK 248,254,63,2
RA 252,7,1792
CF 137,42,43
GS 1
WL 6
CF 138,44,45
CF 139,46,47
LK 40,240,64,0
CF 140,48,49
LK 44,242,64,2
RA 240,7,2048
CF 141,50,51
GS 2
CF 142,248,249
CF 143,250,251
M 3,56,2
WL 4
LL 18,94
LL 119,95
M 3,57,3
LA 18
PK 115,16,113,56
LK 48,244,65,0
CF 144,40,41
LK 248,246,65,2
RA 244,7,2304
CF 145,42,43
GS 3
CF 146,44,45
CF 147,46,47
M 0,22,4
LC_ 119
PK 113,16,113,97
M 0,23,5
RA 220,1,14336
DW 1024
M 1,34,4
WL 6
LO 17,17,118
LU 118,17
M 1,35,5
s_add_u32 s42, s44, s62
RA 224,1,15360
HS 25,118
s_addc_u32 s43, s45, 0
LK 40,248,66,0
CF 148,48,49
LK 44,250,66,2
RA 248,7,2560
CF 149,50,51
GS 4
CF 150,248,249
CF 151,250,251
M 2,46,4
LL 16,56
LL 104,57
M 2,47,5
WL 7
LA 16
PK 118,17,116,56
LK 48,252,67,0
CF 152,40,41
LK 248,254,67,2
RA 252,7,2816
CF 153,42,43
GS 5
CF 154,44,45
CF 155,46,47
M 3,58,4
WL 4
LC_ 104
PK 116,17,116,97
M 3,59,5
RA 228,1,16384
DW 2048
PO 118
Z 6,12,0
LO 18,18,119
LU 119,18
ST 112
M 6,13,1
DQ 3072
HS 25,119
LK 40,240,68,0
CF 156,48,49
LK 44,242,68,2
s_add_u32 s42, s44, s63
CF 157,50,51
GS 6
s_addc_u32 s43, s45, 0
CF 158,248,249
CF 159,250,251
Z 7,24,0
LL 17,58
LL 105,59
M 7,25,1
LA 17
PK 119,18,117,56
Z 8,36,0
s_add_u32 s50, s50, %[d]
LC_ 105
PK 117,18,117,97
M 8,37,1
s_cmp_eq_u32 s50, %[wrap]
RA 232,1,17408
PO 119
LK 48,244,69,0
CF 160,40,41
LK 248,246,69,2
s_cselect_b32 s50, %[rst], s50
CF 161,42,43
HX 25
GS 7
CF 162,44,45
CF 163,46,47
Z 9,48,0
WL 3
LO 104,16,104
LU 16,104
M 9,49,1
RA 236,1,18432
HZ 24,16
M 9,50,2
LL 18,60
LL 108,61
M 9,51,3
LA 18
W1 25,1024
LK 40,248,70,0
CF 164,48,49
LK 44,250,70,2
CF 165,50,51
GS 8
CF 166,248,249
CF 167,250,251
M 8,38,2
LC_ 108
ST 116
M 8,39,3
s_add_u32 s37, s37, 32768
RA 216,1,19456
v_add_u32 v4, s39, v4
LK 48,252,71,0
CF 168,40,41
LK 248,254,71,2
s_cmp_eq_u32 s37, s75
CF 169,42,43
GS 9
s_cselect_b32 s37, s74, s37
CF 170,44,45
CF 171,46,47
M 7,26,2
WL 6
LO 105,17,105
LU 17,105
M 7,27,3
s_add_u32 s38, s38, 32768
RV 36,8,6912
HS 24,17
M 6,14,2
WL 5
LL 16,62
LL 109,63
M 6,15,3
s_cmp_eq_u32 s38, s77
LA 16
RA 220,1,20480
GS 10
s_cselect_b32 s38, s76, s38
CF 172,48,49
M 6,16,4
LC_ 109
XL 78,384
M 6,17,5
CF 173,50,51
CF 174,248,249
M 7,28,4
WL 5
LO 108,18,108
LU 18,108
M 7,29,5
s_mul_i32 s53, s51, 24
RA 224,1,21504
HS 24,18
M 8,40,4
s_bitcmp1_b32 s51, 0
LL 17,64
LL 112,65
M 8,41,5
s_addc_u32 s53, s53, 0
LA 17
CF 175,250,251
M 9,52,4
WL 3
LC_ 112
M 9,53,5
s_lshl_b32 s53, s53, 11
RA 228,1,22528
GS 11
s_bitcmp1_b64 s[90:91], s51
M 9,54,0
WL 4
LO 109,16,109
LU 16,109
M 9,55,1
s_cselect_b64 s[44:45], s[84:85], s[82:83]
HS 24,16
s_add_u32 s44, s44, s53
s_addc_u32 s45, s45, 0
s_add_u32 s51, s51, %[d]
s_cmp_eq_u32 s51, %[wrap]
s_cselect_b32 s51, %[rst], s51
M 8,42,0
LL 18,66
LL 113,67
M 8,43,1
LA 18
RA 232,1,23552
XL 79,448
M 7,30,0
s_lshl_b32 s53, s52, 8
LC_ 113
v_cndmask_b32 v52, v52, v53, s[78:79]
M 7,31,1
s_add_u32 s48, %[alo], s53
v_cndmask_b32 v54, v54, v55, s[78:79]
v_cndmask_b32 v52, v52, v54, s[80:81]
M 6,18,0
WL 3
LO 112,17,112
LU 17,112
M 6,19,1
s_addc_u32 s49, %[ahi], 0
RA 236,1,24576
HS 24,17
M 6,20,2
s_add_u32 s52, s52, %[d]
LL 16,68
LL 116,69
s_cmp_eq_u32 s52, %[wrap]
s_cselect_b32 s52, %[rst], s52
v_mul_f32 v52, 0x3b2aaaab, v52
M 6,21,3
LA 16
XL 80,512
M 7,32,2
LC_ 116
XL 81,576
v_med3_f32 v53, v52, v39, v37
M 7,33,3
v_add_u32 v1, s37, v0
LG 52,53
M 8,44,2
WL 3
LO 113,18,113
LU 18,113
C 52,52,52
M 8,45,3
RA 216,1,0
HS 24,18
v_lshlrev_b32 v52, 16, v52
M 9,56,2
WL 3
LL 17,70
LL 117,71
v_add_f32 v52, v36, v52
M 9,57,3
WV 22
LA 17
XL 82,640
C 52,52,52
M 6,22,4
s_barrier
LC_ 117
v_and_b32 v22, 0xfffffcff, v1
v_lshlrev_b32 v52, 16, v52
M 6,23,5
s_sub_u32 s39, 0, s39
RA 220,1,1024
v_sqrt_f32 v52, v52
M 7,34,4
WL 4
LO 116,16,116
LU 16,116
C 52,52,52
M 7,35,5
RV 252,22,5120
HS 24,16
v_lshlrev_b32 v52, 16, v52
M 8,46,4
LL 18,80
LL 106,81
v_rcp_f32 v52, v52
M 8,47,5
LA 18
RA 224,1,2048
v_mul_f32 v52, v38, v52
M 9,58,4
WL 3
LC_ 106
ds_read_b64 v[14:15], v5 offset:0
CF 52,52,52
M 9,59,5
WL 4
RA 228,1,3072
ds_bpermute_b32 v13, v28, v52
Z 0,12,0
LO 117,17,117
LU 17,117
M 0,13,1
XL 83,704
HS 24,17
Z 1,24,0
s_mul_i32 s53, s50, 25600
LL 16,82
LL 107,83
M 1,25,1
s_add_u32 s40, %[wlo], s53
LA 16
RA 232,1,4096
Z 2,36,0
s_addc_u32 s41, %[whi], 0
LC_ 107
v_subrev_u32 v5, s39, v5
M 2,37,1
s_mov_b32 m0, s38
HX 24
Z 3,48,0
WL 4
LO 18,18,106
LU 106,18
M 3,49,1
RA 236,1,6144
HZ 25,106
M 3,50,2
LL 17,84
LL 110,85
M 3,51,3
WL 4
LA 17
PK 106,18,104,56
XS 128
M 2,38,2
LC_ 110
PK 104,18,104,97
W1 24,0
XS 129
M 2,39,3
RA 216,1,7168
v_add_f32 v14, v14, v15
XS 130
M 1,26,2
WL 5
DW 0
LO 16,16,107
LU 107,16
v_add_f32_dpp v14, v14, v14 quad_perm:[1,0,3,2] row_mask:0xf bank_mask:0xf
XS 131
M 1,27,3
DW 1024
HS 25,107
AW 120,128
M 0,14,2
WL 4
LL 18,86
LL 111,87
v_add_f32_dpp v14, v14, v14 quad_perm:[2,3,0,1] row_mask:0xf bank_mask:0xf
AW 121,129
M 0,15,3
s_add_u32 s42, s44, s60
LA 18
PK 107,16,105,56
AW 122,130
M 0,16,4
s_addc_u32 s43, s45, 0
LC_ 111
PK 105,16,105,97
v_add_f32_dpp v14, v14, v14 row_half_mirror row_mask:0xf bank_mask:0xf
AW 123,131
XS 132
M 0,17,5
RA 220,1,8192
v_cvt_f64_f32 v[14:15], v14
XS 133
M 1,28,4
WL 5
LO 17,17,110
LU 110,17
XS 134
M 1,29,5
RA 224,1,9216
HS 25,110
XS 135
M 2,40,4
)"
R"(WV 25
LL 16,88
LL 114,89
s_mov_b64 exec, s[54:55]
SP
s_mov_b64 exec, -1
DW 2048
BW 72
AW 124,132
M 2,41,5
LA 16
RV 40,8,6144
PK 110,17,108,56
AW 125,133
M 3,52,4
WL 4
LC_ 114
RV 44,8,6208
PK 108,17,108,97
AW 126,134
AW 127,135
M 3,53,5
RA 228,1,10240
PO 110
v_min_u32 v35, v35, v13
M 3,54,0
LO 18,18,111
LU 111,18
ST 104
RV 48,8,6272
XS 136
M 3,55,1
s_add_u32 s40, s40, 3072
BR 72
HS 25,111
s_addc_u32 s41, s41, 0
s_add_u32 m0, s38, 3072
XS 137
M 2,42,0
WV 27
LL 17,90
LL 115,91
XS 138
BW 73
M 2,43,1
LA 17
PK 111,18,109,56
XS 139
M 1,30,0
WV 26
LC_ 115
BR 73
PK 109,18,109,97
AW 128,136
M 1,31,1
WL 4
RA 232,1,11264
DW 0
PO 111
AW 129,137
AW 130,138
LZ 40,72
s_add_u32 s42, s44, s61
BW 74
M 0,18,0
LO 16,16,114
LU 114,16
AW 131,139
M 0,19,1
s_addc_u32 s43, s45, 0
RV 252,22,5392
HS 25,114
XS 140
M 0,20,2
RV 40,8,6336
LL 18,92
LL 118,93
XS 141
M 0,21,3
LA 18
DW 1024
BR 74
PK 114,16,112,56
XS 142
M 1,32,2
LC_ 118
PK 112,16,112,97
XS 143
M 1,33,3
WL 7
RA 236,1,12288
AW 132,140
AW 133,141
LS 44,73
WV 27
BW 75
M 2,44,2
LO 17,17,115
LU 115,17
AW 134,142
M 2,45,3
RA 216,1,13312
HS 25,115
ST 108
RV 44,8,6400
AW 135,143
M 3,56,2
WL 3
DW 2048
BR 75
LL 16,94
LL 119,95
XS 144
M 3,57,3
LA 16
PK 115,17,113,56
XS 145
LS 48,74
WV 28
BW 76
M 0,22,4
LC_ 119
PK 113,17,113,97
XS 146
M 0,23,5
WV 27
RA 220,1,14336
RV 48,8,6464
XS 147
M 1,34,4
WL 5
DQ 3072
LO 18,18,118
LU 118,18
AW 136,144
AW 137,145
M 1,35,5
s_add_u32 s42, s44, s62
RA 224,1,15360
HS 25,118
s_addc_u32 s43, s45, 0
AW 138,146
M 2,46,4
s_add_u32 s50, s50, %[d]
BR 76
LL 17,56
LL 104,57
AW 139,147
M 2,47,5
s_cmp_eq_u32 s50, %[wrap]
LA 17
PK 118,18,116,56
XS 148
LS 40,75
s_cselect_b32 s50, %[rst], s50
BW 77
M 3,58,4
WL 5
LC_ 104
PK 116,18,116,97
XS 149
M 3,59,5
WV 15
RA 228,1,16384
PO 118
XS 150
Z 6,12,0
WL 4
LO 16,16,119
LU 119,16
ST 112
RV 40,8,6528
XS 151
AW 140,148
M 6,13,1
s_add_u32 s38, s38, 32768
BR 77
HS 25,119
AW 141,149
Z 7,24,0
s_cmp_eq_u32 s38, s77
LL 18,58
LL 105,59
s_cselect_b32 s38, s76, s38
AW 142,150
M 7,25,1
s_mul_i32 s53, s50, 25600
LA 18
PK 119,16,117,56
AW 143,151
LS 44,76
s_add_u32 s40, %[wlo], s53
BW 78
Z 8,36,0
s_addc_u32 s41, %[whi], 0
LC_ 105
PK 117,16,117,97
XS 152
M 8,37,1
s_mov_b32 m0, s38
RA 232,1,17408
RV 44,8,6592
PO 119
XS 153
Z 9,48,0
WL 5
BR 78
LO 104,17,104
LU 17,104
XS 154
M 9,49,1
s_add_u32 s42, s44, s63
RA 236,1,18432
HZ 24,17
XS 155
AW 144,152
LS 48,77
s_addc_u32 s43, s45, 0
WV 15
BW 79
M 9,50,2
LL 16,60
LL 108,61
AW 145,153
M 9,51,3
LA 16
DW 0
HX 25
AW 146,154
M 8,38,2
LC_ 108
W1 25,1024
RV 48,8,6656
AW 147,155
M 8,39,3
RA 216,1,19456
BR 79
XS 156
M 7,26,2
WL 11
LO 105,18,105
LU 18,105
XS 157
M 7,27,3
WL 8
ST 116
HS 24,18
XS 158
XS 159
LS 40,78
WV 16
BW 80
M 6,14,2
WL 8
LL 17,62
LL 109,63
AW 148,156
M 6,15,3
LA 17
RA 220,1,20480
DW 1024
AW 149,157
M 6,16,4
s_add_u32 s37, s37, 32768
LC_ 109
RV 40,8,6720
s_cmp_eq_u32 s37, s75
s_cselect_b32 s37, s74, s37
AW 150,158
M 6,17,5
WL 5
DW 2048
BR 80
AW 151,159
LS 44,79
WV 17
BW 81
M 7,28,4
LO 108,16,108
LU 16,108
v_add_u32 v4, s39, v4
XS 160
M 7,29,5
s_add_u32 s40, s40, 3072
RA 224,1,21504
HS 24,16
XS 161
M 8,40,4
s_addc_u32 s41, s41, 0
LL 18,64
LL 112,65
XS 162
XS 163
M 8,41,5
s_add_u32 m0, s38, 3072
LA 18
RV 44,8,6784
AW 152,160
M 9,52,4
WL 7
LC_ 112
BR 81
AW 153,161
M 9,53,5
s_mul_i32 s53, s51, 24
RA 228,1,22528
AW 154,162
M 9,54,0
WL 7
LO 109,17,109
LU 17,109
AW 155,163
M 9,55,1
s_bitcmp1_b32 s51, 0
HS 24,17
s_addc_u32 s53, s53, 0
s_lshl_b32 s53, s53, 11
s_bitcmp1_b64 s[90:91], s51
s_cselect_b64 s[44:45], s[84:85], s[82:83]
s_add_u32 s44, s44, s53
s_addc_u32 s45, s45, 0
s_add_u32 s51, s51, %[d]
s_cmp_eq_u32 s51, %[wrap]
s_cselect_b32 s51, %[rst], s51
XS 164
LS 48,80
WV 16
BW 82
M 8,42,0
RV 48,8,6848
LL 16,66
LL 113,67
XS 165
XS 166
M 8,43,1
WV 15
LA 16
RA 232,1,23552
XS 167
M 7,30,0
WL 5
LC_ 113
DW 0
RA 240,7,0
AW 156,164
M 7,31,1
s_lshl_b32 s53, s52, 8
BR 82
AW 157,165
LS 40,81
s_add_u32 s48, %[alo], s53
BW 83
M 6,18,0
WL 8
LO 112,18,112
LU 18,112
AW 158,166
M 6,19,1
s_addc_u32 s49, %[ahi], 0
RA 236,1,24576
HS 24,18
AW 159,167
M 6,20,2
s_add_u32 s52, s52, %[d]
LL 17,68
LL 116,69
s_cmp_eq_u32 s52, %[wrap]
s_cselect_b32 s52, %[rst], s52
DW 1024
XS 168
M 6,21,3
LA 17
DW 2048
RA 244,7,256
XS 169
XS 170
M 7,32,2
LC_ 116
BR 83
XS 171
M 7,33,3
WL 6
DQ 3072
v_add_u32 v1, s37, v0
AW 160,168
LS 44,82
RA 248,7,512
AW 161,169
M 8,44,2
LO 113,16,113
LU 16,113
M 8,45,3
RA 216,1,0
HS 24,16
AW 162,170
M 9,56,2
WL 2
LL 18,70
LL 117,71
AW 163,171
)"
R"(M 9,57,3
s_add_u32 s50, s50, %[d]
LA 18
v_and_b32 v22, 0xfffffcff, v1
XS 172
XS 173
LS 48,83
s_cmp_eq_u32 s50, %[wrap]
RA 252,7,768
XS 174
M 6,22,4
s_cselect_b32 s50, %[rst], s50
LC_ 117
RA 220,1,1024
M 6,23,5
s_barrier
RV 252,22,5120
XS 175
M 7,34,4
LO 116,17,116
LU 17,116
AW 164,172
M 7,35,5
s_sub_u32 s39, 0, s39
RA 224,1,2048
HS 24,17
AW 165,173
M 8,46,4
s_add_u32 s38, s38, 32768
LL 16,80
LL 106,81
AW 166,174
M 8,47,5
s_cmp_eq_u32 s38, s77
LA 16
ds_read_b64 v[14:15], v5 offset:0
AW 167,175
v_mul_f32 v36, 0xbb2aaaab, v36
v_mul_f32 v37, 0xbb2aaaab, v37
v_mul_f32 v38, 0xbb2aaaab, v38
v_mul_f32 v39, 0xbb2aaaab, v39
s_nop 1
LK 40,240,72,0
LK 44,242,72,2
s_cselect_b32 s38, s76, s38
RA 240,7,1024
LK 48,244,73,0
s_nop 0
CF 128,40,41
LK 248,246,73,2
RA 244,7,1280
CF 129,42,43
M 9,58,4
WL 6
LC_ 106
v_subrev_u32 v5, s39, v5
M 9,59,5
WL 6
RA 228,1,3072
CF 130,44,45
Z 0,12,0
LO 117,18,117
LU 18,117
CF 131,46,47
M 0,13,1
s_mul_i32 s53, s50, 25600
HS 24,18
LK 40,248,74,0
CF 132,48,49
LK 44,250,74,2
s_add_u32 s40, %[wlo], s53
RA 248,7,1536
CF 133,50,51
GZ 0
s_addc_u32 s41, %[whi], 0
CF 134,248,249
CF 135,250,251
LK 48,252,75,0
CF 136,40,41
LK 248,254,75,2
s_mov_b32 m0, s38
RA 252,7,1792
CF 137,42,43
GS 1
WL 5
CF 138,44,45
CF 139,46,47
LK 40,240,76,0
CF 140,48,49
LK 44,242,76,2
RA 240,7,2048
CF 141,50,51
GS 2
WL 5
CF 142,248,249
CF 143,250,251
LK 48,244,77,0
CF 144,40,41
LK 248,246,77,2
RA 244,7,2304
CF 145,42,43
GS 3
CF 146,44,45
CF 147,46,47
Z 1,24,0
LL 17,82
LL 107,83
M 1,25,1
LA 17
RA 232,1,4096
Z 2,36,0
LC_ 107
HX 24
M 2,37,1
WL 6
W1 24,0
LK 40,248,78,0
CF 148,48,49
LK 44,250,78,2
RA 248,7,2560
CF 149,50,51
GS 4
CF 150,248,249
CF 151,250,251
Z 3,48,0
LO 16,16,106
LU 106,16
M 3,49,1
RA 236,1,6144
HZ 25,106
M 3,50,2
LL 18,84
LL 110,85
M 3,51,3
WL 8
LA 18
PK 106,16,104,56
LK 48,252,79,0
CF 152,40,41
LK 248,254,79,2
RA 252,7,2816
CF 153,42,43
GS 5
CF 154,44,45
CF 155,46,47
M 2,38,2
LC_ 110
PK 104,16,104,97
M 2,39,3
RA 216,1,7168
v_add_f32 v14, v14, v15
M 1,26,2
WL 7
LO 17,17,107
LU 107,17
v_add_f32_dpp v14, v14, v14 quad_perm:[1,0,3,2] row_mask:0xf bank_mask:0xf
M 1,27,3
DW 0
HS 25,107
LK 40,240,80,0
CF 156,48,49
LK 44,242,80,2
s_add_u32 s42, s44, s60
CF 157,50,51
v_add_f32_dpp v14, v14, v14 quad_perm:[2,3,0,1] row_mask:0xf bank_mask:0xf
GS 6
s_addc_u32 s43, s45, 0
CF 158,248,249
CF 159,250,251
M 0,14,2
WL 4
LL 16,86
LL 111,87
M 0,15,3
LA 16
PK 107,17,105,56
M 0,16,4
LC_ 111
PK 105,17,105,97
v_add_f32_dpp v14, v14, v14 row_half_mirror row_mask:0xf bank_mask:0xf
M 0,17,5
RA 220,1,8192
v_cvt_f64_f32 v[14:15], v14
LK 48,244,81,0
CF 160,40,41
LK 248,246,81,2
CF 161,42,43
GS 7
CF 162,44,45
CF 163,46,47
M 1,28,4
WL 4
LO 18,18,110
LU 110,18
M 1,29,5
RA 224,1,9216
HS 25,110
M 2,40,4
LL 17,88
LL 114,89
s_mov_b64 exec, s[54:55]
SP
s_mov_b64 exec, -1
M 2,41,5
LA 17
PK 110,18,108,56
LK 40,248,82,0
CF 164,48,49
LK 44,250,82,2
CF 165,50,51
GS 8
CF 166,248,249
CF 167,250,251
M 3,52,4
WL 2
LC_ 114
PK 108,18,108,97
M 3,53,5
RA 228,1,10240
PO 110
M 3,54,0
LO 16,16,111
LU 111,16
ST 104
M 3,55,1
DW 1024
HS 25,111
LK 48,252,83,0
CF 168,40,41
LK 248,254,83,2
s_add_u32 s42, s44, s61
CF 169,42,43
GS 9
s_addc_u32 s43, s45, 0
CF 170,44,45
CF 171,46,47
M 2,42,0
LL 18,90
LL 115,91
M 2,43,1
LA 18
PK 111,16,109,56
M 1,30,0
LC_ 115
PK 109,16,109,97
M 1,31,1
RA 232,1,11264
PO 111
M 0,18,0
WL 3
LO 17,17,114
LU 114,17
M 0,19,1
RV 252,22,5392
HS 25,114
M 0,20,2
LL 16,92
LL 118,93
M 0,21,3
LA 16
PK 114,17,112,56
M 1,32,2
LC_ 118
PK 112,17,112,97
M 1,33,3
RA 236,1,12288
RV 36,8,6912
DW 2048
GS 10
CF 172,48,49
CF 173,50,51
M 2,44,2
WL 6
LO 18,18,115
LU 115,18
M 2,45,3
RA 216,1,13312
HS 25,115
ST 108
M 3,56,2
WL 2
LL 17,94
LL 119,95
M 3,57,3
s_add_u32 s42, s44, s62
LA 17
PK 115,18,113,56
M 0,22,4
s_addc_u32 s43, s45, 0
LC_ 119
PK 113,18,113,97
M 0,23,5
s_add_u32 s40, s40, 3072
RA 220,1,14336
CF 174,248,249
M 1,34,4
LO 16,16,118
LU 118,16
CF 175,250,251
M 1,35,5
s_addc_u32 s41, s41, 0
RA 224,1,15360
HS 25,118
GS 11
s_add_u32 m0, s38, 3072
ST 112
s_nop 5
v_cndmask_b32 v52, v52, v53, s[78:79]
M 2,46,4
LL 18,56
LL 104,57
v_cndmask_b32 v54, v54, v55, s[78:79]
M 2,47,5
LA 18
PK 118,16,116,56
v_cndmask_b32 v52, v52, v54, s[80:81]
M 3,58,4
WL 2
LC_ 104
PK 116,16,116,97
M 3,59,5
RA 228,1,16384
PO 118
v_mul_f32 v52, 0x3b2aaaab, v52
Z 6,12,0
LO 17,17,119
LU 119,17
v_med3_f32 v53, v52, v39, v37
M 6,13,1
DW 0
HS 25,119
LG 52,53
Z 7,24,0
s_add_u32 s42, s44, s63
LL 16,58
LL 105,59
C 52,52,52
M 7,25,1
s_addc_u32 s43, s45, 0
LA 16
PK 119,17,117,56
Z 8,36,0
LC_ 105
PK 117,17,117,97
v_lshlrev_b32 v52, 16, v52
M 8,37,1
RA 232,1,17408
PO 119
v_add_f32 v52, v36, v52
Z 9,48,0
WL 3
LO 104,18,104
LU 18,104
C 52,52,52
M 9,49,1
RA 236,1,18432
HZ 24,18
M 9,50,2
LL 17,60
LL 108,61
v_lshlrev_b32 v52, 16, v52
M 9,51,3
LA 17
ST 116
HX 25
v_sqrt_f32 v52, v52
M 8,38,2
LC_ 108
W1 25,1024
C 52,52,52
M 8,39,3
RA 216,1,19456
v_add_u32 v4, s39, v4
v_lshlrev_b32 v52, 16, v52
M 7,26,2
WL 6
LO 105,16,105
LU 16,105
M 7,27,3
DW 1024
HS 24,16
v_rcp_f32 v52, v52
M 6,14,2
WL 4
LL 18,62
LL 109,63
v_mul_f32 v52, v38, v52
M 6,15,3
s_add_u32 s37, s37, 32768
LA 18
RA 220,1,20480
CF 52,52,52
M 6,16,4
s_cmp_eq_u32 s37, s75
LC_ 109
ds_bpermute_b32 v13, v28, v52
s_cselect_b32 s37, s74, s37
M 6,17,5
DW 2048
M 7,28,4
)"
R"(WL 6
LO 108,17,108
LU 17,108
M 7,29,5
s_mul_i32 s53, s51, 24
RA 224,1,21504
HS 24,17
M 8,40,4
s_bitcmp1_b32 s51, 0
LL 16,64
LL 112,65
M 8,41,5
s_addc_u32 s53, s53, 0
LA 16
M 9,52,4
WL 4
LC_ 112
M 9,53,5
s_lshl_b32 s53, s53, 11
RA 228,1,22528
M 9,54,0
WL 4
LO 109,18,109
LU 18,109
M 9,55,1
s_bitcmp1_b64 s[90:91], s51
HS 24,18
s_cselect_b64 s[44:45], s[84:85], s[82:83]
s_add_u32 s44, s44, s53
s_addc_u32 s45, s45, 0
s_add_u32 s51, s51, %[d]
s_cmp_eq_u32 s51, %[wrap]
s_cselect_b32 s51, %[rst], s51
M 8,42,0
s_lshl_b32 s53, s52, 8
LL 17,66
LL 113,67
M 8,43,1
s_add_u32 s48, %[alo], s53
LA 17
RA 232,1,23552
XS 128
M 7,30,0
s_addc_u32 s49, %[ahi], 0
LC_ 113
XS 129
M 7,31,1
s_add_u32 s52, s52, %[d]
XS 130
XS 131
M 6,18,0
WL 3
LO 112,16,112
LU 16,112
M 6,19,1
s_cmp_eq_u32 s52, %[wrap]
RA 236,1,24576
HS 24,16
AW 168,128
AW 169,129
M 6,20,2
s_cselect_b32 s52, %[rst], s52
DQ 3072
LL 18,68
LL 116,69
AW 170,130
M 6,21,3
LA 18
v_add_u32 v1, s37, v0
AW 171,131
M 7,32,2
s_add_u32 s50, s50, %[d]
LC_ 116
XS 132
M 7,33,3
s_cmp_eq_u32 s50, %[wrap]
RA 216,1,0
v_and_b32 v22, 0xfffffcff, v1
XS 133
M 8,44,2
WL 4
LO 113,17,113
LU 17,113
XS 134
M 8,45,3
s_cselect_b32 s50, %[rst], s50
HS 24,17
XS 135
M 9,56,2
WL 3
LL 16,70
LL 117,71
AW 172,132
M 9,57,3
WV 21
LA 16
RA 220,1,1024
AW 173,133
M 6,22,4
s_barrier
LC_ 117
RV 252,22,5120
AW 174,134
M 6,23,5
s_sub_u32 s39, 0, s39
AW 175,135
v_min_u32 v35, v35, v13
M 7,34,4
WL 5
LO 116,18,116
LU 18,116
M 7,35,5
s_add_u32 s38, s38, 32768
RA 224,1,2048
HS 24,18
XS 136
M 8,46,4
s_cmp_eq_u32 s38, s77
LL 17,80
LL 106,81
XS 137
M 8,47,5
s_cselect_b32 s38, s76, s38
LA 17
XS 138
XS 139
M 9,58,4
WL 4
LC_ 106
AW 176,136
M 9,59,5
WL 3
RA 228,1,3072
AW 177,137
Z 0,12,0
LO 117,16,117
LU 16,117
AW 178,138
M 0,13,1
HS 24,16
AW 179,139
Z 1,24,0
LL 18,82
LL 107,83
XS 140
M 1,25,1
LA 18
RA 232,1,4096
XS 141
Z 2,36,0
LC_ 107
HX 24
XS 142
M 2,37,1
W1 24,0
XS 143
Z 3,48,0
WL 5
LO 17,17,106
LU 106,17
AW 180,140
M 3,49,1
HZ 25,106
AW 181,141
Z 4,60,0
LL 16,84
LL 110,85
AW 182,142
M 4,61,1
LA 16
PK 106,17,104,56
AW 183,143
Z 5,72,0
WL 5
LC_ 110
PK 104,17,104,97
XS 144
M 5,73,1
RA 236,1,6144
XS 145
XS 146
M 5,74,2
WL 4
LO 18,18,107
LU 107,18
XS 147
M 5,75,3
s_add_u32 s42, s44, s60
HS 25,107
AW 184,144
M 4,62,2
s_addc_u32 s43, s45, 0
LL 17,86
LL 111,87
AW 185,145
M 4,63,3
LA 17
PK 107,18,105,56
AW 186,146
M 3,50,2
LC_ 111
PK 105,18,105,97
AW 187,147
M 3,51,3
RA 216,1,7168
XS 148
M 2,38,2
WL 4
LO 16,16,110
LU 110,16
XS 149
M 2,39,3
ST 104
HS 25,110
XS 150
M 1,26,2
LL 18,88
LL 114,89
XS 151
M 1,27,3
LA 18
PK 110,16,108,56
AW 188,148
M 0,14,2
WL 4
LC_ 114
PK 108,16,108,97
AW 189,149
M 0,15,3
s_mov_b32 m0, %[pm]
RA 220,1,8192
PO 110
AW 190,150
M 0,16,4
WL 4
LO 17,17,111
LU 111,17
AW 191,151
M 0,17,5
DF
HS 25,111
XS 152
M 1,28,4
s_add_u32 s42, s44, s61
LL 16,90
LL 115,91
XS 153
XS 154
M 1,29,5
s_addc_u32 s43, s45, 0
LA 16
PK 111,17,109,56
XS 155
M 2,40,4
LC_ 115
PK 109,17,109,97
AW 192,152
M 2,41,5
RA 224,1,9216
PO 111
AW 193,153
M 3,52,4
WL 4
LO 18,18,114
LU 114,18
AW 194,154
M 3,53,5
ST 108
HS 25,114
AW 195,155
M 4,64,4
LL 17,92
LL 118,93
XS 156
XS 157
M 4,65,5
LA 17
PK 114,18,112,56
XS 158
XS 159
AW 196,156
M 5,76,4
WL 4
LC_ 118
PK 112,18,112,97
AW 197,157
AW 198,158
AW 199,159
M 5,77,5
RA 228,1,10240
XS 160
M 5,78,0
WL 4
LO 16,16,115
LU 115,16
M 5,79,1
s_add_u32 s42, s44, s62
HS 25,115
XS 161
M 4,66,0
s_addc_u32 s43, s45, 0
LL 18,94
LL 119,95
M 4,67,1
LA 18
PK 115,16,113,56
M 3,54,0
LC_ 119
PK 113,16,113,97
M 3,55,1
RA 232,1,11264
XS 162
M 2,42,0
WL 4
LO 17,17,118
LU 118,17
XS 163
M 2,43,1
RV 252,22,5392
HS 25,118
AW 200,160
M 1,30,0
ST 112
AW 201,161
M 1,31,1
RA 236,1,12288
PK 118,17,116,56
AW 202,162
M 0,18,0
WL 5
PK 116,17,116,97
AW 203,163
M 0,19,1
PO 118
XS 164
M 0,20,2
WL 3
LO 18,18,119
LU 119,18
XS 165
M 0,21,3
s_add_u32 s42, s44, s63
HS 25,119
XS 166
M 1,32,2
s_addc_u32 s43, s45, 0
HX 25
XS 167
M 1,33,3
RA 216,1,13312
PK 119,18,117,56
AW 204,164
M 2,44,2
W1 25,1024
PK 117,18,117,97
AW 205,165
M 2,45,3
PO 119
v_add_u32 v4, s39, v4
AW 206,166
M 3,56,2
ST 116
AW 207,167
XS 168
M 3,57,3
XS 169
XS 170
M 4,68,2
XS 171
AW 208,168
M 4,69,3
RA 220,1,14336
AW 209,169
M 5,80,2
WL 3
AW 210,170
AW 211,171
M 5,81,3
XS 172
XS 173
M 0,22,4
XS 174
XS 175
M 0,23,5
AW 212,172
AW 213,173
M 1,34,4
AW 214,174
AW 215,175
M 1,35,5
RA 224,1,15360
M 2,46,4
LL 16,56
LL 104,57
M 2,47,5
LA 16
ds_read_b64 v[14:15], v5 offset:0
M 3,58,4
LC_ 104
v_subrev_u32 v5, s39, v5
)"
R"(M 3,59,5
M 4,70,4
M 4,71,5
RA 228,1,16384
M 5,82,4
WL 5
LL 17,58
LL 105,59
M 5,83,5
LA 17
Z 6,12,0
LC_ 105
M 6,13,1
Z 7,24,0
WL 3
LO 104,16,104
LU 16,104
M 7,25,1
RA 232,1,17408
HZ 24,16
Z 8,36,0
LL 18,60
LL 108,61
M 8,37,1
s_mul_i32 s53, s50, 25600
LA 18
v_add_f32 v14, v14, v15
Z 9,48,0
s_add_u32 s40, %[wlo], s53
LC_ 108
v_add_f32_dpp v14, v14, v14 quad_perm:[1,0,3,2] row_mask:0xf bank_mask:0xf
M 9,49,1
s_addc_u32 s41, %[whi], 0
v_add_f32_dpp v14, v14, v14 quad_perm:[2,3,0,1] row_mask:0xf bank_mask:0xf
Z 10,60,0
WL 3
LO 105,17,105
LU 17,105
M 10,61,1
s_mov_b32 m0, s38
RA 236,1,18432
HS 24,17
Z 11,72,0
LL 16,62
LL 109,63
M 11,73,1
LA 16
DW 0
M 11,74,2
LC_ 109
v_add_f32_dpp v14, v14, v14 row_half_mirror row_mask:0xf bank_mask:0xf
M 11,75,3
v_cvt_f64_f32 v[14:15], v14
M 10,62,2
WL 3
LO 108,18,108
LU 18,108
M 10,63,3
s_add_u32 s37, s37, 32768
RA 216,1,19456
HS 24,18
M 9,50,2
s_cmp_eq_u32 s37, s75
LL 17,64
LL 112,65
M 9,51,3
s_cselect_b32 s37, s74, s37
LA 17
s_mov_b64 exec, s[54:55]
SP
s_mov_b64 exec, -1
M 8,38,2
LC_ 112
DW 1024
M 8,39,3
M 7,26,2
WL 3
LO 109,16,109
LU 16,109
M 7,27,3
RA 220,1,20480
HS 24,16
M 6,14,2
LL 18,66
LL 113,67
M 6,15,3
LA 18
DW 2048
M 6,16,4
LC_ 113
M 6,17,5
M 7,28,4
WL 3
LO 112,17,112
LU 17,112
M 7,29,5
s_mul_i32 s53, s51, 24
RA 224,1,21504
HS 24,17
M 8,40,4
s_bitcmp1_b32 s51, 0
LL 16,68
LL 116,69
M 8,41,5
s_addc_u32 s53, s53, 0
LA 16
M 9,52,4
s_lshl_b32 s53, s53, 11
LC_ 116
M 9,53,5
s_bitcmp1_b64 s[90:91], s51
M 10,64,4
WL 3
LO 113,18,113
LU 18,113
M 10,65,5
s_cselect_b64 s[44:45], s[84:85], s[82:83]
RA 228,1,22528
HS 24,18
M 11,76,4
LL 17,70
LL 117,71
M 11,77,5
s_add_u32 s44, s44, s53
LA 17
M 11,78,0
s_addc_u32 s45, s45, 0
LC_ 117
M 11,79,1
s_add_u32 s51, s51, %[d]
M 10,66,0
WL 3
LO 116,16,116
LU 16,116
M 10,67,1
s_cmp_eq_u32 s51, %[wrap]
RA 232,1,23552
HS 24,16
M 9,54,0
s_cselect_b32 s51, %[rst], s51
LL 18,72
LL 120,73
M 9,55,1
s_add_u32 s40, s40, 3072
LA 18
M 8,42,0
s_addc_u32 s41, s41, 0
LC_ 120
s_add_u32 m0, s38, 3072
M 8,43,1
DW 0
M 7,30,0
WL 3
LO 117,17,117
LU 17,117
M 7,31,1
RA 236,1,24576
HS 24,17
M 6,18,0
LL 16,74
LL 121,75
M 6,19,1
LA 16
DW 1024
M 6,20,2
LC_ 121
DW 2048
M 6,21,3
s_lshl_b32 s53, s52, 8
v_add_u32 v1, s37, v0
M 7,32,2
WL 3
LO 120,18,120
LU 18,120
M 7,33,3
s_add_u32 s48, %[alo], s53
RA 216,1,0
HS 24,18
M 8,44,2
s_addc_u32 s49, %[ahi], 0
LL 17,76
LL 124,77
M 8,45,3
s_add_u32 s52, s52, %[d]
LA 17
v_and_b32 v22, 0xfffffcff, v1
M 9,56,2
s_cmp_eq_u32 s52, %[wrap]
LC_ 124
M 9,57,3
s_cselect_b32 s52, %[rst], s52
DQ 3072
M 10,68,2
WL 3
LO 121,16,121
LU 16,121
M 10,69,3
s_add_u32 s50, s50, %[d]
RA 220,1,1024
HS 24,16
M 11,80,2
LL 18,78
LL 125,79
M 11,81,3
s_cmp_eq_u32 s50, %[wrap]
LA 18
RV 252,22,5120
M 6,22,4
s_cselect_b32 s50, %[rst], s50
LC_ 125
M 6,23,5
WV 25
M 7,34,4
WL 4
LO 124,17,124
LU 17,124
M 7,35,5
s_add_u32 s38, s38, 32768
RA 224,1,2048
HS 24,17
M 8,46,4
s_cmp_eq_u32 s38, s77
LL 16,80
LL 106,81
M 8,47,5
s_cselect_b32 s38, s76, s38
LA 16
M 9,58,4
s_barrier
LC_ 106
M 9,59,5
s_sub_u32 s39, 0, s39
M 10,70,4
WL 3
LO 125,18,125
LU 18,125
M 10,71,5
RA 228,1,3072
HS 24,18
M 11,82,4
LL 17,82
LL 107,83
M 11,83,5
LA 17
HX 24
Z 0,12,0
LC_ 107
W1 24,0
M 0,13,1
Z 1,24,0
WL 4
LO 16,16,106
LU 106,16
M 1,25,1
RA 232,1,4096
HZ 25,106
Z 2,36,0
LL 18,84
LL 110,85
M 2,37,1
LA 18
PK 106,16,104,56
Z 3,48,0
LC_ 110
PK 104,16,104,97
M 3,49,1
Z 4,60,0
WL 4
LO 17,17,107
LU 107,17
M 4,61,1
s_add_u32 s42, s44, s60
RA 236,1,6144
HS 25,107
Z 5,72,0
LL 16,86
LL 111,87
M 5,73,1
s_addc_u32 s43, s45, 0
LA 16
PK 107,17,105,56
M 5,74,2
LC_ 111
PK 105,17,105,97
M 5,75,3
ST 104
M 4,62,2
WL 3
LO 18,18,110
LU 110,18
M 4,63,3
RA 216,1,7168
HS 25,110
M 3,50,2
LL 17,88
LL 114,89
M 3,51,3
LA 17
PK 110,18,108,56
M 2,38,2
LC_ 114
PK 108,18,108,97
M 2,39,3
PO 110
M 1,26,2
WL 3
LO 16,16,111
LU 111,16
M 1,27,3
s_add_u32 s42, s44, s61
RA 220,1,8192
HS 25,111
M 0,14,2
LL 18,90
LL 115,91
M 0,15,3
s_addc_u32 s43, s45, 0
LA 18
PK 111,16,109,56
M 0,16,4
s_mov_b32 m0, %[pm]
LC_ 115
PK 109,16,109,97
M 0,17,5
DF
PO 111
M 1,28,4
WL 3
LO 17,17,114
LU 114,17
M 1,29,5
RA 224,1,9216
HS 25,114
M 2,40,4
LL 16,92
LL 118,93
M 2,41,5
LA 16
PK 114,17,112,56
M 3,52,4
LC_ 118
PK 112,17,112,97
M 3,53,5
ST 108
M 4,64,4
WL 3
LO 18,18,115
LU 115,18
M 4,65,5
s_add_u32 s42, s44, s62
RA 228,1,10240
HS 25,115
M 5,76,4
LL 17,94
LL 119,95
M 5,77,5
s_addc_u32 s43, s45, 0
LA 17
PK 115,18,113,56
M 5,78,0
LC_ 119
PK 113,18,113,97
M 5,79,1
ST 112
M 4,66,0
WL 3
LO 16,16,118
LU 118,16
M 4,67,1
RA 232,1,11264
HS 25,118
M 3,54,0
LL 18,96
LL 122,97
M 3,55,1
LA 18
PK 118,16,116,56
M 2,42,0
LC_ 122
PK 116,16,116,97
M 2,43,1
RV 252,22,5392
PO 118
M 1,30,0
WL 4
LO 17,17,119
LU 119,17
M 1,31,1
s_add_u32 s42, s44, s63
RA 236,1,12288
HS 25,119
M 0,18,0
LL 16,98
LL 123,99
M 0,19,1
s_addc_u32 s43, s45, 0
LA 16
PK 119,17,117,56
M 0,20,2
LC_ 123
)"
R"(PK 117,17,117,97
M 0,21,3
PO 119
M 1,32,2
WL 4
LO 18,18,122
LU 122,18
M 1,33,3
RA 216,1,13312
HS 25,122
M 2,44,2
LL 17,100
LL 126,101
M 2,45,3
LA 17
PK 122,18,120,56
M 3,56,2
LC_ 126
PK 120,18,120,97
M 3,57,3
ST 116
M 4,68,2
WL 3
LO 16,16,123
LU 123,16
M 4,69,3
s_add_u32 s42, s44, s64
RA 220,1,14336
HS 25,123
M 5,80,2
LL 18,102
LL 127,103
M 5,81,3
s_addc_u32 s43, s45, 0
LA 18
PK 123,16,121,56
M 0,22,4
LC_ 127
PK 121,16,121,97
M 0,23,5
ST 120
M 1,34,4
WL 3
LO 17,17,126
LU 126,17
M 1,35,5
RA 224,1,15360
HS 25,126
M 2,46,4
LL 16,56
LL 104,57
M 2,47,5
LA 16
PK 126,17,124,56
M 3,58,4
LC_ 104
PK 124,17,124,97
M 3,59,5
ds_read_b64 v[14:15], v5 offset:0
PO 126
M 4,70,4
WL 4
LO 18,18,127
LU 127,18
M 4,71,5
s_add_u32 s42, s44, s65
RA 228,1,16384
HS 25,127
M 5,82,4
LL 17,58
LL 105,59
M 5,83,5
s_addc_u32 s43, s45, 0
LA 17
PK 127,18,125,56
Z 6,12,0
LC_ 105
PK 125,18,125,97
M 6,13,1
PO 127
v_subrev_u32 v5, s39, v5
Z 7,24,0
WL 4
LO 104,16,104
LU 16,104
M 7,25,1
RA 232,1,17408
HZ 24,16
Z 8,36,0
WL 4
LL 18,60
LL 108,61
M 8,37,1
s_mul_i32 s53, s50, 25600
LA 18
v_add_f32 v14, v14, v15
Z 9,48,0
s_add_u32 s40, %[wlo], s53
LC_ 108
v_add_f32_dpp v14, v14, v14 quad_perm:[1,0,3,2] row_mask:0xf bank_mask:0xf
M 9,49,1
s_addc_u32 s41, %[whi], 0
v_add_f32_dpp v14, v14, v14 quad_perm:[2,3,0,1] row_mask:0xf bank_mask:0xf
HX 25
Z 10,60,0
WL 3
LO 105,17,105
LU 17,105
M 10,61,1
s_mov_b32 m0, s38
RA 236,1,18432
HS 24,17
Z 11,72,0
LL 16,62
LL 109,63
M 11,73,1
LA 16
W1 25,1024
M 11,74,2
LC_ 109
ST 124
M 11,75,3
DW 0
v_add_f32_dpp v14, v14, v14 row_half_mirror row_mask:0xf bank_mask:0xf
M 10,62,2
WL 4
LO 108,18,108
LU 18,108
M 10,63,3
s_add_u32 s37, s37, 32768
RA 216,1,19456
HS 24,18
M 9,50,2
s_cmp_eq_u32 s37, s75
LL 17,64
LL 112,65
M 9,51,3
s_cselect_b32 s37, s74, s37
LA 17
DW 1024
M 8,38,2
LC_ 112
v_cvt_f64_f32 v[14:15], v14
M 8,39,3
s_mov_b64 exec, s[54:55]
SP
s_mov_b64 exec, -1
v_add_u32 v4, s39, v4
M 7,26,2
WL 3
LO 109,16,109
LU 16,109
M 7,27,3
RA 220,1,20480
HS 24,16
M 6,14,2
LL 18,66
LL 113,67
M 6,15,3
LA 18
DW 2048
M 6,16,4
LC_ 113
M 6,17,5
M 7,28,4
WL 3
LO 112,17,112
LU 17,112
M 7,29,5
s_mul_i32 s53, s51, 24
RA 224,1,21504
HS 24,17
M 8,40,4
s_bitcmp1_b32 s51, 0
LL 16,68
LL 116,69
M 8,41,5
s_addc_u32 s53, s53, 0
LA 16
M 9,52,4
s_lshl_b32 s53, s53, 11
LC_ 116
M 9,53,5
s_bitcmp1_b64 s[90:91], s51
M 10,64,4
WL 3
LO 113,18,113
LU 18,113
M 10,65,5
s_cselect_b64 s[44:45], s[84:85], s[82:83]
RA 228,1,22528
HS 24,18
M 11,76,4
LL 17,70
LL 117,71
M 11,77,5
s_add_u32 s44, s44, s53
LA 17
M 11,78,0
s_addc_u32 s45, s45, 0
LC_ 117
M 11,79,1
s_add_u32 s51, s51, %[d]
M 10,66,0
WL 3
LO 116,16,116
LU 16,116
M 10,67,1
s_cmp_eq_u32 s51, %[wrap]
RA 232,1,23552
HS 24,16
M 9,54,0
s_cselect_b32 s51, %[rst], s51
LL 18,72
LL 120,73
M 9,55,1
s_add_u32 s40, s40, 3072
LA 18
M 8,42,0
s_addc_u32 s41, s41, 0
LC_ 120
s_add_u32 m0, s38, 3072
M 8,43,1
DW 0
M 7,30,0
WL 3
LO 117,17,117
LU 17,117
M 7,31,1
RA 236,1,24576
HS 24,17
M 6,18,0
LL 16,74
LL 121,75
M 6,19,1
LA 16
DW 1024
M 6,20,2
LC_ 121
DW 2048
M 6,21,3
s_lshl_b32 s53, s52, 8
v_add_u32 v1, s37, v0
M 7,32,2
WL 3
LO 120,18,120
LU 18,120
M 7,33,3
s_add_u32 s48, %[alo], s53
RA 216,1,0
HS 24,18
M 8,44,2
s_addc_u32 s49, %[ahi], 0
LL 17,76
LL 124,77
M 8,45,3
s_add_u32 s52, s52, %[d]
LA 17
v_and_b32 v22, 0xfffffcff, v1
M 9,56,2
s_cmp_eq_u32 s52, %[wrap]
LC_ 124
M 9,57,3
s_cselect_b32 s52, %[rst], s52
DQ 3072
M 10,68,2
WL 3
LO 121,16,121
LU 16,121
M 10,69,3
s_add_u32 s50, s50, %[d]
RA 220,1,1024
HS 24,16
M 11,80,2
LL 18,78
LL 125,79
M 11,81,3
s_cmp_eq_u32 s50, %[wrap]
LA 18
RV 252,22,5120
M 6,22,4
s_cselect_b32 s50, %[rst], s50
LC_ 125
M 6,23,5
WV 28
v_add_u32 v19, 1, v19
M 7,34,4
WL 4
LO 124,17,124
LU 17,124
M 7,35,5
s_add_u32 s38, s38, 32768
RA 224,1,2048
HS 24,17
M 8,46,4
s_cmp_eq_u32 s38, s77
LL 16,80
LL 106,81
M 8,47,5
s_cselect_b32 s38, s76, s38
LA 16
FP
M 9,58,4
s_sub_u32 s39, 0, s39
LC_ 106
M 9,59,5
M 10,70,4
WL 4
LO 125,18,125
LU 18,125
M 10,71,5
RA 228,1,3072
HS 24,18
M 11,82,4
LL 17,82
LL 107,83
M 11,83,5
s_mov_b32 s66, 28
1:
LA 17
HX 24
Z 0,12,0
LC_ 107
M 0,13,1
Z 1,24,0
WL 3
LO 16,16,106
LU 106,16
M 1,25,1
RA 232,1,4096
HZ 25,106
Z 2,36,0
LL 18,84
LL 110,85
M 2,37,1
LA 18
PK 106,16,104,56
Z 3,48,0
LC_ 110
PK 104,16,104,97
M 3,49,1
Z 4,60,0
WL 3
LO 17,17,107
LU 107,17
M 4,61,1
s_add_u32 s42, s44, s60
RA 236,1,6144
HS 25,107
Z 5,72,0
LL 16,86
LL 111,87
M 5,73,1
s_addc_u32 s43, s45, 0
LA 16
PK 107,17,105,56
M 5,74,2
LC_ 111
PK 105,17,105,97
M 5,75,3
ST 104
M 4,62,2
WL 3
LO 18,18,110
LU 110,18
M 4,63,3
RA 216,1,7168
HS 25,110
M 3,50,2
LL 17,88
LL 114,89
M 3,51,3
LA 17
PK 110,18,108,56
M 2,38,2
LC_ 114
PK 108,18,108,97
M 2,39,3
FR
PO 110
M 1,26,2
WL 4
LO 16,16,111
LU 111,16
M 1,27,3
s_add_u32 s42, s44, s61
RA 220,1,8192
HS 25,111
M 0,14,2
LL 18,90
LL 115,91
M 0,15,3
s_addc_u32 s43, s45, 0
LA 18
PK 111,16,109,56
M 0,16,4
s_mov_b32 m0, %[pm]
LC_ 115
PK 109,16,109,97
M 0,17,5
)"
R"(DF
PO 111
M 1,28,4
WL 4
LO 17,17,114
LU 114,17
M 1,29,5
RA 224,1,9216
HS 25,114
M 2,40,4
LL 16,92
LL 118,93
M 2,41,5
LA 16
PK 114,17,112,56
M 3,52,4
LC_ 118
PK 112,17,112,97
M 3,53,5
ST 108
M 4,64,4
WL 3
LO 18,18,115
LU 115,18
M 4,65,5
s_add_u32 s42, s44, s62
RA 228,1,10240
HS 25,115
M 5,76,4
LL 17,94
LL 119,95
M 5,77,5
s_addc_u32 s43, s45, 0
LA 17
PK 115,18,113,56
M 5,78,0
LC_ 119
PK 113,18,113,97
M 5,79,1
ST 112
M 4,66,0
WL 3
LO 16,16,118
LU 118,16
M 4,67,1
RA 232,1,11264
HS 25,118
M 3,54,0
LL 18,96
LL 122,97
M 3,55,1
LA 18
PK 118,16,116,56
v_cmp_gt_u32 vcc, v19, v15
M 2,42,0
LC_ 122
PK 116,16,116,97
M 2,43,1
RV 252,22,5392
PO 118
M 1,30,0
WL 4
LO 17,17,119
LU 119,17
M 1,31,1
s_cbranch_vccnz .Lk1s0
.Lk1r0:
RA 236,1,12288
HS 25,119
M 0,18,0
LL 16,98
LL 123,99
M 0,19,1
s_add_u32 s42, s44, s63
LA 16
PK 119,17,117,56
M 0,20,2
s_addc_u32 s43, s45, 0
LC_ 123
PK 117,17,117,97
M 0,21,3
W1 24,0
PO 119
M 1,32,2
WL 5
LO 18,18,122
LU 122,18
M 1,33,3
RA 216,1,13312
HS 25,122
M 2,44,2
LL 17,100
LL 126,101
M 2,45,3
LA 17
PK 122,18,120,56
M 3,56,2
LC_ 126
PK 120,18,120,97
M 3,57,3
ST 116
M 4,68,2
WL 4
LO 16,16,123
LU 123,16
M 4,69,3
s_add_u32 s42, s44, s64
RA 220,1,14336
HS 25,123
M 5,80,2
LL 18,102
LL 127,103
M 5,81,3
s_addc_u32 s43, s45, 0
LA 18
PK 123,16,121,56
M 0,22,4
LC_ 127
PK 121,16,121,97
M 0,23,5
ST 120
M 1,34,4
WL 3
LO 17,17,126
LU 126,17
M 1,35,5
RA 224,1,15360
HS 25,126
M 2,46,4
LL 16,56
LL 104,57
M 2,47,5
LA 16
PK 126,17,124,56
M 3,58,4
LC_ 104
PK 124,17,124,97
M 3,59,5
ds_read_b64 v[14:15], v5 offset:0
PO 126
M 4,70,4
WL 4
LO 18,18,127
LU 127,18
M 4,71,5
s_add_u32 s42, s44, s65
RA 228,1,16384
HS 25,127
M 5,82,4
LL 17,58
LL 105,59
M 5,83,5
s_addc_u32 s43, s45, 0
LA 17
PK 127,18,125,56
Z 6,12,0
LC_ 105
PK 125,18,125,97
M 6,13,1
PO 127
v_subrev_u32 v5, s39, v5
Z 7,24,0
WL 4
LO 104,16,104
LU 16,104
M 7,25,1
RA 232,1,17408
HZ 24,16
Z 8,36,0
WL 4
LL 18,60
LL 108,61
M 8,37,1
s_mul_i32 s53, s50, 25600
LA 18
v_add_f32 v14, v14, v15
Z 9,48,0
s_add_u32 s40, %[wlo], s53
LC_ 108
v_add_f32_dpp v14, v14, v14 quad_perm:[1,0,3,2] row_mask:0xf bank_mask:0xf
M 9,49,1
s_addc_u32 s41, %[whi], 0
Z 10,60,0
WL 3
LO 105,17,105
LU 17,105
M 10,61,1
s_mov_b32 m0, s38
RA 236,1,18432
HS 24,17
Z 11,72,0
LL 16,62
LL 109,63
M 11,73,1
LA 16
ST 124
M 11,74,2
LC_ 109
DW 0
M 11,75,3
v_add_f32_dpp v14, v14, v14 quad_perm:[2,3,0,1] row_mask:0xf bank_mask:0xf
HX 25
M 10,62,2
WL 3
LO 108,18,108
LU 18,108
W1 25,1024
M 10,63,3
s_add_u32 s37, s37, 32768
RA 216,1,19456
HS 24,18
M 9,50,2
s_cmp_eq_u32 s37, s75
LL 17,64
LL 112,65
M 9,51,3
s_cselect_b32 s37, s74, s37
LA 17
DW 1024
M 8,38,2
LC_ 112
v_add_f32_dpp v14, v14, v14 row_half_mirror row_mask:0xf bank_mask:0xf
M 8,39,3
v_cvt_f64_f32 v[14:15], v14
v_add_u32 v4, s39, v4
M 7,26,2
WL 4
LO 109,16,109
LU 16,109
M 7,27,3
RA 220,1,20480
HS 24,16
M 6,14,2
LL 18,66
LL 113,67
M 6,15,3
LA 18
s_mov_b64 exec, s[54:55]
SP
s_mov_b64 exec, -1
M 6,16,4
LC_ 113
DW 2048
M 6,17,5
M 7,28,4
WL 3
LO 112,17,112
LU 17,112
M 7,29,5
s_mul_i32 s53, s51, 24
RA 224,1,21504
HS 24,17
M 8,40,4
s_bitcmp1_b32 s51, 0
LL 16,68
LL 116,69
M 8,41,5
s_addc_u32 s53, s53, 0
LA 16
M 9,52,4
s_lshl_b32 s53, s53, 11
LC_ 116
M 9,53,5
s_bitcmp1_b64 s[90:91], s51
M 10,64,4
WL 3
LO 113,18,113
LU 18,113
M 10,65,5
s_cselect_b64 s[44:45], s[84:85], s[82:83]
RA 228,1,22528
HS 24,18
M 11,76,4
LL 17,70
LL 117,71
M 11,77,5
s_add_u32 s44, s44, s53
LA 17
M 11,78,0
s_addc_u32 s45, s45, 0
LC_ 117
M 11,79,1
s_add_u32 s51, s51, %[d]
M 10,66,0
WL 3
LO 116,16,116
LU 16,116
M 10,67,1
s_cmp_eq_u32 s51, %[wrap]
RA 232,1,23552
HS 24,16
M 9,54,0
s_cselect_b32 s51, %[rst], s51
LL 18,72
LL 120,73
M 9,55,1
s_add_u32 s40, s40, 3072
LA 18
M 8,42,0
s_addc_u32 s41, s41, 0
LC_ 120
s_add_u32 m0, s38, 3072
M 8,43,1
DW 0
M 7,30,0
WL 3
LO 117,17,117
LU 17,117
M 7,31,1
RA 236,1,24576
HS 24,17
M 6,18,0
LL 16,74
LL 121,75
M 6,19,1
LA 16
DW 1024
M 6,20,2
LC_ 121
DW 2048
M 6,21,3
s_lshl_b32 s53, s52, 8
v_add_u32 v1, s37, v0
M 7,32,2
WL 3
LO 120,18,120
LU 18,120
M 7,33,3
s_add_u32 s48, %[alo], s53
RA 216,1,0
HS 24,18
M 8,44,2
s_addc_u32 s49, %[ahi], 0
LL 17,76
LL 124,77
M 8,45,3
s_add_u32 s52, s52, %[d]
LA 17
v_and_b32 v22, 0xfffffcff, v1
M 9,56,2
s_cmp_eq_u32 s52, %[wrap]
LC_ 124
M 9,57,3
s_cselect_b32 s52, %[rst], s52
DQ 3072
M 10,68,2
WL 3
LO 121,16,121
LU 16,121
M 10,69,3
s_add_u32 s50, s50, %[d]
RA 220,1,1024
HS 24,16
M 11,80,2
LL 18,78
LL 125,79
M 11,81,3
s_cmp_eq_u32 s50, %[wrap]
LA 18
RV 252,22,5120
M 6,22,4
s_cselect_b32 s50, %[rst], s50
LC_ 125
M 6,23,5
WV 30
v_add_u32 v19, 1, v19
M 7,34,4
WL 4
LO 124,17,124
LU 17,124
M 7,35,5
s_add_u32 s38, s38, 32768
RA 224,1,2048
HS 24,17
M 8,46,4
s_cmp_eq_u32 s38, s77
LL 16,80
LL 106,81
M 8,47,5
s_cselect_b32 s38, s76, s38
LA 16
FP
M 9,58,4
s_sub_u32 s39, 0, s39
LC_ 106
M 9,59,5
M 10,70,4
WL 4
LO 125,18,125
LU 18,125
M 10,71,5
RA 228,1,3072
HS 24,18
M 11,82,4
s_sub_u32 s66, s66, 1
LL 17,82
LL 107,83
M 11,83,5
s_cbranch_scc0 1b
7:
)"
R"(LA 17
HX 24
Z 0,12,0
LC_ 107
M 0,13,1
Z 1,24,0
WL 3
LO 16,16,106
LU 106,16
M 1,25,1
RA 232,1,4096
HZ 25,106
Z 2,36,0
LL 18,84
LL 110,85
M 2,37,1
LA 18
PK 106,16,104,56
Z 3,48,0
LC_ 110
PK 104,16,104,97
M 3,49,1
Z 4,60,0
WL 3
LO 17,17,107
LU 107,17
M 4,61,1
s_add_u32 s42, s44, s60
RA 236,1,6144
HS 25,107
Z 5,72,0
LL 16,86
LL 111,87
M 5,73,1
s_addc_u32 s43, s45, 0
LA 16
PK 107,17,105,56
M 5,74,2
LC_ 111
PK 105,17,105,97
M 5,75,3
ST 104
M 4,62,2
WL 3
LO 18,18,110
LU 110,18
M 4,63,3
RA 216,1,7168
HS 25,110
M 3,50,2
LL 17,88
LL 114,89
M 3,51,3
LA 17
PK 110,18,108,56
M 2,38,2
LC_ 114
PK 108,18,108,97
M 2,39,3
FR
PO 110
M 1,26,2
WL 4
LO 16,16,111
LU 111,16
M 1,27,3
s_add_u32 s42, s44, s61
RA 220,1,8192
HS 25,111
M 0,14,2
LL 18,90
LL 115,91
M 0,15,3
s_addc_u32 s43, s45, 0
LA 18
PK 111,16,109,56
M 0,16,4
s_mov_b32 m0, %[pm]
LC_ 115
PK 109,16,109,97
M 0,17,5
DF
PO 111
M 1,28,4
WL 4
LO 17,17,114
LU 114,17
M 1,29,5
RA 224,1,9216
HS 25,114
M 2,40,4
LL 16,92
LL 118,93
M 2,41,5
LA 16
PK 114,17,112,56
M 3,52,4
LC_ 118
PK 112,17,112,97
M 3,53,5
ST 108
M 4,64,4
WL 3
LO 18,18,115
LU 115,18
M 4,65,5
s_add_u32 s42, s44, s62
RA 228,1,10240
HS 25,115
M 5,76,4
LL 17,94
LL 119,95
M 5,77,5
s_addc_u32 s43, s45, 0
LA 17
PK 115,18,113,56
M 5,78,0
LC_ 119
PK 113,18,113,97
M 5,79,1
ST 112
M 4,66,0
WL 3
LO 16,16,118
LU 118,16
M 4,67,1
RA 232,1,11264
HS 25,118
M 3,54,0
LL 18,96
LL 122,97
M 3,55,1
LA 18
PK 118,16,116,56
v_cmp_gt_u32 vcc, v19, v15
M 2,42,0
LC_ 122
PK 116,16,116,97
M 2,43,1
RV 252,22,5392
PO 118
M 1,30,0
WL 4
LO 17,17,119
LU 119,17
M 1,31,1
s_cbranch_vccnz .Lk1s1
.Lk1r1:
RA 236,1,12288
HS 25,119
M 0,18,0
LL 16,98
LL 123,99
M 0,19,1
s_add_u32 s42, s44, s63
LA 16
PK 119,17,117,56
M 0,20,2
s_addc_u32 s43, s45, 0
LC_ 123
PK 117,17,117,97
M 0,21,3
W1 24,0
PO 119
M 1,32,2
WL 5
LO 18,18,122
LU 122,18
M 1,33,3
RA 216,1,13312
HS 25,122
M 2,44,2
LL 17,100
LL 126,101
M 2,45,3
LA 17
PK 122,18,120,56
M 3,56,2
LC_ 126
PK 120,18,120,97
M 3,57,3
ST 116
M 4,68,2
WL 4
LO 16,16,123
LU 123,16
M 4,69,3
s_add_u32 s42, s44, s64
RA 220,1,14336
HS 25,123
M 5,80,2
LL 18,102
LL 127,103
M 5,81,3
s_addc_u32 s43, s45, 0
LA 18
PK 123,16,121,56
M 0,22,4
LC_ 127
PK 121,16,121,97
M 0,23,5
ST 120
M 1,34,4
WL 3
LO 17,17,126
LU 126,17
M 1,35,5
RA 224,1,15360
HS 25,126
M 2,46,4
LL 16,56
LL 104,57
M 2,47,5
LA 16
PK 126,17,124,56
M 3,58,4
LC_ 104
PK 124,17,124,97
M 3,59,5
ds_read_b64 v[14:15], v5 offset:0
PO 126
M 4,70,4
WL 4
LO 18,18,127
LU 127,18
M 4,71,5
s_add_u32 s42, s44, s65
RA 228,1,16384
HS 25,127
M 5,82,4
LL 17,58
LL 105,59
M 5,83,5
s_addc_u32 s43, s45, 0
LA 17
PK 127,18,125,56
Z 6,12,0
LC_ 105
PK 125,18,125,97
M 6,13,1
PO 127
v_subrev_u32 v5, s39, v5
Z 7,24,0
WL 4
LO 104,16,104
LU 16,104
M 7,25,1
RA 232,1,17408
HZ 24,16
Z 8,36,0
WL 4
LL 18,60
LL 108,61
M 8,37,1
s_mul_i32 s53, s50, 25600
LA 18
v_add_f32 v14, v14, v15
Z 9,48,0
s_add_u32 s40, %[wlo], s53
LC_ 108
v_add_f32_dpp v14, v14, v14 quad_perm:[1,0,3,2] row_mask:0xf bank_mask:0xf
M 9,49,1
s_addc_u32 s41, %[whi], 0
Z 10,60,0
WL 3
LO 105,17,105
LU 17,105
M 10,61,1
s_mov_b32 m0, s38
RA 236,1,18432
HS 24,17
Z 11,72,0
LL 16,62
LL 109,63
M 11,73,1
LA 16
ST 124
M 11,74,2
LC_ 109
DW 0
M 11,75,3
v_add_f32_dpp v14, v14, v14 quad_perm:[2,3,0,1] row_mask:0xf bank_mask:0xf
HX 25
M 10,62,2
WL 3
LO 108,18,108
LU 18,108
W1 25,1024
M 10,63,3
s_add_u32 s37, s37, 32768
RA 216,1,19456
HS 24,18
M 9,50,2
s_cmp_eq_u32 s37, s75
LL 17,64
LL 112,65
M 9,51,3
s_cselect_b32 s37, s74, s37
LA 17
DW 1024
M 8,38,2
LC_ 112
v_add_f32_dpp v14, v14, v14 row_half_mirror row_mask:0xf bank_mask:0xf
M 8,39,3
v_cvt_f64_f32 v[14:15], v14
v_add_u32 v4, s39, v4
s_mov_b64 exec, 1
AT
s_mov_b64 exec, -1
M 7,26,2
WL 4
LO 109,16,109
LU 16,109
M 7,27,3
RA 220,1,20480
HS 24,16
M 6,14,2
LL 18,66
LL 113,67
M 6,15,3
LA 18
s_mov_b64 exec, s[54:55]
SP
s_mov_b64 exec, -1
M 6,16,4
LC_ 113
DW 2048
M 6,17,5
M 7,28,4
WL 3
LO 112,17,112
LU 17,112
M 7,29,5
s_mul_i32 s53, s51, 24
RA 224,1,21504
HS 24,17
M 8,40,4
s_bitcmp1_b32 s51, 0
LL 16,68
LL 116,69
M 8,41,5
s_addc_u32 s53, s53, 0
LA 16
M 9,52,4
s_lshl_b32 s53, s53, 11
LC_ 116
M 9,53,5
s_bitcmp1_b64 s[90:91], s51
M 10,64,4
WL 3
LO 113,18,113
LU 18,113
M 10,65,5
s_cselect_b64 s[44:45], s[84:85], s[82:83]
RA 228,1,22528
HS 24,18
M 11,76,4
LL 17,70
LL 117,71
M 11,77,5
s_add_u32 s44, s44, s53
LA 17
M 11,78,0
s_addc_u32 s45, s45, 0
LC_ 117
M 11,79,1
s_add_u32 s51, s51, %[d]
M 10,66,0
WL 3
LO 116,16,116
LU 16,116
M 10,67,1
s_cmp_eq_u32 s51, %[wrap]
RA 232,1,23552
HS 24,16
M 9,54,0
s_cselect_b32 s51, %[rst], s51
LL 18,72
LL 120,73
M 9,55,1
s_add_u32 s40, s40, 3072
LA 18
M 8,42,0
s_addc_u32 s41, s41, 0
LC_ 120
s_add_u32 m0, s38, 3072
M 8,43,1
DW 0
M 7,30,0
WL 3
LO 117,17,117
LU 17,117
M 7,31,1
RA 236,1,24576
HS 24,17
M 6,18,0
LL 16,74
LL 121,75
M 6,19,1
LA 16
)"
R"(DW 1024
M 6,20,2
LC_ 121
DW 2048
M 6,21,3
s_lshl_b32 s53, s52, 8
v_add_u32 v1, s37, v0
M 7,32,2
WL 3
LO 120,18,120
LU 18,120
M 7,33,3
s_add_u32 s48, %[alo], s53
RA 216,1,0
HS 24,18
M 8,44,2
s_addc_u32 s49, %[ahi], 0
LL 17,76
LL 124,77
M 8,45,3
s_add_u32 s52, s52, %[d]
LA 17
v_and_b32 v22, 0xfffffcff, v1
M 9,56,2
s_cmp_eq_u32 s52, %[wrap]
LC_ 124
M 9,57,3
s_cselect_b32 s52, %[rst], s52
DQ 3072
M 10,68,2
WL 3
LO 121,16,121
LU 16,121
M 10,69,3
s_add_u32 s50, s50, %[d]
RA 220,1,1024
HS 24,16
M 11,80,2
LL 18,78
LL 125,79
M 11,81,3
s_cmp_eq_u32 s50, %[wrap]
LA 18
RV 252,22,5120
M 6,22,4
s_cselect_b32 s50, %[rst], s50
LC_ 125
M 6,23,5
WV 31
v_add_u32 v19, 1, v19
M 7,34,4
WL 4
LO 124,17,124
LU 17,124
M 7,35,5
s_add_u32 s38, s38, 32768
RA 224,1,2048
HS 24,17
M 8,46,4
s_cmp_eq_u32 s38, s77
LL 16,80
LL 106,81
M 8,47,5
s_cselect_b32 s38, s76, s38
LA 16
FP
M 9,58,4
s_sub_u32 s39, 0, s39
LC_ 106
M 9,59,5
M 10,70,4
WL 4
LO 125,18,125
LU 18,125
M 10,71,5
RA 228,1,3072
HS 24,18
M 11,82,4
LL 17,82
LL 107,83
M 11,83,5
LA 17
HX 24
Z 0,12,0
LC_ 107
M 0,13,1
Z 1,24,0
WL 3
LO 16,16,106
LU 106,16
M 1,25,1
RA 232,1,4096
HZ 25,106
Z 2,36,0
LL 18,84
LL 110,85
M 2,37,1
LA 18
PK 106,16,104,56
Z 3,48,0
LC_ 110
PK 104,16,104,97
M 3,49,1
Z 4,60,0
WL 3
LO 17,17,107
LU 107,17
M 4,61,1
s_add_u32 s42, s44, s60
RA 236,1,6144
HS 25,107
Z 5,72,0
LL 16,86
LL 111,87
M 5,73,1
s_addc_u32 s43, s45, 0
LA 16
PK 107,17,105,56
M 5,74,2
LC_ 111
PK 105,17,105,97
M 5,75,3
ST 104
M 4,62,2
WL 3
LO 18,18,110
LU 110,18
M 4,63,3
RA 216,1,7168
HS 25,110
M 3,50,2
LL 17,88
LL 114,89
M 3,51,3
LA 17
PK 110,18,108,56
M 2,38,2
LC_ 114
PK 108,18,108,97
M 2,39,3
FR
PO 110
M 1,26,2
WL 4
LO 16,16,111
LU 111,16
M 1,27,3
s_add_u32 s42, s44, s61
RA 220,1,8192
HS 25,111
M 0,14,2
LL 18,90
LL 115,91
M 0,15,3
s_addc_u32 s43, s45, 0
LA 18
PK 111,16,109,56
M 0,16,4
s_mov_b32 m0, %[pm]
LC_ 115
PK 109,16,109,97
M 0,17,5
DF
PO 111
M 1,28,4
WL 4
LO 17,17,114
LU 114,17
M 1,29,5
RA 224,1,9216
HS 25,114
M 2,40,4
LL 16,92
LL 118,93
M 2,41,5
LA 16
PK 114,17,112,56
M 3,52,4
LC_ 118
PK 112,17,112,97
M 3,53,5
ST 108
M 4,64,4
WL 3
LO 18,18,115
LU 115,18
M 4,65,5
s_add_u32 s42, s44, s62
RA 228,1,10240
HS 25,115
M 5,76,4
LL 17,94
LL 119,95
M 5,77,5
s_addc_u32 s43, s45, 0
LA 17
PK 115,18,113,56
M 5,78,0
LC_ 119
PK 113,18,113,97
M 5,79,1
ST 112
M 4,66,0
WL 3
LO 16,16,118
LU 118,16
M 4,67,1
RA 232,1,11264
HS 25,118
M 3,54,0
LL 18,96
LL 122,97
M 3,55,1
LA 18
PK 118,16,116,56
v_cmp_gt_u32 vcc, v19, v15
M 2,42,0
LC_ 122
PK 116,16,116,97
M 2,43,1
RV 252,22,5392
PO 118
M 1,30,0
WL 4
LO 17,17,119
LU 119,17
M 1,31,1
s_cbranch_vccnz .Lk1s2
.Lk1r2:
RA 236,1,12288
HS 25,119
M 0,18,0
LL 16,98
LL 123,99
M 0,19,1
s_add_u32 s42, s44, s63
LA 16
PK 119,17,117,56
M 0,20,2
s_addc_u32 s43, s45, 0
LC_ 123
PK 117,17,117,97
M 0,21,3
W1 24,0
PO 119
M 1,32,2
WL 5
LO 18,18,122
LU 122,18
M 1,33,3
RA 216,1,13312
HS 25,122
M 2,44,2
LL 17,100
LL 126,101
M 2,45,3
LA 17
PK 122,18,120,56
M 3,56,2
LC_ 126
PK 120,18,120,97
M 3,57,3
ST 116
M 4,68,2
WL 4
LO 16,16,123
LU 123,16
M 4,69,3
s_add_u32 s42, s44, s64
RA 220,1,14336
HS 25,123
M 5,80,2
LL 18,102
LL 127,103
M 5,81,3
s_addc_u32 s43, s45, 0
LA 18
PK 123,16,121,56
M 0,22,4
LC_ 127
PK 121,16,121,97
M 0,23,5
ST 120
M 1,34,4
WL 3
LO 17,17,126
LU 126,17
M 1,35,5
RA 224,1,15360
HS 25,126
M 2,46,4
LL 16,56
LL 104,57
M 2,47,5
LA 16
PK 126,17,124,56
M 3,58,4
LC_ 104
PK 124,17,124,97
M 3,59,5
ds_read_b64 v[14:15], v5 offset:0
PO 126
M 4,70,4
WL 4
LO 18,18,127
LU 127,18
M 4,71,5
s_add_u32 s42, s44, s65
RA 228,1,16384
HS 25,127
M 5,82,4
LL 17,58
LL 105,59
M 5,83,5
s_addc_u32 s43, s45, 0
LA 17
PK 127,18,125,56
Z 6,12,0
LC_ 105
PK 125,18,125,97
M 6,13,1
PO 127
v_subrev_u32 v5, s39, v5
Z 7,24,0
WL 4
LO 104,16,104
LU 16,104
M 7,25,1
RA 232,1,17408
HZ 24,16
Z 8,36,0
WL 4
LL 18,60
LL 108,61
M 8,37,1
s_mul_i32 s53, s50, 25600
LA 18
v_add_f32 v14, v14, v15
Z 9,48,0
s_add_u32 s40, %[wlo], s53
LC_ 108
v_add_f32_dpp v14, v14, v14 quad_perm:[1,0,3,2] row_mask:0xf bank_mask:0xf
M 9,49,1
s_addc_u32 s41, %[whi], 0
Z 10,60,0
WL 3
LO 105,17,105
LU 17,105
M 10,61,1
s_mov_b32 m0, s38
RA 236,1,18432
HS 24,17
Z 11,72,0
LL 16,62
LL 109,63
M 11,73,1
LA 16
ST 124
M 11,74,2
LC_ 109
DW 0
M 11,75,3
WV 14
v_add_f32_dpp v14, v14, v14 quad_perm:[2,3,0,1] row_mask:0xf bank_mask:0xf
HX 25
M 10,62,2
WL 3
LO 108,18,108
LU 18,108
W1 25,1024
M 10,63,3
s_add_u32 s37, s37, 32768
RA 216,1,19456
HS 24,18
v_readfirstlane_b32 s72, v11
M 9,50,2
s_cmp_eq_u32 s37, s75
LL 17,64
LL 112,65
v_mov_b32 v12, s72
M 9,51,3
s_cselect_b32 s37, s74, s37
LA 17
W2 12,12,16
M 8,38,2
LC_ 112
DW 1024
M 8,39,3
v_add_f32_dpp v14, v14, v14 row_half_mirror row_mask:0xf bank_mask:0xf
v_add_u32 v4, s39, v4
M 7,26,2
WL 5
LO 109,16,109
LU 16,109
M 7,27,3
RA 220,1,20480
HS 24,16
M 6,14,2
LL 18,66
LL 113,67
)"
R"(M 6,15,3
LA 18
DW 2048
M 6,16,4
LC_ 113
v_cvt_f64_f32 v[14:15], v14
M 6,17,5
s_mov_b64 exec, s[54:55]
SP
s_mov_b64 exec, -1
M 7,28,4
WL 3
LO 112,17,112
LU 17,112
M 7,29,5
s_mul_i32 s53, s51, 24
RA 224,1,21504
HS 24,17
M 8,40,4
s_bitcmp1_b32 s51, 0
LL 16,68
LL 116,69
M 8,41,5
s_addc_u32 s53, s53, 0
LA 16
M 9,52,4
s_lshl_b32 s53, s53, 11
LC_ 116
M 9,53,5
s_bitcmp1_b64 s[90:91], s51
M 10,64,4
WL 3
LO 113,18,113
LU 18,113
M 10,65,5
s_cselect_b64 s[44:45], s[84:85], s[82:83]
RA 228,1,22528
HS 24,18
M 11,76,4
LL 17,70
LL 117,71
M 11,77,5
s_add_u32 s44, s44, s53
LA 17
M 11,78,0
s_addc_u32 s45, s45, 0
LC_ 117
M 11,79,1
s_add_u32 s51, s51, %[d]
M 10,66,0
WL 3
LO 116,16,116
LU 16,116
M 10,67,1
s_cmp_eq_u32 s51, %[wrap]
RA 232,1,23552
HS 24,16
M 9,54,0
s_cselect_b32 s51, %[rst], s51
LL 18,72
LL 120,73
M 9,55,1
s_add_u32 s40, s40, 3072
LA 18
M 8,42,0
s_addc_u32 s41, s41, 0
LC_ 120
s_add_u32 m0, s38, 3072
M 8,43,1
DW 0
M 7,30,0
WL 3
LO 117,17,117
LU 17,117
M 7,31,1
RA 236,1,24576
HS 24,17
M 6,18,0
LL 16,74
LL 121,75
M 6,19,1
LA 16
DW 1024
M 6,20,2
LC_ 121
DW 2048
M 6,21,3
s_lshl_b32 s53, s52, 8
v_add_u32 v1, s37, v0
M 7,32,2
WL 3
LO 120,18,120
LU 18,120
M 7,33,3
s_add_u32 s48, %[alo], s53
RA 216,1,0
HS 24,18
M 8,44,2
s_addc_u32 s49, %[ahi], 0
LL 17,76
LL 124,77
M 8,45,3
s_add_u32 s52, s52, %[d]
LA 17
v_and_b32 v22, 0xfffffcff, v1
M 9,56,2
s_cmp_eq_u32 s52, %[wrap]
LC_ 124
M 9,57,3
s_cselect_b32 s52, %[rst], s52
DQ 3072
M 10,68,2
WL 3
LO 121,16,121
LU 16,121
M 10,69,3
s_add_u32 s50, s50, %[d]
RA 220,1,1024
HS 24,16
M 11,80,2
LL 18,78
LL 125,79
M 11,81,3
s_cmp_eq_u32 s50, %[wrap]
LA 18
RV 252,22,5120
M 6,22,4
s_cselect_b32 s50, %[rst], s50
LC_ 125
M 6,23,5
v_add_u32 v19, 1, v19
M 7,34,4
WL 4
LO 124,17,124
LU 17,124
M 7,35,5
s_add_u32 s38, s38, 32768
RA 224,1,2048
HS 24,17
M 8,46,4
s_cmp_eq_u32 s38, s77
LL 16,80
LL 106,81
M 8,47,5
s_cselect_b32 s38, s76, s38
LA 16
FP
M 9,58,4
s_sub_u32 s39, 0, s39
LC_ 106
M 9,59,5
M 10,70,4
WL 4
LO 125,18,125
LU 18,125
M 10,71,5
RA 228,1,3072
HS 24,18
M 11,82,4
LL 17,82
LL 107,83
M 11,83,5
LA 17
HX 24
Z 0,12,0
LC_ 107
M 0,13,1
Z 1,24,0
WL 3
LO 16,16,106
LU 106,16
M 1,25,1
RA 232,1,4096
HZ 25,106
Z 2,36,0
LL 18,84
LL 110,85
M 2,37,1
LA 18
PK 106,16,104,56
Z 3,48,0
LC_ 110
PK 104,16,104,97
M 3,49,1
Z 4,60,0
WL 3
LO 17,17,107
LU 107,17
M 4,61,1
s_add_u32 s42, s44, s60
RA 236,1,6144
HS 25,107
Z 5,72,0
LL 16,86
LL 111,87
M 5,73,1
s_addc_u32 s43, s45, 0
LA 16
PK 107,17,105,56
M 5,74,2
LC_ 111
PK 105,17,105,97
M 5,75,3
ST 104
M 4,62,2
WL 3
LO 18,18,110
LU 110,18
M 4,63,3
RA 216,1,7168
HS 25,110
M 3,50,2
LL 17,88
LL 114,89
M 3,51,3
LA 17
PK 110,18,108,56
M 2,38,2
LC_ 114
PK 108,18,108,97
M 2,39,3
FR
PO 110
M 1,26,2
WL 4
LO 16,16,111
LU 111,16
M 1,27,3
s_add_u32 s42, s44, s61
RA 220,1,8192
HS 25,111
M 0,14,2
LL 18,90
LL 115,91
M 0,15,3
s_addc_u32 s43, s45, 0
LA 18
PK 111,16,109,56
M 0,16,4
s_mov_b32 m0, %[pm]
LC_ 115
PK 109,16,109,97
M 0,17,5
DF
PO 111
M 1,28,4
WL 4
LO 17,17,114
LU 114,17
M 1,29,5
RA 224,1,9216
HS 25,114
M 2,40,4
LL 16,92
LL 118,93
M 2,41,5
LA 16
PK 114,17,112,56
M 3,52,4
LC_ 118
PK 112,17,112,97
M 3,53,5
ST 108
M 4,64,4
WL 3
LO 18,18,115
LU 115,18
M 4,65,5
s_add_u32 s42, s44, s62
RA 228,1,10240
HS 25,115
M 5,76,4
LL 17,94
LL 119,95
M 5,77,5
s_addc_u32 s43, s45, 0
LA 17
PK 115,18,113,56
M 5,78,0
LC_ 119
PK 113,18,113,97
M 5,79,1
ST 112
M 4,66,0
WL 3
LO 16,16,118
LU 118,16
M 4,67,1
RA 232,1,11264
HS 25,118
M 3,54,0
LL 18,96
LL 122,97
M 3,55,1
LA 18
PK 118,16,116,56
v_cmp_gt_u32 vcc, v19, v15
M 2,42,0
LC_ 122
PK 116,16,116,97
M 2,43,1
RV 252,22,5392
PO 118
M 1,30,0
WL 4
LO 17,17,119
LU 119,17
M 1,31,1
s_cbranch_vccnz .Lk1s3
.Lk1r3:
RA 236,1,12288
HS 25,119
M 0,18,0
LL 16,98
LL 123,99
M 0,19,1
s_add_u32 s42, s44, s63
LA 16
PK 119,17,117,56
M 0,20,2
s_addc_u32 s43, s45, 0
LC_ 123
PK 117,17,117,97
M 0,21,3
ds_read_b32 v20, v5 offset:4096
PO 119
M 1,32,2
WL 5
LO 18,18,122
LU 122,18
M 1,33,3
RA 216,1,13312
HS 25,122
M 2,44,2
LL 17,100
LL 126,101
M 2,45,3
LA 17
PK 122,18,120,56
M 3,56,2
LC_ 126
PK 120,18,120,97
M 3,57,3
M 4,68,2
WL 4
LO 16,16,123
LU 123,16
M 4,69,3
RA 220,1,14336
HS 25,123
M 5,80,2
LL 18,102
LL 127,103
M 5,81,3
LA 18
PK 123,16,121,56
M 0,22,4
LC_ 127
PK 121,16,121,97
W1 24,0
M 0,23,5
M 1,34,4
WL 4
LO 17,17,126
LU 126,17
M 1,35,5
RA 224,1,15360
HS 25,126
M 2,46,4
LL 16,56
LL 104,57
M 2,47,5
LA 16
PK 126,17,124,56
M 3,58,4
LC_ 104
PK 124,17,124,97
M 3,59,5
PO 126
v_readfirstlane_b32 s72, v20
M 4,70,4
WL 4
LO 18,18,127
LU 127,18
M 4,71,5
s_sub_u32 s53, 63, s72
RA 228,1,16384
HS 25,127
M 5,82,4
LL 17,58
LL 105,59
M 5,83,5
s_cmp_eq_u32 %[d], 1
LA 17
PK 127,18,125,56
Z 6,12,0
s_cselect_b32 s72, s72, s53
LC_ 105
PK 125,18,125,97
M 6,13,1
)"
R"(s_and_b32 s72, s72, 63
ds_read_b64 v[14:15], v5 offset:0
PO 127
Z 7,24,0
WL 4
LO 104,16,104
LU 16,104
M 7,25,1
s_add_u32 s53, s72, 128
RA 232,1,17408
HZ 24,16
Z 8,36,0
s_mul_i32 s53, s53, 2359296
LL 18,60
LL 108,61
M 8,37,1
s_add_u32 s86, %[hlo], s53
LA 18
v_subrev_u32 v5, s39, v5
Z 9,48,0
s_addc_u32 s87, %[hhi], 0
LC_ 108
HX 25
M 9,49,1
s_add_u32 s88, s86, 8192
Z 10,60,0
WL 4
LO 105,17,105
LU 17,105
M 10,61,1
s_addc_u32 s89, s87, 0
RA 236,1,18432
HS 24,17
Z 11,72,0
WL 4
LL 16,62
LL 109,63
M 11,73,1
s_and_b32 s92, %[hlo], 0x1fffff
LA 16
W1 25,1024
s_add_u32 s92, s92, s53
s_mov_b32 s93, 3072
ST 116
s_add_u32 s42, s44, s64
s_addc_u32 s43, s45, 0
s_mul_i32 s57, s72, 589824
s_add_u32 s57, s57, %[xs1]
s_lshl_b32 s72, s72, 1
s_add_u32 s72, s72, s96
s_lshl_b32 s53, s72, 6
s_add_u32 s53, s53, 128
s_cmp_lt_u32 %[ca0], 128
s_cselect_b32 s73, s53, %[ca0]
ST 120
v_mov_b32 v9, s73
v_mov_b32 v11, s92
M 11,74,2
s_add_u32 s42, s44, s65
LC_ 109
v_add_f32 v14, v14, v15
s_addc_u32 s43, s45, 0
v_mad_u32_u24 v11, v0, s93, v11
M 11,75,3
v_add_f32_dpp v14, v14, v14 quad_perm:[1,0,3,2] row_mask:0xf bank_mask:0xf
v_add_u32 v4, s39, v4
M 10,62,2
WL 4
LO 108,18,108
LU 18,108
M 10,63,3
RA 216,1,19456
HS 24,18
v_lshrrev_b32 v11, 21, v11
M 9,50,2
LL 17,64
LL 112,65
M 9,51,3
LA 17
v_add_f32_dpp v14, v14, v14 quad_perm:[2,3,0,1] row_mask:0xf bank_mask:0xf
M 8,38,2
LC_ 112
v_add_f32_dpp v14, v14, v14 row_half_mirror row_mask:0xf bank_mask:0xf
v_lshl_add_u32 v11, v11, 2, 64
M 8,39,3
s_mul_i32 s53, s50, 25600
v_cvt_f64_f32 v[14:15], v14
M 7,26,2
WL 3
LO 109,16,109
LU 16,109
s_add_u32 s40, %[wlo], s53
s_addc_u32 s41, %[whi], 0
s_mov_b32 m0, s38
M 7,27,3
RA 220,1,20480
HS 24,16
v_writelane_b32 v11, %[tsel], 63
M 6,14,2
LL 18,66
LL 113,67
ST 124
M 6,15,3
s_sub_u32 s53, s86, %[hlo]
LA 18
s_mov_b64 exec, s[54:55]
SP
s_mov_b64 exec, -1
M 6,16,4
s_lshr_b32 s53, s53, 2
LC_ 113
s_and_b32 s92, %[xlo], 0x1fffff
s_add_u32 s53, s53, s92
s_add_u32 s53, s53, 0x20048000
s_lshr_b32 s53, s53, 21
s_lshl_b32 s92, s53, 2
M 6,17,5
DW 0
v_writelane_b32 v11, s92, 48
M 7,28,4
WL 3
LO 112,17,112
LU 17,112
M 7,29,5
s_add_u32 s37, s37, 32768
RA 224,1,21504
HS 24,17
M 8,40,4
s_cmp_eq_u32 s37, s75
LL 16,68
LL 116,69
M 8,41,5
s_cselect_b32 s37, s74, s37
LA 16
global_load_dword v11, v11, s[68:69] offset:-2048
M 9,52,4
LC_ 116
DW 1024
M 9,53,5
DW 2048
M 10,64,4
WL 3
LO 113,18,113
LU 18,113
M 10,65,5
s_mul_i32 s53, s51, 24
RA 228,1,22528
HS 24,18
M 11,76,4
LL 17,70
LL 117,71
M 11,77,5
s_bitcmp1_b32 s51, 0
LA 17
M 11,78,0
s_addc_u32 s53, s53, 0
LC_ 117
M 11,79,1
s_lshl_b32 s53, s53, 11
M 10,66,0
WL 3
LO 116,16,116
LU 16,116
M 10,67,1
s_bitcmp1_b64 s[90:91], s51
RA 232,1,23552
HS 24,16
M 9,54,0
s_cselect_b64 s[44:45], s[84:85], s[82:83]
LL 18,72
LL 120,73
M 9,55,1
s_add_u32 s44, s44, s53
LA 18
s_addc_u32 s45, s45, 0
s_add_u32 s51, s51, %[d]
s_cmp_eq_u32 s51, %[wrap]
s_cselect_b32 s51, %[rst], s51
M 8,42,0
s_add_u32 s40, s40, 3072
LC_ 120
s_addc_u32 s41, s41, 0
s_add_u32 m0, s38, 3072
M 8,43,1
DW 0
M 7,30,0
WL 3
LO 117,17,117
LU 17,117
M 7,31,1
RA 236,1,24576
HS 24,17
M 6,18,0
LL 16,74
LL 121,75
M 6,19,1
LA 16
DW 1024
M 6,20,2
LC_ 121
DW 2048
M 6,21,3
s_lshl_b32 s53, s52, 8
v_add_u32 v1, s37, v0
M 7,32,2
WL 3
LO 120,18,120
LU 18,120
M 7,33,3
s_add_u32 s48, %[alo], s53
RA 216,1,0
HS 24,18
M 8,44,2
s_addc_u32 s49, %[ahi], 0
LL 17,76
LL 124,77
M 8,45,3
s_add_u32 s52, s52, %[d]
LA 17
v_and_b32 v22, 0xfffffcff, v1
M 9,56,2
s_cmp_eq_u32 s52, %[wrap]
LC_ 124
M 9,57,3
s_cselect_b32 s52, %[rst], s52
DQ 3072
M 10,68,2
WL 3
LO 121,16,121
LU 16,121
M 10,69,3
s_add_u32 s50, s50, %[d]
RA 220,1,1024
HS 24,16
M 11,80,2
LL 18,78
LL 125,79
M 11,81,3
s_cmp_eq_u32 s50, %[wrap]
LA 18
RV 252,22,5120
M 6,22,4
s_cselect_b32 s50, %[rst], s50
LC_ 125
M 6,23,5
WV 31
v_add_u32 v19, 1, v19
M 7,34,4
WL 4
LO 124,17,124
LU 17,124
M 7,35,5
s_add_u32 s38, s38, 32768
RA 224,1,2048
HS 24,17
M 8,46,4
s_cmp_eq_u32 s38, s77
LL 16,80
LL 106,81
M 8,47,5
s_cselect_b32 s38, s76, s38
LA 16
FP
M 9,58,4
s_sub_u32 s39, 0, s39
LC_ 106
M 9,59,5
M 10,70,4
WL 4
LO 125,18,125
LU 18,125
M 10,71,5
RA 228,1,3072
HS 24,18
M 11,82,4
LL 17,82
LL 107,83
M 11,83,5
LA 17
HX 24
Z 0,12,0
LC_ 107
M 0,13,1
Z 1,24,0
WL 3
LO 16,16,106
LU 106,16
M 1,25,1
RA 232,1,4096
HZ 25,106
Z 2,36,0
LL 18,84
LL 110,85
M 2,37,1
LA 18
PK 106,16,104,56
Z 3,48,0
LC_ 110
PK 104,16,104,97
M 3,49,1
Z 4,60,0
WL 3
LO 17,17,107
LU 107,17
M 4,61,1
s_add_u32 s42, s44, s60
RA 236,1,6144
HS 25,107
Z 5,72,0
LL 16,86
LL 111,87
M 5,73,1
s_addc_u32 s43, s45, 0
LA 16
PK 107,17,105,56
M 5,74,2
LC_ 111
PK 105,17,105,97
M 5,75,3
ST 104
M 4,62,2
WL 3
LO 18,18,110
LU 110,18
M 4,63,3
RA 216,1,7168
HS 25,110
M 3,50,2
LL 17,88
LL 114,89
M 3,51,3
LA 17
PK 110,18,108,56
M 2,38,2
LC_ 114
PK 108,18,108,97
M 2,39,3
FR
PO 110
M 1,26,2
WL 4
LO 16,16,111
LU 111,16
M 1,27,3
s_add_u32 s42, s44, s61
RA 220,1,8192
HS 25,111
M 0,14,2
LL 18,90
LL 115,91
M 0,15,3
s_addc_u32 s43, s45, 0
LA 18
PK 111,16,109,56
M 0,16,4
s_mov_b32 m0, %[pm]
LC_ 115
PK 109,16,109,97
M 0,17,5
DF
PO 111
M 1,28,4
WL 4
LO 17,17,114
LU 114,17
M 1,29,5
RA 224,1,9216
HS 25,114
M 2,40,4
LL 16,92
LL 118,93
M 2,41,5
LA 16
PK 114,17,112,56
M 3,52,4
LC_ 118
PK 112,17,112,97
M 3,53,5
ST 108
M 4,64,4
WL 3
LO 18,18,115
LU 115,18
M 4,65,5
s_add_u32 s42, s44, s62
RA 228,1,10240
HS 25,115
M 5,76,4
LL 17,94
LL 119,95
M 5,77,5
s_addc_u32 s43, s45, 0
LA 17
PK 115,18,113,56
M 5,78,0
LC_ 119
PK 113,18,113,97
M 5,79,1
ST 112
M 4,66,0
)"
R"(WL 3
LO 16,16,118
LU 118,16
M 4,67,1
RA 232,1,11264
HS 25,118
M 3,54,0
LL 18,96
LL 122,97
M 3,55,1
LA 18
PK 118,16,116,56
v_cmp_gt_u32 vcc, v19, v15
M 2,42,0
LC_ 122
PK 116,16,116,97
M 2,43,1
RV 252,22,5392
PO 118
M 1,30,0
WL 4
LO 17,17,119
LU 119,17
M 1,31,1
s_cbranch_vccnz .Lk1s4
.Lk1r4:
RA 236,1,12288
HS 25,119
M 0,18,0
LL 16,98
LL 123,99
M 0,19,1
s_add_u32 s42, s44, s63
LA 16
PK 119,17,117,56
M 0,20,2
s_addc_u32 s43, s45, 0
LC_ 123
PK 117,17,117,97
M 0,21,3
W1 24,0
PO 119
M 1,32,2
WL 5
LO 18,18,122
LU 122,18
M 1,33,3
RA 216,1,13312
HS 25,122
M 2,44,2
LL 17,100
LL 126,101
M 2,45,3
LA 17
PK 122,18,120,56
M 3,56,2
LC_ 126
PK 120,18,120,97
M 3,57,3
ST 116
M 4,68,2
WL 4
LO 16,16,123
LU 123,16
M 4,69,3
s_add_u32 s42, s44, s64
RA 220,1,14336
HS 25,123
M 5,80,2
LL 18,102
LL 127,103
M 5,81,3
s_addc_u32 s43, s45, 0
LA 18
PK 123,16,121,56
M 0,22,4
LC_ 127
PK 121,16,121,97
M 0,23,5
ST 120
M 1,34,4
WL 3
LO 17,17,126
LU 126,17
M 1,35,5
RA 224,1,15360
HS 25,126
M 2,46,4
LL 16,56
LL 104,57
M 2,47,5
LA 16
PK 126,17,124,56
M 3,58,4
LC_ 104
PK 124,17,124,97
M 3,59,5
ds_read_b64 v[14:15], v5 offset:0
PO 126
M 4,70,4
WL 4
LO 18,18,127
LU 127,18
M 4,71,5
s_add_u32 s42, s44, s65
RA 228,1,16384
HS 25,127
M 5,82,4
LL 17,58
LL 105,59
M 5,83,5
s_addc_u32 s43, s45, 0
LA 17
PK 127,18,125,56
Z 6,12,0
LC_ 105
PK 125,18,125,97
M 6,13,1
PO 127
v_subrev_u32 v5, s39, v5
Z 7,24,0
WL 4
LO 104,16,104
LU 16,104
M 7,25,1
RA 232,1,17408
HZ 24,16
Z 8,36,0
WL 4
LL 18,60
LL 108,61
M 8,37,1
s_mul_i32 s53, s50, 25600
LA 18
v_add_f32 v14, v14, v15
Z 9,48,0
s_add_u32 s40, %[wlo], s53
LC_ 108
v_add_f32_dpp v14, v14, v14 quad_perm:[1,0,3,2] row_mask:0xf bank_mask:0xf
M 9,49,1
s_addc_u32 s41, %[whi], 0
Z 10,60,0
WL 3
LO 105,17,105
LU 17,105
M 10,61,1
s_mov_b32 m0, s38
RA 236,1,18432
HS 24,17
Z 11,72,0
LL 16,62
LL 109,63
M 11,73,1
LA 16
ST 124
M 11,74,2
LC_ 109
DW 0
M 11,75,3
v_add_f32_dpp v14, v14, v14 quad_perm:[2,3,0,1] row_mask:0xf bank_mask:0xf
HX 25
M 10,62,2
WL 3
LO 108,18,108
LU 18,108
W1 25,1024
M 10,63,3
s_add_u32 s37, s37, 32768
RA 216,1,19456
HS 24,18
M 9,50,2
s_cmp_eq_u32 s37, s75
LL 17,64
LL 112,65
M 9,51,3
s_cselect_b32 s37, s74, s37
LA 17
DW 1024
M 8,38,2
LC_ 112
v_add_f32_dpp v14, v14, v14 row_half_mirror row_mask:0xf bank_mask:0xf
M 8,39,3
v_cvt_f64_f32 v[14:15], v14
v_add_u32 v4, s39, v4
M 7,26,2
WL 4
LO 109,16,109
LU 16,109
M 7,27,3
RA 220,1,20480
HS 24,16
M 6,14,2
LL 18,66
LL 113,67
M 6,15,3
LA 18
s_mov_b64 exec, s[54:55]
SP
s_mov_b64 exec, -1
M 6,16,4
LC_ 113
DW 2048
M 6,17,5
s_and_b32 s53, %[w], 1
M 7,28,4
WL 3
LO 112,17,112
LU 17,112
M 7,29,5
s_lshl_b32 s53, s53, 10
RA 224,1,21504
HS 24,17
M 8,40,4
s_lshr_b32 s72, %[w], 1
LL 16,68
LL 116,69
M 8,41,5
s_mul_i32 s72, s72, 32768
LA 16
M 9,52,4
s_add_u32 s72, s72, s53
LC_ 116
M 9,53,5
s_add_u32 s72, s72, 128000
M 10,64,4
WL 3
LO 113,18,113
LU 18,113
M 10,65,5
s_movk_i32 s53, 0x78
RA 228,1,22528
HS 24,18
M 11,76,4
LL 17,70
LL 117,71
M 11,77,5
LA 17
v_lshrrev_b32 v12, 5, v0
M 11,78,0
LC_ 117
v_and_b32 v12, 6, v12
M 11,79,1
v_bfe_u32 v12, s53, v12, 2
M 10,66,0
WL 3
LO 116,16,116
LU 16,116
M 10,67,1
RA 232,1,23552
HS 24,16
v_lshrrev_b32 v20, 8, v0
v_xor_b32 v20, v12, v20
v_lshl_add_u32 v20, v20, 4, s72
v_and_b32 v12, 0xf0, v0
v_lshl_add_u32 v20, v12, 2, v20
v_add_u32 v12, s72, v0
M 9,54,0
s_mul_i32 s53, s51, 24
LL 18,72
LL 120,73
M 9,55,1
s_bitcmp1_b32 s51, 0
LA 18
s_addc_u32 s53, s53, 0
s_lshl_b32 s53, s53, 11
s_bitcmp1_b64 s[90:91], s51
s_cselect_b64 s[44:45], s[84:85], s[82:83]
s_add_u32 s44, s44, s53
s_addc_u32 s45, s45, 0
s_add_u32 s51, s51, %[d]
s_cmp_eq_u32 s51, %[wrap]
s_cselect_b32 s51, %[rst], s51
M 8,42,0
s_add_u32 s40, s40, 3072
LC_ 120
s_addc_u32 s41, s41, 0
s_add_u32 m0, s38, 3072
M 8,43,1
DW 0
M 7,30,0
WL 3
LO 117,17,117
LU 17,117
M 7,31,1
RA 236,1,24576
HS 24,17
M 6,18,0
LL 16,74
LL 121,75
M 6,19,1
LA 16
DW 1024
M 6,20,2
LC_ 121
DW 2048
M 6,21,3
s_lshl_b32 s53, s52, 8
v_add_u32 v1, s37, v0
M 7,32,2
WL 3
LO 120,18,120
LU 18,120
M 7,33,3
s_add_u32 s48, %[alo], s53
RA 216,1,0
HS 24,18
M 8,44,2
s_addc_u32 s49, %[ahi], 0
LL 17,76
LL 124,77
M 8,45,3
s_add_u32 s52, s52, %[d]
LA 17
v_and_b32 v22, 0xfffffcff, v1
M 9,56,2
s_cmp_eq_u32 s52, %[wrap]
LC_ 124
M 9,57,3
s_cselect_b32 s52, %[rst], s52
DQ 3072
M 10,68,2
WL 3
LO 121,16,121
LU 16,121
M 10,69,3
s_add_u32 s50, s50, %[d]
RA 220,1,1024
HS 24,16
M 11,80,2
LL 18,78
LL 125,79
M 11,81,3
s_cmp_eq_u32 s50, %[wrap]
LA 18
RV 252,22,5120
M 6,22,4
s_cselect_b32 s50, %[rst], s50
LC_ 125
M 6,23,5
WV 31
v_add_u32 v19, 1, v19
M 7,34,4
WL 4
LO 124,17,124
LU 17,124
M 7,35,5
s_add_u32 s38, s38, 32768
RA 224,1,2048
HS 24,17
M 8,46,4
s_cmp_eq_u32 s38, s77
LL 16,80
LL 106,81
M 8,47,5
s_cselect_b32 s38, s76, s38
LA 16
FP
M 9,58,4
s_sub_u32 s39, 0, s39
LC_ 106
M 9,59,5
M 10,70,4
WL 4
LO 125,18,125
LU 18,125
M 10,71,5
RA 228,1,3072
HS 24,18
M 11,82,4
LL 17,82
LL 107,83
M 11,83,5
LA 17
HX 24
Z 0,12,0
LC_ 107
M 0,13,1
Z 1,24,0
WL 3
LO 16,16,106
LU 106,16
M 1,25,1
RA 232,1,4096
HZ 25,106
Z 2,36,0
LL 18,84
LL 110,85
M 2,37,1
LA 18
PK 106,16,104,56
Z 3,48,0
LC_ 110
PK 104,16,104,97
M 3,49,1
WV 21
v_bcnt_u32_b32 v11, v11, 0
Z 4,60,0
WL 3
LO 17,17,107
LU 107,17
)"
R"(M 4,61,1
RA 236,1,6144
HS 25,107
Z 5,72,0
LL 16,86
LL 111,87
M 5,73,1
LA 16
PK 107,17,105,56
M 5,74,2
LC_ 111
PK 105,17,105,97
M 5,75,3
M 4,62,2
WL 3
LO 18,18,110
LU 110,18
M 4,63,3
RA 216,1,7168
HS 25,110
M 3,50,2
LL 17,88
LL 114,89
M 3,51,3
LA 17
PK 110,18,108,56
M 2,38,2
LC_ 114
PK 108,18,108,97
M 2,39,3
FR
PO 110
v_cmp_lt_u32 s[92:93], 2, v11
M 1,26,2
WL 4
LO 16,16,111
LU 111,16
M 1,27,3
s_nop 0
RA 220,1,8192
HS 25,111
M 0,14,2
LL 18,90
LL 115,91
M 0,15,3
s_add_u32 s42, s44, s60
LA 18
PK 111,16,109,56
s_addc_u32 s43, s45, 0
s_sub_u32 s53, 0, s96
s_xor_b32 s92, s92, s53
s_xor_b32 s93, s93, s53
M 0,16,4
s_mul_i32 s53, %[d], 0
LC_ 115
PK 109,16,109,97
M 0,17,5
s_add_u32 s53, s53, s57
PO 111
M 1,28,4
WL 4
LO 17,17,114
LU 114,17
M 1,29,5
s_add_u32 s46, %[xlo], s53
RA 224,1,9216
HS 25,114
s_addc_u32 s47, %[xhi], 0
s_bitcmp1_b64 s[92:93], 48
s_cselect_b32 s58, 16, 0
s_lshr_b32 s58, 0xfe987610, s58
v_lshrrev_b32 v3, 6, v0
v_and_b32 v3, 12, v3
v_bfe_u32 v3, s58, v3, 4
v_mad_u32_u24 v3, v3, v32, v21
M 2,40,4
LL 16,92
LL 118,93
ST 104
M 2,41,5
s_add_u32 s42, s44, s61
LA 16
PK 114,17,112,56
M 3,52,4
s_addc_u32 s43, s45, 0
LC_ 118
PK 112,17,112,97
M 3,53,5
s_mov_b32 m0, %[pm]
M 4,64,4
WL 3
LO 18,18,115
LU 115,18
M 4,65,5
RA 228,1,10240
HS 25,115
M 5,76,4
LL 17,94
LL 119,95
M 5,77,5
LA 17
PK 115,18,113,56
M 5,78,0
LC_ 119
PK 113,18,113,97
M 5,79,1
DF
M 4,66,0
WL 3
LO 16,16,118
LU 118,16
M 4,67,1
RA 232,1,11264
HS 25,118
M 3,54,0
LL 18,96
LL 122,97
M 3,55,1
LA 18
PK 118,16,116,56
v_cmp_gt_u32 vcc, v19, v15
M 2,42,0
LC_ 122
PK 116,16,116,97
M 2,43,1
RV 252,22,5392
PO 118
M 1,30,0
WL 4
LO 17,17,119
LU 119,17
M 1,31,1
s_cbranch_vccnz .Lk1s5
.Lk1r5:
RA 236,1,12288
HS 25,119
M 0,18,0
LL 16,98
LL 123,99
ST 108
XL 0,0
M 0,19,1
s_add_u32 s42, s44, s62
LA 16
PK 119,17,117,56
s_addc_u32 s43, s45, 0
M 0,20,2
LC_ 123
PK 117,17,117,97
M 0,21,3
W1 24,0
PO 119
M 1,32,2
WL 5
LO 18,18,122
LU 122,18
M 1,33,3
RA 216,1,13312
HS 25,122
M 2,44,2
LL 17,100
LL 126,101
ST 112
M 2,45,3
s_add_u32 s42, s44, s63
LA 17
PK 122,18,120,56
M 3,56,2
s_addc_u32 s43, s45, 0
LC_ 126
PK 120,18,120,97
M 3,57,3
M 4,68,2
WL 4
LO 16,16,123
LU 123,16
M 4,69,3
RA 220,1,14336
HS 25,123
M 5,80,2
LL 18,102
LL 127,103
M 5,81,3
LA 18
PK 123,16,121,56
M 0,22,4
LC_ 127
PK 121,16,121,97
M 0,23,5
M 1,34,4
WL 3
LO 17,17,126
LU 126,17
M 1,35,5
RA 224,1,15360
HS 25,126
M 2,46,4
LL 16,56
LL 104,57
M 2,47,5
LA 16
XL 1,64
PK 126,17,124,56
M 3,58,4
LC_ 104
PK 124,17,124,97
M 3,59,5
ST 116
PO 126
M 4,70,4
WL 3
LO 18,18,127
LU 127,18
M 4,71,5
s_add_u32 s42, s44, s64
RA 228,1,16384
HS 25,127
M 5,82,4
LL 17,58
LL 105,59
M 5,83,5
s_addc_u32 s43, s45, 0
LA 17
PK 127,18,125,56
Z 6,12,0
LC_ 105
PK 125,18,125,97
M 6,13,1
ST 120
PO 127
Z 7,24,0
WL 3
LO 104,16,104
LU 16,104
M 7,25,1
s_add_u32 s42, s44, s65
RA 232,1,17408
HZ 24,16
Z 8,36,0
s_addc_u32 s43, s45, 0
LL 18,60
LL 108,61
M 8,37,1
LA 18
ds_read_b64 v[14:15], v5 offset:0
Z 9,48,0
LC_ 108
XL 2,128
M 9,49,1
s_mul_i32 s53, s50, 25600
Z 10,60,0
WL 4
LO 105,17,105
LU 17,105
M 10,61,1
s_add_u32 s40, %[wlo], s53
RA 236,1,18432
HS 24,17
Z 11,72,0
LL 16,62
LL 109,63
M 11,73,1
s_addc_u32 s41, %[whi], 0
LA 16
v_subrev_u32 v5, s39, v5
M 11,74,2
s_mov_b32 m0, s38
LC_ 109
ST 124
M 11,75,3
DW 0
HX 25
M 10,62,2
WL 3
LO 108,18,108
LU 18,108
W1 25,1024
M 10,63,3
s_add_u32 s37, s37, 32768
RA 216,1,19456
HS 24,18
M 9,50,2
s_cmp_eq_u32 s37, s75
LL 17,64
LL 112,65
M 9,51,3
s_cselect_b32 s37, s74, s37
LA 17
XL 3,192
M 8,38,2
LC_ 112
DW 1024
M 8,39,3
v_add_f32 v14, v14, v15
s_nop 1
v_add_f32_dpp v14, v14, v14 quad_perm:[1,0,3,2] row_mask:0xf bank_mask:0xf
v_add_u32 v4, s39, v4
M 7,26,2
WL 4
LO 109,16,109
LU 16,109
M 7,27,3
RA 220,1,20480
HS 24,16
v_add_f32_dpp v14, v14, v14 quad_perm:[2,3,0,1] row_mask:0xf bank_mask:0xf
M 6,14,2
LL 18,66
LL 113,67
M 6,15,3
LA 18
DW 2048
M 6,16,4
LC_ 113
v_add_f32_dpp v14, v14, v14 row_half_mirror row_mask:0xf bank_mask:0xf
M 6,17,5
v_cvt_f64_f32 v[14:15], v14
M 7,28,4
WL 3
LO 112,17,112
LU 17,112
M 7,29,5
s_mul_i32 s53, s51, 24
RA 224,1,21504
HS 24,17
M 8,40,4
s_bitcmp1_b32 s51, 0
LL 16,68
LL 116,69
M 8,41,5
s_addc_u32 s53, s53, 0
LA 16
s_mov_b64 exec, s[54:55]
SP
s_mov_b64 exec, -1
M 9,52,4
s_lshl_b32 s53, s53, 11
LC_ 116
M 9,53,5
s_bitcmp1_b64 s[90:91], s51
M 10,64,4
WL 3
LO 113,18,113
LU 18,113
M 10,65,5
s_cselect_b64 s[44:45], s[84:85], s[82:83]
RA 228,1,22528
HS 24,18
M 11,76,4
LL 17,70
LL 117,71
M 11,77,5
s_add_u32 s44, s44, s53
LA 17
M 11,78,0
s_addc_u32 s45, s45, 0
LC_ 117
M 11,79,1
s_add_u32 s51, s51, %[d]
M 10,66,0
WL 3
LO 116,16,116
LU 16,116
M 10,67,1
s_cmp_eq_u32 s51, %[wrap]
RA 232,1,23552
HS 24,16
M 9,54,0
s_cselect_b32 s51, %[rst], s51
LL 18,72
LL 120,73
M 9,55,1
s_add_u32 s40, s40, 3072
LA 18
M 8,42,0
s_addc_u32 s41, s41, 0
LC_ 120
s_add_u32 m0, s38, 3072
M 8,43,1
DW 0
M 7,30,0
WL 3
LO 117,17,117
LU 17,117
M 7,31,1
RA 236,1,24576
HS 24,17
M 6,18,0
LL 16,74
LL 121,75
M 6,19,1
LA 16
XL 4,256
M 6,20,2
LC_ 121
DW 1024
M 6,21,3
DW 2048
v_add_u32 v1, s37, v0
M 7,32,2
WL 3
)"
R"(LO 120,18,120
LU 18,120
M 7,33,3
s_lshl_b32 s53, s52, 8
RA 216,1,0
HS 24,18
M 8,44,2
s_add_u32 s48, %[alo], s53
LL 17,76
LL 124,77
M 8,45,3
s_addc_u32 s49, %[ahi], 0
LA 17
v_and_b32 v22, 0xfffffcff, v1
M 9,56,2
s_add_u32 s52, s52, %[d]
LC_ 124
M 9,57,3
s_cmp_eq_u32 s52, %[wrap]
M 10,68,2
WL 3
LO 121,16,121
LU 16,121
M 10,69,3
s_cselect_b32 s52, %[rst], s52
RA 220,1,1024
HS 24,16
M 11,80,2
LL 18,78
LL 125,79
M 11,81,3
LA 18
RV 252,22,5120
M 6,22,4
LC_ 125
DQ 3072
M 6,23,5
XL 5,320
M 7,34,4
WL 4
LO 124,17,124
LU 17,124
M 7,35,5
s_add_u32 s50, s50, %[d]
RA 224,1,2048
HS 24,17
M 8,46,4
s_cmp_eq_u32 s50, %[wrap]
LL 16,80
LL 106,81
M 8,47,5
s_cselect_b32 s50, %[rst], s50
LA 16
v_add_u32 v19, 1, v19
M 9,58,4
s_add_u32 s38, s38, 32768
LC_ 106
M 9,59,5
M 10,70,4
WL 3
LO 125,18,125
LU 18,125
M 10,71,5
RA 228,1,3072
HS 24,18
M 11,82,4
LL 17,82
LL 107,83
M 11,83,5
LA 17
HX 24
Z 0,12,0
s_cmp_eq_u32 s38, s77
LC_ 107
M 0,13,1
s_cselect_b32 s38, s76, s38
WV 36
FP
Z 1,24,0
WL 4
LO 16,16,106
LU 106,16
M 1,25,1
s_sub_u32 s39, 0, s39
RA 232,1,4096
HZ 25,106
Z 2,36,0
LL 18,84
LL 110,85
M 2,37,1
LA 18
PK 106,16,104,56
Z 3,48,0
LC_ 110
PK 104,16,104,97
M 3,49,1
XL 6,384
Z 4,60,0
WL 4
LO 17,17,107
LU 107,17
M 4,61,1
s_add_u32 s42, s44, s60
RA 236,1,6144
HS 25,107
Z 5,72,0
LL 16,86
LL 111,87
M 5,73,1
s_addc_u32 s43, s45, 0
LA 16
PK 107,17,105,56
M 5,74,2
LC_ 111
PK 105,17,105,97
M 5,75,3
ST 104
M 4,62,2
WL 3
LO 18,18,110
LU 110,18
M 4,63,3
RA 216,1,7168
HS 25,110
M 3,50,2
LL 17,88
LL 114,89
M 3,51,3
LA 17
PK 110,18,108,56
M 2,38,2
LC_ 114
PK 108,18,108,97
M 2,39,3
FR
PO 110
M 1,26,2
WL 4
LO 16,16,111
LU 111,16
M 1,27,3
RA 220,1,8192
HS 25,111
M 0,14,2
LL 18,90
LL 115,91
M 0,15,3
LA 18
PK 111,16,109,56
M 0,16,4
LC_ 115
PK 109,16,109,97
M 0,17,5
XL 7,448
PO 111
M 1,28,4
WL 4
LO 17,17,114
LU 114,17
M 1,29,5
s_add_u32 s42, s44, s61
RA 224,1,9216
HS 25,114
M 2,40,4
s_addc_u32 s43, s45, 0
LL 16,92
LL 118,93
M 2,41,5
s_mov_b32 m0, %[pm]
LA 16
PK 114,17,112,56
M 3,52,4
LC_ 118
PK 112,17,112,97
M 3,53,5
M 4,64,4
WL 3
LO 18,18,115
LU 115,18
M 4,65,5
RA 228,1,10240
HS 25,115
M 5,76,4
LL 17,94
LL 119,95
M 5,77,5
LA 17
PK 115,18,113,56
M 5,78,0
LC_ 119
PK 113,18,113,97
M 5,79,1
DF
M 4,66,0
WL 3
LO 16,16,118
LU 118,16
M 4,67,1
RA 232,1,11264
HS 25,118
M 3,54,0
LL 18,96
LL 122,97
M 3,55,1
LA 18
PK 118,16,116,56
v_cmp_gt_u32 vcc, v19, v15
M 2,42,0
LC_ 122
PK 116,16,116,97
M 2,43,1
RV 252,22,5392
PO 118
M 1,30,0
WL 4
LO 17,17,119
LU 119,17
M 1,31,1
s_cbranch_vccnz .Lk1s6
.Lk1r6:
RA 236,1,12288
HS 25,119
M 0,18,0
LL 16,98
LL 123,99
ST 108
M 0,19,1
s_add_u32 s42, s44, s62
LA 16
PK 119,17,117,56
s_addc_u32 s43, s45, 0
M 0,20,2
LC_ 123
PK 117,17,117,97
M 0,21,3
W1 24,0
PO 119
M 1,32,2
WL 5
LO 18,18,122
LU 122,18
M 1,33,3
RA 216,1,13312
HS 25,122
M 2,44,2
LL 17,100
LL 126,101
ST 112
M 2,45,3
LA 17
PK 122,18,120,56
M 3,56,2
LC_ 126
XL 8,512
PK 120,18,120,97
M 3,57,3
s_add_u32 s42, s44, s63
M 4,68,2
WL 4
LO 16,16,123
LU 123,16
M 4,69,3
s_addc_u32 s43, s45, 0
RA 220,1,14336
HS 25,123
M 5,80,2
LL 18,102
LL 127,103
M 5,81,3
LA 18
PK 123,16,121,56
M 0,22,4
LC_ 127
PK 121,16,121,97
M 0,23,5
ST 116
M 1,34,4
WL 3
LO 17,17,126
LU 126,17
M 1,35,5
s_add_u32 s42, s44, s64
RA 224,1,15360
HS 25,126
M 2,46,4
s_addc_u32 s43, s45, 0
LL 16,56
LL 104,57
M 2,47,5
LA 16
PK 126,17,124,56
M 3,58,4
LC_ 104
PK 124,17,124,97
M 3,59,5
XL 9,576
PO 126
M 4,70,4
WL 3
LO 18,18,127
LU 127,18
M 4,71,5
RA 228,1,16384
HS 25,127
M 5,82,4
LL 17,58
LL 105,59
M 5,83,5
LA 17
PK 127,18,125,56
Z 6,12,0
LC_ 105
PK 125,18,125,97
M 6,13,1
ST 120
PO 127
Z 7,24,0
WL 3
LO 104,16,104
LU 16,104
M 7,25,1
s_add_u32 s42, s44, s65
RA 232,1,17408
HZ 24,16
Z 8,36,0
s_addc_u32 s43, s45, 0
LL 18,60
LL 108,61
M 8,37,1
s_mul_i32 s53, s50, 25600
LA 18
ds_read_b64 v[14:15], v5 offset:0
Z 9,48,0
s_add_u32 s40, %[wlo], s53
LC_ 108
v_subrev_u32 v5, s39, v5
M 9,49,1
s_addc_u32 s41, %[whi], 0
Z 10,60,0
WL 4
LO 105,17,105
LU 17,105
M 10,61,1
s_mov_b32 m0, s38
RA 236,1,18432
HS 24,17
Z 11,72,0
LL 16,62
LL 109,63
M 11,73,1
LA 16
XL 10,640
M 11,74,2
LC_ 109
ST 124
M 11,75,3
DW 0
HX 25
M 10,62,2
WL 3
LO 108,18,108
LU 18,108
W1 25,1024
M 10,63,3
s_add_u32 s37, s37, 32768
RA 216,1,19456
HS 24,18
M 9,50,2
s_cmp_eq_u32 s37, s75
LL 17,64
LL 112,65
M 9,51,3
s_cselect_b32 s37, s74, s37
LA 17
DW 1024
M 8,38,2
LC_ 112
v_add_f32 v14, v14, v15
s_nop 1
v_add_f32_dpp v14, v14, v14 quad_perm:[1,0,3,2] row_mask:0xf bank_mask:0xf
M 8,39,3
s_nop 0
v_add_f32_dpp v14, v14, v14 quad_perm:[2,3,0,1] row_mask:0xf bank_mask:0xf
v_add_u32 v4, s39, v4
M 7,26,2
WL 4
LO 109,16,109
LU 16,109
M 7,27,3
RA 220,1,20480
HS 24,16
M 6,14,2
LL 18,66
LL 113,67
M 6,15,3
LA 18
DW 2048
M 6,16,4
LC_ 113
XL 11,704
)"
R"(v_add_f32_dpp v14, v14, v14 row_half_mirror row_mask:0xf bank_mask:0xf
M 6,17,5
v_cvt_f64_f32 v[14:15], v14
M 7,28,4
WL 3
LO 112,17,112
LU 17,112
M 7,29,5
s_mul_i32 s53, s51, 24
RA 224,1,21504
HS 24,17
M 8,40,4
s_bitcmp1_b32 s51, 0
LL 16,68
LL 116,69
M 8,41,5
s_addc_u32 s53, s53, 0
LA 16
s_mov_b64 exec, s[54:55]
SP
s_mov_b64 exec, -1
M 9,52,4
s_lshl_b32 s53, s53, 11
LC_ 116
M 9,53,5
s_bitcmp1_b64 s[90:91], s51
M 10,64,4
WL 3
LO 113,18,113
LU 18,113
M 10,65,5
s_cselect_b64 s[44:45], s[84:85], s[82:83]
RA 228,1,22528
HS 24,18
M 11,76,4
LL 17,70
LL 117,71
M 11,77,5
s_add_u32 s44, s44, s53
LA 17
M 11,78,0
s_addc_u32 s45, s45, 0
LC_ 117
M 11,79,1
s_add_u32 s51, s51, %[d]
M 10,66,0
WL 3
LO 116,16,116
LU 16,116
M 10,67,1
s_cmp_eq_u32 s51, %[wrap]
RA 232,1,23552
HS 24,16
M 9,54,0
s_cselect_b32 s51, %[rst], s51
LL 18,72
LL 120,73
M 9,55,1
s_add_u32 s40, s40, 3072
LA 18
M 8,42,0
s_addc_u32 s41, s41, 0
LC_ 120
s_add_u32 m0, s38, 3072
M 8,43,1
DW 0
M 7,30,0
WL 3
LO 117,17,117
LU 17,117
M 7,31,1
RA 236,1,24576
HS 24,17
M 6,18,0
LL 16,74
LL 121,75
M 6,19,1
LA 16
DW 1024
M 6,20,2
s_bitcmp1_b64 s[92:93], 48
LC_ 121
M 6,21,3
s_cselect_b32 s58, 16, 0
v_add_u32 v1, s37, v0
M 7,32,2
WL 3
LO 120,18,120
LU 18,120
M 7,33,3
s_lshr_b32 s58, 0xda43cb52, s58
RA 216,1,0
HS 24,18
M 8,44,2
LL 17,76
LL 124,77
M 8,45,3
LA 17
v_and_b32 v22, 0xfffffcff, v1
M 9,56,2
LC_ 124
v_and_b32 v11, 0x1f0, v0
M 9,57,3
v_min_u32 v11, 368, v11
M 10,68,2
WL 3
LO 121,16,121
LU 16,121
M 10,69,3
RA 220,1,1024
HS 24,16
M 11,80,2
LL 18,78
LL 125,79
M 11,81,3
LA 18
RV 252,22,5120
DW 2048
v_lshlrev_b32 v11, 3, v11
M 6,22,4
s_lshl_b32 s53, s52, 8
LC_ 125
M 6,23,5
s_add_u32 s48, %[alo], s53
M 7,34,4
WL 4
LO 124,17,124
LU 17,124
M 7,35,5
s_addc_u32 s49, %[ahi], 0
RA 224,1,2048
HS 24,17
M 8,46,4
s_add_u32 s52, s52, %[d]
LL 16,80
LL 106,81
s_cmp_eq_u32 s52, %[wrap]
s_cselect_b32 s52, %[rst], s52
M 8,47,5
LA 16
DQ 3072
M 9,58,4
s_add_u32 s50, s50, %[d]
LC_ 106
M 9,59,5
M 10,70,4
WL 3
LO 125,18,125
LU 18,125
M 10,71,5
RA 228,1,3072
HS 24,18
M 11,82,4
LL 17,82
LL 107,83
M 11,83,5
LA 17
HX 24
s_cmp_eq_u32 s50, %[wrap]
s_cselect_b32 s50, %[rst], s50
Z 0,12,0
LC_ 107
v_add_u32 v19, 1, v19
M 0,13,1
s_add_u32 s38, s38, 32768
Z 1,24,0
WL 3
LO 16,16,106
LU 106,16
M 1,25,1
s_cmp_eq_u32 s38, s77
RA 232,1,4096
HZ 25,106
Z 2,36,0
s_cselect_b32 s38, s76, s38
LL 18,84
LL 110,85
M 2,37,1
LA 18
PK 106,16,104,56
Z 3,48,0
LC_ 110
PK 104,16,104,97
M 3,49,1
WV 42
FP
s_sub_u32 s39, 0, s39
Z 4,60,0
WL 4
LO 17,17,107
LU 107,17
M 4,61,1
s_add_u32 s42, s44, s60
RA 236,1,6144
HS 25,107
Z 5,72,0
LL 16,86
LL 111,87
M 5,73,1
s_addc_u32 s43, s45, 0
LA 16
PK 107,17,105,56
M 5,74,2
LC_ 111
PK 105,17,105,97
M 5,75,3
ST 104
M 4,62,2
WL 4
LO 18,18,110
LU 110,18
M 4,63,3
RA 216,1,7168
HS 25,110
M 3,50,2
LL 17,88
LL 114,89
M 3,51,3
LA 17
PK 110,18,108,56
M 2,38,2
LC_ 114
PK 108,18,108,97
M 2,39,3
FR
PO 110
M 1,26,2
WL 4
LO 16,16,111
LU 111,16
M 1,27,3
s_add_u32 s42, s44, s61
RA 220,1,8192
HS 25,111
M 0,14,2
LL 18,90
LL 115,91
M 0,15,3
s_addc_u32 s43, s45, 0
LA 18
PK 111,16,109,56
M 0,16,4
s_mov_b32 m0, %[pm]
LC_ 115
PK 109,16,109,97
M 0,17,5
DF
PO 111
M 1,28,4
WL 4
LO 17,17,114
LU 114,17
M 1,29,5
RA 224,1,9216
HS 25,114
M 2,40,4
LL 16,92
LL 118,93
M 2,41,5
LA 16
PK 114,17,112,56
M 3,52,4
LC_ 118
PK 112,17,112,97
M 3,53,5
WL 6
ST 108
v_cmp_gt_u32 vcc, v19, v15
M 4,64,4
WL 3
LO 18,18,115
LU 115,18
M 4,65,5
s_add_u32 s42, s44, s62
RA 228,1,10240
HS 25,115
M 5,76,4
LL 17,94
LL 119,95
M 5,77,5
s_cbranch_vccnz .Lk1s7
.Lk1r7:
LA 17
PK 115,18,113,56
M 5,78,0
s_addc_u32 s43, s45, 0
LC_ 119
PK 113,18,113,97
M 5,79,1
W1 24,0
M 4,66,0
WL 4
LO 16,16,118
LU 118,16
M 4,67,1
RA 232,1,11264
HS 25,118
M 3,54,0
LL 18,96
LL 122,97
M 3,55,1
LA 18
PK 118,16,116,56
M 2,42,0
LC_ 122
PK 116,16,116,97
M 2,43,1
RV 252,22,5392
PO 118
M 1,30,0
WL 5
LO 17,17,119
LU 119,17
M 1,31,1
RA 236,1,12288
HS 25,119
M 0,18,0
LL 16,98
LL 123,99
M 0,19,1
LA 16
PK 119,17,117,56
M 0,20,2
LC_ 123
PK 117,17,117,97
M 0,21,3
ST 112
PO 119
M 1,32,2
WL 4
LO 18,18,122
LU 122,18
M 1,33,3
RA 216,1,13312
HS 25,122
M 2,44,2
LL 17,100
LL 126,101
M 2,45,3
LA 17
PK 122,18,120,56
M 3,56,2
LC_ 126
PK 120,18,120,97
M 3,57,3
v_lshrrev_b32 v3, 9, v0
M 4,68,2
WL 3
LO 16,16,123
LU 123,16
M 4,69,3
RA 220,1,14336
HS 25,123
v_lshl_add_u32 v3, v3, 2, 0
v_bfe_u32 v3, s58, v3, 4
v_mad_u32_u24 v3, v3, v32, v11
M 5,80,2
LL 18,102
LL 127,103
M 5,81,3
s_add_u32 s42, s44, s63
LA 18
PK 123,16,121,56
M 0,22,4
s_addc_u32 s43, s45, 0
LC_ 127
PK 121,16,121,97
M 0,23,5
ST 116
M 1,34,4
WL 3
LO 17,17,126
LU 126,17
M 1,35,5
s_add_u32 s42, s44, s64
RA 224,1,15360
HS 25,126
M 2,46,4
s_addc_u32 s43, s45, 0
LL 16,56
LL 104,57
M 2,47,5
LA 16
PK 126,17,124,56
M 3,58,4
LC_ 104
PK 124,17,124,97
M 3,59,5
ST 120
PO 126
M 4,70,4
WL 3
LO 18,18,127
LU 127,18
M 4,71,5
s_add_u32 s42, s44, s65
RA 228,1,16384
HS 25,127
M 5,82,4
LL 17,58
)"
R"(LL 105,59
M 5,83,5
s_addc_u32 s43, s45, 0
LA 17
PK 127,18,125,56
Z 6,12,0
LC_ 105
PK 125,18,125,97
M 6,13,1
ds_read_b64 v[14:15], v5 offset:0
PO 127
Z 7,24,0
WL 4
LO 104,16,104
LU 16,104
M 7,25,1
RA 232,1,17408
HZ 24,16
Z 8,36,0
LL 18,60
LL 108,61
M 8,37,1
s_mul_i32 s53, s50, 25600
LA 18
v_subrev_u32 v5, s39, v5
Z 9,48,0
s_add_u32 s40, %[wlo], s53
LC_ 108
HX 25
M 9,49,1
s_addc_u32 s41, %[whi], 0
W1 25,1024
Z 10,60,0
WL 5
LO 105,17,105
LU 17,105
M 10,61,1
s_mov_b32 m0, s38
RA 236,1,18432
HS 24,17
Z 11,72,0
LL 16,62
LL 109,63
M 11,73,1
LA 16
ST 124
M 11,74,2
LC_ 109
DW 0
M 11,75,3
WL 7
v_add_f32 v14, v14, v15
v_add_u32 v4, s39, v4
M 10,62,2
WL 4
LO 108,18,108
LU 18,108
M 10,63,3
s_add_u32 s37, s37, 32768
RA 216,1,19456
HS 24,18
M 9,50,2
s_cmp_eq_u32 s37, s75
LL 17,64
LL 112,65
M 9,51,3
s_cselect_b32 s37, s74, s37
LA 17
DW 1024
M 8,38,2
LC_ 112
v_add_f32_dpp v14, v14, v14 quad_perm:[1,0,3,2] row_mask:0xf bank_mask:0xf
M 8,39,3
s_nop 0
v_add_f32_dpp v14, v14, v14 quad_perm:[2,3,0,1] row_mask:0xf bank_mask:0xf
M 7,26,2
WL 3
LO 109,16,109
LU 16,109
M 7,27,3
RA 220,1,20480
HS 24,16
M 6,14,2
LL 18,66
LL 113,67
M 6,15,3
LA 18
DW 2048
M 6,16,4
LC_ 113
v_add_f32_dpp v14, v14, v14 row_half_mirror row_mask:0xf bank_mask:0xf
M 6,17,5
v_cvt_f64_f32 v[14:15], v14
M 7,28,4
WL 3
LO 112,17,112
LU 17,112
M 7,29,5
s_mul_i32 s53, s51, 24
RA 224,1,21504
HS 24,17
M 8,40,4
s_bitcmp1_b32 s51, 0
LL 16,68
LL 116,69
M 8,41,5
s_addc_u32 s53, s53, 0
LA 16
s_mov_b64 exec, s[54:55]
SP
s_mov_b64 exec, -1
M 9,52,4
s_lshl_b32 s53, s53, 11
LC_ 116
M 9,53,5
s_bitcmp1_b64 s[90:91], s51
M 10,64,4
WL 3
LO 113,18,113
LU 18,113
M 10,65,5
s_cselect_b64 s[44:45], s[84:85], s[82:83]
RA 228,1,22528
HS 24,18
M 11,76,4
LL 17,70
LL 117,71
M 11,77,5
s_add_u32 s44, s44, s53
LA 17
M 11,78,0
s_addc_u32 s45, s45, 0
LC_ 117
M 11,79,1
s_add_u32 s51, s51, %[d]
M 10,66,0
WL 3
LO 116,16,116
LU 16,116
M 10,67,1
s_cmp_eq_u32 s51, %[wrap]
RA 232,1,23552
HS 24,16
M 9,54,0
s_cselect_b32 s51, %[rst], s51
LL 18,72
LL 120,73
M 9,55,1
s_add_u32 s40, s40, 3072
LA 18
M 8,42,0
s_addc_u32 s41, s41, 0
LC_ 120
s_add_u32 m0, s38, 3072
M 8,43,1
DW 0
M 7,30,0
WL 3
LO 117,17,117
LU 17,117
M 7,31,1
RA 236,1,24576
HS 24,17
M 6,18,0
LL 16,74
LL 121,75
M 6,19,1
LA 16
DW 1024
M 6,20,2
LC_ 121
DY
M 6,21,3
DW 2048
v_add_u32 v1, s37, v0
M 7,32,2
WL 3
LO 120,18,120
LU 18,120
M 7,33,3
s_lshl_b32 s53, s52, 8
RA 216,1,0
HS 24,18
M 8,44,2
s_add_u32 s48, %[alo], s53
LL 17,76
LL 124,77
M 8,45,3
s_addc_u32 s49, %[ahi], 0
LA 17
v_and_b32 v22, 0xfffffcff, v1
M 9,56,2
s_add_u32 s52, s52, %[d]
LC_ 124
M 9,57,3
s_cmp_eq_u32 s52, %[wrap]
M 10,68,2
WL 3
LO 121,16,121
LU 16,121
M 10,69,3
s_cselect_b32 s52, %[rst], s52
RA 220,1,1024
HS 24,16
M 11,80,2
LL 18,78
LL 125,79
M 11,81,3
LA 18
RV 252,22,5120
M 6,22,4
LC_ 125
DQ 3072
M 6,23,5
s_add_u32 s50, s50, %[d]
M 7,34,4
WL 4
LO 124,17,124
LU 17,124
M 7,35,5
s_cmp_eq_u32 s50, %[wrap]
RA 224,1,2048
HS 24,17
M 8,46,4
s_cselect_b32 s50, %[rst], s50
LL 16,80
LL 106,81
M 8,47,5
LA 16
v_add_u32 v19, 1, v19
M 9,58,4
s_add_u32 s38, s38, 32768
LC_ 106
M 9,59,5
M 10,70,4
WL 3
LO 125,18,125
LU 18,125
M 10,71,5
RA 228,1,3072
HS 24,18
M 11,82,4
LL 17,82
LL 107,83
M 11,83,5
LA 17
HX 24
Z 0,12,0
s_cmp_eq_u32 s38, s77
LC_ 107
M 0,13,1
s_cselect_b32 s38, s76, s38
WV 38
FP
Z 1,24,0
WL 4
LO 16,16,106
LU 106,16
M 1,25,1
s_sub_u32 s39, 0, s39
RA 232,1,4096
HZ 25,106
Z 2,36,0
LL 18,84
LL 110,85
M 2,37,1
LA 18
PK 106,16,104,56
Z 3,48,0
LC_ 110
PK 104,16,104,97
M 3,49,1
Z 4,60,0
WL 4
LO 17,17,107
LU 107,17
M 4,61,1
s_add_u32 s42, s44, s60
RA 236,1,6144
HS 25,107
Z 5,72,0
LL 16,86
LL 111,87
M 5,73,1
s_addc_u32 s43, s45, 0
LA 16
PK 107,17,105,56
M 5,74,2
LC_ 111
PK 105,17,105,97
M 5,75,3
ST 104
M 4,62,2
WL 3
LO 18,18,110
LU 110,18
M 4,63,3
RA 216,1,7168
HS 25,110
M 3,50,2
LL 17,88
LL 114,89
M 3,51,3
LA 17
PK 110,18,108,56
M 2,38,2
LC_ 114
PK 108,18,108,97
M 2,39,3
FR
PO 110
M 1,26,2
WL 4
LO 16,16,111
LU 111,16
M 1,27,3
s_add_u32 s42, s44, s61
RA 220,1,8192
HS 25,111
M 0,14,2
LL 18,90
LL 115,91
M 0,15,3
s_addc_u32 s43, s45, 0
LA 18
PK 111,16,109,56
M 0,16,4
s_mov_b32 m0, %[pm]
LC_ 115
PK 109,16,109,97
M 0,17,5
DF
PO 111
M 1,28,4
WL 4
LO 17,17,114
LU 114,17
M 1,29,5
RA 224,1,9216
HS 25,114
M 2,40,4
LL 16,92
LL 118,93
M 2,41,5
LA 16
PK 114,17,112,56
M 3,52,4
LC_ 118
PK 112,17,112,97
M 3,53,5
WL 6
ST 108
v_cmp_gt_u32 vcc, v19, v15
M 4,64,4
WL 3
LO 18,18,115
LU 115,18
M 4,65,5
s_add_u32 s42, s44, s62
RA 228,1,10240
HS 25,115
M 5,76,4
LL 17,94
LL 119,95
M 5,77,5
s_cbranch_vccnz .Lk1s8
.Lk1r8:
LA 17
PK 115,18,113,56
M 5,78,0
s_addc_u32 s43, s45, 0
LC_ 119
PK 113,18,113,97
M 5,79,1
W1 24,0
M 4,66,0
WL 4
LO 16,16,118
LU 118,16
M 4,67,1
RA 232,1,11264
HS 25,118
M 3,54,0
LL 18,96
LL 122,97
M 3,55,1
LA 18
PK 118,16,116,56
M 2,42,0
LC_ 122
PK 116,16,116,97
M 2,43,1
RV 252,22,5392
PO 118
M 1,30,0
WL 5
LO 17,17,119
LU 119,17
M 1,31,1
RA 236,1,12288
)"
R"(HS 25,119
M 0,18,0
LL 16,98
LL 123,99
M 0,19,1
LA 16
PK 119,17,117,56
M 0,20,2
LC_ 123
PK 117,17,117,97
BW 0
M 0,21,3
ST 112
PO 119
M 1,32,2
WL 5
RV 40,8,6144
LO 18,18,122
LU 122,18
M 1,33,3
RA 216,1,13312
RV 44,8,6208
HS 25,122
M 2,44,2
LL 17,100
LL 126,101
M 2,45,3
LA 17
PK 122,18,120,56
v_lshrrev_b32 v3, 9, v0
v_lshl_add_u32 v3, v3, 2, 8
v_bfe_u32 v3, s58, v3, 4
v_mad_u32_u24 v3, v3, v32, v11
RV 48,8,6272
M 3,56,2
s_add_u32 s42, s44, s63
LC_ 126
BR 0
PK 120,18,120,97
M 3,57,3
s_addc_u32 s43, s45, 0
ST 116
BW 1
M 4,68,2
WL 9
LO 16,16,123
LU 123,16
M 4,69,3
s_add_u32 s42, s44, s64
RA 220,1,14336
BR 1
HS 25,123
M 5,80,2
WL 3
LL 18,102
LL 127,103
M 5,81,3
s_addc_u32 s43, s45, 0
LA 18
PK 123,16,121,56
LZ 40,0
BW 2
M 0,22,4
LC_ 127
PK 121,16,121,97
M 0,23,5
ST 120
RV 40,8,6336
M 1,34,4
BR 2
LO 17,17,126
LU 126,17
M 1,35,5
WL 5
RA 224,1,15360
HS 25,126
LS 44,1
BW 3
M 2,46,4
LL 16,56
LL 104,57
M 2,47,5
LA 16
PK 126,17,124,56
M 3,58,4
LC_ 104
RV 44,8,6400
PK 124,17,124,97
M 3,59,5
ds_read_b64 v[14:15], v5 offset:0
BR 3
PO 126
M 4,70,4
WL 7
LO 18,18,127
LU 127,18
M 4,71,5
s_add_u32 s42, s44, s65
RA 228,1,16384
HS 25,127
LS 48,2
s_addc_u32 s43, s45, 0
BW 4
M 5,82,4
LL 17,58
LL 105,59
M 5,83,5
LA 17
RV 48,8,6464
PK 127,18,125,56
Z 6,12,0
LC_ 105
PK 125,18,125,97
M 6,13,1
BR 4
PO 127
Z 7,24,0
WL 9
LO 104,16,104
LU 16,104
M 7,25,1
WL 6
RA 232,1,17408
HZ 24,16
LS 40,3
WV 43
BW 5
Z 8,36,0
WV 42
LL 18,60
LL 108,61
M 8,37,1
s_mul_i32 s53, s50, 25600
LA 18
RV 40,8,6528
Z 9,48,0
s_add_u32 s40, %[wlo], s53
LC_ 108
BR 5
M 9,49,1
s_addc_u32 s41, %[whi], 0
v_subrev_u32 v5, s39, v5
HX 25
WL 6
LS 44,4
s_mov_b32 m0, s38
BW 6
Z 10,60,0
LO 105,17,105
LU 17,105
M 10,61,1
RA 236,1,18432
HS 24,17
Z 11,72,0
RV 44,8,6592
LL 16,62
LL 109,63
M 11,73,1
LA 16
W1 25,1024
BR 6
M 11,74,2
LC_ 109
ST 124
M 11,75,3
WL 7
DW 0
v_add_f32 v14, v14, v15
LS 48,5
WV 42
BW 7
M 10,62,2
LO 108,18,108
LU 18,108
M 10,63,3
s_add_u32 s37, s37, 32768
RA 216,1,19456
HS 24,18
M 9,50,2
s_cmp_eq_u32 s37, s75
RV 48,8,6656
LL 17,64
LL 112,65
M 9,51,3
s_cselect_b32 s37, s74, s37
LA 17
DW 1024
BR 7
M 8,38,2
LC_ 112
v_add_f32_dpp v14, v14, v14 quad_perm:[1,0,3,2] row_mask:0xf bank_mask:0xf
M 8,39,3
WL 7
v_add_f32_dpp v14, v14, v14 quad_perm:[2,3,0,1] row_mask:0xf bank_mask:0xf
v_add_u32 v4, s39, v4
LS 40,6
WV 39
BW 8
M 7,26,2
WL 7
LO 109,16,109
LU 16,109
M 7,27,3
RA 220,1,20480
RV 40,8,6720
HS 24,16
M 6,14,2
BR 8
LL 18,66
LL 113,67
M 6,15,3
LA 18
DW 2048
M 6,16,4
LC_ 113
v_add_f32_dpp v14, v14, v14 row_half_mirror row_mask:0xf bank_mask:0xf
M 6,17,5
WL 7
v_cvt_f64_f32 v[14:15], v14
LS 44,7
WV 36
BW 9
M 7,28,4
WL 3
RV 44,8,6784
LO 112,17,112
LU 17,112
M 7,29,5
s_mul_i32 s53, s51, 24
RA 224,1,21504
HS 24,17
M 8,40,4
s_bitcmp1_b32 s51, 0
BR 9
LL 16,68
LL 116,69
M 8,41,5
s_addc_u32 s53, s53, 0
LA 16
s_mov_b64 exec, s[54:55]
SP
s_mov_b64 exec, -1
LS 48,8
s_lshl_b32 s53, s53, 11
BW 10
M 9,52,4
s_bitcmp1_b64 s[90:91], s51
LC_ 116
RV 48,8,6848
M 9,53,5
s_cselect_b64 s[44:45], s[84:85], s[82:83]
RA 240,7,0
BR 10
M 10,64,4
WL 10
LO 113,18,113
LU 18,113
M 10,65,5
s_add_u32 s44, s44, s53
RA 228,1,22528
HS 24,18
M 11,76,4
WL 7
LL 17,70
LL 117,71
M 11,77,5
s_addc_u32 s45, s45, 0
LA 17
M 11,78,0
s_add_u32 s51, s51, %[d]
LC_ 117
M 11,79,1
s_cmp_eq_u32 s51, %[wrap]
LS 40,9
s_cselect_b32 s51, %[rst], s51
WV 32
BW 11
M 10,66,0
WL 4
RA 244,7,256
LO 116,16,116
LU 16,116
M 10,67,1
s_add_u32 s40, s40, 3072
RA 232,1,23552
HS 24,16
M 9,54,0
s_addc_u32 s41, s41, 0
BR 11
LL 18,72
LL 120,73
M 9,55,1
s_add_u32 m0, s38, 3072
LA 18
DW 0
LS 44,10
RA 248,7,512
M 8,42,0
LC_ 120
DY
M 8,43,1
DW 1024
M 7,30,0
WL 7
LO 117,17,117
LU 17,117
M 7,31,1
WL 3
RA 236,1,24576
HS 24,17
LS 48,11
RA 252,7,768
M 6,18,0
LL 16,74
LL 121,75
s_nop 3
v_mul_f32 v36, 0xbb2aaaab, v36
M 6,19,1
LA 16
DW 2048
v_mul_f32 v37, 0xbb2aaaab, v37
M 6,20,2
LC_ 121
v_mul_f32 v38, 0xbb2aaaab, v38
v_mul_f32 v39, 0xbb2aaaab, v39
M 6,21,3
s_lshl_b32 s53, s52, 8
v_add_u32 v1, s37, v0
LK 40,240,0,0
LK 44,242,0,2
s_add_u32 s48, %[alo], s53
RA 240,7,1024
LK 48,244,1,0
s_nop 0
CF 128,40,41
LK 248,246,1,2
s_addc_u32 s49, %[ahi], 0
RA 244,7,1280
CF 129,42,43
M 7,32,2
WL 6
LO 120,18,120
LU 18,120
M 7,33,3
s_add_u32 s52, s52, %[d]
RA 216,1,0
HS 24,18
M 8,44,2
s_cmp_eq_u32 s52, %[wrap]
LL 17,76
LL 124,77
M 8,45,3
s_cselect_b32 s52, %[rst], s52
LA 17
DQ 3072
M 9,56,2
LC_ 124
v_and_b32 v22, 0xfffffcff, v1
CF 130,44,45
M 9,57,3
CF 131,46,47
LK 40,248,2,0
CF 132,48,49
LK 44,250,2,2
RA 248,7,1536
CF 133,50,51
GZ 0
WL 8
CF 134,248,249
CF 135,250,251
LK 48,252,3,0
CF 136,40,41
LK 248,254,3,2
RA 252,7,1792
CF 137,42,43
GS 1
WL 6
CF 138,44,45
CF 139,46,47
LK 40,240,4,0
CF 140,48,49
LK 44,242,4,2
RA 240,7,2048
CF 141,50,51
GS 2
WL 6
CF 142,248,249
CF 143,250,251
LK 48,244,5,0
CF 144,40,41
LK 248,246,5,2
RA 244,7,2304
CF 145,42,43
GS 3
CF 146,44,45
CF 147,46,47
M 10,68,2
WL 3
LO 121,16,121
LU 16,121
)"
R"(M 10,69,3
s_add_u32 s50, s50, %[d]
RA 220,1,1024
HS 24,16
LK 40,248,6,0
WL 3
CF 148,48,49
LK 44,250,6,2
s_cmp_eq_u32 s50, %[wrap]
RA 248,7,2560
CF 149,50,51
GS 4
s_cselect_b32 s50, %[rst], s50
CF 150,248,249
CF 151,250,251
LK 48,252,7,0
CF 152,40,41
LK 248,254,7,2
RA 252,7,2816
CF 153,42,43
GS 5
WL 4
CF 154,44,45
CF 155,46,47
LK 40,240,8,0
CF 156,48,49
LK 44,242,8,2
CF 157,50,51
GS 6
CF 158,248,249
CF 159,250,251
M 11,80,2
LL 18,78
LL 125,79
M 11,81,3
WL 3
LA 18
RV 252,22,5120
LK 48,244,9,0
CF 160,40,41
LK 248,246,9,2
CF 161,42,43
GS 7
CF 162,44,45
CF 163,46,47
M 6,22,4
LC_ 125
M 6,23,5
WL 4
v_add_u32 v19, 1, v19
M 7,34,4
WV 32
LO 124,17,124
LU 17,124
M 7,35,5
s_add_u32 s38, s38, 32768
RA 224,1,2048
HS 24,17
M 8,46,4
s_cmp_eq_u32 s38, s77
LL 16,80
LL 106,81
M 8,47,5
s_cselect_b32 s38, s76, s38
LA 16
FP
LK 40,248,10,0
CF 164,48,49
LK 44,250,10,2
s_sub_u32 s39, 0, s39
CF 165,50,51
GS 8
CF 166,248,249
CF 167,250,251
M 9,58,4
LC_ 106
M 9,59,5
M 10,70,4
WL 4
LO 125,18,125
LU 18,125
M 10,71,5
RA 228,1,3072
HS 24,18
LK 48,252,11,0
CF 168,40,41
LK 248,254,11,2
CF 169,42,43
GS 9
CF 170,44,45
CF 171,46,47
M 11,82,4
LL 17,82
LL 107,83
M 11,83,5
LA 17
HX 24
Z 0,12,0
LC_ 107
RV 36,8,6912
M 0,13,1
GS 10
CF 172,48,49
Z 1,24,0
WL 4
LO 16,16,106
LU 106,16
M 1,25,1
RA 232,1,4096
HZ 25,106
Z 2,36,0
LL 18,84
LL 110,85
M 2,37,1
LA 18
PK 106,16,104,56
Z 3,48,0
LC_ 110
PK 104,16,104,97
M 3,49,1
CF 173,50,51
CF 174,248,249
Z 4,60,0
WL 4
LO 17,17,107
LU 107,17
M 4,61,1
s_add_u32 s42, s44, s60
RA 236,1,6144
HS 25,107
Z 5,72,0
LL 16,86
LL 111,87
M 5,73,1
s_addc_u32 s43, s45, 0
LA 16
PK 107,17,105,56
M 5,74,2
LC_ 111
PK 105,17,105,97
M 5,75,3
ST 104
CF 175,250,251
M 4,62,2
WL 3
LO 18,18,110
LU 110,18
M 4,63,3
RA 216,1,7168
HS 25,110
GS 11
M 3,50,2
LL 17,88
LL 114,89
M 3,51,3
LA 17
PK 110,18,108,56
M 2,38,2
LC_ 114
PK 108,18,108,97
M 2,39,3
FR
PO 110
M 1,26,2
WL 4
LO 16,16,111
LU 111,16
M 1,27,3
s_add_u32 s42, s44, s61
RA 220,1,8192
HS 25,111
M 0,14,2
LL 18,90
LL 115,91
M 0,15,3
s_addc_u32 s43, s45, 0
LA 18
PK 111,16,109,56
M 0,16,4
s_mov_b32 m0, %[pm]
LC_ 115
PK 109,16,109,97
M 0,17,5
DF
PO 111
M 1,28,4
WL 4
LO 17,17,114
LU 114,17
M 1,29,5
RA 224,1,9216
HS 25,114
M 2,40,4
LL 16,92
LL 118,93
M 2,41,5
LA 16
PK 114,17,112,56
M 3,52,4
LC_ 118
PK 112,17,112,97
M 3,53,5
WL 6
ST 108
v_cmp_gt_u32 vcc, v19, v15
M 4,64,4
WL 3
LO 18,18,115
LU 115,18
M 4,65,5
s_add_u32 s42, s44, s62
RA 228,1,10240
HS 25,115
M 5,76,4
LL 17,94
LL 119,95
v_cndmask_b32 v52, v52, v53, s[78:79]
M 5,77,5
s_cbranch_vccnz .Lk1s9
.Lk1r9:
LA 17
PK 115,18,113,56
M 5,78,0
s_addc_u32 s43, s45, 0
LC_ 119
PK 113,18,113,97
v_cndmask_b32 v54, v54, v55, s[78:79]
M 5,79,1
W1 24,0
v_cndmask_b32 v52, v52, v54, s[80:81]
M 4,66,0
WL 4
LO 16,16,118
LU 118,16
v_mul_f32 v52, 0x3b2aaaab, v52
M 4,67,1
RA 232,1,11264
HS 25,118
v_med3_f32 v53, v52, v39, v37
M 3,54,0
LL 18,96
LL 122,97
LG 52,53
M 3,55,1
LA 18
PK 118,16,116,56
C 52,52,52
M 2,42,0
LC_ 122
PK 116,16,116,97
v_lshlrev_b32 v52, 16, v52
M 2,43,1
RV 252,22,5392
PO 118
v_add_f32 v52, v36, v52
M 1,30,0
WL 5
LO 17,17,119
LU 119,17
C 52,52,52
M 1,31,1
RA 236,1,12288
HS 25,119
M 0,18,0
LL 16,98
LL 123,99
v_lshlrev_b32 v52, 16, v52
M 0,19,1
LA 16
PK 119,17,117,56
v_sqrt_f32 v52, v52
M 0,20,2
LC_ 123
PK 117,17,117,97
C 52,52,52
M 0,21,3
ST 112
PO 119
v_lshlrev_b32 v52, 16, v52
M 1,32,2
WL 4
LO 18,18,122
LU 122,18
v_rcp_f32 v52, v52
M 1,33,3
s_add_u32 s42, s44, s63
RA 216,1,13312
HS 25,122
v_mul_f32 v52, v38, v52
M 2,44,2
s_addc_u32 s43, s45, 0
LL 17,100
LL 126,101
CF 52,52,52
M 2,45,3
LA 17
PK 122,18,120,56
ds_bpermute_b32 v13, v28, v52
M 3,56,2
LC_ 126
PK 120,18,120,97
M 3,57,3
ST 116
M 4,68,2
WL 4
LO 16,16,123
LU 123,16
M 4,69,3
s_add_u32 s42, s44, s64
RA 220,1,14336
HS 25,123
M 5,80,2
LL 18,102
LL 127,103
M 5,81,3
s_addc_u32 s43, s45, 0
LA 18
PK 123,16,121,56
M 0,22,4
LC_ 127
PK 121,16,121,97
M 0,23,5
ST 120
M 1,34,4
WL 3
LO 17,17,126
LU 126,17
M 1,35,5
RA 224,1,15360
HS 25,126
XS 128
M 2,46,4
LL 16,56
LL 104,57
M 2,47,5
LA 16
PK 126,17,124,56
M 3,58,4
LC_ 104
PK 124,17,124,97
XS 129
M 3,59,5
ds_read_b64 v[14:15], v5 offset:0
PO 126
M 4,70,4
WL 4
LO 18,18,127
LU 127,18
M 4,71,5
s_add_u32 s42, s44, s65
RA 228,1,16384
HS 25,127
XS 130
M 5,82,4
LL 17,58
LL 105,59
M 5,83,5
s_addc_u32 s43, s45, 0
LA 17
PK 127,18,125,56
Z 6,12,0
LC_ 105
PK 125,18,125,97
XS 131
M 6,13,1
PO 127
v_subrev_u32 v5, s39, v5
Z 7,24,0
WL 4
LO 104,16,104
LU 16,104
XS 132
M 7,25,1
RA 232,1,17408
HZ 24,16
Z 8,36,0
WL 4
LL 18,60
LL 108,61
M 8,37,1
s_mul_i32 s53, s50, 25600
LA 18
v_add_f32 v14, v14, v15
XS 133
Z 9,48,0
s_add_u32 s40, %[wlo], s53
LC_ 108
v_add_f32_dpp v14, v14, v14 quad_perm:[1,0,3,2] row_mask:0xf bank_mask:0xf
M 9,49,1
s_addc_u32 s41, %[whi], 0
v_add_f32_dpp v14, v14, v14 quad_perm:[2,3,0,1] row_mask:0xf bank_mask:0xf
HX 25
Z 10,60,0
WL 3
LO 105,17,105
LU 17,105
XS 134
M 10,61,1
s_mov_b32 m0, s38
RA 236,1,18432
)"
R"(HS 24,17
Z 11,72,0
LL 16,62
LL 109,63
M 11,73,1
LA 16
W1 25,1024
XS 135
M 11,74,2
LC_ 109
ST 124
M 11,75,3
DW 0
v_add_f32_dpp v14, v14, v14 row_half_mirror row_mask:0xf bank_mask:0xf
v_min_u32 v35, v35, v13
M 10,62,2
WL 4
LO 108,18,108
LU 18,108
M 10,63,3
s_add_u32 s37, s37, 32768
RA 216,1,19456
HS 24,18
M 9,50,2
s_cmp_eq_u32 s37, s75
LL 17,64
LL 112,65
XS 136
M 9,51,3
s_cselect_b32 s37, s74, s37
LA 17
v_cvt_f64_f32 v[14:15], v14
M 8,38,2
s_mul_i32 s53, %[d], 0
LC_ 112
s_mov_b64 exec, s[54:55]
SP
s_mov_b64 exec, -1
M 8,39,3
s_add_u32 s53, s53, s57
v_add_u32 v4, s39, v4
XS 137
M 7,26,2
WL 3
LO 109,16,109
LU 16,109
M 7,27,3
s_add_u32 s46, %[xlo], s53
RA 220,1,20480
HS 24,16
M 6,14,2
LL 18,66
LL 113,67
XS 138
M 6,15,3
s_addc_u32 s47, %[xhi], 0
LA 18
XS 139
M 6,16,4
s_bitcmp1_b64 s[92:93], 48
LC_ 113
XS 140
M 6,17,5
s_cselect_b32 s58, 16, 0
XS 141
XS 142
M 7,28,4
WL 3
LO 112,17,112
LU 17,112
M 7,29,5
s_lshr_b32 s58, 0xda43cb52, s58
RA 224,1,21504
HS 24,17
M 8,40,4
LL 16,68
LL 116,69
M 8,41,5
LA 16
v_lshrrev_b32 v3, 6, v0
M 9,52,4
LC_ 116
v_and_b32 v3, 12, v3
v_bfe_u32 v3, s58, v3, 4
v_mad_u32_u24 v3, v3, v32, v21
M 9,53,5
DW 1024
XS 143
M 10,64,4
WL 3
LO 113,18,113
LU 18,113
M 10,65,5
RA 228,1,22528
HS 24,18
M 11,76,4
LL 17,70
LL 117,71
M 11,77,5
LA 17
XL 12,0
M 11,78,0
LC_ 117
DW 2048
M 11,79,1
XL 13,64
XS 144
M 10,66,0
WL 3
LO 116,16,116
LU 16,116
M 10,67,1
s_mul_i32 s53, s51, 24
RA 232,1,23552
HS 24,16
M 9,54,0
s_bitcmp1_b32 s51, 0
LL 18,72
LL 120,73
M 9,55,1
s_addc_u32 s53, s53, 0
LA 18
s_lshl_b32 s53, s53, 11
s_bitcmp1_b64 s[90:91], s51
s_cselect_b64 s[44:45], s[84:85], s[82:83]
s_add_u32 s44, s44, s53
s_addc_u32 s45, s45, 0
s_add_u32 s51, s51, %[d]
s_cmp_eq_u32 s51, %[wrap]
s_cselect_b32 s51, %[rst], s51
XS 145
M 8,42,0
s_add_u32 s40, s40, 3072
LC_ 120
s_addc_u32 s41, s41, 0
s_add_u32 m0, s38, 3072
M 8,43,1
XL 14,128
XS 146
M 7,30,0
WL 3
LO 117,17,117
LU 17,117
M 7,31,1
RA 236,1,24576
HS 24,17
M 6,18,0
LL 16,74
LL 121,75
XS 147
M 6,19,1
LA 16
DW 0
M 6,20,2
LC_ 121
XL 15,192
M 6,21,3
DW 1024
v_add_u32 v1, s37, v0
XS 148
M 7,32,2
WL 3
LO 120,18,120
LU 18,120
M 7,33,3
RA 216,1,0
HS 24,18
M 8,44,2
LL 17,76
LL 124,77
XS 149
M 8,45,3
LA 17
XL 16,256
M 9,56,2
LC_ 124
DW 2048
XS 150
M 9,57,3
XL 17,320
v_and_b32 v22, 0xfffffcff, v1
M 10,68,2
WL 3
LO 121,16,121
LU 16,121
M 10,69,3
s_lshl_b32 s53, s52, 8
RA 220,1,1024
HS 24,16
XS 151
M 11,80,2
LL 18,78
LL 125,79
M 11,81,3
s_add_u32 s48, %[alo], s53
LA 18
RV 252,22,5120
M 6,22,4
s_addc_u32 s49, %[ahi], 0
LC_ 125
XS 152
M 6,23,5
s_add_u32 s52, s52, %[d]
XS 153
XS 154
M 7,34,4
WL 4
LO 124,17,124
LU 17,124
M 7,35,5
s_cmp_eq_u32 s52, %[wrap]
RA 224,1,2048
HS 24,17
M 8,46,4
s_cselect_b32 s52, %[rst], s52
LL 16,80
LL 106,81
M 8,47,5
LA 16
DQ 3072
M 9,58,4
LC_ 106
XL 18,384
M 9,59,5
s_add_u32 s50, s50, %[d]
XS 155
XS 156
M 10,70,4
WL 3
LO 125,18,125
LU 18,125
M 10,71,5
RA 228,1,3072
HS 24,18
M 11,82,4
LL 17,82
LL 107,83
M 11,83,5
LA 17
HX 24
s_cmp_eq_u32 s50, %[wrap]
s_cselect_b32 s50, %[rst], s50
XL 19,448
Z 2,36,0
WV 39
LC_ 107
XL 20,512
v_add_u32 v19, 1, v19
s_add_u32 s38, s38, 32768
s_cmp_eq_u32 s38, s77
s_cselect_b32 s38, s76, s38
FP
s_sub_u32 s39, 0, s39
M 2,37,1
XL 21,576
XS 157
Z 3,48,0
WL 4
LO 16,16,106
LU 106,16
M 3,49,1
RA 232,1,4096
HZ 25,106
Z 4,60,0
LL 18,84
LL 110,85
XS 158
M 4,61,1
LA 18
PK 106,16,104,56
Z 5,72,0
WL 5
LC_ 110
XL 22,640
PK 104,16,104,97
M 5,73,1
RA 236,1,6144
XS 159
M 5,74,2
WL 5
LO 17,17,107
LU 107,17
M 5,75,3
FR
HS 25,107
M 4,62,2
LL 16,86
LL 111,87
XS 160
M 4,63,3
LA 16
PK 107,17,105,56
M 3,50,2
LC_ 111
XL 23,704
PK 105,17,105,97
XS 161
M 3,51,3
s_add_u32 s42, s44, s60
RA 216,1,7168
XS 162
M 2,38,2
WL 4
LO 18,18,110
LU 110,18
M 2,39,3
s_addc_u32 s43, s45, 0
RA 220,1,8192
HS 25,110
M 2,40,4
LL 17,88
LL 114,89
M 2,41,5
LA 17
PK 110,18,108,56
M 3,52,4
LC_ 114
PK 108,18,108,97
XS 163
M 3,53,5
RA 224,1,9216
PO 110
M 4,64,4
WL 5
LO 16,16,111
LU 111,16
ST 104
XS 164
M 4,65,5
s_add_u32 s42, s44, s61
HS 25,111
v_cmp_gt_u32 vcc, v19, v15
M 5,76,4
WL 3
LL 18,90
LL 115,91
M 5,77,5
s_addc_u32 s43, s45, 0
LA 18
PK 111,16,109,56
XS 165
M 5,78,0
s_cbranch_vccnz .Lk1s10
.Lk1r10:
LC_ 115
PK 109,16,109,97
M 5,79,1
WV 17
RA 228,1,10240
PO 111
BW 12
M 4,66,0
WL 5
LO 17,17,114
LU 114,17
XS 166
M 4,67,1
RA 232,1,11264
RV 40,8,6144
HS 25,114
M 3,54,0
RV 44,8,6208
LL 16,92
LL 118,93
M 3,55,1
LA 16
RV 48,8,6272
PK 114,17,112,56
XS 167
M 2,42,0
WL 6
LC_ 118
BR 12
PK 112,17,112,97
M 2,43,1
WV 15
RV 252,22,5392
ds_read_b64 v[14:15], v5 offset:0
W1 24,0
BW 13
XS 168
M 2,44,2
BR 13
LO 18,18,115
LU 115,18
M 2,45,3
WL 5
RA 236,1,12288
HS 25,115
LZ 40,12
WV 14
BW 14
M 3,56,2
LL 17,94
LL 119,95
XS 169
M 3,57,3
LA 17
RV 40,8,6336
PK 115,18,113,56
)"
R"(M 4,68,2
LC_ 119
BR 14
PK 113,18,113,97
M 4,69,3
WL 6
RA 216,1,13312
v_subrev_u32 v5, s39, v5
XS 170
LS 44,13
WV 12
BW 15
M 5,80,2
WL 7
LO 16,16,118
LU 118,16
ST 108
M 5,81,3
s_add_u32 s42, s44, s62
RA 220,1,14336
HS 25,118
s_addc_u32 s43, s45, 0
RV 44,8,6400
M 2,46,4
BR 15
LL 18,96
LL 122,97
XS 171
M 2,47,5
WL 5
LA 18
PK 118,16,116,56
LS 48,14
WV 11
BW 16
M 3,58,4
LC_ 122
PK 116,16,116,97
XS 172
M 3,59,5
RA 224,1,15360
RV 48,8,6464
PO 118
M 4,70,4
LO 17,17,119
LU 119,17
ST 112
M 4,71,5
s_add_u32 s42, s44, s63
BR 16
HS 25,119
XS 173
M 5,82,4
WL 6
LL 16,98
LL 123,99
M 5,83,5
s_addc_u32 s43, s45, 0
LA 16
PK 119,17,117,56
LS 40,15
WV 10
BW 17
Z 8,36,0
LC_ 123
PK 117,17,117,97
v_add_f32 v14, v14, v15
s_nop 1
v_add_f32_dpp v14, v14, v14 quad_perm:[1,0,3,2] row_mask:0xf bank_mask:0xf
s_nop 1
v_add_f32_dpp v14, v14, v14 quad_perm:[2,3,0,1] row_mask:0xf bank_mask:0xf
s_nop 1
v_add_f32_dpp v14, v14, v14 row_half_mirror row_mask:0xf bank_mask:0xf
v_cvt_f64_f32 v[14:15], v14
s_mov_b64 exec, s[54:55]
SP
s_mov_b64 exec, -1
RV 40,8,6528
XS 174
M 8,37,1
WV 9
RA 228,1,16384
BR 17
PO 119
Z 9,48,0
WL 6
LO 18,18,122
LU 122,18
XS 175
M 9,49,1
s_mul_i32 s53, %[d], 196608
RA 232,1,17408
HS 25,122
LS 44,16
s_add_u32 s53, s53, s57
BW 18
Z 10,60,0
s_add_u32 s46, %[xlo], s53
RV 44,8,6592
LL 17,100
LL 126,101
M 10,61,1
s_addc_u32 s47, %[xhi], 0
LA 17
BR 18
PK 122,18,120,56
Z 0,72,0
WL 5
LC_ 126
PK 120,18,120,97
M 0,73,1
s_bitcmp1_b64 s[92:93], 48
RA 236,1,18432
LS 48,17
s_cselect_b32 s58, 16, 0
WV 8
BW 19
M 0,74,2
RV 48,8,6656
LO 16,16,123
LU 123,16
M 0,75,3
s_lshr_b32 s58, 0xfe987610, s58
BR 19
HS 25,123
v_lshrrev_b32 v3, 6, v0
v_and_b32 v3, 12, v3
v_bfe_u32 v3, s58, v3, 4
v_mad_u32_u24 v3, v3, v32, v21
M 10,62,2
s_mul_i32 s53, s50, 25600
LL 18,102
LL 127,103
s_add_u32 s40, %[wlo], s53
s_addc_u32 s41, %[whi], 0
s_mov_b32 m0, s38
M 10,63,3
WL 5
LA 18
PK 123,16,121,56
LS 40,18
WV 7
BW 20
M 9,50,2
LC_ 127
RV 40,8,6720
PK 121,16,121,97
M 9,51,3
WV 6
RA 216,1,19456
BR 20
DW 0
M 8,38,2
WL 6
LO 17,17,126
LU 126,17
ST 116
M 8,39,3
s_add_u32 s42, s44, s64
RA 220,1,20480
HS 25,126
s_addc_u32 s43, s45, 0
LS 44,19
s_add_u32 s37, s37, 32768
BW 21
M 8,40,4
s_cmp_eq_u32 s37, s75
LL 16,64
LL 112,65
s_cselect_b32 s37, s74, s37
M 8,41,5
LA 16
XL 24,0
DW 1024
RV 44,8,6784
PK 126,17,124,56
M 9,52,4
LC_ 112
BR 21
PK 124,17,124,97
M 9,53,5
WL 6
RA 224,1,21504
PO 126
LS 48,20
WV 9
BW 22
M 10,64,4
LO 18,18,127
LU 127,18
ST 120
M 10,65,5
WV 9
DW 2048
RV 48,8,6848
HS 25,127
M 0,76,4
WL 8
RA 240,7,0
LL 17,66
LL 113,67
M 0,77,5
s_add_u32 s40, s40, 3072
LA 17
BR 22
PK 127,18,125,56
M 0,78,0
s_addc_u32 s41, s41, 0
LC_ 113
PK 125,18,125,97
s_add_u32 m0, s38, 3072
M 0,79,1
WL 7
RA 228,1,22528
XL 25,64
PO 127
LS 40,21
s_add_u32 s42, s44, s65
BW 23
M 10,66,0
RA 244,7,256
LO 112,16,112
LU 16,112
M 10,67,1
s_addc_u32 s43, s45, 0
RA 232,1,23552
BR 23
HZ 24,16
M 9,54,0
WL 6
DW 0
LL 18,68
LL 116,69
M 9,55,1
s_lshl_b32 s53, s52, 8
LA 18
HX 25
LS 44,22
s_add_u32 s48, %[alo], s53
RA 248,7,512
M 8,42,0
WL 2
LC_ 116
W1 25,1024
M 8,43,1
s_addc_u32 s49, %[ahi], 0
RA 236,1,24576
v_add_u32 v4, s39, v4
LS 48,23
s_add_u32 s52, s52, %[d]
RA 252,7,768
M 8,44,2
LO 113,17,113
LU 17,113
s_cmp_eq_u32 s52, %[wrap]
s_cselect_b32 s52, %[rst], s52
DW 1024
v_mul_f32 v36, 0xbb2aaaab, v36
M 8,45,3
XL 26,128
HS 24,17
v_mul_f32 v37, 0xbb2aaaab, v37
M 9,56,2
LL 16,70
LL 117,71
v_mul_f32 v38, 0xbb2aaaab, v38
v_mul_f32 v39, 0xbb2aaaab, v39
M 9,57,3
LA 16
DW 2048
LK 40,240,12,0
LK 44,242,12,2
RA 240,7,1024
v_add_u32 v1, s37, v0
LK 48,244,13,0
s_nop 0
CF 176,40,41
LK 248,246,13,2
RA 244,7,1280
CF 177,42,43
M 10,68,2
LC_ 117
RA 216,1,0
M 10,69,3
ST 124
v_and_b32 v22, 0xfffffcff, v1
M 0,80,2
WL 6
LO 116,18,116
LU 18,116
M 0,81,3
s_mul_i32 s53, s51, 24
RA 220,1,1024
HS 24,18
s_bitcmp1_b32 s51, 0
s_addc_u32 s53, s53, 0
s_lshl_b32 s53, s53, 11
s_bitcmp1_b64 s[90:91], s51
s_cselect_b64 s[44:45], s[84:85], s[82:83]
s_add_u32 s44, s44, s53
s_addc_u32 s45, s45, 0
s_add_u32 s51, s51, %[d]
s_cmp_eq_u32 s51, %[wrap]
s_cselect_b32 s51, %[rst], s51
DQ 3072
CF 178,44,45
M 8,46,4
LL 17,72
LL 120,73
CF 179,46,47
M 8,47,5
LA 17
RV 252,22,5120
LK 40,248,14,0
CF 180,48,49
LK 44,250,14,2
RA 248,7,1536
CF 181,50,51
GZ 12
WL 9
CF 182,248,249
CF 183,250,251
LK 48,252,15,0
CF 184,40,41
LK 248,254,15,2
RA 252,7,1792
CF 185,42,43
GS 13
WL 8
CF 186,44,45
CF 187,46,47
LK 40,240,16,0
CF 188,48,49
LK 44,242,16,2
RA 240,7,2048
CF 189,50,51
GS 14
WL 8
CF 190,248,249
CF 191,250,251
LK 48,244,17,0
CF 192,40,41
LK 248,246,17,2
RA 244,7,2304
CF 193,42,43
GS 15
CF 194,44,45
CF 195,46,47
M 9,58,4
WL 3
LC_ 120
XL 27,192
M 9,59,5
s_add_u32 s50, s50, %[d]
RA 224,1,2048
s_cmp_eq_u32 s50, %[wrap]
s_cselect_b32 s50, %[rst], s50
v_add_u32 v19, 1, v19
LK 40,248,18,0
WL 4
CF 196,48,49
LK 44,250,18,2
s_add_u32 s38, s38, 32768
RA 248,7,2560
CF 197,50,51
GS 16
s_cmp_eq_u32 s38, s77
CF 198,248,249
CF 199,250,251
LK 48,252,19,0
CF 200,40,41
LK 248,254,19,2
s_cselect_b32 s38, s76, s38
RA 252,7,2816
CF 201,42,43
GS 17
WL 5
CF 202,44,45
CF 203,46,47
LK 40,240,20,0
CF 204,48,49
LK 44,242,20,2
CF 205,50,51
GS 18
CF 206,248,249
CF 207,250,251
M 10,70,4
LO 117,16,117
LU 16,117
M 10,71,5
FP
HS 24,16
M 0,82,4
LL 18,74
LL 121,75
M 0,83,5
s_sub_u32 s39, 0, s39
LA 18
RA 228,1,3072
Z 2,36,0
)"
R"(LC_ 121
XL 28,256
M 2,37,1
WL 8
LK 48,244,21,0
CF 208,40,41
LK 248,246,21,2
CF 209,42,43
GS 19
CF 210,44,45
CF 211,46,47
Z 3,48,0
WL 7
LO 120,17,120
LU 17,120
M 3,49,1
WL 5
RA 232,1,4096
HS 24,17
LK 40,248,22,0
CF 212,48,49
LK 44,250,22,2
CF 213,50,51
GS 20
CF 214,248,249
CF 215,250,251
Z 4,60,0
LL 16,76
LL 124,77
M 4,61,1
LA 16
FR
Z 6,72,0
WL 4
LC_ 124
XL 29,320
M 6,73,1
RA 236,1,6144
LK 48,252,23,0
CF 216,40,41
LK 248,254,23,2
CF 217,42,43
GS 21
CF 218,44,45
CF 219,46,47
M 6,74,2
WL 5
LO 121,18,121
LU 18,121
M 6,75,3
RV 36,8,6912
HS 24,18
M 4,62,2
LL 17,78
LL 125,79
M 4,63,3
LA 17
RA 216,1,7168
GS 22
M 3,50,2
WL 5
LC_ 125
v_cmp_gt_u32 vcc, v19, v15
M 3,51,3
XL 30,384
CF 220,48,49
M 2,38,2
WL 4
LO 124,16,124
LU 16,124
M 2,39,3
RA 220,1,8192
HS 24,16
M 2,40,4
s_cbranch_vccnz .Lk1s11
.Lk1r11:
LL 18,88
LL 114,89
M 2,41,5
LA 18
ds_read_b64 v[14:15], v5 offset:0
M 3,52,4
LC_ 114
v_subrev_u32 v5, s39, v5
M 3,53,5
RA 224,1,9216
CF 221,50,51
M 4,64,4
WL 5
LO 125,17,125
LU 17,125
M 4,65,5
XL 31,448
HS 24,17
M 6,76,4
WL 4
LL 16,90
LL 115,91
M 6,77,5
LA 16
RA 228,1,10240
M 6,78,0
LC_ 115
HX 24
M 6,79,1
W1 24,0
CF 222,248,249
M 4,66,0
WL 5
LO 18,18,114
LU 114,18
M 4,67,1
RA 232,1,11264
HZ 25,114
M 3,54,0
LL 17,92
LL 118,93
M 3,55,1
LA 17
PK 114,18,112,56
M 2,42,0
WL 4
LC_ 118
PK 112,18,112,97
M 2,43,1
RV 252,22,5392
v_add_f32 v14, v14, v15
M 2,44,2
WL 5
LO 16,16,115
LU 115,16
M 2,45,3
RA 236,1,12288
HS 25,115
M 3,56,2
LL 18,94
LL 119,95
M 3,57,3
LA 18
PK 115,16,113,56
M 4,68,2
LC_ 119
XL 32,512
PK 113,16,113,97
v_add_f32_dpp v14, v14, v14 quad_perm:[1,0,3,2] row_mask:0xf bank_mask:0xf
M 4,69,3
s_add_u32 s42, s44, s62
RA 216,1,13312
v_add_f32_dpp v14, v14, v14 quad_perm:[2,3,0,1] row_mask:0xf bank_mask:0xf
M 6,80,2
WL 3
LO 17,17,118
LU 118,17
CF 223,250,251
M 6,81,3
s_addc_u32 s43, s45, 0
RA 220,1,14336
HS 25,118
GS 23
ST 112
v_add_f32_dpp v14, v14, v14 row_half_mirror row_mask:0xf bank_mask:0xf
s_nop 5
v_cndmask_b32 v52, v52, v53, s[78:79]
M 2,46,4
LL 16,96
LL 122,97
v_cndmask_b32 v54, v54, v55, s[78:79]
M 2,47,5
LA 16
PK 118,17,116,56
v_cndmask_b32 v52, v52, v54, s[80:81]
M 3,58,4
LC_ 122
PK 116,17,116,97
v_cvt_f64_f32 v[14:15], v14
v_mul_f32 v52, 0x3b2aaaab, v52
M 3,59,5
RA 224,1,15360
PO 118
v_med3_f32 v53, v52, v39, v37
M 4,70,4
WL 5
LO 18,18,119
LU 119,18
s_mov_b64 exec, s[54:55]
SP
s_mov_b64 exec, -1
XL 33,576
LG 52,53
s_nop 0
C 52,52,52
M 4,71,5
s_add_u32 s42, s44, s63
HS 25,119
v_lshlrev_b32 v52, 16, v52
M 6,82,4
WL 3
LL 17,98
LL 123,99
v_add_f32 v52, v36, v52
M 6,83,5
s_addc_u32 s43, s45, 0
LA 17
PK 119,18,117,56
C 52,52,52
Z 8,36,0
LC_ 123
PK 117,18,117,97
v_lshlrev_b32 v52, 16, v52
M 8,37,1
RA 228,1,16384
PO 119
v_sqrt_f32 v52, v52
s_nop 0
C 52,52,52
Z 9,48,0
WL 4
LO 16,16,122
LU 122,16
v_lshlrev_b32 v52, 16, v52
M 9,49,1
RA 232,1,17408
HS 25,122
v_rcp_f32 v52, v52
Z 10,60,0
XL 34,640
LL 18,56
LL 126,57
v_mul_f32 v52, v38, v52
M 10,61,1
LA 18
PK 122,16,120,56
CF 52,52,52
Z 11,72,0
WL 2
LC_ 126
PK 120,16,120,97
ds_bpermute_b32 v13, v28, v52
M 11,73,1
RA 236,1,18432
M 11,74,2
LO 17,17,123
LU 123,17
M 11,75,3
ST 116
HS 25,123
M 10,62,2
s_mul_i32 s53, s50, 25600
LL 16,58
LL 127,59
s_add_u32 s40, %[wlo], s53
s_addc_u32 s41, %[whi], 0
s_mov_b32 m0, s38
M 10,63,3
LA 16
PK 123,17,121,56
M 9,50,2
LC_ 127
PK 121,17,121,97
M 9,51,3
RA 216,1,19456
XL 35,704
DW 0
M 8,38,2
WL 3
LO 18,18,126
LU 126,18
M 8,39,3
s_add_u32 s42, s44, s64
RA 220,1,20480
HS 25,126
s_addc_u32 s43, s45, 0
XS 176
M 8,40,4
s_add_u32 s37, s37, 32768
LL 17,64
LL 112,65
s_cmp_eq_u32 s37, s75
s_cselect_b32 s37, s74, s37
M 8,41,5
LA 17
DW 1024
PK 126,18,124,56
XS 177
M 9,52,4
LC_ 112
PK 124,18,124,97
M 9,53,5
RA 224,1,21504
PO 126
XS 178
M 10,64,4
WL 5
LO 16,16,127
LU 127,16
ST 120
M 10,65,5
DW 2048
HS 25,127
XS 179
M 11,76,4
WL 3
LL 18,66
LL 113,67
M 11,77,5
s_add_u32 s40, s40, 3072
LA 18
PK 127,16,125,56
XS 180
M 11,78,0
s_addc_u32 s41, s41, 0
LC_ 113
PK 125,16,125,97
s_add_u32 m0, s38, 3072
M 11,79,1
s_add_u32 s42, s44, s65
RA 228,1,22528
PO 127
XS 181
M 10,66,0
WL 4
LO 112,17,112
LU 17,112
M 10,67,1
s_addc_u32 s43, s45, 0
RA 232,1,23552
HZ 24,17
XS 182
M 9,54,0
DW 0
LL 16,68
LL 116,69
M 9,55,1
s_lshl_b32 s53, s52, 8
LA 16
HX 25
XS 183
M 8,42,0
WL 2
LC_ 116
W1 25,1024
M 8,43,1
s_add_u32 s48, %[alo], s53
RA 236,1,24576
v_add_u32 v4, s39, v4
v_min_u32 v35, v35, v13
M 8,44,2
LO 113,18,113
LU 18,113
s_addc_u32 s49, %[ahi], 0
s_add_u32 s52, s52, %[d]
s_cmp_eq_u32 s52, %[wrap]
s_cselect_b32 s52, %[rst], s52
DW 1024
M 8,45,3
ST 124
HS 24,18
s_bitcmp1_b64 s[92:93], 48
s_cselect_b32 s58, 16, 0
s_lshr_b32 s58, 0xda43cb52, s58
v_and_b32 v11, 0x1f0, v0
v_min_u32 v11, 368, v11
v_lshlrev_b32 v11, 3, v11
XS 184
M 9,56,2
s_mul_i32 s53, s51, 24
LL 17,70
LL 117,71
s_bitcmp1_b32 s51, 0
s_addc_u32 s53, s53, 0
s_lshl_b32 s53, s53, 11
s_bitcmp1_b64 s[90:91], s51
s_cselect_b64 s[44:45], s[84:85], s[82:83]
s_add_u32 s44, s44, s53
s_addc_u32 s45, s45, 0
s_add_u32 s51, s51, %[d]
s_cmp_eq_u32 s51, %[wrap]
s_cselect_b32 s51, %[rst], s51
M 9,57,3
LA 17
DW 2048
XS 185
M 10,68,2
LC_ 117
DQ 3072
M 10,69,3
s_add_u32 s50, s50, %[d]
v_add_u32 v1, s37, v0
XS 186
M 11,80,2
WL 2
LO 116,16,116
LU 16,116
M 11,81,3
s_cmp_eq_u32 s50, %[wrap]
RA 216,1,0
HS 24,16
XS 187
M 8,46,4
s_cselect_b32 s50, %[rst], s50
)"
R"(LL 18,72
LL 120,73
M 8,47,5
LA 18
v_and_b32 v22, 0xfffffcff, v1
XS 188
M 9,58,4
LC_ 120
RA 220,1,1024
M 9,59,5
RV 252,22,5120
v_add_u32 v19, 1, v19
XS 189
M 10,70,4
WL 5
LO 117,17,117
LU 17,117
M 10,71,5
s_add_u32 s38, s38, 32768
RA 224,1,2048
HS 24,17
XS 190
M 11,82,4
WL 1
LL 16,74
LL 121,75
XS 191
M 11,83,5
s_cmp_eq_u32 s38, s77
LA 16
RA 228,1,3072
s_cselect_b32 s38, s76, s38
FP
s_sub_u32 s39, 0, s39
Z 0,0,0
LC_ 121
XS 192
M 0,1,1
XS 193
XS 194
Z 1,12,0
LO 120,18,120
LU 18,120
M 1,13,1
RA 232,1,4096
HS 24,18
Z 2,36,0
LL 17,80
LL 124,81
M 2,37,1
LA 17
XS 195
Z 3,48,0
LC_ 124
XS 196
M 3,49,1
XS 197
XS 198
Z 4,60,0
WL 3
LO 121,16,121
LU 16,121
M 4,61,1
RA 236,1,6144
HS 24,16
Z 5,72,0
LL 18,82
LL 125,83
M 5,73,1
LA 18
XS 199
M 5,74,2
LC_ 125
XS 200
M 5,75,3
XS 201
XS 202
M 4,62,2
WL 3
LO 124,17,124
LU 17,124
M 4,63,3
RA 216,1,7168
HS 24,17
M 3,50,2
LL 16,88
LL 114,89
M 3,51,3
LA 16
FR
M 2,38,2
LC_ 114
v_lshrrev_b32 v3, 9, v0
M 2,39,3
WV 31
BW 24
M 1,14,2
WL 5
LO 125,18,125
LU 18,125
M 1,15,3
RA 220,1,8192
HS 24,18
M 0,2,2
LL 17,90
LL 115,91
XS 203
M 0,3,3
LA 17
RV 40,8,6144
M 0,4,4
LC_ 115
RV 44,8,6208
XS 204
M 0,5,5
RV 48,8,6272
BR 24
M 1,16,4
WL 8
LO 16,16,114
LU 114,16
XS 205
M 1,17,5
RA 224,1,9216
HZ 25,114
M 2,40,4
LL 18,92
LL 118,93
XS 206
M 2,41,5
LA 18
PK 114,16,112,56
XS 207
M 3,52,4
LC_ 118
PK 112,16,112,97
v_lshl_add_u32 v3, v3, 2, 0
v_bfe_u32 v3, s58, v3, 4
v_mad_u32_u24 v3, v3, v32, v11
M 3,53,5
s_mov_b32 m0, %[pm]
s_nop 0
DF
v_cmp_gt_u32 vcc, v19, v15
XS 208
M 4,64,4
WL 6
LO 17,17,115
LU 115,17
M 4,65,5
s_add_u32 s42, s44, s62
RA 228,1,10240
HS 25,115
XS 209
M 5,76,4
LL 16,94
LL 119,95
M 5,77,5
s_cbranch_vccnz .Lk1s12
.Lk1r12:
LA 16
PK 115,17,113,56
XS 210
M 5,78,0
s_addc_u32 s43, s45, 0
LC_ 119
PK 113,17,113,97
M 5,79,1
ST 112
HX 24
XS 211
M 4,66,0
WL 3
LO 18,18,118
LU 118,18
M 4,67,1
RA 232,1,11264
HS 25,118
XS 212
M 3,54,0
LL 17,96
LL 122,97
M 3,55,1
LA 17
PK 118,18,116,56
XS 213
M 2,42,0
LC_ 122
PK 116,18,116,97
M 2,43,1
RV 252,22,5392
PO 118
XS 214
M 1,18,0
WL 4
LO 16,16,119
LU 119,16
M 1,19,1
s_add_u32 s42, s44, s63
RA 236,1,12288
HS 25,119
XS 215
M 0,6,0
LL 18,98
LL 123,99
M 0,7,1
s_addc_u32 s43, s45, 0
LA 18
PK 119,16,117,56
XS 216
M 0,8,2
LC_ 123
PK 117,16,117,97
M 0,9,3
W1 24,0
PO 119
XS 217
M 1,20,2
WL 5
LO 17,17,122
LU 122,17
M 1,21,3
RA 216,1,13312
HS 25,122
XS 218
M 2,44,2
LL 16,100
LL 126,101
M 2,45,3
LA 16
PK 122,17,120,56
XS 219
M 3,56,2
LC_ 126
PK 120,17,120,97
M 3,57,3
WV 29
ST 116
XS 220
BW 25
M 4,68,2
WL 5
LO 18,18,123
LU 123,18
M 4,69,3
RA 220,1,14336
BR 25
HS 25,123
XS 221
LZ 40,24
WV 27
BW 26
M 5,80,2
LL 17,102
LL 127,103
M 5,81,3
LA 17
PK 123,18,121,56
XS 222
M 0,10,4
LC_ 127
PK 121,18,121,97
XS 223
M 0,11,5
WV 23
DY
RV 40,8,6336
M 1,22,4
WL 4
BR 26
LO 16,16,126
LU 126,16
M 1,23,5
s_add_u32 s42, s44, s64
RA 224,1,15360
HS 25,126
LS 44,25
s_addc_u32 s43, s45, 0
BW 27
M 2,46,4
LL 18,56
LL 104,57
M 2,47,5
LA 18
PK 126,16,124,56
M 3,58,4
LC_ 104
RV 44,8,6400
PK 124,16,124,97
M 3,59,5
WV 23
ST 120
BR 27
PO 126
M 4,70,4
WL 6
LO 17,17,127
LU 127,17
M 4,71,5
s_add_u32 s42, s44, s65
RA 228,1,16384
HS 25,127
LS 48,26
s_addc_u32 s43, s45, 0
BW 28
M 5,82,4
LL 16,58
LL 105,59
M 5,83,5
LA 16
RV 48,8,6464
PK 127,17,125,56
Z 6,0,0
LC_ 105
PK 125,17,125,97
M 6,1,1
ds_read_b64 v[14:15], v5 offset:0
BR 28
PO 127
Z 7,12,0
WL 9
LO 104,18,104
LU 18,104
M 7,13,1
WL 7
RA 232,1,17408
HZ 26,18
LS 40,27
WV 23
BW 29
Z 8,36,0
WV 22
LL 17,60
LL 108,61
M 8,37,1
WL 2
LA 17
ST 124
RV 40,8,6528
Z 9,48,0
s_mul_i32 s53, s50, 25600
LC_ 108
BR 29
M 9,49,1
s_add_u32 s40, %[wlo], s53
v_subrev_u32 v5, s39, v5
HX 25
LS 44,28
s_addc_u32 s41, %[whi], 0
BW 30
Z 10,60,0
LO 105,16,105
LU 16,105
M 10,61,1
s_mov_b32 m0, s38
RA 236,1,18432
HS 26,16
Z 11,72,0
RV 44,8,6592
LL 18,62
LL 109,63
M 11,73,1
LA 18
W1 25,1024
BR 30
M 11,74,2
LC_ 109
DW 0
M 11,75,3
WL 7
v_add_f32 v14, v14, v15
v_add_u32 v4, s39, v4
LS 48,29
WV 23
BW 31
M 10,62,2
LO 108,17,108
LU 17,108
M 10,63,3
s_add_u32 s37, s37, 32768
RA 216,1,19456
HS 26,17
M 9,50,2
s_cmp_eq_u32 s37, s75
RV 48,8,6656
LL 16,64
LL 112,65
M 9,51,3
s_cselect_b32 s37, s74, s37
LA 16
DW 1024
BR 31
M 8,38,2
LC_ 112
v_add_f32_dpp v14, v14, v14 quad_perm:[1,0,3,2] row_mask:0xf bank_mask:0xf
M 8,39,3
WL 7
v_add_f32_dpp v14, v14, v14 quad_perm:[2,3,0,1] row_mask:0xf bank_mask:0xf
v_lshrrev_b32 v3, 9, v0
LS 40,30
)"
R"(WV 23
BW 32
M 7,14,2
WL 7
LO 109,18,109
LU 18,109
M 7,15,3
RA 220,1,20480
RV 40,8,6720
HS 26,18
M 6,2,2
BR 32
LL 17,66
LL 113,67
M 6,3,3
LA 17
v_add_f32_dpp v14, v14, v14 row_half_mirror row_mask:0xf bank_mask:0xf
M 6,4,4
WL 6
LC_ 113
v_cvt_f64_f32 v[14:15], v14
M 6,5,5
s_mov_b64 exec, s[54:55]
SP
s_mov_b64 exec, -1
HX 26
LS 44,31
WV 21
BW 33
M 7,16,4
WL 7
RV 44,8,6784
LO 112,16,112
LU 16,112
M 7,17,5
RA 224,1,21504
HZ 24,16
M 8,40,4
BR 33
LL 18,68
LL 116,69
M 8,41,5
WL 6
LA 18
v_lshl_add_u32 v3, v3, 2, 8
LS 48,32
WV 20
BW 34
M 9,52,4
LC_ 116
RV 48,8,6848
M 9,53,5
RA 240,7,0
BR 34
M 10,64,4
WL 10
LO 113,17,113
LU 17,113
M 10,65,5
RA 228,1,22528
HS 24,17
v_bfe_u32 v3, s58, v3, 4
v_mad_u32_u24 v3, v3, v32, v11
M 11,76,4
WV 18
LL 16,70
LL 117,71
M 11,77,5
WL 7
LA 16
DW 2048
M 11,78,0
s_mul_i32 s53, s51, 24
LC_ 117
M 11,79,1
s_bitcmp1_b32 s51, 0
LS 40,33
s_addc_u32 s53, s53, 0
BW 35
M 10,66,0
WL 4
RA 244,7,256
LO 116,18,116
LU 18,116
M 10,67,1
s_lshl_b32 s53, s53, 11
RA 232,1,23552
HS 24,18
M 9,54,0
s_bitcmp1_b64 s[90:91], s51
BR 35
LL 17,72
LL 120,73
M 9,55,1
s_cselect_b64 s[44:45], s[84:85], s[82:83]
LA 17
s_add_u32 s44, s44, s53
s_addc_u32 s45, s45, 0
s_bitcmp1_b64 s[92:93], s51
s_cselect_b64 s[94:95], s[88:89], s[86:87]
s_add_u32 s94, s94, s53
s_addc_u32 s95, s95, 0
s_add_u32 s51, s51, %[d]
s_cmp_eq_u32 s51, %[wrap]
s_cselect_b32 s51, %[rst], s51
s_mul_i32 s53, %[d], 0
s_add_u32 s60, s53, %[sb1]
s_add_u32 s61, s60, 512
LS 44,34
s_add_u32 s40, s40, 3072
RA 248,7,512
M 8,42,0
s_addc_u32 s41, s41, 0
LC_ 120
s_add_u32 m0, s38, 3072
M 8,43,1
DW 0
M 7,18,0
WL 7
LO 117,16,117
LU 16,117
M 7,19,1
WL 3
RA 236,1,24576
HS 24,16
LS 48,35
RA 252,7,768
M 6,6,0
LL 18,74
LL 121,75
s_nop 3
v_mul_f32 v36, 0xbb2aaaab, v36
M 6,7,1
LA 18
DW 1024
v_mul_f32 v37, 0xbb2aaaab, v37
v_mul_f32 v38, 0xbb2aaaab, v38
M 6,8,2
LC_ 121
DW 2048
v_mul_f32 v39, 0xbb2aaaab, v39
M 6,9,3
s_lshl_b32 s53, s52, 8
v_add_u32 v1, s37, v0
LK 40,240,24,0
LK 44,242,24,2
s_add_u32 s48, %[alo], s53
RA 240,7,1024
LK 48,244,25,0
s_nop 0
CF 224,40,41
LK 248,246,25,2
s_addc_u32 s49, %[ahi], 0
RA 244,7,1280
CF 225,42,43
M 7,20,2
WL 6
LO 120,17,120
LU 17,120
M 7,21,3
s_add_u32 s52, s52, %[d]
RA 216,1,0
HS 24,17
M 8,44,2
s_cmp_eq_u32 s52, %[wrap]
LL 16,76
LL 124,77
CF 226,44,45
M 8,45,3
s_cselect_b32 s52, %[rst], s52
LA 16
DQ 3072
CF 227,46,47
LK 40,248,26,0
CF 228,48,49
LK 44,250,26,2
RA 248,7,1536
CF 229,50,51
GZ 24
WL 7
CF 230,248,249
CF 231,250,251
LK 48,252,27,0
CF 232,40,41
LK 248,254,27,2
RA 252,7,1792
CF 233,42,43
GS 25
WL 5
CF 234,44,45
CF 235,46,47
LK 40,240,28,0
CF 236,48,49
LK 44,242,28,2
RA 240,7,2048
CF 237,50,51
GS 26
WL 5
CF 238,248,249
CF 239,250,251
LK 48,244,29,0
CF 240,40,41
LK 248,246,29,2
RA 244,7,2304
CF 241,42,43
GS 27
CF 242,44,45
CF 243,46,47
M 9,56,2
LC_ 124
v_and_b32 v22, 0xfffffcff, v1
M 9,57,3
WL 4
LK 40,248,30,0
CF 244,48,49
LK 44,250,30,2
RA 248,7,2560
CF 245,50,51
GS 28
WL 4
CF 246,248,249
CF 247,250,251
LK 48,252,31,0
CF 40,40,41
LK 248,254,31,2
RA 252,7,2816
CF 42,42,43
GS 29
CF 44,44,45
CF 46,46,47
M 10,68,2
LO 121,18,121
LU 18,121
M 10,69,3
RA 220,1,1024
HS 24,18
M 11,80,2
LL 17,78
LL 125,79
M 11,81,3
LA 17
RV 252,22,5120
M 6,10,4
LC_ 125
DY
M 6,11,5
s_add_u32 s50, s50, %[d]
AW 0,40
AW 1,42
M 7,22,4
WL 6
LO 124,16,124
LU 16,124
M 7,23,5
s_cmp_eq_u32 s50, %[wrap]
RA 224,1,2048
HS 24,16
M 8,46,4
s_cselect_b32 s50, %[rst], s50
LL 18,80
LL 106,81
M 8,47,5
LA 18
v_add_u32 v19, 1, v19
M 9,58,4
s_add_u32 s38, s38, 32768
LC_ 106
AW 2,44
M 9,59,5
AW 3,46
LK 40,240,32,0
CF 48,48,49
LK 44,242,32,2
CF 50,50,51
GS 30
CF 248,248,249
CF 250,250,251
M 10,70,4
WL 3
LO 125,17,125
LU 17,125
M 10,71,5
RA 228,1,3072
HS 24,17
M 11,82,4
LL 16,82
LL 107,83
M 11,83,5
LA 16
HX 24
Z 0,0,0
s_cmp_eq_u32 s38, s77
LC_ 107
AW 4,48
M 0,1,1
s_cselect_b32 s38, s76, s38
FP
AW 5,50
Z 1,12,0
WL 4
LO 18,18,106
LU 106,18
M 1,13,1
s_sub_u32 s39, 0, s39
RA 232,1,4096
HZ 27,106
Z 2,36,0
LL 17,84
LL 110,85
M 2,37,1
LA 17
PK 106,18,104,56
Z 3,48,0
LC_ 110
PK 104,18,104,97
M 3,49,1
AW 6,248
AW 7,250
LK 48,244,33,0
CF 40,40,41
LK 248,246,33,2
CF 42,42,43
GS 31
CF 44,44,45
CF 46,46,47
Z 4,60,0
WL 4
LO 16,16,107
LU 107,16
M 4,61,1
s_add_u32 s42, s94, s60
RA 236,1,6144
HS 27,107
Z 5,72,0
LL 18,86
LL 111,87
M 5,73,1
s_addc_u32 s43, s95, 0
LA 18
PK 107,16,105,56
M 5,74,2
LC_ 111
PK 105,16,105,97
M 5,75,3
SU 104
AW 8,40
M 4,62,2
WL 3
LO 17,17,110
LU 110,17
M 4,63,3
RA 216,1,7168
HS 27,110
M 3,50,2
LL 16,88
LL 114,89
M 3,51,3
LA 16
PK 110,17,108,56
AW 9,42
AW 10,44
M 2,38,2
LC_ 114
PK 108,17,108,97
AW 11,46
M 2,39,3
FR
PO 110
LK 40,248,34,0
CF 48,48,49
LK 44,250,34,2
CF 50,50,51
GS 32
CF 248,248,249
CF 250,250,251
M 1,14,2
WL 4
LO 18,18,111
LU 111,18
M 1,15,3
s_add_u32 s42, s94, s61
RA 220,1,8192
HS 27,111
M 0,2,2
LL 17,90
LL 115,91
AW 12,48
AW 13,50
M 0,3,3
s_addc_u32 s43, s95, 0
LA 17
PK 111,18,109,56
AW 14,248
AW 15,250
LK 48,252,35,0
CF 40,40,41
LK 248,254,35,2
s_mov_b32 m0, %[pm]
CF 42,42,43
HX 27
)"
R"(GS 33
CF 44,44,45
CF 46,46,47
M 0,4,4
LC_ 115
PK 109,18,109,97
M 0,5,5
DF
PO 111
M 1,16,4
WL 4
LO 16,16,114
LU 114,16
M 1,17,5
RA 224,1,9216
HZ 25,114
AW 16,40
M 2,40,4
LL 18,92
LL 118,93
AW 17,42
AW 18,44
M 2,41,5
LA 18
RV 36,8,6912
PK 114,16,112,56
AW 19,46
M 3,52,4
LC_ 118
PK 112,16,112,97
M 3,53,5
WL 7
SU 108
v_cmp_gt_u32 vcc, v19, v15
GS 34
CF 48,48,49
CF 50,50,51
CF 248,248,249
M 4,64,4
WL 4
LO 17,17,115
LU 115,17
CF 250,250,251
AW 20,48
M 4,65,5
s_add_u32 s42, s44, s62
RA 228,1,10240
HS 25,115
AW 21,50
AW 22,248
M 5,76,4
LL 16,94
LL 119,95
AW 23,250
M 5,77,5
s_cbranch_vccnz .Lk1s13
.Lk1r13:
LA 16
PK 115,17,113,56
GS 35
s_addc_u32 s43, s45, 0
W1 24,0
s_nop 5
v_cndmask_b32 v52, v52, v53, s[78:79]
M 5,78,0
LC_ 119
PK 113,17,113,97
v_cndmask_b32 v54, v54, v55, s[78:79]
M 5,79,1
W1 26,2048
v_cndmask_b32 v52, v52, v54, s[80:81]
M 4,66,0
WL 5
LO 18,18,118
LU 118,18
v_mul_f32 v52, 0x3b2aaaab, v52
M 4,67,1
RA 232,1,11264
HS 25,118
v_med3_f32 v53, v52, v39, v37
M 3,54,0
LL 17,96
LL 122,97
LG 52,53
M 3,55,1
LA 17
PK 118,18,116,56
C 52,52,52
M 2,42,0
LC_ 122
PK 116,18,116,97
v_lshlrev_b32 v52, 16, v52
M 2,43,1
RV 252,22,5392
PO 118
v_add_f32 v52, v36, v52
M 1,18,0
WL 5
LO 16,16,119
LU 119,16
C 52,52,52
M 1,19,1
RA 236,1,12288
HS 25,119
M 0,6,0
LL 18,98
LL 123,99
v_lshlrev_b32 v52, 16, v52
M 0,7,1
LA 18
PK 119,16,117,56
v_sqrt_f32 v52, v52
M 0,8,2
LC_ 123
PK 117,16,117,97
C 52,52,52
M 0,9,3
ST 112
PO 119
v_lshlrev_b32 v52, 16, v52
M 1,20,2
WL 4
LO 17,17,122
LU 122,17
v_rcp_f32 v52, v52
M 1,21,3
s_add_u32 s42, s44, s63
RA 216,1,13312
HS 25,122
v_mul_f32 v52, v38, v52
M 2,44,2
s_addc_u32 s43, s45, 0
LL 16,100
LL 126,101
CF 52,52,52
M 2,45,3
LA 16
PK 122,17,120,56
ds_bpermute_b32 v13, v28, v52
M 3,56,2
LC_ 126
PK 120,17,120,97
M 3,57,3
ST 116
AR 248,0
M 4,68,2
WL 4
LO 18,18,123
LU 123,18
M 4,69,3
s_add_u32 s42, s44, s64
RA 220,1,14336
HS 25,123
M 5,80,2
LL 17,102
LL 127,103
M 5,81,3
s_addc_u32 s43, s45, 0
LA 17
PK 123,18,121,56
M 0,10,4
LC_ 127
PK 121,18,121,97
M 0,11,5
ST 120
AR 249,1
M 1,22,4
WL 3
LO 16,16,126
LU 126,16
M 1,23,5
RA 224,1,15360
HS 25,126
M 2,46,4
LL 18,56
LL 104,57
M 2,47,5
LA 18
PK 126,16,124,56
M 3,58,4
LC_ 104
PK 124,16,124,97
AR 250,2
M 3,59,5
ds_read_b64 v[14:15], v5 offset:0
PO 126
M 4,70,4
WL 4
LO 17,17,127
LU 127,17
AR 251,3
M 4,71,5
s_add_u32 s42, s44, s65
RA 228,1,16384
HS 25,127
AR 52,4
M 5,82,4
LL 16,58
LL 105,59
AR 53,5
M 5,83,5
s_addc_u32 s43, s45, 0
LA 16
PK 127,17,125,56
Z 6,0,0
LC_ 105
PK 125,17,125,97
AR 54,6
M 6,1,1
PO 127
v_subrev_u32 v5, s39, v5
AR 55,7
Z 7,12,0
WL 4
LO 104,18,104
LU 18,104
XS 224
M 7,13,1
RA 232,1,17408
HZ 26,18
Z 8,36,0
WL 4
LL 17,60
LL 108,61
XS 225
M 8,37,1
s_mul_i32 s53, s50, 25600
LA 17
v_add_f32 v14, v14, v15
XS 226
Z 9,48,0
s_add_u32 s40, %[wlo], s53
LC_ 108
v_add_f32_dpp v14, v14, v14 quad_perm:[1,0,3,2] row_mask:0xf bank_mask:0xf
M 9,49,1
s_addc_u32 s41, %[whi], 0
v_add_f32_dpp v14, v14, v14 quad_perm:[2,3,0,1] row_mask:0xf bank_mask:0xf
HX 25
XS 227
Z 10,60,0
WL 3
LO 105,16,105
LU 16,105
XS 228
M 10,61,1
s_mov_b32 m0, s38
RA 236,1,18432
HS 26,16
XS 229
Z 11,72,0
LL 18,62
LL 109,63
M 11,73,1
LA 18
W1 25,1024
XS 230
M 11,74,2
LC_ 109
W1 27,3072
XS 231
M 11,75,3
ST 124
v_add_f32_dpp v14, v14, v14 row_half_mirror row_mask:0xf bank_mask:0xf
v_min_u32 v35, v35, v13
M 10,62,2
WL 5
LO 108,17,108
LU 17,108
M 10,63,3
RA 216,1,19456
HS 26,17
XS 232
M 9,50,2
LL 16,64
LL 112,65
XS 233
M 9,51,3
LA 16
DW 0
XS 234
M 8,38,2
s_add_u32 s37, s37, 32768
LC_ 112
v_cvt_f64_f32 v[14:15], v14
M 8,39,3
s_cmp_eq_u32 s37, s75
s_mov_b64 exec, s[54:55]
SP
s_mov_b64 exec, -1
v_add_u32 v4, s39, v4
XS 235
M 7,14,2
WL 4
LO 109,18,109
LU 18,109
XS 236
M 7,15,3
s_cselect_b32 s37, s74, s37
RA 220,1,20480
HS 26,18
M 6,2,2
LL 17,66
LL 113,67
XS 237
M 6,3,3
s_mul_i32 s53, %[d], 196608
LA 17
HX 26
XS 238
M 6,4,4
s_add_u32 s53, s53, s57
LC_ 113
XS 239
M 6,5,5
s_add_u32 s46, %[xlo], s53
XS 240
XS 241
M 7,16,4
WL 3
LO 112,16,112
LU 16,112
M 7,17,5
s_addc_u32 s47, %[xhi], 0
RA 224,1,21504
HZ 24,16
M 8,40,4
s_bitcmp1_b64 s[92:93], 48
LL 18,68
LL 116,69
XS 242
M 8,41,5
s_cselect_b32 s58, 16, 0
LA 18
XS 243
M 9,52,4
s_lshr_b32 s58, 0xda43cb52, s58
LC_ 116
v_lshrrev_b32 v3, 6, v0
v_and_b32 v3, 12, v3
v_bfe_u32 v3, s58, v3, 4
v_mad_u32_u24 v3, v3, v32, v21
M 9,53,5
DW 1024
XS 244
M 10,64,4
WL 3
LO 113,17,113
LU 17,113
XS 245
M 10,65,5
RA 228,1,22528
HS 24,17
M 11,76,4
LL 16,70
LL 117,71
XS 246
M 11,77,5
LA 16
XL 36,0
XS 247
M 11,78,0
LC_ 117
DW 2048
M 11,79,1
XL 37,64
XS 248
M 10,66,0
WL 3
LO 116,18,116
LU 18,116
XS 249
M 10,67,1
s_mul_i32 s53, s51, 24
RA 232,1,23552
HS 24,18
XS 250
M 9,54,0
s_bitcmp1_b32 s51, 0
LL 17,72
LL 120,73
M 9,55,1
s_addc_u32 s53, s53, 0
LA 17
s_lshl_b32 s53, s53, 11
s_bitcmp1_b64 s[90:91], s51
s_cselect_b64 s[44:45], s[84:85], s[82:83]
s_add_u32 s44, s44, s53
s_addc_u32 s45, s45, 0
s_bitcmp1_b64 s[92:93], s51
s_cselect_b64 s[94:95], s[88:89], s[86:87]
s_add_u32 s94, s94, s53
s_addc_u32 s95, s95, 0
s_add_u32 s51, s51, %[d]
s_cmp_eq_u32 s51, %[wrap]
s_cselect_b32 s51, %[rst], s51
XS 251
M 8,42,0
s_add_u32 s40, s40, 3072
LC_ 120
s_addc_u32 s41, s41, 0
)"
R"(s_add_u32 m0, s38, 3072
AW 0,248
M 8,43,1
XL 38,128
AW 1,249
M 7,18,0
WL 3
LO 117,16,117
LU 16,117
M 7,19,1
RA 236,1,24576
HS 24,16
AW 2,250
M 6,6,0
LL 18,74
LL 121,75
AW 3,251
M 6,7,1
LA 18
DW 0
XS 52
M 6,8,2
LC_ 121
XL 39,192
M 6,9,3
DW 1024
v_add_u32 v1, s37, v0
XS 53
M 7,20,2
WL 3
LO 120,17,120
LU 17,120
XS 54
M 7,21,3
RA 216,1,0
HS 24,17
M 8,44,2
LL 16,76
LL 124,77
XS 55
M 8,45,3
LA 16
XL 40,256
AW 4,52
M 9,56,2
LC_ 124
DW 2048
AW 5,53
M 9,57,3
XL 41,320
v_and_b32 v22, 0xfffffcff, v1
M 10,68,2
WL 3
LO 121,18,121
LU 18,121
AW 6,54
M 10,69,3
s_lshl_b32 s53, s52, 8
RA 220,1,1024
HS 24,18
AW 7,55
M 11,80,2
LL 17,78
LL 125,79
AR 248,8
M 11,81,3
s_add_u32 s48, %[alo], s53
LA 17
RV 252,22,5120
M 6,10,4
s_addc_u32 s49, %[ahi], 0
LC_ 125
AR 249,9
M 6,11,5
s_add_u32 s52, s52, %[d]
AR 250,10
AR 251,11
M 7,22,4
WL 4
LO 124,16,124
LU 16,124
M 7,23,5
s_cmp_eq_u32 s52, %[wrap]
RA 224,1,2048
HS 24,16
M 8,46,4
s_cselect_b32 s52, %[rst], s52
LL 18,80
LL 106,81
XS 248
M 8,47,5
LA 18
DQ 3072
XS 249
M 9,58,4
LC_ 106
XL 42,384
M 9,59,5
s_add_u32 s50, s50, %[d]
XS 250
XS 251
M 10,70,4
WL 3
LO 125,17,125
LU 17,125
M 10,71,5
RA 228,1,3072
HS 24,17
AW 8,248
M 11,82,4
LL 16,82
LL 107,83
M 11,83,5
LA 16
HX 24
s_cmp_eq_u32 s50, %[wrap]
s_cselect_b32 s50, %[rst], s50
XL 43,448
AW 9,249
Z 0,0,0
WV 38
LC_ 107
XL 44,512
v_add_u32 v19, 1, v19
s_add_u32 s38, s38, 32768
s_cmp_eq_u32 s38, s77
s_cselect_b32 s38, s76, s38
FP
s_sub_u32 s39, 0, s39
AW 10,250
M 0,1,1
XL 45,576
AW 11,251
Z 1,12,0
WL 4
LO 18,18,106
LU 106,18
M 1,13,1
RA 232,1,4096
HZ 27,106
AR 52,12
Z 4,60,0
LL 17,84
LL 110,85
AR 53,13
M 4,61,1
LA 17
PK 106,18,104,56
AR 54,14
Z 5,72,0
WL 5
LC_ 110
XL 46,640
PK 104,18,104,97
M 5,73,1
RA 236,1,6144
AR 55,15
M 5,74,2
WL 5
LO 16,16,107
LU 107,16
XS 52
M 5,75,3
FR
HS 27,107
M 4,62,2
LL 18,86
LL 111,87
XS 53
M 4,63,3
LA 18
PK 107,16,105,56
XS 54
M 1,14,2
LC_ 111
XL 47,704
PK 105,16,105,97
XS 55
M 1,15,3
s_add_u32 s42, s94, s60
RA 216,1,7168
AW 12,52
M 0,2,2
WL 4
LO 17,17,110
LU 110,17
M 0,3,3
s_addc_u32 s43, s95, 0
RA 220,1,8192
HS 27,110
AW 13,53
M 0,4,4
LL 16,88
LL 114,89
AW 14,54
M 0,5,5
LA 16
PK 110,17,108,56
M 1,16,4
LC_ 114
PK 108,17,108,97
AW 15,55
M 1,17,5
RA 224,1,9216
PO 110
AR 248,16
M 4,64,4
WL 5
LO 18,18,111
LU 111,18
SU 104
AR 249,17
M 4,65,5
s_add_u32 s42, s94, s61
HS 27,111
v_cmp_gt_u32 vcc, v19, v15
M 5,76,4
WL 3
LL 17,90
LL 115,91
AR 250,18
M 5,77,5
s_addc_u32 s43, s95, 0
LA 17
PK 111,18,109,56
AR 251,19
M 5,78,0
s_cbranch_vccnz .Lk1s14
.Lk1r14:
LC_ 115
PK 109,18,109,97
M 5,79,1
WV 17
RA 228,1,10240
PO 111
XS 248
BW 36
M 4,66,0
WL 5
LO 16,16,114
LU 114,16
XS 249
M 4,67,1
RA 232,1,11264
RV 40,8,6144
HZ 25,114
XS 250
M 1,18,0
RV 44,8,6208
LL 18,92
LL 118,93
M 1,19,1
LA 18
RV 48,8,6272
PK 114,16,112,56
XS 251
M 0,6,0
WL 6
LC_ 118
BR 36
PK 112,16,112,97
AW 16,248
M 0,7,1
WV 15
RV 252,22,5392
ds_read_b64 v[14:15], v5 offset:0
W1 24,0
W1 26,2048
HX 27
BW 37
AW 17,249
M 0,8,2
BR 37
LO 17,17,115
LU 115,17
M 0,9,3
WL 6
RA 236,1,12288
HS 25,115
AW 18,250
LZ 40,36
WV 14
BW 38
M 1,20,2
LL 16,94
LL 119,95
AW 19,251
M 1,21,3
LA 16
RV 40,8,6336
PK 115,17,113,56
AR 52,20
M 4,68,2
LC_ 119
BR 38
PK 113,17,113,97
M 4,69,3
WL 6
RA 216,1,13312
AR 53,21
LS 44,37
WV 12
BW 39
M 5,80,2
WL 7
LO 18,18,118
LU 118,18
SU 108
AR 54,22
M 5,81,3
s_add_u32 s42, s44, s62
RA 220,1,14336
HS 25,118
s_addc_u32 s43, s45, 0
RV 44,8,6400
M 0,10,4
BR 39
LL 17,96
LL 122,97
AR 55,23
M 0,11,5
WL 5
LA 17
PK 118,18,116,56
XS 52
LS 48,38
WV 11
BW 40
M 1,22,4
LC_ 122
PK 116,18,116,97
XS 53
M 1,23,5
RA 224,1,15360
RV 48,8,6464
PO 118
M 4,70,4
LO 16,16,119
LU 119,16
ST 112
XS 54
M 4,71,5
s_add_u32 s42, s44, s63
BR 40
HS 25,119
XS 55
M 5,82,4
WL 6
LL 18,98
LL 123,99
AW 20,52
M 5,83,5
s_addc_u32 s43, s45, 0
LA 18
PK 119,16,117,56
LS 40,39
WV 10
BW 41
Z 6,0,0
LC_ 123
PK 117,16,117,97
v_add_f32 v14, v14, v15
s_nop 1
v_add_f32_dpp v14, v14, v14 quad_perm:[1,0,3,2] row_mask:0xf bank_mask:0xf
s_nop 1
v_add_f32_dpp v14, v14, v14 quad_perm:[2,3,0,1] row_mask:0xf bank_mask:0xf
s_nop 1
v_add_f32_dpp v14, v14, v14 row_half_mirror row_mask:0xf bank_mask:0xf
v_cvt_f64_f32 v[14:15], v14
s_mov_b64 exec, s[54:55]
SP
s_mov_b64 exec, -1
s_nop 0
ds_read_b64 v[14:15], v5 offset:2048
RV 40,8,6528
AW 21,53
M 6,1,1
WV 9
RA 228,1,16384
BR 41
PO 119
AW 22,54
Z 7,12,0
WL 7
LO 17,17,122
LU 122,17
AW 23,55
M 7,13,1
s_mul_i32 s53, %[d], 393216
RA 232,1,17408
HS 25,122
LS 44,40
s_add_u32 s53, s53, s57
BW 42
Z 10,60,0
s_add_u32 s46, %[xlo], s53
RV 44,8,6592
LL 16,100
LL 126,101
M 10,61,1
)"
R"(s_addc_u32 s47, %[xhi], 0
LA 16
BR 42
PK 122,17,120,56
Z 2,72,0
WL 5
LC_ 126
PK 120,17,120,97
M 2,73,1
s_bitcmp1_b64 s[92:93], 48
RA 236,1,18432
v_subrev_u32 v5, s39, v5
LS 48,41
s_cselect_b32 s58, 16, 0
WV 8
BW 43
M 2,74,2
RV 48,8,6656
LO 18,18,123
LU 123,18
M 2,75,3
s_lshr_b32 s58, 0xfe987610, s58
BR 43
HS 25,123
v_lshrrev_b32 v3, 6, v0
v_and_b32 v3, 12, v3
v_bfe_u32 v3, s58, v3, 4
v_mad_u32_u24 v3, v3, v32, v21
M 10,62,2
s_mul_i32 s53, s50, 25600
LL 17,102
LL 127,103
s_add_u32 s40, %[wlo], s53
s_addc_u32 s41, %[whi], 0
s_mov_b32 m0, s38
M 10,63,3
WL 5
LA 17
PK 123,18,121,56
LS 40,42
WV 7
BW 44
M 7,14,2
LC_ 127
RV 40,8,6720
PK 121,18,121,97
M 7,15,3
WV 6
RA 216,1,19456
BR 44
DW 0
M 6,2,2
WL 6
LO 16,16,126
LU 126,16
ST 116
M 6,3,3
s_add_u32 s42, s44, s64
RA 220,1,20480
HS 25,126
s_addc_u32 s43, s45, 0
LS 44,43
s_add_u32 s37, s37, 32768
BW 45
M 6,4,4
s_cmp_eq_u32 s37, s75
LL 18,56
LL 104,57
s_cselect_b32 s37, s74, s37
M 6,5,5
LA 18
PK 126,16,124,56
v_add_f32 v14, v14, v15
s_nop 1
v_add_f32_dpp v14, v14, v14 quad_perm:[1,0,3,2] row_mask:0xf bank_mask:0xf
s_nop 1
v_add_f32_dpp v14, v14, v14 quad_perm:[2,3,0,1] row_mask:0xf bank_mask:0xf
s_nop 1
v_add_f32_dpp v14, v14, v14 row_half_mirror row_mask:0xf bank_mask:0xf
v_cvt_f64_f32 v[14:15], v14
s_mov_b64 exec, s[54:55]
SP
s_mov_b64 exec, -1
XL 48,0
DW 1024
RV 44,8,6784
M 7,16,4
LC_ 104
BR 45
PK 124,16,124,97
M 7,17,5
WL 6
RA 224,1,21504
PO 126
LS 48,44
WV 10
BW 46
M 10,64,4
LO 17,17,127
LU 127,17
ST 120
M 10,65,5
WV 10
DW 2048
RV 48,8,6848
HS 25,127
M 2,76,4
WL 8
RA 240,7,0
LL 16,58
LL 105,59
M 2,77,5
s_add_u32 s40, s40, 3072
LA 16
BR 46
PK 127,17,125,56
M 2,78,0
s_addc_u32 s41, s41, 0
LC_ 105
PK 125,17,125,97
s_add_u32 m0, s38, 3072
M 2,79,1
WL 7
RA 228,1,22528
XL 49,64
PO 127
LS 40,45
s_add_u32 s42, s44, s65
BW 47
M 10,66,0
RA 244,7,256
LO 104,18,104
LU 18,104
M 10,67,1
s_addc_u32 s43, s45, 0
RA 232,1,23552
BR 47
HZ 26,18
M 7,18,0
WL 6
DW 0
LL 17,60
LL 108,61
M 7,19,1
s_lshl_b32 s53, s52, 8
LA 17
HX 25
LS 44,46
s_add_u32 s48, %[alo], s53
RA 248,7,512
M 6,6,0
WL 2
LC_ 108
W1 25,1024
W1 27,3072
M 6,7,1
s_addc_u32 s49, %[ahi], 0
RA 236,1,24576
v_add_u32 v4, s39, v4
LS 48,47
s_add_u32 s52, s52, %[d]
RA 252,7,768
M 6,8,2
LO 105,16,105
LU 16,105
s_cmp_eq_u32 s52, %[wrap]
s_cselect_b32 s52, %[rst], s52
DW 1024
v_mul_f32 v36, 0xbb2aaaab, v36
M 6,9,3
XL 50,128
HS 26,16
v_mul_f32 v37, 0xbb2aaaab, v37
v_mul_f32 v38, 0xbb2aaaab, v38
M 7,20,2
LL 18,62
LL 109,63
v_mul_f32 v39, 0xbb2aaaab, v39
M 7,21,3
LA 18
DW 2048
LK 40,240,36,0
LK 44,242,36,2
RA 240,7,1024
v_add_u32 v1, s37, v0
LK 48,244,37,0
s_nop 0
CF 40,40,41
LK 248,246,37,2
RA 244,7,1280
CF 42,42,43
M 10,68,2
LC_ 109
RA 216,1,0
M 10,69,3
ST 124
v_and_b32 v22, 0xfffffcff, v1
CF 44,44,45
CF 46,46,47
M 2,80,2
WL 6
LO 108,17,108
LU 17,108
AW 24,40
AW 25,42
M 2,81,3
s_mul_i32 s53, s51, 24
RA 220,1,1024
HS 26,17
s_bitcmp1_b32 s51, 0
s_addc_u32 s53, s53, 0
s_lshl_b32 s53, s53, 11
s_bitcmp1_b64 s[90:91], s51
s_cselect_b64 s[44:45], s[84:85], s[82:83]
s_add_u32 s44, s44, s53
s_addc_u32 s45, s45, 0
s_bitcmp1_b64 s[92:93], s51
s_cselect_b64 s[94:95], s[88:89], s[86:87]
s_add_u32 s94, s94, s53
s_addc_u32 s95, s95, 0
s_add_u32 s51, s51, %[d]
s_cmp_eq_u32 s51, %[wrap]
s_cselect_b32 s51, %[rst], s51
DQ 3072
AW 26,44
AW 27,46
LK 40,248,38,0
CF 48,48,49
LK 44,250,38,2
RA 248,7,1536
CF 50,50,51
GZ 36
CF 248,248,249
CF 250,250,251
M 6,10,4
LL 16,72
LL 120,73
M 6,11,5
LA 16
RV 252,22,5120
M 7,22,4
WL 4
LC_ 120
XL 51,192
AW 28,48
M 7,23,5
s_add_u32 s50, s50, %[d]
RA 224,1,2048
s_cmp_eq_u32 s50, %[wrap]
s_cselect_b32 s50, %[rst], s50
v_add_u32 v19, 1, v19
AW 29,50
AW 30,248
AW 31,250
LK 48,252,39,0
CF 40,40,41
LK 248,254,39,2
s_add_u32 s38, s38, 32768
RA 252,7,1792
CF 42,42,43
GS 37
s_cmp_eq_u32 s38, s77
CF 44,44,45
CF 46,46,47
M 10,70,4
LO 109,18,109
LU 18,109
M 10,71,5
s_cselect_b32 s38, s76, s38
FP
HS 26,18
M 2,82,4
WL 4
LL 17,74
LL 121,75
M 2,83,5
s_sub_u32 s39, 0, s39
LA 17
RA 228,1,3072
AW 32,40
AW 33,42
Z 0,0,0
LC_ 121
XL 52,256
AW 34,44
AW 35,46
M 0,1,1
HX 26
LK 40,240,40,0
CF 48,48,49
LK 44,242,40,2
RA 240,7,2048
CF 50,50,51
GS 38
CF 248,248,249
CF 250,250,251
Z 1,12,0
WL 7
LO 120,16,120
LU 16,120
M 1,13,1
RA 232,1,4096
HZ 24,16
AW 36,48
Z 4,60,0
LL 18,76
LL 124,77
AW 37,50
AW 38,248
M 4,61,1
LA 18
FR
AW 39,250
LK 48,244,41,0
CF 40,40,41
LK 248,246,41,2
RA 244,7,2304
CF 42,42,43
GS 39
CF 44,44,45
CF 46,46,47
Z 8,72,0
WL 6
LC_ 124
XL 53,320
M 8,73,1
RA 236,1,6144
AW 40,40
M 8,74,2
WL 7
LO 121,17,121
LU 17,121
AW 41,42
M 8,75,3
HS 24,17
AW 42,44
AW 43,46
LK 40,248,42,0
CF 48,48,49
LK 44,250,42,2
RA 248,7,2560
CF 50,50,51
GS 40
CF 248,248,249
CF 250,250,251
M 4,62,2
LL 16,78
LL 125,79
M 4,63,3
LA 16
RA 216,1,7168
M 1,14,2
WL 6
LC_ 125
v_cmp_gt_u32 vcc, v19, v15
M 1,15,3
XL 54,384
AW 44,48
AW 45,50
AW 46,248
M 0,2,2
WL 4
LO 124,18,124
LU 18,124
AW 47,250
M 0,3,3
RA 220,1,8192
HS 24,18
LK 48,252,43,0
CF 40,40,41
LK 248,254,43,2
RA 252,7,2816
CF 42,42,43
GS 41
CF 44,44,45
CF 46,46,47
M 0,4,4
s_cbranch_vccnz .Lk1s15
.Lk1r15:
LL 17,80
LL 106,81
M 0,5,5
LA 17
ds_read_b64 v[14:15], v5 offset:0
AW 48,40
AW 49,42
M 1,16,4
LC_ 106
AW 50,44
AW 51,46
M 1,17,5
RA 224,1,9216
LK 40,240,44,0
CF 48,48,49
LK 44,242,44,2
CF 50,50,51
GS 42
CF 248,248,249
CF 250,250,251
M 4,64,4
)"
R"(WL 6
LO 125,16,125
LU 16,125
M 4,65,5
XL 55,448
HS 24,16
AW 52,48
M 8,76,4
WL 5
LL 18,82
LL 107,83
AW 53,50
AW 54,248
AW 55,250
M 8,77,5
LA 18
RA 228,1,10240
LK 48,244,45,0
CF 40,40,41
LK 248,246,45,2
CF 42,42,43
HX 24
GS 43
CF 44,44,45
CF 46,46,47
M 8,78,0
LC_ 107
W1 24,0
M 8,79,1
W1 26,2048
AW 56,40
M 4,66,0
WL 6
LO 17,17,106
LU 106,17
AW 57,42
AW 58,44
M 4,67,1
RA 232,1,11264
HZ 27,106
AW 59,46
LK 40,248,46,0
CF 48,48,49
LK 44,250,46,2
CF 50,50,51
v_add_f32 v14, v14, v15
GS 44
CF 248,248,249
CF 250,250,251
M 1,18,0
LL 16,84
LL 110,85
M 1,19,1
LA 16
PK 106,17,104,56
M 0,6,0
WL 5
LC_ 110
PK 104,17,104,97
AW 60,48
AW 61,50
AW 62,248
M 0,7,1
RV 252,22,5392
v_add_f32_dpp v14, v14, v14 quad_perm:[1,0,3,2] row_mask:0xf bank_mask:0xf
AW 63,250
LK 48,252,47,0
CF 40,40,41
LK 248,254,47,2
CF 42,42,43
v_add_f32_dpp v14, v14, v14 quad_perm:[2,3,0,1] row_mask:0xf bank_mask:0xf
GS 45
CF 44,44,45
CF 46,46,47
M 0,8,2
WL 6
LO 18,18,107
LU 107,18
M 0,9,3
RA 236,1,12288
HS 27,107
M 1,20,2
LL 17,86
LL 111,87
AW 64,40
AW 65,42
M 1,21,3
LA 17
RV 36,8,6912
PK 107,18,105,56
AW 66,44
AW 67,46
M 4,68,2
LC_ 111
XL 56,512
PK 105,18,105,97
M 4,69,3
s_add_u32 s42, s94, s60
RA 216,1,13312
v_add_f32_dpp v14, v14, v14 row_half_mirror row_mask:0xf bank_mask:0xf
GS 46
s_addc_u32 s43, s95, 0
CF 48,48,49
CF 50,50,51
CF 248,248,249
CF 250,250,251
M 8,80,2
WL 4
LO 16,16,110
LU 110,16
AW 68,48
AW 69,50
AW 70,248
M 8,81,3
RA 220,1,14336
HS 27,110
AW 71,250
s_nop 1
GS 47
SU 104
v_cvt_f64_f32 v[14:15], v14
s_nop 5
v_cndmask_b32 v52, v52, v53, s[78:79]
M 0,10,4
LL 18,96
LL 122,97
v_cndmask_b32 v54, v54, v55, s[78:79]
M 0,11,5
LA 18
PK 110,16,108,56
v_cndmask_b32 v52, v52, v54, s[80:81]
M 1,22,4
LC_ 122
PK 108,16,108,97
v_mul_f32 v52, 0x3b2aaaab, v52
M 1,23,5
WL 5
RA 224,1,15360
PO 110
v_med3_f32 v53, v52, v39, v37
M 4,70,4
WL 5
LO 17,17,111
LU 111,17
s_mov_b64 exec, s[54:55]
SP
s_mov_b64 exec, -1
XL 57,576
LG 52,53
s_nop 0
C 52,52,52
M 4,71,5
s_add_u32 s42, s94, s61
ds_read_b64 v[14:15], v5 offset:2048
HS 27,111
v_lshlrev_b32 v52, 16, v52
M 8,82,4
WL 4
LL 16,98
LL 123,99
v_add_f32 v52, v36, v52
M 8,83,5
s_addc_u32 s43, s95, 0
LA 16
PK 111,17,109,56
C 52,52,52
Z 6,0,0
LC_ 123
PK 109,17,109,97
v_lshlrev_b32 v52, 16, v52
M 6,1,1
RA 228,1,16384
PO 111
v_sqrt_f32 v52, v52
s_nop 0
C 52,52,52
Z 7,12,0
WL 5
LO 18,18,122
LU 122,18
v_lshlrev_b32 v52, 16, v52
M 7,13,1
RA 232,1,17408
HZ 25,122
v_rcp_f32 v52, v52
Z 10,60,0
XL 58,640
LL 17,64
LL 126,65
v_mul_f32 v52, v38, v52
M 10,61,1
LA 17
PK 122,18,120,56
v_subrev_u32 v5, s39, v5
HX 27
CF 52,52,52
Z 11,72,0
WL 2
LC_ 126
PK 120,18,120,97
ds_bpermute_b32 v13, v28, v52
M 11,73,1
RA 236,1,18432
v_add_f32 v14, v14, v15
M 11,74,2
LO 16,16,123
LU 123,16
M 11,75,3
SU 108
HS 25,123
v_add_f32_dpp v14, v14, v14 quad_perm:[1,0,3,2] row_mask:0xf bank_mask:0xf
M 10,62,2
s_mul_i32 s53, s50, 25600
LL 18,66
LL 127,67
s_add_u32 s40, %[wlo], s53
s_addc_u32 s41, %[whi], 0
s_mov_b32 m0, s38
M 10,63,3
LA 18
PK 123,16,121,56
v_add_f32_dpp v14, v14, v14 quad_perm:[2,3,0,1] row_mask:0xf bank_mask:0xf
M 7,14,2
LC_ 127
PK 121,16,121,97
M 7,15,3
RA 216,1,19456
XL 59,704
DW 0
v_add_f32_dpp v14, v14, v14 row_half_mirror row_mask:0xf bank_mask:0xf
M 6,2,2
WL 3
LO 17,17,126
LU 126,17
M 6,3,3
s_add_u32 s42, s44, s64
RA 220,1,20480
HS 25,126
v_cvt_f64_f32 v[14:15], v14
s_addc_u32 s43, s45, 0
AR 248,24
M 6,4,4
s_add_u32 s37, s37, 32768
LL 16,56
LL 104,57
s_cmp_eq_u32 s37, s75
s_cselect_b32 s37, s74, s37
AR 249,25
M 6,5,5
LA 16
PK 126,17,124,56
s_mov_b64 exec, s[54:55]
SP
s_mov_b64 exec, -1
DW 1024
AR 250,26
M 7,16,4
LC_ 104
PK 124,17,124,97
AR 251,27
AR 52,28
M 7,17,5
RA 224,1,21504
PO 126
AR 53,29
M 10,64,4
WL 5
LO 18,18,127
LU 127,18
ST 120
AR 54,30
AR 55,31
M 10,65,5
DW 2048
HS 25,127
XS 248
M 11,76,4
WL 3
LL 17,58
LL 105,59
XS 249
XS 250
M 11,77,5
s_add_u32 s40, s40, 3072
LA 17
PK 127,18,125,56
XS 251
M 11,78,0
s_addc_u32 s41, s41, 0
LC_ 105
PK 125,18,125,97
s_add_u32 m0, s38, 3072
AW 24,248
AW 25,249
M 11,79,1
s_add_u32 s42, s44, s65
RA 228,1,22528
PO 127
AW 26,250
M 10,66,0
WL 4
LO 104,16,104
LU 16,104
AW 27,251
XS 52
M 10,67,1
s_addc_u32 s43, s45, 0
RA 232,1,23552
HZ 26,16
XS 53
M 7,18,0
DW 0
LL 18,60
LL 108,61
XS 54
XS 55
M 7,19,1
s_lshl_b32 s53, s52, 8
LA 18
HX 25
AW 28,52
M 6,6,0
WL 2
LC_ 108
W1 25,1024
W1 27,3072
AW 29,53
AW 30,54
M 6,7,1
s_add_u32 s48, %[alo], s53
RA 236,1,24576
v_add_u32 v4, s39, v4
AW 31,55
M 6,8,2
LO 105,17,105
LU 17,105
s_addc_u32 s49, %[ahi], 0
s_add_u32 s52, s52, %[d]
s_cmp_eq_u32 s52, %[wrap]
s_cselect_b32 s52, %[rst], s52
DW 1024
v_min_u32 v35, v35, v13
AR 248,32
M 6,9,3
ST 124
HS 26,17
s_bitcmp1_b64 s[92:93], 48
s_cselect_b32 s58, 16, 0
s_lshr_b32 s58, 0xda43cb52, s58
v_and_b32 v11, 0x1f0, v0
v_min_u32 v11, 368, v11
v_lshlrev_b32 v11, 3, v11
AR 249,33
M 7,20,2
s_mul_i32 s53, s51, 24
LL 16,62
LL 109,63
s_bitcmp1_b32 s51, 0
s_addc_u32 s53, s53, 0
s_lshl_b32 s53, s53, 11
s_bitcmp1_b64 s[90:91], s51
s_cselect_b64 s[44:45], s[84:85], s[82:83]
s_add_u32 s44, s44, s53
s_addc_u32 s45, s45, 0
s_bitcmp1_b64 s[92:93], s51
s_cselect_b64 s[94:95], s[88:89], s[86:87]
s_add_u32 s94, s94, s53
s_addc_u32 s95, s95, 0
s_add_u32 s51, s51, %[d]
s_cmp_eq_u32 s51, %[wrap]
s_cselect_b32 s51, %[rst], s51
AR 250,34
AR 251,35
M 7,21,3
LA 16
DW 2048
XS 248
M 10,68,2
LC_ 109
DQ 3072
XS 249
XS 250
M 10,69,3
s_add_u32 s50, s50, %[d]
v_add_u32 v1, s37, v0
XS 251
M 11,80,2
WL 2
LO 108,18,108
LU 18,108
AW 32,248
AW 33,249
M 11,81,3
s_cmp_eq_u32 s50, %[wrap]
RA 216,1,0
HS 26,18
AW 34,250
M 6,10,4
s_cselect_b32 s50, %[rst], s50
LL 17,72
)"
R"(LL 120,73
AW 35,251
AR 52,36
M 6,11,5
LA 17
v_and_b32 v22, 0xfffffcff, v1
AR 53,37
M 7,22,4
LC_ 120
RA 220,1,1024
AR 54,38
AR 55,39
M 7,23,5
RV 252,22,5120
v_add_u32 v19, 1, v19
XS 52
M 10,70,4
WL 5
LO 109,16,109
LU 16,109
XS 53
XS 54
M 10,71,5
s_add_u32 s38, s38, 32768
RA 224,1,2048
HS 26,16
XS 55
M 11,82,4
WL 1
LL 18,74
LL 121,75
AW 36,52
AW 37,53
M 11,83,5
s_cmp_eq_u32 s38, s77
LA 18
RA 228,1,3072
s_cselect_b32 s38, s76, s38
FP
s_sub_u32 s39, 0, s39
AW 38,54
Z 0,0,0
LC_ 121
HX 26
AW 39,55
M 0,1,1
AR 248,40
AR 249,41
Z 1,12,0
LO 120,17,120
LU 17,120
AR 250,42
M 1,13,1
RA 232,1,4096
HZ 24,17
AR 251,43
XS 248
Z 2,24,0
LL 16,88
LL 124,89
XS 249
M 2,25,1
LA 16
XS 250
XS 251
Z 3,36,0
LC_ 124
AW 40,248
M 3,37,1
AW 41,249
AW 42,250
Z 4,60,0
WL 3
LO 121,18,121
LU 18,121
AW 43,251
M 4,61,1
RA 236,1,6144
HS 24,18
AR 52,44
AR 53,45
Z 5,72,0
LL 17,90
LL 125,91
AR 54,46
M 5,73,1
LA 17
AR 55,47
XS 52
M 5,74,2
LC_ 125
XS 53
M 5,75,3
XS 54
XS 55
M 4,62,2
WL 3
LO 124,16,124
LU 16,124
AW 44,52
M 4,63,3
RA 216,1,7168
HS 24,16
AW 45,53
AW 46,54
M 3,38,2
LL 18,80
LL 106,81
AW 47,55
M 3,39,3
LA 18
FR
AR 248,48
AR 249,49
M 2,26,2
LC_ 106
v_lshrrev_b32 v3, 9, v0
AR 250,50
M 2,27,3
WV 32
BW 48
AR 251,51
XS 248
M 1,14,2
WL 5
LO 125,17,125
LU 17,125
XS 249
M 1,15,3
RA 220,1,8192
HS 24,17
XS 250
XS 251
M 0,2,2
LL 16,82
LL 107,83
AW 48,248
M 0,3,3
LA 16
RV 40,8,6144
AW 49,249
AW 50,250
M 0,4,4
LC_ 107
RV 44,8,6208
AW 51,251
M 0,5,5
RV 48,8,6272
BR 48
AR 52,52
AR 53,53
M 1,16,4
WL 8
LO 18,18,106
LU 106,18
AR 54,54
M 1,17,5
RA 224,1,9216
HZ 27,106
AR 55,55
XS 52
M 2,28,4
LL 17,84
LL 110,85
XS 53
M 2,29,5
LA 17
PK 106,18,104,56
XS 54
XS 55
M 3,40,4
LC_ 110
PK 104,18,104,97
v_lshl_add_u32 v3, v3, 2, 0
v_bfe_u32 v3, s58, v3, 4
v_mad_u32_u24 v3, v3, v32, v11
AW 52,52
M 3,41,5
s_mov_b32 m0, %[pm]
s_nop 0
DF
v_cmp_gt_u32 vcc, v19, v15
AW 53,53
AW 54,54
M 4,64,4
WL 6
LO 16,16,107
LU 107,16
AW 55,55
M 4,65,5
s_add_u32 s42, s94, s60
RA 228,1,10240
HS 27,107
AR 248,56
M 5,76,4
LL 18,86
LL 111,87
AR 249,57
AR 250,58
M 5,77,5
s_cbranch_vccnz .Lk1s16
.Lk1r16:
LA 18
PK 107,16,105,56
AR 251,59
M 5,78,0
s_addc_u32 s43, s95, 0
LC_ 111
PK 105,16,105,97
XS 248
XS 249
M 5,79,1
SU 104
HX 24
XS 250
M 4,66,0
WL 3
LO 17,17,110
LU 110,17
XS 251
AW 56,248
M 4,67,1
RA 232,1,11264
HS 27,110
AW 57,249
M 3,42,0
LL 16,96
LL 122,97
AW 58,250
AW 59,251
M 3,43,1
LA 16
PK 110,17,108,56
AR 52,60
M 2,30,0
LC_ 122
PK 108,17,108,97
AR 53,61
AR 54,62
M 2,31,1
RV 252,22,5392
PO 110
AR 55,63
M 1,18,0
WL 4
LO 18,18,111
LU 111,18
XS 52
XS 53
M 1,19,1
s_add_u32 s42, s94, s61
RA 236,1,12288
HS 27,111
XS 54
M 0,6,0
LL 17,98
LL 123,99
XS 55
AW 60,52
M 0,7,1
s_addc_u32 s43, s95, 0
LA 17
PK 111,18,109,56
AW 61,53
M 0,8,2
LC_ 123
PK 109,18,109,97
AW 62,54
AW 63,55
M 0,9,3
W1 24,0
PO 111
AR 248,64
M 1,20,2
WL 5
LO 16,16,122
LU 122,16
W1 26,2048
AR 249,65
AR 250,66
M 1,21,3
RA 216,1,13312
HZ 25,122
AR 251,67
M 2,32,2
LL 18,100
LL 126,101
XS 248
XS 249
M 2,33,3
LA 18
PK 122,16,120,56
XS 250
M 3,44,2
LC_ 126
PK 120,16,120,97
XS 251
AW 64,248
M 3,45,3
WV 30
SU 108
HX 27
AW 65,249
BW 49
M 4,68,2
WL 6
LO 17,17,123
LU 123,17
AW 66,250
AW 67,251
M 4,69,3
RA 220,1,14336
BR 49
HS 25,123
AR 52,68
LZ 40,48
WV 28
BW 50
M 5,80,2
LL 16,102
LL 127,103
AR 53,69
AR 54,70
M 5,81,3
LA 16
PK 123,17,121,56
AR 55,71
M 0,10,4
LC_ 127
PK 121,17,121,97
XS 52
XS 53
M 0,11,5
WV 24
DY
RV 40,8,6336
XS 54
M 1,22,4
WL 4
BR 50
LO 18,18,126
LU 126,18
XS 55
AW 68,52
M 1,23,5
s_add_u32 s42, s44, s64
RA 224,1,15360
HS 25,126
AW 69,53
LS 44,49
s_addc_u32 s43, s45, 0
BW 51
M 2,34,4
LL 17,56
LL 104,57
AW 70,54
AW 71,55
M 2,35,5
LA 17
PK 126,18,124,56
M 3,46,4
LC_ 104
RV 44,8,6400
PK 124,18,124,97
M 3,47,5
WV 24
ST 120
BR 51
PO 126
M 4,70,4
WL 6
LO 16,16,127
LU 127,16
M 4,71,5
s_add_u32 s42, s44, s65
RA 228,1,16384
HS 25,127
LS 48,50
s_addc_u32 s43, s45, 0
BW 52
M 5,82,4
LL 18,58
LL 105,59
M 5,83,5
LA 18
RV 48,8,6464
PK 127,16,125,56
Z 6,0,0
LC_ 105
PK 125,16,125,97
M 6,1,1
ds_read_b64 v[14:15], v5 offset:0
BR 52
PO 127
Z 7,12,0
WL 9
LO 104,17,104
LU 17,104
M 7,13,1
WL 7
)"
R"(RA 232,1,17408
HZ 26,17
LS 40,51
WV 24
BW 53
Z 8,24,0
WV 23
LL 16,60
LL 108,61
M 8,25,1
WL 2
LA 16
ST 124
RV 40,8,6528
Z 9,36,0
s_mul_i32 s53, s50, 25600
LC_ 108
BR 53
M 9,37,1
s_add_u32 s40, %[wlo], s53
HX 25
LS 44,52
s_addc_u32 s41, %[whi], 0
BW 54
Z 10,60,0
LO 105,18,105
LU 18,105
M 10,61,1
s_mov_b32 m0, s38
RA 236,1,18432
HS 26,18
Z 11,72,0
RV 44,8,6592
LL 17,62
LL 109,63
M 11,73,1
LA 17
W1 25,1024
BR 54
M 11,74,2
LC_ 109
W1 27,3072
M 11,75,3
WL 8
DW 0
v_add_f32 v14, v14, v15
LS 48,53
WV 24
BW 55
M 10,62,2
LO 108,16,108
LU 16,108
M 10,63,3
s_add_u32 s37, s37, 32768
RA 216,1,19456
HS 26,16
M 9,38,2
s_cmp_eq_u32 s37, s75
RV 48,8,6656
LL 18,64
LL 112,65
M 9,39,3
s_cselect_b32 s37, s74, s37
LA 18
DW 1024
BR 55
M 8,26,2
LC_ 112
v_add_f32_dpp v14, v14, v14 quad_perm:[1,0,3,2] row_mask:0xf bank_mask:0xf
M 8,27,3
WL 8
v_add_f32_dpp v14, v14, v14 quad_perm:[2,3,0,1] row_mask:0xf bank_mask:0xf
v_add_u32 v4, s39, v4
LS 40,54
WV 24
BW 56
M 7,14,2
WL 8
LO 109,17,109
LU 17,109
M 7,15,3
RA 220,1,20480
RV 40,8,6720
HS 26,17
M 6,2,2
BR 56
LL 16,66
LL 113,67
M 6,3,3
LA 16
v_add_f32_dpp v14, v14, v14 row_half_mirror row_mask:0xf bank_mask:0xf
M 6,4,4
WL 6
LC_ 113
v_cvt_f64_f32 v[14:15], v14
M 6,5,5
s_mov_b64 exec, s[54:55]
SP
s_mov_b64 exec, -1
v_lshrrev_b32 v3, 9, v0
LS 44,55
WV 22
BW 57
M 7,16,4
WL 7
RV 44,8,6784
LO 112,18,112
LU 18,112
M 7,17,5
RA 224,1,21504
HZ 29,18
M 8,28,4
BR 57
LL 17,68
LL 116,69
M 8,29,5
WL 6
LA 17
ds_read_b64 v[14:15], v5 offset:2048
LS 48,56
WV 21
BW 58
M 9,40,4
LC_ 116
RV 48,8,6848
M 9,41,5
RA 240,7,0
BR 58
M 10,64,4
WL 11
LO 113,16,113
LU 16,113
M 10,65,5
RA 228,1,22528
HS 29,16
v_lshl_add_u32 v3, v3, 2, 8
v_bfe_u32 v3, s58, v3, 4
v_mad_u32_u24 v3, v3, v32, v11
M 11,76,4
WV 19
LL 18,70
LL 117,71
M 11,77,5
WL 8
LA 18
DW 2048
M 11,78,0
s_mul_i32 s53, s51, 24
LC_ 117
v_subrev_u32 v5, s39, v5
M 11,79,1
s_bitcmp1_b32 s51, 0
LS 40,57
s_addc_u32 s53, s53, 0
BW 59
M 10,66,0
WL 4
RA 244,7,256
LO 116,17,116
LU 17,116
M 10,67,1
s_lshl_b32 s53, s53, 11
RA 232,1,23552
HS 29,17
M 9,42,0
s_bitcmp1_b64 s[90:91], s51
BR 59
LL 16,72
LL 120,73
M 9,43,1
s_cselect_b64 s[44:45], s[84:85], s[82:83]
LA 16
v_add_f32 v14, v14, v15
s_add_u32 s44, s44, s53
s_addc_u32 s45, s45, 0
s_bitcmp1_b64 s[92:93], s51
s_cselect_b64 s[94:95], s[88:89], s[86:87]
s_add_u32 s94, s94, s53
s_addc_u32 s95, s95, 0
s_add_u32 s51, s51, %[d]
s_cmp_eq_u32 s51, %[wrap]
s_cselect_b32 s51, %[rst], s51
s_mul_i32 s53, %[d], 16384
s_add_u32 s62, s53, %[sb1]
s_add_u32 s63, s62, 512
LS 44,58
s_add_u32 s40, s40, 3072
RA 248,7,512
v_add_f32_dpp v14, v14, v14 quad_perm:[1,0,3,2] row_mask:0xf bank_mask:0xf
M 8,30,0
s_addc_u32 s41, s41, 0
LC_ 120
v_add_f32_dpp v14, v14, v14 quad_perm:[2,3,0,1] row_mask:0xf bank_mask:0xf
s_add_u32 m0, s38, 3072
M 8,31,1
DW 0
v_add_f32_dpp v14, v14, v14 row_half_mirror row_mask:0xf bank_mask:0xf
M 7,18,0
WL 7
LO 117,18,117
LU 18,117
M 7,19,1
WL 3
RA 236,1,24576
HS 29,18
LS 48,59
RA 252,7,768
v_cvt_f64_f32 v[14:15], v14
M 6,6,0
LL 17,74
LL 121,75
s_nop 2
v_mul_f32 v36, 0xbb2aaaab, v36
M 6,7,1
LA 17
DW 1024
v_mul_f32 v37, 0xbb2aaaab, v37
v_mul_f32 v38, 0xbb2aaaab, v38
M 6,8,2
LC_ 121
s_mov_b64 exec, s[54:55]
SP
s_mov_b64 exec, -1
v_mul_f32 v39, 0xbb2aaaab, v39
M 6,9,3
DW 2048
v_add_u32 v1, s37, v0
LK 40,240,48,0
LK 44,242,48,2
s_lshl_b32 s53, s52, 8
RA 240,7,1024
v_pk_add_f16 v26, v26, v29
LK 48,244,49,0
CF 40,40,41
LK 248,246,49,2
s_add_u32 s48, %[alo], s53
RA 244,7,1280
CF 42,42,43
M 7,20,2
WL 6
LO 120,16,120
LU 16,120
M 7,21,3
s_addc_u32 s49, %[ahi], 0
RA 216,1,0
HZ 24,16
CF 44,44,45
M 8,32,2
s_add_u32 s52, s52, %[d]
LL 18,76
LL 124,77
CF 46,46,47
AW 72,40
M 8,33,3
s_cmp_eq_u32 s52, %[wrap]
LA 18
v_and_b32 v22, 0xfffffcff, v1
AW 73,42
AW 74,44
M 9,44,2
s_cselect_b32 s52, %[rst], s52
LC_ 124
DQ 3072
AW 75,46
M 9,45,3
HX 26
LK 40,248,50,0
CF 48,48,49
LK 44,250,50,2
RA 248,7,1536
CF 50,50,51
GZ 48
CF 248,248,249
CF 250,250,251
M 10,68,2
WL 6
LO 121,17,121
LU 17,121
M 10,69,3
RA 220,1,1024
HS 24,17
AW 76,48
M 11,80,2
LL 16,78
LL 125,79
AW 77,50
AW 78,248
AW 79,250
M 11,81,3
LA 16
RV 252,22,5120
LK 48,252,51,0
CF 40,40,41
LK 248,254,51,2
RA 252,7,1792
CF 42,42,43
GS 49
CF 44,44,45
CF 46,46,47
M 6,10,4
LC_ 125
DY
M 6,11,5
s_add_u32 s50, s50, %[d]
AW 80,40
AW 81,42
M 7,22,4
WL 6
LO 124,18,124
LU 18,124
M 7,23,5
s_cmp_eq_u32 s50, %[wrap]
RA 224,1,2048
HS 24,18
AW 82,44
AW 83,46
LK 40,240,52,0
CF 48,48,49
LK 44,242,52,2
s_cselect_b32 s50, %[rst], s50
RA 240,7,2048
CF 50,50,51
GS 50
CF 248,248,249
CF 250,250,251
M 8,34,4
LL 17,80
LL 106,81
M 8,35,5
LA 17
v_add_u32 v19, 1, v19
M 9,46,4
s_add_u32 s38, s38, 32768
LC_ 106
AW 84,48
M 9,47,5
AW 85,50
AW 86,248
M 10,70,4
WL 4
LO 125,16,125
LU 16,125
AW 87,250
M 10,71,5
RA 228,1,3072
HS 24,16
LK 48,244,53,0
CF 40,40,41
LK 248,246,53,2
RA 244,7,2304
CF 42,42,43
GS 51
CF 44,44,45
CF 46,46,47
M 11,82,4
LL 18,82
LL 107,83
M 11,83,5
LA 18
HX 24
Z 0,0,0
s_cmp_eq_u32 s38, s77
LC_ 107
AW 88,40
M 0,1,1
s_cselect_b32 s38, s76, s38
FP
AW 89,42
AW 90,44
AW 91,46
LK 40,248,54,0
CF 48,48,49
LK 44,250,54,2
s_sub_u32 s39, 0, s39
RA 248,7,2560
CF 50,50,51
GS 52
CF 248,248,249
CF 250,250,251
Z 1,12,0
WL 6
LO 17,17,106
LU 106,17
M 1,13,1
RA 232,1,4096
HZ 27,106
Z 2,24,0
LL 16,84
LL 110,85
M 2,25,1
LA 16
PK 106,17,104,56
AW 92,48
Z 3,36,0
LC_ 110
PK 104,17,104,97
AW 93,50
AW 94,248
AW 95,250
M 3,37,1
)"
R"(LK 48,252,55,0
CF 40,40,41
LK 248,254,55,2
RA 252,7,2816
CF 42,42,43
GS 53
CF 44,44,45
CF 46,46,47
Z 4,60,0
WL 6
LO 18,18,107
LU 107,18
M 4,61,1
s_add_u32 s42, s94, s60
RA 236,1,6144
HS 27,107
Z 5,72,0
LL 17,86
LL 111,87
AW 96,40
AW 97,42
M 5,73,1
s_addc_u32 s43, s95, 0
LA 17
PK 107,18,105,56
AW 98,44
AW 99,46
LK 40,240,56,0
CF 48,48,49
LK 44,242,56,2
CF 50,50,51
GS 54
CF 248,248,249
CF 250,250,251
M 5,74,2
LC_ 111
PK 105,18,105,97
M 5,75,3
SU 104
AW 100,48
M 4,62,2
WL 4
LO 16,16,110
LU 110,16
M 4,63,3
RA 216,1,7168
HS 27,110
AW 101,50
AW 102,248
M 3,38,2
LL 18,88
LL 114,89
AW 103,250
M 3,39,3
LA 18
PK 110,16,108,56
LK 48,244,57,0
CF 40,40,41
LK 248,246,57,2
CF 42,42,43
GS 55
CF 44,44,45
CF 46,46,47
M 2,26,2
LC_ 114
PK 108,16,108,97
M 2,27,3
FR
PO 110
AW 104,40
M 1,14,2
WL 4
LO 17,17,111
LU 111,17
AW 105,42
AW 106,44
AW 107,46
M 1,15,3
s_add_u32 s42, s94, s61
RA 220,1,8192
HS 27,111
LK 40,248,58,0
CF 48,48,49
LK 44,250,58,2
s_addc_u32 s43, s95, 0
CF 50,50,51
GS 56
CF 248,248,249
CF 250,250,251
M 0,2,2
LL 16,90
LL 115,91
M 0,3,3
s_mov_b32 m0, %[pm]
LA 16
PK 111,17,109,56
M 0,4,4
LC_ 115
PK 109,17,109,97
AW 108,48
AW 109,50
M 0,5,5
DF
PO 111
AW 110,248
AW 111,250
LK 48,252,59,0
CF 40,40,41
LK 248,254,59,2
CF 42,42,43
GS 57
CF 44,44,45
CF 46,46,47
M 1,16,4
WL 4
LO 18,18,114
LU 114,18
M 1,17,5
RA 224,1,9216
HZ 29,114
M 2,28,4
LL 17,92
LL 118,93
AW 112,40
M 2,29,5
LA 17
PK 114,18,112,56
AW 113,42
AW 114,44
M 3,40,4
LC_ 118
RV 36,8,6912
PK 112,18,112,97
AW 115,46
M 3,41,5
WL 7
SU 108
v_cmp_gt_u32 vcc, v19, v15
GS 58
CF 48,48,49
CF 50,50,51
M 4,64,4
WL 4
LO 16,16,115
LU 115,16
CF 248,248,249
CF 250,250,251
AW 116,48
M 4,65,5
s_add_u32 s42, s94, s62
RA 228,1,10240
HS 29,115
AW 117,50
AW 118,248
M 5,76,4
LL 18,94
LL 119,95
AW 119,250
M 5,77,5
s_cbranch_vccnz .Lk1s17
.Lk1r17:
LA 18
PK 115,16,113,56
GS 59
s_addc_u32 s43, s95, 0
W1 24,0
s_nop 5
v_cndmask_b32 v52, v52, v53, s[78:79]
M 5,78,0
LC_ 119
PK 113,16,113,97
v_cndmask_b32 v54, v54, v55, s[78:79]
M 5,79,1
W1 26,2048
v_cndmask_b32 v52, v52, v54, s[80:81]
M 4,66,0
WL 6
LO 17,17,118
LU 118,17
v_mul_f32 v52, 0x3b2aaaab, v52
M 4,67,1
WL 5
RA 232,1,11264
HS 29,118
v_med3_f32 v53, v52, v39, v37
M 3,42,0
LL 16,96
LL 122,97
LG 52,53
M 3,43,1
LA 16
PK 118,17,116,56
C 52,52,52
M 2,30,0
LC_ 122
PK 116,17,116,97
v_lshlrev_b32 v52, 16, v52
M 2,31,1
RV 252,22,5392
PO 118
v_add_f32 v52, v36, v52
M 1,18,0
WL 5
LO 18,18,119
LU 119,18
C 52,52,52
M 1,19,1
RA 236,1,12288
HS 29,119
M 0,6,0
LL 17,98
LL 123,99
v_lshlrev_b32 v52, 16, v52
M 0,7,1
LA 17
PK 119,18,117,56
v_sqrt_f32 v52, v52
M 0,8,2
LC_ 123
PK 117,18,117,97
C 52,52,52
M 0,9,3
SU 112
PO 119
v_lshlrev_b32 v52, 16, v52
M 1,20,2
WL 4
LO 16,16,122
LU 122,16
v_rcp_f32 v52, v52
M 1,21,3
s_add_u32 s42, s94, s63
RA 216,1,13312
HZ 25,122
v_mul_f32 v52, v38, v52
M 2,32,2
s_addc_u32 s43, s95, 0
LL 18,100
LL 126,101
CF 52,52,52
M 2,33,3
LA 18
PK 122,16,120,56
ds_bpermute_b32 v13, v28, v52
M 3,44,2
LC_ 126
PK 120,16,120,97
M 3,45,3
SU 116
v_pk_add_f16 v27, v27, v29
M 4,68,2
WL 4
LO 17,17,123
LU 123,17
M 4,69,3
s_add_u32 s42, s44, s64
RA 220,1,14336
HS 25,123
M 5,80,2
LL 16,102
LL 127,103
M 5,81,3
s_addc_u32 s43, s45, 0
LA 16
PK 123,17,121,56
M 0,10,4
LC_ 127
PK 121,17,121,97
M 0,11,5
ST 120
HX 27
M 1,22,4
WL 3
LO 18,18,126
LU 126,18
M 1,23,5
RA 224,1,15360
HS 25,126
AR 248,72
M 2,34,4
LL 17,56
LL 104,57
AR 249,73
M 2,35,5
LA 17
PK 126,18,124,56
AR 250,74
M 3,46,4
LC_ 104
PK 124,18,124,97
AR 251,75
M 3,47,5
ds_read_b64 v[14:15], v5 offset:0
PO 126
AR 52,76
M 4,70,4
WL 4
LO 16,16,127
LU 127,16
AR 53,77
M 4,71,5
s_add_u32 s42, s44, s65
RA 228,1,16384
HS 25,127
AR 54,78
M 5,82,4
LL 18,58
LL 105,59
AR 55,79
M 5,83,5
s_addc_u32 s43, s45, 0
LA 18
PK 127,16,125,56
XS 248
Z 6,0,0
LC_ 105
PK 125,16,125,97
XS 249
M 6,1,1
PO 127
HX 25
XS 250
Z 7,12,0
WL 4
LO 104,17,104
LU 17,104
XS 251
AW 72,248
M 7,13,1
RA 232,1,17408
HZ 26,17
AW 73,249
Z 8,24,0
WL 4
LL 16,60
LL 108,61
AW 74,250
M 8,25,1
s_mul_i32 s53, s50, 25600
LA 16
W1 25,1024
AW 75,251
Z 9,36,0
s_add_u32 s40, %[wlo], s53
LC_ 108
W1 27,3072
XS 52
M 9,37,1
s_addc_u32 s41, %[whi], 0
v_add_f32 v14, v14, v15
v_add_u32 v4, s39, v4
XS 53
Z 10,60,0
WL 5
LO 105,18,105
LU 18,105
XS 54
M 10,61,1
s_mov_b32 m0, s38
RA 236,1,18432
HS 26,18
XS 55
Z 11,72,0
LL 17,62
LL 109,63
AW 76,52
M 11,73,1
LA 17
ST 124
AW 77,53
M 11,74,2
LC_ 109
DW 0
AW 78,54
AW 79,55
M 11,75,3
v_add_f32_dpp v14, v14, v14 quad_perm:[1,0,3,2] row_mask:0xf bank_mask:0xf
v_min_u32 v35, v35, v13
M 10,62,2
WL 4
LO 108,16,108
LU 16,108
AR 248,80
M 10,63,3
s_add_u32 s37, s37, 32768
RA 216,1,19456
HS 26,16
AR 249,81
M 9,38,2
s_cmp_eq_u32 s37, s75
LL 18,64
LL 112,65
AR 250,82
M 9,39,3
s_cselect_b32 s37, s74, s37
LA 18
v_add_f32_dpp v14, v14, v14 quad_perm:[2,3,0,1] row_mask:0xf bank_mask:0xf
AR 251,83
M 8,26,2
s_mul_i32 s53, %[d], 393216
)"
R"(LC_ 112
v_add_f32_dpp v14, v14, v14 row_half_mirror row_mask:0xf bank_mask:0xf
XS 248
M 8,27,3
s_add_u32 s53, s53, s57
v_cvt_f64_f32 v[14:15], v14
XS 249
M 7,14,2
WL 3
LO 109,17,109
LU 17,109
XS 250
M 7,15,3
s_add_u32 s46, %[xlo], s53
RA 220,1,20480
HS 26,17
XS 251
M 6,2,2
LL 16,66
LL 113,67
AW 80,248
AW 81,249
M 6,3,3
s_addc_u32 s47, %[xhi], 0
LA 16
s_mov_b64 exec, s[54:55]
SP
s_mov_b64 exec, -1
AW 82,250
M 6,4,4
s_bitcmp1_b64 s[92:93], 48
LC_ 113
ds_read_b64 v[14:15], v5 offset:2048
AW 83,251
M 6,5,5
s_cselect_b32 s58, 16, 0
v_subrev_u32 v5, s39, v5
AR 52,84
M 7,16,4
WL 4
LO 112,18,112
LU 18,112
AR 53,85
M 7,17,5
s_lshr_b32 s58, 0xda43cb52, s58
RA 224,1,21504
HZ 29,18
AR 54,86
M 8,28,4
LL 17,68
LL 116,69
AR 55,87
M 8,29,5
LA 17
v_lshrrev_b32 v3, 6, v0
XS 52
M 9,40,4
LC_ 116
v_and_b32 v3, 12, v3
v_bfe_u32 v3, s58, v3, 4
v_mad_u32_u24 v3, v3, v32, v21
XS 53
M 9,41,5
DW 1024
XS 54
M 10,64,4
WL 4
LO 113,16,113
LU 16,113
XS 55
AW 84,52
M 10,65,5
RA 228,1,22528
HS 29,16
AW 85,53
M 11,76,4
LL 18,70
LL 117,71
AW 86,54
M 11,77,5
LA 18
XL 60,0
AW 87,55
M 11,78,0
LC_ 117
DW 2048
AR 248,88
M 11,79,1
WL 6
XL 61,64
v_add_f32 v14, v14, v15
AR 249,89
M 10,66,0
WL 3
LO 116,17,116
LU 17,116
AR 250,90
M 10,67,1
s_mul_i32 s53, s51, 24
RA 232,1,23552
HS 29,17
AR 251,91
M 9,42,0
s_bitcmp1_b32 s51, 0
LL 16,72
LL 120,73
XS 248
M 9,43,1
s_addc_u32 s53, s53, 0
LA 16
v_add_f32_dpp v14, v14, v14 quad_perm:[1,0,3,2] row_mask:0xf bank_mask:0xf
s_lshl_b32 s53, s53, 11
s_bitcmp1_b64 s[90:91], s51
s_cselect_b64 s[44:45], s[84:85], s[82:83]
s_add_u32 s44, s44, s53
s_addc_u32 s45, s45, 0
s_bitcmp1_b64 s[92:93], s51
s_cselect_b64 s[94:95], s[88:89], s[86:87]
s_add_u32 s94, s94, s53
s_addc_u32 s95, s95, 0
s_add_u32 s51, s51, %[d]
s_cmp_eq_u32 s51, %[wrap]
s_cselect_b32 s51, %[rst], s51
XS 249
M 8,30,0
s_add_u32 s40, s40, 3072
LC_ 120
v_add_f32_dpp v14, v14, v14 quad_perm:[2,3,0,1] row_mask:0xf bank_mask:0xf
s_addc_u32 s41, s41, 0
s_add_u32 m0, s38, 3072
XS 250
XS 251
M 8,31,1
XL 62,128
v_add_f32_dpp v14, v14, v14 row_half_mirror row_mask:0xf bank_mask:0xf
AW 88,248
M 7,18,0
WL 3
LO 117,18,117
LU 18,117
AW 89,249
M 7,19,1
RA 236,1,24576
HS 29,18
AW 90,250
M 6,6,0
LL 17,74
LL 121,75
AW 91,251
M 6,7,1
LA 17
DW 0
AR 52,92
M 6,8,2
LC_ 121
XL 63,192
AR 53,93
M 6,9,3
DW 1024
v_add_u32 v1, s37, v0
AR 54,94
M 7,20,2
WL 3
LO 120,16,120
LU 16,120
AR 55,95
M 7,21,3
RA 216,1,0
HZ 24,16
XS 52
M 8,32,2
LL 18,76
LL 124,77
v_cvt_f64_f32 v[14:15], v14
XS 53
XS 54
M 8,33,3
LA 18
s_mov_b64 exec, s[54:55]
SP
s_mov_b64 exec, -1
XS 55
M 9,44,2
LC_ 124
XL 64,256
AW 92,52
M 9,45,3
DW 2048
v_and_b32 v22, 0xfffffcff, v1
AW 93,53
M 10,68,2
WL 3
LO 121,17,121
LU 17,121
AW 94,54
M 10,69,3
RA 220,1,1024
HS 24,17
AW 95,55
M 11,80,2
LL 16,78
LL 125,79
AR 248,96
M 11,81,3
LA 16
RV 252,22,5120
AR 249,97
M 6,10,4
LC_ 125
XL 65,320
AR 250,98
M 6,11,5
s_lshl_b32 s53, s52, 8
v_pk_add_f16 v26, v26, v29
AR 251,99
M 7,22,4
WL 4
LO 124,18,124
LU 18,124
HX 26
XS 248
XS 249
M 7,23,5
s_add_u32 s48, %[alo], s53
RA 224,1,2048
HS 24,18
XS 250
M 8,34,4
s_addc_u32 s49, %[ahi], 0
LL 17,80
LL 106,81
s_add_u32 s52, s52, %[d]
s_cmp_eq_u32 s52, %[wrap]
s_cselect_b32 s52, %[rst], s52
XS 251
M 8,35,5
LA 17
DQ 3072
AW 96,248
M 9,46,4
LC_ 106
XL 66,384
AW 97,249
M 9,47,5
s_add_u32 s50, s50, %[d]
AW 98,250
AW 99,251
M 10,70,4
WL 3
LO 125,16,125
LU 16,125
M 10,71,5
RA 228,1,3072
HS 24,16
AR 52,100
M 11,82,4
LL 18,82
LL 107,83
AR 53,101
M 11,83,5
LA 18
HX 24
s_cmp_eq_u32 s50, %[wrap]
s_cselect_b32 s50, %[rst], s50
XL 67,448
AR 54,102
Z 0,0,0
WV 40
LC_ 107
XL 68,512
v_add_u32 v19, 1, v19
s_add_u32 s38, s38, 32768
s_cmp_eq_u32 s38, s77
s_cselect_b32 s38, s76, s38
FP
s_sub_u32 s39, 0, s39
AR 55,103
XS 52
M 0,1,1
XL 69,576
XS 53
Z 1,12,0
WL 4
LO 17,17,106
LU 106,17
XS 54
M 1,13,1
RA 232,1,4096
HZ 27,106
XS 55
Z 2,24,0
LL 16,84
LL 110,85
AW 100,52
M 2,25,1
LA 16
PK 106,17,104,56
AW 101,53
Z 3,36,0
WL 5
LC_ 110
XL 70,640
PK 104,17,104,97
AW 102,54
M 3,37,1
RA 236,1,6144
AW 103,55
M 3,38,2
WL 5
LO 18,18,107
LU 107,18
AR 248,104
M 3,39,3
FR
HS 27,107
AR 249,105
M 2,26,2
LL 17,86
LL 111,87
AR 250,106
AR 251,107
M 2,27,3
LA 17
PK 107,18,105,56
XS 248
M 1,14,2
LC_ 111
XL 71,704
PK 105,18,105,97
XS 249
M 1,15,3
s_add_u32 s42, s94, s60
RA 216,1,7168
XS 250
M 0,2,2
WL 4
LO 16,16,110
LU 110,16
XS 251
M 0,3,3
s_addc_u32 s43, s95, 0
RA 220,1,8192
HS 27,110
AW 104,248
M 0,4,4
LL 18,88
LL 114,89
AW 105,249
M 0,5,5
LA 18
PK 110,16,108,56
AW 106,250
M 1,16,4
LC_ 114
PK 108,16,108,97
AW 107,251
M 1,17,5
RA 224,1,9216
PO 110
AR 52,108
M 2,28,4
WL 5
LO 17,17,111
LU 111,17
SU 104
AR 53,109
AR 54,110
M 2,29,5
s_add_u32 s42, s94, s61
HS 27,111
v_cmp_gt_u32 vcc, v19, v15
AR 55,111
M 3,40,4
WL 3
LL 16,90
LL 115,91
XS 52
M 3,41,5
s_addc_u32 s43, s95, 0
LA 16
PK 111,17,109,56
XS 53
M 3,42,0
s_cbranch_vccnz .Lk1s18
.Lk1r18:
LC_ 115
PK 109,17,109,97
XS 54
M 3,43,1
WV 18
RA 228,1,10240
PO 111
XS 55
BW 60
M 2,30,0
WL 5
LO 18,18,114
LU 114,18
AW 108,52
M 2,31,1
)"
R"(RA 232,1,11264
RV 40,8,6144
HZ 29,114
AW 109,53
M 1,18,0
RV 44,8,6208
LL 17,92
LL 118,93
AW 110,54
M 1,19,1
LA 17
RV 48,8,6272
PK 114,18,112,56
AW 111,55
M 0,6,0
WL 6
LC_ 118
BR 60
PK 112,18,112,97
AR 248,112
AR 249,113
M 0,7,1
WV 16
RV 252,22,5392
ds_read_b64 v[14:15], v5 offset:0
W1 24,0
W1 26,2048
BW 61
AR 250,114
M 0,8,2
BR 61
LO 16,16,115
LU 115,16
AR 251,115
M 0,9,3
WL 6
RA 236,1,12288
HS 29,115
XS 248
LZ 40,60
WV 15
BW 62
M 1,20,2
LL 18,94
LL 119,95
XS 249
M 1,21,3
LA 18
RV 40,8,6336
PK 115,16,113,56
XS 250
M 2,32,2
LC_ 119
BR 62
PK 113,16,113,97
XS 251
M 2,33,3
WL 6
RA 216,1,13312
AW 112,248
LS 44,61
WV 13
BW 63
M 3,44,2
WL 7
LO 17,17,118
LU 118,17
SU 108
AW 113,249
M 3,45,3
s_add_u32 s42, s94, s62
RA 220,1,14336
HS 29,118
s_addc_u32 s43, s95, 0
RV 44,8,6400
AW 114,250
M 0,10,4
BR 63
LL 16,96
LL 122,97
AW 115,251
AR 52,116
M 0,11,5
WL 5
LA 16
PK 118,17,116,56
AR 53,117
LS 48,62
WV 11
BW 64
M 1,22,4
LC_ 122
PK 116,17,116,97
AR 54,118
M 1,23,5
RA 224,1,15360
RV 48,8,6464
PO 118
AR 55,119
M 2,34,4
LO 18,18,119
LU 119,18
SU 112
XS 52
M 2,35,5
s_add_u32 s42, s94, s63
BR 64
HS 29,119
XS 53
M 3,46,4
WL 6
LL 17,98
LL 123,99
XS 54
M 3,47,5
s_addc_u32 s43, s95, 0
LA 17
PK 119,18,117,56
XS 55
LS 40,63
WV 10
BW 65
Z 6,0,0
LC_ 123
PK 117,18,117,97
v_add_f32 v14, v14, v15
s_nop 1
v_add_f32_dpp v14, v14, v14 quad_perm:[1,0,3,2] row_mask:0xf bank_mask:0xf
s_nop 1
v_add_f32_dpp v14, v14, v14 quad_perm:[2,3,0,1] row_mask:0xf bank_mask:0xf
s_nop 1
v_add_f32_dpp v14, v14, v14 row_half_mirror row_mask:0xf bank_mask:0xf
v_cvt_f64_f32 v[14:15], v14
s_mov_b64 exec, s[54:55]
SP
s_mov_b64 exec, -1
s_nop 0
ds_read_b64 v[14:15], v5 offset:2048
RV 40,8,6528
AW 116,52
M 6,1,1
WL 5
RA 228,1,16384
BR 65
PO 119
AW 117,53
LS 44,64
WV 9
BW 66
Z 7,12,0
LO 16,16,122
LU 122,16
AW 118,54
AW 119,55
M 7,13,1
s_mul_i32 s53, s50, 25600
RA 232,1,17408
HZ 25,122
Z 8,24,0
s_add_u32 s40, %[wlo], s53
RV 44,8,6592
LL 18,100
LL 126,101
M 8,25,1
s_addc_u32 s41, %[whi], 0
LA 18
BR 66
PK 122,16,120,56
v_pk_add_f16 v27, v27, v29
Z 9,36,0
WL 5
LC_ 126
PK 120,16,120,97
HX 27
M 9,37,1
s_mov_b32 m0, s38
RA 236,1,18432
v_subrev_u32 v5, s39, v5
LS 48,65
WV 8
BW 67
M 9,38,2
RV 48,8,6656
LO 17,17,123
LU 123,17
M 9,39,3
DW 0
BR 67
HS 25,123
M 8,26,2
LL 16,102
LL 127,103
M 8,27,3
WL 5
LA 16
PK 123,17,121,56
LS 40,66
WV 8
BW 68
M 7,14,2
LC_ 127
RV 40,8,6720
PK 121,17,121,97
M 7,15,3
WV 7
RA 216,1,19456
BR 68
M 6,2,2
WL 6
LO 18,18,126
LU 126,18
SU 116
M 6,3,3
s_add_u32 s42, s44, s64
RA 220,1,20480
HS 25,126
s_addc_u32 s43, s45, 0
LS 44,67
s_add_u32 s37, s37, 32768
BW 69
M 6,4,4
s_cmp_eq_u32 s37, s75
LL 17,56
LL 104,57
s_cselect_b32 s37, s74, s37
M 6,5,5
LA 17
PK 126,18,124,56
v_add_f32 v14, v14, v15
s_nop 1
v_add_f32_dpp v14, v14, v14 quad_perm:[1,0,3,2] row_mask:0xf bank_mask:0xf
s_nop 1
v_add_f32_dpp v14, v14, v14 quad_perm:[2,3,0,1] row_mask:0xf bank_mask:0xf
s_nop 1
v_add_f32_dpp v14, v14, v14 row_half_mirror row_mask:0xf bank_mask:0xf
v_cvt_f64_f32 v[14:15], v14
s_mov_b64 exec, s[54:55]
SP
s_mov_b64 exec, -1
DW 1024
RV 44,8,6784
M 7,16,4
LC_ 104
BR 69
PK 124,18,124,97
M 7,17,5
WL 6
RA 224,1,21504
PO 126
LS 48,68
WV 9
BW 70
M 8,28,4
LO 16,16,127
LU 127,16
ST 120
M 8,29,5
WV 9
DW 2048
RV 48,8,6848
HS 25,127
M 9,40,4
WL 3
RA 240,7,0
LL 18,58
LL 105,59
M 9,41,5
s_add_u32 s40, s40, 3072
LA 18
BR 70
PK 127,16,125,56
M 9,42,0
s_addc_u32 s41, s41, 0
LC_ 105
PK 125,16,125,97
s_add_u32 m0, s38, 3072
M 9,43,1
s_add_u32 s42, s44, s65
RA 228,1,22528
PO 127
LS 40,69
s_addc_u32 s43, s45, 0
BW 71
M 8,30,0
RA 244,7,256
LO 104,17,104
LU 17,104
M 8,31,1
RA 232,1,23552
BR 71
HZ 26,17
M 7,18,0
WL 6
DW 0
LL 16,60
LL 108,61
M 7,19,1
s_lshl_b32 s53, s52, 8
LA 16
HX 25
LS 44,70
s_add_u32 s48, %[alo], s53
RA 248,7,512
M 6,6,0
WL 2
LC_ 108
W1 25,1024
W1 27,3072
M 6,7,1
s_addc_u32 s49, %[ahi], 0
RA 236,1,24576
v_add_u32 v4, s39, v4
LS 48,71
s_add_u32 s52, s52, %[d]
RA 252,7,768
M 6,8,2
LO 105,18,105
LU 18,105
s_cmp_eq_u32 s52, %[wrap]
s_cselect_b32 s52, %[rst], s52
DW 1024
v_mul_f32 v36, 0xbb2aaaab, v36
M 6,9,3
DW 2048
HS 26,18
v_mul_f32 v37, 0xbb2aaaab, v37
v_mul_f32 v38, 0xbb2aaaab, v38
M 7,20,2
LL 17,62
LL 109,63
v_mul_f32 v39, 0xbb2aaaab, v39
M 7,21,3
LA 17
ST 124
LK 40,240,60,0
LK 44,242,60,2
s_mul_i32 s53, s51, 24
RA 240,7,1024
v_add_u32 v1, s37, v0
LK 48,244,61,0
CF 40,40,41
LK 248,246,61,2
s_bitcmp1_b32 s51, 0
RA 244,7,1280
CF 42,42,43
M 8,32,2
s_addc_u32 s53, s53, 0
LC_ 109
RA 216,1,0
M 8,33,3
s_lshl_b32 s53, s53, 11
v_and_b32 v22, 0xfffffcff, v1
CF 44,44,45
CF 46,46,47
M 9,44,2
WL 6
LO 108,16,108
LU 16,108
AW 120,40
AW 121,42
M 9,45,3
s_bitcmp1_b64 s[92:93], s51
RA 220,1,1024
HS 26,16
s_cselect_b64 s[94:95], s[88:89], s[86:87]
s_add_u32 s94, s94, s53
s_addc_u32 s95, s95, 0
s_add_u32 s51, s51, %[d]
s_cmp_eq_u32 s51, %[wrap]
s_cselect_b32 s51, %[rst], s51
DQ 3072
AW 122,44
AW 123,46
LK 40,248,62,0
CF 48,48,49
LK 44,250,62,2
s_add_u32 s50, s50, %[d]
RA 248,7,1536
CF 50,50,51
GZ 60
s_cmp_eq_u32 s50, %[wrap]
CF 248,248,249
CF 250,250,251
M 6,10,4
s_cselect_b32 s50, %[rst], s50
LL 18,64
LL 112,65
M 6,11,5
LA 18
RV 252,22,5120
M 7,22,4
WL 7
LC_ 112
v_add_u32 v19, 1, v19
AW 124,48
M 7,23,5
s_add_u32 s38, s38, 32768
RA 224,1,2048
AW 125,50
AW 126,248
)"
R"(AW 127,250
LK 48,252,63,0
CF 40,40,41
LK 248,254,63,2
s_cmp_eq_u32 s38, s77
RA 252,7,1792
CF 42,42,43
GS 61
s_cselect_b32 s38, s76, s38
CF 44,44,45
CF 46,46,47
M 8,34,4
WL 7
LO 109,17,109
LU 17,109
M 8,35,5
FP
HS 26,17
M 9,46,4
WL 4
LL 16,66
LL 113,67
M 9,47,5
s_sub_u32 s39, 0, s39
LA 16
RA 228,1,3072
AW 128,40
AW 129,42
Z 0,0,0
LC_ 113
AW 130,44
AW 131,46
M 0,1,1
LK 40,240,64,0
CF 48,48,49
LK 44,242,64,2
RA 240,7,2048
CF 50,50,51
GS 62
CF 248,248,249
CF 250,250,251
Z 1,12,0
WL 7
LO 112,18,112
LU 18,112
M 1,13,1
RA 232,1,4096
HZ 29,18
AW 132,48
Z 2,24,0
LL 17,68
LL 116,69
AW 133,50
AW 134,248
M 2,25,1
LA 17
FR
AW 135,250
LK 48,244,65,0
CF 40,40,41
LK 248,246,65,2
RA 244,7,2304
CF 42,42,43
GS 63
CF 44,44,45
CF 46,46,47
Z 10,36,0
WL 6
LC_ 116
AW 136,40
M 10,37,1
RA 236,1,6144
AW 137,42
M 10,38,2
WL 7
LO 113,16,113
LU 16,113
M 10,39,3
HS 29,16
AW 138,44
AW 139,46
LK 40,248,66,0
CF 48,48,49
LK 44,250,66,2
RA 248,7,2560
CF 50,50,51
GS 64
CF 248,248,249
CF 250,250,251
M 2,26,2
LL 18,70
LL 117,71
M 2,27,3
LA 18
RA 216,1,7168
M 1,14,2
WL 6
LC_ 117
v_cmp_gt_u32 vcc, v19, v15
M 1,15,3
AW 140,48
AW 141,50
AW 142,248
M 0,2,2
WL 4
LO 116,17,116
LU 17,116
AW 143,250
M 0,3,3
RA 220,1,8192
HS 29,17
LK 48,252,67,0
CF 40,40,41
LK 248,254,67,2
RA 252,7,2816
CF 42,42,43
GS 65
CF 44,44,45
CF 46,46,47
M 0,4,4
s_cbranch_vccnz .Lk1s19
.Lk1r19:
LL 16,80
LL 106,81
M 0,5,5
LA 16
ds_read_b64 v[14:15], v5 offset:0
AW 144,40
AW 145,42
M 1,16,4
LC_ 106
AW 146,44
AW 147,46
M 1,17,5
RA 224,1,9216
LK 40,240,68,0
CF 48,48,49
LK 44,242,68,2
CF 50,50,51
GS 66
CF 248,248,249
CF 250,250,251
M 2,28,4
WL 6
LO 117,18,117
LU 18,117
M 2,29,5
HS 29,18
AW 148,48
M 10,40,4
WL 5
LL 17,82
LL 107,83
AW 149,50
AW 150,248
AW 151,250
M 10,41,5
LA 17
RA 228,1,10240
LK 48,244,69,0
CF 40,40,41
LK 248,246,69,2
CF 42,42,43
v_pk_add_f16 v26, v26, v29
GS 67
CF 44,44,45
CF 46,46,47
M 10,42,0
LC_ 107
HX 26
M 10,43,1
W1 26,2048
AW 152,40
M 2,30,0
WL 5
LO 16,16,106
LU 106,16
AW 153,42
AW 154,44
M 2,31,1
RA 232,1,11264
HZ 27,106
AW 155,46
LK 40,248,70,0
CF 48,48,49
LK 44,250,70,2
CF 50,50,51
v_add_f32 v14, v14, v15
GS 68
CF 248,248,249
CF 250,250,251
M 1,18,0
LL 18,84
LL 110,85
M 1,19,1
LA 18
PK 106,16,104,56
M 0,6,0
WL 4
LC_ 110
PK 104,16,104,97
AW 156,48
AW 157,50
AW 158,248
M 0,7,1
RV 252,22,5392
v_add_f32_dpp v14, v14, v14 quad_perm:[1,0,3,2] row_mask:0xf bank_mask:0xf
AW 159,250
LK 48,252,71,0
CF 40,40,41
LK 248,254,71,2
CF 42,42,43
v_add_f32_dpp v14, v14, v14 quad_perm:[2,3,0,1] row_mask:0xf bank_mask:0xf
GS 69
CF 44,44,45
CF 46,46,47
M 0,8,2
WL 5
LO 17,17,107
LU 107,17
M 0,9,3
s_add_u32 s42, s94, s60
RA 236,1,12288
HS 27,107
M 1,20,2
s_addc_u32 s43, s95, 0
LL 16,86
LL 111,87
AW 160,40
AW 161,42
M 1,21,3
LA 16
RV 36,8,6912
PK 107,17,105,56
AW 162,44
AW 163,46
M 2,32,2
LC_ 111
PK 105,17,105,97
M 2,33,3
RA 216,1,13312
v_add_f32_dpp v14, v14, v14 row_half_mirror row_mask:0xf bank_mask:0xf
GS 70
CF 48,48,49
CF 50,50,51
CF 248,248,249
CF 250,250,251
M 10,44,2
WL 4
LO 18,18,110
LU 110,18
AW 164,48
AW 165,50
AW 166,248
M 10,45,3
RA 220,1,14336
HS 27,110
AW 167,250
s_nop 1
GS 71
SU 104
v_cvt_f64_f32 v[14:15], v14
s_nop 5
v_cndmask_b32 v52, v52, v53, s[78:79]
M 0,10,4
LL 17,88
LL 114,89
v_cndmask_b32 v54, v54, v55, s[78:79]
M 0,11,5
LA 17
PK 110,18,108,56
v_cndmask_b32 v52, v52, v54, s[80:81]
M 1,22,4
LC_ 114
PK 108,18,108,97
v_mul_f32 v52, 0x3b2aaaab, v52
M 1,23,5
WL 5
RA 224,1,15360
PO 110
v_med3_f32 v53, v52, v39, v37
M 2,34,4
WL 5
LO 16,16,111
LU 111,16
s_mov_b64 exec, s[54:55]
SP
s_mov_b64 exec, -1
LG 52,53
s_nop 0
C 52,52,52
M 2,35,5
s_add_u32 s42, s94, s61
ds_read_b64 v[14:15], v5 offset:2048
HS 27,111
v_lshlrev_b32 v52, 16, v52
M 10,46,4
WL 4
LL 18,90
LL 115,91
v_add_f32 v52, v36, v52
M 10,47,5
s_addc_u32 s43, s95, 0
LA 18
PK 111,16,109,56
C 52,52,52
Z 6,0,0
LC_ 115
PK 109,16,109,97
v_lshlrev_b32 v52, 16, v52
M 6,1,1
RA 228,1,16384
PO 111
v_sqrt_f32 v52, v52
s_nop 0
C 52,52,52
Z 7,12,0
WL 5
LO 17,17,114
LU 114,17
v_lshlrev_b32 v52, 16, v52
M 7,13,1
RA 232,1,17408
HZ 29,114
v_rcp_f32 v52, v52
Z 8,24,0
LL 16,92
LL 118,93
v_mul_f32 v52, v38, v52
M 8,25,1
LA 16
PK 114,17,112,56
v_subrev_u32 v5, s39, v5
CF 52,52,52
Z 11,36,0
WL 2
LC_ 118
PK 112,17,112,97
ds_bpermute_b32 v13, v28, v52
M 11,37,1
RA 236,1,18432
v_add_f32 v14, v14, v15
M 11,38,2
LO 18,18,115
LU 115,18
M 11,39,3
SU 108
HS 29,115
v_add_f32_dpp v14, v14, v14 quad_perm:[1,0,3,2] row_mask:0xf bank_mask:0xf
M 8,26,2
s_mul_i32 s53, s50, 25600
LL 17,94
LL 119,95
s_add_u32 s40, %[wlo], s53
s_addc_u32 s41, %[whi], 0
s_mov_b32 m0, s38
M 8,27,3
LA 17
PK 115,18,113,56
v_add_f32_dpp v14, v14, v14 quad_perm:[2,3,0,1] row_mask:0xf bank_mask:0xf
M 7,14,2
LC_ 119
PK 113,18,113,97
M 7,15,3
RA 216,1,19456
DW 0
v_add_f32_dpp v14, v14, v14 row_half_mirror row_mask:0xf bank_mask:0xf
M 6,2,2
WL 3
LO 16,16,118
LU 118,16
M 6,3,3
s_add_u32 s42, s94, s62
RA 220,1,20480
HS 29,118
v_cvt_f64_f32 v[14:15], v14
s_addc_u32 s43, s95, 0
AR 248,120
M 6,4,4
s_add_u32 s37, s37, 32768
LL 18,56
LL 104,57
s_cmp_eq_u32 s37, s75
s_cselect_b32 s37, s74, s37
AR 249,121
M 6,5,5
LA 18
PK 118,16,116,56
s_mov_b64 exec, s[54:55]
SP
s_mov_b64 exec, -1
DW 1024
AR 250,122
M 7,16,4
LC_ 104
PK 116,16,116,97
AR 251,123
AR 52,124
M 7,17,5
RA 224,1,21504
)"
R"(PO 118
AR 53,125
M 8,28,4
WL 5
LO 17,17,119
LU 119,17
SU 112
AR 54,126
AR 55,127
M 8,29,5
DW 2048
HS 29,119
XS 248
M 11,40,4
WL 3
LL 16,58
LL 105,59
XS 249
M 11,41,5
s_add_u32 s40, s40, 3072
LA 16
PK 119,17,117,56
XS 250
XS 251
M 11,42,0
s_addc_u32 s41, s41, 0
LC_ 105
PK 117,17,117,97
s_add_u32 m0, s38, 3072
AW 120,248
M 11,43,1
s_add_u32 s42, s94, s63
RA 228,1,22528
PO 119
AW 121,249
AW 122,250
M 8,30,0
WL 4
LO 104,18,104
LU 18,104
AW 123,251
M 8,31,1
s_addc_u32 s43, s95, 0
RA 232,1,23552
HZ 26,18
XS 52
XS 53
M 7,18,0
DW 0
LL 17,60
LL 108,61
XS 54
M 7,19,1
s_lshl_b32 s53, s52, 8
LA 17
v_pk_add_f16 v27, v27, v29
XS 55
M 6,6,0
WL 2
LC_ 108
HX 27
AW 124,52
AW 125,53
M 6,7,1
s_add_u32 s48, %[alo], s53
RA 236,1,24576
W1 27,3072
AW 126,54
M 6,8,2
LO 105,16,105
LU 16,105
s_addc_u32 s49, %[ahi], 0
s_add_u32 s52, s52, %[d]
s_cmp_eq_u32 s52, %[wrap]
s_cselect_b32 s52, %[rst], s52
DW 1024
AW 127,55
v_min_u32 v35, v35, v13
M 6,9,3
SU 116
HS 26,16
AR 248,128
M 7,20,2
s_mul_i32 s53, s51, 24
LL 18,62
LL 109,63
s_bitcmp1_b32 s51, 0
s_addc_u32 s53, s53, 0
s_lshl_b32 s53, s53, 11
s_bitcmp1_b64 s[92:93], s51
s_cselect_b64 s[94:95], s[88:89], s[86:87]
s_add_u32 s94, s94, s53
s_addc_u32 s95, s95, 0
s_add_u32 s51, s51, %[d]
s_cmp_eq_u32 s51, %[wrap]
s_cselect_b32 s51, %[rst], s51
AR 249,129
AR 250,130
M 7,21,3
LA 18
DW 2048
AR 251,131
M 8,32,2
LC_ 109
DQ 3072
XS 248
M 8,33,3
s_add_u32 s50, s50, %[d]
v_add_u32 v1, s37, v0
v_add_u32 v4, s39, v4
XS 249
XS 250
M 11,44,2
WL 3
LO 108,17,108
LU 17,108
XS 251
M 11,45,3
s_cmp_eq_u32 s50, %[wrap]
RA 216,1,0
HS 26,17
AW 128,248
AW 129,249
M 6,10,4
s_cselect_b32 s50, %[rst], s50
LL 16,64
LL 112,65
AW 130,250
M 6,11,5
LA 16
v_and_b32 v22, 0xfffffcff, v1
AW 131,251
AR 52,132
M 7,22,4
LC_ 112
RA 220,1,1024
AR 53,133
M 7,23,5
RV 252,22,5120
v_add_u32 v19, 1, v19
AR 54,134
M 8,34,4
WL 5
LO 109,18,109
LU 18,109
AR 55,135
XS 52
M 8,35,5
s_add_u32 s38, s38, 32768
RA 224,1,2048
HS 26,18
XS 53
M 11,46,4
WL 1
LL 17,66
LL 113,67
XS 54
XS 55
M 11,47,5
s_cmp_eq_u32 s38, s77
LA 17
RA 228,1,3072
s_cselect_b32 s38, s76, s38
FP
s_sub_u32 s39, 0, s39
AW 132,52
Z 0,0,0
LC_ 113
AW 133,53
AW 134,54
M 0,1,1
AW 135,55
AR 248,136
Z 1,12,0
LO 112,16,112
LU 16,112
M 1,13,1
RA 232,1,4096
HZ 29,16
AR 249,137
AR 250,138
Z 2,24,0
LL 18,96
LL 116,97
AR 251,139
M 2,25,1
LA 18
XS 248
XS 249
Z 3,36,0
LC_ 116
XS 250
M 3,37,1
XS 251
AW 136,248
Z 4,48,0
WL 3
LO 113,17,113
LU 17,113
AW 137,249
M 4,49,1
RA 236,1,6144
HS 29,17
AW 138,250
Z 5,60,0
LL 16,98
LL 117,99
AW 139,251
AR 52,140
M 5,61,1
LA 16
AR 53,141
M 5,62,2
LC_ 117
AR 54,142
AR 55,143
M 5,63,3
XS 52
XS 53
M 4,50,2
WL 3
LO 116,18,116
LU 18,116
XS 54
M 4,51,3
RA 216,1,7168
HS 29,18
XS 55
M 3,38,2
LL 17,80
LL 106,81
AW 140,52
M 3,39,3
LA 17
FR
AW 141,53
AW 142,54
M 2,26,2
LC_ 106
AW 143,55
M 2,27,3
AR 248,144
AR 249,145
M 1,14,2
WL 4
LO 117,16,117
LU 16,117
AR 250,146
M 1,15,3
RA 220,1,8192
HS 29,16
AR 251,147
M 0,2,2
LL 18,82
LL 107,83
XS 248
XS 249
M 0,3,3
LA 18
v_pk_add_f16 v26, v26, v29
XS 250
M 0,4,4
WL 3
LC_ 107
v_cmp_gt_u32 vcc, v19, v15
XS 251
AW 144,248
M 0,5,5
HX 26
AW 145,249
M 1,16,4
WL 3
LO 17,17,106
LU 106,17
AW 146,250
AW 147,251
M 1,17,5
RA 224,1,9216
HZ 27,106
AR 52,148
M 2,28,4
s_cbranch_vccnz .Lk1s20
.Lk1r20:
LL 16,84
LL 110,85
AR 53,149
M 2,29,5
LA 16
PK 106,17,104,56
AR 54,150
AR 55,151
M 3,40,4
LC_ 110
PK 104,17,104,97
XS 52
M 3,41,5
W1 26,2048
XS 53
XS 54
M 4,52,4
WL 4
LO 18,18,107
LU 107,18
XS 55
M 4,53,5
s_add_u32 s42, s94, s60
RA 228,1,10240
HS 27,107
AW 148,52
AW 149,53
M 5,64,4
LL 17,86
LL 111,87
AW 150,54
AW 151,55
M 5,65,5
s_addc_u32 s43, s95, 0
LA 17
PK 107,18,105,56
M 5,66,0
LC_ 111
PK 105,18,105,97
AR 248,152
AR 249,153
M 5,67,1
SU 104
AR 250,154
M 4,54,0
WL 4
LO 16,16,110
LU 110,16
AR 251,155
XS 248
M 4,55,1
RA 232,1,11264
HS 27,110
XS 249
M 3,42,0
LL 18,88
LL 114,89
XS 250
XS 251
M 3,43,1
LA 18
PK 110,16,108,56
AW 152,248
M 2,30,0
LC_ 114
PK 108,16,108,97
AW 153,249
M 2,31,1
RV 252,22,5392
PO 110
AW 154,250
AW 155,251
M 1,18,0
WL 4
LO 17,17,111
LU 111,17
AR 52,156
M 1,19,1
s_add_u32 s42, s94, s61
RA 236,1,12288
HS 27,111
AR 53,157
AR 54,158
M 0,6,0
LL 16,90
LL 115,91
AR 55,159
M 0,7,1
s_addc_u32 s43, s95, 0
LA 16
PK 111,17,109,56
XS 52
XS 53
M 0,8,2
LC_ 115
PK 109,17,109,97
XS 54
M 0,9,3
PO 111
XS 55
M 1,20,2
WL 4
LO 18,18,114
LU 114,18
AW 156,52
AW 157,53
M 1,21,3
RA 216,1,13312
HZ 29,114
AW 158,54
M 2,32,2
LL 17,100
LL 118,101
AW 159,55
AR 248,160
M 2,33,3
LA 17
)"
R"(PK 114,18,112,56
AR 249,161
M 3,44,2
LC_ 118
PK 112,18,112,97
AR 250,162
AR 251,163
M 3,45,3
SU 108
XS 248
M 4,56,2
WL 3
LO 16,16,115
LU 115,16
XS 249
M 4,57,3
s_add_u32 s42, s94, s62
RA 220,1,14336
HS 29,115
XS 250
XS 251
M 5,68,2
LL 18,102
LL 119,103
AW 160,248
M 5,69,3
s_addc_u32 s43, s95, 0
LA 18
PK 115,16,113,56
AW 161,249
AW 162,250
M 0,10,4
LC_ 119
PK 113,16,113,97
AW 163,251
M 0,11,5
SU 112
AR 52,164
AR 53,165
M 1,22,4
WL 3
LO 17,17,118
LU 118,17
AR 54,166
M 1,23,5
RA 224,1,15360
HS 29,118
AR 55,167
M 2,34,4
LL 16,56
LL 104,57
XS 52
XS 53
M 2,35,5
LA 16
PK 118,17,116,56
XS 54
M 3,46,4
LC_ 104
PK 116,17,116,97
XS 55
AW 164,52
M 3,47,5
ds_read_b64 v[14:15], v5 offset:2048
PO 118
AW 165,53
M 4,58,4
WL 4
LO 18,18,119
LU 119,18
AW 166,54
AW 167,55
M 4,59,5
s_add_u32 s42, s94, s63
RA 228,1,16384
HS 29,119
M 5,70,4
LL 17,58
LL 105,59
M 5,71,5
s_addc_u32 s43, s95, 0
LA 17
PK 119,18,117,56
Z 6,0,0
LC_ 105
PK 117,18,117,97
M 6,1,1
PO 119
v_subrev_u32 v5, s39, v5
Z 7,12,0
WL 4
LO 104,16,104
LU 16,104
M 7,13,1
RA 232,1,17408
HZ 26,16
Z 8,24,0
LL 18,60
LL 108,61
M 8,25,1
WL 4
LA 18
SU 116
Z 9,36,0
s_mul_i32 s53, s50, 25600
LC_ 108
v_add_f32 v14, v14, v15
M 9,37,1
s_add_u32 s40, %[wlo], s53
v_add_f32_dpp v14, v14, v14 quad_perm:[1,0,3,2] row_mask:0xf bank_mask:0xf
v_pk_add_f16 v27, v27, v29
Z 10,48,0
WL 3
LO 105,17,105
LU 17,105
M 10,49,1
s_addc_u32 s41, %[whi], 0
RA 236,1,18432
HS 26,17
Z 11,60,0
LL 16,62
LL 109,63
M 11,61,1
s_mov_b32 m0, s38
LA 16
DW 0
M 11,62,2
LC_ 109
v_add_f32_dpp v14, v14, v14 quad_perm:[2,3,0,1] row_mask:0xf bank_mask:0xf
M 11,63,3
s_nop 0
v_add_f32_dpp v14, v14, v14 row_half_mirror row_mask:0xf bank_mask:0xf
HX 27
M 10,50,2
WL 3
LO 108,18,108
LU 18,108
M 10,51,3
s_add_u32 s37, s37, 32768
RA 216,1,19456
HS 26,18
W1 27,3072
M 9,38,2
s_cmp_eq_u32 s37, s75
LL 17,64
LL 112,65
M 9,39,3
s_cselect_b32 s37, s74, s37
LA 17
DW 1024
M 8,26,2
LC_ 112
v_cvt_f64_f32 v[14:15], v14
M 8,27,3
s_mov_b64 exec, s[54:55]
SP
s_mov_b64 exec, -1
v_add_u32 v4, s39, v4
M 7,14,2
WL 4
LO 109,16,109
LU 16,109
M 7,15,3
RA 220,1,20480
HS 26,16
M 6,2,2
LL 18,66
LL 113,67
M 6,3,3
LA 18
DW 2048
M 6,4,4
LC_ 113
M 6,5,5
M 7,16,4
WL 3
LO 112,17,112
LU 17,112
M 7,17,5
s_mul_i32 s53, s51, 24
RA 224,1,21504
HZ 29,17
M 8,28,4
s_bitcmp1_b32 s51, 0
LL 16,68
LL 116,69
M 8,29,5
s_addc_u32 s53, s53, 0
LA 16
M 9,40,4
s_lshl_b32 s53, s53, 11
LC_ 116
M 9,41,5
s_bitcmp1_b64 s[92:93], s51
M 10,52,4
WL 3
LO 113,18,113
LU 18,113
M 10,53,5
s_cselect_b64 s[94:95], s[88:89], s[86:87]
RA 228,1,22528
HS 29,18
M 11,64,4
LL 17,70
LL 117,71
M 11,65,5
s_add_u32 s94, s94, s53
LA 17
M 11,66,0
s_addc_u32 s95, s95, 0
LC_ 117
M 11,67,1
s_add_u32 s51, s51, %[d]
M 10,54,0
WL 3
LO 116,16,116
LU 16,116
M 10,55,1
s_cmp_eq_u32 s51, %[wrap]
RA 232,1,23552
HS 29,16
M 9,42,0
s_cselect_b32 s51, %[rst], s51
LL 18,72
LL 120,73
M 9,43,1
s_mul_i32 s53, %[d], 32768
LA 18
s_add_u32 s64, s53, %[sb1]
s_add_u32 s65, s64, 512
M 8,30,0
s_add_u32 s40, s40, 3072
LC_ 120
s_addc_u32 s41, s41, 0
s_add_u32 m0, s38, 3072
M 8,31,1
DW 0
M 7,18,0
WL 3
LO 117,17,117
LU 17,117
M 7,19,1
RA 236,1,24576
HS 29,17
M 6,6,0
LL 16,74
LL 121,75
M 6,7,1
LA 16
DW 1024
M 6,8,2
LC_ 121
DW 2048
M 6,9,3
s_lshl_b32 s53, s52, 8
v_add_u32 v1, s37, v0
M 7,20,2
WL 3
LO 120,18,120
LU 18,120
M 7,21,3
s_add_u32 s48, %[alo], s53
RA 216,1,0
HZ 30,18
M 8,32,2
s_addc_u32 s49, %[ahi], 0
LL 17,76
LL 124,77
M 8,33,3
s_add_u32 s52, s52, %[d]
LA 17
v_and_b32 v22, 0xfffffcff, v1
M 9,44,2
s_cmp_eq_u32 s52, %[wrap]
LC_ 124
M 9,45,3
s_cselect_b32 s52, %[rst], s52
DQ 3072
M 10,56,2
WL 3
LO 121,16,121
LU 16,121
M 10,57,3
s_add_u32 s50, s50, %[d]
RA 220,1,1024
HS 30,16
M 11,68,2
LL 18,78
LL 125,79
M 11,69,3
s_cmp_eq_u32 s50, %[wrap]
LA 18
RV 252,22,5120
M 6,10,4
s_cselect_b32 s50, %[rst], s50
LC_ 125
M 6,11,5
WV 25
v_add_u32 v19, 1, v19
M 7,22,4
WL 4
LO 124,17,124
LU 17,124
M 7,23,5
s_add_u32 s38, s38, 32768
RA 224,1,2048
HS 30,17
M 8,34,4
s_cmp_eq_u32 s38, s77
LL 16,80
LL 106,81
M 8,35,5
s_cselect_b32 s38, s76, s38
LA 16
FP
M 9,46,4
s_sub_u32 s39, 0, s39
LC_ 106
M 9,47,5
M 10,58,4
WL 4
LO 125,18,125
LU 18,125
M 10,59,5
RA 228,1,3072
HS 30,18
M 11,70,4
LL 17,82
LL 107,83
M 11,71,5
LA 17
v_pk_add_f16 v26, v26, v30
Z 0,0,0
LC_ 107
v_pk_add_f16 v26, v26, v29
M 0,1,1
HX 26
Z 1,12,0
WL 3
LO 16,16,106
LU 106,16
M 1,13,1
RA 232,1,4096
HZ 27,106
Z 2,24,0
LL 18,84
LL 110,85
M 2,25,1
LA 18
PK 106,16,104,56
Z 3,36,0
LC_ 110
PK 104,16,104,97
M 3,37,1
Z 4,48,0
WL 3
LO 17,17,107
LU 107,17
M 4,49,1
s_add_u32 s42, s94, s60
RA 236,1,6144
HS 27,107
Z 5,60,0
LL 16,86
LL 111,87
M 5,61,1
s_addc_u32 s43, s95, 0
LA 16
PK 107,17,105,56
M 5,62,2
LC_ 111
PK 105,17,105,97
M 5,63,3
SU 104
M 4,50,2
WL 3
LO 18,18,110
LU 110,18
M 4,51,3
RA 216,1,7168
HS 27,110
M 3,38,2
LL 17,88
LL 114,89
M 3,39,3
LA 17
PK 110,18,108,56
M 2,26,2
LC_ 114
PK 108,18,108,97
M 2,27,3
FR
PO 110
M 1,14,2
WL 4
LO 16,16,111
LU 111,16
M 1,15,3
s_add_u32 s42, s94, s61
RA 220,1,8192
)"
R"(HS 27,111
M 0,2,2
LL 18,90
LL 115,91
M 0,3,3
s_addc_u32 s43, s95, 0
LA 18
PK 111,16,109,56
M 0,4,4
PFN
LC_ 115
PK 109,16,109,97
M 0,5,5
s_mov_b32 m0, %[pm]
s_nop 0
DF
PO 111
M 1,16,4
WL 4
LO 17,17,114
LU 114,17
M 1,17,5
RA 224,1,9216
HZ 29,114
M 2,28,4
LL 16,92
LL 118,93
M 2,29,5
LA 16
PK 114,17,112,56
M 3,40,4
LC_ 118
PK 112,17,112,97
M 3,41,5
WL 6
SU 108
v_cmp_gt_u32 vcc, v19, v15
M 4,52,4
WL 3
LO 18,18,115
LU 115,18
M 4,53,5
s_add_u32 s42, s94, s62
RA 228,1,10240
HS 29,115
M 5,64,4
LL 17,94
LL 119,95
M 5,65,5
s_cbranch_vccnz .Lk1s21
.Lk1r21:
LA 17
PK 115,18,113,56
M 5,66,0
s_addc_u32 s43, s95, 0
LC_ 119
PK 113,18,113,97
M 5,67,1
W1 26,2048
M 4,54,0
WL 4
LO 16,16,118
LU 118,16
M 4,55,1
RA 232,1,11264
HS 29,118
M 3,42,0
LL 18,96
LL 122,97
M 3,43,1
LA 18
PK 118,16,116,56
M 2,30,0
LC_ 122
PK 116,16,116,97
M 2,31,1
RV 252,22,5392
PO 118
M 1,18,0
WL 5
LO 17,17,119
LU 119,17
M 1,19,1
RA 236,1,12288
HS 29,119
M 0,6,0
LL 16,98
LL 123,99
M 0,7,1
LA 16
PK 119,17,117,56
M 0,8,2
LC_ 123
PK 117,17,117,97
M 0,9,3
SU 112
PO 119
M 1,20,2
WL 4
LO 18,18,122
LU 122,18
M 1,21,3
s_add_u32 s42, s94, s63
RA 216,1,13312
HZ 30,122
M 2,32,2
s_addc_u32 s43, s95, 0
LL 17,100
LL 126,101
M 2,33,3
LA 17
PK 122,18,120,56
M 3,44,2
LC_ 126
PK 120,18,120,97
M 3,45,3
SU 116
M 4,56,2
WL 3
LO 16,16,123
LU 123,16
M 4,57,3
s_add_u32 s42, s94, s64
RA 220,1,14336
HS 30,123
M 5,68,2
LL 18,102
LL 127,103
M 5,69,3
s_addc_u32 s43, s95, 0
LA 18
PK 123,16,121,56
M 0,10,4
LC_ 127
PK 121,16,121,97
M 0,11,5
SU 120
M 1,22,4
WL 3
LO 17,17,126
LU 126,17
M 1,23,5
RA 224,1,15360
HS 30,126
M 2,34,4
LL 16,56
LL 104,57
M 2,35,5
LA 16
PK 126,17,124,56
M 3,46,4
LC_ 104
PK 124,17,124,97
M 3,47,5
ds_read_b64 v[14:15], v5 offset:2048
PO 126
M 4,58,4
WL 4
LO 18,18,127
LU 127,18
M 4,59,5
s_add_u32 s42, s94, s65
RA 228,1,16384
HS 30,127
M 5,70,4
LL 17,58
LL 105,59
M 5,71,5
s_addc_u32 s43, s95, 0
LA 17
PK 127,18,125,56
Z 6,0,0
LC_ 105
PK 125,18,125,97
M 6,1,1
PO 127
v_subrev_u32 v5, s39, v5
Z 7,12,0
WL 4
LO 104,16,104
LU 16,104
M 7,13,1
RA 232,1,17408
HZ 26,16
Z 8,24,0
WL 4
LL 18,60
LL 108,61
M 8,25,1
s_mul_i32 s53, s50, 25600
LA 18
v_add_f32 v14, v14, v15
Z 9,36,0
s_add_u32 s40, %[wlo], s53
LC_ 108
v_add_f32_dpp v14, v14, v14 quad_perm:[1,0,3,2] row_mask:0xf bank_mask:0xf
M 9,37,1
s_addc_u32 s41, %[whi], 0
v_add_f32_dpp v14, v14, v14 quad_perm:[2,3,0,1] row_mask:0xf bank_mask:0xf
v_pk_add_f16 v27, v27, v30
Z 10,48,0
WL 3
LO 105,17,105
LU 17,105
M 10,49,1
s_mov_b32 m0, s38
RA 236,1,18432
HS 26,17
Z 11,60,0
LL 16,62
LL 109,63
M 11,61,1
LA 16
SU 124
M 11,62,2
LC_ 109
DW 0
M 11,63,3
v_add_f32_dpp v14, v14, v14 row_half_mirror row_mask:0xf bank_mask:0xf
v_pk_add_f16 v27, v27, v29
M 10,50,2
WL 3
LO 108,18,108
LU 18,108
M 10,51,3
s_add_u32 s37, s37, 32768
RA 216,1,19456
HS 26,18
HX 27
M 9,38,2
s_cmp_eq_u32 s37, s75
LL 17,64
LL 112,65
W1 27,3072
M 9,39,3
s_cselect_b32 s37, s74, s37
LA 17
DW 1024
M 8,26,2
LC_ 112
v_cvt_f64_f32 v[14:15], v14
M 8,27,3
s_mov_b64 exec, s[54:55]
SP
s_mov_b64 exec, -1
v_add_u32 v4, s39, v4
s_mov_b64 exec, 1
AT
s_mov_b64 exec, -1
M 7,14,2
WL 4
LO 109,16,109
LU 16,109
M 7,15,3
RA 220,1,20480
HS 26,16
M 6,2,2
LL 18,66
LL 113,67
M 6,3,3
LA 18
DW 2048
M 6,4,4
LC_ 113
M 6,5,5
M 7,16,4
WL 3
LO 112,17,112
LU 17,112
M 7,17,5
s_mul_i32 s53, s51, 24
RA 224,1,21504
HZ 29,17
M 8,28,4
s_bitcmp1_b32 s51, 0
LL 16,68
LL 116,69
M 8,29,5
s_addc_u32 s53, s53, 0
LA 16
M 9,40,4
s_lshl_b32 s53, s53, 11
LC_ 116
M 9,41,5
s_bitcmp1_b64 s[92:93], s51
M 10,52,4
WL 3
LO 113,18,113
LU 18,113
M 10,53,5
s_cselect_b64 s[94:95], s[88:89], s[86:87]
RA 228,1,22528
HS 29,18
M 11,64,4
LL 17,70
LL 117,71
M 11,65,5
s_add_u32 s94, s94, s53
LA 17
M 11,66,0
s_addc_u32 s95, s95, 0
LC_ 117
M 11,67,1
s_add_u32 s51, s51, %[d]
M 10,54,0
WL 3
LO 116,16,116
LU 16,116
M 10,55,1
s_cmp_eq_u32 s51, %[wrap]
RA 232,1,23552
HS 29,16
M 9,42,0
s_cselect_b32 s51, %[rst], s51
LL 18,72
LL 120,73
M 9,43,1
s_add_u32 s40, s40, 3072
LA 18
M 8,30,0
s_addc_u32 s41, s41, 0
LC_ 120
s_add_u32 m0, s38, 3072
M 8,31,1
DW 0
M 7,18,0
WL 3
LO 117,17,117
LU 17,117
M 7,19,1
RA 236,1,24576
HS 29,17
M 6,6,0
LL 16,74
LL 121,75
M 6,7,1
LA 16
DW 1024
M 6,8,2
LC_ 121
DW 2048
M 6,9,3
s_lshl_b32 s53, s52, 8
v_add_u32 v1, s37, v0
M 7,20,2
WL 3
LO 120,18,120
LU 18,120
M 7,21,3
s_add_u32 s48, %[alo], s53
RA 216,1,0
HZ 30,18
M 8,32,2
s_addc_u32 s49, %[ahi], 0
LL 17,76
LL 124,77
M 8,33,3
s_add_u32 s52, s52, %[d]
LA 17
v_and_b32 v22, 0xfffffcff, v1
M 9,44,2
s_cmp_eq_u32 s52, %[wrap]
LC_ 124
M 9,45,3
s_cselect_b32 s52, %[rst], s52
DQ 3072
M 10,56,2
WL 3
LO 121,16,121
LU 16,121
M 10,57,3
s_add_u32 s50, s50, %[d]
RA 220,1,1024
HS 30,16
M 11,68,2
LL 18,78
LL 125,79
M 11,69,3
s_cmp_eq_u32 s50, %[wrap]
LA 18
RV 252,22,5120
M 6,10,4
s_cselect_b32 s50, %[rst], s50
LC_ 125
M 6,11,5
WV 28
M 7,22,4
WL 4
LO 124,17,124
LU 17,124
M 7,23,5
s_add_u32 s38, s38, 32768
RA 224,1,2048
HS 30,17
M 8,34,4
s_cmp_eq_u32 s38, s77
LL 16,80
LL 106,81
M 8,35,5
s_cselect_b32 s38, s76, s38
)"
R"(LA 16
M 9,46,4
s_barrier
LC_ 106
M 9,47,5
s_sub_u32 s39, 0, s39
M 10,58,4
WL 3
LO 125,18,125
LU 18,125
M 10,59,5
RA 228,1,3072
HS 30,18
M 11,70,4
LL 17,82
LL 107,83
M 11,71,5
LA 17
v_pk_add_f16 v26, v26, v30
Z 0,0,0
LC_ 107
v_pk_add_f16 v26, v26, v29
M 0,1,1
HX 26
Z 1,12,0
WL 3
LO 16,16,106
LU 106,16
M 1,13,1
RA 232,1,4096
HZ 27,106
Z 2,24,0
LL 18,84
LL 110,85
M 2,25,1
LA 18
PK 106,16,104,56
Z 3,36,0
LC_ 110
PK 104,16,104,97
M 3,37,1
W1 26,2048
Z 4,48,0
WL 4
LO 17,17,107
LU 107,17
M 4,49,1
s_add_u32 s42, s94, s60
RA 236,1,6144
HS 27,107
Z 5,60,0
LL 16,86
LL 111,87
M 5,61,1
s_addc_u32 s43, s95, 0
LA 16
PK 107,17,105,56
M 5,62,2
LC_ 111
PK 105,17,105,97
M 5,63,3
SU 104
M 4,50,2
WL 4
LO 18,18,110
LU 110,18
M 4,51,3
RA 216,1,7168
HS 27,110
M 3,38,2
LL 17,88
LL 114,89
M 3,39,3
LA 17
PK 110,18,108,56
M 2,26,2
LC_ 114
PK 108,18,108,97
M 2,27,3
PO 110
M 1,14,2
WL 3
LO 16,16,111
LU 111,16
M 1,15,3
s_add_u32 s42, s94, s61
RA 220,1,8192
HS 27,111
M 0,2,2
LL 18,90
LL 115,91
M 0,3,3
s_addc_u32 s43, s95, 0
LA 18
PK 111,16,109,56
M 0,4,4
PFW
LC_ 115
PK 109,16,109,97
M 0,5,5
s_mov_b32 m0, %[pm]
s_nop 0
DF
PO 111
M 1,16,4
WL 3
LO 17,17,114
LU 114,17
M 1,17,5
RA 224,1,9216
HZ 29,114
M 2,28,4
LL 16,92
LL 118,93
M 2,29,5
LA 16
PK 114,17,112,56
M 3,40,4
LC_ 118
PK 112,17,112,97
M 3,41,5
SU 108
M 4,52,4
WL 3
LO 18,18,115
LU 115,18
M 4,53,5
s_add_u32 s42, s94, s62
RA 228,1,10240
HS 29,115
M 5,64,4
LL 17,94
LL 119,95
M 5,65,5
s_addc_u32 s43, s95, 0
LA 17
PK 115,18,113,56
M 5,66,0
LC_ 119
PK 113,18,113,97
M 5,67,1
SU 112
M 4,54,0
WL 3
LO 16,16,118
LU 118,16
M 4,55,1
RA 232,1,11264
HS 29,118
M 3,42,0
LL 18,96
LL 122,97
M 3,43,1
LA 18
PK 118,16,116,56
M 2,30,0
LC_ 122
PK 116,16,116,97
M 2,31,1
RV 252,22,5392
PO 118
M 1,18,0
WL 4
LO 17,17,119
LU 119,17
M 1,19,1
s_add_u32 s42, s94, s63
RA 236,1,12288
HS 29,119
M 0,6,0
LL 16,98
LL 123,99
M 0,7,1
s_addc_u32 s43, s95, 0
LA 16
PK 119,17,117,56
M 0,8,2
LC_ 123
PK 117,17,117,97
M 0,9,3
PO 119
M 1,20,2
WL 4
LO 18,18,122
LU 122,18
M 1,21,3
RA 216,1,13312
HZ 30,122
M 2,32,2
LL 17,100
LL 126,101
M 2,33,3
LA 17
PK 122,18,120,56
M 3,44,2
LC_ 126
PK 120,18,120,97
M 3,45,3
SU 116
M 4,56,2
WL 3
LO 16,16,123
LU 123,16
M 4,57,3
s_add_u32 s42, s94, s64
RA 220,1,14336
HS 30,123
M 5,68,2
LL 18,102
LL 127,103
M 5,69,3
s_addc_u32 s43, s95, 0
LA 18
PK 123,16,121,56
M 0,10,4
LC_ 127
PK 121,16,121,97
M 0,11,5
SU 120
M 1,22,4
WL 3
LO 17,17,126
LU 126,17
M 1,23,5
RA 224,1,15360
HS 30,126
M 2,34,4
LL 16,56
LL 104,57
M 2,35,5
LA 16
PK 126,17,124,56
M 3,46,4
LC_ 104
PK 124,17,124,97
M 3,47,5
ds_read_b64 v[14:15], v5 offset:2048
PO 126
M 4,58,4
WL 4
LO 18,18,127
LU 127,18
M 4,59,5
s_add_u32 s42, s94, s65
RA 228,1,16384
HS 30,127
M 5,70,4
LL 17,58
LL 105,59
M 5,71,5
s_addc_u32 s43, s95, 0
LA 17
PK 127,18,125,56
Z 6,0,0
LC_ 105
PK 125,18,125,97
M 6,1,1
PO 127
v_subrev_u32 v5, s39, v5
Z 7,12,0
WL 4
LO 104,16,104
LU 16,104
M 7,13,1
RA 232,1,17408
HZ 26,16
Z 8,24,0
WL 4
LL 18,60
LL 108,61
M 8,25,1
s_mul_i32 s53, s50, 25600
LA 18
v_add_f32 v14, v14, v15
Z 9,36,0
s_add_u32 s40, %[wlo], s53
LC_ 108
v_add_f32_dpp v14, v14, v14 quad_perm:[1,0,3,2] row_mask:0xf bank_mask:0xf
M 9,37,1
s_addc_u32 s41, %[whi], 0
Z 10,48,0
WL 3
LO 105,17,105
LU 17,105
M 10,49,1
s_mov_b32 m0, s38
RA 236,1,18432
HS 26,17
Z 11,60,0
LL 16,62
LL 109,63
M 11,61,1
LA 16
SU 124
M 11,62,2
LC_ 109
DW 0
M 11,63,3
WV 13
v_add_f32_dpp v14, v14, v14 quad_perm:[2,3,0,1] row_mask:0xf bank_mask:0xf
v_pk_add_f16 v27, v27, v30
M 10,50,2
WL 3
LO 108,18,108
LU 18,108
v_pk_add_f16 v27, v27, v29
M 10,51,3
s_add_u32 s37, s37, 32768
RA 216,1,19456
HS 26,18
HX 27
M 9,38,2
s_cmp_eq_u32 s37, s75
LL 17,64
LL 112,65
W1 27,3072
M 9,39,3
s_cselect_b32 s37, s74, s37
LA 17
DW 1024
v_readfirstlane_b32 s72, v11
M 8,26,2
LC_ 112
v_add_f32_dpp v14, v14, v14 row_half_mirror row_mask:0xf bank_mask:0xf
v_mov_b32 v12, s72
M 8,27,3
W2 12,12,16
v_cvt_f64_f32 v[14:15], v14
M 7,14,2
WL 5
LO 109,16,109
LU 16,109
M 7,15,3
RA 220,1,20480
HS 26,16
M 6,2,2
LL 18,66
LL 113,67
M 6,3,3
LA 18
s_mov_b64 exec, s[54:55]
SP
s_mov_b64 exec, -1
M 6,4,4
LC_ 113
DW 2048
M 6,5,5
v_add_u32 v4, s39, v4
s_mov_b64 exec, 1
AT
s_mov_b64 exec, -1
M 7,16,4
WL 4
LO 112,17,112
LU 17,112
M 7,17,5
s_mul_i32 s53, s51, 24
RA 224,1,21504
HZ 29,17
M 8,28,4
s_bitcmp1_b32 s51, 0
LL 16,68
LL 116,69
M 8,29,5
s_addc_u32 s53, s53, 0
LA 16
M 9,40,4
s_lshl_b32 s53, s53, 11
LC_ 116
M 9,41,5
s_bitcmp1_b64 s[92:93], s51
M 10,52,4
WL 3
LO 113,18,113
LU 18,113
M 10,53,5
s_cselect_b64 s[94:95], s[88:89], s[86:87]
RA 228,1,22528
HS 29,18
M 11,64,4
LL 17,70
LL 117,71
M 11,65,5
s_add_u32 s94, s94, s53
LA 17
M 11,66,0
s_addc_u32 s95, s95, 0
LC_ 117
M 11,67,1
s_add_u32 s51, s51, %[d]
M 10,54,0
WL 3
LO 116,16,116
LU 16,116
M 10,55,1
s_cmp_eq_u32 s51, %[wrap]
RA 232,1,23552
HS 29,16
M 9,42,0
s_cselect_b32 s51, %[rst], s51
)"
R"(LL 18,72
LL 120,73
M 9,43,1
s_add_u32 s40, s40, 3072
LA 18
M 8,30,0
s_addc_u32 s41, s41, 0
LC_ 120
s_add_u32 m0, s38, 3072
M 8,31,1
DW 0
M 7,18,0
WL 3
LO 117,17,117
LU 17,117
M 7,19,1
RA 236,1,24576
HS 29,17
M 6,6,0
LL 16,74
LL 121,75
M 6,7,1
LA 16
DW 1024
M 6,8,2
LC_ 121
DW 2048
M 6,9,3
s_lshl_b32 s53, s52, 8
v_add_u32 v1, s37, v0
M 7,20,2
WL 3
LO 120,18,120
LU 18,120
M 7,21,3
s_add_u32 s48, %[alo], s53
RA 216,1,0
HZ 30,18
M 8,32,2
s_addc_u32 s49, %[ahi], 0
LL 17,76
LL 124,77
M 8,33,3
s_add_u32 s52, s52, %[d]
LA 17
v_and_b32 v22, 0xfffffcff, v1
M 9,44,2
s_cmp_eq_u32 s52, %[wrap]
LC_ 124
M 9,45,3
s_cselect_b32 s52, %[rst], s52
DQ 3072
M 10,56,2
WL 3
LO 121,16,121
LU 16,121
M 10,57,3
s_add_u32 s50, s50, %[d]
RA 220,1,1024
HS 30,16
M 11,68,2
LL 18,78
LL 125,79
M 11,69,3
s_cmp_eq_u32 s50, %[wrap]
LA 18
RV 252,22,5120
M 6,10,4
s_cselect_b32 s50, %[rst], s50
LC_ 125
M 6,11,5
M 7,22,4
WL 4
LO 124,17,124
LU 17,124
M 7,23,5
s_add_u32 s38, s38, 32768
RA 224,1,2048
HS 30,17
M 8,34,4
s_cmp_eq_u32 s38, s77
LL 16,80
LL 106,81
M 8,35,5
s_cselect_b32 s38, s76, s38
LA 16
M 9,46,4
s_barrier
LC_ 106
M 9,47,5
s_sub_u32 s39, 0, s39
M 10,58,4
WL 3
LO 125,18,125
LU 18,125
M 10,59,5
RA 228,1,3072
HS 30,18
M 11,70,4
LL 17,82
LL 107,83
M 11,71,5
2:
LA 17
v_pk_add_f16 v26, v26, v30
Z 0,0,0
LC_ 107
ds_read_b32 v20, v5 offset:4096
M 0,1,1
v_pk_add_f16 v26, v26, v29
Z 1,12,0
WL 4
LO 16,16,106
LU 106,16
M 1,13,1
RA 232,1,4096
HZ 27,106
Z 2,24,0
LL 18,84
LL 110,85
M 2,25,1
LA 18
PK 106,16,104,56
Z 3,36,0
LC_ 110
PK 104,16,104,97
M 3,37,1
HX 26
Z 4,48,0
WL 4
LO 17,17,107
LU 107,17
M 4,49,1
s_add_u32 s42, s94, s60
RA 236,1,6144
HS 27,107
Z 5,60,0
LL 16,86
LL 111,87
M 5,61,1
s_addc_u32 s43, s95, 0
LA 16
PK 107,17,105,56
M 5,62,2
LC_ 111
PK 105,17,105,97
M 5,63,3
SU 104
M 4,50,2
WL 3
LO 18,18,110
LU 110,18
M 4,51,3
RA 216,1,7168
HS 27,110
M 3,38,2
LL 17,88
LL 114,89
W1 26,2048
M 3,39,3
LA 17
PK 110,18,108,56
M 2,26,2
LC_ 114
PK 108,18,108,97
M 2,27,3
PO 110
v_readfirstlane_b32 s59, v20
M 1,14,2
WL 4
LO 16,16,111
LU 111,16
M 1,15,3
s_add_u32 s42, s94, s61
RA 220,1,8192
HS 27,111
M 0,2,2
LL 18,90
LL 115,91
M 0,3,3
s_addc_u32 s43, s95, 0
LA 18
PK 111,16,109,56
M 0,4,4
LC_ 115
PK 109,16,109,97
M 0,5,5
PO 111
M 1,16,4
WL 3
LO 17,17,114
LU 114,17
M 1,17,5
RA 224,1,9216
HZ 29,114
M 2,28,4
LL 16,92
LL 118,93
M 2,29,5
LA 16
PK 114,17,112,56
M 3,40,4
LC_ 118
PK 112,17,112,97
M 3,41,5
SU 108
M 4,52,4
WL 3
LO 18,18,115
LU 115,18
M 4,53,5
s_add_u32 s42, s94, s62
RA 228,1,10240
HS 29,115
M 5,64,4
LL 17,94
LL 119,95
M 5,65,5
s_addc_u32 s43, s95, 0
LA 17
PK 115,18,113,56
M 5,66,0
LC_ 119
PK 113,18,113,97
M 5,67,1
SU 112
M 4,54,0
WL 3
LO 16,16,118
LU 118,16
M 4,55,1
RA 232,1,11264
HS 29,118
M 3,42,0
LL 18,96
LL 122,97
M 3,43,1
LA 18
PK 118,16,116,56
M 2,30,0
LC_ 122
PK 116,16,116,97
M 2,31,1
RV 252,22,5392
PO 118
M 1,18,0
WL 4
LO 17,17,119
LU 119,17
M 1,19,1
s_add_u32 s42, s94, s63
RA 236,1,12288
HS 29,119
M 0,6,0
LL 16,98
LL 123,99
M 0,7,1
s_addc_u32 s43, s95, 0
LA 16
PK 119,17,117,56
M 0,8,2
LC_ 123
PK 117,17,117,97
M 0,9,3
PO 119
M 1,20,2
WL 4
LO 18,18,122
LU 122,18
M 1,21,3
RA 216,1,13312
HZ 30,122
M 2,32,2
LL 17,100
LL 126,101
M 2,33,3
LA 17
PK 122,18,120,56
M 3,44,2
LC_ 126
PK 120,18,120,97
M 3,45,3
SU 116
M 4,56,2
WL 3
LO 16,16,123
LU 123,16
M 4,57,3
s_add_u32 s42, s94, s64
RA 220,1,14336
HS 30,123
M 5,68,2
LL 18,102
LL 127,103
M 5,69,3
s_addc_u32 s43, s95, 0
LA 18
PK 123,16,121,56
M 0,10,4
LC_ 127
PK 121,16,121,97
M 0,11,5
SU 120
M 1,22,4
WL 3
LO 17,17,126
LU 126,17
M 1,23,5
RA 224,1,15360
HS 30,126
M 2,34,4
LL 16,56
LL 104,57
M 2,35,5
LA 16
PK 126,17,124,56
M 3,46,4
LC_ 104
PK 124,17,124,97
M 3,47,5
ds_read_b64 v[14:15], v5 offset:2048
PO 126
M 4,58,4
WL 4
LO 18,18,127
LU 127,18
M 4,59,5
s_add_u32 s42, s94, s65
RA 228,1,16384
HS 30,127
M 5,70,4
LL 17,58
LL 105,59
M 5,71,5
s_addc_u32 s43, s95, 0
LA 17
PK 127,18,125,56
Z 6,0,0
LC_ 105
PK 125,18,125,97
M 6,1,1
PO 127
v_subrev_u32 v5, s39, v5
Z 7,12,0
WL 4
LO 104,16,104
LU 16,104
M 7,13,1
RA 232,1,17408
HZ 26,16
Z 8,24,0
WL 4
LL 18,60
LL 108,61
M 8,25,1
s_mul_i32 s53, s50, 25600
LA 18
v_add_f32 v14, v14, v15
Z 9,36,0
s_add_u32 s40, %[wlo], s53
LC_ 108
v_add_f32_dpp v14, v14, v14 quad_perm:[1,0,3,2] row_mask:0xf bank_mask:0xf
M 9,37,1
s_addc_u32 s41, %[whi], 0
Z 10,48,0
WL 3
LO 105,17,105
LU 17,105
M 10,49,1
s_mov_b32 m0, s38
RA 236,1,18432
HS 26,17
Z 11,60,0
LL 16,62
LL 109,63
M 11,61,1
LA 16
SU 124
M 11,62,2
LC_ 109
DW 0
M 11,63,3
WV 11
v_add_f32_dpp v14, v14, v14 quad_perm:[2,3,0,1] row_mask:0xf bank_mask:0xf
v_pk_add_f16 v27, v27, v30
M 10,50,2
WL 3
LO 108,18,108
LU 18,108
v_pk_add_f16 v27, v27, v29
M 10,51,3
s_add_u32 s37, s37, 32768
RA 216,1,19456
HS 26,18
HX 27
M 9,38,2
s_cmp_eq_u32 s37, s75
LL 17,64
LL 112,65
W1 27,3072
)"
R"(M 9,39,3
s_cselect_b32 s37, s74, s37
LA 17
DW 1024
v_readfirstlane_b32 s72, v11
M 8,26,2
LC_ 112
v_add_f32_dpp v14, v14, v14 row_half_mirror row_mask:0xf bank_mask:0xf
v_mov_b32 v12, s72
M 8,27,3
s_max_u32 s59, s59, %[nb]
W2 12,12,16
v_cvt_f64_f32 v[14:15], v14
M 7,14,2
WL 5
LO 109,16,109
LU 16,109
M 7,15,3
s_add_u32 %[nb], %[nb], 1
RA 220,1,20480
HS 26,16
M 6,2,2
LL 18,66
LL 113,67
M 6,3,3
LA 18
s_mov_b64 exec, s[54:55]
SP
s_mov_b64 exec, -1
M 6,4,4
LC_ 113
DW 2048
M 6,5,5
v_add_u32 v4, s39, v4
s_mov_b64 exec, 1
AT
s_mov_b64 exec, -1
M 7,16,4
WL 4
LO 112,17,112
LU 17,112
M 7,17,5
s_mul_i32 s53, s51, 24
RA 224,1,21504
HZ 29,17
M 8,28,4
s_bitcmp1_b32 s51, 0
LL 16,68
LL 116,69
M 8,29,5
s_addc_u32 s53, s53, 0
LA 16
M 9,40,4
s_lshl_b32 s53, s53, 11
LC_ 116
M 9,41,5
s_bitcmp1_b64 s[92:93], s51
M 10,52,4
WL 3
LO 113,18,113
LU 18,113
M 10,53,5
s_cselect_b64 s[94:95], s[88:89], s[86:87]
RA 228,1,22528
HS 29,18
M 11,64,4
LL 17,70
LL 117,71
M 11,65,5
s_add_u32 s94, s94, s53
LA 17
M 11,66,0
s_addc_u32 s95, s95, 0
LC_ 117
M 11,67,1
s_add_u32 s51, s51, %[d]
M 10,54,0
WL 3
LO 116,16,116
LU 16,116
M 10,55,1
s_cmp_eq_u32 s51, %[wrap]
RA 232,1,23552
HS 29,16
M 9,42,0
s_cselect_b32 s51, %[rst], s51
LL 18,72
LL 120,73
M 9,43,1
s_add_u32 s40, s40, 3072
LA 18
M 8,30,0
s_addc_u32 s41, s41, 0
LC_ 120
s_add_u32 m0, s38, 3072
M 8,31,1
DW 0
M 7,18,0
WL 3
LO 117,17,117
LU 17,117
M 7,19,1
RA 236,1,24576
HS 29,17
M 6,6,0
LL 16,74
LL 121,75
M 6,7,1
LA 16
DW 1024
M 6,8,2
LC_ 121
DW 2048
M 6,9,3
s_lshl_b32 s53, s52, 8
v_add_u32 v1, s37, v0
M 7,20,2
WL 3
LO 120,18,120
LU 18,120
M 7,21,3
s_add_u32 s48, %[alo], s53
RA 216,1,0
HZ 30,18
M 8,32,2
s_addc_u32 s49, %[ahi], 0
LL 17,76
LL 124,77
M 8,33,3
s_add_u32 s52, s52, %[d]
LA 17
v_and_b32 v22, 0xfffffcff, v1
M 9,44,2
s_cmp_eq_u32 s52, %[wrap]
LC_ 124
M 9,45,3
s_cselect_b32 s52, %[rst], s52
DQ 3072
M 10,56,2
WL 3
LO 121,16,121
LU 16,121
M 10,57,3
s_add_u32 s50, s50, %[d]
RA 220,1,1024
HS 30,16
M 11,68,2
LL 18,78
LL 125,79
M 11,69,3
s_cmp_eq_u32 s50, %[wrap]
LA 18
RV 252,22,5120
M 6,10,4
s_cselect_b32 s50, %[rst], s50
LC_ 125
M 6,11,5
M 7,22,4
WL 4
LO 124,17,124
LU 17,124
M 7,23,5
s_add_u32 s38, s38, 32768
RA 224,1,2048
HS 30,17
M 8,34,4
s_cmp_eq_u32 s38, s77
LL 16,80
LL 106,81
M 8,35,5
s_cselect_b32 s38, s76, s38
LA 16
M 9,46,4
s_barrier
LC_ 106
M 9,47,5
s_sub_u32 s39, 0, s39
M 10,58,4
WL 3
LO 125,18,125
LU 18,125
M 10,59,5
RA 228,1,3072
HS 30,18
M 11,70,4
s_cmp_lt_u32 s59, 32
LL 17,82
LL 107,83
M 11,71,5
s_cbranch_scc1 2b
LA 17
LC_ 107
ds_read_b64 v[14:15], v5 offset:2048
v_pk_add_f16 v26, v26, v30
v_pk_add_f16 v26, v26, v29
HX 26
v_subrev_u32 v5, s39, v5
W1 26,2048
WL 0
LO 16,16,106
LU 106,16
HZ 27,106
LL 18,84
LA 18
LL 110,85
PK 106,16,104,56
v_add_f32 v14, v14, v15
s_nop 1
v_add_f32_dpp v14, v14, v14 quad_perm:[1,0,3,2] row_mask:0xf bank_mask:0xf
s_nop 1
v_add_f32_dpp v14, v14, v14 quad_perm:[2,3,0,1] row_mask:0xf bank_mask:0xf
s_nop 1
v_add_f32_dpp v14, v14, v14 row_half_mirror row_mask:0xf bank_mask:0xf
v_cvt_f64_f32 v[14:15], v14
s_mov_b64 exec, s[54:55]
SP
s_mov_b64 exec, -1
LC_ 110
PK 104,16,104,97
LO 17,17,107
LU 107,17
s_add_u32 s42, s94, s60
s_addc_u32 s43, s95, 0
HS 27,107
LL 16,86
LA 16
LL 111,87
LC_ 111
PK 107,17,105,56
PK 105,17,105,97
SU 104
WL 0
LO 18,18,110
LU 110,18
HS 27,110
LL 17,88
LA 17
LL 114,89
LC_ 114
PK 110,18,108,56
PK 108,18,108,97
PO 110
LO 16,16,111
LU 111,16
HS 27,111
s_add_u32 s42, s94, s61
s_addc_u32 s43, s95, 0
LL 18,90
LA 18
LL 115,91
LC_ 115
PK 111,16,109,56
PK 109,16,109,97
PO 111
WL 0
LO 17,17,114
LU 114,17
HZ 29,114
LL 16,92
LA 16
LL 118,93
LC_ 118
PK 114,17,112,56
PK 112,17,112,97
SU 108
LO 18,18,115
LU 115,18
s_add_u32 s42, s94, s62
s_addc_u32 s43, s95, 0
HS 29,115
LL 17,94
LA 17
LL 119,95
LC_ 119
PK 115,18,113,56
PK 113,18,113,97
SU 112
WL 0
LO 16,16,118
LU 118,16
HS 29,118
LL 18,96
LA 18
LL 122,97
LC_ 122
PK 118,16,116,56
PK 116,16,116,97
PO 118
LO 17,17,119
LU 119,17
HS 29,119
s_add_u32 s42, s94, s63
s_addc_u32 s43, s95, 0
LL 16,98
LA 16
LL 123,99
LC_ 123
PK 119,17,117,56
PK 117,17,117,97
PO 119
WL 0
LO 18,18,122
LU 122,18
HZ 30,122
LL 17,100
LA 17
LL 126,101
LC_ 126
PK 122,18,120,56
PK 120,18,120,97
SU 116
LO 16,16,123
LU 123,16
s_add_u32 s42, s94, s64
s_addc_u32 s43, s95, 0
HS 30,123
LL 18,102
LA 18
LL 127,103
LC_ 127
PK 123,16,121,56
PK 121,16,121,97
SU 120
WL 0
LO 17,17,126
LU 126,17
HS 30,126
PK 126,17,124,56
PK 124,17,124,97
PO 126
LO 18,18,127
LU 127,18
HS 30,127
s_add_u32 s42, s94, s65
s_addc_u32 s43, s95, 0
v_pk_add_f16 v27, v27, v30
PK 127,18,125,56
v_pk_add_f16 v27, v27, v29
PK 125,18,125,97
HX 27
PO 127
W1 27,3072
s_nop 1
v_add_u32 v4, s39, v4
SU 124
s_lshl_b32 s53, s52, 8
s_add_u32 s48, %[alo], s53
s_addc_u32 s49, %[ahi], 0
s_add_u32 s52, s52, %[d]
s_cmp_eq_u32 s52, %[wrap]
s_cselect_b32 s52, %[rst], s52
WL 0
WV 0
s_barrier
s_sub_u32 s39, 0, s39
ds_read_b64 v[14:15], v5 offset:2048
s_nop 1
v_subrev_u32 v5, s39, v5
WL 0
v_add_f32 v14, v14, v15
s_nop 1
v_add_f32_dpp v14, v14, v14 quad_perm:[1,0,3,2] row_mask:0xf bank_mask:0xf
s_nop 1
v_add_f32_dpp v14, v14, v14 quad_perm:[2,3,0,1] row_mask:0xf bank_mask:0xf
s_nop 1
v_add_f32_dpp v14, v14, v14 row_half_mirror row_mask:0xf bank_mask:0xf
v_cvt_f64_f32 v[14:15], v14
s_mov_b64 exec, s[54:55]
SP
s_mov_b64 exec, -1
GE
s_waitcnt vmcnt(0) lgkmcnt(0)
8:
s_branch 6f
.Lk1s0:
FR
s_waitcnt lgkmcnt(0)
v_cmp_gt_u32 vcc, v19, v15
s_nop 4
s_cbranch_vccnz .Lk1s0
s_branch .Lk1r0
.Lk1s1:
FR
s_waitcnt lgkmcnt(0)
v_cmp_gt_u32 vcc, v19, v15
s_nop 4
s_cbranch_vccnz .Lk1s1
s_branch .Lk1r1
.Lk1s2:
FR
s_waitcnt lgkmcnt(0)
v_cmp_gt_u32 vcc, v19, v15
s_nop 4
s_cbranch_vccnz .Lk1s2
s_branch .Lk1r2
.Lk1s3:
FR
s_waitcnt lgkmcnt(0)
v_cmp_gt_u32 vcc, v19, v15
s_nop 4
)"
R"(s_cbranch_vccnz .Lk1s3
s_branch .Lk1r3
.Lk1s4:
FR
s_waitcnt lgkmcnt(0)
v_cmp_gt_u32 vcc, v19, v15
s_nop 4
s_cbranch_vccnz .Lk1s4
s_branch .Lk1r4
.Lk1s5:
FR
s_waitcnt lgkmcnt(0)
v_cmp_gt_u32 vcc, v19, v15
s_nop 4
s_cbranch_vccnz .Lk1s5
s_branch .Lk1r5
.Lk1s6:
FR
s_waitcnt lgkmcnt(0)
v_cmp_gt_u32 vcc, v19, v15
s_nop 4
s_cbranch_vccnz .Lk1s6
s_branch .Lk1r6
.Lk1s7:
FR
s_waitcnt lgkmcnt(0)
v_cmp_gt_u32 vcc, v19, v15
s_nop 4
s_cbranch_vccnz .Lk1s7
s_branch .Lk1r7
.Lk1s8:
FR
s_waitcnt lgkmcnt(0)
v_cmp_gt_u32 vcc, v19, v15
s_nop 4
s_cbranch_vccnz .Lk1s8
s_branch .Lk1r8
.Lk1s9:
FR
s_waitcnt lgkmcnt(0)
v_cmp_gt_u32 vcc, v19, v15
s_nop 4
s_cbranch_vccnz .Lk1s9
s_branch .Lk1r9
.Lk1s10:
FR
s_waitcnt lgkmcnt(0)
v_cmp_gt_u32 vcc, v19, v15
s_nop 4
s_cbranch_vccnz .Lk1s10
s_branch .Lk1r10
.Lk1s11:
FR
s_waitcnt lgkmcnt(0)
v_cmp_gt_u32 vcc, v19, v15
s_nop 4
s_cbranch_vccnz .Lk1s11
s_branch .Lk1r11
.Lk1s12:
FR
s_waitcnt lgkmcnt(0)
v_cmp_gt_u32 vcc, v19, v15
s_nop 4
s_cbranch_vccnz .Lk1s12
s_branch .Lk1r12
.Lk1s13:
FR
s_waitcnt lgkmcnt(0)
v_cmp_gt_u32 vcc, v19, v15
s_nop 4
s_cbranch_vccnz .Lk1s13
s_branch .Lk1r13
.Lk1s14:
FR
s_waitcnt lgkmcnt(0)
v_cmp_gt_u32 vcc, v19, v15
s_nop 4
s_cbranch_vccnz .Lk1s14
s_branch .Lk1r14
.Lk1s15:
FR
s_waitcnt lgkmcnt(0)
v_cmp_gt_u32 vcc, v19, v15
s_nop 4
s_cbranch_vccnz .Lk1s15
s_branch .Lk1r15
.Lk1s16:
FR
s_waitcnt lgkmcnt(0)
v_cmp_gt_u32 vcc, v19, v15
s_nop 4
s_cbranch_vccnz .Lk1s16
s_branch .Lk1r16
.Lk1s17:
FR
s_waitcnt lgkmcnt(0)
v_cmp_gt_u32 vcc, v19, v15
s_nop 4
s_cbranch_vccnz .Lk1s17
s_branch .Lk1r17
.Lk1s18:
FR
s_waitcnt lgkmcnt(0)
v_cmp_gt_u32 vcc, v19, v15
s_nop 4
s_cbranch_vccnz .Lk1s18
s_branch .Lk1r18
.Lk1s19:
FR
s_waitcnt lgkmcnt(0)
v_cmp_gt_u32 vcc, v19, v15
s_nop 4
s_cbranch_vccnz .Lk1s19
s_branch .Lk1r19
.Lk1s20:
FR
s_waitcnt lgkmcnt(0)
v_cmp_gt_u32 vcc, v19, v15
s_nop 4
s_cbranch_vccnz .Lk1s20
s_branch .Lk1r20
.Lk1s21:
FR
s_waitcnt lgkmcnt(0)
v_cmp_gt_u32 vcc, v19, v15
s_nop 4
s_cbranch_vccnz .Lk1s21
s_branch .Lk1r21
6:
.purgem M
.purgem Z
.purgem LZ
.purgem LS
.purgem GZ
.purgem GS
.purgem XL
.purgem C
.purgem LL
.purgem LA
.purgem LC_
.purgem LO
.purgem HZ
.purgem HS
.purgem HX
.purgem AW
.purgem RA
.purgem RV
.purgem W2
.purgem W1
.purgem DW
.purgem DF
.purgem ST
.purgem SU
.purgem SP
.purgem AT
.purgem PFA
.purgem PFN
.purgem PFW
.purgem PF0
.purgem WL
.purgem WV
.purgem LU
.purgem CF
.purgem XS
.purgem AR
.purgem DQ
.purgem GE
.purgem BW
.purgem BR
.purgem LK
.purgem PK
.purgem PO
.purgem LG
.purgem DY
.purgem FP
.purgem FR
)"
      : [nb] "=&s"(nb)
      : [xlo] "s"((unsigned)xp), [xhi] "s"((unsigned)(xp >> 32)), [hlo] "s"((unsigned)hp), [hhi] "s"((unsigned)(hp >> 32)),
        [glo] "s"((unsigned)gp), [ghi] "s"((unsigned)(gp >> 32)), [wlo] "s"((unsigned)wp), [whi] "s"((unsigned)(wp >> 32)),
        [w] "s"(w), [lds] "s"(lds0), [d] "s"(d), [wrap] "s"(wrap), [rst] "s"(rst), [pos0] "s"(pos0), [xo0] "s"(xo0), [xs1] "s"(xs1), [sb1] "s"(sb1), [r0o] "s"(r0o), [tsel] "s"(tsel), [s2] "s"(s2),
        [alo] "s"((unsigned)ap), [ahi] "s"((unsigned)(ap >> 32)), [ca0] "s"(ca0), [pm] "s"(pm), [pf] "s"(pf)
      : K1_CLOB);
}

typedef __bf16 bf16x8_t __attribute__((ext_vector_type(8)));
DI f32x4 mfma16(u32x4 a, u32x4 b, f32x4 c) {
  return __builtin_amdgcn_mfma_f32_16x16x32_bf16(__builtin_bit_cast(bf16x8_t, a), __builtin_bit_cast(bf16x8_t, b), c, 0, 0, 0);
}
DI float grn_gx(double s, float scale) { return round_bf16(__builtin_amdgcn_sqrtf(round_bf16(round_bf16((float)s * scale) + 1e-6f))); }
DI void slow_fc1(const u16* __restrict__ x, const u16* __restrict__ dw, char* __restrict__ hws, float* sacc, int w) {
  const int lane = __builtin_amdgcn_mbcnt_hi(~0u, __builtin_amdgcn_mbcnt_lo(~0u, 0u)), m = lane & 15, g = lane >> 4;
  for (int i = lane; i < HID; i += 64) sacc[i] = 0.f;
#pragma unroll 1
  for (int it = 0; it < 9; ++it) {
    const int sub = blockIdx.x * 36 + it * 4 + w;
    const unsigned px = sub_pixel(x, sub, m);
    u32x4 A[12];
    float sum = 0.f;
    const u16* dq = dw;
    asm volatile("" : "+s"(dq));
#pragma unroll
    for (int ks = 0; ks < 12; ++ks) {
      const u32x4 v = *(const u32x4*)(x + (size_t)px * CH + 32 * ks + 8 * g);
      const u32x4 d = *(const u32x4*)(dq + 32 * ks + 8 * g);
#pragma unroll
      for (int j = 0; j < 4; ++j) {
        A[ks][j] = pack_bf16(bf_lo(v[j]) * bf_lo(d[j]), bf_hi(v[j]) * bf_hi(d[j]));
        sum += bf_lo(A[ks][j]) + bf_hi(A[ks][j]);
      }
      asm volatile("" : "+v"(A[ks]));
    }
    sum += __shfl_xor(sum, 16); sum += __shfl_xor(sum, 32);
    const float mean = sum * (1.0f / CH);
    float var = 0.f;
#pragma unroll
    for (int ks = 0; ks < 12; ++ks) {
#pragma unroll
      for (int j = 0; j < 4; ++j) {
        const float a = bf_lo(A[ks][j]) - mean, b = bf_hi(A[ks][j]) - mean;
        var += a * a + b * b;
      }
      asm volatile("" : "+v"(A[ks]));
    }
    var += __shfl_xor(var, 16); var += __shfl_xor(var, 32);
    const float mb = round_bf16(mean);
    const float rd = 1.0f / round_bf16(sqrtf(round_bf16(round_bf16(var * (1.0f / CH)) + 1e-5f)));
#pragma unroll
    for (int ks = 0; ks < 12; ++ks)
#pragma unroll
      for (int j = 0; j < 4; ++j) A[ks][j] = pack_bf16((bf_lo(A[ks][j]) - mb) * rd, (bf_hi(A[ks][j]) - mb) * rd);
#pragma unroll 1
    for (int tc = 0; tc < 96; ++tc) {
      f32x4 acc = {0.f, 0.f, 0.f, 0.f};
#pragma unroll
      for (int ks = 0; ks < 12; ++ks) acc = mfma16(A[ks], *(const u32x4*)(g_buf + (size_t)(tc * 12 + ks) * 1024 + lane * 16), acc);
      float q = 0.f;
#pragma unroll
      for (int r = 0; r < 4; ++r) { acc[r] = gelu_tanh(acc[r]); q = __builtin_fmaf(acc[r], acc[r], q); }
      typedef unsigned u32x2_ __attribute__((ext_vector_type(2)));
      *(u32x2_*)(hws + h_piece(hws, sub, tc >> 1) + g * 256 + ((m + 4 * g) & 15) * 16 + (tc & 1) * 8) = (u32x2_){pack_bf16(acc[0], acc[1]), pack_bf16(acc[2], acc[3])};
      q += __shfl_xor(q, 16); q += __shfl_xor(q, 32);
      if (g == 0) sacc[tc * 16 + m] += q;
    }
  }
  __builtin_amdgcn_wave_barrier();
  __builtin_amdgcn_fence(__ATOMIC_ACQ_REL, "workgroup");
  for (int i = lane; i < HID; i += 64) atomicAdd((double*)(g_buf + G_ACC2) + i, (double)sacc[i]);
}
DI void slow_bar(unsigned* c, int w) {
  const int lane = __builtin_amdgcn_mbcnt_hi(~0u, __builtin_amdgcn_mbcnt_lo(~0u, 0u));
  asm volatile("s_waitcnt vmcnt(0)" ::: "memory");
  __syncthreads();
  if (w == 0 && lane == 0) {
    __threadfence();
    asm volatile("s_waitcnt vmcnt(0)" ::: "memory");
    __hip_atomic_fetch_add(c, 1u, __ATOMIC_RELAXED, __HIP_MEMORY_SCOPE_AGENT);
    for (int i = 0; i < (1 << 22) && __hip_atomic_load(c, __ATOMIC_RELAXED, __HIP_MEMORY_SCOPE_AGENT) < (unsigned)NWG; ++i) __builtin_amdgcn_s_sleep(32);
    __threadfence();
    asm volatile("s_waitcnt vmcnt(0)" ::: "memory");
  }
  __syncthreads();
}

#define K2_CLOB "memory", "vcc", "scc", "s48", "s49", "s50", "s51", "s52", "s53", "s54", "s55", "s56", "s57", "s58", "s59", "s60", "s61", "s62", "s63", "s64", "s65", "s66", "s67", \
  "s68", "s69", "s70", "s71", "s72", "s73", "s74", "s75", "s76", "s77", "s78", "s79", "s80", "s81", "s82", "s83", "s84", "s85", "s86", "s87", "s88", "s89", "s90", "s91", "s92", "s93", "s94", "s95", "s96", "s97", "s98", "s99", "s38", "s39", "s40", "s41", "s42", "s43", "s44", "s45", "s46", "s47", VA_CLOB
DI void slow_fc2(const u16* __restrict__ x, const char* __restrict__ hws, u16* __restrict__ out, const u16* __restrict__ grn_g, const u16* __restrict__ w2,
                 char* smem, int w) {
  float* sc = (float*)smem;
  float* red = sc + HID;
  u16* wf = (u16*)(smem + 8192);
  const int lane = __builtin_amdgcn_mbcnt_hi(~0u, __builtin_amdgcn_mbcnt_lo(~0u, 0u)), tid = w * 64 + lane, m = lane & 15, g = lane >> 4;
  __syncthreads();
  float part = 0.f;
  for (int c = tid; c < HID; c += 256) {
    const float gx = grn_gx(((const double*)(g_buf + G_ACC2))[c], 1.0f);
    sc[c] = gx; part += gx;
  }
#pragma unroll
  for (int o = 32; o > 0; o >>= 1) part += __shfl_xor(part, o);
  if (lane == 0) red[tid >> 6] = part;
  __syncthreads();
  const float gmean = round_bf16(round_bf16(((red[0] + red[1]) + (red[2] + red[3])) * (1.0f / HID)) + 1e-6f);
  for (int c = tid; c < HID; c += 256) sc[c] = (1.0f + ld_bf16(grn_g + c) * round_bf16(sc[c] / gmean)) ;
#pragma unroll 1
  for (int it = 0; it < 9; ++it) {
    const int sub = blockIdx.x * 36 + it * 4 + (tid >> 6);
    f32x4 acc[24];
#pragma unroll
    for (int i = 0; i < 24; ++i) acc[i] = (f32x4){0.f, 0.f, 0.f, 0.f};
#pragma unroll 1
    for (int t = 0; t < 48; ++t) {
      __syncthreads();
      for (int e = tid; e < 32 * CH; e += 256) {
        const int k = e / CH, n = e % CH;
        wf[((n >> 4) * 64 + (k >> 3) * 16 + (n & 15)) * 8 + (k & 7)] = (u16)pack_bf16(sc[32 * t + k] * ld_bf16(w2 + (size_t)(32 * t + k) * CH + n), 0.f);
      }
      __syncthreads();
      const char* hp = hws + h_piece(hws, sub, t) + (m >> 2) * 256 + (g >> 1) * 8 + 2 * (m & 3);
      u32x4 A;
#pragma unroll
      for (int j = 0; j < 4; ++j) {
        unsigned v[2];
#pragma unroll
        for (int k = 0; k < 2; ++k) {
          v[k] = *(const u16*)(hp + ((8 * (g & 1) + 2 * j + k + (m & 12)) & 15) * 16);
        }
        A[j] = v[0] | (v[1] << 16);
      }
#pragma unroll
      for (int i = 0; i < 24; ++i) acc[i] = mfma16(A, *(const u32x4*)(wf + (i * 64 + lane) * 8), acc[i]);
    }
#pragma unroll
    for (int i = 0; i < 24; ++i) {
      const int n = 16 * i + m;
      const float* bp = g_cs + 4 * 384 + cs_index(n);
      const float bias = (bp[0] + bp[384]) + (bp[768] + bp[1152]);
#pragma unroll
      for (int r = 0; r < 4; ++r) {
        const size_t o = (size_t)(sub_pixel(x, sub, 4 * g) + r) * CH + n;
        out[o] = (u16)pack_bf16(acc[i][r] + bias + ld_bf16(x + o), 0.f);
      }
    }
  }
}
__global__ __launch_bounds__(256, 1) void k2_fc2(const u16* __restrict__ x, char* __restrict__ hws, u16* __restrict__ out,
                                                 const u16* __restrict__ grn_g, const u16* __restrict__ w2, const u16* __restrict__ dw,
                                                 const u16* __restrict__ w1) {
  extern __shared__ __attribute__((aligned(16))) char smem[];
  const int w = __builtin_amdgcn_readfirstlane(threadIdx.x >> 6);
  const unsigned pg = w & 1, nh = w >> 1;
  const unsigned lds0 = lds_addr(smem) + 1024;
  const uint64_t pp = uni64((uint64_t)g_prm + (blockIdx.x * 4 + w) * 256), ap = (uint64_t)g_buf + K1_G_ACC + w * 1024;
  const unsigned wm = lds0 + w * 6144, ab = lds0 + nh * 12288;
  const unsigned hb = lds0 + K2_HB + pg * 2 * K2_RSZ, hm = hb + nh * K2_RSZ;
  const unsigned bm = w * 384, bb = lds0 + K2_E_OFF + nh * 768;
  const unsigned pm = lds0 + K2_PF_OFF, sb = lds0 + K2_S16_OFF, sw = sb + w * 256;
  uint64_t flg;
#define K2_OPS [pp] "s"(pp), [alo] "s"((unsigned)ap), [ahi] "s"((unsigned)(ap >> 32)), [wm] "s"(wm), [hm] "s"(hm), [bm] "s"(bm), [ab] "s"(ab), [hb] "s"(hb), [bb] "s"(bb), \
         [sb] "s"(sb), [sw] "s"(sw), [pm] "s"(pm)
  asm volatile(
R"(.macro MA t,a,b
v_mfma_f32_16x16x32_f16 a[\t:\t+3], v[\b:\b+3], a[\a:\a+3], a[\t:\t+3]
.endm
.macro MV t,a,b
v_mfma_f32_16x16x32_f16 v[\t:\t+3], v[\b:\b+3], a[\a:\a+3], v[\t:\t+3]
.endm
.macro NA t,a,b
v_mfma_f32_16x16x32_f16 a[\t:\t+3], a[\b:\b+3], a[\a:\a+3], a[\t:\t+3]
.endm
.macro NV t,a,b
v_mfma_f32_16x16x32_f16 v[\t:\t+3], a[\b:\b+3], a[\a:\a+3], v[\t:\t+3]
.endm
.macro AN d,s
v_and_b32 v[\d], s93, v[\s]
.endm
.macro SH d
v_lshrrev_b64 v[\d:\d+1], 4, v[\d:\d+1]
.endm
.macro PA_ d,h,r
v_perm_b32 v[\d], v[\h], v[\r], s54
.endm
.macro PB_ d,h,r
v_perm_b32 v[\d], v[\h], v[\r], s55
.endm
.macro AW a,s
v_accvgpr_write_b32 a[\a], v[\s]
.endm
.macro AR d,a
v_accvgpr_read_b32 v[\d], a[\a]
.endm
.macro UL d,x
v_lshlrev_b32 v[\d], 16, v[\x]
.endm
.macro UH d,x
v_and_b32 v[\d], s38, v[\x]
.endm
.macro AD d,b
v_fmac_f32 v[\d], s53, v[\b]
.endm
.macro EF d,e
v_fma_f32 v[\d], v[\d], s39, v[\e]
.endm
.macro TY d,o
ds_read_b64_tr_b16 v[\d:\d+1], v2 offset:\o
.endm
.macro TX d,o
ds_read_b64_tr_b16 v[\d:\d+1], v51 offset:\o
.endm
.macro RA d,o
ds_read_b128 a[\d:\d+3], v1 offset:\o
.endm
.macro RC d,o
ds_read_b128 a[\d:\d+3], v5 offset:\o
.endm
.macro BV d,o
ds_read_b128 v[\d:\d+3], v4 offset:\o
.endm
.macro PA a
s_mov_b32 m0, %[pm]
s_getpc_b64 s[46:47]
s_andn2_b32 s46, s46, 0x7f
s_add_u32 s46, s46, s81
s_addc_u32 s47, s47, 0
s_add_u32 s46, s46, \a
s_addc_u32 s47, s47, 0
.endm
.macro DF
global_load_lds_dwordx4 v0, s[46:47]
.endm
.macro DW o
global_load_lds_dwordx4 v0, s[40:41] offset:\o sc1
.endm
.macro DR o
global_load_lds_dwordx4 v0, s[40:41] offset:\o
.endm
.macro DH o
global_load_lds_dwordx4 v0, s[40:41] offset:\o nt
.endm
.macro DG o
s_mov_b32 exec_hi, 0
global_load_lds_dwordx4 v0, s[40:41] offset:\o nt
s_mov_b32 exec_hi, -1
.endm
.macro XL d,o
global_load_dwordx2 v[\d:\d+1], v47, s[44:45] offset:\o
.endm
.macro ST d,o
global_store_dwordx2 v50, v[\d:\d+1], s[42:43] offset:\o nt sc1
.endm
.macro SA d,o
global_store_dwordx2 v50, a[\d:\d+1], s[42:43] offset:\o nt sc1
.endm
.macro CV d,a,b
v_cvt_pk_bf16_f32 v[\d], v[\a], v[\b]
.endm
.macro RS d,o
ds_read_b128 v[\d:\d+3], v46 offset:\o
.endm
.macro PM d,s
v_pk_mul_f16 v[\d], v[\d], v[\s]
.endm
.macro PF d,n
v_pk_fma_f16 v[\d], v[\d], v[14+\n], v[22+\n]
.endm
.macro FS
global_store_dword v0, v54, s[96:97] sc1
.endm
.macro FL
global_load_dword v54, v55, s[98:99] sc1
.endm
.macro FB
s_cbranch_vccnz 21b
22:
.endm
.macro FC
s_cbranch_vccnz 26b
27:
.endm
.macro FG
s_or_b64 s[54:55], s[44:45], vcc
s_cbranch_scc1 23b
24:
.endm
.macro FX
s_branch 25f
21:
s_sleep 4
global_load_dword v54, v55, s[98:99] sc1
s_waitcnt vmcnt(0)
v_cmp_eq_u32 vcc, 0, v54
s_sub_u32 s80, s80, 1
s_cbranch_scc1 22f
s_nop 2
s_cbranch_vccnz 21b
s_branch 22f
26:
s_sleep 4
global_load_dword v54, v55, s[98:99] sc1
s_waitcnt vmcnt(0)
v_cmp_ne_u32 vcc, 2, v54
s_sub_u32 s80, s80, 1
s_cbranch_scc1 27f
s_nop 2
s_cbranch_vccnz 26b
s_branch 27f
23:
v_mov_b32 v10, 1
s_mov_b64 exec, 1
global_store_dword v0, v10, s[78:79] offset:384 sc1
s_mov_b64 exec, -1
s_nop 4
s_branch 24f
25:
.endm
.macro FP
ds_inc_u32 v0, v128
.endm
.macro FR
ds_read_b32 v10, v0
.endm
.macro FQ n
v_cmp_gt_u32 vcc, \n, v10
.endm
.macro FZ n
s_cbranch_vccnz .Lk2s\n
.Lk2r\n:
.endm
.macro FY n
.Lk2s\n:
FR
s_waitcnt lgkmcnt(0)
FQ \n
s_nop 4
s_cbranch_vccnz .Lk2s\n
s_branch .Lk2r\n
.endm
.macro WL n
s_waitcnt lgkmcnt(\n)
.endm
.macro WV n
s_waitcnt vmcnt(\n)
.endm
.macro M n
.if \n < 82
v_mfma_f32_16x16x32_f16 a[36+4*(\n/2):36+4*(\n/2)+3], v[56+8*((\n/2)%%6)+4*(\n&1):56+8*((\n/2)%%6)+4*(\n&1)+3], a[8*((\n/12)%%4)+4*(\n&1):8*((\n/12)%%4)+4*(\n&1)+3], a[36+4*(\n/2):36+4*(\n/2)+3]
.else
v_mfma_f32_16x16x32_f16 v[-32+4*(\n/2):-32+4*(\n/2)+3], v[56+8*((\n/2)%%6)+4*(\n&1):56+8*((\n/2)%%6)+4*(\n&1)+3], a[8*((\n/12)%%4)+4*(\n&1):8*((\n/12)%%4)+4*(\n&1)+3], v[-32+4*(\n/2):-32+4*(\n/2)+3]
.endif
.endm
.macro R1 n
.if \n < 64
RA 8*(((\n%%24)/2+2*((\n/24)&1))%%4)+4*(\n&1),1024*\n
.else
RC 8*(((\n%%24)/2+2*((\n/24)&1))%%4)+4*(\n&1),1024*\n-49152
.endif
.endm
.macro R n
R1 \n
R1 (\n+1)
.endm
.macro P n
.if \n & 2
PB_ 56+\n,104+8*(\n/16)+(\n&4)+2*((\n/8)&1)+(\n&1),56+(\n|2)
.else
PA_ 56+\n,104+8*(\n/16)+(\n&4)+2*((\n/8)&1)+(\n&1),56+(\n|2)
.endif
.endm
.macro Q n
P \n
P (\n+1)
.endm
.macro mw k
s_add_u32 m0, %[wm], 1024*\k
.endm
.macro mh k
s_add_u32 m0, %[hm], 512*\k
.endm
.macro ad r,k
s_add_u32 s40, s[\r], 512*\k
s_addc_u32 s41, s[\r+1], 0
.endm
.macro ae d,r,k
s_add_u32 s[\d], s[\r], 512*\k
s_addc_u32 s[\d+1], s[\r+1], 0
.endm
.macro bc m,b,d,r
s_bitcmp1_b32 s[\m], \b
s_cselect_b64 s[\d:\d+1], s[\r+2:\r+3], s[\r:\r+1]
.endm
.macro W k
DW 1024*\k
.endm
.macro H k
DH 1024*\k
.endm
k2_code:
s_load_dwordx16 s[48:63], %[pp], 0x0
s_load_dwordx16 s[64:79], %[pp], 0x40
s_load_dwordx16 s[84:99], %[pp], 0x80
s_load_dword s42, %[pp], 0xc0
v_mbcnt_lo_u32_b32 v7, -1, 0
v_mbcnt_hi_u32_b32 v7, -1, v7
v_lshlrev_b32 v0, 4, v7
v_add_u32 v88, 0x1000, v0
v_add_u32 v89, 0x2000, v0
s_mov_b32 s44, %[alo]
s_mov_b32 s45, %[ahi]
global_load_dwordx4 v[132:135], v0, s[44:45]
global_load_dwordx4 v[144:147], v88, s[44:45]
global_load_dwordx4 v[148:151], v89, s[44:45]
ae 44,44,24
global_load_dwordx4 v[168:171], v0, s[44:45]
global_load_dwordx4 v[172:175], v88, s[44:45]
global_load_dwordx4 v[192:195], v89, s[44:45]
s_waitcnt lgkmcnt(0)
s_mov_b64 s[82:83], s[66:67]
bc 49,0,66,60
mh 0
ad 66,0
H 0
DG 1024
ae 44,44,24
global_load_dwordx4 v[196:199], v0, s[44:45]
global_load_dwordx4 v[216:219], v88, s[44:45]
global_load_dwordx4 v[220:223], v89, s[44:45]
ae 44,44,24
global_load_dwordx4 v[224:227], v0, s[44:45]
global_load_dwordx4 v[228:231], v88, s[44:45]
global_load_dwordx4 v[240:243], v89, s[44:45]
v_and_b32 v8, 15, v7
v_lshrrev_b32 v9, 4, v7
v_lshlrev_b32 v10, 2, v7
v_add_u32 v202, s65, v0
global_load_dword v244, v10, s[92:93] offset:0
global_load_dword v245, v10, s[92:93] offset:1024
global_load_dword v246, v10, s[92:93] offset:2048
global_load_dwordx4 v[152:155], v0, s[88:89]
s_mov_b64 exec, 0xff
global_load_dwordx4 v[156:159], v202, s[88:89]
s_mov_b64 exec, -1
s_branch 28f
FY 4
FY 8
FY 12
FY 16
FY 20
FY 24
FY 28
FY 32
FY 36
FY 40
FY 44
FY 48
FY 52
FY 56
FY 60
FY 64
FY 68
FY 72
FY 76
FY 80
FY 84
FY 88
FY 92
FY 96
FY 100
FY 104
FY 108
FY 112
FY 116
FY 120
FY 124
FY 128
28:
FX
s_mov_b32 s92, s64
s_mov_b32 s93, 0x0f0f0f0f
v_add_u32 v1, %[ab], v0
v_add_u32 v5, 0xc000, v1
v_and_b32 v10, 3, v7
v_lshrrev_b32 v11, 2, v7
v_lshl_add_u32 v11, v10, 2, v11
v_and_b32 v11, 15, v11
mw 0
ad 86,0
DR 0
v_lshl_add_u32 v11, v10, 4, v11
v_lshl_add_u32 v2, v11, 4, %[hb]
v_lshl_add_u32 v51, v11, 3, %[hb]
v_lshl_add_u32 v4, v8, 4, %[bb]
s_mov_b32 s38, 0xffff0000
s_mov_b32 s80, 0x40000
s_mov_b32 s81, s52
s_mov_b32 s39, s54
v_lshl_add_u32 v46, v9, 4, %[sb]
v_add_u32 v46, 8192, v46
v_bfe_u32 v10, v7, 1, 2
DR 1024
v_bfe_u32 v11, v7, 3, 1
v_and_b32 v12, 1, v7
v_lshl_add_u32 v11, v11, 1, v12
v_lshl_add_u32 v10, v10, 2, v11
v_lshl_add_u32 v10, v9, 4, v10
v_lshl_add_u32 v90, v10, 2, %[sw]
DR 2048
v_lshl_add_u32 v91, v7, 2, %[sw]
s_and_b32 s42, s51, 0xffff
s_lshr_b32 s43, s51, 16
s_add_u32 s42, s42, %[sb]
s_add_u32 s43, s43, %[sb]
v_lshl_add_u32 v200, v9, 4, s42
v_mov_b32 v201, s43
v_mov_b32 v128, 0x362aaa8f
v_mov_b32 v129, 0x362aaa8f
v_mov_b32 v130, 0x362aaa8f
v_mov_b32 v131, 0x362aaa8f
mw 3
ad 86,6
DR 0
AW 32,128
AW 33,129
AW 34,130
AW 35,131
v_mov_b32 v95, 0
ds_write_b32 v0, v95
s_nop 1
v_mov_b32 v95, 0x7f800000
s_mov_b32 s44, 0x44100000
WV 19
v_add_f64 v[132:133], v[132:133], v[168:169]
WV 18
v_add_f64 v[144:145], v[144:145], v[172:173]
DR 1024
WV 18
v_add_f64 v[148:149], v[148:149], v[192:193]
v_add_f64 v[134:135], v[134:135], v[170:171]
v_add_f64 v[146:147], v[146:147], v[174:175]
v_add_f64 v[150:151], v[150:151], v[194:195]
WV 15
v_add_f64 v[132:133], v[132:133], v[196:197]
WV 14
v_add_f64 v[144:145], v[144:145], v[216:217]
DR 2048
WV 14
v_add_f64 v[148:149], v[148:149], v[220:221]
v_add_f64 v[134:135], v[134:135], v[198:199]
v_add_f64 v[146:147], v[146:147], v[218:219]
v_add_f64 v[150:151], v[150:151], v[222:223]
WV 13
v_add_f64 v[132:133], v[132:133], v[224:225]
WV 12
v_add_f64 v[144:145], v[144:145], v[228:229]
bc 49,1,66,60
mh 9
ad 66,192
H 0
WV 12
)"
R"(v_add_f64 v[148:149], v[148:149], v[240:241]
v_add_f64 v[134:135], v[134:135], v[226:227]
v_add_f64 v[146:147], v[146:147], v[230:231]
v_add_f64 v[150:151], v[150:151], v[242:243]
v_cvt_f32_f64 v248, v[132:133]
v_cvt_f32_f64 v252, v[144:145]
DG 1024
v_cvt_f32_f64 v72, v[148:149]
v_cvt_f32_f64 v249, v[134:135]
v_cvt_f32_f64 v253, v[146:147]
v_cvt_f32_f64 v73, v[150:151]
v_mul_f32 v250, s55, v248
v_mul_f32 v254, s55, v252
bc 48,1,64,56
mh 12
ad 64,192
H 0
v_mul_f32 v74, s55, v72
v_mul_f32 v251, s55, v249
v_mul_f32 v255, s55, v253
v_mul_f32 v75, s55, v73
CV 251,250,251
CV 255,254,255
DG 1024
CV 75,74,75
UL 250,251
UL 254,255
UL 74,75
UH 251,251
UH 255,255
mw 24
ad 86,48
DR 0
UH 75,75
v_add_f32 v250, 0x358637bd, v250
v_add_f32 v254, 0x358637bd, v254
v_add_f32 v74, 0x358637bd, v74
v_add_f32 v251, 0x358637bd, v251
v_add_f32 v255, 0x358637bd, v255
DR 1024
v_add_f32 v75, 0x358637bd, v75
CV 251,250,251
CV 255,254,255
CV 75,74,75
UL 250,251
UL 254,255
DR 2048
UL 74,75
UH 251,251
UH 255,255
UH 75,75
v_sqrt_f32 v250, v250
v_sqrt_f32 v254, v254
mw 27
ad 86,54
DR 0
v_sqrt_f32 v74, v74
v_sqrt_f32 v251, v251
v_sqrt_f32 v255, v255
v_sqrt_f32 v75, v75
CV 251,250,251
CV 255,254,255
DR 1024
CV 75,74,75
UL 250,251
UL 254,255
UL 74,75
UH 251,251
UH 255,255
DR 2048
UH 75,75
v_cmp_le_f32 vcc, s44, v248
v_cmp_le_f32 s[54:55], s44, v249
s_nop 0
v_cndmask_b32 v248, v95, v250, vcc
v_cndmask_b32 v249, v95, v251, s[54:55]
v_add_f32 v248, v248, v249
v_cmp_le_f32 vcc, s44, v252
v_cmp_le_f32 s[54:55], s44, v253
s_nop 0
v_cndmask_b32 v252, v95, v254, vcc
v_cndmask_b32 v253, v95, v255, s[54:55]
v_add_f32 v252, v252, v253
v_cmp_le_f32 vcc, s44, v72
v_cmp_le_f32 s[54:55], s44, v73
s_nop 0
v_cndmask_b32 v72, v95, v74, vcc
v_cndmask_b32 v73, v95, v75, s[54:55]
bc 49,2,66,60
mh 18
ad 66,384
H 0
v_add_f32 v72, v72, v73
v_add_f32 v92, v248, v252
v_add_f32 v92, v92, v72
s_nop 1
v_add_f32_dpp v92, v92, v92 row_shr:1 row_mask:0xf bank_mask:0xf
s_nop 1
v_add_f32_dpp v92, v92, v92 row_shr:2 row_mask:0xf bank_mask:0xf
s_nop 1
v_add_f32_dpp v92, v92, v92 row_shr:4 row_mask:0xf bank_mask:0xf
s_nop 1
v_add_f32_dpp v92, v92, v92 row_shr:8 row_mask:0xf bank_mask:0xf
s_nop 1
v_add_f32_dpp v92, v92, v92 row_bcast:15 row_mask:0xa bank_mask:0xf
s_nop 1
v_add_f32_dpp v92, v92, v92 row_bcast:31 row_mask:0xc bank_mask:0xf
ds_write_b32 v91, v92 offset:7168
v_mov_b32 v10, %[sb]
WL 0
s_barrier
ds_read_b32 v96, v10 offset:7420
ds_read_b32 v97, v10 offset:7676
ds_read_b32 v98, v10 offset:7932
ds_read_b32 v99, v10 offset:8188
WV 21
UL 76,244
WV 20
UL 80,245
WV 19
UL 84,246
UH 77,244
DG 1024
UH 81,245
UH 85,246
v_mov_b32 v93, 0
WL 0
v_add_f32 v96, v96, v97
v_add_f32 v98, v98, v99
v_add_f32 v96, v96, v98
bc 48,2,64,56
mh 21
ad 64,384
H 0
v_mul_f32 v94, 0x3a2aaaab, v96
v_cmp_nlt_f32 s[44:45], v96, v95
CV 94,94,94
UL 94,94
v_add_f32 v94, 0x358637bd, v94
CV 94,94,94
UL 94,94
H 1
v_rcp_f32 v94, v94
s_nop 0
v_mul_f32 v250, v94, v250
v_mul_f32 v254, v94, v254
v_mul_f32 v74, v94, v74
v_mul_f32 v251, v94, v251
v_mul_f32 v255, v94, v255
v_mul_f32 v75, v94, v75
H 2
CV 251,250,251
CV 255,254,255
CV 75,74,75
UL 250,251
UL 254,255
UL 74,75
mw 48
ad 86,96
DR 0
UH 251,251
UH 255,255
UH 75,75
v_fma_f32 v250, v76, v250, 1.0
v_fma_f32 v254, v80, v254, 1.0
v_fma_f32 v74, v84, v74, 1.0
DR 1024
v_fma_f32 v251, v77, v251, 1.0
v_fma_f32 v255, v81, v255, 1.0
v_fma_f32 v75, v85, v75, 1.0
v_cvt_pk_f16_f32 v76, v250, v251
v_cvt_pk_f16_f32 v80, v254, v255
v_cvt_pk_f16_f32 v84, v74, v75
ds_write_b32 v90, v76 offset:8192
ds_write_b32 v90, v80 offset:1024
ds_write_b32 v90, v84 offset:2048
v_max3_f32 v93, |v250|, |v251|, v93
DR 2048
v_max3_f32 v93, |v254|, |v255|, v93
v_max3_f32 v93, |v74|, |v75|, v93
v_cmp_lt_f32 vcc, 0x43000000, v93
WL 0
WV 16
s_barrier
TY 106,520
TY 110,14344
TX 58,0
TX 62,13824
TY 66,512
TY 70,14336
RS 14,0
RS 18,64
ds_read_b128 v[176:179], v200
ds_read_b128 v[180:183], v201
R 0
R 2
FG
s_mov_b32 s54, 0x06020400
s_mov_b32 s55, 0x07030501
WL 13
AN 104,106
AN 105,107
SH 106
AN 106,106
mw 51
ad 86,102
DR 0
AN 107,107
WL 12
AN 108,110
AN 109,111
SH 110
AN 110,110
AN 111,111
DR 1024
WL 10
Q 0
Q 2
Q 4
DR 2048
Q 6
R 4
R 6
WL 9
v_pk_mul_f16 v22, v14, s92
v_pk_mul_f16 v23, v15, s92
v_pk_mul_f16 v24, v16, s92
v_pk_mul_f16 v25, v17, s92
v_pk_mul_f16 v26, v18, s92
v_pk_mul_f16 v27, v19, s92
v_pk_mul_f16 v28, v20, s92
v_pk_mul_f16 v29, v21, s92
PF 56,0
PF 57,1
PF 58,2
PF 59,3
PF 60,4
PF 61,5
PF 62,6
PF 63,7
Q 8
Q 10
Q 12
Q 14
PF 64,0
PF 65,1
PF 66,2
PF 67,3
PF 68,4
PF 69,5
PF 70,6
PF 71,7
PM 152,176
PM 153,177
PM 154,178
PM 155,179
WL 2
PM 156,180
PM 157,181
PM 158,182
PM 159,183
s_mov_b32 s42, 0x1c001c00
v_pk_mul_f16 v152, v152, s42
v_pk_mul_f16 v153, v153, s42
v_pk_mul_f16 v154, v154, s42
v_pk_mul_f16 v155, v155, s42
v_pk_mul_f16 v156, v156, s42
v_pk_mul_f16 v157, v157, s42
v_pk_mul_f16 v158, v158, s42
v_pk_mul_f16 v159, v159, s42
global_store_dwordx4 v0, v[152:155], s[90:91] sc1
s_mov_b64 exec, 0xff
global_store_dwordx4 v202, v[156:159], s[90:91] sc1
s_mov_b64 exec, -1
s_mov_b32 s42, 0x3c003c00
v_dot2_f32_f16 v52, v152, s42, 0
v_dot2_f32_f16 v53, v156, s42, 0
s_nop 1
v_dot2_f32_f16 v52, v153, s42, v52
v_dot2_f32_f16 v53, v157, s42, v53
s_nop 1
v_dot2_f32_f16 v52, v154, s42, v52
v_dot2_f32_f16 v53, v158, s42, v53
s_nop 1
v_dot2_f32_f16 v52, v155, s42, v52
v_dot2_f32_f16 v53, v159, s42, v53
s_nop 1
v_mfma_f32_16x16x32_f16 a[36:39], v[56:59], a[0:3], a[32:35]
mw 72
RS 14,128
RS 18,192
TY 106,5128
M 1
ad 86,144
DR 0
TY 110,18952
v_mfma_f32_16x16x32_f16 a[60:63], v[60:63], a[12:15], a[32:35]
DR 1024
TX 32,4608
TX 36,18432
M 12
DR 2048
TY 40,5120
TY 44,18944
v_mfma_f32_16x16x32_f16 a[64:67], v[64:67], a[8:11], a[32:35]
mw 75
M 15
R 10
v_mfma_f32_16x16x32_f16 a[40:43], v[68:71], a[4:7], a[32:35]
ad 86,150
DR 0
M 2
R 8
v_mfma_f32_16x16x32_f16 a[88:91], v[64:67], a[16:19], a[32:35]
WL 10
DR 1024
v_pk_mul_f16 v22, v14, s92
M 27
DR 2048
v_pk_mul_f16 v23, v15, s92
v_mfma_f32_16x16x32_f16 a[84:87], v[56:59], a[16:19], a[32:35]
v_pk_mul_f16 v24, v16, s92
v_pk_mul_f16 v25, v17, s92
M 25
v_pk_mul_f16 v26, v18, s92
v_pk_mul_f16 v27, v19, s92
v_mfma_f32_16x16x32_f16 a[108:111], v[56:59], a[24:27], a[32:35]
WV 19
v_pk_mul_f16 v28, v20, s92
v_pk_mul_f16 v29, v21, s92
M 37
WL 0
AN 104,106
AN 105,107
v_mfma_f32_16x16x32_f16 a[112:115], v[64:67], a[24:27], a[32:35]
s_barrier
R 24
M 39
R 26
v_mfma_f32_16x16x32_f16 a[136:139], v[64:67], a[0:3], a[32:35]
mw 0
SH 106
AN 106,106
M 51
ad 86,192
DR 0
v_mfma_f32_16x16x32_f16 a[132:135], v[56:59], a[0:3], a[32:35]
DR 1024
AN 107,107
M 49
R 28
v_mfma_f32_16x16x32_f16 a[156:159], v[56:59], a[8:11], a[32:35]
DR 2048
AN 108,110
M 61
mw 3
AN 109,111
SH 110
v_mfma_f32_16x16x32_f16 a[160:163], v[64:67], a[8:11], a[32:35]
ad 86,198
DR 0
M 63
WL 4
R 30
v_mfma_f32_16x16x32_f16 a[184:187], v[64:67], a[16:19], a[32:35]
DR 1024
AN 110,110
M 75
DR 2048
AN 111,111
v_mfma_f32_16x16x32_f16 a[180:183], v[56:59], a[16:19], a[32:35]
PA_ 30,104,32
PA_ 31,105,33
M 73
WL 4
R 32
v_mfma_f32_16x16x32_f16 v[136:139], v[56:59], a[24:27], v[128:131]
PB_ 32,104,32
PB_ 33,105,33
M 85
PA_ 34,108,36
PA_ 35,109,37
v_mfma_f32_16x16x32_f16 v[140:143], v[64:67], a[24:27], v[128:131]
PB_ 36,108,36
PB_ 37,109,37
M 87
WL 4
R 34
v_mfma_f32_16x16x32_f16 v[164:167], v[64:67], a[0:3], v[128:131]
PF 30,0
PF 31,1
M 99
PF 32,2
PF 33,3
v_mfma_f32_16x16x32_f16 v[160:163], v[56:59], a[0:3], v[128:131]
PF 34,4
PF 35,5
M 97
WL 4
PF 36,6
)"
R"(PF 37,7
v_mfma_f32_16x16x32_f16 v[184:187], v[56:59], a[8:11], v[128:131]
WV 14
AW 200,30
AW 201,31
M 109
WL 0
AW 202,32
AW 203,33
v_mfma_f32_16x16x32_f16 v[188:191], v[64:67], a[8:11], v[128:131]
s_barrier
R 48
TY 114,6664
M 111
R 50
TY 118,20488
v_mfma_f32_16x16x32_f16 v[208:211], v[56:59], a[16:19], v[128:131]
mw 24
AW 204,34
AW 205,35
M 121
ad 86,240
DR 0
v_mfma_f32_16x16x32_f16 v[232:235], v[60:63], a[28:31], v[128:131]
DR 1024
AW 206,36
M 132
DR 2048
AW 207,37
PA_ 38,106,40
PA_ 39,107,41
PB_ 40,106,40
PB_ 41,107,41
v_mfma_f32_16x16x32_f16 v[236:239], v[64:67], a[24:27], v[128:131]
mw 27
PA_ 42,110,44
PA_ 43,111,45
PB_ 44,110,44
PB_ 45,111,45
PF 38,0
PF 39,1
M 135
WL 0
R 54
PF 40,2
PF 41,3
PF 42,4
PF 43,5
AN 112,114
AN 113,115
SH 114
AN 114,114
v_mfma_f32_16x16x32_f16 v[212:215], v[68:71], a[20:23], v[128:131]
ad 86,246
DR 0
PF 44,6
PF 45,7
AW 208,38
AW 209,39
AN 115,115
AN 116,118
AN 117,119
SH 118
M 122
R 52
AW 210,40
AW 211,41
AW 212,42
AW 213,43
AN 118,118
AN 119,119
NA 36,0,200
DR 1024
AW 214,44
AW 215,45
NA 36,4,204
DR 2048
TX 32,6144
TX 36,19968
NA 60,12,204
bc 49,3,66,60
TY 40,6656
TY 44,20480
NA 60,8,200
mh 0
WL 2
PA_ 30,112,32
PA_ 31,113,33
PB_ 32,112,32
PB_ 33,113,33
PA_ 34,116,36
PA_ 35,117,37
PB_ 36,116,36
PB_ 37,117,37
PF 30,0
PF 31,1
NA 64,8,208
ad 66,576
H 0
PF 32,2
PF 33,3
PF 34,4
PF 35,5
PF 36,6
PF 37,7
AW 216,30
AW 217,31
NA 64,12,212
WL 0
DG 1024
AW 218,32
AW 219,33
AW 220,34
AW 221,35
AW 222,36
AW 223,37
NA 40,4,212
bc 48,3,64,56
TY 106,9736
TY 110,23560
NA 40,0,208
mh 3
PA_ 38,114,40
PA_ 39,115,41
PB_ 40,114,40
PB_ 41,115,41
PA_ 42,118,44
PA_ 43,119,45
PB_ 44,118,44
PB_ 45,119,45
PF 38,0
PF 39,1
v_mfma_f32_16x16x32_f16 a[44:47], a[216:219], a[0:3], a[32:35]
ad 64,576
H 0
PF 40,2
PF 41,3
PF 42,4
PF 43,5
PF 44,6
PF 45,7
AW 224,38
AW 225,39
NA 44,4,220
H 1
AW 226,40
AW 227,41
AW 228,42
AW 229,43
AW 230,44
AW 231,45
v_mfma_f32_16x16x32_f16 a[68:71], a[220:223], a[12:15], a[32:35]
H 2
RS 14,256
TX 58,9216
NA 68,8,216
RS 18,320
TX 62,23040
TY 66,9728
v_mfma_f32_16x16x32_f16 a[72:75], a[224:227], a[8:11], a[32:35]
WL 5
TY 70,23552
AN 104,106
NA 72,12,228
R 58
v_mfma_f32_16x16x32_f16 a[48:51], a[228:231], a[4:7], a[32:35]
AN 105,107
SH 106
NA 48,0,224
R 56
v_mfma_f32_16x16x32_f16 a[96:99], a[224:227], a[16:19], a[32:35]
AN 106,106
AN 107,107
NA 96,20,228
WL 7
v_pk_mul_f16 v22, v14, s92
v_pk_mul_f16 v23, v15, s92
v_mfma_f32_16x16x32_f16 a[92:95], a[216:219], a[16:19], a[32:35]
v_pk_mul_f16 v24, v16, s92
v_pk_mul_f16 v25, v17, s92
NA 92,20,220
v_pk_mul_f16 v26, v18, s92
v_pk_mul_f16 v27, v19, s92
NA 88,16,208
v_pk_mul_f16 v28, v20, s92
v_pk_mul_f16 v29, v21, s92
NA 88,20,212
AN 108,110
AN 109,111
NA 84,16,200
SH 110
AN 110,110
NA 84,20,204
WL 6
AN 111,111
P 0
NA 108,24,200
Q 1
NA 108,28,204
Q 3
NA 112,24,208
Q 5
NA 112,28,212
P 7
PF 56,0
v_mfma_f32_16x16x32_f16 a[116:119], a[216:219], a[24:27], a[32:35]
WV 17
PF 57,1
PF 58,2
NA 116,28,220
WL 0
PF 59,3
PF 60,4
v_mfma_f32_16x16x32_f16 a[120:123], a[224:227], a[24:27], a[32:35]
s_barrier
R 72
TY 114,11272
NA 120,28,228
R 74
TY 118,25096
v_mfma_f32_16x16x32_f16 a[144:147], a[224:227], a[0:3], a[32:35]
mw 48
v_mov_b32 v54, 1
s_mov_b64 exec, 1
FS
s_mov_b64 exec, -1
NA 144,4,228
ad 86,288
DR 0
TX 74,10752
v_mfma_f32_16x16x32_f16 a[140:143], a[216:219], a[0:3], a[32:35]
PF 61,5
PF 62,6
TX 78,24576
NA 140,4,220
DR 1024
PF 63,7
TY 82,11264
NA 136,0,208
Q 8
TY 86,25088
NA 136,4,212
DR 2048
P 10
TY 122,12808
NA 132,0,200
Q 11
TY 126,26632
NA 132,4,204
WL 6
R 76
TX 90,12288
NA 156,8,200
mw 51
Q 13
TX 94,26112
NA 156,12,204
ad 86,294
DR 0
TY 98,12800
NA 160,8,208
P 15
PF 64,0
TY 102,26624
NA 160,12,212
DR 1024
PF 65,1
v_mfma_f32_16x16x32_f16 a[164:167], a[216:219], a[8:11], a[32:35]
PF 66,2
PF 67,3
NA 164,12,220
DR 2048
PF 68,4
v_mfma_f32_16x16x32_f16 a[168:171], a[224:227], a[8:11], a[32:35]
PF 69,5
PF 70,6
NA 168,12,228
R 78
v_mfma_f32_16x16x32_f16 a[192:195], a[224:227], a[16:19], a[32:35]
bc 49,4,66,60
PF 71,7
NA 192,20,228
mh 9
AN 112,114
AN 113,115
v_mfma_f32_16x16x32_f16 a[188:191], a[216:219], a[16:19], a[32:35]
ad 66,768
H 0
NA 188,20,220
SH 114
AN 114,114
NA 184,16,208
DG 1024
NA 184,20,212
AN 115,115
AN 116,118
NA 180,16,200
bc 48,4,64,56
AN 117,119
NA 180,20,204
WL 2
R 80
NV 136,24,200
mh 12
SH 118
AN 118,118
NV 136,28,204
ad 64,768
H 0
NV 140,24,208
AN 119,119
P 16
NV 140,28,212
H 1
P 17
v_mfma_f32_16x16x32_f16 v[144:147], a[216:219], a[24:27], v[128:131]
Q 18
NV 144,28,220
H 2
P 20
v_mfma_f32_16x16x32_f16 v[148:151], a[224:227], a[24:27], v[128:131]
Q 21
NV 148,28,228
R 82
v_mfma_f32_16x16x32_f16 v[172:175], a[224:227], a[0:3], v[128:131]
P 23
PF 72,0
NV 172,4,228
PF 73,1
PF 74,2
v_mfma_f32_16x16x32_f16 v[168:171], a[216:219], a[0:3], v[128:131]
PF 75,3
PF 76,4
NV 168,4,220
PF 77,5
PF 78,6
NV 164,0,208
PF 79,7
P 24
NV 164,4,212
Q 25
NV 160,0,200
Q 27
NV 160,4,204
WL 2
Q 29
NV 184,8,200
P 31
PF 80,0
NV 184,12,204
PF 81,1
PF 82,2
NV 188,8,208
PF 83,3
PF 84,4
NV 188,12,212
PF 85,5
PF 86,6
v_mfma_f32_16x16x32_f16 v[192:195], a[216:219], a[8:11], v[128:131]
WV 23
PF 87,7
AN 120,122
NV 192,12,220
WL 0
AN 121,123
SH 122
v_mfma_f32_16x16x32_f16 v[196:199], a[224:227], a[8:11], v[128:131]
s_barrier
R 0
NV 196,12,228
R 2
NV 208,16,200
mw 72
AN 122,122
AN 123,123
NV 208,20,204
ad 86,336
DR 0
NV 232,28,204
AN 124,126
AN 125,127
NV 232,24,200
DR 1024
SH 126
NV 236,24,208
AN 126,126
AN 127,127
NV 236,28,212
DR 2048
P 32
NV 212,20,212
Q 33
NV 212,16,208
mw 75
Q 35
v_mfma_f32_16x16x32_f16 v[216:219], a[216:219], a[16:19], v[128:131]
ad 86,342
DR 0
NV 216,20,220
Q 37
)"
R"(v_mfma_f32_16x16x32_f16 v[240:243], a[220:223], a[28:31], v[128:131]
DR 1024
P 39
NV 240,24,216
PF 88,0
PF 89,1
v_mfma_f32_16x16x32_f16 v[244:247], a[224:227], a[24:27], v[128:131]
DR 2048
PF 90,2
NV 244,28,228
R 6
v_mfma_f32_16x16x32_f16 v[220:223], a[228:231], a[20:23], v[128:131]
PF 91,3
PF 92,4
NV 220,16,224
WL 2
R 4
M 0
PF 93,5
PF 94,6
M 1
PF 95,7
P 40
M 13
Q 41
M 12
Q 43
M 14
Q 45
M 15
P 47
PF 96,0
M 3
PF 97,1
PF 98,2
M 2
PF 99,3
PF 100,4
M 4
PF 101,5
PF 102,6
M 5
PF 103,7
M 17
RS 14,384
RS 18,448
M 16
M 18
M 19
M 7
M 6
v_mfma_f32_16x16x32_f16 a[52:55], v[88:91], a[0:3], a[32:35]
WL 0
v_pk_mul_f16 v22, v14, s92
v_pk_mul_f16 v23, v15, s92
M 9
v_pk_mul_f16 v24, v16, s92
v_pk_mul_f16 v25, v17, s92
v_mfma_f32_16x16x32_f16 a[76:79], v[92:95], a[12:15], a[32:35]
v_pk_mul_f16 v26, v18, s92
v_pk_mul_f16 v27, v19, s92
M 20
v_pk_mul_f16 v28, v20, s92
v_pk_mul_f16 v29, v21, s92
v_mfma_f32_16x16x32_f16 a[80:83], v[96:99], a[8:11], a[32:35]
v_lshrrev_b32 v9, 8, v0
v_bfe_u32 v8, v0, 4, 4
M 23
R 10
v_mfma_f32_16x16x32_f16 a[56:59], v[100:103], a[4:7], a[32:35]
v_lshlrev_b32 v10, 2, v9
M 10
R 8
v_mfma_f32_16x16x32_f16 a[104:107], v[96:99], a[16:19], a[32:35]
s_mov_b32 s42, 0xcb527610
M 35
s_movk_i32 s43, 0xc00
v_lshlrev_b32 v11, 3, v8
v_bfe_u32 v3, s42, v10, 4
v_mfma_f32_16x16x32_f16 a[100:103], v[88:91], a[16:19], a[32:35]
v_add_u32 v10, 16, v10
v_mad_u32_u24 v3, v3, s43, v11
M 33
v_bfe_u32 v48, s42, v10, 4
v_add_u32 v10, 1, v9
M 30
v_mad_u32_u24 v48, v48, s43, v11
v_and_b32 v10, 2, v10
M 31
v_sub_u32 v10, 1, v10
v_mul_i32_i24 v49, s43, v10
M 28
M 29
M 26
M 27
M 24
M 25
M 36
M 37
M 38
M 39
M 40
M 41
M 42
M 43
WV 18
v_mfma_f32_16x16x32_f16 a[124:127], v[88:91], a[24:27], a[32:35]
WL 0
M 45
s_barrier
R 24
TY 106,520
v_mfma_f32_16x16x32_f16 a[128:131], v[96:99], a[24:27], a[32:35]
TY 110,14344
TX 32,0
TX 36,13824
M 47
R 26
TY 40,512
v_mfma_f32_16x16x32_f16 a[152:155], v[96:99], a[0:3], a[32:35]
TY 44,14336
TY 114,2056
TY 118,15880
M 59
v_mfma_f32_16x16x32_f16 a[148:151], v[88:91], a[0:3], a[32:35]
M 57
M 54
WL 9
AN 104,106
AN 105,107
M 55
bc 49,5,66,60
SH 106
M 52
mh 18
AN 106,106
AN 107,107
M 53
ad 66,960
H 0
M 50
WL 8
AN 108,110
AN 109,111
M 51
SH 110
AN 110,110
M 48
WL 6
AN 111,111
PA_ 30,104,32
M 49
R 28
M 60
DG 1024
M 61
PA_ 31,105,33
PB_ 32,104,32
M 62
PB_ 33,105,33
PA_ 34,108,36
M 63
PA_ 35,109,37
PB_ 36,108,36
M 64
PB_ 37,109,37
PF 30,0
M 65
PF 31,1
PF 32,2
M 66
v_lshlrev_b32 v55, 4, v0
PF 33,3
M 67
FL
PF 34,4
v_mfma_f32_16x16x32_f16 a[172:175], v[88:91], a[8:11], a[32:35]
PF 35,5
PF 36,6
M 69
PF 37,7
AW 200,30
v_mfma_f32_16x16x32_f16 a[176:179], v[96:99], a[8:11], a[32:35]
AW 201,31
AW 202,32
M 71
R 30
v_mfma_f32_16x16x32_f16 v[132:135], v[96:99], a[16:19], v[128:131]
AW 203,33
AW 204,34
M 83
AW 205,35
AW 206,36
v_mfma_f32_16x16x32_f16 a[196:199], v[88:91], a[16:19], a[32:35]
WL 2
AW 207,37
PA_ 38,106,40
M 81
PA_ 39,107,41
PB_ 40,106,40
TX 32,1536
M 78
PB_ 41,107,41
PA_ 42,110,44
TX 36,15360
M 79
PA_ 43,111,45
PB_ 44,110,44
M 76
PB_ 45,111,45
PF 38,0
M 77
PF 39,1
PF 40,2
M 74
PF 41,3
PF 42,4
M 75
PF 43,5
PF 44,6
M 72
PF 45,7
AW 208,38
M 73
R 32
M 84
AW 209,39
AW 210,40
M 85
bc 48,5,64,56
AW 211,41
M 86
mh 21
AW 212,42
AW 213,43
M 87
ad 64,960
H 0
M 88
AW 214,44
AW 215,45
M 89
TY 40,2048
TY 44,15872
TY 122,3592
M 90
AN 112,114
AN 113,115
TY 126,17416
M 91
H 1
SH 114
v_mfma_f32_16x16x32_f16 v[152:155], v[88:91], a[24:27], v[128:131]
AN 114,114
AN 115,115
M 93
AN 116,118
AN 117,119
v_mfma_f32_16x16x32_f16 v[156:159], v[96:99], a[24:27], v[128:131]
SH 118
AN 118,118
M 95
R 34
v_mfma_f32_16x16x32_f16 v[180:183], v[96:99], a[0:3], v[128:131]
H 2
AN 119,119
M 107
WL 6
PA_ 30,112,32
PA_ 31,113,33
v_mfma_f32_16x16x32_f16 v[176:179], v[88:91], a[0:3], v[128:131]
PB_ 32,112,32
PB_ 33,113,33
M 105
PA_ 34,116,36
PA_ 35,117,37
M 102
PB_ 36,116,36
PB_ 37,117,37
M 103
PF 30,0
PF 31,1
M 100
PF 32,2
PF 33,3
M 101
PF 34,4
PF 35,5
M 98
PF 36,6
PF 37,7
M 99
AW 216,30
AW 217,31
M 96
AW 218,32
AW 219,33
M 97
AW 220,34
AW 221,35
M 108
AW 222,36
AW 223,37
M 109
WL 0
PA_ 38,114,40
PA_ 39,115,41
TX 32,3072
M 110
WV 3
v_cmp_eq_u32 vcc, 0, v54
PB_ 40,114,40
TX 36,16896
M 111
PB_ 41,115,41
PA_ 42,118,44
M 112
PA_ 43,119,45
PB_ 44,118,44
M 113
FB
PB_ 45,119,45
PF 38,0
M 114
PF 39,1
PF 40,2
M 115
PF 41,3
PF 42,4
v_mfma_f32_16x16x32_f16 v[200:203], v[88:91], a[8:11], v[128:131]
PF 43,5
PF 44,6
M 117
s_barrier
R 48
v_mfma_f32_16x16x32_f16 v[204:207], v[96:99], a[8:11], v[128:131]
mw 0
PF 45,7
AW 224,38
M 119
R 50
M 120
ad 84,384
W 0
M 121
W 1
AW 225,39
M 133
W 2
AW 226,40
M 132
mw 3
AW 227,41
AW 228,42
M 134
ad 84,390
W 0
M 135
W 1
AW 229,43
M 123
W 2
AW 230,44
M 122
mw 24
AW 231,45
AN 120,122
M 124
ad 84,432
W 0
TY 40,3584
M 125
W 1
TY 44,17408
M 137
W 2
AN 121,123
M 136
mw 27
SH 122
AN 122,122
M 138
ad 84,438
W 0
M 139
W 1
AN 123,123
M 127
W 2
AN 124,126
M 126
AN 125,127
SH 126
v_mfma_f32_16x16x32_f16 v[224:227], v[88:91], a[16:19], v[128:131]
AN 126,126
AN 127,127
M 129
WL 2
PA_ 30,120,32
PA_ 31,121,33
)"
R"(v_mfma_f32_16x16x32_f16 v[248:251], v[92:95], a[28:31], v[128:131]
PB_ 32,120,32
PB_ 33,121,33
M 140
PA_ 34,124,36
PA_ 35,125,37
v_mfma_f32_16x16x32_f16 v[252:255], v[96:99], a[24:27], v[128:131]
PB_ 36,124,36
PB_ 37,125,37
M 143
R 54
v_mfma_f32_16x16x32_f16 v[228:231], v[100:103], a[20:23], v[128:131]
PF 30,0
PF 31,1
M 130
R 52
NA 36,0,200
PF 32,2
PF 33,3
NA 36,4,204
PF 34,4
PF 35,5
NA 60,12,204
PF 36,6
PF 37,7
NA 60,8,200
AW 232,30
AW 233,31
NA 64,8,208
AW 234,32
AW 235,33
NA 64,12,212
AW 236,34
AW 237,35
NA 40,4,212
AW 238,36
AW 239,37
NA 40,0,208
WL 4
PA_ 38,122,40
PA_ 39,123,41
NA 44,0,216
PB_ 40,122,40
PB_ 41,123,41
NA 44,4,220
PA_ 42,126,44
PA_ 43,127,45
NA 68,12,220
PB_ 44,126,44
PB_ 45,127,45
NA 68,8,216
PF 38,0
PF 39,1
NA 72,8,224
PF 40,2
PF 41,3
NA 72,12,228
PF 42,4
PF 43,5
NA 48,4,228
PF 44,6
PF 45,7
NA 48,0,224
AW 240,38
AW 241,39
NA 52,0,232
AW 242,40
AW 243,41
NA 52,4,236
AW 244,42
AW 245,43
NA 76,12,236
AW 246,44
AW 247,45
NA 76,8,232
NA 80,8,240
NA 80,12,244
R 58
NA 56,4,244
NA 56,0,240
WL 2
R 56
NA 104,16,240
NA 104,20,244
NA 100,16,232
NA 100,20,236
NA 96,16,224
NA 96,20,228
NA 92,16,216
NA 92,20,220
NA 88,16,208
NA 88,20,212
NA 84,16,200
NA 84,20,204
NA 108,24,200
NA 108,28,204
NA 112,24,208
NA 112,28,212
NA 116,24,216
NA 116,28,220
NA 120,24,224
NA 120,28,228
NA 124,24,232
WL 0
NA 124,28,236
s_barrier
R 72
TY 106,5128
NA 128,24,240
mw 48
TY 110,18952
TX 58,4608
TX 62,18432
NA 128,28,244
R 74
TY 66,5120
NA 152,0,240
ad 84,480
W 0
TY 70,18944
NA 152,4,244
v_lshrrev_b32 v8, 1, v0
s_mov_b64 exec, s[76:77]
global_load_dwordx2 v[6:7], v8, s[78:79] sc1
s_mov_b64 exec, -1
NA 148,0,232
TY 114,6664
TY 118,20488
TX 74,6144
NA 148,4,236
WL 7
TX 78,19968
TY 82,6656
TY 86,20480
NA 144,0,224
W 1
AN 104,106
TY 122,8200
NA 144,4,228
AN 105,107
SH 106
TY 126,22024
NA 140,0,216
AN 106,106
AN 107,107
TX 90,7680
NA 140,4,220
AN 108,110
AN 109,111
TX 94,21504
NA 136,0,208
W 2
SH 110
TY 98,8192
NA 136,4,212
WL 9
AN 110,110
AN 111,111
TY 102,22016
NA 132,0,200
Q 0
NA 132,4,204
R 76
NA 156,8,200
mw 51
Q 2
NA 156,12,204
ad 84,486
W 0
NA 160,8,208
Q 4
NA 160,12,212
Q 6
NA 164,8,216
Q 8
NA 164,12,220
W 1
P 10
NA 168,8,224
Q 11
NA 168,12,228
Q 13
NA 172,8,232
P 15
AN 112,114
NA 172,12,236
W 2
AN 113,115
NA 176,8,240
SH 114
AN 114,114
NA 176,12,244
R 78
NV 132,16,240
AN 115,115
AN 116,118
NV 132,20,244
bc 49,6,66,60
AN 117,119
NA 196,16,232
mh 0
SH 118
AN 118,118
NA 196,20,236
ad 66,1152
H 0
NA 192,16,224
WL 2
AN 119,119
P 16
NA 192,20,228
Q 17
NA 188,16,216
Q 19
NA 188,20,220
DG 1024
NA 184,16,208
Q 21
NA 184,20,212
Q 23
NA 180,16,200
Q 25
NA 180,20,204
R 80
NV 136,24,200
bc 48,6,64,56
P 27
NV 136,28,204
mh 3
Q 28
NV 140,24,208
ad 64,1152
H 0
NV 140,28,212
Q 30
NV 144,24,216
AN 120,122
AN 121,123
NV 144,28,220
SH 122
AN 122,122
NV 148,24,224
H 1
AN 123,123
NV 148,28,228
AN 124,126
AN 125,127
NV 152,24,232
SH 126
AN 126,126
NV 152,28,236
AN 127,127
P 32
NV 156,24,240
H 2
P 33
NV 156,28,244
R 82
NV 180,0,240
Q 34
NV 180,4,244
Q 36
NV 176,0,232
Q 38
NV 176,4,236
Q 40
NV 172,0,224
Q 42
NV 172,4,228
Q 44
NV 168,0,216
Q 46
NV 168,4,220
NV 164,0,208
NV 164,4,212
NV 160,0,200
NV 160,4,204
WL 2
NV 184,8,200
NV 184,12,204
NV 188,8,208
NV 188,12,212
NV 192,8,216
NV 192,12,220
NV 196,8,224
NV 196,12,228
WV 18
NV 200,8,232
WL 0
NV 200,12,236
s_barrier
R 0
NV 204,8,240
mw 72
NV 204,12,244
R 2
NV 208,16,200
ad 84,528
W 0
NV 208,20,204
NV 232,28,204
NV 232,24,200
NV 236,24,208
W 1
NV 236,28,212
NV 212,20,212
NV 212,16,208
NV 216,16,216
W 2
NV 216,20,220
ae 44,70,0
NV 240,28,220
s_bitcmp1_b32 s50, 0
s_cselect_b32 s51, 24576, 0
v_add_u32 v47, s51, v3
NV 240,24,216
XL 14,0
NV 244,24,224
mw 75
NV 244,28,228
ad 84,534
W 0
NV 220,20,228
XL 16,768
NV 220,16,224
NV 224,16,232
NV 224,20,236
W 1
NV 248,28,236
NV 248,24,232
NV 252,24,240
XL 18,1536
NV 252,28,244
R 6
NV 228,20,244
W 2
NV 228,16,240
WL 2
R 4
M 0
M 1
M 13
XL 20,2304
M 12
M 14
M 15
WV 20
v_or_b32 v6, v6, v7
s_mov_b64 exec, s[76:77]
v_cmp_ne_u32 %[flg], 0, v6
s_mov_b64 exec, -1
M 3
M 2
M 4
M 5
M 17
M 16
M 18
M 19
M 7
M 6
M 8
M 9
M 21
M 20
M 22
M 23
R 10
M 11
M 10
WL 2
R 8
M 34
M 35
M 32
M 33
M 30
M 31
M 28
M 29
M 26
M 27
M 24
M 25
M 36
M 37
M 38
M 39
M 40
M 41
M 42
M 43
WV 15
M 44
WL 0
M 45
s_barrier
R 24
M 46
mw 0
)"
R"(M 47
R 26
M 58
ad 84,576
W 0
M 59
TY 106,9736
TY 110,23560
M 56
M 57
W 1
M 54
M 55
M 52
W 2
M 53
M 50
M 51
mw 3
M 48
ad 84,582
W 0
M 49
R 28
M 60
WL 3
AN 104,106
AN 105,107
M 61
W 1
SH 106
M 62
AN 106,106
AN 107,107
M 63
WL 2
AN 108,110
AN 109,111
M 64
W 2
SH 110
M 65
AN 110,110
AN 111,111
M 66
TY 114,11272
TY 118,25096
M 67
bc 49,7,66,60
M 68
mh 9
M 69
ad 66,1344
H 0
M 70
M 71
R 30
M 82
DG 1024
M 83
M 80
WL 3
M 81
bc 48,7,64,56
AN 112,114
M 78
mh 12
AN 113,115
SH 114
M 79
ad 64,1344
H 0
M 76
AN 114,114
AN 115,115
M 77
WL 2
AN 116,118
AN 117,119
M 74
H 1
SH 118
M 75
AN 118,118
AN 119,119
M 72
TY 122,12808
TY 126,26632
M 73
R 32
M 84
H 2
M 85
M 86
M 87
s_load_dwordx4 s[88:91], %[pp], 0xc0
s_load_dwordx2 s[86:87], %[pp], 0xd0
M 88
M 89
M 90
M 91
WL 3
AN 120,122
AN 121,123
M 92
SH 122
AN 122,122
M 93
WL 2
AN 123,123
AN 124,126
M 94
AN 125,127
SH 126
M 95
R 34
M 106
AN 126,126
AN 127,127
M 107
M 104
M 105
M 102
M 103
M 100
M 101
M 98
M 99
M 96
M 97
R 48
M 108
M 109
M 110
M 111
M 112
M 113
M 114
M 115
M 116
s_waitcnt lgkmcnt(0)
M 117
M 118
M 119
R 50
M 120
v_mov_b32 v54, v52
M 121
M 133
M 132
TX 58,9216
TX 62,23040
M 134
v_permlane32_swap_b32 v52, v54
M 135
M 123
M 122
TY 66,9728
TY 70,23552
M 124
v_add_f32 v52, v52, v54
v_mov_b32 v54, v52
M 125
M 137
ae 44,70,0
M 136
TX 74,10752
TX 78,24576
M 138
s_bfe_u32 s51, s50, 0x10001
WL 4
Q 0
M 139
Q 2
M 127
Q 4
M 126
TY 82,11264
TY 86,25088
M 128
Q 6
M 129
WL 4
Q 8
M 141
Q 10
M 140
TX 90,12288
TX 94,26112
M 142
Q 12
M 143
R 54
M 131
Q 14
M 130
TY 98,12800
TY 102,26624
M 0
R 52
M 1
WL 10
Q 16
M 13
Q 18
M 12
Q 20
M 14
Q 22
M 15
WL 8
Q 24
M 3
Q 26
M 2
Q 28
M 4
Q 30
M 5
WL 6
Q 32
M 17
Q 34
M 16
Q 36
M 18
Q 38
M 19
WL 2
Q 40
M 7
Q 42
M 6
Q 44
M 8
Q 46
M 9
v_permlane16_swap_b32 v52, v54
v_mad_i32_i24 v47, v49, s51, v48
M 21
XL 22,0
M 20
XL 24,768
M 22
XL 26,1536
v_add_f32 v52, v52, v54
M 23
R 58
M 11
XL 28,2304
M 10
WL 2
R 56
M 34
v_permlane16_swap_b32 v52, v53
v_mov_b32 v54, s88
M 35
v_mov_b32 v53, s89
M 32
M 33
M 30
v_permlane16_swap_b32 v54, v53
v_bfe_u32 v53, v0, 2, 6
M 31
M 28
M 29
v_add_u32 v54, v53, v54
M 26
M 27
WV 9
M 24
WL 0
FP
M 25
M 36
M 37
M 38
M 39
M 40
UL 6,14
UH 7,14
M 41
AR 8,36
AR 9,60
M 42
AD 8,6
AD 9,7
M 43
AW 36,8
AW 60,9
M 44
UL 6,15
UH 7,15
M 45
FR
AR 8,84
M 46
AR 9,108
AD 8,6
M 47
AD 9,7
AW 84,8
M 58
AW 108,9
UL 6,16
M 59
UH 7,16
AR 8,37
M 56
AR 9,61
AD 8,6
M 57
AD 9,7
AW 37,8
M 54
AW 61,9
UL 6,17
M 55
WL 0
FQ 4
UH 7,17
M 52
AR 8,85
AR 9,109
M 53
AD 8,6
AD 9,7
M 50
FZ 4
R 72
M 51
mw 24
R 74
M 48
TY 106,520
TY 110,14344
M 49
R 76
M 60
ad 84,624
W 0
M 61
AW 85,8
AW 109,9
M 62
UL 6,18
UH 7,18
M 63
W 1
AR 8,38
M 64
AR 9,62
AD 8,6
M 65
AD 9,7
AW 38,8
M 66
W 2
AW 62,9
M 67
WL 3
AN 104,106
AN 105,107
M 68
WL 2
SH 106
AN 106,106
M 69
mw 27
AN 107,107
AN 108,110
M 70
AN 109,111
SH 110
M 71
R 78
M 82
AN 110,110
AN 111,111
M 83
TY 114,2056
TY 118,15880
M 80
ad 84,630
W 0
M 81
UL 6,19
UH 7,19
M 78
AR 8,86
AR 9,110
M 79
W 1
AD 8,6
M 76
AD 9,7
AW 86,8
M 77
AW 110,9
UL 6,20
)"
R"(M 74
W 2
UH 7,20
M 75
AR 8,39
AR 9,63
M 72
WL 1
AN 112,114
AN 113,115
M 73
R 80
M 84
mw 48
SH 114
AN 114,114
M 85
WL 2
AN 115,115
AN 116,118
M 86
AN 117,119
SH 118
M 87
AN 118,118
AN 119,119
M 88
TY 122,3592
TY 126,17416
M 89
ad 84,672
W 0
M 90
AD 8,6
AD 9,7
M 91
AW 39,8
AW 63,9
M 92
W 1
UL 6,21
M 93
UH 7,21
AR 8,87
M 94
AR 9,111
AD 8,6
M 95
R 82
M 106
W 2
AD 9,7
M 107
WL 3
AN 120,122
AN 121,123
M 104
WL 2
SH 122
AN 122,122
M 105
mw 51
AN 123,123
AN 124,126
M 102
AN 125,127
SH 126
M 103
AN 126,126
AN 127,127
M 100
ad 84,678
W 0
M 101
AW 87,8
AW 111,9
M 98
M 99
W 1
M 96
M 97
R 0
M 108
W 2
M 109
M 110
M 111
bc 49,8,66,60
M 112
mh 18
M 113
ad 66,1536
H 0
M 114
M 115
M 116
DG 1024
M 117
M 118
M 119
WL 2
R 2
M 120
bc 48,8,64,56
M 121
mh 21
s_bitcmp1_b32 s87, 0
s_cselect_b32 exec_lo, 0xffffff, 0
s_mov_b32 exec_hi, 0
global_atomic_add_f32 v54, v52, s[90:91] sc1
s_mov_b64 exec, -1
M 133
ad 64,1536
H 0
M 132
TX 58,0
TX 62,13824
M 134
M 135
H 1
M 123
M 122
TY 66,512
TY 70,14336
M 124
H 2
M 125
M 137
M 136
TX 74,1536
TX 78,15360
M 138
WL 4
Q 0
M 139
Q 2
M 127
Q 4
M 126
TY 82,2048
TY 86,15872
M 128
Q 6
M 129
WL 4
Q 8
M 141
Q 10
M 140
TX 90,3072
TX 94,16896
M 142
Q 12
M 143
R 6
M 131
Q 14
M 130
TY 98,3584
TY 102,17408
M 0
R 4
M 1
WL 10
Q 16
M 13
Q 18
M 12
Q 20
M 14
Q 22
M 15
WL 8
Q 24
M 3
Q 26
M 2
Q 28
M 4
Q 30
M 5
WL 6
Q 32
M 17
Q 34
M 16
Q 36
M 18
Q 38
M 19
WL 2
Q 40
M 7
Q 42
M 6
Q 44
M 8
Q 46
M 9
ae 44,68,0
M 21
s_bitcmp1_b32 s50, 2
s_cselect_b32 s51, 24576, 0
v_add_u32 v47, s51, v3
M 20
XL 30,0
M 22
XL 32,768
M 23
R 10
M 11
XL 34,1536
M 10
WL 2
R 8
M 34
XL 36,2304
M 35
M 32
M 33
M 30
M 31
M 28
M 29
M 26
M 27
WV 10
M 24
WL 0
FP
M 25
M 36
M 37
s_bitcmp1_b32 s87, 1
s_cselect_b32 exec_lo, 0xffffff, 0
s_mov_b32 exec_hi, 0
global_atomic_add_f32 v54, v52, s[90:91] sc1
s_mov_b64 exec, -1
M 38
M 39
M 40
M 41
M 42
UL 6,22
UH 7,22
M 43
AR 8,40
AR 9,64
M 44
AD 8,6
AD 9,7
M 45
FR
AW 40,8
M 46
AW 64,9
UL 6,23
M 47
UH 7,23
AR 8,88
M 58
AR 9,112
AD 8,6
M 59
AD 9,7
AW 88,8
M 56
AW 112,9
UL 6,24
M 57
UH 7,24
AR 8,41
M 54
AR 9,65
AD 8,6
M 55
WL 0
FQ 8
AD 9,7
M 52
AW 41,8
AW 65,9
M 53
UL 6,25
UH 7,25
M 50
FZ 8
R 24
M 51
mw 72
R 26
M 48
TY 106,5128
TY 110,18952
M 49
R 28
M 60
ad 84,720
W 0
M 61
AR 8,89
AR 9,113
M 62
AD 8,6
AD 9,7
M 63
W 1
AW 89,8
M 64
AW 113,9
UL 6,26
M 65
UH 7,26
AR 8,42
M 66
W 2
AR 9,66
M 67
WL 3
AN 104,106
AN 105,107
M 68
WL 2
SH 106
AN 106,106
M 69
mw 75
AN 107,107
AN 108,110
M 70
AN 109,111
SH 110
M 71
R 30
M 82
AN 110,110
AN 111,111
M 83
TY 114,6664
TY 118,20488
M 80
ad 84,726
W 0
M 81
AD 8,6
AD 9,7
M 78
AW 42,8
AW 66,9
M 79
W 1
UL 6,27
M 76
UH 7,27
AR 8,90
M 77
AR 9,114
AD 8,6
M 74
W 2
AD 9,7
M 75
AW 90,8
AW 114,9
M 72
WL 1
AN 112,114
AN 113,115
M 73
R 32
M 84
mw 0
SH 114
AN 114,114
M 85
WL 2
AN 115,115
AN 116,118
M 86
AN 117,119
SH 118
M 87
AN 118,118
AN 119,119
M 88
TY 122,8200
TY 126,22024
M 89
ad 84,768
W 0
M 90
UL 6,28
UH 7,28
M 91
AR 8,43
AR 9,67
M 92
W 1
AD 8,6
M 93
)"
R"(AD 9,7
AW 43,8
M 94
AW 67,9
UL 6,29
M 95
R 34
M 106
W 2
UH 7,29
M 107
WL 3
AN 120,122
AN 121,123
M 104
WL 2
SH 122
AN 122,122
M 105
mw 3
AN 123,123
AN 124,126
M 102
AN 125,127
SH 126
M 103
AN 126,126
AN 127,127
M 100
ad 84,774
W 0
M 101
AR 8,91
AR 9,115
M 98
AD 8,6
AD 9,7
M 99
W 1
AW 91,8
M 96
AW 115,9
M 97
R 48
M 108
W 2
M 109
M 110
M 111
bc 49,9,66,60
M 112
mh 0
M 113
ad 66,1728
H 0
M 114
M 115
M 116
DG 1024
M 117
M 118
M 119
WL 2
R 50
M 120
bc 48,9,64,56
M 121
mh 3
s_bitcmp1_b32 s87, 2
s_cselect_b32 exec_lo, 0xffffff, 0
s_mov_b32 exec_hi, 0
global_atomic_add_f32 v54, v52, s[90:91] sc1
s_mov_b64 exec, -1
M 133
ad 64,1728
H 0
M 132
TX 58,4608
TX 62,18432
M 134
M 135
H 1
M 123
M 122
TY 66,5120
TY 70,18944
M 124
H 2
M 125
M 137
M 136
TX 74,6144
TX 78,19968
M 138
WL 4
Q 0
M 139
Q 2
M 127
Q 4
M 126
TY 82,6656
TY 86,20480
M 128
Q 6
M 129
WL 4
Q 8
M 141
Q 10
M 140
TX 90,7680
TX 94,21504
M 142
Q 12
M 143
R 54
M 131
Q 14
M 130
TY 98,8192
TY 102,22016
M 0
R 52
M 1
WL 10
Q 16
M 13
Q 18
M 12
Q 20
M 14
Q 22
M 15
WL 8
Q 24
M 3
Q 26
M 2
Q 28
M 4
Q 30
M 5
WL 6
Q 32
M 17
Q 34
M 16
Q 36
M 18
Q 38
M 19
WL 2
Q 40
M 7
Q 42
M 6
Q 44
M 8
Q 46
M 9
ae 44,68,0
M 21
s_bfe_u32 s51, s50, 0x10003
v_mad_i32_i24 v47, v49, s51, v48
XL 38,0
M 20
XL 40,768
M 22
XL 42,1536
M 23
R 58
M 11
XL 44,2304
M 10
WL 2
R 56
M 34
M 35
M 32
M 33
M 30
M 31
M 28
M 29
M 26
M 27
WV 10
M 24
WL 0
FP
M 25
M 36
M 37
s_bitcmp1_b32 s87, 3
s_cselect_b32 exec_lo, 0xffffff, 0
s_mov_b32 exec_hi, 0
global_atomic_add_f32 v54, v52, s[90:91] sc1
s_mov_b64 exec, -1
M 38
M 39
M 40
M 41
M 42
M 43
M 44
UL 6,30
UH 7,30
M 45
FR
AR 8,44
M 46
AR 9,68
AD 8,6
M 47
AD 9,7
AW 44,8
M 58
AW 68,9
UL 6,31
M 59
UH 7,31
AR 8,92
M 56
AR 9,116
AD 8,6
M 57
AD 9,7
AW 92,8
M 54
AW 116,9
UL 6,32
M 55
WL 0
FQ 12
UH 7,32
M 52
AR 8,45
AR 9,69
M 53
AD 8,6
AD 9,7
M 50
FZ 12
R 72
M 51
mw 24
R 74
M 48
TY 106,9736
TY 110,23560
M 49
R 76
M 60
ad 84,816
W 0
M 61
AW 45,8
AW 69,9
M 62
UL 6,33
UH 7,33
M 63
W 1
AR 8,93
M 64
AR 9,117
AD 8,6
M 65
AD 9,7
AW 93,8
M 66
W 2
AW 117,9
M 67
WL 3
AN 104,106
AN 105,107
M 68
WL 2
SH 106
AN 106,106
M 69
mw 27
AN 107,107
AN 108,110
M 70
AN 109,111
SH 110
M 71
R 78
M 82
AN 110,110
AN 111,111
M 83
TY 114,11272
TY 118,25096
M 80
ad 84,822
W 0
M 81
UL 6,34
UH 7,34
M 78
AR 8,46
AR 9,70
M 79
W 1
AD 8,6
M 76
AD 9,7
AW 46,8
M 77
AW 70,9
UL 6,35
M 74
W 2
UH 7,35
M 75
AR 8,94
AR 9,118
M 72
WL 1
AN 112,114
AN 113,115
M 73
R 80
M 84
mw 48
SH 114
AN 114,114
M 85
WL 2
AN 115,115
AN 116,118
M 86
AN 117,119
SH 118
M 87
AN 118,118
AN 119,119
M 88
TY 122,12808
TY 126,26632
M 89
ad 84,864
W 0
M 90
AD 8,6
AD 9,7
M 91
AW 94,8
AW 118,9
M 92
W 1
UL 6,36
M 93
UH 7,36
AR 8,47
M 94
AR 9,71
AD 8,6
M 95
R 82
M 106
W 2
AD 9,7
M 107
WL 3
AN 120,122
AN 121,123
M 104
WL 2
SH 122
AN 122,122
M 105
mw 51
AN 123,123
AN 124,126
M 102
AN 125,127
SH 126
M 103
AN 126,126
AN 127,127
M 100
ad 84,870
W 0
M 101
AW 47,8
AW 71,9
M 98
UL 6,37
UH 7,37
M 99
W 1
AR 8,95
M 96
AR 9,119
AD 8,6
)"
R"(M 97
R 0
M 108
W 2
AD 9,7
M 109
AW 95,8
AW 119,9
M 110
M 111
bc 49,10,66,60
M 112
mh 9
M 113
ad 66,1920
H 0
M 114
M 115
M 116
DG 1024
M 117
M 118
M 119
WL 2
R 2
M 120
bc 48,10,64,56
M 121
mh 12
s_bitcmp1_b32 s87, 4
s_cselect_b32 exec_lo, 0xffffff, 0
s_mov_b32 exec_hi, 0
global_atomic_add_f32 v54, v52, s[90:91] sc1
s_mov_b64 exec, -1
M 133
ad 64,1920
H 0
M 132
TX 58,9216
TX 62,23040
M 134
M 135
H 1
M 123
M 122
TY 66,9728
TY 70,23552
M 124
H 2
M 125
M 137
M 136
TX 74,10752
TX 78,24576
M 138
WL 4
Q 0
M 139
Q 2
M 127
Q 4
M 126
TY 82,11264
TY 86,25088
M 128
Q 6
M 129
WL 4
Q 8
M 141
Q 10
M 140
TX 90,12288
TX 94,26112
M 142
Q 12
M 143
R 6
M 131
Q 14
M 130
TY 98,12800
TY 102,26624
M 0
R 4
M 1
WL 10
Q 16
M 13
Q 18
M 12
Q 20
M 14
Q 22
M 15
WL 8
Q 24
M 3
Q 26
M 2
Q 28
M 4
Q 30
M 5
WL 6
Q 32
M 17
Q 34
M 16
Q 36
M 18
Q 38
M 19
WL 2
Q 40
M 7
Q 42
M 6
Q 44
M 8
Q 46
M 9
ae 44,68,96
M 21
s_bitcmp1_b32 s50, 4
s_cselect_b32 s51, 24576, 0
v_add_u32 v47, s51, v3
M 20
XL 14,0
M 22
XL 16,768
M 23
R 10
M 11
XL 18,1536
M 10
WL 2
R 8
M 34
XL 20,2304
M 35
M 32
M 33
M 30
M 31
M 28
M 29
M 26
M 27
WV 10
M 24
WL 0
FP
M 25
M 36
M 37
s_bitcmp1_b32 s87, 5
s_cselect_b32 exec_lo, 0xffffff, 0
s_mov_b32 exec_hi, 0
global_atomic_add_f32 v54, v52, s[90:91] sc1
s_mov_b64 exec, -1
M 38
M 39
M 40
M 41
M 42
M 43
M 44
M 45
FR
M 46
UL 6,38
UH 7,38
M 47
AR 8,48
AR 9,72
M 58
AD 8,6
AD 9,7
M 59
AW 48,8
AW 72,9
M 56
UL 6,39
UH 7,39
M 57
AR 8,96
AR 9,120
M 54
AD 8,6
AD 9,7
M 55
WL 0
FQ 16
AW 96,8
M 52
AW 120,9
UL 6,40
M 53
UH 7,40
AR 8,49
M 50
FZ 16
R 24
M 51
mw 72
R 26
M 48
TY 106,520
TY 110,14344
M 49
R 28
M 60
ad 84,912
W 0
M 61
AR 9,73
AD 8,6
M 62
AD 9,7
AW 49,8
M 63
W 1
AW 73,9
M 64
UL 6,41
UH 7,41
M 65
AR 8,97
AR 9,121
M 66
W 2
AD 8,6
M 67
WL 3
AN 104,106
AN 105,107
M 68
WL 2
SH 106
AN 106,106
M 69
mw 75
AN 107,107
AN 108,110
M 70
AN 109,111
SH 110
M 71
R 30
M 82
AN 110,110
AN 111,111
M 83
TY 114,2056
TY 118,15880
M 80
ad 84,918
W 0
M 81
AD 9,7
AW 97,8
M 78
AW 121,9
UL 6,42
M 79
W 1
UH 7,42
M 76
AR 8,50
AR 9,74
M 77
AD 8,6
AD 9,7
M 74
W 2
AW 50,8
M 75
AW 74,9
UL 6,43
M 72
WL 1
AN 112,114
AN 113,115
M 73
R 32
M 84
mw 0
SH 114
AN 114,114
M 85
WL 2
AN 115,115
AN 116,118
M 86
AN 117,119
SH 118
M 87
AN 118,118
AN 119,119
M 88
TY 122,3592
TY 126,17416
M 89
ad 84,960
W 0
M 90
UH 7,43
AR 8,98
M 91
AR 9,122
AD 8,6
M 92
W 1
AD 9,7
M 93
AW 98,8
AW 122,9
M 94
UL 6,44
UH 7,44
M 95
R 34
M 106
W 2
AR 8,51
M 107
WL 3
AN 120,122
AN 121,123
M 104
WL 2
SH 122
AN 122,122
M 105
mw 3
AN 123,123
AN 124,126
M 102
AN 125,127
SH 126
M 103
AN 126,126
AN 127,127
M 100
ad 84,966
W 0
M 101
AR 9,75
AD 8,6
M 98
AD 9,7
AW 51,8
M 99
W 1
AW 75,9
M 96
UL 6,45
UH 7,45
M 97
R 48
M 108
W 2
AR 8,99
M 109
AR 9,123
AD 8,6
M 110
AD 9,7
AW 99,8
M 111
bc 49,11,66,60
AW 123,9
M 112
mh 18
M 113
ad 66,2112
H 0
M 114
M 115
M 116
DG 1024
M 117
M 118
M 119
WL 2
R 50
M 120
bc 48,11,64,56
M 121
mh 21
s_bitcmp1_b32 s87, 6
s_cselect_b32 exec_lo, 0xffffff, 0
s_mov_b32 exec_hi, 0
global_atomic_add_f32 v54, v52, s[90:91] sc1
s_mov_b64 exec, -1
M 133
ad 64,2112
H 0
M 132
)"
R"(TX 58,0
TX 62,13824
M 134
M 135
H 1
M 123
M 122
TY 66,512
TY 70,14336
M 124
H 2
M 125
M 137
M 136
TX 74,1536
TX 78,15360
PA 12288
DF
M 138
WL 4
Q 0
M 139
Q 2
M 127
Q 4
M 126
TY 82,2048
TY 86,15872
M 128
Q 6
M 129
WL 4
Q 8
M 141
Q 10
M 140
TX 90,3072
TX 94,16896
M 142
Q 12
M 143
R 54
M 131
Q 14
M 130
TY 98,3584
TY 102,17408
M 0
R 52
M 1
WL 10
Q 16
M 13
Q 18
M 12
Q 20
M 14
Q 22
M 15
WL 8
Q 24
M 3
Q 26
M 2
Q 28
M 4
Q 30
M 5
WL 6
Q 32
M 17
Q 34
M 16
Q 36
M 18
Q 38
M 19
WL 2
Q 40
M 7
Q 42
M 6
Q 44
M 8
Q 46
M 9
ae 44,68,96
M 21
s_bfe_u32 s51, s50, 0x10005
v_mad_i32_i24 v47, v49, s51, v48
XL 22,0
M 20
XL 24,768
M 22
XL 26,1536
M 23
R 58
M 11
XL 28,2304
M 10
WL 2
R 56
M 34
M 35
M 32
M 33
M 30
M 31
M 28
M 29
M 26
M 27
WV 11
M 24
WL 0
FP
M 25
M 36
M 37
s_bitcmp1_b32 s87, 7
s_cselect_b32 exec_lo, 0xffffff, 0
s_mov_b32 exec_hi, 0
global_atomic_add_f32 v54, v52, s[90:91] sc1
s_mov_b64 exec, -1
M 38
M 39
M 40
M 41
M 42
M 43
M 44
M 45
FR
M 46
M 47
M 58
UL 6,14
UH 7,14
M 59
AR 8,52
AR 9,76
M 56
AD 8,6
AD 9,7
M 57
AW 52,8
AW 76,9
M 54
UL 6,15
UH 7,15
M 55
WL 0
FQ 20
AR 8,100
M 52
AR 9,124
AD 8,6
M 53
AD 9,7
AW 100,8
M 50
FZ 20
R 72
M 51
mw 24
R 74
M 48
TY 106,5128
TY 110,18952
M 49
R 76
M 60
ad 84,1008
W 0
M 61
AW 124,9
UL 6,16
M 62
UH 7,16
AR 8,53
M 63
W 1
AR 9,77
M 64
AD 8,6
AD 9,7
M 65
AW 53,8
AW 77,9
M 66
W 2
UL 6,17
M 67
WL 3
AN 104,106
AN 105,107
M 68
WL 2
SH 106
AN 106,106
M 69
mw 27
AN 107,107
AN 108,110
M 70
AN 109,111
SH 110
M 71
R 78
M 82
AN 110,110
AN 111,111
M 83
TY 114,6664
TY 118,20488
M 80
ad 84,1014
W 0
M 81
UH 7,17
AR 8,101
M 78
AR 9,125
AD 8,6
M 79
W 1
AD 9,7
M 76
AW 101,8
AW 125,9
M 77
UL 6,18
UH 7,18
M 74
W 2
AR 8,54
M 75
AR 9,78
AD 8,6
M 72
WL 1
AN 112,114
AN 113,115
M 73
R 80
M 84
mw 48
SH 114
AN 114,114
M 85
WL 2
AN 115,115
AN 116,118
M 86
AN 117,119
SH 118
M 87
AN 118,118
AN 119,119
M 88
TY 122,8200
TY 126,22024
M 89
ad 84,1056
W 0
M 90
AD 9,7
AW 54,8
M 91
AW 78,9
UL 6,19
M 92
W 1
UH 7,19
M 93
AR 8,102
AR 9,126
M 94
AD 8,6
AD 9,7
M 95
R 82
M 106
W 2
AW 102,8
M 107
WL 3
AN 120,122
AN 121,123
M 104
WL 2
SH 122
AN 122,122
M 105
mw 51
AN 123,123
AN 124,126
M 102
AN 125,127
SH 126
M 103
AN 126,126
AN 127,127
M 100
ad 84,1062
W 0
M 101
AW 126,9
UL 6,20
M 98
UH 7,20
AR 8,55
M 99
W 1
AR 9,79
M 96
AD 8,6
AD 9,7
M 97
R 0
M 108
W 2
AW 55,8
M 109
AW 79,9
UL 6,21
M 110
UH 7,21
AR 8,103
M 111
bc 49,12,66,60
AR 9,127
M 112
mh 0
AD 8,6
AD 9,7
M 113
ad 66,2304
H 0
M 114
AW 103,8
AW 127,9
M 115
M 116
DG 1024
M 117
M 118
M 119
WL 2
R 2
M 120
bc 48,12,64,56
M 121
mh 3
M 133
ad 64,2304
H 0
M 132
TX 58,4608
TX 62,18432
M 134
M 135
H 1
M 123
M 122
TY 66,5120
TY 70,18944
M 124
H 2
M 125
M 137
M 136
TX 74,6144
TX 78,19968
M 138
WL 4
Q 0
M 139
Q 2
M 127
Q 4
M 126
TY 82,6656
TY 86,20480
M 128
Q 6
M 129
WL 4
Q 8
M 141
Q 10
M 140
TX 90,7680
TX 94,21504
M 142
Q 12
M 143
R 6
M 131
Q 14
)"
R"(M 130
TY 98,8192
TY 102,22016
M 0
R 4
M 1
WL 10
Q 16
M 13
Q 18
M 12
Q 20
M 14
Q 22
M 15
WL 8
Q 24
M 3
Q 26
M 2
Q 28
M 4
Q 30
M 5
WL 6
Q 32
M 17
Q 34
M 16
Q 36
M 18
Q 38
M 19
WL 2
Q 40
M 7
Q 42
M 6
Q 44
M 8
Q 46
M 9
M 21
M 20
M 22
M 23
R 10
M 11
M 10
WL 2
R 8
M 34
M 35
M 32
M 33
M 30
M 31
M 28
M 29
M 26
ae 44,70,0
M 27
WV 5
M 24
WL 0
FP
M 25
M 36
s_bitcmp1_b32 s50, 0
s_cselect_b32 s51, 24576, 0
v_add_u32 v47, s51, v3
M 37
XL 30,128
M 38
XL 32,896
M 39
M 40
M 41
M 42
M 43
M 44
XL 34,1664
M 45
FR
M 46
M 47
M 58
M 59
M 56
XL 36,2432
UL 6,22
M 57
UH 7,22
AR 8,56
M 54
AR 9,80
AD 8,6
M 55
WL 0
FQ 24
AD 9,7
M 52
AW 56,8
AW 80,9
M 53
UL 6,23
UH 7,23
M 50
FZ 24
R 24
M 51
mw 72
R 26
M 48
TY 106,9736
TY 110,23560
M 49
R 28
M 60
ad 84,1104
W 0
M 61
AR 8,104
AR 9,128
M 62
AD 8,6
AD 9,7
M 63
W 1
AW 104,8
M 64
AW 128,9
UL 6,24
M 65
UH 7,24
AR 8,57
M 66
W 2
AR 9,81
M 67
WL 3
AN 104,106
AN 105,107
M 68
WL 2
SH 106
AN 106,106
M 69
mw 75
AN 107,107
AN 108,110
M 70
AN 109,111
SH 110
M 71
R 30
M 82
AN 110,110
AN 111,111
M 83
TY 114,11272
TY 118,25096
M 80
ad 84,1110
W 0
M 81
AD 8,6
AD 9,7
M 78
AW 57,8
AW 81,9
M 79
W 1
UL 6,25
M 76
UH 7,25
AR 8,105
M 77
AR 9,129
AD 8,6
M 74
W 2
AD 9,7
M 75
AW 105,8
AW 129,9
M 72
WL 1
AN 112,114
AN 113,115
M 73
R 32
M 84
mw 0
SH 114
AN 114,114
M 85
WL 2
AN 115,115
AN 116,118
M 86
AN 117,119
SH 118
M 87
AN 118,118
AN 119,119
M 88
TY 122,12808
TY 126,26632
M 89
ad 84,1152
W 0
M 90
UL 6,26
UH 7,26
M 91
AR 8,58
AR 9,82
M 92
W 1
AD 8,6
M 93
AD 9,7
AW 58,8
M 94
AW 82,9
UL 6,27
M 95
R 34
M 106
W 2
UH 7,27
M 107
WL 3
AN 120,122
AN 121,123
M 104
WL 2
SH 122
AN 122,122
M 105
mw 3
AN 123,123
AN 124,126
M 102
AN 125,127
SH 126
M 103
AN 126,126
AN 127,127
M 100
ad 84,1158
W 0
M 101
AR 8,106
AR 9,130
M 98
AD 8,6
AD 9,7
M 99
W 1
AW 106,8
M 96
AW 130,9
UL 6,28
M 97
R 48
M 108
W 2
UH 7,28
M 109
AR 8,59
AR 9,83
M 110
AD 8,6
AD 9,7
M 111
bc 49,13,66,60
AW 59,8
M 112
mh 9
AW 83,9
UL 6,29
M 113
ad 66,2496
H 0
M 114
UH 7,29
AR 8,107
M 115
AR 9,131
AD 8,6
M 116
DG 1024
M 117
AD 9,7
AW 107,8
M 118
AW 131,9
M 119
WL 1
R 50
M 120
bc 48,13,64,56
M 121
mh 12
M 133
ad 64,2496
H 0
M 132
TX 58,9216
TX 62,23040
M 134
M 135
H 1
M 123
M 122
TY 66,9728
TY 70,23552
M 124
H 2
M 125
M 137
M 136
TX 74,10752
TX 78,24576
PA 12288
DF
M 138
WL 4
Q 0
M 139
Q 2
M 127
Q 4
M 126
TY 82,11264
TY 86,25088
M 128
Q 6
M 129
WL 4
Q 8
M 141
Q 10
M 140
TX 90,12288
TX 94,26112
M 142
Q 12
M 143
R 54
M 131
Q 14
M 130
TY 98,12800
TY 102,26624
M 0
R 52
M 1
WL 10
Q 16
M 13
Q 18
M 12
Q 20
M 14
Q 22
M 15
WL 8
Q 24
M 3
Q 26
M 2
Q 28
M 4
Q 30
M 5
WL 6
Q 32
M 17
Q 34
M 16
Q 36
M 18
Q 38
M 19
WL 2
Q 40
M 7
Q 42
M 6
Q 44
M 8
Q 46
M 9
M 21
M 20
M 22
M 23
)"
R"(R 58
M 11
M 10
WL 2
R 56
M 34
M 35
M 32
M 33
M 30
M 31
M 28
M 29
M 26
M 27
WV 6
M 24
WL 0
FP
M 25
M 36
ae 44,70,0
M 37
s_bfe_u32 s51, s50, 0x10001
v_mad_i32_i24 v47, v49, s51, v48
XL 38,128
M 38
M 39
M 40
XL 40,896
M 41
M 42
M 43
M 44
M 45
FR
M 46
XL 42,1664
M 47
M 58
M 59
M 56
M 57
M 54
XL 44,2432
M 55
WL 0
FQ 28
M 52
M 53
M 50
FZ 28
R 72
M 51
R 74
M 48
TY 106,520
TY 110,14344
M 49
R 76
M 60
mw 24
v_mov_b32 v54, 2
s_mov_b64 exec, 1
FS
s_mov_b64 exec, -1
M 61
ad 84,1200
W 0
M 62
M 63
M 64
W 1
M 65
M 66
M 67
WL 3
AN 104,106
AN 105,107
M 68
SH 106
AN 106,106
M 69
WL 2
AN 107,107
AN 108,110
M 70
AN 109,111
SH 110
M 71
R 78
M 82
AN 110,110
AN 111,111
M 83
TY 114,2056
TY 118,15880
M 80
W 2
M 81
M 78
M 79
mw 27
M 76
ad 84,1206
W 0
M 77
M 74
M 75
W 1
M 72
WL 1
AN 112,114
AN 113,115
M 73
R 80
M 84
SH 114
AN 114,114
M 85
WL 2
AN 115,115
AN 116,118
M 86
AN 117,119
SH 118
M 87
AN 118,118
AN 119,119
M 88
TY 122,3592
TY 126,17416
M 89
W 2
UL 6,30
M 90
UH 7,30
AR 8,132
M 91
AR 9,156
AD 8,6
M 92
mw 48
AD 9,7
AW 132,8
M 93
ad 84,1248
W 0
M 94
AW 156,9
UL 6,31
M 95
R 82
M 106
W 1
UH 7,31
M 107
WL 3
AN 120,122
AN 121,123
M 104
SH 122
AN 122,122
M 105
WL 2
AN 123,123
AN 124,126
M 102
AN 125,127
SH 126
M 103
AN 126,126
AN 127,127
M 100
W 2
AR 8,180
M 101
AD 8,6
AD 136,7
M 98
AW 180,8
UL 6,32
M 99
mw 51
UH 7,32
AR 8,133
M 96
ad 84,1254
W 0
M 97
R 0
M 108
AR 9,157
AD 8,6
M 109
W 1
AD 9,7
M 110
AW 133,8
AW 157,9
M 111
UL 6,33
UH 7,33
M 112
W 2
AR 8,181
M 113
AD 8,6
AD 137,7
M 114
AW 181,8
UL 6,34
M 115
bc 49,14,66,60
UH 7,34
M 116
mh 18
AR 8,134
AR 9,158
M 117
ad 66,2688
H 0
M 118
AD 8,6
AD 9,7
M 119
WL 0
R 2
M 120
DG 1024
M 121
AW 134,8
AW 158,9
M 133
UL 6,35
UH 7,35
M 132
TX 58,0
TX 62,13824
M 134
bc 48,14,64,56
AR 8,182
M 135
mh 21
AD 8,6
AD 138,7
M 123
ad 64,2688
H 0
M 122
TY 66,512
TY 70,14336
M 124
AW 182,8
UL 6,36
M 125
H 1
UH 7,36
M 137
AR 8,135
AR 9,159
M 136
TX 74,1536
TX 78,15360
M 138
WL 4
Q 0
M 139
Q 2
M 127
Q 4
M 126
TY 82,2048
TY 86,15872
M 128
Q 6
M 129
WL 4
Q 8
M 141
Q 10
M 140
TX 90,3072
TX 94,16896
M 142
Q 12
M 143
R 6
M 131
Q 14
M 130
TY 98,3584
TY 102,17408
M 0
R 4
M 1
WL 10
Q 16
M 13
Q 18
M 12
Q 20
M 14
Q 22
M 15
WL 8
Q 24
M 3
Q 26
M 2
Q 28
M 4
Q 30
M 5
WL 6
Q 32
M 17
Q 34
M 16
Q 36
M 18
Q 38
M 19
WL 2
Q 40
M 7
Q 42
M 6
Q 44
M 8
Q 46
M 9
H 2
AD 8,6
M 21
AD 9,7
AW 135,8
M 20
AW 159,9
UL 6,37
M 22
UH 7,37
AR 8,183
M 23
R 10
M 11
AD 8,6
AD 139,7
M 10
WL 2
R 8
M 34
AW 183,8
M 35
M 32
M 33
M 30
M 31
M 28
M 29
M 26
M 27
WV 5
M 24
WL 0
FP
M 25
M 36
ae 44,68,0
M 37
s_bitcmp1_b32 s50, 2
s_cselect_b32 s51, 24576, 0
v_add_u32 v47, s51, v3
M 38
XL 14,128
M 39
M 40
M 41
M 42
XL 16,896
M 43
M 44
M 45
FR
M 46
M 47
M 58
XL 18,1664
M 59
M 56
M 57
M 54
M 55
WL 0
FQ 32
M 52
XL 20,2432
M 53
M 50
FZ 32
)"
R"(R 24
M 51
mw 72
R 26
M 48
TY 106,5128
TY 110,18952
M 49
R 28
M 60
ad 84,1296
W 0
M 61
M 62
M 63
W 1
M 64
M 65
M 66
W 2
M 67
WL 3
AN 104,106
AN 105,107
M 68
WL 2
SH 106
AN 106,106
M 69
mw 75
AN 107,107
AN 108,110
M 70
AN 109,111
SH 110
M 71
R 30
M 82
AN 110,110
AN 111,111
M 83
TY 114,6664
TY 118,20488
M 80
ad 84,1302
W 0
M 81
M 78
M 79
W 1
M 76
M 77
M 74
W 2
M 75
M 72
WL 1
AN 112,114
AN 113,115
M 73
R 32
M 84
mw 0
SH 114
AN 114,114
M 85
WL 2
AN 115,115
AN 116,118
M 86
AN 117,119
SH 118
M 87
AN 118,118
AN 119,119
M 88
TY 122,8200
TY 126,22024
M 89
ad 84,1344
W 0
M 90
UL 6,38
UH 7,38
M 91
AR 8,136
AR 9,160
M 92
W 1
AD 8,6
M 93
AD 9,7
AW 136,8
M 94
AW 160,9
UL 6,39
M 95
R 34
M 106
W 2
UH 7,39
M 107
WL 3
AN 120,122
AN 121,123
M 104
WL 2
SH 122
AN 122,122
M 105
mw 3
AN 123,123
AN 124,126
M 102
AN 125,127
SH 126
M 103
AN 126,126
AN 127,127
M 100
ad 84,1350
W 0
M 101
AR 8,184
AD 8,6
M 98
AD 140,7
AW 184,8
M 99
W 1
UL 6,40
M 96
UH 7,40
AR 8,137
M 97
R 48
M 108
W 2
AR 9,161
M 109
AD 8,6
AD 9,7
M 110
AW 137,8
AW 161,9
M 111
bc 49,15,66,60
UL 6,41
M 112
mh 0
UH 7,41
AR 8,185
M 113
ad 66,2880
H 0
M 114
AD 8,6
AD 141,7
M 115
AW 185,8
UL 6,42
M 116
DG 1024
M 117
UH 7,42
AR 8,138
M 118
AR 9,162
AD 8,6
M 119
WL 0
R 50
M 120
bc 48,15,64,56
AD 9,7
M 121
mh 3
AW 138,8
AW 162,9
M 133
ad 64,2880
H 0
M 132
TX 58,4608
TX 62,18432
M 134
UL 6,43
UH 7,43
M 135
H 1
AR 8,186
M 123
AD 8,6
AD 142,7
M 122
TY 66,5120
TY 70,18944
M 124
H 2
AW 186,8
M 125
UL 6,44
UH 7,44
M 137
AR 8,139
AR 9,163
M 136
TX 74,6144
TX 78,19968
PA 12288
DF
M 138
WL 4
Q 0
M 139
Q 2
M 127
Q 4
M 126
TY 82,6656
TY 86,20480
M 128
Q 6
M 129
WL 4
Q 8
M 141
Q 10
M 140
TX 90,7680
TX 94,21504
M 142
Q 12
M 143
R 54
M 131
Q 14
M 130
TY 98,8192
TY 102,22016
M 0
R 52
M 1
WL 10
Q 16
M 13
Q 18
M 12
Q 20
M 14
Q 22
M 15
WL 8
Q 24
M 3
Q 26
M 2
Q 28
M 4
Q 30
M 5
WL 6
Q 32
M 17
Q 34
M 16
Q 36
M 18
Q 38
M 19
WL 2
Q 40
M 7
Q 42
M 6
Q 44
M 8
Q 46
M 9
AD 8,6
AD 9,7
M 21
AW 139,8
AW 163,9
M 20
UL 6,45
UH 7,45
M 22
AR 8,187
AD 8,6
M 23
R 58
M 11
AD 143,7
AW 187,8
M 10
WL 2
R 56
M 34
M 35
M 32
M 33
M 30
M 31
M 28
M 29
M 26
M 27
WV 6
M 24
WL 0
FP
M 25
M 36
M 37
M 38
ae 44,68,0
M 39
s_bfe_u32 s51, s50, 0x10003
v_mad_i32_i24 v47, v49, s51, v48
XL 22,128
M 40
M 41
M 42
M 43
M 44
XL 24,896
M 45
FR
M 46
M 47
M 58
M 59
M 56
XL 26,1664
M 57
M 54
M 55
WL 0
FQ 36
M 52
M 53
M 50
FZ 36
R 72
M 51
mw 24
R 74
M 48
TY 106,9736
TY 110,23560
M 49
R 76
M 60
ad 84,1392
W 0
M 61
XL 28,2432
M 62
M 63
W 1
M 64
M 65
M 66
W 2
M 67
WL 3
AN 104,106
AN 105,107
M 68
WL 2
SH 106
AN 106,106
M 69
mw 27
AN 107,107
AN 108,110
M 70
AN 109,111
SH 110
M 71
R 78
M 82
AN 110,110
AN 111,111
M 83
TY 114,11272
TY 118,25096
M 80
ad 84,1398
W 0
M 81
M 78
M 79
W 1
M 76
M 77
M 74
W 2
M 75
)"
R"(M 72
WL 1
AN 112,114
AN 113,115
M 73
R 80
M 84
mw 48
SH 114
AN 114,114
M 85
WL 2
AN 115,115
AN 116,118
M 86
AN 117,119
SH 118
M 87
AN 118,118
AN 119,119
M 88
TY 122,12808
TY 126,26632
M 89
ad 84,1440
W 0
M 90
M 91
M 92
W 1
UL 6,14
M 93
UH 7,14
AR 8,140
M 94
AR 9,164
AD 8,6
M 95
R 82
M 106
W 2
AD 9,7
M 107
WL 3
AN 120,122
AN 121,123
M 104
WL 2
SH 122
AN 122,122
M 105
mw 51
AN 123,123
AN 124,126
M 102
AN 125,127
SH 126
M 103
AN 126,126
AN 127,127
M 100
ad 84,1446
W 0
M 101
AW 140,8
AW 164,9
M 98
UL 6,15
UH 7,15
M 99
W 1
AR 8,188
M 96
AD 8,6
AD 144,7
M 97
R 0
M 108
W 2
AW 188,8
M 109
UL 6,16
UH 7,16
M 110
AR 8,141
AR 9,165
M 111
bc 49,16,66,60
AD 8,6
M 112
mh 9
AD 9,7
AW 141,8
M 113
ad 66,3072
H 0
M 114
AW 165,9
UL 6,17
M 115
UH 7,17
AR 8,189
M 116
DG 1024
M 117
AD 8,6
AD 145,7
M 118
AW 189,8
UL 6,18
M 119
WL 0
R 2
M 120
bc 48,16,64,56
UH 7,18
M 121
mh 12
AR 8,142
AR 9,166
M 133
ad 64,3072
H 0
M 132
TX 58,9216
TX 62,23040
M 134
AD 8,6
AD 9,7
M 135
H 1
AW 142,8
M 123
AW 166,9
UL 6,19
M 122
TY 66,9728
TY 70,23552
M 124
H 2
UH 7,19
M 125
AR 8,190
AD 8,6
M 137
AD 146,7
AW 190,8
M 136
TX 74,10752
TX 78,24576
M 138
WL 4
Q 0
M 139
Q 2
M 127
Q 4
M 126
TY 82,11264
TY 86,25088
M 128
Q 6
M 129
WL 4
Q 8
M 141
Q 10
M 140
TX 90,12288
TX 94,26112
M 142
Q 12
M 143
R 6
M 131
Q 14
M 130
TY 98,12800
TY 102,26624
M 0
R 4
M 1
WL 10
Q 16
M 13
Q 18
M 12
Q 20
M 14
Q 22
M 15
WL 8
Q 24
M 3
Q 26
M 2
Q 28
M 4
Q 30
M 5
WL 6
Q 32
M 17
Q 34
M 16
Q 36
M 18
Q 38
M 19
WL 2
Q 40
M 7
Q 42
M 6
Q 44
M 8
Q 46
M 9
UL 6,20
UH 7,20
M 21
AR 8,143
AR 9,167
M 20
AD 8,6
AD 9,7
M 22
AW 143,8
AW 167,9
M 23
R 10
M 11
UL 6,21
UH 7,21
M 10
WL 2
R 8
M 34
AR 8,191
AD 8,6
M 35
AD 147,7
AW 191,8
M 32
M 33
M 30
M 31
M 28
M 29
M 26
M 27
WV 5
M 24
WL 0
FP
M 25
M 36
M 37
M 38
M 39
M 40
ae 44,68,96
M 41
s_bitcmp1_b32 s50, 4
s_cselect_b32 s51, 24576, 0
v_add_u32 v47, s51, v3
M 42
XL 30,128
M 43
M 44
M 45
FR
M 46
XL 32,896
M 47
M 58
M 59
M 56
M 57
M 54
XL 34,1664
M 55
WL 0
FQ 40
M 52
M 53
M 50
FZ 40
R 24
M 51
mw 72
R 26
M 48
TY 106,520
TY 110,14344
M 49
R 28
M 60
ad 84,1488
W 0
M 61
XL 36,2432
v_lshlrev_b32 v55, 4, v0
M 62
FL
M 63
W 1
M 64
M 65
M 66
W 2
M 67
WL 3
AN 104,106
AN 105,107
M 68
WL 2
SH 106
AN 106,106
M 69
mw 75
AN 107,107
AN 108,110
M 70
AN 109,111
SH 110
M 71
R 30
M 82
AN 110,110
AN 111,111
M 83
TY 114,2056
TY 118,15880
M 80
ad 84,1494
W 0
M 81
M 78
M 79
W 1
M 76
M 77
M 74
W 2
M 75
M 72
WL 1
AN 112,114
AN 113,115
M 73
R 32
M 84
mw 0
SH 114
AN 114,114
M 85
WL 2
AN 115,115
AN 116,118
M 86
AN 117,119
SH 118
M 87
AN 118,118
AN 119,119
M 88
TY 122,3592
TY 126,17416
M 89
ad 84,1536
W 0
M 90
M 91
M 92
W 1
M 93
M 94
UL 6,22
UH 7,22
M 95
R 34
M 106
W 2
AR 8,144
M 107
WL 3
AN 120,122
AN 121,123
M 104
WL 2
SH 122
AN 122,122
M 105
mw 3
AN 123,123
AN 124,126
M 102
AN 125,127
SH 126
)"
R"(M 103
AN 126,126
AN 127,127
M 100
ad 84,1542
W 0
M 101
AR 9,168
AD 8,6
M 98
AD 9,7
AW 144,8
M 99
W 1
AW 168,9
M 96
UL 6,23
UH 7,23
M 97
R 48
M 108
W 2
AR 8,192
M 109
AD 8,6
AD 148,7
M 110
AW 192,8
UL 6,24
M 111
bc 49,17,66,60
UH 7,24
M 112
mh 18
AR 8,145
AR 9,169
M 113
ad 66,3264
H 0
M 114
AD 8,6
AD 9,7
M 115
AW 145,8
AW 169,9
M 116
DG 1024
M 117
UL 6,25
UH 7,25
M 118
AR 8,193
AD 8,6
M 119
WL 0
R 50
M 120
bc 48,17,64,56
AD 149,7
M 121
mh 21
AW 193,8
UL 6,26
M 133
ad 64,3264
H 0
M 132
TX 58,0
TX 62,13824
M 134
UH 7,26
AR 8,146
M 135
H 1
AR 9,170
M 123
AD 8,6
AD 9,7
M 122
TY 66,512
TY 70,14336
M 124
H 2
AW 146,8
M 125
AW 170,9
UL 6,27
M 137
UH 7,27
AR 8,194
M 136
TX 74,1536
TX 78,15360
PA 12288
DF
M 138
WL 4
Q 0
M 139
Q 2
M 127
Q 4
M 126
TY 82,2048
TY 86,15872
M 128
Q 6
M 129
WL 4
Q 8
M 141
Q 10
M 140
TX 90,3072
TX 94,16896
M 142
Q 12
M 143
R 54
M 131
Q 14
M 130
TY 98,3584
TY 102,17408
M 0
R 52
M 1
WL 10
Q 16
M 13
Q 18
M 12
Q 20
M 14
Q 22
M 15
WL 8
Q 24
M 3
Q 26
M 2
Q 28
M 4
Q 30
M 5
WL 6
Q 32
M 17
Q 34
M 16
Q 36
M 18
Q 38
M 19
WL 2
Q 40
M 7
Q 42
M 6
Q 44
M 8
Q 46
M 9
AD 8,6
AD 150,7
M 21
AW 194,8
UL 6,28
M 20
UH 7,28
AR 8,147
M 22
AR 9,171
AD 8,6
M 23
R 58
M 11
AD 9,7
AW 147,8
M 10
WL 2
R 56
M 34
AW 171,9
UL 6,29
M 35
UH 7,29
AR 8,195
M 32
AD 8,6
AD 151,7
M 33
AW 195,8
M 30
M 31
M 28
M 29
M 26
M 27
WV 6
M 24
WL 0
FP
M 25
M 36
M 37
M 38
M 39
M 40
M 41
M 42
ae 44,68,96
M 43
s_bfe_u32 s51, s50, 0x10005
v_mad_i32_i24 v47, v49, s51, v48
XL 38,128
M 44
M 45
FR
M 46
M 47
M 58
XL 40,896
M 59
M 56
M 57
M 54
M 55
WL 0
FQ 44
M 52
XL 42,1664
M 53
M 50
FZ 44
R 72
M 51
mw 24
R 74
M 48
TY 106,5128
TY 110,18952
M 49
R 76
M 60
ad 84,1584
W 0
M 61
XL 44,2432
M 62
M 63
W 1
M 64
M 65
M 66
W 2
M 67
WL 3
AN 104,106
AN 105,107
M 68
WL 2
SH 106
AN 106,106
M 69
mw 27
AN 107,107
AN 108,110
M 70
AN 109,111
SH 110
M 71
R 78
M 82
AN 110,110
AN 111,111
M 83
TY 114,6664
TY 118,20488
M 80
ad 84,1590
W 0
M 81
M 78
M 79
W 1
M 76
M 77
M 74
W 2
M 75
M 72
WL 1
AN 112,114
AN 113,115
M 73
R 80
M 84
mw 48
SH 114
AN 114,114
M 85
WL 2
AN 115,115
AN 116,118
M 86
AN 117,119
SH 118
M 87
AN 118,118
AN 119,119
M 88
TY 122,8200
TY 126,22024
M 89
ad 84,1632
W 0
M 90
M 91
M 92
W 1
M 93
M 94
M 95
R 82
M 106
W 2
UL 6,30
M 107
WL 3
AN 120,122
AN 121,123
M 104
WL 2
SH 122
AN 122,122
M 105
mw 51
AN 123,123
AN 124,126
M 102
AN 125,127
SH 126
M 103
AN 126,126
AN 127,127
M 100
ad 84,1638
W 0
M 101
UH 7,30
AR 8,148
M 98
AR 9,172
AD 8,6
M 99
W 1
AD 9,7
M 96
AW 148,8
AW 172,9
M 97
R 0
M 108
W 2
UL 6,31
M 109
UH 7,31
AR 8,196
M 110
AD 8,6
AD 152,7
M 111
bc 49,18,66,60
AW 196,8
M 112
mh 0
UL 6,32
UH 7,32
M 113
ad 66,3456
H 0
M 114
AR 8,149
AR 9,173
M 115
AD 8,6
AD 9,7
M 116
DG 1024
M 117
AW 149,8
AW 173,9
M 118
UL 6,33
UH 7,33
M 119
WL 0
R 2
M 120
)"
R"(bc 48,18,64,56
AR 8,197
M 121
mh 3
AD 8,6
AD 153,7
M 133
ad 64,3456
H 0
M 132
TX 58,4608
TX 62,18432
M 134
AW 197,8
UL 6,34
M 135
H 1
UH 7,34
M 123
AR 8,150
AR 9,174
M 122
TY 66,5120
TY 70,18944
M 124
H 2
AD 8,6
M 125
AD 9,7
AW 150,8
M 137
AW 174,9
UL 6,35
M 136
TX 74,6144
TX 78,19968
M 138
WL 4
Q 0
M 139
Q 2
M 127
Q 4
M 126
TY 82,6656
TY 86,20480
M 128
Q 6
M 129
WL 4
Q 8
M 141
Q 10
M 140
TX 90,7680
TX 94,21504
M 142
Q 12
M 143
R 6
M 131
Q 14
M 130
TY 98,8192
TY 102,22016
M 0
R 4
M 1
WL 10
Q 16
M 13
Q 18
M 12
Q 20
M 14
Q 22
M 15
WL 8
Q 24
M 3
Q 26
M 2
Q 28
M 4
Q 30
M 5
WL 6
Q 32
M 17
Q 34
M 16
Q 36
M 18
Q 38
M 19
WL 2
Q 40
M 7
Q 42
M 6
Q 44
M 8
Q 46
M 9
UH 7,35
AR 8,198
M 21
AD 8,6
AD 154,7
M 20
AW 198,8
UL 6,36
M 22
UH 7,36
AR 8,151
M 23
R 10
M 11
AR 9,175
AD 8,6
M 10
WL 2
R 8
M 34
AD 9,7
AW 151,8
M 35
AW 175,9
UL 6,37
M 32
UH 7,37
AR 8,199
M 33
v_cmp_ne_u32 vcc, 2, v54
AD 8,6
M 30
AD 155,7
AW 199,8
M 31
M 28
FC
M 29
M 26
M 27
WV 5
M 24
WL 0
FP
M 25
M 36
M 37
M 38
M 39
M 40
M 41
M 42
M 43
M 44
M 45
FR
M 46
M 47
M 58
M 59
M 56
M 57
M 54
M 55
WL 0
FQ 48
M 52
M 53
M 50
FZ 48
R 24
M 51
mw 72
R 26
M 48
TY 106,9736
TY 110,23560
M 49
R 28
M 60
ad 84,1680
W 0
M 61
M 62
M 63
W 1
M 64
M 65
M 66
W 2
M 67
WL 3
AN 104,106
AN 105,107
M 68
WL 2
SH 106
AN 106,106
M 69
mw 75
AN 107,107
AN 108,110
M 70
AN 109,111
SH 110
M 71
R 30
M 82
AN 110,110
AN 111,111
M 83
TY 114,11272
TY 118,25096
M 80
ad 84,1686
W 0
M 81
ae 44,70,0
M 78
s_bitcmp1_b32 s50, 0
s_cselect_b32 s51, 24576, 0
v_add_u32 v47, s51, v3
M 79
W 1
XL 14,256
M 76
XL 16,1024
M 77
M 74
W 2
M 75
M 72
WL 1
AN 112,114
AN 113,115
M 73
R 32
M 84
mw 0
SH 114
AN 114,114
M 85
WL 2
AN 115,115
AN 116,118
M 86
AN 117,119
SH 118
M 87
AN 118,118
AN 119,119
M 88
TY 122,12808
TY 126,26632
M 89
ad 84,1728
W 0
M 90
XL 18,1792
M 91
XL 20,2560
M 92
W 1
M 93
M 94
M 95
R 34
M 106
W 2
M 107
WL 3
AN 120,122
AN 121,123
M 104
WL 2
SH 122
AN 122,122
M 105
mw 3
AN 123,123
AN 124,126
M 102
AN 125,127
SH 126
M 103
AN 126,126
AN 127,127
M 100
ad 84,1734
W 0
M 101
UL 6,38
UH 7,38
M 98
AR 8,152
AR 9,176
M 99
W 1
AD 8,6
M 96
AD 9,7
AW 152,8
M 97
R 48
M 108
W 2
AW 176,9
M 109
UL 6,39
UH 7,39
M 110
AD 132,6
AD 156,7
M 111
bc 49,19,66,60
UL 6,40
M 112
mh 9
UH 7,40
AR 8,153
M 113
ad 66,3648
H 0
M 114
AR 9,177
AD 8,6
M 115
AD 9,7
AW 153,8
M 116
DG 1024
M 117
AW 177,9
UL 6,41
M 118
UH 7,41
AD 133,6
M 119
WL 0
R 50
M 120
bc 48,19,64,56
AD 157,7
M 121
mh 12
UL 6,42
UH 7,42
M 133
ad 64,3648
H 0
M 132
TX 58,9216
TX 62,23040
M 134
AR 8,154
AR 9,178
M 135
H 1
AD 8,6
M 123
AD 9,7
AW 154,8
M 122
TY 66,9728
TY 70,23552
M 124
H 2
AW 178,9
M 125
UL 6,43
UH 7,43
M 137
AD 134,6
AD 158,7
M 136
TX 74,10752
TX 78,24576
v_min_u32 v52, 752, v0
v_lshrrev_b32 v52, 1, v52
v_add_u32 v53, 0x1800, v52
global_load_dwordx2 a[200:201], v52, s[94:95] offset:-2048 sc1
global_load_dwordx2 a[202:203], v52, s[94:95] offset:-512 sc1
global_load_dwordx2 a[204:205], v52, s[94:95] offset:1024 sc1
global_load_dwordx2 a[206:207], v52, s[94:95] offset:2560 sc1
global_load_dwordx2 a[208:209], v53, s[94:95] offset:-2048 sc1
global_load_dwordx2 a[210:211], v53, s[94:95] offset:-512 sc1
global_load_dwordx2 a[212:213], v53, s[94:95] offset:1024 sc1
global_load_dwordx2 a[214:215], v53, s[94:95] offset:2560 sc1
PA 12288
DF
M 138
WL 4
Q 0
M 139
Q 2
M 127
)"
R"(Q 4
M 126
TY 82,11264
TY 86,25088
M 128
Q 6
M 129
WL 4
Q 8
M 141
Q 10
M 140
TX 90,12288
TX 94,26112
M 142
Q 12
M 143
R 54
M 131
Q 14
M 130
TY 98,12800
TY 102,26624
M 0
R 52
M 1
WL 10
Q 16
M 13
Q 18
M 12
Q 20
M 14
Q 22
M 15
WL 8
Q 24
M 3
Q 26
M 2
Q 28
M 4
Q 30
M 5
WL 6
Q 32
M 17
Q 34
M 16
Q 36
M 18
Q 38
M 19
WL 2
Q 40
M 7
Q 42
M 6
Q 44
M 8
Q 46
M 9
UL 6,44
UH 7,44
M 21
AR 8,155
AR 9,179
M 20
AD 8,6
AD 9,7
M 22
AW 155,8
AW 179,9
M 23
R 58
M 11
UL 6,45
UH 7,45
M 10
WL 2
R 56
M 34
AD 135,6
AD 159,7
M 35
M 32
M 33
M 30
M 31
M 28
M 29
M 26
M 27
WV 14
M 24
WL 0
FP
M 25
M 36
M 37
M 38
M 39
M 40
M 41
M 42
M 43
M 44
M 45
FR
M 46
M 47
M 58
M 59
M 56
M 57
M 54
M 55
WL 0
FQ 52
M 52
M 53
M 50
FZ 52
R 72
M 51
mw 24
R 74
M 48
TY 106,520
TY 110,14344
M 49
R 76
M 60
ad 84,1776
W 0
M 61
M 62
M 63
W 1
M 64
M 65
M 66
W 2
M 67
WL 3
AN 104,106
AN 105,107
M 68
WL 2
SH 106
AN 106,106
M 69
mw 27
AN 107,107
AN 108,110
M 70
AN 109,111
SH 110
M 71
R 78
M 82
AN 110,110
AN 111,111
M 83
TY 114,2056
TY 118,15880
M 80
ad 84,1782
W 0
M 81
ae 44,70,0
M 78
s_bfe_u32 s51, s50, 0x10001
v_mad_i32_i24 v47, v49, s51, v48
XL 22,256
M 79
W 1
M 76
M 77
M 74
W 2
XL 24,1024
M 75
M 72
WL 1
AN 112,114
AN 113,115
M 73
R 80
M 84
mw 48
SH 114
AN 114,114
M 85
WL 2
AN 115,115
AN 116,118
M 86
AN 117,119
SH 118
M 87
AN 118,118
AN 119,119
M 88
TY 122,3592
TY 126,17416
M 89
ad 84,1824
W 0
M 90
XL 26,1792
M 91
M 92
W 1
XL 28,2560
M 93
M 94
M 95
R 82
M 106
W 2
M 107
WL 3
AN 120,122
AN 121,123
M 104
WL 2
SH 122
AN 122,122
M 105
mw 51
AN 123,123
AN 124,126
M 102
AN 125,127
SH 126
M 103
AN 126,126
AN 127,127
M 100
ad 84,1830
W 0
M 101
M 98
M 99
W 1
M 96
M 97
R 0
M 108
W 2
M 109
M 110
M 111
bc 49,20,66,60
M 112
mh 18
M 113
ad 66,3840
H 0
M 114
M 115
M 116
DG 1024
M 117
M 118
M 119
WL 2
R 2
M 120
bc 48,20,64,56
M 121
mh 21
M 133
ad 64,3840
H 0
M 132
TX 58,0
TX 62,13824
M 134
M 135
H 1
M 123
UL 6,14
UH 7,14
M 122
TY 66,512
TY 70,14336
M 124
H 2
AD 160,6
M 125
AD 184,7
UL 6,15
M 137
UH 7,15
AD 208,6
M 136
TX 74,1536
TX 78,15360
M 138
WL 4
Q 0
M 139
Q 2
M 127
Q 4
M 126
TY 82,2048
TY 86,15872
M 128
Q 6
M 129
WL 4
Q 8
M 141
Q 10
M 140
TX 90,3072
TX 94,16896
M 142
Q 12
M 143
R 6
M 131
Q 14
M 130
TY 98,3584
TY 102,17408
M 0
R 4
M 1
WL 10
Q 16
M 13
Q 18
M 12
Q 20
M 14
Q 22
M 15
WL 8
Q 24
M 3
Q 26
M 2
Q 28
M 4
Q 30
M 5
WL 6
Q 32
M 17
Q 34
M 16
Q 36
M 18
Q 38
M 19
WL 2
Q 40
M 7
Q 42
M 6
Q 44
M 8
Q 46
M 9
AD 232,7
UL 6,16
M 21
UH 7,16
AD 161,6
M 20
AD 185,7
UL 6,17
M 22
UH 7,17
AD 209,6
M 23
R 10
M 11
AD 233,7
UL 6,18
M 10
WL 2
R 8
M 34
UH 7,18
AD 162,6
M 35
AD 186,7
UL 6,19
M 32
UH 7,19
AD 210,6
M 33
AD 234,7
UL 6,20
M 30
)"
R"(UH 7,20
AD 163,6
M 31
AD 187,7
UL 6,21
M 28
UH 7,21
AD 211,6
M 29
AD 235,7
M 26
M 27
WV 5
M 24
WL 0
FP
M 25
M 36
M 37
M 38
M 39
M 40
M 41
M 42
M 43
M 44
M 45
FR
M 46
M 47
M 58
M 59
M 56
M 57
M 54
M 55
WL 0
FQ 56
M 52
M 53
M 50
FZ 56
R 24
M 51
mw 72
R 26
M 48
TY 106,5128
TY 110,18952
M 49
R 28
M 60
ad 84,1872
W 0
M 61
M 62
M 63
W 1
M 64
M 65
M 66
W 2
M 67
WL 3
AN 104,106
AN 105,107
M 68
WL 2
SH 106
AN 106,106
M 69
mw 75
AN 107,107
AN 108,110
M 70
AN 109,111
SH 110
M 71
R 30
M 82
AN 110,110
AN 111,111
M 83
TY 114,6664
TY 118,20488
M 80
ad 84,1878
W 0
M 81
M 78
M 79
W 1
M 76
ae 44,68,0
M 77
s_bitcmp1_b32 s50, 2
s_cselect_b32 s51, 24576, 0
v_add_u32 v47, s51, v3
M 74
W 2
XL 30,256
M 75
M 72
WL 1
AN 112,114
AN 113,115
M 73
R 32
M 84
mw 0
SH 114
AN 114,114
M 85
WL 2
AN 115,115
AN 116,118
M 86
AN 117,119
SH 118
M 87
AN 118,118
AN 119,119
M 88
TY 122,8200
TY 126,22024
M 89
ad 84,1920
W 0
M 90
XL 32,1024
M 91
XL 34,1792
M 92
W 1
M 93
M 94
M 95
R 34
M 106
W 2
XL 36,2560
M 107
WL 3
AN 120,122
AN 121,123
M 104
WL 2
SH 122
AN 122,122
M 105
mw 3
AN 123,123
AN 124,126
M 102
AN 125,127
SH 126
M 103
AN 126,126
AN 127,127
M 100
ad 84,1926
W 0
M 101
M 98
M 99
W 1
M 96
M 97
R 48
M 108
W 2
M 109
M 110
M 111
bc 49,21,66,60
M 112
mh 0
M 113
ad 66,4032
H 0
M 114
M 115
M 116
DG 1024
M 117
M 118
M 119
WL 2
R 50
M 120
bc 48,21,64,56
M 121
mh 3
v_mov_b32 v54, s39
v_mul_f32 v54, 0xb62aaa8f, v54
M 133
ad 64,4032
H 0
M 132
TX 58,4608
TX 62,18432
M 134
v_add_u32 v52, %[bm], v52
v_bfe_u32 v53, v52, 6, 2
M 135
H 1
v_and_b32 v55, 63, v52
M 123
v_add_u32 v53, v53, v55
v_and_b32 v52, 0x700, v52
M 122
TY 66,5120
TY 70,18944
M 124
H 2
v_lshl_add_u32 v52, v53, 2, v52
M 125
v_add_u32 v52, %[sb], v52
AR 12,214
M 137
AR 13,215
AR 8,208
M 136
TX 74,6144
TX 78,19968
PA 12288
DF
M 138
WL 4
Q 0
M 139
Q 2
M 127
Q 4
M 126
TY 82,6656
TY 86,20480
M 128
Q 6
M 129
WL 4
Q 8
M 141
Q 10
M 140
TX 90,7680
TX 94,21504
M 142
Q 12
M 143
R 54
M 131
Q 14
M 130
TY 98,8192
TY 102,22016
M 0
R 52
M 1
WL 10
Q 16
M 13
Q 18
M 12
Q 20
M 14
Q 22
M 15
WL 8
Q 24
M 3
Q 26
M 2
Q 28
M 4
Q 30
M 5
WL 6
Q 32
M 17
Q 34
M 16
Q 36
M 18
Q 38
M 19
WL 2
Q 40
M 7
Q 42
M 6
Q 44
M 8
Q 46
M 9
AR 9,209
v_add_f32 v12, v12, v8
M 21
v_add_f32 v13, v13, v9
AR 8,210
M 20
AR 9,211
v_add_f32 v12, v12, v8
M 22
v_add_f32 v13, v13, v9
AR 8,212
M 23
R 58
M 11
AR 9,213
v_add_f32 v12, v12, v8
M 10
WL 2
R 56
M 34
v_add_f32 v13, v13, v9
AR 6,206
M 35
AR 7,207
v_add_f32 v12, v12, v54
M 32
v_add_f32 v13, v13, v54
v_fma_f32 v8, v6, s86, v12
M 33
ds_write_b32 v52, v8 offset:1024
v_fma_f32 v9, v7, s86, v13
M 30
ds_write_b32 v52, v9 offset:1040
AR 8,200
M 31
AR 9,201
v_add_f32 v6, v6, v8
M 28
v_add_f32 v7, v7, v9
v_fma_f32 v8, v6, s86, v12
M 29
ds_write_b32 v52, v8 offset:2560
v_fma_f32 v9, v7, s86, v13
M 26
ds_write_b32 v52, v9 offset:2576
AR 8,202
M 27
WV 6
AR 9,203
v_add_f32 v6, v6, v8
M 24
WL 4
FP
v_add_f32 v7, v7, v9
M 25
v_fma_f32 v8, v6, s86, v12
v_fma_f32 v9, v7, s86, v13
M 36
ds_write_b32 v52, v8 offset:4096
ds_write_b32 v52, v9 offset:4112
M 37
AR 8,204
AR 9,205
M 38
v_add_f32 v6, v6, v8
v_add_f32 v7, v7, v9
M 39
v_fma_f32 v8, v6, s86, v12
v_fma_f32 v9, v7, s86, v13
M 40
ds_write_b32 v52, v8 offset:5632
ds_write_b32 v52, v9 offset:5648
M 41
UL 6,22
UH 7,22
M 42
AD 164,6
AD 188,7
M 43
UL 6,23
UH 7,23
M 44
AD 212,6
AD 236,7
M 45
FR
UL 6,24
M 46
UH 7,24
AD 165,6
M 47
AD 189,7
UL 6,25
M 58
UH 7,25
AD 213,6
M 59
AD 237,7
UL 6,26
M 56
UH 7,26
AD 166,6
M 57
AD 190,7
UL 6,27
)"
R"(M 54
UH 7,27
AD 214,6
M 55
WL 0
FQ 60
AD 238,7
M 52
UL 6,28
UH 7,28
M 53
AD 167,6
AD 191,7
M 50
FZ 60
R 72
M 51
mw 24
R 74
M 48
TY 106,9736
TY 110,23560
M 49
R 76
M 60
ad 84,1968
W 0
M 61
UL 6,29
UH 7,29
M 62
AD 215,6
AD 239,7
M 63
W 1
M 64
M 65
M 66
W 2
M 67
WL 3
AN 104,106
AN 105,107
M 68
WL 2
SH 106
AN 106,106
M 69
mw 27
AN 107,107
AN 108,110
M 70
AN 109,111
SH 110
M 71
R 78
M 82
AN 110,110
AN 111,111
M 83
TY 114,11272
TY 118,25096
M 80
ad 84,1974
W 0
M 81
M 78
M 79
W 1
M 76
M 77
M 74
W 2
M 75
M 72
WL 1
AN 112,114
AN 113,115
M 73
R 80
M 84
mw 48
SH 114
AN 114,114
M 85
WL 2
AN 115,115
AN 116,118
M 86
AN 117,119
SH 118
M 87
AN 118,118
AN 119,119
M 88
TY 122,12808
TY 126,26632
M 89
ad 84,2016
W 0
M 90
ae 44,68,0
M 91
s_bfe_u32 s51, s50, 0x10003
v_mad_i32_i24 v47, v49, s51, v48
XL 38,256
M 92
W 1
XL 40,1024
M 93
M 94
XL 42,1792
M 95
R 82
M 106
W 2
M 107
WL 3
AN 120,122
AN 121,123
M 104
WL 2
SH 122
AN 122,122
M 105
mw 51
AN 123,123
AN 124,126
M 102
AN 125,127
SH 126
M 103
AN 126,126
AN 127,127
M 100
ad 84,2022
W 0
M 101
XL 44,2560
M 98
M 99
W 1
M 96
M 97
R 0
M 108
W 2
M 109
M 110
M 111
bc 49,22,66,60
M 112
mh 9
M 113
ad 66,4224
H 0
M 114
M 115
M 116
DG 1024
M 117
M 118
M 119
WL 2
R 2
M 120
bc 48,22,64,56
M 121
mh 12
M 133
ad 64,4224
H 0
M 132
TX 58,9216
TX 62,23040
M 134
M 135
H 1
M 123
M 122
TY 66,9728
TY 70,23552
M 124
H 2
M 125
M 137
M 136
TX 74,10752
TX 78,24576
M 138
WL 4
Q 0
M 139
Q 2
M 127
Q 4
M 126
TY 82,11264
TY 86,25088
M 128
Q 6
M 129
WL 4
Q 8
M 141
Q 10
M 140
TX 90,12288
TX 94,26112
M 142
Q 12
M 143
R 6
M 131
Q 14
M 130
TY 98,12800
TY 102,26624
M 0
R 4
M 1
WL 10
Q 16
M 13
Q 18
M 12
Q 20
M 14
Q 22
M 15
WL 8
Q 24
M 3
Q 26
M 2
Q 28
M 4
Q 30
M 5
WL 6
Q 32
M 17
Q 34
M 16
Q 36
M 18
Q 38
M 19
WL 2
Q 40
M 7
Q 42
M 6
Q 44
M 8
Q 46
M 9
UL 6,30
UH 7,30
M 21
AD 168,6
AD 192,7
M 20
UL 6,31
UH 7,31
M 22
AD 216,6
AD 240,7
M 23
R 10
M 11
UL 6,32
UH 7,32
M 10
WL 2
R 8
M 34
AD 169,6
AD 193,7
M 35
UL 6,33
UH 7,33
M 32
AD 217,6
AD 241,7
M 33
UL 6,34
UH 7,34
M 30
AD 170,6
AD 194,7
M 31
UL 6,35
UH 7,35
M 28
AD 218,6
AD 242,7
M 29
UL 6,36
UH 7,36
M 26
AD 171,6
AD 195,7
M 27
WV 5
UL 6,37
UH 7,37
M 24
WL 0
FP
AD 219,6
M 25
AD 243,7
M 36
M 37
M 38
M 39
M 40
M 41
M 42
M 43
M 44
M 45
FR
M 46
M 47
M 58
M 59
M 56
M 57
M 54
M 55
WL 0
FQ 64
M 52
M 53
M 50
FZ 64
R 24
M 51
mw 72
R 26
M 48
TY 106,520
TY 110,14344
M 49
R 28
M 60
ad 84,2064
W 0
M 61
M 62
M 63
W 1
M 64
M 65
M 66
W 2
M 67
WL 3
AN 104,106
AN 105,107
M 68
WL 2
SH 106
AN 106,106
M 69
mw 75
AN 107,107
AN 108,110
M 70
AN 109,111
SH 110
M 71
R 30
M 82
AN 110,110
AN 111,111
M 83
TY 114,2056
TY 118,15880
M 80
ad 84,2070
W 0
M 81
M 78
M 79
W 1
M 76
M 77
M 74
W 2
M 75
M 72
WL 1
AN 112,114
AN 113,115
M 73
R 32
M 84
)"
R"(mw 0
SH 114
AN 114,114
M 85
WL 2
AN 115,115
AN 116,118
M 86
AN 117,119
SH 118
M 87
AN 118,118
AN 119,119
M 88
TY 122,3592
TY 126,17416
M 89
ad 84,2112
W 0
M 90
ae 44,68,96
M 91
s_bitcmp1_b32 s50, 4
s_cselect_b32 s51, 24576, 0
v_add_u32 v47, s51, v3
M 92
W 1
XL 14,256
M 93
XL 16,1024
M 94
M 95
R 34
M 106
W 2
M 107
WL 3
AN 120,122
AN 121,123
M 104
WL 2
SH 122
AN 122,122
M 105
mw 3
AN 123,123
AN 124,126
M 102
AN 125,127
SH 126
M 103
AN 126,126
AN 127,127
M 100
ad 84,2118
W 0
M 101
XL 18,1792
M 98
XL 20,2560
M 99
W 1
M 96
M 97
R 48
M 108
W 2
M 109
M 110
M 111
bc 49,23,66,60
M 112
mh 18
M 113
ad 66,4416
H 0
M 114
M 115
M 116
DG 1024
M 117
M 118
M 119
WL 2
R 50
M 120
bc 48,23,64,56
M 121
mh 21
M 133
ad 64,4416
H 0
M 132
TX 58,0
TX 62,13824
M 134
M 135
H 1
M 123
M 122
TY 66,512
TY 70,14336
M 124
H 2
M 125
M 137
M 136
TX 74,1536
TX 78,15360
PA 12288
DF
M 138
WL 4
Q 0
M 139
Q 2
M 127
Q 4
M 126
TY 82,2048
TY 86,15872
M 128
Q 6
M 129
WL 4
Q 8
M 141
Q 10
M 140
TX 90,3072
TX 94,16896
M 142
Q 12
M 143
R 54
M 131
Q 14
M 130
TY 98,3584
TY 102,17408
M 0
R 52
M 1
WL 10
Q 16
M 13
Q 18
M 12
Q 20
M 14
Q 22
M 15
WL 8
Q 24
M 3
Q 26
M 2
Q 28
M 4
Q 30
M 5
WL 6
Q 32
M 17
Q 34
M 16
Q 36
M 18
Q 38
M 19
WL 2
Q 40
M 7
Q 42
M 6
Q 44
M 8
Q 46
M 9
UL 6,38
UH 7,38
M 21
AD 172,6
AD 196,7
M 20
UL 6,39
UH 7,39
M 22
AD 220,6
AD 244,7
M 23
R 58
M 11
UL 6,40
UH 7,40
M 10
WL 2
R 56
M 34
AD 173,6
AD 197,7
M 35
UL 6,41
UH 7,41
M 32
AD 221,6
AD 245,7
M 33
UL 6,42
UH 7,42
M 30
AD 174,6
AD 198,7
M 31
UL 6,43
UH 7,43
M 28
AD 222,6
AD 246,7
M 29
UL 6,44
UH 7,44
M 26
AD 175,6
AD 199,7
M 27
WV 6
UL 6,45
UH 7,45
M 24
WL 0
FP
AD 223,6
M 25
AD 247,7
M 36
M 37
M 38
M 39
M 40
M 41
M 42
M 43
M 44
M 45
FR
M 46
M 47
M 58
M 59
M 56
M 57
M 54
M 55
WL 0
FQ 68
M 52
M 53
M 50
FZ 68
R 72
M 51
mw 24
R 74
M 48
TY 106,5128
TY 110,18952
M 49
R 76
M 60
ad 84,2160
W 0
M 61
M 62
M 63
W 1
M 64
M 65
M 66
W 2
M 67
WL 3
AN 104,106
AN 105,107
M 68
WL 2
SH 106
AN 106,106
M 69
mw 27
AN 107,107
AN 108,110
M 70
AN 109,111
SH 110
M 71
R 78
M 82
AN 110,110
AN 111,111
M 83
TY 114,6664
TY 118,20488
M 80
ad 84,2166
W 0
M 81
M 78
M 79
W 1
M 76
M 77
M 74
W 2
M 75
M 72
WL 1
AN 112,114
AN 113,115
M 73
R 80
M 84
mw 48
SH 114
AN 114,114
M 85
WL 2
AN 115,115
AN 116,118
M 86
AN 117,119
SH 118
M 87
AN 118,118
AN 119,119
M 88
TY 122,8200
TY 126,22024
M 89
ad 84,2208
W 0
M 90
ae 44,68,96
M 91
s_bfe_u32 s51, s50, 0x10005
v_mad_i32_i24 v47, v49, s51, v48
XL 22,256
M 92
W 1
M 93
M 94
M 95
R 82
M 106
W 2
XL 24,1024
M 107
WL 3
AN 120,122
AN 121,123
M 104
WL 2
SH 122
AN 122,122
M 105
mw 51
AN 123,123
AN 124,126
M 102
AN 125,127
SH 126
M 103
AN 126,126
AN 127,127
M 100
ad 84,2214
W 0
M 101
XL 26,1792
M 98
M 99
W 1
M 96
M 97
R 0
M 108
W 2
XL 28,2560
M 109
M 110
M 111
bc 49,0,66,60
M 112
mh 0
M 113
ad 66,32
H 0
M 114
M 115
M 116
)"
R"(DG 1024
M 117
M 118
M 119
WL 2
R 2
M 120
bc 48,0,64,56
M 121
mh 3
M 133
ad 64,0
H 0
M 132
TX 58,4608
TX 62,18432
M 134
M 135
H 1
M 123
M 122
TY 66,5120
TY 70,18944
M 124
H 2
M 125
M 137
M 136
TX 74,6144
TX 78,19968
M 138
WL 4
Q 0
M 139
Q 2
M 127
Q 4
M 126
TY 82,6656
TY 86,20480
M 128
Q 6
M 129
WL 4
Q 8
M 141
Q 10
M 140
TX 90,7680
TX 94,21504
M 142
Q 12
M 143
R 6
M 131
Q 14
M 130
TY 98,8192
TY 102,22016
M 0
R 4
M 1
WL 10
Q 16
M 13
Q 18
M 12
Q 20
M 14
Q 22
M 15
WL 8
Q 24
M 3
Q 26
M 2
Q 28
M 4
Q 30
M 5
WL 6
Q 32
M 17
Q 34
M 16
Q 36
M 18
Q 38
M 19
WL 2
Q 40
M 7
Q 42
M 6
Q 44
M 8
Q 46
M 9
UL 6,14
UH 7,14
M 21
AD 176,6
AD 200,7
M 20
UL 6,15
UH 7,15
M 22
AD 224,6
AD 248,7
M 23
R 10
M 11
UL 6,16
UH 7,16
M 10
WL 2
R 8
M 34
AD 177,6
AD 201,7
M 35
UL 6,17
UH 7,17
M 32
AD 225,6
AD 249,7
M 33
UL 6,18
UH 7,18
M 30
AD 178,6
AD 202,7
M 31
UL 6,19
UH 7,19
M 28
AD 226,6
AD 250,7
M 29
UL 6,20
UH 7,20
M 26
AD 179,6
AD 203,7
M 27
WV 6
UL 6,21
UH 7,21
M 24
WL 0
FP
AD 227,6
M 25
AD 251,7
M 36
M 37
M 38
M 39
M 40
M 41
M 42
M 43
M 44
M 45
FR
M 46
M 47
M 58
M 59
M 56
M 57
M 54
M 55
WL 0
FQ 72
M 52
M 53
M 50
FZ 72
R 24
M 51
mw 72
R 26
M 48
TY 106,9736
TY 110,23560
M 49
R 28
M 60
ad 84,2256
W 0
M 61
M 62
M 63
W 1
M 64
M 65
M 66
W 2
M 67
WL 3
AN 104,106
AN 105,107
M 68
WL 2
SH 106
AN 106,106
M 69
mw 75
AN 107,107
AN 108,110
M 70
AN 109,111
SH 110
M 71
R 30
M 82
AN 110,110
AN 111,111
M 83
TY 114,11272
TY 118,25096
M 80
ad 84,2262
W 0
M 81
M 78
M 79
W 1
M 76
M 77
M 74
W 2
M 75
M 72
WL 1
AN 112,114
AN 113,115
M 73
R 32
M 84
mw 0
SH 114
AN 114,114
M 85
WL 2
AN 115,115
AN 116,118
M 86
AN 117,119
SH 118
M 87
AN 118,118
AN 119,119
M 88
TY 122,12808
TY 126,26632
M 89
ad 84,0
W 0
M 90
M 91
M 92
W 1
M 93
M 94
M 95
R 34
M 106
W 2
M 107
WL 3
AN 120,122
AN 121,123
M 104
WL 2
SH 122
AN 122,122
M 105
mw 3
AN 123,123
AN 124,126
M 102
AN 125,127
SH 126
M 103
AN 126,126
AN 127,127
M 100
ad 84,6
W 0
M 101
M 98
M 99
W 1
M 96
M 97
R 48
M 108
W 2
M 109
M 110
M 111
bc 49,1,66,60
M 112
mh 9
M 113
ad 66,224
H 0
M 114
M 115
M 116
DG 1024
M 117
M 118
M 119
WL 2
R 50
M 120
bc 48,1,64,56
M 121
mh 12
M 133
ad 64,224
H 0
M 132
TX 58,9216
TX 62,23040
M 134
M 135
DG 1024
M 123
M 122
TY 66,9728
TY 70,23552
M 124
mh 15
M 125
ad 64,195
H 0
M 137
M 136
TX 74,10752
TX 78,24576
M 138
WL 4
Q 0
M 139
Q 2
M 127
Q 4
M 126
TY 82,11264
TY 86,25088
M 128
Q 6
M 129
WL 4
Q 8
M 141
Q 10
M 140
TX 90,12288
TX 94,26112
M 142
Q 12
M 143
R 54
M 131
Q 14
M 130
TY 98,12800
TY 102,26624
M 0
R 52
M 1
WL 10
Q 16
M 13
Q 18
M 12
Q 20
M 14
Q 22
M 15
WL 8
Q 24
M 3
Q 26
M 2
Q 28
M 4
Q 30
M 5
WL 6
Q 32
M 17
Q 34
M 16
Q 36
)"
R"(M 18
Q 38
M 19
WL 2
Q 40
M 7
Q 42
M 6
Q 44
M 8
Q 46
M 9
DG 1024
M 21
UL 6,22
UH 7,22
M 20
AD 180,6
AD 204,7
M 22
PA 12288
DF
UL 6,23
M 23
R 58
M 11
UH 7,23
AD 228,6
M 10
WL 2
R 56
M 34
AD 252,7
UL 6,24
M 35
UH 7,24
AD 181,6
M 32
AD 205,7
UL 6,25
BV 52,0
M 33
UH 7,25
AD 229,6
M 30
AD 253,7
UL 6,26
M 31
UH 7,26
AD 182,6
M 28
AD 206,7
UL 6,27
M 29
UH 7,27
AD 230,6
M 26
WV 24
AD 254,7
UL 6,28
M 27
WV 7
UH 7,28
AD 183,6
M 24
WL 1
FP
AD 207,7
M 25
UL 6,29
UH 7,29
M 36
AD 231,6
AD 255,7
M 37
M 38
M 39
M 40
ds_read_b64 v[12:13], v4 offset:256
ds_read_b64 v[46:47], v4 offset:264
M 41
AR 6,36
AR 7,60
M 42
AR 8,84
AR 9,108
M 43
WL 3
EF 6,52
EF 7,53
M 44
EF 8,54
EF 9,55
M 45
FR
CV 30,6,7
M 46
CV 31,8,9
AR 6,37
M 47
AR 7,61
AR 8,85
M 58
AR 9,109
EF 6,52
M 59
EF 7,53
EF 8,54
M 56
EF 9,55
CV 32,6,7
M 57
CV 33,8,9
AR 6,38
M 54
AR 7,62
AR 8,86
M 55
WL 0
FQ 76
AR 9,110
M 52
EF 6,52
EF 7,53
M 53
EF 8,54
EF 9,55
M 50
FZ 76
R 72
M 51
mw 24
R 74
M 48
TY 106,520
TY 110,14344
M 49
R 76
M 60
ad 84,48
W 0
M 61
CV 34,6,7
CV 35,8,9
M 62
AR 6,39
AR 7,63
M 63
W 1
AR 8,87
M 64
AR 9,111
EF 6,52
M 65
EF 7,53
EF 8,54
M 66
W 2
EF 9,55
M 67
WL 3
AN 104,106
AN 105,107
M 68
WL 2
SH 106
AN 106,106
M 69
mw 27
AN 107,107
AN 108,110
M 70
AN 109,111
SH 110
M 71
R 78
M 82
AN 110,110
AN 111,111
M 83
TY 114,2056
TY 118,15880
M 80
ad 84,54
W 0
M 81
CV 36,6,7
CV 37,8,9
M 78
ae 42,74,0
AR 6,40
M 79
W 1
AR 7,64
M 76
s_bitcmp1_b32 s50, 0
s_cselect_b32 s52, 24576, 0
v_add_u32 v50, s52, v3
ST 30,0
M 77
AR 8,88
AR 9,112
M 74
W 2
EF 6,52
M 75
EF 7,53
EF 8,54
M 72
WL 1
AN 112,114
AN 113,115
M 73
R 80
M 84
mw 48
SH 114
AN 114,114
M 85
WL 2
AN 115,115
AN 116,118
M 86
AN 117,119
SH 118
M 87
AN 118,118
AN 119,119
M 88
TY 122,3592
TY 126,17416
M 89
ad 84,96
W 0
M 90
EF 9,55
CV 38,6,7
M 91
ST 32,768
CV 39,8,9
M 92
W 1
AR 6,41
M 93
AR 7,65
AR 8,89
M 94
AR 9,113
EF 6,52
M 95
R 82
M 106
W 2
EF 7,53
M 107
WL 3
AN 120,122
AN 121,123
M 104
WL 2
SH 122
AN 122,122
M 105
mw 51
AN 123,123
AN 124,126
M 102
AN 125,127
SH 126
M 103
AN 126,126
AN 127,127
M 100
ad 84,102
W 0
M 101
EF 8,54
EF 9,55
M 98
ST 34,1536
CV 40,6,7
M 99
W 1
CV 41,8,9
M 96
AR 6,42
AR 7,66
M 97
R 0
M 108
W 2
AR 8,90
M 109
AR 9,114
EF 6,52
M 110
EF 7,53
EF 8,54
M 111
bc 49,2,66,60
EF 9,55
M 112
mh 18
CV 42,6,7
CV 43,8,9
M 113
ad 66,416
H 0
M 114
AR 6,43
AR 7,67
M 115
AR 8,91
AR 9,115
M 116
DG 1024
M 117
ST 36,2304
EF 6,52
M 118
EF 7,53
EF 8,54
M 119
WL 0
R 2
M 120
bc 48,2,64,56
EF 9,55
M 121
mh 21
CV 44,6,7
CV 45,8,9
M 133
ad 64,416
H 0
BV 52,512
M 132
TX 58,0
TX 62,13824
M 134
AR 6,132
AR 7,156
M 135
H 1
AR 8,180
M 123
EF 6,12
EF 7,13
M 122
TY 66,512
TY 70,14336
M 124
H 2
EF 8,46
M 125
EF 136,47
CV 14,6,7
M 137
ae 42,74,0
CV 15,8,136
M 136
WL 2
TX 74,1536
TX 78,15360
M 138
s_bfe_u32 s52, s50, 0x10001
Q 0
v_mad_i32_i24 v50, v49, s52, v48
M 139
Q 2
ST 38,0
M 127
Q 4
M 126
TY 82,2048
TY 86,15872
M 128
Q 6
M 129
WL 4
Q 8
M 141
Q 10
M 140
TX 90,3072
TX 94,16896
M 142
Q 12
M 143
R 6
M 131
Q 14
M 130
TY 98,3584
TY 102,17408
v_mfma_f32_16x16x32_f16 a[36:39], v[56:59], a[0:3], a[32:35]
R 4
M 1
WL 10
Q 16
)"
R"(v_mfma_f32_16x16x32_f16 a[60:63], v[60:63], a[12:15], a[32:35]
Q 18
ST 40,768
M 12
Q 20
v_mfma_f32_16x16x32_f16 a[64:67], v[64:67], a[8:11], a[32:35]
Q 22
M 15
WL 8
Q 24
v_mfma_f32_16x16x32_f16 a[40:43], v[68:71], a[4:7], a[32:35]
Q 26
M 2
Q 28
M 4
Q 30
M 5
WL 6
Q 32
M 17
Q 34
M 16
Q 36
M 18
Q 38
M 19
WL 1
Q 40
M 7
Q 42
M 6
Q 44
ST 42,1536
M 8
Q 46
M 9
AR 6,133
AR 7,157
M 21
AR 8,181
EF 6,12
M 20
EF 7,13
EF 8,46
M 22
EF 137,47
CV 16,6,7
M 23
R 10
M 11
CV 17,8,137
AR 6,134
M 10
WL 2
R 8
M 34
AR 7,158
AR 8,182
M 35
EF 6,12
EF 7,13
M 32
EF 8,46
EF 138,47
M 33
CV 18,6,7
CV 19,8,138
M 30
ST 44,2304
AR 6,135
M 31
AR 7,159
AR 8,183
M 28
EF 6,12
EF 7,13
M 29
EF 8,46
EF 139,47
v_mfma_f32_16x16x32_f16 a[88:91], v[64:67], a[16:19], a[32:35]
CV 20,6,7
CV 21,8,139
M 27
WV 10
AR 6,136
AR 7,160
AR 8,184
v_mfma_f32_16x16x32_f16 a[84:87], v[56:59], a[16:19], a[32:35]
WL 0
FP
EF 6,12
EF 7,13
M 25
EF 8,46
EF 140,47
CV 22,6,7
v_mfma_f32_16x16x32_f16 a[108:111], v[56:59], a[24:27], a[32:35]
CV 23,8,140
AR 6,137
AR 7,161
M 37
AR 8,185
EF 6,12
EF 7,13
v_mfma_f32_16x16x32_f16 a[112:115], v[64:67], a[24:27], a[32:35]
EF 8,46
EF 141,47
CV 24,6,7
M 39
CV 25,8,141
AR 6,138
AR 7,162
M 40
AR 8,186
EF 6,12
EF 7,13
M 41
ae 42,74,0
EF 8,46
EF 142,47
M 42
s_bitcmp1_b32 s50, 0
s_cselect_b32 s52, 24576, 0
v_add_u32 v50, s52, v3
ST 14,128
M 43
CV 26,6,7
CV 27,8,142
AR 6,139
M 44
AR 7,163
AR 8,187
EF 6,12
M 45
FR
EF 7,13
EF 8,46
M 46
EF 143,47
CV 28,6,7
CV 29,8,143
M 47
ds_read_b64 v[12:13], v4 offset:1536
ds_read_b64 v[46:47], v4 offset:1544
M 58
EF 160,52
EF 184,53
M 59
EF 208,54
EF 232,55
M 56
CV 30,160,184
CV 31,208,232
M 57
EF 161,52
EF 185,53
M 54
EF 209,54
EF 233,55
M 55
WL 2
FQ 80
CV 32,161,185
M 52
CV 33,209,233
EF 162,52
M 53
ST 16,896
EF 186,53
v_mfma_f32_16x16x32_f16 a[136:139], v[64:67], a[0:3], a[32:35]
FZ 80
R 24
M 51
mw 72
R 26
v_mfma_f32_16x16x32_f16 a[132:135], v[56:59], a[0:3], a[32:35]
TY 106,5128
TY 110,18952
M 49
R 28
v_mfma_f32_16x16x32_f16 a[156:159], v[56:59], a[8:11], a[32:35]
ad 84,144
W 0
M 61
EF 210,54
EF 234,55
v_mfma_f32_16x16x32_f16 a[160:163], v[64:67], a[8:11], a[32:35]
CV 34,162,186
CV 35,210,234
M 63
W 1
EF 163,52
M 64
EF 187,53
EF 211,54
M 65
EF 235,55
CV 36,163,187
M 66
W 2
CV 37,211,235
M 67
WL 3
AN 104,106
AN 105,107
EF 164,52
M 68
WL 2
SH 106
AN 106,106
ST 18,1664
M 69
mw 75
AN 107,107
AN 108,110
EF 188,53
M 70
AN 109,111
SH 110
EF 212,54
M 71
R 30
EF 236,55
M 82
AN 110,110
AN 111,111
CV 38,164,188
M 83
TY 114,6664
TY 118,20488
CV 39,212,236
M 80
ad 84,150
W 0
EF 165,52
M 81
EF 189,53
EF 213,54
EF 237,55
M 78
CV 40,165,189
CV 41,213,237
EF 166,52
M 79
W 1
EF 190,53
EF 214,54
M 76
EF 238,55
CV 42,166,190
CV 43,214,238
M 77
EF 167,52
EF 191,53
EF 215,54
v_mfma_f32_16x16x32_f16 a[184:187], v[64:67], a[16:19], a[32:35]
W 2
EF 239,55
CV 44,167,191
M 75
ST 20,2432
CV 45,215,239
v_mfma_f32_16x16x32_f16 a[180:183], v[56:59], a[16:19], a[32:35]
WL 1
AN 112,114
AN 113,115
BV 52,1792
M 73
R 32
v_mfma_f32_16x16x32_f16 v[136:139], v[56:59], a[24:27], v[128:131]
mw 0
SH 114
AN 114,114
M 85
WL 3
AN 115,115
AN 116,118
v_mfma_f32_16x16x32_f16 v[140:143], v[64:67], a[24:27], v[128:131]
AN 117,119
SH 118
M 87
AN 118,118
AN 119,119
M 88
TY 122,8200
TY 126,22024
M 89
ad 84,192
W 0
M 90
AR 6,44
AR 7,68
M 91
AR 8,92
AR 9,116
M 92
W 1
EF 6,12
M 93
EF 7,13
EF 8,46
M 94
ae 42,74,0
EF 9,47
M 95
R 34
M 106
WL 3
W 2
CV 14,6,7
M 107
s_bfe_u32 s52, s50, 0x10001
AN 120,122
AN 121,123
v_mad_i32_i24 v50, v49, s52, v48
M 104
WL 2
SH 122
AN 122,122
ST 22,128
M 105
mw 3
AN 123,123
AN 124,126
M 102
AN 125,127
SH 126
M 103
AN 126,126
AN 127,127
M 100
ad 84,198
W 0
M 101
CV 15,8,9
AR 6,45
v_mfma_f32_16x16x32_f16 v[164:167], v[64:67], a[0:3], v[128:131]
AR 7,69
AR 8,93
M 99
W 1
AR 9,117
v_mfma_f32_16x16x32_f16 v[160:163], v[56:59], a[0:3], v[128:131]
EF 6,12
EF 7,13
M 97
R 48
v_mfma_f32_16x16x32_f16 v[184:187], v[56:59], a[8:11], v[128:131]
W 2
EF 8,46
M 109
EF 9,47
CV 16,6,7
v_mfma_f32_16x16x32_f16 v[188:191], v[64:67], a[8:11], v[128:131]
CV 17,8,9
AR 6,46
M 111
bc 49,3,66,60
ST 24,896
M 112
mh 0
AR 7,70
AR 8,94
M 113
ad 66,608
H 0
M 114
AR 9,118
EF 6,12
M 115
EF 7,13
EF 8,46
M 116
DG 1024
M 117
EF 9,47
CV 18,6,7
M 118
CV 19,8,9
AR 6,47
M 119
WL 0
R 50
v_mfma_f32_16x16x32_f16 v[208:211], v[56:59], a[16:19], v[128:131]
bc 48,3,64,56
AR 7,71
M 121
mh 3
AR 8,95
AR 9,119
v_mfma_f32_16x16x32_f16 v[232:235], v[60:63], a[28:31], v[128:131]
ad 64,608
H 0
M 132
TX 58,4608
TX 62,18432
v_mfma_f32_16x16x32_f16 v[236:239], v[64:67], a[24:27], v[128:131]
ST 26,1664
EF 6,12
M 135
)"
R"(H 1
EF 7,13
v_mfma_f32_16x16x32_f16 v[212:215], v[68:71], a[20:23], v[128:131]
EF 8,46
EF 9,47
M 122
TY 66,5120
TY 70,18944
M 124
H 2
CV 20,6,7
M 125
CV 21,8,9
M 137
AR 6,48
AR 7,72
AR 8,96
AR 9,120
EF 6,12
EF 7,13
M 136
TX 74,6144
TX 78,19968
EF 8,46
EF 9,47
CV 6,6,7
CV 7,8,9
M 138
WL 4
Q 0
AW 200,6
AW 201,7
AR 6,49
AR 7,73
M 139
Q 2
AR 8,97
AR 9,121
EF 6,12
EF 7,13
M 127
Q 4
EF 8,46
EF 9,47
CV 6,6,7
CV 7,8,9
M 126
TY 82,6656
TY 86,20480
AW 202,6
AW 203,7
AR 6,50
AR 7,74
M 128
Q 6
AR 8,98
AR 9,122
EF 6,12
EF 7,13
M 129
WL 2
Q 8
ST 28,2432
EF 8,46
EF 9,47
CV 6,6,7
M 141
Q 10
CV 7,8,9
AW 204,6
AW 205,7
AR 6,51
M 140
TX 90,7680
TX 94,21504
AR 7,75
AR 8,99
AR 9,123
EF 6,12
M 142
Q 12
EF 7,13
EF 8,46
EF 9,47
CV 6,6,7
M 143
R 54
CV 7,8,9
AW 206,6
AW 207,7
M 131
Q 14
ds_read_b64 v[12:13], v4 offset:2048
M 130
TY 98,8192
TY 102,22016
ds_read_b64 v[46:47], v4 offset:2056
M 0
R 52
M 1
Q 16
M 13
Q 18
M 12
Q 20
M 14
Q 22
M 15
WL 8
Q 24
M 3
Q 26
M 2
Q 28
v_mfma_f32_16x16x32_f16 a[44:47], v[72:75], a[0:3], a[32:35]
Q 30
M 5
Q 32
v_mfma_f32_16x16x32_f16 a[68:71], v[76:79], a[12:15], a[32:35]
Q 34
M 16
Q 36
v_mfma_f32_16x16x32_f16 a[72:75], v[80:83], a[8:11], a[32:35]
Q 38
M 19
WL 2
Q 40
v_mfma_f32_16x16x32_f16 a[48:51], v[84:87], a[4:7], a[32:35]
Q 42
M 6
Q 44
M 8
Q 46
M 9
ae 42,74,0
M 21
s_bitcmp1_b32 s50, 0
s_cselect_b32 s52, 24576, 0
v_add_u32 v50, s52, v3
ST 30,256
M 20
AR 6,140
AR 7,164
M 22
AR 8,188
EF 6,52
M 23
R 58
M 11
EF 7,53
EF 8,54
M 10
WL 2
R 56
M 34
EF 144,55
CV 22,6,7
M 35
CV 23,8,144
AR 6,141
M 32
AR 7,165
AR 8,189
M 33
EF 6,52
EF 7,53
v_mfma_f32_16x16x32_f16 a[96:99], v[80:83], a[16:19], a[32:35]
EF 8,54
EF 145,55
M 31
CV 24,6,7
CV 25,8,145
v_mfma_f32_16x16x32_f16 a[92:95], v[72:75], a[16:19], a[32:35]
AR 6,142
AR 7,166
M 29
ST 32,1024
AR 8,190
M 26
EF 6,52
EF 7,53
M 27
WV 10
EF 8,54
EF 146,55
M 24
WL 0
FP
CV 26,6,7
M 25
CV 27,8,146
AR 6,143
M 36
AR 7,167
AR 8,191
M 37
EF 6,52
EF 7,53
M 38
EF 8,54
EF 147,55
M 39
CV 28,6,7
CV 29,8,147
v_mfma_f32_16x16x32_f16 a[116:119], v[72:75], a[24:27], a[32:35]
AR 6,144
AR 7,168
AR 8,192
EF 6,52
M 41
EF 7,53
EF 8,54
EF 148,55
CV 6,6,7
v_mfma_f32_16x16x32_f16 a[120:123], v[80:83], a[24:27], a[32:35]
CV 7,8,148
AW 208,6
AW 209,7
AR 6,145
M 43
AR 7,169
AR 8,193
EF 6,52
EF 7,53
M 44
ST 34,1792
EF 8,54
EF 149,55
CV 6,6,7
M 45
FR
CV 7,8,149
AW 210,6
AW 211,7
M 46
AR 6,146
AR 7,170
AR 8,194
EF 6,52
M 47
EF 7,53
EF 8,54
EF 150,55
CV 6,6,7
M 58
CV 7,8,150
AW 212,6
AW 213,7
AR 6,147
M 59
AR 7,171
AR 8,195
EF 6,52
EF 7,53
M 56
EF 8,54
EF 151,55
CV 6,6,7
CV 7,8,151
M 57
AW 214,6
AW 215,7
v_mfma_f32_16x16x32_f16 a[144:147], v[80:83], a[0:3], a[32:35]
BV 52,3072
EF 168,12
EF 192,13
M 55
WL 1
FQ 84
EF 216,46
EF 240,47
v_mfma_f32_16x16x32_f16 a[140:143], v[72:75], a[0:3], a[32:35]
CV 6,168,192
CV 7,216,240
AW 216,6
M 53
AW 217,7
EF 169,12
EF 193,13
M 50
FZ 84
R 72
EF 217,46
M 51
mw 24
R 74
ST 36,2560
M 48
TY 106,9736
TY 110,23560
EF 241,47
M 49
R 76
CV 6,169,193
M 60
ad 84,240
W 0
CV 7,217,241
M 61
AW 218,6
AW 219,7
EF 170,12
M 62
EF 194,13
EF 218,46
EF 242,47
M 63
W 1
CV 6,170,194
CV 7,218,242
v_mfma_f32_16x16x32_f16 a[164:167], v[72:75], a[8:11], a[32:35]
AW 220,6
AW 221,7
EF 171,12
M 65
EF 195,13
EF 219,46
EF 243,47
v_mfma_f32_16x16x32_f16 a[168:171], v[80:83], a[8:11], a[32:35]
W 2
CV 6,171,195
CV 7,219,243
M 67
WL 3
AN 104,106
AN 105,107
AW 222,6
M 68
WL 0
SH 106
AN 106,106
AW 223,7
M 69
mw 27
AN 107,107
AN 108,110
EF 172,12
M 70
AN 109,111
SH 110
EF 196,13
M 71
R 78
EF 220,46
M 82
AN 110,110
AN 111,111
EF 244,47
M 83
TY 114,11272
TY 118,25096
CV 30,172,196
M 80
ad 84,246
W 0
CV 31,220,244
M 81
ae 42,74,0
EF 173,12
EF 197,13
v_mfma_f32_16x16x32_f16 a[192:195], v[80:83], a[16:19], a[32:35]
s_bfe_u32 s52, s50, 0x10001
v_mad_i32_i24 v50, v49, s52, v48
ST 38,256
EF 221,46
M 79
W 1
EF 245,47
CV 32,173,197
v_mfma_f32_16x16x32_f16 a[188:191], v[72:75], a[16:19], a[32:35]
CV 33,221,245
EF 174,12
EF 198,13
M 77
EF 222,46
EF 246,47
CV 34,174,198
M 74
W 2
CV 35,222,246
EF 175,12
M 75
EF 199,13
EF 223,46
EF 247,47
M 72
WL 1
AN 112,114
AN 113,115
CV 36,175,199
M 73
R 80
CV 37,223,247
M 84
mw 48
SH 114
AN 114,114
ds_read_b64 v[12:13], v4 offset:3328
M 85
WL 3
)"
R"(AN 115,115
AN 116,118
ds_read_b64 v[46:47], v4 offset:3336
M 86
AN 117,119
SH 118
AR 6,52
M 87
AN 118,118
AN 119,119
AR 7,76
v_mfma_f32_16x16x32_f16 v[144:147], v[72:75], a[24:27], v[128:131]
TY 122,12808
TY 126,26632
AR 8,100
M 89
ad 84,288
W 0
ST 40,1024
v_mfma_f32_16x16x32_f16 v[148:151], v[80:83], a[24:27], v[128:131]
AR 9,124
EF 6,52
EF 7,53
M 91
EF 8,54
EF 9,55
CV 6,6,7
M 92
W 1
CV 7,8,9
AW 224,6
M 93
AW 225,7
AR 6,53
AR 7,77
M 94
AR 8,101
AR 9,125
EF 6,52
M 95
R 82
EF 7,53
M 106
W 2
EF 8,54
EF 9,55
M 107
WL 3
AN 120,122
AN 121,123
CV 6,6,7
M 104
WL 2
SH 122
AN 122,122
CV 7,8,9
M 105
mw 51
AN 123,123
AN 124,126
AW 226,6
v_mfma_f32_16x16x32_f16 v[172:175], v[80:83], a[0:3], v[128:131]
AN 125,127
SH 126
AW 227,7
M 103
AN 126,126
AN 127,127
AR 6,54
v_mfma_f32_16x16x32_f16 v[168:171], v[72:75], a[0:3], v[128:131]
ad 84,294
W 0
ST 42,1792
M 101
AR 7,78
AR 8,102
AR 9,126
M 98
EF 6,52
EF 7,53
EF 8,54
M 99
W 1
EF 9,55
CV 6,6,7
M 96
CV 7,8,9
AW 228,6
AW 229,7
M 97
R 0
AR 6,55
M 108
W 2
AR 7,79
AR 8,103
M 109
AR 9,127
EF 6,52
EF 7,53
M 110
EF 8,54
EF 9,55
CV 6,6,7
M 111
bc 49,4,66,60
CV 7,8,9
AW 230,6
v_mfma_f32_16x16x32_f16 v[192:195], v[72:75], a[8:11], v[128:131]
mh 9
AW 231,7
M 113
ad 66,800
H 0
AR 6,56
v_mfma_f32_16x16x32_f16 v[196:199], v[80:83], a[8:11], v[128:131]
AR 7,80
AR 8,104
AR 9,128
M 115
ST 44,2560
EF 6,52
EF 7,53
M 116
DG 1024
EF 8,54
M 117
EF 9,55
CV 6,6,7
CV 7,8,9
M 118
AW 232,6
AW 233,7
AR 6,57
M 119
WL 0
R 2
AR 7,81
M 120
bc 48,4,64,56
AR 8,105
AR 9,129
M 121
mh 12
EF 6,52
EF 7,53
EF 8,54
M 133
ad 64,800
H 0
EF 9,55
M 132
TX 58,9216
TX 62,23040
CV 6,6,7
M 134
CV 7,8,9
AW 234,6
AW 235,7
M 135
H 1
AR 6,58
AR 7,82
M 123
AR 8,106
AR 9,130
EF 6,52
M 122
TY 66,9728
TY 70,23552
EF 7,53
v_mfma_f32_16x16x32_f16 v[216:219], v[72:75], a[16:19], v[128:131]
H 2
EF 8,54
EF 9,55
M 125
ae 42,72,0
CV 6,6,7
CV 7,8,9
v_mfma_f32_16x16x32_f16 v[240:243], v[76:79], a[28:31], v[128:131]
s_bitcmp1_b32 s50, 2
s_cselect_b32 s52, 24576, 0
v_add_u32 v50, s52, v3
ST 14,0
M 136
TX 74,10752
TX 78,24576
PA 12288
DF
v_mfma_f32_16x16x32_f16 v[244:247], v[80:83], a[24:27], v[128:131]
WL 4
Q 0
AW 236,6
M 139
Q 2
AW 237,7
v_mfma_f32_16x16x32_f16 v[220:223], v[84:87], a[20:23], v[128:131]
Q 4
AR 6,59
M 126
TY 82,11264
TY 86,25088
AR 7,83
M 128
Q 6
AR 8,107
M 129
WL 4
Q 8
AR 9,131
M 141
Q 10
EF 6,52
M 140
TX 90,12288
TX 94,26112
EF 7,53
M 142
Q 12
EF 8,54
M 143
R 6
EF 9,55
M 131
Q 14
CV 6,6,7
M 130
TY 98,12800
TY 102,26624
ST 16,768
M 0
R 4
CV 7,8,9
M 1
WL 8
Q 16
AW 238,6
M 13
Q 18
AW 239,7
M 12
Q 20
BV 52,3584
M 14
Q 22
M 15
Q 24
M 3
Q 26
M 2
Q 28
M 4
Q 30
M 5
WL 5
Q 32
M 17
Q 34
M 16
Q 36
M 18
Q 38
ST 18,1536
M 19
WL 1
Q 40
M 7
Q 42
M 6
Q 44
v_mfma_f32_16x16x32_f16 a[52:55], v[88:91], a[0:3], a[32:35]
Q 46
M 9
AR 6,148
AR 7,172
v_mfma_f32_16x16x32_f16 a[76:79], v[92:95], a[12:15], a[32:35]
AR 8,196
EF 6,12
M 20
EF 7,13
EF 8,46
v_mfma_f32_16x16x32_f16 a[80:83], v[96:99], a[8:11], a[32:35]
EF 152,47
CV 38,6,7
M 23
R 10
v_mfma_f32_16x16x32_f16 a[56:59], v[100:103], a[4:7], a[32:35]
CV 39,8,152
AR 6,149
M 10
R 8
v_mfma_f32_16x16x32_f16 a[104:107], v[96:99], a[16:19], a[32:35]
AR 7,173
AR 8,197
M 35
ST 20,2304
EF 6,12
v_mfma_f32_16x16x32_f16 a[100:103], v[88:91], a[16:19], a[32:35]
EF 7,13
EF 8,46
M 33
EF 153,47
CV 40,6,7
M 30
CV 41,8,153
AR 6,150
M 31
AR 7,174
AR 8,198
M 28
EF 6,12
EF 7,13
M 29
EF 8,46
EF 154,47
M 26
CV 42,6,7
CV 43,8,154
M 27
WV 11
AR 6,151
AR 7,175
M 24
WL 0
FP
AR 8,199
M 25
EF 6,12
EF 7,13
M 36
EF 8,46
EF 155,47
M 37
CV 44,6,7
CV 45,8,155
M 38
ae 42,72,0
ST 22,128
AR 6,152
AR 7,176
M 39
EF 6,12
EF 7,13
EF 132,46
EF 156,47
M 40
CV 14,6,7
CV 15,132,156
AR 6,153
AR 7,177
M 41
EF 6,12
EF 7,13
EF 133,46
EF 157,47
M 42
CV 16,6,7
CV 17,133,157
AR 6,154
AR 7,178
M 43
EF 6,12
EF 7,13
EF 134,46
EF 158,47
v_mfma_f32_16x16x32_f16 a[124:127], v[88:91], a[24:27], a[32:35]
CV 18,6,7
CV 19,134,158
AR 6,155
AR 7,179
M 45
FR
EF 6,12
EF 7,13
EF 135,46
v_mfma_f32_16x16x32_f16 a[128:131], v[96:99], a[24:27], a[32:35]
EF 159,47
CV 20,6,7
CV 21,135,159
M 47
EF 176,52
EF 200,53
v_mfma_f32_16x16x32_f16 a[152:155], v[96:99], a[0:3], a[32:35]
EF 224,54
EF 248,55
M 59
CV 6,176,200
CV 7,224,248
v_mfma_f32_16x16x32_f16 a[148:151], v[88:91], a[0:3], a[32:35]
AW 240,6
AW 241,7
M 57
ST 24,896
EF 177,52
M 54
EF 201,53
EF 225,54
M 55
WL 0
FQ 88
)"
R"(EF 249,55
M 52
CV 6,177,201
CV 7,225,249
M 53
AW 242,6
AW 243,7
M 50
FZ 88
R 24
M 51
mw 72
R 26
M 48
TY 106,520
TY 110,14344
M 49
R 28
M 60
ad 84,336
W 0
M 61
EF 178,52
EF 202,53
M 62
EF 226,54
EF 250,55
M 63
W 1
CV 6,178,202
M 64
ST 26,1664
CV 7,226,250
M 65
AW 244,6
AW 245,7
M 66
W 2
EF 179,52
M 67
WL 3
AN 104,106
AN 105,107
v_mfma_f32_16x16x32_f16 a[172:175], v[88:91], a[8:11], a[32:35]
WL 2
SH 106
AN 106,106
M 69
mw 75
AN 107,107
AN 108,110
v_mfma_f32_16x16x32_f16 a[176:179], v[96:99], a[8:11], a[32:35]
AN 109,111
SH 110
M 71
R 30
v_mfma_f32_16x16x32_f16 v[132:135], v[96:99], a[16:19], v[128:131]
AN 110,110
AN 111,111
M 83
TY 114,2056
TY 118,15880
v_mfma_f32_16x16x32_f16 a[196:199], v[88:91], a[16:19], a[32:35]
ad 84,342
W 0
M 81
EF 203,53
EF 227,54
M 78
EF 251,55
CV 6,179,203
M 79
W 1
ST 28,2432
M 76
CV 7,227,251
AW 246,6
M 77
AW 247,7
M 74
W 2
EF 180,52
EF 204,53
EF 228,54
M 75
EF 252,55
CV 6,180,204
CV 7,228,252
AW 248,6
M 72
WL 1
AN 112,114
AN 113,115
AW 249,7
EF 181,52
M 73
R 32
EF 205,53
EF 229,54
M 84
mw 0
SH 114
AN 114,114
EF 253,55
CV 6,181,205
M 85
WL 2
AN 115,115
AN 116,118
CV 7,229,253
AW 250,6
M 86
AN 117,119
SH 118
AW 251,7
EF 182,52
M 87
AN 118,118
AN 119,119
EF 206,53
EF 230,54
M 88
TY 122,3592
TY 126,17416
EF 254,55
CV 6,182,206
M 89
ad 84,384
W 0
CV 7,230,254
AW 252,6
M 90
ae 42,72,0
AW 253,7
EF 183,52
EF 207,53
M 91
s_bfe_u32 s52, s50, 0x10003
v_mad_i32_i24 v50, v49, s52, v48
ST 30,256
EF 231,54
EF 255,55
v_mfma_f32_16x16x32_f16 v[152:155], v[88:91], a[24:27], v[128:131]
W 1
CV 6,183,207
CV 7,231,255
AW 254,6
M 93
AW 255,7
v_mfma_f32_16x16x32_f16 v[156:159], v[96:99], a[24:27], v[128:131]
M 95
R 34
v_mfma_f32_16x16x32_f16 v[180:183], v[96:99], a[0:3], v[128:131]
W 2
M 107
WL 3
AN 120,122
AN 121,123
v_mfma_f32_16x16x32_f16 v[176:179], v[88:91], a[0:3], v[128:131]
WL 2
SH 122
AN 122,122
M 105
mw 3
AN 123,123
AN 124,126
M 102
AN 125,127
SH 126
M 103
AN 126,126
AN 127,127
M 100
ad 84,390
W 0
M 101
M 98
ST 32,1024
M 99
W 1
M 96
M 97
R 48
M 108
W 2
M 109
M 110
M 111
bc 49,5,66,60
M 112
mh 18
M 113
ad 66,992
H 0
M 114
M 115
v_mfma_f32_16x16x32_f16 v[200:203], v[88:91], a[8:11], v[128:131]
DG 1024
M 117
ST 34,1792
v_mfma_f32_16x16x32_f16 v[204:207], v[96:99], a[8:11], v[128:131]
M 119
WL 2
R 50
M 120
bc 48,5,64,56
M 121
mh 21
M 133
ad 64,992
H 0
M 132
TX 58,0
TX 62,13824
M 134
M 135
H 1
M 123
M 122
TY 66,512
TY 70,14336
M 124
H 2
M 125
M 137
ST 36,2560
M 136
TX 74,1536
TX 78,15360
M 138
WL 4
Q 0
M 139
Q 2
M 127
Q 4
M 126
TY 82,2048
TY 86,15872
v_mfma_f32_16x16x32_f16 v[224:227], v[88:91], a[16:19], v[128:131]
Q 6
M 129
WL 4
Q 8
v_mfma_f32_16x16x32_f16 v[248:251], v[92:95], a[28:31], v[128:131]
Q 10
M 140
TX 90,3072
TX 94,16896
v_mfma_f32_16x16x32_f16 v[252:255], v[96:99], a[24:27], v[128:131]
Q 12
M 143
R 54
v_mfma_f32_16x16x32_f16 v[228:231], v[100:103], a[20:23], v[128:131]
Q 14
M 130
TY 98,3584
TY 102,17408
M 0
R 52
M 1
WL 10
Q 16
M 13
Q 18
M 12
Q 20
M 14
Q 22
M 15
WL 8
Q 24
M 3
Q 26
M 2
Q 28
M 4
Q 30
M 5
WL 6
Q 32
M 17
Q 34
M 16
Q 36
M 18
Q 38
M 19
WL 2
Q 40
M 7
Q 42
M 6
Q 44
M 8
Q 46
M 9
ae 42,72,96
M 21
s_bitcmp1_b32 s50, 4
s_cselect_b32 s52, 24576, 0
v_add_u32 v50, s52, v3
ST 38,128
M 20
M 22
M 23
R 58
M 11
M 10
WL 2
R 56
M 34
M 35
M 32
M 33
M 30
M 31
M 28
M 29
ST 40,896
M 26
M 27
WV 9
M 24
WL 0
FP
M 25
M 36
M 37
M 38
M 39
M 40
M 41
M 42
M 43
M 44
ST 42,1664
M 45
FR
M 46
M 47
M 58
M 59
M 56
M 57
M 54
M 55
WL 0
FQ 92
M 52
M 53
M 50
FZ 92
R 72
M 51
mw 24
R 74
ST 44,2432
M 48
TY 106,5128
TY 110,18952
M 49
R 76
M 60
ad 84,432
W 0
M 61
M 62
M 63
W 1
M 64
M 65
M 66
W 2
M 67
WL 3
AN 104,106
AN 105,107
M 68
WL 2
SH 106
AN 106,106
M 69
mw 27
AN 107,107
AN 108,110
M 70
AN 109,111
SH 110
M 71
R 78
M 82
AN 110,110
AN 111,111
M 83
TY 114,6664
TY 118,20488
M 80
ad 84,438
W 0
)"
R"(M 81
ae 42,72,96
M 78
s_bfe_u32 s52, s50, 0x10005
v_mad_i32_i24 v50, v49, s52, v48
ST 14,128
M 79
W 1
M 76
M 77
M 74
W 2
M 75
M 72
WL 1
AN 112,114
AN 113,115
M 73
R 80
M 84
mw 48
SH 114
AN 114,114
M 85
WL 2
AN 115,115
AN 116,118
M 86
AN 117,119
SH 118
M 87
AN 118,118
AN 119,119
M 88
TY 122,8200
TY 126,22024
M 89
ad 84,480
W 0
ST 16,896
M 90
M 91
M 92
W 1
M 93
M 94
M 95
R 82
M 106
W 2
M 107
WL 3
AN 120,122
AN 121,123
M 104
WL 2
SH 122
AN 122,122
M 105
mw 51
AN 123,123
AN 124,126
M 102
AN 125,127
SH 126
M 103
AN 126,126
AN 127,127
M 100
ad 84,486
W 0
ST 18,1664
M 101
M 98
M 99
W 1
M 96
M 97
R 0
M 108
W 2
M 109
M 110
M 111
bc 49,6,66,60
M 112
mh 0
M 113
ad 66,1184
H 0
M 114
M 115
ST 20,2432
M 116
DG 1024
M 117
M 118
M 119
WL 2
R 2
M 120
bc 48,6,64,56
M 121
mh 3
M 133
ad 64,1184
H 0
M 132
TX 58,4608
TX 62,18432
M 134
M 135
H 1
M 123
M 122
TY 66,5120
TY 70,18944
M 124
H 2
M 125
ae 44,70,384
M 137
s_bitcmp1_b32 s50, 6
s_cselect_b32 s51, 24576, 0
v_add_u32 v47, s51, v3
M 136
TX 74,6144
TX 78,19968
PA 12288
DF
M 138
WL 4
Q 0
M 139
Q 2
M 127
Q 4
M 126
TY 82,6656
TY 86,20480
M 128
Q 6
M 129
WL 4
Q 8
M 141
Q 10
M 140
TX 90,7680
TX 94,21504
M 142
Q 12
M 143
R 6
M 131
Q 14
M 130
TY 98,8192
TY 102,22016
M 0
R 4
M 1
WL 10
Q 16
M 13
Q 18
M 12
Q 20
M 14
Q 22
M 15
WL 8
Q 24
M 3
Q 26
M 2
Q 28
M 4
Q 30
M 5
WL 6
Q 32
M 17
Q 34
M 16
Q 36
M 18
Q 38
M 19
WL 2
Q 40
M 7
Q 42
M 6
Q 44
M 8
Q 46
M 9
XL 22,0
M 21
XL 24,768
M 20
XL 26,1536
M 22
XL 28,2304
M 23
R 10
M 11
M 10
WL 2
R 8
M 34
M 35
M 32
M 33
M 30
M 31
M 28
M 29
M 26
M 27
WV 11
M 24
WL 0
FP
M 25
M 36
ae 42,72,0
M 37
s_bfe_u32 s52, s50, 0x10003
v_mad_i32_i24 v50, v49, s52, v48
SA 200,0
M 38
M 39
M 40
M 41
M 42
M 43
M 44
M 45
FR
M 46
M 47
M 58
M 59
M 56
M 57
M 54
M 55
WL 0
FQ 96
M 52
M 53
M 50
FZ 96
R 24
M 51
mw 72
R 26
M 48
TY 106,9736
TY 110,23560
M 49
R 28
M 60
ad 84,528
W 0
M 61
M 62
M 63
W 1
M 64
M 65
M 66
W 2
M 67
WL 3
AN 104,106
AN 105,107
M 68
WL 2
SH 106
AN 106,106
M 69
mw 75
AN 107,107
AN 108,110
M 70
AN 109,111
SH 110
M 71
R 30
M 82
AN 110,110
AN 111,111
M 83
TY 114,11272
TY 118,25096
M 80
ad 84,534
W 0
M 81
M 78
M 79
W 1
M 76
M 77
M 74
W 2
M 75
M 72
WL 1
AN 112,114
AN 113,115
M 73
R 32
M 84
mw 0
SH 114
AN 114,114
M 85
WL 2
AN 115,115
AN 116,118
M 86
AN 117,119
SH 118
M 87
AN 118,118
AN 119,119
M 88
TY 122,12808
TY 126,26632
M 89
ad 84,576
W 0
M 90
M 91
M 92
W 1
M 93
M 94
M 95
R 34
M 106
W 2
M 107
WL 3
AN 120,122
AN 121,123
M 104
WL 2
SH 122
AN 122,122
M 105
mw 3
AN 123,123
AN 124,126
M 102
AN 125,127
SH 126
M 103
AN 126,126
AN 127,127
M 100
ad 84,582
W 0
M 101
SA 202,768
M 98
M 99
W 1
M 96
M 97
R 48
M 108
W 2
M 109
M 110
M 111
bc 49,7,66,60
M 112
mh 9
M 113
ad 66,1376
H 0
M 114
M 115
M 116
DG 1024
M 117
M 118
M 119
WL 2
R 50
M 120
bc 48,7,64,56
M 121
mh 12
M 133
)"
R"(ad 64,1376
H 0
M 132
TX 58,9216
TX 62,23040
M 134
M 135
H 1
M 123
M 122
TY 66,9728
TY 70,23552
M 124
H 2
M 125
M 137
ae 44,70,384
M 136
TX 74,10752
TX 78,24576
M 138
s_bfe_u32 s51, s50, 0x10007
WL 4
Q 0
M 139
Q 2
M 127
Q 4
M 126
TY 82,11264
TY 86,25088
M 128
Q 6
M 129
WL 4
Q 8
M 141
Q 10
M 140
TX 90,12288
TX 94,26112
M 142
Q 12
M 143
R 54
M 131
Q 14
M 130
TY 98,12800
TY 102,26624
M 0
R 52
M 1
WL 10
Q 16
M 13
Q 18
M 12
Q 20
M 14
Q 22
M 15
WL 8
Q 24
M 3
Q 26
M 2
Q 28
M 4
Q 30
M 5
WL 6
Q 32
M 17
Q 34
M 16
Q 36
M 18
Q 38
M 19
WL 2
Q 40
M 7
Q 42
M 6
Q 44
M 8
Q 46
M 9
v_mad_i32_i24 v47, v49, s51, v48
XL 30,0
M 21
XL 32,768
M 20
XL 34,1536
M 22
XL 36,2304
M 23
R 58
M 11
M 10
WL 2
R 56
M 34
SA 204,1536
M 35
M 32
M 33
M 30
M 31
M 28
M 29
M 26
M 27
WV 10
M 24
WL 0
FP
M 25
M 36
M 37
M 38
M 39
M 40
UL 6,22
UH 7,22
M 41
AR 8,36
AR 9,60
M 42
AD 8,6
AD 9,7
M 43
AW 36,8
AW 60,9
M 44
UL 6,23
UH 7,23
M 45
FR
AR 8,84
M 46
AR 9,108
AD 8,6
M 47
AD 9,7
AW 84,8
M 58
AW 108,9
UL 6,24
M 59
UH 7,24
AR 8,37
M 56
AR 9,61
AD 8,6
M 57
AD 9,7
AW 37,8
M 54
AW 61,9
UL 6,25
M 55
WL 0
FQ 100
UH 7,25
M 52
AR 8,85
AR 9,109
M 53
AD 8,6
AD 9,7
M 50
FZ 100
R 72
M 51
mw 24
R 74
M 48
TY 106,520
TY 110,14344
M 49
R 76
M 60
ad 84,624
W 0
M 61
AW 85,8
AW 109,9
M 62
UL 6,26
UH 7,26
M 63
W 1
AR 8,38
M 64
AR 9,62
AD 8,6
M 65
AD 9,7
AW 38,8
M 66
W 2
AW 62,9
M 67
WL 3
AN 104,106
AN 105,107
M 68
WL 2
SH 106
AN 106,106
M 69
mw 27
AN 107,107
AN 108,110
M 70
AN 109,111
SH 110
M 71
R 78
M 82
AN 110,110
AN 111,111
M 83
TY 114,2056
TY 118,15880
M 80
ad 84,630
W 0
M 81
UL 6,27
UH 7,27
M 78
AR 8,86
AR 9,110
M 79
W 1
AD 8,6
M 76
AD 9,7
AW 86,8
M 77
AW 110,9
UL 6,28
M 74
W 2
UH 7,28
M 75
AR 8,39
AR 9,63
M 72
WL 1
AN 112,114
AN 113,115
M 73
R 80
M 84
mw 48
SH 114
AN 114,114
M 85
WL 2
AN 115,115
AN 116,118
M 86
AN 117,119
SH 118
M 87
AN 118,118
AN 119,119
M 88
TY 122,3592
TY 126,17416
M 89
ad 84,672
W 0
M 90
SA 206,2304
AD 8,6
M 91
AD 9,7
AW 39,8
M 92
W 1
AW 63,9
M 93
UL 6,29
UH 7,29
M 94
AR 8,87
AR 9,111
M 95
R 82
M 106
W 2
AD 8,6
M 107
WL 3
AN 120,122
AN 121,123
M 104
WL 2
SH 122
AN 122,122
M 105
mw 51
AN 123,123
AN 124,126
M 102
AN 125,127
SH 126
M 103
AN 126,126
AN 127,127
M 100
ad 84,678
W 0
M 101
AD 9,7
AW 87,8
M 98
AW 111,9
M 99
W 1
M 96
M 97
R 0
M 108
W 2
M 109
M 110
M 111
bc 49,8,66,60
M 112
mh 18
M 113
ad 66,1568
H 0
M 114
M 115
M 116
DG 1024
M 117
M 118
M 119
WL 2
R 2
M 120
bc 48,8,64,56
M 121
mh 21
M 133
ad 64,1568
H 0
M 132
TX 58,0
TX 62,13824
M 134
M 135
H 1
M 123
M 122
TY 66,512
TY 70,14336
M 124
H 2
M 125
M 137
M 136
TX 74,1536
TX 78,15360
PA 12288
DF
M 138
WL 4
Q 0
M 139
Q 2
M 127
Q 4
M 126
TY 82,2048
TY 86,15872
M 128
Q 6
M 129
WL 4
Q 8
M 141
Q 10
M 140
TX 90,3072
TX 94,16896
M 142
Q 12
M 143
R 6
M 131
Q 14
)"
R"(M 130
TY 98,3584
TY 102,17408
M 0
R 4
M 1
WL 10
Q 16
M 13
Q 18
M 12
Q 20
M 14
Q 22
M 15
WL 8
Q 24
M 3
Q 26
M 2
Q 28
M 4
Q 30
M 5
WL 6
Q 32
M 17
Q 34
M 16
Q 36
M 18
Q 38
M 19
WL 2
Q 40
M 7
Q 42
M 6
Q 44
M 8
Q 46
M 9
ae 42,72,0
M 21
SA 208,128
M 20
ae 44,68,384
M 22
s_bitcmp1_b32 s50, 8
s_cselect_b32 s51, 24576, 0
v_add_u32 v47, s51, v3
M 23
R 10
M 11
XL 38,0
M 10
WL 2
R 8
M 34
XL 40,768
M 35
XL 42,1536
M 32
XL 44,2304
M 33
M 30
M 31
M 28
M 29
M 26
M 27
WV 11
M 24
WL 0
FP
M 25
M 36
M 37
M 38
M 39
M 40
M 41
M 42
UL 6,30
UH 7,30
M 43
AR 8,40
AR 9,64
M 44
AD 8,6
AD 9,7
M 45
FR
AW 40,8
M 46
AW 64,9
UL 6,31
M 47
UH 7,31
AR 8,88
M 58
AR 9,112
AD 8,6
M 59
AD 9,7
AW 88,8
M 56
AW 112,9
UL 6,32
M 57
UH 7,32
AR 8,41
M 54
AR 9,65
AD 8,6
M 55
WL 0
FQ 104
AD 9,7
M 52
AW 41,8
AW 65,9
M 53
UL 6,33
UH 7,33
M 50
FZ 104
R 24
M 51
mw 72
R 26
M 48
TY 106,5128
TY 110,18952
M 49
R 28
M 60
ad 84,720
W 0
M 61
AR 8,89
AR 9,113
M 62
AD 8,6
AD 9,7
M 63
W 1
AW 89,8
M 64
AW 113,9
UL 6,34
M 65
UH 7,34
AR 8,42
M 66
W 2
AR 9,66
M 67
WL 3
AN 104,106
AN 105,107
M 68
WL 2
SH 106
AN 106,106
M 69
mw 75
AN 107,107
AN 108,110
M 70
AN 109,111
SH 110
M 71
R 30
M 82
AN 110,110
AN 111,111
M 83
TY 114,6664
TY 118,20488
M 80
ad 84,726
W 0
M 81
AD 8,6
AD 9,7
M 78
AW 42,8
AW 66,9
M 79
W 1
UL 6,35
M 76
UH 7,35
AR 8,90
M 77
AR 9,114
AD 8,6
M 74
W 2
AD 9,7
M 75
AW 90,8
AW 114,9
M 72
WL 1
AN 112,114
AN 113,115
M 73
R 32
M 84
mw 0
SH 114
AN 114,114
M 85
WL 2
AN 115,115
AN 116,118
M 86
AN 117,119
SH 118
M 87
AN 118,118
AN 119,119
M 88
TY 122,8200
TY 126,22024
M 89
ad 84,768
W 0
M 90
SA 210,896
UL 6,36
M 91
UH 7,36
AR 8,43
M 92
W 1
AR 9,67
M 93
AD 8,6
AD 9,7
M 94
AW 43,8
AW 67,9
M 95
R 34
M 106
W 2
UL 6,37
M 107
WL 3
AN 120,122
AN 121,123
M 104
WL 2
SH 122
AN 122,122
M 105
mw 3
AN 123,123
AN 124,126
M 102
AN 125,127
SH 126
M 103
AN 126,126
AN 127,127
M 100
ad 84,774
W 0
M 101
UH 7,37
AR 8,91
M 98
AR 9,115
AD 8,6
M 99
W 1
AD 9,7
M 96
AW 91,8
AW 115,9
M 97
R 48
M 108
W 2
M 109
M 110
M 111
bc 49,9,66,60
M 112
mh 0
M 113
ad 66,1760
H 0
M 114
M 115
M 116
DG 1024
M 117
M 118
M 119
WL 2
R 50
M 120
bc 48,9,64,56
M 121
mh 3
M 133
ad 64,1760
H 0
M 132
TX 58,4608
TX 62,18432
M 134
M 135
H 1
M 123
M 122
TY 66,5120
TY 70,18944
M 124
H 2
M 125
M 137
M 136
TX 74,6144
TX 78,19968
M 138
WL 4
Q 0
M 139
Q 2
M 127
Q 4
M 126
TY 82,6656
TY 86,20480
M 128
Q 6
M 129
WL 4
Q 8
M 141
Q 10
M 140
TX 90,7680
TX 94,21504
M 142
Q 12
M 143
R 54
M 131
Q 14
M 130
TY 98,8192
TY 102,22016
M 0
R 52
M 1
WL 10
Q 16
M 13
Q 18
M 12
Q 20
M 14
Q 22
M 15
WL 8
Q 24
M 3
Q 26
M 2
Q 28
M 4
Q 30
M 5
WL 6
Q 32
M 17
Q 34
M 16
Q 36
M 18
Q 38
M 19
WL 2
Q 40
M 7
Q 42
M 6
Q 44
M 8
Q 46
M 9
SA 212,1664
M 21
ae 44,68,384
)"
R"(M 20
s_bfe_u32 s51, s50, 0x10009
v_mad_i32_i24 v47, v49, s51, v48
XL 14,0
M 22
XL 16,768
M 23
R 58
M 11
XL 18,1536
M 10
WL 2
R 56
M 34
XL 20,2304
M 35
M 32
M 33
M 30
M 31
M 28
M 29
M 26
M 27
WV 10
M 24
WL 0
FP
M 25
M 36
M 37
M 38
M 39
M 40
M 41
M 42
M 43
M 44
UL 6,38
UH 7,38
M 45
FR
AR 8,44
M 46
AR 9,68
AD 8,6
M 47
AD 9,7
AW 44,8
M 58
AW 68,9
UL 6,39
M 59
UH 7,39
AR 8,92
M 56
AR 9,116
AD 8,6
M 57
AD 9,7
AW 92,8
M 54
AW 116,9
UL 6,40
M 55
WL 0
FQ 108
UH 7,40
M 52
AR 8,45
AR 9,69
M 53
AD 8,6
AD 9,7
M 50
FZ 108
R 72
M 51
mw 24
R 74
M 48
TY 106,9736
TY 110,23560
M 49
R 76
M 60
ad 84,816
W 0
M 61
AW 45,8
AW 69,9
M 62
UL 6,41
UH 7,41
M 63
W 1
AR 8,93
M 64
AR 9,117
AD 8,6
M 65
AD 9,7
AW 93,8
M 66
W 2
AW 117,9
M 67
WL 3
AN 104,106
AN 105,107
M 68
WL 2
SH 106
AN 106,106
M 69
mw 27
AN 107,107
AN 108,110
M 70
AN 109,111
SH 110
M 71
R 78
M 82
AN 110,110
AN 111,111
M 83
TY 114,11272
TY 118,25096
M 80
ad 84,822
W 0
M 81
UL 6,42
UH 7,42
M 78
AR 8,46
AR 9,70
M 79
W 1
AD 8,6
M 76
AD 9,7
AW 46,8
M 77
AW 70,9
UL 6,43
M 74
W 2
UH 7,43
M 75
SA 214,2432
AR 8,94
M 72
WL 1
AN 112,114
AN 113,115
M 73
R 80
M 84
mw 48
SH 114
AN 114,114
M 85
WL 2
AN 115,115
AN 116,118
M 86
AN 117,119
SH 118
M 87
AN 118,118
AN 119,119
M 88
TY 122,12808
TY 126,26632
M 89
ad 84,864
W 0
M 90
AR 9,118
AD 8,6
M 91
AD 9,7
AW 94,8
M 92
W 1
AW 118,9
M 93
UL 6,44
UH 7,44
M 94
AR 8,47
AR 9,71
M 95
R 82
M 106
W 2
AD 8,6
M 107
WL 3
AN 120,122
AN 121,123
M 104
WL 2
SH 122
AN 122,122
M 105
mw 51
AN 123,123
AN 124,126
M 102
AN 125,127
SH 126
M 103
AN 126,126
AN 127,127
M 100
ad 84,870
W 0
M 101
AD 9,7
AW 47,8
M 98
AW 71,9
UL 6,45
M 99
W 1
UH 7,45
M 96
AR 8,95
AR 9,119
M 97
R 0
M 108
W 2
AD 8,6
M 109
AD 9,7
AW 95,8
M 110
AW 119,9
M 111
bc 49,10,66,60
M 112
mh 9
M 113
ad 66,1952
H 0
M 114
M 115
M 116
DG 1024
M 117
M 118
M 119
WL 2
R 2
M 120
bc 48,10,64,56
M 121
mh 12
M 133
ad 64,1952
H 0
M 132
TX 58,9216
TX 62,23040
M 134
M 135
H 1
M 123
M 122
TY 66,9728
TY 70,23552
M 124
H 2
M 125
M 137
M 136
TX 74,10752
TX 78,24576
PA 12288
DF
M 138
WL 4
Q 0
M 139
Q 2
M 127
Q 4
M 126
TY 82,11264
TY 86,25088
M 128
Q 6
M 129
WL 4
Q 8
M 141
Q 10
M 140
TX 90,12288
TX 94,26112
M 142
Q 12
M 143
R 6
M 131
Q 14
M 130
TY 98,12800
TY 102,26624
M 0
R 4
M 1
WL 10
Q 16
M 13
Q 18
M 12
Q 20
M 14
Q 22
M 15
WL 8
Q 24
M 3
Q 26
M 2
Q 28
M 4
Q 30
M 5
WL 6
Q 32
M 17
Q 34
M 16
Q 36
M 18
Q 38
M 19
WL 2
Q 40
M 7
Q 42
M 6
Q 44
M 8
Q 46
M 9
ae 42,72,0
M 21
s_bitcmp1_b32 s50, 2
s_cselect_b32 s52, 24576, 0
v_add_u32 v50, s52, v3
M 20
SA 216,256
M 22
ae 44,68,480
M 23
R 10
M 11
s_bitcmp1_b32 s50, 10
s_cselect_b32 s51, 24576, 0
v_add_u32 v47, s51, v3
M 10
WL 2
R 8
M 34
XL 22,0
M 35
XL 24,768
M 32
XL 26,1536
M 33
XL 28,2304
M 30
M 31
M 28
M 29
M 26
M 27
WV 11
M 24
WL 0
FP
M 25
M 36
M 37
M 38
M 39
M 40
M 41
M 42
M 43
M 44
M 45
FR
)"
R"(M 46
UL 6,14
UH 7,14
M 47
AR 8,48
AR 9,72
M 58
AD 8,6
AD 9,7
M 59
AW 48,8
AW 72,9
M 56
UL 6,15
UH 7,15
M 57
AR 8,96
AR 9,120
M 54
AD 8,6
AD 9,7
M 55
WL 0
FQ 112
AW 96,8
M 52
AW 120,9
UL 6,16
M 53
UH 7,16
AR 8,49
M 50
FZ 112
R 24
M 51
mw 72
R 26
M 48
TY 106,520
TY 110,14344
M 49
R 28
M 60
ad 84,912
W 0
M 61
AR 9,73
AD 8,6
M 62
AD 9,7
AW 49,8
M 63
W 1
AW 73,9
M 64
UL 6,17
UH 7,17
M 65
AR 8,97
AR 9,121
M 66
W 2
AD 8,6
M 67
WL 3
AN 104,106
AN 105,107
M 68
WL 2
SH 106
AN 106,106
M 69
mw 75
AN 107,107
AN 108,110
M 70
AN 109,111
SH 110
M 71
R 30
M 82
AN 110,110
AN 111,111
M 83
TY 114,2056
TY 118,15880
M 80
ad 84,918
W 0
M 81
AD 9,7
AW 97,8
M 78
AW 121,9
UL 6,18
M 79
W 1
UH 7,18
M 76
AR 8,50
AR 9,74
M 77
AD 8,6
AD 9,7
M 74
W 2
AW 50,8
M 75
AW 74,9
UL 6,19
M 72
WL 1
AN 112,114
AN 113,115
M 73
R 32
M 84
mw 0
SH 114
AN 114,114
M 85
WL 2
AN 115,115
AN 116,118
M 86
AN 117,119
SH 118
M 87
AN 118,118
AN 119,119
M 88
TY 122,3592
TY 126,17416
M 89
ad 84,960
W 0
M 90
SA 218,1024
UH 7,19
M 91
AR 8,98
AR 9,122
M 92
W 1
AD 8,6
M 93
AD 9,7
AW 98,8
M 94
AW 122,9
UL 6,20
M 95
R 34
M 106
W 2
UH 7,20
M 107
WL 3
AN 120,122
AN 121,123
M 104
WL 2
SH 122
AN 122,122
M 105
mw 3
AN 123,123
AN 124,126
M 102
AN 125,127
SH 126
M 103
AN 126,126
AN 127,127
M 100
ad 84,966
W 0
M 101
AR 8,51
AR 9,75
M 98
AD 8,6
AD 9,7
M 99
W 1
AW 51,8
M 96
AW 75,9
UL 6,21
M 97
R 48
M 108
W 2
UH 7,21
M 109
AR 8,99
AR 9,123
M 110
AD 8,6
AD 9,7
M 111
bc 49,11,66,60
AW 99,8
M 112
mh 18
AW 123,9
M 113
ad 66,2144
H 0
M 114
M 115
M 116
DG 1024
M 117
M 118
M 119
WL 2
R 50
M 120
bc 48,11,64,56
M 121
mh 21
M 133
ad 64,2144
H 0
M 132
TX 58,0
TX 62,13824
M 134
M 135
H 1
M 123
M 122
TY 66,512
TY 70,14336
M 124
H 2
M 125
M 137
M 136
TX 74,1536
TX 78,15360
M 138
WL 4
Q 0
M 139
Q 2
M 127
Q 4
M 126
TY 82,2048
TY 86,15872
M 128
Q 6
M 129
WL 4
Q 8
M 141
Q 10
M 140
TX 90,3072
TX 94,16896
M 142
Q 12
M 143
R 54
M 131
Q 14
M 130
TY 98,3584
TY 102,17408
M 0
R 52
M 1
WL 10
Q 16
M 13
Q 18
M 12
Q 20
M 14
Q 22
M 15
WL 8
Q 24
M 3
Q 26
M 2
Q 28
M 4
Q 30
M 5
WL 6
Q 32
M 17
Q 34
M 16
Q 36
M 18
Q 38
M 19
WL 2
Q 40
M 7
Q 42
M 6
Q 44
M 8
Q 46
M 9
SA 220,1792
M 21
ae 44,68,480
M 20
s_bfe_u32 s51, s50, 0x1000b
v_mad_i32_i24 v47, v49, s51, v48
XL 30,0
M 22
XL 32,768
M 23
R 58
M 11
XL 34,1536
M 10
WL 2
R 56
M 34
XL 36,2304
M 35
M 32
M 33
M 30
M 31
M 28
M 29
M 26
M 27
WV 10
M 24
WL 0
FP
M 25
M 36
M 37
M 38
M 39
M 40
M 41
M 42
M 43
M 44
M 45
FR
M 46
M 47
M 58
UL 6,22
UH 7,22
M 59
AR 8,52
AR 9,76
M 56
AD 8,6
AD 9,7
M 57
AW 52,8
AW 76,9
M 54
UL 6,23
UH 7,23
M 55
WL 0
FQ 116
AR 8,100
M 52
AR 9,124
AD 8,6
M 53
AD 9,7
AW 100,8
M 50
FZ 116
R 72
M 51
mw 24
R 74
M 48
TY 106,5128
TY 110,18952
M 49
R 76
M 60
ad 84,1008
W 0
M 61
AW 124,9
UL 6,24
M 62
UH 7,24
AR 8,53
)"
R"(M 63
W 1
AR 9,77
M 64
AD 8,6
AD 9,7
M 65
AW 53,8
AW 77,9
M 66
W 2
UL 6,25
M 67
WL 3
AN 104,106
AN 105,107
M 68
WL 2
SH 106
AN 106,106
M 69
mw 27
AN 107,107
AN 108,110
M 70
AN 109,111
SH 110
M 71
R 78
M 82
AN 110,110
AN 111,111
M 83
TY 114,6664
TY 118,20488
M 80
ad 84,1014
W 0
M 81
UH 7,25
AR 8,101
M 78
AR 9,125
AD 8,6
M 79
W 1
AD 9,7
M 76
AW 101,8
AW 125,9
M 77
UL 6,26
UH 7,26
M 74
W 2
AR 8,54
M 75
SA 222,2560
AR 9,78
M 72
WL 1
AN 112,114
AN 113,115
M 73
R 80
M 84
mw 48
SH 114
AN 114,114
M 85
WL 2
AN 115,115
AN 116,118
M 86
AN 117,119
SH 118
M 87
AN 118,118
AN 119,119
M 88
TY 122,8200
TY 126,22024
M 89
ad 84,1056
W 0
M 90
AD 8,6
AD 9,7
M 91
AW 54,8
AW 78,9
M 92
W 1
UL 6,27
M 93
UH 7,27
AR 8,102
M 94
AR 9,126
AD 8,6
M 95
R 82
M 106
W 2
AD 9,7
M 107
WL 3
AN 120,122
AN 121,123
M 104
WL 2
SH 122
AN 122,122
M 105
mw 51
AN 123,123
AN 124,126
M 102
AN 125,127
SH 126
M 103
AN 126,126
AN 127,127
M 100
ad 84,1062
W 0
M 101
AW 102,8
AW 126,9
M 98
UL 6,28
UH 7,28
M 99
W 1
AR 8,55
M 96
AR 9,79
AD 8,6
M 97
R 0
M 108
W 2
AD 9,7
M 109
AW 55,8
AW 79,9
M 110
UL 6,29
UH 7,29
M 111
bc 49,12,66,60
AR 8,103
M 112
mh 0
AR 9,127
AD 8,6
M 113
ad 66,2336
H 0
M 114
AD 9,7
AW 103,8
M 115
AW 127,9
M 116
DG 1024
M 117
M 118
M 119
WL 2
R 2
M 120
bc 48,12,64,56
M 121
mh 3
M 133
ad 64,2336
H 0
M 132
TX 58,4608
TX 62,18432
M 134
M 135
H 1
M 123
M 122
TY 66,5120
TY 70,18944
M 124
H 2
M 125
M 137
M 136
TX 74,6144
TX 78,19968
PA 12288
DF
M 138
WL 4
Q 0
M 139
Q 2
M 127
Q 4
M 126
TY 82,6656
TY 86,20480
M 128
Q 6
M 129
WL 4
Q 8
M 141
Q 10
M 140
TX 90,7680
TX 94,21504
M 142
Q 12
M 143
R 6
M 131
Q 14
M 130
TY 98,8192
TY 102,22016
M 0
R 4
M 1
WL 10
Q 16
M 13
Q 18
M 12
Q 20
M 14
Q 22
M 15
WL 8
Q 24
M 3
Q 26
M 2
Q 28
M 4
Q 30
M 5
WL 6
Q 32
M 17
Q 34
M 16
Q 36
M 18
Q 38
M 19
WL 2
Q 40
M 7
Q 42
M 6
Q 44
M 8
Q 46
M 9
ae 42,72,96
M 21
s_bitcmp1_b32 s50, 4
s_cselect_b32 s52, 24576, 0
v_add_u32 v50, s52, v3
M 20
SA 224,0
M 22
M 23
R 10
M 11
M 10
WL 2
R 8
M 34
M 35
M 32
M 33
M 30
M 31
M 28
M 29
M 26
ae 44,70,384
M 27
WV 7
M 24
WL 0
FP
M 25
M 36
s_bitcmp1_b32 s50, 6
s_cselect_b32 s51, 24576, 0
v_add_u32 v47, s51, v3
M 37
XL 38,128
M 38
XL 40,896
M 39
M 40
M 41
M 42
M 43
M 44
XL 42,1664
M 45
FR
M 46
M 47
M 58
M 59
M 56
XL 44,2432
UL 6,30
M 57
UH 7,30
AR 8,56
M 54
AR 9,80
AD 8,6
M 55
WL 0
FQ 120
AD 9,7
M 52
AW 56,8
AW 80,9
M 53
UL 6,31
UH 7,31
M 50
FZ 120
R 24
M 51
mw 72
R 26
M 48
TY 106,9736
TY 110,23560
M 49
R 28
M 60
ad 84,1104
W 0
M 61
AR 8,104
AR 9,128
M 62
AD 8,6
AD 9,7
M 63
W 1
AW 104,8
M 64
AW 128,9
UL 6,32
M 65
UH 7,32
AR 8,57
M 66
W 2
AR 9,81
M 67
WL 3
AN 104,106
AN 105,107
M 68
WL 2
SH 106
AN 106,106
M 69
mw 75
AN 107,107
AN 108,110
M 70
AN 109,111
SH 110
M 71
R 30
M 82
AN 110,110
AN 111,111
M 83
TY 114,11272
TY 118,25096
M 80
ad 84,1110
W 0
M 81
AD 8,6
AD 9,7
M 78
AW 57,8
AW 81,9
M 79
)"
R"(W 1
UL 6,33
M 76
UH 7,33
AR 8,105
M 77
AR 9,129
AD 8,6
M 74
W 2
AD 9,7
M 75
AW 105,8
AW 129,9
M 72
WL 1
AN 112,114
AN 113,115
M 73
R 32
M 84
mw 0
SH 114
AN 114,114
M 85
WL 2
AN 115,115
AN 116,118
M 86
AN 117,119
SH 118
M 87
AN 118,118
AN 119,119
M 88
TY 122,12808
TY 126,26632
M 89
ad 84,1152
W 0
M 90
SA 226,768
UL 6,34
M 91
UH 7,34
AR 8,58
M 92
W 1
AR 9,82
M 93
AD 8,6
AD 9,7
M 94
AW 58,8
AW 82,9
M 95
R 34
M 106
W 2
UL 6,35
M 107
WL 3
AN 120,122
AN 121,123
M 104
WL 2
SH 122
AN 122,122
M 105
mw 3
AN 123,123
AN 124,126
M 102
AN 125,127
SH 126
M 103
AN 126,126
AN 127,127
M 100
ad 84,1158
W 0
M 101
UH 7,35
AR 8,106
M 98
AR 9,130
AD 8,6
M 99
W 1
AD 9,7
M 96
AW 106,8
AW 130,9
M 97
R 48
M 108
W 2
UL 6,36
M 109
UH 7,36
AR 8,59
M 110
AR 9,83
AD 8,6
M 111
bc 49,13,66,60
AD 9,7
M 112
mh 9
AW 59,8
AW 83,9
M 113
ad 66,2528
H 0
M 114
UL 6,37
UH 7,37
M 115
AR 8,107
AR 9,131
M 116
DG 1024
M 117
AD 8,6
AD 9,7
M 118
AW 107,8
AW 131,9
M 119
WL 0
R 50
M 120
bc 48,13,64,56
M 121
mh 12
M 133
ad 64,2528
H 0
M 132
TX 58,9216
TX 62,23040
M 134
M 135
H 1
M 123
M 122
TY 66,9728
TY 70,23552
M 124
H 2
M 125
M 137
M 136
TX 74,10752
TX 78,24576
M 138
WL 4
Q 0
M 139
Q 2
M 127
Q 4
M 126
TY 82,11264
TY 86,25088
M 128
Q 6
M 129
WL 4
Q 8
M 141
Q 10
M 140
TX 90,12288
TX 94,26112
M 142
Q 12
M 143
R 54
M 131
Q 14
M 130
TY 98,12800
TY 102,26624
M 0
R 52
M 1
WL 10
Q 16
M 13
Q 18
M 12
Q 20
M 14
Q 22
M 15
WL 8
Q 24
M 3
Q 26
M 2
Q 28
M 4
Q 30
M 5
WL 6
Q 32
M 17
Q 34
M 16
Q 36
M 18
Q 38
M 19
WL 2
Q 40
M 7
Q 42
M 6
Q 44
M 8
Q 46
M 9
SA 228,1536
M 21
M 20
M 22
M 23
R 58
M 11
M 10
WL 2
R 56
M 34
M 35
M 32
M 33
M 30
M 31
M 28
M 29
M 26
M 27
WV 6
M 24
WL 0
FP
M 25
M 36
ae 44,70,384
M 37
s_bfe_u32 s51, s50, 0x10007
v_mad_i32_i24 v47, v49, s51, v48
XL 14,128
M 38
M 39
M 40
XL 16,896
M 41
M 42
M 43
M 44
M 45
FR
M 46
XL 18,1664
M 47
M 58
M 59
M 56
M 57
M 54
XL 20,2432
M 55
WL 0
FQ 124
M 52
M 53
M 50
FZ 124
R 72
M 51
mw 24
R 74
M 48
TY 106,520
TY 110,14344
M 49
R 76
M 60
ad 84,1200
W 0
M 61
M 62
M 63
W 1
M 64
M 65
M 66
W 2
M 67
WL 3
AN 104,106
AN 105,107
M 68
WL 2
SH 106
AN 106,106
M 69
mw 27
AN 107,107
AN 108,110
M 70
AN 109,111
SH 110
M 71
R 78
M 82
AN 110,110
AN 111,111
M 83
TY 114,2056
TY 118,15880
M 80
ad 84,1206
W 0
M 81
M 78
M 79
W 1
M 76
M 77
M 74
W 2
M 75
SA 230,2304
M 72
WL 1
AN 112,114
AN 113,115
M 73
R 80
M 84
mw 48
SH 114
AN 114,114
M 85
WL 2
AN 115,115
AN 116,118
M 86
AN 117,119
SH 118
M 87
AN 118,118
AN 119,119
M 88
TY 122,3592
TY 126,17416
M 89
ad 84,1248
W 0
M 90
UL 6,38
UH 7,38
M 91
AR 8,132
AR 9,156
M 92
W 1
AD 8,6
M 93
AD 9,7
AW 132,8
M 94
AW 156,9
UL 6,39
M 95
R 82
M 106
W 2
UH 7,39
M 107
WL 3
AN 120,122
AN 121,123
M 104
WL 2
SH 122
AN 122,122
M 105
mw 51
AN 123,123
AN 124,126
M 102
AN 125,127
SH 126
M 103
AN 126,126
AN 127,127
)"
R"(M 100
ad 84,1254
W 0
M 101
AR 8,180
AD 8,6
M 98
AD 136,7
AW 180,8
M 99
W 1
UL 6,40
M 96
UH 7,40
AR 8,133
M 97
R 0
M 108
W 2
AR 9,157
M 109
AD 8,6
AD 9,7
M 110
AW 133,8
AW 157,9
M 111
bc 49,14,66,60
UL 6,41
M 112
mh 18
UH 7,41
AR 8,181
M 113
ad 66,2720
H 0
M 114
AD 8,6
AD 137,7
M 115
AW 181,8
UL 6,42
M 116
DG 1024
M 117
UH 7,42
AR 8,134
M 118
AR 9,158
AD 8,6
M 119
WL 0
R 2
M 120
bc 48,14,64,56
AD 9,7
M 121
mh 21
AW 134,8
AW 158,9
M 133
ad 64,2720
H 0
M 132
TX 58,0
TX 62,13824
M 134
UL 6,43
UH 7,43
M 135
H 1
AR 8,182
M 123
AD 8,6
AD 138,7
M 122
TY 66,512
TY 70,14336
M 124
H 2
AW 182,8
M 125
UL 6,44
UH 7,44
M 137
AR 8,135
AR 9,159
M 136
TX 74,1536
TX 78,15360
PA 12288
DF
M 138
WL 4
Q 0
M 139
Q 2
M 127
Q 4
M 126
TY 82,2048
TY 86,15872
M 128
Q 6
M 129
WL 4
Q 8
M 141
Q 10
M 140
TX 90,3072
TX 94,16896
M 142
Q 12
M 143
R 6
M 131
Q 14
M 130
TY 98,3584
TY 102,17408
M 0
R 4
M 1
WL 10
Q 16
M 13
Q 18
M 12
Q 20
M 14
Q 22
M 15
WL 8
Q 24
M 3
Q 26
M 2
Q 28
M 4
Q 30
M 5
WL 6
Q 32
M 17
Q 34
M 16
Q 36
M 18
Q 38
M 19
WL 2
Q 40
M 7
Q 42
M 6
Q 44
M 8
Q 46
M 9
ae 42,72,96
M 21
s_bfe_u32 s52, s50, 0x10005
v_mad_i32_i24 v50, v49, s52, v48
SA 232,0
M 20
AD 8,6
AD 9,7
M 22
AW 135,8
AW 159,9
M 23
R 10
M 11
UL 6,45
UH 7,45
M 10
WL 2
R 8
M 34
AR 8,183
AD 8,6
M 35
AD 139,7
AW 183,8
M 32
M 33
M 30
M 31
M 28
M 29
M 26
M 27
WV 7
M 24
WL 0
FP
M 25
M 36
ae 44,68,384
M 37
s_bitcmp1_b32 s50, 8
s_cselect_b32 s51, 24576, 0
v_add_u32 v47, s51, v3
M 38
XL 22,128
M 39
M 40
M 41
M 42
XL 24,896
M 43
M 44
M 45
FR
M 46
M 47
M 58
XL 26,1664
M 59
M 56
M 57
M 54
M 55
WL 0
FQ 128
M 52
XL 28,2432
M 53
M 50
FZ 128
R 24
M 51
mw 72
R 26
M 48
TY 106,5128
TY 110,18952
M 49
R 28
M 60
ad 84,1296
W 0
M 61
M 62
M 63
W 1
M 64
M 65
M 66
W 2
M 67
WL 3
AN 104,106
AN 105,107
M 68
WL 2
SH 106
AN 106,106
M 69
mw 75
AN 107,107
AN 108,110
M 70
AN 109,111
SH 110
M 71
R 30
M 82
AN 110,110
AN 111,111
M 83
TY 114,6664
TY 118,20488
M 80
ad 84,1302
W 0
M 81
M 78
M 79
W 1
M 76
M 77
M 74
W 2
M 75
M 72
WL 1
AN 112,114
AN 113,115
M 73
R 32
M 84
mw 0
SH 114
AN 114,114
M 85
WL 2
AN 115,115
AN 116,118
M 86
AN 117,119
SH 118
M 87
AN 118,118
AN 119,119
M 88
TY 122,8200
TY 126,22024
M 89
ad 84,1344
W 0
M 90
SA 234,768
UL 6,14
M 91
UH 7,14
AR 8,136
M 92
W 1
AR 9,160
M 93
AD 8,6
AD 9,7
M 94
AW 136,8
AW 160,9
M 95
R 34
M 106
W 2
UL 6,15
M 107
WL 3
AN 120,122
AN 121,123
M 104
WL 2
SH 122
AN 122,122
M 105
mw 3
AN 123,123
AN 124,126
M 102
AN 125,127
SH 126
M 103
AN 126,126
AN 127,127
M 100
ad 84,1350
W 0
M 101
UH 7,15
AR 8,184
M 98
AD 8,6
AD 140,7
M 99
W 1
AW 184,8
M 96
UL 6,16
UH 7,16
M 97
R 48
M 108
W 2
AR 8,137
M 109
AR 9,161
AD 8,6
M 110
AD 9,7
AW 137,8
M 111
bc 49,15,66,60
AW 161,9
M 112
mh 0
UL 6,17
UH 7,17
M 113
ad 66,2912
H 0
M 114
AR 8,185
AD 8,6
M 115
AD 141,7
AW 185,8
M 116
DG 1024
M 117
UL 6,18
UH 7,18
M 118
AR 8,138
AR 9,162
)"
R"(M 119
WL 0
R 50
M 120
bc 48,15,64,56
AD 8,6
M 121
mh 3
AD 9,7
AW 138,8
M 133
ad 64,2912
H 0
M 132
TX 58,4608
TX 62,18432
M 134
AW 162,9
UL 6,19
M 135
H 1
UH 7,19
M 123
AR 8,186
AD 8,6
M 122
TY 66,5120
TY 70,18944
M 124
H 2
AD 142,7
M 125
AW 186,8
UL 6,20
M 137
UH 7,20
AR 8,139
M 136
TX 74,6144
TX 78,19968
M 138
WL 4
Q 0
M 139
Q 2
M 127
Q 4
M 126
TY 82,6656
TY 86,20480
M 128
Q 6
M 129
WL 4
Q 8
M 141
Q 10
M 140
TX 90,7680
TX 94,21504
M 142
Q 12
M 143
R 54
M 131
Q 14
M 130
TY 98,8192
TY 102,22016
M 0
R 52
M 1
WL 10
Q 16
M 13
Q 18
M 12
Q 20
M 14
Q 22
M 15
WL 8
Q 24
M 3
Q 26
M 2
Q 28
M 4
Q 30
M 5
WL 6
Q 32
M 17
Q 34
M 16
Q 36
M 18
Q 38
M 19
WL 2
Q 40
M 7
Q 42
M 6
Q 44
M 8
Q 46
M 9
SA 236,1536
AR 9,163
M 21
AD 8,6
AD 9,7
M 20
AW 139,8
AW 163,9
M 22
UL 6,21
UH 7,21
M 23
R 58
M 11
AR 8,187
AD 8,6
M 10
WL 2
R 56
M 34
AD 143,7
AW 187,8
M 35
M 32
M 33
M 30
M 31
M 28
M 29
M 26
M 27
WV 6
M 24
WL 0
FP
M 25
M 36
M 37
M 38
ae 44,68,384
M 39
s_bfe_u32 s51, s50, 0x10009
v_mad_i32_i24 v47, v49, s51, v48
XL 30,128
M 40
M 41
M 42
M 43
M 44
XL 32,896
M 45
FR
M 46
M 47
M 58
M 59
M 56
XL 34,1664
M 57
M 54
M 55
WL 0
FQ 132
M 52
M 53
M 50
FZ 132
R 72
M 51
mw 24
R 74
M 48
TY 106,9736
TY 110,23560
M 49
R 76
M 60
ad 84,1392
W 0
M 61
XL 36,2432
M 62
M 63
W 1
M 64
M 65
M 66
W 2
M 67
WL 3
AN 104,106
AN 105,107
M 68
WL 2
SH 106
AN 106,106
M 69
mw 27
AN 107,107
AN 108,110
M 70
AN 109,111
SH 110
M 71
R 78
M 82
AN 110,110
AN 111,111
M 83
TY 114,11272
TY 118,25096
M 80
ad 84,1398
W 0
M 81
M 78
M 79
W 1
M 76
M 77
M 74
W 2
M 75
SA 238,2304
M 72
WL 1
AN 112,114
AN 113,115
M 73
R 80
M 84
mw 48
SH 114
AN 114,114
M 85
WL 2
AN 115,115
AN 116,118
M 86
AN 117,119
SH 118
M 87
AN 118,118
AN 119,119
M 88
TY 122,12808
TY 126,26632
M 89
ad 84,1440
W 0
M 90
M 91
M 92
W 1
UL 6,22
M 93
UH 7,22
AR 8,140
M 94
AR 9,164
AD 8,6
M 95
R 82
M 106
W 2
AD 9,7
M 107
WL 3
AN 120,122
AN 121,123
M 104
WL 2
SH 122
AN 122,122
M 105
mw 51
AN 123,123
AN 124,126
M 102
AN 125,127
SH 126
M 103
AN 126,126
AN 127,127
M 100
ad 84,1446
W 0
M 101
AW 140,8
AW 164,9
M 98
UL 6,23
UH 7,23
M 99
W 1
AR 8,188
M 96
AD 8,6
AD 144,7
M 97
R 0
M 108
W 2
AW 188,8
M 109
UL 6,24
UH 7,24
M 110
AR 8,141
AR 9,165
M 111
bc 49,16,66,60
AD 8,6
M 112
mh 9
AD 9,7
AW 141,8
M 113
ad 66,3104
H 0
M 114
AW 165,9
UL 6,25
M 115
UH 7,25
AR 8,189
M 116
DG 1024
M 117
AD 8,6
AD 145,7
M 118
AW 189,8
UL 6,26
M 119
WL 0
R 2
M 120
bc 48,16,64,56
UH 7,26
M 121
mh 12
AR 8,142
AR 9,166
M 133
ad 64,3104
H 0
M 132
TX 58,9216
TX 62,23040
M 134
AD 8,6
AD 9,7
M 135
H 1
AW 142,8
M 123
AW 166,9
UL 6,27
M 122
TY 66,9728
TY 70,23552
M 124
H 2
UH 7,27
M 125
AR 8,190
AD 8,6
M 137
AD 146,7
AW 190,8
M 136
TX 74,10752
TX 78,24576
PA 12288
DF
M 138
WL 4
Q 0
M 139
Q 2
M 127
Q 4
M 126
TY 82,11264
TY 86,25088
M 128
Q 6
M 129
WL 4
Q 8
M 141
)"
R"(Q 10
M 140
TX 90,12288
TX 94,26112
M 142
Q 12
M 143
R 6
M 131
Q 14
M 130
TY 98,12800
TY 102,26624
M 0
R 4
M 1
WL 10
Q 16
M 13
Q 18
M 12
Q 20
M 14
Q 22
M 15
WL 8
Q 24
M 3
Q 26
M 2
Q 28
M 4
Q 30
M 5
WL 6
Q 32
M 17
Q 34
M 16
Q 36
M 18
Q 38
M 19
WL 2
Q 40
M 7
Q 42
M 6
Q 44
M 8
Q 46
M 9
ae 42,72,96
M 21
s_bitcmp1_b32 s50, 4
s_cselect_b32 s52, 24576, 0
v_add_u32 v50, s52, v3
M 20
SA 240,256
UL 6,28
M 22
UH 7,28
AR 8,143
M 23
R 10
M 11
AR 9,167
AD 8,6
M 10
WL 2
R 8
M 34
AD 9,7
AW 143,8
M 35
AW 167,9
UL 6,29
M 32
UH 7,29
AR 8,191
M 33
AD 8,6
AD 147,7
M 30
AW 191,8
M 31
M 28
M 29
M 26
M 27
WV 7
M 24
WL 0
FP
M 25
M 36
M 37
M 38
M 39
M 40
ae 44,68,480
M 41
s_bitcmp1_b32 s50, 10
s_cselect_b32 s51, 24576, 0
v_add_u32 v47, s51, v3
M 42
XL 38,128
M 43
M 44
M 45
FR
M 46
XL 40,896
M 47
M 58
M 59
M 56
M 57
M 54
XL 42,1664
M 55
WL 0
FQ 136
M 52
M 53
M 50
FZ 136
R 24
M 51
mw 72
R 26
M 48
TY 106,520
TY 110,14344
M 49
R 28
M 60
ad 84,1488
W 0
M 61
XL 44,2432
M 62
M 63
W 1
M 64
M 65
M 66
W 2
M 67
WL 3
AN 104,106
AN 105,107
M 68
WL 2
SH 106
AN 106,106
M 69
mw 75
AN 107,107
AN 108,110
M 70
AN 109,111
SH 110
M 71
R 30
M 82
AN 110,110
AN 111,111
M 83
TY 114,2056
TY 118,15880
M 80
ad 84,1494
W 0
M 81
M 78
M 79
W 1
M 76
M 77
M 74
W 2
M 75
M 72
WL 1
AN 112,114
AN 113,115
M 73
R 32
M 84
mw 0
SH 114
AN 114,114
M 85
WL 2
AN 115,115
AN 116,118
M 86
AN 117,119
SH 118
M 87
AN 118,118
AN 119,119
M 88
TY 122,3592
TY 126,17416
M 89
ad 84,1536
W 0
M 90
SA 242,1024
M 91
M 92
W 1
M 93
M 94
UL 6,30
UH 7,30
M 95
R 34
M 106
W 2
AR 8,144
M 107
WL 3
AN 120,122
AN 121,123
M 104
WL 2
SH 122
AN 122,122
M 105
mw 3
AN 123,123
AN 124,126
M 102
AN 125,127
SH 126
M 103
AN 126,126
AN 127,127
M 100
ad 84,1542
W 0
M 101
AR 9,168
AD 8,6
M 98
AD 9,7
AW 144,8
M 99
W 1
AW 168,9
M 96
UL 6,31
UH 7,31
M 97
R 48
M 108
W 2
AR 8,192
M 109
AD 8,6
AD 148,7
M 110
AW 192,8
UL 6,32
M 111
bc 49,17,66,60
UH 7,32
M 112
mh 18
AR 8,145
AR 9,169
M 113
ad 66,3296
H 0
M 114
AD 8,6
AD 9,7
M 115
AW 145,8
AW 169,9
M 116
DG 1024
M 117
UL 6,33
UH 7,33
M 118
AR 8,193
AD 8,6
M 119
WL 0
R 50
M 120
bc 48,17,64,56
AD 149,7
M 121
mh 21
AW 193,8
UL 6,34
M 133
ad 64,3296
H 0
M 132
TX 58,0
TX 62,13824
M 134
UH 7,34
AR 8,146
M 135
H 1
AR 9,170
M 123
AD 8,6
AD 9,7
M 122
TY 66,512
TY 70,14336
M 124
H 2
AW 146,8
M 125
AW 170,9
UL 6,35
M 137
UH 7,35
AR 8,194
M 136
TX 74,1536
TX 78,15360
M 138
WL 4
Q 0
M 139
Q 2
M 127
Q 4
M 126
TY 82,2048
TY 86,15872
M 128
Q 6
M 129
WL 4
Q 8
M 141
Q 10
M 140
TX 90,3072
TX 94,16896
M 142
Q 12
M 143
R 54
M 131
Q 14
M 130
TY 98,3584
TY 102,17408
M 0
R 52
M 1
WL 10
Q 16
M 13
Q 18
M 12
Q 20
M 14
Q 22
M 15
WL 8
Q 24
M 3
Q 26
M 2
Q 28
M 4
Q 30
M 5
WL 6
Q 32
M 17
Q 34
M 16
Q 36
M 18
Q 38
M 19
WL 2
Q 40
M 7
Q 42
M 6
Q 44
M 8
Q 46
M 9
SA 244,1792
)"
R"(AD 8,6
M 21
AD 150,7
AW 194,8
M 20
UL 6,36
UH 7,36
M 22
AR 8,147
AR 9,171
M 23
R 58
M 11
AD 8,6
AD 9,7
M 10
WL 2
R 56
M 34
AW 147,8
AW 171,9
M 35
UL 6,37
UH 7,37
M 32
AR 8,195
AD 8,6
M 33
AD 151,7
AW 195,8
M 30
M 31
M 28
M 29
M 26
M 27
WV 6
M 24
WL 0
FP
M 25
M 36
M 37
M 38
M 39
M 40
M 41
M 42
ae 44,68,480
M 43
s_bfe_u32 s51, s50, 0x1000b
v_mad_i32_i24 v47, v49, s51, v48
XL 14,128
M 44
M 45
FR
M 46
M 47
M 58
XL 16,896
M 59
M 56
M 57
M 54
M 55
WL 0
FQ 140
M 52
XL 18,1664
M 53
M 50
FZ 140
R 72
M 51
mw 24
R 74
M 48
TY 106,5128
TY 110,18952
M 49
R 76
M 60
ad 84,1584
W 0
M 61
XL 20,2432
M 62
M 63
W 1
M 64
M 65
M 66
W 2
M 67
WL 3
AN 104,106
AN 105,107
M 68
WL 2
SH 106
AN 106,106
M 69
mw 27
AN 107,107
AN 108,110
M 70
AN 109,111
SH 110
M 71
R 78
M 82
AN 110,110
AN 111,111
M 83
TY 114,6664
TY 118,20488
M 80
ad 84,1590
W 0
M 81
M 78
M 79
W 1
M 76
M 77
M 74
W 2
M 75
SA 246,2560
M 72
WL 1
AN 112,114
AN 113,115
M 73
R 80
M 84
mw 48
SH 114
AN 114,114
M 85
WL 2
AN 115,115
AN 116,118
M 86
AN 117,119
SH 118
M 87
AN 118,118
AN 119,119
M 88
TY 122,8200
TY 126,22024
M 89
ad 84,1632
W 0
M 90
M 91
M 92
W 1
M 93
M 94
M 95
R 82
M 106
W 2
UL 6,38
M 107
WL 3
AN 120,122
AN 121,123
M 104
WL 2
SH 122
AN 122,122
M 105
mw 51
AN 123,123
AN 124,126
M 102
AN 125,127
SH 126
M 103
AN 126,126
AN 127,127
M 100
ad 84,1638
W 0
M 101
UH 7,38
AR 8,148
M 98
AR 9,172
AD 8,6
M 99
W 1
AD 9,7
M 96
AW 148,8
AW 172,9
M 97
R 0
M 108
W 2
UL 6,39
M 109
UH 7,39
AR 8,196
M 110
AD 8,6
AD 152,7
M 111
bc 49,18,66,60
AW 196,8
M 112
mh 0
UL 6,40
UH 7,40
M 113
ad 66,3488
H 0
M 114
AR 8,149
AR 9,173
M 115
AD 8,6
AD 9,7
M 116
DG 1024
M 117
AW 149,8
AW 173,9
M 118
UL 6,41
UH 7,41
M 119
WL 0
R 2
M 120
bc 48,18,64,56
AR 8,197
M 121
mh 3
AD 8,6
AD 153,7
M 133
ad 64,3488
H 0
M 132
TX 58,4608
TX 62,18432
M 134
AW 197,8
UL 6,42
M 135
H 1
UH 7,42
M 123
AR 8,150
AR 9,174
M 122
TY 66,5120
TY 70,18944
M 124
H 2
AD 8,6
M 125
AD 9,7
AW 150,8
M 137
AW 174,9
UL 6,43
M 136
TX 74,6144
TX 78,19968
PA 12288
DF
M 138
WL 4
Q 0
M 139
Q 2
M 127
Q 4
M 126
TY 82,6656
TY 86,20480
M 128
Q 6
M 129
WL 4
Q 8
M 141
Q 10
M 140
TX 90,7680
TX 94,21504
M 142
Q 12
M 143
R 6
M 131
Q 14
M 130
TY 98,8192
TY 102,22016
M 0
R 4
M 1
WL 10
Q 16
M 13
Q 18
M 12
Q 20
M 14
Q 22
M 15
WL 8
Q 24
M 3
Q 26
M 2
Q 28
M 4
Q 30
M 5
WL 6
Q 32
M 17
Q 34
M 16
Q 36
M 18
Q 38
M 19
WL 2
Q 40
M 7
Q 42
M 6
Q 44
M 8
Q 46
M 9
ae 42,72,96
M 21
s_bfe_u32 s52, s50, 0x10005
v_mad_i32_i24 v50, v49, s52, v48
SA 248,256
M 20
UH 7,43
AR 8,198
M 22
AD 8,6
AD 154,7
M 23
R 10
M 11
AW 198,8
UL 6,44
M 10
WL 2
R 8
M 34
UH 7,44
AR 8,151
M 35
AR 9,175
AD 8,6
M 32
AD 9,7
AW 151,8
M 33
AW 175,9
UL 6,45
M 30
UH 7,45
AR 8,199
M 31
AD 8,6
AD 155,7
M 28
AW 199,8
M 29
M 26
M 27
WV 7
M 24
WL 0
FP
M 25
M 36
M 37
M 38
M 39
M 40
M 41
M 42
M 43
M 44
)"
R"(M 45
FR
M 46
M 47
M 58
M 59
M 56
M 57
M 54
M 55
WL 0
FQ 144
M 52
M 53
M 50
FZ 144
R 24
M 51
mw 72
R 26
M 48
TY 106,9736
TY 110,23560
M 49
R 28
M 60
ad 84,1680
W 0
M 61
M 62
M 63
W 1
M 64
M 65
M 66
W 2
M 67
WL 3
AN 104,106
AN 105,107
M 68
WL 2
SH 106
AN 106,106
M 69
mw 75
AN 107,107
AN 108,110
M 70
AN 109,111
SH 110
M 71
R 30
M 82
AN 110,110
AN 111,111
M 83
TY 114,11272
TY 118,25096
M 80
ad 84,1686
W 0
M 81
ae 44,70,384
M 78
s_bitcmp1_b32 s50, 6
s_cselect_b32 s51, 24576, 0
v_add_u32 v47, s51, v3
M 79
W 1
XL 22,256
M 76
XL 24,1024
M 77
M 74
W 2
M 75
M 72
WL 1
AN 112,114
AN 113,115
M 73
R 32
M 84
mw 0
SH 114
AN 114,114
M 85
WL 2
AN 115,115
AN 116,118
M 86
AN 117,119
SH 118
M 87
AN 118,118
AN 119,119
M 88
TY 122,12808
TY 126,26632
M 89
ad 84,1728
W 0
M 90
SA 250,1024
XL 26,1792
M 91
XL 28,2560
M 92
W 1
M 93
M 94
M 95
R 34
M 106
W 2
M 107
WL 3
AN 120,122
AN 121,123
M 104
WL 2
SH 122
AN 122,122
M 105
mw 3
AN 123,123
AN 124,126
M 102
AN 125,127
SH 126
M 103
AN 126,126
AN 127,127
M 100
ad 84,1734
W 0
M 101
UL 6,14
UH 7,14
M 98
AR 8,152
AR 9,176
M 99
W 1
AD 8,6
M 96
AD 9,7
AW 152,8
M 97
R 48
M 108
W 2
AW 176,9
M 109
UL 6,15
UH 7,15
M 110
AD 132,6
AD 156,7
M 111
bc 49,19,66,60
UL 6,16
M 112
mh 9
UH 7,16
AR 8,153
M 113
ad 66,3680
H 0
M 114
AR 9,177
AD 8,6
M 115
AD 9,7
AW 153,8
M 116
DG 1024
M 117
AW 177,9
UL 6,17
M 118
UH 7,17
AD 133,6
M 119
WL 0
R 50
M 120
bc 48,19,64,56
AD 157,7
M 121
mh 12
UL 6,18
UH 7,18
M 133
ad 64,3680
H 0
M 132
TX 58,9216
TX 62,23040
M 134
AR 8,154
AR 9,178
M 135
H 1
AD 8,6
M 123
AD 9,7
AW 154,8
M 122
TY 66,9728
TY 70,23552
M 124
H 2
AW 178,9
M 125
UL 6,19
UH 7,19
M 137
AD 134,6
AD 158,7
M 136
TX 74,10752
TX 78,24576
M 138
WL 4
Q 0
M 139
Q 2
M 127
Q 4
M 126
TY 82,11264
TY 86,25088
M 128
Q 6
M 129
WL 4
Q 8
M 141
Q 10
M 140
TX 90,12288
TX 94,26112
M 142
Q 12
M 143
R 54
M 131
Q 14
M 130
TY 98,12800
TY 102,26624
M 0
R 52
M 1
WL 10
Q 16
M 13
Q 18
M 12
Q 20
M 14
Q 22
M 15
WL 8
Q 24
M 3
Q 26
M 2
Q 28
M 4
Q 30
M 5
WL 6
Q 32
M 17
Q 34
M 16
Q 36
M 18
Q 38
M 19
WL 2
Q 40
M 7
Q 42
M 6
Q 44
M 8
Q 46
M 9
SA 252,1792
UL 6,20
M 21
UH 7,20
AR 8,155
M 20
AR 9,179
AD 8,6
M 22
AD 9,7
AW 155,8
M 23
R 58
M 11
AW 179,9
UL 6,21
M 10
WL 2
R 56
M 34
UH 7,21
AD 135,6
M 35
AD 159,7
M 32
M 33
M 30
M 31
M 28
M 29
M 26
M 27
WV 6
M 24
WL 0
FP
M 25
M 36
M 37
M 38
M 39
M 40
M 41
M 42
M 43
M 44
M 45
FR
M 46
M 47
M 58
M 59
M 56
M 57
M 54
M 55
WL 0
FQ 148
M 52
M 53
M 50
FZ 148
R 72
M 51
mw 24
R 74
M 48
TY 106,520
TY 110,14344
M 49
R 76
M 60
ad 84,1776
W 0
M 61
M 62
M 63
W 1
M 64
M 65
M 66
W 2
M 67
WL 3
AN 104,106
AN 105,107
M 68
WL 2
SH 106
AN 106,106
M 69
mw 27
AN 107,107
AN 108,110
M 70
AN 109,111
SH 110
M 71
R 78
M 82
AN 110,110
AN 111,111
M 83
TY 114,2056
TY 118,15880
M 80
ad 84,1782
W 0
)"
R"(M 81
ae 44,70,384
M 78
s_bfe_u32 s51, s50, 0x10007
v_mad_i32_i24 v47, v49, s51, v48
XL 30,256
M 79
W 1
M 76
M 77
M 74
W 2
XL 32,1024
M 75
SA 254,2560
M 72
WL 1
AN 112,114
AN 113,115
M 73
R 80
M 84
mw 48
SH 114
AN 114,114
M 85
WL 2
AN 115,115
AN 116,118
M 86
AN 117,119
SH 118
M 87
AN 118,118
AN 119,119
M 88
TY 122,3592
TY 126,17416
M 89
ad 84,1824
W 0
M 90
XL 34,1792
M 91
M 92
W 1
XL 36,2560
M 93
M 94
M 95
R 82
M 106
W 2
M 107
WL 3
AN 120,122
AN 121,123
M 104
WL 2
SH 122
AN 122,122
M 105
mw 51
AN 123,123
AN 124,126
M 102
AN 125,127
SH 126
M 103
AN 126,126
AN 127,127
M 100
ad 84,1830
W 0
M 101
M 98
M 99
W 1
M 96
M 97
R 0
M 108
W 2
M 109
M 110
M 111
bc 49,20,66,60
M 112
mh 18
M 113
ad 66,3872
H 0
M 114
M 115
M 116
DG 1024
M 117
M 118
M 119
WL 2
R 2
M 120
bc 48,20,64,56
M 121
mh 21
M 133
ad 64,3872
H 0
M 132
TX 58,0
TX 62,13824
M 134
M 135
H 1
M 123
UL 6,22
UH 7,22
M 122
TY 66,512
TY 70,14336
M 124
H 2
AD 160,6
M 125
AD 184,7
UL 6,23
M 137
UH 7,23
AD 208,6
M 136
TX 74,1536
TX 78,15360
PA 12288
DF
M 138
WL 4
Q 0
M 139
Q 2
M 127
Q 4
M 126
TY 82,2048
TY 86,15872
M 128
Q 6
M 129
WL 4
Q 8
M 141
Q 10
M 140
TX 90,3072
TX 94,16896
M 142
Q 12
M 143
R 6
M 131
Q 14
M 130
TY 98,3584
TY 102,17408
M 0
R 4
M 1
WL 10
Q 16
M 13
Q 18
M 12
Q 20
M 14
Q 22
M 15
WL 8
Q 24
M 3
Q 26
M 2
Q 28
M 4
Q 30
M 5
WL 6
Q 32
M 17
Q 34
M 16
Q 36
M 18
Q 38
M 19
WL 2
Q 40
M 7
Q 42
M 6
Q 44
M 8
Q 46
M 9
AD 232,7
UL 6,24
M 21
UH 7,24
AD 161,6
M 20
AD 185,7
UL 6,25
M 22
UH 7,25
AD 209,6
M 23
R 10
M 11
AD 233,7
UL 6,26
M 10
WL 2
R 8
M 34
UH 7,26
AD 162,6
M 35
AD 186,7
UL 6,27
M 32
UH 7,27
AD 210,6
M 33
AD 234,7
UL 6,28
M 30
UH 7,28
AD 163,6
M 31
AD 187,7
UL 6,29
M 28
UH 7,29
AD 211,6
M 29
AD 235,7
M 26
M 27
WV 6
M 24
WL 0
FP
M 25
M 36
M 37
M 38
M 39
M 40
M 41
M 42
M 43
M 44
M 45
FR
M 46
M 47
M 58
M 59
M 56
M 57
M 54
M 55
WL 0
FQ 152
M 52
M 53
M 50
FZ 152
R 24
M 51
mw 72
R 26
M 48
TY 106,5128
TY 110,18952
M 49
R 28
M 60
ad 84,1872
W 0
M 61
M 62
M 63
W 1
M 64
M 65
M 66
W 2
M 67
WL 3
AN 104,106
AN 105,107
M 68
WL 2
SH 106
AN 106,106
M 69
mw 75
AN 107,107
AN 108,110
M 70
AN 109,111
SH 110
M 71
R 30
M 82
AN 110,110
AN 111,111
M 83
TY 114,6664
TY 118,20488
M 80
ad 84,1878
W 0
M 81
M 78
M 79
W 1
M 76
ae 44,68,384
M 77
s_bitcmp1_b32 s50, 8
s_cselect_b32 s51, 24576, 0
v_add_u32 v47, s51, v3
M 74
W 2
XL 38,256
M 75
M 72
WL 1
AN 112,114
AN 113,115
M 73
R 32
M 84
mw 0
SH 114
AN 114,114
M 85
WL 2
AN 115,115
AN 116,118
M 86
AN 117,119
SH 118
M 87
AN 118,118
AN 119,119
M 88
TY 122,8200
TY 126,22024
M 89
ad 84,1920
W 0
M 90
XL 40,1024
M 91
XL 42,1792
M 92
W 1
M 93
M 94
M 95
R 34
M 106
W 2
XL 44,2560
M 107
WL 3
AN 120,122
AN 121,123
M 104
WL 2
SH 122
AN 122,122
M 105
mw 3
AN 123,123
AN 124,126
M 102
AN 125,127
SH 126
M 103
AN 126,126
AN 127,127
M 100
ad 84,1926
W 0
M 101
M 98
M 99
W 1
)"
R"(M 96
M 97
R 48
M 108
W 2
M 109
M 110
M 111
bc 49,21,66,60
M 112
mh 0
M 113
ad 66,4064
H 0
M 114
M 115
M 116
DG 1024
M 117
M 118
M 119
WL 2
R 50
M 120
bc 48,21,64,56
M 121
mh 3
M 133
ad 64,4064
H 0
M 132
TX 58,4608
TX 62,18432
M 134
M 135
H 1
M 123
M 122
TY 66,5120
TY 70,18944
M 124
H 2
M 125
M 137
UL 6,30
UH 7,30
M 136
TX 74,6144
TX 78,19968
M 138
WL 4
Q 0
M 139
Q 2
M 127
Q 4
M 126
TY 82,6656
TY 86,20480
M 128
Q 6
M 129
WL 4
Q 8
M 141
Q 10
M 140
TX 90,7680
TX 94,21504
M 142
Q 12
M 143
R 54
M 131
Q 14
M 130
TY 98,8192
TY 102,22016
M 0
R 52
M 1
WL 10
Q 16
M 13
Q 18
M 12
Q 20
M 14
Q 22
M 15
WL 8
Q 24
M 3
Q 26
M 2
Q 28
M 4
Q 30
M 5
WL 6
Q 32
M 17
Q 34
M 16
Q 36
M 18
Q 38
M 19
WL 2
Q 40
M 7
Q 42
M 6
Q 44
M 8
Q 46
M 9
AD 164,6
AD 188,7
M 21
UL 6,31
UH 7,31
M 20
AD 212,6
AD 236,7
M 22
UL 6,32
UH 7,32
M 23
R 58
M 11
AD 165,6
AD 189,7
M 10
WL 2
R 56
M 34
UL 6,33
UH 7,33
M 35
AD 213,6
AD 237,7
M 32
UL 6,34
UH 7,34
M 33
AD 166,6
AD 190,7
M 30
UL 6,35
UH 7,35
M 31
AD 214,6
AD 238,7
M 28
UL 6,36
UH 7,36
M 29
AD 167,6
AD 191,7
M 26
UL 6,37
UH 7,37
M 27
WV 5
AD 215,6
AD 239,7
M 24
WL 0
FP
M 25
M 36
M 37
M 38
M 39
M 40
M 41
M 42
M 43
M 44
M 45
FR
M 46
M 47
M 58
M 59
M 56
M 57
M 54
M 55
WL 0
FQ 156
M 52
M 53
M 50
FZ 156
R 72
M 51
mw 24
R 74
M 48
TY 106,9736
TY 110,23560
M 49
R 76
M 60
ad 84,1968
W 0
M 61
M 62
M 63
W 1
M 64
M 65
M 66
W 2
M 67
WL 3
AN 104,106
AN 105,107
M 68
WL 2
SH 106
AN 106,106
M 69
mw 27
AN 107,107
AN 108,110
M 70
AN 109,111
SH 110
M 71
R 78
M 82
AN 110,110
AN 111,111
M 83
TY 114,11272
TY 118,25096
M 80
ad 84,1974
W 0
M 81
M 78
M 79
W 1
M 76
M 77
M 74
W 2
M 75
M 72
WL 1
AN 112,114
AN 113,115
M 73
R 80
M 84
mw 48
SH 114
AN 114,114
M 85
WL 2
AN 115,115
AN 116,118
M 86
AN 117,119
SH 118
M 87
AN 118,118
AN 119,119
M 88
TY 122,12808
TY 126,26632
M 89
ad 84,2016
W 0
M 90
ae 44,68,384
M 91
s_bfe_u32 s51, s50, 0x10009
v_mad_i32_i24 v47, v49, s51, v48
XL 14,256
M 92
W 1
XL 16,1024
M 93
M 94
XL 18,1792
M 95
R 82
M 106
W 2
M 107
WL 3
AN 120,122
AN 121,123
M 104
WL 2
SH 122
AN 122,122
M 105
mw 51
AN 123,123
AN 124,126
M 102
AN 125,127
SH 126
M 103
AN 126,126
AN 127,127
M 100
ad 84,2022
W 0
M 101
XL 20,2560
M 98
M 99
W 1
M 96
M 97
R 0
M 108
W 2
M 109
M 110
M 111
bc 49,22,66,60
M 112
mh 9
M 113
ad 66,4256
H 0
M 114
M 115
M 116
DG 1024
M 117
M 118
M 119
WL 2
R 2
M 120
bc 48,22,64,56
M 121
mh 12
M 133
ad 64,4256
H 0
M 132
TX 58,9216
TX 62,23040
M 134
M 135
H 1
M 123
M 122
TY 66,9728
TY 70,23552
M 124
H 2
M 125
M 137
M 136
TX 74,10752
TX 78,24576
PA 12288
DF
M 138
WL 4
Q 0
M 139
Q 2
M 127
Q 4
M 126
TY 82,11264
TY 86,25088
M 128
Q 6
M 129
WL 4
Q 8
M 141
Q 10
M 140
TX 90,12288
TX 94,26112
M 142
Q 12
M 143
R 6
M 131
Q 14
M 130
TY 98,12800
TY 102,26624
M 0
R 4
M 1
)"
R"(WL 10
Q 16
M 13
Q 18
M 12
Q 20
M 14
Q 22
M 15
WL 8
Q 24
M 3
Q 26
M 2
Q 28
M 4
Q 30
M 5
WL 6
Q 32
M 17
Q 34
M 16
Q 36
M 18
Q 38
M 19
WL 2
Q 40
M 7
Q 42
M 6
Q 44
M 8
Q 46
M 9
UL 6,38
UH 7,38
M 21
AD 168,6
AD 192,7
M 20
UL 6,39
UH 7,39
M 22
AD 216,6
AD 240,7
M 23
R 10
M 11
UL 6,40
UH 7,40
M 10
WL 2
R 8
M 34
AD 169,6
AD 193,7
M 35
UL 6,41
UH 7,41
M 32
AD 217,6
AD 241,7
M 33
UL 6,42
UH 7,42
M 30
AD 170,6
AD 194,7
M 31
UL 6,43
UH 7,43
M 28
AD 218,6
AD 242,7
M 29
UL 6,44
UH 7,44
M 26
AD 171,6
AD 195,7
M 27
WV 6
UL 6,45
UH 7,45
M 24
WL 0
FP
AD 219,6
M 25
AD 243,7
M 36
M 37
M 38
M 39
M 40
M 41
M 42
M 43
M 44
M 45
FR
M 46
M 47
M 58
M 59
M 56
M 57
M 54
M 55
WL 0
FQ 160
M 52
M 53
M 50
FZ 160
R 24
M 51
mw 72
R 26
M 48
TY 106,520
TY 110,14344
M 49
R 28
M 60
ad 84,2064
W 0
M 61
M 62
M 63
W 1
M 64
M 65
M 66
W 2
M 67
WL 3
AN 104,106
AN 105,107
M 68
WL 2
SH 106
AN 106,106
M 69
mw 75
AN 107,107
AN 108,110
M 70
AN 109,111
SH 110
M 71
R 30
M 82
AN 110,110
AN 111,111
M 83
TY 114,2056
TY 118,15880
M 80
ad 84,2070
W 0
M 81
M 78
M 79
W 1
M 76
M 77
M 74
W 2
M 75
M 72
WL 1
AN 112,114
AN 113,115
M 73
R 32
M 84
mw 0
SH 114
AN 114,114
M 85
WL 2
AN 115,115
AN 116,118
M 86
AN 117,119
SH 118
M 87
AN 118,118
AN 119,119
M 88
TY 122,3592
TY 126,17416
M 89
ad 84,2112
W 0
M 90
ae 44,68,480
M 91
s_bitcmp1_b32 s50, 10
s_cselect_b32 s51, 24576, 0
v_add_u32 v47, s51, v3
M 92
W 1
XL 22,256
M 93
XL 24,1024
M 94
M 95
R 34
M 106
W 2
M 107
WL 3
AN 120,122
AN 121,123
M 104
WL 2
SH 122
AN 122,122
M 105
mw 3
AN 123,123
AN 124,126
M 102
AN 125,127
SH 126
M 103
AN 126,126
AN 127,127
M 100
ad 84,2118
W 0
M 101
XL 26,1792
M 98
XL 28,2560
M 99
W 1
M 96
M 97
R 48
M 108
W 2
M 109
M 110
M 111
bc 49,23,66,60
M 112
mh 18
M 113
ad 66,4448
H 0
M 114
M 115
M 116
DG 1024
M 117
M 118
M 119
WL 2
R 50
M 120
bc 48,23,64,56
M 121
mh 21
M 133
ad 64,4448
H 0
M 132
TX 58,0
TX 62,13824
M 134
M 135
H 1
M 123
M 122
TY 66,512
TY 70,14336
M 124
H 2
M 125
M 137
M 136
TX 74,1536
TX 78,15360
M 138
WL 4
Q 0
M 139
Q 2
M 127
Q 4
M 126
TY 82,2048
TY 86,15872
M 128
Q 6
M 129
WL 4
Q 8
M 141
Q 10
M 140
TX 90,3072
TX 94,16896
M 142
Q 12
M 143
R 54
M 131
Q 14
M 130
TY 98,3584
TY 102,17408
M 0
R 52
M 1
WL 10
Q 16
M 13
Q 18
M 12
Q 20
M 14
Q 22
M 15
WL 8
Q 24
M 3
Q 26
M 2
Q 28
M 4
Q 30
M 5
WL 6
Q 32
M 17
Q 34
M 16
Q 36
M 18
Q 38
M 19
WL 2
Q 40
M 7
Q 42
M 6
Q 44
M 8
Q 46
M 9
UL 6,14
UH 7,14
M 21
AD 172,6
AD 196,7
M 20
UL 6,15
UH 7,15
M 22
AD 220,6
AD 244,7
M 23
R 58
M 11
UL 6,16
UH 7,16
M 10
WL 2
R 56
M 34
AD 173,6
AD 197,7
M 35
UL 6,17
UH 7,17
M 32
AD 221,6
AD 245,7
M 33
UL 6,18
UH 7,18
M 30
AD 174,6
AD 198,7
M 31
UL 6,19
UH 7,19
M 28
AD 222,6
AD 246,7
M 29
UL 6,20
UH 7,20
M 26
)"
R"(AD 175,6
AD 199,7
M 27
WV 5
UL 6,21
UH 7,21
M 24
WL 0
FP
AD 223,6
M 25
AD 247,7
M 36
M 37
M 38
M 39
M 40
M 41
M 42
M 43
M 44
M 45
FR
M 46
M 47
M 58
M 59
M 56
M 57
M 54
M 55
WL 0
FQ 164
M 52
M 53
M 50
FZ 164
R 72
M 51
mw 24
R 74
M 48
TY 106,5128
TY 110,18952
M 49
R 76
M 60
ad 84,2160
W 0
M 61
M 62
M 63
W 1
M 64
M 65
M 66
W 2
M 67
WL 3
AN 104,106
AN 105,107
M 68
WL 2
SH 106
AN 106,106
M 69
mw 27
AN 107,107
AN 108,110
M 70
AN 109,111
SH 110
M 71
R 78
M 82
AN 110,110
AN 111,111
M 83
TY 114,6664
TY 118,20488
M 80
ad 84,2166
W 0
M 81
M 78
M 79
W 1
M 76
M 77
M 74
W 2
M 75
M 72
WL 1
AN 112,114
AN 113,115
M 73
R 80
M 84
mw 48
SH 114
AN 114,114
M 85
WL 2
AN 115,115
AN 116,118
M 86
AN 117,119
SH 118
M 87
AN 118,118
AN 119,119
M 88
TY 122,8200
TY 126,22024
M 89
ad 84,2208
W 0
M 90
ae 44,68,480
M 91
s_bfe_u32 s51, s50, 0x1000b
v_mad_i32_i24 v47, v49, s51, v48
XL 30,256
M 92
W 1
M 93
M 94
M 95
R 82
M 106
W 2
XL 32,1024
M 107
WL 3
AN 120,122
AN 121,123
M 104
WL 2
SH 122
AN 122,122
M 105
mw 51
AN 123,123
AN 124,126
M 102
AN 125,127
SH 126
M 103
AN 126,126
AN 127,127
M 100
ad 84,2214
W 0
M 101
XL 34,1792
M 98
M 99
W 1
M 96
M 97
R 0
M 108
W 2
XL 36,2560
M 109
M 110
M 111
bc 49,0,66,60
M 112
mh 0
M 113
ad 66,64
H 0
M 114
M 115
M 116
DG 1024
M 117
M 118
M 119
WL 2
R 2
M 120
bc 48,0,64,56
M 121
mh 3
M 133
ad 64,32
H 0
M 132
TX 58,4608
TX 62,18432
M 134
M 135
H 1
M 123
M 122
TY 66,5120
TY 70,18944
M 124
H 2
M 125
M 137
M 136
TX 74,6144
TX 78,19968
PA 12288
DF
M 138
WL 4
Q 0
M 139
Q 2
M 127
Q 4
M 126
TY 82,6656
TY 86,20480
M 128
Q 6
M 129
WL 4
Q 8
M 141
Q 10
M 140
TX 90,7680
TX 94,21504
M 142
Q 12
M 143
R 6
M 131
Q 14
M 130
TY 98,8192
TY 102,22016
M 0
R 4
M 1
WL 10
Q 16
M 13
Q 18
M 12
Q 20
M 14
Q 22
M 15
WL 8
Q 24
M 3
Q 26
M 2
Q 28
M 4
Q 30
M 5
WL 6
Q 32
M 17
Q 34
M 16
Q 36
M 18
Q 38
M 19
WL 2
Q 40
M 7
Q 42
M 6
Q 44
M 8
Q 46
M 9
UL 6,22
UH 7,22
M 21
AD 176,6
AD 200,7
M 20
UL 6,23
UH 7,23
M 22
AD 224,6
AD 248,7
M 23
R 10
M 11
UL 6,24
UH 7,24
M 10
WL 2
R 8
M 34
AD 177,6
AD 201,7
M 35
UL 6,25
UH 7,25
M 32
AD 225,6
AD 249,7
M 33
UL 6,26
UH 7,26
M 30
AD 178,6
AD 202,7
M 31
UL 6,27
UH 7,27
M 28
AD 226,6
AD 250,7
M 29
UL 6,28
UH 7,28
M 26
AD 179,6
AD 203,7
M 27
WV 7
UL 6,29
UH 7,29
M 24
WL 0
FP
AD 227,6
M 25
AD 251,7
M 36
M 37
M 38
M 39
M 40
M 41
M 42
M 43
M 44
M 45
FR
M 46
M 47
M 58
M 59
M 56
M 57
M 54
M 55
WL 0
FQ 168
M 52
M 53
M 50
FZ 168
R 24
M 51
mw 72
R 26
M 48
TY 106,9736
TY 110,23560
M 49
R 28
M 60
ad 84,2256
W 0
M 61
M 62
M 63
W 1
M 64
M 65
M 66
W 2
M 67
WL 3
AN 104,106
AN 105,107
M 68
WL 2
SH 106
AN 106,106
M 69
mw 75
AN 107,107
AN 108,110
M 70
AN 109,111
SH 110
M 71
R 30
M 82
AN 110,110
AN 111,111
M 83
TY 114,11272
)"
R"(TY 118,25096
M 80
ad 84,2262
W 0
M 81
M 78
M 79
W 1
M 76
M 77
M 74
W 2
M 75
M 72
WL 1
AN 112,114
AN 113,115
M 73
R 32
M 84
mw 0
SH 114
AN 114,114
M 85
WL 2
AN 115,115
AN 116,118
M 86
AN 117,119
SH 118
M 87
AN 118,118
AN 119,119
M 88
TY 122,12808
TY 126,26632
M 89
ad 84,0
W 0
M 90
M 91
M 92
W 1
M 93
M 94
M 95
R 34
M 106
W 2
M 107
WL 3
AN 120,122
AN 121,123
M 104
WL 2
SH 122
AN 122,122
M 105
mw 3
AN 123,123
AN 124,126
M 102
AN 125,127
SH 126
M 103
AN 126,126
AN 127,127
M 100
ad 84,6
W 0
M 101
M 98
M 99
W 1
M 96
M 97
R 48
M 108
W 2
M 109
M 110
M 111
bc 49,1,66,60
M 112
mh 9
M 113
ad 66,256
H 0
M 114
M 115
M 116
DG 1024
M 117
M 118
M 119
WL 2
R 50
M 120
bc 48,1,64,56
M 121
mh 12
M 133
ad 64,256
H 0
M 132
TX 58,9216
TX 62,23040
M 134
M 135
DG 1024
M 123
M 122
TY 66,9728
TY 70,23552
M 124
mh 15
M 125
ad 64,227
H 0
M 137
M 136
TX 74,10752
TX 78,24576
M 138
WL 4
Q 0
M 139
Q 2
M 127
Q 4
M 126
TY 82,11264
TY 86,25088
M 128
Q 6
M 129
WL 4
Q 8
M 141
Q 10
M 140
TX 90,12288
TX 94,26112
M 142
Q 12
M 143
R 54
M 131
Q 14
M 130
TY 98,12800
TY 102,26624
M 0
R 52
M 1
WL 10
Q 16
M 13
Q 18
M 12
Q 20
M 14
Q 22
M 15
WL 8
Q 24
M 3
Q 26
M 2
Q 28
M 4
Q 30
M 5
WL 6
Q 32
M 17
Q 34
M 16
Q 36
M 18
Q 38
M 19
WL 2
Q 40
M 7
Q 42
M 6
Q 44
M 8
Q 46
M 9
DG 1024
M 21
UL 6,30
UH 7,30
M 20
AD 180,6
AD 204,7
M 22
UL 6,31
UH 7,31
M 23
R 58
M 11
AD 228,6
AD 252,7
M 10
WL 2
R 56
M 34
UL 6,32
UH 7,32
M 35
AD 181,6
AD 205,7
M 32
UL 6,33
UH 7,33
BV 52,4608
M 33
AD 229,6
AD 253,7
M 30
UL 6,34
UH 7,34
M 31
AD 182,6
AD 206,7
M 28
UL 6,35
UH 7,35
M 29
AD 230,6
AD 254,7
M 26
WV 24
UL 6,36
UH 7,36
M 27
WV 6
AD 183,6
AD 207,7
M 24
WL 1
FP
UL 6,37
M 25
UH 7,37
AD 231,6
M 36
AD 255,7
M 37
M 38
M 39
M 40
ds_read_b64 v[12:13], v4 offset:4864
ds_read_b64 v[46:47], v4 offset:4872
M 41
AR 6,36
AR 7,60
M 42
AR 8,84
AR 9,108
M 43
WL 3
EF 6,52
EF 7,53
M 44
EF 8,54
EF 9,55
M 45
FR
CV 38,6,7
M 46
CV 39,8,9
AR 6,37
M 47
AR 7,61
AR 8,85
M 58
AR 9,109
EF 6,52
M 59
EF 7,53
EF 8,54
M 56
EF 9,55
CV 40,6,7
M 57
CV 41,8,9
AR 6,38
M 54
AR 7,62
AR 8,86
M 55
WL 0
FQ 172
AR 9,110
M 52
EF 6,52
EF 7,53
M 53
EF 8,54
EF 9,55
M 50
FZ 172
R 72
M 51
mw 24
R 74
M 48
TY 106,520
TY 110,14344
M 49
R 76
M 60
ad 84,48
W 0
M 61
CV 42,6,7
CV 43,8,9
M 62
AR 6,39
AR 7,63
M 63
W 1
AR 8,87
M 64
AR 9,111
EF 6,52
M 65
EF 7,53
EF 8,54
M 66
W 2
EF 9,55
M 67
WL 3
AN 104,106
AN 105,107
M 68
WL 2
SH 106
AN 106,106
M 69
mw 27
AN 107,107
AN 108,110
M 70
AN 109,111
SH 110
M 71
R 78
M 82
AN 110,110
AN 111,111
M 83
TY 114,2056
TY 118,15880
M 80
ad 84,54
W 0
M 81
CV 44,6,7
CV 45,8,9
M 78
ae 42,74,384
AR 6,40
M 79
W 1
AR 7,64
M 76
s_bitcmp1_b32 s50, 6
s_cselect_b32 s52, 24576, 0
v_add_u32 v50, s52, v3
ST 38,0
M 77
AR 8,88
AR 9,112
M 74
W 2
EF 6,52
M 75
EF 7,53
EF 8,54
M 72
WL 1
AN 112,114
AN 113,115
M 73
R 80
M 84
mw 48
SH 114
AN 114,114
M 85
WL 2
AN 115,115
AN 116,118
M 86
)"
R"(AN 117,119
SH 118
M 87
AN 118,118
AN 119,119
M 88
TY 122,3592
TY 126,17416
M 89
ad 84,96
W 0
M 90
EF 9,55
CV 14,6,7
M 91
ST 40,768
CV 15,8,9
M 92
W 1
AR 6,41
M 93
AR 7,65
AR 8,89
M 94
AR 9,113
EF 6,52
M 95
R 82
M 106
W 2
EF 7,53
M 107
WL 3
AN 120,122
AN 121,123
M 104
WL 2
SH 122
AN 122,122
M 105
mw 51
AN 123,123
AN 124,126
M 102
AN 125,127
SH 126
M 103
AN 126,126
AN 127,127
M 100
ad 84,102
W 0
M 101
EF 8,54
EF 9,55
M 98
ST 42,1536
CV 16,6,7
M 99
W 1
CV 17,8,9
M 96
AR 6,42
AR 7,66
M 97
R 0
M 108
W 2
AR 8,90
M 109
AR 9,114
EF 6,52
M 110
EF 7,53
EF 8,54
M 111
bc 49,2,66,60
EF 9,55
M 112
mh 18
CV 18,6,7
CV 19,8,9
M 113
ad 66,448
H 0
M 114
AR 6,43
AR 7,67
M 115
AR 8,91
AR 9,115
M 116
DG 1024
M 117
ST 44,2304
EF 6,52
M 118
EF 7,53
EF 8,54
M 119
WL 0
R 2
M 120
bc 48,2,64,56
EF 9,55
M 121
mh 21
CV 20,6,7
CV 21,8,9
M 133
ad 64,448
H 0
BV 52,5120
M 132
TX 58,0
TX 62,13824
M 134
AR 6,132
AR 7,156
M 135
H 1
AR 8,180
M 123
EF 6,12
EF 7,13
M 122
TY 66,512
TY 70,14336
M 124
H 2
EF 8,46
M 125
EF 136,47
CV 22,6,7
M 137
ae 42,74,384
CV 23,8,136
M 136
WL 2
TX 74,1536
TX 78,15360
PA 12288
DF
M 138
s_bfe_u32 s52, s50, 0x10007
Q 0
v_mad_i32_i24 v50, v49, s52, v48
M 139
Q 2
ST 14,0
M 127
Q 4
M 126
TY 82,2048
TY 86,15872
M 128
Q 6
M 129
WL 4
Q 8
M 141
Q 10
M 140
TX 90,3072
TX 94,16896
M 142
Q 12
M 143
R 6
M 131
Q 14
M 130
TY 98,3584
TY 102,17408
v_mfma_f32_16x16x32_f16 a[36:39], v[56:59], a[0:3], a[32:35]
R 4
M 1
WL 10
Q 16
v_mfma_f32_16x16x32_f16 a[60:63], v[60:63], a[12:15], a[32:35]
Q 18
ST 16,768
M 12
Q 20
v_mfma_f32_16x16x32_f16 a[64:67], v[64:67], a[8:11], a[32:35]
Q 22
M 15
WL 8
Q 24
v_mfma_f32_16x16x32_f16 a[40:43], v[68:71], a[4:7], a[32:35]
Q 26
M 2
Q 28
M 4
Q 30
M 5
WL 6
Q 32
M 17
Q 34
M 16
Q 36
M 18
Q 38
M 19
WL 1
Q 40
M 7
Q 42
M 6
Q 44
ST 18,1536
M 8
Q 46
M 9
AR 6,133
AR 7,157
M 21
AR 8,181
EF 6,12
M 20
EF 7,13
EF 8,46
M 22
EF 137,47
CV 24,6,7
M 23
R 10
M 11
CV 25,8,137
AR 6,134
M 10
WL 2
R 8
M 34
AR 7,158
AR 8,182
M 35
EF 6,12
EF 7,13
M 32
EF 8,46
EF 138,47
M 33
CV 26,6,7
CV 27,8,138
M 30
ST 20,2304
AR 6,135
M 31
AR 7,159
AR 8,183
M 28
EF 6,12
EF 7,13
M 29
EF 8,46
EF 139,47
v_mfma_f32_16x16x32_f16 a[88:91], v[64:67], a[16:19], a[32:35]
CV 28,6,7
CV 29,8,139
M 27
WV 11
AR 6,136
AR 7,160
AR 8,184
v_mfma_f32_16x16x32_f16 a[84:87], v[56:59], a[16:19], a[32:35]
WL 0
FP
EF 6,12
EF 7,13
M 25
EF 8,46
EF 140,47
CV 30,6,7
v_mfma_f32_16x16x32_f16 a[108:111], v[56:59], a[24:27], a[32:35]
CV 31,8,140
AR 6,137
AR 7,161
M 37
AR 8,185
EF 6,12
EF 7,13
v_mfma_f32_16x16x32_f16 a[112:115], v[64:67], a[24:27], a[32:35]
EF 8,46
EF 141,47
CV 32,6,7
M 39
CV 33,8,141
AR 6,138
AR 7,162
M 40
AR 8,186
EF 6,12
EF 7,13
M 41
ae 42,74,384
EF 8,46
EF 142,47
M 42
s_bitcmp1_b32 s50, 6
s_cselect_b32 s52, 24576, 0
v_add_u32 v50, s52, v3
ST 22,128
M 43
CV 34,6,7
CV 35,8,142
AR 6,139
M 44
AR 7,163
AR 8,187
EF 6,12
M 45
FR
EF 7,13
EF 8,46
M 46
EF 143,47
CV 36,6,7
CV 37,8,143
M 47
ds_read_b64 v[12:13], v4 offset:4608
ds_read_b64 v[46:47], v4 offset:4616
M 58
EF 160,52
EF 184,53
M 59
EF 208,54
EF 232,55
M 56
CV 38,160,184
CV 39,208,232
M 57
EF 161,52
EF 185,53
M 54
EF 209,54
EF 233,55
M 55
WL 2
FQ 176
CV 40,161,185
M 52
CV 41,209,233
EF 162,52
M 53
ST 24,896
EF 186,53
v_mfma_f32_16x16x32_f16 a[136:139], v[64:67], a[0:3], a[32:35]
FZ 176
R 24
M 51
mw 72
R 26
v_mfma_f32_16x16x32_f16 a[132:135], v[56:59], a[0:3], a[32:35]
TY 106,5128
TY 110,18952
M 49
R 28
v_mfma_f32_16x16x32_f16 a[156:159], v[56:59], a[8:11], a[32:35]
ad 84,144
W 0
M 61
EF 210,54
EF 234,55
v_mfma_f32_16x16x32_f16 a[160:163], v[64:67], a[8:11], a[32:35]
CV 42,162,186
CV 43,210,234
M 63
W 1
EF 163,52
M 64
EF 187,53
EF 211,54
M 65
EF 235,55
CV 44,163,187
M 66
W 2
CV 45,211,235
M 67
WL 3
AN 104,106
AN 105,107
EF 164,52
M 68
WL 2
SH 106
AN 106,106
ST 26,1664
M 69
mw 75
AN 107,107
AN 108,110
EF 188,53
M 70
AN 109,111
SH 110
EF 212,54
M 71
)"
R"(R 30
EF 236,55
M 82
AN 110,110
AN 111,111
CV 14,164,188
M 83
TY 114,6664
TY 118,20488
CV 15,212,236
M 80
ad 84,150
W 0
EF 165,52
M 81
EF 189,53
EF 213,54
EF 237,55
M 78
CV 16,165,189
CV 17,213,237
EF 166,52
M 79
W 1
EF 190,53
EF 214,54
M 76
EF 238,55
CV 18,166,190
CV 19,214,238
M 77
EF 167,52
EF 191,53
EF 215,54
v_mfma_f32_16x16x32_f16 a[184:187], v[64:67], a[16:19], a[32:35]
W 2
EF 239,55
CV 20,167,191
M 75
ST 28,2432
CV 21,215,239
v_mfma_f32_16x16x32_f16 a[180:183], v[56:59], a[16:19], a[32:35]
WL 1
AN 112,114
AN 113,115
BV 52,4864
M 73
R 32
v_mfma_f32_16x16x32_f16 v[136:139], v[56:59], a[24:27], v[128:131]
mw 0
SH 114
AN 114,114
M 85
WL 3
AN 115,115
AN 116,118
v_mfma_f32_16x16x32_f16 v[140:143], v[64:67], a[24:27], v[128:131]
AN 117,119
SH 118
M 87
AN 118,118
AN 119,119
M 88
TY 122,8200
TY 126,22024
M 89
ad 84,192
W 0
M 90
AR 6,44
AR 7,68
M 91
AR 8,92
AR 9,116
M 92
W 1
EF 6,12
M 93
EF 7,13
EF 8,46
M 94
ae 42,74,384
EF 9,47
M 95
R 34
M 106
WL 3
W 2
CV 22,6,7
M 107
s_bfe_u32 s52, s50, 0x10007
AN 120,122
AN 121,123
v_mad_i32_i24 v50, v49, s52, v48
M 104
WL 2
SH 122
AN 122,122
ST 30,128
M 105
mw 3
AN 123,123
AN 124,126
M 102
AN 125,127
SH 126
M 103
AN 126,126
AN 127,127
M 100
ad 84,198
W 0
M 101
CV 23,8,9
AR 6,45
v_mfma_f32_16x16x32_f16 v[164:167], v[64:67], a[0:3], v[128:131]
AR 7,69
AR 8,93
M 99
W 1
AR 9,117
v_mfma_f32_16x16x32_f16 v[160:163], v[56:59], a[0:3], v[128:131]
EF 6,12
EF 7,13
M 97
R 48
v_mfma_f32_16x16x32_f16 v[184:187], v[56:59], a[8:11], v[128:131]
W 2
EF 8,46
M 109
EF 9,47
CV 24,6,7
v_mfma_f32_16x16x32_f16 v[188:191], v[64:67], a[8:11], v[128:131]
CV 25,8,9
AR 6,46
M 111
bc 49,3,66,60
ST 32,896
M 112
mh 0
AR 7,70
AR 8,94
M 113
ad 66,640
H 0
M 114
AR 9,118
EF 6,12
M 115
EF 7,13
EF 8,46
M 116
DG 1024
M 117
EF 9,47
CV 26,6,7
M 118
CV 27,8,9
AR 6,47
M 119
WL 0
R 50
v_mfma_f32_16x16x32_f16 v[208:211], v[56:59], a[16:19], v[128:131]
bc 48,3,64,56
AR 7,71
M 121
mh 3
AR 8,95
AR 9,119
v_mfma_f32_16x16x32_f16 v[232:235], v[60:63], a[28:31], v[128:131]
ad 64,640
H 0
M 132
TX 58,4608
TX 62,18432
v_mfma_f32_16x16x32_f16 v[236:239], v[64:67], a[24:27], v[128:131]
ST 34,1664
EF 6,12
M 135
H 1
EF 7,13
v_mfma_f32_16x16x32_f16 v[212:215], v[68:71], a[20:23], v[128:131]
EF 8,46
EF 9,47
M 122
TY 66,5120
TY 70,18944
M 124
H 2
CV 28,6,7
M 125
CV 29,8,9
M 137
AR 6,48
AR 7,72
AR 8,96
AR 9,120
EF 6,12
EF 7,13
M 136
TX 74,6144
TX 78,19968
EF 8,46
EF 9,47
CV 6,6,7
CV 7,8,9
M 138
WL 4
Q 0
AW 200,6
AW 201,7
AR 6,49
AR 7,73
M 139
Q 2
AR 8,97
AR 9,121
EF 6,12
EF 7,13
M 127
Q 4
EF 8,46
EF 9,47
CV 6,6,7
CV 7,8,9
M 126
TY 82,6656
TY 86,20480
AW 202,6
AW 203,7
AR 6,50
AR 7,74
M 128
Q 6
AR 8,98
AR 9,122
EF 6,12
EF 7,13
M 129
WL 2
Q 8
ST 36,2432
EF 8,46
EF 9,47
CV 6,6,7
M 141
Q 10
CV 7,8,9
AW 204,6
AW 205,7
AR 6,51
M 140
TX 90,7680
TX 94,21504
AR 7,75
AR 8,99
AR 9,123
EF 6,12
M 142
Q 12
EF 7,13
EF 8,46
EF 9,47
CV 6,6,7
M 143
R 54
CV 7,8,9
AW 206,6
AW 207,7
M 131
Q 14
ds_read_b64 v[12:13], v4 offset:5120
M 130
TY 98,8192
TY 102,22016
ds_read_b64 v[46:47], v4 offset:5128
M 0
R 52
M 1
Q 16
M 13
Q 18
M 12
Q 20
M 14
Q 22
M 15
WL 8
Q 24
M 3
Q 26
M 2
Q 28
v_mfma_f32_16x16x32_f16 a[44:47], v[72:75], a[0:3], a[32:35]
Q 30
M 5
Q 32
v_mfma_f32_16x16x32_f16 a[68:71], v[76:79], a[12:15], a[32:35]
Q 34
M 16
Q 36
v_mfma_f32_16x16x32_f16 a[72:75], v[80:83], a[8:11], a[32:35]
Q 38
M 19
WL 2
Q 40
v_mfma_f32_16x16x32_f16 a[48:51], v[84:87], a[4:7], a[32:35]
Q 42
M 6
Q 44
M 8
Q 46
M 9
ae 42,74,384
M 21
s_bitcmp1_b32 s50, 6
s_cselect_b32 s52, 24576, 0
v_add_u32 v50, s52, v3
ST 38,256
M 20
AR 6,140
AR 7,164
M 22
AR 8,188
EF 6,52
M 23
R 58
M 11
EF 7,53
EF 8,54
M 10
WL 2
R 56
M 34
EF 144,55
CV 30,6,7
M 35
CV 31,8,144
AR 6,141
M 32
AR 7,165
AR 8,189
M 33
EF 6,52
EF 7,53
v_mfma_f32_16x16x32_f16 a[96:99], v[80:83], a[16:19], a[32:35]
EF 8,54
EF 145,55
M 31
CV 32,6,7
CV 33,8,145
v_mfma_f32_16x16x32_f16 a[92:95], v[72:75], a[16:19], a[32:35]
AR 6,142
AR 7,166
M 29
ST 40,1024
AR 8,190
M 26
EF 6,52
EF 7,53
M 27
WV 10
EF 8,54
EF 146,55
M 24
WL 0
FP
CV 34,6,7
M 25
CV 35,8,146
AR 6,143
M 36
AR 7,167
AR 8,191
M 37
EF 6,52
EF 7,53
M 38
EF 8,54
EF 147,55
M 39
CV 36,6,7
CV 37,8,147
v_mfma_f32_16x16x32_f16 a[116:119], v[72:75], a[24:27], a[32:35]
AR 6,144
AR 7,168
AR 8,192
EF 6,52
M 41
EF 7,53
EF 8,54
EF 148,55
CV 6,6,7
v_mfma_f32_16x16x32_f16 a[120:123], v[80:83], a[24:27], a[32:35]
CV 7,8,148
AW 208,6
AW 209,7
AR 6,145
M 43
AR 7,169
)"
R"(AR 8,193
EF 6,52
EF 7,53
M 44
ST 42,1792
EF 8,54
EF 149,55
CV 6,6,7
M 45
FR
CV 7,8,149
AW 210,6
AW 211,7
M 46
AR 6,146
AR 7,170
AR 8,194
EF 6,52
M 47
EF 7,53
EF 8,54
EF 150,55
CV 6,6,7
M 58
CV 7,8,150
AW 212,6
AW 213,7
AR 6,147
M 59
AR 7,171
AR 8,195
EF 6,52
EF 7,53
M 56
EF 8,54
EF 151,55
CV 6,6,7
CV 7,8,151
M 57
AW 214,6
AW 215,7
v_mfma_f32_16x16x32_f16 a[144:147], v[80:83], a[0:3], a[32:35]
BV 52,4608
EF 168,12
EF 192,13
M 55
WL 1
FQ 180
EF 216,46
EF 240,47
v_mfma_f32_16x16x32_f16 a[140:143], v[72:75], a[0:3], a[32:35]
CV 6,168,192
CV 7,216,240
AW 216,6
M 53
AW 217,7
EF 169,12
EF 193,13
M 50
FZ 180
R 72
EF 217,46
M 51
mw 24
R 74
ST 44,2560
M 48
TY 106,9736
TY 110,23560
EF 241,47
M 49
R 76
CV 6,169,193
M 60
ad 84,240
W 0
CV 7,217,241
M 61
AW 218,6
AW 219,7
EF 170,12
M 62
EF 194,13
EF 218,46
EF 242,47
M 63
W 1
CV 6,170,194
CV 7,218,242
v_mfma_f32_16x16x32_f16 a[164:167], v[72:75], a[8:11], a[32:35]
AW 220,6
AW 221,7
EF 171,12
M 65
EF 195,13
EF 219,46
EF 243,47
v_mfma_f32_16x16x32_f16 a[168:171], v[80:83], a[8:11], a[32:35]
W 2
CV 6,171,195
CV 7,219,243
M 67
WL 3
AN 104,106
AN 105,107
AW 222,6
M 68
WL 0
SH 106
AN 106,106
AW 223,7
M 69
mw 27
AN 107,107
AN 108,110
EF 172,12
M 70
AN 109,111
SH 110
EF 196,13
M 71
R 78
EF 220,46
M 82
AN 110,110
AN 111,111
EF 244,47
M 83
TY 114,11272
TY 118,25096
CV 38,172,196
M 80
ad 84,246
W 0
CV 39,220,244
M 81
ae 42,74,384
EF 173,12
EF 197,13
v_mfma_f32_16x16x32_f16 a[192:195], v[80:83], a[16:19], a[32:35]
s_bfe_u32 s52, s50, 0x10007
v_mad_i32_i24 v50, v49, s52, v48
ST 14,256
EF 221,46
M 79
W 1
EF 245,47
CV 40,173,197
v_mfma_f32_16x16x32_f16 a[188:191], v[72:75], a[16:19], a[32:35]
CV 41,221,245
EF 174,12
EF 198,13
M 77
EF 222,46
EF 246,47
CV 42,174,198
M 74
W 2
CV 43,222,246
EF 175,12
M 75
EF 199,13
EF 223,46
EF 247,47
M 72
WL 1
AN 112,114
AN 113,115
CV 44,175,199
M 73
R 80
CV 45,223,247
M 84
mw 48
SH 114
AN 114,114
ds_read_b64 v[12:13], v4 offset:4864
M 85
WL 3
AN 115,115
AN 116,118
ds_read_b64 v[46:47], v4 offset:4872
M 86
AN 117,119
SH 118
AR 6,52
M 87
AN 118,118
AN 119,119
AR 7,76
v_mfma_f32_16x16x32_f16 v[144:147], v[72:75], a[24:27], v[128:131]
TY 122,12808
TY 126,26632
AR 8,100
M 89
ad 84,288
W 0
ST 16,1024
v_mfma_f32_16x16x32_f16 v[148:151], v[80:83], a[24:27], v[128:131]
AR 9,124
EF 6,52
EF 7,53
M 91
EF 8,54
EF 9,55
CV 6,6,7
M 92
W 1
CV 7,8,9
AW 224,6
M 93
AW 225,7
AR 6,53
AR 7,77
M 94
AR 8,101
AR 9,125
EF 6,52
M 95
R 82
EF 7,53
M 106
W 2
EF 8,54
EF 9,55
M 107
WL 3
AN 120,122
AN 121,123
CV 6,6,7
M 104
WL 2
SH 122
AN 122,122
CV 7,8,9
M 105
mw 51
AN 123,123
AN 124,126
AW 226,6
v_mfma_f32_16x16x32_f16 v[172:175], v[80:83], a[0:3], v[128:131]
AN 125,127
SH 126
AW 227,7
M 103
AN 126,126
AN 127,127
AR 6,54
v_mfma_f32_16x16x32_f16 v[168:171], v[72:75], a[0:3], v[128:131]
ad 84,294
W 0
ST 18,1792
M 101
AR 7,78
AR 8,102
AR 9,126
M 98
EF 6,52
EF 7,53
EF 8,54
M 99
W 1
EF 9,55
CV 6,6,7
M 96
CV 7,8,9
AW 228,6
AW 229,7
M 97
R 0
AR 6,55
M 108
W 2
AR 7,79
AR 8,103
M 109
AR 9,127
EF 6,52
EF 7,53
M 110
EF 8,54
EF 9,55
CV 6,6,7
M 111
bc 49,4,66,60
CV 7,8,9
AW 230,6
v_mfma_f32_16x16x32_f16 v[192:195], v[72:75], a[8:11], v[128:131]
mh 9
AW 231,7
M 113
ad 66,832
H 0
AR 6,56
v_mfma_f32_16x16x32_f16 v[196:199], v[80:83], a[8:11], v[128:131]
AR 7,80
AR 8,104
AR 9,128
M 115
ST 20,2560
EF 6,52
EF 7,53
M 116
DG 1024
EF 8,54
M 117
EF 9,55
CV 6,6,7
CV 7,8,9
M 118
AW 232,6
AW 233,7
AR 6,57
M 119
WL 0
R 2
AR 7,81
M 120
bc 48,4,64,56
AR 8,105
AR 9,129
M 121
mh 12
EF 6,52
EF 7,53
EF 8,54
M 133
ad 64,832
H 0
EF 9,55
M 132
TX 58,9216
TX 62,23040
CV 6,6,7
M 134
CV 7,8,9
AW 234,6
AW 235,7
M 135
H 1
AR 6,58
AR 7,82
M 123
AR 8,106
AR 9,130
EF 6,52
M 122
TY 66,9728
TY 70,23552
EF 7,53
v_mfma_f32_16x16x32_f16 v[216:219], v[72:75], a[16:19], v[128:131]
H 2
EF 8,54
EF 9,55
M 125
ae 42,72,384
CV 6,6,7
CV 7,8,9
v_mfma_f32_16x16x32_f16 v[240:243], v[76:79], a[28:31], v[128:131]
s_bitcmp1_b32 s50, 8
s_cselect_b32 s52, 24576, 0
v_add_u32 v50, s52, v3
ST 22,0
M 136
TX 74,10752
TX 78,24576
PA 12288
DF
v_mfma_f32_16x16x32_f16 v[244:247], v[80:83], a[24:27], v[128:131]
WL 4
Q 0
AW 236,6
M 139
Q 2
AW 237,7
v_mfma_f32_16x16x32_f16 v[220:223], v[84:87], a[20:23], v[128:131]
Q 4
AR 6,59
M 126
TY 82,11264
TY 86,25088
AR 7,83
M 128
Q 6
AR 8,107
M 129
WL 4
Q 8
AR 9,131
M 141
Q 10
EF 6,52
M 140
TX 90,12288
TX 94,26112
EF 7,53
M 142
Q 12
EF 8,54
M 143
)"
R"(R 6
EF 9,55
M 131
Q 14
CV 6,6,7
M 130
TY 98,12800
TY 102,26624
ST 24,768
M 0
R 4
CV 7,8,9
M 1
WL 8
Q 16
AW 238,6
M 13
Q 18
AW 239,7
M 12
Q 20
BV 52,5120
M 14
Q 22
M 15
Q 24
M 3
Q 26
M 2
Q 28
M 4
Q 30
M 5
WL 5
Q 32
M 17
Q 34
M 16
Q 36
M 18
Q 38
ST 26,1536
M 19
WL 1
Q 40
M 7
Q 42
M 6
Q 44
v_mfma_f32_16x16x32_f16 a[52:55], v[88:91], a[0:3], a[32:35]
Q 46
M 9
AR 6,148
AR 7,172
v_mfma_f32_16x16x32_f16 a[76:79], v[92:95], a[12:15], a[32:35]
AR 8,196
EF 6,12
M 20
EF 7,13
EF 8,46
v_mfma_f32_16x16x32_f16 a[80:83], v[96:99], a[8:11], a[32:35]
EF 152,47
CV 14,6,7
M 23
R 10
v_mfma_f32_16x16x32_f16 a[56:59], v[100:103], a[4:7], a[32:35]
CV 15,8,152
AR 6,149
M 10
R 8
v_mfma_f32_16x16x32_f16 a[104:107], v[96:99], a[16:19], a[32:35]
AR 7,173
AR 8,197
M 35
ST 28,2304
EF 6,12
v_mfma_f32_16x16x32_f16 a[100:103], v[88:91], a[16:19], a[32:35]
EF 7,13
EF 8,46
M 33
EF 153,47
CV 16,6,7
M 30
CV 17,8,153
AR 6,150
M 31
AR 7,174
AR 8,198
M 28
EF 6,12
EF 7,13
M 29
EF 8,46
EF 154,47
M 26
CV 18,6,7
CV 19,8,154
M 27
WV 11
AR 6,151
AR 7,175
M 24
WL 0
FP
AR 8,199
M 25
EF 6,12
EF 7,13
M 36
EF 8,46
EF 155,47
M 37
CV 20,6,7
CV 21,8,155
M 38
ae 42,72,384
ST 30,128
AR 6,152
AR 7,176
M 39
EF 6,12
EF 7,13
EF 132,46
EF 156,47
M 40
CV 22,6,7
CV 23,132,156
AR 6,153
AR 7,177
M 41
EF 6,12
EF 7,13
EF 133,46
EF 157,47
M 42
CV 24,6,7
CV 25,133,157
AR 6,154
AR 7,178
M 43
EF 6,12
EF 7,13
EF 134,46
EF 158,47
v_mfma_f32_16x16x32_f16 a[124:127], v[88:91], a[24:27], a[32:35]
CV 26,6,7
CV 27,134,158
AR 6,155
AR 7,179
M 45
FR
EF 6,12
EF 7,13
EF 135,46
v_mfma_f32_16x16x32_f16 a[128:131], v[96:99], a[24:27], a[32:35]
EF 159,47
CV 28,6,7
CV 29,135,159
M 47
EF 176,52
EF 200,53
v_mfma_f32_16x16x32_f16 a[152:155], v[96:99], a[0:3], a[32:35]
EF 224,54
EF 248,55
M 59
CV 6,176,200
CV 7,224,248
v_mfma_f32_16x16x32_f16 a[148:151], v[88:91], a[0:3], a[32:35]
AW 240,6
AW 241,7
M 57
ST 32,896
EF 177,52
M 54
EF 201,53
EF 225,54
M 55
WL 0
FQ 184
EF 249,55
M 52
CV 6,177,201
CV 7,225,249
M 53
AW 242,6
AW 243,7
M 50
FZ 184
R 24
M 51
mw 72
R 26
M 48
TY 106,520
TY 110,14344
M 49
R 28
M 60
ad 84,336
W 0
M 61
EF 178,52
EF 202,53
M 62
EF 226,54
EF 250,55
M 63
W 1
CV 6,178,202
M 64
ST 34,1664
CV 7,226,250
M 65
AW 244,6
AW 245,7
M 66
W 2
EF 179,52
M 67
WL 3
AN 104,106
AN 105,107
v_mfma_f32_16x16x32_f16 a[172:175], v[88:91], a[8:11], a[32:35]
WL 2
SH 106
AN 106,106
M 69
mw 75
AN 107,107
AN 108,110
v_mfma_f32_16x16x32_f16 a[176:179], v[96:99], a[8:11], a[32:35]
AN 109,111
SH 110
M 71
R 30
v_mfma_f32_16x16x32_f16 v[132:135], v[96:99], a[16:19], v[128:131]
AN 110,110
AN 111,111
M 83
TY 114,2056
TY 118,15880
v_mfma_f32_16x16x32_f16 a[196:199], v[88:91], a[16:19], a[32:35]
ad 84,342
W 0
M 81
EF 203,53
EF 227,54
M 78
EF 251,55
CV 6,179,203
M 79
W 1
ST 36,2432
M 76
CV 7,227,251
AW 246,6
M 77
AW 247,7
M 74
W 2
EF 180,52
EF 204,53
EF 228,54
M 75
EF 252,55
CV 6,180,204
CV 7,228,252
AW 248,6
M 72
WL 1
AN 112,114
AN 113,115
AW 249,7
EF 181,52
M 73
R 32
EF 205,53
EF 229,54
M 84
mw 0
SH 114
AN 114,114
EF 253,55
CV 6,181,205
M 85
WL 2
AN 115,115
AN 116,118
CV 7,229,253
AW 250,6
M 86
AN 117,119
SH 118
AW 251,7
EF 182,52
M 87
AN 118,118
AN 119,119
EF 206,53
EF 230,54
M 88
TY 122,3592
TY 126,17416
EF 254,55
CV 6,182,206
M 89
ad 84,384
W 0
CV 7,230,254
AW 252,6
M 90
ae 42,72,384
AW 253,7
EF 183,52
EF 207,53
M 91
s_bfe_u32 s52, s50, 0x10009
v_mad_i32_i24 v50, v49, s52, v48
ST 38,256
EF 231,54
EF 255,55
v_mfma_f32_16x16x32_f16 v[152:155], v[88:91], a[24:27], v[128:131]
W 1
CV 6,183,207
CV 7,231,255
AW 254,6
M 93
AW 255,7
v_mfma_f32_16x16x32_f16 v[156:159], v[96:99], a[24:27], v[128:131]
M 95
R 34
v_mfma_f32_16x16x32_f16 v[180:183], v[96:99], a[0:3], v[128:131]
W 2
M 107
WL 3
AN 120,122
AN 121,123
v_mfma_f32_16x16x32_f16 v[176:179], v[88:91], a[0:3], v[128:131]
WL 2
SH 122
AN 122,122
M 105
mw 3
AN 123,123
AN 124,126
M 102
AN 125,127
SH 126
M 103
AN 126,126
AN 127,127
M 100
ad 84,390
W 0
M 101
M 98
ST 40,1024
M 99
W 1
M 96
M 97
R 48
M 108
W 2
M 109
M 110
M 111
bc 49,5,66,60
M 112
mh 18
M 113
ad 66,1024
H 0
M 114
M 115
v_mfma_f32_16x16x32_f16 v[200:203], v[88:91], a[8:11], v[128:131]
DG 1024
M 117
ST 42,1792
v_mfma_f32_16x16x32_f16 v[204:207], v[96:99], a[8:11], v[128:131]
M 119
WL 2
R 50
M 120
bc 48,5,64,56
M 121
mh 21
M 133
ad 64,1024
H 0
M 132
TX 58,0
TX 62,13824
M 134
M 135
H 1
M 123
M 122
TY 66,512
TY 70,14336
)"
R"(M 124
H 2
M 125
M 137
ST 44,2560
M 136
TX 74,1536
TX 78,15360
M 138
WL 4
Q 0
M 139
Q 2
M 127
Q 4
M 126
TY 82,2048
TY 86,15872
v_mfma_f32_16x16x32_f16 v[224:227], v[88:91], a[16:19], v[128:131]
Q 6
M 129
WL 4
Q 8
v_mfma_f32_16x16x32_f16 v[248:251], v[92:95], a[28:31], v[128:131]
Q 10
M 140
TX 90,3072
TX 94,16896
v_mfma_f32_16x16x32_f16 v[252:255], v[96:99], a[24:27], v[128:131]
Q 12
M 143
R 54
v_mfma_f32_16x16x32_f16 v[228:231], v[100:103], a[20:23], v[128:131]
Q 14
M 130
TY 98,3584
TY 102,17408
M 0
R 52
M 1
WL 10
Q 16
M 13
Q 18
M 12
Q 20
M 14
Q 22
M 15
WL 8
Q 24
M 3
Q 26
M 2
Q 28
M 4
Q 30
M 5
WL 6
Q 32
M 17
Q 34
M 16
Q 36
M 18
Q 38
M 19
WL 2
Q 40
M 7
Q 42
M 6
Q 44
M 8
Q 46
M 9
ae 42,72,480
M 21
s_bitcmp1_b32 s50, 10
s_cselect_b32 s52, 24576, 0
v_add_u32 v50, s52, v3
ST 14,128
M 20
M 22
M 23
R 58
M 11
M 10
WL 2
R 56
M 34
M 35
M 32
M 33
M 30
M 31
M 28
M 29
ST 16,896
M 26
M 27
WV 9
M 24
WL 0
FP
M 25
M 36
M 37
M 38
M 39
M 40
M 41
M 42
M 43
M 44
ST 18,1664
M 45
FR
M 46
M 47
M 58
M 59
M 56
M 57
M 54
M 55
WL 0
FQ 188
M 52
M 53
M 50
FZ 188
R 72
M 51
mw 24
R 74
ST 20,2432
M 48
TY 106,5128
TY 110,18952
M 49
R 76
M 60
ad 84,432
W 0
M 61
M 62
M 63
W 1
M 64
M 65
M 66
W 2
M 67
WL 3
AN 104,106
AN 105,107
M 68
WL 2
SH 106
AN 106,106
M 69
mw 27
AN 107,107
AN 108,110
M 70
AN 109,111
SH 110
M 71
R 78
M 82
AN 110,110
AN 111,111
M 83
TY 114,6664
TY 118,20488
M 80
ad 84,438
W 0
M 81
ae 42,72,480
M 78
s_bfe_u32 s52, s50, 0x1000b
v_mad_i32_i24 v50, v49, s52, v48
ST 22,128
M 79
W 1
M 76
M 77
M 74
W 2
M 75
M 72
WL 1
AN 112,114
AN 113,115
M 73
R 80
M 84
mw 48
SH 114
AN 114,114
M 85
WL 2
AN 115,115
AN 116,118
M 86
AN 117,119
SH 118
M 87
AN 118,118
AN 119,119
M 88
TY 122,8200
TY 126,22024
M 89
ad 84,480
W 0
ST 24,896
M 90
M 91
M 92
W 1
M 93
M 94
M 95
R 82
M 106
W 2
M 107
WL 3
AN 120,122
AN 121,123
M 104
WL 2
SH 122
AN 122,122
M 105
mw 51
AN 123,123
AN 124,126
M 102
AN 125,127
SH 126
M 103
AN 126,126
AN 127,127
M 100
ad 84,486
W 0
ST 26,1664
M 101
M 98
M 99
W 1
M 96
M 97
R 0
M 108
W 2
M 109
M 110
M 111
bc 49,6,66,60
M 112
mh 0
M 113
ad 66,1216
H 0
M 114
M 115
ST 28,2432
M 116
DG 1024
M 117
M 118
M 119
WL 2
R 2
M 120
bc 48,6,64,56
M 121
mh 3
M 133
ad 64,1216
H 0
M 132
TX 58,4608
TX 62,18432
M 134
M 135
H 1
M 123
M 122
TY 66,5120
TY 70,18944
M 124
H 2
M 125
ae 44,70,768
M 137
s_bitcmp1_b32 s50, 12
s_cselect_b32 s51, 24576, 0
v_add_u32 v47, s51, v3
M 136
TX 74,6144
TX 78,19968
PA 12288
DF
M 138
WL 4
Q 0
M 139
Q 2
M 127
Q 4
M 126
TY 82,6656
TY 86,20480
M 128
Q 6
M 129
WL 4
Q 8
M 141
Q 10
M 140
TX 90,7680
TX 94,21504
M 142
Q 12
M 143
R 6
M 131
Q 14
M 130
TY 98,8192
TY 102,22016
M 0
R 4
M 1
WL 10
Q 16
M 13
Q 18
M 12
Q 20
M 14
Q 22
M 15
WL 8
Q 24
M 3
Q 26
M 2
Q 28
M 4
Q 30
M 5
WL 6
Q 32
M 17
Q 34
M 16
Q 36
M 18
Q 38
M 19
WL 2
Q 40
M 7
Q 42
M 6
Q 44
M 8
Q 46
M 9
XL 30,0
M 21
XL 32,768
M 20
XL 34,1536
M 22
XL 36,2304
M 23
R 10
M 11
M 10
WL 2
R 8
M 34
M 35
M 32
M 33
M 30
M 31
M 28
M 29
M 26
M 27
)"
R"(WV 11
M 24
WL 0
FP
M 25
M 36
ae 42,72,384
M 37
s_bfe_u32 s52, s50, 0x10009
v_mad_i32_i24 v50, v49, s52, v48
SA 200,0
M 38
M 39
M 40
M 41
M 42
M 43
M 44
M 45
FR
M 46
M 47
M 58
M 59
M 56
M 57
M 54
M 55
WL 0
FQ 192
M 52
M 53
M 50
FZ 192
R 24
M 51
mw 72
R 26
M 48
TY 106,9736
TY 110,23560
M 49
R 28
M 60
ad 84,528
W 0
M 61
M 62
M 63
W 1
M 64
M 65
M 66
W 2
M 67
WL 3
AN 104,106
AN 105,107
M 68
WL 2
SH 106
AN 106,106
M 69
mw 75
AN 107,107
AN 108,110
M 70
AN 109,111
SH 110
M 71
R 30
M 82
AN 110,110
AN 111,111
M 83
TY 114,11272
TY 118,25096
M 80
ad 84,534
W 0
M 81
M 78
M 79
W 1
M 76
M 77
M 74
W 2
M 75
M 72
WL 1
AN 112,114
AN 113,115
M 73
R 32
M 84
mw 0
SH 114
AN 114,114
M 85
WL 2
AN 115,115
AN 116,118
M 86
AN 117,119
SH 118
M 87
AN 118,118
AN 119,119
M 88
TY 122,12808
TY 126,26632
M 89
ad 84,576
W 0
M 90
M 91
M 92
W 1
M 93
M 94
M 95
R 34
M 106
W 2
M 107
WL 3
AN 120,122
AN 121,123
M 104
WL 2
SH 122
AN 122,122
M 105
mw 3
AN 123,123
AN 124,126
M 102
AN 125,127
SH 126
M 103
AN 126,126
AN 127,127
M 100
ad 84,582
W 0
M 101
SA 202,768
M 98
M 99
W 1
M 96
M 97
R 48
M 108
W 2
M 109
M 110
M 111
bc 49,7,66,60
M 112
mh 9
M 113
ad 66,1408
H 0
M 114
M 115
M 116
DG 1024
M 117
M 118
M 119
WL 2
R 50
M 120
bc 48,7,64,56
M 121
mh 12
M 133
ad 64,1408
H 0
M 132
TX 58,9216
TX 62,23040
M 134
M 135
H 1
M 123
M 122
TY 66,9728
TY 70,23552
M 124
H 2
M 125
M 137
ae 44,70,768
M 136
TX 74,10752
TX 78,24576
M 138
s_bfe_u32 s51, s50, 0x1000d
WL 4
Q 0
M 139
Q 2
M 127
Q 4
M 126
TY 82,11264
TY 86,25088
M 128
Q 6
M 129
WL 4
Q 8
M 141
Q 10
M 140
TX 90,12288
TX 94,26112
M 142
Q 12
M 143
R 54
M 131
Q 14
M 130
TY 98,12800
TY 102,26624
M 0
R 52
M 1
WL 10
Q 16
M 13
Q 18
M 12
Q 20
M 14
Q 22
M 15
WL 8
Q 24
M 3
Q 26
M 2
Q 28
M 4
Q 30
M 5
WL 6
Q 32
M 17
Q 34
M 16
Q 36
M 18
Q 38
M 19
WL 2
Q 40
M 7
Q 42
M 6
Q 44
M 8
Q 46
M 9
v_mad_i32_i24 v47, v49, s51, v48
XL 38,0
M 21
XL 40,768
M 20
XL 42,1536
M 22
XL 44,2304
M 23
R 58
M 11
M 10
WL 2
R 56
M 34
SA 204,1536
M 35
M 32
M 33
M 30
M 31
M 28
M 29
M 26
M 27
WV 10
M 24
WL 0
FP
M 25
M 36
M 37
M 38
M 39
M 40
UL 6,30
UH 7,30
M 41
AR 8,36
AR 9,60
M 42
AD 8,6
AD 9,7
M 43
AW 36,8
AW 60,9
M 44
UL 6,31
UH 7,31
M 45
FR
AR 8,84
M 46
AR 9,108
AD 8,6
M 47
AD 9,7
AW 84,8
M 58
AW 108,9
UL 6,32
M 59
UH 7,32
AR 8,37
M 56
AR 9,61
AD 8,6
M 57
AD 9,7
AW 37,8
M 54
AW 61,9
UL 6,33
M 55
WL 0
FQ 196
UH 7,33
M 52
AR 8,85
AR 9,109
M 53
AD 8,6
AD 9,7
M 50
FZ 196
R 72
M 51
mw 24
R 74
M 48
TY 106,520
TY 110,14344
M 49
R 76
M 60
ad 84,624
W 0
M 61
AW 85,8
AW 109,9
M 62
UL 6,34
UH 7,34
M 63
W 1
AR 8,38
M 64
AR 9,62
AD 8,6
M 65
AD 9,7
AW 38,8
M 66
W 2
AW 62,9
M 67
WL 3
AN 104,106
AN 105,107
M 68
WL 2
SH 106
AN 106,106
M 69
mw 27
AN 107,107
AN 108,110
M 70
AN 109,111
SH 110
M 71
R 78
)"
R"(M 82
AN 110,110
AN 111,111
M 83
TY 114,2056
TY 118,15880
M 80
ad 84,630
W 0
M 81
UL 6,35
UH 7,35
M 78
AR 8,86
AR 9,110
M 79
W 1
AD 8,6
M 76
AD 9,7
AW 86,8
M 77
AW 110,9
UL 6,36
M 74
W 2
UH 7,36
M 75
AR 8,39
AR 9,63
M 72
WL 1
AN 112,114
AN 113,115
M 73
R 80
M 84
mw 48
SH 114
AN 114,114
M 85
WL 2
AN 115,115
AN 116,118
M 86
AN 117,119
SH 118
M 87
AN 118,118
AN 119,119
M 88
TY 122,3592
TY 126,17416
M 89
ad 84,672
W 0
M 90
SA 206,2304
AD 8,6
M 91
AD 9,7
AW 39,8
M 92
W 1
AW 63,9
M 93
UL 6,37
UH 7,37
M 94
AR 8,87
AR 9,111
M 95
R 82
M 106
W 2
AD 8,6
M 107
WL 3
AN 120,122
AN 121,123
M 104
WL 2
SH 122
AN 122,122
M 105
mw 51
AN 123,123
AN 124,126
M 102
AN 125,127
SH 126
M 103
AN 126,126
AN 127,127
M 100
ad 84,678
W 0
M 101
AD 9,7
AW 87,8
M 98
AW 111,9
M 99
W 1
M 96
M 97
R 0
M 108
W 2
M 109
M 110
M 111
bc 49,8,66,60
M 112
mh 18
M 113
ad 66,1600
H 0
M 114
M 115
M 116
DG 1024
M 117
M 118
M 119
WL 2
R 2
M 120
bc 48,8,64,56
M 121
mh 21
M 133
ad 64,1600
H 0
M 132
TX 58,0
TX 62,13824
M 134
M 135
H 1
M 123
M 122
TY 66,512
TY 70,14336
M 124
H 2
M 125
M 137
M 136
TX 74,1536
TX 78,15360
PA 12288
DF
M 138
WL 4
Q 0
M 139
Q 2
M 127
Q 4
M 126
TY 82,2048
TY 86,15872
M 128
Q 6
M 129
WL 4
Q 8
M 141
Q 10
M 140
TX 90,3072
TX 94,16896
M 142
Q 12
M 143
R 6
M 131
Q 14
M 130
TY 98,3584
TY 102,17408
M 0
R 4
M 1
WL 10
Q 16
M 13
Q 18
M 12
Q 20
M 14
Q 22
M 15
WL 8
Q 24
M 3
Q 26
M 2
Q 28
M 4
Q 30
M 5
WL 6
Q 32
M 17
Q 34
M 16
Q 36
M 18
Q 38
M 19
WL 2
Q 40
M 7
Q 42
M 6
Q 44
M 8
Q 46
M 9
ae 42,72,384
M 21
SA 208,128
M 20
ae 44,68,768
M 22
s_bitcmp1_b32 s50, 14
s_cselect_b32 s51, 24576, 0
v_add_u32 v47, s51, v3
M 23
R 10
M 11
XL 14,0
M 10
WL 2
R 8
M 34
XL 16,768
M 35
XL 18,1536
M 32
XL 20,2304
M 33
M 30
M 31
M 28
M 29
M 26
M 27
WV 11
M 24
WL 0
FP
M 25
M 36
M 37
M 38
M 39
M 40
M 41
M 42
UL 6,38
UH 7,38
M 43
AR 8,40
AR 9,64
M 44
AD 8,6
AD 9,7
M 45
FR
AW 40,8
M 46
AW 64,9
UL 6,39
M 47
UH 7,39
AR 8,88
M 58
AR 9,112
AD 8,6
M 59
AD 9,7
AW 88,8
M 56
AW 112,9
UL 6,40
M 57
UH 7,40
AR 8,41
M 54
AR 9,65
AD 8,6
M 55
WL 0
FQ 200
AD 9,7
M 52
AW 41,8
AW 65,9
M 53
UL 6,41
UH 7,41
M 50
FZ 200
R 24
M 51
mw 72
R 26
M 48
TY 106,5128
TY 110,18952
M 49
R 28
M 60
ad 84,720
W 0
M 61
AR 8,89
AR 9,113
M 62
AD 8,6
AD 9,7
M 63
W 1
AW 89,8
M 64
AW 113,9
UL 6,42
M 65
UH 7,42
AR 8,42
M 66
W 2
AR 9,66
M 67
WL 3
AN 104,106
AN 105,107
M 68
WL 2
SH 106
AN 106,106
M 69
mw 75
AN 107,107
AN 108,110
M 70
AN 109,111
SH 110
M 71
R 30
M 82
AN 110,110
AN 111,111
M 83
TY 114,6664
TY 118,20488
M 80
ad 84,726
W 0
M 81
AD 8,6
AD 9,7
M 78
AW 42,8
AW 66,9
M 79
W 1
UL 6,43
M 76
UH 7,43
AR 8,90
M 77
AR 9,114
AD 8,6
M 74
W 2
AD 9,7
M 75
AW 90,8
AW 114,9
M 72
WL 1
AN 112,114
AN 113,115
M 73
R 32
M 84
mw 0
SH 114
AN 114,114
M 85
WL 2
AN 115,115
AN 116,118
M 86
AN 117,119
SH 118
)"
R"(M 87
AN 118,118
AN 119,119
M 88
TY 122,8200
TY 126,22024
M 89
ad 84,768
W 0
M 90
SA 210,896
UL 6,44
M 91
UH 7,44
AR 8,43
M 92
W 1
AR 9,67
M 93
AD 8,6
AD 9,7
M 94
AW 43,8
AW 67,9
M 95
R 34
M 106
W 2
UL 6,45
M 107
WL 3
AN 120,122
AN 121,123
M 104
WL 2
SH 122
AN 122,122
M 105
mw 3
AN 123,123
AN 124,126
M 102
AN 125,127
SH 126
M 103
AN 126,126
AN 127,127
M 100
ad 84,774
W 0
M 101
UH 7,45
AR 8,91
M 98
AR 9,115
AD 8,6
M 99
W 1
AD 9,7
M 96
AW 91,8
AW 115,9
M 97
R 48
M 108
W 2
M 109
M 110
M 111
bc 49,9,66,60
M 112
mh 0
M 113
ad 66,1792
H 0
M 114
M 115
M 116
DG 1024
M 117
M 118
M 119
WL 2
R 50
M 120
bc 48,9,64,56
M 121
mh 3
M 133
ad 64,1792
H 0
M 132
TX 58,4608
TX 62,18432
M 134
M 135
H 1
M 123
M 122
TY 66,5120
TY 70,18944
M 124
H 2
M 125
M 137
M 136
TX 74,6144
TX 78,19968
M 138
WL 4
Q 0
M 139
Q 2
M 127
Q 4
M 126
TY 82,6656
TY 86,20480
M 128
Q 6
M 129
WL 4
Q 8
M 141
Q 10
M 140
TX 90,7680
TX 94,21504
M 142
Q 12
M 143
R 54
M 131
Q 14
M 130
TY 98,8192
TY 102,22016
M 0
R 52
M 1
WL 10
Q 16
M 13
Q 18
M 12
Q 20
M 14
Q 22
M 15
WL 8
Q 24
M 3
Q 26
M 2
Q 28
M 4
Q 30
M 5
WL 6
Q 32
M 17
Q 34
M 16
Q 36
M 18
Q 38
M 19
WL 2
Q 40
M 7
Q 42
M 6
Q 44
M 8
Q 46
M 9
SA 212,1664
M 21
ae 44,68,768
M 20
s_bfe_u32 s51, s50, 0x1000f
v_mad_i32_i24 v47, v49, s51, v48
XL 22,0
M 22
XL 24,768
M 23
R 58
M 11
XL 26,1536
M 10
WL 2
R 56
M 34
XL 28,2304
M 35
M 32
M 33
M 30
M 31
M 28
M 29
M 26
M 27
WV 10
M 24
WL 0
FP
M 25
M 36
M 37
M 38
M 39
M 40
M 41
M 42
M 43
M 44
UL 6,14
UH 7,14
M 45
FR
AR 8,44
M 46
AR 9,68
AD 8,6
M 47
AD 9,7
AW 44,8
M 58
AW 68,9
UL 6,15
M 59
UH 7,15
AR 8,92
M 56
AR 9,116
AD 8,6
M 57
AD 9,7
AW 92,8
M 54
AW 116,9
UL 6,16
M 55
WL 0
FQ 204
UH 7,16
M 52
AR 8,45
AR 9,69
M 53
AD 8,6
AD 9,7
M 50
FZ 204
R 72
M 51
mw 24
R 74
M 48
TY 106,9736
TY 110,23560
M 49
R 76
M 60
ad 84,816
W 0
M 61
AW 45,8
AW 69,9
M 62
UL 6,17
UH 7,17
M 63
W 1
AR 8,93
M 64
AR 9,117
AD 8,6
M 65
AD 9,7
AW 93,8
M 66
W 2
AW 117,9
M 67
WL 3
AN 104,106
AN 105,107
M 68
WL 2
SH 106
AN 106,106
M 69
mw 27
AN 107,107
AN 108,110
M 70
AN 109,111
SH 110
M 71
R 78
M 82
AN 110,110
AN 111,111
M 83
TY 114,11272
TY 118,25096
M 80
ad 84,822
W 0
M 81
UL 6,18
UH 7,18
M 78
AR 8,46
AR 9,70
M 79
W 1
AD 8,6
M 76
AD 9,7
AW 46,8
M 77
AW 70,9
UL 6,19
M 74
W 2
UH 7,19
M 75
SA 214,2432
AR 8,94
M 72
WL 1
AN 112,114
AN 113,115
M 73
R 80
M 84
mw 48
SH 114
AN 114,114
M 85
WL 2
AN 115,115
AN 116,118
M 86
AN 117,119
SH 118
M 87
AN 118,118
AN 119,119
M 88
TY 122,12808
TY 126,26632
M 89
ad 84,864
W 0
M 90
AR 9,118
AD 8,6
M 91
AD 9,7
AW 94,8
M 92
W 1
AW 118,9
M 93
UL 6,20
UH 7,20
M 94
AR 8,47
AR 9,71
M 95
R 82
M 106
W 2
AD 8,6
M 107
WL 3
AN 120,122
AN 121,123
M 104
WL 2
SH 122
AN 122,122
M 105
mw 51
AN 123,123
AN 124,126
M 102
AN 125,127
SH 126
M 103
AN 126,126
AN 127,127
M 100
ad 84,870
W 0
M 101
)"
R"(AD 9,7
AW 47,8
M 98
AW 71,9
UL 6,21
M 99
W 1
UH 7,21
M 96
AR 8,95
AR 9,119
M 97
R 0
M 108
W 2
AD 8,6
M 109
AD 9,7
AW 95,8
M 110
AW 119,9
M 111
bc 49,10,66,60
M 112
mh 9
M 113
ad 66,1984
H 0
M 114
M 115
M 116
DG 1024
M 117
M 118
M 119
WL 2
R 2
M 120
bc 48,10,64,56
M 121
mh 12
M 133
ad 64,1984
H 0
M 132
TX 58,9216
TX 62,23040
M 134
M 135
H 1
M 123
M 122
TY 66,9728
TY 70,23552
M 124
H 2
M 125
M 137
M 136
TX 74,10752
TX 78,24576
PA 12288
DF
M 138
WL 4
Q 0
M 139
Q 2
M 127
Q 4
M 126
TY 82,11264
TY 86,25088
M 128
Q 6
M 129
WL 4
Q 8
M 141
Q 10
M 140
TX 90,12288
TX 94,26112
M 142
Q 12
M 143
R 6
M 131
Q 14
M 130
TY 98,12800
TY 102,26624
M 0
R 4
M 1
WL 10
Q 16
M 13
Q 18
M 12
Q 20
M 14
Q 22
M 15
WL 8
Q 24
M 3
Q 26
M 2
Q 28
M 4
Q 30
M 5
WL 6
Q 32
M 17
Q 34
M 16
Q 36
M 18
Q 38
M 19
WL 2
Q 40
M 7
Q 42
M 6
Q 44
M 8
Q 46
M 9
ae 42,72,384
M 21
s_bitcmp1_b32 s50, 8
s_cselect_b32 s52, 24576, 0
v_add_u32 v50, s52, v3
M 20
SA 216,256
M 22
ae 44,68,864
M 23
R 10
M 11
s_bitcmp1_b32 s50, 16
s_cselect_b32 s51, 24576, 0
v_add_u32 v47, s51, v3
M 10
WL 2
R 8
M 34
XL 30,0
M 35
XL 32,768
M 32
XL 34,1536
M 33
XL 36,2304
M 30
M 31
M 28
M 29
M 26
M 27
WV 11
M 24
WL 0
FP
M 25
M 36
M 37
M 38
M 39
M 40
M 41
M 42
M 43
M 44
M 45
FR
M 46
UL 6,22
UH 7,22
M 47
AR 8,48
AR 9,72
M 58
AD 8,6
AD 9,7
M 59
AW 48,8
AW 72,9
M 56
UL 6,23
UH 7,23
M 57
AR 8,96
AR 9,120
M 54
AD 8,6
AD 9,7
M 55
WL 0
FQ 208
AW 96,8
M 52
AW 120,9
UL 6,24
M 53
UH 7,24
AR 8,49
M 50
FZ 208
R 24
M 51
mw 72
R 26
M 48
TY 106,520
TY 110,14344
M 49
R 28
M 60
ad 84,912
W 0
M 61
AR 9,73
AD 8,6
M 62
AD 9,7
AW 49,8
M 63
W 1
AW 73,9
M 64
UL 6,25
UH 7,25
M 65
AR 8,97
AR 9,121
M 66
W 2
AD 8,6
M 67
WL 3
AN 104,106
AN 105,107
M 68
WL 2
SH 106
AN 106,106
M 69
mw 75
AN 107,107
AN 108,110
M 70
AN 109,111
SH 110
M 71
R 30
M 82
AN 110,110
AN 111,111
M 83
TY 114,2056
TY 118,15880
M 80
ad 84,918
W 0
M 81
AD 9,7
AW 97,8
M 78
AW 121,9
UL 6,26
M 79
W 1
UH 7,26
M 76
AR 8,50
AR 9,74
M 77
AD 8,6
AD 9,7
M 74
W 2
AW 50,8
M 75
AW 74,9
UL 6,27
M 72
WL 1
AN 112,114
AN 113,115
M 73
R 32
M 84
mw 0
SH 114
AN 114,114
M 85
WL 2
AN 115,115
AN 116,118
M 86
AN 117,119
SH 118
M 87
AN 118,118
AN 119,119
M 88
TY 122,3592
TY 126,17416
M 89
ad 84,960
W 0
M 90
SA 218,1024
UH 7,27
M 91
AR 8,98
AR 9,122
M 92
W 1
AD 8,6
M 93
AD 9,7
AW 98,8
M 94
AW 122,9
UL 6,28
M 95
R 34
M 106
W 2
UH 7,28
M 107
WL 3
AN 120,122
AN 121,123
M 104
WL 2
SH 122
AN 122,122
M 105
mw 3
AN 123,123
AN 124,126
M 102
AN 125,127
SH 126
M 103
AN 126,126
AN 127,127
M 100
ad 84,966
W 0
M 101
AR 8,51
AR 9,75
M 98
AD 8,6
AD 9,7
M 99
W 1
AW 51,8
M 96
AW 75,9
UL 6,29
M 97
R 48
M 108
W 2
UH 7,29
M 109
AR 8,99
AR 9,123
M 110
AD 8,6
AD 9,7
M 111
bc 49,11,66,60
AW 99,8
M 112
mh 18
AW 123,9
M 113
ad 66,2176
H 0
M 114
M 115
M 116
DG 1024
M 117
M 118
M 119
WL 2
R 50
M 120
bc 48,11,64,56
M 121
)"
R"(mh 21
M 133
ad 64,2176
H 0
M 132
TX 58,0
TX 62,13824
M 134
M 135
H 1
M 123
M 122
TY 66,512
TY 70,14336
M 124
H 2
M 125
M 137
M 136
TX 74,1536
TX 78,15360
M 138
WL 4
Q 0
M 139
Q 2
M 127
Q 4
M 126
TY 82,2048
TY 86,15872
M 128
Q 6
M 129
WL 4
Q 8
M 141
Q 10
M 140
TX 90,3072
TX 94,16896
M 142
Q 12
M 143
R 54
M 131
Q 14
M 130
TY 98,3584
TY 102,17408
M 0
R 52
M 1
WL 10
Q 16
M 13
Q 18
M 12
Q 20
M 14
Q 22
M 15
WL 8
Q 24
M 3
Q 26
M 2
Q 28
M 4
Q 30
M 5
WL 6
Q 32
M 17
Q 34
M 16
Q 36
M 18
Q 38
M 19
WL 2
Q 40
M 7
Q 42
M 6
Q 44
M 8
Q 46
M 9
SA 220,1792
M 21
ae 44,68,864
M 20
s_bfe_u32 s51, s50, 0x10011
v_mad_i32_i24 v47, v49, s51, v48
XL 38,0
M 22
XL 40,768
M 23
R 58
M 11
XL 42,1536
M 10
WL 2
R 56
M 34
XL 44,2304
M 35
M 32
M 33
M 30
M 31
M 28
M 29
M 26
M 27
WV 10
M 24
WL 0
FP
M 25
M 36
M 37
M 38
M 39
M 40
M 41
M 42
M 43
M 44
M 45
FR
M 46
M 47
M 58
UL 6,30
UH 7,30
M 59
AR 8,52
AR 9,76
M 56
AD 8,6
AD 9,7
M 57
AW 52,8
AW 76,9
M 54
UL 6,31
UH 7,31
M 55
WL 0
FQ 212
AR 8,100
M 52
AR 9,124
AD 8,6
M 53
AD 9,7
AW 100,8
M 50
FZ 212
R 72
M 51
mw 24
R 74
M 48
TY 106,5128
TY 110,18952
M 49
R 76
M 60
ad 84,1008
W 0
M 61
AW 124,9
UL 6,32
M 62
UH 7,32
AR 8,53
M 63
W 1
AR 9,77
M 64
AD 8,6
AD 9,7
M 65
AW 53,8
AW 77,9
M 66
W 2
UL 6,33
M 67
WL 3
AN 104,106
AN 105,107
M 68
WL 2
SH 106
AN 106,106
M 69
mw 27
AN 107,107
AN 108,110
M 70
AN 109,111
SH 110
M 71
R 78
M 82
AN 110,110
AN 111,111
M 83
TY 114,6664
TY 118,20488
M 80
ad 84,1014
W 0
M 81
UH 7,33
AR 8,101
M 78
AR 9,125
AD 8,6
M 79
W 1
AD 9,7
M 76
AW 101,8
AW 125,9
M 77
UL 6,34
UH 7,34
M 74
W 2
AR 8,54
M 75
SA 222,2560
AR 9,78
M 72
WL 1
AN 112,114
AN 113,115
M 73
R 80
M 84
mw 48
SH 114
AN 114,114
M 85
WL 2
AN 115,115
AN 116,118
M 86
AN 117,119
SH 118
M 87
AN 118,118
AN 119,119
M 88
TY 122,8200
TY 126,22024
M 89
ad 84,1056
W 0
M 90
AD 8,6
AD 9,7
M 91
AW 54,8
AW 78,9
M 92
W 1
UL 6,35
M 93
UH 7,35
AR 8,102
M 94
AR 9,126
AD 8,6
M 95
R 82
M 106
W 2
AD 9,7
M 107
WL 3
AN 120,122
AN 121,123
M 104
WL 2
SH 122
AN 122,122
M 105
mw 51
AN 123,123
AN 124,126
M 102
AN 125,127
SH 126
M 103
AN 126,126
AN 127,127
M 100
ad 84,1062
W 0
M 101
AW 102,8
AW 126,9
M 98
UL 6,36
UH 7,36
M 99
W 1
AR 8,55
M 96
AR 9,79
AD 8,6
M 97
R 0
M 108
W 2
AD 9,7
M 109
AW 55,8
AW 79,9
M 110
UL 6,37
UH 7,37
M 111
bc 49,12,66,60
AR 8,103
M 112
mh 0
AR 9,127
AD 8,6
M 113
ad 66,2368
H 0
M 114
AD 9,7
AW 103,8
M 115
AW 127,9
M 116
DG 1024
M 117
M 118
M 119
WL 2
R 2
M 120
bc 48,12,64,56
M 121
mh 3
M 133
ad 64,2368
H 0
M 132
TX 58,4608
TX 62,18432
M 134
M 135
H 1
M 123
M 122
TY 66,5120
TY 70,18944
M 124
H 2
M 125
M 137
M 136
TX 74,6144
TX 78,19968
PA 12288
DF
M 138
WL 4
Q 0
M 139
Q 2
M 127
Q 4
M 126
TY 82,6656
TY 86,20480
M 128
Q 6
M 129
WL 4
Q 8
M 141
Q 10
M 140
TX 90,7680
TX 94,21504
M 142
Q 12
M 143
R 6
)"
R"(M 131
Q 14
M 130
TY 98,8192
TY 102,22016
M 0
R 4
M 1
WL 10
Q 16
M 13
Q 18
M 12
Q 20
M 14
Q 22
M 15
WL 8
Q 24
M 3
Q 26
M 2
Q 28
M 4
Q 30
M 5
WL 6
Q 32
M 17
Q 34
M 16
Q 36
M 18
Q 38
M 19
WL 2
Q 40
M 7
Q 42
M 6
Q 44
M 8
Q 46
M 9
ae 42,72,480
M 21
s_bitcmp1_b32 s50, 10
s_cselect_b32 s52, 24576, 0
v_add_u32 v50, s52, v3
M 20
SA 224,0
M 22
M 23
R 10
M 11
M 10
WL 2
R 8
M 34
M 35
M 32
M 33
M 30
M 31
M 28
M 29
M 26
ae 44,70,768
M 27
WV 7
M 24
WL 0
FP
M 25
M 36
s_bitcmp1_b32 s50, 12
s_cselect_b32 s51, 24576, 0
v_add_u32 v47, s51, v3
M 37
XL 14,128
M 38
XL 16,896
M 39
M 40
M 41
M 42
M 43
M 44
XL 18,1664
M 45
FR
M 46
M 47
M 58
M 59
M 56
XL 20,2432
UL 6,38
M 57
UH 7,38
AR 8,56
M 54
AR 9,80
AD 8,6
M 55
WL 0
FQ 216
AD 9,7
M 52
AW 56,8
AW 80,9
M 53
UL 6,39
UH 7,39
M 50
FZ 216
R 24
M 51
mw 72
R 26
M 48
TY 106,9736
TY 110,23560
M 49
R 28
M 60
ad 84,1104
W 0
M 61
AR 8,104
AR 9,128
M 62
AD 8,6
AD 9,7
M 63
W 1
AW 104,8
M 64
AW 128,9
UL 6,40
M 65
UH 7,40
AR 8,57
M 66
W 2
AR 9,81
M 67
WL 3
AN 104,106
AN 105,107
M 68
WL 2
SH 106
AN 106,106
M 69
mw 75
AN 107,107
AN 108,110
M 70
AN 109,111
SH 110
M 71
R 30
M 82
AN 110,110
AN 111,111
M 83
TY 114,11272
TY 118,25096
M 80
ad 84,1110
W 0
M 81
AD 8,6
AD 9,7
M 78
AW 57,8
AW 81,9
M 79
W 1
UL 6,41
M 76
UH 7,41
AR 8,105
M 77
AR 9,129
AD 8,6
M 74
W 2
AD 9,7
M 75
AW 105,8
AW 129,9
M 72
WL 1
AN 112,114
AN 113,115
M 73
R 32
M 84
mw 0
SH 114
AN 114,114
M 85
WL 2
AN 115,115
AN 116,118
M 86
AN 117,119
SH 118
M 87
AN 118,118
AN 119,119
M 88
TY 122,12808
TY 126,26632
M 89
ad 84,1152
W 0
M 90
SA 226,768
UL 6,42
M 91
UH 7,42
AR 8,58
M 92
W 1
AR 9,82
M 93
AD 8,6
AD 9,7
M 94
AW 58,8
AW 82,9
M 95
R 34
M 106
W 2
UL 6,43
M 107
WL 3
AN 120,122
AN 121,123
M 104
WL 2
SH 122
AN 122,122
M 105
mw 3
AN 123,123
AN 124,126
M 102
AN 125,127
SH 126
M 103
AN 126,126
AN 127,127
M 100
ad 84,1158
W 0
M 101
UH 7,43
AR 8,106
M 98
AR 9,130
AD 8,6
M 99
W 1
AD 9,7
M 96
AW 106,8
AW 130,9
M 97
R 48
M 108
W 2
UL 6,44
M 109
UH 7,44
AR 8,59
M 110
AR 9,83
AD 8,6
M 111
bc 49,13,66,60
AD 9,7
M 112
mh 9
AW 59,8
AW 83,9
M 113
ad 66,2560
H 0
M 114
UL 6,45
UH 7,45
M 115
AR 8,107
AR 9,131
M 116
DG 1024
M 117
AD 8,6
AD 9,7
M 118
AW 107,8
AW 131,9
M 119
WL 0
R 50
M 120
bc 48,13,64,56
M 121
mh 12
M 133
ad 64,2560
H 0
M 132
TX 58,9216
TX 62,23040
M 134
M 135
H 1
M 123
M 122
TY 66,9728
TY 70,23552
M 124
H 2
M 125
M 137
M 136
TX 74,10752
TX 78,24576
M 138
WL 4
Q 0
M 139
Q 2
M 127
Q 4
M 126
TY 82,11264
TY 86,25088
M 128
Q 6
M 129
WL 4
Q 8
M 141
Q 10
M 140
TX 90,12288
TX 94,26112
M 142
Q 12
M 143
R 54
M 131
Q 14
M 130
TY 98,12800
TY 102,26624
M 0
R 52
M 1
WL 10
Q 16
M 13
Q 18
M 12
Q 20
M 14
Q 22
M 15
WL 8
Q 24
M 3
Q 26
M 2
Q 28
M 4
Q 30
M 5
WL 6
Q 32
M 17
Q 34
M 16
Q 36
M 18
Q 38
M 19
WL 2
Q 40
M 7
Q 42
M 6
Q 44
M 8
)"
R"(Q 46
M 9
SA 228,1536
M 21
M 20
M 22
M 23
R 58
M 11
M 10
WL 2
R 56
M 34
M 35
M 32
M 33
M 30
M 31
M 28
M 29
M 26
M 27
WV 6
M 24
WL 0
FP
M 25
M 36
ae 44,70,768
M 37
s_bfe_u32 s51, s50, 0x1000d
v_mad_i32_i24 v47, v49, s51, v48
XL 22,128
M 38
M 39
M 40
XL 24,896
M 41
M 42
M 43
M 44
M 45
FR
M 46
XL 26,1664
M 47
M 58
M 59
M 56
M 57
M 54
XL 28,2432
M 55
WL 0
FQ 220
M 52
M 53
M 50
FZ 220
R 72
M 51
mw 24
R 74
M 48
TY 106,520
TY 110,14344
M 49
R 76
M 60
ad 84,1200
W 0
M 61
M 62
M 63
W 1
M 64
M 65
M 66
W 2
M 67
WL 3
AN 104,106
AN 105,107
M 68
WL 2
SH 106
AN 106,106
M 69
mw 27
AN 107,107
AN 108,110
M 70
AN 109,111
SH 110
M 71
R 78
M 82
AN 110,110
AN 111,111
M 83
TY 114,2056
TY 118,15880
M 80
ad 84,1206
W 0
M 81
M 78
M 79
W 1
M 76
M 77
M 74
W 2
M 75
SA 230,2304
M 72
WL 1
AN 112,114
AN 113,115
M 73
R 80
M 84
mw 48
SH 114
AN 114,114
M 85
WL 2
AN 115,115
AN 116,118
M 86
AN 117,119
SH 118
M 87
AN 118,118
AN 119,119
M 88
TY 122,3592
TY 126,17416
M 89
ad 84,1248
W 0
M 90
UL 6,14
UH 7,14
M 91
AR 8,132
AR 9,156
M 92
W 1
AD 8,6
M 93
AD 9,7
AW 132,8
M 94
AW 156,9
UL 6,15
M 95
R 82
M 106
W 2
UH 7,15
M 107
WL 3
AN 120,122
AN 121,123
M 104
WL 2
SH 122
AN 122,122
M 105
mw 51
AN 123,123
AN 124,126
M 102
AN 125,127
SH 126
M 103
AN 126,126
AN 127,127
M 100
ad 84,1254
W 0
M 101
AR 8,180
AD 8,6
M 98
AD 136,7
AW 180,8
M 99
W 1
UL 6,16
M 96
UH 7,16
AR 8,133
M 97
R 0
M 108
W 2
AR 9,157
M 109
AD 8,6
AD 9,7
M 110
AW 133,8
AW 157,9
M 111
bc 49,14,66,60
UL 6,17
M 112
mh 18
UH 7,17
AR 8,181
M 113
ad 66,2752
H 0
M 114
AD 8,6
AD 137,7
M 115
AW 181,8
UL 6,18
M 116
DG 1024
M 117
UH 7,18
AR 8,134
M 118
AR 9,158
AD 8,6
M 119
WL 0
R 2
M 120
bc 48,14,64,56
AD 9,7
M 121
mh 21
AW 134,8
AW 158,9
M 133
ad 64,2752
H 0
M 132
TX 58,0
TX 62,13824
M 134
UL 6,19
UH 7,19
M 135
H 1
AR 8,182
M 123
AD 8,6
AD 138,7
M 122
TY 66,512
TY 70,14336
M 124
H 2
AW 182,8
M 125
UL 6,20
UH 7,20
M 137
AR 8,135
AR 9,159
M 136
TX 74,1536
TX 78,15360
PA 12288
DF
M 138
WL 4
Q 0
M 139
Q 2
M 127
Q 4
M 126
TY 82,2048
TY 86,15872
M 128
Q 6
M 129
WL 4
Q 8
M 141
Q 10
M 140
TX 90,3072
TX 94,16896
M 142
Q 12
M 143
R 6
M 131
Q 14
M 130
TY 98,3584
TY 102,17408
M 0
R 4
M 1
WL 10
Q 16
M 13
Q 18
M 12
Q 20
M 14
Q 22
M 15
WL 8
Q 24
M 3
Q 26
M 2
Q 28
M 4
Q 30
M 5
WL 6
Q 32
M 17
Q 34
M 16
Q 36
M 18
Q 38
M 19
WL 2
Q 40
M 7
Q 42
M 6
Q 44
M 8
Q 46
M 9
ae 42,72,480
M 21
s_bfe_u32 s52, s50, 0x1000b
v_mad_i32_i24 v50, v49, s52, v48
SA 232,0
M 20
AD 8,6
AD 9,7
M 22
AW 135,8
AW 159,9
M 23
R 10
M 11
UL 6,21
UH 7,21
M 10
WL 2
R 8
M 34
AR 8,183
AD 8,6
M 35
AD 139,7
AW 183,8
M 32
M 33
M 30
M 31
M 28
M 29
M 26
M 27
WV 7
M 24
WL 0
FP
M 25
M 36
ae 44,68,768
M 37
s_bitcmp1_b32 s50, 14
s_cselect_b32 s51, 24576, 0
v_add_u32 v47, s51, v3
M 38
XL 30,128
M 39
M 40
M 41
M 42
XL 32,896
M 43
M 44
M 45
FR
M 46
M 47
M 58
XL 34,1664
M 59
M 56
)"
R"(M 57
M 54
M 55
WL 0
FQ 224
M 52
XL 36,2432
M 53
M 50
FZ 224
R 24
M 51
mw 72
R 26
M 48
TY 106,5128
TY 110,18952
M 49
R 28
M 60
ad 84,1296
W 0
M 61
M 62
M 63
W 1
M 64
M 65
M 66
W 2
M 67
WL 3
AN 104,106
AN 105,107
M 68
WL 2
SH 106
AN 106,106
M 69
mw 75
AN 107,107
AN 108,110
M 70
AN 109,111
SH 110
M 71
R 30
M 82
AN 110,110
AN 111,111
M 83
TY 114,6664
TY 118,20488
M 80
ad 84,1302
W 0
M 81
M 78
M 79
W 1
M 76
M 77
M 74
W 2
M 75
M 72
WL 1
AN 112,114
AN 113,115
M 73
R 32
M 84
mw 0
SH 114
AN 114,114
M 85
WL 2
AN 115,115
AN 116,118
M 86
AN 117,119
SH 118
M 87
AN 118,118
AN 119,119
M 88
TY 122,8200
TY 126,22024
M 89
ad 84,1344
W 0
M 90
SA 234,768
UL 6,22
M 91
UH 7,22
AR 8,136
M 92
W 1
AR 9,160
M 93
AD 8,6
AD 9,7
M 94
AW 136,8
AW 160,9
M 95
R 34
M 106
W 2
UL 6,23
M 107
WL 3
AN 120,122
AN 121,123
M 104
WL 2
SH 122
AN 122,122
M 105
mw 3
AN 123,123
AN 124,126
M 102
AN 125,127
SH 126
M 103
AN 126,126
AN 127,127
M 100
ad 84,1350
W 0
M 101
UH 7,23
AR 8,184
M 98
AD 8,6
AD 140,7
M 99
W 1
AW 184,8
M 96
UL 6,24
UH 7,24
M 97
R 48
M 108
W 2
AR 8,137
M 109
AR 9,161
AD 8,6
M 110
AD 9,7
AW 137,8
M 111
bc 49,15,66,60
AW 161,9
M 112
mh 0
UL 6,25
UH 7,25
M 113
ad 66,2944
H 0
M 114
AR 8,185
AD 8,6
M 115
AD 141,7
AW 185,8
M 116
DG 1024
M 117
UL 6,26
UH 7,26
M 118
AR 8,138
AR 9,162
M 119
WL 0
R 50
M 120
bc 48,15,64,56
AD 8,6
M 121
mh 3
AD 9,7
AW 138,8
M 133
ad 64,2944
H 0
M 132
TX 58,4608
TX 62,18432
M 134
AW 162,9
UL 6,27
M 135
H 1
UH 7,27
M 123
AR 8,186
AD 8,6
M 122
TY 66,5120
TY 70,18944
M 124
H 2
AD 142,7
M 125
AW 186,8
UL 6,28
M 137
UH 7,28
AR 8,139
M 136
TX 74,6144
TX 78,19968
M 138
WL 4
Q 0
M 139
Q 2
M 127
Q 4
M 126
TY 82,6656
TY 86,20480
M 128
Q 6
M 129
WL 4
Q 8
M 141
Q 10
M 140
TX 90,7680
TX 94,21504
M 142
Q 12
M 143
R 54
M 131
Q 14
M 130
TY 98,8192
TY 102,22016
M 0
R 52
M 1
WL 10
Q 16
M 13
Q 18
M 12
Q 20
M 14
Q 22
M 15
WL 8
Q 24
M 3
Q 26
M 2
Q 28
M 4
Q 30
M 5
WL 6
Q 32
M 17
Q 34
M 16
Q 36
M 18
Q 38
M 19
WL 2
Q 40
M 7
Q 42
M 6
Q 44
M 8
Q 46
M 9
SA 236,1536
AR 9,163
M 21
AD 8,6
AD 9,7
M 20
AW 139,8
AW 163,9
M 22
UL 6,29
UH 7,29
M 23
R 58
M 11
AR 8,187
AD 8,6
M 10
WL 2
R 56
M 34
AD 143,7
AW 187,8
M 35
M 32
M 33
M 30
M 31
M 28
M 29
M 26
M 27
WV 6
M 24
WL 0
FP
M 25
M 36
M 37
M 38
ae 44,68,768
M 39
s_bfe_u32 s51, s50, 0x1000f
v_mad_i32_i24 v47, v49, s51, v48
XL 38,128
M 40
M 41
M 42
M 43
M 44
XL 40,896
M 45
FR
M 46
M 47
M 58
M 59
M 56
XL 42,1664
M 57
M 54
M 55
WL 0
FQ 228
M 52
M 53
M 50
FZ 228
R 72
M 51
mw 24
R 74
M 48
TY 106,9736
TY 110,23560
M 49
R 76
M 60
ad 84,1392
W 0
M 61
XL 44,2432
M 62
M 63
W 1
M 64
M 65
M 66
W 2
M 67
WL 3
AN 104,106
AN 105,107
M 68
WL 2
SH 106
AN 106,106
M 69
mw 27
AN 107,107
AN 108,110
M 70
AN 109,111
SH 110
M 71
R 78
M 82
AN 110,110
AN 111,111
M 83
TY 114,11272
TY 118,25096
M 80
ad 84,1398
)"
R"(W 0
M 81
M 78
M 79
W 1
M 76
M 77
M 74
W 2
M 75
SA 238,2304
M 72
WL 1
AN 112,114
AN 113,115
M 73
R 80
M 84
mw 48
SH 114
AN 114,114
M 85
WL 2
AN 115,115
AN 116,118
M 86
AN 117,119
SH 118
M 87
AN 118,118
AN 119,119
M 88
TY 122,12808
TY 126,26632
M 89
ad 84,1440
W 0
M 90
M 91
M 92
W 1
UL 6,30
M 93
UH 7,30
AR 8,140
M 94
AR 9,164
AD 8,6
M 95
R 82
M 106
W 2
AD 9,7
M 107
WL 3
AN 120,122
AN 121,123
M 104
WL 2
SH 122
AN 122,122
M 105
mw 51
AN 123,123
AN 124,126
M 102
AN 125,127
SH 126
M 103
AN 126,126
AN 127,127
M 100
ad 84,1446
W 0
M 101
AW 140,8
AW 164,9
M 98
UL 6,31
UH 7,31
M 99
W 1
AR 8,188
M 96
AD 8,6
AD 144,7
M 97
R 0
M 108
W 2
AW 188,8
M 109
UL 6,32
UH 7,32
M 110
AR 8,141
AR 9,165
M 111
bc 49,16,66,60
AD 8,6
M 112
mh 9
AD 9,7
AW 141,8
M 113
ad 66,3136
H 0
M 114
AW 165,9
UL 6,33
M 115
UH 7,33
AR 8,189
M 116
DG 1024
M 117
AD 8,6
AD 145,7
M 118
AW 189,8
UL 6,34
M 119
WL 0
R 2
M 120
bc 48,16,64,56
UH 7,34
M 121
mh 12
AR 8,142
AR 9,166
M 133
ad 64,3136
H 0
M 132
TX 58,9216
TX 62,23040
M 134
AD 8,6
AD 9,7
M 135
H 1
AW 142,8
M 123
AW 166,9
UL 6,35
M 122
TY 66,9728
TY 70,23552
M 124
H 2
UH 7,35
M 125
AR 8,190
AD 8,6
M 137
AD 146,7
AW 190,8
M 136
TX 74,10752
TX 78,24576
PA 12288
DF
M 138
WL 4
Q 0
M 139
Q 2
M 127
Q 4
M 126
TY 82,11264
TY 86,25088
M 128
Q 6
M 129
WL 4
Q 8
M 141
Q 10
M 140
TX 90,12288
TX 94,26112
M 142
Q 12
M 143
R 6
M 131
Q 14
M 130
TY 98,12800
TY 102,26624
M 0
R 4
M 1
WL 10
Q 16
M 13
Q 18
M 12
Q 20
M 14
Q 22
M 15
WL 8
Q 24
M 3
Q 26
M 2
Q 28
M 4
Q 30
M 5
WL 6
Q 32
M 17
Q 34
M 16
Q 36
M 18
Q 38
M 19
WL 2
Q 40
M 7
Q 42
M 6
Q 44
M 8
Q 46
M 9
ae 42,72,480
M 21
s_bitcmp1_b32 s50, 10
s_cselect_b32 s52, 24576, 0
v_add_u32 v50, s52, v3
M 20
SA 240,256
UL 6,36
M 22
UH 7,36
AR 8,143
M 23
R 10
M 11
AR 9,167
AD 8,6
M 10
WL 2
R 8
M 34
AD 9,7
AW 143,8
M 35
AW 167,9
UL 6,37
M 32
UH 7,37
AR 8,191
M 33
AD 8,6
AD 147,7
M 30
AW 191,8
M 31
M 28
M 29
M 26
M 27
WV 7
M 24
WL 0
FP
M 25
M 36
M 37
M 38
M 39
M 40
ae 44,68,864
M 41
s_bitcmp1_b32 s50, 16
s_cselect_b32 s51, 24576, 0
v_add_u32 v47, s51, v3
M 42
XL 14,128
M 43
M 44
M 45
FR
M 46
XL 16,896
M 47
M 58
M 59
M 56
M 57
M 54
XL 18,1664
M 55
WL 0
FQ 232
M 52
M 53
M 50
FZ 232
R 24
M 51
mw 72
R 26
M 48
TY 106,520
TY 110,14344
M 49
R 28
M 60
ad 84,1488
W 0
M 61
XL 20,2432
M 62
M 63
W 1
M 64
M 65
M 66
W 2
M 67
WL 3
AN 104,106
AN 105,107
M 68
WL 2
SH 106
AN 106,106
M 69
mw 75
AN 107,107
AN 108,110
M 70
AN 109,111
SH 110
M 71
R 30
M 82
AN 110,110
AN 111,111
M 83
TY 114,2056
TY 118,15880
M 80
ad 84,1494
W 0
M 81
M 78
M 79
W 1
M 76
M 77
M 74
W 2
M 75
M 72
WL 1
AN 112,114
AN 113,115
M 73
R 32
M 84
mw 0
SH 114
AN 114,114
M 85
WL 2
AN 115,115
AN 116,118
M 86
AN 117,119
SH 118
M 87
AN 118,118
AN 119,119
M 88
TY 122,3592
TY 126,17416
M 89
ad 84,1536
W 0
M 90
SA 242,1024
M 91
M 92
W 1
M 93
M 94
UL 6,38
UH 7,38
M 95
R 34
M 106
)"
R"(W 2
AR 8,144
M 107
WL 3
AN 120,122
AN 121,123
M 104
WL 2
SH 122
AN 122,122
M 105
mw 3
AN 123,123
AN 124,126
M 102
AN 125,127
SH 126
M 103
AN 126,126
AN 127,127
M 100
ad 84,1542
W 0
M 101
AR 9,168
AD 8,6
M 98
AD 9,7
AW 144,8
M 99
W 1
AW 168,9
M 96
UL 6,39
UH 7,39
M 97
R 48
M 108
W 2
AR 8,192
M 109
AD 8,6
AD 148,7
M 110
AW 192,8
UL 6,40
M 111
bc 49,17,66,60
UH 7,40
M 112
mh 18
AR 8,145
AR 9,169
M 113
ad 66,3328
H 0
M 114
AD 8,6
AD 9,7
M 115
AW 145,8
AW 169,9
M 116
DG 1024
M 117
UL 6,41
UH 7,41
M 118
AR 8,193
AD 8,6
M 119
WL 0
R 50
M 120
bc 48,17,64,56
AD 149,7
M 121
mh 21
AW 193,8
UL 6,42
M 133
ad 64,3328
H 0
M 132
TX 58,0
TX 62,13824
M 134
UH 7,42
AR 8,146
M 135
H 1
AR 9,170
M 123
AD 8,6
AD 9,7
M 122
TY 66,512
TY 70,14336
M 124
H 2
AW 146,8
M 125
AW 170,9
UL 6,43
M 137
UH 7,43
AR 8,194
M 136
TX 74,1536
TX 78,15360
M 138
WL 4
Q 0
M 139
Q 2
M 127
Q 4
M 126
TY 82,2048
TY 86,15872
M 128
Q 6
M 129
WL 4
Q 8
M 141
Q 10
M 140
TX 90,3072
TX 94,16896
M 142
Q 12
M 143
R 54
M 131
Q 14
M 130
TY 98,3584
TY 102,17408
M 0
R 52
M 1
WL 10
Q 16
M 13
Q 18
M 12
Q 20
M 14
Q 22
M 15
WL 8
Q 24
M 3
Q 26
M 2
Q 28
M 4
Q 30
M 5
WL 6
Q 32
M 17
Q 34
M 16
Q 36
M 18
Q 38
M 19
WL 2
Q 40
M 7
Q 42
M 6
Q 44
M 8
Q 46
M 9
SA 244,1792
AD 8,6
M 21
AD 150,7
AW 194,8
M 20
UL 6,44
UH 7,44
M 22
AR 8,147
AR 9,171
M 23
R 58
M 11
AD 8,6
AD 9,7
M 10
WL 2
R 56
M 34
AW 147,8
AW 171,9
M 35
UL 6,45
UH 7,45
M 32
AR 8,195
AD 8,6
M 33
AD 151,7
AW 195,8
M 30
M 31
M 28
M 29
M 26
M 27
WV 6
M 24
WL 0
FP
M 25
M 36
M 37
M 38
M 39
M 40
M 41
M 42
ae 44,68,864
M 43
s_bfe_u32 s51, s50, 0x10011
v_mad_i32_i24 v47, v49, s51, v48
XL 22,128
M 44
M 45
FR
M 46
M 47
M 58
XL 24,896
M 59
M 56
M 57
M 54
M 55
WL 0
FQ 236
M 52
XL 26,1664
M 53
M 50
FZ 236
R 72
M 51
mw 24
R 74
M 48
TY 106,5128
TY 110,18952
M 49
R 76
M 60
ad 84,1584
W 0
M 61
XL 28,2432
M 62
M 63
W 1
M 64
M 65
M 66
W 2
M 67
WL 3
AN 104,106
AN 105,107
M 68
WL 2
SH 106
AN 106,106
M 69
mw 27
AN 107,107
AN 108,110
M 70
AN 109,111
SH 110
M 71
R 78
M 82
AN 110,110
AN 111,111
M 83
TY 114,6664
TY 118,20488
M 80
ad 84,1590
W 0
M 81
M 78
M 79
W 1
M 76
M 77
M 74
W 2
M 75
SA 246,2560
M 72
WL 1
AN 112,114
AN 113,115
M 73
R 80
M 84
mw 48
SH 114
AN 114,114
M 85
WL 2
AN 115,115
AN 116,118
M 86
AN 117,119
SH 118
M 87
AN 118,118
AN 119,119
M 88
TY 122,8200
TY 126,22024
M 89
ad 84,1632
W 0
M 90
M 91
M 92
W 1
M 93
M 94
M 95
R 82
M 106
W 2
UL 6,14
M 107
WL 3
AN 120,122
AN 121,123
M 104
WL 2
SH 122
AN 122,122
M 105
mw 51
AN 123,123
AN 124,126
M 102
AN 125,127
SH 126
M 103
AN 126,126
AN 127,127
M 100
ad 84,1638
W 0
M 101
UH 7,14
AR 8,148
M 98
AR 9,172
AD 8,6
M 99
W 1
AD 9,7
M 96
AW 148,8
AW 172,9
M 97
R 0
M 108
W 2
UL 6,15
M 109
UH 7,15
AR 8,196
M 110
AD 8,6
AD 152,7
M 111
bc 49,18,66,60
AW 196,8
M 112
mh 0
UL 6,16
UH 7,16
M 113
ad 66,3520
H 0
M 114
)"
R"(AR 8,149
AR 9,173
M 115
AD 8,6
AD 9,7
M 116
DG 1024
M 117
AW 149,8
AW 173,9
M 118
UL 6,17
UH 7,17
M 119
WL 0
R 2
M 120
bc 48,18,64,56
AR 8,197
M 121
mh 3
AD 8,6
AD 153,7
M 133
ad 64,3520
H 0
M 132
TX 58,4608
TX 62,18432
M 134
AW 197,8
UL 6,18
M 135
H 1
UH 7,18
M 123
AR 8,150
AR 9,174
M 122
TY 66,5120
TY 70,18944
M 124
H 2
AD 8,6
M 125
AD 9,7
AW 150,8
M 137
AW 174,9
UL 6,19
M 136
TX 74,6144
TX 78,19968
PA 12288
DF
M 138
WL 4
Q 0
M 139
Q 2
M 127
Q 4
M 126
TY 82,6656
TY 86,20480
M 128
Q 6
M 129
WL 4
Q 8
M 141
Q 10
M 140
TX 90,7680
TX 94,21504
M 142
Q 12
M 143
R 6
M 131
Q 14
M 130
TY 98,8192
TY 102,22016
M 0
R 4
M 1
WL 10
Q 16
M 13
Q 18
M 12
Q 20
M 14
Q 22
M 15
WL 8
Q 24
M 3
Q 26
M 2
Q 28
M 4
Q 30
M 5
WL 6
Q 32
M 17
Q 34
M 16
Q 36
M 18
Q 38
M 19
WL 2
Q 40
M 7
Q 42
M 6
Q 44
M 8
Q 46
M 9
ae 42,72,480
M 21
s_bfe_u32 s52, s50, 0x1000b
v_mad_i32_i24 v50, v49, s52, v48
SA 248,256
M 20
UH 7,19
AR 8,198
M 22
AD 8,6
AD 154,7
M 23
R 10
M 11
AW 198,8
UL 6,20
M 10
WL 2
R 8
M 34
UH 7,20
AR 8,151
M 35
AR 9,175
AD 8,6
M 32
AD 9,7
AW 151,8
M 33
AW 175,9
UL 6,21
M 30
UH 7,21
AR 8,199
M 31
AD 8,6
AD 155,7
M 28
AW 199,8
M 29
M 26
M 27
WV 7
M 24
WL 0
FP
M 25
M 36
M 37
M 38
M 39
M 40
M 41
M 42
M 43
M 44
M 45
FR
M 46
M 47
M 58
M 59
M 56
M 57
M 54
M 55
WL 0
FQ 240
M 52
M 53
M 50
FZ 240
R 24
M 51
mw 72
R 26
M 48
TY 106,9736
TY 110,23560
M 49
R 28
M 60
ad 84,1680
W 0
M 61
M 62
M 63
W 1
M 64
M 65
M 66
W 2
M 67
WL 3
AN 104,106
AN 105,107
M 68
WL 2
SH 106
AN 106,106
M 69
mw 75
AN 107,107
AN 108,110
M 70
AN 109,111
SH 110
M 71
R 30
M 82
AN 110,110
AN 111,111
M 83
TY 114,11272
TY 118,25096
M 80
ad 84,1686
W 0
M 81
ae 44,70,768
M 78
s_bitcmp1_b32 s50, 12
s_cselect_b32 s51, 24576, 0
v_add_u32 v47, s51, v3
M 79
W 1
XL 30,256
M 76
XL 32,1024
M 77
M 74
W 2
M 75
M 72
WL 1
AN 112,114
AN 113,115
M 73
R 32
M 84
mw 0
SH 114
AN 114,114
M 85
WL 2
AN 115,115
AN 116,118
M 86
AN 117,119
SH 118
M 87
AN 118,118
AN 119,119
M 88
TY 122,12808
TY 126,26632
M 89
ad 84,1728
W 0
M 90
SA 250,1024
XL 34,1792
M 91
XL 36,2560
M 92
W 1
M 93
M 94
M 95
R 34
M 106
W 2
M 107
WL 3
AN 120,122
AN 121,123
M 104
WL 2
SH 122
AN 122,122
M 105
mw 3
AN 123,123
AN 124,126
M 102
AN 125,127
SH 126
M 103
AN 126,126
AN 127,127
M 100
ad 84,1734
W 0
M 101
UL 6,22
UH 7,22
M 98
AR 8,152
AR 9,176
M 99
W 1
AD 8,6
M 96
AD 9,7
AW 152,8
M 97
R 48
M 108
W 2
AW 176,9
M 109
UL 6,23
UH 7,23
M 110
AD 132,6
AD 156,7
M 111
bc 49,19,66,60
UL 6,24
M 112
mh 9
UH 7,24
AR 8,153
M 113
ad 66,3712
H 0
M 114
AR 9,177
AD 8,6
M 115
AD 9,7
AW 153,8
M 116
DG 1024
M 117
AW 177,9
UL 6,25
M 118
UH 7,25
AD 133,6
M 119
WL 0
R 50
M 120
bc 48,19,64,56
AD 157,7
M 121
mh 12
UL 6,26
UH 7,26
M 133
ad 64,3712
H 0
M 132
TX 58,9216
TX 62,23040
M 134
AR 8,154
AR 9,178
M 135
H 1
AD 8,6
M 123
AD 9,7
AW 154,8
M 122
TY 66,9728
TY 70,23552
M 124
H 2
AW 178,9
M 125
UL 6,27
UH 7,27
M 137
AD 134,6
AD 158,7
)"
R"(M 136
TX 74,10752
TX 78,24576
M 138
WL 4
Q 0
M 139
Q 2
M 127
Q 4
M 126
TY 82,11264
TY 86,25088
M 128
Q 6
M 129
WL 4
Q 8
M 141
Q 10
M 140
TX 90,12288
TX 94,26112
M 142
Q 12
M 143
R 54
M 131
Q 14
M 130
TY 98,12800
TY 102,26624
M 0
R 52
M 1
WL 10
Q 16
M 13
Q 18
M 12
Q 20
M 14
Q 22
M 15
WL 8
Q 24
M 3
Q 26
M 2
Q 28
M 4
Q 30
M 5
WL 6
Q 32
M 17
Q 34
M 16
Q 36
M 18
Q 38
M 19
WL 2
Q 40
M 7
Q 42
M 6
Q 44
M 8
Q 46
M 9
SA 252,1792
UL 6,28
M 21
UH 7,28
AR 8,155
M 20
AR 9,179
AD 8,6
M 22
AD 9,7
AW 155,8
M 23
R 58
M 11
AW 179,9
UL 6,29
M 10
WL 2
R 56
M 34
UH 7,29
AD 135,6
M 35
AD 159,7
M 32
M 33
M 30
M 31
M 28
M 29
M 26
M 27
WV 6
M 24
WL 0
FP
M 25
M 36
M 37
M 38
M 39
M 40
M 41
M 42
M 43
M 44
M 45
FR
M 46
M 47
M 58
M 59
M 56
M 57
M 54
M 55
WL 0
FQ 244
M 52
M 53
M 50
FZ 244
R 72
M 51
mw 24
R 74
M 48
TY 106,520
TY 110,14344
M 49
R 76
M 60
ad 84,1776
W 0
M 61
M 62
M 63
W 1
M 64
M 65
M 66
W 2
M 67
WL 3
AN 104,106
AN 105,107
M 68
WL 2
SH 106
AN 106,106
M 69
mw 27
AN 107,107
AN 108,110
M 70
AN 109,111
SH 110
M 71
R 78
M 82
AN 110,110
AN 111,111
M 83
TY 114,2056
TY 118,15880
M 80
ad 84,1782
W 0
M 81
ae 44,70,768
M 78
s_bfe_u32 s51, s50, 0x1000d
v_mad_i32_i24 v47, v49, s51, v48
XL 38,256
M 79
W 1
M 76
M 77
M 74
W 2
XL 40,1024
M 75
SA 254,2560
M 72
WL 1
AN 112,114
AN 113,115
M 73
R 80
M 84
mw 48
SH 114
AN 114,114
M 85
WL 2
AN 115,115
AN 116,118
M 86
AN 117,119
SH 118
M 87
AN 118,118
AN 119,119
M 88
TY 122,3592
TY 126,17416
M 89
ad 84,1824
W 0
M 90
XL 42,1792
M 91
M 92
W 1
XL 44,2560
M 93
M 94
M 95
R 82
M 106
W 2
M 107
WL 3
AN 120,122
AN 121,123
M 104
WL 2
SH 122
AN 122,122
M 105
mw 51
AN 123,123
AN 124,126
M 102
AN 125,127
SH 126
M 103
AN 126,126
AN 127,127
M 100
ad 84,1830
W 0
M 101
M 98
M 99
W 1
M 96
M 97
R 0
M 108
W 2
M 109
M 110
M 111
bc 49,20,66,60
M 112
mh 18
M 113
ad 66,3904
H 0
M 114
M 115
M 116
DG 1024
M 117
M 118
M 119
WL 2
R 2
M 120
bc 48,20,64,56
M 121
mh 21
M 133
ad 64,3904
H 0
M 132
TX 58,0
TX 62,13824
M 134
M 135
H 1
M 123
UL 6,30
UH 7,30
M 122
TY 66,512
TY 70,14336
M 124
H 2
AD 160,6
M 125
AD 184,7
UL 6,31
M 137
UH 7,31
AD 208,6
M 136
TX 74,1536
TX 78,15360
M 138
WL 4
Q 0
M 139
Q 2
M 127
Q 4
M 126
TY 82,2048
TY 86,15872
M 128
Q 6
M 129
WL 4
Q 8
M 141
Q 10
M 140
TX 90,3072
TX 94,16896
M 142
Q 12
M 143
R 6
M 131
Q 14
M 130
TY 98,3584
TY 102,17408
M 0
R 4
M 1
WL 10
Q 16
M 13
Q 18
M 12
Q 20
M 14
Q 22
M 15
WL 8
Q 24
M 3
Q 26
M 2
Q 28
M 4
Q 30
M 5
WL 6
Q 32
M 17
Q 34
M 16
Q 36
M 18
Q 38
M 19
WL 2
Q 40
M 7
Q 42
M 6
Q 44
M 8
Q 46
M 9
AD 232,7
UL 6,32
M 21
UH 7,32
AD 161,6
M 20
AD 185,7
UL 6,33
M 22
UH 7,33
AD 209,6
M 23
R 10
M 11
AD 233,7
UL 6,34
M 10
WL 2
R 8
M 34
UH 7,34
)"
R"(AD 162,6
M 35
AD 186,7
UL 6,35
M 32
UH 7,35
AD 210,6
M 33
AD 234,7
UL 6,36
M 30
UH 7,36
AD 163,6
M 31
AD 187,7
UL 6,37
M 28
UH 7,37
AD 211,6
M 29
AD 235,7
M 26
M 27
WV 5
M 24
WL 0
FP
M 25
M 36
M 37
M 38
M 39
M 40
M 41
M 42
M 43
M 44
M 45
FR
M 46
M 47
M 58
M 59
M 56
M 57
M 54
M 55
WL 0
FQ 248
M 52
M 53
M 50
FZ 248
R 24
M 51
mw 72
R 26
M 48
TY 106,5128
TY 110,18952
M 49
R 28
M 60
ad 84,1872
W 0
M 61
M 62
M 63
W 1
M 64
M 65
M 66
W 2
M 67
WL 3
AN 104,106
AN 105,107
M 68
WL 2
SH 106
AN 106,106
M 69
mw 75
AN 107,107
AN 108,110
M 70
AN 109,111
SH 110
M 71
R 30
M 82
AN 110,110
AN 111,111
M 83
TY 114,6664
TY 118,20488
M 80
ad 84,1878
W 0
M 81
M 78
M 79
W 1
M 76
ae 44,68,768
M 77
s_bitcmp1_b32 s50, 14
s_cselect_b32 s51, 24576, 0
v_add_u32 v47, s51, v3
M 74
W 2
XL 14,256
M 75
M 72
WL 1
AN 112,114
AN 113,115
M 73
R 32
M 84
mw 0
SH 114
AN 114,114
M 85
WL 2
AN 115,115
AN 116,118
M 86
AN 117,119
SH 118
M 87
AN 118,118
AN 119,119
M 88
TY 122,8200
TY 126,22024
M 89
ad 84,1920
W 0
M 90
XL 16,1024
M 91
XL 18,1792
M 92
W 1
M 93
M 94
M 95
R 34
M 106
W 2
XL 20,2560
M 107
WL 3
AN 120,122
AN 121,123
M 104
WL 2
SH 122
AN 122,122
M 105
mw 3
AN 123,123
AN 124,126
M 102
AN 125,127
SH 126
M 103
AN 126,126
AN 127,127
M 100
ad 84,1926
W 0
M 101
M 98
M 99
W 1
M 96
M 97
R 48
M 108
W 2
M 109
M 110
M 111
bc 49,21,66,60
M 112
mh 0
M 113
ad 66,4096
H 0
M 114
M 115
M 116
DG 1024
M 117
M 118
M 119
WL 2
R 50
M 120
bc 48,21,64,56
M 121
mh 3
M 133
ad 64,4096
H 0
M 132
TX 58,4608
TX 62,18432
M 134
M 135
H 1
M 123
M 122
TY 66,5120
TY 70,18944
M 124
H 2
M 125
M 137
UL 6,38
UH 7,38
M 136
TX 74,6144
TX 78,19968
M 138
WL 4
Q 0
M 139
Q 2
M 127
Q 4
M 126
TY 82,6656
TY 86,20480
M 128
Q 6
M 129
WL 4
Q 8
M 141
Q 10
M 140
TX 90,7680
TX 94,21504
M 142
Q 12
M 143
R 54
M 131
Q 14
M 130
TY 98,8192
TY 102,22016
M 0
R 52
M 1
WL 10
Q 16
M 13
Q 18
M 12
Q 20
M 14
Q 22
M 15
WL 8
Q 24
M 3
Q 26
M 2
Q 28
M 4
Q 30
M 5
WL 6
Q 32
M 17
Q 34
M 16
Q 36
M 18
Q 38
M 19
WL 2
Q 40
M 7
Q 42
M 6
Q 44
M 8
Q 46
M 9
AD 164,6
AD 188,7
M 21
UL 6,39
UH 7,39
M 20
AD 212,6
AD 236,7
M 22
UL 6,40
UH 7,40
M 23
R 58
M 11
AD 165,6
AD 189,7
M 10
WL 2
R 56
M 34
UL 6,41
UH 7,41
M 35
AD 213,6
AD 237,7
M 32
UL 6,42
UH 7,42
M 33
AD 166,6
AD 190,7
M 30
UL 6,43
UH 7,43
M 31
AD 214,6
AD 238,7
M 28
UL 6,44
UH 7,44
M 29
AD 167,6
AD 191,7
M 26
UL 6,45
UH 7,45
M 27
WV 5
AD 215,6
AD 239,7
M 24
WL 0
FP
M 25
M 36
M 37
M 38
M 39
M 40
M 41
M 42
M 43
M 44
M 45
FR
M 46
M 47
M 58
M 59
M 56
M 57
M 54
M 55
WL 0
FQ 252
M 52
M 53
M 50
FZ 252
R 72
M 51
mw 24
R 74
M 48
TY 106,9736
TY 110,23560
M 49
R 76
M 60
ad 84,1968
W 0
M 61
M 62
M 63
W 1
M 64
M 65
M 66
W 2
M 67
WL 3
AN 104,106
AN 105,107
M 68
WL 2
SH 106
)"
R"(AN 106,106
M 69
mw 27
AN 107,107
AN 108,110
M 70
AN 109,111
SH 110
M 71
R 78
M 82
AN 110,110
AN 111,111
M 83
TY 114,11272
TY 118,25096
M 80
ad 84,1974
W 0
M 81
M 78
M 79
W 1
M 76
M 77
M 74
W 2
M 75
M 72
WL 1
AN 112,114
AN 113,115
M 73
R 80
M 84
mw 48
SH 114
AN 114,114
M 85
WL 2
AN 115,115
AN 116,118
M 86
AN 117,119
SH 118
M 87
AN 118,118
AN 119,119
M 88
TY 122,12808
TY 126,26632
M 89
ad 84,2016
W 0
M 90
ae 44,68,768
M 91
s_bfe_u32 s51, s50, 0x1000f
v_mad_i32_i24 v47, v49, s51, v48
XL 22,256
M 92
W 1
XL 24,1024
M 93
M 94
XL 26,1792
M 95
R 82
M 106
W 2
M 107
WL 3
AN 120,122
AN 121,123
M 104
WL 2
SH 122
AN 122,122
M 105
mw 51
AN 123,123
AN 124,126
M 102
AN 125,127
SH 126
M 103
AN 126,126
AN 127,127
M 100
ad 84,2022
W 0
M 101
XL 28,2560
M 98
M 99
W 1
M 96
M 97
R 0
M 108
W 2
M 109
M 110
M 111
bc 49,22,66,60
M 112
mh 9
M 113
ad 66,4288
H 0
M 114
M 115
M 116
DG 1024
M 117
M 118
M 119
WL 2
R 2
M 120
bc 48,22,64,56
M 121
mh 12
M 133
ad 64,4288
H 0
M 132
TX 58,9216
TX 62,23040
M 134
M 135
H 1
M 123
M 122
TY 66,9728
TY 70,23552
M 124
H 2
M 125
M 137
M 136
TX 74,10752
TX 78,24576
M 138
WL 4
Q 0
M 139
Q 2
M 127
Q 4
M 126
TY 82,11264
TY 86,25088
M 128
Q 6
M 129
WL 4
Q 8
M 141
Q 10
M 140
TX 90,12288
TX 94,26112
M 142
Q 12
M 143
R 6
M 131
Q 14
M 130
TY 98,12800
TY 102,26624
M 0
R 4
M 1
WL 10
Q 16
M 13
Q 18
M 12
Q 20
M 14
Q 22
M 15
WL 8
Q 24
M 3
Q 26
M 2
Q 28
M 4
Q 30
M 5
WL 6
Q 32
M 17
Q 34
M 16
Q 36
M 18
Q 38
M 19
WL 2
Q 40
M 7
Q 42
M 6
Q 44
M 8
Q 46
M 9
UL 6,14
UH 7,14
M 21
AD 168,6
AD 192,7
M 20
UL 6,15
UH 7,15
M 22
AD 216,6
AD 240,7
M 23
R 10
M 11
UL 6,16
UH 7,16
M 10
WL 2
R 8
M 34
AD 169,6
AD 193,7
M 35
UL 6,17
UH 7,17
M 32
AD 217,6
AD 241,7
M 33
UL 6,18
UH 7,18
M 30
AD 170,6
AD 194,7
M 31
UL 6,19
UH 7,19
M 28
AD 218,6
AD 242,7
M 29
UL 6,20
UH 7,20
M 26
AD 171,6
AD 195,7
M 27
WV 5
UL 6,21
UH 7,21
M 24
WL 0
FP
AD 219,6
M 25
AD 243,7
M 36
M 37
M 38
M 39
M 40
M 41
M 42
M 43
M 44
M 45
FR
M 46
M 47
M 58
M 59
M 56
M 57
M 54
M 55
WL 0
FQ 256
M 52
M 53
M 50
FZ 256
R 24
M 51
mw 72
R 26
M 48
TY 106,520
TY 110,14344
M 49
R 28
M 60
ad 84,2064
W 0
M 61
M 62
M 63
W 1
M 64
M 65
M 66
W 2
M 67
WL 3
AN 104,106
AN 105,107
M 68
WL 2
SH 106
AN 106,106
M 69
mw 75
AN 107,107
AN 108,110
M 70
AN 109,111
SH 110
M 71
R 30
M 82
AN 110,110
AN 111,111
M 83
TY 114,2056
TY 118,15880
M 80
ad 84,2070
W 0
M 81
M 78
M 79
W 1
M 76
M 77
M 74
W 2
M 75
M 72
WL 1
AN 112,114
AN 113,115
M 73
R 32
M 84
mw 0
SH 114
AN 114,114
M 85
WL 2
AN 115,115
AN 116,118
M 86
AN 117,119
SH 118
M 87
AN 118,118
AN 119,119
M 88
TY 122,3592
TY 126,17416
M 89
ad 84,2112
W 0
M 90
ae 44,68,864
M 91
s_bitcmp1_b32 s50, 16
s_cselect_b32 s51, 24576, 0
v_add_u32 v47, s51, v3
M 92
W 1
XL 30,256
M 93
XL 32,1024
M 94
M 95
R 34
M 106
W 2
M 107
WL 3
AN 120,122
AN 121,123
M 104
WL 2
SH 122
AN 122,122
M 105
mw 3
AN 123,123
)"
R"(AN 124,126
M 102
AN 125,127
SH 126
M 103
AN 126,126
AN 127,127
M 100
ad 84,2118
W 0
M 101
XL 34,1792
M 98
XL 36,2560
M 99
W 1
M 96
M 97
R 48
M 108
W 2
M 109
M 110
M 111
bc 49,23,66,60
M 112
mh 18
M 113
ad 66,4480
H 0
M 114
M 115
M 116
DG 1024
M 117
M 118
M 119
WL 2
R 50
M 120
bc 48,23,64,56
M 121
mh 21
M 133
ad 64,4480
H 0
M 132
TX 58,0
TX 62,13824
M 134
M 135
H 1
M 123
M 122
TY 66,512
TY 70,14336
M 124
H 2
M 125
M 137
M 136
TX 74,1536
TX 78,15360
M 138
WL 4
Q 0
M 139
Q 2
M 127
Q 4
M 126
TY 82,2048
TY 86,15872
M 128
Q 6
M 129
WL 4
Q 8
M 141
Q 10
M 140
TX 90,3072
TX 94,16896
M 142
Q 12
M 143
R 54
M 131
Q 14
M 130
TY 98,3584
TY 102,17408
M 0
R 52
M 1
WL 10
Q 16
M 13
Q 18
M 12
Q 20
M 14
Q 22
M 15
WL 8
Q 24
M 3
Q 26
M 2
Q 28
M 4
Q 30
M 5
WL 6
Q 32
M 17
Q 34
M 16
Q 36
M 18
Q 38
M 19
WL 2
Q 40
M 7
Q 42
M 6
Q 44
M 8
Q 46
M 9
UL 6,22
UH 7,22
M 21
AD 172,6
AD 196,7
M 20
UL 6,23
UH 7,23
M 22
AD 220,6
AD 244,7
M 23
R 58
M 11
UL 6,24
UH 7,24
M 10
WL 2
R 56
M 34
AD 173,6
AD 197,7
M 35
UL 6,25
UH 7,25
M 32
AD 221,6
AD 245,7
M 33
UL 6,26
UH 7,26
M 30
AD 174,6
AD 198,7
M 31
UL 6,27
UH 7,27
M 28
AD 222,6
AD 246,7
M 29
UL 6,28
UH 7,28
M 26
AD 175,6
AD 199,7
M 27
WV 5
UL 6,29
UH 7,29
M 24
WL 0
FP
AD 223,6
M 25
AD 247,7
M 36
M 37
M 38
M 39
M 40
M 41
M 42
M 43
M 44
M 45
FR
M 46
M 47
M 58
M 59
M 56
M 57
M 54
M 55
WL 0
FQ 260
M 52
M 53
M 50
FZ 260
R 72
M 51
mw 24
R 74
M 48
TY 106,5128
TY 110,18952
M 49
R 76
M 60
ad 84,2160
W 0
M 61
M 62
M 63
W 1
M 64
M 65
M 66
W 2
M 67
WL 3
AN 104,106
AN 105,107
M 68
WL 2
SH 106
AN 106,106
M 69
mw 27
AN 107,107
AN 108,110
M 70
AN 109,111
SH 110
M 71
R 78
M 82
AN 110,110
AN 111,111
M 83
TY 114,6664
TY 118,20488
M 80
ad 84,2166
W 0
M 81
M 78
M 79
W 1
M 76
M 77
M 74
W 2
M 75
M 72
WL 1
AN 112,114
AN 113,115
M 73
R 80
M 84
mw 48
SH 114
AN 114,114
M 85
WL 2
AN 115,115
AN 116,118
M 86
AN 117,119
SH 118
M 87
AN 118,118
AN 119,119
M 88
TY 122,8200
TY 126,22024
M 89
ad 84,2208
W 0
M 90
ae 44,68,864
M 91
s_bfe_u32 s51, s50, 0x10011
v_mad_i32_i24 v47, v49, s51, v48
XL 38,256
M 92
W 1
M 93
M 94
M 95
R 82
M 106
W 2
XL 40,1024
M 107
WL 3
AN 120,122
AN 121,123
M 104
WL 2
SH 122
AN 122,122
M 105
mw 51
AN 123,123
AN 124,126
M 102
AN 125,127
SH 126
M 103
AN 126,126
AN 127,127
M 100
ad 84,2214
W 0
M 101
XL 42,1792
M 98
M 99
W 1
M 96
M 97
R 0
M 108
W 2
XL 44,2560
M 109
M 110
M 111
bc 48,0,64,56
M 112
mh 3
M 113
ad 64,64
H 0
M 114
M 115
M 116
H 1
M 117
M 118
M 119
WL 2
R 2
M 120
H 2
M 121
M 133
M 132
TX 58,4608
TX 62,18432
M 134
M 135
M 123
M 122
TY 66,5120
TY 70,18944
M 124
M 125
M 137
M 136
TX 74,6144
TX 78,19968
M 138
WL 4
Q 0
M 139
Q 2
M 127
Q 4
M 126
TY 82,6656
TY 86,20480
M 128
Q 6
M 129
WL 4
Q 8
M 141
Q 10
M 140
TX 90,7680
TX 94,21504
M 142
Q 12
M 143
)"
R"(R 6
M 131
Q 14
M 130
TY 98,8192
TY 102,22016
M 0
R 4
M 1
WL 10
Q 16
M 13
Q 18
M 12
Q 20
M 14
Q 22
M 15
WL 8
Q 24
M 3
Q 26
M 2
Q 28
M 4
Q 30
M 5
WL 6
Q 32
M 17
Q 34
M 16
Q 36
M 18
Q 38
M 19
WL 2
Q 40
M 7
Q 42
M 6
Q 44
M 8
Q 46
M 9
UL 6,30
UH 7,30
M 21
AD 176,6
AD 200,7
M 20
UL 6,31
UH 7,31
M 22
AD 224,6
AD 248,7
M 23
R 10
M 11
UL 6,32
UH 7,32
M 10
WL 2
R 8
M 34
AD 177,6
AD 201,7
M 35
UL 6,33
UH 7,33
M 32
AD 225,6
AD 249,7
M 33
UL 6,34
UH 7,34
M 30
AD 178,6
AD 202,7
M 31
UL 6,35
UH 7,35
M 28
AD 226,6
AD 250,7
M 29
UL 6,36
UH 7,36
M 26
AD 179,6
AD 203,7
M 27
WV 4
UL 6,37
UH 7,37
M 24
WL 0
FP
AD 227,6
M 25
AD 251,7
M 36
M 37
M 38
M 39
M 40
M 41
M 42
M 43
M 44
M 45
FR
M 46
M 47
M 58
M 59
M 56
M 57
M 54
M 55
WL 0
FQ 264
M 52
M 53
M 50
FZ 264
R 24
M 51
mw 72
R 26
M 48
TY 106,9736
TY 110,23560
M 49
R 28
M 60
ad 84,2256
W 0
M 61
M 62
M 63
W 1
M 64
M 65
M 66
W 2
M 67
WL 3
AN 104,106
AN 105,107
M 68
WL 2
SH 106
AN 106,106
M 69
mw 75
AN 107,107
AN 108,110
M 70
AN 109,111
SH 110
M 71
R 30
M 82
AN 110,110
AN 111,111
M 83
TY 114,11272
TY 118,25096
M 80
ad 84,2262
W 0
M 81
M 78
M 79
W 1
M 76
M 77
M 74
W 2
M 75
M 72
WL 1
AN 112,114
AN 113,115
M 73
R 32
M 84
mw 0
SH 114
AN 114,114
M 85
WL 2
AN 115,115
AN 116,118
M 86
AN 117,119
SH 118
M 87
AN 118,118
AN 119,119
M 88
TY 122,12808
TY 126,26632
M 89
ad 84,0
W 0
M 90
M 91
M 92
W 1
M 93
M 94
M 95
R 34
M 106
W 2
M 107
WL 3
AN 120,122
AN 121,123
M 104
WL 2
SH 122
AN 122,122
M 105
mw 3
AN 123,123
AN 124,126
M 102
AN 125,127
SH 126
M 103
AN 126,126
AN 127,127
M 100
ad 84,6
W 0
M 101
M 98
M 99
W 1
M 96
M 97
R 48
M 108
W 2
M 109
M 110
M 111
bc 48,1,64,56
M 112
mh 15
M 113
ad 64,259
H 0
M 114
M 115
M 116
DG 1024
M 117
M 118
M 119
WL 2
R 50
M 120
M 121
M 133
M 132
TX 58,9216
TX 62,23040
M 134
M 135
M 123
M 122
TY 66,9728
TY 70,23552
M 124
M 125
M 137
M 136
TX 74,10752
TX 78,24576
M 138
WL 4
Q 0
M 139
Q 2
M 127
Q 4
M 126
TY 82,11264
TY 86,25088
M 128
Q 6
M 129
WL 4
Q 8
M 141
Q 10
M 140
TX 90,12288
TX 94,26112
M 142
Q 12
M 143
R 54
M 131
Q 14
M 130
TY 98,12800
TY 102,26624
M 0
R 52
M 1
WL 10
Q 16
M 13
Q 18
M 12
Q 20
M 14
Q 22
M 15
WL 8
Q 24
M 3
Q 26
M 2
Q 28
M 4
Q 30
M 5
WL 6
Q 32
M 17
Q 34
M 16
Q 36
M 18
Q 38
M 19
WL 2
Q 40
M 7
Q 42
M 6
Q 44
M 8
Q 46
M 9
UL 6,38
UH 7,38
M 21
AD 180,6
AD 204,7
M 20
UL 6,39
UH 7,39
M 22
AD 228,6
AD 252,7
M 23
R 58
M 11
UL 6,40
UH 7,40
M 10
WL 2
R 56
M 34
AD 181,6
AD 205,7
M 35
UL 6,41
UH 7,41
M 32
AD 229,6
AD 253,7
BV 52,4608
M 33
UL 6,42
UH 7,42
M 30
AD 182,6
AD 206,7
M 31
UL 6,43
UH 7,43
M 28
AD 230,6
AD 254,7
M 29
WV 17
UL 6,44
UH 7,44
M 26
AD 183,6
AD 207,7
M 27
)"
R"(WV 8
UL 6,45
UH 7,45
M 24
WL 1
FP
AD 231,6
M 25
AD 255,7
M 36
M 37
M 38
M 39
M 40
ds_read_b64 v[12:13], v4 offset:4864
ds_read_b64 v[46:47], v4 offset:4872
M 41
AR 6,36
AR 7,60
M 42
AR 8,84
AR 9,108
M 43
WL 3
EF 6,52
EF 7,53
M 44
EF 8,54
EF 9,55
M 45
FR
CV 14,6,7
M 46
CV 15,8,9
AR 6,37
M 47
AR 7,61
AR 8,85
M 58
AR 9,109
EF 6,52
M 59
EF 7,53
EF 8,54
M 56
EF 9,55
CV 16,6,7
M 57
CV 17,8,9
AR 6,38
M 54
AR 7,62
AR 8,86
M 55
WL 0
FQ 268
AR 9,110
M 52
EF 6,52
EF 7,53
M 53
EF 8,54
EF 9,55
M 50
FZ 268
R 72
M 51
mw 24
R 74
M 48
TY 114,2056
TY 118,15880
M 49
R 76
M 60
ad 84,48
W 0
M 61
CV 18,6,7
CV 19,8,9
M 62
AR 6,39
AR 7,63
M 63
W 1
AR 8,87
M 64
AR 9,111
EF 6,52
M 65
EF 7,53
EF 8,54
M 66
W 2
EF 9,55
M 67
WL 3
AN 112,114
AN 113,115
M 68
WL 2
SH 114
AN 114,114
M 69
mw 27
AN 115,115
AN 116,118
M 70
AN 117,119
SH 118
M 71
R 78
M 82
AN 118,118
AN 119,119
M 83
TY 122,3592
TY 126,17416
M 80
ad 84,54
W 0
M 81
CV 20,6,7
CV 21,8,9
M 78
ae 42,74,768
AR 6,40
M 79
W 1
AR 7,64
M 76
s_bitcmp1_b32 s50, 12
s_cselect_b32 s52, 24576, 0
v_add_u32 v50, s52, v3
M 77
ST 14,0
AR 8,88
M 74
W 2
AR 9,112
M 75
EF 6,52
EF 7,53
M 72
WL 1
AN 120,122
AN 121,123
M 73
R 80
M 84
mw 48
SH 122
AN 122,122
M 85
WL 2
AN 123,123
AN 124,126
M 86
AN 125,127
SH 126
M 87
AN 126,126
AN 127,127
M 88
ad 84,96
W 0
M 89
ST 16,768
EF 8,54
M 90
EF 9,55
CV 22,6,7
M 91
W 1
CV 23,8,9
M 92
AR 6,41
AR 7,65
M 93
ST 18,1536
AR 8,89
M 94
W 2
AR 9,113
M 95
R 82
M 106
EF 6,52
EF 7,53
M 107
mw 51
ST 20,2304
EF 8,54
M 104
ad 84,102
W 0
M 105
EF 9,55
CV 24,6,7
M 102
CV 25,8,9
AR 6,42
M 103
W 1
AR 7,66
M 100
AR 8,90
AR 9,114
M 101
EF 6,52
EF 7,53
M 98
W 2
EF 8,54
M 99
EF 9,55
CV 26,6,7
M 96
CV 27,8,9
AR 6,43
M 97
AR 7,67
AR 8,91
M 108
AR 9,115
EF 6,52
M 109
EF 7,53
EF 8,54
M 110
EF 9,55
CV 28,6,7
M 111
CV 29,8,9
M 112
ae 42,74,768
BV 52,5120
M 113
s_bfe_u32 s52, s50, 0x1000d
v_mad_i32_i24 v50, v49, s52, v48
ST 22,0
M 114
AR 6,132
AR 7,156
M 115
WV 19
AR 8,180
EF 6,12
M 116
WL 1
EF 7,13
EF 8,46
M 117
s_barrier
R 0
M 118
mw 72
ST 24,768
EF 136,47
M 119
R 2
M 120
ad 84,144
W 0
M 121
CV 30,6,7
CV 31,8,136
M 133
ST 26,1536
AR 6,133
M 132
AR 7,157
AR 8,181
M 134
W 1
EF 6,12
M 135
EF 7,13
EF 8,46
M 123
ST 28,2304
EF 137,47
M 122
CV 32,6,7
CV 33,8,137
M 124
W 2
AR 6,134
M 125
AR 7,158
AR 8,182
M 137
EF 6,12
EF 7,13
M 136
TX 74,1536
TX 78,15360
M 138
mw 75
EF 8,46
EF 138,47
M 139
ad 84,150
W 0
M 127
CV 34,6,7
CV 35,8,138
M 126
TY 82,2048
TY 86,15872
M 128
AR 6,135
AR 7,159
M 129
W 1
AR 8,183
M 141
EF 6,12
EF 7,13
M 140
WL 2
TX 90,3072
TX 94,16896
P 16
M 142
Q 17
P 19
M 143
R 6
P 20
M 131
Q 21
P 23
M 130
WL 4
TY 98,3584
TY 102,17408
P 24
M 4
R 4
P 25
M 5
Q 26
P 28
M 17
Q 29
P 31
M 16
WL 6
Q 32
M 18
Q 34
M 19
Q 36
M 7
Q 38
M 6
WL 2
Q 40
M 8
Q 42
M 9
Q 44
M 21
Q 46
M 20
W 2
EF 8,46
M 22
EF 139,47
CV 36,6,7
M 23
R 10
M 11
CV 37,8,139
M 10
WL 2
R 8
M 34
ae 42,74,768
AR 6,136
M 35
s_bitcmp1_b32 s50, 12
s_cselect_b32 s52, 24576, 0
v_add_u32 v50, s52, v3
M 32
ST 30,128
AR 7,160
M 33
AR 8,184
EF 6,12
M 30
EF 7,13
EF 8,46
M 31
EF 140,47
CV 38,6,7
M 28
ST 32,896
CV 39,8,140
)"
R"(M 29
AR 6,137
AR 7,161
M 40
AR 8,185
EF 6,12
M 41
EF 7,13
EF 8,46
M 42
ST 34,1664
EF 141,47
M 43
CV 40,6,7
CV 41,8,141
M 44
WV 22
AR 6,138
AR 7,162
M 45
WL 0
AR 8,186
EF 6,12
M 46
s_barrier
R 24
M 47
R 26
M 58
TY 122,8200
TY 126,22024
M 59
ST 36,2432
EF 7,13
M 56
EF 8,46
EF 142,47
M 57
CV 42,6,7
CV 43,8,142
M 54
AR 6,139
AR 7,163
M 55
AR 8,187
EF 6,12
M 52
EF 7,13
EF 8,46
M 53
R 28
M 64
EF 143,47
CV 44,6,7
M 65
WL 3
AN 120,122
AN 121,123
M 66
SH 122
AN 122,122
M 67
WL 2
AN 123,123
AN 124,126
M 68
AN 125,127
SH 126
M 69
AN 126,126
AN 127,127
M 70
CV 45,8,143
M 71
R 30
ds_read_b64 v[12:13], v4 offset:4608
M 82
ae 42,74,768
ds_read_b64 v[46:47], v4 offset:4616
M 83
s_bfe_u32 s52, s50, 0x1000d
v_mad_i32_i24 v50, v49, s52, v48
ST 38,128
M 80
EF 160,52
EF 184,53
M 81
EF 208,54
EF 232,55
M 78
CV 14,160,184
CV 15,208,232
M 79
ST 40,896
EF 161,52
M 76
EF 185,53
EF 209,54
M 77
R 32
M 88
TX 58,7680
TX 62,21504
M 89
ST 42,1664
EF 233,55
M 90
CV 16,161,185
CV 17,209,233
M 91
EF 162,52
EF 186,53
M 92
TY 66,8192
TY 70,22016
M 93
ST 44,2432
EF 210,54
M 94
EF 234,55
CV 18,162,186
M 95
WL 6
R 34
M 106
CV 19,210,234
EF 163,52
M 107
WL 4
PA_ 56,120,58
PA_ 57,121,59
M 104
PB_ 58,120,58
PB_ 59,121,59
M 105
PA_ 60,124,62
PA_ 61,125,63
M 102
PB_ 62,124,62
PB_ 63,125,63
M 103
WL 2
PA_ 64,122,66
PA_ 65,123,67
M 100
PB_ 66,122,66
PB_ 67,123,67
M 101
PA_ 68,126,70
PA_ 69,127,71
M 112
PB_ 70,126,70
PB_ 71,127,71
M 113
EF 187,53
EF 211,54
M 114
EF 235,55
CV 20,163,187
M 115
CV 21,211,235
M 116
WV 18
EF 164,52
EF 188,53
M 117
WL 0
EF 212,54
EF 236,55
M 118
s_barrier
R 48
M 119
R 50
M 124
ae 42,74,768
CV 22,164,188
M 125
s_bitcmp1_b32 s50, 12
s_cselect_b32 s52, 24576, 0
v_add_u32 v50, s52, v3
M 137
ST 14,256
CV 23,212,236
M 136
EF 165,52
EF 189,53
M 138
EF 213,54
EF 237,55
M 139
CV 24,165,189
CV 25,213,237
M 127
ST 16,1024
EF 166,52
M 126
EF 190,53
EF 214,54
M 128
EF 238,55
CV 26,166,190
M 129
CV 27,214,238
EF 167,52
M 141
ST 18,1792
EF 191,53
M 140
EF 215,54
EF 239,55
M 142
CV 28,167,191
CV 29,215,239
M 143
R 54
BV 52,4864
M 131
ST 20,2560
AR 6,44
M 130
WL 3
R 52
MA 52,0,56
AR 7,68
AR 8,92
MA 52,4,60
AR 9,116
EF 6,12
MA 76,12,60
ae 42,74,768
EF 7,13
MA 76,8,56
s_bfe_u32 s52, s50, 0x1000d
v_mad_i32_i24 v50, v49, s52, v48
ST 22,256
MA 80,8,64
EF 8,46
EF 9,47
MA 80,12,68
R 58
MA 56,4,68
CV 30,6,7
CV 31,8,9
MA 56,0,64
WL 2
R 56
MA 104,16,64
ST 24,1024
AR 6,45
MA 104,20,68
AR 7,69
AR 8,93
MA 100,16,56
AR 9,117
EF 6,12
MA 100,20,60
EF 7,13
EF 8,46
MA 124,24,56
WV 14
ST 26,1792
EF 9,47
MA 124,28,60
WL 0
CV 32,6,7
CV 33,8,9
MA 128,24,64
s_barrier
R 72
MA 128,28,68
R 74
MA 152,0,64
ST 28,2560
AR 6,46
MA 152,4,68
AR 7,70
AR 8,94
MA 148,0,56
AR 9,118
EF 6,12
MA 148,4,60
R 76
MA 172,8,56
EF 7,13
EF 8,46
MA 172,12,60
EF 9,47
CV 34,6,7
MA 176,8,64
CV 35,8,9
AR 6,47
MA 176,12,68
WL 4
R 78
MV 132,16,64
AR 7,71
AR 8,95
MV 132,20,68
AR 9,119
EF 6,12
MA 196,16,56
EF 7,13
EF 8,46
MA 196,20,60
WL 4
R 80
MV 152,24,56
EF 9,47
CV 36,6,7
MV 152,28,60
CV 37,8,9
MV 156,24,64
ae 42,72,768
AR 6,48
AR 7,72
MV 156,28,68
WL 4
R 82
AR 8,96
MV 180,0,64
s_bitcmp1_b32 s50, 14
s_cselect_b32 s52, 24576, 0
v_add_u32 v50, s52, v3
AR 9,120
MV 180,4,68
ST 30,0
EF 6,12
EF 7,13
MV 176,0,56
EF 8,46
EF 9,47
CV 38,6,7
MV 176,4,60
WL 4
CV 39,8,9
AR 6,49
AR 7,73
MV 200,8,56
AR 8,97
AR 9,121
EF 6,12
MV 200,12,60
ST 32,768
EF 7,13
EF 8,46
MV 204,8,64
EF 9,47
CV 40,6,7
CV 41,8,9
MV 204,12,68
WL 0
AR 6,50
AR 7,74
AR 8,98
MV 224,16,56
AR 9,122
EF 6,12
EF 7,13
MV 224,20,60
ST 34,1536
EF 8,46
EF 9,47
MV 248,28,60
CV 42,6,7
CV 43,8,9
AR 6,51
MV 248,24,56
AR 7,75
AR 8,99
AR 9,123
MV 252,24,64
EF 6,12
EF 7,13
EF 8,46
MV 252,28,68
ST 36,2304
EF 9,47
CV 44,6,7
MV 228,20,68
WV 14
CV 45,8,9
MV 228,16,64
ae 42,72,768
s_bfe_u32 s52, s50, 0x1000f
v_mad_i32_i24 v50, v49, s52, v48
ST 38,0
ST 40,768
ST 42,1536
ST 44,2304
ds_read_b64 v[12:13], v4 offset:5120
ds_read_b64 v[46:47], v4 offset:5128
AR 6,140
AR 7,164
AR 8,188
EF 6,52
EF 7,53
EF 8,54
EF 144,55
CV 14,6,7
)"
R"(CV 15,8,144
AR 6,141
AR 7,165
AR 8,189
EF 6,52
EF 7,53
EF 8,54
EF 145,55
CV 16,6,7
CV 17,8,145
AR 6,142
AR 7,166
AR 8,190
EF 6,52
EF 7,53
EF 8,54
EF 146,55
CV 18,6,7
CV 19,8,146
AR 6,143
AR 7,167
AR 8,191
EF 6,52
EF 7,53
EF 8,54
EF 147,55
CV 20,6,7
CV 21,8,147
ae 42,72,768
s_bitcmp1_b32 s50, 14
s_cselect_b32 s52, 24576, 0
v_add_u32 v50, s52, v3
ST 14,128
ST 16,896
ST 18,1664
ST 20,2432
AR 6,144
AR 7,168
AR 8,192
EF 6,52
EF 7,53
EF 8,54
EF 148,55
CV 22,6,7
CV 23,8,148
AR 6,145
AR 7,169
AR 8,193
EF 6,52
EF 7,53
EF 8,54
EF 149,55
CV 24,6,7
CV 25,8,149
AR 6,146
AR 7,170
AR 8,194
EF 6,52
EF 7,53
EF 8,54
EF 150,55
CV 26,6,7
CV 27,8,150
AR 6,147
AR 7,171
AR 8,195
EF 6,52
EF 7,53
EF 8,54
EF 151,55
CV 28,6,7
CV 29,8,151
ae 42,72,768
s_bfe_u32 s52, s50, 0x1000f
v_mad_i32_i24 v50, v49, s52, v48
ST 22,128
ST 24,896
ST 26,1664
ST 28,2432
BV 52,4608
WL 1
EF 168,12
EF 192,13
EF 216,46
EF 240,47
CV 30,168,192
CV 31,216,240
EF 169,12
EF 193,13
EF 217,46
EF 241,47
CV 32,169,193
CV 33,217,241
EF 170,12
EF 194,13
EF 218,46
EF 242,47
CV 34,170,194
CV 35,218,242
EF 171,12
EF 195,13
EF 219,46
EF 243,47
CV 36,171,195
CV 37,219,243
ae 42,72,768
s_bitcmp1_b32 s50, 14
s_cselect_b32 s52, 24576, 0
v_add_u32 v50, s52, v3
ST 30,256
ST 32,1024
ST 34,1792
ST 36,2560
EF 172,12
EF 196,13
EF 220,46
EF 244,47
CV 38,172,196
CV 39,220,244
EF 173,12
EF 197,13
EF 221,46
EF 245,47
CV 40,173,197
CV 41,221,245
EF 174,12
EF 198,13
EF 222,46
EF 246,47
CV 42,174,198
CV 43,222,246
EF 175,12
EF 199,13
EF 223,46
EF 247,47
CV 44,175,199
CV 45,223,247
ae 42,72,768
s_bfe_u32 s52, s50, 0x1000f
v_mad_i32_i24 v50, v49, s52, v48
ST 38,256
ST 40,1024
ST 42,1792
ST 44,2560
ds_read_b64 v[12:13], v4 offset:4864
ds_read_b64 v[46:47], v4 offset:4872
AR 6,52
AR 7,76
AR 8,100
AR 9,124
WL 2
EF 6,52
EF 7,53
EF 8,54
EF 9,55
CV 14,6,7
CV 15,8,9
AR 6,53
AR 7,77
AR 8,101
AR 9,125
EF 6,52
EF 7,53
EF 8,54
EF 9,55
CV 16,6,7
CV 17,8,9
AR 6,54
AR 7,78
AR 8,102
AR 9,126
EF 6,52
EF 7,53
EF 8,54
EF 9,55
CV 18,6,7
CV 19,8,9
AR 6,55
AR 7,79
AR 8,103
AR 9,127
EF 6,52
EF 7,53
EF 8,54
EF 9,55
CV 20,6,7
CV 21,8,9
ae 42,72,864
s_bitcmp1_b32 s50, 16
s_cselect_b32 s52, 24576, 0
v_add_u32 v50, s52, v3
ST 14,0
ST 16,768
ST 18,1536
ST 20,2304
AR 6,56
AR 7,80
AR 8,104
AR 9,128
EF 6,52
EF 7,53
EF 8,54
EF 9,55
CV 22,6,7
CV 23,8,9
AR 6,57
AR 7,81
AR 8,105
AR 9,129
EF 6,52
EF 7,53
EF 8,54
EF 9,55
CV 24,6,7
CV 25,8,9
AR 6,58
AR 7,82
AR 8,106
AR 9,130
EF 6,52
EF 7,53
EF 8,54
EF 9,55
CV 26,6,7
CV 27,8,9
AR 6,59
AR 7,83
AR 8,107
AR 9,131
EF 6,52
EF 7,53
EF 8,54
EF 9,55
CV 28,6,7
CV 29,8,9
ae 42,72,864
s_bfe_u32 s52, s50, 0x10011
v_mad_i32_i24 v50, v49, s52, v48
ST 22,0
ST 24,768
ST 26,1536
ST 28,2304
BV 52,5120
AR 6,148
AR 7,172
AR 8,196
WL 1
EF 6,12
EF 7,13
EF 8,46
EF 152,47
CV 30,6,7
CV 31,8,152
AR 6,149
AR 7,173
AR 8,197
EF 6,12
EF 7,13
EF 8,46
EF 153,47
CV 32,6,7
CV 33,8,153
AR 6,150
AR 7,174
AR 8,198
EF 6,12
EF 7,13
EF 8,46
EF 154,47
CV 34,6,7
CV 35,8,154
AR 6,151
AR 7,175
AR 8,199
EF 6,12
EF 7,13
EF 8,46
EF 155,47
CV 36,6,7
CV 37,8,155
ae 42,72,864
s_bitcmp1_b32 s50, 16
s_cselect_b32 s52, 24576, 0
v_add_u32 v50, s52, v3
ST 30,128
ST 32,896
ST 34,1664
ST 36,2432
AR 6,152
AR 7,176
EF 6,12
EF 7,13
EF 132,46
EF 156,47
CV 38,6,7
CV 39,132,156
AR 6,153
AR 7,177
EF 6,12
EF 7,13
EF 133,46
EF 157,47
CV 40,6,7
CV 41,133,157
AR 6,154
AR 7,178
EF 6,12
EF 7,13
EF 134,46
EF 158,47
CV 42,6,7
CV 43,134,158
AR 6,155
AR 7,179
EF 6,12
EF 7,13
EF 135,46
EF 159,47
CV 44,6,7
CV 45,135,159
ae 42,72,864
s_bfe_u32 s52, s50, 0x10011
v_mad_i32_i24 v50, v49, s52, v48
ST 38,128
ST 40,896
ST 42,1664
ST 44,2432
WL 0
EF 176,52
EF 200,53
EF 224,54
EF 248,55
CV 14,176,200
CV 15,224,248
EF 177,52
EF 201,53
EF 225,54
EF 249,55
CV 16,177,201
CV 17,225,249
EF 178,52
EF 202,53
EF 226,54
EF 250,55
CV 18,178,202
CV 19,226,250
EF 179,52
EF 203,53
EF 227,54
EF 251,55
CV 20,179,203
CV 21,227,251
ae 42,72,864
s_bitcmp1_b32 s50, 16
s_cselect_b32 s52, 24576, 0
v_add_u32 v50, s52, v3
ST 14,256
ST 16,1024
ST 18,1792
ST 20,2560
EF 180,52
EF 204,53
EF 228,54
EF 252,55
CV 22,180,204
CV 23,228,252
EF 181,52
EF 205,53
EF 229,54
EF 253,55
CV 24,181,205
CV 25,229,253
EF 182,52
EF 206,53
EF 230,54
EF 254,55
CV 26,182,206
CV 27,230,254
EF 183,52
EF 207,53
EF 231,54
EF 255,55
CV 28,183,207
CV 29,231,255
ae 42,72,864
s_bfe_u32 s52, s50, 0x10011
v_mad_i32_i24 v50, v49, s52, v48
ST 22,256
ST 24,1024
ST 26,1792
ST 28,2560
s_waitcnt vmcnt(0) lgkmcnt(0)
s_branch 29f
FY 132
FY 136
FY 140
FY 144
FY 148
FY 152
FY 156
)"
R"(FY 160
FY 164
FY 168
FY 172
FY 176
FY 180
FY 184
FY 188
FY 192
FY 196
FY 200
FY 204
FY 208
FY 212
FY 216
FY 220
FY 224
FY 228
FY 232
FY 236
FY 240
FY 244
FY 248
FY 252
FY 256
FY 260
FY 264
FY 268
29:
.purgem MA
.purgem MV
.purgem NA
.purgem NV
.purgem AN
.purgem SH
.purgem PA_
.purgem PB_
.purgem AW
.purgem AR
.purgem UL
.purgem UH
.purgem AD
.purgem EF
.purgem TY
.purgem TX
.purgem RA
.purgem RC
.purgem BV
.purgem PA
.purgem DF
.purgem DW
.purgem DR
.purgem DH
.purgem DG
.purgem XL
.purgem ST
.purgem SA
.purgem CV
.purgem RS
.purgem PM
.purgem PF
.purgem FS
.purgem FL
.purgem FB
.purgem FC
.purgem FG
.purgem FX
.purgem FP
.purgem FR
.purgem FQ
.purgem FZ
.purgem FY
.purgem WL
.purgem WV
.purgem M
.purgem R1
.purgem R
.purgem P
.purgem Q
.purgem mw
.purgem mh
.purgem ad
.purgem ae
.purgem bc
.purgem W
.purgem H
)"
      : [flg] "=&s"(flg) : K2_OPS : K2_CLOB);
  if (flg) {
    const unsigned lane = __builtin_amdgcn_mbcnt_hi(~0u, __builtin_amdgcn_mbcnt_lo(~0u, 0u));
    const u16* const volatile __attribute__((address_space(4)))* ka = (const u16* const volatile __attribute__((address_space(4)))*)__builtin_amdgcn_kernarg_segment_ptr();
    {
      const u16* w1p = ka[6];
#pragma unroll 1
      for (unsigned f = blockIdx.x * 4 + w; f < 1152; f += 1024) {
        const u16* p = w1p + (32 * (f % 12) + 8 * (lane >> 4)) * HID + 16 * (f / 12) + (lane & 15);
        u32x4 v;
#pragma unroll
        for (int j = 0; j < 4; ++j) v[j] = p[2 * j * HID] | (unsigned)p[(2 * j + 1) * HID] << 16;
        *(u32x4*)(g_buf + f * 1024 + lane * 16) = v;
      }
    }
    slow_bar((unsigned*)(g_buf + G_BAR), w);
    slow_fc1(ka[0], ka[5], (char*)ka[1], (float*)smem + w * HID, w);
    slow_bar((unsigned*)(g_buf + G_BAR + 64), w);
    slow_fc2(ka[0], (const char*)ka[1], (u16*)ka[2], ka[3], ka[4], smem, w);
  }
}

extern "C" void kernel_launch(void* const* d_in, const int* in_sizes, int n_in,
                              void* d_out, int out_size, void* d_ws, size_t ws_size,
                              hipStream_t stream) {
  const u16* x = (const u16*)d_in[0];
  const u16* dw = (const u16*)d_in[1];
  const u16* w1 = (const u16*)d_in[2];
  const u16* w2 = (const u16*)d_in[3];
  const u16* grn_g = (const u16*)d_in[4];
  const u16* grn_b = (const u16*)d_in[5];
  char* ws = (char*)d_ws;
  if (ws_size < H_BYTES) return;
  (void)hipFuncSetAttribute((const void*)k1_fc1, hipFuncAttributeMaxDynamicSharedMemorySize, K1_LDS);
  (void)hipFuncSetAttribute((const void*)k2_fc2, hipFuncAttributeMaxDynamicSharedMemorySize, K2_LDS);

  k0_prep<<<K0_NB + NPAGE + NPAGX, 256, 0, stream>>>(w1, dw, ws, (const char*)x, grn_b, w2);
  k1_fc1<<<NWG + 8, 256, K1_LDS, stream>>>(x, ws, (const u16*)d_out, grn_g);
  k2_fc2<<<NWG, 256, K2_LDS, stream>>>(x, ws, (u16*)d_out, grn_g, w2, dw, w1);
}
```

```cpp
#include <hip/hip_runtime.h>
#include <hip/hip_bf16.h>
#include <stdint.h>

typedef float f32x4 __attribute__((ext_vector_type(4)));
typedef float f32x2 __attribute__((ext_vector_type(2)));
typedef unsigned u32x4 __attribute__((ext_vector_type(4)));
typedef __bf16 bf16x2_t __attribute__((ext_vector_type(2)));
typedef unsigned short u16;

constexpr float TZ = 175.f, TH = 175.f, TW2 = 175.f;
#define DI __device__ __forceinline__
#define LAS __attribute__((address_space(3)))

constexpr int NPIX = 147456;
constexpr int CH = 384;
constexpr int HID = 1536;
constexpr int NWG = 256;

constexpr size_t H_BYTES = (size_t)NPIX * HID * 2;
constexpr unsigned K1_LDS = 163840, K1_PF_OFF = 130048, K1_SMAX = 12, K1_NREP = 4, K1_G_TAB = 1228800, K1_G_CNT = 1239040, K1_G_LOC = 1236992, K1_G_DUM = 1255424,
  K1_G_ACC = 1320960, K1_G_LUT = 1382400, K1_G_BYTES = 1398784;
constexpr unsigned K1_GST = 25600, K1_WCH = 6400, K1_ZC = 1365;
constexpr unsigned K1_SB1 = 32768;
constexpr unsigned K2_SHT = 12483;
constexpr int K2_LDS = 163840, K2_HB = 98304, K2_RSZ = 13824, K2_E_OFF = 153600, K2_PF_OFF = 159744, K2_S16_OFF = 152576, K2_S16_D0 = 8192, K2_NAT = 8;

__device__ __attribute__((aligned(256))) char g_buf[K1_G_BYTES];
__device__ __attribute__((aligned(256))) float g_cs[8 * 384];
__device__ __attribute__((aligned(256))) char g_w2r[96 * 12 * 1024];
__device__ __attribute__((aligned(256))) char g_w2f[96 * 12 * 1024];
__device__ __attribute__((aligned(256))) char g_w2q[16 * 12 * 1024];
__device__ __attribute__((aligned(256))) float g_sc[64];
constexpr unsigned G_FLAG = K1_G_CNT + 14336;
constexpr unsigned G_PUB = K1_G_DUM;
__device__ __attribute__((aligned(256))) unsigned g_prm[1024 * 64];
constexpr unsigned G_BAR = K1_G_CNT + 13312;
constexpr unsigned H_TS = 49152, H_RS = 48 * H_TS, NPAGE = 217, NPAGX = 55;

DI unsigned pack_bf16(float a, float b) {
  f32x2 v = {a, b};
  bf16x2_t r = __builtin_convertvector(v, bf16x2_t);
  return __builtin_bit_cast(unsigned, r);
}
typedef _Float16 f16x2_t __attribute__((ext_vector_type(2)));
DI unsigned pack_f16(float a, float b) {
  f32x2 v = {a, b};
  f16x2_t r = __builtin_convertvector(v, f16x2_t);
  return __builtin_bit_cast(unsigned, r);
}
DI float bf_lo(unsigned u) { return __uint_as_float(u << 16); }
DI float bf_hi(unsigned u) { return __uint_as_float(u & 0xffff0000u); }
DI float round_bf16(float a) { return bf_lo(pack_bf16(a, 0.f)); }
DI float ld_bf16(const u16* p) { return __uint_as_float((unsigned)(*p) << 16); }

DI unsigned lds_addr(const char* p) { return (unsigned)(size_t)(const LAS char*)p; }

DI float gelu_tanh(float x) {
  const float c0 = -2.0f * 0.7978845608028654f * 1.4426950408889634f;
  const float c1 = c0 * 0.044715f;
  float x2 = x * x;
  float t = __builtin_fmaf(x2, c1, c0);
  float e = __builtin_amdgcn_exp2f(x * t);
  float r = __builtin_amdgcn_rcpf(1.0f + e);
  return x * r;
}

DI void st16_wt(void* p, u32x4 v) { asm volatile("global_store_dwordx4 %0, %1, off\n\ts_nop 1" :: "v"(p), "v"(v) : "memory"); }
template <int I> struct IC { static constexpr int value = I; };
template <int N, int I = 0, typename F> DI void static_for(F&& f) {
  if constexpr (I < N) { f(IC<I>{}); static_for<N, I + 1>(f); }
}
template <int C, int M> DI float dpp_add(float v) { return v + __int_as_float(__builtin_amdgcn_update_dpp(0, __float_as_int(v), C, M, 0xf, false)); }
DI float wave_sum63(float v) {
  v = dpp_add<0x111, 0xf>(v); v = dpp_add<0x112, 0xf>(v); v = dpp_add<0x114, 0xf>(v); v = dpp_add<0x118, 0xf>(v);
  v = dpp_add<0x142, 0xa>(v); return dpp_add<0x143, 0xc>(v);
}
DI unsigned cs_index(unsigned n) { return (n & ~63u) + (n & 3) * 16 + ((n >> 2) & 15); }
DI uint64_t uni64(uint64_t v) { return (uint64_t)__builtin_amdgcn_readfirstlane(v >> 32) << 32 | (unsigned)__builtin_amdgcn_readfirstlane(v); }
DI unsigned wg_side(unsigned b) { return (((const unsigned*)(g_buf + K1_G_LOC))[b & 3] ^ (b >> 2)) & 1; }
DI unsigned h_flip(const char* ws, unsigned region, unsigned t) { return __popc(((const unsigned*)(g_buf + K1_G_LOC + 64))[(((unsigned)(size_t)ws & 0x1fffffu) + region * H_RS + t * H_TS) >> 21]) > 2; }
DI unsigned x_flip(const u16* x, unsigned region) { return __popc(((const unsigned*)(g_buf + K1_G_LOC + 1024))[(((unsigned)(size_t)x & 0x1fffffu) + (12 * region + 6) * 49152u) >> 21]) > 2; }
DI unsigned sub_pixel(const u16* x, unsigned sub, unsigned m) {
  const unsigned blk = sub / 24, u = sub % 24;
  const unsigned region = blk < 256 ? (blk >> 3) * 4 + (blk & 3) : 128 + ((blk - 256) >> 1), side = blk < 256 ? wg_side(blk) : blk & 1;
  return (12 * region + (u >> 1)) * 64 + ((0xda43cb52fe987610ull >> (32 * (u & 1) + 16 * (side ^ x_flip(x, region)) + (m & 12))) & 15) * 4 + (m & 3);
}
DI unsigned h_piece(const char* ws, unsigned sub, unsigned t) {
  const unsigned blk = sub / 24, u = sub % 24;
  const unsigned region = blk < 256 ? (blk >> 3) * 4 + (blk & 3) : 128 + ((blk - 256) >> 1), side = blk < 256 ? wg_side(blk) : blk & 1;
  return region * H_RS + t * H_TS + (u >> 3) * 16384 + (u & 7) * 1024 + ((side ^ h_flip(ws, region, t)) << 13);
}
constexpr unsigned K0_NB = 145 + 24 + 96 + 16 + 48;
constexpr float U24 = 0x1p-24f, EPS = 1e-5f;
DI float wave_all(float v) { return __int_as_float(__builtin_amdgcn_readlane(__float_as_int(wave_sum63(v)), 63)); }
DI float pow2f(float v) { return __uint_as_float(__float_as_uint(v) & 0x7f800000u); }
DI u32x4 w_load(const u16* w) { return *(const u32x4*)(w + ((threadIdx.x >> 3) & 7) * 73408 + 36800 + (threadIdx.x & 7) * 8); }
DI float w_ms(u32x4 a) {
  float q = 0.f;
#pragma unroll
  for (int i = 0; i < 4; ++i) { const float lo = bf_lo(a[i]), hi = bf_hi(a[i]); q = __builtin_fmaf(lo, lo, __builtin_fmaf(hi, hi, q)); }
  return wave_all(q) * (1.0f / 512);
}
DI float w1_scale(float ms) { return pow2f(300.f * U24 * __builtin_amdgcn_rsqf(ms)); }
DI float w2_scale(float ms) { return TW2 * U24 * __builtin_amdgcn_rsqf(ms); }
struct Sc { float w1, zs, g, h, vlo, vhi, c; };
DI Sc scales(const u16* w1, const u16* x, const u16* dw) {
  const u32x4 a = w_load(w1);
  const unsigned l = threadIdx.x & 63;
  float s = 0.f, q = 0.f;
#pragma unroll
  for (int k = 0; k < 6; ++k) {
    const u32x4 v = *(const u32x4*)(x + (9001 + 18397 * (l >> 3)) * CH + 64 * k + 8 * (l & 7)), d = *(const u32x4*)(dw + 64 * k + 8 * (l & 7));
#pragma unroll
    for (int i = 0; i < 4; ++i) { const float lo = bf_lo(v[i]) * bf_lo(d[i]), hi = bf_hi(v[i]) * bf_hi(d[i]); s += lo + hi; q = __builtin_fmaf(lo, lo, __builtin_fmaf(hi, hi, q)); }
  }
  s = dpp_add<0x141, 0xf>(dpp_add<0x4e, 0xf>(dpp_add<0xb1, 0xf>(s))); q = dpp_add<0x141, 0xf>(dpp_add<0x4e, 0xf>(dpp_add<0xb1, 0xf>(q)));
  const float var = fmaxf(q * (1.0f / CH) - s * s * (1.0f / (CH * CH)), 0.f);
  const float r2 = wave_all(var * __builtin_amdgcn_rcpf(var + EPS)) * (1.0f / 64), rden = __builtin_amdgcn_exp2f(wave_all(__builtin_amdgcn_logf(var + EPS)) * (-1.0f / 128));
  const float s1 = w_ms(a), p2 = r2 * CH * s1;
  Sc c;
  c.w1 = w1_scale(s1);
  c.g = TZ * U24 * __builtin_amdgcn_rsqf(r2);
  c.h = TH * U24 * __builtin_amdgcn_rsqf(p2 * (0.25f + 0.5f * p2 * __builtin_amdgcn_rcpf(1.0f + 2.0f * p2)));
  c.c = fminf(__builtin_ceilf(0.1701f * 0x1p24f * c.h) + 1.0f, 1400.f);
  c.zs = fminf(pow2f(c.g * 2530.f), pow2f(c.g * 0x1p12f * rden));
  const float e = EPS * c.zs * c.zs, z2 = r2 * (660.f * 660.f / (TZ * TZ));
  c.vlo = 4.2f * c.g * c.g - e;
  c.vhi = fminf(0x1.ep29f * c.g * c.g - e, z2 < 1.0f ? e * z2 * __builtin_amdgcn_rcpf(1.0f - z2) : 0x1p120f);
  return c;
}
__global__ __launch_bounds__(256) void k0_prep(const u16* __restrict__ w1, const u16* __restrict__ dw, const char* __restrict__ ws, const char* __restrict__ x,
                                                const u16* __restrict__ grn_b, const u16* __restrict__ w2) {
  uint64_t pc; int d1;
  asm volatile("s_getpc_b64 %0\n1:\n\ts_mov_b32 %1, k1_code-1b" : "=s"(pc), "=s"(d1));
  if (threadIdx.x >= 192) pc += (int64_t)d1 + ((int)(blockIdx.x >> 3) - 3) * 8192;
  unsigned pfd = 0;
  if (blockIdx.x < 64 && (threadIdx.x < 150 || (blockIdx.x < 48 && threadIdx.x >= 192))) pfd = *(const __attribute__((address_space(1))) unsigned*)((pc & ~127ull) + threadIdx.x * 128);
#define K0_END() do { asm volatile("" :: "v"(pfd)); } while (0)
  unsigned xcc;
  asm volatile("s_getreg_b32 %0, hwreg(HW_REG_XCC_ID)" : "=s"(xcc));
  xcc = (xcc >> 2) & 1;
  if (blockIdx.x < 4 && threadIdx.x == 0) ((unsigned*)(g_buf + K1_G_LOC))[blockIdx.x] = 7 * xcc;
  constexpr unsigned NP = NPAGE + NPAGX;
  __shared__ Sc ssc;
  __shared__ float ss2;
  const bool sw = threadIdx.x >= 192;
  const unsigned bi = blockIdx.x;
  const unsigned bx = bi < NP ? bi + K0_NB : bi - NP;
  if (bx >= K0_NB) {
    __shared__ unsigned lat[8];
    const unsigned pg = bx - K0_NB, w = threadIdx.x >> 6, lane = threadIdx.x & 63;
    auto gran = [&](unsigned p) {
      const bool h = p < NPAGE;
      const char* buf = h ? ws : x;
      const int o0 = (int)((h ? p : p - NPAGE) << 21) - (int)((unsigned)(size_t)buf & 0x1fffffu);
      const unsigned al = h ? 49152 : 12 * 49152;
      const unsigned q = ((((unsigned)max(o0, 0) + al - 1) >> (h ? 14 : 16)) * (h ? 87382u : 29128u)) >> 18;
      return buf + min(q * al, (unsigned)(h ? H_BYTES : H_BYTES / 4) - 16384u);
    };
    if (lane < 5) {
      const char* a = gran(lane < 4 ? min(pg + NP - 1 - (lane >> 1), pg - 1 - (lane >> 1)) : pg) + (lane & 1) * 8192 + w * 2048 + (lane < 4 ? 1024 : 0);
      const char* b = a;
      const char* own = (const char*)uni64(__builtin_amdgcn_readlane((uint64_t)a >> 32, 4) * 4294967296ull + (unsigned)__builtin_amdgcn_readlane((unsigned)(uint64_t)a, 4));
      unsigned long long t0, t1, t2;
      int n = 2;
      asm volatile("" : "+s"(n));
#pragma unroll 1
      do {
        unsigned v;
        asm volatile("s_cmp_eq_u32 %6, 2\n\ts_cbranch_scc1 1f\n\ts_sleep 4\n1:\n\ts_memtime %0\n\ts_waitcnt lgkmcnt(0)\n\tglobal_load_dword %3, %4, off sc1\n\ts_waitcnt vmcnt(0)\n\ts_memtime %1\n\ts_waitcnt lgkmcnt(0)\n\t"
                     "global_load_dword %3, %5, off sc1\n\ts_waitcnt vmcnt(0)\n\ts_memtime %2\n\ts_waitcnt lgkmcnt(0)" : "=&s"(t0), "=&s"(t1), "=&s"(t2), "=&v"(v) : "v"(a), "v"(b), "s"(n) : "memory", "scc");
        a = own + 1024 + (w & 1) * 8192; b = a + 8192 - (w & 1) * 16384;
      } while (--n);
      lat[w + 4 * (w & 1)] = (unsigned)(t1 - t0); lat[w + 4 - 4 * (w & 1)] = (unsigned)(t2 - t1);
    }
    __syncthreads();
    if (threadIdx.x == 0) {
      auto fastest = [&](const unsigned* l) { unsigned m = 9999; for (int i = 0; i < 4; ++i) m = l[i] < 300 ? m : min(m, l[i]); return (int)m; };
      const int df = fastest(lat) - fastest(lat + 4);
      const unsigned v = xcc ^ (df > 0), sure = abs(df) > 80;
      char* e = g_buf + K1_G_LOC + 64 + 4 * (pg < NPAGE ? pg : pg + 240 - NPAGE);
      *(u16*)(e + 2) = v ? 3 + 4 * sure : 1 - sure;
      if (pg == 0 || pg == NPAGE) e[0] = v; else e[-3] = v ^ 1;
      if (pg == NPAGE - 1 || pg == NP - 1) e[1] = v; else e[4] = v ^ 1;
    }
    K0_END();
    return;
  }
  if (bx >= 145 + 24 + 96 + 16) {
    __shared__ __attribute__((aligned(16))) float red[2][16][32];
    const int t = bx - 281, c8 = (threadIdx.x & 3) * 8;
    u32x4 sm, v[6];
    if (sw) sm = w_load(w1);
#pragma unroll
    for (int k = 0; k < 6; ++k) v[k] = *(const u32x4*)(w1 + (size_t)(64 * k + (threadIdx.x >> 2)) * HID + 32 * t + c8);
    if (sw) ss2 = w1_scale(w_ms(sm));
    __syncthreads();
    const float s1 = ss2;
    float a[8], q[8];
#pragma unroll
    for (int i = 0; i < 8; ++i) {
      a[i] = q[i] = 0.f;
#pragma unroll
      for (int k = 0; k < 6; ++k) {
        const float f = (float)(_Float16)(s1 * (i & 1 ? bf_hi(v[k][i >> 1]) : bf_lo(v[k][i >> 1]))) * 0x1p24f;
        a[i] += f; q[i] = __builtin_fmaf(f, f, q[i]);
      }
      a[i] = dpp_add<0x118, 0xf>(dpp_add<0x114, 0xf>(a[i])); q[i] = dpp_add<0x118, 0xf>(dpp_add<0x114, 0xf>(q[i]));
    }
    if ((threadIdx.x & 12) == 12) {
#pragma unroll
      for (int i = 0; i < 2; ++i) {
        *(f32x4*)&red[0][threadIdx.x >> 4][c8 + 4 * i] = (f32x4){a[4 * i], a[4 * i + 1], a[4 * i + 2], a[4 * i + 3]};
        *(f32x4*)&red[1][threadIdx.x >> 4][c8 + 4 * i] = (f32x4){q[4 * i], q[4 * i + 1], q[4 * i + 2], q[4 * i + 3]};
      }
    }
    __syncthreads();
    if (threadIdx.x < 64) {
      const int n = threadIdx.x & 31;
      float s = 0.f;
#pragma unroll
      for (int i = 0; i < 16; ++i) s += red[threadIdx.x >> 5][i][n];
      const float c = (0.f - (float)K1_ZC * s) * 0x1p-48f;
      char* p = g_buf + t * K1_GST + 5120;
      if (threadIdx.x < 32) *(f32x4*)(p + 272 * (n >> 4) + (n & 15) * 16) = (f32x4){c, c, c, c};
      const bool any = __ballot(threadIdx.x >= 32 && !(s >= 384.f * 64 * 64 && s <= 384.f * 1024 * 1024)) != 0;
      if (threadIdx.x == 0) g_buf[G_FLAG + 4 * 98 + t] = any;
    }
    K0_END();
    return;
  }
  if (bx >= 145 + 24 + 96) {
    if (sw) ssc = scales(w1, (const u16*)x, dw);
    __syncthreads();
    const Sc sc = ssc;
    const unsigned e = (bx - 265) * 256 + threadIdx.x;
    const float u = 1.0f / (sc.g * sc.w1);
    float r[2];
#pragma unroll
    for (int i = 0; i < 2; ++i) {
      const unsigned c = 2 * e + i;
      r[i] = __builtin_fmaf(sc.h, gelu_tanh(u * __uint_as_float((c >> 12) << 31 | ((c & 4095) + 79 * 128) << 16 | 0x8000u)), sc.c * U24);
      if (!(r[i] >= 0.f && r[i] < 8189.f * U24)) r[i] = __builtin_inff();
    }
    *(unsigned*)(g_buf + K1_G_LUT + 4 * e) = pack_f16(r[0], r[1]);
    K0_END();
    return;
  }
  if (bx >= 145 + 24) {
    __shared__ __attribute__((aligned(16))) u16 tw[32][200];
    const int kappa = (bx - 169) >> 1, nh = (bx - 169) & 1;
    u32x4 sm, v[3];
    if (sw) sm = w_load(w2);
#pragma unroll
    for (int i = 0; i < 3; ++i) { const int e = threadIdx.x + 256 * i; v[i] = *(const u32x4*)(w2 + (size_t)(32 * kappa + e / 24) * CH + 192 * nh + (e % 24) * 8); }
    if (sw) ss2 = w2_scale(w_ms(sm));
#pragma unroll
    for (int i = 0; i < 3; ++i) { const int e = threadIdx.x + 256 * i; *(u32x4*)&tw[e / 24][(e % 24) * 8] = v[i]; }
    __syncthreads();
    const int lane = threadIdx.x & 63, g = lane >> 4;
    const float s2 = 256.f * ss2;
    bool bad = false;
#pragma unroll
    for (int i = 0; i < 3; ++i) {
      const int fi = (threadIdx.x >> 6) + 4 * i, n = 64 * (fi >> 2) + 4 * (lane & 15) + (fi & 3);
      float wv[8];
      unsigned bits = 0;
#pragma unroll
      for (int j = 0; j < 8; ++j) {
        wv[j] = ld_bf16(&tw[16 * (j >> 2) + 4 * g + (j & 3)][n]);
        bits |= wv[j] != 0.f ? 256u << j : 0u;
        wv[j] *= s2;
        bits |= fabsf(wv[j]) >= 48 * 256 * U24 ? 1u << j : 0u;
        bad |= !(fabsf(wv[j]) <= 500.f);
      }
      const unsigned o = (((kappa & ~1) + fi / 6) * 24 + nh * 12 + (fi % 6) * 2 + (kappa & 1)) * 1024 + lane * 16;
      st16_wt(g_w2r + o, (u32x4){pack_f16(wv[0], wv[1]), pack_f16(wv[2], wv[3]), pack_f16(wv[4], wv[5]), pack_f16(wv[6], wv[7])});
      constexpr float q = 1.0f / 256;
      if (kappa < 8) st16_wt(g_w2q + o, (u32x4){pack_f16(q * wv[0], q * wv[1]), pack_f16(q * wv[2], q * wv[3]), pack_f16(q * wv[4], q * wv[5]), pack_f16(q * wv[6], q * wv[7])});
      static_for<4>([&](auto ic) { bits |= __builtin_amdgcn_update_dpp(0u, bits, 0x120 + (1 << decltype(ic)::value), 0xf, 0xf, false); });
      bad |= ((bits >> 8) & ~bits & 255u) != 0;
    }
    const bool any = __ballot(bad) != 0;
    if (lane == 0) ((char*)(g_buf + G_FLAG))[4 * (bx - 169) + (threadIdx.x >> 6)] = any;
    K0_END();
    return;
  }
  if (bx >= 145) {
    __shared__ __attribute__((aligned(16))) float red[16][64];
    const int c0 = 64 * ((bx - 145) >> 2), rg = (bx - 145) & 3, c8 = (threadIdx.x & 7) * 8;
    float a[8];
#pragma unroll
    for (int i = 0; i < 8; ++i) a[i] = 0.f;
#pragma unroll
    for (int k = 0; k < 12; ++k) {
      const int ch = 384 * rg + 32 * k + (threadIdx.x >> 3);
      const float b = ld_bf16(grn_b + ch);
      const u32x4 v = *(const u32x4*)(w2 + (size_t)ch * CH + c0 + c8);
#pragma unroll
      for (int i = 0; i < 4; ++i) { a[2 * i] = __builtin_fmaf(b, bf_lo(v[i]), a[2 * i]); a[2 * i + 1] = __builtin_fmaf(b, bf_hi(v[i]), a[2 * i + 1]); }
    }
#pragma unroll
    for (int i = 0; i < 8; ++i) a[i] = dpp_add<0x118, 0xf>(a[i]);
    if (threadIdx.x & 8) {
      *(f32x4*)&red[threadIdx.x >> 4][c8] = (f32x4){a[0], a[1], a[2], a[3]};
      *(f32x4*)&red[threadIdx.x >> 4][c8 + 4] = (f32x4){a[4], a[5], a[6], a[7]};
    }
    __syncthreads();
    if (threadIdx.x < 64) {
      float s = 0.f;
#pragma unroll
      for (int i = 0; i < 16; ++i) s += red[i][threadIdx.x];
      g_cs[(4 + rg) * 384 + cs_index(c0 + threadIdx.x)] = s;
      g_cs[64 * (bx - 145) + threadIdx.x] = 0.f;
    }
    K0_END();
    return;
  }
  const unsigned bid = bx;
  if (bid == 0) {
    if (sw) ssc = scales(w1, (const u16*)x, dw);
    if ((threadIdx.x >> 6) == 2) ss2 = w2_scale(w_ms(w_load(w2)));
#pragma unroll
    for (int i = 0; i < 4; ++i) if (threadIdx.x + 256 * i < 896) *(u32x4*)(g_buf + K1_G_CNT + (threadIdx.x + 256 * i) * 16) = (u32x4){0u, 0u, 0u, 0u};
    *(unsigned*)(g_buf + G_PUB + threadIdx.x * 256) = 0u;
    const int e = threadIdx.x, e2 = e < 192 ? 32 * (e >> 4) + 8 * ((e & 15) >> 2) : 8 * min(e - 192, 47);
    const u32x4 d = *(const u32x4*)(dw + e2);
    __syncthreads();
    const Sc sc = ssc;
    const float s2 = ss2;
    u32x4 v;
#pragma unroll
    for (int k = 0; k < 4; ++k) v[k] = pack_bf16(sc.zs * bf_lo(d[k]), sc.zs * bf_hi(d[k]));
    if (e < 192) {
#pragma unroll
      for (int k = 0; k < 4; ++k) v[k] &= (k & 1) != ((e >> 1) & 1) ? 0u : e & 1 ? 0xffff0000u : 0xffffu;
    }
    if (e >= 240) v = (u32x4){__float_as_uint(EPS * sc.zs * sc.zs), __float_as_uint(sc.vhi), __float_as_uint(sc.g), __float_as_uint(sc.vlo)};
    *(u32x4*)(g_buf + K1_G_TAB + (e < 192 ? e : e + 192) * 16) = v;
    auto sane = [](float v) { return v >= 0x1p-45f && v <= 0x1p20f; };
    if (threadIdx.x < 2) ((unsigned*)(g_buf + G_FLAG))[96 + threadIdx.x] = !(sane(sc.w1) && sane(sc.zs) && sane(sc.g) && sane(sc.h) && sane(s2));
    if (threadIdx.x == 0) {
      const float ik = 1.0f / (sc.h * s2);
      *(f32x4*)g_sc = (f32x4){sc.h * s2, ik, 0x1p-28f / (sc.h * sc.h), -sc.c * U24 * ik};
      g_sc[4] = __uint_as_float(pack_f16(-sc.c * U24, -sc.c * U24));
      *(unsigned*)(g_buf + K1_G_CNT + 16320) = pack_f16(-sc.c * 0x1p-10f, -sc.c * 0x1p-10f);
    }
    if (threadIdx.x < 48) *(u32x4*)(g_buf + threadIdx.x * K1_GST + 5376) = (u32x4){pack_f16(sc.c * U24, 0.f), 0u, 0u, 0u};
    K0_END();
    return;
  }
  __shared__ __attribute__((aligned(16))) u16 tl[32][136];
  const int e = (bid - 1) * 256 + threadIdx.x, ks = (bid - 1) % 12, cb = (bid - 1) / 12;
  u32x4 sm, v[2];
  if (sw) sm = w_load(w1);
#pragma unroll
  for (int i = 0; i < 2; ++i) v[i] = *(const u32x4*)(w1 + (size_t)(32 * ks + (threadIdx.x >> 4) + 16 * i) * HID + 128 * cb + (threadIdx.x & 15) * 8);
  if (sw) ss2 = w1_scale(w_ms(sm));
#pragma unroll
  for (int i = 0; i < 2; ++i) *(u32x4*)&tl[(threadIdx.x >> 4) + 16 * i][(threadIdx.x & 15) * 8] = v[i];
  if (e < (int)(K1_NREP + 1) * 768) *(u32x4*)(g_buf + K1_G_ACC + e * 16) = (u32x4){0u, 0u, 0u, 0u};
  __syncthreads();
  const float s1 = ss2;
#pragma unroll
  for (int i = 0; i < 2; ++i) {
    const int lane = threadIdx.x & 63, f = (threadIdx.x >> 6) + 4 * i, n = 16 * f + (lane & 15), k0 = 8 * (lane >> 4);
    u32x4 P;
#pragma unroll
    for (int jj = 0; jj < 4; ++jj) P[jj] = pack_f16(s1 * ld_bf16(&tl[k0 + 2 * jj][n]), s1 * ld_bf16(&tl[k0 + 2 * jj + 1][n]));
    const int fs = (f & 1) * 12 + ks;
    st16_wt(g_buf + (4 * cb + (f >> 1)) * K1_GST + (fs + (fs >= 5)) * 1024 + lane * 16, P);
  }
  K0_END();
}

constexpr unsigned G_ACC2 = K1_G_ACC + K1_NREP * 12288;

#define VA_CLOB \
  "v0", "v1", "v2", "v3", "v4", "v5", "v6", "v7", "v8", "v9", "v10", "v11", "v12", "v13", "v14", "v15", "v16", "v17", "v18", "v19", "v20", "v21", "v22", "v23", "v24", "v25", "v26", "v27", "v28", "v29", "v30", "v31", \
  "v32", "v33", "v34", "v35", "v36", "v37", "v38", "v39", "v40", "v41", "v42", "v43", "v44", "v45", "v46", "v47", "v48", "v49", "v50", "v51", "v52", "v53", "v54", "v55", "v56", "v57", "v58", "v59", "v60", "v61", "v62", "v63", \
  "v64", "v65", "v66", "v67", "v68", "v69", "v70", "v71", "v72", "v73", "v74", "v75", "v76", "v77", "v78", "v79", "v80", "v81", "v82", "v83", "v84", "v85", "v86", "v87", "v88", "v89", "v90", "v91", "v92", "v93", "v94", "v95", \
  "v96", "v97", "v98", "v99", "v100", "v101", "v102", "v103", "v104", "v105", "v106", "v107", "v108", "v109", "v110", "v111", "v112", "v113", "v114", "v115", "v116", "v117", "v118", "v119", "v120", "v121", "v122", "v123", "v124", "v125", "v126", "v127", \
  "v128", "v129", "v130", "v131", "v132", "v133", "v134", "v135", "v136", "v137", "v138", "v139", "v140", "v141", "v142", "v143", "v144", "v145", "v146", "v147", "v148", "v149", "v150", "v151", "v152", "v153", "v154", "v155", "v156", "v157", "v158", "v159", \
  "v160", "v161", "v162", "v163", "v164", "v165", "v166", "v167", "v168", "v169", "v170", "v171", "v172", "v173", "v174", "v175", "v176", "v177", "v178", "v179", "v180", "v181", "v182", "v183", "v184", "v185", "v186", "v187", "v188", "v189", "v190", "v191", \
  "v192", "v193", "v194", "v195", "v196", "v197", "v198", "v199", "v200", "v201", "v202", "v203", "v204", "v205", "v206", "v207", "v208", "v209", "v210", "v211", "v212", "v213", "v214", "v215", "v216", "v217", "v218", "v219", "v220", "v221", "v222", "v223", \
  "v224", "v225", "v226", "v227", "v228", "v229", "v230", "v231", "v232", "v233", "v234", "v235", "v236", "v237", "v238", "v239", "v240", "v241", "v242", "v243", "v244", "v245", "v246", "v247", "v248", "v249", "v250", "v251", "v252", "v253", "v254", "v255", \
  "a0", "a1", "a2", "a3", "a4", "a5", "a6", "a7", "a8", "a9", "a10", "a11", "a12", "a13", "a14", "a15", "a16", "a17", "a18", "a19", "a20", "a21", "a22", "a23", "a24", "a25", "a26", "a27", "a28", "a29", "a30", "a31", \
  "a32", "a33", "a34", "a35", "a36", "a37", "a38", "a39", "a40", "a41", "a42", "a43", "a44", "a45", "a46", "a47", "a48", "a49", "a50", "a51", "a52", "a53", "a54", "a55", "a56", "a57", "a58", "a59", "a60", "a61", "a62", "a63", \
  "a64", "a65", "a66", "a67", "a68", "a69", "a70", "a71", "a72", "a73", "a74", "a75", "a76", "a77", "a78", "a79", "a80", "a81", "a82", "a83", "a84", "a85", "a86", "a87", "a88", "a89", "a90", "a91", "a92", "a93", "a94", "a95", \
  "a96", "a97", "a98", "a99", "a100", "a101", "a102", "a103", "a104", "a105", "a106", "a107", "a108", "a109", "a110", "a111", "a112", "a113", "a114", "a115", "a116", "a117", "a118", "a119", "a120", "a121", "a122", "a123", "a124", "a125", "a126", "a127", \
  "a128", "a129", "a130", "a131", "a132", "a133", "a134", "a135", "a136", "a137", "a138", "a139", "a140", "a141", "a142", "a143", "a144", "a145", "a146", "a147", "a148", "a149", "a150", "a151", "a152", "a153", "a154", "a155", "a156", "a157", "a158", "a159", \
  "a160", "a161", "a162", "a163", "a164", "a165", "a166", "a167", "a168", "a169", "a170", "a171", "a172", "a173", "a174", "a175", "a176", "a177", "a178", "a179", "a180", "a181", "a182", "a183", "a184", "a185", "a186", "a187", "a188", "a189", "a190", "a191", \
  "a192", "a193", "a194", "a195", "a196", "a197", "a198", "a199", "a200", "a201", "a202", "a203", "a204", "a205", "a206", "a207", "a208", "a209", "a210", "a211", "a212", "a213", "a214", "a215", "a216", "a217", "a218", "a219", "a220", "a221", "a222", "a223", \
  "a224", "a225", "a226", "a227", "a228", "a229", "a230", "a231", "a232", "a233", "a234", "a235", "a236", "a237", "a238", "a239", "a240", "a241", "a242", "a243", "a244", "a245", "a246", "a247", "a248", "a249", "a250", "a251", "a252", "a253", "a254", "a255"
#define K1_CLOB "memory", "vcc", "scc", "s36", "s37", "s38", "s39", "s40", "s41", "s42", "s43", "s44", "s45", "s46", "s47", "s48", "s49", "s50", "s51", "s52", "s53", "s54", "s55", "s56", "s57", "s58", "s59", "s60", "s61", "s62", "s63", "s64", "s65", "s66", "s67", "s68", "s69", "s70", "s71", "s72", "s73", "s74", "s75", "s76", "s77", "s78", "s79", "s80", "s81", "s82", "s83", "s84", "s85", "s86", "s87", "s88", "s89", "s90", "s91", "s92", "s93", "s94", "s95", "s96", "s97", VA_CLOB
DI void k2_params(const u16* x, const char* hws, const u16* out, const u16* grn_g, unsigned tid) {
  const unsigned e = (tid >> 2) * 32 + blockIdx.x * 4 + (tid & 3);
  const unsigned b = e >> 2, w = e & 3, pg = w & 1, nh = w >> 1, q = (b >> 3) * 4 + (b & 3), rs = 128 + (q >> 1), hf = q & 1, side = wg_side(b);
  auto fmask = [&](unsigned region) {
    const unsigned o = ((unsigned)(size_t)hws & 0x1fffffu) + region * H_RS + nh * H_TS;
    const unsigned* p = (const unsigned*)(g_buf + K1_G_LOC + 64) + (o >> 21);
    const unsigned r = 0x200000u + 2 * H_TS - 1 - (o & 0x1fffffu), d1 = ((r >> 15) * 171) >> 9, d2 = (((r + 0x200000u) >> 15) * 171) >> 9;
    const unsigned a1 = ~0u << d1, a2 = d2 < 32 ? ~0u << d2 : 0u;
    return ((__popc(p[0]) > 2 ? ~a1 : 0u) | (__popc(p[1]) > 2 ? a1 & ~a2 : 0u) | (__popc(p[2]) > 2 ? a2 : 0u)) ^ (0u - side);
  };
  const unsigned f0 = fmask(q), f1 = fmask(rs);
  const unsigned fx = ((side ^ x_flip(x, q)) ? ~K2_SHT : 0u) | ((side ^ x_flip(x, rs)) ? K2_SHT : 0u);
  const uint64_t h0 = (uint64_t)hws + q * H_RS + nh * (H_TS + 2048) + pg * 3072, h1 = (uint64_t)hws + rs * H_RS + nh * (H_TS + 2048) + hf * 3072 + pg * 1536;
  const unsigned p0 = (12 * q + 2 * pg) * 49152 + nh * 384, p1 = (12 * rs + 2 * hf + pg) * 49152 + nh * 384;
  const unsigned e1 = 1024 + (e >> 3);
  auto kap = [](unsigned f) { return f / 48 * 2 + (f & 1); };
  auto sco = [&](unsigned f) { return kap(f) * 64 + (kap(f) < 16 ? K2_S16_D0 : 0); };
  auto cso = [&](unsigned f) { const unsigned r = f % 48; return ((f < 192 ? min(f / 48, 2u) : 3) * 384 + (r / 12 % 2 * 12 + r / 24 * 6 + r % 12 / 2) * 16) * 4; };
  typedef uint64_t u64x2 __attribute__((ext_vector_type(2)));
  u64x2* P = (u64x2*)(g_prm + 64 * e);
  P[0] = (u64x2){f0 | (uint64_t)f1 << 32, fx | (uint64_t)(sco(e) | (sco(e1) + (e & 6) * 8) << 16) << 32};
  const u32x4 sc = *(const u32x4*)g_sc;
  P[1] = (u64x2){(((b >> 3) * 4 + w) & 7) * 1024 | (uint64_t)sc[0] << 32, sc[1] | (uint64_t)sc[2] << 32};
  P[2] = (u64x2){h0, h0 + 8192};
  P[3] = (u64x2){h1, h1 + 8192};
  P[4] = (u64x2){__float_as_uint(g_sc[4]) | (uint64_t)((e1 - e) * 1024 + (e & 7) * 128) << 32, 0};
  P[5] = (u64x2){(uint64_t)x + p0, (uint64_t)x + p1};
  P[6] = (u64x2){(uint64_t)out + p0, (uint64_t)out + p1};
  P[7] = (u64x2){0x7fffffffffffffull, (uint64_t)g_buf + G_FLAG};
  P[8] = (u64x2){(uint64_t)g_w2f + w * 6144, (uint64_t)g_w2q + w * 6144};
  P[9] = (u64x2){(uint64_t)g_w2r + e * 1024, (uint64_t)g_w2f + e * 1024};
  P[10] = (u64x2){(uint64_t)grn_g + w * 256, (uint64_t)g_cs + w * 384 + 2048};
  P[11] = (u64x2){(uint64_t)g_buf + G_PUB + b * 256, (uint64_t)g_buf + G_PUB + w * 16384};
  P[12] = (u64x2){cso(e) | (uint64_t)(cso(e1) + (e & 1) * 32) << 32, (uint64_t)g_cs};
  P[13] = (u64x2){sc[3] | (uint64_t)1 << (32 + (b >> 3) % K2_NAT), 0};
}
__global__ __launch_bounds__(256, 1) void k1_fc1(const u16* __restrict__ x, char* __restrict__ hws, const u16* __restrict__ out, const u16* __restrict__ grn_g) {
  extern __shared__ __attribute__((aligned(16))) char smem[];
  const unsigned w = __builtin_amdgcn_readfirstlane(threadIdx.x >> 6);
  if (blockIdx.x < 8) {
    if (w < 2) k2_params(x, hws, out, grn_g, w * 64 + __builtin_amdgcn_mbcnt_hi(~0u, __builtin_amdgcn_mbcnt_lo(~0u, 0u)));
    return;
  }
  const unsigned lds0 = lds_addr(smem);
  const unsigned b = blockIdx.x - 8;
  const unsigned dir = (b >> 3) & 1;
  const uint64_t xp = (uint64_t)x, hp = (uint64_t)hws, gp = (uint64_t)g_buf, wp = gp + w * K1_WCH;
  const unsigned d = dir ? ~0u : 1u, wrap = dir ? ~0u : 48u, rst = dir ? 47u : 0u, pos0 = dir ? K1_SMAX - 1 : 0u;
  const unsigned xo0 = (12 * ((b >> 3) * 4 + (b & 3)) + w) * 49152, xs1 = (1536 + w + (dir ? 8 : 0)) * 49152, sb1 = 1536 * w + (dir ? K1_SB1 : 0);
  const unsigned r0o = ((b >> 3) * 4 + (b & 3)) * H_RS, tsel = 4 * (b & 3), s2 = (b >> 2) & 1;
  const uint64_t ap = gp + K1_G_ACC + (b % K1_NREP) * 12288;
  const unsigned ca0 = w == 0 ? dir * 64 : K1_G_DUM - K1_G_CNT + (4 * b + w) * 64;
  const unsigned pm = lds0 + K1_PF_OFF, pf = ((b >> 3) * 4 + w) * 1024;
  unsigned nb;
  asm volatile(
R"(.macro M t,f,w
.if \f < 30
v_mfma_f32_16x16x32_f16 v[56+4*\t:56+4*\t+3], v[128+4*\f:128+4*\f+3], a[216+4*\w:216+4*\w+3], v[56+4*\t:56+4*\t+3]
.else
v_mfma_f32_16x16x32_f16 v[56+4*\t:56+4*\t+3], a[4*\f-4*30:4*\f-4*30+3], a[216+4*\w:216+4*\w+3], v[56+4*\t:56+4*\t+3]
.endif
.endm
.macro Z t,f,w
.if \f < 30
v_mfma_f32_16x16x32_f16 v[56+4*\t:56+4*\t+3], v[128+4*\f:128+4*\f+3], a[216+4*\w:216+4*\w+3], v[252:255]
.else
v_mfma_f32_16x16x32_f16 v[56+4*\t:56+4*\t+3], a[4*\f-4*30:4*\f-4*30+3], a[216+4*\w:216+4*\w+3], v[252:255]
.endif
.endm
.macro LZ a,f
.if \f < 30
v_mfma_f32_16x16x32_bf16 v[36:36+3], v[\a:\a+3], v[128+4*\f:128+4*\f+3], 0
.else
v_mfma_f32_16x16x32_bf16 v[36:36+3], v[\a:\a+3], a[4*\f-4*30:4*\f-4*30+3], 0
.endif
.endm
.macro LS a,f
.if \f < 30
v_mfma_f32_16x16x32_bf16 v[36:36+3], v[\a:\a+3], v[128+4*\f:128+4*\f+3], v[36:36+3]
.else
v_mfma_f32_16x16x32_bf16 v[36:36+3], v[\a:\a+3], a[4*\f-4*30:4*\f-4*30+3], v[36:36+3]
.endif
.endm
.macro GZ f
.if \f < 30
v_mfma_f32_16x16x32_f16 v[52:55], v[128+4*\f:128+4*\f+3], v[128+4*\f:128+4*\f+3], 0
.else
v_mfma_f32_16x16x32_f16 v[52:55], a[4*\f-4*30:4*\f-4*30+3], a[4*\f-4*30:4*\f-4*30+3], 0
.endif
.endm
.macro GS f
.if \f < 30
v_mfma_f32_16x16x32_f16 v[52:55], v[128+4*\f:128+4*\f+3], v[128+4*\f:128+4*\f+3], v[52:55]
.else
v_mfma_f32_16x16x32_f16 v[52:55], a[4*\f-4*30:4*\f-4*30+3], a[4*\f-4*30:4*\f-4*30+3], v[52:55]
.endif
.endm
.macro XL f,o
.if \f < 30
global_load_dwordx4 v[128+4*\f:128+4*\f+3], v3, s[46:47] offset:\o
.else
global_load_dwordx4 a[4*\f-4*30:4*\f-4*30+3], v3, s[46:47] offset:\o
.endif
.endm
.macro C d,a,b
v_cvt_pk_bf16_f32 v[\d], v[\a], v[\b]
.endm
.macro LL t,z
v_lshlrev_b32_sdwa v[\t], 1, v[\z] dst_sel:DWORD dst_unused:UNUSED_PAD src0_sel:DWORD src1_sel:WORD_1
.endm
.macro LA t
ds_read_u16 v[\t], v[\t] offset:5376
.endm
.macro LC_ t
ds_read_u16_d16_hi v[\t], v[\t] offset:5376
.endm
.macro LO p,a,b
v_bfi_b32 v[\p], s36, v[\a], v[\b]
.endm
.macro HZ q,p
v_pk_mul_f16 v[\q], v[\p], v[\p]
.endm
.macro HS q,p
v_pk_fma_f16 v[\q], v[\p], v[\p], v[\q]
.endm
.macro HX q
v_fma_mix_f32 v[\q], v[\q], 1.0, v[\q] op_sel:[0,0,1] op_sel_hi:[1,0,1]
.endm
.macro AW a,s
v_accvgpr_write_b32 a[\a], v[\s]
.endm
.macro RA d,v,o
ds_read_b128 a[\d:\d+3], v[\v] offset:\o
.endm
.macro RV d,v,o
ds_read_b128 v[\d:\d+3], v[\v] offset:\o
.endm
.macro W2 a,b,o
ds_write2st64_b32 v4, v[\a], v[\b] offset0:\o offset1:\o+4
.endm
.macro W1 a,o
ds_write_b32 v4, v[\a] offset:\o
.endm
.macro DW o
global_load_lds_dwordx4 v0, s[40:41] offset:\o
.endm
.macro DF
global_load_lds_dwordx4 v0, s[70:71]
.endm
.macro ST d
.if ((\d - 104) / 4) & 1
global_store_dwordx4 v2, v[\d:\d+3], s[42:43] nt sc1
.else
global_store_dwordx2 v31, v[\d:\d+1], s[42:43] nt sc1
.endif
.endm
.macro SU d
.if ((\d - 104) / 4) & 1
global_store_dwordx4 v2, v[\d:\d+3], s[42:43]  sc1
.else
global_store_dwordx2 v31, v[\d:\d+1], s[42:43]  sc1
.endif
.endm
.macro SP
global_atomic_add_f64 v6, v[14:15], s[48:49]
.endm
.macro AT
global_atomic_add v11, v9, v10, s[68:69] sc0
.endm
.macro PFA
s_getpc_b64 s[70:71]
s_add_u32 s70, s70, 7f-.
s_addc_u32 s71, s71, 0
s_andn2_b32 s70, s70, 0x7f
s_min_u32 s53, %[pf], 8f-7f-1152
s_andn2_b32 s53, s53, 0x7f
s_add_u32 s70, s70, s53
s_addc_u32 s71, s71, 0
.endm
.macro PFN
s_getpc_b64 s[70:71]
s_add_u32 s70, s70, k2_code@rel32@lo-1020
s_addc_u32 s71, s71, k2_code@rel32@hi-1012
s_add_u32 s70, s70, %[pf]
s_addc_u32 s71, s71, 0
s_andn2_b32 s70, s70, 0x7f
.endm
.macro PFW
s_getpc_b64 s[70:71]
s_add_u32 s70, s70, g_w2q@rel32@lo+4
s_addc_u32 s71, s71, g_w2q@rel32@hi+12
s_add_u32 s70, s70, %[pf]
s_addc_u32 s71, s71, 0
.endm
.macro PF0
s_getpc_b64 s[70:71]
s_andn2_b32 s70, s70, 0x7f
s_min_u32 s53, %[pf], 7f-.-1152
s_andn2_b32 s53, s53, 0x7f
s_add_u32 s70, s70, s53
s_addc_u32 s71, s71, 0
.endm
.macro WL n
s_waitcnt lgkmcnt(\n)
.endm
.macro WV n
s_waitcnt vmcnt(\n)
.endm
.macro LU t,p
v_pk_fma_f16 v[\t], v[\p], v34, s67
.endm
.macro CF d,a,b
v_cvt_pk_f16_f32 v[\d], v[\a], v[\b]
.endm
.macro XS d
v_pk_fma_f16 v[\d], v[\d], v13, v33
.endm
.macro AR d,a
v_accvgpr_read_b32 v[\d], a[\a]
.endm
.macro DQ o
global_load_lds_dword v23, s[40:41] offset:\o
.endm
.macro GE
v_cmp_eq_u32 vcc, 0, v35
s_nop 4
s_cbranch_vccz 9f
v_mov_b32 v35, 14720
s_mov_b64 exec, 1
global_store_dword v35, v10, s[68:69] sc1
s_mov_b64 exec, -1
9:
.endm
.macro BW f
.if \f < 30
ds_write_b128 v12, v[128+4*\f:128+4*\f+3]
.else
ds_write_b128 v12, a[4*\f-120:4*\f-120+3]
.endif
.endm
.macro BR f
.if \f < 30
ds_read_b128 v[128+4*\f:128+4*\f+3], v20
.else
ds_read_b128 a[4*\f-120:4*\f-120+3], v20
.endif
.endm
.macro LK d,a,f,h
.if \f < 30
v_mfma_f32_4x4x4_16b_bf16 v[\d:\d+3], a[\a:\a+1], v[128+4*\f+\h:128+4*\f+\h+1], v[36:39]
.else
v_mfma_f32_4x4x4_16b_bf16 v[\d:\d+3], a[\a:\a+1], a[4*\f-120+\h:4*\f-120+\h+1], v[36:39]
.endif
.endm
.macro PK d,a,b,s
v_perm_b32 v[\d], v[\a], v[\b], s[\s]
.endm
.macro PO d
v_lshl_or_b32 v[\d], v[\d], 4, v[\d-4]
.endm
.macro LG a,b
v_cmp_eq_f32 vcc, v[\a], v[\b]
v_mov_b32 v[\b], 0x7f800000
s_nop 0
v_cndmask_b32 v[\a], v[\b], v[\a], vcc
.endm
.macro DY
s_mov_b32 s72, m0
s_mov_b32 m0, %[pm]
s_nop 0
global_load_lds_dword v3, s[46:47]
s_mov_b32 m0, s72
s_nop 0
.endm
.macro FP
ds_write_b32 v6, v19 offset:61440
.endm
.macro FR
ds_read_b32 v15, v31 offset:61440
.endm
k1_code:
v_mbcnt_lo_u32_b32 v16, -1, 0
v_mbcnt_hi_u32_b32 v16, -1, v16
v_lshlrev_b32 v0, 4, v16
v_and_b32 v17, 15, v16
v_lshrrev_b32 v18, 4, v16
s_add_u32 s68, %[glo], 1239040
s_addc_u32 s69, %[ghi], 0
s_mov_b32 s53, %[r0o]
s_load_dword s67, s[68:69], 16320
s_add_u32 s82, %[hlo], s53
s_addc_u32 s83, %[hhi], 0
s_add_u32 s84, s82, 8192
s_addc_u32 s85, s83, 0
s_and_b32 s90, %[hlo], 0x1fffff
s_add_u32 s90, s90, s53
s_mov_b32 s91, 3072
v_mov_b32 v11, s90
v_mad_u32_u24 v11, v0, s91, v11
v_lshrrev_b32 v11, 21, v11
v_lshl_add_u32 v11, v11, 2, 64
v_writelane_b32 v11, %[tsel], 63
s_sub_u32 s53, s82, %[hlo]
s_lshr_b32 s53, s53, 2
s_and_b32 s90, %[xlo], 0x1fffff
s_add_u32 s53, s53, s90
s_add_u32 s53, s53, 0x20048000
s_lshr_b32 s53, s53, 21
s_lshl_b32 s90, s53, 2
v_writelane_b32 v11, s90, 48
global_load_dword v11, v11, s[68:69] offset:-2048
v_lshlrev_b32 v19, 1, v18
s_movk_i32 s53, 0x78
v_bfe_u32 v21, v16, 2, 2
v_lshrrev_b32 v19, v19, s53
v_mul_u32_u24 v21, 0x300, v21
v_xor_b32 v19, v19, v16
v_and_b32 v19, 3, v19
v_lshl_add_u32 v21, v19, 4, v21
v_mov_b32 v32, 3072
s_mov_b32 s50, %[pos0]
s_mov_b32 s51, %[pos0]
s_mov_b32 s52, %[pos0]
s_add_u32 s74, %[lds], 0
s_add_u32 s75, s74, 163840
s_mov_b32 s37, s74
s_mul_i32 s53, %[w], 6400
s_add_u32 s76, s74, s53
s_add_u32 s77, s76, 163840
s_mov_b32 s38, s76
s_mul_i32 s60, %[w], 1536
s_add_u32 s61, s60, 512
s_add_u32 s62, s60, 16384
s_add_u32 s63, s60, 16896
s_add_u32 s64, s60, 32768
s_add_u32 s65, s60, 33280
v_lshlrev_b32 v23, 2, v16
s_lshl_b32 s53, %[w], 11
s_min_u32 s53, s53, 5120
s_add_u32 s72, s53, 1228800
s_add_u32 s40, %[glo], s72
s_addc_u32 s41, %[ghi], 0
s_add_u32 s53, s53, %[lds]
s_add_u32 m0, s53, 58368
s_nop 0
DW 0
DW 1024
s_nop 0 ;PS
s_mul_i32 s53, s50, 25600
s_add_u32 s40, %[wlo], s53
s_addc_u32 s41, %[whi], 0
)"
R"(s_mov_b32 m0, s38
s_nop 0
DW 0
DW 1024
DW 2048
s_add_u32 s40, s40, 3072
s_addc_u32 s41, s41, 0
s_add_u32 m0, s38, 3072
s_nop 0
DW 0
DW 1024
DW 2048
DQ 3072
s_add_u32 s50, s50, %[d]
s_cmp_eq_u32 s50, %[wrap]
s_cselect_b32 s50, %[rst], s50
s_add_u32 s38, s38, 32768
s_cmp_eq_u32 s38, s77
s_cselect_b32 s38, s76, s38
v_add_u32 v1, s37, v0
v_lshl_add_u32 v19, v18, 2, v17
v_and_b32 v19, 15, v19
v_lshlrev_b32 v19, 4, v19
v_lshl_add_u32 v2, v18, 8, v19
s_lshl_b32 s53, %[w], 2
s_add_u32 s53, s53, %[lds]
s_add_u32 s53, s53, 123904
s_add_u32 s53, %[lds], 123904
v_bfe_u32 v4, v17, 1, 2
v_and_b32 v5, 8, v17
v_add3_u32 v4, v4, v5, %[w]
v_lshl_add_u32 v4, v18, 2, v4
v_and_b32 v4, 15, v4
v_lshl_add_u32 v4, v17, 4, v4
v_lshl_add_u32 v4, v4, 2, s53
s_lshl_b32 s53, %[w], 9
s_add_u32 s53, s53, %[lds]
s_add_u32 s53, s53, 123904
v_lshlrev_b32 v5, 3, v16
v_add_u32 v5, s53, v5
s_lshl_b32 s53, %[w], 6
v_and_b32 v6, 0x38, v16
v_add_u32 v6, s53, v6
s_add_u32 s53, %[lds], 58368
v_and_b32 v19, 3, v16
v_lshl_add_u32 v19, v18, 2, v19
v_lshl_add_u32 v7, v19, 4, s53
v_lshlrev_b32 v8, 4, v18
v_add_u32 v8, s53, v8
v_mov_b32 v10, 1
s_mov_b32 s36, 0xffff
s_mov_b32 s56, 0x5550555
s_mov_b32 s39, 32768
s_mov_b32 s54, 0x01010101
s_mov_b32 s55, 0x01010101
s_mov_b32 %[nb], 0
v_and_b32 v28, 12, v17
v_lshl_add_u32 v28, v28, 2, v17
v_lshlrev_b32 v28, 2, v28
v_mov_b32 v34, 0x74007400
v_mov_b32 v35, -1
v_and_b32 v22, 0xfffffcff, v1
v_lshrrev_b32 v31, 1, v2
s_mov_b32 s97, 0x06020400
s_mov_b32 s56, 0x07030501
v_mov_b32 v33, 0x5550555
s_waitcnt lgkmcnt(0)
s_mov_b32 s78, 0xaaaaaaaa
s_mov_b32 s79, 0xaaaaaaaa
s_mov_b32 s80, 0xcccccccc
s_mov_b32 s81, 0xcccccccc
s_and_b32 s53, %[w], 1
s_lshl_b32 s53, s53, 10
s_lshr_b32 s72, %[w], 1
s_mul_i32 s72, s72, 32768
s_add_u32 s72, s72, s53
s_add_u32 s72, s72, 128000
s_movk_i32 s53, 0x78
v_lshrrev_b32 v12, 5, v0
v_and_b32 v12, 6, v12
v_bfe_u32 v12, s53, v12, 2
v_lshrrev_b32 v20, 8, v0
v_xor_b32 v20, v12, v20
v_lshl_add_u32 v20, v20, 4, s72
v_and_b32 v12, 0xf0, v0
v_lshl_add_u32 v20, v12, 2, v20
v_add_u32 v12, s72, v0
WV 9
v_bcnt_u32_b32 v11, v11, 0
v_cmp_lt_u32 s[90:91], 2, v11
s_nop 0
s_lshr_b32 s96, s91, 31
s_xor_b32 s96, s96, %[s2]
s_sub_u32 s53, 0, s96
s_xor_b32 s90, s90, s53
s_xor_b32 s91, s91, s53
s_add_u32 s53, %[xo0], 0
s_add_u32 s46, %[xlo], s53
s_addc_u32 s47, %[xhi], 0
s_bitcmp1_b64 s[90:91], 48
s_cselect_b32 s58, 16, 0
s_lshr_b32 s58, 0xfe987610, s58
v_lshrrev_b32 v3, 6, v0
v_and_b32 v3, 12, v3
v_bfe_u32 v3, s58, v3, 4
v_mad_u32_u24 v3, v3, v32, v21
XL 12,0
XL 13,64
XL 14,128
XL 15,192
XL 16,256
XL 17,320
XL 18,384
XL 19,448
XL 20,512
XL 21,576
XL 22,640
XL 23,704
s_mul_i32 s53, s96, 8320
s_cmp_lt_u32 %[ca0], 128
s_cselect_b32 s53, s53, 0
s_add_u32 s73, s53, %[ca0]
v_mov_b32 v9, s73
WV 19
s_barrier
v_lshrrev_b32 v19, 8, v31
v_sub_u32 v19, 0, v19
ds_write_b32 v31, v19 offset:61440
s_nop 1
s_nop 0
v_mov_b32 v19, 0
WV 11
BW 12
RV 40,8,6144
RV 44,8,6208
RV 48,8,6272
RV 248,8,6336
BR 12
WV 10
BW 13
s_nop 1
BR 13
WL 2
LZ 40,12
WV 9
BW 14
RV 40,8,6400
s_nop 0
BR 14
s_add_u32 s53, %[xo0], 0
s_add_u32 s46, %[xlo], s53
s_addc_u32 s47, %[xhi], 0
s_bitcmp1_b64 s[90:91], 48
s_cselect_b32 s58, 16, 0
s_lshr_b32 s58, 0xda43cb52, s58
v_lshrrev_b32 v3, 6, v0
v_and_b32 v3, 12, v3
v_bfe_u32 v3, s58, v3, 4
v_mad_u32_u24 v3, v3, v32, v21
WL 3
LS 44,13
WV 8
BW 15
RV 44,8,6464
s_nop 0
BR 15
XL 24,0
WL 3
LS 48,14
WV 8
BW 16
RV 48,8,6528
s_nop 0
BR 16
WL 3
LS 248,15
WV 7
BW 17
RV 248,8,6592
s_nop 0
BR 17
XL 25,64
WL 3
LS 40,16
WV 7
BW 18
RV 40,8,6656
s_nop 0
BR 18
XL 26,128
WL 3
LS 44,17
WV 7
BW 19
RV 44,8,6720
s_nop 0
BR 19
WL 3
LS 48,18
WV 6
BW 20
RV 48,8,6784
s_nop 0
BR 20
XL 27,192
WL 3
LS 248,19
WV 6
BW 21
RV 248,8,6848
s_nop 0
BR 21
XL 28,256
WL 3
LS 40,20
WV 6
BW 22
RA 240,7,0
s_nop 0
BR 22
WL 3
LS 44,21
WV 5
BW 23
RA 244,7,256
s_nop 0
BR 23
XL 29,320
WL 3
LS 48,22
RA 248,7,512
XL 30,384
WL 1
LS 248,23
RA 252,7,768
s_nop 6
v_mul_f32 v36, 0xbb2aaaab, v36
v_mul_f32 v37, 0xbb2aaaab, v37
v_mul_f32 v38, 0xbb2aaaab, v38
v_mul_f32 v39, 0xbb2aaaab, v39
s_nop 1
LK 40,240,12,0
LK 44,242,12,2
RA 240,7,1024
XL 31,448
LK 48,244,13,0
s_nop 0
CF 176,40,41
LK 248,246,13,2
WL 2
RA 244,7,1280
CF 177,42,43
CF 178,44,45
CF 179,46,47
XL 32,512
LK 40,248,14,0
CF 180,48,49
LK 44,250,14,2
RA 248,7,1536
CF 181,50,51
GZ 12
WL 3
XL 33,576
CF 182,248,249
CF 183,250,251
LK 48,252,15,0
CF 184,40,41
LK 248,254,15,2
RA 252,7,1792
CF 185,42,43
XL 34,640
GS 13
WL 3
CF 186,44,45
CF 187,46,47
LK 40,240,16,0
CF 188,48,49
LK 44,242,16,2
WL 2
RA 240,7,2048
CF 189,50,51
XL 35,704
GS 14
s_mul_i32 s53, s50, 25600
s_add_u32 s40, %[wlo], s53
s_addc_u32 s41, %[whi], 0
s_mov_b32 m0, s38
CF 190,248,249
CF 191,250,251
LK 48,244,17,0
CF 192,40,41
LK 248,246,17,2
RA 244,7,2304
CF 193,42,43
GS 15
WL 3
DW 0
CF 194,44,45
CF 195,46,47
LK 40,248,18,0
CF 196,48,49
LK 44,250,18,2
RA 248,7,2560
CF 197,50,51
DW 1024
GS 16
WL 3
CF 198,248,249
CF 199,250,251
LK 48,252,19,0
CF 200,40,41
LK 248,254,19,2
WL 2
RA 252,7,2816
CF 201,42,43
DW 2048
GS 17
s_add_u32 s40, s40, 3072
s_addc_u32 s41, s41, 0
s_add_u32 m0, s38, 3072
CF 202,44,45
CF 203,46,47
LK 40,240,20,0
CF 204,48,49
LK 44,242,20,2
CF 205,50,51
GS 18
WL 2
DW 0
CF 206,248,249
CF 207,250,251
LK 48,244,21,0
CF 208,40,41
LK 248,246,21,2
CF 209,42,43
DW 1024
GS 19
WL 1
CF 210,44,45
CF 211,46,47
LK 40,248,22,0
CF 212,48,49
LK 44,250,22,2
CF 213,50,51
DW 2048
GS 20
WL 0
DQ 3072
CF 214,248,249
CF 215,250,251
LK 48,252,23,0
CF 216,40,41
LK 248,254,23,2
CF 217,42,43
GS 21
s_add_u32 s50, s50, %[d]
s_cmp_eq_u32 s50, %[wrap]
s_cselect_b32 s50, %[rst], s50
CF 218,44,45
CF 219,46,47
RV 36,8,6912
s_nop 0
GS 22
s_add_u32 s38, s38, 32768
s_cmp_eq_u32 s38, s77
s_cselect_b32 s38, s76, s38
CF 220,48,49
CF 221,50,51
CF 222,248,249
CF 223,250,251
s_nop 1
GS 23
s_nop 7
v_cndmask_b32 v52, v52, v53, s[78:79]
v_cndmask_b32 v54, v54, v55, s[78:79]
v_cndmask_b32 v52, v52, v54, s[80:81]
v_mul_f32 v52, 0x3b2aaaab, v52
WL 0
v_med3_f32 v53, v52, v39, v37
LG 52,53
s_nop 0
C 52,52,52
v_lshlrev_b32 v52, 16, v52
v_add_f32 v52, v36, v52
C 52,52,52
v_lshlrev_b32 v52, 16, v52
v_sqrt_f32 v52, v52
s_nop 0
C 52,52,52
v_lshlrev_b32 v52, 16, v52
v_rcp_f32 v52, v52
s_nop 0
v_mul_f32 v52, v38, v52
CF 52,52,52
ds_bpermute_b32 v13, v28, v52
WV 18
BW 24
RV 40,8,6144
RV 44,8,6208
RV 48,8,6272
)"
R"(BR 24
WV 17
BW 25
s_nop 1
BR 25
WL 7
XS 176
WL 2
LZ 40,24
s_and_b32 s53, %[w], 1
s_mul_i32 s53, s53, 3072
s_lshr_b32 s72, %[w], 1
s_lshl_b32 s40, s72, 16
s_add_u32 s40, s40, s53
s_add_u32 m0, s40, 25600
s_lshl_b32 s72, s72, 13
s_add_u32 s53, s53, s72
s_add_u32 s53, s53, 1382400
s_add_u32 s40, %[glo], s53
s_addc_u32 s41, %[ghi], 0
XS 177
WV 16
BW 26
XS 178
RV 40,8,6336
XS 179
BR 26
XS 180
WL 3
LS 44,25
WV 15
DW 0
XS 181
BW 27
XS 182
RV 44,8,6400
XS 183
BR 27
v_min_u32 v35, v35, v13
WL 3
LS 48,26
WV 15
DW 1024
XS 184
BW 28
XS 185
RV 48,8,6464
XS 186
BR 28
XS 187
WL 3
LS 40,27
WV 15
XS 188
BW 29
XS 189
RV 40,8,6528
XS 190
XS 191
BR 29
XS 192
WL 3
LS 44,28
WV 14
DW 2048
XS 193
BW 30
XS 194
RV 44,8,6592
XS 195
BR 30
XS 196
WL 3
LS 48,29
WV 14
DW 3072
XS 197
BW 31
XS 198
RV 48,8,6656
XS 199
BR 31
XS 200
WL 3
LS 40,30
WV 13
XS 201
BW 32
XS 202
RV 40,8,6720
XS 203
BR 32
XS 204
WL 3
LS 44,31
PF0
s_mov_b32 m0, %[pm]
s_nop 0
DF
PFA
XS 205
BW 33
XS 206
XS 207
RV 44,8,6784
XS 208
BR 33
XS 209
WL 3
LS 48,32
s_mul_i32 s53, s50, 25600
s_add_u32 s40, %[wlo], s53
s_addc_u32 s41, %[whi], 0
s_mov_b32 m0, s38
XS 210
WV 13
BW 34
XS 211
RV 48,8,6848
XS 212
RA 240,7,0
XS 213
BR 34
XS 214
WL 4
LS 40,33
WV 12
XS 215
BW 35
XS 216
RA 244,7,256
XS 217
BR 35
XS 218
WL 3
LS 44,34
DW 0
XS 219
RA 248,7,512
XS 220
WL 1
LS 48,35
DW 1024
XS 221
RA 252,7,768
XS 222
XS 223
s_nop 2
v_mul_f32 v36, 0xbb2aaaab, v36
v_mul_f32 v37, 0xbb2aaaab, v37
v_mul_f32 v38, 0xbb2aaaab, v38
v_mul_f32 v39, 0xbb2aaaab, v39
s_nop 1
LK 40,240,24,0
LK 44,242,24,2
RA 240,7,1024
LK 48,244,25,0
s_nop 1
CF 224,40,41
LK 248,246,25,2
WL 2
RA 244,7,1280
CF 225,42,43
CF 226,44,45
CF 227,46,47
DW 2048
LK 40,248,26,0
CF 228,48,49
LK 44,250,26,2
RA 248,7,1536
CF 229,50,51
s_add_u32 s40, s40, 3072
s_addc_u32 s41, s41, 0
s_add_u32 m0, s38, 3072
GZ 24
WL 3
CF 230,248,249
CF 231,250,251
LK 48,252,27,0
CF 232,40,41
LK 248,254,27,2
RA 252,7,1792
CF 233,42,43
DW 0
GS 25
WL 3
DW 1024
CF 234,44,45
CF 235,46,47
LK 40,240,28,0
CF 236,48,49
LK 44,242,28,2
RA 240,7,2048
CF 237,50,51
GS 26
WL 3
DW 2048
CF 238,248,249
CF 239,250,251
LK 48,244,29,0
CF 240,40,41
LK 248,246,29,2
RA 244,7,2304
CF 241,42,43
DQ 3072
GS 27
WL 3
CF 242,44,45
CF 243,46,47
LK 40,248,30,0
CF 244,48,49
LK 44,250,30,2
WL 2
RA 248,7,2560
CF 245,50,51
s_add_u32 s50, s50, %[d]
s_cmp_eq_u32 s50, %[wrap]
s_cselect_b32 s50, %[rst], s50
GS 28
s_add_u32 s38, s38, 32768
s_cmp_eq_u32 s38, s77
s_cselect_b32 s38, s76, s38
CF 246,248,249
CF 247,250,251
LK 48,252,31,0
CF 152,40,41
LK 248,254,31,2
WL 2
RA 252,7,2816
CF 153,42,43
GS 29
s_add_u32 s53, %[xo0], 196608
s_add_u32 s46, %[xlo], s53
s_addc_u32 s47, %[xhi], 0
s_bitcmp1_b64 s[90:91], 48
s_cselect_b32 s58, 16, 0
s_lshr_b32 s58, 0xfe987610, s58
v_lshrrev_b32 v3, 6, v0
v_and_b32 v3, 12, v3
v_bfe_u32 v3, s58, v3, 4
v_mad_u32_u24 v3, v3, v32, v21
CF 154,44,45
CF 155,46,47
LK 40,240,32,0
CF 156,48,49
LK 44,242,32,2
CF 157,50,51
XL 36,0
GS 6
WL 2
CF 158,248,249
CF 159,250,251
LK 48,244,33,0
CF 160,40,41
LK 248,246,33,2
CF 161,42,43
XL 37,64
GS 7
WL 1
XL 38,128
CF 162,44,45
CF 163,46,47
LK 40,248,34,0
CF 164,48,49
LK 44,250,34,2
CF 165,50,51
GS 8
WL 0
XL 39,192
CF 166,248,249
CF 167,250,251
LK 48,252,35,0
CF 168,40,41
LK 248,254,35,2
CF 169,42,43
XL 40,256
GS 9
CF 170,44,45
CF 171,46,47
RV 36,8,6912
s_nop 0
GS 10
XL 41,320
CF 172,48,49
CF 173,50,51
CF 174,248,249
CF 175,250,251
s_nop 1
GS 11
XL 42,384
WV 15
s_barrier
RA 216,1,0
RA 220,1,1024
RA 224,1,2048
RA 228,1,3072
RV 252,22,5120
v_cndmask_b32 v52, v52, v53, s[78:79]
v_cndmask_b32 v54, v54, v55, s[78:79]
v_cndmask_b32 v52, v52, v54, s[80:81]
v_mul_f32 v52, 0x3b2aaaab, v52
WL 5
v_med3_f32 v53, v52, v39, v37
LG 52,53
s_nop 0
C 52,52,52
v_lshlrev_b32 v52, 16, v52
v_add_f32 v52, v36, v52
C 52,52,52
XL 43,448
v_lshlrev_b32 v52, 16, v52
v_sqrt_f32 v52, v52
s_nop 0
C 52,52,52
v_lshlrev_b32 v52, 16, v52
v_rcp_f32 v52, v52
XL 44,512
v_mul_f32 v52, v38, v52
CF 52,52,52
ds_bpermute_b32 v13, v28, v52
WL 0
XS 224
XS 225
XS 226
XS 227
XS 228
XS 229
XS 230
XL 45,576
XS 231
v_min_u32 v35, v35, v13
XS 232
XS 233
XS 234
XL 46,640
XS 235
XS 236
XS 237
XS 238
XS 239
XL 47,704
Z 0,12,0
XS 240
XS 241
XS 242
XS 243
XS 244
XS 245
XS 246
XS 247
XS 152
XS 153
XS 154
XS 155
M 0,13,1
s_mul_i32 s53, s50, 25600
RA 232,1,4096
AW 0,152
AW 1,153
Z 1,24,0
s_add_u32 s40, %[wlo], s53
AW 2,154
AW 3,155
M 1,25,1
s_addc_u32 s41, %[whi], 0
RA 236,1,6144
XS 156
XS 157
M 1,26,2
s_mov_b32 m0, s38
s_nop 0
DW 0
XS 158
XS 159
AW 4,156
M 1,27,3
RA 216,1,7168
AW 5,157
AW 6,158
M 0,14,2
DW 1024
AW 7,159
XS 160
M 0,15,3
WL 2
RA 220,1,8192
XS 161
XS 162
M 0,16,4
WL 2
XS 163
AW 8,160
M 0,17,5
RA 224,1,9216
AW 9,161
AW 10,162
AW 11,163
M 1,28,4
WL 1
DW 2048
)"
R"(XS 164
XS 165
M 1,29,5
s_add_u32 s40, s40, 3072
RA 228,1,10240
XS 166
XS 167
M 1,30,0
s_addc_u32 s41, s41, 0
AW 12,164
AW 13,165
M 1,31,1
s_add_u32 m0, s38, 3072
RA 232,1,11264
AW 14,166
AW 15,167
M 0,18,0
RV 252,22,5392
XS 168
XS 169
M 0,19,1
WL 3
RA 236,1,12288
XS 170
XS 171
AW 16,168
M 0,20,2
WL 3
DW 0
AW 17,169
AW 18,170
M 0,21,3
RA 216,1,13312
AW 19,171
XS 172
M 1,32,2
DW 1024
XS 173
XS 174
M 1,33,3
WL 3
RA 220,1,14336
XS 175
AW 20,172
M 0,22,4
WL 2
DW 2048
AW 21,173
AW 22,174
AW 23,175
M 0,23,5
RA 224,1,15360
M 1,34,4
DQ 3072
M 1,35,5
WL 1
RA 228,1,16384
Z 6,12,0
s_add_u32 s50, s50, %[d]
LL 16,56
LL 104,57
M 6,13,1
s_cmp_eq_u32 s50, %[wrap]
LA 16
RA 232,1,17408
Z 7,24,0
s_cselect_b32 s50, %[rst], s50
LC_ 104
M 7,25,1
s_add_u32 s38, s38, 32768
RA 236,1,18432
WL 5
M 7,26,2
s_cmp_eq_u32 s38, s77
WV 18
BW 36
s_cselect_b32 s38, s76, s38
WL 5
M 7,27,3
s_add_u32 s37, s37, 32768
RA 216,1,19456
RV 40,8,6144
M 6,14,2
s_cmp_eq_u32 s37, s75
LL 17,58
LL 105,59
M 6,15,3
s_cselect_b32 s37, s74, s37
LA 17
RA 220,1,20480
s_add_u32 s53, %[xo0], 196608
s_add_u32 s46, %[xlo], s53
s_addc_u32 s47, %[xhi], 0
s_bitcmp1_b64 s[90:91], 48
s_cselect_b32 s58, 16, 0
s_lshr_b32 s58, 0xda43cb52, s58
v_lshrrev_b32 v3, 6, v0
v_and_b32 v3, 12, v3
v_bfe_u32 v3, s58, v3, 4
RV 44,8,6208
RV 48,8,6272
v_mad_u32_u24 v3, v3, v32, v21
WL 9
M 6,16,4
WL 7
LC_ 105
XL 48,0
BR 36
M 6,17,5
s_mul_i32 s53, s51, 24
RA 224,1,21504
WV 18
BW 37
s_nop 1
BR 37
M 7,28,4
WV 17
LO 104,16,104
LU 16,104
M 7,29,5
s_bitcmp1_b32 s51, 0
RA 228,1,22528
HZ 24,16
WL 4
LZ 40,36
s_addc_u32 s53, s53, 0
BW 38
RV 40,8,6336
M 7,30,0
s_lshl_b32 s53, s53, 11
LL 18,60
LL 108,61
s_bitcmp1_b64 s[90:91], s51
s_cselect_b64 s[44:45], s[84:85], s[82:83]
s_add_u32 s44, s44, s53
s_addc_u32 s45, s45, 0
s_add_u32 s51, s51, %[d]
s_cmp_eq_u32 s51, %[wrap]
s_cselect_b32 s51, %[rst], s51
XL 49,64
BR 38
M 7,31,1
WL 4
LA 18
RA 232,1,23552
LS 44,37
WV 17
BW 39
M 6,18,0
LC_ 108
XL 50,128
RV 44,8,6400
M 6,19,1
RA 236,1,24576
BR 39
WL 7
LS 48,38
WV 17
BW 40
M 6,20,2
LO 105,17,105
LU 17,105
M 6,21,3
XL 51,192
RV 48,8,6464
BR 40
HS 24,17
M 7,32,2
LL 16,62
LL 109,63
v_add_u32 v1, s37, v0
M 7,33,3
WL 3
LA 16
RA 216,1,0
XL 52,256
LS 40,39
WV 18
BW 41
RV 40,8,6528
M 6,22,4
WL 4
LC_ 109
XL 53,320
v_and_b32 v22, 0xfffffcff, v1
BR 41
M 6,23,5
s_barrier
RA 220,1,1024
LS 44,40
WV 18
BW 42
M 7,34,4
RV 44,8,6592
LO 108,18,108
LU 18,108
M 7,35,5
RV 252,22,5120
BR 42
HS 24,18
WL 1
Z 0,12,0
WV 17
RA 224,1,2048
LL 17,80
LL 106,81
M 0,13,1
s_mul_i32 s53, s50, 25600
LA 17
RA 228,1,3072
LS 48,41
s_add_u32 s40, %[wlo], s53
BW 43
RV 48,8,6656
s_nop 0
BR 43
Z 1,24,0
s_addc_u32 s41, %[whi], 0
LC_ 106
RA 232,1,4096
M 1,25,1
s_mov_b32 m0, s38
RA 236,1,6144
WL 9
LS 40,42
WV 16
BW 44
RV 40,8,6720
WL 10
M 1,26,2
WL 8
BR 44
LO 109,16,109
LU 16,109
M 1,27,3
WL 6
RA 216,1,7168
HS 24,16
LS 44,43
WV 15
BW 45
M 0,14,2
DW 0
RV 44,8,6784
LL 18,82
LL 107,83
M 0,15,3
WL 3
LA 18
RA 220,1,8192
BR 45
LS 48,44
WV 15
BW 46
M 0,16,4
LC_ 107
DW 1024
M 0,17,5
RA 224,1,9216
RV 48,8,6848
RA 240,7,0
HX 24
M 1,28,4
LO 17,17,106
LU 106,17
W1 24,0
BR 46
M 1,29,5
WL 7
RA 228,1,10240
DW 2048
HZ 25,106
LS 40,45
WV 16
BW 47
M 1,30,0
s_add_u32 s40, s40, 3072
RA 244,7,256
PK 106,17,104,56
PK 104,17,104,97
LL 16,84
LL 110,85
M 1,31,1
s_addc_u32 s41, s41, 0
LA 16
RA 232,1,11264
BR 47
WL 6
LS 44,46
s_add_u32 m0, s38, 3072
RA 248,7,512
M 0,18,0
LC_ 110
RV 252,22,5392
M 0,19,1
RA 236,1,12288
WL 4
LS 48,47
RA 252,7,768
M 0,20,2
LO 18,18,107
LU 107,18
s_nop 3
v_mul_f32 v36, 0xbb2aaaab, v36
v_mul_f32 v37, 0xbb2aaaab, v37
v_mul_f32 v38, 0xbb2aaaab, v38
M 0,21,3
RA 216,1,13312
DW 0
HS 25,107
v_mul_f32 v39, 0xbb2aaaab, v39
s_nop 1
LK 40,240,36,0
LK 44,242,36,2
RA 240,7,1024
LK 48,244,37,0
s_nop 1
CF 128,40,41
LK 248,246,37,2
RA 244,7,1280
CF 129,42,43
M 1,32,2
PK 107,18,105,56
PK 105,18,105,97
LL 17,86
LL 111,87
M 1,33,3
WL 7
LA 17
RA 220,1,14336
DW 1024
CF 130,44,45
CF 131,46,47
LK 40,248,38,0
WL 5
CF 132,48,49
LK 44,250,38,2
s_add_u32 s42, s44, s60
RA 248,7,1536
CF 133,50,51
GZ 0
s_addc_u32 s43, s45, 0
CF 134,248,249
CF 135,250,251
LK 48,252,39,0
CF 136,40,41
LK 248,254,39,2
RA 252,7,1792
CF 137,42,43
GS 1
WL 5
CF 138,44,45
CF 139,46,47
LK 40,240,40,0
CF 140,48,49
LK 44,242,40,2
RA 240,7,2048
CF 141,50,51
GS 2
WL 5
CF 142,248,249
CF 143,250,251
LK 48,244,41,0
CF 144,40,41
LK 248,246,41,2
RA 244,7,2304
CF 145,42,43
GS 3
CF 146,44,45
CF 147,46,47
M 0,22,4
LC_ 111
ST 104
M 0,23,5
WL 4
RA 224,1,15360
LK 40,248,42,0
CF 148,48,49
LK 44,250,42,2
RA 248,7,2560
CF 149,50,51
GS 4
CF 150,248,249
CF 151,250,251
M 1,34,4
DW 2048
LO 16,16,110
LU 110,16
M 1,35,5
WL 5
RA 228,1,16384
HS 25,110
LK 48,252,43,0
CF 152,40,41
LK 248,254,43,2
RA 252,7,2816
CF 153,42,43
GS 5
CF 154,44,45
CF 155,46,47
)"
R"(Z 6,12,0
PK 110,16,108,56
PK 108,16,108,97
LL 18,56
LL 104,57
M 6,13,1
WL 6
LA 18
DQ 3072
PO 110
LK 40,240,44,0
CF 156,48,49
LK 44,242,44,2
s_add_u32 s50, s50, %[d]
CF 157,50,51
GS 6
s_cmp_eq_u32 s50, %[wrap]
CF 158,248,249
CF 159,250,251
Z 7,24,0
s_cselect_b32 s50, %[rst], s50
LC_ 104
RA 232,1,17408
M 7,25,1
WL 8
RA 236,1,18432
LK 48,244,45,0
CF 160,40,41
LK 248,246,45,2
CF 161,42,43
GS 7
WL 7
CF 162,44,45
CF 163,46,47
M 7,26,2
WL 5
LO 17,17,111
LU 111,17
M 7,27,3
s_add_u32 s38, s38, 32768
RA 216,1,19456
HS 25,111
LK 40,248,46,0
WL 5
CF 164,48,49
LK 44,250,46,2
s_cmp_eq_u32 s38, s77
CF 165,50,51
GS 8
s_cselect_b32 s38, s76, s38
CF 166,248,249
CF 167,250,251
M 6,14,2
s_add_u32 s37, s37, 32768
PK 111,17,109,56
PK 109,17,109,97
LL 16,58
LL 105,59
M 6,15,3
s_cmp_eq_u32 s37, s75
LA 16
PO 111
s_cselect_b32 s37, s74, s37
LK 48,252,47,0
CF 168,40,41
LK 248,254,47,2
CF 169,42,43
HX 25
GS 9
WL 3
CF 170,44,45
CF 171,46,47
M 6,16,4
WL 2
LC_ 105
RA 220,1,20480
XL 54,384
M 6,17,5
s_add_u32 s42, s44, s61
RA 224,1,21504
RV 36,8,6912
s_addc_u32 s43, s45, 0
GS 10
CF 172,48,49
CF 173,50,51
M 7,28,4
LO 104,18,104
LU 18,104
W1 25,1024
M 7,29,5
WL 3
RA 228,1,22528
HZ 24,18
ST 108
XL 55,448
M 7,30,0
s_mul_i32 s53, s51, 24
LL 17,60
LL 108,61
s_bitcmp1_b32 s51, 0
s_addc_u32 s53, s53, 0
s_lshl_b32 s53, s53, 11
s_bitcmp1_b64 s[90:91], s51
s_cselect_b64 s[44:45], s[84:85], s[82:83]
s_add_u32 s44, s44, s53
s_addc_u32 s45, s45, 0
s_add_u32 s51, s51, %[d]
s_cmp_eq_u32 s51, %[wrap]
s_cselect_b32 s51, %[rst], s51
M 7,31,1
LA 17
RA 232,1,23552
M 6,18,0
LC_ 108
XL 56,512
v_add_u32 v4, s39, v4
CF 174,248,249
CF 175,250,251
M 6,19,1
RA 236,1,24576
s_nop 1
v_add_u32 v1, s37, v0
GS 11
WL 7
XL 57,576
s_nop 5
v_cndmask_b32 v52, v52, v53, s[78:79]
v_cndmask_b32 v54, v54, v55, s[78:79]
M 6,20,2
WL 4
LO 105,16,105
LU 16,105
v_cndmask_b32 v52, v52, v54, s[80:81]
v_mul_f32 v52, 0x3b2aaaab, v52
M 6,21,3
RA 216,1,0
HS 24,16
v_med3_f32 v53, v52, v39, v37
M 7,32,2
LL 18,62
LL 109,63
LG 52,53
s_nop 0
C 52,52,52
M 7,33,3
WL 1
LA 18
XL 58,640
v_lshlrev_b32 v52, 16, v52
v_add_f32 v52, v36, v52
M 6,22,4
s_lshl_b32 s53, s52, 8
LC_ 109
v_and_b32 v22, 0xfffffcff, v1
C 52,52,52
v_lshlrev_b32 v52, 16, v52
M 6,23,5
s_add_u32 s48, %[alo], s53
RA 220,1,1024
v_sqrt_f32 v52, v52
s_nop 0
C 52,52,52
M 7,34,4
WV 20
LO 108,17,108
LU 17,108
s_addc_u32 s49, %[ahi], 0
s_add_u32 s52, s52, %[d]
s_cmp_eq_u32 s52, %[wrap]
s_cselect_b32 s52, %[rst], s52
XL 59,704
v_lshlrev_b32 v52, 16, v52
v_rcp_f32 v52, v52
M 7,35,5
s_barrier
RV 252,22,5120
HS 24,17
v_mul_f32 v52, v38, v52
CF 52,52,52
WL 0
Z 0,12,0
s_sub_u32 s39, 0, s39
RA 224,1,2048
LL 16,80
LL 106,81
ds_bpermute_b32 v13, v28, v52
M 0,13,1
s_mul_i32 s53, s50, 25600
LA 16
RA 228,1,3072
Z 1,24,0
s_add_u32 s40, %[wlo], s53
LC_ 106
RA 232,1,4096
M 1,25,1
s_addc_u32 s41, %[whi], 0
RA 236,1,6144
ds_read_b64 v[14:15], v5 offset:0
WL 7
M 1,26,2
LO 109,18,109
LU 18,109
s_mov_b32 m0, s38
WL 4
M 1,27,3
RA 216,1,7168
HS 24,18
M 0,14,2
DW 0
LL 17,82
LL 107,83
M 0,15,3
WL 3
LA 17
RA 220,1,8192
v_subrev_u32 v5, s39, v5
M 0,16,4
WL 4
LC_ 107
DW 1024
M 0,17,5
WL 4
RA 224,1,9216
v_add_f32 v14, v14, v15
s_nop 1
v_add_f32_dpp v14, v14, v14 quad_perm:[1,0,3,2] row_mask:0xf bank_mask:0xf
s_nop 1
v_add_f32_dpp v14, v14, v14 quad_perm:[2,3,0,1] row_mask:0xf bank_mask:0xf
s_nop 1
v_add_f32_dpp v14, v14, v14 row_half_mirror row_mask:0xf bank_mask:0xf
HX 24
XS 128
M 1,28,4
WV 21
LO 16,16,106
LU 106,16
v_cvt_f64_f32 v[14:15], v14
W1 24,0
XS 129
BW 48
XS 130
M 1,29,5
WL 4
RA 228,1,10240
HZ 25,106
s_mov_b64 exec, s[54:55]
SP
s_mov_b64 exec, -1
DW 2048
RV 40,8,6144
RV 44,8,6208
XS 131
AW 24,128
AW 25,129
M 1,30,0
s_add_u32 s40, s40, 3072
RV 48,8,6272
PK 106,16,104,56
PK 104,16,104,97
LL 18,84
LL 110,85
AW 26,130
AW 27,131
M 1,31,1
s_addc_u32 s41, s41, 0
LA 18
RA 232,1,11264
s_add_u32 m0, s38, 3072
BR 48
XS 132
XS 133
XS 134
BW 49
M 0,18,0
LC_ 110
RV 252,22,5392
BR 49
XS 135
AW 28,132
M 0,19,1
WL 10
RA 236,1,12288
AW 29,133
AW 30,134
AW 31,135
WL 5
LZ 40,48
WV 22
BW 50
M 0,20,2
DW 0
RV 40,8,6336
LO 17,17,107
LU 107,17
v_min_u32 v35, v35, v13
XS 136
XS 137
M 0,21,3
WL 3
RA 216,1,13312
BR 50
HS 25,107
XS 138
XS 139
LS 44,49
WV 22
BW 51
M 1,32,2
WL 5
DW 1024
PK 107,17,105,56
PK 105,17,105,97
LL 16,86
LL 111,87
AW 32,136
AW 33,137
AW 34,138
M 1,33,3
s_add_u32 s42, s44, s60
LA 16
RA 220,1,14336
RV 44,8,6400
BR 51
AW 35,139
XS 140
M 0,22,4
s_addc_u32 s43, s45, 0
LC_ 111
DW 2048
XS 141
XS 142
XS 143
M 0,23,5
WL 6
RA 224,1,15360
AW 36,140
AW 37,141
AW 38,142
LS 48,50
WV 23
BW 52
RV 48,8,6464
M 1,34,4
LO 18,18,110
LU 110,18
ST 104
BR 52
AW 39,143
XS 144
M 1,35,5
WL 5
RA 228,1,16384
DQ 3072
HS 25,110
XS 145
XS 146
XS 147
LS 40,51
WV 15
BW 53
Z 6,12,0
s_add_u32 s50, s50, %[d]
RV 40,8,6528
PK 110,18,108,56
PK 108,18,108,97
LL 17,56
LL 104,57
AW 40,144
AW 41,145
M 6,13,1
s_cmp_eq_u32 s50, %[wrap]
LA 17
BR 53
PO 110
AW 42,146
AW 43,147
XS 148
WL 5
LS 44,52
s_cselect_b32 s50, %[rst], s50
BW 54
Z 7,24,0
s_add_u32 s38, s38, 32768
LC_ 104
RA 232,1,17408
XS 149
XS 150
XS 151
M 7,25,1
s_cmp_eq_u32 s38, s77
RA 236,1,18432
RV 44,8,6592
BR 54
AW 44,148
AW 45,149
M 7,26,2
WL 6
LO 16,16,111
LU 111,16
s_cselect_b32 s38, s76, s38
)"
R"(AW 46,150
AW 47,151
XS 152
M 7,27,3
s_add_u32 s37, s37, 32768
RA 216,1,19456
HS 25,111
XS 153
XS 154
LS 48,53
s_cmp_eq_u32 s37, s75
WV 13
BW 55
RV 48,8,6656
M 6,14,2
s_cselect_b32 s37, s74, s37
BR 55
PK 111,16,109,56
PK 109,16,109,97
LL 18,58
LL 105,59
XS 155
AW 48,152
AW 49,153
M 6,15,3
s_add_u32 s53, %[xo0], 393216
LA 18
PO 111
s_add_u32 s46, %[xlo], s53
s_addc_u32 s47, %[xhi], 0
s_bitcmp1_b64 s[90:91], 48
s_cselect_b32 s58, 16, 0
s_lshr_b32 s58, 0xfe987610, s58
v_lshrrev_b32 v3, 6, v0
v_and_b32 v3, 12, v3
v_bfe_u32 v3, s58, v3, 4
v_mad_u32_u24 v3, v3, v32, v21
AW 50,154
AW 51,155
WL 5
LS 40,54
s_add_u32 s42, s44, s61
WV 12
BW 56
M 6,16,4
s_addc_u32 s43, s45, 0
LC_ 105
RA 220,1,20480
RV 40,8,6720
XS 156
XS 157
XS 158
M 6,17,5
WL 5
RA 224,1,21504
BR 56
XL 60,0
HX 25
XS 159
AW 52,156
AW 53,157
LS 44,55
WV 12
BW 57
M 7,28,4
LO 104,17,104
LU 17,104
W1 25,1024
AW 54,158
AW 55,159
M 7,29,5
WL 5
RA 228,1,22528
HZ 24,17
ST 108
RV 44,8,6784
BR 57
XS 160
XS 161
XS 162
M 7,30,0
s_mul_i32 s53, s51, 24
LL 16,60
LL 108,61
s_bitcmp1_b32 s51, 0
s_addc_u32 s53, s53, 0
s_lshl_b32 s53, s53, 11
s_bitcmp1_b64 s[90:91], s51
s_cselect_b64 s[44:45], s[84:85], s[82:83]
s_add_u32 s44, s44, s53
s_addc_u32 s45, s45, 0
s_add_u32 s51, s51, %[d]
s_cmp_eq_u32 s51, %[wrap]
s_cselect_b32 s51, %[rst], s51
XL 61,64
XS 163
AW 56,160
M 7,31,1
WL 5
LA 16
RA 232,1,23552
AW 57,161
AW 58,162
AW 59,163
LS 48,56
WV 13
BW 58
RV 48,8,6848
M 6,18,0
LC_ 108
XL 62,128
v_add_u32 v4, s39, v4
RA 240,7,0
XS 164
XS 165
XS 166
M 6,19,1
WL 6
RA 236,1,24576
BR 58
XS 167
AW 60,164
LS 40,57
WV 13
BW 59
M 6,20,2
LO 105,18,105
LU 18,105
AW 61,165
AW 62,166
AW 63,167
M 6,21,3
XL 63,192
RA 244,7,256
BR 59
HS 24,18
XS 168
XS 169
M 7,32,2
LL 17,62
LL 109,63
v_add_u32 v1, s37, v0
XS 170
XS 171
AW 64,168
M 7,33,3
WL 0
LA 17
RA 216,1,0
XL 64,256
AW 65,169
AW 66,170
AW 67,171
LS 44,58
s_lshl_b32 s53, s52, 8
RA 248,7,512
XS 172
LS 48,59
s_add_u32 s48, %[alo], s53
RA 252,7,768
XS 173
M 6,22,4
s_addc_u32 s49, %[ahi], 0
LC_ 109
v_and_b32 v22, 0xfffffcff, v1
M 6,23,5
s_add_u32 s52, s52, %[d]
RA 220,1,1024
XS 174
XS 175
AW 68,172
M 7,34,4
LO 108,16,108
LU 16,108
s_cmp_eq_u32 s52, %[wrap]
s_cselect_b32 s52, %[rst], s52
XL 65,320
AW 69,173
AW 70,174
M 7,35,5
s_barrier
RV 252,22,5120
HS 24,16
AW 71,175
v_mul_f32 v36, 0xbb2aaaab, v36
v_mul_f32 v37, 0xbb2aaaab, v37
v_mul_f32 v38, 0xbb2aaaab, v38
v_mul_f32 v39, 0xbb2aaaab, v39
WL 0
Z 0,12,0
s_sub_u32 s39, 0, s39
RA 224,1,2048
LL 18,80
LL 106,81
M 0,13,1
s_mul_i32 s53, s50, 25600
LA 18
RA 228,1,3072
LK 40,240,48,0
LK 44,242,48,2
s_add_u32 s40, %[wlo], s53
RA 240,7,1024
LK 48,244,49,0
s_nop 0
CF 128,40,41
LK 248,246,49,2
s_addc_u32 s41, %[whi], 0
RA 244,7,1280
CF 129,42,43
CF 130,44,45
CF 131,46,47
LK 40,248,50,0
CF 132,48,49
LK 44,250,50,2
s_mov_b32 m0, s38
RA 248,7,1536
CF 133,50,51
GZ 0
CF 134,248,249
CF 135,250,251
LK 48,252,51,0
CF 136,40,41
LK 248,254,51,2
RA 252,7,1792
CF 137,42,43
GS 1
CF 138,44,45
CF 139,46,47
Z 1,24,0
LC_ 106
RA 232,1,4096
M 1,25,1
WL 8
RA 236,1,6144
ds_read_b64 v[14:15], v5 offset:0
M 1,26,2
WL 8
LO 109,17,109
LU 17,109
M 1,27,3
WL 7
RA 216,1,7168
HS 24,17
LK 40,240,52,0
CF 140,48,49
LK 44,242,52,2
RA 240,7,2048
CF 141,50,51
GS 2
WL 8
CF 142,248,249
CF 143,250,251
LK 48,244,53,0
CF 144,40,41
LK 248,246,53,2
RA 244,7,2304
CF 145,42,43
GS 3
WL 8
CF 146,44,45
CF 147,46,47
LK 40,248,54,0
CF 148,48,49
LK 44,250,54,2
RA 248,7,2560
CF 149,50,51
GS 4
WL 8
CF 150,248,249
CF 151,250,251
LK 48,252,55,0
CF 152,40,41
LK 248,254,55,2
RA 252,7,2816
CF 153,42,43
GS 5
CF 154,44,45
CF 155,46,47
M 0,14,2
DW 0
LL 16,82
LL 107,83
M 0,15,3
WL 3
LA 16
RA 220,1,8192
v_subrev_u32 v5, s39, v5
LK 40,240,56,0
CF 156,48,49
LK 44,242,56,2
CF 157,50,51
HX 24
GS 6
CF 158,248,249
CF 159,250,251
M 0,16,4
LC_ 107
DW 1024
M 0,17,5
WL 5
RA 224,1,9216
v_add_f32 v14, v14, v15
s_nop 1
v_add_f32_dpp v14, v14, v14 quad_perm:[1,0,3,2] row_mask:0xf bank_mask:0xf
s_nop 1
v_add_f32_dpp v14, v14, v14 quad_perm:[2,3,0,1] row_mask:0xf bank_mask:0xf
s_nop 1
v_add_f32_dpp v14, v14, v14 row_half_mirror row_mask:0xf bank_mask:0xf
LK 48,244,57,0
CF 160,40,41
LK 248,246,57,2
CF 161,42,43
v_cvt_f64_f32 v[14:15], v14
GS 7
CF 162,44,45
CF 163,46,47
M 1,28,4
LO 18,18,106
LU 106,18
W1 24,0
M 1,29,5
WL 6
RA 228,1,10240
HZ 25,106
s_mov_b64 exec, s[54:55]
SP
s_mov_b64 exec, -1
DW 2048
LK 40,248,58,0
CF 164,48,49
LK 44,250,58,2
CF 165,50,51
GS 8
WL 4
CF 166,248,249
CF 167,250,251
M 1,30,0
s_add_u32 s40, s40, 3072
PK 106,18,104,56
PK 104,18,104,97
LL 17,84
LL 110,85
M 1,31,1
s_addc_u32 s41, s41, 0
LA 17
RA 232,1,11264
LK 48,252,59,0
CF 168,40,41
LK 248,254,59,2
s_add_u32 m0, s38, 3072
CF 169,42,43
GS 9
CF 170,44,45
CF 171,46,47
M 0,18,0
LC_ 110
RV 252,22,5392
M 0,19,1
RA 236,1,12288
RV 36,8,6912
GS 10
WL 8
CF 172,48,49
CF 173,50,51
M 0,20,2
WL 6
LO 16,16,107
LU 107,16
M 0,21,3
RA 216,1,13312
DW 0
HS 25,107
M 1,32,2
PK 107,16,105,56
PK 105,16,105,97
LL 18,86
LL 111,87
M 1,33,3
WL 2
LA 18
RA 220,1,14336
DW 1024
M 0,22,4
s_add_u32 s42, s44, s60
LC_ 111
s_addc_u32 s43, s45, 0
M 0,23,5
RA 224,1,15360
CF 174,248,249
CF 175,250,251
s_nop 1
GS 11
ST 104
M 1,34,4
DW 2048
LO 17,17,110
LU 110,17
s_nop 2
v_cndmask_b32 v52, v52, v53, s[78:79]
v_cndmask_b32 v54, v54, v55, s[78:79]
)"
R"(M 1,35,5
WL 4
RA 228,1,16384
HS 25,110
v_cndmask_b32 v52, v52, v54, s[80:81]
Z 6,12,0
WL 3
PK 110,17,108,56
PK 108,17,108,97
LL 16,56
LL 104,57
v_mul_f32 v52, 0x3b2aaaab, v52
v_med3_f32 v53, v52, v39, v37
M 6,13,1
WL 1
LA 16
DQ 3072
PO 110
LG 52,53
s_nop 0
C 52,52,52
Z 7,24,0
s_add_u32 s50, s50, %[d]
LC_ 104
RA 232,1,17408
v_lshlrev_b32 v52, 16, v52
M 7,25,1
s_cmp_eq_u32 s50, %[wrap]
RA 236,1,18432
s_cselect_b32 s50, %[rst], s50
v_add_f32 v52, v36, v52
C 52,52,52
M 7,26,2
WL 4
LO 18,18,111
LU 111,18
v_lshlrev_b32 v52, 16, v52
M 7,27,3
s_add_u32 s38, s38, 32768
RA 216,1,19456
HS 25,111
v_sqrt_f32 v52, v52
s_nop 0
C 52,52,52
M 6,14,2
s_cmp_eq_u32 s38, s77
PK 111,18,109,56
PK 109,18,109,97
LL 17,58
LL 105,59
s_cselect_b32 s38, s76, s38
v_lshlrev_b32 v52, 16, v52
M 6,15,3
s_add_u32 s37, s37, 32768
LA 17
PO 111
s_cmp_eq_u32 s37, s75
s_cselect_b32 s37, s74, s37
v_rcp_f32 v52, v52
s_nop 0
v_mul_f32 v52, v38, v52
WL 3
M 6,16,4
WL 2
LC_ 105
RA 220,1,20480
XL 66,384
CF 52,52,52
ds_bpermute_b32 v13, v28, v52
M 6,17,5
s_add_u32 s42, s44, s61
RA 224,1,21504
HX 25
s_addc_u32 s43, s45, 0
M 7,28,4
LO 104,16,104
LU 16,104
W1 25,1024
M 7,29,5
WL 3
RA 228,1,22528
HZ 24,16
ST 108
XL 67,448
M 7,30,0
s_mul_i32 s53, s51, 24
LL 18,60
LL 108,61
s_bitcmp1_b32 s51, 0
s_addc_u32 s53, s53, 0
s_lshl_b32 s53, s53, 11
s_bitcmp1_b64 s[90:91], s51
s_cselect_b64 s[44:45], s[84:85], s[82:83]
s_add_u32 s44, s44, s53
s_addc_u32 s45, s45, 0
s_add_u32 s51, s51, %[d]
s_cmp_eq_u32 s51, %[wrap]
s_cselect_b32 s51, %[rst], s51
M 7,31,1
LA 18
RA 232,1,23552
M 6,18,0
LC_ 108
XL 68,512
v_add_u32 v4, s39, v4
M 6,19,1
WL 5
RA 236,1,24576
s_nop 1
v_add_u32 v1, s37, v0
M 6,20,2
WL 4
XL 69,576
LO 105,17,105
LU 17,105
M 6,21,3
RA 216,1,0
HS 24,17
M 7,32,2
LL 16,62
LL 109,63
XS 128
M 7,33,3
WL 1
LA 16
XL 70,640
XS 129
XS 130
M 6,22,4
s_lshl_b32 s53, s52, 8
LC_ 109
v_and_b32 v22, 0xfffffcff, v1
XS 131
AW 72,128
M 6,23,5
s_add_u32 s48, %[alo], s53
RA 220,1,1024
AW 73,129
AW 74,130
M 7,34,4
WV 22
LO 108,18,108
LU 18,108
s_addc_u32 s49, %[ahi], 0
s_add_u32 s52, s52, %[d]
s_cmp_eq_u32 s52, %[wrap]
s_cselect_b32 s52, %[rst], s52
XL 71,704
AW 75,131
XS 132
M 7,35,5
s_barrier
RV 252,22,5120
HS 24,18
XS 133
XS 134
XS 135
WL 0
Z 0,12,0
s_sub_u32 s39, 0, s39
LL 17,80
LL 106,81
AW 76,132
AW 77,133
AW 78,134
M 0,13,1
s_mul_i32 s53, s50, 25600
LA 17
RA 224,1,2048
AW 79,135
v_min_u32 v35, v35, v13
XS 136
Z 1,24,0
s_add_u32 s40, %[wlo], s53
LC_ 106
RA 228,1,3072
XS 137
XS 138
XS 139
M 1,25,1
s_addc_u32 s41, %[whi], 0
RA 232,1,4096
AW 80,136
AW 81,137
AW 82,138
Z 2,36,0
LO 109,16,109
LU 16,109
AW 83,139
XS 140
XS 141
M 2,37,1
s_mov_b32 m0, s38
ds_read_b64 v[14:15], v5 offset:0
HS 24,16
XS 142
XS 143
AW 84,140
Z 3,48,0
WL 2
LL 18,82
LL 107,83
AW 85,141
AW 86,142
AW 87,143
M 3,49,1
LA 18
RA 236,1,6144
M 3,50,2
LC_ 107
DW 0
M 3,51,3
v_subrev_u32 v5, s39, v5
HX 24
M 2,38,2
LO 17,17,106
LU 106,17
M 2,39,3
RA 216,1,7168
HZ 25,106
XS 144
M 1,26,2
PK 106,17,104,56
PK 104,17,104,97
LL 16,84
LL 110,85
XS 145
M 1,27,3
LA 16
W1 24,0
XS 146
XS 147
AW 88,144
M 0,14,2
WL 4
LC_ 110
DW 1024
AW 89,145
AW 90,146
AW 91,147
M 0,15,3
RA 220,1,8192
v_add_f32 v14, v14, v15
XS 148
XS 149
XS 150
M 0,16,4
WL 5
LO 18,18,107
LU 107,18
XS 151
AW 92,148
AW 93,149
M 0,17,5
DW 2048
HS 25,107
v_add_f32_dpp v14, v14, v14 quad_perm:[1,0,3,2] row_mask:0xf bank_mask:0xf
AW 94,150
AW 95,151
XS 152
M 1,28,4
s_add_u32 s42, s44, s60
PK 107,18,105,56
PK 105,18,105,97
LL 17,86
LL 111,87
XS 153
XS 154
XS 155
M 1,29,5
s_addc_u32 s43, s45, 0
LA 17
RA 224,1,9216
v_add_f32_dpp v14, v14, v14 quad_perm:[2,3,0,1] row_mask:0xf bank_mask:0xf
AW 96,152
AW 97,153
AW 98,154
M 2,40,4
LC_ 111
ST 104
AW 99,155
XS 156
XS 157
M 2,41,5
v_add_f32_dpp v14, v14, v14 row_half_mirror row_mask:0xf bank_mask:0xf
XS 158
XS 159
AW 100,156
M 3,52,4
WL 3
LO 16,16,110
LU 110,16
AW 101,157
AW 102,158
AW 103,159
M 3,53,5
s_add_u32 s40, s40, 3072
RA 228,1,10240
HS 25,110
v_cvt_f64_f32 v[14:15], v14
M 3,54,0
s_addc_u32 s41, s41, 0
PK 110,16,108,56
PK 108,16,108,97
M 3,55,1
s_add_u32 m0, s38, 3072
s_mov_b64 exec, s[54:55]
SP
s_mov_b64 exec, -1
PO 110
M 2,42,0
XS 160
XS 161
M 2,43,1
RA 232,1,11264
XS 162
M 1,30,0
WL 2
LO 17,17,111
LU 111,17
M 1,31,1
RV 252,22,5392
HS 25,111
M 0,18,0
WL 2
PK 111,17,109,56
PK 109,17,109,97
XS 163
AW 104,160
M 0,19,1
RA 236,1,12288
PO 111
AW 105,161
AW 106,162
M 0,20,2
DW 0
HX 25
AW 107,163
XS 164
M 0,21,3
s_add_u32 s42, s44, s61
W1 25,1024
XS 165
XS 166
M 1,32,2
s_addc_u32 s43, s45, 0
ST 108
v_add_u32 v4, s39, v4
XS 167
M 1,33,3
RA 216,1,13312
AW 108,164
AW 109,165
M 2,44,2
DW 1024
AW 110,166
AW 111,167
M 2,45,3
XS 168
XS 169
M 3,56,2
WL 2
XS 170
XS 171
M 3,57,3
RA 220,1,14336
AW 112,168
AW 113,169
M 0,22,4
DW 2048
AW 114,170
AW 115,171
M 0,23,5
XS 172
XS 173
M 1,34,4
XS 174
XS 175
M 1,35,5
RA 224,1,15360
AW 116,172
AW 117,173
M 2,46,4
LL 18,56
LL 104,57
AW 118,174
AW 119,175
M 2,47,5
LA 18
DQ 3072
M 3,58,4
WL 2
LC_ 104
M 3,59,5
RA 228,1,16384
Z 6,12,0
WV 32
BW 60
M 6,13,1
s_add_u32 s50, s50, %[d]
)"
R"(RV 40,8,6144
RV 44,8,6208
Z 7,24,0
s_cmp_eq_u32 s50, %[wrap]
LL 16,58
LL 105,59
M 7,25,1
s_cselect_b32 s50, %[rst], s50
LA 16
RA 232,1,17408
Z 8,36,0
LC_ 105
RV 48,8,6272
M 8,37,1
BR 60
Z 9,48,0
WL 8
LO 104,18,104
LU 18,104
M 9,49,1
s_add_u32 s38, s38, 32768
RA 236,1,18432
HZ 24,18
M 9,50,2
s_cmp_eq_u32 s38, s77
LL 17,60
LL 108,61
M 9,51,3
s_cselect_b32 s38, s76, s38
LA 17
M 8,38,2
s_add_u32 s37, s37, 32768
LC_ 108
M 8,39,3
s_cmp_eq_u32 s37, s75
RA 216,1,19456
M 7,26,2
WL 6
LO 105,16,105
LU 16,105
M 7,27,3
s_cselect_b32 s37, s74, s37
HS 24,16
M 6,14,2
WL 3
LL 18,62
LL 109,63
M 6,15,3
s_add_u32 s53, %[xo0], 393216
LA 18
RA 220,1,20480
M 6,16,4
s_add_u32 s46, %[xlo], s53
LC_ 109
M 6,17,5
s_addc_u32 s47, %[xhi], 0
WV 30
BW 61
M 7,28,4
WL 5
LO 108,17,108
LU 17,108
s_bitcmp1_b64 s[90:91], 48
s_cselect_b32 s58, 16, 0
s_lshr_b32 s58, 0xda43cb52, s58
v_lshrrev_b32 v3, 4, v0
v_and_b32 v3, 12, v3
v_bfe_u32 v3, s58, v3, 4
BR 61
v_bfe_u32 v11, v0, 4, 2
v_lshl_add_u32 v3, v3, 2, v11
v_mul_u32_u24 v3, 0x300, v3
v_lshrrev_b32 v11, 8, v0
v_lshl_add_u32 v3, v11, 4, v3
M 7,29,5
RA 224,1,21504
HS 24,17
LZ 40,60
WV 29
BW 62
M 8,40,4
LL 16,64
LL 112,65
M 8,41,5
LA 16
XL 72,0
M 9,52,4
WL 6
LC_ 112
RV 40,8,6336
M 9,53,5
s_mul_i32 s53, s51, 24
RA 228,1,22528
BR 62
M 9,54,0
WL 7
LO 109,18,109
LU 18,109
M 9,55,1
s_bitcmp1_b32 s51, 0
HS 24,18
s_addc_u32 s53, s53, 0
s_lshl_b32 s53, s53, 11
s_bitcmp1_b64 s[90:91], s51
s_cselect_b64 s[44:45], s[84:85], s[82:83]
s_add_u32 s44, s44, s53
s_addc_u32 s45, s45, 0
s_add_u32 s51, s51, %[d]
s_cmp_eq_u32 s51, %[wrap]
s_cselect_b32 s51, %[rst], s51
LS 44,61
WV 29
BW 63
M 8,42,0
LL 17,66
LL 113,67
M 8,43,1
WV 28
LA 17
RA 232,1,23552
RV 44,8,6400
M 7,30,0
WL 4
LC_ 113
XL 73,64
M 7,31,1
s_lshl_b32 s53, s52, 8
BR 63
LS 48,62
s_add_u32 s48, %[alo], s53
BW 64
M 6,18,0
LO 112,16,112
LU 16,112
M 6,19,1
s_addc_u32 s49, %[ahi], 0
RA 236,1,24576
HS 24,16
M 6,20,2
s_add_u32 s52, s52, %[d]
LL 18,68
LL 116,69
s_cmp_eq_u32 s52, %[wrap]
s_cselect_b32 s52, %[rst], s52
M 6,21,3
LA 18
XL 74,128
RV 48,8,6464
M 7,32,2
LC_ 116
XL 75,192
BR 64
M 7,33,3
WL 6
v_add_u32 v1, s37, v0
LS 40,63
WV 30
BW 65
M 8,44,2
LO 113,17,113
LU 17,113
M 8,45,3
RA 216,1,0
HS 24,17
M 9,56,2
WL 2
RV 40,8,6528
LL 16,70
LL 117,71
M 9,57,3
WV 21
LA 16
XL 76,256
BR 65
M 6,22,4
s_barrier
LC_ 117
v_and_b32 v22, 0xfffffcff, v1
M 6,23,5
s_sub_u32 s39, 0, s39
RA 220,1,1024
LS 44,64
WV 21
BW 66
M 7,34,4
LO 116,18,116
LU 18,116
M 7,35,5
RV 252,22,5120
RV 44,8,6592
HS 24,18
M 8,46,4
LL 17,80
LL 106,81
M 8,47,5
LA 17
RA 224,1,2048
BR 66
M 9,58,4
WL 6
LC_ 106
ds_read_b64 v[14:15], v5 offset:0
M 9,59,5
WL 6
RA 228,1,3072
v_subrev_u32 v5, s39, v5
Z 0,12,0
LO 117,16,117
LU 16,117
M 0,13,1
WV 18
XL 77,320
HS 24,16
LS 48,65
s_mul_i32 s53, s50, 25600
BW 67
RV 48,8,6656
Z 1,24,0
s_add_u32 s40, %[wlo], s53
BR 67
LL 18,82
LL 107,83
M 1,25,1
s_addc_u32 s41, %[whi], 0
LA 18
RA 232,1,4096
WL 8
LS 40,66
s_mov_b32 m0, s38
BW 68
Z 2,36,0
LC_ 107
RV 40,8,6720
M 2,37,1
BR 68
HX 24
Z 3,48,0
WL 9
LO 17,17,106
LU 106,17
M 3,49,1
RA 236,1,6144
HZ 25,106
M 3,50,2
LL 16,84
LL 110,85
M 3,51,3
WL 7
LA 16
PK 106,17,104,56
LS 44,67
WV 18
BW 69
M 2,38,2
LC_ 110
PK 104,17,104,97
W1 24,0
M 2,39,3
WV 17
RA 216,1,7168
RV 44,8,6784
M 1,26,2
WL 9
LO 18,18,107
LU 107,18
M 1,27,3
WL 7
DW 0
BR 69
HS 25,107
LS 48,68
s_add_u32 s42, s44, s60
BW 70
M 0,14,2
WL 8
LL 17,86
LL 111,87
M 0,15,3
s_addc_u32 s43, s45, 0
LA 17
PK 107,18,105,56
v_add_f32 v14, v14, v15
s_nop 1
v_add_f32_dpp v14, v14, v14 quad_perm:[1,0,3,2] row_mask:0xf bank_mask:0xf
s_nop 1
v_add_f32_dpp v14, v14, v14 quad_perm:[2,3,0,1] row_mask:0xf bank_mask:0xf
M 0,16,4
LC_ 111
PK 105,18,105,97
v_add_f32_dpp v14, v14, v14 row_half_mirror row_mask:0xf bank_mask:0xf
M 0,17,5
RA 220,1,8192
RV 48,8,6848
M 1,28,4
WL 9
RA 240,7,0
LO 16,16,110
LU 110,16
v_cvt_f64_f32 v[14:15], v14
M 1,29,5
RA 224,1,9216
DW 1024
BR 70
HS 25,110
M 2,40,4
LL 18,88
LL 114,89
s_mov_b64 exec, s[54:55]
SP
s_mov_b64 exec, -1
M 2,41,5
WL 8
LA 18
PK 110,16,108,56
LS 40,69
WV 19
BW 71
M 3,52,4
WL 6
LC_ 114
PK 108,16,108,97
M 3,53,5
RA 228,1,10240
RA 244,7,256
PO 110
M 3,54,0
WL 5
LO 17,17,111
LU 111,17
ST 104
DW 2048
M 3,55,1
s_add_u32 s40, s40, 3072
BR 71
HS 25,111
M 2,42,0
s_addc_u32 s41, s41, 0
LL 16,90
LL 115,91
M 2,43,1
s_add_u32 m0, s38, 3072
LA 16
PK 111,17,109,56
LS 44,70
s_add_u32 s42, s44, s61
RA 248,7,512
M 1,30,0
s_addc_u32 s43, s45, 0
LC_ 115
PK 109,17,109,97
M 1,31,1
WL 3
RA 232,1,11264
PO 111
LS 48,71
RA 252,7,768
M 0,18,0
LO 18,18,114
LU 114,18
M 0,19,1
RV 252,22,5392
HS 25,114
s_nop 0
v_mul_f32 v36, 0xbb2aaaab, v36
M 0,20,2
LL 17,92
LL 118,93
v_mul_f32 v37, 0xbb2aaaab, v37
v_mul_f32 v38, 0xbb2aaaab, v38
M 0,21,3
LA 17
PK 114,18,112,56
v_mul_f32 v39, 0xbb2aaaab, v39
s_nop 1
LK 40,240,60,0
LK 44,242,60,2
RA 240,7,1024
LK 48,244,61,0
s_nop 1
CF 128,40,41
LK 248,246,61,2
RA 244,7,1280
CF 129,42,43
M 1,32,2
LC_ 118
PK 112,18,112,97
M 1,33,3
RA 236,1,12288
CF 130,44,45
M 2,44,2
WL 8
DW 0
LO 16,16,115
LU 115,16
M 2,45,3
RA 216,1,13312
HS 25,115
ST 108
CF 131,46,47
LK 40,248,62,0
)"
R"(CF 132,48,49
LK 44,250,62,2
RA 248,7,1536
CF 133,50,51
GZ 0
WL 8
CF 134,248,249
CF 135,250,251
LK 48,252,63,0
CF 136,40,41
LK 248,254,63,2
RA 252,7,1792
CF 137,42,43
GS 1
WL 6
CF 138,44,45
CF 139,46,47
LK 40,240,64,0
CF 140,48,49
LK 44,242,64,2
RA 240,7,2048
CF 141,50,51
GS 2
CF 142,248,249
CF 143,250,251
M 3,56,2
WL 4
LL 18,94
LL 119,95
M 3,57,3
LA 18
PK 115,16,113,56
LK 48,244,65,0
CF 144,40,41
LK 248,246,65,2
RA 244,7,2304
CF 145,42,43
GS 3
CF 146,44,45
CF 147,46,47
M 0,22,4
LC_ 119
PK 113,16,113,97
M 0,23,5
RA 220,1,14336
DW 1024
M 1,34,4
WL 6
LO 17,17,118
LU 118,17
M 1,35,5
s_add_u32 s42, s44, s62
RA 224,1,15360
HS 25,118
s_addc_u32 s43, s45, 0
LK 40,248,66,0
CF 148,48,49
LK 44,250,66,2
RA 248,7,2560
CF 149,50,51
GS 4
CF 150,248,249
CF 151,250,251
M 2,46,4
LL 16,56
LL 104,57
M 2,47,5
WL 7
LA 16
PK 118,17,116,56
LK 48,252,67,0
CF 152,40,41
LK 248,254,67,2
RA 252,7,2816
CF 153,42,43
GS 5
CF 154,44,45
CF 155,46,47
M 3,58,4
WL 4
LC_ 104
PK 116,17,116,97
M 3,59,5
RA 228,1,16384
DW 2048
PO 118
Z 6,12,0
LO 18,18,119
LU 119,18
ST 112
M 6,13,1
DQ 3072
HS 25,119
LK 40,240,68,0
CF 156,48,49
LK 44,242,68,2
s_add_u32 s42, s44, s63
CF 157,50,51
GS 6
s_addc_u32 s43, s45, 0
CF 158,248,249
CF 159,250,251
Z 7,24,0
LL 17,58
LL 105,59
M 7,25,1
LA 17
PK 119,18,117,56
Z 8,36,0
s_add_u32 s50, s50, %[d]
LC_ 105
PK 117,18,117,97
M 8,37,1
s_cmp_eq_u32 s50, %[wrap]
RA 232,1,17408
PO 119
LK 48,244,69,0
CF 160,40,41
LK 248,246,69,2
s_cselect_b32 s50, %[rst], s50
CF 161,42,43
HX 25
GS 7
CF 162,44,45
CF 163,46,47
Z 9,48,0
WL 3
LO 104,16,104
LU 16,104
M 9,49,1
RA 236,1,18432
HZ 24,16
M 9,50,2
LL 18,60
LL 108,61
M 9,51,3
LA 18
W1 25,1024
LK 40,248,70,0
CF 164,48,49
LK 44,250,70,2
CF 165,50,51
GS 8
CF 166,248,249
CF 167,250,251
M 8,38,2
LC_ 108
ST 116
M 8,39,3
s_add_u32 s37, s37, 32768
RA 216,1,19456
v_add_u32 v4, s39, v4
LK 48,252,71,0
CF 168,40,41
LK 248,254,71,2
s_cmp_eq_u32 s37, s75
CF 169,42,43
GS 9
s_cselect_b32 s37, s74, s37
CF 170,44,45
CF 171,46,47
M 7,26,2
WL 6
LO 105,17,105
LU 17,105
M 7,27,3
s_add_u32 s38, s38, 32768
RV 36,8,6912
HS 24,17
M 6,14,2
WL 5
LL 16,62
LL 109,63
M 6,15,3
s_cmp_eq_u32 s38, s77
LA 16
RA 220,1,20480
GS 10
s_cselect_b32 s38, s76, s38
CF 172,48,49
M 6,16,4
LC_ 109
XL 78,384
M 6,17,5
CF 173,50,51
CF 174,248,249
M 7,28,4
WL 5
LO 108,18,108
LU 18,108
M 7,29,5
s_mul_i32 s53, s51, 24
RA 224,1,21504
HS 24,18
M 8,40,4
s_bitcmp1_b32 s51, 0
LL 17,64
LL 112,65
M 8,41,5
s_addc_u32 s53, s53, 0
LA 17
CF 175,250,251
M 9,52,4
WL 3
LC_ 112
M 9,53,5
s_lshl_b32 s53, s53, 11
RA 228,1,22528
GS 11
s_bitcmp1_b64 s[90:91], s51
M 9,54,0
WL 4
LO 109,16,109
LU 16,109
M 9,55,1
s_cselect_b64 s[44:45], s[84:85], s[82:83]
HS 24,16
s_add_u32 s44, s44, s53
s_addc_u32 s45, s45, 0
s_add_u32 s51, s51, %[d]
s_cmp_eq_u32 s51, %[wrap]
s_cselect_b32 s51, %[rst], s51
M 8,42,0
LL 18,66
LL 113,67
M 8,43,1
LA 18
RA 232,1,23552
XL 79,448
M 7,30,0
s_lshl_b32 s53, s52, 8
LC_ 113
v_cndmask_b32 v52, v52, v53, s[78:79]
M 7,31,1
s_add_u32 s48, %[alo], s53
v_cndmask_b32 v54, v54, v55, s[78:79]
v_cndmask_b32 v52, v52, v54, s[80:81]
M 6,18,0
WL 3
LO 112,17,112
LU 17,112
M 6,19,1
s_addc_u32 s49, %[ahi], 0
RA 236,1,24576
HS 24,17
M 6,20,2
s_add_u32 s52, s52, %[d]
LL 16,68
LL 116,69
s_cmp_eq_u32 s52, %[wrap]
s_cselect_b32 s52, %[rst], s52
v_mul_f32 v52, 0x3b2aaaab, v52
M 6,21,3
LA 16
XL 80,512
M 7,32,2
LC_ 116
XL 81,576
v_med3_f32 v53, v52, v39, v37
M 7,33,3
v_add_u32 v1, s37, v0
LG 52,53
M 8,44,2
WL 3
LO 113,18,113
LU 18,113
C 52,52,52
M 8,45,3
RA 216,1,0
HS 24,18
v_lshlrev_b32 v52, 16, v52
M 9,56,2
WL 3
LL 17,70
LL 117,71
v_add_f32 v52, v36, v52
M 9,57,3
WV 22
LA 17
XL 82,640
C 52,52,52
M 6,22,4
s_barrier
LC_ 117
v_and_b32 v22, 0xfffffcff, v1
v_lshlrev_b32 v52, 16, v52
M 6,23,5
s_sub_u32 s39, 0, s39
RA 220,1,1024
v_sqrt_f32 v52, v52
M 7,34,4
WL 4
LO 116,16,116
LU 16,116
C 52,52,52
M 7,35,5
RV 252,22,5120
HS 24,16
v_lshlrev_b32 v52, 16, v52
M 8,46,4
LL 18,80
LL 106,81
v_rcp_f32 v52, v52
M 8,47,5
LA 18
RA 224,1,2048
v_mul_f32 v52, v38, v52
M 9,58,4
WL 3
LC_ 106
ds_read_b64 v[14:15], v5 offset:0
CF 52,52,52
M 9,59,5
WL 4
RA 228,1,3072
ds_bpermute_b32 v13, v28, v52
Z 0,12,0
LO 117,17,117
LU 17,117
M 0,13,1
XL 83,704
HS 24,17
Z 1,24,0
s_mul_i32 s53, s50, 25600
LL 16,82
LL 107,83
M 1,25,1
s_add_u32 s40, %[wlo], s53
LA 16
RA 232,1,4096
Z 2,36,0
s_addc_u32 s41, %[whi], 0
LC_ 107
v_subrev_u32 v5, s39, v5
M 2,37,1
s_mov_b32 m0, s38
HX 24
Z 3,48,0
WL 4
LO 18,18,106
LU 106,18
M 3,49,1
RA 236,1,6144
HZ 25,106
M 3,50,2
LL 17,84
LL 110,85
M 3,51,3
WL 4
LA 17
PK 106,18,104,56
XS 128
M 2,38,2
LC_ 110
PK 104,18,104,97
W1 24,0
XS 129
M 2,39,3
RA 216,1,7168
RV 40,8,6144
XS 130
M 1,26,2
WL 6
DW 0
LO 16,16,107
LU 107,16
XS 131
M 1,27,3
DW 1024
HS 25,107
AW 120,128
M 0,14,2
WL 5
LL 18,86
LL 111,87
AW 121,129
M 0,15,3
s_add_u32 s42, s44, s60
LA 18
PK 107,16,105,56
v_add_f32 v14, v14, v15
s_nop 1
v_add_f32_dpp v14, v14, v14 quad_perm:[1,0,3,2] row_mask:0xf bank_mask:0xf
s_nop 1
v_add_f32_dpp v14, v14, v14 quad_perm:[2,3,0,1] row_mask:0xf bank_mask:0xf
AW 122,130
M 0,16,4
s_addc_u32 s43, s45, 0
LC_ 111
PK 105,16,105,97
v_add_f32_dpp v14, v14, v14 row_half_mirror row_mask:0xf bank_mask:0xf
AW 123,131
XS 132
M 0,17,5
RA 220,1,8192
RV 44,8,6208
XS 133
M 1,28,4
WL 7
LO 17,17,110
)"
R"(LU 110,17
v_cvt_f64_f32 v[14:15], v14
XS 134
M 1,29,5
RA 224,1,9216
HS 25,110
XS 135
M 2,40,4
LL 16,88
LL 114,89
s_mov_b64 exec, s[54:55]
SP
s_mov_b64 exec, -1
DW 2048
AW 124,132
M 2,41,5
LA 16
PK 110,17,108,56
AW 125,133
M 3,52,4
WL 3
LC_ 114
PK 108,17,108,97
AW 126,134
AW 127,135
M 3,53,5
RA 228,1,10240
PO 110
v_min_u32 v35, v35, v13
M 3,54,0
WV 26
LO 18,18,111
LU 111,18
ST 104
XS 136
M 3,55,1
s_add_u32 s40, s40, 3072
RV 48,8,6272
HS 25,111
s_addc_u32 s41, s41, 0
s_add_u32 m0, s38, 3072
XS 137
LZ 40,72
WL 5
RV 40,8,6336
XS 138
LS 44,73
RV 44,8,6400
XS 139
M 2,42,0
LL 17,90
LL 115,91
M 2,43,1
LA 17
PK 111,18,109,56
M 1,30,0
LC_ 115
PK 109,18,109,97
AW 128,136
M 1,31,1
WV 25
RA 232,1,11264
DW 0
PO 111
AW 129,137
AW 130,138
M 0,18,0
WL 4
LO 16,16,114
LU 114,16
AW 131,139
M 0,19,1
s_add_u32 s42, s44, s61
RV 252,22,5392
HS 25,114
XS 140
LS 48,74
s_addc_u32 s43, s45, 0
RV 48,8,6464
XS 141
LS 40,75
XS 142
XS 143
M 0,20,2
LL 18,92
LL 118,93
M 0,21,3
LA 18
DW 1024
PK 114,16,112,56
M 1,32,2
WV 26
LC_ 118
PK 112,16,112,97
M 1,33,3
WL 7
RA 236,1,12288
RV 40,8,6528
AW 132,140
AW 133,141
LS 44,76
AW 134,142
AW 135,143
M 2,44,2
WL 7
LO 17,17,115
LU 115,17
M 2,45,3
RA 216,1,13312
HS 25,115
ST 108
M 3,56,2
WL 2
DW 2048
LL 16,94
LL 119,95
XS 144
M 3,57,3
LA 16
PK 115,17,113,56
XS 145
M 0,22,4
LC_ 119
PK 113,17,113,97
XS 146
M 0,23,5
WV 27
RA 220,1,14336
RV 44,8,6592
XS 147
LS 48,77
AW 136,144
AW 137,145
M 1,34,4
DQ 3072
LO 18,18,118
LU 118,18
M 1,35,5
s_add_u32 s42, s44, s62
RA 224,1,15360
HS 25,118
s_addc_u32 s43, s45, 0
AW 138,146
M 2,46,4
s_add_u32 s50, s50, %[d]
LL 17,56
LL 104,57
AW 139,147
M 2,47,5
s_cmp_eq_u32 s50, %[wrap]
LA 17
PK 118,18,116,56
XS 148
M 3,58,4
WL 3
LC_ 104
PK 116,18,116,97
s_cselect_b32 s50, %[rst], s50
XS 149
M 3,59,5
WL 3
RA 228,1,16384
PO 118
XS 150
Z 6,12,0
WV 14
LO 16,16,119
LU 119,16
ST 112
XS 151
AW 140,148
M 6,13,1
s_add_u32 s38, s38, 32768
RV 48,8,6656
HS 25,119
AW 141,149
LS 40,78
s_cmp_eq_u32 s38, s77
RV 40,8,6720
AW 142,150
Z 7,24,0
s_cselect_b32 s38, s76, s38
LL 18,58
LL 105,59
M 7,25,1
s_mul_i32 s53, s50, 25600
LA 18
PK 119,16,117,56
AW 143,151
LS 44,79
s_add_u32 s40, %[wlo], s53
XS 152
XS 153
Z 8,36,0
s_addc_u32 s41, %[whi], 0
LC_ 105
PK 117,16,117,97
M 8,37,1
s_mov_b32 m0, s38
RA 232,1,17408
PO 119
Z 9,48,0
WL 5
LO 104,17,104
LU 17,104
XS 154
M 9,49,1
s_add_u32 s42, s44, s63
RA 236,1,18432
HZ 24,17
XS 155
AW 144,152
M 9,50,2
s_addc_u32 s43, s45, 0
LL 16,60
LL 108,61
AW 145,153
M 9,51,3
LA 16
DW 0
HX 25
AW 146,154
M 8,38,2
WV 15
LC_ 108
W1 25,1024
AW 147,155
M 8,39,3
WL 8
RA 216,1,19456
RV 44,8,6784
XS 156
LS 48,80
RV 48,8,6848
XS 157
M 7,26,2
WL 8
LO 105,18,105
LU 18,105
M 7,27,3
ST 116
HS 24,18
XS 158
XS 159
M 6,14,2
WL 6
LL 17,62
LL 109,63
AW 148,156
M 6,15,3
LA 17
RA 220,1,20480
DW 1024
AW 149,157
M 6,16,4
s_add_u32 s37, s37, 32768
LC_ 109
RA 240,7,0
s_cmp_eq_u32 s37, s75
s_cselect_b32 s37, s74, s37
AW 150,158
M 6,17,5
WV 16
DW 2048
v_add_u32 v4, s39, v4
AW 151,159
LS 40,81
WV 16
RA 244,7,256
XS 160
M 7,28,4
WL 6
LO 108,16,108
LU 16,108
M 7,29,5
s_add_u32 s40, s40, 3072
RA 224,1,21504
HS 24,16
XS 161
LS 44,82
s_addc_u32 s41, s41, 0
XS 162
XS 163
M 8,40,4
s_add_u32 m0, s38, 3072
LL 18,64
LL 112,65
M 8,41,5
s_mul_i32 s53, s51, 24
LA 18
AW 152,160
M 9,52,4
WL 5
LC_ 112
RA 248,7,512
AW 153,161
M 9,53,5
s_bitcmp1_b32 s51, 0
RA 228,1,22528
AW 154,162
M 9,54,0
WL 7
LO 109,17,109
LU 17,109
AW 155,163
M 9,55,1
s_addc_u32 s53, s53, 0
HS 24,17
s_lshl_b32 s53, s53, 11
s_bitcmp1_b64 s[90:91], s51
s_cselect_b64 s[44:45], s[84:85], s[82:83]
s_add_u32 s44, s44, s53
s_addc_u32 s45, s45, 0
s_add_u32 s51, s51, %[d]
s_cmp_eq_u32 s51, %[wrap]
s_cselect_b32 s51, %[rst], s51
XS 164
WV 15
LS 48,83
XS 165
XS 166
M 8,42,0
LL 16,66
LL 113,67
M 8,43,1
LA 16
RA 232,1,23552
XS 167
M 7,30,0
LC_ 113
DW 0
AW 156,164
M 7,31,1
s_lshl_b32 s53, s52, 8
RA 252,7,768
AW 157,165
M 6,18,0
WL 4
LO 112,18,112
LU 18,112
AW 158,166
M 6,19,1
s_add_u32 s48, %[alo], s53
RA 236,1,24576
HS 24,18
AW 159,167
M 6,20,2
s_addc_u32 s49, %[ahi], 0
LL 17,68
LL 116,69
s_add_u32 s52, s52, %[d]
s_cmp_eq_u32 s52, %[wrap]
s_cselect_b32 s52, %[rst], s52
DW 1024
XS 168
M 6,21,3
LA 17
DW 2048
XS 169
XS 170
M 7,32,2
LC_ 116
v_add_u32 v1, s37, v0
XS 171
M 7,33,3
RA 216,1,0
v_and_b32 v22, 0xfffffcff, v1
AW 160,168
M 8,44,2
WL 5
LO 113,16,113
LU 16,113
AW 161,169
M 8,45,3
DQ 3072
HS 24,16
AW 162,170
M 9,56,2
WL 3
LL 18,70
LL 117,71
AW 163,171
M 9,57,3
s_add_u32 s50, s50, %[d]
LA 18
RA 220,1,1024
XS 172
XS 173
M 6,22,4
s_cmp_eq_u32 s50, %[wrap]
LC_ 117
RV 252,22,5120
XS 174
M 6,23,5
s_cselect_b32 s50, %[rst], s50
XS 175
AW 164,172
M 7,34,4
WL 5
LO 116,17,116
)"
R"(LU 17,116
M 7,35,5
s_barrier
RA 224,1,2048
HS 24,17
AW 165,173
M 8,46,4
s_sub_u32 s39, 0, s39
LL 16,80
LL 106,81
AW 166,174
M 8,47,5
s_add_u32 s38, s38, 32768
LA 16
ds_read_b64 v[14:15], v5 offset:0
AW 167,175
v_mul_f32 v36, 0xbb2aaaab, v36
v_mul_f32 v37, 0xbb2aaaab, v37
v_mul_f32 v38, 0xbb2aaaab, v38
v_mul_f32 v39, 0xbb2aaaab, v39
s_nop 1
LK 40,240,72,0
LK 44,242,72,2
s_cmp_eq_u32 s38, s77
RA 240,7,1024
LK 48,244,73,0
s_nop 0
CF 128,40,41
LK 248,246,73,2
s_cselect_b32 s38, s76, s38
RA 244,7,1280
CF 129,42,43
M 9,58,4
WL 7
LC_ 106
v_subrev_u32 v5, s39, v5
M 9,59,5
WL 6
RA 228,1,3072
CF 130,44,45
Z 0,12,0
LO 117,18,117
LU 18,117
CF 131,46,47
M 0,13,1
s_mul_i32 s53, s50, 25600
HS 24,18
LK 40,248,74,0
CF 132,48,49
LK 44,250,74,2
s_add_u32 s40, %[wlo], s53
RA 248,7,1536
CF 133,50,51
GZ 0
s_addc_u32 s41, %[whi], 0
CF 134,248,249
CF 135,250,251
LK 48,252,75,0
CF 136,40,41
LK 248,254,75,2
s_mov_b32 m0, s38
RA 252,7,1792
CF 137,42,43
GS 1
WL 5
CF 138,44,45
CF 139,46,47
LK 40,240,76,0
CF 140,48,49
LK 44,242,76,2
RA 240,7,2048
CF 141,50,51
GS 2
WL 5
CF 142,248,249
CF 143,250,251
LK 48,244,77,0
CF 144,40,41
LK 248,246,77,2
RA 244,7,2304
CF 145,42,43
GS 3
CF 146,44,45
CF 147,46,47
Z 1,24,0
LL 17,82
LL 107,83
M 1,25,1
LA 17
RA 232,1,4096
Z 2,36,0
LC_ 107
HX 24
M 2,37,1
WL 6
W1 24,0
LK 40,248,78,0
CF 148,48,49
LK 44,250,78,2
RA 248,7,2560
CF 149,50,51
GS 4
CF 150,248,249
CF 151,250,251
Z 3,48,0
LO 16,16,106
LU 106,16
M 3,49,1
RA 236,1,6144
HZ 25,106
M 3,50,2
LL 18,84
LL 110,85
M 3,51,3
WL 8
LA 18
PK 106,16,104,56
LK 48,252,79,0
CF 152,40,41
LK 248,254,79,2
RA 252,7,2816
CF 153,42,43
GS 5
CF 154,44,45
CF 155,46,47
M 2,38,2
LC_ 110
PK 104,16,104,97
M 2,39,3
RA 216,1,7168
v_add_f32 v14, v14, v15
M 1,26,2
WL 7
LO 17,17,107
LU 107,17
v_add_f32_dpp v14, v14, v14 quad_perm:[1,0,3,2] row_mask:0xf bank_mask:0xf
M 1,27,3
DW 0
HS 25,107
LK 40,240,80,0
CF 156,48,49
LK 44,242,80,2
s_add_u32 s42, s44, s60
CF 157,50,51
v_add_f32_dpp v14, v14, v14 quad_perm:[2,3,0,1] row_mask:0xf bank_mask:0xf
GS 6
s_addc_u32 s43, s45, 0
CF 158,248,249
CF 159,250,251
M 0,14,2
WL 4
LL 16,86
LL 111,87
M 0,15,3
LA 16
PK 107,17,105,56
M 0,16,4
LC_ 111
PK 105,17,105,97
v_add_f32_dpp v14, v14, v14 row_half_mirror row_mask:0xf bank_mask:0xf
M 0,17,5
RA 220,1,8192
v_cvt_f64_f32 v[14:15], v14
LK 48,244,81,0
CF 160,40,41
LK 248,246,81,2
CF 161,42,43
GS 7
CF 162,44,45
CF 163,46,47
M 1,28,4
WL 4
LO 18,18,110
LU 110,18
M 1,29,5
RA 224,1,9216
HS 25,110
M 2,40,4
LL 17,88
LL 114,89
s_mov_b64 exec, s[54:55]
SP
s_mov_b64 exec, -1
M 2,41,5
LA 17
PK 110,18,108,56
LK 40,248,82,0
CF 164,48,49
LK 44,250,82,2
CF 165,50,51
GS 8
CF 166,248,249
CF 167,250,251
M 3,52,4
WL 2
LC_ 114
PK 108,18,108,97
M 3,53,5
RA 228,1,10240
PO 110
M 3,54,0
LO 16,16,111
LU 111,16
ST 104
M 3,55,1
DW 1024
HS 25,111
LK 48,252,83,0
CF 168,40,41
LK 248,254,83,2
s_add_u32 s42, s44, s61
CF 169,42,43
GS 9
s_addc_u32 s43, s45, 0
CF 170,44,45
CF 171,46,47
M 2,42,0
LL 18,90
LL 115,91
M 2,43,1
LA 18
PK 111,16,109,56
M 1,30,0
LC_ 115
PK 109,16,109,97
M 1,31,1
RA 232,1,11264
PO 111
M 0,18,0
WL 3
LO 17,17,114
LU 114,17
M 0,19,1
RV 252,22,5392
HS 25,114
M 0,20,2
LL 16,92
LL 118,93
M 0,21,3
LA 16
PK 114,17,112,56
M 1,32,2
LC_ 118
PK 112,17,112,97
M 1,33,3
RA 236,1,12288
RV 36,8,6912
DW 2048
GS 10
CF 172,48,49
CF 173,50,51
M 2,44,2
WL 6
LO 18,18,115
LU 115,18
M 2,45,3
RA 216,1,13312
HS 25,115
ST 108
M 3,56,2
WL 2
LL 17,94
LL 119,95
M 3,57,3
s_add_u32 s42, s44, s62
LA 17
PK 115,18,113,56
M 0,22,4
s_addc_u32 s43, s45, 0
LC_ 119
PK 113,18,113,97
M 0,23,5
s_add_u32 s40, s40, 3072
RA 220,1,14336
CF 174,248,249
M 1,34,4
LO 16,16,118
LU 118,16
CF 175,250,251
M 1,35,5
s_addc_u32 s41, s41, 0
RA 224,1,15360
HS 25,118
GS 11
s_add_u32 m0, s38, 3072
ST 112
s_nop 5
v_cndmask_b32 v52, v52, v53, s[78:79]
M 2,46,4
LL 18,56
LL 104,57
v_cndmask_b32 v54, v54, v55, s[78:79]
M 2,47,5
LA 18
PK 118,16,116,56
v_cndmask_b32 v52, v52, v54, s[80:81]
M 3,58,4
WL 2
LC_ 104
PK 116,16,116,97
M 3,59,5
RA 228,1,16384
PO 118
v_mul_f32 v52, 0x3b2aaaab, v52
Z 6,12,0
LO 17,17,119
LU 119,17
v_med3_f32 v53, v52, v39, v37
M 6,13,1
DW 0
HS 25,119
LG 52,53
Z 7,24,0
s_add_u32 s42, s44, s63
LL 16,58
LL 105,59
C 52,52,52
M 7,25,1
s_addc_u32 s43, s45, 0
LA 16
PK 119,17,117,56
Z 8,36,0
LC_ 105
PK 117,17,117,97
v_lshlrev_b32 v52, 16, v52
M 8,37,1
RA 232,1,17408
PO 119
v_add_f32 v52, v36, v52
Z 9,48,0
WL 3
LO 104,18,104
LU 18,104
C 52,52,52
M 9,49,1
RA 236,1,18432
HZ 24,18
M 9,50,2
LL 17,60
LL 108,61
v_lshlrev_b32 v52, 16, v52
M 9,51,3
LA 17
ST 116
HX 25
v_sqrt_f32 v52, v52
M 8,38,2
LC_ 108
W1 25,1024
C 52,52,52
M 8,39,3
RA 216,1,19456
v_add_u32 v4, s39, v4
v_lshlrev_b32 v52, 16, v52
M 7,26,2
WL 6
LO 105,16,105
LU 16,105
M 7,27,3
DW 1024
HS 24,16
v_rcp_f32 v52, v52
M 6,14,2
WL 4
LL 18,62
LL 109,63
v_mul_f32 v52, v38, v52
M 6,15,3
s_add_u32 s37, s37, 32768
LA 18
RA 220,1,20480
CF 52,52,52
M 6,16,4
s_cmp_eq_u32 s37, s75
LC_ 109
ds_bpermute_b32 v13, v28, v52
s_cselect_b32 s37, s74, s37
M 6,17,5
DW 2048
M 7,28,4
WL 6
LO 108,17,108
LU 17,108
M 7,29,5
s_mul_i32 s53, s51, 24
RA 224,1,21504
HS 24,17
M 8,40,4
s_bitcmp1_b32 s51, 0
LL 16,64
LL 112,65
M 8,41,5
s_addc_u32 s53, s53, 0
LA 16
M 9,52,4
WL 4
LC_ 112
M 9,53,5
s_lshl_b32 s53, s53, 11
RA 228,1,22528
)"
R"(M 9,54,0
WL 4
LO 109,18,109
LU 18,109
M 9,55,1
s_bitcmp1_b64 s[90:91], s51
HS 24,18
s_cselect_b64 s[44:45], s[84:85], s[82:83]
s_add_u32 s44, s44, s53
s_addc_u32 s45, s45, 0
s_add_u32 s51, s51, %[d]
s_cmp_eq_u32 s51, %[wrap]
s_cselect_b32 s51, %[rst], s51
M 8,42,0
s_lshl_b32 s53, s52, 8
LL 17,66
LL 113,67
M 8,43,1
s_add_u32 s48, %[alo], s53
LA 17
RA 232,1,23552
XS 128
M 7,30,0
s_addc_u32 s49, %[ahi], 0
LC_ 113
XS 129
M 7,31,1
s_add_u32 s52, s52, %[d]
XS 130
XS 131
M 6,18,0
WL 3
LO 112,16,112
LU 16,112
M 6,19,1
s_cmp_eq_u32 s52, %[wrap]
RA 236,1,24576
HS 24,16
AW 168,128
AW 169,129
M 6,20,2
s_cselect_b32 s52, %[rst], s52
DQ 3072
LL 18,68
LL 116,69
AW 170,130
M 6,21,3
LA 18
v_add_u32 v1, s37, v0
AW 171,131
M 7,32,2
s_add_u32 s50, s50, %[d]
LC_ 116
XS 132
M 7,33,3
s_cmp_eq_u32 s50, %[wrap]
RA 216,1,0
v_and_b32 v22, 0xfffffcff, v1
XS 133
M 8,44,2
WL 4
LO 113,17,113
LU 17,113
XS 134
M 8,45,3
s_cselect_b32 s50, %[rst], s50
HS 24,17
XS 135
M 9,56,2
WL 3
LL 16,70
LL 117,71
AW 172,132
M 9,57,3
WV 21
LA 16
RA 220,1,1024
AW 173,133
M 6,22,4
s_barrier
LC_ 117
RV 252,22,5120
AW 174,134
M 6,23,5
s_sub_u32 s39, 0, s39
AW 175,135
v_min_u32 v35, v35, v13
M 7,34,4
WL 5
LO 116,18,116
LU 18,116
M 7,35,5
s_add_u32 s38, s38, 32768
RA 224,1,2048
HS 24,18
XS 136
M 8,46,4
s_cmp_eq_u32 s38, s77
LL 17,80
LL 106,81
XS 137
M 8,47,5
s_cselect_b32 s38, s76, s38
LA 17
XS 138
XS 139
M 9,58,4
WL 4
LC_ 106
AW 176,136
M 9,59,5
WL 3
RA 228,1,3072
AW 177,137
Z 0,12,0
LO 117,16,117
LU 16,117
AW 178,138
M 0,13,1
HS 24,16
AW 179,139
Z 1,24,0
LL 18,82
LL 107,83
XS 140
M 1,25,1
LA 18
RA 232,1,4096
XS 141
Z 2,36,0
LC_ 107
HX 24
XS 142
M 2,37,1
W1 24,0
XS 143
Z 3,48,0
WL 5
LO 17,17,106
LU 106,17
AW 180,140
M 3,49,1
HZ 25,106
AW 181,141
Z 4,60,0
LL 16,84
LL 110,85
AW 182,142
M 4,61,1
LA 16
PK 106,17,104,56
AW 183,143
Z 5,72,0
WL 5
LC_ 110
PK 104,17,104,97
XS 144
M 5,73,1
RA 236,1,6144
XS 145
XS 146
M 5,74,2
WL 4
LO 18,18,107
LU 107,18
XS 147
M 5,75,3
s_add_u32 s42, s44, s60
HS 25,107
AW 184,144
M 4,62,2
s_addc_u32 s43, s45, 0
LL 17,86
LL 111,87
AW 185,145
M 4,63,3
LA 17
PK 107,18,105,56
AW 186,146
M 3,50,2
LC_ 111
PK 105,18,105,97
AW 187,147
M 3,51,3
RA 216,1,7168
XS 148
M 2,38,2
WL 4
LO 16,16,110
LU 110,16
XS 149
M 2,39,3
ST 104
HS 25,110
XS 150
M 1,26,2
LL 18,88
LL 114,89
XS 151
M 1,27,3
LA 18
PK 110,16,108,56
AW 188,148
M 0,14,2
WL 4
LC_ 114
PK 108,16,108,97
AW 189,149
M 0,15,3
s_mov_b32 m0, %[pm]
RA 220,1,8192
PO 110
AW 190,150
M 0,16,4
WL 4
LO 17,17,111
LU 111,17
AW 191,151
M 0,17,5
DF
HS 25,111
XS 152
M 1,28,4
s_add_u32 s42, s44, s61
LL 16,90
LL 115,91
XS 153
XS 154
M 1,29,5
s_addc_u32 s43, s45, 0
LA 16
PK 111,17,109,56
XS 155
M 2,40,4
LC_ 115
PK 109,17,109,97
AW 192,152
M 2,41,5
RA 224,1,9216
PO 111
AW 193,153
M 3,52,4
WL 4
LO 18,18,114
LU 114,18
AW 194,154
M 3,53,5
ST 108
HS 25,114
AW 195,155
M 4,64,4
LL 17,92
LL 118,93
XS 156
XS 157
M 4,65,5
LA 17
PK 114,18,112,56
XS 158
XS 159
AW 196,156
M 5,76,4
WL 4
LC_ 118
PK 112,18,112,97
AW 197,157
AW 198,158
AW 199,159
M 5,77,5
RA 228,1,10240
XS 160
M 5,78,0
WL 4
LO 16,16,115
LU 115,16
M 5,79,1
s_add_u32 s42, s44, s62
HS 25,115
XS 161
M 4,66,0
s_addc_u32 s43, s45, 0
LL 18,94
LL 119,95
M 4,67,1
LA 18
PK 115,16,113,56
M 3,54,0
LC_ 119
PK 113,16,113,97
M 3,55,1
RA 232,1,11264
XS 162
M 2,42,0
WL 4
LO 17,17,118
LU 118,17
XS 163
M 2,43,1
RV 252,22,5392
HS 25,118
AW 200,160
M 1,30,0
ST 112
AW 201,161
M 1,31,1
RA 236,1,12288
PK 118,17,116,56
AW 202,162
M 0,18,0
WL 5
PK 116,17,116,97
AW 203,163
M 0,19,1
PO 118
XS 164
M 0,20,2
WL 3
LO 18,18,119
LU 119,18
XS 165
M 0,21,3
s_add_u32 s42, s44, s63
HS 25,119
XS 166
M 1,32,2
s_addc_u32 s43, s45, 0
HX 25
XS 167
M 1,33,3
RA 216,1,13312
PK 119,18,117,56
AW 204,164
M 2,44,2
W1 25,1024
PK 117,18,117,97
AW 205,165
M 2,45,3
PO 119
v_add_u32 v4, s39, v4
AW 206,166
M 3,56,2
ST 116
AW 207,167
XS 168
M 3,57,3
XS 169
XS 170
M 4,68,2
XS 171
AW 208,168
M 4,69,3
RA 220,1,14336
AW 209,169
M 5,80,2
WL 3
AW 210,170
AW 211,171
M 5,81,3
XS 172
XS 173
M 0,22,4
XS 174
XS 175
M 0,23,5
AW 212,172
AW 213,173
M 1,34,4
AW 214,174
AW 215,175
M 1,35,5
RA 224,1,15360
M 2,46,4
LL 16,56
LL 104,57
M 2,47,5
LA 16
ds_read_b64 v[14:15], v5 offset:0
M 3,58,4
LC_ 104
v_subrev_u32 v5, s39, v5
M 3,59,5
M 4,70,4
M 4,71,5
RA 228,1,16384
M 5,82,4
WL 5
LL 17,58
LL 105,59
M 5,83,5
LA 17
Z 6,12,0
LC_ 105
M 6,13,1
Z 7,24,0
WL 3
LO 104,16,104
LU 16,104
M 7,25,1
RA 232,1,17408
HZ 24,16
)"
R"(Z 8,36,0
LL 18,60
LL 108,61
M 8,37,1
s_mul_i32 s53, s50, 25600
LA 18
v_add_f32 v14, v14, v15
Z 9,48,0
s_add_u32 s40, %[wlo], s53
LC_ 108
v_add_f32_dpp v14, v14, v14 quad_perm:[1,0,3,2] row_mask:0xf bank_mask:0xf
M 9,49,1
s_addc_u32 s41, %[whi], 0
v_add_f32_dpp v14, v14, v14 quad_perm:[2,3,0,1] row_mask:0xf bank_mask:0xf
Z 10,60,0
WL 3
LO 105,17,105
LU 17,105
M 10,61,1
s_mov_b32 m0, s38
RA 236,1,18432
HS 24,17
Z 11,72,0
LL 16,62
LL 109,63
M 11,73,1
LA 16
DW 0
M 11,74,2
LC_ 109
v_add_f32_dpp v14, v14, v14 row_half_mirror row_mask:0xf bank_mask:0xf
M 11,75,3
v_cvt_f64_f32 v[14:15], v14
M 10,62,2
WL 3
LO 108,18,108
LU 18,108
M 10,63,3
s_add_u32 s37, s37, 32768
RA 216,1,19456
HS 24,18
M 9,50,2
s_cmp_eq_u32 s37, s75
LL 17,64
LL 112,65
M 9,51,3
s_cselect_b32 s37, s74, s37
LA 17
s_mov_b64 exec, s[54:55]
SP
s_mov_b64 exec, -1
M 8,38,2
LC_ 112
DW 1024
M 8,39,3
M 7,26,2
WL 3
LO 109,16,109
LU 16,109
M 7,27,3
RA 220,1,20480
HS 24,16
M 6,14,2
LL 18,66
LL 113,67
M 6,15,3
LA 18
DW 2048
M 6,16,4
LC_ 113
M 6,17,5
M 7,28,4
WL 3
LO 112,17,112
LU 17,112
M 7,29,5
s_mul_i32 s53, s51, 24
RA 224,1,21504
HS 24,17
M 8,40,4
s_bitcmp1_b32 s51, 0
LL 16,68
LL 116,69
M 8,41,5
s_addc_u32 s53, s53, 0
LA 16
M 9,52,4
s_lshl_b32 s53, s53, 11
LC_ 116
M 9,53,5
s_bitcmp1_b64 s[90:91], s51
M 10,64,4
WL 3
LO 113,18,113
LU 18,113
M 10,65,5
s_cselect_b64 s[44:45], s[84:85], s[82:83]
RA 228,1,22528
HS 24,18
M 11,76,4
LL 17,70
LL 117,71
M 11,77,5
s_add_u32 s44, s44, s53
LA 17
M 11,78,0
s_addc_u32 s45, s45, 0
LC_ 117
M 11,79,1
s_add_u32 s51, s51, %[d]
M 10,66,0
WL 3
LO 116,16,116
LU 16,116
M 10,67,1
s_cmp_eq_u32 s51, %[wrap]
RA 232,1,23552
HS 24,16
M 9,54,0
s_cselect_b32 s51, %[rst], s51
LL 18,72
LL 120,73
M 9,55,1
s_add_u32 s40, s40, 3072
LA 18
M 8,42,0
s_addc_u32 s41, s41, 0
LC_ 120
s_add_u32 m0, s38, 3072
M 8,43,1
DW 0
M 7,30,0
WL 3
LO 117,17,117
LU 17,117
M 7,31,1
RA 236,1,24576
HS 24,17
M 6,18,0
LL 16,74
LL 121,75
M 6,19,1
LA 16
DW 1024
M 6,20,2
LC_ 121
DW 2048
M 6,21,3
s_lshl_b32 s53, s52, 8
v_add_u32 v1, s37, v0
M 7,32,2
WL 3
LO 120,18,120
LU 18,120
M 7,33,3
s_add_u32 s48, %[alo], s53
RA 216,1,0
HS 24,18
M 8,44,2
s_addc_u32 s49, %[ahi], 0
LL 17,76
LL 124,77
M 8,45,3
s_add_u32 s52, s52, %[d]
LA 17
v_and_b32 v22, 0xfffffcff, v1
M 9,56,2
s_cmp_eq_u32 s52, %[wrap]
LC_ 124
M 9,57,3
s_cselect_b32 s52, %[rst], s52
DQ 3072
M 10,68,2
WL 3
LO 121,16,121
LU 16,121
M 10,69,3
s_add_u32 s50, s50, %[d]
RA 220,1,1024
HS 24,16
M 11,80,2
LL 18,78
LL 125,79
M 11,81,3
s_cmp_eq_u32 s50, %[wrap]
LA 18
RV 252,22,5120
M 6,22,4
s_cselect_b32 s50, %[rst], s50
LC_ 125
M 6,23,5
WV 25
M 7,34,4
WL 4
LO 124,17,124
LU 17,124
M 7,35,5
s_add_u32 s38, s38, 32768
RA 224,1,2048
HS 24,17
M 8,46,4
s_cmp_eq_u32 s38, s77
LL 16,80
LL 106,81
M 8,47,5
s_cselect_b32 s38, s76, s38
LA 16
M 9,58,4
s_barrier
LC_ 106
M 9,59,5
s_sub_u32 s39, 0, s39
M 10,70,4
WL 3
LO 125,18,125
LU 18,125
M 10,71,5
RA 228,1,3072
HS 24,18
M 11,82,4
LL 17,82
LL 107,83
M 11,83,5
LA 17
HX 24
Z 0,12,0
LC_ 107
W1 24,0
M 0,13,1
Z 1,24,0
WL 4
LO 16,16,106
LU 106,16
M 1,25,1
RA 232,1,4096
HZ 25,106
Z 2,36,0
LL 18,84
LL 110,85
M 2,37,1
LA 18
PK 106,16,104,56
Z 3,48,0
LC_ 110
PK 104,16,104,97
M 3,49,1
Z 4,60,0
WL 4
LO 17,17,107
LU 107,17
M 4,61,1
s_add_u32 s42, s44, s60
RA 236,1,6144
HS 25,107
Z 5,72,0
LL 16,86
LL 111,87
M 5,73,1
s_addc_u32 s43, s45, 0
LA 16
PK 107,17,105,56
M 5,74,2
LC_ 111
PK 105,17,105,97
M 5,75,3
ST 104
M 4,62,2
WL 3
LO 18,18,110
LU 110,18
M 4,63,3
RA 216,1,7168
HS 25,110
M 3,50,2
LL 17,88
LL 114,89
M 3,51,3
LA 17
PK 110,18,108,56
M 2,38,2
LC_ 114
PK 108,18,108,97
M 2,39,3
PO 110
M 1,26,2
WL 3
LO 16,16,111
LU 111,16
M 1,27,3
s_add_u32 s42, s44, s61
RA 220,1,8192
HS 25,111
M 0,14,2
LL 18,90
LL 115,91
M 0,15,3
s_addc_u32 s43, s45, 0
LA 18
PK 111,16,109,56
M 0,16,4
s_mov_b32 m0, %[pm]
LC_ 115
PK 109,16,109,97
M 0,17,5
DF
PO 111
M 1,28,4
WL 3
LO 17,17,114
LU 114,17
M 1,29,5
RA 224,1,9216
HS 25,114
M 2,40,4
LL 16,92
LL 118,93
M 2,41,5
LA 16
PK 114,17,112,56
M 3,52,4
LC_ 118
PK 112,17,112,97
M 3,53,5
ST 108
M 4,64,4
WL 3
LO 18,18,115
LU 115,18
M 4,65,5
s_add_u32 s42, s44, s62
RA 228,1,10240
HS 25,115
M 5,76,4
LL 17,94
LL 119,95
M 5,77,5
s_addc_u32 s43, s45, 0
LA 17
PK 115,18,113,56
M 5,78,0
LC_ 119
PK 113,18,113,97
M 5,79,1
ST 112
M 4,66,0
WL 3
LO 16,16,118
LU 118,16
M 4,67,1
RA 232,1,11264
HS 25,118
M 3,54,0
LL 18,96
LL 122,97
M 3,55,1
LA 18
PK 118,16,116,56
M 2,42,0
LC_ 122
PK 116,16,116,97
M 2,43,1
RV 252,22,5392
PO 118
M 1,30,0
WL 4
LO 17,17,119
LU 119,17
M 1,31,1
s_add_u32 s42, s44, s63
RA 236,1,12288
HS 25,119
M 0,18,0
LL 16,98
LL 123,99
M 0,19,1
s_addc_u32 s43, s45, 0
LA 16
PK 119,17,117,56
M 0,20,2
LC_ 123
PK 117,17,117,97
M 0,21,3
PO 119
M 1,32,2
WL 4
LO 18,18,122
LU 122,18
M 1,33,3
RA 216,1,13312
HS 25,122
M 2,44,2
LL 17,100
LL 126,101
M 2,45,3
LA 17
PK 122,18,120,56
M 3,56,2
LC_ 126
PK 120,18,120,97
M 3,57,3
)"
R"(ST 116
M 4,68,2
WL 3
LO 16,16,123
LU 123,16
M 4,69,3
s_add_u32 s42, s44, s64
RA 220,1,14336
HS 25,123
M 5,80,2
LL 18,102
LL 127,103
M 5,81,3
s_addc_u32 s43, s45, 0
LA 18
PK 123,16,121,56
M 0,22,4
LC_ 127
PK 121,16,121,97
M 0,23,5
ST 120
M 1,34,4
WL 3
LO 17,17,126
LU 126,17
M 1,35,5
RA 224,1,15360
HS 25,126
M 2,46,4
LL 16,56
LL 104,57
M 2,47,5
LA 16
PK 126,17,124,56
M 3,58,4
LC_ 104
PK 124,17,124,97
M 3,59,5
ds_read_b64 v[14:15], v5 offset:0
PO 126
M 4,70,4
WL 4
LO 18,18,127
LU 127,18
M 4,71,5
s_add_u32 s42, s44, s65
RA 228,1,16384
HS 25,127
M 5,82,4
LL 17,58
LL 105,59
M 5,83,5
s_addc_u32 s43, s45, 0
LA 17
PK 127,18,125,56
Z 6,12,0
LC_ 105
PK 125,18,125,97
M 6,13,1
PO 127
v_subrev_u32 v5, s39, v5
Z 7,24,0
WL 4
LO 104,16,104
LU 16,104
M 7,25,1
RA 232,1,17408
HZ 24,16
Z 8,36,0
WL 4
LL 18,60
LL 108,61
M 8,37,1
s_mul_i32 s53, s50, 25600
LA 18
v_add_f32 v14, v14, v15
Z 9,48,0
s_add_u32 s40, %[wlo], s53
LC_ 108
v_add_f32_dpp v14, v14, v14 quad_perm:[1,0,3,2] row_mask:0xf bank_mask:0xf
M 9,49,1
s_addc_u32 s41, %[whi], 0
v_add_f32_dpp v14, v14, v14 quad_perm:[2,3,0,1] row_mask:0xf bank_mask:0xf
HX 25
Z 10,60,0
WL 3
LO 105,17,105
LU 17,105
M 10,61,1
s_mov_b32 m0, s38
RA 236,1,18432
HS 24,17
Z 11,72,0
LL 16,62
LL 109,63
M 11,73,1
LA 16
W1 25,1024
M 11,74,2
LC_ 109
ST 124
M 11,75,3
DW 0
v_add_f32_dpp v14, v14, v14 row_half_mirror row_mask:0xf bank_mask:0xf
M 10,62,2
WL 4
LO 108,18,108
LU 18,108
M 10,63,3
s_add_u32 s37, s37, 32768
RA 216,1,19456
HS 24,18
M 9,50,2
s_cmp_eq_u32 s37, s75
LL 17,64
LL 112,65
M 9,51,3
s_cselect_b32 s37, s74, s37
LA 17
DW 1024
M 8,38,2
LC_ 112
v_cvt_f64_f32 v[14:15], v14
M 8,39,3
s_mov_b64 exec, s[54:55]
SP
s_mov_b64 exec, -1
v_add_u32 v4, s39, v4
M 7,26,2
WL 3
LO 109,16,109
LU 16,109
M 7,27,3
RA 220,1,20480
HS 24,16
M 6,14,2
LL 18,66
LL 113,67
M 6,15,3
LA 18
DW 2048
M 6,16,4
LC_ 113
M 6,17,5
M 7,28,4
WL 3
LO 112,17,112
LU 17,112
M 7,29,5
s_mul_i32 s53, s51, 24
RA 224,1,21504
HS 24,17
M 8,40,4
s_bitcmp1_b32 s51, 0
LL 16,68
LL 116,69
M 8,41,5
s_addc_u32 s53, s53, 0
LA 16
M 9,52,4
s_lshl_b32 s53, s53, 11
LC_ 116
M 9,53,5
s_bitcmp1_b64 s[90:91], s51
M 10,64,4
WL 3
LO 113,18,113
LU 18,113
M 10,65,5
s_cselect_b64 s[44:45], s[84:85], s[82:83]
RA 228,1,22528
HS 24,18
M 11,76,4
LL 17,70
LL 117,71
M 11,77,5
s_add_u32 s44, s44, s53
LA 17
M 11,78,0
s_addc_u32 s45, s45, 0
LC_ 117
M 11,79,1
s_add_u32 s51, s51, %[d]
M 10,66,0
WL 3
LO 116,16,116
LU 16,116
M 10,67,1
s_cmp_eq_u32 s51, %[wrap]
RA 232,1,23552
HS 24,16
M 9,54,0
s_cselect_b32 s51, %[rst], s51
LL 18,72
LL 120,73
M 9,55,1
s_add_u32 s40, s40, 3072
LA 18
M 8,42,0
s_addc_u32 s41, s41, 0
LC_ 120
s_add_u32 m0, s38, 3072
M 8,43,1
DW 0
M 7,30,0
WL 3
LO 117,17,117
LU 17,117
M 7,31,1
RA 236,1,24576
HS 24,17
M 6,18,0
LL 16,74
LL 121,75
M 6,19,1
LA 16
DW 1024
M 6,20,2
LC_ 121
DW 2048
M 6,21,3
s_lshl_b32 s53, s52, 8
v_add_u32 v1, s37, v0
M 7,32,2
WL 3
LO 120,18,120
LU 18,120
M 7,33,3
s_add_u32 s48, %[alo], s53
RA 216,1,0
HS 24,18
M 8,44,2
s_addc_u32 s49, %[ahi], 0
LL 17,76
LL 124,77
M 8,45,3
s_add_u32 s52, s52, %[d]
LA 17
v_and_b32 v22, 0xfffffcff, v1
M 9,56,2
s_cmp_eq_u32 s52, %[wrap]
LC_ 124
M 9,57,3
s_cselect_b32 s52, %[rst], s52
DQ 3072
M 10,68,2
WL 3
LO 121,16,121
LU 16,121
M 10,69,3
s_add_u32 s50, s50, %[d]
RA 220,1,1024
HS 24,16
M 11,80,2
LL 18,78
LL 125,79
M 11,81,3
s_cmp_eq_u32 s50, %[wrap]
LA 18
RV 252,22,5120
M 6,22,4
s_cselect_b32 s50, %[rst], s50
LC_ 125
M 6,23,5
WV 28
v_add_u32 v19, 1, v19
M 7,34,4
WL 4
LO 124,17,124
LU 17,124
M 7,35,5
s_add_u32 s38, s38, 32768
RA 224,1,2048
HS 24,17
M 8,46,4
s_cmp_eq_u32 s38, s77
LL 16,80
LL 106,81
M 8,47,5
s_cselect_b32 s38, s76, s38
LA 16
FP
M 9,58,4
s_sub_u32 s39, 0, s39
LC_ 106
M 9,59,5
M 10,70,4
WL 4
LO 125,18,125
LU 18,125
M 10,71,5
RA 228,1,3072
HS 24,18
M 11,82,4
LL 17,82
LL 107,83
M 11,83,5
s_mov_b32 s66, 28
1:
LA 17
HX 24
Z 0,12,0
LC_ 107
M 0,13,1
Z 1,24,0
WL 3
LO 16,16,106
LU 106,16
M 1,25,1
RA 232,1,4096
HZ 25,106
Z 2,36,0
LL 18,84
LL 110,85
M 2,37,1
LA 18
PK 106,16,104,56
Z 3,48,0
LC_ 110
PK 104,16,104,97
M 3,49,1
Z 4,60,0
WL 3
LO 17,17,107
LU 107,17
M 4,61,1
s_add_u32 s42, s44, s60
RA 236,1,6144
HS 25,107
Z 5,72,0
LL 16,86
LL 111,87
M 5,73,1
s_addc_u32 s43, s45, 0
LA 16
PK 107,17,105,56
M 5,74,2
LC_ 111
PK 105,17,105,97
M 5,75,3
ST 104
M 4,62,2
WL 3
LO 18,18,110
LU 110,18
M 4,63,3
RA 216,1,7168
HS 25,110
M 3,50,2
LL 17,88
LL 114,89
M 3,51,3
LA 17
PK 110,18,108,56
M 2,38,2
LC_ 114
PK 108,18,108,97
M 2,39,3
FR
PO 110
M 1,26,2
WL 4
LO 16,16,111
LU 111,16
M 1,27,3
s_add_u32 s42, s44, s61
RA 220,1,8192
HS 25,111
M 0,14,2
LL 18,90
LL 115,91
M 0,15,3
s_addc_u32 s43, s45, 0
LA 18
PK 111,16,109,56
M 0,16,4
s_mov_b32 m0, %[pm]
LC_ 115
PK 109,16,109,97
M 0,17,5
DF
PO 111
M 1,28,4
WL 4
LO 17,17,114
LU 114,17
M 1,29,5
RA 224,1,9216
HS 25,114
M 2,40,4
LL 16,92
LL 118,93
M 2,41,5
LA 16
PK 114,17,112,56
M 3,52,4
LC_ 118
PK 112,17,112,97
M 3,53,5
ST 108
)"
R"(M 4,64,4
WL 3
LO 18,18,115
LU 115,18
M 4,65,5
s_add_u32 s42, s44, s62
RA 228,1,10240
HS 25,115
M 5,76,4
LL 17,94
LL 119,95
M 5,77,5
s_addc_u32 s43, s45, 0
LA 17
PK 115,18,113,56
M 5,78,0
LC_ 119
PK 113,18,113,97
M 5,79,1
ST 112
M 4,66,0
WL 3
LO 16,16,118
LU 118,16
M 4,67,1
RA 232,1,11264
HS 25,118
M 3,54,0
LL 18,96
LL 122,97
M 3,55,1
LA 18
PK 118,16,116,56
v_cmp_gt_u32 vcc, v19, v15
M 2,42,0
LC_ 122
PK 116,16,116,97
M 2,43,1
RV 252,22,5392
PO 118
M 1,30,0
WL 4
LO 17,17,119
LU 119,17
M 1,31,1
s_cbranch_vccnz .Lk1s0
.Lk1r0:
RA 236,1,12288
HS 25,119
M 0,18,0
LL 16,98
LL 123,99
M 0,19,1
s_add_u32 s42, s44, s63
LA 16
PK 119,17,117,56
M 0,20,2
s_addc_u32 s43, s45, 0
LC_ 123
PK 117,17,117,97
M 0,21,3
W1 24,0
PO 119
M 1,32,2
WL 5
LO 18,18,122
LU 122,18
M 1,33,3
RA 216,1,13312
HS 25,122
M 2,44,2
LL 17,100
LL 126,101
M 2,45,3
LA 17
PK 122,18,120,56
M 3,56,2
LC_ 126
PK 120,18,120,97
M 3,57,3
ST 116
M 4,68,2
WL 4
LO 16,16,123
LU 123,16
M 4,69,3
s_add_u32 s42, s44, s64
RA 220,1,14336
HS 25,123
M 5,80,2
LL 18,102
LL 127,103
M 5,81,3
s_addc_u32 s43, s45, 0
LA 18
PK 123,16,121,56
M 0,22,4
LC_ 127
PK 121,16,121,97
M 0,23,5
ST 120
M 1,34,4
WL 3
LO 17,17,126
LU 126,17
M 1,35,5
RA 224,1,15360
HS 25,126
M 2,46,4
LL 16,56
LL 104,57
M 2,47,5
LA 16
PK 126,17,124,56
M 3,58,4
LC_ 104
PK 124,17,124,97
M 3,59,5
ds_read_b64 v[14:15], v5 offset:0
PO 126
M 4,70,4
WL 4
LO 18,18,127
LU 127,18
M 4,71,5
s_add_u32 s42, s44, s65
RA 228,1,16384
HS 25,127
M 5,82,4
LL 17,58
LL 105,59
M 5,83,5
s_addc_u32 s43, s45, 0
LA 17
PK 127,18,125,56
Z 6,12,0
LC_ 105
PK 125,18,125,97
M 6,13,1
PO 127
v_subrev_u32 v5, s39, v5
Z 7,24,0
WL 4
LO 104,16,104
LU 16,104
M 7,25,1
RA 232,1,17408
HZ 24,16
Z 8,36,0
WL 4
LL 18,60
LL 108,61
M 8,37,1
s_mul_i32 s53, s50, 25600
LA 18
v_add_f32 v14, v14, v15
Z 9,48,0
s_add_u32 s40, %[wlo], s53
LC_ 108
v_add_f32_dpp v14, v14, v14 quad_perm:[1,0,3,2] row_mask:0xf bank_mask:0xf
M 9,49,1
s_addc_u32 s41, %[whi], 0
Z 10,60,0
WL 3
LO 105,17,105
LU 17,105
M 10,61,1
s_mov_b32 m0, s38
RA 236,1,18432
HS 24,17
Z 11,72,0
LL 16,62
LL 109,63
M 11,73,1
LA 16
ST 124
M 11,74,2
LC_ 109
DW 0
M 11,75,3
v_add_f32_dpp v14, v14, v14 quad_perm:[2,3,0,1] row_mask:0xf bank_mask:0xf
HX 25
M 10,62,2
WL 3
LO 108,18,108
LU 18,108
W1 25,1024
M 10,63,3
s_add_u32 s37, s37, 32768
RA 216,1,19456
HS 24,18
M 9,50,2
s_cmp_eq_u32 s37, s75
LL 17,64
LL 112,65
M 9,51,3
s_cselect_b32 s37, s74, s37
LA 17
DW 1024
M 8,38,2
LC_ 112
v_add_f32_dpp v14, v14, v14 row_half_mirror row_mask:0xf bank_mask:0xf
M 8,39,3
v_cvt_f64_f32 v[14:15], v14
v_add_u32 v4, s39, v4
M 7,26,2
WL 4
LO 109,16,109
LU 16,109
M 7,27,3
RA 220,1,20480
HS 24,16
M 6,14,2
LL 18,66
LL 113,67
M 6,15,3
LA 18
s_mov_b64 exec, s[54:55]
SP
s_mov_b64 exec, -1
M 6,16,4
LC_ 113
DW 2048
M 6,17,5
M 7,28,4
WL 3
LO 112,17,112
LU 17,112
M 7,29,5
s_mul_i32 s53, s51, 24
RA 224,1,21504
HS 24,17
M 8,40,4
s_bitcmp1_b32 s51, 0
LL 16,68
LL 116,69
M 8,41,5
s_addc_u32 s53, s53, 0
LA 16
M 9,52,4
s_lshl_b32 s53, s53, 11
LC_ 116
M 9,53,5
s_bitcmp1_b64 s[90:91], s51
M 10,64,4
WL 3
LO 113,18,113
LU 18,113
M 10,65,5
s_cselect_b64 s[44:45], s[84:85], s[82:83]
RA 228,1,22528
HS 24,18
M 11,76,4
LL 17,70
LL 117,71
M 11,77,5
s_add_u32 s44, s44, s53
LA 17
M 11,78,0
s_addc_u32 s45, s45, 0
LC_ 117
M 11,79,1
s_add_u32 s51, s51, %[d]
M 10,66,0
WL 3
LO 116,16,116
LU 16,116
M 10,67,1
s_cmp_eq_u32 s51, %[wrap]
RA 232,1,23552
HS 24,16
M 9,54,0
s_cselect_b32 s51, %[rst], s51
LL 18,72
LL 120,73
M 9,55,1
s_add_u32 s40, s40, 3072
LA 18
M 8,42,0
s_addc_u32 s41, s41, 0
LC_ 120
s_add_u32 m0, s38, 3072
M 8,43,1
DW 0
M 7,30,0
WL 3
LO 117,17,117
LU 17,117
M 7,31,1
RA 236,1,24576
HS 24,17
M 6,18,0
LL 16,74
LL 121,75
M 6,19,1
LA 16
DW 1024
M 6,20,2
LC_ 121
DW 2048
M 6,21,3
s_lshl_b32 s53, s52, 8
v_add_u32 v1, s37, v0
M 7,32,2
WL 3
LO 120,18,120
LU 18,120
M 7,33,3
s_add_u32 s48, %[alo], s53
RA 216,1,0
HS 24,18
M 8,44,2
s_addc_u32 s49, %[ahi], 0
LL 17,76
LL 124,77
M 8,45,3
s_add_u32 s52, s52, %[d]
LA 17
v_and_b32 v22, 0xfffffcff, v1
M 9,56,2
s_cmp_eq_u32 s52, %[wrap]
LC_ 124
M 9,57,3
s_cselect_b32 s52, %[rst], s52
DQ 3072
M 10,68,2
WL 3
LO 121,16,121
LU 16,121
M 10,69,3
s_add_u32 s50, s50, %[d]
RA 220,1,1024
HS 24,16
M 11,80,2
LL 18,78
LL 125,79
M 11,81,3
s_cmp_eq_u32 s50, %[wrap]
LA 18
RV 252,22,5120
M 6,22,4
s_cselect_b32 s50, %[rst], s50
LC_ 125
M 6,23,5
WV 30
v_add_u32 v19, 1, v19
M 7,34,4
WL 4
LO 124,17,124
LU 17,124
M 7,35,5
s_add_u32 s38, s38, 32768
RA 224,1,2048
HS 24,17
M 8,46,4
s_cmp_eq_u32 s38, s77
LL 16,80
LL 106,81
M 8,47,5
s_cselect_b32 s38, s76, s38
LA 16
FP
M 9,58,4
s_sub_u32 s39, 0, s39
LC_ 106
M 9,59,5
M 10,70,4
WL 4
LO 125,18,125
LU 18,125
M 10,71,5
RA 228,1,3072
HS 24,18
M 11,82,4
s_sub_u32 s66, s66, 1
LL 17,82
LL 107,83
M 11,83,5
s_cbranch_scc0 1b
7:
LA 17
HX 24
Z 0,12,0
LC_ 107
M 0,13,1
Z 1,24,0
WL 3
LO 16,16,106
LU 106,16
M 1,25,1
RA 232,1,4096
HZ 25,106
Z 2,36,0
LL 18,84
LL 110,85
M 2,37,1
LA 18
PK 106,16,104,56
Z 3,48,0
LC_ 110
)"
R"(PK 104,16,104,97
M 3,49,1
Z 4,60,0
WL 3
LO 17,17,107
LU 107,17
M 4,61,1
s_add_u32 s42, s44, s60
RA 236,1,6144
HS 25,107
Z 5,72,0
LL 16,86
LL 111,87
M 5,73,1
s_addc_u32 s43, s45, 0
LA 16
PK 107,17,105,56
M 5,74,2
LC_ 111
PK 105,17,105,97
M 5,75,3
ST 104
M 4,62,2
WL 3
LO 18,18,110
LU 110,18
M 4,63,3
RA 216,1,7168
HS 25,110
M 3,50,2
LL 17,88
LL 114,89
M 3,51,3
LA 17
PK 110,18,108,56
M 2,38,2
LC_ 114
PK 108,18,108,97
M 2,39,3
FR
PO 110
M 1,26,2
WL 4
LO 16,16,111
LU 111,16
M 1,27,3
s_add_u32 s42, s44, s61
RA 220,1,8192
HS 25,111
M 0,14,2
LL 18,90
LL 115,91
M 0,15,3
s_addc_u32 s43, s45, 0
LA 18
PK 111,16,109,56
M 0,16,4
s_mov_b32 m0, %[pm]
LC_ 115
PK 109,16,109,97
M 0,17,5
DF
PO 111
M 1,28,4
WL 4
LO 17,17,114
LU 114,17
M 1,29,5
RA 224,1,9216
HS 25,114
M 2,40,4
LL 16,92
LL 118,93
M 2,41,5
LA 16
PK 114,17,112,56
M 3,52,4
LC_ 118
PK 112,17,112,97
M 3,53,5
ST 108
M 4,64,4
WL 3
LO 18,18,115
LU 115,18
M 4,65,5
s_add_u32 s42, s44, s62
RA 228,1,10240
HS 25,115
M 5,76,4
LL 17,94
LL 119,95
M 5,77,5
s_addc_u32 s43, s45, 0
LA 17
PK 115,18,113,56
M 5,78,0
LC_ 119
PK 113,18,113,97
M 5,79,1
ST 112
M 4,66,0
WL 3
LO 16,16,118
LU 118,16
M 4,67,1
RA 232,1,11264
HS 25,118
M 3,54,0
LL 18,96
LL 122,97
M 3,55,1
LA 18
PK 118,16,116,56
v_cmp_gt_u32 vcc, v19, v15
M 2,42,0
LC_ 122
PK 116,16,116,97
M 2,43,1
RV 252,22,5392
PO 118
M 1,30,0
WL 4
LO 17,17,119
LU 119,17
M 1,31,1
s_cbranch_vccnz .Lk1s1
.Lk1r1:
RA 236,1,12288
HS 25,119
M 0,18,0
LL 16,98
LL 123,99
M 0,19,1
s_add_u32 s42, s44, s63
LA 16
PK 119,17,117,56
M 0,20,2
s_addc_u32 s43, s45, 0
LC_ 123
PK 117,17,117,97
M 0,21,3
W1 24,0
PO 119
M 1,32,2
WL 5
LO 18,18,122
LU 122,18
M 1,33,3
RA 216,1,13312
HS 25,122
M 2,44,2
LL 17,100
LL 126,101
M 2,45,3
LA 17
PK 122,18,120,56
M 3,56,2
LC_ 126
PK 120,18,120,97
M 3,57,3
ST 116
M 4,68,2
WL 4
LO 16,16,123
LU 123,16
M 4,69,3
s_add_u32 s42, s44, s64
RA 220,1,14336
HS 25,123
M 5,80,2
LL 18,102
LL 127,103
M 5,81,3
s_addc_u32 s43, s45, 0
LA 18
PK 123,16,121,56
M 0,22,4
LC_ 127
PK 121,16,121,97
M 0,23,5
ST 120
M 1,34,4
WL 3
LO 17,17,126
LU 126,17
M 1,35,5
RA 224,1,15360
HS 25,126
M 2,46,4
LL 16,56
LL 104,57
M 2,47,5
LA 16
PK 126,17,124,56
M 3,58,4
LC_ 104
PK 124,17,124,97
M 3,59,5
ds_read_b64 v[14:15], v5 offset:0
PO 126
M 4,70,4
WL 4
LO 18,18,127
LU 127,18
M 4,71,5
s_add_u32 s42, s44, s65
RA 228,1,16384
HS 25,127
M 5,82,4
LL 17,58
LL 105,59
M 5,83,5
s_addc_u32 s43, s45, 0
LA 17
PK 127,18,125,56
Z 6,12,0
LC_ 105
PK 125,18,125,97
M 6,13,1
PO 127
v_subrev_u32 v5, s39, v5
Z 7,24,0
WL 4
LO 104,16,104
LU 16,104
M 7,25,1
RA 232,1,17408
HZ 24,16
Z 8,36,0
WL 4
LL 18,60
LL 108,61
M 8,37,1
s_mul_i32 s53, s50, 25600
LA 18
v_add_f32 v14, v14, v15
Z 9,48,0
s_add_u32 s40, %[wlo], s53
LC_ 108
v_add_f32_dpp v14, v14, v14 quad_perm:[1,0,3,2] row_mask:0xf bank_mask:0xf
M 9,49,1
s_addc_u32 s41, %[whi], 0
Z 10,60,0
WL 3
LO 105,17,105
LU 17,105
M 10,61,1
s_mov_b32 m0, s38
RA 236,1,18432
HS 24,17
Z 11,72,0
LL 16,62
LL 109,63
M 11,73,1
LA 16
ST 124
M 11,74,2
LC_ 109
DW 0
M 11,75,3
v_add_f32_dpp v14, v14, v14 quad_perm:[2,3,0,1] row_mask:0xf bank_mask:0xf
HX 25
M 10,62,2
WL 3
LO 108,18,108
LU 18,108
W1 25,1024
M 10,63,3
s_add_u32 s37, s37, 32768
RA 216,1,19456
HS 24,18
M 9,50,2
s_cmp_eq_u32 s37, s75
LL 17,64
LL 112,65
M 9,51,3
s_cselect_b32 s37, s74, s37
LA 17
DW 1024
M 8,38,2
LC_ 112
v_add_f32_dpp v14, v14, v14 row_half_mirror row_mask:0xf bank_mask:0xf
M 8,39,3
v_cvt_f64_f32 v[14:15], v14
v_add_u32 v4, s39, v4
s_mov_b64 exec, 1
AT
s_mov_b64 exec, -1
M 7,26,2
WL 4
LO 109,16,109
LU 16,109
M 7,27,3
RA 220,1,20480
HS 24,16
M 6,14,2
LL 18,66
LL 113,67
M 6,15,3
LA 18
s_mov_b64 exec, s[54:55]
SP
s_mov_b64 exec, -1
M 6,16,4
LC_ 113
DW 2048
M 6,17,5
M 7,28,4
WL 3
LO 112,17,112
LU 17,112
M 7,29,5
s_mul_i32 s53, s51, 24
RA 224,1,21504
HS 24,17
M 8,40,4
s_bitcmp1_b32 s51, 0
LL 16,68
LL 116,69
M 8,41,5
s_addc_u32 s53, s53, 0
LA 16
M 9,52,4
s_lshl_b32 s53, s53, 11
LC_ 116
M 9,53,5
s_bitcmp1_b64 s[90:91], s51
M 10,64,4
WL 3
LO 113,18,113
LU 18,113
M 10,65,5
s_cselect_b64 s[44:45], s[84:85], s[82:83]
RA 228,1,22528
HS 24,18
M 11,76,4
LL 17,70
LL 117,71
M 11,77,5
s_add_u32 s44, s44, s53
LA 17
M 11,78,0
s_addc_u32 s45, s45, 0
LC_ 117
M 11,79,1
s_add_u32 s51, s51, %[d]
M 10,66,0
WL 3
LO 116,16,116
LU 16,116
M 10,67,1
s_cmp_eq_u32 s51, %[wrap]
RA 232,1,23552
HS 24,16
M 9,54,0
s_cselect_b32 s51, %[rst], s51
LL 18,72
LL 120,73
M 9,55,1
s_add_u32 s40, s40, 3072
LA 18
M 8,42,0
s_addc_u32 s41, s41, 0
LC_ 120
s_add_u32 m0, s38, 3072
M 8,43,1
DW 0
M 7,30,0
WL 3
LO 117,17,117
LU 17,117
M 7,31,1
RA 236,1,24576
HS 24,17
M 6,18,0
LL 16,74
LL 121,75
M 6,19,1
LA 16
DW 1024
M 6,20,2
LC_ 121
DW 2048
M 6,21,3
s_lshl_b32 s53, s52, 8
v_add_u32 v1, s37, v0
M 7,32,2
WL 3
LO 120,18,120
LU 18,120
M 7,33,3
s_add_u32 s48, %[alo], s53
RA 216,1,0
HS 24,18
M 8,44,2
s_addc_u32 s49, %[ahi], 0
LL 17,76
LL 124,77
M 8,45,3
)"
R"(s_add_u32 s52, s52, %[d]
LA 17
v_and_b32 v22, 0xfffffcff, v1
M 9,56,2
s_cmp_eq_u32 s52, %[wrap]
LC_ 124
M 9,57,3
s_cselect_b32 s52, %[rst], s52
DQ 3072
M 10,68,2
WL 3
LO 121,16,121
LU 16,121
M 10,69,3
s_add_u32 s50, s50, %[d]
RA 220,1,1024
HS 24,16
M 11,80,2
LL 18,78
LL 125,79
M 11,81,3
s_cmp_eq_u32 s50, %[wrap]
LA 18
RV 252,22,5120
M 6,22,4
s_cselect_b32 s50, %[rst], s50
LC_ 125
M 6,23,5
WV 31
v_add_u32 v19, 1, v19
M 7,34,4
WL 4
LO 124,17,124
LU 17,124
M 7,35,5
s_add_u32 s38, s38, 32768
RA 224,1,2048
HS 24,17
M 8,46,4
s_cmp_eq_u32 s38, s77
LL 16,80
LL 106,81
M 8,47,5
s_cselect_b32 s38, s76, s38
LA 16
FP
M 9,58,4
s_sub_u32 s39, 0, s39
LC_ 106
M 9,59,5
M 10,70,4
WL 4
LO 125,18,125
LU 18,125
M 10,71,5
RA 228,1,3072
HS 24,18
M 11,82,4
LL 17,82
LL 107,83
M 11,83,5
LA 17
HX 24
Z 0,12,0
LC_ 107
M 0,13,1
Z 1,24,0
WL 3
LO 16,16,106
LU 106,16
M 1,25,1
RA 232,1,4096
HZ 25,106
Z 2,36,0
LL 18,84
LL 110,85
M 2,37,1
LA 18
PK 106,16,104,56
Z 3,48,0
LC_ 110
PK 104,16,104,97
M 3,49,1
Z 4,60,0
WL 3
LO 17,17,107
LU 107,17
M 4,61,1
s_add_u32 s42, s44, s60
RA 236,1,6144
HS 25,107
Z 5,72,0
LL 16,86
LL 111,87
M 5,73,1
s_addc_u32 s43, s45, 0
LA 16
PK 107,17,105,56
M 5,74,2
LC_ 111
PK 105,17,105,97
M 5,75,3
ST 104
M 4,62,2
WL 3
LO 18,18,110
LU 110,18
M 4,63,3
RA 216,1,7168
HS 25,110
M 3,50,2
LL 17,88
LL 114,89
M 3,51,3
LA 17
PK 110,18,108,56
M 2,38,2
LC_ 114
PK 108,18,108,97
M 2,39,3
FR
PO 110
M 1,26,2
WL 4
LO 16,16,111
LU 111,16
M 1,27,3
s_add_u32 s42, s44, s61
RA 220,1,8192
HS 25,111
M 0,14,2
LL 18,90
LL 115,91
M 0,15,3
s_addc_u32 s43, s45, 0
LA 18
PK 111,16,109,56
M 0,16,4
s_mov_b32 m0, %[pm]
LC_ 115
PK 109,16,109,97
M 0,17,5
DF
PO 111
M 1,28,4
WL 4
LO 17,17,114
LU 114,17
M 1,29,5
RA 224,1,9216
HS 25,114
M 2,40,4
LL 16,92
LL 118,93
M 2,41,5
LA 16
PK 114,17,112,56
M 3,52,4
LC_ 118
PK 112,17,112,97
M 3,53,5
ST 108
M 4,64,4
WL 3
LO 18,18,115
LU 115,18
M 4,65,5
s_add_u32 s42, s44, s62
RA 228,1,10240
HS 25,115
M 5,76,4
LL 17,94
LL 119,95
M 5,77,5
s_addc_u32 s43, s45, 0
LA 17
PK 115,18,113,56
M 5,78,0
LC_ 119
PK 113,18,113,97
M 5,79,1
ST 112
M 4,66,0
WL 3
LO 16,16,118
LU 118,16
M 4,67,1
RA 232,1,11264
HS 25,118
M 3,54,0
LL 18,96
LL 122,97
M 3,55,1
LA 18
PK 118,16,116,56
v_cmp_gt_u32 vcc, v19, v15
M 2,42,0
LC_ 122
PK 116,16,116,97
M 2,43,1
RV 252,22,5392
PO 118
M 1,30,0
WL 4
LO 17,17,119
LU 119,17
M 1,31,1
s_cbranch_vccnz .Lk1s2
.Lk1r2:
RA 236,1,12288
HS 25,119
M 0,18,0
LL 16,98
LL 123,99
M 0,19,1
s_add_u32 s42, s44, s63
LA 16
PK 119,17,117,56
M 0,20,2
s_addc_u32 s43, s45, 0
LC_ 123
PK 117,17,117,97
M 0,21,3
W1 24,0
PO 119
M 1,32,2
WL 5
LO 18,18,122
LU 122,18
M 1,33,3
RA 216,1,13312
HS 25,122
M 2,44,2
LL 17,100
LL 126,101
M 2,45,3
LA 17
PK 122,18,120,56
M 3,56,2
LC_ 126
PK 120,18,120,97
M 3,57,3
ST 116
M 4,68,2
WL 4
LO 16,16,123
LU 123,16
M 4,69,3
s_add_u32 s42, s44, s64
RA 220,1,14336
HS 25,123
M 5,80,2
LL 18,102
LL 127,103
M 5,81,3
s_addc_u32 s43, s45, 0
LA 18
PK 123,16,121,56
M 0,22,4
LC_ 127
PK 121,16,121,97
M 0,23,5
ST 120
M 1,34,4
WL 3
LO 17,17,126
LU 126,17
M 1,35,5
RA 224,1,15360
HS 25,126
M 2,46,4
LL 16,56
LL 104,57
M 2,47,5
LA 16
PK 126,17,124,56
M 3,58,4
LC_ 104
PK 124,17,124,97
M 3,59,5
ds_read_b64 v[14:15], v5 offset:0
PO 126
M 4,70,4
WL 4
LO 18,18,127
LU 127,18
M 4,71,5
s_add_u32 s42, s44, s65
RA 228,1,16384
HS 25,127
M 5,82,4
LL 17,58
LL 105,59
M 5,83,5
s_addc_u32 s43, s45, 0
LA 17
PK 127,18,125,56
Z 6,12,0
LC_ 105
PK 125,18,125,97
M 6,13,1
PO 127
v_subrev_u32 v5, s39, v5
Z 7,24,0
WL 4
LO 104,16,104
LU 16,104
M 7,25,1
RA 232,1,17408
HZ 24,16
Z 8,36,0
WL 4
LL 18,60
LL 108,61
M 8,37,1
s_mul_i32 s53, s50, 25600
LA 18
v_add_f32 v14, v14, v15
Z 9,48,0
s_add_u32 s40, %[wlo], s53
LC_ 108
v_add_f32_dpp v14, v14, v14 quad_perm:[1,0,3,2] row_mask:0xf bank_mask:0xf
M 9,49,1
s_addc_u32 s41, %[whi], 0
Z 10,60,0
WL 3
LO 105,17,105
LU 17,105
M 10,61,1
s_mov_b32 m0, s38
RA 236,1,18432
HS 24,17
Z 11,72,0
LL 16,62
LL 109,63
M 11,73,1
LA 16
ST 124
M 11,74,2
LC_ 109
DW 0
M 11,75,3
WV 14
v_add_f32_dpp v14, v14, v14 quad_perm:[2,3,0,1] row_mask:0xf bank_mask:0xf
HX 25
M 10,62,2
WL 3
LO 108,18,108
LU 18,108
W1 25,1024
M 10,63,3
s_add_u32 s37, s37, 32768
RA 216,1,19456
HS 24,18
v_readfirstlane_b32 s72, v11
M 9,50,2
s_cmp_eq_u32 s37, s75
LL 17,64
LL 112,65
v_mov_b32 v12, s72
M 9,51,3
s_cselect_b32 s37, s74, s37
LA 17
W2 12,12,16
M 8,38,2
LC_ 112
DW 1024
M 8,39,3
v_add_f32_dpp v14, v14, v14 row_half_mirror row_mask:0xf bank_mask:0xf
v_add_u32 v4, s39, v4
M 7,26,2
WL 5
LO 109,16,109
LU 16,109
M 7,27,3
RA 220,1,20480
HS 24,16
M 6,14,2
LL 18,66
LL 113,67
M 6,15,3
LA 18
DW 2048
M 6,16,4
LC_ 113
v_cvt_f64_f32 v[14:15], v14
M 6,17,5
s_mov_b64 exec, s[54:55]
SP
s_mov_b64 exec, -1
M 7,28,4
WL 3
LO 112,17,112
LU 17,112
M 7,29,5
s_mul_i32 s53, s51, 24
RA 224,1,21504
HS 24,17
M 8,40,4
s_bitcmp1_b32 s51, 0
)"
R"(LL 16,68
LL 116,69
M 8,41,5
s_addc_u32 s53, s53, 0
LA 16
M 9,52,4
s_lshl_b32 s53, s53, 11
LC_ 116
M 9,53,5
s_bitcmp1_b64 s[90:91], s51
M 10,64,4
WL 3
LO 113,18,113
LU 18,113
M 10,65,5
s_cselect_b64 s[44:45], s[84:85], s[82:83]
RA 228,1,22528
HS 24,18
M 11,76,4
LL 17,70
LL 117,71
M 11,77,5
s_add_u32 s44, s44, s53
LA 17
M 11,78,0
s_addc_u32 s45, s45, 0
LC_ 117
M 11,79,1
s_add_u32 s51, s51, %[d]
M 10,66,0
WL 3
LO 116,16,116
LU 16,116
M 10,67,1
s_cmp_eq_u32 s51, %[wrap]
RA 232,1,23552
HS 24,16
M 9,54,0
s_cselect_b32 s51, %[rst], s51
LL 18,72
LL 120,73
M 9,55,1
s_add_u32 s40, s40, 3072
LA 18
M 8,42,0
s_addc_u32 s41, s41, 0
LC_ 120
s_add_u32 m0, s38, 3072
M 8,43,1
DW 0
M 7,30,0
WL 3
LO 117,17,117
LU 17,117
M 7,31,1
RA 236,1,24576
HS 24,17
M 6,18,0
LL 16,74
LL 121,75
M 6,19,1
LA 16
DW 1024
M 6,20,2
LC_ 121
DW 2048
M 6,21,3
s_lshl_b32 s53, s52, 8
v_add_u32 v1, s37, v0
M 7,32,2
WL 3
LO 120,18,120
LU 18,120
M 7,33,3
s_add_u32 s48, %[alo], s53
RA 216,1,0
HS 24,18
M 8,44,2
s_addc_u32 s49, %[ahi], 0
LL 17,76
LL 124,77
M 8,45,3
s_add_u32 s52, s52, %[d]
LA 17
v_and_b32 v22, 0xfffffcff, v1
M 9,56,2
s_cmp_eq_u32 s52, %[wrap]
LC_ 124
M 9,57,3
s_cselect_b32 s52, %[rst], s52
DQ 3072
M 10,68,2
WL 3
LO 121,16,121
LU 16,121
M 10,69,3
s_add_u32 s50, s50, %[d]
RA 220,1,1024
HS 24,16
M 11,80,2
LL 18,78
LL 125,79
M 11,81,3
s_cmp_eq_u32 s50, %[wrap]
LA 18
RV 252,22,5120
M 6,22,4
s_cselect_b32 s50, %[rst], s50
LC_ 125
M 6,23,5
v_add_u32 v19, 1, v19
M 7,34,4
WL 4
LO 124,17,124
LU 17,124
M 7,35,5
s_add_u32 s38, s38, 32768
RA 224,1,2048
HS 24,17
M 8,46,4
s_cmp_eq_u32 s38, s77
LL 16,80
LL 106,81
M 8,47,5
s_cselect_b32 s38, s76, s38
LA 16
FP
M 9,58,4
s_sub_u32 s39, 0, s39
LC_ 106
M 9,59,5
M 10,70,4
WL 4
LO 125,18,125
LU 18,125
M 10,71,5
RA 228,1,3072
HS 24,18
M 11,82,4
LL 17,82
LL 107,83
M 11,83,5
LA 17
HX 24
Z 0,12,0
LC_ 107
M 0,13,1
Z 1,24,0
WL 3
LO 16,16,106
LU 106,16
M 1,25,1
RA 232,1,4096
HZ 25,106
Z 2,36,0
LL 18,84
LL 110,85
M 2,37,1
LA 18
PK 106,16,104,56
Z 3,48,0
LC_ 110
PK 104,16,104,97
M 3,49,1
Z 4,60,0
WL 3
LO 17,17,107
LU 107,17
M 4,61,1
s_add_u32 s42, s44, s60
RA 236,1,6144
HS 25,107
Z 5,72,0
LL 16,86
LL 111,87
M 5,73,1
s_addc_u32 s43, s45, 0
LA 16
PK 107,17,105,56
M 5,74,2
LC_ 111
PK 105,17,105,97
M 5,75,3
ST 104
M 4,62,2
WL 3
LO 18,18,110
LU 110,18
M 4,63,3
RA 216,1,7168
HS 25,110
M 3,50,2
LL 17,88
LL 114,89
M 3,51,3
LA 17
PK 110,18,108,56
M 2,38,2
LC_ 114
PK 108,18,108,97
M 2,39,3
FR
PO 110
M 1,26,2
WL 4
LO 16,16,111
LU 111,16
M 1,27,3
s_add_u32 s42, s44, s61
RA 220,1,8192
HS 25,111
M 0,14,2
LL 18,90
LL 115,91
M 0,15,3
s_addc_u32 s43, s45, 0
LA 18
PK 111,16,109,56
M 0,16,4
s_mov_b32 m0, %[pm]
LC_ 115
PK 109,16,109,97
M 0,17,5
DF
PO 111
M 1,28,4
WL 4
LO 17,17,114
LU 114,17
M 1,29,5
RA 224,1,9216
HS 25,114
M 2,40,4
LL 16,92
LL 118,93
M 2,41,5
LA 16
PK 114,17,112,56
M 3,52,4
LC_ 118
PK 112,17,112,97
M 3,53,5
ST 108
M 4,64,4
WL 3
LO 18,18,115
LU 115,18
M 4,65,5
s_add_u32 s42, s44, s62
RA 228,1,10240
HS 25,115
M 5,76,4
LL 17,94
LL 119,95
M 5,77,5
s_addc_u32 s43, s45, 0
LA 17
PK 115,18,113,56
M 5,78,0
LC_ 119
PK 113,18,113,97
M 5,79,1
ST 112
M 4,66,0
WL 3
LO 16,16,118
LU 118,16
M 4,67,1
RA 232,1,11264
HS 25,118
M 3,54,0
LL 18,96
LL 122,97
M 3,55,1
LA 18
PK 118,16,116,56
v_cmp_gt_u32 vcc, v19, v15
M 2,42,0
LC_ 122
PK 116,16,116,97
M 2,43,1
RV 252,22,5392
PO 118
M 1,30,0
WL 4
LO 17,17,119
LU 119,17
M 1,31,1
s_cbranch_vccnz .Lk1s3
.Lk1r3:
RA 236,1,12288
HS 25,119
M 0,18,0
LL 16,98
LL 123,99
M 0,19,1
s_add_u32 s42, s44, s63
LA 16
PK 119,17,117,56
M 0,20,2
s_addc_u32 s43, s45, 0
LC_ 123
PK 117,17,117,97
M 0,21,3
ds_read_b32 v20, v5 offset:4096
PO 119
M 1,32,2
WL 5
LO 18,18,122
LU 122,18
M 1,33,3
RA 216,1,13312
HS 25,122
M 2,44,2
LL 17,100
LL 126,101
M 2,45,3
LA 17
PK 122,18,120,56
M 3,56,2
LC_ 126
PK 120,18,120,97
M 3,57,3
M 4,68,2
WL 4
LO 16,16,123
LU 123,16
M 4,69,3
RA 220,1,14336
HS 25,123
M 5,80,2
LL 18,102
LL 127,103
M 5,81,3
LA 18
PK 123,16,121,56
M 0,22,4
LC_ 127
PK 121,16,121,97
W1 24,0
M 0,23,5
M 1,34,4
WL 4
LO 17,17,126
LU 126,17
M 1,35,5
RA 224,1,15360
HS 25,126
M 2,46,4
LL 16,56
LL 104,57
M 2,47,5
LA 16
PK 126,17,124,56
M 3,58,4
LC_ 104
PK 124,17,124,97
M 3,59,5
PO 126
v_readfirstlane_b32 s72, v20
M 4,70,4
WL 4
LO 18,18,127
LU 127,18
M 4,71,5
s_sub_u32 s53, 63, s72
RA 228,1,16384
HS 25,127
M 5,82,4
LL 17,58
LL 105,59
M 5,83,5
s_cmp_eq_u32 %[d], 1
LA 17
PK 127,18,125,56
Z 6,12,0
s_cselect_b32 s72, s72, s53
LC_ 105
PK 125,18,125,97
M 6,13,1
s_and_b32 s72, s72, 63
ds_read_b64 v[14:15], v5 offset:0
PO 127
Z 7,24,0
WL 4
LO 104,16,104
LU 16,104
M 7,25,1
s_add_u32 s53, s72, 128
RA 232,1,17408
HZ 24,16
Z 8,36,0
s_mul_i32 s53, s53, 2359296
LL 18,60
LL 108,61
M 8,37,1
s_add_u32 s86, %[hlo], s53
LA 18
v_subrev_u32 v5, s39, v5
Z 9,48,0
)"
R"(s_addc_u32 s87, %[hhi], 0
LC_ 108
HX 25
M 9,49,1
s_add_u32 s88, s86, 8192
Z 10,60,0
WL 4
LO 105,17,105
LU 17,105
M 10,61,1
s_addc_u32 s89, s87, 0
RA 236,1,18432
HS 24,17
Z 11,72,0
WL 4
LL 16,62
LL 109,63
M 11,73,1
s_and_b32 s92, %[hlo], 0x1fffff
LA 16
W1 25,1024
s_add_u32 s92, s92, s53
s_mov_b32 s93, 3072
ST 116
s_add_u32 s42, s44, s64
s_addc_u32 s43, s45, 0
s_mul_i32 s57, s72, 589824
s_add_u32 s57, s57, %[xs1]
s_lshl_b32 s72, s72, 1
s_add_u32 s72, s72, s96
s_lshl_b32 s53, s72, 6
s_add_u32 s53, s53, 128
s_cmp_lt_u32 %[ca0], 128
s_cselect_b32 s73, s53, %[ca0]
ST 120
v_mov_b32 v9, s73
v_mov_b32 v11, s92
M 11,74,2
s_add_u32 s42, s44, s65
LC_ 109
v_add_f32 v14, v14, v15
s_addc_u32 s43, s45, 0
v_mad_u32_u24 v11, v0, s93, v11
M 11,75,3
v_add_f32_dpp v14, v14, v14 quad_perm:[1,0,3,2] row_mask:0xf bank_mask:0xf
v_add_u32 v4, s39, v4
M 10,62,2
WL 4
LO 108,18,108
LU 18,108
M 10,63,3
RA 216,1,19456
HS 24,18
v_lshrrev_b32 v11, 21, v11
M 9,50,2
LL 17,64
LL 112,65
M 9,51,3
LA 17
v_add_f32_dpp v14, v14, v14 quad_perm:[2,3,0,1] row_mask:0xf bank_mask:0xf
M 8,38,2
LC_ 112
v_add_f32_dpp v14, v14, v14 row_half_mirror row_mask:0xf bank_mask:0xf
v_lshl_add_u32 v11, v11, 2, 64
M 8,39,3
s_mul_i32 s53, s50, 25600
v_cvt_f64_f32 v[14:15], v14
M 7,26,2
WL 3
LO 109,16,109
LU 16,109
s_add_u32 s40, %[wlo], s53
s_addc_u32 s41, %[whi], 0
s_mov_b32 m0, s38
M 7,27,3
RA 220,1,20480
HS 24,16
v_writelane_b32 v11, %[tsel], 63
M 6,14,2
LL 18,66
LL 113,67
ST 124
M 6,15,3
s_sub_u32 s53, s86, %[hlo]
LA 18
s_mov_b64 exec, s[54:55]
SP
s_mov_b64 exec, -1
M 6,16,4
s_lshr_b32 s53, s53, 2
LC_ 113
s_and_b32 s92, %[xlo], 0x1fffff
s_add_u32 s53, s53, s92
s_add_u32 s53, s53, 0x20048000
s_lshr_b32 s53, s53, 21
s_lshl_b32 s92, s53, 2
M 6,17,5
DW 0
v_writelane_b32 v11, s92, 48
M 7,28,4
WL 3
LO 112,17,112
LU 17,112
M 7,29,5
s_add_u32 s37, s37, 32768
RA 224,1,21504
HS 24,17
M 8,40,4
s_cmp_eq_u32 s37, s75
LL 16,68
LL 116,69
M 8,41,5
s_cselect_b32 s37, s74, s37
LA 16
global_load_dword v11, v11, s[68:69] offset:-2048
M 9,52,4
LC_ 116
DW 1024
M 9,53,5
DW 2048
M 10,64,4
WL 3
LO 113,18,113
LU 18,113
M 10,65,5
s_mul_i32 s53, s51, 24
RA 228,1,22528
HS 24,18
M 11,76,4
LL 17,70
LL 117,71
M 11,77,5
s_bitcmp1_b32 s51, 0
LA 17
M 11,78,0
s_addc_u32 s53, s53, 0
LC_ 117
M 11,79,1
s_lshl_b32 s53, s53, 11
M 10,66,0
WL 3
LO 116,16,116
LU 16,116
M 10,67,1
s_bitcmp1_b64 s[90:91], s51
RA 232,1,23552
HS 24,16
M 9,54,0
s_cselect_b64 s[44:45], s[84:85], s[82:83]
LL 18,72
LL 120,73
M 9,55,1
s_add_u32 s44, s44, s53
LA 18
s_addc_u32 s45, s45, 0
s_add_u32 s51, s51, %[d]
s_cmp_eq_u32 s51, %[wrap]
s_cselect_b32 s51, %[rst], s51
M 8,42,0
s_add_u32 s40, s40, 3072
LC_ 120
s_addc_u32 s41, s41, 0
s_add_u32 m0, s38, 3072
M 8,43,1
DW 0
M 7,30,0
WL 3
LO 117,17,117
LU 17,117
M 7,31,1
RA 236,1,24576
HS 24,17
M 6,18,0
LL 16,74
LL 121,75
M 6,19,1
LA 16
DW 1024
M 6,20,2
LC_ 121
DW 2048
M 6,21,3
s_lshl_b32 s53, s52, 8
v_add_u32 v1, s37, v0
M 7,32,2
WL 3
LO 120,18,120
LU 18,120
M 7,33,3
s_add_u32 s48, %[alo], s53
RA 216,1,0
HS 24,18
M 8,44,2
s_addc_u32 s49, %[ahi], 0
LL 17,76
LL 124,77
M 8,45,3
s_add_u32 s52, s52, %[d]
LA 17
v_and_b32 v22, 0xfffffcff, v1
M 9,56,2
s_cmp_eq_u32 s52, %[wrap]
LC_ 124
M 9,57,3
s_cselect_b32 s52, %[rst], s52
DQ 3072
M 10,68,2
WL 3
LO 121,16,121
LU 16,121
M 10,69,3
s_add_u32 s50, s50, %[d]
RA 220,1,1024
HS 24,16
M 11,80,2
LL 18,78
LL 125,79
M 11,81,3
s_cmp_eq_u32 s50, %[wrap]
LA 18
RV 252,22,5120
M 6,22,4
s_cselect_b32 s50, %[rst], s50
LC_ 125
M 6,23,5
WV 31
v_add_u32 v19, 1, v19
M 7,34,4
WL 4
LO 124,17,124
LU 17,124
M 7,35,5
s_add_u32 s38, s38, 32768
RA 224,1,2048
HS 24,17
M 8,46,4
s_cmp_eq_u32 s38, s77
LL 16,80
LL 106,81
M 8,47,5
s_cselect_b32 s38, s76, s38
LA 16
FP
M 9,58,4
s_sub_u32 s39, 0, s39
LC_ 106
M 9,59,5
M 10,70,4
WL 4
LO 125,18,125
LU 18,125
M 10,71,5
RA 228,1,3072
HS 24,18
M 11,82,4
LL 17,82
LL 107,83
M 11,83,5
LA 17
HX 24
Z 0,12,0
LC_ 107
M 0,13,1
Z 1,24,0
WL 3
LO 16,16,106
LU 106,16
M 1,25,1
RA 232,1,4096
HZ 25,106
Z 2,36,0
LL 18,84
LL 110,85
M 2,37,1
LA 18
PK 106,16,104,56
Z 3,48,0
LC_ 110
PK 104,16,104,97
M 3,49,1
Z 4,60,0
WL 3
LO 17,17,107
LU 107,17
M 4,61,1
s_add_u32 s42, s44, s60
RA 236,1,6144
HS 25,107
Z 5,72,0
LL 16,86
LL 111,87
M 5,73,1
s_addc_u32 s43, s45, 0
LA 16
PK 107,17,105,56
M 5,74,2
LC_ 111
PK 105,17,105,97
M 5,75,3
ST 104
M 4,62,2
WL 3
LO 18,18,110
LU 110,18
M 4,63,3
RA 216,1,7168
HS 25,110
M 3,50,2
LL 17,88
LL 114,89
M 3,51,3
LA 17
PK 110,18,108,56
M 2,38,2
LC_ 114
PK 108,18,108,97
M 2,39,3
FR
PO 110
M 1,26,2
WL 4
LO 16,16,111
LU 111,16
M 1,27,3
s_add_u32 s42, s44, s61
RA 220,1,8192
HS 25,111
M 0,14,2
LL 18,90
LL 115,91
M 0,15,3
s_addc_u32 s43, s45, 0
LA 18
PK 111,16,109,56
M 0,16,4
s_mov_b32 m0, %[pm]
LC_ 115
PK 109,16,109,97
M 0,17,5
DF
PO 111
M 1,28,4
WL 4
LO 17,17,114
LU 114,17
M 1,29,5
RA 224,1,9216
HS 25,114
M 2,40,4
LL 16,92
LL 118,93
M 2,41,5
LA 16
PK 114,17,112,56
M 3,52,4
LC_ 118
PK 112,17,112,97
M 3,53,5
ST 108
M 4,64,4
WL 3
LO 18,18,115
LU 115,18
M 4,65,5
s_add_u32 s42, s44, s62
RA 228,1,10240
HS 25,115
M 5,76,4
LL 17,94
LL 119,95
M 5,77,5
s_addc_u32 s43, s45, 0
LA 17
PK 115,18,113,56
M 5,78,0
LC_ 119
PK 113,18,113,97
M 5,79,1
ST 112
M 4,66,0
WL 3
LO 16,16,118
LU 118,16
M 4,67,1
RA 232,1,11264
HS 25,118
M 3,54,0
LL 18,96
LL 122,97
M 3,55,1
LA 18
PK 118,16,116,56
v_cmp_gt_u32 vcc, v19, v15
M 2,42,0
LC_ 122
PK 116,16,116,97
M 2,43,1
RV 252,22,5392
PO 118
M 1,30,0
)"
R"(WL 4
LO 17,17,119
LU 119,17
M 1,31,1
s_cbranch_vccnz .Lk1s4
.Lk1r4:
RA 236,1,12288
HS 25,119
M 0,18,0
LL 16,98
LL 123,99
M 0,19,1
s_add_u32 s42, s44, s63
LA 16
PK 119,17,117,56
M 0,20,2
s_addc_u32 s43, s45, 0
LC_ 123
PK 117,17,117,97
M 0,21,3
W1 24,0
PO 119
M 1,32,2
WL 5
LO 18,18,122
LU 122,18
M 1,33,3
RA 216,1,13312
HS 25,122
M 2,44,2
LL 17,100
LL 126,101
M 2,45,3
LA 17
PK 122,18,120,56
M 3,56,2
LC_ 126
PK 120,18,120,97
M 3,57,3
ST 116
M 4,68,2
WL 4
LO 16,16,123
LU 123,16
M 4,69,3
s_add_u32 s42, s44, s64
RA 220,1,14336
HS 25,123
M 5,80,2
LL 18,102
LL 127,103
M 5,81,3
s_addc_u32 s43, s45, 0
LA 18
PK 123,16,121,56
M 0,22,4
LC_ 127
PK 121,16,121,97
M 0,23,5
ST 120
M 1,34,4
WL 3
LO 17,17,126
LU 126,17
M 1,35,5
RA 224,1,15360
HS 25,126
M 2,46,4
LL 16,56
LL 104,57
M 2,47,5
LA 16
PK 126,17,124,56
M 3,58,4
LC_ 104
PK 124,17,124,97
M 3,59,5
ds_read_b64 v[14:15], v5 offset:0
PO 126
M 4,70,4
WL 4
LO 18,18,127
LU 127,18
M 4,71,5
s_add_u32 s42, s44, s65
RA 228,1,16384
HS 25,127
M 5,82,4
LL 17,58
LL 105,59
M 5,83,5
s_addc_u32 s43, s45, 0
LA 17
PK 127,18,125,56
Z 6,12,0
LC_ 105
PK 125,18,125,97
M 6,13,1
PO 127
v_subrev_u32 v5, s39, v5
Z 7,24,0
WL 4
LO 104,16,104
LU 16,104
M 7,25,1
RA 232,1,17408
HZ 24,16
Z 8,36,0
WL 4
LL 18,60
LL 108,61
M 8,37,1
s_mul_i32 s53, s50, 25600
LA 18
v_add_f32 v14, v14, v15
Z 9,48,0
s_add_u32 s40, %[wlo], s53
LC_ 108
v_add_f32_dpp v14, v14, v14 quad_perm:[1,0,3,2] row_mask:0xf bank_mask:0xf
M 9,49,1
s_addc_u32 s41, %[whi], 0
Z 10,60,0
WL 3
LO 105,17,105
LU 17,105
M 10,61,1
s_mov_b32 m0, s38
RA 236,1,18432
HS 24,17
Z 11,72,0
LL 16,62
LL 109,63
M 11,73,1
LA 16
ST 124
M 11,74,2
LC_ 109
DW 0
M 11,75,3
v_add_f32_dpp v14, v14, v14 quad_perm:[2,3,0,1] row_mask:0xf bank_mask:0xf
HX 25
M 10,62,2
WL 3
LO 108,18,108
LU 18,108
W1 25,1024
M 10,63,3
s_add_u32 s37, s37, 32768
RA 216,1,19456
HS 24,18
M 9,50,2
s_cmp_eq_u32 s37, s75
LL 17,64
LL 112,65
M 9,51,3
s_cselect_b32 s37, s74, s37
LA 17
DW 1024
M 8,38,2
LC_ 112
v_add_f32_dpp v14, v14, v14 row_half_mirror row_mask:0xf bank_mask:0xf
M 8,39,3
v_cvt_f64_f32 v[14:15], v14
v_add_u32 v4, s39, v4
M 7,26,2
WL 4
LO 109,16,109
LU 16,109
M 7,27,3
RA 220,1,20480
HS 24,16
M 6,14,2
LL 18,66
LL 113,67
M 6,15,3
LA 18
s_mov_b64 exec, s[54:55]
SP
s_mov_b64 exec, -1
M 6,16,4
LC_ 113
DW 2048
M 6,17,5
s_and_b32 s53, %[w], 1
M 7,28,4
WL 3
LO 112,17,112
LU 17,112
M 7,29,5
s_lshl_b32 s53, s53, 10
RA 224,1,21504
HS 24,17
M 8,40,4
s_lshr_b32 s72, %[w], 1
LL 16,68
LL 116,69
M 8,41,5
s_mul_i32 s72, s72, 32768
LA 16
M 9,52,4
s_add_u32 s72, s72, s53
LC_ 116
M 9,53,5
s_add_u32 s72, s72, 128000
M 10,64,4
WL 3
LO 113,18,113
LU 18,113
M 10,65,5
s_movk_i32 s53, 0x78
RA 228,1,22528
HS 24,18
M 11,76,4
LL 17,70
LL 117,71
M 11,77,5
LA 17
v_lshrrev_b32 v12, 5, v0
M 11,78,0
LC_ 117
v_and_b32 v12, 6, v12
M 11,79,1
v_bfe_u32 v12, s53, v12, 2
M 10,66,0
WL 3
LO 116,16,116
LU 16,116
M 10,67,1
RA 232,1,23552
HS 24,16
v_lshrrev_b32 v20, 8, v0
v_xor_b32 v20, v12, v20
v_lshl_add_u32 v20, v20, 4, s72
v_and_b32 v12, 0xf0, v0
v_lshl_add_u32 v20, v12, 2, v20
v_add_u32 v12, s72, v0
M 9,54,0
s_mul_i32 s53, s51, 24
LL 18,72
LL 120,73
M 9,55,1
s_bitcmp1_b32 s51, 0
LA 18
s_addc_u32 s53, s53, 0
s_lshl_b32 s53, s53, 11
s_bitcmp1_b64 s[90:91], s51
s_cselect_b64 s[44:45], s[84:85], s[82:83]
s_add_u32 s44, s44, s53
s_addc_u32 s45, s45, 0
s_add_u32 s51, s51, %[d]
s_cmp_eq_u32 s51, %[wrap]
s_cselect_b32 s51, %[rst], s51
M 8,42,0
s_add_u32 s40, s40, 3072
LC_ 120
s_addc_u32 s41, s41, 0
s_add_u32 m0, s38, 3072
M 8,43,1
DW 0
M 7,30,0
WL 3
LO 117,17,117
LU 17,117
M 7,31,1
RA 236,1,24576
HS 24,17
M 6,18,0
LL 16,74
LL 121,75
M 6,19,1
LA 16
DW 1024
M 6,20,2
LC_ 121
DW 2048
M 6,21,3
s_lshl_b32 s53, s52, 8
v_add_u32 v1, s37, v0
M 7,32,2
WL 3
LO 120,18,120
LU 18,120
M 7,33,3
s_add_u32 s48, %[alo], s53
RA 216,1,0
HS 24,18
M 8,44,2
s_addc_u32 s49, %[ahi], 0
LL 17,76
LL 124,77
M 8,45,3
s_add_u32 s52, s52, %[d]
LA 17
v_and_b32 v22, 0xfffffcff, v1
M 9,56,2
s_cmp_eq_u32 s52, %[wrap]
LC_ 124
M 9,57,3
s_cselect_b32 s52, %[rst], s52
DQ 3072
M 10,68,2
WL 3
LO 121,16,121
LU 16,121
M 10,69,3
s_add_u32 s50, s50, %[d]
RA 220,1,1024
HS 24,16
M 11,80,2
LL 18,78
LL 125,79
M 11,81,3
s_cmp_eq_u32 s50, %[wrap]
LA 18
RV 252,22,5120
M 6,22,4
s_cselect_b32 s50, %[rst], s50
LC_ 125
M 6,23,5
WV 31
v_add_u32 v19, 1, v19
M 7,34,4
WL 4
LO 124,17,124
LU 17,124
M 7,35,5
s_add_u32 s38, s38, 32768
RA 224,1,2048
HS 24,17
M 8,46,4
s_cmp_eq_u32 s38, s77
LL 16,80
LL 106,81
M 8,47,5
s_cselect_b32 s38, s76, s38
LA 16
FP
M 9,58,4
s_sub_u32 s39, 0, s39
LC_ 106
M 9,59,5
M 10,70,4
WL 4
LO 125,18,125
LU 18,125
M 10,71,5
RA 228,1,3072
HS 24,18
M 11,82,4
LL 17,82
LL 107,83
M 11,83,5
LA 17
HX 24
Z 0,12,0
LC_ 107
M 0,13,1
Z 1,24,0
WL 3
LO 16,16,106
LU 106,16
M 1,25,1
RA 232,1,4096
HZ 25,106
Z 2,36,0
LL 18,84
LL 110,85
M 2,37,1
LA 18
PK 106,16,104,56
Z 3,48,0
LC_ 110
PK 104,16,104,97
M 3,49,1
WV 21
v_bcnt_u32_b32 v11, v11, 0
Z 4,60,0
WL 3
LO 17,17,107
LU 107,17
M 4,61,1
RA 236,1,6144
HS 25,107
Z 5,72,0
LL 16,86
LL 111,87
M 5,73,1
LA 16
PK 107,17,105,56
M 5,74,2
LC_ 111
PK 105,17,105,97
M 5,75,3
M 4,62,2
WL 3
LO 18,18,110
LU 110,18
M 4,63,3
RA 216,1,7168
HS 25,110
)"
R"(M 3,50,2
LL 17,88
LL 114,89
M 3,51,3
LA 17
PK 110,18,108,56
M 2,38,2
LC_ 114
PK 108,18,108,97
M 2,39,3
FR
PO 110
v_cmp_lt_u32 s[92:93], 2, v11
M 1,26,2
WL 4
LO 16,16,111
LU 111,16
M 1,27,3
s_nop 0
RA 220,1,8192
HS 25,111
M 0,14,2
LL 18,90
LL 115,91
M 0,15,3
s_add_u32 s42, s44, s60
LA 18
PK 111,16,109,56
s_addc_u32 s43, s45, 0
s_sub_u32 s53, 0, s96
s_xor_b32 s92, s92, s53
s_xor_b32 s93, s93, s53
M 0,16,4
s_mul_i32 s53, %[d], 0
LC_ 115
PK 109,16,109,97
M 0,17,5
s_add_u32 s53, s53, s57
PO 111
M 1,28,4
WL 4
LO 17,17,114
LU 114,17
M 1,29,5
s_add_u32 s46, %[xlo], s53
RA 224,1,9216
HS 25,114
s_addc_u32 s47, %[xhi], 0
s_bitcmp1_b64 s[92:93], 48
s_cselect_b32 s58, 16, 0
s_lshr_b32 s58, 0xfe987610, s58
v_lshrrev_b32 v3, 4, v0
v_and_b32 v3, 12, v3
v_bfe_u32 v3, s58, v3, 4
v_bfe_u32 v11, v0, 4, 2
v_lshl_add_u32 v3, v3, 2, v11
v_mul_u32_u24 v3, 0x300, v3
v_lshrrev_b32 v11, 8, v0
v_lshl_add_u32 v3, v11, 4, v3
M 2,40,4
LL 16,92
LL 118,93
ST 104
M 2,41,5
s_add_u32 s42, s44, s61
LA 16
PK 114,17,112,56
M 3,52,4
s_addc_u32 s43, s45, 0
LC_ 118
PK 112,17,112,97
M 3,53,5
s_mov_b32 m0, %[pm]
M 4,64,4
WL 3
LO 18,18,115
LU 115,18
M 4,65,5
RA 228,1,10240
HS 25,115
M 5,76,4
LL 17,94
LL 119,95
M 5,77,5
LA 17
PK 115,18,113,56
M 5,78,0
LC_ 119
PK 113,18,113,97
M 5,79,1
DF
M 4,66,0
WL 3
LO 16,16,118
LU 118,16
M 4,67,1
RA 232,1,11264
HS 25,118
M 3,54,0
LL 18,96
LL 122,97
M 3,55,1
LA 18
PK 118,16,116,56
v_cmp_gt_u32 vcc, v19, v15
M 2,42,0
LC_ 122
PK 116,16,116,97
M 2,43,1
RV 252,22,5392
PO 118
M 1,30,0
WL 4
LO 17,17,119
LU 119,17
M 1,31,1
s_cbranch_vccnz .Lk1s5
.Lk1r5:
RA 236,1,12288
HS 25,119
M 0,18,0
LL 16,98
LL 123,99
ST 108
XL 0,0
M 0,19,1
s_add_u32 s42, s44, s62
LA 16
PK 119,17,117,56
s_addc_u32 s43, s45, 0
M 0,20,2
LC_ 123
PK 117,17,117,97
M 0,21,3
W1 24,0
PO 119
M 1,32,2
WL 5
LO 18,18,122
LU 122,18
M 1,33,3
RA 216,1,13312
HS 25,122
M 2,44,2
LL 17,100
LL 126,101
ST 112
M 2,45,3
s_add_u32 s42, s44, s63
LA 17
PK 122,18,120,56
M 3,56,2
s_addc_u32 s43, s45, 0
LC_ 126
PK 120,18,120,97
M 3,57,3
M 4,68,2
WL 4
LO 16,16,123
LU 123,16
M 4,69,3
RA 220,1,14336
HS 25,123
M 5,80,2
LL 18,102
LL 127,103
M 5,81,3
LA 18
PK 123,16,121,56
M 0,22,4
LC_ 127
PK 121,16,121,97
M 0,23,5
M 1,34,4
WL 3
LO 17,17,126
LU 126,17
M 1,35,5
RA 224,1,15360
HS 25,126
M 2,46,4
LL 16,56
LL 104,57
M 2,47,5
LA 16
XL 1,64
PK 126,17,124,56
M 3,58,4
LC_ 104
PK 124,17,124,97
M 3,59,5
ST 116
PO 126
M 4,70,4
WL 3
LO 18,18,127
LU 127,18
M 4,71,5
s_add_u32 s42, s44, s64
RA 228,1,16384
HS 25,127
M 5,82,4
LL 17,58
LL 105,59
M 5,83,5
s_addc_u32 s43, s45, 0
LA 17
PK 127,18,125,56
Z 6,12,0
LC_ 105
PK 125,18,125,97
M 6,13,1
ST 120
PO 127
Z 7,24,0
WL 3
LO 104,16,104
LU 16,104
M 7,25,1
s_add_u32 s42, s44, s65
RA 232,1,17408
HZ 24,16
Z 8,36,0
s_addc_u32 s43, s45, 0
LL 18,60
LL 108,61
M 8,37,1
LA 18
ds_read_b64 v[14:15], v5 offset:0
Z 9,48,0
LC_ 108
XL 2,128
M 9,49,1
s_mul_i32 s53, s50, 25600
Z 10,60,0
WL 4
LO 105,17,105
LU 17,105
M 10,61,1
s_add_u32 s40, %[wlo], s53
RA 236,1,18432
HS 24,17
Z 11,72,0
LL 16,62
LL 109,63
M 11,73,1
s_addc_u32 s41, %[whi], 0
LA 16
v_subrev_u32 v5, s39, v5
M 11,74,2
s_mov_b32 m0, s38
LC_ 109
ST 124
M 11,75,3
DW 0
HX 25
M 10,62,2
WL 3
LO 108,18,108
LU 18,108
W1 25,1024
M 10,63,3
s_add_u32 s37, s37, 32768
RA 216,1,19456
HS 24,18
M 9,50,2
s_cmp_eq_u32 s37, s75
LL 17,64
LL 112,65
M 9,51,3
s_cselect_b32 s37, s74, s37
LA 17
XL 3,192
M 8,38,2
LC_ 112
DW 1024
M 8,39,3
v_add_f32 v14, v14, v15
s_nop 1
v_add_f32_dpp v14, v14, v14 quad_perm:[1,0,3,2] row_mask:0xf bank_mask:0xf
v_add_u32 v4, s39, v4
M 7,26,2
WL 4
LO 109,16,109
LU 16,109
M 7,27,3
RA 220,1,20480
HS 24,16
v_add_f32_dpp v14, v14, v14 quad_perm:[2,3,0,1] row_mask:0xf bank_mask:0xf
M 6,14,2
LL 18,66
LL 113,67
M 6,15,3
LA 18
DW 2048
M 6,16,4
LC_ 113
v_add_f32_dpp v14, v14, v14 row_half_mirror row_mask:0xf bank_mask:0xf
M 6,17,5
v_cvt_f64_f32 v[14:15], v14
M 7,28,4
WL 3
LO 112,17,112
LU 17,112
M 7,29,5
s_mul_i32 s53, s51, 24
RA 224,1,21504
HS 24,17
M 8,40,4
s_bitcmp1_b32 s51, 0
LL 16,68
LL 116,69
M 8,41,5
s_addc_u32 s53, s53, 0
LA 16
s_mov_b64 exec, s[54:55]
SP
s_mov_b64 exec, -1
M 9,52,4
s_lshl_b32 s53, s53, 11
LC_ 116
M 9,53,5
s_bitcmp1_b64 s[90:91], s51
M 10,64,4
WL 3
LO 113,18,113
LU 18,113
M 10,65,5
s_cselect_b64 s[44:45], s[84:85], s[82:83]
RA 228,1,22528
HS 24,18
M 11,76,4
LL 17,70
LL 117,71
M 11,77,5
s_add_u32 s44, s44, s53
LA 17
M 11,78,0
s_addc_u32 s45, s45, 0
LC_ 117
M 11,79,1
s_add_u32 s51, s51, %[d]
M 10,66,0
WL 3
LO 116,16,116
LU 16,116
M 10,67,1
s_cmp_eq_u32 s51, %[wrap]
RA 232,1,23552
HS 24,16
M 9,54,0
s_cselect_b32 s51, %[rst], s51
LL 18,72
LL 120,73
M 9,55,1
s_add_u32 s40, s40, 3072
LA 18
M 8,42,0
s_addc_u32 s41, s41, 0
LC_ 120
s_add_u32 m0, s38, 3072
M 8,43,1
DW 0
M 7,30,0
WL 3
LO 117,17,117
LU 17,117
M 7,31,1
RA 236,1,24576
HS 24,17
M 6,18,0
LL 16,74
LL 121,75
M 6,19,1
LA 16
XL 4,256
M 6,20,2
LC_ 121
DW 1024
M 6,21,3
DW 2048
v_add_u32 v1, s37, v0
M 7,32,2
WL 3
LO 120,18,120
LU 18,120
M 7,33,3
s_lshl_b32 s53, s52, 8
RA 216,1,0
HS 24,18
M 8,44,2
s_add_u32 s48, %[alo], s53
LL 17,76
LL 124,77
M 8,45,3
s_addc_u32 s49, %[ahi], 0
LA 17
v_and_b32 v22, 0xfffffcff, v1
M 9,56,2
s_add_u32 s52, s52, %[d]
)"
R"(LC_ 124
M 9,57,3
s_cmp_eq_u32 s52, %[wrap]
M 10,68,2
WL 3
LO 121,16,121
LU 16,121
M 10,69,3
s_cselect_b32 s52, %[rst], s52
RA 220,1,1024
HS 24,16
M 11,80,2
LL 18,78
LL 125,79
M 11,81,3
LA 18
RV 252,22,5120
M 6,22,4
LC_ 125
DQ 3072
M 6,23,5
XL 5,320
M 7,34,4
WL 4
LO 124,17,124
LU 17,124
M 7,35,5
s_add_u32 s50, s50, %[d]
RA 224,1,2048
HS 24,17
M 8,46,4
s_cmp_eq_u32 s50, %[wrap]
LL 16,80
LL 106,81
M 8,47,5
s_cselect_b32 s50, %[rst], s50
LA 16
v_add_u32 v19, 1, v19
M 9,58,4
s_add_u32 s38, s38, 32768
LC_ 106
M 9,59,5
M 10,70,4
WL 3
LO 125,18,125
LU 18,125
M 10,71,5
RA 228,1,3072
HS 24,18
M 11,82,4
LL 17,82
LL 107,83
M 11,83,5
LA 17
HX 24
Z 0,12,0
s_cmp_eq_u32 s38, s77
LC_ 107
M 0,13,1
s_cselect_b32 s38, s76, s38
WV 36
FP
Z 1,24,0
WL 4
LO 16,16,106
LU 106,16
M 1,25,1
s_sub_u32 s39, 0, s39
RA 232,1,4096
HZ 25,106
Z 2,36,0
LL 18,84
LL 110,85
M 2,37,1
LA 18
PK 106,16,104,56
Z 3,48,0
LC_ 110
PK 104,16,104,97
M 3,49,1
XL 6,384
Z 4,60,0
WL 4
LO 17,17,107
LU 107,17
M 4,61,1
s_add_u32 s42, s44, s60
RA 236,1,6144
HS 25,107
Z 5,72,0
LL 16,86
LL 111,87
M 5,73,1
s_addc_u32 s43, s45, 0
LA 16
PK 107,17,105,56
M 5,74,2
LC_ 111
PK 105,17,105,97
M 5,75,3
ST 104
M 4,62,2
WL 3
LO 18,18,110
LU 110,18
M 4,63,3
RA 216,1,7168
HS 25,110
M 3,50,2
LL 17,88
LL 114,89
M 3,51,3
LA 17
PK 110,18,108,56
M 2,38,2
LC_ 114
PK 108,18,108,97
M 2,39,3
FR
PO 110
M 1,26,2
WL 4
LO 16,16,111
LU 111,16
M 1,27,3
RA 220,1,8192
HS 25,111
M 0,14,2
LL 18,90
LL 115,91
M 0,15,3
LA 18
PK 111,16,109,56
M 0,16,4
LC_ 115
PK 109,16,109,97
M 0,17,5
XL 7,448
PO 111
M 1,28,4
WL 4
LO 17,17,114
LU 114,17
M 1,29,5
s_add_u32 s42, s44, s61
RA 224,1,9216
HS 25,114
M 2,40,4
s_addc_u32 s43, s45, 0
LL 16,92
LL 118,93
M 2,41,5
s_mov_b32 m0, %[pm]
LA 16
PK 114,17,112,56
M 3,52,4
LC_ 118
PK 112,17,112,97
M 3,53,5
M 4,64,4
WL 3
LO 18,18,115
LU 115,18
M 4,65,5
RA 228,1,10240
HS 25,115
M 5,76,4
LL 17,94
LL 119,95
M 5,77,5
LA 17
PK 115,18,113,56
M 5,78,0
LC_ 119
PK 113,18,113,97
M 5,79,1
DF
M 4,66,0
WL 3
LO 16,16,118
LU 118,16
M 4,67,1
RA 232,1,11264
HS 25,118
M 3,54,0
LL 18,96
LL 122,97
M 3,55,1
LA 18
PK 118,16,116,56
v_cmp_gt_u32 vcc, v19, v15
M 2,42,0
LC_ 122
PK 116,16,116,97
M 2,43,1
RV 252,22,5392
PO 118
M 1,30,0
WL 4
LO 17,17,119
LU 119,17
M 1,31,1
s_cbranch_vccnz .Lk1s6
.Lk1r6:
RA 236,1,12288
HS 25,119
M 0,18,0
LL 16,98
LL 123,99
ST 108
M 0,19,1
s_add_u32 s42, s44, s62
LA 16
PK 119,17,117,56
s_addc_u32 s43, s45, 0
M 0,20,2
LC_ 123
PK 117,17,117,97
M 0,21,3
W1 24,0
PO 119
M 1,32,2
WL 5
LO 18,18,122
LU 122,18
M 1,33,3
RA 216,1,13312
HS 25,122
M 2,44,2
LL 17,100
LL 126,101
ST 112
M 2,45,3
LA 17
PK 122,18,120,56
M 3,56,2
LC_ 126
XL 8,512
PK 120,18,120,97
M 3,57,3
s_add_u32 s42, s44, s63
M 4,68,2
WL 4
LO 16,16,123
LU 123,16
M 4,69,3
s_addc_u32 s43, s45, 0
RA 220,1,14336
HS 25,123
M 5,80,2
LL 18,102
LL 127,103
M 5,81,3
LA 18
PK 123,16,121,56
M 0,22,4
LC_ 127
PK 121,16,121,97
M 0,23,5
ST 116
M 1,34,4
WL 3
LO 17,17,126
LU 126,17
M 1,35,5
s_add_u32 s42, s44, s64
RA 224,1,15360
HS 25,126
M 2,46,4
s_addc_u32 s43, s45, 0
LL 16,56
LL 104,57
M 2,47,5
LA 16
PK 126,17,124,56
M 3,58,4
LC_ 104
PK 124,17,124,97
M 3,59,5
XL 9,576
PO 126
M 4,70,4
WL 3
LO 18,18,127
LU 127,18
M 4,71,5
RA 228,1,16384
HS 25,127
M 5,82,4
LL 17,58
LL 105,59
M 5,83,5
LA 17
PK 127,18,125,56
Z 6,12,0
LC_ 105
PK 125,18,125,97
M 6,13,1
ST 120
PO 127
Z 7,24,0
WL 3
LO 104,16,104
LU 16,104
M 7,25,1
s_add_u32 s42, s44, s65
RA 232,1,17408
HZ 24,16
Z 8,36,0
s_addc_u32 s43, s45, 0
LL 18,60
LL 108,61
M 8,37,1
s_mul_i32 s53, s50, 25600
LA 18
ds_read_b64 v[14:15], v5 offset:0
Z 9,48,0
s_add_u32 s40, %[wlo], s53
LC_ 108
v_subrev_u32 v5, s39, v5
M 9,49,1
s_addc_u32 s41, %[whi], 0
Z 10,60,0
WL 4
LO 105,17,105
LU 17,105
M 10,61,1
s_mov_b32 m0, s38
RA 236,1,18432
HS 24,17
Z 11,72,0
LL 16,62
LL 109,63
M 11,73,1
LA 16
XL 10,640
M 11,74,2
LC_ 109
ST 124
M 11,75,3
DW 0
HX 25
M 10,62,2
WL 3
LO 108,18,108
LU 18,108
W1 25,1024
M 10,63,3
s_add_u32 s37, s37, 32768
RA 216,1,19456
HS 24,18
M 9,50,2
s_cmp_eq_u32 s37, s75
LL 17,64
LL 112,65
M 9,51,3
s_cselect_b32 s37, s74, s37
LA 17
DW 1024
M 8,38,2
LC_ 112
v_add_f32 v14, v14, v15
s_nop 1
v_add_f32_dpp v14, v14, v14 quad_perm:[1,0,3,2] row_mask:0xf bank_mask:0xf
M 8,39,3
s_nop 0
v_add_f32_dpp v14, v14, v14 quad_perm:[2,3,0,1] row_mask:0xf bank_mask:0xf
v_add_u32 v4, s39, v4
M 7,26,2
WL 4
LO 109,16,109
LU 16,109
M 7,27,3
RA 220,1,20480
HS 24,16
M 6,14,2
LL 18,66
LL 113,67
M 6,15,3
LA 18
DW 2048
M 6,16,4
LC_ 113
XL 11,704
v_add_f32_dpp v14, v14, v14 row_half_mirror row_mask:0xf bank_mask:0xf
M 6,17,5
v_cvt_f64_f32 v[14:15], v14
M 7,28,4
WL 3
LO 112,17,112
LU 17,112
M 7,29,5
s_mul_i32 s53, s51, 24
RA 224,1,21504
HS 24,17
M 8,40,4
s_bitcmp1_b32 s51, 0
LL 16,68
LL 116,69
M 8,41,5
)"
R"(s_addc_u32 s53, s53, 0
LA 16
s_mov_b64 exec, s[54:55]
SP
s_mov_b64 exec, -1
M 9,52,4
s_lshl_b32 s53, s53, 11
LC_ 116
M 9,53,5
s_bitcmp1_b64 s[90:91], s51
M 10,64,4
WL 3
LO 113,18,113
LU 18,113
M 10,65,5
s_cselect_b64 s[44:45], s[84:85], s[82:83]
RA 228,1,22528
HS 24,18
M 11,76,4
LL 17,70
LL 117,71
M 11,77,5
s_add_u32 s44, s44, s53
LA 17
M 11,78,0
s_addc_u32 s45, s45, 0
LC_ 117
M 11,79,1
s_add_u32 s51, s51, %[d]
M 10,66,0
WL 3
LO 116,16,116
LU 16,116
M 10,67,1
s_cmp_eq_u32 s51, %[wrap]
RA 232,1,23552
HS 24,16
M 9,54,0
s_cselect_b32 s51, %[rst], s51
LL 18,72
LL 120,73
M 9,55,1
s_add_u32 s40, s40, 3072
LA 18
M 8,42,0
s_addc_u32 s41, s41, 0
LC_ 120
s_add_u32 m0, s38, 3072
M 8,43,1
DW 0
M 7,30,0
WL 3
LO 117,17,117
LU 17,117
M 7,31,1
RA 236,1,24576
HS 24,17
M 6,18,0
LL 16,74
LL 121,75
M 6,19,1
LA 16
DW 1024
M 6,20,2
s_bitcmp1_b64 s[92:93], 48
LC_ 121
M 6,21,3
s_cselect_b32 s58, 16, 0
v_add_u32 v1, s37, v0
M 7,32,2
WL 3
LO 120,18,120
LU 18,120
M 7,33,3
s_lshr_b32 s58, 0xda43cb52, s58
RA 216,1,0
HS 24,18
M 8,44,2
LL 17,76
LL 124,77
M 8,45,3
LA 17
v_and_b32 v22, 0xfffffcff, v1
M 9,56,2
LC_ 124
v_and_b32 v11, 0x1f0, v0
M 9,57,3
v_min_u32 v11, 368, v11
M 10,68,2
WL 3
LO 121,16,121
LU 16,121
M 10,69,3
RA 220,1,1024
HS 24,16
M 11,80,2
LL 18,78
LL 125,79
M 11,81,3
LA 18
RV 252,22,5120
DW 2048
v_lshlrev_b32 v11, 3, v11
M 6,22,4
s_lshl_b32 s53, s52, 8
LC_ 125
M 6,23,5
s_add_u32 s48, %[alo], s53
M 7,34,4
WL 4
LO 124,17,124
LU 17,124
M 7,35,5
s_addc_u32 s49, %[ahi], 0
RA 224,1,2048
HS 24,17
M 8,46,4
s_add_u32 s52, s52, %[d]
LL 16,80
LL 106,81
s_cmp_eq_u32 s52, %[wrap]
s_cselect_b32 s52, %[rst], s52
M 8,47,5
LA 16
DQ 3072
M 9,58,4
s_add_u32 s50, s50, %[d]
LC_ 106
M 9,59,5
M 10,70,4
WL 3
LO 125,18,125
LU 18,125
M 10,71,5
RA 228,1,3072
HS 24,18
M 11,82,4
LL 17,82
LL 107,83
M 11,83,5
LA 17
HX 24
s_cmp_eq_u32 s50, %[wrap]
s_cselect_b32 s50, %[rst], s50
Z 0,12,0
LC_ 107
v_add_u32 v19, 1, v19
M 0,13,1
s_add_u32 s38, s38, 32768
Z 1,24,0
WL 3
LO 16,16,106
LU 106,16
M 1,25,1
s_cmp_eq_u32 s38, s77
RA 232,1,4096
HZ 25,106
Z 2,36,0
s_cselect_b32 s38, s76, s38
LL 18,84
LL 110,85
M 2,37,1
LA 18
PK 106,16,104,56
Z 3,48,0
LC_ 110
PK 104,16,104,97
M 3,49,1
WV 42
FP
s_sub_u32 s39, 0, s39
Z 4,60,0
WL 4
LO 17,17,107
LU 107,17
M 4,61,1
s_add_u32 s42, s44, s60
RA 236,1,6144
HS 25,107
Z 5,72,0
LL 16,86
LL 111,87
M 5,73,1
s_addc_u32 s43, s45, 0
LA 16
PK 107,17,105,56
M 5,74,2
LC_ 111
PK 105,17,105,97
M 5,75,3
ST 104
M 4,62,2
WL 4
LO 18,18,110
LU 110,18
M 4,63,3
RA 216,1,7168
HS 25,110
M 3,50,2
LL 17,88
LL 114,89
M 3,51,3
LA 17
PK 110,18,108,56
M 2,38,2
LC_ 114
PK 108,18,108,97
M 2,39,3
FR
PO 110
M 1,26,2
WL 4
LO 16,16,111
LU 111,16
M 1,27,3
s_add_u32 s42, s44, s61
RA 220,1,8192
HS 25,111
M 0,14,2
LL 18,90
LL 115,91
M 0,15,3
s_addc_u32 s43, s45, 0
LA 18
PK 111,16,109,56
M 0,16,4
s_mov_b32 m0, %[pm]
LC_ 115
PK 109,16,109,97
M 0,17,5
DF
PO 111
M 1,28,4
WL 4
LO 17,17,114
LU 114,17
M 1,29,5
RA 224,1,9216
HS 25,114
M 2,40,4
LL 16,92
LL 118,93
M 2,41,5
LA 16
PK 114,17,112,56
M 3,52,4
LC_ 118
PK 112,17,112,97
M 3,53,5
WL 6
ST 108
v_cmp_gt_u32 vcc, v19, v15
M 4,64,4
WL 3
LO 18,18,115
LU 115,18
M 4,65,5
s_add_u32 s42, s44, s62
RA 228,1,10240
HS 25,115
M 5,76,4
LL 17,94
LL 119,95
M 5,77,5
s_cbranch_vccnz .Lk1s7
.Lk1r7:
LA 17
PK 115,18,113,56
M 5,78,0
s_addc_u32 s43, s45, 0
LC_ 119
PK 113,18,113,97
M 5,79,1
W1 24,0
M 4,66,0
WL 4
LO 16,16,118
LU 118,16
M 4,67,1
RA 232,1,11264
HS 25,118
M 3,54,0
LL 18,96
LL 122,97
M 3,55,1
LA 18
PK 118,16,116,56
M 2,42,0
LC_ 122
PK 116,16,116,97
M 2,43,1
RV 252,22,5392
PO 118
M 1,30,0
WL 5
LO 17,17,119
LU 119,17
M 1,31,1
RA 236,1,12288
HS 25,119
M 0,18,0
LL 16,98
LL 123,99
M 0,19,1
LA 16
PK 119,17,117,56
M 0,20,2
LC_ 123
PK 117,17,117,97
M 0,21,3
ST 112
PO 119
M 1,32,2
WL 4
LO 18,18,122
LU 122,18
M 1,33,3
RA 216,1,13312
HS 25,122
M 2,44,2
LL 17,100
LL 126,101
M 2,45,3
LA 17
PK 122,18,120,56
M 3,56,2
LC_ 126
PK 120,18,120,97
M 3,57,3
v_lshrrev_b32 v3, 9, v0
M 4,68,2
WL 3
LO 16,16,123
LU 123,16
M 4,69,3
RA 220,1,14336
HS 25,123
v_lshl_add_u32 v3, v3, 2, 0
v_bfe_u32 v3, s58, v3, 4
v_mad_u32_u24 v3, v3, v32, v11
M 5,80,2
LL 18,102
LL 127,103
M 5,81,3
s_add_u32 s42, s44, s63
LA 18
PK 123,16,121,56
M 0,22,4
s_addc_u32 s43, s45, 0
LC_ 127
PK 121,16,121,97
M 0,23,5
ST 116
M 1,34,4
WL 3
LO 17,17,126
LU 126,17
M 1,35,5
s_add_u32 s42, s44, s64
RA 224,1,15360
HS 25,126
M 2,46,4
s_addc_u32 s43, s45, 0
LL 16,56
LL 104,57
M 2,47,5
LA 16
PK 126,17,124,56
M 3,58,4
LC_ 104
PK 124,17,124,97
M 3,59,5
ST 120
PO 126
M 4,70,4
WL 3
LO 18,18,127
LU 127,18
M 4,71,5
s_add_u32 s42, s44, s65
RA 228,1,16384
HS 25,127
M 5,82,4
LL 17,58
LL 105,59
M 5,83,5
s_addc_u32 s43, s45, 0
LA 17
PK 127,18,125,56
Z 6,12,0
LC_ 105
PK 125,18,125,97
M 6,13,1
ds_read_b64 v[14:15], v5 offset:0
PO 127
Z 7,24,0
WL 4
LO 104,16,104
LU 16,104
M 7,25,1
)"
R"(RA 232,1,17408
HZ 24,16
Z 8,36,0
LL 18,60
LL 108,61
M 8,37,1
s_mul_i32 s53, s50, 25600
LA 18
v_subrev_u32 v5, s39, v5
Z 9,48,0
s_add_u32 s40, %[wlo], s53
LC_ 108
HX 25
M 9,49,1
s_addc_u32 s41, %[whi], 0
W1 25,1024
Z 10,60,0
WL 5
LO 105,17,105
LU 17,105
M 10,61,1
s_mov_b32 m0, s38
RA 236,1,18432
HS 24,17
Z 11,72,0
LL 16,62
LL 109,63
M 11,73,1
LA 16
ST 124
M 11,74,2
LC_ 109
DW 0
M 11,75,3
WL 7
v_add_f32 v14, v14, v15
v_add_u32 v4, s39, v4
M 10,62,2
WL 4
LO 108,18,108
LU 18,108
M 10,63,3
s_add_u32 s37, s37, 32768
RA 216,1,19456
HS 24,18
M 9,50,2
s_cmp_eq_u32 s37, s75
LL 17,64
LL 112,65
M 9,51,3
s_cselect_b32 s37, s74, s37
LA 17
DW 1024
M 8,38,2
LC_ 112
v_add_f32_dpp v14, v14, v14 quad_perm:[1,0,3,2] row_mask:0xf bank_mask:0xf
M 8,39,3
s_nop 0
v_add_f32_dpp v14, v14, v14 quad_perm:[2,3,0,1] row_mask:0xf bank_mask:0xf
M 7,26,2
WL 3
LO 109,16,109
LU 16,109
M 7,27,3
RA 220,1,20480
HS 24,16
M 6,14,2
LL 18,66
LL 113,67
M 6,15,3
LA 18
DW 2048
M 6,16,4
LC_ 113
v_add_f32_dpp v14, v14, v14 row_half_mirror row_mask:0xf bank_mask:0xf
M 6,17,5
v_cvt_f64_f32 v[14:15], v14
M 7,28,4
WL 3
LO 112,17,112
LU 17,112
M 7,29,5
s_mul_i32 s53, s51, 24
RA 224,1,21504
HS 24,17
M 8,40,4
s_bitcmp1_b32 s51, 0
LL 16,68
LL 116,69
M 8,41,5
s_addc_u32 s53, s53, 0
LA 16
s_mov_b64 exec, s[54:55]
SP
s_mov_b64 exec, -1
M 9,52,4
s_lshl_b32 s53, s53, 11
LC_ 116
M 9,53,5
s_bitcmp1_b64 s[90:91], s51
M 10,64,4
WL 3
LO 113,18,113
LU 18,113
M 10,65,5
s_cselect_b64 s[44:45], s[84:85], s[82:83]
RA 228,1,22528
HS 24,18
M 11,76,4
LL 17,70
LL 117,71
M 11,77,5
s_add_u32 s44, s44, s53
LA 17
M 11,78,0
s_addc_u32 s45, s45, 0
LC_ 117
M 11,79,1
s_add_u32 s51, s51, %[d]
M 10,66,0
WL 3
LO 116,16,116
LU 16,116
M 10,67,1
s_cmp_eq_u32 s51, %[wrap]
RA 232,1,23552
HS 24,16
M 9,54,0
s_cselect_b32 s51, %[rst], s51
LL 18,72
LL 120,73
M 9,55,1
s_add_u32 s40, s40, 3072
LA 18
M 8,42,0
s_addc_u32 s41, s41, 0
LC_ 120
s_add_u32 m0, s38, 3072
M 8,43,1
DW 0
M 7,30,0
WL 3
LO 117,17,117
LU 17,117
M 7,31,1
RA 236,1,24576
HS 24,17
M 6,18,0
LL 16,74
LL 121,75
M 6,19,1
LA 16
DW 1024
M 6,20,2
LC_ 121
DY
M 6,21,3
DW 2048
v_add_u32 v1, s37, v0
M 7,32,2
WL 3
LO 120,18,120
LU 18,120
M 7,33,3
s_lshl_b32 s53, s52, 8
RA 216,1,0
HS 24,18
M 8,44,2
s_add_u32 s48, %[alo], s53
LL 17,76
LL 124,77
M 8,45,3
s_addc_u32 s49, %[ahi], 0
LA 17
v_and_b32 v22, 0xfffffcff, v1
M 9,56,2
s_add_u32 s52, s52, %[d]
LC_ 124
M 9,57,3
s_cmp_eq_u32 s52, %[wrap]
M 10,68,2
WL 3
LO 121,16,121
LU 16,121
M 10,69,3
s_cselect_b32 s52, %[rst], s52
RA 220,1,1024
HS 24,16
M 11,80,2
LL 18,78
LL 125,79
M 11,81,3
LA 18
RV 252,22,5120
M 6,22,4
LC_ 125
DQ 3072
M 6,23,5
s_add_u32 s50, s50, %[d]
M 7,34,4
WL 4
LO 124,17,124
LU 17,124
M 7,35,5
s_cmp_eq_u32 s50, %[wrap]
RA 224,1,2048
HS 24,17
M 8,46,4
s_cselect_b32 s50, %[rst], s50
LL 16,80
LL 106,81
M 8,47,5
LA 16
v_add_u32 v19, 1, v19
M 9,58,4
s_add_u32 s38, s38, 32768
LC_ 106
M 9,59,5
M 10,70,4
WL 3
LO 125,18,125
LU 18,125
M 10,71,5
RA 228,1,3072
HS 24,18
M 11,82,4
LL 17,82
LL 107,83
M 11,83,5
LA 17
HX 24
Z 0,12,0
s_cmp_eq_u32 s38, s77
LC_ 107
M 0,13,1
s_cselect_b32 s38, s76, s38
WV 38
FP
Z 1,24,0
WL 4
LO 16,16,106
LU 106,16
M 1,25,1
s_sub_u32 s39, 0, s39
RA 232,1,4096
HZ 25,106
Z 2,36,0
LL 18,84
LL 110,85
M 2,37,1
LA 18
PK 106,16,104,56
Z 3,48,0
LC_ 110
PK 104,16,104,97
M 3,49,1
Z 4,60,0
WL 4
LO 17,17,107
LU 107,17
M 4,61,1
s_add_u32 s42, s44, s60
RA 236,1,6144
HS 25,107
Z 5,72,0
LL 16,86
LL 111,87
M 5,73,1
s_addc_u32 s43, s45, 0
LA 16
PK 107,17,105,56
M 5,74,2
LC_ 111
PK 105,17,105,97
M 5,75,3
ST 104
M 4,62,2
WL 3
LO 18,18,110
LU 110,18
M 4,63,3
RA 216,1,7168
HS 25,110
M 3,50,2
LL 17,88
LL 114,89
M 3,51,3
LA 17
PK 110,18,108,56
M 2,38,2
LC_ 114
PK 108,18,108,97
M 2,39,3
FR
PO 110
M 1,26,2
WL 4
LO 16,16,111
LU 111,16
M 1,27,3
s_add_u32 s42, s44, s61
RA 220,1,8192
HS 25,111
M 0,14,2
LL 18,90
LL 115,91
M 0,15,3
s_addc_u32 s43, s45, 0
LA 18
PK 111,16,109,56
M 0,16,4
s_mov_b32 m0, %[pm]
LC_ 115
PK 109,16,109,97
M 0,17,5
DF
PO 111
M 1,28,4
WL 4
LO 17,17,114
LU 114,17
M 1,29,5
RA 224,1,9216
HS 25,114
M 2,40,4
LL 16,92
LL 118,93
M 2,41,5
LA 16
PK 114,17,112,56
M 3,52,4
LC_ 118
PK 112,17,112,97
M 3,53,5
WL 6
ST 108
v_cmp_gt_u32 vcc, v19, v15
M 4,64,4
WL 3
LO 18,18,115
LU 115,18
M 4,65,5
s_add_u32 s42, s44, s62
RA 228,1,10240
HS 25,115
M 5,76,4
LL 17,94
LL 119,95
M 5,77,5
s_cbranch_vccnz .Lk1s8
.Lk1r8:
LA 17
PK 115,18,113,56
M 5,78,0
s_addc_u32 s43, s45, 0
LC_ 119
PK 113,18,113,97
M 5,79,1
W1 24,0
M 4,66,0
WL 4
LO 16,16,118
LU 118,16
M 4,67,1
RA 232,1,11264
HS 25,118
M 3,54,0
LL 18,96
LL 122,97
M 3,55,1
LA 18
PK 118,16,116,56
M 2,42,0
LC_ 122
PK 116,16,116,97
M 2,43,1
RV 252,22,5392
PO 118
M 1,30,0
WL 5
LO 17,17,119
LU 119,17
M 1,31,1
RA 236,1,12288
HS 25,119
M 0,18,0
LL 16,98
LL 123,99
M 0,19,1
LA 16
PK 119,17,117,56
M 0,20,2
LC_ 123
PK 117,17,117,97
M 0,21,3
ST 112
PO 119
M 1,32,2
WL 4
LO 18,18,122
)"
R"(LU 122,18
M 1,33,3
RA 216,1,13312
HS 25,122
M 2,44,2
LL 17,100
LL 126,101
M 2,45,3
LA 17
PK 122,18,120,56
v_lshrrev_b32 v3, 9, v0
v_lshl_add_u32 v3, v3, 2, 8
v_bfe_u32 v3, s58, v3, 4
v_mad_u32_u24 v3, v3, v32, v11
M 3,56,2
s_add_u32 s42, s44, s63
LC_ 126
PK 120,18,120,97
M 3,57,3
s_addc_u32 s43, s45, 0
ST 116
M 4,68,2
WL 3
LO 16,16,123
LU 123,16
M 4,69,3
s_add_u32 s42, s44, s64
RA 220,1,14336
HS 25,123
M 5,80,2
LL 18,102
LL 127,103
M 5,81,3
s_addc_u32 s43, s45, 0
LA 18
PK 123,16,121,56
M 0,22,4
LC_ 127
PK 121,16,121,97
M 0,23,5
ST 120
RV 40,8,6144
M 1,34,4
WL 4
LO 17,17,126
LU 126,17
M 1,35,5
RA 224,1,15360
HS 25,126
M 2,46,4
LL 16,56
LL 104,57
M 2,47,5
LA 16
PK 126,17,124,56
M 3,58,4
LC_ 104
PK 124,17,124,97
M 3,59,5
ds_read_b64 v[14:15], v5 offset:0
PO 126
M 4,70,4
WL 5
LO 18,18,127
LU 127,18
M 4,71,5
s_add_u32 s42, s44, s65
RA 228,1,16384
HS 25,127
M 5,82,4
LL 17,58
LL 105,59
M 5,83,5
s_addc_u32 s43, s45, 0
LA 17
PK 127,18,125,56
Z 6,12,0
LC_ 105
PK 125,18,125,97
M 6,13,1
RV 44,8,6208
PO 127
Z 7,24,0
WL 5
LO 104,16,104
LU 16,104
M 7,25,1
RA 232,1,17408
HZ 24,16
Z 8,36,0
WL 5
LL 18,60
LL 108,61
M 8,37,1
s_mul_i32 s53, s50, 25600
LA 18
RV 48,8,6272
LZ 40,0
s_add_u32 s40, %[wlo], s53
RV 40,8,6336
v_subrev_u32 v5, s39, v5
Z 9,48,0
s_addc_u32 s41, %[whi], 0
LC_ 108
v_add_f32 v14, v14, v15
M 9,49,1
s_mov_b32 m0, s38
ST 124
v_add_f32_dpp v14, v14, v14 quad_perm:[1,0,3,2] row_mask:0xf bank_mask:0xf
Z 10,60,0
WL 6
LO 105,17,105
LU 17,105
M 10,61,1
WL 5
RA 236,1,18432
HS 24,17
LS 44,1
RV 44,8,6400
v_add_f32_dpp v14, v14, v14 quad_perm:[2,3,0,1] row_mask:0xf bank_mask:0xf
Z 11,72,0
LL 16,62
LL 109,63
M 11,73,1
LA 16
DW 0
M 11,74,2
LC_ 109
v_add_f32_dpp v14, v14, v14 row_half_mirror row_mask:0xf bank_mask:0xf
M 11,75,3
WL 6
v_cvt_f64_f32 v[14:15], v14
HX 25
LS 48,2
WL 5
RV 48,8,6464
LS 40,3
RV 40,8,6528
M 10,62,2
WL 4
LO 108,18,108
LU 18,108
W1 25,1024
M 10,63,3
s_add_u32 s37, s37, 32768
RA 216,1,19456
HS 24,18
M 9,50,2
s_cmp_eq_u32 s37, s75
LL 17,64
LL 112,65
M 9,51,3
s_cselect_b32 s37, s74, s37
LA 17
s_mov_b64 exec, s[54:55]
SP
s_mov_b64 exec, -1
LS 44,4
RV 44,8,6592
v_add_u32 v4, s39, v4
M 8,38,2
LC_ 112
DW 1024
M 8,39,3
WV 46
M 7,26,2
WL 7
LO 109,16,109
LU 16,109
M 7,27,3
WL 6
RA 220,1,20480
HS 24,16
LS 48,5
WL 6
RV 48,8,6656
LS 40,6
RV 40,8,6720
M 6,14,2
LL 18,66
LL 113,67
M 6,15,3
LA 18
DW 2048
M 6,16,4
WV 45
LC_ 113
M 6,17,5
WL 6
LS 44,7
WV 39
RV 44,8,6784
M 7,28,4
WL 3
LO 112,17,112
LU 17,112
M 7,29,5
s_mul_i32 s53, s51, 24
RA 224,1,21504
HS 24,17
M 8,40,4
s_bitcmp1_b32 s51, 0
LL 16,68
LL 116,69
M 8,41,5
s_addc_u32 s53, s53, 0
LA 16
LS 48,8
s_lshl_b32 s53, s53, 11
RV 48,8,6848
RA 240,7,0
LS 40,9
s_bitcmp1_b64 s[90:91], s51
RA 244,7,256
M 9,52,4
s_cselect_b64 s[44:45], s[84:85], s[82:83]
LC_ 116
M 9,53,5
s_add_u32 s44, s44, s53
M 10,64,4
WL 6
LO 113,18,113
LU 18,113
M 10,65,5
s_addc_u32 s45, s45, 0
RA 228,1,22528
HS 24,18
WV 37
LS 44,10
s_add_u32 s51, s51, %[d]
RA 248,7,512
M 11,76,4
WL 5
LL 17,70
LL 117,71
M 11,77,5
s_cmp_eq_u32 s51, %[wrap]
LA 17
M 11,78,0
s_cselect_b32 s51, %[rst], s51
LC_ 117
M 11,79,1
s_add_u32 s40, s40, 3072
WV 32
LS 48,11
s_addc_u32 s41, s41, 0
RA 252,7,768
M 10,66,0
WL 5
LO 116,16,116
LU 16,116
M 10,67,1
s_add_u32 m0, s38, 3072
RA 232,1,23552
HS 24,16
M 9,54,0
LL 18,72
LL 120,73
M 9,55,1
LA 18
DW 0
M 8,42,0
LC_ 120
DY
M 8,43,1
DW 1024
v_mul_f32 v36, 0xbb2aaaab, v36
M 7,30,0
WL 4
LO 117,17,117
LU 17,117
M 7,31,1
RA 236,1,24576
HS 24,17
M 6,18,0
LL 16,74
LL 121,75
M 6,19,1
LA 16
DW 2048
M 6,20,2
LC_ 121
v_mul_f32 v37, 0xbb2aaaab, v37
M 6,21,3
s_lshl_b32 s53, s52, 8
v_add_u32 v1, s37, v0
v_mul_f32 v38, 0xbb2aaaab, v38
M 7,32,2
WL 3
LO 120,18,120
LU 18,120
M 7,33,3
s_add_u32 s48, %[alo], s53
RA 216,1,0
HS 24,18
M 8,44,2
s_addc_u32 s49, %[ahi], 0
LL 17,76
LL 124,77
M 8,45,3
s_add_u32 s52, s52, %[d]
LA 17
v_and_b32 v22, 0xfffffcff, v1
M 9,56,2
s_cmp_eq_u32 s52, %[wrap]
LC_ 124
v_mul_f32 v39, 0xbb2aaaab, v39
M 9,57,3
s_cselect_b32 s52, %[rst], s52
DQ 3072
LK 40,240,0,0
LK 44,242,0,2
RA 240,7,1024
LK 48,244,1,0
s_nop 1
CF 128,40,41
LK 248,246,1,2
RA 244,7,1280
CF 129,42,43
M 10,68,2
WL 5
LO 121,16,121
LU 16,121
M 10,69,3
s_add_u32 s50, s50, %[d]
RA 220,1,1024
HS 24,16
M 11,80,2
LL 18,78
LL 125,79
M 11,81,3
s_cmp_eq_u32 s50, %[wrap]
LA 18
RV 252,22,5120
M 6,22,4
s_cselect_b32 s50, %[rst], s50
LC_ 125
CF 130,44,45
M 6,23,5
v_add_u32 v19, 1, v19
CF 131,46,47
LK 40,248,2,0
CF 132,48,49
LK 44,250,2,2
RA 248,7,1536
CF 133,50,51
GZ 0
CF 134,248,249
CF 135,250,251
LK 48,252,3,0
CF 136,40,41
LK 248,254,3,2
RA 252,7,1792
CF 137,42,43
GS 1
WL 7
CF 138,44,45
CF 139,46,47
LK 40,240,4,0
CF 140,48,49
LK 44,242,4,2
RA 240,7,2048
CF 141,50,51
GS 2
WL 7
CF 142,248,249
CF 143,250,251
LK 48,244,5,0
CF 144,40,41
LK 248,246,5,2
RA 244,7,2304
CF 145,42,43
GS 3
CF 146,44,45
CF 147,46,47
M 7,34,4
WL 3
LO 124,17,124
LU 17,124
M 7,35,5
s_add_u32 s38, s38, 32768
RA 224,1,2048
HS 24,17
LK 40,248,6,0
WL 3
CF 148,48,49
LK 44,250,6,2
s_cmp_eq_u32 s38, s77
RA 248,7,2560
CF 149,50,51
GS 4
s_cselect_b32 s38, s76, s38
CF 150,248,249
CF 151,250,251
LK 48,252,7,0
CF 152,40,41
LK 248,254,7,2
RA 252,7,2816
CF 153,42,43
GS 5
)"
R"(WL 4
CF 154,44,45
CF 155,46,47
LK 40,240,8,0
CF 156,48,49
LK 44,242,8,2
CF 157,50,51
GS 6
WL 3
CF 158,248,249
CF 159,250,251
LK 48,244,9,0
CF 160,40,41
LK 248,246,9,2
CF 161,42,43
GS 7
CF 162,44,45
CF 163,46,47
M 8,46,4
LL 16,80
LL 106,81
M 8,47,5
WV 32
LA 16
FP
M 9,58,4
s_sub_u32 s39, 0, s39
LC_ 106
M 9,59,5
WL 4
LK 40,248,10,0
CF 164,48,49
LK 44,250,10,2
CF 165,50,51
GS 8
CF 166,248,249
CF 167,250,251
M 10,70,4
LO 125,18,125
LU 18,125
M 10,71,5
WL 3
RA 228,1,3072
HS 24,18
LK 48,252,11,0
CF 168,40,41
LK 248,254,11,2
CF 169,42,43
GS 9
CF 170,44,45
CF 171,46,47
M 11,82,4
LL 17,82
LL 107,83
M 11,83,5
LA 17
HX 24
Z 0,12,0
LC_ 107
RV 36,8,6912
M 0,13,1
GS 10
Z 1,24,0
WL 4
LO 16,16,106
LU 106,16
M 1,25,1
RA 232,1,4096
HZ 25,106
Z 2,36,0
LL 18,84
LL 110,85
M 2,37,1
LA 18
PK 106,16,104,56
Z 3,48,0
LC_ 110
PK 104,16,104,97
M 3,49,1
CF 172,48,49
CF 173,50,51
Z 4,60,0
WL 4
LO 17,17,107
LU 107,17
M 4,61,1
s_add_u32 s42, s44, s60
RA 236,1,6144
HS 25,107
Z 5,72,0
LL 16,86
LL 111,87
M 5,73,1
s_addc_u32 s43, s45, 0
LA 16
PK 107,17,105,56
M 5,74,2
LC_ 111
PK 105,17,105,97
M 5,75,3
ST 104
CF 174,248,249
M 4,62,2
WL 3
LO 18,18,110
LU 110,18
M 4,63,3
RA 216,1,7168
HS 25,110
M 3,50,2
LL 17,88
LL 114,89
M 3,51,3
LA 17
PK 110,18,108,56
M 2,38,2
LC_ 114
PK 108,18,108,97
M 2,39,3
FR
PO 110
M 1,26,2
WL 4
LO 16,16,111
LU 111,16
M 1,27,3
s_add_u32 s42, s44, s61
RA 220,1,8192
HS 25,111
M 0,14,2
LL 18,90
LL 115,91
M 0,15,3
s_addc_u32 s43, s45, 0
LA 18
PK 111,16,109,56
M 0,16,4
s_mov_b32 m0, %[pm]
LC_ 115
PK 109,16,109,97
M 0,17,5
DF
PO 111
M 1,28,4
WL 4
LO 17,17,114
LU 114,17
M 1,29,5
RA 224,1,9216
HS 25,114
M 2,40,4
LL 16,92
LL 118,93
M 2,41,5
LA 16
PK 114,17,112,56
M 3,52,4
LC_ 118
PK 112,17,112,97
M 3,53,5
WL 6
ST 108
v_cmp_gt_u32 vcc, v19, v15
M 4,64,4
WL 3
LO 18,18,115
LU 115,18
M 4,65,5
s_add_u32 s42, s44, s62
RA 228,1,10240
HS 25,115
CF 175,250,251
M 5,76,4
LL 17,94
LL 119,95
M 5,77,5
s_cbranch_vccnz .Lk1s9
.Lk1r9:
LA 17
PK 115,18,113,56
GS 11
s_addc_u32 s43, s45, 0
W1 24,0
s_nop 5
v_cndmask_b32 v52, v52, v53, s[78:79]
M 5,78,0
LC_ 119
PK 113,18,113,97
v_cndmask_b32 v54, v54, v55, s[78:79]
M 5,79,1
ST 112
v_cndmask_b32 v52, v52, v54, s[80:81]
M 4,66,0
WL 4
LO 16,16,118
LU 118,16
v_mul_f32 v52, 0x3b2aaaab, v52
M 4,67,1
RA 232,1,11264
HS 25,118
v_med3_f32 v53, v52, v39, v37
M 3,54,0
LL 18,96
LL 122,97
LG 52,53
M 3,55,1
LA 18
PK 118,16,116,56
C 52,52,52
M 2,42,0
LC_ 122
PK 116,16,116,97
v_lshlrev_b32 v52, 16, v52
M 2,43,1
RV 252,22,5392
PO 118
v_add_f32 v52, v36, v52
M 1,30,0
WL 4
LO 17,17,119
LU 119,17
C 52,52,52
M 1,31,1
s_add_u32 s42, s44, s63
RA 236,1,12288
HS 25,119
M 0,18,0
LL 16,98
LL 123,99
v_lshlrev_b32 v52, 16, v52
M 0,19,1
s_addc_u32 s43, s45, 0
LA 16
PK 119,17,117,56
v_sqrt_f32 v52, v52
M 0,20,2
LC_ 123
PK 117,17,117,97
C 52,52,52
M 0,21,3
PO 119
v_lshlrev_b32 v52, 16, v52
M 1,32,2
WL 4
LO 18,18,122
LU 122,18
v_rcp_f32 v52, v52
M 1,33,3
RA 216,1,13312
HS 25,122
v_mul_f32 v52, v38, v52
M 2,44,2
LL 17,100
LL 126,101
CF 52,52,52
M 2,45,3
LA 17
PK 122,18,120,56
ds_bpermute_b32 v13, v28, v52
M 3,56,2
LC_ 126
PK 120,18,120,97
M 3,57,3
ST 116
M 4,68,2
WL 4
LO 16,16,123
LU 123,16
M 4,69,3
s_add_u32 s42, s44, s64
RA 220,1,14336
HS 25,123
M 5,80,2
LL 18,102
LL 127,103
M 5,81,3
s_addc_u32 s43, s45, 0
LA 18
PK 123,16,121,56
M 0,22,4
LC_ 127
PK 121,16,121,97
M 0,23,5
ST 120
M 1,34,4
WL 3
LO 17,17,126
LU 126,17
M 1,35,5
RA 224,1,15360
HS 25,126
XS 128
M 2,46,4
LL 16,56
LL 104,57
M 2,47,5
LA 16
PK 126,17,124,56
M 3,58,4
LC_ 104
PK 124,17,124,97
XS 129
M 3,59,5
ds_read_b64 v[14:15], v5 offset:0
PO 126
M 4,70,4
WL 4
LO 18,18,127
LU 127,18
M 4,71,5
s_add_u32 s42, s44, s65
RA 228,1,16384
HS 25,127
XS 130
M 5,82,4
LL 17,58
LL 105,59
M 5,83,5
s_addc_u32 s43, s45, 0
LA 17
PK 127,18,125,56
Z 6,12,0
LC_ 105
PK 125,18,125,97
XS 131
M 6,13,1
PO 127
v_subrev_u32 v5, s39, v5
Z 7,24,0
WL 4
LO 104,16,104
LU 16,104
XS 132
M 7,25,1
RA 232,1,17408
HZ 24,16
Z 8,36,0
WL 4
LL 18,60
LL 108,61
M 8,37,1
s_mul_i32 s53, s50, 25600
LA 18
v_add_f32 v14, v14, v15
XS 133
Z 9,48,0
s_add_u32 s40, %[wlo], s53
LC_ 108
v_add_f32_dpp v14, v14, v14 quad_perm:[1,0,3,2] row_mask:0xf bank_mask:0xf
M 9,49,1
s_addc_u32 s41, %[whi], 0
v_add_f32_dpp v14, v14, v14 quad_perm:[2,3,0,1] row_mask:0xf bank_mask:0xf
HX 25
Z 10,60,0
WL 3
LO 105,17,105
LU 17,105
XS 134
M 10,61,1
s_mov_b32 m0, s38
RA 236,1,18432
HS 24,17
Z 11,72,0
LL 16,62
LL 109,63
M 11,73,1
LA 16
W1 25,1024
XS 135
M 11,74,2
LC_ 109
ST 124
M 11,75,3
DW 0
v_add_f32_dpp v14, v14, v14 row_half_mirror row_mask:0xf bank_mask:0xf
v_min_u32 v35, v35, v13
M 10,62,2
WL 4
LO 108,18,108
LU 18,108
M 10,63,3
s_add_u32 s37, s37, 32768
RA 216,1,19456
HS 24,18
M 9,50,2
s_cmp_eq_u32 s37, s75
LL 17,64
LL 112,65
XS 136
M 9,51,3
s_cselect_b32 s37, s74, s37
LA 17
v_cvt_f64_f32 v[14:15], v14
M 8,38,2
s_mul_i32 s53, %[d], 0
LC_ 112
s_mov_b64 exec, s[54:55]
SP
s_mov_b64 exec, -1
M 8,39,3
)"
R"(s_add_u32 s53, s53, s57
v_add_u32 v4, s39, v4
XS 137
M 7,26,2
WL 3
LO 109,16,109
LU 16,109
M 7,27,3
s_add_u32 s46, %[xlo], s53
RA 220,1,20480
HS 24,16
M 6,14,2
LL 18,66
LL 113,67
XS 138
M 6,15,3
s_addc_u32 s47, %[xhi], 0
LA 18
XS 139
M 6,16,4
s_bitcmp1_b64 s[92:93], 48
LC_ 113
XS 140
M 6,17,5
s_cselect_b32 s58, 16, 0
XS 141
XS 142
M 7,28,4
WL 3
LO 112,17,112
LU 17,112
M 7,29,5
s_lshr_b32 s58, 0xda43cb52, s58
RA 224,1,21504
HS 24,17
M 8,40,4
LL 16,68
LL 116,69
M 8,41,5
LA 16
v_lshrrev_b32 v3, 6, v0
M 9,52,4
LC_ 116
v_and_b32 v3, 12, v3
v_bfe_u32 v3, s58, v3, 4
v_mad_u32_u24 v3, v3, v32, v21
M 9,53,5
DW 1024
XS 143
M 10,64,4
WL 3
LO 113,18,113
LU 18,113
M 10,65,5
RA 228,1,22528
HS 24,18
M 11,76,4
LL 17,70
LL 117,71
M 11,77,5
LA 17
XL 12,0
M 11,78,0
LC_ 117
DW 2048
M 11,79,1
XL 13,64
XS 144
M 10,66,0
WL 3
LO 116,16,116
LU 16,116
M 10,67,1
s_mul_i32 s53, s51, 24
RA 232,1,23552
HS 24,16
M 9,54,0
s_bitcmp1_b32 s51, 0
LL 18,72
LL 120,73
M 9,55,1
s_addc_u32 s53, s53, 0
LA 18
s_lshl_b32 s53, s53, 11
s_bitcmp1_b64 s[90:91], s51
s_cselect_b64 s[44:45], s[84:85], s[82:83]
s_add_u32 s44, s44, s53
s_addc_u32 s45, s45, 0
s_add_u32 s51, s51, %[d]
s_cmp_eq_u32 s51, %[wrap]
s_cselect_b32 s51, %[rst], s51
XS 145
M 8,42,0
s_add_u32 s40, s40, 3072
LC_ 120
s_addc_u32 s41, s41, 0
s_add_u32 m0, s38, 3072
M 8,43,1
XL 14,128
XS 146
M 7,30,0
WL 3
LO 117,17,117
LU 17,117
M 7,31,1
RA 236,1,24576
HS 24,17
M 6,18,0
LL 16,74
LL 121,75
XS 147
M 6,19,1
LA 16
DW 0
M 6,20,2
LC_ 121
XL 15,192
M 6,21,3
DW 1024
v_add_u32 v1, s37, v0
XS 148
M 7,32,2
WL 3
LO 120,18,120
LU 18,120
M 7,33,3
RA 216,1,0
HS 24,18
M 8,44,2
LL 17,76
LL 124,77
XS 149
M 8,45,3
LA 17
XL 16,256
M 9,56,2
LC_ 124
DW 2048
XS 150
M 9,57,3
XL 17,320
v_and_b32 v22, 0xfffffcff, v1
M 10,68,2
WL 3
LO 121,16,121
LU 16,121
M 10,69,3
s_lshl_b32 s53, s52, 8
RA 220,1,1024
HS 24,16
XS 151
M 11,80,2
LL 18,78
LL 125,79
M 11,81,3
s_add_u32 s48, %[alo], s53
LA 18
RV 252,22,5120
M 6,22,4
s_addc_u32 s49, %[ahi], 0
LC_ 125
XS 152
M 6,23,5
s_add_u32 s52, s52, %[d]
XS 153
XS 154
M 7,34,4
WL 4
LO 124,17,124
LU 17,124
M 7,35,5
s_cmp_eq_u32 s52, %[wrap]
RA 224,1,2048
HS 24,17
M 8,46,4
s_cselect_b32 s52, %[rst], s52
LL 16,80
LL 106,81
M 8,47,5
LA 16
DQ 3072
M 9,58,4
LC_ 106
XL 18,384
M 9,59,5
s_add_u32 s50, s50, %[d]
XS 155
XS 156
M 10,70,4
WL 3
LO 125,18,125
LU 18,125
M 10,71,5
RA 228,1,3072
HS 24,18
M 11,82,4
LL 17,82
LL 107,83
M 11,83,5
LA 17
HX 24
s_cmp_eq_u32 s50, %[wrap]
s_cselect_b32 s50, %[rst], s50
XL 19,448
Z 2,36,0
WV 39
LC_ 107
XL 20,512
v_add_u32 v19, 1, v19
s_add_u32 s38, s38, 32768
s_cmp_eq_u32 s38, s77
s_cselect_b32 s38, s76, s38
FP
s_sub_u32 s39, 0, s39
M 2,37,1
XL 21,576
XS 157
Z 3,48,0
WL 4
LO 16,16,106
LU 106,16
M 3,49,1
RA 232,1,4096
HZ 25,106
Z 4,60,0
LL 18,84
LL 110,85
XS 158
M 4,61,1
LA 18
PK 106,16,104,56
Z 5,72,0
WL 5
LC_ 110
XL 22,640
PK 104,16,104,97
M 5,73,1
RA 236,1,6144
XS 159
M 5,74,2
WL 5
LO 17,17,107
LU 107,17
M 5,75,3
FR
HS 25,107
M 4,62,2
LL 16,86
LL 111,87
XS 160
M 4,63,3
LA 16
PK 107,17,105,56
M 3,50,2
LC_ 111
XL 23,704
PK 105,17,105,97
XS 161
M 3,51,3
s_add_u32 s42, s44, s60
RA 216,1,7168
XS 162
M 2,38,2
WL 4
LO 18,18,110
LU 110,18
M 2,39,3
s_addc_u32 s43, s45, 0
RA 220,1,8192
HS 25,110
M 2,40,4
LL 17,88
LL 114,89
M 2,41,5
LA 17
PK 110,18,108,56
M 3,52,4
LC_ 114
PK 108,18,108,97
XS 163
M 3,53,5
RA 224,1,9216
PO 110
M 4,64,4
WL 5
LO 16,16,111
LU 111,16
ST 104
XS 164
M 4,65,5
s_add_u32 s42, s44, s61
HS 25,111
v_cmp_gt_u32 vcc, v19, v15
M 5,76,4
WL 3
LL 18,90
LL 115,91
M 5,77,5
s_addc_u32 s43, s45, 0
LA 18
PK 111,16,109,56
XS 165
M 5,78,0
s_cbranch_vccnz .Lk1s10
.Lk1r10:
LC_ 115
PK 109,16,109,97
M 5,79,1
WV 17
RA 228,1,10240
PO 111
BW 12
M 4,66,0
WL 5
LO 17,17,114
LU 114,17
XS 166
M 4,67,1
RA 232,1,11264
RV 40,8,6144
HS 25,114
M 3,54,0
RV 44,8,6208
LL 16,92
LL 118,93
M 3,55,1
LA 16
RV 48,8,6272
PK 114,17,112,56
XS 167
M 2,42,0
WL 6
LC_ 118
BR 12
PK 112,17,112,97
M 2,43,1
WV 15
RV 252,22,5392
ds_read_b64 v[14:15], v5 offset:0
W1 24,0
BW 13
XS 168
M 2,44,2
BR 13
LO 18,18,115
LU 115,18
M 2,45,3
WL 5
RA 236,1,12288
HS 25,115
LZ 40,12
WV 14
BW 14
M 3,56,2
LL 17,94
LL 119,95
XS 169
M 3,57,3
LA 17
RV 40,8,6336
PK 115,18,113,56
M 4,68,2
LC_ 119
BR 14
PK 113,18,113,97
M 4,69,3
WL 6
RA 216,1,13312
v_subrev_u32 v5, s39, v5
XS 170
LS 44,13
WV 12
BW 15
M 5,80,2
WL 7
LO 16,16,118
LU 118,16
ST 108
M 5,81,3
s_add_u32 s42, s44, s62
RA 220,1,14336
HS 25,118
s_addc_u32 s43, s45, 0
RV 44,8,6400
M 2,46,4
BR 15
LL 18,96
LL 122,97
XS 171
M 2,47,5
WL 5
LA 18
PK 118,16,116,56
LS 48,14
WV 11
BW 16
M 3,58,4
LC_ 122
PK 116,16,116,97
XS 172
)"
R"(M 3,59,5
RA 224,1,15360
RV 48,8,6464
PO 118
M 4,70,4
LO 17,17,119
LU 119,17
ST 112
M 4,71,5
s_add_u32 s42, s44, s63
BR 16
HS 25,119
XS 173
M 5,82,4
WL 6
LL 16,98
LL 123,99
M 5,83,5
s_addc_u32 s43, s45, 0
LA 16
PK 119,17,117,56
LS 40,15
WV 10
BW 17
Z 8,36,0
LC_ 123
PK 117,17,117,97
v_add_f32 v14, v14, v15
s_nop 1
v_add_f32_dpp v14, v14, v14 quad_perm:[1,0,3,2] row_mask:0xf bank_mask:0xf
s_nop 1
v_add_f32_dpp v14, v14, v14 quad_perm:[2,3,0,1] row_mask:0xf bank_mask:0xf
s_nop 1
v_add_f32_dpp v14, v14, v14 row_half_mirror row_mask:0xf bank_mask:0xf
v_cvt_f64_f32 v[14:15], v14
s_mov_b64 exec, s[54:55]
SP
s_mov_b64 exec, -1
RV 40,8,6528
XS 174
M 8,37,1
WV 9
RA 228,1,16384
BR 17
PO 119
Z 9,48,0
WL 6
LO 18,18,122
LU 122,18
XS 175
M 9,49,1
s_mul_i32 s53, %[d], 196608
RA 232,1,17408
HS 25,122
LS 44,16
s_add_u32 s53, s53, s57
BW 18
Z 10,60,0
s_add_u32 s46, %[xlo], s53
RV 44,8,6592
LL 17,100
LL 126,101
M 10,61,1
s_addc_u32 s47, %[xhi], 0
LA 17
BR 18
PK 122,18,120,56
Z 0,72,0
WL 5
LC_ 126
PK 120,18,120,97
M 0,73,1
s_bitcmp1_b64 s[92:93], 48
RA 236,1,18432
LS 48,17
s_cselect_b32 s58, 16, 0
WV 8
BW 19
M 0,74,2
RV 48,8,6656
LO 16,16,123
LU 123,16
M 0,75,3
s_lshr_b32 s58, 0xfe987610, s58
BR 19
HS 25,123
v_lshrrev_b32 v3, 6, v0
v_and_b32 v3, 12, v3
v_bfe_u32 v3, s58, v3, 4
v_mad_u32_u24 v3, v3, v32, v21
M 10,62,2
s_mul_i32 s53, s50, 25600
LL 18,102
LL 127,103
s_add_u32 s40, %[wlo], s53
s_addc_u32 s41, %[whi], 0
s_mov_b32 m0, s38
M 10,63,3
WL 5
LA 18
PK 123,16,121,56
LS 40,18
WV 7
BW 20
M 9,50,2
LC_ 127
RV 40,8,6720
PK 121,16,121,97
M 9,51,3
WV 6
RA 216,1,19456
BR 20
DW 0
M 8,38,2
WL 6
LO 17,17,126
LU 126,17
ST 116
M 8,39,3
s_add_u32 s42, s44, s64
RA 220,1,20480
HS 25,126
s_addc_u32 s43, s45, 0
LS 44,19
s_add_u32 s37, s37, 32768
BW 21
M 8,40,4
s_cmp_eq_u32 s37, s75
LL 16,64
LL 112,65
s_cselect_b32 s37, s74, s37
M 8,41,5
LA 16
XL 24,0
DW 1024
RV 44,8,6784
PK 126,17,124,56
M 9,52,4
LC_ 112
BR 21
PK 124,17,124,97
M 9,53,5
WL 6
RA 224,1,21504
PO 126
LS 48,20
WV 9
BW 22
M 10,64,4
LO 18,18,127
LU 127,18
ST 120
M 10,65,5
WV 9
DW 2048
RV 48,8,6848
HS 25,127
M 0,76,4
WL 8
RA 240,7,0
LL 17,66
LL 113,67
M 0,77,5
s_add_u32 s40, s40, 3072
LA 17
BR 22
PK 127,18,125,56
M 0,78,0
s_addc_u32 s41, s41, 0
LC_ 113
PK 125,18,125,97
s_add_u32 m0, s38, 3072
M 0,79,1
WL 7
RA 228,1,22528
XL 25,64
PO 127
LS 40,21
s_add_u32 s42, s44, s65
BW 23
M 10,66,0
RA 244,7,256
LO 112,16,112
LU 16,112
M 10,67,1
s_addc_u32 s43, s45, 0
RA 232,1,23552
BR 23
HZ 24,16
M 9,54,0
WL 6
DW 0
LL 18,68
LL 116,69
M 9,55,1
s_lshl_b32 s53, s52, 8
LA 18
HX 25
LS 44,22
s_add_u32 s48, %[alo], s53
RA 248,7,512
M 8,42,0
WL 2
LC_ 116
W1 25,1024
M 8,43,1
s_addc_u32 s49, %[ahi], 0
RA 236,1,24576
v_add_u32 v4, s39, v4
LS 48,23
s_add_u32 s52, s52, %[d]
RA 252,7,768
M 8,44,2
LO 113,17,113
LU 17,113
s_cmp_eq_u32 s52, %[wrap]
s_cselect_b32 s52, %[rst], s52
DW 1024
v_mul_f32 v36, 0xbb2aaaab, v36
M 8,45,3
XL 26,128
HS 24,17
v_mul_f32 v37, 0xbb2aaaab, v37
M 9,56,2
LL 16,70
LL 117,71
v_mul_f32 v38, 0xbb2aaaab, v38
v_mul_f32 v39, 0xbb2aaaab, v39
M 9,57,3
LA 16
DW 2048
LK 40,240,12,0
LK 44,242,12,2
RA 240,7,1024
v_add_u32 v1, s37, v0
LK 48,244,13,0
s_nop 0
CF 176,40,41
LK 248,246,13,2
RA 244,7,1280
CF 177,42,43
M 10,68,2
LC_ 117
RA 216,1,0
M 10,69,3
ST 124
v_and_b32 v22, 0xfffffcff, v1
M 0,80,2
WL 6
LO 116,18,116
LU 18,116
M 0,81,3
s_mul_i32 s53, s51, 24
RA 220,1,1024
HS 24,18
s_bitcmp1_b32 s51, 0
s_addc_u32 s53, s53, 0
s_lshl_b32 s53, s53, 11
s_bitcmp1_b64 s[90:91], s51
s_cselect_b64 s[44:45], s[84:85], s[82:83]
s_add_u32 s44, s44, s53
s_addc_u32 s45, s45, 0
s_add_u32 s51, s51, %[d]
s_cmp_eq_u32 s51, %[wrap]
s_cselect_b32 s51, %[rst], s51
DQ 3072
CF 178,44,45
M 8,46,4
LL 17,72
LL 120,73
CF 179,46,47
M 8,47,5
LA 17
RV 252,22,5120
LK 40,248,14,0
CF 180,48,49
LK 44,250,14,2
RA 248,7,1536
CF 181,50,51
GZ 12
WL 9
CF 182,248,249
CF 183,250,251
LK 48,252,15,0
CF 184,40,41
LK 248,254,15,2
RA 252,7,1792
CF 185,42,43
GS 13
WL 8
CF 186,44,45
CF 187,46,47
LK 40,240,16,0
CF 188,48,49
LK 44,242,16,2
RA 240,7,2048
CF 189,50,51
GS 14
WL 8
CF 190,248,249
CF 191,250,251
LK 48,244,17,0
CF 192,40,41
LK 248,246,17,2
RA 244,7,2304
CF 193,42,43
GS 15
CF 194,44,45
CF 195,46,47
M 9,58,4
WL 3
LC_ 120
XL 27,192
M 9,59,5
s_add_u32 s50, s50, %[d]
RA 224,1,2048
s_cmp_eq_u32 s50, %[wrap]
s_cselect_b32 s50, %[rst], s50
v_add_u32 v19, 1, v19
LK 40,248,18,0
WL 4
CF 196,48,49
LK 44,250,18,2
s_add_u32 s38, s38, 32768
RA 248,7,2560
CF 197,50,51
GS 16
s_cmp_eq_u32 s38, s77
CF 198,248,249
CF 199,250,251
LK 48,252,19,0
CF 200,40,41
LK 248,254,19,2
s_cselect_b32 s38, s76, s38
RA 252,7,2816
CF 201,42,43
GS 17
WL 5
CF 202,44,45
CF 203,46,47
LK 40,240,20,0
CF 204,48,49
LK 44,242,20,2
CF 205,50,51
GS 18
CF 206,248,249
CF 207,250,251
M 10,70,4
LO 117,16,117
LU 16,117
M 10,71,5
FP
HS 24,16
M 0,82,4
LL 18,74
LL 121,75
M 0,83,5
s_sub_u32 s39, 0, s39
LA 18
RA 228,1,3072
Z 2,36,0
LC_ 121
XL 28,256
M 2,37,1
WL 8
LK 48,244,21,0
CF 208,40,41
LK 248,246,21,2
CF 209,42,43
GS 19
CF 210,44,45
CF 211,46,47
Z 3,48,0
WL 7
LO 120,17,120
LU 17,120
M 3,49,1
WL 5
RA 232,1,4096
HS 24,17
LK 40,248,22,0
CF 212,48,49
LK 44,250,22,2
CF 213,50,51
GS 20
CF 214,248,249
CF 215,250,251
Z 4,60,0
LL 16,76
LL 124,77
M 4,61,1
LA 16
FR
Z 6,72,0
WL 4
LC_ 124
XL 29,320
M 6,73,1
RA 236,1,6144
LK 48,252,23,0
)"
R"(CF 216,40,41
LK 248,254,23,2
CF 217,42,43
GS 21
CF 218,44,45
CF 219,46,47
M 6,74,2
WL 5
LO 121,18,121
LU 18,121
M 6,75,3
RV 36,8,6912
HS 24,18
M 4,62,2
LL 17,78
LL 125,79
M 4,63,3
LA 17
RA 216,1,7168
GS 22
M 3,50,2
WL 5
LC_ 125
v_cmp_gt_u32 vcc, v19, v15
M 3,51,3
XL 30,384
CF 220,48,49
M 2,38,2
WL 4
LO 124,16,124
LU 16,124
M 2,39,3
RA 220,1,8192
HS 24,16
M 2,40,4
s_cbranch_vccnz .Lk1s11
.Lk1r11:
LL 18,88
LL 114,89
M 2,41,5
LA 18
ds_read_b64 v[14:15], v5 offset:0
M 3,52,4
LC_ 114
v_subrev_u32 v5, s39, v5
M 3,53,5
RA 224,1,9216
CF 221,50,51
M 4,64,4
WL 5
LO 125,17,125
LU 17,125
M 4,65,5
XL 31,448
HS 24,17
M 6,76,4
WL 4
LL 16,90
LL 115,91
M 6,77,5
LA 16
RA 228,1,10240
M 6,78,0
LC_ 115
HX 24
M 6,79,1
W1 24,0
CF 222,248,249
M 4,66,0
WL 5
LO 18,18,114
LU 114,18
M 4,67,1
RA 232,1,11264
HZ 25,114
M 3,54,0
LL 17,92
LL 118,93
M 3,55,1
LA 17
PK 114,18,112,56
M 2,42,0
WL 4
LC_ 118
PK 112,18,112,97
M 2,43,1
RV 252,22,5392
v_add_f32 v14, v14, v15
M 2,44,2
WL 5
LO 16,16,115
LU 115,16
M 2,45,3
RA 236,1,12288
HS 25,115
M 3,56,2
LL 18,94
LL 119,95
M 3,57,3
LA 18
PK 115,16,113,56
M 4,68,2
LC_ 119
XL 32,512
PK 113,16,113,97
v_add_f32_dpp v14, v14, v14 quad_perm:[1,0,3,2] row_mask:0xf bank_mask:0xf
M 4,69,3
s_add_u32 s42, s44, s62
RA 216,1,13312
v_add_f32_dpp v14, v14, v14 quad_perm:[2,3,0,1] row_mask:0xf bank_mask:0xf
M 6,80,2
WL 3
LO 17,17,118
LU 118,17
CF 223,250,251
M 6,81,3
s_addc_u32 s43, s45, 0
RA 220,1,14336
HS 25,118
GS 23
ST 112
v_add_f32_dpp v14, v14, v14 row_half_mirror row_mask:0xf bank_mask:0xf
s_nop 5
v_cndmask_b32 v52, v52, v53, s[78:79]
M 2,46,4
LL 16,96
LL 122,97
v_cndmask_b32 v54, v54, v55, s[78:79]
M 2,47,5
LA 16
PK 118,17,116,56
v_cndmask_b32 v52, v52, v54, s[80:81]
M 3,58,4
LC_ 122
PK 116,17,116,97
v_cvt_f64_f32 v[14:15], v14
v_mul_f32 v52, 0x3b2aaaab, v52
M 3,59,5
RA 224,1,15360
PO 118
v_med3_f32 v53, v52, v39, v37
M 4,70,4
WL 5
LO 18,18,119
LU 119,18
s_mov_b64 exec, s[54:55]
SP
s_mov_b64 exec, -1
XL 33,576
LG 52,53
s_nop 0
C 52,52,52
M 4,71,5
s_add_u32 s42, s44, s63
HS 25,119
v_lshlrev_b32 v52, 16, v52
M 6,82,4
WL 3
LL 17,98
LL 123,99
v_add_f32 v52, v36, v52
M 6,83,5
s_addc_u32 s43, s45, 0
LA 17
PK 119,18,117,56
C 52,52,52
Z 8,36,0
LC_ 123
PK 117,18,117,97
v_lshlrev_b32 v52, 16, v52
M 8,37,1
RA 228,1,16384
PO 119
v_sqrt_f32 v52, v52
s_nop 0
C 52,52,52
Z 9,48,0
WL 4
LO 16,16,122
LU 122,16
v_lshlrev_b32 v52, 16, v52
M 9,49,1
RA 232,1,17408
HS 25,122
v_rcp_f32 v52, v52
Z 10,60,0
XL 34,640
LL 18,56
LL 126,57
v_mul_f32 v52, v38, v52
M 10,61,1
LA 18
PK 122,16,120,56
CF 52,52,52
Z 11,72,0
WL 2
LC_ 126
PK 120,16,120,97
ds_bpermute_b32 v13, v28, v52
M 11,73,1
RA 236,1,18432
M 11,74,2
LO 17,17,123
LU 123,17
M 11,75,3
ST 116
HS 25,123
M 10,62,2
s_mul_i32 s53, s50, 25600
LL 16,58
LL 127,59
s_add_u32 s40, %[wlo], s53
s_addc_u32 s41, %[whi], 0
s_mov_b32 m0, s38
M 10,63,3
LA 16
PK 123,17,121,56
M 9,50,2
LC_ 127
PK 121,17,121,97
M 9,51,3
RA 216,1,19456
XL 35,704
DW 0
M 8,38,2
WL 3
LO 18,18,126
LU 126,18
M 8,39,3
s_add_u32 s42, s44, s64
RA 220,1,20480
HS 25,126
s_addc_u32 s43, s45, 0
XS 176
M 8,40,4
s_add_u32 s37, s37, 32768
LL 17,64
LL 112,65
s_cmp_eq_u32 s37, s75
s_cselect_b32 s37, s74, s37
M 8,41,5
LA 17
DW 1024
PK 126,18,124,56
XS 177
M 9,52,4
LC_ 112
PK 124,18,124,97
M 9,53,5
RA 224,1,21504
PO 126
XS 178
M 10,64,4
WL 5
LO 16,16,127
LU 127,16
ST 120
M 10,65,5
DW 2048
HS 25,127
XS 179
M 11,76,4
WL 3
LL 18,66
LL 113,67
M 11,77,5
s_add_u32 s40, s40, 3072
LA 18
PK 127,16,125,56
XS 180
M 11,78,0
s_addc_u32 s41, s41, 0
LC_ 113
PK 125,16,125,97
s_add_u32 m0, s38, 3072
M 11,79,1
s_add_u32 s42, s44, s65
RA 228,1,22528
PO 127
XS 181
M 10,66,0
WL 4
LO 112,17,112
LU 17,112
M 10,67,1
s_addc_u32 s43, s45, 0
RA 232,1,23552
HZ 24,17
XS 182
M 9,54,0
DW 0
LL 16,68
LL 116,69
M 9,55,1
s_lshl_b32 s53, s52, 8
LA 16
HX 25
XS 183
M 8,42,0
WL 2
LC_ 116
W1 25,1024
M 8,43,1
s_add_u32 s48, %[alo], s53
RA 236,1,24576
v_add_u32 v4, s39, v4
v_min_u32 v35, v35, v13
M 8,44,2
LO 113,18,113
LU 18,113
s_addc_u32 s49, %[ahi], 0
s_add_u32 s52, s52, %[d]
s_cmp_eq_u32 s52, %[wrap]
s_cselect_b32 s52, %[rst], s52
DW 1024
M 8,45,3
ST 124
HS 24,18
s_bitcmp1_b64 s[92:93], 48
s_cselect_b32 s58, 16, 0
s_lshr_b32 s58, 0xda43cb52, s58
v_and_b32 v11, 0x1f0, v0
v_min_u32 v11, 368, v11
v_lshlrev_b32 v11, 3, v11
XS 184
M 9,56,2
s_mul_i32 s53, s51, 24
LL 17,70
LL 117,71
s_bitcmp1_b32 s51, 0
s_addc_u32 s53, s53, 0
s_lshl_b32 s53, s53, 11
s_bitcmp1_b64 s[90:91], s51
s_cselect_b64 s[44:45], s[84:85], s[82:83]
s_add_u32 s44, s44, s53
s_addc_u32 s45, s45, 0
s_add_u32 s51, s51, %[d]
s_cmp_eq_u32 s51, %[wrap]
s_cselect_b32 s51, %[rst], s51
M 9,57,3
LA 17
DW 2048
XS 185
M 10,68,2
LC_ 117
DQ 3072
M 10,69,3
s_add_u32 s50, s50, %[d]
v_add_u32 v1, s37, v0
XS 186
M 11,80,2
WL 2
LO 116,16,116
LU 16,116
M 11,81,3
s_cmp_eq_u32 s50, %[wrap]
RA 216,1,0
HS 24,16
XS 187
M 8,46,4
s_cselect_b32 s50, %[rst], s50
LL 18,72
LL 120,73
M 8,47,5
LA 18
v_and_b32 v22, 0xfffffcff, v1
XS 188
M 9,58,4
LC_ 120
RA 220,1,1024
M 9,59,5
RV 252,22,5120
v_add_u32 v19, 1, v19
XS 189
M 10,70,4
WL 5
LO 117,17,117
LU 17,117
M 10,71,5
s_add_u32 s38, s38, 32768
RA 224,1,2048
HS 24,17
XS 190
M 11,82,4
WL 1
LL 16,74
LL 121,75
XS 191
M 11,83,5
s_cmp_eq_u32 s38, s77
LA 16
RA 228,1,3072
s_cselect_b32 s38, s76, s38
FP
s_sub_u32 s39, 0, s39
Z 0,0,0
LC_ 121
XS 192
M 0,1,1
XS 193
)"
R"(XS 194
Z 1,12,0
LO 120,18,120
LU 18,120
M 1,13,1
RA 232,1,4096
HS 24,18
Z 2,36,0
LL 17,80
LL 124,81
M 2,37,1
LA 17
XS 195
Z 3,48,0
LC_ 124
XS 196
M 3,49,1
XS 197
XS 198
Z 4,60,0
WL 3
LO 121,16,121
LU 16,121
M 4,61,1
RA 236,1,6144
HS 24,16
Z 5,72,0
LL 18,82
LL 125,83
M 5,73,1
LA 18
XS 199
M 5,74,2
LC_ 125
XS 200
M 5,75,3
XS 201
XS 202
M 4,62,2
WL 3
LO 124,17,124
LU 17,124
M 4,63,3
RA 216,1,7168
HS 24,17
M 3,50,2
LL 16,88
LL 114,89
M 3,51,3
LA 16
FR
M 2,38,2
LC_ 114
v_lshrrev_b32 v3, 9, v0
M 2,39,3
WV 31
BW 24
M 1,14,2
WL 5
LO 125,18,125
LU 18,125
M 1,15,3
RA 220,1,8192
HS 24,18
M 0,2,2
LL 17,90
LL 115,91
XS 203
M 0,3,3
LA 17
RV 40,8,6144
M 0,4,4
LC_ 115
RV 44,8,6208
XS 204
M 0,5,5
RV 48,8,6272
BR 24
M 1,16,4
WL 8
LO 16,16,114
LU 114,16
XS 205
M 1,17,5
RA 224,1,9216
HZ 25,114
M 2,40,4
LL 18,92
LL 118,93
XS 206
M 2,41,5
LA 18
PK 114,16,112,56
XS 207
M 3,52,4
LC_ 118
PK 112,16,112,97
v_lshl_add_u32 v3, v3, 2, 0
v_bfe_u32 v3, s58, v3, 4
v_mad_u32_u24 v3, v3, v32, v11
M 3,53,5
s_mov_b32 m0, %[pm]
s_nop 0
DF
v_cmp_gt_u32 vcc, v19, v15
XS 208
M 4,64,4
WL 6
LO 17,17,115
LU 115,17
M 4,65,5
s_add_u32 s42, s44, s62
RA 228,1,10240
HS 25,115
XS 209
M 5,76,4
LL 16,94
LL 119,95
M 5,77,5
s_cbranch_vccnz .Lk1s12
.Lk1r12:
LA 16
PK 115,17,113,56
XS 210
M 5,78,0
s_addc_u32 s43, s45, 0
LC_ 119
PK 113,17,113,97
M 5,79,1
ST 112
HX 24
XS 211
M 4,66,0
WL 3
LO 18,18,118
LU 118,18
M 4,67,1
RA 232,1,11264
HS 25,118
XS 212
M 3,54,0
LL 17,96
LL 122,97
M 3,55,1
LA 17
PK 118,18,116,56
XS 213
M 2,42,0
LC_ 122
PK 116,18,116,97
M 2,43,1
RV 252,22,5392
PO 118
XS 214
M 1,18,0
WL 4
LO 16,16,119
LU 119,16
M 1,19,1
s_add_u32 s42, s44, s63
RA 236,1,12288
HS 25,119
XS 215
M 0,6,0
LL 18,98
LL 123,99
M 0,7,1
s_addc_u32 s43, s45, 0
LA 18
PK 119,16,117,56
XS 216
M 0,8,2
LC_ 123
PK 117,16,117,97
M 0,9,3
W1 24,0
PO 119
XS 217
M 1,20,2
WL 5
LO 17,17,122
LU 122,17
M 1,21,3
RA 216,1,13312
HS 25,122
XS 218
M 2,44,2
LL 16,100
LL 126,101
M 2,45,3
LA 16
PK 122,17,120,56
XS 219
M 3,56,2
LC_ 126
PK 120,17,120,97
M 3,57,3
WV 29
ST 116
XS 220
BW 25
M 4,68,2
WL 5
LO 18,18,123
LU 123,18
M 4,69,3
RA 220,1,14336
BR 25
HS 25,123
XS 221
LZ 40,24
WV 27
BW 26
M 5,80,2
LL 17,102
LL 127,103
M 5,81,3
LA 17
PK 123,18,121,56
XS 222
M 0,10,4
LC_ 127
PK 121,18,121,97
XS 223
M 0,11,5
WV 23
DY
RV 40,8,6336
M 1,22,4
WL 4
BR 26
LO 16,16,126
LU 126,16
M 1,23,5
s_add_u32 s42, s44, s64
RA 224,1,15360
HS 25,126
LS 44,25
s_addc_u32 s43, s45, 0
BW 27
M 2,46,4
LL 18,56
LL 104,57
M 2,47,5
LA 18
PK 126,16,124,56
M 3,58,4
LC_ 104
RV 44,8,6400
PK 124,16,124,97
M 3,59,5
WV 23
ST 120
BR 27
PO 126
M 4,70,4
WL 6
LO 17,17,127
LU 127,17
M 4,71,5
s_add_u32 s42, s44, s65
RA 228,1,16384
HS 25,127
LS 48,26
s_addc_u32 s43, s45, 0
BW 28
M 5,82,4
LL 16,58
LL 105,59
M 5,83,5
LA 16
RV 48,8,6464
PK 127,17,125,56
Z 6,0,0
LC_ 105
PK 125,17,125,97
M 6,1,1
ds_read_b64 v[14:15], v5 offset:0
BR 28
PO 127
Z 7,12,0
WL 9
LO 104,18,104
LU 18,104
M 7,13,1
WL 7
RA 232,1,17408
HZ 26,18
LS 40,27
WV 23
BW 29
Z 8,36,0
WV 22
LL 17,60
LL 108,61
M 8,37,1
WL 2
LA 17
ST 124
RV 40,8,6528
Z 9,48,0
s_mul_i32 s53, s50, 25600
LC_ 108
BR 29
M 9,49,1
s_add_u32 s40, %[wlo], s53
v_subrev_u32 v5, s39, v5
HX 25
LS 44,28
s_addc_u32 s41, %[whi], 0
BW 30
Z 10,60,0
LO 105,16,105
LU 16,105
M 10,61,1
s_mov_b32 m0, s38
RA 236,1,18432
HS 26,16
Z 11,72,0
RV 44,8,6592
LL 18,62
LL 109,63
M 11,73,1
LA 18
W1 25,1024
BR 30
M 11,74,2
LC_ 109
DW 0
M 11,75,3
WL 7
v_add_f32 v14, v14, v15
v_add_u32 v4, s39, v4
LS 48,29
WV 23
BW 31
M 10,62,2
LO 108,17,108
LU 17,108
M 10,63,3
s_add_u32 s37, s37, 32768
RA 216,1,19456
HS 26,17
M 9,50,2
s_cmp_eq_u32 s37, s75
RV 48,8,6656
LL 16,64
LL 112,65
M 9,51,3
s_cselect_b32 s37, s74, s37
LA 16
DW 1024
BR 31
M 8,38,2
LC_ 112
v_add_f32_dpp v14, v14, v14 quad_perm:[1,0,3,2] row_mask:0xf bank_mask:0xf
M 8,39,3
WL 7
v_add_f32_dpp v14, v14, v14 quad_perm:[2,3,0,1] row_mask:0xf bank_mask:0xf
v_lshrrev_b32 v3, 9, v0
LS 40,30
WV 23
BW 32
M 7,14,2
WL 7
LO 109,18,109
LU 18,109
M 7,15,3
RA 220,1,20480
RV 40,8,6720
HS 26,18
M 6,2,2
BR 32
LL 17,66
LL 113,67
M 6,3,3
LA 17
v_add_f32_dpp v14, v14, v14 row_half_mirror row_mask:0xf bank_mask:0xf
M 6,4,4
WL 6
LC_ 113
v_cvt_f64_f32 v[14:15], v14
M 6,5,5
s_mov_b64 exec, s[54:55]
SP
s_mov_b64 exec, -1
HX 26
LS 44,31
WV 21
BW 33
M 7,16,4
WL 7
RV 44,8,6784
LO 112,16,112
LU 16,112
M 7,17,5
RA 224,1,21504
HZ 24,16
M 8,40,4
BR 33
)"
R"(LL 18,68
LL 116,69
M 8,41,5
WL 6
LA 18
v_lshl_add_u32 v3, v3, 2, 8
LS 48,32
WV 20
BW 34
M 9,52,4
LC_ 116
RV 48,8,6848
M 9,53,5
RA 240,7,0
BR 34
M 10,64,4
WL 10
LO 113,17,113
LU 17,113
M 10,65,5
RA 228,1,22528
HS 24,17
v_bfe_u32 v3, s58, v3, 4
v_mad_u32_u24 v3, v3, v32, v11
M 11,76,4
WV 18
LL 16,70
LL 117,71
M 11,77,5
WL 7
LA 16
DW 2048
M 11,78,0
s_mul_i32 s53, s51, 24
LC_ 117
M 11,79,1
s_bitcmp1_b32 s51, 0
LS 40,33
s_addc_u32 s53, s53, 0
BW 35
M 10,66,0
WL 4
RA 244,7,256
LO 116,18,116
LU 18,116
M 10,67,1
s_lshl_b32 s53, s53, 11
RA 232,1,23552
HS 24,18
M 9,54,0
s_bitcmp1_b64 s[90:91], s51
BR 35
LL 17,72
LL 120,73
M 9,55,1
s_cselect_b64 s[44:45], s[84:85], s[82:83]
LA 17
s_add_u32 s44, s44, s53
s_addc_u32 s45, s45, 0
s_bitcmp1_b64 s[92:93], s51
s_cselect_b64 s[94:95], s[88:89], s[86:87]
s_add_u32 s94, s94, s53
s_addc_u32 s95, s95, 0
s_add_u32 s51, s51, %[d]
s_cmp_eq_u32 s51, %[wrap]
s_cselect_b32 s51, %[rst], s51
s_mul_i32 s53, %[d], 0
s_add_u32 s60, s53, %[sb1]
s_add_u32 s61, s60, 512
LS 44,34
s_add_u32 s40, s40, 3072
RA 248,7,512
M 8,42,0
s_addc_u32 s41, s41, 0
LC_ 120
s_add_u32 m0, s38, 3072
M 8,43,1
DW 0
M 7,18,0
WL 7
LO 117,16,117
LU 16,117
M 7,19,1
WL 3
RA 236,1,24576
HS 24,16
LS 48,35
RA 252,7,768
M 6,6,0
LL 18,74
LL 121,75
s_nop 3
v_mul_f32 v36, 0xbb2aaaab, v36
M 6,7,1
LA 18
DW 1024
v_mul_f32 v37, 0xbb2aaaab, v37
v_mul_f32 v38, 0xbb2aaaab, v38
M 6,8,2
LC_ 121
DW 2048
v_mul_f32 v39, 0xbb2aaaab, v39
M 6,9,3
s_lshl_b32 s53, s52, 8
v_add_u32 v1, s37, v0
LK 40,240,24,0
LK 44,242,24,2
s_add_u32 s48, %[alo], s53
RA 240,7,1024
LK 48,244,25,0
s_nop 0
CF 224,40,41
LK 248,246,25,2
s_addc_u32 s49, %[ahi], 0
RA 244,7,1280
CF 225,42,43
M 7,20,2
WL 6
LO 120,17,120
LU 17,120
M 7,21,3
s_add_u32 s52, s52, %[d]
RA 216,1,0
HS 24,17
M 8,44,2
s_cmp_eq_u32 s52, %[wrap]
LL 16,76
LL 124,77
CF 226,44,45
M 8,45,3
s_cselect_b32 s52, %[rst], s52
LA 16
DQ 3072
CF 227,46,47
LK 40,248,26,0
CF 228,48,49
LK 44,250,26,2
RA 248,7,1536
CF 229,50,51
GZ 24
WL 7
CF 230,248,249
CF 231,250,251
LK 48,252,27,0
CF 232,40,41
LK 248,254,27,2
RA 252,7,1792
CF 233,42,43
GS 25
WL 5
CF 234,44,45
CF 235,46,47
LK 40,240,28,0
CF 236,48,49
LK 44,242,28,2
RA 240,7,2048
CF 237,50,51
GS 26
WL 5
CF 238,248,249
CF 239,250,251
LK 48,244,29,0
CF 240,40,41
LK 248,246,29,2
RA 244,7,2304
CF 241,42,43
GS 27
CF 242,44,45
CF 243,46,47
M 9,56,2
LC_ 124
v_and_b32 v22, 0xfffffcff, v1
M 9,57,3
WL 4
LK 40,248,30,0
CF 244,48,49
LK 44,250,30,2
RA 248,7,2560
CF 245,50,51
GS 28
WL 4
CF 246,248,249
CF 247,250,251
LK 48,252,31,0
CF 40,40,41
LK 248,254,31,2
RA 252,7,2816
CF 42,42,43
GS 29
CF 44,44,45
CF 46,46,47
M 10,68,2
LO 121,18,121
LU 18,121
M 10,69,3
RA 220,1,1024
HS 24,18
M 11,80,2
LL 17,78
LL 125,79
M 11,81,3
LA 17
RV 252,22,5120
M 6,10,4
LC_ 125
DY
M 6,11,5
s_add_u32 s50, s50, %[d]
AW 0,40
AW 1,42
M 7,22,4
WL 6
LO 124,16,124
LU 16,124
M 7,23,5
s_cmp_eq_u32 s50, %[wrap]
RA 224,1,2048
HS 24,16
M 8,46,4
s_cselect_b32 s50, %[rst], s50
LL 18,80
LL 106,81
M 8,47,5
LA 18
v_add_u32 v19, 1, v19
M 9,58,4
s_add_u32 s38, s38, 32768
LC_ 106
AW 2,44
M 9,59,5
AW 3,46
LK 40,240,32,0
CF 48,48,49
LK 44,242,32,2
CF 50,50,51
GS 30
CF 248,248,249
CF 250,250,251
M 10,70,4
WL 3
LO 125,17,125
LU 17,125
M 10,71,5
RA 228,1,3072
HS 24,17
M 11,82,4
LL 16,82
LL 107,83
M 11,83,5
LA 16
HX 24
Z 0,0,0
s_cmp_eq_u32 s38, s77
LC_ 107
AW 4,48
M 0,1,1
s_cselect_b32 s38, s76, s38
FP
AW 5,50
Z 1,12,0
WL 4
LO 18,18,106
LU 106,18
M 1,13,1
s_sub_u32 s39, 0, s39
RA 232,1,4096
HZ 27,106
Z 2,36,0
LL 17,84
LL 110,85
M 2,37,1
LA 17
PK 106,18,104,56
Z 3,48,0
LC_ 110
PK 104,18,104,97
M 3,49,1
AW 6,248
AW 7,250
LK 48,244,33,0
CF 40,40,41
LK 248,246,33,2
CF 42,42,43
GS 31
CF 44,44,45
CF 46,46,47
Z 4,60,0
WL 4
LO 16,16,107
LU 107,16
M 4,61,1
s_add_u32 s42, s94, s60
RA 236,1,6144
HS 27,107
Z 5,72,0
LL 18,86
LL 111,87
M 5,73,1
s_addc_u32 s43, s95, 0
LA 18
PK 107,16,105,56
M 5,74,2
LC_ 111
PK 105,16,105,97
M 5,75,3
SU 104
AW 8,40
M 4,62,2
WL 3
LO 17,17,110
LU 110,17
M 4,63,3
RA 216,1,7168
HS 27,110
M 3,50,2
LL 16,88
LL 114,89
M 3,51,3
LA 16
PK 110,17,108,56
AW 9,42
AW 10,44
M 2,38,2
LC_ 114
PK 108,17,108,97
AW 11,46
M 2,39,3
FR
PO 110
LK 40,248,34,0
CF 48,48,49
LK 44,250,34,2
CF 50,50,51
GS 32
CF 248,248,249
CF 250,250,251
M 1,14,2
WL 4
LO 18,18,111
LU 111,18
M 1,15,3
s_add_u32 s42, s94, s61
RA 220,1,8192
HS 27,111
M 0,2,2
LL 17,90
LL 115,91
AW 12,48
AW 13,50
M 0,3,3
s_addc_u32 s43, s95, 0
LA 17
PK 111,18,109,56
AW 14,248
AW 15,250
LK 48,252,35,0
CF 40,40,41
LK 248,254,35,2
s_mov_b32 m0, %[pm]
CF 42,42,43
HX 27
GS 33
CF 44,44,45
CF 46,46,47
M 0,4,4
LC_ 115
PK 109,18,109,97
M 0,5,5
DF
PO 111
M 1,16,4
WL 4
LO 16,16,114
LU 114,16
M 1,17,5
RA 224,1,9216
HZ 25,114
AW 16,40
M 2,40,4
LL 18,92
LL 118,93
AW 17,42
AW 18,44
M 2,41,5
LA 18
RV 36,8,6912
PK 114,16,112,56
AW 19,46
M 3,52,4
LC_ 118
PK 112,16,112,97
M 3,53,5
WL 7
SU 108
v_cmp_gt_u32 vcc, v19, v15
GS 34
CF 48,48,49
CF 50,50,51
CF 248,248,249
M 4,64,4
)"
R"(WL 4
LO 17,17,115
LU 115,17
CF 250,250,251
AW 20,48
M 4,65,5
s_add_u32 s42, s44, s62
RA 228,1,10240
HS 25,115
AW 21,50
AW 22,248
M 5,76,4
LL 16,94
LL 119,95
AW 23,250
M 5,77,5
s_cbranch_vccnz .Lk1s13
.Lk1r13:
LA 16
PK 115,17,113,56
GS 35
s_addc_u32 s43, s45, 0
W1 24,0
s_nop 5
v_cndmask_b32 v52, v52, v53, s[78:79]
M 5,78,0
LC_ 119
PK 113,17,113,97
v_cndmask_b32 v54, v54, v55, s[78:79]
M 5,79,1
W1 26,2048
v_cndmask_b32 v52, v52, v54, s[80:81]
M 4,66,0
WL 5
LO 18,18,118
LU 118,18
v_mul_f32 v52, 0x3b2aaaab, v52
M 4,67,1
RA 232,1,11264
HS 25,118
v_med3_f32 v53, v52, v39, v37
M 3,54,0
LL 17,96
LL 122,97
LG 52,53
M 3,55,1
LA 17
PK 118,18,116,56
C 52,52,52
M 2,42,0
LC_ 122
PK 116,18,116,97
v_lshlrev_b32 v52, 16, v52
M 2,43,1
RV 252,22,5392
PO 118
v_add_f32 v52, v36, v52
M 1,18,0
WL 5
LO 16,16,119
LU 119,16
C 52,52,52
M 1,19,1
RA 236,1,12288
HS 25,119
M 0,6,0
LL 18,98
LL 123,99
v_lshlrev_b32 v52, 16, v52
M 0,7,1
LA 18
PK 119,16,117,56
v_sqrt_f32 v52, v52
M 0,8,2
LC_ 123
PK 117,16,117,97
C 52,52,52
M 0,9,3
ST 112
PO 119
v_lshlrev_b32 v52, 16, v52
M 1,20,2
WL 4
LO 17,17,122
LU 122,17
v_rcp_f32 v52, v52
M 1,21,3
s_add_u32 s42, s44, s63
RA 216,1,13312
HS 25,122
v_mul_f32 v52, v38, v52
M 2,44,2
s_addc_u32 s43, s45, 0
LL 16,100
LL 126,101
CF 52,52,52
M 2,45,3
LA 16
PK 122,17,120,56
ds_bpermute_b32 v13, v28, v52
M 3,56,2
LC_ 126
PK 120,17,120,97
M 3,57,3
ST 116
AR 248,0
M 4,68,2
WL 4
LO 18,18,123
LU 123,18
M 4,69,3
s_add_u32 s42, s44, s64
RA 220,1,14336
HS 25,123
M 5,80,2
LL 17,102
LL 127,103
M 5,81,3
s_addc_u32 s43, s45, 0
LA 17
PK 123,18,121,56
M 0,10,4
LC_ 127
PK 121,18,121,97
M 0,11,5
ST 120
AR 249,1
M 1,22,4
WL 3
LO 16,16,126
LU 126,16
M 1,23,5
RA 224,1,15360
HS 25,126
M 2,46,4
LL 18,56
LL 104,57
M 2,47,5
LA 18
PK 126,16,124,56
M 3,58,4
LC_ 104
PK 124,16,124,97
AR 250,2
M 3,59,5
ds_read_b64 v[14:15], v5 offset:0
PO 126
M 4,70,4
WL 4
LO 17,17,127
LU 127,17
AR 251,3
M 4,71,5
s_add_u32 s42, s44, s65
RA 228,1,16384
HS 25,127
AR 52,4
M 5,82,4
LL 16,58
LL 105,59
AR 53,5
M 5,83,5
s_addc_u32 s43, s45, 0
LA 16
PK 127,17,125,56
Z 6,0,0
LC_ 105
PK 125,17,125,97
AR 54,6
M 6,1,1
PO 127
v_subrev_u32 v5, s39, v5
AR 55,7
Z 7,12,0
WL 4
LO 104,18,104
LU 18,104
XS 224
M 7,13,1
RA 232,1,17408
HZ 26,18
Z 8,36,0
WL 4
LL 17,60
LL 108,61
XS 225
M 8,37,1
s_mul_i32 s53, s50, 25600
LA 17
v_add_f32 v14, v14, v15
XS 226
Z 9,48,0
s_add_u32 s40, %[wlo], s53
LC_ 108
v_add_f32_dpp v14, v14, v14 quad_perm:[1,0,3,2] row_mask:0xf bank_mask:0xf
M 9,49,1
s_addc_u32 s41, %[whi], 0
v_add_f32_dpp v14, v14, v14 quad_perm:[2,3,0,1] row_mask:0xf bank_mask:0xf
HX 25
XS 227
Z 10,60,0
WL 3
LO 105,16,105
LU 16,105
XS 228
M 10,61,1
s_mov_b32 m0, s38
RA 236,1,18432
HS 26,16
XS 229
Z 11,72,0
LL 18,62
LL 109,63
M 11,73,1
LA 18
W1 25,1024
XS 230
M 11,74,2
LC_ 109
W1 27,3072
XS 231
M 11,75,3
ST 124
v_add_f32_dpp v14, v14, v14 row_half_mirror row_mask:0xf bank_mask:0xf
v_min_u32 v35, v35, v13
M 10,62,2
WL 5
LO 108,17,108
LU 17,108
M 10,63,3
RA 216,1,19456
HS 26,17
XS 232
M 9,50,2
LL 16,64
LL 112,65
XS 233
M 9,51,3
LA 16
DW 0
XS 234
M 8,38,2
s_add_u32 s37, s37, 32768
LC_ 112
v_cvt_f64_f32 v[14:15], v14
M 8,39,3
s_cmp_eq_u32 s37, s75
s_mov_b64 exec, s[54:55]
SP
s_mov_b64 exec, -1
v_add_u32 v4, s39, v4
XS 235
M 7,14,2
WL 4
LO 109,18,109
LU 18,109
XS 236
M 7,15,3
s_cselect_b32 s37, s74, s37
RA 220,1,20480
HS 26,18
M 6,2,2
LL 17,66
LL 113,67
XS 237
M 6,3,3
s_mul_i32 s53, %[d], 196608
LA 17
HX 26
XS 238
M 6,4,4
s_add_u32 s53, s53, s57
LC_ 113
XS 239
M 6,5,5
s_add_u32 s46, %[xlo], s53
XS 240
XS 241
M 7,16,4
WL 3
LO 112,16,112
LU 16,112
M 7,17,5
s_addc_u32 s47, %[xhi], 0
RA 224,1,21504
HZ 24,16
M 8,40,4
s_bitcmp1_b64 s[92:93], 48
LL 18,68
LL 116,69
XS 242
M 8,41,5
s_cselect_b32 s58, 16, 0
LA 18
XS 243
M 9,52,4
s_lshr_b32 s58, 0xda43cb52, s58
LC_ 116
v_lshrrev_b32 v3, 6, v0
v_and_b32 v3, 12, v3
v_bfe_u32 v3, s58, v3, 4
v_mad_u32_u24 v3, v3, v32, v21
M 9,53,5
DW 1024
XS 244
M 10,64,4
WL 3
LO 113,17,113
LU 17,113
XS 245
M 10,65,5
RA 228,1,22528
HS 24,17
M 11,76,4
LL 16,70
LL 117,71
XS 246
M 11,77,5
LA 16
XL 36,0
XS 247
M 11,78,0
LC_ 117
DW 2048
M 11,79,1
XL 37,64
XS 248
M 10,66,0
WL 3
LO 116,18,116
LU 18,116
XS 249
M 10,67,1
s_mul_i32 s53, s51, 24
RA 232,1,23552
HS 24,18
XS 250
M 9,54,0
s_bitcmp1_b32 s51, 0
LL 17,72
LL 120,73
M 9,55,1
s_addc_u32 s53, s53, 0
LA 17
s_lshl_b32 s53, s53, 11
s_bitcmp1_b64 s[90:91], s51
s_cselect_b64 s[44:45], s[84:85], s[82:83]
s_add_u32 s44, s44, s53
s_addc_u32 s45, s45, 0
s_bitcmp1_b64 s[92:93], s51
s_cselect_b64 s[94:95], s[88:89], s[86:87]
s_add_u32 s94, s94, s53
s_addc_u32 s95, s95, 0
s_add_u32 s51, s51, %[d]
s_cmp_eq_u32 s51, %[wrap]
s_cselect_b32 s51, %[rst], s51
XS 251
M 8,42,0
s_add_u32 s40, s40, 3072
LC_ 120
s_addc_u32 s41, s41, 0
s_add_u32 m0, s38, 3072
AW 0,248
M 8,43,1
XL 38,128
AW 1,249
M 7,18,0
WL 3
LO 117,16,117
LU 16,117
M 7,19,1
RA 236,1,24576
HS 24,16
AW 2,250
M 6,6,0
LL 18,74
LL 121,75
AW 3,251
M 6,7,1
LA 18
DW 0
XS 52
M 6,8,2
LC_ 121
XL 39,192
M 6,9,3
DW 1024
v_add_u32 v1, s37, v0
XS 53
M 7,20,2
WL 3
LO 120,17,120
LU 17,120
XS 54
M 7,21,3
RA 216,1,0
HS 24,17
M 8,44,2
LL 16,76
LL 124,77
)"
R"(XS 55
M 8,45,3
LA 16
XL 40,256
AW 4,52
M 9,56,2
LC_ 124
DW 2048
AW 5,53
M 9,57,3
XL 41,320
v_and_b32 v22, 0xfffffcff, v1
M 10,68,2
WL 3
LO 121,18,121
LU 18,121
AW 6,54
M 10,69,3
s_lshl_b32 s53, s52, 8
RA 220,1,1024
HS 24,18
AW 7,55
M 11,80,2
LL 17,78
LL 125,79
AR 248,8
M 11,81,3
s_add_u32 s48, %[alo], s53
LA 17
RV 252,22,5120
M 6,10,4
s_addc_u32 s49, %[ahi], 0
LC_ 125
AR 249,9
M 6,11,5
s_add_u32 s52, s52, %[d]
AR 250,10
AR 251,11
M 7,22,4
WL 4
LO 124,16,124
LU 16,124
M 7,23,5
s_cmp_eq_u32 s52, %[wrap]
RA 224,1,2048
HS 24,16
M 8,46,4
s_cselect_b32 s52, %[rst], s52
LL 18,80
LL 106,81
XS 248
M 8,47,5
LA 18
DQ 3072
XS 249
M 9,58,4
LC_ 106
XL 42,384
M 9,59,5
s_add_u32 s50, s50, %[d]
XS 250
XS 251
M 10,70,4
WL 3
LO 125,17,125
LU 17,125
M 10,71,5
RA 228,1,3072
HS 24,17
AW 8,248
M 11,82,4
LL 16,82
LL 107,83
M 11,83,5
LA 16
HX 24
s_cmp_eq_u32 s50, %[wrap]
s_cselect_b32 s50, %[rst], s50
XL 43,448
AW 9,249
Z 0,0,0
WV 38
LC_ 107
XL 44,512
v_add_u32 v19, 1, v19
s_add_u32 s38, s38, 32768
s_cmp_eq_u32 s38, s77
s_cselect_b32 s38, s76, s38
FP
s_sub_u32 s39, 0, s39
AW 10,250
M 0,1,1
XL 45,576
AW 11,251
Z 1,12,0
WL 4
LO 18,18,106
LU 106,18
M 1,13,1
RA 232,1,4096
HZ 27,106
AR 52,12
Z 4,60,0
LL 17,84
LL 110,85
AR 53,13
M 4,61,1
LA 17
PK 106,18,104,56
AR 54,14
Z 5,72,0
WL 5
LC_ 110
XL 46,640
PK 104,18,104,97
M 5,73,1
RA 236,1,6144
AR 55,15
M 5,74,2
WL 5
LO 16,16,107
LU 107,16
XS 52
M 5,75,3
FR
HS 27,107
M 4,62,2
LL 18,86
LL 111,87
XS 53
M 4,63,3
LA 18
PK 107,16,105,56
XS 54
M 1,14,2
LC_ 111
XL 47,704
PK 105,16,105,97
XS 55
M 1,15,3
s_add_u32 s42, s94, s60
RA 216,1,7168
AW 12,52
M 0,2,2
WL 4
LO 17,17,110
LU 110,17
M 0,3,3
s_addc_u32 s43, s95, 0
RA 220,1,8192
HS 27,110
AW 13,53
M 0,4,4
LL 16,88
LL 114,89
AW 14,54
M 0,5,5
LA 16
PK 110,17,108,56
M 1,16,4
LC_ 114
PK 108,17,108,97
AW 15,55
M 1,17,5
WV 16
RA 224,1,9216
PO 110
AR 248,16
M 4,64,4
WL 5
LO 18,18,111
LU 111,18
SU 104
AR 249,17
M 4,65,5
s_mul_i32 s53, %[d], 393216
HS 27,111
v_cmp_gt_u32 vcc, v19, v15
M 5,76,4
WL 3
LL 17,90
LL 115,91
s_add_u32 s53, s53, s57
s_add_u32 s46, %[xlo], s53
s_addc_u32 s47, %[xhi], 0
s_bitcmp1_b64 s[92:93], 48
s_cselect_b32 s58, 16, 0
s_lshr_b32 s58, 0xfe987610, s58
v_lshrrev_b32 v3, 4, v0
v_and_b32 v3, 12, v3
v_bfe_u32 v3, s58, v3, 4
v_bfe_u32 v11, v0, 4, 2
v_lshl_add_u32 v3, v3, 2, v11
v_mul_u32_u24 v3, 0x300, v3
v_lshrrev_b32 v11, 8, v0
v_lshl_add_u32 v3, v11, 4, v3
AR 250,18
M 5,77,5
s_add_u32 s42, s94, s61
LA 17
PK 111,18,109,56
AR 251,19
M 5,78,0
s_cbranch_vccnz .Lk1s14
.Lk1r14:
LC_ 115
PK 109,18,109,97
M 5,79,1
s_addc_u32 s43, s95, 0
RA 228,1,10240
PO 111
XS 248
BW 36
M 4,66,0
WL 5
LO 16,16,114
LU 114,16
XS 249
M 4,67,1
RA 232,1,11264
RV 40,8,6144
HZ 25,114
XS 250
M 1,18,0
RV 44,8,6208
LL 18,92
LL 118,93
M 1,19,1
LA 18
RV 48,8,6272
PK 114,16,112,56
XS 251
M 0,6,0
WL 6
LC_ 118
BR 36
PK 112,16,112,97
AW 16,248
M 0,7,1
WV 15
RV 252,22,5392
ds_read_b64 v[14:15], v5 offset:0
W1 24,0
W1 26,2048
HX 27
BW 37
AW 17,249
M 0,8,2
BR 37
LO 17,17,115
LU 115,17
M 0,9,3
WL 6
RA 236,1,12288
HS 25,115
AW 18,250
LZ 40,36
WV 14
BW 38
M 1,20,2
LL 16,94
LL 119,95
AW 19,251
M 1,21,3
LA 16
RV 40,8,6336
PK 115,17,113,56
AR 52,20
M 4,68,2
LC_ 119
BR 38
PK 113,17,113,97
M 4,69,3
WL 6
RA 216,1,13312
AR 53,21
LS 44,37
WV 12
BW 39
M 5,80,2
WL 7
LO 18,18,118
LU 118,18
SU 108
AR 54,22
M 5,81,3
s_add_u32 s42, s44, s62
RA 220,1,14336
HS 25,118
s_addc_u32 s43, s45, 0
XL 48,0
RV 44,8,6400
M 0,10,4
BR 39
LL 17,96
LL 122,97
AR 55,23
M 0,11,5
WL 5
LA 17
PK 118,18,116,56
XS 52
LS 48,38
WV 12
BW 40
M 1,22,4
LC_ 122
PK 116,18,116,97
XS 53
M 1,23,5
RA 224,1,15360
RV 48,8,6464
PO 118
M 4,70,4
LO 16,16,119
LU 119,16
ST 112
XS 54
M 4,71,5
s_add_u32 s42, s44, s63
BR 40
HS 25,119
XS 55
M 5,82,4
WL 6
LL 18,98
LL 123,99
AW 20,52
M 5,83,5
s_addc_u32 s43, s45, 0
LA 18
PK 119,16,117,56
LS 40,39
WV 11
BW 41
Z 6,0,0
LC_ 123
PK 117,16,117,97
v_add_f32 v14, v14, v15
s_nop 1
v_add_f32_dpp v14, v14, v14 quad_perm:[1,0,3,2] row_mask:0xf bank_mask:0xf
s_nop 1
v_add_f32_dpp v14, v14, v14 quad_perm:[2,3,0,1] row_mask:0xf bank_mask:0xf
s_nop 1
v_add_f32_dpp v14, v14, v14 row_half_mirror row_mask:0xf bank_mask:0xf
v_cvt_f64_f32 v[14:15], v14
s_mov_b64 exec, s[54:55]
SP
s_mov_b64 exec, -1
s_nop 0
ds_read_b64 v[14:15], v5 offset:2048
RV 40,8,6528
AW 21,53
M 6,1,1
RA 228,1,16384
BR 41
PO 119
AW 22,54
Z 7,12,0
WL 10
LO 17,17,122
LU 122,17
AW 23,55
M 7,13,1
WL 7
RA 232,1,17408
HS 25,122
LS 44,40
WV 10
BW 42
Z 10,60,0
RV 44,8,6592
LL 16,100
LL 126,101
M 10,61,1
LA 16
BR 42
PK 122,17,120,56
Z 2,72,0
WL 6
LC_ 126
PK 120,17,120,97
M 2,73,1
WL 6
RA 236,1,18432
v_subrev_u32 v5, s39, v5
LS 48,41
WV 9
BW 43
M 2,74,2
RV 48,8,6656
LO 18,18,123
LU 123,18
M 2,75,3
XL 49,64
BR 43
HS 25,123
M 10,62,2
s_mul_i32 s53, s50, 25600
LL 17,102
)"
R"(LL 127,103
s_add_u32 s40, %[wlo], s53
s_addc_u32 s41, %[whi], 0
s_mov_b32 m0, s38
M 10,63,3
WL 5
LA 17
PK 123,18,121,56
LS 40,42
WV 9
BW 44
M 7,14,2
LC_ 127
RV 40,8,6720
PK 121,18,121,97
M 7,15,3
WV 8
RA 216,1,19456
BR 44
DW 0
M 6,2,2
WL 6
LO 16,16,126
LU 126,16
ST 116
M 6,3,3
s_add_u32 s42, s44, s64
RA 220,1,20480
HS 25,126
s_addc_u32 s43, s45, 0
LS 44,43
s_add_u32 s37, s37, 32768
BW 45
M 6,4,4
s_cmp_eq_u32 s37, s75
LL 18,56
LL 104,57
s_cselect_b32 s37, s74, s37
M 6,5,5
LA 18
PK 126,16,124,56
v_add_f32 v14, v14, v15
s_nop 1
v_add_f32_dpp v14, v14, v14 quad_perm:[1,0,3,2] row_mask:0xf bank_mask:0xf
s_nop 1
v_add_f32_dpp v14, v14, v14 quad_perm:[2,3,0,1] row_mask:0xf bank_mask:0xf
s_nop 1
v_add_f32_dpp v14, v14, v14 row_half_mirror row_mask:0xf bank_mask:0xf
v_cvt_f64_f32 v[14:15], v14
s_mov_b64 exec, s[54:55]
SP
s_mov_b64 exec, -1
DW 1024
RV 44,8,6784
M 7,16,4
LC_ 104
BR 45
PK 124,16,124,97
M 7,17,5
WL 6
RA 224,1,21504
PO 126
LS 48,44
WV 11
BW 46
M 10,64,4
LO 17,17,127
LU 127,17
ST 120
M 10,65,5
DW 2048
RV 48,8,6848
HS 25,127
M 2,76,4
WL 8
RA 240,7,0
LL 16,58
LL 105,59
M 2,77,5
s_add_u32 s40, s40, 3072
LA 16
BR 46
PK 127,17,125,56
M 2,78,0
s_addc_u32 s41, s41, 0
LC_ 105
PK 125,17,125,97
s_add_u32 m0, s38, 3072
M 2,79,1
WL 7
RA 228,1,22528
PO 127
LS 40,45
WV 12
BW 47
M 10,66,0
RA 244,7,256
LO 104,18,104
LU 18,104
M 10,67,1
WL 4
RA 232,1,23552
XL 50,128
BR 47
HZ 26,18
M 7,18,0
s_add_u32 s42, s44, s65
LL 17,60
LL 108,61
s_addc_u32 s43, s45, 0
DW 0
M 7,19,1
s_lshl_b32 s53, s52, 8
LA 17
HX 25
LS 44,46
s_add_u32 s48, %[alo], s53
RA 248,7,512
M 6,6,0
WL 2
LC_ 108
W1 25,1024
W1 27,3072
M 6,7,1
s_addc_u32 s49, %[ahi], 0
RA 236,1,24576
v_add_u32 v4, s39, v4
LS 48,47
s_add_u32 s52, s52, %[d]
RA 252,7,768
M 6,8,2
LO 105,16,105
LU 16,105
s_cmp_eq_u32 s52, %[wrap]
s_cselect_b32 s52, %[rst], s52
DW 1024
v_mul_f32 v36, 0xbb2aaaab, v36
M 6,9,3
DW 2048
HS 26,16
v_mul_f32 v37, 0xbb2aaaab, v37
v_mul_f32 v38, 0xbb2aaaab, v38
M 7,20,2
LL 18,62
LL 109,63
v_mul_f32 v39, 0xbb2aaaab, v39
M 7,21,3
LA 18
ST 124
LK 40,240,36,0
LK 44,242,36,2
s_mul_i32 s53, s51, 24
RA 240,7,1024
v_add_u32 v1, s37, v0
LK 48,244,37,0
CF 40,40,41
LK 248,246,37,2
s_bitcmp1_b32 s51, 0
RA 244,7,1280
CF 42,42,43
M 10,68,2
s_addc_u32 s53, s53, 0
LC_ 109
RA 216,1,0
M 10,69,3
s_lshl_b32 s53, s53, 11
v_and_b32 v22, 0xfffffcff, v1
CF 44,44,45
CF 46,46,47
M 2,80,2
WL 6
LO 108,17,108
LU 17,108
AW 24,40
AW 25,42
M 2,81,3
s_bitcmp1_b64 s[90:91], s51
RA 220,1,1024
HS 26,17
s_cselect_b64 s[44:45], s[84:85], s[82:83]
s_add_u32 s44, s44, s53
s_addc_u32 s45, s45, 0
s_bitcmp1_b64 s[92:93], s51
s_cselect_b64 s[94:95], s[88:89], s[86:87]
s_add_u32 s94, s94, s53
s_addc_u32 s95, s95, 0
s_add_u32 s51, s51, %[d]
s_cmp_eq_u32 s51, %[wrap]
s_cselect_b32 s51, %[rst], s51
DQ 3072
AW 26,44
AW 27,46
LK 40,248,38,0
CF 48,48,49
LK 44,250,38,2
s_add_u32 s50, s50, %[d]
RA 248,7,1536
CF 50,50,51
GZ 36
s_cmp_eq_u32 s50, %[wrap]
CF 248,248,249
CF 250,250,251
M 6,10,4
s_cselect_b32 s50, %[rst], s50
LL 16,72
LL 120,73
M 6,11,5
LA 16
RV 252,22,5120
M 7,22,4
LC_ 120
v_add_u32 v19, 1, v19
AW 28,48
M 7,23,5
WL 7
RA 224,1,2048
AW 29,50
AW 30,248
AW 31,250
LK 48,252,39,0
CF 40,40,41
LK 248,254,39,2
RA 252,7,1792
CF 42,42,43
GS 37
CF 44,44,45
CF 46,46,47
M 10,70,4
WL 7
LO 109,18,109
LU 18,109
M 10,71,5
XL 51,192
HS 26,18
M 2,82,4
WL 3
LL 17,74
LL 121,75
M 2,83,5
s_add_u32 s38, s38, 32768
LA 17
RA 228,1,3072
s_cmp_eq_u32 s38, s77
s_cselect_b32 s38, s76, s38
FP
s_sub_u32 s39, 0, s39
AW 32,40
AW 33,42
Z 0,0,0
LC_ 121
HX 26
AW 34,44
AW 35,46
M 0,1,1
LK 40,240,40,0
CF 48,48,49
LK 44,242,40,2
RA 240,7,2048
CF 50,50,51
GS 38
CF 248,248,249
CF 250,250,251
Z 1,12,0
WL 7
LO 120,16,120
LU 16,120
M 1,13,1
RA 232,1,4096
HZ 24,16
AW 36,48
Z 4,60,0
LL 18,76
LL 124,77
AW 37,50
AW 38,248
M 4,61,1
LA 18
FR
AW 39,250
LK 48,244,41,0
CF 40,40,41
LK 248,246,41,2
RA 244,7,2304
CF 42,42,43
GS 39
CF 44,44,45
CF 46,46,47
Z 8,72,0
WL 7
LC_ 124
AW 40,40
M 8,73,1
RA 236,1,6144
AW 41,42
M 8,74,2
WL 7
LO 121,17,121
LU 17,121
M 8,75,3
XL 52,256
HS 24,17
AW 42,44
AW 43,46
LK 40,248,42,0
CF 48,48,49
LK 44,250,42,2
RA 248,7,2560
CF 50,50,51
GS 40
CF 248,248,249
CF 250,250,251
M 4,62,2
LL 16,78
LL 125,79
M 4,63,3
LA 16
RA 216,1,7168
M 1,14,2
WL 6
LC_ 125
v_cmp_gt_u32 vcc, v19, v15
M 1,15,3
AW 44,48
AW 45,50
AW 46,248
M 0,2,2
WL 4
LO 124,18,124
LU 18,124
AW 47,250
M 0,3,3
RA 220,1,8192
HS 24,18
LK 48,252,43,0
CF 40,40,41
LK 248,254,43,2
RA 252,7,2816
CF 42,42,43
GS 41
CF 44,44,45
CF 46,46,47
M 0,4,4
s_cbranch_vccnz .Lk1s15
.Lk1r15:
LL 17,80
LL 106,81
M 0,5,5
LA 17
ds_read_b64 v[14:15], v5 offset:0
AW 48,40
AW 49,42
M 1,16,4
LC_ 106
AW 50,44
AW 51,46
M 1,17,5
RA 224,1,9216
LK 40,240,44,0
CF 48,48,49
LK 44,242,44,2
CF 50,50,51
GS 42
CF 248,248,249
CF 250,250,251
M 4,64,4
WL 6
LO 125,16,125
LU 16,125
M 4,65,5
HS 24,16
AW 52,48
M 8,76,4
WL 5
LL 18,82
LL 107,83
AW 53,50
AW 54,248
AW 55,250
M 8,77,5
LA 18
RA 228,1,10240
LK 48,244,45,0
CF 40,40,41
LK 248,246,45,2
CF 42,42,43
HX 24
GS 43
CF 44,44,45
CF 46,46,47
M 8,78,0
LC_ 107
W1 24,0
M 8,79,1
W1 26,2048
AW 56,40
M 4,66,0
WL 6
LO 17,17,106
LU 106,17
AW 57,42
)"
R"(AW 58,44
M 4,67,1
RA 232,1,11264
HZ 27,106
AW 59,46
LK 40,248,46,0
CF 48,48,49
LK 44,250,46,2
CF 50,50,51
v_add_f32 v14, v14, v15
GS 44
CF 248,248,249
CF 250,250,251
M 1,18,0
LL 16,84
LL 110,85
M 1,19,1
LA 16
PK 106,17,104,56
M 0,6,0
WL 5
LC_ 110
PK 104,17,104,97
AW 60,48
AW 61,50
AW 62,248
M 0,7,1
RV 252,22,5392
v_add_f32_dpp v14, v14, v14 quad_perm:[1,0,3,2] row_mask:0xf bank_mask:0xf
AW 63,250
LK 48,252,47,0
CF 40,40,41
LK 248,254,47,2
CF 42,42,43
v_add_f32_dpp v14, v14, v14 quad_perm:[2,3,0,1] row_mask:0xf bank_mask:0xf
GS 45
CF 44,44,45
CF 46,46,47
M 0,8,2
WL 6
LO 18,18,107
LU 107,18
M 0,9,3
RA 236,1,12288
XL 53,320
HS 27,107
M 1,20,2
s_add_u32 s42, s94, s60
LL 17,86
LL 111,87
AW 64,40
AW 65,42
M 1,21,3
s_addc_u32 s43, s95, 0
LA 17
RV 36,8,6912
PK 107,18,105,56
AW 66,44
AW 67,46
M 4,68,2
LC_ 111
PK 105,18,105,97
M 4,69,3
RA 216,1,13312
v_add_f32_dpp v14, v14, v14 row_half_mirror row_mask:0xf bank_mask:0xf
GS 46
CF 48,48,49
CF 50,50,51
CF 248,248,249
CF 250,250,251
M 8,80,2
WL 4
LO 16,16,110
LU 110,16
AW 68,48
AW 69,50
AW 70,248
M 8,81,3
RA 220,1,14336
HS 27,110
AW 71,250
s_nop 1
GS 47
SU 104
v_cvt_f64_f32 v[14:15], v14
s_nop 5
v_cndmask_b32 v52, v52, v53, s[78:79]
M 0,10,4
LL 18,96
LL 122,97
v_cndmask_b32 v54, v54, v55, s[78:79]
M 0,11,5
LA 18
PK 110,16,108,56
v_cndmask_b32 v52, v52, v54, s[80:81]
M 1,22,4
LC_ 122
PK 108,16,108,97
v_mul_f32 v52, 0x3b2aaaab, v52
M 1,23,5
WL 5
RA 224,1,15360
PO 110
v_med3_f32 v53, v52, v39, v37
M 4,70,4
WL 5
LO 17,17,111
LU 111,17
s_mov_b64 exec, s[54:55]
SP
s_mov_b64 exec, -1
LG 52,53
s_nop 0
C 52,52,52
M 4,71,5
ds_read_b64 v[14:15], v5 offset:2048
HS 27,111
v_lshlrev_b32 v52, 16, v52
M 8,82,4
WL 4
LL 16,98
LL 123,99
v_add_f32 v52, v36, v52
M 8,83,5
LA 16
PK 111,17,109,56
C 52,52,52
Z 6,0,0
LC_ 123
PK 109,17,109,97
v_lshlrev_b32 v52, 16, v52
M 6,1,1
RA 228,1,16384
PO 111
v_sqrt_f32 v52, v52
s_nop 0
C 52,52,52
Z 7,12,0
WL 5
LO 18,18,122
LU 122,18
v_lshlrev_b32 v52, 16, v52
M 7,13,1
RA 232,1,17408
XL 54,384
HZ 25,122
v_rcp_f32 v52, v52
Z 10,60,0
s_add_u32 s42, s94, s61
LL 17,64
LL 126,65
s_addc_u32 s43, s95, 0
v_mul_f32 v52, v38, v52
M 10,61,1
LA 17
PK 122,18,120,56
v_subrev_u32 v5, s39, v5
HX 27
CF 52,52,52
Z 11,72,0
WL 2
LC_ 126
PK 120,18,120,97
ds_bpermute_b32 v13, v28, v52
M 11,73,1
RA 236,1,18432
v_add_f32 v14, v14, v15
M 11,74,2
LO 16,16,123
LU 123,16
M 11,75,3
SU 108
HS 25,123
v_add_f32_dpp v14, v14, v14 quad_perm:[1,0,3,2] row_mask:0xf bank_mask:0xf
M 10,62,2
s_mul_i32 s53, s50, 25600
LL 18,66
LL 127,67
s_add_u32 s40, %[wlo], s53
s_addc_u32 s41, %[whi], 0
s_mov_b32 m0, s38
M 10,63,3
LA 18
PK 123,16,121,56
v_add_f32_dpp v14, v14, v14 quad_perm:[2,3,0,1] row_mask:0xf bank_mask:0xf
M 7,14,2
LC_ 127
PK 121,16,121,97
M 7,15,3
RA 216,1,19456
DW 0
v_add_f32_dpp v14, v14, v14 row_half_mirror row_mask:0xf bank_mask:0xf
M 6,2,2
WL 3
LO 17,17,126
LU 126,17
M 6,3,3
s_add_u32 s42, s44, s64
RA 220,1,20480
HS 25,126
v_cvt_f64_f32 v[14:15], v14
s_addc_u32 s43, s45, 0
AR 248,24
M 6,4,4
s_add_u32 s37, s37, 32768
LL 16,56
LL 104,57
s_cmp_eq_u32 s37, s75
s_cselect_b32 s37, s74, s37
AR 249,25
M 6,5,5
LA 16
PK 126,17,124,56
s_mov_b64 exec, s[54:55]
SP
s_mov_b64 exec, -1
DW 1024
AR 250,26
M 7,16,4
LC_ 104
PK 124,17,124,97
AR 251,27
AR 52,28
M 7,17,5
RA 224,1,21504
PO 126
AR 53,29
M 10,64,4
WL 5
LO 18,18,127
LU 127,18
ST 120
AR 54,30
AR 55,31
M 10,65,5
DW 2048
XL 55,448
HS 25,127
XS 248
M 11,76,4
WL 3
LL 17,58
LL 105,59
XS 249
XS 250
M 11,77,5
s_add_u32 s40, s40, 3072
LA 17
PK 127,18,125,56
XS 251
M 11,78,0
s_addc_u32 s41, s41, 0
LC_ 105
PK 125,18,125,97
s_add_u32 m0, s38, 3072
AW 24,248
AW 25,249
M 11,79,1
s_add_u32 s42, s44, s65
RA 228,1,22528
PO 127
AW 26,250
M 10,66,0
WL 4
LO 104,16,104
LU 16,104
AW 27,251
XS 52
M 10,67,1
s_addc_u32 s43, s45, 0
RA 232,1,23552
HZ 26,16
XS 53
M 7,18,0
DW 0
LL 18,60
LL 108,61
XS 54
XS 55
M 7,19,1
s_lshl_b32 s53, s52, 8
LA 18
HX 25
AW 28,52
M 6,6,0
WL 2
LC_ 108
W1 25,1024
W1 27,3072
AW 29,53
AW 30,54
M 6,7,1
s_add_u32 s48, %[alo], s53
RA 236,1,24576
v_add_u32 v4, s39, v4
AW 31,55
M 6,8,2
LO 105,17,105
LU 17,105
s_addc_u32 s49, %[ahi], 0
s_add_u32 s52, s52, %[d]
s_cmp_eq_u32 s52, %[wrap]
s_cselect_b32 s52, %[rst], s52
DW 1024
v_min_u32 v35, v35, v13
AR 248,32
M 6,9,3
ST 124
HS 26,17
AR 249,33
M 7,20,2
s_mul_i32 s53, s51, 24
LL 16,62
LL 109,63
s_bitcmp1_b32 s51, 0
s_addc_u32 s53, s53, 0
s_lshl_b32 s53, s53, 11
s_bitcmp1_b64 s[90:91], s51
s_cselect_b64 s[44:45], s[84:85], s[82:83]
s_add_u32 s44, s44, s53
s_addc_u32 s45, s45, 0
s_bitcmp1_b64 s[92:93], s51
s_cselect_b64 s[94:95], s[88:89], s[86:87]
s_add_u32 s94, s94, s53
s_addc_u32 s95, s95, 0
s_add_u32 s51, s51, %[d]
s_cmp_eq_u32 s51, %[wrap]
s_cselect_b32 s51, %[rst], s51
AR 250,34
AR 251,35
M 7,21,3
LA 16
DW 2048
XS 248
M 10,68,2
LC_ 109
XL 56,512
XS 249
XS 250
M 10,69,3
DQ 3072
v_add_u32 v1, s37, v0
XS 251
M 11,80,2
WL 2
LO 108,18,108
LU 18,108
AW 32,248
AW 33,249
M 11,81,3
s_add_u32 s50, s50, %[d]
RA 216,1,0
HS 26,18
AW 34,250
M 6,10,4
s_cmp_eq_u32 s50, %[wrap]
LL 17,72
LL 120,73
AW 35,251
AR 52,36
M 6,11,5
s_cselect_b32 s50, %[rst], s50
LA 17
v_and_b32 v22, 0xfffffcff, v1
AR 53,37
M 7,22,4
LC_ 120
RA 220,1,1024
AR 54,38
AR 55,39
M 7,23,5
RV 252,22,5120
v_add_u32 v19, 1, v19
XS 52
M 10,70,4
WL 5
LO 109,16,109
LU 16,109
XS 53
XS 54
M 10,71,5
s_add_u32 s38, s38, 32768
RA 224,1,2048
HS 26,16
XS 55
M 11,82,4
WL 1
LL 18,74
LL 121,75
AW 36,52
AW 37,53
M 11,83,5
s_cmp_eq_u32 s38, s77
LA 18
RA 228,1,3072
s_cselect_b32 s38, s76, s38
FP
s_sub_u32 s39, 0, s39
AW 38,54
Z 0,0,0
)"
R"(LC_ 121
HX 26
AW 39,55
M 0,1,1
AR 248,40
AR 249,41
Z 1,12,0
LO 120,17,120
LU 17,120
AR 250,42
M 1,13,1
RA 232,1,4096
HZ 24,17
AR 251,43
XS 248
Z 2,24,0
LL 16,88
LL 124,89
XS 249
M 2,25,1
LA 16
XL 57,576
XS 250
XS 251
Z 3,36,0
LC_ 124
AW 40,248
M 3,37,1
AW 41,249
AW 42,250
Z 4,60,0
WL 3
LO 121,18,121
LU 18,121
AW 43,251
M 4,61,1
RA 236,1,6144
HS 24,18
AR 52,44
AR 53,45
Z 5,72,0
LL 17,90
LL 125,91
AR 54,46
M 5,73,1
LA 17
AR 55,47
XS 52
M 5,74,2
LC_ 125
XS 53
M 5,75,3
XS 54
XS 55
M 4,62,2
WL 3
LO 124,16,124
LU 16,124
AW 44,52
M 4,63,3
RA 216,1,7168
HS 24,16
AW 45,53
AW 46,54
M 3,38,2
LL 18,80
LL 106,81
AW 47,55
M 3,39,3
LA 18
FR
AR 248,48
AR 249,49
M 2,26,2
LC_ 106
AR 250,50
M 2,27,3
AR 251,51
XS 248
M 1,14,2
WL 4
LO 125,17,125
LU 17,125
XS 249
M 1,15,3
RA 220,1,8192
HS 24,17
XS 250
XS 251
M 0,2,2
LL 16,82
LL 107,83
AW 48,248
M 0,3,3
LA 16
XL 58,640
AW 49,249
AW 50,250
M 0,4,4
s_mov_b32 m0, %[pm]
LC_ 107
DF
AW 51,251
M 0,5,5
WL 4
v_cmp_gt_u32 vcc, v19, v15
HX 24
AR 52,52
AR 53,53
M 1,16,4
WL 3
LO 18,18,106
LU 106,18
AR 54,54
M 1,17,5
RA 224,1,9216
HZ 27,106
AR 55,55
XS 52
M 2,28,4
s_cbranch_vccnz .Lk1s16
.Lk1r16:
LL 17,84
LL 110,85
XS 53
M 2,29,5
LA 17
PK 106,18,104,56
XS 54
XS 55
M 3,40,4
LC_ 110
PK 104,18,104,97
AW 52,52
M 3,41,5
W1 24,0
AW 53,53
AW 54,54
M 4,64,4
WL 4
LO 16,16,107
LU 107,16
AW 55,55
M 4,65,5
s_add_u32 s42, s94, s60
RA 228,1,10240
HS 27,107
AR 248,56
M 5,76,4
LL 18,86
LL 111,87
AR 249,57
AR 250,58
M 5,77,5
s_addc_u32 s43, s95, 0
LA 18
PK 107,16,105,56
AR 251,59
M 5,78,0
LC_ 111
PK 105,16,105,97
XS 248
XS 249
M 5,79,1
W1 26,2048
XS 250
M 4,66,0
WL 5
LO 17,17,110
LU 110,17
XS 251
AW 56,248
M 4,67,1
RA 232,1,11264
HS 27,110
AW 57,249
M 3,42,0
LL 16,96
LL 122,97
AW 58,250
AW 59,251
M 3,43,1
LA 16
PK 110,17,108,56
AR 52,60
M 2,30,0
LC_ 122
PK 108,17,108,97
AR 53,61
AR 54,62
M 2,31,1
RV 252,22,5392
PO 110
AR 55,63
M 1,18,0
WL 5
LO 18,18,111
LU 111,18
XS 52
XS 53
M 1,19,1
RA 236,1,12288
HS 27,111
XS 54
M 0,6,0
LL 17,98
LL 123,99
XS 55
AW 60,52
M 0,7,1
LA 17
PK 111,18,109,56
AW 61,53
M 0,8,2
LC_ 123
PK 109,18,109,97
AW 62,54
AW 63,55
M 0,9,3
SU 104
PO 111
AR 248,64
M 1,20,2
WL 4
LO 16,16,122
LU 122,16
AR 249,65
AR 250,66
M 1,21,3
RA 216,1,13312
HZ 25,122
AR 251,67
M 2,32,2
LL 18,100
LL 126,101
XS 248
XS 249
M 2,33,3
LA 18
XL 59,704
PK 122,16,120,56
XS 250
M 3,44,2
s_add_u32 s42, s94, s61
LC_ 126
ds_read_b64 v[14:15], v5 offset:0
PK 120,16,120,97
XS 251
AW 64,248
M 3,45,3
s_addc_u32 s43, s95, 0
SU 108
HX 27
AW 65,249
M 4,68,2
WL 4
LO 17,17,123
LU 123,17
AW 66,250
AW 67,251
M 4,69,3
s_add_u32 s42, s44, s64
RA 220,1,14336
HS 25,123
AR 52,68
M 5,80,2
LL 16,102
LL 127,103
AR 53,69
AR 54,70
M 5,81,3
s_addc_u32 s43, s45, 0
LA 16
PK 123,17,121,56
AR 55,71
M 0,10,4
LC_ 127
PK 121,17,121,97
XS 52
XS 53
M 0,11,5
ST 120
RV 40,8,6144
XS 54
M 1,22,4
WL 5
LO 18,18,126
LU 126,18
XS 55
AW 68,52
M 1,23,5
RA 224,1,15360
HS 25,126
AW 69,53
M 2,34,4
LL 17,56
LL 104,57
AW 70,54
AW 71,55
M 2,35,5
LA 17
PK 126,18,124,56
M 3,46,4
LC_ 104
PK 124,18,124,97
M 3,47,5
RV 44,8,6208
PO 126
M 4,70,4
WL 5
LO 16,16,127
LU 127,16
M 4,71,5
s_add_u32 s42, s44, s65
RA 228,1,16384
HS 25,127
M 5,82,4
LL 18,58
LL 105,59
M 5,83,5
s_addc_u32 s43, s45, 0
LA 18
PK 127,16,125,56
Z 6,0,0
WV 41
LC_ 105
PK 125,16,125,97
M 6,1,1
WL 7
RV 48,8,6272
PO 127
LZ 40,48
WV 38
RV 40,8,6336
v_add_f32 v14, v14, v15
s_nop 1
v_add_f32_dpp v14, v14, v14 quad_perm:[1,0,3,2] row_mask:0xf bank_mask:0xf
s_nop 1
v_add_f32_dpp v14, v14, v14 quad_perm:[2,3,0,1] row_mask:0xf bank_mask:0xf
s_nop 1
v_add_f32_dpp v14, v14, v14 row_half_mirror row_mask:0xf bank_mask:0xf
Z 7,12,0
WL 6
LO 104,17,104
LU 17,104
M 7,13,1
WL 5
RA 232,1,17408
HZ 26,17
v_cvt_f64_f32 v[14:15], v14
LS 44,49
RV 44,8,6400
HX 25
Z 8,24,0
LL 16,60
LL 108,61
M 8,25,1
LA 16
s_mov_b64 exec, s[54:55]
SP
s_mov_b64 exec, -1
Z 9,36,0
WV 26
LC_ 108
ds_read_b64 v[14:15], v5 offset:2048
M 9,37,1
WL 6
W1 25,1024
v_subrev_u32 v5, s39, v5
LS 48,50
WL 6
RV 48,8,6464
LS 40,51
RV 40,8,6528
Z 10,60,0
LO 105,18,105
LU 18,105
M 10,61,1
RA 236,1,18432
HS 26,18
Z 11,72,0
WV 25
LL 17,62
LL 109,63
M 11,73,1
WL 7
LA 17
W1 27,3072
LS 44,52
RV 44,8,6592
M 11,74,2
WV 21
LC_ 109
ST 124
M 11,75,3
s_mul_i32 s53, s50, 25600
v_add_u32 v4, s39, v4
M 10,62,2
WL 5
LO 108,16,108
)"
R"(LU 16,108
M 10,63,3
s_add_u32 s40, %[wlo], s53
RA 216,1,19456
HS 26,16
LS 48,53
s_addc_u32 s41, %[whi], 0
RV 48,8,6656
v_add_f32 v14, v14, v15
LS 40,54
s_mov_b32 m0, s38
RV 40,8,6720
v_add_f32_dpp v14, v14, v14 quad_perm:[1,0,3,2] row_mask:0xf bank_mask:0xf
M 9,38,2
WV 15
LL 18,64
LL 112,65
M 9,39,3
WL 4
LA 18
DW 0
M 8,26,2
s_add_u32 s37, s37, 32768
LC_ 112
v_add_f32_dpp v14, v14, v14 quad_perm:[2,3,0,1] row_mask:0xf bank_mask:0xf
M 8,27,3
s_cmp_eq_u32 s37, s75
v_add_f32_dpp v14, v14, v14 row_half_mirror row_mask:0xf bank_mask:0xf
LS 44,55
s_cselect_b32 s37, s74, s37
RV 44,8,6784
v_cvt_f64_f32 v[14:15], v14
M 7,14,2
WL 6
LO 109,17,109
LU 17,109
M 7,15,3
RA 220,1,20480
HS 26,17
M 6,2,2
WV 9
LL 16,66
LL 113,67
M 6,3,3
WL 5
LA 16
s_mov_b64 exec, s[54:55]
SP
s_mov_b64 exec, -1
LS 48,56
WL 5
RV 48,8,6848
RA 240,7,0
LS 40,57
RA 244,7,256
M 6,4,4
LC_ 113
DW 1024
M 6,5,5
WV 10
DW 2048
M 7,16,4
WL 6
LO 112,18,112
LU 18,112
M 7,17,5
s_mul_i32 s53, s51, 24
RA 224,1,21504
HZ 29,18
LS 44,58
s_bitcmp1_b32 s51, 0
RA 248,7,512
M 8,28,4
s_addc_u32 s53, s53, 0
LL 17,68
LL 116,69
M 8,29,5
s_lshl_b32 s53, s53, 11
LA 17
M 9,40,4
s_bitcmp1_b64 s[90:91], s51
LC_ 116
M 9,41,5
s_cselect_b64 s[44:45], s[84:85], s[82:83]
WL 7
WV 8
LS 48,59
s_add_u32 s44, s44, s53
RA 252,7,768
M 10,64,4
WL 5
LO 113,16,113
LU 16,113
M 10,65,5
s_addc_u32 s45, s45, 0
RA 228,1,22528
HS 29,16
M 11,76,4
LL 18,70
LL 117,71
M 11,77,5
s_bitcmp1_b64 s[92:93], s51
LA 18
v_mul_f32 v36, 0xbb2aaaab, v36
M 11,78,0
s_cselect_b64 s[94:95], s[88:89], s[86:87]
LC_ 117
v_mul_f32 v37, 0xbb2aaaab, v37
M 11,79,1
s_add_u32 s94, s94, s53
v_mul_f32 v38, 0xbb2aaaab, v38
v_mul_f32 v39, 0xbb2aaaab, v39
M 10,66,0
WL 4
LO 116,17,116
LU 17,116
M 10,67,1
s_addc_u32 s95, s95, 0
RA 232,1,23552
HS 29,17
LK 40,240,48,0
LK 44,242,48,2
s_add_u32 s51, s51, %[d]
RA 240,7,1024
LK 48,244,49,0
s_nop 0
CF 40,40,41
LK 248,246,49,2
s_cmp_eq_u32 s51, %[wrap]
RA 244,7,1280
CF 42,42,43
M 9,42,0
s_cselect_b32 s51, %[rst], s51
LL 16,72
LL 120,73
M 9,43,1
s_mul_i32 s53, %[d], 16384
LA 16
s_add_u32 s62, s53, %[sb1]
s_add_u32 s63, s62, 512
M 8,30,0
s_add_u32 s40, s40, 3072
LC_ 120
s_addc_u32 s41, s41, 0
s_add_u32 m0, s38, 3072
M 8,31,1
DW 0
CF 44,44,45
M 7,18,0
WL 5
LO 117,18,117
LU 18,117
M 7,19,1
RA 236,1,24576
HS 29,18
M 6,6,0
LL 17,74
LL 121,75
M 6,7,1
LA 17
DW 1024
M 6,8,2
LC_ 121
DW 2048
M 6,9,3
s_lshl_b32 s53, s52, 8
v_add_u32 v1, s37, v0
v_pk_add_f16 v26, v26, v29
M 7,20,2
WL 3
LO 120,16,120
LU 16,120
M 7,21,3
s_add_u32 s48, %[alo], s53
RA 216,1,0
HZ 24,16
M 8,32,2
s_addc_u32 s49, %[ahi], 0
LL 18,76
LL 124,77
M 8,33,3
s_add_u32 s52, s52, %[d]
LA 18
v_and_b32 v22, 0xfffffcff, v1
M 9,44,2
s_cmp_eq_u32 s52, %[wrap]
LC_ 124
HX 26
M 9,45,3
s_cselect_b32 s52, %[rst], s52
DQ 3072
CF 46,46,47
M 10,68,2
WL 3
LO 121,17,121
LU 17,121
M 10,69,3
s_add_u32 s50, s50, %[d]
RA 220,1,1024
HS 24,17
M 11,80,2
LL 16,78
LL 125,79
M 11,81,3
s_cmp_eq_u32 s50, %[wrap]
LA 16
RV 252,22,5120
M 6,10,4
s_cselect_b32 s50, %[rst], s50
LC_ 125
AW 72,40
M 6,11,5
v_add_u32 v19, 1, v19
AW 73,42
M 7,22,4
WL 4
LO 124,18,124
LU 18,124
AW 74,44
AW 75,46
M 7,23,5
s_add_u32 s38, s38, 32768
RA 224,1,2048
HS 24,18
LK 40,248,50,0
CF 48,48,49
LK 44,250,50,2
s_cmp_eq_u32 s38, s77
RA 248,7,1536
CF 50,50,51
GZ 48
s_cselect_b32 s38, s76, s38
CF 248,248,249
CF 250,250,251
M 8,34,4
LL 17,80
LL 106,81
M 8,35,5
LA 17
FP
AW 76,48
M 9,46,4
s_sub_u32 s39, 0, s39
LC_ 106
AW 77,50
AW 78,248
AW 79,250
M 9,47,5
LK 48,252,51,0
CF 40,40,41
LK 248,254,51,2
RA 252,7,1792
CF 42,42,43
GS 49
CF 44,44,45
CF 46,46,47
M 10,70,4
WL 6
LO 125,16,125
LU 16,125
M 10,71,5
RA 228,1,3072
HS 24,16
M 11,82,4
LL 18,82
LL 107,83
AW 80,40
AW 81,42
AW 82,44
M 11,83,5
LA 18
HX 24
AW 83,46
Z 0,0,0
LC_ 107
M 0,1,1
LK 40,240,52,0
CF 48,48,49
LK 44,242,52,2
RA 240,7,2048
CF 50,50,51
GS 50
CF 248,248,249
CF 250,250,251
Z 1,12,0
WL 5
LO 17,17,106
LU 106,17
AW 84,48
AW 85,50
AW 86,248
M 1,13,1
RA 232,1,4096
HZ 27,106
AW 87,250
LK 48,244,53,0
CF 40,40,41
LK 248,246,53,2
RA 244,7,2304
CF 42,42,43
GS 51
CF 44,44,45
CF 46,46,47
Z 2,24,0
LL 16,84
LL 110,85
M 2,25,1
LA 16
PK 106,17,104,56
Z 3,36,0
LC_ 110
PK 104,17,104,97
AW 88,40
AW 89,42
M 3,37,1
AW 90,44
AW 91,46
LK 40,248,54,0
CF 48,48,49
LK 44,250,54,2
RA 248,7,2560
CF 50,50,51
GS 52
CF 248,248,249
CF 250,250,251
Z 4,60,0
WL 6
LO 18,18,107
LU 107,18
M 4,61,1
s_add_u32 s42, s94, s60
RA 236,1,6144
HS 27,107
Z 5,72,0
LL 17,86
LL 111,87
AW 92,48
AW 93,50
M 5,73,1
s_addc_u32 s43, s95, 0
LA 17
PK 107,18,105,56
AW 94,248
AW 95,250
LK 48,252,55,0
CF 40,40,41
LK 248,254,55,2
RA 252,7,2816
CF 42,42,43
GS 53
CF 44,44,45
CF 46,46,47
M 5,74,2
LC_ 111
PK 105,18,105,97
M 5,75,3
SU 104
AW 96,40
M 4,62,2
WL 5
LO 16,16,110
LU 110,16
AW 97,42
M 4,63,3
RA 216,1,7168
HS 27,110
AW 98,44
AW 99,46
LK 40,240,56,0
CF 48,48,49
LK 44,242,56,2
CF 50,50,51
GS 54
CF 248,248,249
CF 250,250,251
M 3,38,2
LL 18,88
LL 114,89
M 3,39,3
LA 18
PK 110,16,108,56
M 2,26,2
LC_ 114
PK 108,16,108,97
AW 100,48
AW 101,50
M 2,27,3
FR
PO 110
AW 102,248
AW 103,250
LK 48,244,57,0
CF 40,40,41
LK 248,246,57,2
CF 42,42,43
GS 55
CF 44,44,45
CF 46,46,47
M 1,14,2
WL 4
)"
R"(LO 17,17,111
LU 111,17
M 1,15,3
s_add_u32 s42, s94, s61
RA 220,1,8192
HS 27,111
M 0,2,2
LL 16,90
LL 115,91
AW 104,40
AW 105,42
AW 106,44
M 0,3,3
s_addc_u32 s43, s95, 0
LA 16
PK 111,17,109,56
AW 107,46
LK 40,248,58,0
CF 48,48,49
LK 44,250,58,2
s_mov_b32 m0, %[pm]
CF 50,50,51
GS 56
CF 248,248,249
CF 250,250,251
M 0,4,4
LC_ 115
PK 109,17,109,97
M 0,5,5
DF
PO 111
M 1,16,4
WL 4
LO 18,18,114
LU 114,18
AW 108,48
AW 109,50
AW 110,248
M 1,17,5
RA 224,1,9216
HZ 29,114
AW 111,250
LK 48,252,59,0
CF 40,40,41
LK 248,254,59,2
WL 4
CF 42,42,43
v_cmp_gt_u32 vcc, v19, v15
GS 57
CF 44,44,45
CF 46,46,47
M 2,28,4
LL 17,92
LL 118,93
M 2,29,5
LA 17
PK 114,18,112,56
AW 112,40
M 3,40,4
s_cbranch_vccnz .Lk1s17
.Lk1r17:
LC_ 118
PK 112,18,112,97
AW 113,42
AW 114,44
AW 115,46
M 3,41,5
W1 24,0
RV 36,8,6912
GS 58
CF 48,48,49
CF 50,50,51
M 4,64,4
WL 5
LO 16,16,115
LU 115,16
CF 248,248,249
CF 250,250,251
M 4,65,5
RA 228,1,10240
HS 29,115
AW 116,48
AW 117,50
AW 118,248
M 5,76,4
LL 18,94
LL 119,95
AW 119,250
M 5,77,5
LA 18
PK 115,16,113,56
GS 59
W1 26,2048
s_nop 6
v_cndmask_b32 v52, v52, v53, s[78:79]
M 5,78,0
LC_ 119
PK 113,16,113,97
v_cndmask_b32 v54, v54, v55, s[78:79]
M 5,79,1
SU 108
v_cndmask_b32 v52, v52, v54, s[80:81]
M 4,66,0
WL 4
LO 17,17,118
LU 118,17
v_mul_f32 v52, 0x3b2aaaab, v52
M 4,67,1
s_add_u32 s42, s94, s62
RA 232,1,11264
HS 29,118
v_med3_f32 v53, v52, v39, v37
M 3,42,0
s_addc_u32 s43, s95, 0
LL 16,96
LL 122,97
LG 52,53
M 3,43,1
LA 16
PK 118,17,116,56
C 52,52,52
M 2,30,0
LC_ 122
PK 116,17,116,97
v_lshlrev_b32 v52, 16, v52
M 2,31,1
RV 252,22,5392
PO 118
v_add_f32 v52, v36, v52
M 1,18,0
WL 4
LO 18,18,119
LU 119,18
C 52,52,52
M 1,19,1
RA 236,1,12288
HS 29,119
M 0,6,0
LL 17,98
LL 123,99
v_lshlrev_b32 v52, 16, v52
M 0,7,1
LA 17
PK 119,18,117,56
v_sqrt_f32 v52, v52
M 0,8,2
LC_ 123
PK 117,18,117,97
C 52,52,52
M 0,9,3
SU 112
PO 119
v_lshlrev_b32 v52, 16, v52
M 1,20,2
WL 4
LO 16,16,122
LU 122,16
v_rcp_f32 v52, v52
M 1,21,3
s_add_u32 s42, s94, s63
RA 216,1,13312
HZ 25,122
v_mul_f32 v52, v38, v52
M 2,32,2
s_addc_u32 s43, s95, 0
LL 18,100
LL 126,101
CF 52,52,52
M 2,33,3
LA 18
PK 122,16,120,56
ds_bpermute_b32 v13, v28, v52
M 3,44,2
LC_ 126
ds_read_b64 v[14:15], v5 offset:0
PK 120,16,120,97
M 3,45,3
SU 116
v_pk_add_f16 v27, v27, v29
M 4,68,2
WL 5
LO 17,17,123
LU 123,17
M 4,69,3
s_add_u32 s42, s44, s64
RA 220,1,14336
HS 25,123
M 5,80,2
LL 16,102
LL 127,103
M 5,81,3
s_addc_u32 s43, s45, 0
LA 16
PK 123,17,121,56
M 0,10,4
LC_ 127
PK 121,17,121,97
M 0,11,5
ST 120
HX 27
M 1,22,4
WL 4
LO 18,18,126
LU 126,18
M 1,23,5
RA 224,1,15360
HS 25,126
AR 248,72
M 2,34,4
LL 17,56
LL 104,57
AR 249,73
M 2,35,5
LA 17
PK 126,18,124,56
AR 250,74
M 3,46,4
LC_ 104
PK 124,18,124,97
AR 251,75
M 3,47,5
WL 6
PO 126
v_add_f32 v14, v14, v15
s_nop 1
v_add_f32_dpp v14, v14, v14 quad_perm:[1,0,3,2] row_mask:0xf bank_mask:0xf
AR 52,76
M 4,70,4
WL 3
LO 16,16,127
LU 127,16
AR 53,77
M 4,71,5
s_add_u32 s42, s44, s65
RA 228,1,16384
HS 25,127
v_add_f32_dpp v14, v14, v14 quad_perm:[2,3,0,1] row_mask:0xf bank_mask:0xf
AR 54,78
M 5,82,4
LL 18,58
LL 105,59
AR 55,79
M 5,83,5
s_addc_u32 s43, s45, 0
LA 18
PK 127,16,125,56
XS 248
Z 6,0,0
LC_ 105
PK 125,16,125,97
v_add_f32_dpp v14, v14, v14 row_half_mirror row_mask:0xf bank_mask:0xf
XS 249
M 6,1,1
PO 127
v_cvt_f64_f32 v[14:15], v14
XS 250
Z 7,12,0
WL 3
LO 104,17,104
LU 17,104
XS 251
AW 72,248
M 7,13,1
RA 232,1,17408
HZ 26,17
AW 73,249
Z 8,24,0
LL 16,60
LL 108,61
AW 74,250
M 8,25,1
s_mul_i32 s53, s50, 25600
LA 16
s_mov_b64 exec, s[54:55]
SP
s_mov_b64 exec, -1
AW 75,251
Z 9,36,0
s_add_u32 s40, %[wlo], s53
LC_ 108
ds_read_b64 v[14:15], v5 offset:2048
XS 52
M 9,37,1
s_addc_u32 s41, %[whi], 0
v_subrev_u32 v5, s39, v5
HX 25
XS 53
Z 10,60,0
WL 4
LO 105,18,105
LU 18,105
XS 54
M 10,61,1
s_mov_b32 m0, s38
RA 236,1,18432
HS 26,18
XS 55
Z 11,72,0
LL 17,62
LL 109,63
AW 76,52
M 11,73,1
LA 17
W1 25,1024
AW 77,53
M 11,74,2
LC_ 109
W1 27,3072
AW 78,54
AW 79,55
M 11,75,3
ST 124
v_add_u32 v4, s39, v4
v_min_u32 v35, v35, v13
M 10,62,2
WL 6
LO 108,16,108
LU 16,108
AR 248,80
M 10,63,3
RA 216,1,19456
HS 26,16
AR 249,81
M 9,38,2
LL 18,64
LL 112,65
AR 250,82
M 9,39,3
WL 6
LA 18
DW 0
AR 251,83
M 8,26,2
s_add_u32 s37, s37, 32768
LC_ 112
v_add_f32 v14, v14, v15
XS 248
M 8,27,3
s_cmp_eq_u32 s37, s75
v_add_f32_dpp v14, v14, v14 quad_perm:[1,0,3,2] row_mask:0xf bank_mask:0xf
XS 249
M 7,14,2
WL 4
LO 109,17,109
LU 17,109
XS 250
M 7,15,3
s_cselect_b32 s37, s74, s37
RA 220,1,20480
HS 26,17
XS 251
M 6,2,2
LL 16,66
LL 113,67
v_add_f32_dpp v14, v14, v14 quad_perm:[2,3,0,1] row_mask:0xf bank_mask:0xf
AW 80,248
AW 81,249
M 6,3,3
s_mul_i32 s53, %[d], 393216
LA 16
v_add_f32_dpp v14, v14, v14 row_half_mirror row_mask:0xf bank_mask:0xf
AW 82,250
M 6,4,4
s_add_u32 s53, s53, s57
LC_ 113
v_cvt_f64_f32 v[14:15], v14
AW 83,251
M 6,5,5
s_add_u32 s46, %[xlo], s53
s_mov_b64 exec, s[54:55]
SP
s_mov_b64 exec, -1
AR 52,84
M 7,16,4
WL 3
LO 112,18,112
LU 18,112
AR 53,85
M 7,17,5
s_addc_u32 s47, %[xhi], 0
RA 224,1,21504
HZ 29,18
AR 54,86
M 8,28,4
s_bitcmp1_b64 s[92:93], 48
LL 17,68
LL 116,69
AR 55,87
M 8,29,5
s_cselect_b32 s58, 16, 0
LA 17
XS 52
M 9,40,4
s_lshr_b32 s58, 0xda43cb52, s58
LC_ 116
v_lshrrev_b32 v3, 6, v0
v_and_b32 v3, 12, v3
v_bfe_u32 v3, s58, v3, 4
v_mad_u32_u24 v3, v3, v32, v21
XS 53
)"
R"(M 9,41,5
DW 1024
XS 54
M 10,64,4
WL 3
LO 113,16,113
LU 16,113
XS 55
AW 84,52
M 10,65,5
RA 228,1,22528
HS 29,16
AW 85,53
M 11,76,4
LL 18,70
LL 117,71
AW 86,54
M 11,77,5
LA 18
XL 60,0
AW 87,55
M 11,78,0
LC_ 117
DW 2048
AR 248,88
M 11,79,1
XL 61,64
AR 249,89
M 10,66,0
WL 3
LO 116,17,116
LU 17,116
AR 250,90
M 10,67,1
s_mul_i32 s53, s51, 24
RA 232,1,23552
HS 29,17
AR 251,91
M 9,42,0
s_bitcmp1_b32 s51, 0
LL 16,72
LL 120,73
XS 248
M 9,43,1
s_addc_u32 s53, s53, 0
LA 16
s_lshl_b32 s53, s53, 11
s_bitcmp1_b64 s[90:91], s51
s_cselect_b64 s[44:45], s[84:85], s[82:83]
s_add_u32 s44, s44, s53
s_addc_u32 s45, s45, 0
s_bitcmp1_b64 s[92:93], s51
s_cselect_b64 s[94:95], s[88:89], s[86:87]
s_add_u32 s94, s94, s53
s_addc_u32 s95, s95, 0
s_add_u32 s51, s51, %[d]
s_cmp_eq_u32 s51, %[wrap]
s_cselect_b32 s51, %[rst], s51
XS 249
M 8,30,0
s_add_u32 s40, s40, 3072
LC_ 120
s_addc_u32 s41, s41, 0
s_add_u32 m0, s38, 3072
XS 250
XS 251
M 8,31,1
XL 62,128
AW 88,248
M 7,18,0
WL 3
LO 117,18,117
LU 18,117
AW 89,249
M 7,19,1
RA 236,1,24576
HS 29,18
AW 90,250
M 6,6,0
LL 17,74
LL 121,75
AW 91,251
M 6,7,1
LA 17
DW 0
AR 52,92
M 6,8,2
LC_ 121
XL 63,192
AR 53,93
M 6,9,3
DW 1024
v_add_u32 v1, s37, v0
AR 54,94
M 7,20,2
WL 3
LO 120,16,120
LU 16,120
AR 55,95
M 7,21,3
RA 216,1,0
HZ 24,16
XS 52
M 8,32,2
LL 18,76
LL 124,77
XS 53
XS 54
M 8,33,3
LA 18
XL 64,256
XS 55
M 9,44,2
LC_ 124
DW 2048
AW 92,52
M 9,45,3
XL 65,320
v_and_b32 v22, 0xfffffcff, v1
AW 93,53
M 10,68,2
WL 3
LO 121,17,121
LU 17,121
AW 94,54
M 10,69,3
s_lshl_b32 s53, s52, 8
RA 220,1,1024
HS 24,17
AW 95,55
M 11,80,2
LL 16,78
LL 125,79
AR 248,96
M 11,81,3
s_add_u32 s48, %[alo], s53
LA 16
RV 252,22,5120
AR 249,97
M 6,10,4
s_addc_u32 s49, %[ahi], 0
LC_ 125
v_pk_add_f16 v26, v26, v29
AR 250,98
M 6,11,5
s_add_u32 s52, s52, %[d]
HX 26
AR 251,99
M 7,22,4
WL 4
LO 124,18,124
LU 18,124
XS 248
XS 249
M 7,23,5
s_cmp_eq_u32 s52, %[wrap]
RA 224,1,2048
HS 24,18
XS 250
M 8,34,4
s_cselect_b32 s52, %[rst], s52
LL 17,80
LL 106,81
XS 251
M 8,35,5
LA 17
DQ 3072
AW 96,248
M 9,46,4
LC_ 106
XL 66,384
AW 97,249
M 9,47,5
s_add_u32 s50, s50, %[d]
AW 98,250
AW 99,251
M 10,70,4
WL 3
LO 125,16,125
LU 16,125
M 10,71,5
RA 228,1,3072
HS 24,16
AR 52,100
M 11,82,4
LL 18,82
LL 107,83
AR 53,101
M 11,83,5
LA 18
HX 24
s_cmp_eq_u32 s50, %[wrap]
s_cselect_b32 s50, %[rst], s50
XL 67,448
AR 54,102
Z 0,0,0
LC_ 107
XL 68,512
v_add_u32 v19, 1, v19
s_add_u32 s38, s38, 32768
s_cmp_eq_u32 s38, s77
s_cselect_b32 s38, s76, s38
FP
s_sub_u32 s39, 0, s39
AR 55,103
XS 52
M 0,1,1
XL 69,576
XS 53
Z 1,12,0
WL 4
LO 17,17,106
LU 106,17
XS 54
M 1,13,1
RA 232,1,4096
HZ 27,106
XS 55
Z 2,24,0
LL 16,84
LL 110,85
AW 100,52
M 2,25,1
LA 16
PK 106,17,104,56
AW 101,53
Z 3,36,0
WL 5
LC_ 110
XL 70,640
PK 104,17,104,97
AW 102,54
M 3,37,1
RA 236,1,6144
AW 103,55
M 3,38,2
WL 5
LO 18,18,107
LU 107,18
AR 248,104
M 3,39,3
FR
HS 27,107
AR 249,105
M 2,26,2
LL 17,86
LL 111,87
AR 250,106
AR 251,107
M 2,27,3
LA 17
PK 107,18,105,56
XS 248
M 1,14,2
LC_ 111
XL 71,704
PK 105,18,105,97
XS 249
M 1,15,3
s_add_u32 s42, s94, s60
RA 216,1,7168
XS 250
M 0,2,2
WL 4
LO 16,16,110
LU 110,16
XS 251
M 0,3,3
s_addc_u32 s43, s95, 0
RA 220,1,8192
HS 27,110
AW 104,248
M 0,4,4
LL 18,88
LL 114,89
AW 105,249
M 0,5,5
LA 18
PK 110,16,108,56
AW 106,250
M 1,16,4
LC_ 114
PK 108,16,108,97
AW 107,251
M 1,17,5
RA 224,1,9216
PO 110
AR 52,108
M 2,28,4
WL 5
LO 17,17,111
LU 111,17
SU 104
AR 53,109
AR 54,110
M 2,29,5
s_add_u32 s42, s94, s61
HS 27,111
v_cmp_gt_u32 vcc, v19, v15
AR 55,111
M 3,40,4
WL 3
LL 16,90
LL 115,91
XS 52
M 3,41,5
s_addc_u32 s43, s95, 0
LA 16
PK 111,17,109,56
XS 53
M 3,42,0
s_cbranch_vccnz .Lk1s18
.Lk1r18:
LC_ 115
PK 109,17,109,97
XS 54
M 3,43,1
WV 17
RA 228,1,10240
PO 111
XS 55
BW 60
M 2,30,0
WL 5
LO 18,18,114
LU 114,18
AW 108,52
M 2,31,1
RA 232,1,11264
RV 40,8,6144
HZ 29,114
AW 109,53
M 1,18,0
RV 44,8,6208
LL 17,92
LL 118,93
AW 110,54
M 1,19,1
LA 17
RV 48,8,6272
PK 114,18,112,56
AW 111,55
M 0,6,0
WL 6
LC_ 118
BR 60
PK 112,18,112,97
AR 248,112
AR 249,113
M 0,7,1
WV 15
RV 252,22,5392
ds_read_b64 v[14:15], v5 offset:0
W1 24,0
W1 26,2048
BW 61
AR 250,114
M 0,8,2
BR 61
LO 16,16,115
LU 115,16
AR 251,115
M 0,9,3
WL 6
RA 236,1,12288
HS 29,115
XS 248
LZ 40,60
WV 14
BW 62
M 1,20,2
LL 18,94
LL 119,95
XS 249
M 1,21,3
LA 18
RV 40,8,6336
PK 115,16,113,56
XS 250
M 2,32,2
LC_ 119
BR 62
PK 113,16,113,97
XS 251
M 2,33,3
WL 6
RA 216,1,13312
AW 112,248
LS 44,61
WV 12
BW 63
M 3,44,2
WL 7
LO 17,17,118
LU 118,17
SU 108
AW 113,249
M 3,45,3
s_add_u32 s42, s94, s62
RA 220,1,14336
)"
R"(HS 29,118
s_addc_u32 s43, s95, 0
RV 44,8,6400
AW 114,250
M 0,10,4
BR 63
LL 16,96
LL 122,97
AW 115,251
AR 52,116
M 0,11,5
WL 5
LA 16
PK 118,17,116,56
AR 53,117
LS 48,62
WV 11
BW 64
M 1,22,4
LC_ 122
PK 116,17,116,97
AR 54,118
M 1,23,5
RA 224,1,15360
RV 48,8,6464
PO 118
AR 55,119
M 2,34,4
LO 18,18,119
LU 119,18
SU 112
XS 52
M 2,35,5
s_add_u32 s42, s94, s63
BR 64
HS 29,119
XS 53
M 3,46,4
WL 6
LL 17,98
LL 123,99
XS 54
M 3,47,5
s_addc_u32 s43, s95, 0
LA 17
PK 119,18,117,56
XS 55
LS 40,63
WV 10
BW 65
Z 6,0,0
LC_ 123
PK 117,18,117,97
v_add_f32 v14, v14, v15
s_nop 1
v_add_f32_dpp v14, v14, v14 quad_perm:[1,0,3,2] row_mask:0xf bank_mask:0xf
s_nop 1
v_add_f32_dpp v14, v14, v14 quad_perm:[2,3,0,1] row_mask:0xf bank_mask:0xf
s_nop 1
v_add_f32_dpp v14, v14, v14 row_half_mirror row_mask:0xf bank_mask:0xf
v_cvt_f64_f32 v[14:15], v14
s_mov_b64 exec, s[54:55]
SP
s_mov_b64 exec, -1
s_nop 0
ds_read_b64 v[14:15], v5 offset:2048
RV 40,8,6528
AW 116,52
M 6,1,1
WL 5
RA 228,1,16384
BR 65
PO 119
AW 117,53
LS 44,64
WV 9
BW 66
Z 7,12,0
LO 16,16,122
LU 122,16
AW 118,54
AW 119,55
M 7,13,1
s_mul_i32 s53, s50, 25600
RA 232,1,17408
HZ 25,122
Z 8,24,0
s_add_u32 s40, %[wlo], s53
RV 44,8,6592
LL 18,100
LL 126,101
M 8,25,1
s_addc_u32 s41, %[whi], 0
LA 18
BR 66
PK 122,16,120,56
v_pk_add_f16 v27, v27, v29
Z 9,36,0
WL 5
LC_ 126
PK 120,16,120,97
HX 27
M 9,37,1
s_mov_b32 m0, s38
RA 236,1,18432
v_subrev_u32 v5, s39, v5
LS 48,65
WV 8
BW 67
M 9,38,2
RV 48,8,6656
LO 17,17,123
LU 123,17
M 9,39,3
DW 0
BR 67
HS 25,123
M 8,26,2
LL 16,102
LL 127,103
M 8,27,3
WL 5
LA 16
PK 123,17,121,56
LS 40,66
WV 8
BW 68
M 7,14,2
LC_ 127
RV 40,8,6720
PK 121,17,121,97
M 7,15,3
WV 7
RA 216,1,19456
BR 68
M 6,2,2
WL 6
LO 18,18,126
LU 126,18
SU 116
M 6,3,3
s_add_u32 s42, s44, s64
RA 220,1,20480
HS 25,126
s_addc_u32 s43, s45, 0
LS 44,67
s_add_u32 s37, s37, 32768
BW 69
M 6,4,4
s_cmp_eq_u32 s37, s75
LL 17,56
LL 104,57
s_cselect_b32 s37, s74, s37
M 6,5,5
LA 17
PK 126,18,124,56
v_add_f32 v14, v14, v15
s_nop 1
v_add_f32_dpp v14, v14, v14 quad_perm:[1,0,3,2] row_mask:0xf bank_mask:0xf
s_nop 1
v_add_f32_dpp v14, v14, v14 quad_perm:[2,3,0,1] row_mask:0xf bank_mask:0xf
s_nop 1
v_add_f32_dpp v14, v14, v14 row_half_mirror row_mask:0xf bank_mask:0xf
v_cvt_f64_f32 v[14:15], v14
s_mov_b64 exec, s[54:55]
SP
s_mov_b64 exec, -1
DW 1024
RV 44,8,6784
M 7,16,4
LC_ 104
BR 69
PK 124,18,124,97
M 7,17,5
WL 6
RA 224,1,21504
PO 126
LS 48,68
WV 9
BW 70
M 8,28,4
LO 16,16,127
LU 127,16
ST 120
M 8,29,5
WV 9
DW 2048
RV 48,8,6848
HS 25,127
M 9,40,4
WL 3
RA 240,7,0
LL 18,58
LL 105,59
M 9,41,5
s_add_u32 s40, s40, 3072
LA 18
BR 70
PK 127,16,125,56
M 9,42,0
s_addc_u32 s41, s41, 0
LC_ 105
PK 125,16,125,97
s_add_u32 m0, s38, 3072
M 9,43,1
s_add_u32 s42, s44, s65
RA 228,1,22528
PO 127
LS 40,69
s_addc_u32 s43, s45, 0
BW 71
M 8,30,0
RA 244,7,256
LO 104,17,104
LU 17,104
M 8,31,1
RA 232,1,23552
BR 71
HZ 26,17
M 7,18,0
WL 6
DW 0
LL 16,60
LL 108,61
M 7,19,1
s_lshl_b32 s53, s52, 8
LA 16
HX 25
LS 44,70
s_add_u32 s48, %[alo], s53
RA 248,7,512
M 6,6,0
WL 2
LC_ 108
W1 25,1024
W1 27,3072
M 6,7,1
s_addc_u32 s49, %[ahi], 0
RA 236,1,24576
v_add_u32 v4, s39, v4
LS 48,71
s_add_u32 s52, s52, %[d]
RA 252,7,768
M 6,8,2
LO 105,18,105
LU 18,105
s_cmp_eq_u32 s52, %[wrap]
s_cselect_b32 s52, %[rst], s52
DW 1024
v_mul_f32 v36, 0xbb2aaaab, v36
M 6,9,3
DW 2048
HS 26,18
v_mul_f32 v37, 0xbb2aaaab, v37
v_mul_f32 v38, 0xbb2aaaab, v38
M 7,20,2
LL 17,62
LL 109,63
v_mul_f32 v39, 0xbb2aaaab, v39
M 7,21,3
LA 17
ST 124
LK 40,240,60,0
LK 44,242,60,2
s_mul_i32 s53, s51, 24
RA 240,7,1024
v_add_u32 v1, s37, v0
LK 48,244,61,0
CF 40,40,41
LK 248,246,61,2
s_bitcmp1_b32 s51, 0
RA 244,7,1280
CF 42,42,43
M 8,32,2
s_addc_u32 s53, s53, 0
LC_ 109
RA 216,1,0
M 8,33,3
s_lshl_b32 s53, s53, 11
v_and_b32 v22, 0xfffffcff, v1
CF 44,44,45
CF 46,46,47
M 9,44,2
WL 6
LO 108,16,108
LU 16,108
AW 120,40
AW 121,42
M 9,45,3
s_bitcmp1_b64 s[92:93], s51
RA 220,1,1024
HS 26,16
s_cselect_b64 s[94:95], s[88:89], s[86:87]
s_add_u32 s94, s94, s53
s_addc_u32 s95, s95, 0
s_add_u32 s51, s51, %[d]
s_cmp_eq_u32 s51, %[wrap]
s_cselect_b32 s51, %[rst], s51
DQ 3072
AW 122,44
AW 123,46
LK 40,248,62,0
CF 48,48,49
LK 44,250,62,2
s_add_u32 s50, s50, %[d]
RA 248,7,1536
CF 50,50,51
GZ 60
s_cmp_eq_u32 s50, %[wrap]
CF 248,248,249
CF 250,250,251
M 6,10,4
s_cselect_b32 s50, %[rst], s50
LL 18,64
LL 112,65
M 6,11,5
LA 18
RV 252,22,5120
M 7,22,4
WL 7
LC_ 112
v_add_u32 v19, 1, v19
AW 124,48
M 7,23,5
s_add_u32 s38, s38, 32768
RA 224,1,2048
AW 125,50
AW 126,248
AW 127,250
LK 48,252,63,0
CF 40,40,41
LK 248,254,63,2
s_cmp_eq_u32 s38, s77
RA 252,7,1792
CF 42,42,43
GS 61
s_cselect_b32 s38, s76, s38
CF 44,44,45
CF 46,46,47
M 8,34,4
WL 7
LO 109,17,109
LU 17,109
M 8,35,5
FP
HS 26,17
M 9,46,4
WL 4
LL 16,66
LL 113,67
M 9,47,5
s_sub_u32 s39, 0, s39
LA 16
RA 228,1,3072
AW 128,40
AW 129,42
Z 0,0,0
LC_ 113
AW 130,44
AW 131,46
M 0,1,1
LK 40,240,64,0
CF 48,48,49
LK 44,242,64,2
RA 240,7,2048
CF 50,50,51
GS 62
CF 248,248,249
CF 250,250,251
Z 1,12,0
WL 7
LO 112,18,112
LU 18,112
M 1,13,1
RA 232,1,4096
HZ 29,18
AW 132,48
Z 2,24,0
LL 17,68
LL 116,69
AW 133,50
AW 134,248
M 2,25,1
LA 17
FR
AW 135,250
LK 48,244,65,0
CF 40,40,41
LK 248,246,65,2
RA 244,7,2304
CF 42,42,43
GS 63
CF 44,44,45
CF 46,46,47
Z 10,36,0
WL 6
LC_ 116
AW 136,40
M 10,37,1
RA 236,1,6144
)"
R"(AW 137,42
M 10,38,2
WL 7
LO 113,16,113
LU 16,113
M 10,39,3
HS 29,16
AW 138,44
AW 139,46
LK 40,248,66,0
CF 48,48,49
LK 44,250,66,2
RA 248,7,2560
CF 50,50,51
GS 64
CF 248,248,249
CF 250,250,251
M 2,26,2
LL 18,70
LL 117,71
M 2,27,3
LA 18
RA 216,1,7168
M 1,14,2
WL 6
LC_ 117
v_cmp_gt_u32 vcc, v19, v15
M 1,15,3
AW 140,48
AW 141,50
AW 142,248
M 0,2,2
WL 4
LO 116,17,116
LU 17,116
AW 143,250
M 0,3,3
RA 220,1,8192
HS 29,17
LK 48,252,67,0
CF 40,40,41
LK 248,254,67,2
RA 252,7,2816
CF 42,42,43
GS 65
CF 44,44,45
CF 46,46,47
M 0,4,4
s_cbranch_vccnz .Lk1s19
.Lk1r19:
LL 16,80
LL 106,81
M 0,5,5
LA 16
ds_read_b64 v[14:15], v5 offset:0
AW 144,40
AW 145,42
M 1,16,4
LC_ 106
AW 146,44
AW 147,46
M 1,17,5
RA 224,1,9216
LK 40,240,68,0
CF 48,48,49
LK 44,242,68,2
CF 50,50,51
GS 66
CF 248,248,249
CF 250,250,251
M 2,28,4
WL 6
LO 117,18,117
LU 18,117
M 2,29,5
HS 29,18
AW 148,48
M 10,40,4
WL 5
LL 17,82
LL 107,83
AW 149,50
AW 150,248
AW 151,250
M 10,41,5
LA 17
RA 228,1,10240
LK 48,244,69,0
CF 40,40,41
LK 248,246,69,2
CF 42,42,43
v_pk_add_f16 v26, v26, v29
GS 67
CF 44,44,45
CF 46,46,47
M 10,42,0
LC_ 107
HX 26
M 10,43,1
W1 26,2048
AW 152,40
M 2,30,0
WL 5
LO 16,16,106
LU 106,16
AW 153,42
AW 154,44
M 2,31,1
RA 232,1,11264
HZ 27,106
AW 155,46
LK 40,248,70,0
CF 48,48,49
LK 44,250,70,2
CF 50,50,51
v_add_f32 v14, v14, v15
GS 68
CF 248,248,249
CF 250,250,251
M 1,18,0
LL 18,84
LL 110,85
M 1,19,1
LA 18
PK 106,16,104,56
M 0,6,0
WL 4
LC_ 110
PK 104,16,104,97
AW 156,48
AW 157,50
AW 158,248
M 0,7,1
RV 252,22,5392
v_add_f32_dpp v14, v14, v14 quad_perm:[1,0,3,2] row_mask:0xf bank_mask:0xf
AW 159,250
LK 48,252,71,0
CF 40,40,41
LK 248,254,71,2
CF 42,42,43
v_add_f32_dpp v14, v14, v14 quad_perm:[2,3,0,1] row_mask:0xf bank_mask:0xf
GS 69
CF 44,44,45
CF 46,46,47
M 0,8,2
WL 5
LO 17,17,107
LU 107,17
M 0,9,3
s_add_u32 s42, s94, s60
RA 236,1,12288
HS 27,107
M 1,20,2
s_addc_u32 s43, s95, 0
LL 16,86
LL 111,87
AW 160,40
AW 161,42
M 1,21,3
LA 16
RV 36,8,6912
PK 107,17,105,56
AW 162,44
AW 163,46
M 2,32,2
LC_ 111
PK 105,17,105,97
M 2,33,3
RA 216,1,13312
v_add_f32_dpp v14, v14, v14 row_half_mirror row_mask:0xf bank_mask:0xf
GS 70
CF 48,48,49
CF 50,50,51
CF 248,248,249
CF 250,250,251
M 10,44,2
WL 4
LO 18,18,110
LU 110,18
AW 164,48
AW 165,50
AW 166,248
M 10,45,3
RA 220,1,14336
HS 27,110
AW 167,250
s_nop 1
GS 71
SU 104
v_cvt_f64_f32 v[14:15], v14
s_nop 5
v_cndmask_b32 v52, v52, v53, s[78:79]
M 0,10,4
LL 17,88
LL 114,89
v_cndmask_b32 v54, v54, v55, s[78:79]
M 0,11,5
LA 17
PK 110,18,108,56
v_cndmask_b32 v52, v52, v54, s[80:81]
M 1,22,4
LC_ 114
PK 108,18,108,97
v_mul_f32 v52, 0x3b2aaaab, v52
M 1,23,5
WL 5
RA 224,1,15360
PO 110
v_med3_f32 v53, v52, v39, v37
M 2,34,4
WL 5
LO 16,16,111
LU 111,16
s_mov_b64 exec, s[54:55]
SP
s_mov_b64 exec, -1
LG 52,53
s_nop 0
C 52,52,52
M 2,35,5
s_add_u32 s42, s94, s61
ds_read_b64 v[14:15], v5 offset:2048
HS 27,111
v_lshlrev_b32 v52, 16, v52
M 10,46,4
WL 4
LL 18,90
LL 115,91
v_add_f32 v52, v36, v52
M 10,47,5
s_addc_u32 s43, s95, 0
LA 18
PK 111,16,109,56
C 52,52,52
Z 6,0,0
LC_ 115
PK 109,16,109,97
v_lshlrev_b32 v52, 16, v52
M 6,1,1
RA 228,1,16384
PO 111
v_sqrt_f32 v52, v52
s_nop 0
C 52,52,52
Z 7,12,0
WL 5
LO 17,17,114
LU 114,17
v_lshlrev_b32 v52, 16, v52
M 7,13,1
RA 232,1,17408
HZ 29,114
v_rcp_f32 v52, v52
Z 8,24,0
LL 16,92
LL 118,93
v_mul_f32 v52, v38, v52
M 8,25,1
LA 16
PK 114,17,112,56
v_subrev_u32 v5, s39, v5
CF 52,52,52
Z 11,36,0
WL 2
LC_ 118
PK 112,17,112,97
ds_bpermute_b32 v13, v28, v52
M 11,37,1
RA 236,1,18432
v_add_f32 v14, v14, v15
M 11,38,2
LO 18,18,115
LU 115,18
M 11,39,3
SU 108
HS 29,115
v_add_f32_dpp v14, v14, v14 quad_perm:[1,0,3,2] row_mask:0xf bank_mask:0xf
M 8,26,2
s_mul_i32 s53, s50, 25600
LL 17,94
LL 119,95
s_add_u32 s40, %[wlo], s53
s_addc_u32 s41, %[whi], 0
s_mov_b32 m0, s38
M 8,27,3
LA 17
PK 115,18,113,56
v_add_f32_dpp v14, v14, v14 quad_perm:[2,3,0,1] row_mask:0xf bank_mask:0xf
M 7,14,2
LC_ 119
PK 113,18,113,97
M 7,15,3
RA 216,1,19456
DW 0
v_add_f32_dpp v14, v14, v14 row_half_mirror row_mask:0xf bank_mask:0xf
M 6,2,2
WL 3
LO 16,16,118
LU 118,16
M 6,3,3
s_add_u32 s42, s94, s62
RA 220,1,20480
HS 29,118
v_cvt_f64_f32 v[14:15], v14
s_addc_u32 s43, s95, 0
AR 248,120
M 6,4,4
s_add_u32 s37, s37, 32768
LL 18,56
LL 104,57
s_cmp_eq_u32 s37, s75
s_cselect_b32 s37, s74, s37
AR 249,121
M 6,5,5
LA 18
PK 118,16,116,56
s_mov_b64 exec, s[54:55]
SP
s_mov_b64 exec, -1
DW 1024
AR 250,122
M 7,16,4
LC_ 104
PK 116,16,116,97
AR 251,123
AR 52,124
M 7,17,5
RA 224,1,21504
PO 118
AR 53,125
M 8,28,4
WL 5
LO 17,17,119
LU 119,17
SU 112
AR 54,126
AR 55,127
M 8,29,5
DW 2048
HS 29,119
XS 248
M 11,40,4
WL 3
LL 16,58
LL 105,59
XS 249
M 11,41,5
s_add_u32 s40, s40, 3072
LA 16
PK 119,17,117,56
XS 250
XS 251
M 11,42,0
s_addc_u32 s41, s41, 0
LC_ 105
PK 117,17,117,97
s_add_u32 m0, s38, 3072
AW 120,248
M 11,43,1
s_add_u32 s42, s94, s63
RA 228,1,22528
PO 119
AW 121,249
AW 122,250
M 8,30,0
WL 4
LO 104,18,104
LU 18,104
AW 123,251
M 8,31,1
s_addc_u32 s43, s95, 0
RA 232,1,23552
HZ 26,18
XS 52
XS 53
M 7,18,0
DW 0
LL 17,60
LL 108,61
XS 54
M 7,19,1
s_lshl_b32 s53, s52, 8
LA 17
v_pk_add_f16 v27, v27, v29
XS 55
M 6,6,0
WL 2
LC_ 108
HX 27
AW 124,52
AW 125,53
M 6,7,1
s_add_u32 s48, %[alo], s53
RA 236,1,24576
W1 27,3072
AW 126,54
M 6,8,2
LO 105,16,105
LU 16,105
s_addc_u32 s49, %[ahi], 0
)"
R"(s_add_u32 s52, s52, %[d]
s_cmp_eq_u32 s52, %[wrap]
s_cselect_b32 s52, %[rst], s52
DW 1024
AW 127,55
v_min_u32 v35, v35, v13
M 6,9,3
SU 116
HS 26,16
AR 248,128
M 7,20,2
s_mul_i32 s53, s51, 24
LL 18,62
LL 109,63
s_bitcmp1_b32 s51, 0
s_addc_u32 s53, s53, 0
s_lshl_b32 s53, s53, 11
s_bitcmp1_b64 s[92:93], s51
s_cselect_b64 s[94:95], s[88:89], s[86:87]
s_add_u32 s94, s94, s53
s_addc_u32 s95, s95, 0
s_add_u32 s51, s51, %[d]
s_cmp_eq_u32 s51, %[wrap]
s_cselect_b32 s51, %[rst], s51
AR 249,129
AR 250,130
M 7,21,3
LA 18
DW 2048
AR 251,131
M 8,32,2
LC_ 109
DQ 3072
XS 248
M 8,33,3
s_add_u32 s50, s50, %[d]
v_add_u32 v1, s37, v0
v_add_u32 v4, s39, v4
XS 249
XS 250
M 11,44,2
WL 3
LO 108,17,108
LU 17,108
XS 251
M 11,45,3
s_cmp_eq_u32 s50, %[wrap]
RA 216,1,0
HS 26,17
AW 128,248
AW 129,249
M 6,10,4
s_cselect_b32 s50, %[rst], s50
LL 16,64
LL 112,65
AW 130,250
M 6,11,5
LA 16
v_and_b32 v22, 0xfffffcff, v1
AW 131,251
AR 52,132
M 7,22,4
LC_ 112
RA 220,1,1024
AR 53,133
M 7,23,5
RV 252,22,5120
v_add_u32 v19, 1, v19
AR 54,134
M 8,34,4
WL 5
LO 109,18,109
LU 18,109
AR 55,135
XS 52
M 8,35,5
s_add_u32 s38, s38, 32768
RA 224,1,2048
HS 26,18
XS 53
M 11,46,4
WL 1
LL 17,66
LL 113,67
XS 54
XS 55
M 11,47,5
s_cmp_eq_u32 s38, s77
LA 17
RA 228,1,3072
s_cselect_b32 s38, s76, s38
FP
s_sub_u32 s39, 0, s39
AW 132,52
Z 0,0,0
LC_ 113
AW 133,53
AW 134,54
M 0,1,1
AW 135,55
AR 248,136
Z 1,12,0
LO 112,16,112
LU 16,112
M 1,13,1
RA 232,1,4096
HZ 29,16
AR 249,137
AR 250,138
Z 2,24,0
LL 18,96
LL 116,97
AR 251,139
M 2,25,1
LA 18
XS 248
XS 249
Z 3,36,0
LC_ 116
XS 250
M 3,37,1
XS 251
AW 136,248
Z 4,48,0
WL 3
LO 113,17,113
LU 17,113
AW 137,249
M 4,49,1
RA 236,1,6144
HS 29,17
AW 138,250
Z 5,60,0
LL 16,98
LL 117,99
AW 139,251
AR 52,140
M 5,61,1
LA 16
AR 53,141
M 5,62,2
LC_ 117
AR 54,142
AR 55,143
M 5,63,3
XS 52
XS 53
M 4,50,2
WL 3
LO 116,18,116
LU 18,116
XS 54
M 4,51,3
RA 216,1,7168
HS 29,18
XS 55
M 3,38,2
LL 17,80
LL 106,81
AW 140,52
M 3,39,3
LA 17
FR
AW 141,53
AW 142,54
M 2,26,2
LC_ 106
AW 143,55
M 2,27,3
AR 248,144
AR 249,145
M 1,14,2
WL 4
LO 117,16,117
LU 16,117
AR 250,146
M 1,15,3
RA 220,1,8192
HS 29,16
AR 251,147
M 0,2,2
LL 18,82
LL 107,83
XS 248
XS 249
M 0,3,3
LA 18
v_pk_add_f16 v26, v26, v29
XS 250
M 0,4,4
WL 3
LC_ 107
v_cmp_gt_u32 vcc, v19, v15
XS 251
AW 144,248
M 0,5,5
HX 26
AW 145,249
M 1,16,4
WL 3
LO 17,17,106
LU 106,17
AW 146,250
AW 147,251
M 1,17,5
RA 224,1,9216
HZ 27,106
AR 52,148
M 2,28,4
s_cbranch_vccnz .Lk1s20
.Lk1r20:
LL 16,84
LL 110,85
AR 53,149
M 2,29,5
LA 16
PK 106,17,104,56
AR 54,150
AR 55,151
M 3,40,4
LC_ 110
PK 104,17,104,97
XS 52
M 3,41,5
W1 26,2048
XS 53
XS 54
M 4,52,4
WL 4
LO 18,18,107
LU 107,18
XS 55
M 4,53,5
s_add_u32 s42, s94, s60
RA 228,1,10240
HS 27,107
AW 148,52
AW 149,53
M 5,64,4
LL 17,86
LL 111,87
AW 150,54
AW 151,55
M 5,65,5
s_addc_u32 s43, s95, 0
LA 17
PK 107,18,105,56
M 5,66,0
LC_ 111
PK 105,18,105,97
AR 248,152
AR 249,153
M 5,67,1
SU 104
AR 250,154
M 4,54,0
WL 4
LO 16,16,110
LU 110,16
AR 251,155
XS 248
M 4,55,1
RA 232,1,11264
HS 27,110
XS 249
M 3,42,0
LL 18,88
LL 114,89
XS 250
XS 251
M 3,43,1
LA 18
PK 110,16,108,56
AW 152,248
M 2,30,0
LC_ 114
PK 108,16,108,97
AW 153,249
M 2,31,1
RV 252,22,5392
PO 110
AW 154,250
AW 155,251
M 1,18,0
WL 4
LO 17,17,111
LU 111,17
AR 52,156
M 1,19,1
s_add_u32 s42, s94, s61
RA 236,1,12288
HS 27,111
AR 53,157
AR 54,158
M 0,6,0
LL 16,90
LL 115,91
AR 55,159
M 0,7,1
s_addc_u32 s43, s95, 0
LA 16
PK 111,17,109,56
XS 52
XS 53
M 0,8,2
LC_ 115
PK 109,17,109,97
XS 54
M 0,9,3
PO 111
XS 55
M 1,20,2
WL 4
LO 18,18,114
LU 114,18
AW 156,52
AW 157,53
M 1,21,3
RA 216,1,13312
HZ 29,114
AW 158,54
M 2,32,2
LL 17,100
LL 118,101
AW 159,55
AR 248,160
M 2,33,3
LA 17
PK 114,18,112,56
AR 249,161
M 3,44,2
LC_ 118
PK 112,18,112,97
AR 250,162
AR 251,163
M 3,45,3
SU 108
XS 248
M 4,56,2
WL 3
LO 16,16,115
LU 115,16
XS 249
M 4,57,3
s_add_u32 s42, s94, s62
RA 220,1,14336
HS 29,115
XS 250
XS 251
M 5,68,2
LL 18,102
LL 119,103
AW 160,248
M 5,69,3
s_addc_u32 s43, s95, 0
LA 18
PK 115,16,113,56
AW 161,249
AW 162,250
M 0,10,4
LC_ 119
PK 113,16,113,97
AW 163,251
M 0,11,5
SU 112
AR 52,164
AR 53,165
M 1,22,4
WL 3
LO 17,17,118
LU 118,17
AR 54,166
M 1,23,5
RA 224,1,15360
HS 29,118
AR 55,167
M 2,34,4
LL 16,56
LL 104,57
XS 52
XS 53
M 2,35,5
LA 16
PK 118,17,116,56
XS 54
M 3,46,4
LC_ 104
PK 116,17,116,97
XS 55
AW 164,52
M 3,47,5
ds_read_b64 v[14:15], v5 offset:2048
PO 118
AW 165,53
M 4,58,4
WL 4
LO 18,18,119
LU 119,18
AW 166,54
AW 167,55
)"
R"(M 4,59,5
s_add_u32 s42, s94, s63
RA 228,1,16384
HS 29,119
M 5,70,4
LL 17,58
LL 105,59
M 5,71,5
s_addc_u32 s43, s95, 0
LA 17
PK 119,18,117,56
Z 6,0,0
LC_ 105
PK 117,18,117,97
M 6,1,1
PO 119
v_subrev_u32 v5, s39, v5
Z 7,12,0
WL 4
LO 104,16,104
LU 16,104
M 7,13,1
RA 232,1,17408
HZ 26,16
Z 8,24,0
LL 18,60
LL 108,61
M 8,25,1
WL 4
LA 18
SU 116
Z 9,36,0
s_mul_i32 s53, s50, 25600
LC_ 108
v_add_f32 v14, v14, v15
M 9,37,1
s_add_u32 s40, %[wlo], s53
v_add_f32_dpp v14, v14, v14 quad_perm:[1,0,3,2] row_mask:0xf bank_mask:0xf
v_pk_add_f16 v27, v27, v29
Z 10,48,0
WL 3
LO 105,17,105
LU 17,105
M 10,49,1
s_addc_u32 s41, %[whi], 0
RA 236,1,18432
HS 26,17
Z 11,60,0
LL 16,62
LL 109,63
M 11,61,1
s_mov_b32 m0, s38
LA 16
DW 0
M 11,62,2
LC_ 109
v_add_f32_dpp v14, v14, v14 quad_perm:[2,3,0,1] row_mask:0xf bank_mask:0xf
M 11,63,3
s_nop 0
v_add_f32_dpp v14, v14, v14 row_half_mirror row_mask:0xf bank_mask:0xf
HX 27
M 10,50,2
WL 3
LO 108,18,108
LU 18,108
M 10,51,3
s_add_u32 s37, s37, 32768
RA 216,1,19456
HS 26,18
W1 27,3072
M 9,38,2
s_cmp_eq_u32 s37, s75
LL 17,64
LL 112,65
M 9,39,3
s_cselect_b32 s37, s74, s37
LA 17
DW 1024
M 8,26,2
LC_ 112
v_cvt_f64_f32 v[14:15], v14
M 8,27,3
s_mov_b64 exec, s[54:55]
SP
s_mov_b64 exec, -1
v_add_u32 v4, s39, v4
M 7,14,2
WL 4
LO 109,16,109
LU 16,109
M 7,15,3
RA 220,1,20480
HS 26,16
M 6,2,2
LL 18,66
LL 113,67
M 6,3,3
LA 18
DW 2048
M 6,4,4
LC_ 113
M 6,5,5
M 7,16,4
WL 3
LO 112,17,112
LU 17,112
M 7,17,5
s_mul_i32 s53, s51, 24
RA 224,1,21504
HZ 29,17
M 8,28,4
s_bitcmp1_b32 s51, 0
LL 16,68
LL 116,69
M 8,29,5
s_addc_u32 s53, s53, 0
LA 16
M 9,40,4
s_lshl_b32 s53, s53, 11
LC_ 116
M 9,41,5
s_bitcmp1_b64 s[92:93], s51
M 10,52,4
WL 3
LO 113,18,113
LU 18,113
M 10,53,5
s_cselect_b64 s[94:95], s[88:89], s[86:87]
RA 228,1,22528
HS 29,18
M 11,64,4
LL 17,70
LL 117,71
M 11,65,5
s_add_u32 s94, s94, s53
LA 17
M 11,66,0
s_addc_u32 s95, s95, 0
LC_ 117
M 11,67,1
s_add_u32 s51, s51, %[d]
M 10,54,0
WL 3
LO 116,16,116
LU 16,116
M 10,55,1
s_cmp_eq_u32 s51, %[wrap]
RA 232,1,23552
HS 29,16
M 9,42,0
s_cselect_b32 s51, %[rst], s51
LL 18,72
LL 120,73
M 9,43,1
s_mul_i32 s53, %[d], 32768
LA 18
s_add_u32 s64, s53, %[sb1]
s_add_u32 s65, s64, 512
M 8,30,0
s_add_u32 s40, s40, 3072
LC_ 120
s_addc_u32 s41, s41, 0
s_add_u32 m0, s38, 3072
M 8,31,1
DW 0
M 7,18,0
WL 3
LO 117,17,117
LU 17,117
M 7,19,1
RA 236,1,24576
HS 29,17
M 6,6,0
LL 16,74
LL 121,75
M 6,7,1
LA 16
DW 1024
M 6,8,2
LC_ 121
DW 2048
M 6,9,3
s_lshl_b32 s53, s52, 8
v_add_u32 v1, s37, v0
M 7,20,2
WL 3
LO 120,18,120
LU 18,120
M 7,21,3
s_add_u32 s48, %[alo], s53
RA 216,1,0
HZ 30,18
M 8,32,2
s_addc_u32 s49, %[ahi], 0
LL 17,76
LL 124,77
M 8,33,3
s_add_u32 s52, s52, %[d]
LA 17
v_and_b32 v22, 0xfffffcff, v1
M 9,44,2
s_cmp_eq_u32 s52, %[wrap]
LC_ 124
M 9,45,3
s_cselect_b32 s52, %[rst], s52
DQ 3072
M 10,56,2
WL 3
LO 121,16,121
LU 16,121
M 10,57,3
s_add_u32 s50, s50, %[d]
RA 220,1,1024
HS 30,16
M 11,68,2
LL 18,78
LL 125,79
M 11,69,3
s_cmp_eq_u32 s50, %[wrap]
LA 18
RV 252,22,5120
M 6,10,4
s_cselect_b32 s50, %[rst], s50
LC_ 125
M 6,11,5
WV 25
v_add_u32 v19, 1, v19
M 7,22,4
WL 4
LO 124,17,124
LU 17,124
M 7,23,5
s_add_u32 s38, s38, 32768
RA 224,1,2048
HS 30,17
M 8,34,4
s_cmp_eq_u32 s38, s77
LL 16,80
LL 106,81
M 8,35,5
s_cselect_b32 s38, s76, s38
LA 16
FP
M 9,46,4
s_sub_u32 s39, 0, s39
LC_ 106
M 9,47,5
M 10,58,4
WL 4
LO 125,18,125
LU 18,125
M 10,59,5
RA 228,1,3072
HS 30,18
M 11,70,4
LL 17,82
LL 107,83
M 11,71,5
LA 17
v_pk_add_f16 v26, v26, v30
Z 0,0,0
LC_ 107
v_pk_add_f16 v26, v26, v29
M 0,1,1
HX 26
Z 1,12,0
WL 3
LO 16,16,106
LU 106,16
M 1,13,1
RA 232,1,4096
HZ 27,106
Z 2,24,0
LL 18,84
LL 110,85
M 2,25,1
LA 18
PK 106,16,104,56
Z 3,36,0
LC_ 110
PK 104,16,104,97
M 3,37,1
Z 4,48,0
WL 3
LO 17,17,107
LU 107,17
M 4,49,1
s_add_u32 s42, s94, s60
RA 236,1,6144
HS 27,107
Z 5,60,0
LL 16,86
LL 111,87
M 5,61,1
s_addc_u32 s43, s95, 0
LA 16
PK 107,17,105,56
M 5,62,2
LC_ 111
PK 105,17,105,97
M 5,63,3
SU 104
M 4,50,2
WL 3
LO 18,18,110
LU 110,18
M 4,51,3
RA 216,1,7168
HS 27,110
M 3,38,2
LL 17,88
LL 114,89
M 3,39,3
LA 17
PK 110,18,108,56
M 2,26,2
LC_ 114
PK 108,18,108,97
M 2,27,3
FR
PO 110
M 1,14,2
WL 4
LO 16,16,111
LU 111,16
M 1,15,3
s_add_u32 s42, s94, s61
RA 220,1,8192
HS 27,111
M 0,2,2
LL 18,90
LL 115,91
M 0,3,3
s_addc_u32 s43, s95, 0
LA 18
PK 111,16,109,56
M 0,4,4
PFN
LC_ 115
PK 109,16,109,97
M 0,5,5
s_mov_b32 m0, %[pm]
s_nop 0
DF
PO 111
M 1,16,4
WL 4
LO 17,17,114
LU 114,17
M 1,17,5
RA 224,1,9216
HZ 29,114
M 2,28,4
LL 16,92
LL 118,93
M 2,29,5
LA 16
PK 114,17,112,56
M 3,40,4
LC_ 118
PK 112,17,112,97
M 3,41,5
WL 6
SU 108
v_cmp_gt_u32 vcc, v19, v15
M 4,52,4
WL 3
LO 18,18,115
LU 115,18
M 4,53,5
s_add_u32 s42, s94, s62
RA 228,1,10240
HS 29,115
M 5,64,4
LL 17,94
LL 119,95
M 5,65,5
s_cbranch_vccnz .Lk1s21
.Lk1r21:
LA 17
PK 115,18,113,56
M 5,66,0
s_addc_u32 s43, s95, 0
LC_ 119
PK 113,18,113,97
M 5,67,1
W1 26,2048
M 4,54,0
WL 4
LO 16,16,118
LU 118,16
M 4,55,1
RA 232,1,11264
HS 29,118
M 3,42,0
LL 18,96
LL 122,97
M 3,43,1
LA 18
PK 118,16,116,56
)"
R"(M 2,30,0
LC_ 122
PK 116,16,116,97
M 2,31,1
RV 252,22,5392
PO 118
M 1,18,0
WL 5
LO 17,17,119
LU 119,17
M 1,19,1
RA 236,1,12288
HS 29,119
M 0,6,0
LL 16,98
LL 123,99
M 0,7,1
LA 16
PK 119,17,117,56
M 0,8,2
LC_ 123
PK 117,17,117,97
M 0,9,3
SU 112
PO 119
M 1,20,2
WL 4
LO 18,18,122
LU 122,18
M 1,21,3
s_add_u32 s42, s94, s63
RA 216,1,13312
HZ 30,122
M 2,32,2
s_addc_u32 s43, s95, 0
LL 17,100
LL 126,101
M 2,33,3
LA 17
PK 122,18,120,56
M 3,44,2
LC_ 126
PK 120,18,120,97
M 3,45,3
SU 116
M 4,56,2
WL 3
LO 16,16,123
LU 123,16
M 4,57,3
s_add_u32 s42, s94, s64
RA 220,1,14336
HS 30,123
M 5,68,2
LL 18,102
LL 127,103
M 5,69,3
s_addc_u32 s43, s95, 0
LA 18
PK 123,16,121,56
M 0,10,4
LC_ 127
PK 121,16,121,97
M 0,11,5
SU 120
M 1,22,4
WL 3
LO 17,17,126
LU 126,17
M 1,23,5
RA 224,1,15360
HS 30,126
M 2,34,4
LL 16,56
LL 104,57
M 2,35,5
LA 16
PK 126,17,124,56
M 3,46,4
LC_ 104
PK 124,17,124,97
M 3,47,5
ds_read_b64 v[14:15], v5 offset:2048
PO 126
M 4,58,4
WL 4
LO 18,18,127
LU 127,18
M 4,59,5
s_add_u32 s42, s94, s65
RA 228,1,16384
HS 30,127
M 5,70,4
LL 17,58
LL 105,59
M 5,71,5
s_addc_u32 s43, s95, 0
LA 17
PK 127,18,125,56
Z 6,0,0
LC_ 105
PK 125,18,125,97
M 6,1,1
PO 127
v_subrev_u32 v5, s39, v5
Z 7,12,0
WL 4
LO 104,16,104
LU 16,104
M 7,13,1
RA 232,1,17408
HZ 26,16
Z 8,24,0
WL 4
LL 18,60
LL 108,61
M 8,25,1
s_mul_i32 s53, s50, 25600
LA 18
v_add_f32 v14, v14, v15
Z 9,36,0
s_add_u32 s40, %[wlo], s53
LC_ 108
v_add_f32_dpp v14, v14, v14 quad_perm:[1,0,3,2] row_mask:0xf bank_mask:0xf
M 9,37,1
s_addc_u32 s41, %[whi], 0
v_add_f32_dpp v14, v14, v14 quad_perm:[2,3,0,1] row_mask:0xf bank_mask:0xf
v_pk_add_f16 v27, v27, v30
Z 10,48,0
WL 3
LO 105,17,105
LU 17,105
M 10,49,1
s_mov_b32 m0, s38
RA 236,1,18432
HS 26,17
Z 11,60,0
LL 16,62
LL 109,63
M 11,61,1
LA 16
SU 124
M 11,62,2
LC_ 109
DW 0
M 11,63,3
v_add_f32_dpp v14, v14, v14 row_half_mirror row_mask:0xf bank_mask:0xf
v_pk_add_f16 v27, v27, v29
M 10,50,2
WL 3
LO 108,18,108
LU 18,108
M 10,51,3
s_add_u32 s37, s37, 32768
RA 216,1,19456
HS 26,18
HX 27
M 9,38,2
s_cmp_eq_u32 s37, s75
LL 17,64
LL 112,65
W1 27,3072
M 9,39,3
s_cselect_b32 s37, s74, s37
LA 17
DW 1024
M 8,26,2
LC_ 112
v_cvt_f64_f32 v[14:15], v14
M 8,27,3
s_mov_b64 exec, s[54:55]
SP
s_mov_b64 exec, -1
v_add_u32 v4, s39, v4
s_mov_b64 exec, 1
AT
s_mov_b64 exec, -1
M 7,14,2
WL 4
LO 109,16,109
LU 16,109
M 7,15,3
RA 220,1,20480
HS 26,16
M 6,2,2
LL 18,66
LL 113,67
M 6,3,3
LA 18
DW 2048
M 6,4,4
LC_ 113
M 6,5,5
M 7,16,4
WL 3
LO 112,17,112
LU 17,112
M 7,17,5
s_mul_i32 s53, s51, 24
RA 224,1,21504
HZ 29,17
M 8,28,4
s_bitcmp1_b32 s51, 0
LL 16,68
LL 116,69
M 8,29,5
s_addc_u32 s53, s53, 0
LA 16
M 9,40,4
s_lshl_b32 s53, s53, 11
LC_ 116
M 9,41,5
s_bitcmp1_b64 s[92:93], s51
M 10,52,4
WL 3
LO 113,18,113
LU 18,113
M 10,53,5
s_cselect_b64 s[94:95], s[88:89], s[86:87]
RA 228,1,22528
HS 29,18
M 11,64,4
LL 17,70
LL 117,71
M 11,65,5
s_add_u32 s94, s94, s53
LA 17
M 11,66,0
s_addc_u32 s95, s95, 0
LC_ 117
M 11,67,1
s_add_u32 s51, s51, %[d]
M 10,54,0
WL 3
LO 116,16,116
LU 16,116
M 10,55,1
s_cmp_eq_u32 s51, %[wrap]
RA 232,1,23552
HS 29,16
M 9,42,0
s_cselect_b32 s51, %[rst], s51
LL 18,72
LL 120,73
M 9,43,1
s_add_u32 s40, s40, 3072
LA 18
M 8,30,0
s_addc_u32 s41, s41, 0
LC_ 120
s_add_u32 m0, s38, 3072
M 8,31,1
DW 0
M 7,18,0
WL 3
LO 117,17,117
LU 17,117
M 7,19,1
RA 236,1,24576
HS 29,17
M 6,6,0
LL 16,74
LL 121,75
M 6,7,1
LA 16
DW 1024
M 6,8,2
LC_ 121
DW 2048
M 6,9,3
s_lshl_b32 s53, s52, 8
v_add_u32 v1, s37, v0
M 7,20,2
WL 3
LO 120,18,120
LU 18,120
M 7,21,3
s_add_u32 s48, %[alo], s53
RA 216,1,0
HZ 30,18
M 8,32,2
s_addc_u32 s49, %[ahi], 0
LL 17,76
LL 124,77
M 8,33,3
s_add_u32 s52, s52, %[d]
LA 17
v_and_b32 v22, 0xfffffcff, v1
M 9,44,2
s_cmp_eq_u32 s52, %[wrap]
LC_ 124
M 9,45,3
s_cselect_b32 s52, %[rst], s52
DQ 3072
M 10,56,2
WL 3
LO 121,16,121
LU 16,121
M 10,57,3
s_add_u32 s50, s50, %[d]
RA 220,1,1024
HS 30,16
M 11,68,2
LL 18,78
LL 125,79
M 11,69,3
s_cmp_eq_u32 s50, %[wrap]
LA 18
RV 252,22,5120
M 6,10,4
s_cselect_b32 s50, %[rst], s50
LC_ 125
M 6,11,5
WV 28
M 7,22,4
WL 4
LO 124,17,124
LU 17,124
M 7,23,5
s_add_u32 s38, s38, 32768
RA 224,1,2048
HS 30,17
M 8,34,4
s_cmp_eq_u32 s38, s77
LL 16,80
LL 106,81
M 8,35,5
s_cselect_b32 s38, s76, s38
LA 16
M 9,46,4
s_barrier
LC_ 106
M 9,47,5
s_sub_u32 s39, 0, s39
M 10,58,4
WL 3
LO 125,18,125
LU 18,125
M 10,59,5
RA 228,1,3072
HS 30,18
M 11,70,4
LL 17,82
LL 107,83
M 11,71,5
LA 17
v_pk_add_f16 v26, v26, v30
Z 0,0,0
LC_ 107
v_pk_add_f16 v26, v26, v29
M 0,1,1
HX 26
Z 1,12,0
WL 3
LO 16,16,106
LU 106,16
M 1,13,1
RA 232,1,4096
HZ 27,106
Z 2,24,0
LL 18,84
LL 110,85
M 2,25,1
LA 18
PK 106,16,104,56
Z 3,36,0
LC_ 110
PK 104,16,104,97
M 3,37,1
W1 26,2048
Z 4,48,0
WL 4
LO 17,17,107
LU 107,17
M 4,49,1
s_add_u32 s42, s94, s60
RA 236,1,6144
HS 27,107
Z 5,60,0
LL 16,86
LL 111,87
M 5,61,1
s_addc_u32 s43, s95, 0
LA 16
PK 107,17,105,56
M 5,62,2
LC_ 111
PK 105,17,105,97
M 5,63,3
SU 104
M 4,50,2
WL 4
LO 18,18,110
LU 110,18
M 4,51,3
RA 216,1,7168
HS 27,110
M 3,38,2
LL 17,88
LL 114,89
)"
R"(M 3,39,3
LA 17
PK 110,18,108,56
M 2,26,2
LC_ 114
PK 108,18,108,97
M 2,27,3
PO 110
M 1,14,2
WL 3
LO 16,16,111
LU 111,16
M 1,15,3
s_add_u32 s42, s94, s61
RA 220,1,8192
HS 27,111
M 0,2,2
LL 18,90
LL 115,91
M 0,3,3
s_addc_u32 s43, s95, 0
LA 18
PK 111,16,109,56
M 0,4,4
PFW
LC_ 115
PK 109,16,109,97
M 0,5,5
s_mov_b32 m0, %[pm]
s_nop 0
DF
PO 111
M 1,16,4
WL 3
LO 17,17,114
LU 114,17
M 1,17,5
RA 224,1,9216
HZ 29,114
M 2,28,4
LL 16,92
LL 118,93
M 2,29,5
LA 16
PK 114,17,112,56
M 3,40,4
LC_ 118
PK 112,17,112,97
M 3,41,5
SU 108
M 4,52,4
WL 3
LO 18,18,115
LU 115,18
M 4,53,5
s_add_u32 s42, s94, s62
RA 228,1,10240
HS 29,115
M 5,64,4
LL 17,94
LL 119,95
M 5,65,5
s_addc_u32 s43, s95, 0
LA 17
PK 115,18,113,56
M 5,66,0
LC_ 119
PK 113,18,113,97
M 5,67,1
SU 112
M 4,54,0
WL 3
LO 16,16,118
LU 118,16
M 4,55,1
RA 232,1,11264
HS 29,118
M 3,42,0
LL 18,96
LL 122,97
M 3,43,1
LA 18
PK 118,16,116,56
M 2,30,0
LC_ 122
PK 116,16,116,97
M 2,31,1
RV 252,22,5392
PO 118
M 1,18,0
WL 4
LO 17,17,119
LU 119,17
M 1,19,1
s_add_u32 s42, s94, s63
RA 236,1,12288
HS 29,119
M 0,6,0
LL 16,98
LL 123,99
M 0,7,1
s_addc_u32 s43, s95, 0
LA 16
PK 119,17,117,56
M 0,8,2
LC_ 123
PK 117,17,117,97
M 0,9,3
PO 119
M 1,20,2
WL 4
LO 18,18,122
LU 122,18
M 1,21,3
RA 216,1,13312
HZ 30,122
M 2,32,2
LL 17,100
LL 126,101
M 2,33,3
LA 17
PK 122,18,120,56
M 3,44,2
LC_ 126
PK 120,18,120,97
M 3,45,3
SU 116
M 4,56,2
WL 3
LO 16,16,123
LU 123,16
M 4,57,3
s_add_u32 s42, s94, s64
RA 220,1,14336
HS 30,123
M 5,68,2
LL 18,102
LL 127,103
M 5,69,3
s_addc_u32 s43, s95, 0
LA 18
PK 123,16,121,56
M 0,10,4
LC_ 127
PK 121,16,121,97
M 0,11,5
SU 120
M 1,22,4
WL 3
LO 17,17,126
LU 126,17
M 1,23,5
RA 224,1,15360
HS 30,126
M 2,34,4
LL 16,56
LL 104,57
M 2,35,5
LA 16
PK 126,17,124,56
M 3,46,4
LC_ 104
PK 124,17,124,97
M 3,47,5
ds_read_b64 v[14:15], v5 offset:2048
PO 126
M 4,58,4
WL 4
LO 18,18,127
LU 127,18
M 4,59,5
s_add_u32 s42, s94, s65
RA 228,1,16384
HS 30,127
M 5,70,4
LL 17,58
LL 105,59
M 5,71,5
s_addc_u32 s43, s95, 0
LA 17
PK 127,18,125,56
Z 6,0,0
LC_ 105
PK 125,18,125,97
M 6,1,1
PO 127
v_subrev_u32 v5, s39, v5
Z 7,12,0
WL 4
LO 104,16,104
LU 16,104
M 7,13,1
RA 232,1,17408
HZ 26,16
Z 8,24,0
WL 4
LL 18,60
LL 108,61
M 8,25,1
s_mul_i32 s53, s50, 25600
LA 18
v_add_f32 v14, v14, v15
Z 9,36,0
s_add_u32 s40, %[wlo], s53
LC_ 108
v_add_f32_dpp v14, v14, v14 quad_perm:[1,0,3,2] row_mask:0xf bank_mask:0xf
M 9,37,1
s_addc_u32 s41, %[whi], 0
Z 10,48,0
WL 3
LO 105,17,105
LU 17,105
M 10,49,1
s_mov_b32 m0, s38
RA 236,1,18432
HS 26,17
Z 11,60,0
LL 16,62
LL 109,63
M 11,61,1
LA 16
SU 124
M 11,62,2
LC_ 109
DW 0
M 11,63,3
WV 13
v_add_f32_dpp v14, v14, v14 quad_perm:[2,3,0,1] row_mask:0xf bank_mask:0xf
v_pk_add_f16 v27, v27, v30
M 10,50,2
WL 3
LO 108,18,108
LU 18,108
v_pk_add_f16 v27, v27, v29
M 10,51,3
s_add_u32 s37, s37, 32768
RA 216,1,19456
HS 26,18
HX 27
M 9,38,2
s_cmp_eq_u32 s37, s75
LL 17,64
LL 112,65
W1 27,3072
M 9,39,3
s_cselect_b32 s37, s74, s37
LA 17
DW 1024
v_readfirstlane_b32 s72, v11
M 8,26,2
LC_ 112
v_add_f32_dpp v14, v14, v14 row_half_mirror row_mask:0xf bank_mask:0xf
v_mov_b32 v12, s72
M 8,27,3
W2 12,12,16
v_cvt_f64_f32 v[14:15], v14
M 7,14,2
WL 5
LO 109,16,109
LU 16,109
M 7,15,3
RA 220,1,20480
HS 26,16
M 6,2,2
LL 18,66
LL 113,67
M 6,3,3
LA 18
s_mov_b64 exec, s[54:55]
SP
s_mov_b64 exec, -1
M 6,4,4
LC_ 113
DW 2048
M 6,5,5
v_add_u32 v4, s39, v4
s_mov_b64 exec, 1
AT
s_mov_b64 exec, -1
M 7,16,4
WL 4
LO 112,17,112
LU 17,112
M 7,17,5
s_mul_i32 s53, s51, 24
RA 224,1,21504
HZ 29,17
M 8,28,4
s_bitcmp1_b32 s51, 0
LL 16,68
LL 116,69
M 8,29,5
s_addc_u32 s53, s53, 0
LA 16
M 9,40,4
s_lshl_b32 s53, s53, 11
LC_ 116
M 9,41,5
s_bitcmp1_b64 s[92:93], s51
M 10,52,4
WL 3
LO 113,18,113
LU 18,113
M 10,53,5
s_cselect_b64 s[94:95], s[88:89], s[86:87]
RA 228,1,22528
HS 29,18
M 11,64,4
LL 17,70
LL 117,71
M 11,65,5
s_add_u32 s94, s94, s53
LA 17
M 11,66,0
s_addc_u32 s95, s95, 0
LC_ 117
M 11,67,1
s_add_u32 s51, s51, %[d]
M 10,54,0
WL 3
LO 116,16,116
LU 16,116
M 10,55,1
s_cmp_eq_u32 s51, %[wrap]
RA 232,1,23552
HS 29,16
M 9,42,0
s_cselect_b32 s51, %[rst], s51
LL 18,72
LL 120,73
M 9,43,1
s_add_u32 s40, s40, 3072
LA 18
M 8,30,0
s_addc_u32 s41, s41, 0
LC_ 120
s_add_u32 m0, s38, 3072
M 8,31,1
DW 0
M 7,18,0
WL 3
LO 117,17,117
LU 17,117
M 7,19,1
RA 236,1,24576
HS 29,17
M 6,6,0
LL 16,74
LL 121,75
M 6,7,1
LA 16
DW 1024
M 6,8,2
LC_ 121
DW 2048
M 6,9,3
s_lshl_b32 s53, s52, 8
v_add_u32 v1, s37, v0
M 7,20,2
WL 3
LO 120,18,120
LU 18,120
M 7,21,3
s_add_u32 s48, %[alo], s53
RA 216,1,0
HZ 30,18
M 8,32,2
s_addc_u32 s49, %[ahi], 0
LL 17,76
LL 124,77
M 8,33,3
s_add_u32 s52, s52, %[d]
LA 17
v_and_b32 v22, 0xfffffcff, v1
M 9,44,2
s_cmp_eq_u32 s52, %[wrap]
LC_ 124
M 9,45,3
s_cselect_b32 s52, %[rst], s52
DQ 3072
M 10,56,2
WL 3
LO 121,16,121
LU 16,121
M 10,57,3
s_add_u32 s50, s50, %[d]
RA 220,1,1024
HS 30,16
M 11,68,2
LL 18,78
LL 125,79
M 11,69,3
s_cmp_eq_u32 s50, %[wrap]
LA 18
RV 252,22,5120
M 6,10,4
s_cselect_b32 s50, %[rst], s50
LC_ 125
M 6,11,5
M 7,22,4
)"
R"(WL 4
LO 124,17,124
LU 17,124
M 7,23,5
s_add_u32 s38, s38, 32768
RA 224,1,2048
HS 30,17
M 8,34,4
s_cmp_eq_u32 s38, s77
LL 16,80
LL 106,81
M 8,35,5
s_cselect_b32 s38, s76, s38
LA 16
M 9,46,4
s_barrier
LC_ 106
M 9,47,5
s_sub_u32 s39, 0, s39
M 10,58,4
WL 3
LO 125,18,125
LU 18,125
M 10,59,5
RA 228,1,3072
HS 30,18
M 11,70,4
LL 17,82
LL 107,83
M 11,71,5
2:
LA 17
v_pk_add_f16 v26, v26, v30
Z 0,0,0
LC_ 107
ds_read_b32 v20, v5 offset:4096
M 0,1,1
v_pk_add_f16 v26, v26, v29
Z 1,12,0
WL 4
LO 16,16,106
LU 106,16
M 1,13,1
RA 232,1,4096
HZ 27,106
Z 2,24,0
LL 18,84
LL 110,85
M 2,25,1
LA 18
PK 106,16,104,56
Z 3,36,0
LC_ 110
PK 104,16,104,97
M 3,37,1
HX 26
Z 4,48,0
WL 4
LO 17,17,107
LU 107,17
M 4,49,1
s_add_u32 s42, s94, s60
RA 236,1,6144
HS 27,107
Z 5,60,0
LL 16,86
LL 111,87
M 5,61,1
s_addc_u32 s43, s95, 0
LA 16
PK 107,17,105,56
M 5,62,2
LC_ 111
PK 105,17,105,97
M 5,63,3
SU 104
M 4,50,2
WL 3
LO 18,18,110
LU 110,18
M 4,51,3
RA 216,1,7168
HS 27,110
M 3,38,2
LL 17,88
LL 114,89
W1 26,2048
M 3,39,3
LA 17
PK 110,18,108,56
M 2,26,2
LC_ 114
PK 108,18,108,97
M 2,27,3
PO 110
v_readfirstlane_b32 s59, v20
M 1,14,2
WL 4
LO 16,16,111
LU 111,16
M 1,15,3
s_add_u32 s42, s94, s61
RA 220,1,8192
HS 27,111
M 0,2,2
LL 18,90
LL 115,91
M 0,3,3
s_addc_u32 s43, s95, 0
LA 18
PK 111,16,109,56
M 0,4,4
LC_ 115
PK 109,16,109,97
M 0,5,5
PO 111
M 1,16,4
WL 3
LO 17,17,114
LU 114,17
M 1,17,5
RA 224,1,9216
HZ 29,114
M 2,28,4
LL 16,92
LL 118,93
M 2,29,5
LA 16
PK 114,17,112,56
M 3,40,4
LC_ 118
PK 112,17,112,97
M 3,41,5
SU 108
M 4,52,4
WL 3
LO 18,18,115
LU 115,18
M 4,53,5
s_add_u32 s42, s94, s62
RA 228,1,10240
HS 29,115
M 5,64,4
LL 17,94
LL 119,95
M 5,65,5
s_addc_u32 s43, s95, 0
LA 17
PK 115,18,113,56
M 5,66,0
LC_ 119
PK 113,18,113,97
M 5,67,1
SU 112
M 4,54,0
WL 3
LO 16,16,118
LU 118,16
M 4,55,1
RA 232,1,11264
HS 29,118
M 3,42,0
LL 18,96
LL 122,97
M 3,43,1
LA 18
PK 118,16,116,56
M 2,30,0
LC_ 122
PK 116,16,116,97
M 2,31,1
RV 252,22,5392
PO 118
M 1,18,0
WL 4
LO 17,17,119
LU 119,17
M 1,19,1
s_add_u32 s42, s94, s63
RA 236,1,12288
HS 29,119
M 0,6,0
LL 16,98
LL 123,99
M 0,7,1
s_addc_u32 s43, s95, 0
LA 16
PK 119,17,117,56
M 0,8,2
LC_ 123
PK 117,17,117,97
M 0,9,3
PO 119
M 1,20,2
WL 4
LO 18,18,122
LU 122,18
M 1,21,3
RA 216,1,13312
HZ 30,122
M 2,32,2
LL 17,100
LL 126,101
M 2,33,3
LA 17
PK 122,18,120,56
M 3,44,2
LC_ 126
PK 120,18,120,97
M 3,45,3
SU 116
M 4,56,2
WL 3
LO 16,16,123
LU 123,16
M 4,57,3
s_add_u32 s42, s94, s64
RA 220,1,14336
HS 30,123
M 5,68,2
LL 18,102
LL 127,103
M 5,69,3
s_addc_u32 s43, s95, 0
LA 18
PK 123,16,121,56
M 0,10,4
LC_ 127
PK 121,16,121,97
M 0,11,5
SU 120
M 1,22,4
WL 3
LO 17,17,126
LU 126,17
M 1,23,5
RA 224,1,15360
HS 30,126
M 2,34,4
LL 16,56
LL 104,57
M 2,35,5
LA 16
PK 126,17,124,56
M 3,46,4
LC_ 104
PK 124,17,124,97
M 3,47,5
ds_read_b64 v[14:15], v5 offset:2048
PO 126
M 4,58,4
WL 4
LO 18,18,127
LU 127,18
M 4,59,5
s_add_u32 s42, s94, s65
RA 228,1,16384
HS 30,127
M 5,70,4
LL 17,58
LL 105,59
M 5,71,5
s_addc_u32 s43, s95, 0
LA 17
PK 127,18,125,56
Z 6,0,0
LC_ 105
PK 125,18,125,97
M 6,1,1
PO 127
v_subrev_u32 v5, s39, v5
Z 7,12,0
WL 4
LO 104,16,104
LU 16,104
M 7,13,1
RA 232,1,17408
HZ 26,16
Z 8,24,0
WL 4
LL 18,60
LL 108,61
M 8,25,1
s_mul_i32 s53, s50, 25600
LA 18
v_add_f32 v14, v14, v15
Z 9,36,0
s_add_u32 s40, %[wlo], s53
LC_ 108
v_add_f32_dpp v14, v14, v14 quad_perm:[1,0,3,2] row_mask:0xf bank_mask:0xf
M 9,37,1
s_addc_u32 s41, %[whi], 0
Z 10,48,0
WL 3
LO 105,17,105
LU 17,105
M 10,49,1
s_mov_b32 m0, s38
RA 236,1,18432
HS 26,17
Z 11,60,0
LL 16,62
LL 109,63
M 11,61,1
LA 16
SU 124
M 11,62,2
LC_ 109
DW 0
M 11,63,3
WV 11
v_add_f32_dpp v14, v14, v14 quad_perm:[2,3,0,1] row_mask:0xf bank_mask:0xf
v_pk_add_f16 v27, v27, v30
M 10,50,2
WL 3
LO 108,18,108
LU 18,108
v_pk_add_f16 v27, v27, v29
M 10,51,3
s_add_u32 s37, s37, 32768
RA 216,1,19456
HS 26,18
HX 27
M 9,38,2
s_cmp_eq_u32 s37, s75
LL 17,64
LL 112,65
W1 27,3072
M 9,39,3
s_cselect_b32 s37, s74, s37
LA 17
DW 1024
v_readfirstlane_b32 s72, v11
M 8,26,2
LC_ 112
v_add_f32_dpp v14, v14, v14 row_half_mirror row_mask:0xf bank_mask:0xf
v_mov_b32 v12, s72
M 8,27,3
s_max_u32 s59, s59, %[nb]
W2 12,12,16
v_cvt_f64_f32 v[14:15], v14
M 7,14,2
WL 5
LO 109,16,109
LU 16,109
M 7,15,3
s_add_u32 %[nb], %[nb], 1
RA 220,1,20480
HS 26,16
M 6,2,2
LL 18,66
LL 113,67
M 6,3,3
LA 18
s_mov_b64 exec, s[54:55]
SP
s_mov_b64 exec, -1
M 6,4,4
LC_ 113
DW 2048
M 6,5,5
v_add_u32 v4, s39, v4
s_mov_b64 exec, 1
AT
s_mov_b64 exec, -1
M 7,16,4
WL 4
LO 112,17,112
LU 17,112
M 7,17,5
s_mul_i32 s53, s51, 24
RA 224,1,21504
HZ 29,17
M 8,28,4
s_bitcmp1_b32 s51, 0
LL 16,68
LL 116,69
M 8,29,5
s_addc_u32 s53, s53, 0
LA 16
M 9,40,4
s_lshl_b32 s53, s53, 11
LC_ 116
M 9,41,5
s_bitcmp1_b64 s[92:93], s51
M 10,52,4
WL 3
LO 113,18,113
LU 18,113
M 10,53,5
s_cselect_b64 s[94:95], s[88:89], s[86:87]
RA 228,1,22528
HS 29,18
M 11,64,4
LL 17,70
LL 117,71
M 11,65,5
s_add_u32 s94, s94, s53
LA 17
M 11,66,0
)"
R"(s_addc_u32 s95, s95, 0
LC_ 117
M 11,67,1
s_add_u32 s51, s51, %[d]
M 10,54,0
WL 3
LO 116,16,116
LU 16,116
M 10,55,1
s_cmp_eq_u32 s51, %[wrap]
RA 232,1,23552
HS 29,16
M 9,42,0
s_cselect_b32 s51, %[rst], s51
LL 18,72
LL 120,73
M 9,43,1
s_add_u32 s40, s40, 3072
LA 18
M 8,30,0
s_addc_u32 s41, s41, 0
LC_ 120
s_add_u32 m0, s38, 3072
M 8,31,1
DW 0
M 7,18,0
WL 3
LO 117,17,117
LU 17,117
M 7,19,1
RA 236,1,24576
HS 29,17
M 6,6,0
LL 16,74
LL 121,75
M 6,7,1
LA 16
DW 1024
M 6,8,2
LC_ 121
DW 2048
M 6,9,3
s_lshl_b32 s53, s52, 8
v_add_u32 v1, s37, v0
M 7,20,2
WL 3
LO 120,18,120
LU 18,120
M 7,21,3
s_add_u32 s48, %[alo], s53
RA 216,1,0
HZ 30,18
M 8,32,2
s_addc_u32 s49, %[ahi], 0
LL 17,76
LL 124,77
M 8,33,3
s_add_u32 s52, s52, %[d]
LA 17
v_and_b32 v22, 0xfffffcff, v1
M 9,44,2
s_cmp_eq_u32 s52, %[wrap]
LC_ 124
M 9,45,3
s_cselect_b32 s52, %[rst], s52
DQ 3072
M 10,56,2
WL 3
LO 121,16,121
LU 16,121
M 10,57,3
s_add_u32 s50, s50, %[d]
RA 220,1,1024
HS 30,16
M 11,68,2
LL 18,78
LL 125,79
M 11,69,3
s_cmp_eq_u32 s50, %[wrap]
LA 18
RV 252,22,5120
M 6,10,4
s_cselect_b32 s50, %[rst], s50
LC_ 125
M 6,11,5
M 7,22,4
WL 4
LO 124,17,124
LU 17,124
M 7,23,5
s_add_u32 s38, s38, 32768
RA 224,1,2048
HS 30,17
M 8,34,4
s_cmp_eq_u32 s38, s77
LL 16,80
LL 106,81
M 8,35,5
s_cselect_b32 s38, s76, s38
LA 16
M 9,46,4
s_barrier
LC_ 106
M 9,47,5
s_sub_u32 s39, 0, s39
M 10,58,4
WL 3
LO 125,18,125
LU 18,125
M 10,59,5
RA 228,1,3072
HS 30,18
M 11,70,4
s_cmp_lt_u32 s59, 32
LL 17,82
LL 107,83
M 11,71,5
s_cbranch_scc1 2b
LA 17
LC_ 107
ds_read_b64 v[14:15], v5 offset:2048
v_pk_add_f16 v26, v26, v30
v_pk_add_f16 v26, v26, v29
HX 26
v_subrev_u32 v5, s39, v5
W1 26,2048
WL 0
LO 16,16,106
LU 106,16
HZ 27,106
LL 18,84
LA 18
LL 110,85
PK 106,16,104,56
v_add_f32 v14, v14, v15
s_nop 1
v_add_f32_dpp v14, v14, v14 quad_perm:[1,0,3,2] row_mask:0xf bank_mask:0xf
s_nop 1
v_add_f32_dpp v14, v14, v14 quad_perm:[2,3,0,1] row_mask:0xf bank_mask:0xf
s_nop 1
v_add_f32_dpp v14, v14, v14 row_half_mirror row_mask:0xf bank_mask:0xf
v_cvt_f64_f32 v[14:15], v14
s_mov_b64 exec, s[54:55]
SP
s_mov_b64 exec, -1
LC_ 110
PK 104,16,104,97
LO 17,17,107
LU 107,17
s_add_u32 s42, s94, s60
s_addc_u32 s43, s95, 0
HS 27,107
LL 16,86
LA 16
LL 111,87
LC_ 111
PK 107,17,105,56
PK 105,17,105,97
SU 104
WL 0
LO 18,18,110
LU 110,18
HS 27,110
LL 17,88
LA 17
LL 114,89
LC_ 114
PK 110,18,108,56
PK 108,18,108,97
PO 110
LO 16,16,111
LU 111,16
HS 27,111
s_add_u32 s42, s94, s61
s_addc_u32 s43, s95, 0
LL 18,90
LA 18
LL 115,91
LC_ 115
PK 111,16,109,56
PK 109,16,109,97
PO 111
WL 0
LO 17,17,114
LU 114,17
HZ 29,114
LL 16,92
LA 16
LL 118,93
LC_ 118
PK 114,17,112,56
PK 112,17,112,97
SU 108
LO 18,18,115
LU 115,18
s_add_u32 s42, s94, s62
s_addc_u32 s43, s95, 0
HS 29,115
LL 17,94
LA 17
LL 119,95
LC_ 119
PK 115,18,113,56
PK 113,18,113,97
SU 112
WL 0
LO 16,16,118
LU 118,16
HS 29,118
LL 18,96
LA 18
LL 122,97
LC_ 122
PK 118,16,116,56
PK 116,16,116,97
PO 118
LO 17,17,119
LU 119,17
HS 29,119
s_add_u32 s42, s94, s63
s_addc_u32 s43, s95, 0
LL 16,98
LA 16
LL 123,99
LC_ 123
PK 119,17,117,56
PK 117,17,117,97
PO 119
WL 0
LO 18,18,122
LU 122,18
HZ 30,122
LL 17,100
LA 17
LL 126,101
LC_ 126
PK 122,18,120,56
PK 120,18,120,97
SU 116
LO 16,16,123
LU 123,16
s_add_u32 s42, s94, s64
s_addc_u32 s43, s95, 0
HS 30,123
LL 18,102
LA 18
LL 127,103
LC_ 127
PK 123,16,121,56
PK 121,16,121,97
SU 120
WL 0
LO 17,17,126
LU 126,17
HS 30,126
PK 126,17,124,56
PK 124,17,124,97
PO 126
LO 18,18,127
LU 127,18
HS 30,127
s_add_u32 s42, s94, s65
s_addc_u32 s43, s95, 0
v_pk_add_f16 v27, v27, v30
PK 127,18,125,56
v_pk_add_f16 v27, v27, v29
PK 125,18,125,97
HX 27
PO 127
W1 27,3072
s_nop 1
v_add_u32 v4, s39, v4
SU 124
s_lshl_b32 s53, s52, 8
s_add_u32 s48, %[alo], s53
s_addc_u32 s49, %[ahi], 0
s_add_u32 s52, s52, %[d]
s_cmp_eq_u32 s52, %[wrap]
s_cselect_b32 s52, %[rst], s52
WL 0
WV 0
s_barrier
s_sub_u32 s39, 0, s39
ds_read_b64 v[14:15], v5 offset:2048
s_nop 1
v_subrev_u32 v5, s39, v5
WL 0
v_add_f32 v14, v14, v15
s_nop 1
v_add_f32_dpp v14, v14, v14 quad_perm:[1,0,3,2] row_mask:0xf bank_mask:0xf
s_nop 1
v_add_f32_dpp v14, v14, v14 quad_perm:[2,3,0,1] row_mask:0xf bank_mask:0xf
s_nop 1
v_add_f32_dpp v14, v14, v14 row_half_mirror row_mask:0xf bank_mask:0xf
v_cvt_f64_f32 v[14:15], v14
s_mov_b64 exec, s[54:55]
SP
s_mov_b64 exec, -1
GE
s_waitcnt vmcnt(0) lgkmcnt(0)
8:
s_branch 6f
.Lk1s0:
FR
s_waitcnt lgkmcnt(0)
v_cmp_gt_u32 vcc, v19, v15
s_nop 4
s_cbranch_vccnz .Lk1s0
s_branch .Lk1r0
.Lk1s1:
FR
s_waitcnt lgkmcnt(0)
v_cmp_gt_u32 vcc, v19, v15
s_nop 4
s_cbranch_vccnz .Lk1s1
s_branch .Lk1r1
.Lk1s2:
FR
s_waitcnt lgkmcnt(0)
v_cmp_gt_u32 vcc, v19, v15
s_nop 4
s_cbranch_vccnz .Lk1s2
s_branch .Lk1r2
.Lk1s3:
FR
s_waitcnt lgkmcnt(0)
v_cmp_gt_u32 vcc, v19, v15
s_nop 4
s_cbranch_vccnz .Lk1s3
s_branch .Lk1r3
.Lk1s4:
FR
s_waitcnt lgkmcnt(0)
v_cmp_gt_u32 vcc, v19, v15
s_nop 4
s_cbranch_vccnz .Lk1s4
s_branch .Lk1r4
.Lk1s5:
FR
s_waitcnt lgkmcnt(0)
v_cmp_gt_u32 vcc, v19, v15
s_nop 4
s_cbranch_vccnz .Lk1s5
s_branch .Lk1r5
.Lk1s6:
FR
s_waitcnt lgkmcnt(0)
v_cmp_gt_u32 vcc, v19, v15
s_nop 4
s_cbranch_vccnz .Lk1s6
s_branch .Lk1r6
.Lk1s7:
FR
s_waitcnt lgkmcnt(0)
v_cmp_gt_u32 vcc, v19, v15
s_nop 4
s_cbranch_vccnz .Lk1s7
s_branch .Lk1r7
.Lk1s8:
FR
s_waitcnt lgkmcnt(0)
v_cmp_gt_u32 vcc, v19, v15
s_nop 4
s_cbranch_vccnz .Lk1s8
s_branch .Lk1r8
.Lk1s9:
FR
s_waitcnt lgkmcnt(0)
v_cmp_gt_u32 vcc, v19, v15
s_nop 4
s_cbranch_vccnz .Lk1s9
s_branch .Lk1r9
.Lk1s10:
FR
s_waitcnt lgkmcnt(0)
v_cmp_gt_u32 vcc, v19, v15
s_nop 4
s_cbranch_vccnz .Lk1s10
s_branch .Lk1r10
.Lk1s11:
FR
s_waitcnt lgkmcnt(0)
v_cmp_gt_u32 vcc, v19, v15
s_nop 4
s_cbranch_vccnz .Lk1s11
s_branch .Lk1r11
.Lk1s12:
FR
s_waitcnt lgkmcnt(0)
v_cmp_gt_u32 vcc, v19, v15
s_nop 4
s_cbranch_vccnz .Lk1s12
s_branch .Lk1r12
.Lk1s13:
FR
s_waitcnt lgkmcnt(0)
v_cmp_gt_u32 vcc, v19, v15
s_nop 4
s_cbranch_vccnz .Lk1s13
s_branch .Lk1r13
)"
R"(.Lk1s14:
FR
s_waitcnt lgkmcnt(0)
v_cmp_gt_u32 vcc, v19, v15
s_nop 4
s_cbranch_vccnz .Lk1s14
s_branch .Lk1r14
.Lk1s15:
FR
s_waitcnt lgkmcnt(0)
v_cmp_gt_u32 vcc, v19, v15
s_nop 4
s_cbranch_vccnz .Lk1s15
s_branch .Lk1r15
.Lk1s16:
FR
s_waitcnt lgkmcnt(0)
v_cmp_gt_u32 vcc, v19, v15
s_nop 4
s_cbranch_vccnz .Lk1s16
s_branch .Lk1r16
.Lk1s17:
FR
s_waitcnt lgkmcnt(0)
v_cmp_gt_u32 vcc, v19, v15
s_nop 4
s_cbranch_vccnz .Lk1s17
s_branch .Lk1r17
.Lk1s18:
FR
s_waitcnt lgkmcnt(0)
v_cmp_gt_u32 vcc, v19, v15
s_nop 4
s_cbranch_vccnz .Lk1s18
s_branch .Lk1r18
.Lk1s19:
FR
s_waitcnt lgkmcnt(0)
v_cmp_gt_u32 vcc, v19, v15
s_nop 4
s_cbranch_vccnz .Lk1s19
s_branch .Lk1r19
.Lk1s20:
FR
s_waitcnt lgkmcnt(0)
v_cmp_gt_u32 vcc, v19, v15
s_nop 4
s_cbranch_vccnz .Lk1s20
s_branch .Lk1r20
.Lk1s21:
FR
s_waitcnt lgkmcnt(0)
v_cmp_gt_u32 vcc, v19, v15
s_nop 4
s_cbranch_vccnz .Lk1s21
s_branch .Lk1r21
6:
.purgem M
.purgem Z
.purgem LZ
.purgem LS
.purgem GZ
.purgem GS
.purgem XL
.purgem C
.purgem LL
.purgem LA
.purgem LC_
.purgem LO
.purgem HZ
.purgem HS
.purgem HX
.purgem AW
.purgem RA
.purgem RV
.purgem W2
.purgem W1
.purgem DW
.purgem DF
.purgem ST
.purgem SU
.purgem SP
.purgem AT
.purgem PFA
.purgem PFN
.purgem PFW
.purgem PF0
.purgem WL
.purgem WV
.purgem LU
.purgem CF
.purgem XS
.purgem AR
.purgem DQ
.purgem GE
.purgem BW
.purgem BR
.purgem LK
.purgem PK
.purgem PO
.purgem LG
.purgem DY
.purgem FP
.purgem FR
)"
      : [nb] "=&s"(nb)
      : [xlo] "s"((unsigned)xp), [xhi] "s"((unsigned)(xp >> 32)), [hlo] "s"((unsigned)hp), [hhi] "s"((unsigned)(hp >> 32)),
        [glo] "s"((unsigned)gp), [ghi] "s"((unsigned)(gp >> 32)), [wlo] "s"((unsigned)wp), [whi] "s"((unsigned)(wp >> 32)),
        [w] "s"(w), [lds] "s"(lds0), [d] "s"(d), [wrap] "s"(wrap), [rst] "s"(rst), [pos0] "s"(pos0), [xo0] "s"(xo0), [xs1] "s"(xs1), [sb1] "s"(sb1), [r0o] "s"(r0o), [tsel] "s"(tsel), [s2] "s"(s2),
        [alo] "s"((unsigned)ap), [ahi] "s"((unsigned)(ap >> 32)), [ca0] "s"(ca0), [pm] "s"(pm), [pf] "s"(pf)
      : K1_CLOB);
}

typedef __bf16 bf16x8_t __attribute__((ext_vector_type(8)));
DI f32x4 mfma16(u32x4 a, u32x4 b, f32x4 c) {
  return __builtin_amdgcn_mfma_f32_16x16x32_bf16(__builtin_bit_cast(bf16x8_t, a), __builtin_bit_cast(bf16x8_t, b), c, 0, 0, 0);
}
DI float grn_gx(double s, float scale) { return round_bf16(__builtin_amdgcn_sqrtf(round_bf16(round_bf16((float)s * scale) + 1e-6f))); }
DI void slow_fc1(const u16* __restrict__ x, const u16* __restrict__ dw, char* __restrict__ hws, float* sacc, int w) {
  const int lane = __builtin_amdgcn_mbcnt_hi(~0u, __builtin_amdgcn_mbcnt_lo(~0u, 0u)), m = lane & 15, g = lane >> 4;
  for (int i = lane; i < HID; i += 64) sacc[i] = 0.f;
#pragma unroll 1
  for (int it = 0; it < 9; ++it) {
    const int sub = blockIdx.x * 36 + it * 4 + w;
    const unsigned px = sub_pixel(x, sub, m);
    u32x4 A[12];
    float sum = 0.f;
    const u16* dq = dw;
    asm volatile("" : "+s"(dq));
#pragma unroll
    for (int ks = 0; ks < 12; ++ks) {
      const u32x4 v = *(const u32x4*)(x + (size_t)px * CH + 32 * ks + 8 * g);
      const u32x4 d = *(const u32x4*)(dq + 32 * ks + 8 * g);
#pragma unroll
      for (int j = 0; j < 4; ++j) {
        A[ks][j] = pack_bf16(bf_lo(v[j]) * bf_lo(d[j]), bf_hi(v[j]) * bf_hi(d[j]));
        sum += bf_lo(A[ks][j]) + bf_hi(A[ks][j]);
      }
      asm volatile("" : "+v"(A[ks]));
    }
    sum += __shfl_xor(sum, 16); sum += __shfl_xor(sum, 32);
    const float mean = sum * (1.0f / CH);
    float var = 0.f;
#pragma unroll
    for (int ks = 0; ks < 12; ++ks) {
#pragma unroll
      for (int j = 0; j < 4; ++j) {
        const float a = bf_lo(A[ks][j]) - mean, b = bf_hi(A[ks][j]) - mean;
        var += a * a + b * b;
      }
      asm volatile("" : "+v"(A[ks]));
    }
    var += __shfl_xor(var, 16); var += __shfl_xor(var, 32);
    const float mb = round_bf16(mean);
    const float rd = 1.0f / round_bf16(sqrtf(round_bf16(round_bf16(var * (1.0f / CH)) + 1e-5f)));
#pragma unroll
    for (int ks = 0; ks < 12; ++ks)
#pragma unroll
      for (int j = 0; j < 4; ++j) A[ks][j] = pack_bf16((bf_lo(A[ks][j]) - mb) * rd, (bf_hi(A[ks][j]) - mb) * rd);
#pragma unroll 1
    for (int tc = 0; tc < 96; ++tc) {
      f32x4 acc = {0.f, 0.f, 0.f, 0.f};
#pragma unroll
      for (int ks = 0; ks < 12; ++ks) acc = mfma16(A[ks], *(const u32x4*)(g_buf + (size_t)(tc * 12 + ks) * 1024 + lane * 16), acc);
      float q = 0.f;
#pragma unroll
      for (int r = 0; r < 4; ++r) { acc[r] = gelu_tanh(acc[r]); q = __builtin_fmaf(acc[r], acc[r], q); }
      typedef unsigned u32x2_ __attribute__((ext_vector_type(2)));
      *(u32x2_*)(hws + h_piece(hws, sub, tc >> 1) + g * 256 + ((m + 4 * g) & 15) * 16 + (tc & 1) * 8) = (u32x2_){pack_bf16(acc[0], acc[1]), pack_bf16(acc[2], acc[3])};
      q += __shfl_xor(q, 16); q += __shfl_xor(q, 32);
      if (g == 0) sacc[tc * 16 + m] += q;
    }
  }
  __builtin_amdgcn_wave_barrier();
  __builtin_amdgcn_fence(__ATOMIC_ACQ_REL, "workgroup");
  for (int i = lane; i < HID; i += 64) atomicAdd((double*)(g_buf + G_ACC2) + i, (double)sacc[i]);
}
DI void slow_bar(unsigned* c, int w) {
  const int lane = __builtin_amdgcn_mbcnt_hi(~0u, __builtin_amdgcn_mbcnt_lo(~0u, 0u));
  asm volatile("s_waitcnt vmcnt(0)" ::: "memory");
  __syncthreads();
  if (w == 0 && lane == 0) {
    __threadfence();
    asm volatile("s_waitcnt vmcnt(0)" ::: "memory");
    __hip_atomic_fetch_add(c, 1u, __ATOMIC_RELAXED, __HIP_MEMORY_SCOPE_AGENT);
    for (int i = 0; i < (1 << 22) && __hip_atomic_load(c, __ATOMIC_RELAXED, __HIP_MEMORY_SCOPE_AGENT) < (unsigned)NWG; ++i) __builtin_amdgcn_s_sleep(32);
    __threadfence();
    asm volatile("s_waitcnt vmcnt(0)" ::: "memory");
  }
  __syncthreads();
}

#define K2_CLOB "memory", "vcc", "scc", "s48", "s49", "s50", "s51", "s52", "s53", "s54", "s55", "s56", "s57", "s58", "s59", "s60", "s61", "s62", "s63", "s64", "s65", "s66", "s67", \
  "s68", "s69", "s70", "s71", "s72", "s73", "s74", "s75", "s76", "s77", "s78", "s79", "s80", "s81", "s82", "s83", "s84", "s85", "s86", "s87", "s88", "s89", "s90", "s91", "s92", "s93", "s94", "s95", "s96", "s97", "s98", "s99", "s38", "s39", "s40", "s41", "s42", "s43", "s44", "s45", "s46", "s47", VA_CLOB
DI void slow_fc2(const u16* __restrict__ x, const char* __restrict__ hws, u16* __restrict__ out, const u16* __restrict__ grn_g, const u16* __restrict__ w2,
                 char* smem, int w) {
  float* sc = (float*)smem;
  float* red = sc + HID;
  u16* wf = (u16*)(smem + 8192);
  const int lane = __builtin_amdgcn_mbcnt_hi(~0u, __builtin_amdgcn_mbcnt_lo(~0u, 0u)), tid = w * 64 + lane, m = lane & 15, g = lane >> 4;
  __syncthreads();
  float part = 0.f;
  for (int c = tid; c < HID; c += 256) {
    const float gx = grn_gx(((const double*)(g_buf + G_ACC2))[c], 1.0f);
    sc[c] = gx; part += gx;
  }
#pragma unroll
  for (int o = 32; o > 0; o >>= 1) part += __shfl_xor(part, o);
  if (lane == 0) red[tid >> 6] = part;
  __syncthreads();
  const float gmean = round_bf16(round_bf16(((red[0] + red[1]) + (red[2] + red[3])) * (1.0f / HID)) + 1e-6f);
  for (int c = tid; c < HID; c += 256) sc[c] = (1.0f + ld_bf16(grn_g + c) * round_bf16(sc[c] / gmean)) ;
#pragma unroll 1
  for (int it = 0; it < 9; ++it) {
    const int sub = blockIdx.x * 36 + it * 4 + (tid >> 6);
    f32x4 acc[24];
#pragma unroll
    for (int i = 0; i < 24; ++i) acc[i] = (f32x4){0.f, 0.f, 0.f, 0.f};
#pragma unroll 1
    for (int t = 0; t < 48; ++t) {
      __syncthreads();
      for (int e = tid; e < 32 * CH; e += 256) {
        const int k = e / CH, n = e % CH;
        wf[((n >> 4) * 64 + (k >> 3) * 16 + (n & 15)) * 8 + (k & 7)] = (u16)pack_bf16(sc[32 * t + k] * ld_bf16(w2 + (size_t)(32 * t + k) * CH + n), 0.f);
      }
      __syncthreads();
      const char* hp = hws + h_piece(hws, sub, t) + (m >> 2) * 256 + (g >> 1) * 8 + 2 * (m & 3);
      u32x4 A;
#pragma unroll
      for (int j = 0; j < 4; ++j) {
        unsigned v[2];
#pragma unroll
        for (int k = 0; k < 2; ++k) {
          v[k] = *(const u16*)(hp + ((8 * (g & 1) + 2 * j + k + (m & 12)) & 15) * 16);
        }
        A[j] = v[0] | (v[1] << 16);
      }
#pragma unroll
      for (int i = 0; i < 24; ++i) acc[i] = mfma16(A, *(const u32x4*)(wf + (i * 64 + lane) * 8), acc[i]);
    }
#pragma unroll
    for (int i = 0; i < 24; ++i) {
      const int n = 16 * i + m;
      const float* bp = g_cs + 4 * 384 + cs_index(n);
      const float bias = (bp[0] + bp[384]) + (bp[768] + bp[1152]);
#pragma unroll
      for (int r = 0; r < 4; ++r) {
        const size_t o = (size_t)(sub_pixel(x, sub, 4 * g) + r) * CH + n;
        out[o] = (u16)pack_bf16(acc[i][r] + bias + ld_bf16(x + o), 0.f);
      }
    }
  }
}
__global__ __launch_bounds__(256, 1) void k2_fc2(const u16* __restrict__ x, char* __restrict__ hws, u16* __restrict__ out,
                                                 const u16* __restrict__ grn_g, const u16* __restrict__ w2, const u16* __restrict__ dw,
                                                 const u16* __restrict__ w1) {
  extern __shared__ __attribute__((aligned(16))) char smem[];
  const int w = __builtin_amdgcn_readfirstlane(threadIdx.x >> 6);
  const unsigned pg = w & 1, nh = w >> 1;
  const unsigned lds0 = lds_addr(smem) + 1024;
  const uint64_t pp = uni64((uint64_t)g_prm + (blockIdx.x * 4 + w) * 256), ap = (uint64_t)g_buf + K1_G_ACC + w * 1024;
  const unsigned wm = lds0 + w * 6144, ab = lds0 + nh * 12288;
  const unsigned hb = lds0 + K2_HB + pg * 2 * K2_RSZ, hm = hb + nh * K2_RSZ;
  const unsigned bm = w * 384, bb = lds0 + K2_E_OFF + nh * 768;
  const unsigned pm = lds0 + K2_PF_OFF, sb = lds0 + K2_S16_OFF, sw = sb + w * 256;
  uint64_t flg;
#define K2_OPS [pp] "s"(pp), [alo] "s"((unsigned)ap), [ahi] "s"((unsigned)(ap >> 32)), [wm] "s"(wm), [hm] "s"(hm), [bm] "s"(bm), [ab] "s"(ab), [hb] "s"(hb), [bb] "s"(bb), \
         [sb] "s"(sb), [sw] "s"(sw), [pm] "s"(pm)
  asm volatile(
R"(.macro MA t,a,b
v_mfma_f32_16x16x32_f16 a[\t:\t+3], v[\b:\b+3], a[\a:\a+3], a[\t:\t+3]
.endm
.macro MV t,a,b
v_mfma_f32_16x16x32_f16 v[\t:\t+3], v[\b:\b+3], a[\a:\a+3], v[\t:\t+3]
.endm
.macro NA t,a,b
v_mfma_f32_16x16x32_f16 a[\t:\t+3], a[\b:\b+3], a[\a:\a+3], a[\t:\t+3]
.endm
.macro NV t,a,b
v_mfma_f32_16x16x32_f16 v[\t:\t+3], a[\b:\b+3], a[\a:\a+3], v[\t:\t+3]
.endm
.macro AN d,s
v_and_b32 v[\d], s93, v[\s]
.endm
.macro SH d
v_lshrrev_b64 v[\d:\d+1], 4, v[\d:\d+1]
.endm
.macro PA_ d,h,r
v_perm_b32 v[\d], v[\h], v[\r], s54
.endm
.macro PB_ d,h,r
v_perm_b32 v[\d], v[\h], v[\r], s55
.endm
.macro AW a,s
v_accvgpr_write_b32 a[\a], v[\s]
.endm
.macro AR d,a
v_accvgpr_read_b32 v[\d], a[\a]
.endm
.macro UL d,x
v_lshlrev_b32 v[\d], 16, v[\x]
.endm
.macro UH d,x
v_and_b32 v[\d], s38, v[\x]
.endm
.macro AD d,b
v_fmac_f32 v[\d], s53, v[\b]
.endm
.macro EF d,e
v_fma_f32 v[\d], v[\d], s39, v[\e]
.endm
.macro TY d,o
ds_read_b64_tr_b16 v[\d:\d+1], v2 offset:\o
.endm
.macro TX d,o
ds_read_b64_tr_b16 v[\d:\d+1], v51 offset:\o
.endm
.macro RA d,o
ds_read_b128 a[\d:\d+3], v1 offset:\o
.endm
.macro RC d,o
ds_read_b128 a[\d:\d+3], v5 offset:\o
.endm
.macro BV d,o
ds_read_b128 v[\d:\d+3], v4 offset:\o
.endm
.macro PA a
s_mov_b32 m0, %[pm]
s_getpc_b64 s[46:47]
s_andn2_b32 s46, s46, 0x7f
s_add_u32 s46, s46, s81
s_addc_u32 s47, s47, 0
s_add_u32 s46, s46, \a
s_addc_u32 s47, s47, 0
.endm
.macro DF
global_load_lds_dwordx4 v0, s[46:47]
.endm
.macro DW o
global_load_lds_dwordx4 v0, s[40:41] offset:\o sc1
.endm
.macro DR o
global_load_lds_dwordx4 v0, s[40:41] offset:\o
.endm
.macro DH o
global_load_lds_dwordx4 v0, s[40:41] offset:\o nt
.endm
.macro DG o
s_mov_b32 exec_hi, 0
global_load_lds_dwordx4 v0, s[40:41] offset:\o nt
s_mov_b32 exec_hi, -1
.endm
.macro XL d,o
global_load_dwordx2 v[\d:\d+1], v47, s[44:45] offset:\o
.endm
.macro ST d,o
global_store_dwordx2 v50, v[\d:\d+1], s[42:43] offset:\o nt sc1
.endm
.macro SA d,o
global_store_dwordx2 v50, a[\d:\d+1], s[42:43] offset:\o nt sc1
.endm
.macro CV d,a,b
v_cvt_pk_bf16_f32 v[\d], v[\a], v[\b]
.endm
.macro RS d,o
ds_read_b128 v[\d:\d+3], v46 offset:\o
.endm
.macro PM d,s
v_pk_mul_f16 v[\d], v[\d], v[\s]
.endm
.macro PF d,n
v_pk_fma_f16 v[\d], v[\d], v[14+\n], v[22+\n]
.endm
.macro FS
global_store_dword v0, v54, s[96:97] sc1
.endm
.macro FL
global_load_dword v54, v55, s[98:99] sc1
.endm
.macro FB
s_cbranch_vccnz 21b
22:
.endm
.macro FC
s_cbranch_vccnz 26b
27:
.endm
.macro FG
s_or_b64 s[54:55], s[44:45], vcc
s_cbranch_scc1 23b
24:
.endm
.macro FX
s_branch 25f
21:
s_sleep 4
global_load_dword v54, v55, s[98:99] sc1
s_waitcnt vmcnt(0)
v_cmp_eq_u32 vcc, 0, v54
s_sub_u32 s80, s80, 1
s_cbranch_scc1 22f
s_nop 2
s_cbranch_vccnz 21b
s_branch 22f
26:
s_sleep 4
global_load_dword v54, v55, s[98:99] sc1
s_waitcnt vmcnt(0)
v_cmp_ne_u32 vcc, 2, v54
s_sub_u32 s80, s80, 1
s_cbranch_scc1 27f
s_nop 2
s_cbranch_vccnz 26b
s_branch 27f
23:
v_mov_b32 v10, 1
s_mov_b64 exec, 1
global_store_dword v0, v10, s[78:79] offset:384 sc1
s_mov_b64 exec, -1
s_nop 4
s_branch 24f
25:
.endm
.macro FP
ds_inc_u32 v0, v128
.endm
.macro FR
ds_read_b32 v10, v0
.endm
.macro FQ n
v_cmp_gt_u32 vcc, \n, v10
.endm
.macro FZ n
s_cbranch_vccnz .Lk2s\n
.Lk2r\n:
.endm
.macro FY n
.Lk2s\n:
FR
s_waitcnt lgkmcnt(0)
FQ \n
s_nop 4
s_cbranch_vccnz .Lk2s\n
s_branch .Lk2r\n
.endm
.macro WL n
s_waitcnt lgkmcnt(\n)
.endm
.macro WV n
s_waitcnt vmcnt(\n)
.endm
.macro M n
.if \n < 82
v_mfma_f32_16x16x32_f16 a[36+4*(\n/2):36+4*(\n/2)+3], v[56+8*((\n/2)%%6)+4*(\n&1):56+8*((\n/2)%%6)+4*(\n&1)+3], a[8*((\n/12)%%4)+4*(\n&1):8*((\n/12)%%4)+4*(\n&1)+3], a[36+4*(\n/2):36+4*(\n/2)+3]
.else
v_mfma_f32_16x16x32_f16 v[-32+4*(\n/2):-32+4*(\n/2)+3], v[56+8*((\n/2)%%6)+4*(\n&1):56+8*((\n/2)%%6)+4*(\n&1)+3], a[8*((\n/12)%%4)+4*(\n&1):8*((\n/12)%%4)+4*(\n&1)+3], v[-32+4*(\n/2):-32+4*(\n/2)+3]
.endif
.endm
.macro R1 n
.if \n < 64
RA 8*(((\n%%24)/2+2*((\n/24)&1))%%4)+4*(\n&1),1024*\n
.else
RC 8*(((\n%%24)/2+2*((\n/24)&1))%%4)+4*(\n&1),1024*\n-49152
.endif
.endm
.macro R n
R1 \n
R1 (\n+1)
.endm
.macro P n
.if \n & 2
PB_ 56+\n,104+8*(\n/16)+(\n&4)+2*((\n/8)&1)+(\n&1),56+(\n|2)
.else
PA_ 56+\n,104+8*(\n/16)+(\n&4)+2*((\n/8)&1)+(\n&1),56+(\n|2)
.endif
.endm
.macro Q n
P \n
P (\n+1)
.endm
.macro mw k
s_add_u32 m0, %[wm], 1024*\k
.endm
.macro mh k
s_add_u32 m0, %[hm], 512*\k
.endm
.macro ad r,k
s_add_u32 s40, s[\r], 512*\k
s_addc_u32 s41, s[\r+1], 0
.endm
.macro ae d,r,k
s_add_u32 s[\d], s[\r], 512*\k
s_addc_u32 s[\d+1], s[\r+1], 0
.endm
.macro bc m,b,d,r
s_bitcmp1_b32 s[\m], \b
s_cselect_b64 s[\d:\d+1], s[\r+2:\r+3], s[\r:\r+1]
.endm
.macro W k
DW 1024*\k
.endm
.macro H k
DH 1024*\k
.endm
k2_code:
s_load_dwordx16 s[48:63], %[pp], 0x0
s_load_dwordx16 s[64:79], %[pp], 0x40
s_load_dwordx16 s[84:99], %[pp], 0x80
s_load_dword s42, %[pp], 0xc0
v_mbcnt_lo_u32_b32 v7, -1, 0
v_mbcnt_hi_u32_b32 v7, -1, v7
v_lshlrev_b32 v0, 4, v7
v_add_u32 v88, 0x1000, v0
v_add_u32 v89, 0x2000, v0
s_mov_b32 s44, %[alo]
s_mov_b32 s45, %[ahi]
global_load_dwordx4 v[132:135], v0, s[44:45]
global_load_dwordx4 v[144:147], v88, s[44:45]
global_load_dwordx4 v[148:151], v89, s[44:45]
ae 44,44,24
global_load_dwordx4 v[168:171], v0, s[44:45]
global_load_dwordx4 v[172:175], v88, s[44:45]
global_load_dwordx4 v[192:195], v89, s[44:45]
s_waitcnt lgkmcnt(0)
s_mov_b64 s[82:83], s[66:67]
bc 49,0,66,60
mh 0
ad 66,0
H 0
DG 1024
ae 44,44,24
global_load_dwordx4 v[196:199], v0, s[44:45]
global_load_dwordx4 v[216:219], v88, s[44:45]
global_load_dwordx4 v[220:223], v89, s[44:45]
ae 44,44,24
global_load_dwordx4 v[224:227], v0, s[44:45]
global_load_dwordx4 v[228:231], v88, s[44:45]
global_load_dwordx4 v[240:243], v89, s[44:45]
v_and_b32 v8, 15, v7
v_lshrrev_b32 v9, 4, v7
v_lshlrev_b32 v10, 2, v7
v_add_u32 v202, s65, v0
global_load_dword v244, v10, s[92:93] offset:0
global_load_dword v245, v10, s[92:93] offset:1024
global_load_dword v246, v10, s[92:93] offset:2048
global_load_dwordx4 v[152:155], v0, s[88:89]
s_mov_b64 exec, 0xff
global_load_dwordx4 v[156:159], v202, s[88:89]
s_mov_b64 exec, -1
s_branch 28f
FY 4
FY 8
FY 12
FY 16
FY 20
FY 24
FY 28
FY 32
FY 36
FY 40
FY 44
FY 48
FY 52
FY 56
FY 60
FY 64
FY 68
FY 72
FY 76
FY 80
FY 84
FY 88
FY 92
FY 96
FY 100
FY 104
FY 108
FY 112
FY 116
FY 120
FY 124
FY 128
28:
FX
s_mov_b32 s92, s64
s_mov_b32 s93, 0x0f0f0f0f
v_add_u32 v1, %[ab], v0
v_add_u32 v5, 0xc000, v1
v_and_b32 v10, 3, v7
v_lshrrev_b32 v11, 2, v7
v_lshl_add_u32 v11, v10, 2, v11
v_and_b32 v11, 15, v11
mw 0
ad 86,0
DR 0
v_lshl_add_u32 v11, v10, 4, v11
v_lshl_add_u32 v2, v11, 4, %[hb]
v_lshl_add_u32 v51, v11, 3, %[hb]
v_lshl_add_u32 v4, v8, 4, %[bb]
s_mov_b32 s38, 0xffff0000
s_mov_b32 s80, 0x40000
s_mov_b32 s81, s52
s_mov_b32 s39, s54
v_lshl_add_u32 v46, v9, 4, %[sb]
v_add_u32 v46, 8192, v46
v_bfe_u32 v10, v7, 1, 2
DR 1024
v_bfe_u32 v11, v7, 3, 1
v_and_b32 v12, 1, v7
v_lshl_add_u32 v11, v11, 1, v12
v_lshl_add_u32 v10, v10, 2, v11
v_lshl_add_u32 v10, v9, 4, v10
v_lshl_add_u32 v90, v10, 2, %[sw]
DR 2048
v_lshl_add_u32 v91, v7, 2, %[sw]
s_and_b32 s42, s51, 0xffff
s_lshr_b32 s43, s51, 16
s_add_u32 s42, s42, %[sb]
s_add_u32 s43, s43, %[sb]
v_lshl_add_u32 v200, v9, 4, s42
v_mov_b32 v201, s43
v_mov_b32 v128, 0x362aaa8f
v_mov_b32 v129, 0x362aaa8f
v_mov_b32 v130, 0x362aaa8f
v_mov_b32 v131, 0x362aaa8f
mw 3
ad 86,6
DR 0
AW 32,128
AW 33,129
AW 34,130
AW 35,131
v_mov_b32 v95, 0
ds_write_b32 v0, v95
s_nop 1
v_mov_b32 v95, 0x7f800000
s_mov_b32 s44, 0x44100000
WV 19
v_add_f64 v[132:133], v[132:133], v[168:169]
WV 18
v_add_f64 v[144:145], v[144:145], v[172:173]
DR 1024
WV 18
v_add_f64 v[148:149], v[148:149], v[192:193]
v_add_f64 v[134:135], v[134:135], v[170:171]
v_add_f64 v[146:147], v[146:147], v[174:175]
v_add_f64 v[150:151], v[150:151], v[194:195]
WV 15
v_add_f64 v[132:133], v[132:133], v[196:197]
WV 14
v_add_f64 v[144:145], v[144:145], v[216:217]
DR 2048
WV 14
v_add_f64 v[148:149], v[148:149], v[220:221]
v_add_f64 v[134:135], v[134:135], v[198:199]
v_add_f64 v[146:147], v[146:147], v[218:219]
v_add_f64 v[150:151], v[150:151], v[222:223]
WV 13
v_add_f64 v[132:133], v[132:133], v[224:225]
WV 12
v_add_f64 v[144:145], v[144:145], v[228:229]
bc 49,1,66,60
mh 9
ad 66,192
H 0
WV 12
)"
R"(v_add_f64 v[148:149], v[148:149], v[240:241]
v_add_f64 v[134:135], v[134:135], v[226:227]
v_add_f64 v[146:147], v[146:147], v[230:231]
v_add_f64 v[150:151], v[150:151], v[242:243]
v_cvt_f32_f64 v248, v[132:133]
v_cvt_f32_f64 v252, v[144:145]
DG 1024
v_cvt_f32_f64 v72, v[148:149]
v_cvt_f32_f64 v249, v[134:135]
v_cvt_f32_f64 v253, v[146:147]
v_cvt_f32_f64 v73, v[150:151]
v_mul_f32 v250, s55, v248
v_mul_f32 v254, s55, v252
bc 48,1,64,56
mh 12
ad 64,192
H 0
v_mul_f32 v74, s55, v72
v_mul_f32 v251, s55, v249
v_mul_f32 v255, s55, v253
v_mul_f32 v75, s55, v73
CV 251,250,251
CV 255,254,255
DG 1024
CV 75,74,75
UL 250,251
UL 254,255
UL 74,75
UH 251,251
UH 255,255
mw 24
ad 86,48
DR 0
UH 75,75
v_add_f32 v250, 0x358637bd, v250
v_add_f32 v254, 0x358637bd, v254
v_add_f32 v74, 0x358637bd, v74
v_add_f32 v251, 0x358637bd, v251
v_add_f32 v255, 0x358637bd, v255
DR 1024
v_add_f32 v75, 0x358637bd, v75
CV 251,250,251
CV 255,254,255
CV 75,74,75
UL 250,251
UL 254,255
DR 2048
UL 74,75
UH 251,251
UH 255,255
UH 75,75
v_sqrt_f32 v250, v250
v_sqrt_f32 v254, v254
mw 27
ad 86,54
DR 0
v_sqrt_f32 v74, v74
v_sqrt_f32 v251, v251
v_sqrt_f32 v255, v255
v_sqrt_f32 v75, v75
CV 251,250,251
CV 255,254,255
DR 1024
CV 75,74,75
UL 250,251
UL 254,255
UL 74,75
UH 251,251
UH 255,255
DR 2048
UH 75,75
v_cmp_le_f32 vcc, s44, v248
v_cmp_le_f32 s[54:55], s44, v249
s_nop 0
v_cndmask_b32 v248, v95, v250, vcc
v_cndmask_b32 v249, v95, v251, s[54:55]
v_add_f32 v248, v248, v249
v_cmp_le_f32 vcc, s44, v252
v_cmp_le_f32 s[54:55], s44, v253
s_nop 0
v_cndmask_b32 v252, v95, v254, vcc
v_cndmask_b32 v253, v95, v255, s[54:55]
v_add_f32 v252, v252, v253
v_cmp_le_f32 vcc, s44, v72
v_cmp_le_f32 s[54:55], s44, v73
s_nop 0
v_cndmask_b32 v72, v95, v74, vcc
v_cndmask_b32 v73, v95, v75, s[54:55]
bc 49,2,66,60
mh 18
ad 66,384
H 0
v_add_f32 v72, v72, v73
v_add_f32 v92, v248, v252
v_add_f32 v92, v92, v72
s_nop 1
v_add_f32_dpp v92, v92, v92 row_shr:1 row_mask:0xf bank_mask:0xf
s_nop 1
v_add_f32_dpp v92, v92, v92 row_shr:2 row_mask:0xf bank_mask:0xf
s_nop 1
v_add_f32_dpp v92, v92, v92 row_shr:4 row_mask:0xf bank_mask:0xf
s_nop 1
v_add_f32_dpp v92, v92, v92 row_shr:8 row_mask:0xf bank_mask:0xf
s_nop 1
v_add_f32_dpp v92, v92, v92 row_bcast:15 row_mask:0xa bank_mask:0xf
s_nop 1
v_add_f32_dpp v92, v92, v92 row_bcast:31 row_mask:0xc bank_mask:0xf
ds_write_b32 v91, v92 offset:7168
v_mov_b32 v10, %[sb]
WL 0
s_barrier
ds_read_b32 v96, v10 offset:7420
ds_read_b32 v97, v10 offset:7676
ds_read_b32 v98, v10 offset:7932
ds_read_b32 v99, v10 offset:8188
WV 21
UL 76,244
WV 20
UL 80,245
WV 19
UL 84,246
UH 77,244
DG 1024
UH 81,245
UH 85,246
v_mov_b32 v93, 0
WL 0
v_add_f32 v96, v96, v97
v_add_f32 v98, v98, v99
v_add_f32 v96, v96, v98
bc 48,2,64,56
mh 21
ad 64,384
H 0
v_mul_f32 v94, 0x3a2aaaab, v96
v_cmp_nlt_f32 s[44:45], v96, v95
CV 94,94,94
UL 94,94
v_add_f32 v94, 0x358637bd, v94
CV 94,94,94
UL 94,94
H 1
v_rcp_f32 v94, v94
s_nop 0
v_mul_f32 v250, v94, v250
v_mul_f32 v254, v94, v254
v_mul_f32 v74, v94, v74
v_mul_f32 v251, v94, v251
v_mul_f32 v255, v94, v255
v_mul_f32 v75, v94, v75
H 2
CV 251,250,251
CV 255,254,255
CV 75,74,75
UL 250,251
UL 254,255
UL 74,75
mw 48
ad 86,96
DR 0
UH 251,251
UH 255,255
UH 75,75
v_fma_f32 v250, v76, v250, 1.0
v_fma_f32 v254, v80, v254, 1.0
v_fma_f32 v74, v84, v74, 1.0
DR 1024
v_fma_f32 v251, v77, v251, 1.0
v_fma_f32 v255, v81, v255, 1.0
v_fma_f32 v75, v85, v75, 1.0
v_cvt_pk_f16_f32 v76, v250, v251
v_cvt_pk_f16_f32 v80, v254, v255
v_cvt_pk_f16_f32 v84, v74, v75
ds_write_b32 v90, v76 offset:8192
ds_write_b32 v90, v80 offset:1024
ds_write_b32 v90, v84 offset:2048
v_max3_f32 v93, |v250|, |v251|, v93
DR 2048
v_max3_f32 v93, |v254|, |v255|, v93
v_max3_f32 v93, |v74|, |v75|, v93
v_cmp_lt_f32 vcc, 0x43000000, v93
WL 0
WV 16
s_barrier
TY 106,520
TY 110,14344
TX 58,0
TX 62,13824
TY 66,512
TY 70,14336
RS 14,0
RS 18,64
ds_read_b128 v[176:179], v200
ds_read_b128 v[180:183], v201
R 0
R 2
FG
s_mov_b32 s54, 0x06020400
s_mov_b32 s55, 0x07030501
WL 13
AN 104,106
AN 105,107
SH 106
AN 106,106
mw 51
ad 86,102
DR 0
AN 107,107
WL 12
AN 108,110
AN 109,111
SH 110
AN 110,110
AN 111,111
DR 1024
WL 10
Q 0
Q 2
Q 4
DR 2048
Q 6
R 4
R 6
WL 9
v_pk_mul_f16 v22, v14, s92
v_pk_mul_f16 v23, v15, s92
v_pk_mul_f16 v24, v16, s92
v_pk_mul_f16 v25, v17, s92
v_pk_mul_f16 v26, v18, s92
v_pk_mul_f16 v27, v19, s92
v_pk_mul_f16 v28, v20, s92
v_pk_mul_f16 v29, v21, s92
PF 56,0
PF 57,1
PF 58,2
PF 59,3
PF 60,4
PF 61,5
PF 62,6
PF 63,7
Q 8
Q 10
Q 12
Q 14
PF 64,0
PF 65,1
PF 66,2
PF 67,3
PF 68,4
PF 69,5
PF 70,6
PF 71,7
PM 152,176
PM 153,177
PM 154,178
PM 155,179
WL 2
PM 156,180
PM 157,181
PM 158,182
PM 159,183
s_mov_b32 s42, 0x1c001c00
v_pk_mul_f16 v152, v152, s42
v_pk_mul_f16 v153, v153, s42
v_pk_mul_f16 v154, v154, s42
v_pk_mul_f16 v155, v155, s42
v_pk_mul_f16 v156, v156, s42
v_pk_mul_f16 v157, v157, s42
v_pk_mul_f16 v158, v158, s42
v_pk_mul_f16 v159, v159, s42
global_store_dwordx4 v0, v[152:155], s[90:91] sc1
s_mov_b64 exec, 0xff
global_store_dwordx4 v202, v[156:159], s[90:91] sc1
s_mov_b64 exec, -1
s_mov_b32 s42, 0x3c003c00
v_dot2_f32_f16 v52, v152, s42, 0
v_dot2_f32_f16 v53, v156, s42, 0
s_nop 1
v_dot2_f32_f16 v52, v153, s42, v52
v_dot2_f32_f16 v53, v157, s42, v53
s_nop 1
v_dot2_f32_f16 v52, v154, s42, v52
v_dot2_f32_f16 v53, v158, s42, v53
s_nop 1
v_dot2_f32_f16 v52, v155, s42, v52
v_dot2_f32_f16 v53, v159, s42, v53
s_nop 1
v_mfma_f32_16x16x32_f16 a[36:39], v[56:59], a[0:3], a[32:35]
mw 72
RS 14,128
RS 18,192
TY 106,5128
M 1
ad 86,144
DR 0
TY 110,18952
v_mfma_f32_16x16x32_f16 a[60:63], v[60:63], a[12:15], a[32:35]
DR 1024
TX 32,4608
TX 36,18432
M 12
DR 2048
TY 40,5120
TY 44,18944
v_mfma_f32_16x16x32_f16 a[64:67], v[64:67], a[8:11], a[32:35]
mw 75
M 15
R 10
v_mfma_f32_16x16x32_f16 a[40:43], v[68:71], a[4:7], a[32:35]
ad 86,150
DR 0
M 2
R 8
v_mfma_f32_16x16x32_f16 a[88:91], v[64:67], a[16:19], a[32:35]
WL 10
DR 1024
v_pk_mul_f16 v22, v14, s92
M 27
DR 2048
v_pk_mul_f16 v23, v15, s92
v_mfma_f32_16x16x32_f16 a[84:87], v[56:59], a[16:19], a[32:35]
v_pk_mul_f16 v24, v16, s92
v_pk_mul_f16 v25, v17, s92
M 25
v_pk_mul_f16 v26, v18, s92
v_pk_mul_f16 v27, v19, s92
v_mfma_f32_16x16x32_f16 a[108:111], v[56:59], a[24:27], a[32:35]
WV 19
v_pk_mul_f16 v28, v20, s92
v_pk_mul_f16 v29, v21, s92
M 37
WL 0
AN 104,106
AN 105,107
v_mfma_f32_16x16x32_f16 a[112:115], v[64:67], a[24:27], a[32:35]
s_barrier
R 24
M 39
R 26
v_mfma_f32_16x16x32_f16 a[136:139], v[64:67], a[0:3], a[32:35]
mw 0
SH 106
AN 106,106
M 51
ad 86,192
DR 0
v_mfma_f32_16x16x32_f16 a[132:135], v[56:59], a[0:3], a[32:35]
DR 1024
AN 107,107
M 49
R 28
v_mfma_f32_16x16x32_f16 a[156:159], v[56:59], a[8:11], a[32:35]
DR 2048
AN 108,110
M 61
mw 3
AN 109,111
SH 110
v_mfma_f32_16x16x32_f16 a[160:163], v[64:67], a[8:11], a[32:35]
ad 86,198
DR 0
M 63
WL 4
R 30
v_mfma_f32_16x16x32_f16 a[184:187], v[64:67], a[16:19], a[32:35]
DR 1024
AN 110,110
M 75
DR 2048
AN 111,111
v_mfma_f32_16x16x32_f16 a[180:183], v[56:59], a[16:19], a[32:35]
PA_ 30,104,32
PA_ 31,105,33
M 73
WL 4
R 32
v_mfma_f32_16x16x32_f16 v[136:139], v[56:59], a[24:27], v[128:131]
PB_ 32,104,32
PB_ 33,105,33
M 85
PA_ 34,108,36
PA_ 35,109,37
v_mfma_f32_16x16x32_f16 v[140:143], v[64:67], a[24:27], v[128:131]
PB_ 36,108,36
PB_ 37,109,37
M 87
WL 4
R 34
v_mfma_f32_16x16x32_f16 v[164:167], v[64:67], a[0:3], v[128:131]
PF 30,0
PF 31,1
M 99
PF 32,2
PF 33,3
v_mfma_f32_16x16x32_f16 v[160:163], v[56:59], a[0:3], v[128:131]
PF 34,4
PF 35,5
M 97
WL 4
PF 36,6
)"
R"(PF 37,7
v_mfma_f32_16x16x32_f16 v[184:187], v[56:59], a[8:11], v[128:131]
WV 14
AW 200,30
AW 201,31
M 109
WL 0
AW 202,32
AW 203,33
v_mfma_f32_16x16x32_f16 v[188:191], v[64:67], a[8:11], v[128:131]
s_barrier
R 48
TY 114,6664
M 111
R 50
TY 118,20488
v_mfma_f32_16x16x32_f16 v[208:211], v[56:59], a[16:19], v[128:131]
mw 24
AW 204,34
AW 205,35
M 121
ad 86,240
DR 0
v_mfma_f32_16x16x32_f16 v[232:235], v[60:63], a[28:31], v[128:131]
DR 1024
AW 206,36
M 132
DR 2048
AW 207,37
PA_ 38,106,40
PA_ 39,107,41
PB_ 40,106,40
PB_ 41,107,41
v_mfma_f32_16x16x32_f16 v[236:239], v[64:67], a[24:27], v[128:131]
mw 27
PA_ 42,110,44
PA_ 43,111,45
PB_ 44,110,44
PB_ 45,111,45
PF 38,0
PF 39,1
M 135
WL 0
R 54
PF 40,2
PF 41,3
PF 42,4
PF 43,5
AN 112,114
AN 113,115
SH 114
AN 114,114
v_mfma_f32_16x16x32_f16 v[212:215], v[68:71], a[20:23], v[128:131]
ad 86,246
DR 0
PF 44,6
PF 45,7
AW 208,38
AW 209,39
AN 115,115
AN 116,118
AN 117,119
SH 118
M 122
R 52
AW 210,40
AW 211,41
AW 212,42
AW 213,43
AN 118,118
AN 119,119
NA 36,0,200
DR 1024
AW 214,44
AW 215,45
NA 36,4,204
DR 2048
TX 32,6144
TX 36,19968
NA 60,12,204
bc 49,3,66,60
TY 40,6656
TY 44,20480
NA 60,8,200
mh 0
WL 2
PA_ 30,112,32
PA_ 31,113,33
PB_ 32,112,32
PB_ 33,113,33
PA_ 34,116,36
PA_ 35,117,37
PB_ 36,116,36
PB_ 37,117,37
PF 30,0
PF 31,1
NA 64,8,208
ad 66,576
H 0
PF 32,2
PF 33,3
PF 34,4
PF 35,5
PF 36,6
PF 37,7
AW 216,30
AW 217,31
NA 64,12,212
WL 0
DG 1024
AW 218,32
AW 219,33
AW 220,34
AW 221,35
AW 222,36
AW 223,37
NA 40,4,212
bc 48,3,64,56
TY 106,9736
TY 110,23560
NA 40,0,208
mh 3
PA_ 38,114,40
PA_ 39,115,41
PB_ 40,114,40
PB_ 41,115,41
PA_ 42,118,44
PA_ 43,119,45
PB_ 44,118,44
PB_ 45,119,45
PF 38,0
PF 39,1
v_mfma_f32_16x16x32_f16 a[44:47], a[216:219], a[0:3], a[32:35]
ad 64,576
H 0
PF 40,2
PF 41,3
PF 42,4
PF 43,5
PF 44,6
PF 45,7
AW 224,38
AW 225,39
NA 44,4,220
H 1
AW 226,40
AW 227,41
AW 228,42
AW 229,43
AW 230,44
AW 231,45
v_mfma_f32_16x16x32_f16 a[68:71], a[220:223], a[12:15], a[32:35]
H 2
RS 14,256
TX 58,9216
NA 68,8,216
RS 18,320
TX 62,23040
TY 66,9728
v_mfma_f32_16x16x32_f16 a[72:75], a[224:227], a[8:11], a[32:35]
WL 5
TY 70,23552
AN 104,106
NA 72,12,228
R 58
v_mfma_f32_16x16x32_f16 a[48:51], a[228:231], a[4:7], a[32:35]
AN 105,107
SH 106
NA 48,0,224
R 56
v_mfma_f32_16x16x32_f16 a[96:99], a[224:227], a[16:19], a[32:35]
AN 106,106
AN 107,107
NA 96,20,228
WL 7
v_pk_mul_f16 v22, v14, s92
v_pk_mul_f16 v23, v15, s92
v_mfma_f32_16x16x32_f16 a[92:95], a[216:219], a[16:19], a[32:35]
v_pk_mul_f16 v24, v16, s92
v_pk_mul_f16 v25, v17, s92
NA 92,20,220
v_pk_mul_f16 v26, v18, s92
v_pk_mul_f16 v27, v19, s92
NA 88,16,208
v_pk_mul_f16 v28, v20, s92
v_pk_mul_f16 v29, v21, s92
NA 88,20,212
AN 108,110
AN 109,111
NA 84,16,200
SH 110
AN 110,110
NA 84,20,204
WL 6
AN 111,111
P 0
NA 108,24,200
Q 1
NA 108,28,204
Q 3
NA 112,24,208
Q 5
NA 112,28,212
P 7
PF 56,0
v_mfma_f32_16x16x32_f16 a[116:119], a[216:219], a[24:27], a[32:35]
WV 17
PF 57,1
PF 58,2
NA 116,28,220
WL 0
PF 59,3
PF 60,4
v_mfma_f32_16x16x32_f16 a[120:123], a[224:227], a[24:27], a[32:35]
s_barrier
R 72
TY 114,11272
NA 120,28,228
R 74
TY 118,25096
v_mfma_f32_16x16x32_f16 a[144:147], a[224:227], a[0:3], a[32:35]
mw 48
v_mov_b32 v54, 1
s_mov_b64 exec, 1
FS
s_mov_b64 exec, -1
NA 144,4,228
ad 86,288
DR 0
TX 74,10752
v_mfma_f32_16x16x32_f16 a[140:143], a[216:219], a[0:3], a[32:35]
PF 61,5
PF 62,6
TX 78,24576
NA 140,4,220
DR 1024
PF 63,7
TY 82,11264
NA 136,0,208
Q 8
TY 86,25088
NA 136,4,212
DR 2048
P 10
TY 122,12808
NA 132,0,200
Q 11
TY 126,26632
NA 132,4,204
WL 6
R 76
TX 90,12288
NA 156,8,200
mw 51
Q 13
TX 94,26112
NA 156,12,204
ad 86,294
DR 0
TY 98,12800
NA 160,8,208
P 15
PF 64,0
TY 102,26624
NA 160,12,212
DR 1024
PF 65,1
v_mfma_f32_16x16x32_f16 a[164:167], a[216:219], a[8:11], a[32:35]
PF 66,2
PF 67,3
NA 164,12,220
DR 2048
PF 68,4
v_mfma_f32_16x16x32_f16 a[168:171], a[224:227], a[8:11], a[32:35]
PF 69,5
PF 70,6
NA 168,12,228
R 78
v_mfma_f32_16x16x32_f16 a[192:195], a[224:227], a[16:19], a[32:35]
bc 49,4,66,60
PF 71,7
NA 192,20,228
mh 9
AN 112,114
AN 113,115
v_mfma_f32_16x16x32_f16 a[188:191], a[216:219], a[16:19], a[32:35]
ad 66,768
H 0
NA 188,20,220
SH 114
AN 114,114
NA 184,16,208
DG 1024
NA 184,20,212
AN 115,115
AN 116,118
NA 180,16,200
bc 48,4,64,56
AN 117,119
NA 180,20,204
WL 2
R 80
NV 136,24,200
mh 12
SH 118
AN 118,118
NV 136,28,204
ad 64,768
H 0
NV 140,24,208
AN 119,119
P 16
NV 140,28,212
H 1
P 17
v_mfma_f32_16x16x32_f16 v[144:147], a[216:219], a[24:27], v[128:131]
Q 18
NV 144,28,220
H 2
P 20
v_mfma_f32_16x16x32_f16 v[148:151], a[224:227], a[24:27], v[128:131]
Q 21
NV 148,28,228
R 82
v_mfma_f32_16x16x32_f16 v[172:175], a[224:227], a[0:3], v[128:131]
P 23
PF 72,0
NV 172,4,228
PF 73,1
PF 74,2
v_mfma_f32_16x16x32_f16 v[168:171], a[216:219], a[0:3], v[128:131]
PF 75,3
PF 76,4
NV 168,4,220
PF 77,5
PF 78,6
NV 164,0,208
PF 79,7
P 24
NV 164,4,212
Q 25
NV 160,0,200
Q 27
NV 160,4,204
WL 2
Q 29
NV 184,8,200
P 31
PF 80,0
NV 184,12,204
PF 81,1
PF 82,2
NV 188,8,208
PF 83,3
PF 84,4
NV 188,12,212
PF 85,5
PF 86,6
v_mfma_f32_16x16x32_f16 v[192:195], a[216:219], a[8:11], v[128:131]
WV 23
PF 87,7
AN 120,122
NV 192,12,220
WL 0
AN 121,123
SH 122
v_mfma_f32_16x16x32_f16 v[196:199], a[224:227], a[8:11], v[128:131]
s_barrier
R 0
NV 196,12,228
R 2
NV 208,16,200
mw 72
AN 122,122
AN 123,123
NV 208,20,204
ad 86,336
DR 0
NV 232,28,204
AN 124,126
AN 125,127
NV 232,24,200
DR 1024
SH 126
NV 236,24,208
AN 126,126
AN 127,127
NV 236,28,212
DR 2048
P 32
NV 212,20,212
Q 33
NV 212,16,208
mw 75
Q 35
v_mfma_f32_16x16x32_f16 v[216:219], a[216:219], a[16:19], v[128:131]
ad 86,342
DR 0
NV 216,20,220
Q 37
)"
R"(v_mfma_f32_16x16x32_f16 v[240:243], a[220:223], a[28:31], v[128:131]
DR 1024
P 39
NV 240,24,216
PF 88,0
PF 89,1
v_mfma_f32_16x16x32_f16 v[244:247], a[224:227], a[24:27], v[128:131]
DR 2048
PF 90,2
NV 244,28,228
R 6
v_mfma_f32_16x16x32_f16 v[220:223], a[228:231], a[20:23], v[128:131]
PF 91,3
PF 92,4
NV 220,16,224
WL 2
R 4
M 0
PF 93,5
PF 94,6
M 1
PF 95,7
P 40
M 13
Q 41
M 12
Q 43
M 14
Q 45
M 15
P 47
PF 96,0
M 3
PF 97,1
PF 98,2
M 2
PF 99,3
PF 100,4
M 4
PF 101,5
PF 102,6
M 5
PF 103,7
M 17
RS 14,384
RS 18,448
M 16
M 18
M 19
M 7
M 6
v_mfma_f32_16x16x32_f16 a[52:55], v[88:91], a[0:3], a[32:35]
WL 0
v_pk_mul_f16 v22, v14, s92
v_pk_mul_f16 v23, v15, s92
M 9
v_pk_mul_f16 v24, v16, s92
v_pk_mul_f16 v25, v17, s92
v_mfma_f32_16x16x32_f16 a[76:79], v[92:95], a[12:15], a[32:35]
v_pk_mul_f16 v26, v18, s92
v_pk_mul_f16 v27, v19, s92
M 20
v_pk_mul_f16 v28, v20, s92
v_pk_mul_f16 v29, v21, s92
v_mfma_f32_16x16x32_f16 a[80:83], v[96:99], a[8:11], a[32:35]
v_lshrrev_b32 v9, 8, v0
v_bfe_u32 v8, v0, 4, 4
M 23
R 10
v_mfma_f32_16x16x32_f16 a[56:59], v[100:103], a[4:7], a[32:35]
v_lshlrev_b32 v10, 2, v9
M 10
R 8
v_mfma_f32_16x16x32_f16 a[104:107], v[96:99], a[16:19], a[32:35]
s_mov_b32 s42, 0xcb527610
M 35
s_movk_i32 s43, 0xc00
v_lshlrev_b32 v11, 3, v8
v_bfe_u32 v3, s42, v10, 4
v_mfma_f32_16x16x32_f16 a[100:103], v[88:91], a[16:19], a[32:35]
v_add_u32 v10, 16, v10
v_mad_u32_u24 v3, v3, s43, v11
M 33
v_bfe_u32 v48, s42, v10, 4
v_add_u32 v10, 1, v9
M 30
v_mad_u32_u24 v48, v48, s43, v11
v_and_b32 v10, 2, v10
M 31
v_sub_u32 v10, 1, v10
v_mul_i32_i24 v49, s43, v10
M 28
M 29
M 26
M 27
M 24
M 25
M 36
M 37
M 38
M 39
M 40
M 41
M 42
M 43
WV 18
v_mfma_f32_16x16x32_f16 a[124:127], v[88:91], a[24:27], a[32:35]
WL 0
M 45
s_barrier
R 24
TY 106,520
v_mfma_f32_16x16x32_f16 a[128:131], v[96:99], a[24:27], a[32:35]
TY 110,14344
TX 32,0
TX 36,13824
M 47
R 26
TY 40,512
v_mfma_f32_16x16x32_f16 a[152:155], v[96:99], a[0:3], a[32:35]
TY 44,14336
TY 114,2056
TY 118,15880
M 59
v_mfma_f32_16x16x32_f16 a[148:151], v[88:91], a[0:3], a[32:35]
M 57
M 54
WL 9
AN 104,106
AN 105,107
M 55
bc 49,5,66,60
SH 106
M 52
mh 18
AN 106,106
AN 107,107
M 53
ad 66,960
H 0
M 50
WL 8
AN 108,110
AN 109,111
M 51
SH 110
AN 110,110
M 48
WL 6
AN 111,111
PA_ 30,104,32
M 49
R 28
M 60
DG 1024
M 61
PA_ 31,105,33
PB_ 32,104,32
M 62
PB_ 33,105,33
PA_ 34,108,36
M 63
PA_ 35,109,37
PB_ 36,108,36
M 64
PB_ 37,109,37
PF 30,0
M 65
PF 31,1
PF 32,2
M 66
v_lshlrev_b32 v55, 4, v0
PF 33,3
M 67
FL
PF 34,4
v_mfma_f32_16x16x32_f16 a[172:175], v[88:91], a[8:11], a[32:35]
PF 35,5
PF 36,6
M 69
PF 37,7
AW 200,30
v_mfma_f32_16x16x32_f16 a[176:179], v[96:99], a[8:11], a[32:35]
AW 201,31
AW 202,32
M 71
R 30
v_mfma_f32_16x16x32_f16 v[132:135], v[96:99], a[16:19], v[128:131]
AW 203,33
AW 204,34
M 83
AW 205,35
AW 206,36
v_mfma_f32_16x16x32_f16 a[196:199], v[88:91], a[16:19], a[32:35]
WL 2
AW 207,37
PA_ 38,106,40
M 81
PA_ 39,107,41
PB_ 40,106,40
TX 32,1536
M 78
PB_ 41,107,41
PA_ 42,110,44
TX 36,15360
M 79
PA_ 43,111,45
PB_ 44,110,44
M 76
PB_ 45,111,45
PF 38,0
M 77
PF 39,1
PF 40,2
M 74
PF 41,3
PF 42,4
M 75
PF 43,5
PF 44,6
M 72
PF 45,7
AW 208,38
M 73
R 32
M 84
AW 209,39
AW 210,40
M 85
bc 48,5,64,56
AW 211,41
M 86
mh 21
AW 212,42
AW 213,43
M 87
ad 64,960
H 0
M 88
AW 214,44
AW 215,45
M 89
TY 40,2048
TY 44,15872
TY 122,3592
M 90
AN 112,114
AN 113,115
TY 126,17416
M 91
H 1
SH 114
v_mfma_f32_16x16x32_f16 v[152:155], v[88:91], a[24:27], v[128:131]
AN 114,114
AN 115,115
M 93
AN 116,118
AN 117,119
v_mfma_f32_16x16x32_f16 v[156:159], v[96:99], a[24:27], v[128:131]
SH 118
AN 118,118
M 95
R 34
v_mfma_f32_16x16x32_f16 v[180:183], v[96:99], a[0:3], v[128:131]
H 2
AN 119,119
M 107
WL 6
PA_ 30,112,32
PA_ 31,113,33
v_mfma_f32_16x16x32_f16 v[176:179], v[88:91], a[0:3], v[128:131]
PB_ 32,112,32
PB_ 33,113,33
M 105
PA_ 34,116,36
PA_ 35,117,37
M 102
PB_ 36,116,36
PB_ 37,117,37
M 103
PF 30,0
PF 31,1
M 100
PF 32,2
PF 33,3
M 101
PF 34,4
PF 35,5
M 98
PF 36,6
PF 37,7
M 99
AW 216,30
AW 217,31
M 96
AW 218,32
AW 219,33
M 97
AW 220,34
AW 221,35
M 108
AW 222,36
AW 223,37
M 109
WL 0
PA_ 38,114,40
PA_ 39,115,41
TX 32,3072
M 110
WV 3
v_cmp_eq_u32 vcc, 0, v54
PB_ 40,114,40
TX 36,16896
M 111
PB_ 41,115,41
PA_ 42,118,44
M 112
PA_ 43,119,45
PB_ 44,118,44
M 113
FB
PB_ 45,119,45
PF 38,0
M 114
PF 39,1
PF 40,2
M 115
PF 41,3
PF 42,4
v_mfma_f32_16x16x32_f16 v[200:203], v[88:91], a[8:11], v[128:131]
PF 43,5
PF 44,6
M 117
s_barrier
R 48
v_mfma_f32_16x16x32_f16 v[204:207], v[96:99], a[8:11], v[128:131]
mw 0
PF 45,7
AW 224,38
M 119
R 50
M 120
ad 84,384
W 0
M 121
W 1
AW 225,39
M 133
W 2
AW 226,40
M 132
mw 3
AW 227,41
AW 228,42
M 134
ad 84,390
W 0
M 135
W 1
AW 229,43
M 123
W 2
AW 230,44
M 122
mw 24
AW 231,45
AN 120,122
M 124
ad 84,432
W 0
TY 40,3584
M 125
W 1
TY 44,17408
M 137
W 2
AN 121,123
M 136
mw 27
SH 122
AN 122,122
M 138
ad 84,438
W 0
M 139
W 1
AN 123,123
M 127
W 2
AN 124,126
M 126
AN 125,127
SH 126
v_mfma_f32_16x16x32_f16 v[224:227], v[88:91], a[16:19], v[128:131]
AN 126,126
AN 127,127
M 129
WL 2
PA_ 30,120,32
PA_ 31,121,33
)"
R"(v_mfma_f32_16x16x32_f16 v[248:251], v[92:95], a[28:31], v[128:131]
PB_ 32,120,32
PB_ 33,121,33
M 140
PA_ 34,124,36
PA_ 35,125,37
v_mfma_f32_16x16x32_f16 v[252:255], v[96:99], a[24:27], v[128:131]
PB_ 36,124,36
PB_ 37,125,37
M 143
R 54
v_mfma_f32_16x16x32_f16 v[228:231], v[100:103], a[20:23], v[128:131]
PF 30,0
PF 31,1
M 130
R 52
NA 36,0,200
PF 32,2
PF 33,3
NA 36,4,204
PF 34,4
PF 35,5
NA 60,12,204
PF 36,6
PF 37,7
NA 60,8,200
AW 232,30
AW 233,31
NA 64,8,208
AW 234,32
AW 235,33
NA 64,12,212
AW 236,34
AW 237,35
NA 40,4,212
AW 238,36
AW 239,37
NA 40,0,208
WL 4
PA_ 38,122,40
PA_ 39,123,41
NA 44,0,216
PB_ 40,122,40
PB_ 41,123,41
NA 44,4,220
PA_ 42,126,44
PA_ 43,127,45
NA 68,12,220
PB_ 44,126,44
PB_ 45,127,45
NA 68,8,216
PF 38,0
PF 39,1
NA 72,8,224
PF 40,2
PF 41,3
NA 72,12,228
PF 42,4
PF 43,5
NA 48,4,228
PF 44,6
PF 45,7
NA 48,0,224
AW 240,38
AW 241,39
NA 52,0,232
AW 242,40
AW 243,41
NA 52,4,236
AW 244,42
AW 245,43
NA 76,12,236
AW 246,44
AW 247,45
NA 76,8,232
NA 80,8,240
NA 80,12,244
R 58
NA 56,4,244
NA 56,0,240
WL 2
R 56
NA 104,16,240
NA 104,20,244
NA 100,16,232
NA 100,20,236
NA 96,16,224
NA 96,20,228
NA 92,16,216
NA 92,20,220
NA 88,16,208
NA 88,20,212
NA 84,16,200
NA 84,20,204
NA 108,24,200
NA 108,28,204
NA 112,24,208
NA 112,28,212
NA 116,24,216
NA 116,28,220
NA 120,24,224
NA 120,28,228
NA 124,24,232
WL 0
NA 124,28,236
s_barrier
R 72
TY 106,5128
NA 128,24,240
mw 48
TY 110,18952
TX 58,4608
TX 62,18432
NA 128,28,244
R 74
TY 66,5120
NA 152,0,240
ad 84,480
W 0
TY 70,18944
NA 152,4,244
v_lshrrev_b32 v8, 1, v0
s_mov_b64 exec, s[76:77]
global_load_dwordx2 v[6:7], v8, s[78:79] sc1
s_mov_b64 exec, -1
NA 148,0,232
TY 114,6664
TY 118,20488
TX 74,6144
NA 148,4,236
WL 7
TX 78,19968
TY 82,6656
TY 86,20480
NA 144,0,224
W 1
AN 104,106
TY 122,8200
NA 144,4,228
AN 105,107
SH 106
TY 126,22024
NA 140,0,216
AN 106,106
AN 107,107
TX 90,7680
NA 140,4,220
AN 108,110
AN 109,111
TX 94,21504
NA 136,0,208
W 2
SH 110
TY 98,8192
NA 136,4,212
WL 9
AN 110,110
AN 111,111
TY 102,22016
NA 132,0,200
Q 0
NA 132,4,204
R 76
NA 156,8,200
mw 51
Q 2
NA 156,12,204
ad 84,486
W 0
NA 160,8,208
Q 4
NA 160,12,212
Q 6
NA 164,8,216
Q 8
NA 164,12,220
W 1
P 10
NA 168,8,224
Q 11
NA 168,12,228
Q 13
NA 172,8,232
P 15
AN 112,114
NA 172,12,236
W 2
AN 113,115
NA 176,8,240
SH 114
AN 114,114
NA 176,12,244
R 78
NV 132,16,240
AN 115,115
AN 116,118
NV 132,20,244
bc 49,6,66,60
AN 117,119
NA 196,16,232
mh 0
SH 118
AN 118,118
NA 196,20,236
ad 66,1152
H 0
NA 192,16,224
WL 2
AN 119,119
P 16
NA 192,20,228
Q 17
NA 188,16,216
Q 19
NA 188,20,220
DG 1024
NA 184,16,208
Q 21
NA 184,20,212
Q 23
NA 180,16,200
Q 25
NA 180,20,204
R 80
NV 136,24,200
bc 48,6,64,56
P 27
NV 136,28,204
mh 3
Q 28
NV 140,24,208
ad 64,1152
H 0
NV 140,28,212
Q 30
NV 144,24,216
AN 120,122
AN 121,123
NV 144,28,220
SH 122
AN 122,122
NV 148,24,224
H 1
AN 123,123
NV 148,28,228
AN 124,126
AN 125,127
NV 152,24,232
SH 126
AN 126,126
NV 152,28,236
AN 127,127
P 32
NV 156,24,240
H 2
P 33
NV 156,28,244
R 82
NV 180,0,240
Q 34
NV 180,4,244
Q 36
NV 176,0,232
Q 38
NV 176,4,236
Q 40
NV 172,0,224
Q 42
NV 172,4,228
Q 44
NV 168,0,216
Q 46
NV 168,4,220
NV 164,0,208
NV 164,4,212
NV 160,0,200
NV 160,4,204
WL 2
NV 184,8,200
NV 184,12,204
NV 188,8,208
NV 188,12,212
NV 192,8,216
NV 192,12,220
NV 196,8,224
NV 196,12,228
WV 18
NV 200,8,232
WL 0
NV 200,12,236
s_barrier
R 0
NV 204,8,240
mw 72
NV 204,12,244
R 2
NV 208,16,200
ad 84,528
W 0
NV 208,20,204
NV 232,28,204
NV 232,24,200
NV 236,24,208
W 1
NV 236,28,212
NV 212,20,212
NV 212,16,208
NV 216,16,216
W 2
NV 216,20,220
ae 44,70,0
NV 240,28,220
s_bitcmp1_b32 s50, 0
s_cselect_b32 s51, 24576, 0
v_add_u32 v47, s51, v3
NV 240,24,216
XL 14,0
NV 244,24,224
mw 75
NV 244,28,228
ad 84,534
W 0
NV 220,20,228
XL 16,768
NV 220,16,224
NV 224,16,232
NV 224,20,236
W 1
NV 248,28,236
NV 248,24,232
NV 252,24,240
XL 18,1536
NV 252,28,244
R 6
NV 228,20,244
W 2
NV 228,16,240
WL 2
R 4
M 0
M 1
M 13
XL 20,2304
M 12
M 14
M 15
WV 20
v_or_b32 v6, v6, v7
s_mov_b64 exec, s[76:77]
v_cmp_ne_u32 %[flg], 0, v6
s_mov_b64 exec, -1
M 3
M 2
M 4
M 5
M 17
M 16
M 18
M 19
M 7
M 6
M 8
M 9
M 21
M 20
M 22
M 23
R 10
M 11
M 10
WL 2
R 8
M 34
M 35
M 32
M 33
M 30
M 31
M 28
M 29
M 26
M 27
M 24
M 25
M 36
M 37
M 38
M 39
M 40
M 41
M 42
M 43
WV 15
M 44
WL 0
M 45
s_barrier
R 24
M 46
mw 0
)"
R"(M 47
R 26
M 58
ad 84,576
W 0
M 59
TY 106,9736
TY 110,23560
M 56
M 57
W 1
M 54
M 55
M 52
W 2
M 53
M 50
M 51
mw 3
M 48
ad 84,582
W 0
M 49
R 28
M 60
WL 3
AN 104,106
AN 105,107
M 61
W 1
SH 106
M 62
AN 106,106
AN 107,107
M 63
WL 2
AN 108,110
AN 109,111
M 64
W 2
SH 110
M 65
AN 110,110
AN 111,111
M 66
TY 114,11272
TY 118,25096
M 67
bc 49,7,66,60
M 68
mh 9
M 69
ad 66,1344
H 0
M 70
M 71
R 30
M 82
DG 1024
M 83
M 80
WL 3
M 81
bc 48,7,64,56
AN 112,114
M 78
mh 12
AN 113,115
SH 114
M 79
ad 64,1344
H 0
M 76
AN 114,114
AN 115,115
M 77
WL 2
AN 116,118
AN 117,119
M 74
H 1
SH 118
M 75
AN 118,118
AN 119,119
M 72
TY 122,12808
TY 126,26632
M 73
R 32
M 84
H 2
M 85
M 86
M 87
s_load_dwordx4 s[88:91], %[pp], 0xc0
s_load_dwordx2 s[86:87], %[pp], 0xd0
M 88
M 89
M 90
M 91
WL 3
AN 120,122
AN 121,123
M 92
SH 122
AN 122,122
M 93
WL 2
AN 123,123
AN 124,126
M 94
AN 125,127
SH 126
M 95
R 34
M 106
AN 126,126
AN 127,127
M 107
M 104
M 105
M 102
M 103
M 100
M 101
M 98
M 99
M 96
M 97
R 48
M 108
M 109
M 110
M 111
M 112
M 113
M 114
M 115
M 116
s_waitcnt lgkmcnt(0)
M 117
M 118
M 119
R 50
M 120
v_mov_b32 v54, v52
M 121
M 133
M 132
TX 58,9216
TX 62,23040
M 134
v_permlane32_swap_b32 v52, v54
M 135
M 123
M 122
TY 66,9728
TY 70,23552
M 124
v_add_f32 v52, v52, v54
v_mov_b32 v54, v52
M 125
M 137
ae 44,70,0
M 136
TX 74,10752
TX 78,24576
M 138
s_bfe_u32 s51, s50, 0x10001
WL 4
Q 0
M 139
Q 2
M 127
Q 4
M 126
TY 82,11264
TY 86,25088
M 128
Q 6
M 129
WL 4
Q 8
M 141
Q 10
M 140
TX 90,12288
TX 94,26112
M 142
Q 12
M 143
R 54
M 131
Q 14
M 130
TY 98,12800
TY 102,26624
M 0
R 52
M 1
WL 10
Q 16
M 13
Q 18
M 12
Q 20
M 14
Q 22
M 15
WL 8
Q 24
M 3
Q 26
M 2
Q 28
M 4
Q 30
M 5
WL 6
Q 32
M 17
Q 34
M 16
Q 36
M 18
Q 38
M 19
WL 2
Q 40
M 7
Q 42
M 6
Q 44
M 8
Q 46
M 9
v_permlane16_swap_b32 v52, v54
v_mad_i32_i24 v47, v49, s51, v48
M 21
XL 22,0
M 20
XL 24,768
M 22
XL 26,1536
v_add_f32 v52, v52, v54
M 23
R 58
M 11
XL 28,2304
M 10
WL 2
R 56
M 34
v_permlane16_swap_b32 v52, v53
v_mov_b32 v54, s88
M 35
v_mov_b32 v53, s89
M 32
M 33
M 30
v_permlane16_swap_b32 v54, v53
v_bfe_u32 v53, v0, 2, 6
M 31
M 28
M 29
v_add_u32 v54, v53, v54
M 26
M 27
WV 9
M 24
WL 0
FP
M 25
M 36
M 37
M 38
M 39
M 40
UL 6,14
UH 7,14
M 41
AR 8,36
AR 9,60
M 42
AD 8,6
AD 9,7
M 43
AW 36,8
AW 60,9
M 44
UL 6,15
UH 7,15
M 45
FR
AR 8,84
M 46
AR 9,108
AD 8,6
M 47
AD 9,7
AW 84,8
M 58
AW 108,9
UL 6,16
M 59
UH 7,16
AR 8,37
M 56
AR 9,61
AD 8,6
M 57
AD 9,7
AW 37,8
M 54
AW 61,9
UL 6,17
M 55
WL 0
FQ 4
UH 7,17
M 52
AR 8,85
AR 9,109
M 53
AD 8,6
AD 9,7
M 50
FZ 4
R 72
M 51
mw 24
R 74
M 48
TY 106,520
TY 110,14344
M 49
R 76
M 60
ad 84,624
W 0
M 61
AW 85,8
AW 109,9
M 62
UL 6,18
UH 7,18
M 63
W 1
AR 8,38
M 64
AR 9,62
AD 8,6
M 65
AD 9,7
AW 38,8
M 66
W 2
AW 62,9
M 67
WL 3
AN 104,106
AN 105,107
M 68
WL 2
SH 106
AN 106,106
M 69
mw 27
AN 107,107
AN 108,110
M 70
AN 109,111
SH 110
M 71
R 78
M 82
AN 110,110
AN 111,111
M 83
TY 114,2056
TY 118,15880
M 80
ad 84,630
W 0
M 81
UL 6,19
UH 7,19
M 78
AR 8,86
AR 9,110
M 79
W 1
AD 8,6
M 76
AD 9,7
AW 86,8
M 77
AW 110,9
UL 6,20
)"
R"(M 74
W 2
UH 7,20
M 75
AR 8,39
AR 9,63
M 72
WL 1
AN 112,114
AN 113,115
M 73
R 80
M 84
mw 48
SH 114
AN 114,114
M 85
WL 2
AN 115,115
AN 116,118
M 86
AN 117,119
SH 118
M 87
AN 118,118
AN 119,119
M 88
TY 122,3592
TY 126,17416
M 89
ad 84,672
W 0
M 90
AD 8,6
AD 9,7
M 91
AW 39,8
AW 63,9
M 92
W 1
UL 6,21
M 93
UH 7,21
AR 8,87
M 94
AR 9,111
AD 8,6
M 95
R 82
M 106
W 2
AD 9,7
M 107
WL 3
AN 120,122
AN 121,123
M 104
WL 2
SH 122
AN 122,122
M 105
mw 51
AN 123,123
AN 124,126
M 102
AN 125,127
SH 126
M 103
AN 126,126
AN 127,127
M 100
ad 84,678
W 0
M 101
AW 87,8
AW 111,9
M 98
M 99
W 1
M 96
M 97
R 0
M 108
W 2
M 109
M 110
M 111
bc 49,8,66,60
M 112
mh 18
M 113
ad 66,1536
H 0
M 114
M 115
M 116
DG 1024
M 117
M 118
M 119
WL 2
R 2
M 120
bc 48,8,64,56
M 121
mh 21
s_bitcmp1_b32 s87, 0
s_cselect_b32 exec_lo, 0xffffff, 0
s_mov_b32 exec_hi, 0
global_atomic_add_f32 v54, v52, s[90:91] sc1
s_mov_b64 exec, -1
M 133
ad 64,1536
H 0
M 132
TX 58,0
TX 62,13824
M 134
M 135
H 1
M 123
M 122
TY 66,512
TY 70,14336
M 124
H 2
M 125
M 137
M 136
TX 74,1536
TX 78,15360
M 138
WL 4
Q 0
M 139
Q 2
M 127
Q 4
M 126
TY 82,2048
TY 86,15872
M 128
Q 6
M 129
WL 4
Q 8
M 141
Q 10
M 140
TX 90,3072
TX 94,16896
M 142
Q 12
M 143
R 6
M 131
Q 14
M 130
TY 98,3584
TY 102,17408
M 0
R 4
M 1
WL 10
Q 16
M 13
Q 18
M 12
Q 20
M 14
Q 22
M 15
WL 8
Q 24
M 3
Q 26
M 2
Q 28
M 4
Q 30
M 5
WL 6
Q 32
M 17
Q 34
M 16
Q 36
M 18
Q 38
M 19
WL 2
Q 40
M 7
Q 42
M 6
Q 44
M 8
Q 46
M 9
ae 44,68,0
M 21
s_bitcmp1_b32 s50, 2
s_cselect_b32 s51, 24576, 0
v_add_u32 v47, s51, v3
M 20
XL 30,0
M 22
XL 32,768
M 23
R 10
M 11
XL 34,1536
M 10
WL 2
R 8
M 34
XL 36,2304
M 35
M 32
M 33
M 30
M 31
M 28
M 29
M 26
M 27
WV 10
M 24
WL 0
FP
M 25
M 36
M 37
s_bitcmp1_b32 s87, 1
s_cselect_b32 exec_lo, 0xffffff, 0
s_mov_b32 exec_hi, 0
global_atomic_add_f32 v54, v52, s[90:91] sc1
s_mov_b64 exec, -1
M 38
M 39
M 40
M 41
M 42
UL 6,22
UH 7,22
M 43
AR 8,40
AR 9,64
M 44
AD 8,6
AD 9,7
M 45
FR
AW 40,8
M 46
AW 64,9
UL 6,23
M 47
UH 7,23
AR 8,88
M 58
AR 9,112
AD 8,6
M 59
AD 9,7
AW 88,8
M 56
AW 112,9
UL 6,24
M 57
UH 7,24
AR 8,41
M 54
AR 9,65
AD 8,6
M 55
WL 0
FQ 8
AD 9,7
M 52
AW 41,8
AW 65,9
M 53
UL 6,25
UH 7,25
M 50
FZ 8
R 24
M 51
mw 72
R 26
M 48
TY 106,5128
TY 110,18952
M 49
R 28
M 60
ad 84,720
W 0
M 61
AR 8,89
AR 9,113
M 62
AD 8,6
AD 9,7
M 63
W 1
AW 89,8
M 64
AW 113,9
UL 6,26
M 65
UH 7,26
AR 8,42
M 66
W 2
AR 9,66
M 67
WL 3
AN 104,106
AN 105,107
M 68
WL 2
SH 106
AN 106,106
M 69
mw 75
AN 107,107
AN 108,110
M 70
AN 109,111
SH 110
M 71
R 30
M 82
AN 110,110
AN 111,111
M 83
TY 114,6664
TY 118,20488
M 80
ad 84,726
W 0
M 81
AD 8,6
AD 9,7
M 78
AW 42,8
AW 66,9
M 79
W 1
UL 6,27
M 76
UH 7,27
AR 8,90
M 77
AR 9,114
AD 8,6
M 74
W 2
AD 9,7
M 75
AW 90,8
AW 114,9
M 72
WL 1
AN 112,114
AN 113,115
M 73
R 32
M 84
mw 0
SH 114
AN 114,114
M 85
WL 2
AN 115,115
AN 116,118
M 86
AN 117,119
SH 118
M 87
AN 118,118
AN 119,119
M 88
TY 122,8200
TY 126,22024
M 89
ad 84,768
W 0
M 90
UL 6,28
UH 7,28
M 91
AR 8,43
AR 9,67
M 92
W 1
AD 8,6
M 93
)"
R"(AD 9,7
AW 43,8
M 94
AW 67,9
UL 6,29
M 95
R 34
M 106
W 2
UH 7,29
M 107
WL 3
AN 120,122
AN 121,123
M 104
WL 2
SH 122
AN 122,122
M 105
mw 3
AN 123,123
AN 124,126
M 102
AN 125,127
SH 126
M 103
AN 126,126
AN 127,127
M 100
ad 84,774
W 0
M 101
AR 8,91
AR 9,115
M 98
AD 8,6
AD 9,7
M 99
W 1
AW 91,8
M 96
AW 115,9
M 97
R 48
M 108
W 2
M 109
M 110
M 111
bc 49,9,66,60
M 112
mh 0
M 113
ad 66,1728
H 0
M 114
M 115
M 116
DG 1024
M 117
M 118
M 119
WL 2
R 50
M 120
bc 48,9,64,56
M 121
mh 3
s_bitcmp1_b32 s87, 2
s_cselect_b32 exec_lo, 0xffffff, 0
s_mov_b32 exec_hi, 0
global_atomic_add_f32 v54, v52, s[90:91] sc1
s_mov_b64 exec, -1
M 133
ad 64,1728
H 0
M 132
TX 58,4608
TX 62,18432
M 134
M 135
H 1
M 123
M 122
TY 66,5120
TY 70,18944
M 124
H 2
M 125
M 137
M 136
TX 74,6144
TX 78,19968
M 138
WL 4
Q 0
M 139
Q 2
M 127
Q 4
M 126
TY 82,6656
TY 86,20480
M 128
Q 6
M 129
WL 4
Q 8
M 141
Q 10
M 140
TX 90,7680
TX 94,21504
M 142
Q 12
M 143
R 54
M 131
Q 14
M 130
TY 98,8192
TY 102,22016
M 0
R 52
M 1
WL 10
Q 16
M 13
Q 18
M 12
Q 20
M 14
Q 22
M 15
WL 8
Q 24
M 3
Q 26
M 2
Q 28
M 4
Q 30
M 5
WL 6
Q 32
M 17
Q 34
M 16
Q 36
M 18
Q 38
M 19
WL 2
Q 40
M 7
Q 42
M 6
Q 44
M 8
Q 46
M 9
ae 44,68,0
M 21
s_bfe_u32 s51, s50, 0x10003
v_mad_i32_i24 v47, v49, s51, v48
XL 38,0
M 20
XL 40,768
M 22
XL 42,1536
M 23
R 58
M 11
XL 44,2304
M 10
WL 2
R 56
M 34
M 35
M 32
M 33
M 30
M 31
M 28
M 29
M 26
M 27
WV 10
M 24
WL 0
FP
M 25
M 36
M 37
s_bitcmp1_b32 s87, 3
s_cselect_b32 exec_lo, 0xffffff, 0
s_mov_b32 exec_hi, 0
global_atomic_add_f32 v54, v52, s[90:91] sc1
s_mov_b64 exec, -1
M 38
M 39
M 40
M 41
M 42
M 43
M 44
UL 6,30
UH 7,30
M 45
FR
AR 8,44
M 46
AR 9,68
AD 8,6
M 47
AD 9,7
AW 44,8
M 58
AW 68,9
UL 6,31
M 59
UH 7,31
AR 8,92
M 56
AR 9,116
AD 8,6
M 57
AD 9,7
AW 92,8
M 54
AW 116,9
UL 6,32
M 55
WL 0
FQ 12
UH 7,32
M 52
AR 8,45
AR 9,69
M 53
AD 8,6
AD 9,7
M 50
FZ 12
R 72
M 51
mw 24
R 74
M 48
TY 106,9736
TY 110,23560
M 49
R 76
M 60
ad 84,816
W 0
M 61
AW 45,8
AW 69,9
M 62
UL 6,33
UH 7,33
M 63
W 1
AR 8,93
M 64
AR 9,117
AD 8,6
M 65
AD 9,7
AW 93,8
M 66
W 2
AW 117,9
M 67
WL 3
AN 104,106
AN 105,107
M 68
WL 2
SH 106
AN 106,106
M 69
mw 27
AN 107,107
AN 108,110
M 70
AN 109,111
SH 110
M 71
R 78
M 82
AN 110,110
AN 111,111
M 83
TY 114,11272
TY 118,25096
M 80
ad 84,822
W 0
M 81
UL 6,34
UH 7,34
M 78
AR 8,46
AR 9,70
M 79
W 1
AD 8,6
M 76
AD 9,7
AW 46,8
M 77
AW 70,9
UL 6,35
M 74
W 2
UH 7,35
M 75
AR 8,94
AR 9,118
M 72
WL 1
AN 112,114
AN 113,115
M 73
R 80
M 84
mw 48
SH 114
AN 114,114
M 85
WL 2
AN 115,115
AN 116,118
M 86
AN 117,119
SH 118
M 87
AN 118,118
AN 119,119
M 88
TY 122,12808
TY 126,26632
M 89
ad 84,864
W 0
M 90
AD 8,6
AD 9,7
M 91
AW 94,8
AW 118,9
M 92
W 1
UL 6,36
M 93
UH 7,36
AR 8,47
M 94
AR 9,71
AD 8,6
M 95
R 82
M 106
W 2
AD 9,7
M 107
WL 3
AN 120,122
AN 121,123
M 104
WL 2
SH 122
AN 122,122
M 105
mw 51
AN 123,123
AN 124,126
M 102
AN 125,127
SH 126
M 103
AN 126,126
AN 127,127
M 100
ad 84,870
W 0
M 101
AW 47,8
AW 71,9
M 98
UL 6,37
UH 7,37
M 99
W 1
AR 8,95
M 96
AR 9,119
AD 8,6
)"
R"(M 97
R 0
M 108
W 2
AD 9,7
M 109
AW 95,8
AW 119,9
M 110
M 111
bc 49,10,66,60
M 112
mh 9
M 113
ad 66,1920
H 0
M 114
M 115
M 116
DG 1024
M 117
M 118
M 119
WL 2
R 2
M 120
bc 48,10,64,56
M 121
mh 12
s_bitcmp1_b32 s87, 4
s_cselect_b32 exec_lo, 0xffffff, 0
s_mov_b32 exec_hi, 0
global_atomic_add_f32 v54, v52, s[90:91] sc1
s_mov_b64 exec, -1
M 133
ad 64,1920
H 0
M 132
TX 58,9216
TX 62,23040
M 134
M 135
H 1
M 123
M 122
TY 66,9728
TY 70,23552
M 124
H 2
M 125
M 137
M 136
TX 74,10752
TX 78,24576
M 138
WL 4
Q 0
M 139
Q 2
M 127
Q 4
M 126
TY 82,11264
TY 86,25088
M 128
Q 6
M 129
WL 4
Q 8
M 141
Q 10
M 140
TX 90,12288
TX 94,26112
M 142
Q 12
M 143
R 6
M 131
Q 14
M 130
TY 98,12800
TY 102,26624
M 0
R 4
M 1
WL 10
Q 16
M 13
Q 18
M 12
Q 20
M 14
Q 22
M 15
WL 8
Q 24
M 3
Q 26
M 2
Q 28
M 4
Q 30
M 5
WL 6
Q 32
M 17
Q 34
M 16
Q 36
M 18
Q 38
M 19
WL 2
Q 40
M 7
Q 42
M 6
Q 44
M 8
Q 46
M 9
ae 44,68,96
M 21
s_bitcmp1_b32 s50, 4
s_cselect_b32 s51, 24576, 0
v_add_u32 v47, s51, v3
M 20
XL 14,0
M 22
XL 16,768
M 23
R 10
M 11
XL 18,1536
M 10
WL 2
R 8
M 34
XL 20,2304
M 35
M 32
M 33
M 30
M 31
M 28
M 29
M 26
M 27
WV 10
M 24
WL 0
FP
M 25
M 36
M 37
s_bitcmp1_b32 s87, 5
s_cselect_b32 exec_lo, 0xffffff, 0
s_mov_b32 exec_hi, 0
global_atomic_add_f32 v54, v52, s[90:91] sc1
s_mov_b64 exec, -1
M 38
M 39
M 40
M 41
M 42
M 43
M 44
M 45
FR
M 46
UL 6,38
UH 7,38
M 47
AR 8,48
AR 9,72
M 58
AD 8,6
AD 9,7
M 59
AW 48,8
AW 72,9
M 56
UL 6,39
UH 7,39
M 57
AR 8,96
AR 9,120
M 54
AD 8,6
AD 9,7
M 55
WL 0
FQ 16
AW 96,8
M 52
AW 120,9
UL 6,40
M 53
UH 7,40
AR 8,49
M 50
FZ 16
R 24
M 51
mw 72
R 26
M 48
TY 106,520
TY 110,14344
M 49
R 28
M 60
ad 84,912
W 0
M 61
AR 9,73
AD 8,6
M 62
AD 9,7
AW 49,8
M 63
W 1
AW 73,9
M 64
UL 6,41
UH 7,41
M 65
AR 8,97
AR 9,121
M 66
W 2
AD 8,6
M 67
WL 3
AN 104,106
AN 105,107
M 68
WL 2
SH 106
AN 106,106
M 69
mw 75
AN 107,107
AN 108,110
M 70
AN 109,111
SH 110
M 71
R 30
M 82
AN 110,110
AN 111,111
M 83
TY 114,2056
TY 118,15880
M 80
ad 84,918
W 0
M 81
AD 9,7
AW 97,8
M 78
AW 121,9
UL 6,42
M 79
W 1
UH 7,42
M 76
AR 8,50
AR 9,74
M 77
AD 8,6
AD 9,7
M 74
W 2
AW 50,8
M 75
AW 74,9
UL 6,43
M 72
WL 1
AN 112,114
AN 113,115
M 73
R 32
M 84
mw 0
SH 114
AN 114,114
M 85
WL 2
AN 115,115
AN 116,118
M 86
AN 117,119
SH 118
M 87
AN 118,118
AN 119,119
M 88
TY 122,3592
TY 126,17416
M 89
ad 84,960
W 0
M 90
UH 7,43
AR 8,98
M 91
AR 9,122
AD 8,6
M 92
W 1
AD 9,7
M 93
AW 98,8
AW 122,9
M 94
UL 6,44
UH 7,44
M 95
R 34
M 106
W 2
AR 8,51
M 107
WL 3
AN 120,122
AN 121,123
M 104
WL 2
SH 122
AN 122,122
M 105
mw 3
AN 123,123
AN 124,126
M 102
AN 125,127
SH 126
M 103
AN 126,126
AN 127,127
M 100
ad 84,966
W 0
M 101
AR 9,75
AD 8,6
M 98
AD 9,7
AW 51,8
M 99
W 1
AW 75,9
M 96
UL 6,45
UH 7,45
M 97
R 48
M 108
W 2
AR 8,99
M 109
AR 9,123
AD 8,6
M 110
AD 9,7
AW 99,8
M 111
bc 49,11,66,60
AW 123,9
M 112
mh 18
M 113
ad 66,2112
H 0
M 114
M 115
M 116
DG 1024
M 117
M 118
M 119
WL 2
R 50
M 120
bc 48,11,64,56
M 121
mh 21
s_bitcmp1_b32 s87, 6
s_cselect_b32 exec_lo, 0xffffff, 0
s_mov_b32 exec_hi, 0
global_atomic_add_f32 v54, v52, s[90:91] sc1
s_mov_b64 exec, -1
M 133
ad 64,2112
H 0
M 132
)"
R"(TX 58,0
TX 62,13824
M 134
M 135
H 1
M 123
M 122
TY 66,512
TY 70,14336
M 124
H 2
M 125
M 137
M 136
TX 74,1536
TX 78,15360
PA 12288
DF
M 138
WL 4
Q 0
M 139
Q 2
M 127
Q 4
M 126
TY 82,2048
TY 86,15872
M 128
Q 6
M 129
WL 4
Q 8
M 141
Q 10
M 140
TX 90,3072
TX 94,16896
M 142
Q 12
M 143
R 54
M 131
Q 14
M 130
TY 98,3584
TY 102,17408
M 0
R 52
M 1
WL 10
Q 16
M 13
Q 18
M 12
Q 20
M 14
Q 22
M 15
WL 8
Q 24
M 3
Q 26
M 2
Q 28
M 4
Q 30
M 5
WL 6
Q 32
M 17
Q 34
M 16
Q 36
M 18
Q 38
M 19
WL 2
Q 40
M 7
Q 42
M 6
Q 44
M 8
Q 46
M 9
ae 44,68,96
M 21
s_bfe_u32 s51, s50, 0x10005
v_mad_i32_i24 v47, v49, s51, v48
XL 22,0
M 20
XL 24,768
M 22
XL 26,1536
M 23
R 58
M 11
XL 28,2304
M 10
WL 2
R 56
M 34
M 35
M 32
M 33
M 30
M 31
M 28
M 29
M 26
M 27
WV 11
M 24
WL 0
FP
M 25
M 36
M 37
s_bitcmp1_b32 s87, 7
s_cselect_b32 exec_lo, 0xffffff, 0
s_mov_b32 exec_hi, 0
global_atomic_add_f32 v54, v52, s[90:91] sc1
s_mov_b64 exec, -1
M 38
M 39
M 40
M 41
M 42
M 43
M 44
M 45
FR
M 46
M 47
M 58
UL 6,14
UH 7,14
M 59
AR 8,52
AR 9,76
M 56
AD 8,6
AD 9,7
M 57
AW 52,8
AW 76,9
M 54
UL 6,15
UH 7,15
M 55
WL 0
FQ 20
AR 8,100
M 52
AR 9,124
AD 8,6
M 53
AD 9,7
AW 100,8
M 50
FZ 20
R 72
M 51
mw 24
R 74
M 48
TY 106,5128
TY 110,18952
M 49
R 76
M 60
ad 84,1008
W 0
M 61
AW 124,9
UL 6,16
M 62
UH 7,16
AR 8,53
M 63
W 1
AR 9,77
M 64
AD 8,6
AD 9,7
M 65
AW 53,8
AW 77,9
M 66
W 2
UL 6,17
M 67
WL 3
AN 104,106
AN 105,107
M 68
WL 2
SH 106
AN 106,106
M 69
mw 27
AN 107,107
AN 108,110
M 70
AN 109,111
SH 110
M 71
R 78
M 82
AN 110,110
AN 111,111
M 83
TY 114,6664
TY 118,20488
M 80
ad 84,1014
W 0
M 81
UH 7,17
AR 8,101
M 78
AR 9,125
AD 8,6
M 79
W 1
AD 9,7
M 76
AW 101,8
AW 125,9
M 77
UL 6,18
UH 7,18
M 74
W 2
AR 8,54
M 75
AR 9,78
AD 8,6
M 72
WL 1
AN 112,114
AN 113,115
M 73
R 80
M 84
mw 48
SH 114
AN 114,114
M 85
WL 2
AN 115,115
AN 116,118
M 86
AN 117,119
SH 118
M 87
AN 118,118
AN 119,119
M 88
TY 122,8200
TY 126,22024
M 89
ad 84,1056
W 0
M 90
AD 9,7
AW 54,8
M 91
AW 78,9
UL 6,19
M 92
W 1
UH 7,19
M 93
AR 8,102
AR 9,126
M 94
AD 8,6
AD 9,7
M 95
R 82
M 106
W 2
AW 102,8
M 107
WL 3
AN 120,122
AN 121,123
M 104
WL 2
SH 122
AN 122,122
M 105
mw 51
AN 123,123
AN 124,126
M 102
AN 125,127
SH 126
M 103
AN 126,126
AN 127,127
M 100
ad 84,1062
W 0
M 101
AW 126,9
UL 6,20
M 98
UH 7,20
AR 8,55
M 99
W 1
AR 9,79
M 96
AD 8,6
AD 9,7
M 97
R 0
M 108
W 2
AW 55,8
M 109
AW 79,9
UL 6,21
M 110
UH 7,21
AR 8,103
M 111
bc 49,12,66,60
AR 9,127
M 112
mh 0
AD 8,6
AD 9,7
M 113
ad 66,2304
H 0
M 114
AW 103,8
AW 127,9
M 115
M 116
DG 1024
M 117
M 118
M 119
WL 2
R 2
M 120
bc 48,12,64,56
M 121
mh 3
M 133
ad 64,2304
H 0
M 132
TX 58,4608
TX 62,18432
M 134
M 135
H 1
M 123
M 122
TY 66,5120
TY 70,18944
M 124
H 2
M 125
M 137
M 136
TX 74,6144
TX 78,19968
M 138
WL 4
Q 0
M 139
Q 2
M 127
Q 4
M 126
TY 82,6656
TY 86,20480
M 128
Q 6
M 129
WL 4
Q 8
M 141
Q 10
M 140
TX 90,7680
TX 94,21504
M 142
Q 12
M 143
R 6
M 131
Q 14
)"
R"(M 130
TY 98,8192
TY 102,22016
M 0
R 4
M 1
WL 10
Q 16
M 13
Q 18
M 12
Q 20
M 14
Q 22
M 15
WL 8
Q 24
M 3
Q 26
M 2
Q 28
M 4
Q 30
M 5
WL 6
Q 32
M 17
Q 34
M 16
Q 36
M 18
Q 38
M 19
WL 2
Q 40
M 7
Q 42
M 6
Q 44
M 8
Q 46
M 9
M 21
M 20
M 22
M 23
R 10
M 11
M 10
WL 2
R 8
M 34
M 35
M 32
M 33
M 30
M 31
M 28
M 29
M 26
ae 44,70,0
M 27
WV 5
M 24
WL 0
FP
M 25
M 36
s_bitcmp1_b32 s50, 0
s_cselect_b32 s51, 24576, 0
v_add_u32 v47, s51, v3
M 37
XL 30,128
M 38
XL 32,896
M 39
M 40
M 41
M 42
M 43
M 44
XL 34,1664
M 45
FR
M 46
M 47
M 58
M 59
M 56
XL 36,2432
UL 6,22
M 57
UH 7,22
AR 8,56
M 54
AR 9,80
AD 8,6
M 55
WL 0
FQ 24
AD 9,7
M 52
AW 56,8
AW 80,9
M 53
UL 6,23
UH 7,23
M 50
FZ 24
R 24
M 51
mw 72
R 26
M 48
TY 106,9736
TY 110,23560
M 49
R 28
M 60
ad 84,1104
W 0
M 61
AR 8,104
AR 9,128
M 62
AD 8,6
AD 9,7
M 63
W 1
AW 104,8
M 64
AW 128,9
UL 6,24
M 65
UH 7,24
AR 8,57
M 66
W 2
AR 9,81
M 67
WL 3
AN 104,106
AN 105,107
M 68
WL 2
SH 106
AN 106,106
M 69
mw 75
AN 107,107
AN 108,110
M 70
AN 109,111
SH 110
M 71
R 30
M 82
AN 110,110
AN 111,111
M 83
TY 114,11272
TY 118,25096
M 80
ad 84,1110
W 0
M 81
AD 8,6
AD 9,7
M 78
AW 57,8
AW 81,9
M 79
W 1
UL 6,25
M 76
UH 7,25
AR 8,105
M 77
AR 9,129
AD 8,6
M 74
W 2
AD 9,7
M 75
AW 105,8
AW 129,9
M 72
WL 1
AN 112,114
AN 113,115
M 73
R 32
M 84
mw 0
SH 114
AN 114,114
M 85
WL 2
AN 115,115
AN 116,118
M 86
AN 117,119
SH 118
M 87
AN 118,118
AN 119,119
M 88
TY 122,12808
TY 126,26632
M 89
ad 84,1152
W 0
M 90
UL 6,26
UH 7,26
M 91
AR 8,58
AR 9,82
M 92
W 1
AD 8,6
M 93
AD 9,7
AW 58,8
M 94
AW 82,9
UL 6,27
M 95
R 34
M 106
W 2
UH 7,27
M 107
WL 3
AN 120,122
AN 121,123
M 104
WL 2
SH 122
AN 122,122
M 105
mw 3
AN 123,123
AN 124,126
M 102
AN 125,127
SH 126
M 103
AN 126,126
AN 127,127
M 100
ad 84,1158
W 0
M 101
AR 8,106
AR 9,130
M 98
AD 8,6
AD 9,7
M 99
W 1
AW 106,8
M 96
AW 130,9
UL 6,28
M 97
R 48
M 108
W 2
UH 7,28
M 109
AR 8,59
AR 9,83
M 110
AD 8,6
AD 9,7
M 111
bc 49,13,66,60
AW 59,8
M 112
mh 9
AW 83,9
UL 6,29
M 113
ad 66,2496
H 0
M 114
UH 7,29
AR 8,107
M 115
AR 9,131
AD 8,6
M 116
DG 1024
M 117
AD 9,7
AW 107,8
M 118
AW 131,9
M 119
WL 1
R 50
M 120
bc 48,13,64,56
M 121
mh 12
M 133
ad 64,2496
H 0
M 132
TX 58,9216
TX 62,23040
M 134
M 135
H 1
M 123
M 122
TY 66,9728
TY 70,23552
M 124
H 2
M 125
M 137
M 136
TX 74,10752
TX 78,24576
PA 12288
DF
M 138
WL 4
Q 0
M 139
Q 2
M 127
Q 4
M 126
TY 82,11264
TY 86,25088
M 128
Q 6
M 129
WL 4
Q 8
M 141
Q 10
M 140
TX 90,12288
TX 94,26112
M 142
Q 12
M 143
R 54
M 131
Q 14
M 130
TY 98,12800
TY 102,26624
M 0
R 52
M 1
WL 10
Q 16
M 13
Q 18
M 12
Q 20
M 14
Q 22
M 15
WL 8
Q 24
M 3
Q 26
M 2
Q 28
M 4
Q 30
M 5
WL 6
Q 32
M 17
Q 34
M 16
Q 36
M 18
Q 38
M 19
WL 2
Q 40
M 7
Q 42
M 6
Q 44
M 8
Q 46
M 9
M 21
M 20
M 22
M 23
)"
R"(R 58
M 11
M 10
WL 2
R 56
M 34
M 35
M 32
M 33
M 30
M 31
M 28
M 29
M 26
M 27
WV 6
M 24
WL 0
FP
M 25
M 36
ae 44,70,0
M 37
s_bfe_u32 s51, s50, 0x10001
v_mad_i32_i24 v47, v49, s51, v48
XL 38,128
M 38
M 39
M 40
XL 40,896
M 41
M 42
M 43
M 44
M 45
FR
M 46
XL 42,1664
M 47
M 58
M 59
M 56
M 57
M 54
XL 44,2432
M 55
WL 0
FQ 28
M 52
M 53
M 50
FZ 28
R 72
M 51
R 74
M 48
TY 106,520
TY 110,14344
M 49
R 76
M 60
mw 24
v_mov_b32 v54, 2
s_mov_b64 exec, 1
FS
s_mov_b64 exec, -1
M 61
ad 84,1200
W 0
M 62
M 63
M 64
W 1
M 65
M 66
M 67
WL 3
AN 104,106
AN 105,107
M 68
SH 106
AN 106,106
M 69
WL 2
AN 107,107
AN 108,110
M 70
AN 109,111
SH 110
M 71
R 78
M 82
AN 110,110
AN 111,111
M 83
TY 114,2056
TY 118,15880
M 80
W 2
M 81
M 78
M 79
mw 27
M 76
ad 84,1206
W 0
M 77
M 74
M 75
W 1
M 72
WL 1
AN 112,114
AN 113,115
M 73
R 80
M 84
SH 114
AN 114,114
M 85
WL 2
AN 115,115
AN 116,118
M 86
AN 117,119
SH 118
M 87
AN 118,118
AN 119,119
M 88
TY 122,3592
TY 126,17416
M 89
W 2
UL 6,30
M 90
UH 7,30
AR 8,132
M 91
AR 9,156
AD 8,6
M 92
mw 48
AD 9,7
AW 132,8
M 93
ad 84,1248
W 0
M 94
AW 156,9
UL 6,31
M 95
R 82
M 106
W 1
UH 7,31
M 107
WL 3
AN 120,122
AN 121,123
M 104
SH 122
AN 122,122
M 105
WL 2
AN 123,123
AN 124,126
M 102
AN 125,127
SH 126
M 103
AN 126,126
AN 127,127
M 100
W 2
AR 8,180
M 101
AD 8,6
AD 136,7
M 98
AW 180,8
UL 6,32
M 99
mw 51
UH 7,32
AR 8,133
M 96
ad 84,1254
W 0
M 97
R 0
M 108
AR 9,157
AD 8,6
M 109
W 1
AD 9,7
M 110
AW 133,8
AW 157,9
M 111
UL 6,33
UH 7,33
M 112
W 2
AR 8,181
M 113
AD 8,6
AD 137,7
M 114
AW 181,8
UL 6,34
M 115
bc 49,14,66,60
UH 7,34
M 116
mh 18
AR 8,134
AR 9,158
M 117
ad 66,2688
H 0
M 118
AD 8,6
AD 9,7
M 119
WL 0
R 2
M 120
DG 1024
M 121
AW 134,8
AW 158,9
M 133
UL 6,35
UH 7,35
M 132
TX 58,0
TX 62,13824
M 134
bc 48,14,64,56
AR 8,182
M 135
mh 21
AD 8,6
AD 138,7
M 123
ad 64,2688
H 0
M 122
TY 66,512
TY 70,14336
M 124
AW 182,8
UL 6,36
M 125
H 1
UH 7,36
M 137
AR 8,135
AR 9,159
M 136
TX 74,1536
TX 78,15360
M 138
WL 4
Q 0
M 139
Q 2
M 127
Q 4
M 126
TY 82,2048
TY 86,15872
M 128
Q 6
M 129
WL 4
Q 8
M 141
Q 10
M 140
TX 90,3072
TX 94,16896
M 142
Q 12
M 143
R 6
M 131
Q 14
M 130
TY 98,3584
TY 102,17408
M 0
R 4
M 1
WL 10
Q 16
M 13
Q 18
M 12
Q 20
M 14
Q 22
M 15
WL 8
Q 24
M 3
Q 26
M 2
Q 28
M 4
Q 30
M 5
WL 6
Q 32
M 17
Q 34
M 16
Q 36
M 18
Q 38
M 19
WL 2
Q 40
M 7
Q 42
M 6
Q 44
M 8
Q 46
M 9
H 2
AD 8,6
M 21
AD 9,7
AW 135,8
M 20
AW 159,9
UL 6,37
M 22
UH 7,37
AR 8,183
M 23
R 10
M 11
AD 8,6
AD 139,7
M 10
WL 2
R 8
M 34
AW 183,8
M 35
M 32
M 33
M 30
M 31
M 28
M 29
M 26
M 27
WV 5
M 24
WL 0
FP
M 25
M 36
ae 44,68,0
M 37
s_bitcmp1_b32 s50, 2
s_cselect_b32 s51, 24576, 0
v_add_u32 v47, s51, v3
M 38
XL 14,128
M 39
M 40
M 41
M 42
XL 16,896
M 43
M 44
M 45
FR
M 46
M 47
M 58
XL 18,1664
M 59
M 56
M 57
M 54
M 55
WL 0
FQ 32
M 52
XL 20,2432
M 53
M 50
FZ 32
)"
R"(R 24
M 51
mw 72
R 26
M 48
TY 106,5128
TY 110,18952
M 49
R 28
M 60
ad 84,1296
W 0
M 61
M 62
M 63
W 1
M 64
M 65
M 66
W 2
M 67
WL 3
AN 104,106
AN 105,107
M 68
WL 2
SH 106
AN 106,106
M 69
mw 75
AN 107,107
AN 108,110
M 70
AN 109,111
SH 110
M 71
R 30
M 82
AN 110,110
AN 111,111
M 83
TY 114,6664
TY 118,20488
M 80
ad 84,1302
W 0
M 81
M 78
M 79
W 1
M 76
M 77
M 74
W 2
M 75
M 72
WL 1
AN 112,114
AN 113,115
M 73
R 32
M 84
mw 0
SH 114
AN 114,114
M 85
WL 2
AN 115,115
AN 116,118
M 86
AN 117,119
SH 118
M 87
AN 118,118
AN 119,119
M 88
TY 122,8200
TY 126,22024
M 89
ad 84,1344
W 0
M 90
UL 6,38
UH 7,38
M 91
AR 8,136
AR 9,160
M 92
W 1
AD 8,6
M 93
AD 9,7
AW 136,8
M 94
AW 160,9
UL 6,39
M 95
R 34
M 106
W 2
UH 7,39
M 107
WL 3
AN 120,122
AN 121,123
M 104
WL 2
SH 122
AN 122,122
M 105
mw 3
AN 123,123
AN 124,126
M 102
AN 125,127
SH 126
M 103
AN 126,126
AN 127,127
M 100
ad 84,1350
W 0
M 101
AR 8,184
AD 8,6
M 98
AD 140,7
AW 184,8
M 99
W 1
UL 6,40
M 96
UH 7,40
AR 8,137
M 97
R 48
M 108
W 2
AR 9,161
M 109
AD 8,6
AD 9,7
M 110
AW 137,8
AW 161,9
M 111
bc 49,15,66,60
UL 6,41
M 112
mh 0
UH 7,41
AR 8,185
M 113
ad 66,2880
H 0
M 114
AD 8,6
AD 141,7
M 115
AW 185,8
UL 6,42
M 116
DG 1024
M 117
UH 7,42
AR 8,138
M 118
AR 9,162
AD 8,6
M 119
WL 0
R 50
M 120
bc 48,15,64,56
AD 9,7
M 121
mh 3
AW 138,8
AW 162,9
M 133
ad 64,2880
H 0
M 132
TX 58,4608
TX 62,18432
M 134
UL 6,43
UH 7,43
M 135
H 1
AR 8,186
M 123
AD 8,6
AD 142,7
M 122
TY 66,5120
TY 70,18944
M 124
H 2
AW 186,8
M 125
UL 6,44
UH 7,44
M 137
AR 8,139
AR 9,163
M 136
TX 74,6144
TX 78,19968
PA 12288
DF
M 138
WL 4
Q 0
M 139
Q 2
M 127
Q 4
M 126
TY 82,6656
TY 86,20480
M 128
Q 6
M 129
WL 4
Q 8
M 141
Q 10
M 140
TX 90,7680
TX 94,21504
M 142
Q 12
M 143
R 54
M 131
Q 14
M 130
TY 98,8192
TY 102,22016
M 0
R 52
M 1
WL 10
Q 16
M 13
Q 18
M 12
Q 20
M 14
Q 22
M 15
WL 8
Q 24
M 3
Q 26
M 2
Q 28
M 4
Q 30
M 5
WL 6
Q 32
M 17
Q 34
M 16
Q 36
M 18
Q 38
M 19
WL 2
Q 40
M 7
Q 42
M 6
Q 44
M 8
Q 46
M 9
AD 8,6
AD 9,7
M 21
AW 139,8
AW 163,9
M 20
UL 6,45
UH 7,45
M 22
AR 8,187
AD 8,6
M 23
R 58
M 11
AD 143,7
AW 187,8
M 10
WL 2
R 56
M 34
M 35
M 32
M 33
M 30
M 31
M 28
M 29
M 26
M 27
WV 6
M 24
WL 0
FP
M 25
M 36
M 37
M 38
ae 44,68,0
M 39
s_bfe_u32 s51, s50, 0x10003
v_mad_i32_i24 v47, v49, s51, v48
XL 22,128
M 40
M 41
M 42
M 43
M 44
XL 24,896
M 45
FR
M 46
M 47
M 58
M 59
M 56
XL 26,1664
M 57
M 54
M 55
WL 0
FQ 36
M 52
M 53
M 50
FZ 36
R 72
M 51
mw 24
R 74
M 48
TY 106,9736
TY 110,23560
M 49
R 76
M 60
ad 84,1392
W 0
M 61
XL 28,2432
M 62
M 63
W 1
M 64
M 65
M 66
W 2
M 67
WL 3
AN 104,106
AN 105,107
M 68
WL 2
SH 106
AN 106,106
M 69
mw 27
AN 107,107
AN 108,110
M 70
AN 109,111
SH 110
M 71
R 78
M 82
AN 110,110
AN 111,111
M 83
TY 114,11272
TY 118,25096
M 80
ad 84,1398
W 0
M 81
M 78
M 79
W 1
M 76
M 77
M 74
W 2
M 75
)"
R"(M 72
WL 1
AN 112,114
AN 113,115
M 73
R 80
M 84
mw 48
SH 114
AN 114,114
M 85
WL 2
AN 115,115
AN 116,118
M 86
AN 117,119
SH 118
M 87
AN 118,118
AN 119,119
M 88
TY 122,12808
TY 126,26632
M 89
ad 84,1440
W 0
M 90
M 91
M 92
W 1
UL 6,14
M 93
UH 7,14
AR 8,140
M 94
AR 9,164
AD 8,6
M 95
R 82
M 106
W 2
AD 9,7
M 107
WL 3
AN 120,122
AN 121,123
M 104
WL 2
SH 122
AN 122,122
M 105
mw 51
AN 123,123
AN 124,126
M 102
AN 125,127
SH 126
M 103
AN 126,126
AN 127,127
M 100
ad 84,1446
W 0
M 101
AW 140,8
AW 164,9
M 98
UL 6,15
UH 7,15
M 99
W 1
AR 8,188
M 96
AD 8,6
AD 144,7
M 97
R 0
M 108
W 2
AW 188,8
M 109
UL 6,16
UH 7,16
M 110
AR 8,141
AR 9,165
M 111
bc 49,16,66,60
AD 8,6
M 112
mh 9
AD 9,7
AW 141,8
M 113
ad 66,3072
H 0
M 114
AW 165,9
UL 6,17
M 115
UH 7,17
AR 8,189
M 116
DG 1024
M 117
AD 8,6
AD 145,7
M 118
AW 189,8
UL 6,18
M 119
WL 0
R 2
M 120
bc 48,16,64,56
UH 7,18
M 121
mh 12
AR 8,142
AR 9,166
M 133
ad 64,3072
H 0
M 132
TX 58,9216
TX 62,23040
M 134
AD 8,6
AD 9,7
M 135
H 1
AW 142,8
M 123
AW 166,9
UL 6,19
M 122
TY 66,9728
TY 70,23552
M 124
H 2
UH 7,19
M 125
AR 8,190
AD 8,6
M 137
AD 146,7
AW 190,8
M 136
TX 74,10752
TX 78,24576
M 138
WL 4
Q 0
M 139
Q 2
M 127
Q 4
M 126
TY 82,11264
TY 86,25088
M 128
Q 6
M 129
WL 4
Q 8
M 141
Q 10
M 140
TX 90,12288
TX 94,26112
M 142
Q 12
M 143
R 6
M 131
Q 14
M 130
TY 98,12800
TY 102,26624
M 0
R 4
M 1
WL 10
Q 16
M 13
Q 18
M 12
Q 20
M 14
Q 22
M 15
WL 8
Q 24
M 3
Q 26
M 2
Q 28
M 4
Q 30
M 5
WL 6
Q 32
M 17
Q 34
M 16
Q 36
M 18
Q 38
M 19
WL 2
Q 40
M 7
Q 42
M 6
Q 44
M 8
Q 46
M 9
UL 6,20
UH 7,20
M 21
AR 8,143
AR 9,167
M 20
AD 8,6
AD 9,7
M 22
AW 143,8
AW 167,9
M 23
R 10
M 11
UL 6,21
UH 7,21
M 10
WL 2
R 8
M 34
AR 8,191
AD 8,6
M 35
AD 147,7
AW 191,8
M 32
M 33
M 30
M 31
M 28
M 29
M 26
M 27
WV 5
M 24
WL 0
FP
M 25
M 36
M 37
M 38
M 39
M 40
ae 44,68,96
M 41
s_bitcmp1_b32 s50, 4
s_cselect_b32 s51, 24576, 0
v_add_u32 v47, s51, v3
M 42
XL 30,128
M 43
M 44
M 45
FR
M 46
XL 32,896
M 47
M 58
M 59
M 56
M 57
M 54
XL 34,1664
M 55
WL 0
FQ 40
M 52
M 53
M 50
FZ 40
R 24
M 51
mw 72
R 26
M 48
TY 106,520
TY 110,14344
M 49
R 28
M 60
ad 84,1488
W 0
M 61
XL 36,2432
v_lshlrev_b32 v55, 4, v0
M 62
FL
M 63
W 1
M 64
M 65
M 66
W 2
M 67
WL 3
AN 104,106
AN 105,107
M 68
WL 2
SH 106
AN 106,106
M 69
mw 75
AN 107,107
AN 108,110
M 70
AN 109,111
SH 110
M 71
R 30
M 82
AN 110,110
AN 111,111
M 83
TY 114,2056
TY 118,15880
M 80
ad 84,1494
W 0
M 81
M 78
M 79
W 1
M 76
M 77
M 74
W 2
M 75
M 72
WL 1
AN 112,114
AN 113,115
M 73
R 32
M 84
mw 0
SH 114
AN 114,114
M 85
WL 2
AN 115,115
AN 116,118
M 86
AN 117,119
SH 118
M 87
AN 118,118
AN 119,119
M 88
TY 122,3592
TY 126,17416
M 89
ad 84,1536
W 0
M 90
M 91
M 92
W 1
M 93
M 94
UL 6,22
UH 7,22
M 95
R 34
M 106
W 2
AR 8,144
M 107
WL 3
AN 120,122
AN 121,123
M 104
WL 2
SH 122
AN 122,122
M 105
mw 3
AN 123,123
AN 124,126
M 102
AN 125,127
SH 126
)"
R"(M 103
AN 126,126
AN 127,127
M 100
ad 84,1542
W 0
M 101
AR 9,168
AD 8,6
M 98
AD 9,7
AW 144,8
M 99
W 1
AW 168,9
M 96
UL 6,23
UH 7,23
M 97
R 48
M 108
W 2
AR 8,192
M 109
AD 8,6
AD 148,7
M 110
AW 192,8
UL 6,24
M 111
bc 49,17,66,60
UH 7,24
M 112
mh 18
AR 8,145
AR 9,169
M 113
ad 66,3264
H 0
M 114
AD 8,6
AD 9,7
M 115
AW 145,8
AW 169,9
M 116
DG 1024
M 117
UL 6,25
UH 7,25
M 118
AR 8,193
AD 8,6
M 119
WL 0
R 50
M 120
bc 48,17,64,56
AD 149,7
M 121
mh 21
AW 193,8
UL 6,26
M 133
ad 64,3264
H 0
M 132
TX 58,0
TX 62,13824
M 134
UH 7,26
AR 8,146
M 135
H 1
AR 9,170
M 123
AD 8,6
AD 9,7
M 122
TY 66,512
TY 70,14336
M 124
H 2
AW 146,8
M 125
AW 170,9
UL 6,27
M 137
UH 7,27
AR 8,194
M 136
TX 74,1536
TX 78,15360
PA 12288
DF
M 138
WL 4
Q 0
M 139
Q 2
M 127
Q 4
M 126
TY 82,2048
TY 86,15872
M 128
Q 6
M 129
WL 4
Q 8
M 141
Q 10
M 140
TX 90,3072
TX 94,16896
M 142
Q 12
M 143
R 54
M 131
Q 14
M 130
TY 98,3584
TY 102,17408
M 0
R 52
M 1
WL 10
Q 16
M 13
Q 18
M 12
Q 20
M 14
Q 22
M 15
WL 8
Q 24
M 3
Q 26
M 2
Q 28
M 4
Q 30
M 5
WL 6
Q 32
M 17
Q 34
M 16
Q 36
M 18
Q 38
M 19
WL 2
Q 40
M 7
Q 42
M 6
Q 44
M 8
Q 46
M 9
AD 8,6
AD 150,7
M 21
AW 194,8
UL 6,28
M 20
UH 7,28
AR 8,147
M 22
AR 9,171
AD 8,6
M 23
R 58
M 11
AD 9,7
AW 147,8
M 10
WL 2
R 56
M 34
AW 171,9
UL 6,29
M 35
UH 7,29
AR 8,195
M 32
AD 8,6
AD 151,7
M 33
AW 195,8
M 30
M 31
M 28
M 29
M 26
M 27
WV 6
M 24
WL 0
FP
M 25
M 36
M 37
M 38
M 39
M 40
M 41
M 42
ae 44,68,96
M 43
s_bfe_u32 s51, s50, 0x10005
v_mad_i32_i24 v47, v49, s51, v48
XL 38,128
M 44
M 45
FR
M 46
M 47
M 58
XL 40,896
M 59
M 56
M 57
M 54
M 55
WL 0
FQ 44
M 52
XL 42,1664
M 53
M 50
FZ 44
R 72
M 51
mw 24
R 74
M 48
TY 106,5128
TY 110,18952
M 49
R 76
M 60
ad 84,1584
W 0
M 61
XL 44,2432
M 62
M 63
W 1
M 64
M 65
M 66
W 2
M 67
WL 3
AN 104,106
AN 105,107
M 68
WL 2
SH 106
AN 106,106
M 69
mw 27
AN 107,107
AN 108,110
M 70
AN 109,111
SH 110
M 71
R 78
M 82
AN 110,110
AN 111,111
M 83
TY 114,6664
TY 118,20488
M 80
ad 84,1590
W 0
M 81
M 78
M 79
W 1
M 76
M 77
M 74
W 2
M 75
M 72
WL 1
AN 112,114
AN 113,115
M 73
R 80
M 84
mw 48
SH 114
AN 114,114
M 85
WL 2
AN 115,115
AN 116,118
M 86
AN 117,119
SH 118
M 87
AN 118,118
AN 119,119
M 88
TY 122,8200
TY 126,22024
M 89
ad 84,1632
W 0
M 90
M 91
M 92
W 1
M 93
M 94
M 95
R 82
M 106
W 2
UL 6,30
M 107
WL 3
AN 120,122
AN 121,123
M 104
WL 2
SH 122
AN 122,122
M 105
mw 51
AN 123,123
AN 124,126
M 102
AN 125,127
SH 126
M 103
AN 126,126
AN 127,127
M 100
ad 84,1638
W 0
M 101
UH 7,30
AR 8,148
M 98
AR 9,172
AD 8,6
M 99
W 1
AD 9,7
M 96
AW 148,8
AW 172,9
M 97
R 0
M 108
W 2
UL 6,31
M 109
UH 7,31
AR 8,196
M 110
AD 8,6
AD 152,7
M 111
bc 49,18,66,60
AW 196,8
M 112
mh 0
UL 6,32
UH 7,32
M 113
ad 66,3456
H 0
M 114
AR 8,149
AR 9,173
M 115
AD 8,6
AD 9,7
M 116
DG 1024
M 117
AW 149,8
AW 173,9
M 118
UL 6,33
UH 7,33
M 119
WL 0
R 2
M 120
)"
R"(bc 48,18,64,56
AR 8,197
M 121
mh 3
AD 8,6
AD 153,7
M 133
ad 64,3456
H 0
M 132
TX 58,4608
TX 62,18432
M 134
AW 197,8
UL 6,34
M 135
H 1
UH 7,34
M 123
AR 8,150
AR 9,174
M 122
TY 66,5120
TY 70,18944
M 124
H 2
AD 8,6
M 125
AD 9,7
AW 150,8
M 137
AW 174,9
UL 6,35
M 136
TX 74,6144
TX 78,19968
M 138
WL 4
Q 0
M 139
Q 2
M 127
Q 4
M 126
TY 82,6656
TY 86,20480
M 128
Q 6
M 129
WL 4
Q 8
M 141
Q 10
M 140
TX 90,7680
TX 94,21504
M 142
Q 12
M 143
R 6
M 131
Q 14
M 130
TY 98,8192
TY 102,22016
M 0
R 4
M 1
WL 10
Q 16
M 13
Q 18
M 12
Q 20
M 14
Q 22
M 15
WL 8
Q 24
M 3
Q 26
M 2
Q 28
M 4
Q 30
M 5
WL 6
Q 32
M 17
Q 34
M 16
Q 36
M 18
Q 38
M 19
WL 2
Q 40
M 7
Q 42
M 6
Q 44
M 8
Q 46
M 9
UH 7,35
AR 8,198
M 21
AD 8,6
AD 154,7
M 20
AW 198,8
UL 6,36
M 22
UH 7,36
AR 8,151
M 23
R 10
M 11
AR 9,175
AD 8,6
M 10
WL 2
R 8
M 34
AD 9,7
AW 151,8
M 35
AW 175,9
UL 6,37
M 32
UH 7,37
AR 8,199
M 33
v_cmp_ne_u32 vcc, 2, v54
AD 8,6
M 30
AD 155,7
AW 199,8
M 31
M 28
FC
M 29
M 26
M 27
WV 5
M 24
WL 0
FP
M 25
M 36
M 37
M 38
M 39
M 40
M 41
M 42
M 43
M 44
M 45
FR
M 46
M 47
M 58
M 59
M 56
M 57
M 54
M 55
WL 0
FQ 48
M 52
M 53
M 50
FZ 48
R 24
M 51
mw 72
R 26
M 48
TY 106,9736
TY 110,23560
M 49
R 28
M 60
ad 84,1680
W 0
M 61
M 62
M 63
W 1
M 64
M 65
M 66
W 2
M 67
WL 3
AN 104,106
AN 105,107
M 68
WL 2
SH 106
AN 106,106
M 69
mw 75
AN 107,107
AN 108,110
M 70
AN 109,111
SH 110
M 71
R 30
M 82
AN 110,110
AN 111,111
M 83
TY 114,11272
TY 118,25096
M 80
ad 84,1686
W 0
M 81
ae 44,70,0
M 78
s_bitcmp1_b32 s50, 0
s_cselect_b32 s51, 24576, 0
v_add_u32 v47, s51, v3
M 79
W 1
XL 14,256
M 76
XL 16,1024
M 77
M 74
W 2
M 75
M 72
WL 1
AN 112,114
AN 113,115
M 73
R 32
M 84
mw 0
SH 114
AN 114,114
M 85
WL 2
AN 115,115
AN 116,118
M 86
AN 117,119
SH 118
M 87
AN 118,118
AN 119,119
M 88
TY 122,12808
TY 126,26632
M 89
ad 84,1728
W 0
M 90
XL 18,1792
M 91
XL 20,2560
M 92
W 1
M 93
M 94
M 95
R 34
M 106
W 2
M 107
WL 3
AN 120,122
AN 121,123
M 104
WL 2
SH 122
AN 122,122
M 105
mw 3
AN 123,123
AN 124,126
M 102
AN 125,127
SH 126
M 103
AN 126,126
AN 127,127
M 100
ad 84,1734
W 0
M 101
UL 6,38
UH 7,38
M 98
AR 8,152
AR 9,176
M 99
W 1
AD 8,6
M 96
AD 9,7
AW 152,8
M 97
R 48
M 108
W 2
AW 176,9
M 109
UL 6,39
UH 7,39
M 110
AD 132,6
AD 156,7
M 111
bc 49,19,66,60
UL 6,40
M 112
mh 9
UH 7,40
AR 8,153
M 113
ad 66,3648
H 0
M 114
AR 9,177
AD 8,6
M 115
AD 9,7
AW 153,8
M 116
DG 1024
M 117
AW 177,9
UL 6,41
M 118
UH 7,41
AD 133,6
M 119
WL 0
R 50
M 120
bc 48,19,64,56
AD 157,7
M 121
mh 12
UL 6,42
UH 7,42
M 133
ad 64,3648
H 0
M 132
TX 58,9216
TX 62,23040
M 134
AR 8,154
AR 9,178
M 135
H 1
AD 8,6
M 123
AD 9,7
AW 154,8
M 122
TY 66,9728
TY 70,23552
M 124
H 2
AW 178,9
M 125
UL 6,43
UH 7,43
M 137
AD 134,6
AD 158,7
M 136
TX 74,10752
TX 78,24576
v_min_u32 v52, 752, v0
v_lshrrev_b32 v52, 1, v52
v_add_u32 v53, 0x1800, v52
global_load_dwordx2 a[200:201], v52, s[94:95] offset:-2048 sc1
global_load_dwordx2 a[202:203], v52, s[94:95] offset:-512 sc1
global_load_dwordx2 a[204:205], v52, s[94:95] offset:1024 sc1
global_load_dwordx2 a[206:207], v52, s[94:95] offset:2560 sc1
global_load_dwordx2 a[208:209], v53, s[94:95] offset:-2048 sc1
global_load_dwordx2 a[210:211], v53, s[94:95] offset:-512 sc1
global_load_dwordx2 a[212:213], v53, s[94:95] offset:1024 sc1
global_load_dwordx2 a[214:215], v53, s[94:95] offset:2560 sc1
PA 12288
DF
M 138
WL 4
Q 0
M 139
Q 2
M 127
)"
R"(Q 4
M 126
TY 82,11264
TY 86,25088
M 128
Q 6
M 129
WL 4
Q 8
M 141
Q 10
M 140
TX 90,12288
TX 94,26112
M 142
Q 12
M 143
R 54
M 131
Q 14
M 130
TY 98,12800
TY 102,26624
M 0
R 52
M 1
WL 10
Q 16
M 13
Q 18
M 12
Q 20
M 14
Q 22
M 15
WL 8
Q 24
M 3
Q 26
M 2
Q 28
M 4
Q 30
M 5
WL 6
Q 32
M 17
Q 34
M 16
Q 36
M 18
Q 38
M 19
WL 2
Q 40
M 7
Q 42
M 6
Q 44
M 8
Q 46
M 9
UL 6,44
UH 7,44
M 21
AR 8,155
AR 9,179
M 20
AD 8,6
AD 9,7
M 22
AW 155,8
AW 179,9
M 23
R 58
M 11
UL 6,45
UH 7,45
M 10
WL 2
R 56
M 34
AD 135,6
AD 159,7
M 35
M 32
M 33
M 30
M 31
M 28
M 29
M 26
M 27
WV 14
M 24
WL 0
FP
M 25
M 36
M 37
M 38
M 39
M 40
M 41
M 42
M 43
M 44
M 45
FR
M 46
M 47
M 58
M 59
M 56
M 57
M 54
M 55
WL 0
FQ 52
M 52
M 53
M 50
FZ 52
R 72
M 51
mw 24
R 74
M 48
TY 106,520
TY 110,14344
M 49
R 76
M 60
ad 84,1776
W 0
M 61
M 62
M 63
W 1
M 64
M 65
M 66
W 2
M 67
WL 3
AN 104,106
AN 105,107
M 68
WL 2
SH 106
AN 106,106
M 69
mw 27
AN 107,107
AN 108,110
M 70
AN 109,111
SH 110
M 71
R 78
M 82
AN 110,110
AN 111,111
M 83
TY 114,2056
TY 118,15880
M 80
ad 84,1782
W 0
M 81
ae 44,70,0
M 78
s_bfe_u32 s51, s50, 0x10001
v_mad_i32_i24 v47, v49, s51, v48
XL 22,256
M 79
W 1
M 76
M 77
M 74
W 2
XL 24,1024
M 75
M 72
WL 1
AN 112,114
AN 113,115
M 73
R 80
M 84
mw 48
SH 114
AN 114,114
M 85
WL 2
AN 115,115
AN 116,118
M 86
AN 117,119
SH 118
M 87
AN 118,118
AN 119,119
M 88
TY 122,3592
TY 126,17416
M 89
ad 84,1824
W 0
M 90
XL 26,1792
M 91
M 92
W 1
XL 28,2560
M 93
M 94
M 95
R 82
M 106
W 2
M 107
WL 3
AN 120,122
AN 121,123
M 104
WL 2
SH 122
AN 122,122
M 105
mw 51
AN 123,123
AN 124,126
M 102
AN 125,127
SH 126
M 103
AN 126,126
AN 127,127
M 100
ad 84,1830
W 0
M 101
M 98
M 99
W 1
M 96
M 97
R 0
M 108
W 2
M 109
M 110
M 111
bc 49,20,66,60
M 112
mh 18
M 113
ad 66,3840
H 0
M 114
M 115
M 116
DG 1024
M 117
M 118
M 119
WL 2
R 2
M 120
bc 48,20,64,56
M 121
mh 21
M 133
ad 64,3840
H 0
M 132
TX 58,0
TX 62,13824
M 134
M 135
H 1
M 123
UL 6,14
UH 7,14
M 122
TY 66,512
TY 70,14336
M 124
H 2
AD 160,6
M 125
AD 184,7
UL 6,15
M 137
UH 7,15
AD 208,6
M 136
TX 74,1536
TX 78,15360
M 138
WL 4
Q 0
M 139
Q 2
M 127
Q 4
M 126
TY 82,2048
TY 86,15872
M 128
Q 6
M 129
WL 4
Q 8
M 141
Q 10
M 140
TX 90,3072
TX 94,16896
M 142
Q 12
M 143
R 6
M 131
Q 14
M 130
TY 98,3584
TY 102,17408
M 0
R 4
M 1
WL 10
Q 16
M 13
Q 18
M 12
Q 20
M 14
Q 22
M 15
WL 8
Q 24
M 3
Q 26
M 2
Q 28
M 4
Q 30
M 5
WL 6
Q 32
M 17
Q 34
M 16
Q 36
M 18
Q 38
M 19
WL 2
Q 40
M 7
Q 42
M 6
Q 44
M 8
Q 46
M 9
AD 232,7
UL 6,16
M 21
UH 7,16
AD 161,6
M 20
AD 185,7
UL 6,17
M 22
UH 7,17
AD 209,6
M 23
R 10
M 11
AD 233,7
UL 6,18
M 10
WL 2
R 8
M 34
UH 7,18
AD 162,6
M 35
AD 186,7
UL 6,19
M 32
UH 7,19
AD 210,6
M 33
AD 234,7
UL 6,20
M 30
)"
R"(UH 7,20
AD 163,6
M 31
AD 187,7
UL 6,21
M 28
UH 7,21
AD 211,6
M 29
AD 235,7
M 26
M 27
WV 5
M 24
WL 0
FP
M 25
M 36
M 37
M 38
M 39
M 40
M 41
M 42
M 43
M 44
M 45
FR
M 46
M 47
M 58
M 59
M 56
M 57
M 54
M 55
WL 0
FQ 56
M 52
M 53
M 50
FZ 56
R 24
M 51
mw 72
R 26
M 48
TY 106,5128
TY 110,18952
M 49
R 28
M 60
ad 84,1872
W 0
M 61
M 62
M 63
W 1
M 64
M 65
M 66
W 2
M 67
WL 3
AN 104,106
AN 105,107
M 68
WL 2
SH 106
AN 106,106
M 69
mw 75
AN 107,107
AN 108,110
M 70
AN 109,111
SH 110
M 71
R 30
M 82
AN 110,110
AN 111,111
M 83
TY 114,6664
TY 118,20488
M 80
ad 84,1878
W 0
M 81
M 78
M 79
W 1
M 76
ae 44,68,0
M 77
s_bitcmp1_b32 s50, 2
s_cselect_b32 s51, 24576, 0
v_add_u32 v47, s51, v3
M 74
W 2
XL 30,256
M 75
M 72
WL 1
AN 112,114
AN 113,115
M 73
R 32
M 84
mw 0
SH 114
AN 114,114
M 85
WL 2
AN 115,115
AN 116,118
M 86
AN 117,119
SH 118
M 87
AN 118,118
AN 119,119
M 88
TY 122,8200
TY 126,22024
M 89
ad 84,1920
W 0
M 90
XL 32,1024
M 91
XL 34,1792
M 92
W 1
M 93
M 94
M 95
R 34
M 106
W 2
XL 36,2560
M 107
WL 3
AN 120,122
AN 121,123
M 104
WL 2
SH 122
AN 122,122
M 105
mw 3
AN 123,123
AN 124,126
M 102
AN 125,127
SH 126
M 103
AN 126,126
AN 127,127
M 100
ad 84,1926
W 0
M 101
M 98
M 99
W 1
M 96
M 97
R 48
M 108
W 2
M 109
M 110
M 111
bc 49,21,66,60
M 112
mh 0
M 113
ad 66,4032
H 0
M 114
M 115
M 116
DG 1024
M 117
M 118
M 119
WL 2
R 50
M 120
bc 48,21,64,56
M 121
mh 3
v_mov_b32 v54, s39
v_mul_f32 v54, 0xb62aaa8f, v54
M 133
ad 64,4032
H 0
M 132
TX 58,4608
TX 62,18432
M 134
v_add_u32 v52, %[bm], v52
v_bfe_u32 v53, v52, 6, 2
M 135
H 1
v_and_b32 v55, 63, v52
M 123
v_add_u32 v53, v53, v55
v_and_b32 v52, 0x700, v52
M 122
TY 66,5120
TY 70,18944
M 124
H 2
v_lshl_add_u32 v52, v53, 2, v52
M 125
v_add_u32 v52, %[sb], v52
AR 12,214
M 137
AR 13,215
AR 8,208
M 136
TX 74,6144
TX 78,19968
PA 12288
DF
M 138
WL 4
Q 0
M 139
Q 2
M 127
Q 4
M 126
TY 82,6656
TY 86,20480
M 128
Q 6
M 129
WL 4
Q 8
M 141
Q 10
M 140
TX 90,7680
TX 94,21504
M 142
Q 12
M 143
R 54
M 131
Q 14
M 130
TY 98,8192
TY 102,22016
M 0
R 52
M 1
WL 10
Q 16
M 13
Q 18
M 12
Q 20
M 14
Q 22
M 15
WL 8
Q 24
M 3
Q 26
M 2
Q 28
M 4
Q 30
M 5
WL 6
Q 32
M 17
Q 34
M 16
Q 36
M 18
Q 38
M 19
WL 2
Q 40
M 7
Q 42
M 6
Q 44
M 8
Q 46
M 9
AR 9,209
v_add_f32 v12, v12, v8
M 21
v_add_f32 v13, v13, v9
AR 8,210
M 20
AR 9,211
v_add_f32 v12, v12, v8
M 22
v_add_f32 v13, v13, v9
AR 8,212
M 23
R 58
M 11
AR 9,213
v_add_f32 v12, v12, v8
M 10
WL 2
R 56
M 34
v_add_f32 v13, v13, v9
AR 6,206
M 35
AR 7,207
v_add_f32 v12, v12, v54
M 32
v_add_f32 v13, v13, v54
v_fma_f32 v8, v6, s86, v12
M 33
ds_write_b32 v52, v8 offset:1024
v_fma_f32 v9, v7, s86, v13
M 30
ds_write_b32 v52, v9 offset:1040
AR 8,200
M 31
AR 9,201
v_add_f32 v6, v6, v8
M 28
v_add_f32 v7, v7, v9
v_fma_f32 v8, v6, s86, v12
M 29
ds_write_b32 v52, v8 offset:2560
v_fma_f32 v9, v7, s86, v13
M 26
ds_write_b32 v52, v9 offset:2576
AR 8,202
M 27
WV 6
AR 9,203
v_add_f32 v6, v6, v8
M 24
WL 4
FP
v_add_f32 v7, v7, v9
M 25
v_fma_f32 v8, v6, s86, v12
v_fma_f32 v9, v7, s86, v13
M 36
ds_write_b32 v52, v8 offset:4096
ds_write_b32 v52, v9 offset:4112
M 37
AR 8,204
AR 9,205
M 38
v_add_f32 v6, v6, v8
v_add_f32 v7, v7, v9
M 39
v_fma_f32 v8, v6, s86, v12
v_fma_f32 v9, v7, s86, v13
M 40
ds_write_b32 v52, v8 offset:5632
ds_write_b32 v52, v9 offset:5648
M 41
UL 6,22
UH 7,22
M 42
AD 164,6
AD 188,7
M 43
UL 6,23
UH 7,23
M 44
AD 212,6
AD 236,7
M 45
FR
UL 6,24
M 46
UH 7,24
AD 165,6
M 47
AD 189,7
UL 6,25
M 58
UH 7,25
AD 213,6
M 59
AD 237,7
UL 6,26
M 56
UH 7,26
AD 166,6
M 57
AD 190,7
UL 6,27
)"
R"(M 54
UH 7,27
AD 214,6
M 55
WL 0
FQ 60
AD 238,7
M 52
UL 6,28
UH 7,28
M 53
AD 167,6
AD 191,7
M 50
FZ 60
R 72
M 51
mw 24
R 74
M 48
TY 106,9736
TY 110,23560
M 49
R 76
M 60
ad 84,1968
W 0
M 61
UL 6,29
UH 7,29
M 62
AD 215,6
AD 239,7
M 63
W 1
M 64
M 65
M 66
W 2
M 67
WL 3
AN 104,106
AN 105,107
M 68
WL 2
SH 106
AN 106,106
M 69
mw 27
AN 107,107
AN 108,110
M 70
AN 109,111
SH 110
M 71
R 78
M 82
AN 110,110
AN 111,111
M 83
TY 114,11272
TY 118,25096
M 80
ad 84,1974
W 0
M 81
M 78
M 79
W 1
M 76
M 77
M 74
W 2
M 75
M 72
WL 1
AN 112,114
AN 113,115
M 73
R 80
M 84
mw 48
SH 114
AN 114,114
M 85
WL 2
AN 115,115
AN 116,118
M 86
AN 117,119
SH 118
M 87
AN 118,118
AN 119,119
M 88
TY 122,12808
TY 126,26632
M 89
ad 84,2016
W 0
M 90
ae 44,68,0
M 91
s_bfe_u32 s51, s50, 0x10003
v_mad_i32_i24 v47, v49, s51, v48
XL 38,256
M 92
W 1
XL 40,1024
M 93
M 94
XL 42,1792
M 95
R 82
M 106
W 2
M 107
WL 3
AN 120,122
AN 121,123
M 104
WL 2
SH 122
AN 122,122
M 105
mw 51
AN 123,123
AN 124,126
M 102
AN 125,127
SH 126
M 103
AN 126,126
AN 127,127
M 100
ad 84,2022
W 0
M 101
XL 44,2560
M 98
M 99
W 1
M 96
M 97
R 0
M 108
W 2
M 109
M 110
M 111
bc 49,22,66,60
M 112
mh 9
M 113
ad 66,4224
H 0
M 114
M 115
M 116
DG 1024
M 117
M 118
M 119
WL 2
R 2
M 120
bc 48,22,64,56
M 121
mh 12
M 133
ad 64,4224
H 0
M 132
TX 58,9216
TX 62,23040
M 134
M 135
H 1
M 123
M 122
TY 66,9728
TY 70,23552
M 124
H 2
M 125
M 137
M 136
TX 74,10752
TX 78,24576
M 138
WL 4
Q 0
M 139
Q 2
M 127
Q 4
M 126
TY 82,11264
TY 86,25088
M 128
Q 6
M 129
WL 4
Q 8
M 141
Q 10
M 140
TX 90,12288
TX 94,26112
M 142
Q 12
M 143
R 6
M 131
Q 14
M 130
TY 98,12800
TY 102,26624
M 0
R 4
M 1
WL 10
Q 16
M 13
Q 18
M 12
Q 20
M 14
Q 22
M 15
WL 8
Q 24
M 3
Q 26
M 2
Q 28
M 4
Q 30
M 5
WL 6
Q 32
M 17
Q 34
M 16
Q 36
M 18
Q 38
M 19
WL 2
Q 40
M 7
Q 42
M 6
Q 44
M 8
Q 46
M 9
UL 6,30
UH 7,30
M 21
AD 168,6
AD 192,7
M 20
UL 6,31
UH 7,31
M 22
AD 216,6
AD 240,7
M 23
R 10
M 11
UL 6,32
UH 7,32
M 10
WL 2
R 8
M 34
AD 169,6
AD 193,7
M 35
UL 6,33
UH 7,33
M 32
AD 217,6
AD 241,7
M 33
UL 6,34
UH 7,34
M 30
AD 170,6
AD 194,7
M 31
UL 6,35
UH 7,35
M 28
AD 218,6
AD 242,7
M 29
UL 6,36
UH 7,36
M 26
AD 171,6
AD 195,7
M 27
WV 5
UL 6,37
UH 7,37
M 24
WL 0
FP
AD 219,6
M 25
AD 243,7
M 36
M 37
M 38
M 39
M 40
M 41
M 42
M 43
M 44
M 45
FR
M 46
M 47
M 58
M 59
M 56
M 57
M 54
M 55
WL 0
FQ 64
M 52
M 53
M 50
FZ 64
R 24
M 51
mw 72
R 26
M 48
TY 106,520
TY 110,14344
M 49
R 28
M 60
ad 84,2064
W 0
M 61
M 62
M 63
W 1
M 64
M 65
M 66
W 2
M 67
WL 3
AN 104,106
AN 105,107
M 68
WL 2
SH 106
AN 106,106
M 69
mw 75
AN 107,107
AN 108,110
M 70
AN 109,111
SH 110
M 71
R 30
M 82
AN 110,110
AN 111,111
M 83
TY 114,2056
TY 118,15880
M 80
ad 84,2070
W 0
M 81
M 78
M 79
W 1
M 76
M 77
M 74
W 2
M 75
M 72
WL 1
AN 112,114
AN 113,115
M 73
R 32
M 84
)"
R"(mw 0
SH 114
AN 114,114
M 85
WL 2
AN 115,115
AN 116,118
M 86
AN 117,119
SH 118
M 87
AN 118,118
AN 119,119
M 88
TY 122,3592
TY 126,17416
M 89
ad 84,2112
W 0
M 90
ae 44,68,96
M 91
s_bitcmp1_b32 s50, 4
s_cselect_b32 s51, 24576, 0
v_add_u32 v47, s51, v3
M 92
W 1
XL 14,256
M 93
XL 16,1024
M 94
M 95
R 34
M 106
W 2
M 107
WL 3
AN 120,122
AN 121,123
M 104
WL 2
SH 122
AN 122,122
M 105
mw 3
AN 123,123
AN 124,126
M 102
AN 125,127
SH 126
M 103
AN 126,126
AN 127,127
M 100
ad 84,2118
W 0
M 101
XL 18,1792
M 98
XL 20,2560
M 99
W 1
M 96
M 97
R 48
M 108
W 2
M 109
M 110
M 111
bc 49,23,66,60
M 112
mh 18
M 113
ad 66,4416
H 0
M 114
M 115
M 116
DG 1024
M 117
M 118
M 119
WL 2
R 50
M 120
bc 48,23,64,56
M 121
mh 21
M 133
ad 64,4416
H 0
M 132
TX 58,0
TX 62,13824
M 134
M 135
H 1
M 123
M 122
TY 66,512
TY 70,14336
M 124
H 2
M 125
M 137
M 136
TX 74,1536
TX 78,15360
PA 12288
DF
M 138
WL 4
Q 0
M 139
Q 2
M 127
Q 4
M 126
TY 82,2048
TY 86,15872
M 128
Q 6
M 129
WL 4
Q 8
M 141
Q 10
M 140
TX 90,3072
TX 94,16896
M 142
Q 12
M 143
R 54
M 131
Q 14
M 130
TY 98,3584
TY 102,17408
M 0
R 52
M 1
WL 10
Q 16
M 13
Q 18
M 12
Q 20
M 14
Q 22
M 15
WL 8
Q 24
M 3
Q 26
M 2
Q 28
M 4
Q 30
M 5
WL 6
Q 32
M 17
Q 34
M 16
Q 36
M 18
Q 38
M 19
WL 2
Q 40
M 7
Q 42
M 6
Q 44
M 8
Q 46
M 9
UL 6,38
UH 7,38
M 21
AD 172,6
AD 196,7
M 20
UL 6,39
UH 7,39
M 22
AD 220,6
AD 244,7
M 23
R 58
M 11
UL 6,40
UH 7,40
M 10
WL 2
R 56
M 34
AD 173,6
AD 197,7
M 35
UL 6,41
UH 7,41
M 32
AD 221,6
AD 245,7
M 33
UL 6,42
UH 7,42
M 30
AD 174,6
AD 198,7
M 31
UL 6,43
UH 7,43
M 28
AD 222,6
AD 246,7
M 29
UL 6,44
UH 7,44
M 26
AD 175,6
AD 199,7
M 27
WV 6
UL 6,45
UH 7,45
M 24
WL 0
FP
AD 223,6
M 25
AD 247,7
M 36
M 37
M 38
M 39
M 40
M 41
M 42
M 43
M 44
M 45
FR
M 46
M 47
M 58
M 59
M 56
M 57
M 54
M 55
WL 0
FQ 68
M 52
M 53
M 50
FZ 68
R 72
M 51
mw 24
R 74
M 48
TY 106,5128
TY 110,18952
M 49
R 76
M 60
ad 84,2160
W 0
M 61
M 62
M 63
W 1
M 64
M 65
M 66
W 2
M 67
WL 3
AN 104,106
AN 105,107
M 68
WL 2
SH 106
AN 106,106
M 69
mw 27
AN 107,107
AN 108,110
M 70
AN 109,111
SH 110
M 71
R 78
M 82
AN 110,110
AN 111,111
M 83
TY 114,6664
TY 118,20488
M 80
ad 84,2166
W 0
M 81
M 78
M 79
W 1
M 76
M 77
M 74
W 2
M 75
M 72
WL 1
AN 112,114
AN 113,115
M 73
R 80
M 84
mw 48
SH 114
AN 114,114
M 85
WL 2
AN 115,115
AN 116,118
M 86
AN 117,119
SH 118
M 87
AN 118,118
AN 119,119
M 88
TY 122,8200
TY 126,22024
M 89
ad 84,2208
W 0
M 90
ae 44,68,96
M 91
s_bfe_u32 s51, s50, 0x10005
v_mad_i32_i24 v47, v49, s51, v48
XL 22,256
M 92
W 1
M 93
M 94
M 95
R 82
M 106
W 2
XL 24,1024
M 107
WL 3
AN 120,122
AN 121,123
M 104
WL 2
SH 122
AN 122,122
M 105
mw 51
AN 123,123
AN 124,126
M 102
AN 125,127
SH 126
M 103
AN 126,126
AN 127,127
M 100
ad 84,2214
W 0
M 101
XL 26,1792
M 98
M 99
W 1
M 96
M 97
R 0
M 108
W 2
XL 28,2560
M 109
M 110
M 111
bc 49,0,66,60
M 112
mh 0
M 113
ad 66,32
H 0
M 114
M 115
M 116
)"
R"(DG 1024
M 117
M 118
M 119
WL 2
R 2
M 120
bc 48,0,64,56
M 121
mh 3
M 133
ad 64,0
H 0
M 132
TX 58,4608
TX 62,18432
M 134
M 135
H 1
M 123
M 122
TY 66,5120
TY 70,18944
M 124
H 2
M 125
M 137
M 136
TX 74,6144
TX 78,19968
M 138
WL 4
Q 0
M 139
Q 2
M 127
Q 4
M 126
TY 82,6656
TY 86,20480
M 128
Q 6
M 129
WL 4
Q 8
M 141
Q 10
M 140
TX 90,7680
TX 94,21504
M 142
Q 12
M 143
R 6
M 131
Q 14
M 130
TY 98,8192
TY 102,22016
M 0
R 4
M 1
WL 10
Q 16
M 13
Q 18
M 12
Q 20
M 14
Q 22
M 15
WL 8
Q 24
M 3
Q 26
M 2
Q 28
M 4
Q 30
M 5
WL 6
Q 32
M 17
Q 34
M 16
Q 36
M 18
Q 38
M 19
WL 2
Q 40
M 7
Q 42
M 6
Q 44
M 8
Q 46
M 9
UL 6,14
UH 7,14
M 21
AD 176,6
AD 200,7
M 20
UL 6,15
UH 7,15
M 22
AD 224,6
AD 248,7
M 23
R 10
M 11
UL 6,16
UH 7,16
M 10
WL 2
R 8
M 34
AD 177,6
AD 201,7
M 35
UL 6,17
UH 7,17
M 32
AD 225,6
AD 249,7
M 33
UL 6,18
UH 7,18
M 30
AD 178,6
AD 202,7
M 31
UL 6,19
UH 7,19
M 28
AD 226,6
AD 250,7
M 29
UL 6,20
UH 7,20
M 26
AD 179,6
AD 203,7
M 27
WV 6
UL 6,21
UH 7,21
M 24
WL 0
FP
AD 227,6
M 25
AD 251,7
M 36
M 37
M 38
M 39
M 40
M 41
M 42
M 43
M 44
M 45
FR
M 46
M 47
M 58
M 59
M 56
M 57
M 54
M 55
WL 0
FQ 72
M 52
M 53
M 50
FZ 72
R 24
M 51
mw 72
R 26
M 48
TY 106,9736
TY 110,23560
M 49
R 28
M 60
ad 84,2256
W 0
M 61
M 62
M 63
W 1
M 64
M 65
M 66
W 2
M 67
WL 3
AN 104,106
AN 105,107
M 68
WL 2
SH 106
AN 106,106
M 69
mw 75
AN 107,107
AN 108,110
M 70
AN 109,111
SH 110
M 71
R 30
M 82
AN 110,110
AN 111,111
M 83
TY 114,11272
TY 118,25096
M 80
ad 84,2262
W 0
M 81
M 78
M 79
W 1
M 76
M 77
M 74
W 2
M 75
M 72
WL 1
AN 112,114
AN 113,115
M 73
R 32
M 84
mw 0
SH 114
AN 114,114
M 85
WL 2
AN 115,115
AN 116,118
M 86
AN 117,119
SH 118
M 87
AN 118,118
AN 119,119
M 88
TY 122,12808
TY 126,26632
M 89
ad 84,0
W 0
M 90
M 91
M 92
W 1
M 93
M 94
M 95
R 34
M 106
W 2
M 107
WL 3
AN 120,122
AN 121,123
M 104
WL 2
SH 122
AN 122,122
M 105
mw 3
AN 123,123
AN 124,126
M 102
AN 125,127
SH 126
M 103
AN 126,126
AN 127,127
M 100
ad 84,6
W 0
M 101
M 98
M 99
W 1
M 96
M 97
R 48
M 108
W 2
M 109
M 110
M 111
bc 49,1,66,60
M 112
mh 9
M 113
ad 66,224
H 0
M 114
M 115
M 116
DG 1024
M 117
M 118
M 119
WL 2
R 50
M 120
bc 48,1,64,56
M 121
mh 12
M 133
ad 64,224
H 0
M 132
TX 58,9216
TX 62,23040
M 134
M 135
DG 1024
M 123
M 122
TY 66,9728
TY 70,23552
M 124
mh 15
M 125
ad 64,195
H 0
M 137
M 136
TX 74,10752
TX 78,24576
M 138
WL 4
Q 0
M 139
Q 2
M 127
Q 4
M 126
TY 82,11264
TY 86,25088
M 128
Q 6
M 129
WL 4
Q 8
M 141
Q 10
M 140
TX 90,12288
TX 94,26112
M 142
Q 12
M 143
R 54
M 131
Q 14
M 130
TY 98,12800
TY 102,26624
M 0
R 52
M 1
WL 10
Q 16
M 13
Q 18
M 12
Q 20
M 14
Q 22
M 15
WL 8
Q 24
M 3
Q 26
M 2
Q 28
M 4
Q 30
M 5
WL 6
Q 32
M 17
Q 34
M 16
Q 36
)"
R"(M 18
Q 38
M 19
WL 2
Q 40
M 7
Q 42
M 6
Q 44
M 8
Q 46
M 9
DG 1024
M 21
UL 6,22
UH 7,22
M 20
AD 180,6
AD 204,7
M 22
PA 12288
DF
UL 6,23
M 23
R 58
M 11
UH 7,23
AD 228,6
M 10
WL 2
R 56
M 34
AD 252,7
UL 6,24
M 35
UH 7,24
AD 181,6
M 32
AD 205,7
UL 6,25
BV 52,0
M 33
UH 7,25
AD 229,6
M 30
AD 253,7
UL 6,26
M 31
UH 7,26
AD 182,6
M 28
AD 206,7
UL 6,27
M 29
UH 7,27
AD 230,6
M 26
WV 24
AD 254,7
UL 6,28
M 27
WV 7
UH 7,28
AD 183,6
M 24
WL 1
FP
AD 207,7
M 25
UL 6,29
UH 7,29
M 36
AD 231,6
AD 255,7
M 37
M 38
M 39
M 40
ds_read_b64 v[12:13], v4 offset:256
ds_read_b64 v[46:47], v4 offset:264
M 41
AR 6,36
AR 7,60
M 42
AR 8,84
AR 9,108
M 43
WL 3
EF 6,52
EF 7,53
M 44
EF 8,54
EF 9,55
M 45
FR
CV 30,6,7
M 46
CV 31,8,9
AR 6,37
M 47
AR 7,61
AR 8,85
M 58
AR 9,109
EF 6,52
M 59
EF 7,53
EF 8,54
M 56
EF 9,55
CV 32,6,7
M 57
CV 33,8,9
AR 6,38
M 54
AR 7,62
AR 8,86
M 55
WL 0
FQ 76
AR 9,110
M 52
EF 6,52
EF 7,53
M 53
EF 8,54
EF 9,55
M 50
FZ 76
R 72
M 51
mw 24
R 74
M 48
TY 106,520
TY 110,14344
M 49
R 76
M 60
ad 84,48
W 0
M 61
CV 34,6,7
CV 35,8,9
M 62
AR 6,39
AR 7,63
M 63
W 1
AR 8,87
M 64
AR 9,111
EF 6,52
M 65
EF 7,53
EF 8,54
M 66
W 2
EF 9,55
M 67
WL 3
AN 104,106
AN 105,107
M 68
WL 2
SH 106
AN 106,106
M 69
mw 27
AN 107,107
AN 108,110
M 70
AN 109,111
SH 110
M 71
R 78
M 82
AN 110,110
AN 111,111
M 83
TY 114,2056
TY 118,15880
M 80
ad 84,54
W 0
M 81
CV 36,6,7
CV 37,8,9
M 78
ae 42,74,0
AR 6,40
M 79
W 1
AR 7,64
M 76
s_bitcmp1_b32 s50, 0
s_cselect_b32 s52, 24576, 0
v_add_u32 v50, s52, v3
ST 30,0
M 77
AR 8,88
AR 9,112
M 74
W 2
EF 6,52
M 75
EF 7,53
EF 8,54
M 72
WL 1
AN 112,114
AN 113,115
M 73
R 80
M 84
mw 48
SH 114
AN 114,114
M 85
WL 2
AN 115,115
AN 116,118
M 86
AN 117,119
SH 118
M 87
AN 118,118
AN 119,119
M 88
TY 122,3592
TY 126,17416
M 89
ad 84,96
W 0
M 90
EF 9,55
CV 38,6,7
M 91
ST 32,768
CV 39,8,9
M 92
W 1
AR 6,41
M 93
AR 7,65
AR 8,89
M 94
AR 9,113
EF 6,52
M 95
R 82
M 106
W 2
EF 7,53
M 107
WL 3
AN 120,122
AN 121,123
M 104
WL 2
SH 122
AN 122,122
M 105
mw 51
AN 123,123
AN 124,126
M 102
AN 125,127
SH 126
M 103
AN 126,126
AN 127,127
M 100
ad 84,102
W 0
M 101
EF 8,54
EF 9,55
M 98
ST 34,1536
CV 40,6,7
M 99
W 1
CV 41,8,9
M 96
AR 6,42
AR 7,66
M 97
R 0
M 108
W 2
AR 8,90
M 109
AR 9,114
EF 6,52
M 110
EF 7,53
EF 8,54
M 111
bc 49,2,66,60
EF 9,55
M 112
mh 18
CV 42,6,7
CV 43,8,9
M 113
ad 66,416
H 0
M 114
AR 6,43
AR 7,67
M 115
AR 8,91
AR 9,115
M 116
DG 1024
M 117
ST 36,2304
EF 6,52
M 118
EF 7,53
EF 8,54
M 119
WL 0
R 2
M 120
bc 48,2,64,56
EF 9,55
M 121
mh 21
CV 44,6,7
CV 45,8,9
M 133
ad 64,416
H 0
BV 52,512
M 132
TX 58,0
TX 62,13824
M 134
AR 6,132
AR 7,156
M 135
H 1
AR 8,180
M 123
EF 6,12
EF 7,13
M 122
TY 66,512
TY 70,14336
M 124
H 2
EF 8,46
M 125
EF 136,47
CV 14,6,7
M 137
ae 42,74,0
CV 15,8,136
M 136
WL 2
TX 74,1536
TX 78,15360
M 138
s_bfe_u32 s52, s50, 0x10001
Q 0
v_mad_i32_i24 v50, v49, s52, v48
M 139
Q 2
ST 38,0
M 127
Q 4
M 126
TY 82,2048
TY 86,15872
M 128
Q 6
M 129
WL 4
Q 8
M 141
Q 10
M 140
TX 90,3072
TX 94,16896
M 142
Q 12
M 143
R 6
M 131
Q 14
M 130
TY 98,3584
TY 102,17408
v_mfma_f32_16x16x32_f16 a[36:39], v[56:59], a[0:3], a[32:35]
R 4
M 1
WL 10
Q 16
)"
R"(v_mfma_f32_16x16x32_f16 a[60:63], v[60:63], a[12:15], a[32:35]
Q 18
ST 40,768
M 12
Q 20
v_mfma_f32_16x16x32_f16 a[64:67], v[64:67], a[8:11], a[32:35]
Q 22
M 15
WL 8
Q 24
v_mfma_f32_16x16x32_f16 a[40:43], v[68:71], a[4:7], a[32:35]
Q 26
M 2
Q 28
M 4
Q 30
M 5
WL 6
Q 32
M 17
Q 34
M 16
Q 36
M 18
Q 38
M 19
WL 1
Q 40
M 7
Q 42
M 6
Q 44
ST 42,1536
M 8
Q 46
M 9
AR 6,133
AR 7,157
M 21
AR 8,181
EF 6,12
M 20
EF 7,13
EF 8,46
M 22
EF 137,47
CV 16,6,7
M 23
R 10
M 11
CV 17,8,137
AR 6,134
M 10
WL 2
R 8
M 34
AR 7,158
AR 8,182
M 35
EF 6,12
EF 7,13
M 32
EF 8,46
EF 138,47
M 33
CV 18,6,7
CV 19,8,138
M 30
ST 44,2304
AR 6,135
M 31
AR 7,159
AR 8,183
M 28
EF 6,12
EF 7,13
M 29
EF 8,46
EF 139,47
v_mfma_f32_16x16x32_f16 a[88:91], v[64:67], a[16:19], a[32:35]
CV 20,6,7
CV 21,8,139
M 27
WV 10
AR 6,136
AR 7,160
AR 8,184
v_mfma_f32_16x16x32_f16 a[84:87], v[56:59], a[16:19], a[32:35]
WL 0
FP
EF 6,12
EF 7,13
M 25
EF 8,46
EF 140,47
CV 22,6,7
v_mfma_f32_16x16x32_f16 a[108:111], v[56:59], a[24:27], a[32:35]
CV 23,8,140
AR 6,137
AR 7,161
M 37
AR 8,185
EF 6,12
EF 7,13
v_mfma_f32_16x16x32_f16 a[112:115], v[64:67], a[24:27], a[32:35]
EF 8,46
EF 141,47
CV 24,6,7
M 39
CV 25,8,141
AR 6,138
AR 7,162
M 40
AR 8,186
EF 6,12
EF 7,13
M 41
ae 42,74,0
EF 8,46
EF 142,47
M 42
s_bitcmp1_b32 s50, 0
s_cselect_b32 s52, 24576, 0
v_add_u32 v50, s52, v3
ST 14,128
M 43
CV 26,6,7
CV 27,8,142
AR 6,139
M 44
AR 7,163
AR 8,187
EF 6,12
M 45
FR
EF 7,13
EF 8,46
M 46
EF 143,47
CV 28,6,7
CV 29,8,143
M 47
ds_read_b64 v[12:13], v4 offset:1536
ds_read_b64 v[46:47], v4 offset:1544
M 58
EF 160,52
EF 184,53
M 59
EF 208,54
EF 232,55
M 56
CV 30,160,184
CV 31,208,232
M 57
EF 161,52
EF 185,53
M 54
EF 209,54
EF 233,55
M 55
WL 2
FQ 80
CV 32,161,185
M 52
CV 33,209,233
EF 162,52
M 53
ST 16,896
EF 186,53
v_mfma_f32_16x16x32_f16 a[136:139], v[64:67], a[0:3], a[32:35]
FZ 80
R 24
M 51
mw 72
R 26
v_mfma_f32_16x16x32_f16 a[132:135], v[56:59], a[0:3], a[32:35]
TY 106,5128
TY 110,18952
M 49
R 28
v_mfma_f32_16x16x32_f16 a[156:159], v[56:59], a[8:11], a[32:35]
ad 84,144
W 0
M 61
EF 210,54
EF 234,55
v_mfma_f32_16x16x32_f16 a[160:163], v[64:67], a[8:11], a[32:35]
CV 34,162,186
CV 35,210,234
M 63
W 1
EF 163,52
M 64
EF 187,53
EF 211,54
M 65
EF 235,55
CV 36,163,187
M 66
W 2
CV 37,211,235
M 67
WL 3
AN 104,106
AN 105,107
EF 164,52
M 68
WL 2
SH 106
AN 106,106
ST 18,1664
M 69
mw 75
AN 107,107
AN 108,110
EF 188,53
M 70
AN 109,111
SH 110
EF 212,54
M 71
R 30
EF 236,55
M 82
AN 110,110
AN 111,111
CV 38,164,188
M 83
TY 114,6664
TY 118,20488
CV 39,212,236
M 80
ad 84,150
W 0
EF 165,52
M 81
EF 189,53
EF 213,54
EF 237,55
M 78
CV 40,165,189
CV 41,213,237
EF 166,52
M 79
W 1
EF 190,53
EF 214,54
M 76
EF 238,55
CV 42,166,190
CV 43,214,238
M 77
EF 167,52
EF 191,53
EF 215,54
v_mfma_f32_16x16x32_f16 a[184:187], v[64:67], a[16:19], a[32:35]
W 2
EF 239,55
CV 44,167,191
M 75
ST 20,2432
CV 45,215,239
v_mfma_f32_16x16x32_f16 a[180:183], v[56:59], a[16:19], a[32:35]
WL 1
AN 112,114
AN 113,115
BV 52,1792
M 73
R 32
v_mfma_f32_16x16x32_f16 v[136:139], v[56:59], a[24:27], v[128:131]
mw 0
SH 114
AN 114,114
M 85
WL 3
AN 115,115
AN 116,118
v_mfma_f32_16x16x32_f16 v[140:143], v[64:67], a[24:27], v[128:131]
AN 117,119
SH 118
M 87
AN 118,118
AN 119,119
M 88
TY 122,8200
TY 126,22024
M 89
ad 84,192
W 0
M 90
AR 6,44
AR 7,68
M 91
AR 8,92
AR 9,116
M 92
W 1
EF 6,12
M 93
EF 7,13
EF 8,46
M 94
ae 42,74,0
EF 9,47
M 95
R 34
M 106
WL 3
W 2
CV 14,6,7
M 107
s_bfe_u32 s52, s50, 0x10001
AN 120,122
AN 121,123
v_mad_i32_i24 v50, v49, s52, v48
M 104
WL 2
SH 122
AN 122,122
ST 22,128
M 105
mw 3
AN 123,123
AN 124,126
M 102
AN 125,127
SH 126
M 103
AN 126,126
AN 127,127
M 100
ad 84,198
W 0
M 101
CV 15,8,9
AR 6,45
v_mfma_f32_16x16x32_f16 v[164:167], v[64:67], a[0:3], v[128:131]
AR 7,69
AR 8,93
M 99
W 1
AR 9,117
v_mfma_f32_16x16x32_f16 v[160:163], v[56:59], a[0:3], v[128:131]
EF 6,12
EF 7,13
M 97
R 48
v_mfma_f32_16x16x32_f16 v[184:187], v[56:59], a[8:11], v[128:131]
W 2
EF 8,46
M 109
EF 9,47
CV 16,6,7
v_mfma_f32_16x16x32_f16 v[188:191], v[64:67], a[8:11], v[128:131]
CV 17,8,9
AR 6,46
M 111
bc 49,3,66,60
ST 24,896
M 112
mh 0
AR 7,70
AR 8,94
M 113
ad 66,608
H 0
M 114
AR 9,118
EF 6,12
M 115
EF 7,13
EF 8,46
M 116
DG 1024
M 117
EF 9,47
CV 18,6,7
M 118
CV 19,8,9
AR 6,47
M 119
WL 0
R 50
v_mfma_f32_16x16x32_f16 v[208:211], v[56:59], a[16:19], v[128:131]
bc 48,3,64,56
AR 7,71
M 121
mh 3
AR 8,95
AR 9,119
v_mfma_f32_16x16x32_f16 v[232:235], v[60:63], a[28:31], v[128:131]
ad 64,608
H 0
M 132
TX 58,4608
TX 62,18432
v_mfma_f32_16x16x32_f16 v[236:239], v[64:67], a[24:27], v[128:131]
ST 26,1664
EF 6,12
M 135
)"
R"(H 1
EF 7,13
v_mfma_f32_16x16x32_f16 v[212:215], v[68:71], a[20:23], v[128:131]
EF 8,46
EF 9,47
M 122
TY 66,5120
TY 70,18944
M 124
H 2
CV 20,6,7
M 125
CV 21,8,9
M 137
AR 6,48
AR 7,72
AR 8,96
AR 9,120
EF 6,12
EF 7,13
M 136
TX 74,6144
TX 78,19968
EF 8,46
EF 9,47
CV 6,6,7
CV 7,8,9
M 138
WL 4
Q 0
AW 200,6
AW 201,7
AR 6,49
AR 7,73
M 139
Q 2
AR 8,97
AR 9,121
EF 6,12
EF 7,13
M 127
Q 4
EF 8,46
EF 9,47
CV 6,6,7
CV 7,8,9
M 126
TY 82,6656
TY 86,20480
AW 202,6
AW 203,7
AR 6,50
AR 7,74
M 128
Q 6
AR 8,98
AR 9,122
EF 6,12
EF 7,13
M 129
WL 2
Q 8
ST 28,2432
EF 8,46
EF 9,47
CV 6,6,7
M 141
Q 10
CV 7,8,9
AW 204,6
AW 205,7
AR 6,51
M 140
TX 90,7680
TX 94,21504
AR 7,75
AR 8,99
AR 9,123
EF 6,12
M 142
Q 12
EF 7,13
EF 8,46
EF 9,47
CV 6,6,7
M 143
R 54
CV 7,8,9
AW 206,6
AW 207,7
M 131
Q 14
ds_read_b64 v[12:13], v4 offset:2048
M 130
TY 98,8192
TY 102,22016
ds_read_b64 v[46:47], v4 offset:2056
M 0
R 52
M 1
Q 16
M 13
Q 18
M 12
Q 20
M 14
Q 22
M 15
WL 8
Q 24
M 3
Q 26
M 2
Q 28
v_mfma_f32_16x16x32_f16 a[44:47], v[72:75], a[0:3], a[32:35]
Q 30
M 5
Q 32
v_mfma_f32_16x16x32_f16 a[68:71], v[76:79], a[12:15], a[32:35]
Q 34
M 16
Q 36
v_mfma_f32_16x16x32_f16 a[72:75], v[80:83], a[8:11], a[32:35]
Q 38
M 19
WL 2
Q 40
v_mfma_f32_16x16x32_f16 a[48:51], v[84:87], a[4:7], a[32:35]
Q 42
M 6
Q 44
M 8
Q 46
M 9
ae 42,74,0
M 21
s_bitcmp1_b32 s50, 0
s_cselect_b32 s52, 24576, 0
v_add_u32 v50, s52, v3
ST 30,256
M 20
AR 6,140
AR 7,164
M 22
AR 8,188
EF 6,52
M 23
R 58
M 11
EF 7,53
EF 8,54
M 10
WL 2
R 56
M 34
EF 144,55
CV 22,6,7
M 35
CV 23,8,144
AR 6,141
M 32
AR 7,165
AR 8,189
M 33
EF 6,52
EF 7,53
v_mfma_f32_16x16x32_f16 a[96:99], v[80:83], a[16:19], a[32:35]
EF 8,54
EF 145,55
M 31
CV 24,6,7
CV 25,8,145
v_mfma_f32_16x16x32_f16 a[92:95], v[72:75], a[16:19], a[32:35]
AR 6,142
AR 7,166
M 29
ST 32,1024
AR 8,190
M 26
EF 6,52
EF 7,53
M 27
WV 10
EF 8,54
EF 146,55
M 24
WL 0
FP
CV 26,6,7
M 25
CV 27,8,146
AR 6,143
M 36
AR 7,167
AR 8,191
M 37
EF 6,52
EF 7,53
M 38
EF 8,54
EF 147,55
M 39
CV 28,6,7
CV 29,8,147
v_mfma_f32_16x16x32_f16 a[116:119], v[72:75], a[24:27], a[32:35]
AR 6,144
AR 7,168
AR 8,192
EF 6,52
M 41
EF 7,53
EF 8,54
EF 148,55
CV 6,6,7
v_mfma_f32_16x16x32_f16 a[120:123], v[80:83], a[24:27], a[32:35]
CV 7,8,148
AW 208,6
AW 209,7
AR 6,145
M 43
AR 7,169
AR 8,193
EF 6,52
EF 7,53
M 44
ST 34,1792
EF 8,54
EF 149,55
CV 6,6,7
M 45
FR
CV 7,8,149
AW 210,6
AW 211,7
M 46
AR 6,146
AR 7,170
AR 8,194
EF 6,52
M 47
EF 7,53
EF 8,54
EF 150,55
CV 6,6,7
M 58
CV 7,8,150
AW 212,6
AW 213,7
AR 6,147
M 59
AR 7,171
AR 8,195
EF 6,52
EF 7,53
M 56
EF 8,54
EF 151,55
CV 6,6,7
CV 7,8,151
M 57
AW 214,6
AW 215,7
v_mfma_f32_16x16x32_f16 a[144:147], v[80:83], a[0:3], a[32:35]
BV 52,3072
EF 168,12
EF 192,13
M 55
WL 1
FQ 84
EF 216,46
EF 240,47
v_mfma_f32_16x16x32_f16 a[140:143], v[72:75], a[0:3], a[32:35]
CV 6,168,192
CV 7,216,240
AW 216,6
M 53
AW 217,7
EF 169,12
EF 193,13
M 50
FZ 84
R 72
EF 217,46
M 51
mw 24
R 74
ST 36,2560
M 48
TY 106,9736
TY 110,23560
EF 241,47
M 49
R 76
CV 6,169,193
M 60
ad 84,240
W 0
CV 7,217,241
M 61
AW 218,6
AW 219,7
EF 170,12
M 62
EF 194,13
EF 218,46
EF 242,47
M 63
W 1
CV 6,170,194
CV 7,218,242
v_mfma_f32_16x16x32_f16 a[164:167], v[72:75], a[8:11], a[32:35]
AW 220,6
AW 221,7
EF 171,12
M 65
EF 195,13
EF 219,46
EF 243,47
v_mfma_f32_16x16x32_f16 a[168:171], v[80:83], a[8:11], a[32:35]
W 2
CV 6,171,195
CV 7,219,243
M 67
WL 3
AN 104,106
AN 105,107
AW 222,6
M 68
WL 0
SH 106
AN 106,106
AW 223,7
M 69
mw 27
AN 107,107
AN 108,110
EF 172,12
M 70
AN 109,111
SH 110
EF 196,13
M 71
R 78
EF 220,46
M 82
AN 110,110
AN 111,111
EF 244,47
M 83
TY 114,11272
TY 118,25096
CV 30,172,196
M 80
ad 84,246
W 0
CV 31,220,244
M 81
ae 42,74,0
EF 173,12
EF 197,13
v_mfma_f32_16x16x32_f16 a[192:195], v[80:83], a[16:19], a[32:35]
s_bfe_u32 s52, s50, 0x10001
v_mad_i32_i24 v50, v49, s52, v48
ST 38,256
EF 221,46
M 79
W 1
EF 245,47
CV 32,173,197
v_mfma_f32_16x16x32_f16 a[188:191], v[72:75], a[16:19], a[32:35]
CV 33,221,245
EF 174,12
EF 198,13
M 77
EF 222,46
EF 246,47
CV 34,174,198
M 74
W 2
CV 35,222,246
EF 175,12
M 75
EF 199,13
EF 223,46
EF 247,47
M 72
WL 1
AN 112,114
AN 113,115
CV 36,175,199
M 73
R 80
CV 37,223,247
M 84
mw 48
SH 114
AN 114,114
ds_read_b64 v[12:13], v4 offset:3328
M 85
WL 3
)"
R"(AN 115,115
AN 116,118
ds_read_b64 v[46:47], v4 offset:3336
M 86
AN 117,119
SH 118
AR 6,52
M 87
AN 118,118
AN 119,119
AR 7,76
v_mfma_f32_16x16x32_f16 v[144:147], v[72:75], a[24:27], v[128:131]
TY 122,12808
TY 126,26632
AR 8,100
M 89
ad 84,288
W 0
ST 40,1024
v_mfma_f32_16x16x32_f16 v[148:151], v[80:83], a[24:27], v[128:131]
AR 9,124
EF 6,52
EF 7,53
M 91
EF 8,54
EF 9,55
CV 6,6,7
M 92
W 1
CV 7,8,9
AW 224,6
M 93
AW 225,7
AR 6,53
AR 7,77
M 94
AR 8,101
AR 9,125
EF 6,52
M 95
R 82
EF 7,53
M 106
W 2
EF 8,54
EF 9,55
M 107
WL 3
AN 120,122
AN 121,123
CV 6,6,7
M 104
WL 2
SH 122
AN 122,122
CV 7,8,9
M 105
mw 51
AN 123,123
AN 124,126
AW 226,6
v_mfma_f32_16x16x32_f16 v[172:175], v[80:83], a[0:3], v[128:131]
AN 125,127
SH 126
AW 227,7
M 103
AN 126,126
AN 127,127
AR 6,54
v_mfma_f32_16x16x32_f16 v[168:171], v[72:75], a[0:3], v[128:131]
ad 84,294
W 0
ST 42,1792
M 101
AR 7,78
AR 8,102
AR 9,126
M 98
EF 6,52
EF 7,53
EF 8,54
M 99
W 1
EF 9,55
CV 6,6,7
M 96
CV 7,8,9
AW 228,6
AW 229,7
M 97
R 0
AR 6,55
M 108
W 2
AR 7,79
AR 8,103
M 109
AR 9,127
EF 6,52
EF 7,53
M 110
EF 8,54
EF 9,55
CV 6,6,7
M 111
bc 49,4,66,60
CV 7,8,9
AW 230,6
v_mfma_f32_16x16x32_f16 v[192:195], v[72:75], a[8:11], v[128:131]
mh 9
AW 231,7
M 113
ad 66,800
H 0
AR 6,56
v_mfma_f32_16x16x32_f16 v[196:199], v[80:83], a[8:11], v[128:131]
AR 7,80
AR 8,104
AR 9,128
M 115
ST 44,2560
EF 6,52
EF 7,53
M 116
DG 1024
EF 8,54
M 117
EF 9,55
CV 6,6,7
CV 7,8,9
M 118
AW 232,6
AW 233,7
AR 6,57
M 119
WL 0
R 2
AR 7,81
M 120
bc 48,4,64,56
AR 8,105
AR 9,129
M 121
mh 12
EF 6,52
EF 7,53
EF 8,54
M 133
ad 64,800
H 0
EF 9,55
M 132
TX 58,9216
TX 62,23040
CV 6,6,7
M 134
CV 7,8,9
AW 234,6
AW 235,7
M 135
H 1
AR 6,58
AR 7,82
M 123
AR 8,106
AR 9,130
EF 6,52
M 122
TY 66,9728
TY 70,23552
EF 7,53
v_mfma_f32_16x16x32_f16 v[216:219], v[72:75], a[16:19], v[128:131]
H 2
EF 8,54
EF 9,55
M 125
ae 42,72,0
CV 6,6,7
CV 7,8,9
v_mfma_f32_16x16x32_f16 v[240:243], v[76:79], a[28:31], v[128:131]
s_bitcmp1_b32 s50, 2
s_cselect_b32 s52, 24576, 0
v_add_u32 v50, s52, v3
ST 14,0
M 136
TX 74,10752
TX 78,24576
PA 12288
DF
v_mfma_f32_16x16x32_f16 v[244:247], v[80:83], a[24:27], v[128:131]
WL 4
Q 0
AW 236,6
M 139
Q 2
AW 237,7
v_mfma_f32_16x16x32_f16 v[220:223], v[84:87], a[20:23], v[128:131]
Q 4
AR 6,59
M 126
TY 82,11264
TY 86,25088
AR 7,83
M 128
Q 6
AR 8,107
M 129
WL 4
Q 8
AR 9,131
M 141
Q 10
EF 6,52
M 140
TX 90,12288
TX 94,26112
EF 7,53
M 142
Q 12
EF 8,54
M 143
R 6
EF 9,55
M 131
Q 14
CV 6,6,7
M 130
TY 98,12800
TY 102,26624
ST 16,768
M 0
R 4
CV 7,8,9
M 1
WL 8
Q 16
AW 238,6
M 13
Q 18
AW 239,7
M 12
Q 20
BV 52,3584
M 14
Q 22
M 15
Q 24
M 3
Q 26
M 2
Q 28
M 4
Q 30
M 5
WL 5
Q 32
M 17
Q 34
M 16
Q 36
M 18
Q 38
ST 18,1536
M 19
WL 1
Q 40
M 7
Q 42
M 6
Q 44
v_mfma_f32_16x16x32_f16 a[52:55], v[88:91], a[0:3], a[32:35]
Q 46
M 9
AR 6,148
AR 7,172
v_mfma_f32_16x16x32_f16 a[76:79], v[92:95], a[12:15], a[32:35]
AR 8,196
EF 6,12
M 20
EF 7,13
EF 8,46
v_mfma_f32_16x16x32_f16 a[80:83], v[96:99], a[8:11], a[32:35]
EF 152,47
CV 38,6,7
M 23
R 10
v_mfma_f32_16x16x32_f16 a[56:59], v[100:103], a[4:7], a[32:35]
CV 39,8,152
AR 6,149
M 10
R 8
v_mfma_f32_16x16x32_f16 a[104:107], v[96:99], a[16:19], a[32:35]
AR 7,173
AR 8,197
M 35
ST 20,2304
EF 6,12
v_mfma_f32_16x16x32_f16 a[100:103], v[88:91], a[16:19], a[32:35]
EF 7,13
EF 8,46
M 33
EF 153,47
CV 40,6,7
M 30
CV 41,8,153
AR 6,150
M 31
AR 7,174
AR 8,198
M 28
EF 6,12
EF 7,13
M 29
EF 8,46
EF 154,47
M 26
CV 42,6,7
CV 43,8,154
M 27
WV 11
AR 6,151
AR 7,175
M 24
WL 0
FP
AR 8,199
M 25
EF 6,12
EF 7,13
M 36
EF 8,46
EF 155,47
M 37
CV 44,6,7
CV 45,8,155
M 38
ae 42,72,0
ST 22,128
AR 6,152
AR 7,176
M 39
EF 6,12
EF 7,13
EF 132,46
EF 156,47
M 40
CV 14,6,7
CV 15,132,156
AR 6,153
AR 7,177
M 41
EF 6,12
EF 7,13
EF 133,46
EF 157,47
M 42
CV 16,6,7
CV 17,133,157
AR 6,154
AR 7,178
M 43
EF 6,12
EF 7,13
EF 134,46
EF 158,47
v_mfma_f32_16x16x32_f16 a[124:127], v[88:91], a[24:27], a[32:35]
CV 18,6,7
CV 19,134,158
AR 6,155
AR 7,179
M 45
FR
EF 6,12
EF 7,13
EF 135,46
v_mfma_f32_16x16x32_f16 a[128:131], v[96:99], a[24:27], a[32:35]
EF 159,47
CV 20,6,7
CV 21,135,159
M 47
EF 176,52
EF 200,53
v_mfma_f32_16x16x32_f16 a[152:155], v[96:99], a[0:3], a[32:35]
EF 224,54
EF 248,55
M 59
CV 6,176,200
CV 7,224,248
v_mfma_f32_16x16x32_f16 a[148:151], v[88:91], a[0:3], a[32:35]
AW 240,6
AW 241,7
M 57
ST 24,896
EF 177,52
M 54
EF 201,53
EF 225,54
M 55
WL 0
FQ 88
)"
R"(EF 249,55
M 52
CV 6,177,201
CV 7,225,249
M 53
AW 242,6
AW 243,7
M 50
FZ 88
R 24
M 51
mw 72
R 26
M 48
TY 106,520
TY 110,14344
M 49
R 28
M 60
ad 84,336
W 0
M 61
EF 178,52
EF 202,53
M 62
EF 226,54
EF 250,55
M 63
W 1
CV 6,178,202
M 64
ST 26,1664
CV 7,226,250
M 65
AW 244,6
AW 245,7
M 66
W 2
EF 179,52
M 67
WL 3
AN 104,106
AN 105,107
v_mfma_f32_16x16x32_f16 a[172:175], v[88:91], a[8:11], a[32:35]
WL 2
SH 106
AN 106,106
M 69
mw 75
AN 107,107
AN 108,110
v_mfma_f32_16x16x32_f16 a[176:179], v[96:99], a[8:11], a[32:35]
AN 109,111
SH 110
M 71
R 30
v_mfma_f32_16x16x32_f16 v[132:135], v[96:99], a[16:19], v[128:131]
AN 110,110
AN 111,111
M 83
TY 114,2056
TY 118,15880
v_mfma_f32_16x16x32_f16 a[196:199], v[88:91], a[16:19], a[32:35]
ad 84,342
W 0
M 81
EF 203,53
EF 227,54
M 78
EF 251,55
CV 6,179,203
M 79
W 1
ST 28,2432
M 76
CV 7,227,251
AW 246,6
M 77
AW 247,7
M 74
W 2
EF 180,52
EF 204,53
EF 228,54
M 75
EF 252,55
CV 6,180,204
CV 7,228,252
AW 248,6
M 72
WL 1
AN 112,114
AN 113,115
AW 249,7
EF 181,52
M 73
R 32
EF 205,53
EF 229,54
M 84
mw 0
SH 114
AN 114,114
EF 253,55
CV 6,181,205
M 85
WL 2
AN 115,115
AN 116,118
CV 7,229,253
AW 250,6
M 86
AN 117,119
SH 118
AW 251,7
EF 182,52
M 87
AN 118,118
AN 119,119
EF 206,53
EF 230,54
M 88
TY 122,3592
TY 126,17416
EF 254,55
CV 6,182,206
M 89
ad 84,384
W 0
CV 7,230,254
AW 252,6
M 90
ae 42,72,0
AW 253,7
EF 183,52
EF 207,53
M 91
s_bfe_u32 s52, s50, 0x10003
v_mad_i32_i24 v50, v49, s52, v48
ST 30,256
EF 231,54
EF 255,55
v_mfma_f32_16x16x32_f16 v[152:155], v[88:91], a[24:27], v[128:131]
W 1
CV 6,183,207
CV 7,231,255
AW 254,6
M 93
AW 255,7
v_mfma_f32_16x16x32_f16 v[156:159], v[96:99], a[24:27], v[128:131]
M 95
R 34
v_mfma_f32_16x16x32_f16 v[180:183], v[96:99], a[0:3], v[128:131]
W 2
M 107
WL 3
AN 120,122
AN 121,123
v_mfma_f32_16x16x32_f16 v[176:179], v[88:91], a[0:3], v[128:131]
WL 2
SH 122
AN 122,122
M 105
mw 3
AN 123,123
AN 124,126
M 102
AN 125,127
SH 126
M 103
AN 126,126
AN 127,127
M 100
ad 84,390
W 0
M 101
M 98
ST 32,1024
M 99
W 1
M 96
M 97
R 48
M 108
W 2
M 109
M 110
M 111
bc 49,5,66,60
M 112
mh 18
M 113
ad 66,992
H 0
M 114
M 115
v_mfma_f32_16x16x32_f16 v[200:203], v[88:91], a[8:11], v[128:131]
DG 1024
M 117
ST 34,1792
v_mfma_f32_16x16x32_f16 v[204:207], v[96:99], a[8:11], v[128:131]
M 119
WL 2
R 50
M 120
bc 48,5,64,56
M 121
mh 21
M 133
ad 64,992
H 0
M 132
TX 58,0
TX 62,13824
M 134
M 135
H 1
M 123
M 122
TY 66,512
TY 70,14336
M 124
H 2
M 125
M 137
ST 36,2560
M 136
TX 74,1536
TX 78,15360
M 138
WL 4
Q 0
M 139
Q 2
M 127
Q 4
M 126
TY 82,2048
TY 86,15872
v_mfma_f32_16x16x32_f16 v[224:227], v[88:91], a[16:19], v[128:131]
Q 6
M 129
WL 4
Q 8
v_mfma_f32_16x16x32_f16 v[248:251], v[92:95], a[28:31], v[128:131]
Q 10
M 140
TX 90,3072
TX 94,16896
v_mfma_f32_16x16x32_f16 v[252:255], v[96:99], a[24:27], v[128:131]
Q 12
M 143
R 54
v_mfma_f32_16x16x32_f16 v[228:231], v[100:103], a[20:23], v[128:131]
Q 14
M 130
TY 98,3584
TY 102,17408
M 0
R 52
M 1
WL 10
Q 16
M 13
Q 18
M 12
Q 20
M 14
Q 22
M 15
WL 8
Q 24
M 3
Q 26
M 2
Q 28
M 4
Q 30
M 5
WL 6
Q 32
M 17
Q 34
M 16
Q 36
M 18
Q 38
M 19
WL 2
Q 40
M 7
Q 42
M 6
Q 44
M 8
Q 46
M 9
ae 42,72,96
M 21
s_bitcmp1_b32 s50, 4
s_cselect_b32 s52, 24576, 0
v_add_u32 v50, s52, v3
ST 38,128
M 20
M 22
M 23
R 58
M 11
M 10
WL 2
R 56
M 34
M 35
M 32
M 33
M 30
M 31
M 28
M 29
ST 40,896
M 26
M 27
WV 9
M 24
WL 0
FP
M 25
M 36
M 37
M 38
M 39
M 40
M 41
M 42
M 43
M 44
ST 42,1664
M 45
FR
M 46
M 47
M 58
M 59
M 56
M 57
M 54
M 55
WL 0
FQ 92
M 52
M 53
M 50
FZ 92
R 72
M 51
mw 24
R 74
ST 44,2432
M 48
TY 106,5128
TY 110,18952
M 49
R 76
M 60
ad 84,432
W 0
M 61
M 62
M 63
W 1
M 64
M 65
M 66
W 2
M 67
WL 3
AN 104,106
AN 105,107
M 68
WL 2
SH 106
AN 106,106
M 69
mw 27
AN 107,107
AN 108,110
M 70
AN 109,111
SH 110
M 71
R 78
M 82
AN 110,110
AN 111,111
M 83
TY 114,6664
TY 118,20488
M 80
ad 84,438
W 0
)"
R"(M 81
ae 42,72,96
M 78
s_bfe_u32 s52, s50, 0x10005
v_mad_i32_i24 v50, v49, s52, v48
ST 14,128
M 79
W 1
M 76
M 77
M 74
W 2
M 75
M 72
WL 1
AN 112,114
AN 113,115
M 73
R 80
M 84
mw 48
SH 114
AN 114,114
M 85
WL 2
AN 115,115
AN 116,118
M 86
AN 117,119
SH 118
M 87
AN 118,118
AN 119,119
M 88
TY 122,8200
TY 126,22024
M 89
ad 84,480
W 0
ST 16,896
M 90
M 91
M 92
W 1
M 93
M 94
M 95
R 82
M 106
W 2
M 107
WL 3
AN 120,122
AN 121,123
M 104
WL 2
SH 122
AN 122,122
M 105
mw 51
AN 123,123
AN 124,126
M 102
AN 125,127
SH 126
M 103
AN 126,126
AN 127,127
M 100
ad 84,486
W 0
ST 18,1664
M 101
M 98
M 99
W 1
M 96
M 97
R 0
M 108
W 2
M 109
M 110
M 111
bc 49,6,66,60
M 112
mh 0
M 113
ad 66,1184
H 0
M 114
M 115
ST 20,2432
M 116
DG 1024
M 117
M 118
M 119
WL 2
R 2
M 120
bc 48,6,64,56
M 121
mh 3
M 133
ad 64,1184
H 0
M 132
TX 58,4608
TX 62,18432
M 134
M 135
H 1
M 123
M 122
TY 66,5120
TY 70,18944
M 124
H 2
M 125
ae 44,70,384
M 137
s_bitcmp1_b32 s50, 6
s_cselect_b32 s51, 24576, 0
v_add_u32 v47, s51, v3
M 136
TX 74,6144
TX 78,19968
PA 12288
DF
M 138
WL 4
Q 0
M 139
Q 2
M 127
Q 4
M 126
TY 82,6656
TY 86,20480
M 128
Q 6
M 129
WL 4
Q 8
M 141
Q 10
M 140
TX 90,7680
TX 94,21504
M 142
Q 12
M 143
R 6
M 131
Q 14
M 130
TY 98,8192
TY 102,22016
M 0
R 4
M 1
WL 10
Q 16
M 13
Q 18
M 12
Q 20
M 14
Q 22
M 15
WL 8
Q 24
M 3
Q 26
M 2
Q 28
M 4
Q 30
M 5
WL 6
Q 32
M 17
Q 34
M 16
Q 36
M 18
Q 38
M 19
WL 2
Q 40
M 7
Q 42
M 6
Q 44
M 8
Q 46
M 9
XL 22,0
M 21
XL 24,768
M 20
XL 26,1536
M 22
XL 28,2304
M 23
R 10
M 11
M 10
WL 2
R 8
M 34
M 35
M 32
M 33
M 30
M 31
M 28
M 29
M 26
M 27
WV 11
M 24
WL 0
FP
M 25
M 36
ae 42,72,0
M 37
s_bfe_u32 s52, s50, 0x10003
v_mad_i32_i24 v50, v49, s52, v48
SA 200,0
M 38
M 39
M 40
M 41
M 42
M 43
M 44
M 45
FR
M 46
M 47
M 58
M 59
M 56
M 57
M 54
M 55
WL 0
FQ 96
M 52
M 53
M 50
FZ 96
R 24
M 51
mw 72
R 26
M 48
TY 106,9736
TY 110,23560
M 49
R 28
M 60
ad 84,528
W 0
M 61
M 62
M 63
W 1
M 64
M 65
M 66
W 2
M 67
WL 3
AN 104,106
AN 105,107
M 68
WL 2
SH 106
AN 106,106
M 69
mw 75
AN 107,107
AN 108,110
M 70
AN 109,111
SH 110
M 71
R 30
M 82
AN 110,110
AN 111,111
M 83
TY 114,11272
TY 118,25096
M 80
ad 84,534
W 0
M 81
M 78
M 79
W 1
M 76
M 77
M 74
W 2
M 75
M 72
WL 1
AN 112,114
AN 113,115
M 73
R 32
M 84
mw 0
SH 114
AN 114,114
M 85
WL 2
AN 115,115
AN 116,118
M 86
AN 117,119
SH 118
M 87
AN 118,118
AN 119,119
M 88
TY 122,12808
TY 126,26632
M 89
ad 84,576
W 0
M 90
M 91
M 92
W 1
M 93
M 94
M 95
R 34
M 106
W 2
M 107
WL 3
AN 120,122
AN 121,123
M 104
WL 2
SH 122
AN 122,122
M 105
mw 3
AN 123,123
AN 124,126
M 102
AN 125,127
SH 126
M 103
AN 126,126
AN 127,127
M 100
ad 84,582
W 0
M 101
SA 202,768
M 98
M 99
W 1
M 96
M 97
R 48
M 108
W 2
M 109
M 110
M 111
bc 49,7,66,60
M 112
mh 9
M 113
ad 66,1376
H 0
M 114
M 115
M 116
DG 1024
M 117
M 118
M 119
WL 2
R 50
M 120
bc 48,7,64,56
M 121
mh 12
M 133
)"
R"(ad 64,1376
H 0
M 132
TX 58,9216
TX 62,23040
M 134
M 135
H 1
M 123
M 122
TY 66,9728
TY 70,23552
M 124
H 2
M 125
M 137
ae 44,70,384
M 136
TX 74,10752
TX 78,24576
M 138
s_bfe_u32 s51, s50, 0x10007
WL 4
Q 0
M 139
Q 2
M 127
Q 4
M 126
TY 82,11264
TY 86,25088
M 128
Q 6
M 129
WL 4
Q 8
M 141
Q 10
M 140
TX 90,12288
TX 94,26112
M 142
Q 12
M 143
R 54
M 131
Q 14
M 130
TY 98,12800
TY 102,26624
M 0
R 52
M 1
WL 10
Q 16
M 13
Q 18
M 12
Q 20
M 14
Q 22
M 15
WL 8
Q 24
M 3
Q 26
M 2
Q 28
M 4
Q 30
M 5
WL 6
Q 32
M 17
Q 34
M 16
Q 36
M 18
Q 38
M 19
WL 2
Q 40
M 7
Q 42
M 6
Q 44
M 8
Q 46
M 9
v_mad_i32_i24 v47, v49, s51, v48
XL 30,0
M 21
XL 32,768
M 20
XL 34,1536
M 22
XL 36,2304
M 23
R 58
M 11
M 10
WL 2
R 56
M 34
SA 204,1536
M 35
M 32
M 33
M 30
M 31
M 28
M 29
M 26
M 27
WV 10
M 24
WL 0
FP
M 25
M 36
M 37
M 38
M 39
M 40
UL 6,22
UH 7,22
M 41
AR 8,36
AR 9,60
M 42
AD 8,6
AD 9,7
M 43
AW 36,8
AW 60,9
M 44
UL 6,23
UH 7,23
M 45
FR
AR 8,84
M 46
AR 9,108
AD 8,6
M 47
AD 9,7
AW 84,8
M 58
AW 108,9
UL 6,24
M 59
UH 7,24
AR 8,37
M 56
AR 9,61
AD 8,6
M 57
AD 9,7
AW 37,8
M 54
AW 61,9
UL 6,25
M 55
WL 0
FQ 100
UH 7,25
M 52
AR 8,85
AR 9,109
M 53
AD 8,6
AD 9,7
M 50
FZ 100
R 72
M 51
mw 24
R 74
M 48
TY 106,520
TY 110,14344
M 49
R 76
M 60
ad 84,624
W 0
M 61
AW 85,8
AW 109,9
M 62
UL 6,26
UH 7,26
M 63
W 1
AR 8,38
M 64
AR 9,62
AD 8,6
M 65
AD 9,7
AW 38,8
M 66
W 2
AW 62,9
M 67
WL 3
AN 104,106
AN 105,107
M 68
WL 2
SH 106
AN 106,106
M 69
mw 27
AN 107,107
AN 108,110
M 70
AN 109,111
SH 110
M 71
R 78
M 82
AN 110,110
AN 111,111
M 83
TY 114,2056
TY 118,15880
M 80
ad 84,630
W 0
M 81
UL 6,27
UH 7,27
M 78
AR 8,86
AR 9,110
M 79
W 1
AD 8,6
M 76
AD 9,7
AW 86,8
M 77
AW 110,9
UL 6,28
M 74
W 2
UH 7,28
M 75
AR 8,39
AR 9,63
M 72
WL 1
AN 112,114
AN 113,115
M 73
R 80
M 84
mw 48
SH 114
AN 114,114
M 85
WL 2
AN 115,115
AN 116,118
M 86
AN 117,119
SH 118
M 87
AN 118,118
AN 119,119
M 88
TY 122,3592
TY 126,17416
M 89
ad 84,672
W 0
M 90
SA 206,2304
AD 8,6
M 91
AD 9,7
AW 39,8
M 92
W 1
AW 63,9
M 93
UL 6,29
UH 7,29
M 94
AR 8,87
AR 9,111
M 95
R 82
M 106
W 2
AD 8,6
M 107
WL 3
AN 120,122
AN 121,123
M 104
WL 2
SH 122
AN 122,122
M 105
mw 51
AN 123,123
AN 124,126
M 102
AN 125,127
SH 126
M 103
AN 126,126
AN 127,127
M 100
ad 84,678
W 0
M 101
AD 9,7
AW 87,8
M 98
AW 111,9
M 99
W 1
M 96
M 97
R 0
M 108
W 2
M 109
M 110
M 111
bc 49,8,66,60
M 112
mh 18
M 113
ad 66,1568
H 0
M 114
M 115
M 116
DG 1024
M 117
M 118
M 119
WL 2
R 2
M 120
bc 48,8,64,56
M 121
mh 21
M 133
ad 64,1568
H 0
M 132
TX 58,0
TX 62,13824
M 134
M 135
H 1
M 123
M 122
TY 66,512
TY 70,14336
M 124
H 2
M 125
M 137
M 136
TX 74,1536
TX 78,15360
PA 12288
DF
M 138
WL 4
Q 0
M 139
Q 2
M 127
Q 4
M 126
TY 82,2048
TY 86,15872
M 128
Q 6
M 129
WL 4
Q 8
M 141
Q 10
M 140
TX 90,3072
TX 94,16896
M 142
Q 12
M 143
R 6
M 131
Q 14
)"
R"(M 130
TY 98,3584
TY 102,17408
M 0
R 4
M 1
WL 10
Q 16
M 13
Q 18
M 12
Q 20
M 14
Q 22
M 15
WL 8
Q 24
M 3
Q 26
M 2
Q 28
M 4
Q 30
M 5
WL 6
Q 32
M 17
Q 34
M 16
Q 36
M 18
Q 38
M 19
WL 2
Q 40
M 7
Q 42
M 6
Q 44
M 8
Q 46
M 9
ae 42,72,0
M 21
SA 208,128
M 20
ae 44,68,384
M 22
s_bitcmp1_b32 s50, 8
s_cselect_b32 s51, 24576, 0
v_add_u32 v47, s51, v3
M 23
R 10
M 11
XL 38,0
M 10
WL 2
R 8
M 34
XL 40,768
M 35
XL 42,1536
M 32
XL 44,2304
M 33
M 30
M 31
M 28
M 29
M 26
M 27
WV 11
M 24
WL 0
FP
M 25
M 36
M 37
M 38
M 39
M 40
M 41
M 42
UL 6,30
UH 7,30
M 43
AR 8,40
AR 9,64
M 44
AD 8,6
AD 9,7
M 45
FR
AW 40,8
M 46
AW 64,9
UL 6,31
M 47
UH 7,31
AR 8,88
M 58
AR 9,112
AD 8,6
M 59
AD 9,7
AW 88,8
M 56
AW 112,9
UL 6,32
M 57
UH 7,32
AR 8,41
M 54
AR 9,65
AD 8,6
M 55
WL 0
FQ 104
AD 9,7
M 52
AW 41,8
AW 65,9
M 53
UL 6,33
UH 7,33
M 50
FZ 104
R 24
M 51
mw 72
R 26
M 48
TY 106,5128
TY 110,18952
M 49
R 28
M 60
ad 84,720
W 0
M 61
AR 8,89
AR 9,113
M 62
AD 8,6
AD 9,7
M 63
W 1
AW 89,8
M 64
AW 113,9
UL 6,34
M 65
UH 7,34
AR 8,42
M 66
W 2
AR 9,66
M 67
WL 3
AN 104,106
AN 105,107
M 68
WL 2
SH 106
AN 106,106
M 69
mw 75
AN 107,107
AN 108,110
M 70
AN 109,111
SH 110
M 71
R 30
M 82
AN 110,110
AN 111,111
M 83
TY 114,6664
TY 118,20488
M 80
ad 84,726
W 0
M 81
AD 8,6
AD 9,7
M 78
AW 42,8
AW 66,9
M 79
W 1
UL 6,35
M 76
UH 7,35
AR 8,90
M 77
AR 9,114
AD 8,6
M 74
W 2
AD 9,7
M 75
AW 90,8
AW 114,9
M 72
WL 1
AN 112,114
AN 113,115
M 73
R 32
M 84
mw 0
SH 114
AN 114,114
M 85
WL 2
AN 115,115
AN 116,118
M 86
AN 117,119
SH 118
M 87
AN 118,118
AN 119,119
M 88
TY 122,8200
TY 126,22024
M 89
ad 84,768
W 0
M 90
SA 210,896
UL 6,36
M 91
UH 7,36
AR 8,43
M 92
W 1
AR 9,67
M 93
AD 8,6
AD 9,7
M 94
AW 43,8
AW 67,9
M 95
R 34
M 106
W 2
UL 6,37
M 107
WL 3
AN 120,122
AN 121,123
M 104
WL 2
SH 122
AN 122,122
M 105
mw 3
AN 123,123
AN 124,126
M 102
AN 125,127
SH 126
M 103
AN 126,126
AN 127,127
M 100
ad 84,774
W 0
M 101
UH 7,37
AR 8,91
M 98
AR 9,115
AD 8,6
M 99
W 1
AD 9,7
M 96
AW 91,8
AW 115,9
M 97
R 48
M 108
W 2
M 109
M 110
M 111
bc 49,9,66,60
M 112
mh 0
M 113
ad 66,1760
H 0
M 114
M 115
M 116
DG 1024
M 117
M 118
M 119
WL 2
R 50
M 120
bc 48,9,64,56
M 121
mh 3
M 133
ad 64,1760
H 0
M 132
TX 58,4608
TX 62,18432
M 134
M 135
H 1
M 123
M 122
TY 66,5120
TY 70,18944
M 124
H 2
M 125
M 137
M 136
TX 74,6144
TX 78,19968
M 138
WL 4
Q 0
M 139
Q 2
M 127
Q 4
M 126
TY 82,6656
TY 86,20480
M 128
Q 6
M 129
WL 4
Q 8
M 141
Q 10
M 140
TX 90,7680
TX 94,21504
M 142
Q 12
M 143
R 54
M 131
Q 14
M 130
TY 98,8192
TY 102,22016
M 0
R 52
M 1
WL 10
Q 16
M 13
Q 18
M 12
Q 20
M 14
Q 22
M 15
WL 8
Q 24
M 3
Q 26
M 2
Q 28
M 4
Q 30
M 5
WL 6
Q 32
M 17
Q 34
M 16
Q 36
M 18
Q 38
M 19
WL 2
Q 40
M 7
Q 42
M 6
Q 44
M 8
Q 46
M 9
SA 212,1664
M 21
ae 44,68,384
)"
R"(M 20
s_bfe_u32 s51, s50, 0x10009
v_mad_i32_i24 v47, v49, s51, v48
XL 14,0
M 22
XL 16,768
M 23
R 58
M 11
XL 18,1536
M 10
WL 2
R 56
M 34
XL 20,2304
M 35
M 32
M 33
M 30
M 31
M 28
M 29
M 26
M 27
WV 10
M 24
WL 0
FP
M 25
M 36
M 37
M 38
M 39
M 40
M 41
M 42
M 43
M 44
UL 6,38
UH 7,38
M 45
FR
AR 8,44
M 46
AR 9,68
AD 8,6
M 47
AD 9,7
AW 44,8
M 58
AW 68,9
UL 6,39
M 59
UH 7,39
AR 8,92
M 56
AR 9,116
AD 8,6
M 57
AD 9,7
AW 92,8
M 54
AW 116,9
UL 6,40
M 55
WL 0
FQ 108
UH 7,40
M 52
AR 8,45
AR 9,69
M 53
AD 8,6
AD 9,7
M 50
FZ 108
R 72
M 51
mw 24
R 74
M 48
TY 106,9736
TY 110,23560
M 49
R 76
M 60
ad 84,816
W 0
M 61
AW 45,8
AW 69,9
M 62
UL 6,41
UH 7,41
M 63
W 1
AR 8,93
M 64
AR 9,117
AD 8,6
M 65
AD 9,7
AW 93,8
M 66
W 2
AW 117,9
M 67
WL 3
AN 104,106
AN 105,107
M 68
WL 2
SH 106
AN 106,106
M 69
mw 27
AN 107,107
AN 108,110
M 70
AN 109,111
SH 110
M 71
R 78
M 82
AN 110,110
AN 111,111
M 83
TY 114,11272
TY 118,25096
M 80
ad 84,822
W 0
M 81
UL 6,42
UH 7,42
M 78
AR 8,46
AR 9,70
M 79
W 1
AD 8,6
M 76
AD 9,7
AW 46,8
M 77
AW 70,9
UL 6,43
M 74
W 2
UH 7,43
M 75
SA 214,2432
AR 8,94
M 72
WL 1
AN 112,114
AN 113,115
M 73
R 80
M 84
mw 48
SH 114
AN 114,114
M 85
WL 2
AN 115,115
AN 116,118
M 86
AN 117,119
SH 118
M 87
AN 118,118
AN 119,119
M 88
TY 122,12808
TY 126,26632
M 89
ad 84,864
W 0
M 90
AR 9,118
AD 8,6
M 91
AD 9,7
AW 94,8
M 92
W 1
AW 118,9
M 93
UL 6,44
UH 7,44
M 94
AR 8,47
AR 9,71
M 95
R 82
M 106
W 2
AD 8,6
M 107
WL 3
AN 120,122
AN 121,123
M 104
WL 2
SH 122
AN 122,122
M 105
mw 51
AN 123,123
AN 124,126
M 102
AN 125,127
SH 126
M 103
AN 126,126
AN 127,127
M 100
ad 84,870
W 0
M 101
AD 9,7
AW 47,8
M 98
AW 71,9
UL 6,45
M 99
W 1
UH 7,45
M 96
AR 8,95
AR 9,119
M 97
R 0
M 108
W 2
AD 8,6
M 109
AD 9,7
AW 95,8
M 110
AW 119,9
M 111
bc 49,10,66,60
M 112
mh 9
M 113
ad 66,1952
H 0
M 114
M 115
M 116
DG 1024
M 117
M 118
M 119
WL 2
R 2
M 120
bc 48,10,64,56
M 121
mh 12
M 133
ad 64,1952
H 0
M 132
TX 58,9216
TX 62,23040
M 134
M 135
H 1
M 123
M 122
TY 66,9728
TY 70,23552
M 124
H 2
M 125
M 137
M 136
TX 74,10752
TX 78,24576
PA 12288
DF
M 138
WL 4
Q 0
M 139
Q 2
M 127
Q 4
M 126
TY 82,11264
TY 86,25088
M 128
Q 6
M 129
WL 4
Q 8
M 141
Q 10
M 140
TX 90,12288
TX 94,26112
M 142
Q 12
M 143
R 6
M 131
Q 14
M 130
TY 98,12800
TY 102,26624
M 0
R 4
M 1
WL 10
Q 16
M 13
Q 18
M 12
Q 20
M 14
Q 22
M 15
WL 8
Q 24
M 3
Q 26
M 2
Q 28
M 4
Q 30
M 5
WL 6
Q 32
M 17
Q 34
M 16
Q 36
M 18
Q 38
M 19
WL 2
Q 40
M 7
Q 42
M 6
Q 44
M 8
Q 46
M 9
ae 42,72,0
M 21
s_bitcmp1_b32 s50, 2
s_cselect_b32 s52, 24576, 0
v_add_u32 v50, s52, v3
M 20
SA 216,256
M 22
ae 44,68,480
M 23
R 10
M 11
s_bitcmp1_b32 s50, 10
s_cselect_b32 s51, 24576, 0
v_add_u32 v47, s51, v3
M 10
WL 2
R 8
M 34
XL 22,0
M 35
XL 24,768
M 32
XL 26,1536
M 33
XL 28,2304
M 30
M 31
M 28
M 29
M 26
M 27
WV 11
M 24
WL 0
FP
M 25
M 36
M 37
M 38
M 39
M 40
M 41
M 42
M 43
M 44
M 45
FR
)"
R"(M 46
UL 6,14
UH 7,14
M 47
AR 8,48
AR 9,72
M 58
AD 8,6
AD 9,7
M 59
AW 48,8
AW 72,9
M 56
UL 6,15
UH 7,15
M 57
AR 8,96
AR 9,120
M 54
AD 8,6
AD 9,7
M 55
WL 0
FQ 112
AW 96,8
M 52
AW 120,9
UL 6,16
M 53
UH 7,16
AR 8,49
M 50
FZ 112
R 24
M 51
mw 72
R 26
M 48
TY 106,520
TY 110,14344
M 49
R 28
M 60
ad 84,912
W 0
M 61
AR 9,73
AD 8,6
M 62
AD 9,7
AW 49,8
M 63
W 1
AW 73,9
M 64
UL 6,17
UH 7,17
M 65
AR 8,97
AR 9,121
M 66
W 2
AD 8,6
M 67
WL 3
AN 104,106
AN 105,107
M 68
WL 2
SH 106
AN 106,106
M 69
mw 75
AN 107,107
AN 108,110
M 70
AN 109,111
SH 110
M 71
R 30
M 82
AN 110,110
AN 111,111
M 83
TY 114,2056
TY 118,15880
M 80
ad 84,918
W 0
M 81
AD 9,7
AW 97,8
M 78
AW 121,9
UL 6,18
M 79
W 1
UH 7,18
M 76
AR 8,50
AR 9,74
M 77
AD 8,6
AD 9,7
M 74
W 2
AW 50,8
M 75
AW 74,9
UL 6,19
M 72
WL 1
AN 112,114
AN 113,115
M 73
R 32
M 84
mw 0
SH 114
AN 114,114
M 85
WL 2
AN 115,115
AN 116,118
M 86
AN 117,119
SH 118
M 87
AN 118,118
AN 119,119
M 88
TY 122,3592
TY 126,17416
M 89
ad 84,960
W 0
M 90
SA 218,1024
UH 7,19
M 91
AR 8,98
AR 9,122
M 92
W 1
AD 8,6
M 93
AD 9,7
AW 98,8
M 94
AW 122,9
UL 6,20
M 95
R 34
M 106
W 2
UH 7,20
M 107
WL 3
AN 120,122
AN 121,123
M 104
WL 2
SH 122
AN 122,122
M 105
mw 3
AN 123,123
AN 124,126
M 102
AN 125,127
SH 126
M 103
AN 126,126
AN 127,127
M 100
ad 84,966
W 0
M 101
AR 8,51
AR 9,75
M 98
AD 8,6
AD 9,7
M 99
W 1
AW 51,8
M 96
AW 75,9
UL 6,21
M 97
R 48
M 108
W 2
UH 7,21
M 109
AR 8,99
AR 9,123
M 110
AD 8,6
AD 9,7
M 111
bc 49,11,66,60
AW 99,8
M 112
mh 18
AW 123,9
M 113
ad 66,2144
H 0
M 114
M 115
M 116
DG 1024
M 117
M 118
M 119
WL 2
R 50
M 120
bc 48,11,64,56
M 121
mh 21
M 133
ad 64,2144
H 0
M 132
TX 58,0
TX 62,13824
M 134
M 135
H 1
M 123
M 122
TY 66,512
TY 70,14336
M 124
H 2
M 125
M 137
M 136
TX 74,1536
TX 78,15360
M 138
WL 4
Q 0
M 139
Q 2
M 127
Q 4
M 126
TY 82,2048
TY 86,15872
M 128
Q 6
M 129
WL 4
Q 8
M 141
Q 10
M 140
TX 90,3072
TX 94,16896
M 142
Q 12
M 143
R 54
M 131
Q 14
M 130
TY 98,3584
TY 102,17408
M 0
R 52
M 1
WL 10
Q 16
M 13
Q 18
M 12
Q 20
M 14
Q 22
M 15
WL 8
Q 24
M 3
Q 26
M 2
Q 28
M 4
Q 30
M 5
WL 6
Q 32
M 17
Q 34
M 16
Q 36
M 18
Q 38
M 19
WL 2
Q 40
M 7
Q 42
M 6
Q 44
M 8
Q 46
M 9
SA 220,1792
M 21
ae 44,68,480
M 20
s_bfe_u32 s51, s50, 0x1000b
v_mad_i32_i24 v47, v49, s51, v48
XL 30,0
M 22
XL 32,768
M 23
R 58
M 11
XL 34,1536
M 10
WL 2
R 56
M 34
XL 36,2304
M 35
M 32
M 33
M 30
M 31
M 28
M 29
M 26
M 27
WV 10
M 24
WL 0
FP
M 25
M 36
M 37
M 38
M 39
M 40
M 41
M 42
M 43
M 44
M 45
FR
M 46
M 47
M 58
UL 6,22
UH 7,22
M 59
AR 8,52
AR 9,76
M 56
AD 8,6
AD 9,7
M 57
AW 52,8
AW 76,9
M 54
UL 6,23
UH 7,23
M 55
WL 0
FQ 116
AR 8,100
M 52
AR 9,124
AD 8,6
M 53
AD 9,7
AW 100,8
M 50
FZ 116
R 72
M 51
mw 24
R 74
M 48
TY 106,5128
TY 110,18952
M 49
R 76
M 60
ad 84,1008
W 0
M 61
AW 124,9
UL 6,24
M 62
UH 7,24
AR 8,53
)"
R"(M 63
W 1
AR 9,77
M 64
AD 8,6
AD 9,7
M 65
AW 53,8
AW 77,9
M 66
W 2
UL 6,25
M 67
WL 3
AN 104,106
AN 105,107
M 68
WL 2
SH 106
AN 106,106
M 69
mw 27
AN 107,107
AN 108,110
M 70
AN 109,111
SH 110
M 71
R 78
M 82
AN 110,110
AN 111,111
M 83
TY 114,6664
TY 118,20488
M 80
ad 84,1014
W 0
M 81
UH 7,25
AR 8,101
M 78
AR 9,125
AD 8,6
M 79
W 1
AD 9,7
M 76
AW 101,8
AW 125,9
M 77
UL 6,26
UH 7,26
M 74
W 2
AR 8,54
M 75
SA 222,2560
AR 9,78
M 72
WL 1
AN 112,114
AN 113,115
M 73
R 80
M 84
mw 48
SH 114
AN 114,114
M 85
WL 2
AN 115,115
AN 116,118
M 86
AN 117,119
SH 118
M 87
AN 118,118
AN 119,119
M 88
TY 122,8200
TY 126,22024
M 89
ad 84,1056
W 0
M 90
AD 8,6
AD 9,7
M 91
AW 54,8
AW 78,9
M 92
W 1
UL 6,27
M 93
UH 7,27
AR 8,102
M 94
AR 9,126
AD 8,6
M 95
R 82
M 106
W 2
AD 9,7
M 107
WL 3
AN 120,122
AN 121,123
M 104
WL 2
SH 122
AN 122,122
M 105
mw 51
AN 123,123
AN 124,126
M 102
AN 125,127
SH 126
M 103
AN 126,126
AN 127,127
M 100
ad 84,1062
W 0
M 101
AW 102,8
AW 126,9
M 98
UL 6,28
UH 7,28
M 99
W 1
AR 8,55
M 96
AR 9,79
AD 8,6
M 97
R 0
M 108
W 2
AD 9,7
M 109
AW 55,8
AW 79,9
M 110
UL 6,29
UH 7,29
M 111
bc 49,12,66,60
AR 8,103
M 112
mh 0
AR 9,127
AD 8,6
M 113
ad 66,2336
H 0
M 114
AD 9,7
AW 103,8
M 115
AW 127,9
M 116
DG 1024
M 117
M 118
M 119
WL 2
R 2
M 120
bc 48,12,64,56
M 121
mh 3
M 133
ad 64,2336
H 0
M 132
TX 58,4608
TX 62,18432
M 134
M 135
H 1
M 123
M 122
TY 66,5120
TY 70,18944
M 124
H 2
M 125
M 137
M 136
TX 74,6144
TX 78,19968
PA 12288
DF
M 138
WL 4
Q 0
M 139
Q 2
M 127
Q 4
M 126
TY 82,6656
TY 86,20480
M 128
Q 6
M 129
WL 4
Q 8
M 141
Q 10
M 140
TX 90,7680
TX 94,21504
M 142
Q 12
M 143
R 6
M 131
Q 14
M 130
TY 98,8192
TY 102,22016
M 0
R 4
M 1
WL 10
Q 16
M 13
Q 18
M 12
Q 20
M 14
Q 22
M 15
WL 8
Q 24
M 3
Q 26
M 2
Q 28
M 4
Q 30
M 5
WL 6
Q 32
M 17
Q 34
M 16
Q 36
M 18
Q 38
M 19
WL 2
Q 40
M 7
Q 42
M 6
Q 44
M 8
Q 46
M 9
ae 42,72,96
M 21
s_bitcmp1_b32 s50, 4
s_cselect_b32 s52, 24576, 0
v_add_u32 v50, s52, v3
M 20
SA 224,0
M 22
M 23
R 10
M 11
M 10
WL 2
R 8
M 34
M 35
M 32
M 33
M 30
M 31
M 28
M 29
M 26
ae 44,70,384
M 27
WV 7
M 24
WL 0
FP
M 25
M 36
s_bitcmp1_b32 s50, 6
s_cselect_b32 s51, 24576, 0
v_add_u32 v47, s51, v3
M 37
XL 38,128
M 38
XL 40,896
M 39
M 40
M 41
M 42
M 43
M 44
XL 42,1664
M 45
FR
M 46
M 47
M 58
M 59
M 56
XL 44,2432
UL 6,30
M 57
UH 7,30
AR 8,56
M 54
AR 9,80
AD 8,6
M 55
WL 0
FQ 120
AD 9,7
M 52
AW 56,8
AW 80,9
M 53
UL 6,31
UH 7,31
M 50
FZ 120
R 24
M 51
mw 72
R 26
M 48
TY 106,9736
TY 110,23560
M 49
R 28
M 60
ad 84,1104
W 0
M 61
AR 8,104
AR 9,128
M 62
AD 8,6
AD 9,7
M 63
W 1
AW 104,8
M 64
AW 128,9
UL 6,32
M 65
UH 7,32
AR 8,57
M 66
W 2
AR 9,81
M 67
WL 3
AN 104,106
AN 105,107
M 68
WL 2
SH 106
AN 106,106
M 69
mw 75
AN 107,107
AN 108,110
M 70
AN 109,111
SH 110
M 71
R 30
M 82
AN 110,110
AN 111,111
M 83
TY 114,11272
TY 118,25096
M 80
ad 84,1110
W 0
M 81
AD 8,6
AD 9,7
M 78
AW 57,8
AW 81,9
M 79
)"
R"(W 1
UL 6,33
M 76
UH 7,33
AR 8,105
M 77
AR 9,129
AD 8,6
M 74
W 2
AD 9,7
M 75
AW 105,8
AW 129,9
M 72
WL 1
AN 112,114
AN 113,115
M 73
R 32
M 84
mw 0
SH 114
AN 114,114
M 85
WL 2
AN 115,115
AN 116,118
M 86
AN 117,119
SH 118
M 87
AN 118,118
AN 119,119
M 88
TY 122,12808
TY 126,26632
M 89
ad 84,1152
W 0
M 90
SA 226,768
UL 6,34
M 91
UH 7,34
AR 8,58
M 92
W 1
AR 9,82
M 93
AD 8,6
AD 9,7
M 94
AW 58,8
AW 82,9
M 95
R 34
M 106
W 2
UL 6,35
M 107
WL 3
AN 120,122
AN 121,123
M 104
WL 2
SH 122
AN 122,122
M 105
mw 3
AN 123,123
AN 124,126
M 102
AN 125,127
SH 126
M 103
AN 126,126
AN 127,127
M 100
ad 84,1158
W 0
M 101
UH 7,35
AR 8,106
M 98
AR 9,130
AD 8,6
M 99
W 1
AD 9,7
M 96
AW 106,8
AW 130,9
M 97
R 48
M 108
W 2
UL 6,36
M 109
UH 7,36
AR 8,59
M 110
AR 9,83
AD 8,6
M 111
bc 49,13,66,60
AD 9,7
M 112
mh 9
AW 59,8
AW 83,9
M 113
ad 66,2528
H 0
M 114
UL 6,37
UH 7,37
M 115
AR 8,107
AR 9,131
M 116
DG 1024
M 117
AD 8,6
AD 9,7
M 118
AW 107,8
AW 131,9
M 119
WL 0
R 50
M 120
bc 48,13,64,56
M 121
mh 12
M 133
ad 64,2528
H 0
M 132
TX 58,9216
TX 62,23040
M 134
M 135
H 1
M 123
M 122
TY 66,9728
TY 70,23552
M 124
H 2
M 125
M 137
M 136
TX 74,10752
TX 78,24576
M 138
WL 4
Q 0
M 139
Q 2
M 127
Q 4
M 126
TY 82,11264
TY 86,25088
M 128
Q 6
M 129
WL 4
Q 8
M 141
Q 10
M 140
TX 90,12288
TX 94,26112
M 142
Q 12
M 143
R 54
M 131
Q 14
M 130
TY 98,12800
TY 102,26624
M 0
R 52
M 1
WL 10
Q 16
M 13
Q 18
M 12
Q 20
M 14
Q 22
M 15
WL 8
Q 24
M 3
Q 26
M 2
Q 28
M 4
Q 30
M 5
WL 6
Q 32
M 17
Q 34
M 16
Q 36
M 18
Q 38
M 19
WL 2
Q 40
M 7
Q 42
M 6
Q 44
M 8
Q 46
M 9
SA 228,1536
M 21
M 20
M 22
M 23
R 58
M 11
M 10
WL 2
R 56
M 34
M 35
M 32
M 33
M 30
M 31
M 28
M 29
M 26
M 27
WV 6
M 24
WL 0
FP
M 25
M 36
ae 44,70,384
M 37
s_bfe_u32 s51, s50, 0x10007
v_mad_i32_i24 v47, v49, s51, v48
XL 14,128
M 38
M 39
M 40
XL 16,896
M 41
M 42
M 43
M 44
M 45
FR
M 46
XL 18,1664
M 47
M 58
M 59
M 56
M 57
M 54
XL 20,2432
M 55
WL 0
FQ 124
M 52
M 53
M 50
FZ 124
R 72
M 51
mw 24
R 74
M 48
TY 106,520
TY 110,14344
M 49
R 76
M 60
ad 84,1200
W 0
M 61
M 62
M 63
W 1
M 64
M 65
M 66
W 2
M 67
WL 3
AN 104,106
AN 105,107
M 68
WL 2
SH 106
AN 106,106
M 69
mw 27
AN 107,107
AN 108,110
M 70
AN 109,111
SH 110
M 71
R 78
M 82
AN 110,110
AN 111,111
M 83
TY 114,2056
TY 118,15880
M 80
ad 84,1206
W 0
M 81
M 78
M 79
W 1
M 76
M 77
M 74
W 2
M 75
SA 230,2304
M 72
WL 1
AN 112,114
AN 113,115
M 73
R 80
M 84
mw 48
SH 114
AN 114,114
M 85
WL 2
AN 115,115
AN 116,118
M 86
AN 117,119
SH 118
M 87
AN 118,118
AN 119,119
M 88
TY 122,3592
TY 126,17416
M 89
ad 84,1248
W 0
M 90
UL 6,38
UH 7,38
M 91
AR 8,132
AR 9,156
M 92
W 1
AD 8,6
M 93
AD 9,7
AW 132,8
M 94
AW 156,9
UL 6,39
M 95
R 82
M 106
W 2
UH 7,39
M 107
WL 3
AN 120,122
AN 121,123
M 104
WL 2
SH 122
AN 122,122
M 105
mw 51
AN 123,123
AN 124,126
M 102
AN 125,127
SH 126
M 103
AN 126,126
AN 127,127
)"
R"(M 100
ad 84,1254
W 0
M 101
AR 8,180
AD 8,6
M 98
AD 136,7
AW 180,8
M 99
W 1
UL 6,40
M 96
UH 7,40
AR 8,133
M 97
R 0
M 108
W 2
AR 9,157
M 109
AD 8,6
AD 9,7
M 110
AW 133,8
AW 157,9
M 111
bc 49,14,66,60
UL 6,41
M 112
mh 18
UH 7,41
AR 8,181
M 113
ad 66,2720
H 0
M 114
AD 8,6
AD 137,7
M 115
AW 181,8
UL 6,42
M 116
DG 1024
M 117
UH 7,42
AR 8,134
M 118
AR 9,158
AD 8,6
M 119
WL 0
R 2
M 120
bc 48,14,64,56
AD 9,7
M 121
mh 21
AW 134,8
AW 158,9
M 133
ad 64,2720
H 0
M 132
TX 58,0
TX 62,13824
M 134
UL 6,43
UH 7,43
M 135
H 1
AR 8,182
M 123
AD 8,6
AD 138,7
M 122
TY 66,512
TY 70,14336
M 124
H 2
AW 182,8
M 125
UL 6,44
UH 7,44
M 137
AR 8,135
AR 9,159
M 136
TX 74,1536
TX 78,15360
PA 12288
DF
M 138
WL 4
Q 0
M 139
Q 2
M 127
Q 4
M 126
TY 82,2048
TY 86,15872
M 128
Q 6
M 129
WL 4
Q 8
M 141
Q 10
M 140
TX 90,3072
TX 94,16896
M 142
Q 12
M 143
R 6
M 131
Q 14
M 130
TY 98,3584
TY 102,17408
M 0
R 4
M 1
WL 10
Q 16
M 13
Q 18
M 12
Q 20
M 14
Q 22
M 15
WL 8
Q 24
M 3
Q 26
M 2
Q 28
M 4
Q 30
M 5
WL 6
Q 32
M 17
Q 34
M 16
Q 36
M 18
Q 38
M 19
WL 2
Q 40
M 7
Q 42
M 6
Q 44
M 8
Q 46
M 9
ae 42,72,96
M 21
s_bfe_u32 s52, s50, 0x10005
v_mad_i32_i24 v50, v49, s52, v48
SA 232,0
M 20
AD 8,6
AD 9,7
M 22
AW 135,8
AW 159,9
M 23
R 10
M 11
UL 6,45
UH 7,45
M 10
WL 2
R 8
M 34
AR 8,183
AD 8,6
M 35
AD 139,7
AW 183,8
M 32
M 33
M 30
M 31
M 28
M 29
M 26
M 27
WV 7
M 24
WL 0
FP
M 25
M 36
ae 44,68,384
M 37
s_bitcmp1_b32 s50, 8
s_cselect_b32 s51, 24576, 0
v_add_u32 v47, s51, v3
M 38
XL 22,128
M 39
M 40
M 41
M 42
XL 24,896
M 43
M 44
M 45
FR
M 46
M 47
M 58
XL 26,1664
M 59
M 56
M 57
M 54
M 55
WL 0
FQ 128
M 52
XL 28,2432
M 53
M 50
FZ 128
R 24
M 51
mw 72
R 26
M 48
TY 106,5128
TY 110,18952
M 49
R 28
M 60
ad 84,1296
W 0
M 61
M 62
M 63
W 1
M 64
M 65
M 66
W 2
M 67
WL 3
AN 104,106
AN 105,107
M 68
WL 2
SH 106
AN 106,106
M 69
mw 75
AN 107,107
AN 108,110
M 70
AN 109,111
SH 110
M 71
R 30
M 82
AN 110,110
AN 111,111
M 83
TY 114,6664
TY 118,20488
M 80
ad 84,1302
W 0
M 81
M 78
M 79
W 1
M 76
M 77
M 74
W 2
M 75
M 72
WL 1
AN 112,114
AN 113,115
M 73
R 32
M 84
mw 0
SH 114
AN 114,114
M 85
WL 2
AN 115,115
AN 116,118
M 86
AN 117,119
SH 118
M 87
AN 118,118
AN 119,119
M 88
TY 122,8200
TY 126,22024
M 89
ad 84,1344
W 0
M 90
SA 234,768
UL 6,14
M 91
UH 7,14
AR 8,136
M 92
W 1
AR 9,160
M 93
AD 8,6
AD 9,7
M 94
AW 136,8
AW 160,9
M 95
R 34
M 106
W 2
UL 6,15
M 107
WL 3
AN 120,122
AN 121,123
M 104
WL 2
SH 122
AN 122,122
M 105
mw 3
AN 123,123
AN 124,126
M 102
AN 125,127
SH 126
M 103
AN 126,126
AN 127,127
M 100
ad 84,1350
W 0
M 101
UH 7,15
AR 8,184
M 98
AD 8,6
AD 140,7
M 99
W 1
AW 184,8
M 96
UL 6,16
UH 7,16
M 97
R 48
M 108
W 2
AR 8,137
M 109
AR 9,161
AD 8,6
M 110
AD 9,7
AW 137,8
M 111
bc 49,15,66,60
AW 161,9
M 112
mh 0
UL 6,17
UH 7,17
M 113
ad 66,2912
H 0
M 114
AR 8,185
AD 8,6
M 115
AD 141,7
AW 185,8
M 116
DG 1024
M 117
UL 6,18
UH 7,18
M 118
AR 8,138
AR 9,162
)"
R"(M 119
WL 0
R 50
M 120
bc 48,15,64,56
AD 8,6
M 121
mh 3
AD 9,7
AW 138,8
M 133
ad 64,2912
H 0
M 132
TX 58,4608
TX 62,18432
M 134
AW 162,9
UL 6,19
M 135
H 1
UH 7,19
M 123
AR 8,186
AD 8,6
M 122
TY 66,5120
TY 70,18944
M 124
H 2
AD 142,7
M 125
AW 186,8
UL 6,20
M 137
UH 7,20
AR 8,139
M 136
TX 74,6144
TX 78,19968
M 138
WL 4
Q 0
M 139
Q 2
M 127
Q 4
M 126
TY 82,6656
TY 86,20480
M 128
Q 6
M 129
WL 4
Q 8
M 141
Q 10
M 140
TX 90,7680
TX 94,21504
M 142
Q 12
M 143
R 54
M 131
Q 14
M 130
TY 98,8192
TY 102,22016
M 0
R 52
M 1
WL 10
Q 16
M 13
Q 18
M 12
Q 20
M 14
Q 22
M 15
WL 8
Q 24
M 3
Q 26
M 2
Q 28
M 4
Q 30
M 5
WL 6
Q 32
M 17
Q 34
M 16
Q 36
M 18
Q 38
M 19
WL 2
Q 40
M 7
Q 42
M 6
Q 44
M 8
Q 46
M 9
SA 236,1536
AR 9,163
M 21
AD 8,6
AD 9,7
M 20
AW 139,8
AW 163,9
M 22
UL 6,21
UH 7,21
M 23
R 58
M 11
AR 8,187
AD 8,6
M 10
WL 2
R 56
M 34
AD 143,7
AW 187,8
M 35
M 32
M 33
M 30
M 31
M 28
M 29
M 26
M 27
WV 6
M 24
WL 0
FP
M 25
M 36
M 37
M 38
ae 44,68,384
M 39
s_bfe_u32 s51, s50, 0x10009
v_mad_i32_i24 v47, v49, s51, v48
XL 30,128
M 40
M 41
M 42
M 43
M 44
XL 32,896
M 45
FR
M 46
M 47
M 58
M 59
M 56
XL 34,1664
M 57
M 54
M 55
WL 0
FQ 132
M 52
M 53
M 50
FZ 132
R 72
M 51
mw 24
R 74
M 48
TY 106,9736
TY 110,23560
M 49
R 76
M 60
ad 84,1392
W 0
M 61
XL 36,2432
M 62
M 63
W 1
M 64
M 65
M 66
W 2
M 67
WL 3
AN 104,106
AN 105,107
M 68
WL 2
SH 106
AN 106,106
M 69
mw 27
AN 107,107
AN 108,110
M 70
AN 109,111
SH 110
M 71
R 78
M 82
AN 110,110
AN 111,111
M 83
TY 114,11272
TY 118,25096
M 80
ad 84,1398
W 0
M 81
M 78
M 79
W 1
M 76
M 77
M 74
W 2
M 75
SA 238,2304
M 72
WL 1
AN 112,114
AN 113,115
M 73
R 80
M 84
mw 48
SH 114
AN 114,114
M 85
WL 2
AN 115,115
AN 116,118
M 86
AN 117,119
SH 118
M 87
AN 118,118
AN 119,119
M 88
TY 122,12808
TY 126,26632
M 89
ad 84,1440
W 0
M 90
M 91
M 92
W 1
UL 6,22
M 93
UH 7,22
AR 8,140
M 94
AR 9,164
AD 8,6
M 95
R 82
M 106
W 2
AD 9,7
M 107
WL 3
AN 120,122
AN 121,123
M 104
WL 2
SH 122
AN 122,122
M 105
mw 51
AN 123,123
AN 124,126
M 102
AN 125,127
SH 126
M 103
AN 126,126
AN 127,127
M 100
ad 84,1446
W 0
M 101
AW 140,8
AW 164,9
M 98
UL 6,23
UH 7,23
M 99
W 1
AR 8,188
M 96
AD 8,6
AD 144,7
M 97
R 0
M 108
W 2
AW 188,8
M 109
UL 6,24
UH 7,24
M 110
AR 8,141
AR 9,165
M 111
bc 49,16,66,60
AD 8,6
M 112
mh 9
AD 9,7
AW 141,8
M 113
ad 66,3104
H 0
M 114
AW 165,9
UL 6,25
M 115
UH 7,25
AR 8,189
M 116
DG 1024
M 117
AD 8,6
AD 145,7
M 118
AW 189,8
UL 6,26
M 119
WL 0
R 2
M 120
bc 48,16,64,56
UH 7,26
M 121
mh 12
AR 8,142
AR 9,166
M 133
ad 64,3104
H 0
M 132
TX 58,9216
TX 62,23040
M 134
AD 8,6
AD 9,7
M 135
H 1
AW 142,8
M 123
AW 166,9
UL 6,27
M 122
TY 66,9728
TY 70,23552
M 124
H 2
UH 7,27
M 125
AR 8,190
AD 8,6
M 137
AD 146,7
AW 190,8
M 136
TX 74,10752
TX 78,24576
PA 12288
DF
M 138
WL 4
Q 0
M 139
Q 2
M 127
Q 4
M 126
TY 82,11264
TY 86,25088
M 128
Q 6
M 129
WL 4
Q 8
M 141
)"
R"(Q 10
M 140
TX 90,12288
TX 94,26112
M 142
Q 12
M 143
R 6
M 131
Q 14
M 130
TY 98,12800
TY 102,26624
M 0
R 4
M 1
WL 10
Q 16
M 13
Q 18
M 12
Q 20
M 14
Q 22
M 15
WL 8
Q 24
M 3
Q 26
M 2
Q 28
M 4
Q 30
M 5
WL 6
Q 32
M 17
Q 34
M 16
Q 36
M 18
Q 38
M 19
WL 2
Q 40
M 7
Q 42
M 6
Q 44
M 8
Q 46
M 9
ae 42,72,96
M 21
s_bitcmp1_b32 s50, 4
s_cselect_b32 s52, 24576, 0
v_add_u32 v50, s52, v3
M 20
SA 240,256
UL 6,28
M 22
UH 7,28
AR 8,143
M 23
R 10
M 11
AR 9,167
AD 8,6
M 10
WL 2
R 8
M 34
AD 9,7
AW 143,8
M 35
AW 167,9
UL 6,29
M 32
UH 7,29
AR 8,191
M 33
AD 8,6
AD 147,7
M 30
AW 191,8
M 31
M 28
M 29
M 26
M 27
WV 7
M 24
WL 0
FP
M 25
M 36
M 37
M 38
M 39
M 40
ae 44,68,480
M 41
s_bitcmp1_b32 s50, 10
s_cselect_b32 s51, 24576, 0
v_add_u32 v47, s51, v3
M 42
XL 38,128
M 43
M 44
M 45
FR
M 46
XL 40,896
M 47
M 58
M 59
M 56
M 57
M 54
XL 42,1664
M 55
WL 0
FQ 136
M 52
M 53
M 50
FZ 136
R 24
M 51
mw 72
R 26
M 48
TY 106,520
TY 110,14344
M 49
R 28
M 60
ad 84,1488
W 0
M 61
XL 44,2432
M 62
M 63
W 1
M 64
M 65
M 66
W 2
M 67
WL 3
AN 104,106
AN 105,107
M 68
WL 2
SH 106
AN 106,106
M 69
mw 75
AN 107,107
AN 108,110
M 70
AN 109,111
SH 110
M 71
R 30
M 82
AN 110,110
AN 111,111
M 83
TY 114,2056
TY 118,15880
M 80
ad 84,1494
W 0
M 81
M 78
M 79
W 1
M 76
M 77
M 74
W 2
M 75
M 72
WL 1
AN 112,114
AN 113,115
M 73
R 32
M 84
mw 0
SH 114
AN 114,114
M 85
WL 2
AN 115,115
AN 116,118
M 86
AN 117,119
SH 118
M 87
AN 118,118
AN 119,119
M 88
TY 122,3592
TY 126,17416
M 89
ad 84,1536
W 0
M 90
SA 242,1024
M 91
M 92
W 1
M 93
M 94
UL 6,30
UH 7,30
M 95
R 34
M 106
W 2
AR 8,144
M 107
WL 3
AN 120,122
AN 121,123
M 104
WL 2
SH 122
AN 122,122
M 105
mw 3
AN 123,123
AN 124,126
M 102
AN 125,127
SH 126
M 103
AN 126,126
AN 127,127
M 100
ad 84,1542
W 0
M 101
AR 9,168
AD 8,6
M 98
AD 9,7
AW 144,8
M 99
W 1
AW 168,9
M 96
UL 6,31
UH 7,31
M 97
R 48
M 108
W 2
AR 8,192
M 109
AD 8,6
AD 148,7
M 110
AW 192,8
UL 6,32
M 111
bc 49,17,66,60
UH 7,32
M 112
mh 18
AR 8,145
AR 9,169
M 113
ad 66,3296
H 0
M 114
AD 8,6
AD 9,7
M 115
AW 145,8
AW 169,9
M 116
DG 1024
M 117
UL 6,33
UH 7,33
M 118
AR 8,193
AD 8,6
M 119
WL 0
R 50
M 120
bc 48,17,64,56
AD 149,7
M 121
mh 21
AW 193,8
UL 6,34
M 133
ad 64,3296
H 0
M 132
TX 58,0
TX 62,13824
M 134
UH 7,34
AR 8,146
M 135
H 1
AR 9,170
M 123
AD 8,6
AD 9,7
M 122
TY 66,512
TY 70,14336
M 124
H 2
AW 146,8
M 125
AW 170,9
UL 6,35
M 137
UH 7,35
AR 8,194
M 136
TX 74,1536
TX 78,15360
M 138
WL 4
Q 0
M 139
Q 2
M 127
Q 4
M 126
TY 82,2048
TY 86,15872
M 128
Q 6
M 129
WL 4
Q 8
M 141
Q 10
M 140
TX 90,3072
TX 94,16896
M 142
Q 12
M 143
R 54
M 131
Q 14
M 130
TY 98,3584
TY 102,17408
M 0
R 52
M 1
WL 10
Q 16
M 13
Q 18
M 12
Q 20
M 14
Q 22
M 15
WL 8
Q 24
M 3
Q 26
M 2
Q 28
M 4
Q 30
M 5
WL 6
Q 32
M 17
Q 34
M 16
Q 36
M 18
Q 38
M 19
WL 2
Q 40
M 7
Q 42
M 6
Q 44
M 8
Q 46
M 9
SA 244,1792
)"
R"(AD 8,6
M 21
AD 150,7
AW 194,8
M 20
UL 6,36
UH 7,36
M 22
AR 8,147
AR 9,171
M 23
R 58
M 11
AD 8,6
AD 9,7
M 10
WL 2
R 56
M 34
AW 147,8
AW 171,9
M 35
UL 6,37
UH 7,37
M 32
AR 8,195
AD 8,6
M 33
AD 151,7
AW 195,8
M 30
M 31
M 28
M 29
M 26
M 27
WV 6
M 24
WL 0
FP
M 25
M 36
M 37
M 38
M 39
M 40
M 41
M 42
ae 44,68,480
M 43
s_bfe_u32 s51, s50, 0x1000b
v_mad_i32_i24 v47, v49, s51, v48
XL 14,128
M 44
M 45
FR
M 46
M 47
M 58
XL 16,896
M 59
M 56
M 57
M 54
M 55
WL 0
FQ 140
M 52
XL 18,1664
M 53
M 50
FZ 140
R 72
M 51
mw 24
R 74
M 48
TY 106,5128
TY 110,18952
M 49
R 76
M 60
ad 84,1584
W 0
M 61
XL 20,2432
M 62
M 63
W 1
M 64
M 65
M 66
W 2
M 67
WL 3
AN 104,106
AN 105,107
M 68
WL 2
SH 106
AN 106,106
M 69
mw 27
AN 107,107
AN 108,110
M 70
AN 109,111
SH 110
M 71
R 78
M 82
AN 110,110
AN 111,111
M 83
TY 114,6664
TY 118,20488
M 80
ad 84,1590
W 0
M 81
M 78
M 79
W 1
M 76
M 77
M 74
W 2
M 75
SA 246,2560
M 72
WL 1
AN 112,114
AN 113,115
M 73
R 80
M 84
mw 48
SH 114
AN 114,114
M 85
WL 2
AN 115,115
AN 116,118
M 86
AN 117,119
SH 118
M 87
AN 118,118
AN 119,119
M 88
TY 122,8200
TY 126,22024
M 89
ad 84,1632
W 0
M 90
M 91
M 92
W 1
M 93
M 94
M 95
R 82
M 106
W 2
UL 6,38
M 107
WL 3
AN 120,122
AN 121,123
M 104
WL 2
SH 122
AN 122,122
M 105
mw 51
AN 123,123
AN 124,126
M 102
AN 125,127
SH 126
M 103
AN 126,126
AN 127,127
M 100
ad 84,1638
W 0
M 101
UH 7,38
AR 8,148
M 98
AR 9,172
AD 8,6
M 99
W 1
AD 9,7
M 96
AW 148,8
AW 172,9
M 97
R 0
M 108
W 2
UL 6,39
M 109
UH 7,39
AR 8,196
M 110
AD 8,6
AD 152,7
M 111
bc 49,18,66,60
AW 196,8
M 112
mh 0
UL 6,40
UH 7,40
M 113
ad 66,3488
H 0
M 114
AR 8,149
AR 9,173
M 115
AD 8,6
AD 9,7
M 116
DG 1024
M 117
AW 149,8
AW 173,9
M 118
UL 6,41
UH 7,41
M 119
WL 0
R 2
M 120
bc 48,18,64,56
AR 8,197
M 121
mh 3
AD 8,6
AD 153,7
M 133
ad 64,3488
H 0
M 132
TX 58,4608
TX 62,18432
M 134
AW 197,8
UL 6,42
M 135
H 1
UH 7,42
M 123
AR 8,150
AR 9,174
M 122
TY 66,5120
TY 70,18944
M 124
H 2
AD 8,6
M 125
AD 9,7
AW 150,8
M 137
AW 174,9
UL 6,43
M 136
TX 74,6144
TX 78,19968
PA 12288
DF
M 138
WL 4
Q 0
M 139
Q 2
M 127
Q 4
M 126
TY 82,6656
TY 86,20480
M 128
Q 6
M 129
WL 4
Q 8
M 141
Q 10
M 140
TX 90,7680
TX 94,21504
M 142
Q 12
M 143
R 6
M 131
Q 14
M 130
TY 98,8192
TY 102,22016
M 0
R 4
M 1
WL 10
Q 16
M 13
Q 18
M 12
Q 20
M 14
Q 22
M 15
WL 8
Q 24
M 3
Q 26
M 2
Q 28
M 4
Q 30
M 5
WL 6
Q 32
M 17
Q 34
M 16
Q 36
M 18
Q 38
M 19
WL 2
Q 40
M 7
Q 42
M 6
Q 44
M 8
Q 46
M 9
ae 42,72,96
M 21
s_bfe_u32 s52, s50, 0x10005
v_mad_i32_i24 v50, v49, s52, v48
SA 248,256
M 20
UH 7,43
AR 8,198
M 22
AD 8,6
AD 154,7
M 23
R 10
M 11
AW 198,8
UL 6,44
M 10
WL 2
R 8
M 34
UH 7,44
AR 8,151
M 35
AR 9,175
AD 8,6
M 32
AD 9,7
AW 151,8
M 33
AW 175,9
UL 6,45
M 30
UH 7,45
AR 8,199
M 31
AD 8,6
AD 155,7
M 28
AW 199,8
M 29
M 26
M 27
WV 7
M 24
WL 0
FP
M 25
M 36
M 37
M 38
M 39
M 40
M 41
M 42
M 43
M 44
)"
R"(M 45
FR
M 46
M 47
M 58
M 59
M 56
M 57
M 54
M 55
WL 0
FQ 144
M 52
M 53
M 50
FZ 144
R 24
M 51
mw 72
R 26
M 48
TY 106,9736
TY 110,23560
M 49
R 28
M 60
ad 84,1680
W 0
M 61
M 62
M 63
W 1
M 64
M 65
M 66
W 2
M 67
WL 3
AN 104,106
AN 105,107
M 68
WL 2
SH 106
AN 106,106
M 69
mw 75
AN 107,107
AN 108,110
M 70
AN 109,111
SH 110
M 71
R 30
M 82
AN 110,110
AN 111,111
M 83
TY 114,11272
TY 118,25096
M 80
ad 84,1686
W 0
M 81
ae 44,70,384
M 78
s_bitcmp1_b32 s50, 6
s_cselect_b32 s51, 24576, 0
v_add_u32 v47, s51, v3
M 79
W 1
XL 22,256
M 76
XL 24,1024
M 77
M 74
W 2
M 75
M 72
WL 1
AN 112,114
AN 113,115
M 73
R 32
M 84
mw 0
SH 114
AN 114,114
M 85
WL 2
AN 115,115
AN 116,118
M 86
AN 117,119
SH 118
M 87
AN 118,118
AN 119,119
M 88
TY 122,12808
TY 126,26632
M 89
ad 84,1728
W 0
M 90
SA 250,1024
XL 26,1792
M 91
XL 28,2560
M 92
W 1
M 93
M 94
M 95
R 34
M 106
W 2
M 107
WL 3
AN 120,122
AN 121,123
M 104
WL 2
SH 122
AN 122,122
M 105
mw 3
AN 123,123
AN 124,126
M 102
AN 125,127
SH 126
M 103
AN 126,126
AN 127,127
M 100
ad 84,1734
W 0
M 101
UL 6,14
UH 7,14
M 98
AR 8,152
AR 9,176
M 99
W 1
AD 8,6
M 96
AD 9,7
AW 152,8
M 97
R 48
M 108
W 2
AW 176,9
M 109
UL 6,15
UH 7,15
M 110
AD 132,6
AD 156,7
M 111
bc 49,19,66,60
UL 6,16
M 112
mh 9
UH 7,16
AR 8,153
M 113
ad 66,3680
H 0
M 114
AR 9,177
AD 8,6
M 115
AD 9,7
AW 153,8
M 116
DG 1024
M 117
AW 177,9
UL 6,17
M 118
UH 7,17
AD 133,6
M 119
WL 0
R 50
M 120
bc 48,19,64,56
AD 157,7
M 121
mh 12
UL 6,18
UH 7,18
M 133
ad 64,3680
H 0
M 132
TX 58,9216
TX 62,23040
M 134
AR 8,154
AR 9,178
M 135
H 1
AD 8,6
M 123
AD 9,7
AW 154,8
M 122
TY 66,9728
TY 70,23552
M 124
H 2
AW 178,9
M 125
UL 6,19
UH 7,19
M 137
AD 134,6
AD 158,7
M 136
TX 74,10752
TX 78,24576
M 138
WL 4
Q 0
M 139
Q 2
M 127
Q 4
M 126
TY 82,11264
TY 86,25088
M 128
Q 6
M 129
WL 4
Q 8
M 141
Q 10
M 140
TX 90,12288
TX 94,26112
M 142
Q 12
M 143
R 54
M 131
Q 14
M 130
TY 98,12800
TY 102,26624
M 0
R 52
M 1
WL 10
Q 16
M 13
Q 18
M 12
Q 20
M 14
Q 22
M 15
WL 8
Q 24
M 3
Q 26
M 2
Q 28
M 4
Q 30
M 5
WL 6
Q 32
M 17
Q 34
M 16
Q 36
M 18
Q 38
M 19
WL 2
Q 40
M 7
Q 42
M 6
Q 44
M 8
Q 46
M 9
SA 252,1792
UL 6,20
M 21
UH 7,20
AR 8,155
M 20
AR 9,179
AD 8,6
M 22
AD 9,7
AW 155,8
M 23
R 58
M 11
AW 179,9
UL 6,21
M 10
WL 2
R 56
M 34
UH 7,21
AD 135,6
M 35
AD 159,7
M 32
M 33
M 30
M 31
M 28
M 29
M 26
M 27
WV 6
M 24
WL 0
FP
M 25
M 36
M 37
M 38
M 39
M 40
M 41
M 42
M 43
M 44
M 45
FR
M 46
M 47
M 58
M 59
M 56
M 57
M 54
M 55
WL 0
FQ 148
M 52
M 53
M 50
FZ 148
R 72
M 51
mw 24
R 74
M 48
TY 106,520
TY 110,14344
M 49
R 76
M 60
ad 84,1776
W 0
M 61
M 62
M 63
W 1
M 64
M 65
M 66
W 2
M 67
WL 3
AN 104,106
AN 105,107
M 68
WL 2
SH 106
AN 106,106
M 69
mw 27
AN 107,107
AN 108,110
M 70
AN 109,111
SH 110
M 71
R 78
M 82
AN 110,110
AN 111,111
M 83
TY 114,2056
TY 118,15880
M 80
ad 84,1782
W 0
)"
R"(M 81
ae 44,70,384
M 78
s_bfe_u32 s51, s50, 0x10007
v_mad_i32_i24 v47, v49, s51, v48
XL 30,256
M 79
W 1
M 76
M 77
M 74
W 2
XL 32,1024
M 75
SA 254,2560
M 72
WL 1
AN 112,114
AN 113,115
M 73
R 80
M 84
mw 48
SH 114
AN 114,114
M 85
WL 2
AN 115,115
AN 116,118
M 86
AN 117,119
SH 118
M 87
AN 118,118
AN 119,119
M 88
TY 122,3592
TY 126,17416
M 89
ad 84,1824
W 0
M 90
XL 34,1792
M 91
M 92
W 1
XL 36,2560
M 93
M 94
M 95
R 82
M 106
W 2
M 107
WL 3
AN 120,122
AN 121,123
M 104
WL 2
SH 122
AN 122,122
M 105
mw 51
AN 123,123
AN 124,126
M 102
AN 125,127
SH 126
M 103
AN 126,126
AN 127,127
M 100
ad 84,1830
W 0
M 101
M 98
M 99
W 1
M 96
M 97
R 0
M 108
W 2
M 109
M 110
M 111
bc 49,20,66,60
M 112
mh 18
M 113
ad 66,3872
H 0
M 114
M 115
M 116
DG 1024
M 117
M 118
M 119
WL 2
R 2
M 120
bc 48,20,64,56
M 121
mh 21
M 133
ad 64,3872
H 0
M 132
TX 58,0
TX 62,13824
M 134
M 135
H 1
M 123
UL 6,22
UH 7,22
M 122
TY 66,512
TY 70,14336
M 124
H 2
AD 160,6
M 125
AD 184,7
UL 6,23
M 137
UH 7,23
AD 208,6
M 136
TX 74,1536
TX 78,15360
PA 12288
DF
M 138
WL 4
Q 0
M 139
Q 2
M 127
Q 4
M 126
TY 82,2048
TY 86,15872
M 128
Q 6
M 129
WL 4
Q 8
M 141
Q 10
M 140
TX 90,3072
TX 94,16896
M 142
Q 12
M 143
R 6
M 131
Q 14
M 130
TY 98,3584
TY 102,17408
M 0
R 4
M 1
WL 10
Q 16
M 13
Q 18
M 12
Q 20
M 14
Q 22
M 15
WL 8
Q 24
M 3
Q 26
M 2
Q 28
M 4
Q 30
M 5
WL 6
Q 32
M 17
Q 34
M 16
Q 36
M 18
Q 38
M 19
WL 2
Q 40
M 7
Q 42
M 6
Q 44
M 8
Q 46
M 9
AD 232,7
UL 6,24
M 21
UH 7,24
AD 161,6
M 20
AD 185,7
UL 6,25
M 22
UH 7,25
AD 209,6
M 23
R 10
M 11
AD 233,7
UL 6,26
M 10
WL 2
R 8
M 34
UH 7,26
AD 162,6
M 35
AD 186,7
UL 6,27
M 32
UH 7,27
AD 210,6
M 33
AD 234,7
UL 6,28
M 30
UH 7,28
AD 163,6
M 31
AD 187,7
UL 6,29
M 28
UH 7,29
AD 211,6
M 29
AD 235,7
M 26
M 27
WV 6
M 24
WL 0
FP
M 25
M 36
M 37
M 38
M 39
M 40
M 41
M 42
M 43
M 44
M 45
FR
M 46
M 47
M 58
M 59
M 56
M 57
M 54
M 55
WL 0
FQ 152
M 52
M 53
M 50
FZ 152
R 24
M 51
mw 72
R 26
M 48
TY 106,5128
TY 110,18952
M 49
R 28
M 60
ad 84,1872
W 0
M 61
M 62
M 63
W 1
M 64
M 65
M 66
W 2
M 67
WL 3
AN 104,106
AN 105,107
M 68
WL 2
SH 106
AN 106,106
M 69
mw 75
AN 107,107
AN 108,110
M 70
AN 109,111
SH 110
M 71
R 30
M 82
AN 110,110
AN 111,111
M 83
TY 114,6664
TY 118,20488
M 80
ad 84,1878
W 0
M 81
M 78
M 79
W 1
M 76
ae 44,68,384
M 77
s_bitcmp1_b32 s50, 8
s_cselect_b32 s51, 24576, 0
v_add_u32 v47, s51, v3
M 74
W 2
XL 38,256
M 75
M 72
WL 1
AN 112,114
AN 113,115
M 73
R 32
M 84
mw 0
SH 114
AN 114,114
M 85
WL 2
AN 115,115
AN 116,118
M 86
AN 117,119
SH 118
M 87
AN 118,118
AN 119,119
M 88
TY 122,8200
TY 126,22024
M 89
ad 84,1920
W 0
M 90
XL 40,1024
M 91
XL 42,1792
M 92
W 1
M 93
M 94
M 95
R 34
M 106
W 2
XL 44,2560
M 107
WL 3
AN 120,122
AN 121,123
M 104
WL 2
SH 122
AN 122,122
M 105
mw 3
AN 123,123
AN 124,126
M 102
AN 125,127
SH 126
M 103
AN 126,126
AN 127,127
M 100
ad 84,1926
W 0
M 101
M 98
M 99
W 1
)"
R"(M 96
M 97
R 48
M 108
W 2
M 109
M 110
M 111
bc 49,21,66,60
M 112
mh 0
M 113
ad 66,4064
H 0
M 114
M 115
M 116
DG 1024
M 117
M 118
M 119
WL 2
R 50
M 120
bc 48,21,64,56
M 121
mh 3
M 133
ad 64,4064
H 0
M 132
TX 58,4608
TX 62,18432
M 134
M 135
H 1
M 123
M 122
TY 66,5120
TY 70,18944
M 124
H 2
M 125
M 137
UL 6,30
UH 7,30
M 136
TX 74,6144
TX 78,19968
M 138
WL 4
Q 0
M 139
Q 2
M 127
Q 4
M 126
TY 82,6656
TY 86,20480
M 128
Q 6
M 129
WL 4
Q 8
M 141
Q 10
M 140
TX 90,7680
TX 94,21504
M 142
Q 12
M 143
R 54
M 131
Q 14
M 130
TY 98,8192
TY 102,22016
M 0
R 52
M 1
WL 10
Q 16
M 13
Q 18
M 12
Q 20
M 14
Q 22
M 15
WL 8
Q 24
M 3
Q 26
M 2
Q 28
M 4
Q 30
M 5
WL 6
Q 32
M 17
Q 34
M 16
Q 36
M 18
Q 38
M 19
WL 2
Q 40
M 7
Q 42
M 6
Q 44
M 8
Q 46
M 9
AD 164,6
AD 188,7
M 21
UL 6,31
UH 7,31
M 20
AD 212,6
AD 236,7
M 22
UL 6,32
UH 7,32
M 23
R 58
M 11
AD 165,6
AD 189,7
M 10
WL 2
R 56
M 34
UL 6,33
UH 7,33
M 35
AD 213,6
AD 237,7
M 32
UL 6,34
UH 7,34
M 33
AD 166,6
AD 190,7
M 30
UL 6,35
UH 7,35
M 31
AD 214,6
AD 238,7
M 28
UL 6,36
UH 7,36
M 29
AD 167,6
AD 191,7
M 26
UL 6,37
UH 7,37
M 27
WV 5
AD 215,6
AD 239,7
M 24
WL 0
FP
M 25
M 36
M 37
M 38
M 39
M 40
M 41
M 42
M 43
M 44
M 45
FR
M 46
M 47
M 58
M 59
M 56
M 57
M 54
M 55
WL 0
FQ 156
M 52
M 53
M 50
FZ 156
R 72
M 51
mw 24
R 74
M 48
TY 106,9736
TY 110,23560
M 49
R 76
M 60
ad 84,1968
W 0
M 61
M 62
M 63
W 1
M 64
M 65
M 66
W 2
M 67
WL 3
AN 104,106
AN 105,107
M 68
WL 2
SH 106
AN 106,106
M 69
mw 27
AN 107,107
AN 108,110
M 70
AN 109,111
SH 110
M 71
R 78
M 82
AN 110,110
AN 111,111
M 83
TY 114,11272
TY 118,25096
M 80
ad 84,1974
W 0
M 81
M 78
M 79
W 1
M 76
M 77
M 74
W 2
M 75
M 72
WL 1
AN 112,114
AN 113,115
M 73
R 80
M 84
mw 48
SH 114
AN 114,114
M 85
WL 2
AN 115,115
AN 116,118
M 86
AN 117,119
SH 118
M 87
AN 118,118
AN 119,119
M 88
TY 122,12808
TY 126,26632
M 89
ad 84,2016
W 0
M 90
ae 44,68,384
M 91
s_bfe_u32 s51, s50, 0x10009
v_mad_i32_i24 v47, v49, s51, v48
XL 14,256
M 92
W 1
XL 16,1024
M 93
M 94
XL 18,1792
M 95
R 82
M 106
W 2
M 107
WL 3
AN 120,122
AN 121,123
M 104
WL 2
SH 122
AN 122,122
M 105
mw 51
AN 123,123
AN 124,126
M 102
AN 125,127
SH 126
M 103
AN 126,126
AN 127,127
M 100
ad 84,2022
W 0
M 101
XL 20,2560
M 98
M 99
W 1
M 96
M 97
R 0
M 108
W 2
M 109
M 110
M 111
bc 49,22,66,60
M 112
mh 9
M 113
ad 66,4256
H 0
M 114
M 115
M 116
DG 1024
M 117
M 118
M 119
WL 2
R 2
M 120
bc 48,22,64,56
M 121
mh 12
M 133
ad 64,4256
H 0
M 132
TX 58,9216
TX 62,23040
M 134
M 135
H 1
M 123
M 122
TY 66,9728
TY 70,23552
M 124
H 2
M 125
M 137
M 136
TX 74,10752
TX 78,24576
PA 12288
DF
M 138
WL 4
Q 0
M 139
Q 2
M 127
Q 4
M 126
TY 82,11264
TY 86,25088
M 128
Q 6
M 129
WL 4
Q 8
M 141
Q 10
M 140
TX 90,12288
TX 94,26112
M 142
Q 12
M 143
R 6
M 131
Q 14
M 130
TY 98,12800
TY 102,26624
M 0
R 4
M 1
)"
R"(WL 10
Q 16
M 13
Q 18
M 12
Q 20
M 14
Q 22
M 15
WL 8
Q 24
M 3
Q 26
M 2
Q 28
M 4
Q 30
M 5
WL 6
Q 32
M 17
Q 34
M 16
Q 36
M 18
Q 38
M 19
WL 2
Q 40
M 7
Q 42
M 6
Q 44
M 8
Q 46
M 9
UL 6,38
UH 7,38
M 21
AD 168,6
AD 192,7
M 20
UL 6,39
UH 7,39
M 22
AD 216,6
AD 240,7
M 23
R 10
M 11
UL 6,40
UH 7,40
M 10
WL 2
R 8
M 34
AD 169,6
AD 193,7
M 35
UL 6,41
UH 7,41
M 32
AD 217,6
AD 241,7
M 33
UL 6,42
UH 7,42
M 30
AD 170,6
AD 194,7
M 31
UL 6,43
UH 7,43
M 28
AD 218,6
AD 242,7
M 29
UL 6,44
UH 7,44
M 26
AD 171,6
AD 195,7
M 27
WV 6
UL 6,45
UH 7,45
M 24
WL 0
FP
AD 219,6
M 25
AD 243,7
M 36
M 37
M 38
M 39
M 40
M 41
M 42
M 43
M 44
M 45
FR
M 46
M 47
M 58
M 59
M 56
M 57
M 54
M 55
WL 0
FQ 160
M 52
M 53
M 50
FZ 160
R 24
M 51
mw 72
R 26
M 48
TY 106,520
TY 110,14344
M 49
R 28
M 60
ad 84,2064
W 0
M 61
M 62
M 63
W 1
M 64
M 65
M 66
W 2
M 67
WL 3
AN 104,106
AN 105,107
M 68
WL 2
SH 106
AN 106,106
M 69
mw 75
AN 107,107
AN 108,110
M 70
AN 109,111
SH 110
M 71
R 30
M 82
AN 110,110
AN 111,111
M 83
TY 114,2056
TY 118,15880
M 80
ad 84,2070
W 0
M 81
M 78
M 79
W 1
M 76
M 77
M 74
W 2
M 75
M 72
WL 1
AN 112,114
AN 113,115
M 73
R 32
M 84
mw 0
SH 114
AN 114,114
M 85
WL 2
AN 115,115
AN 116,118
M 86
AN 117,119
SH 118
M 87
AN 118,118
AN 119,119
M 88
TY 122,3592
TY 126,17416
M 89
ad 84,2112
W 0
M 90
ae 44,68,480
M 91
s_bitcmp1_b32 s50, 10
s_cselect_b32 s51, 24576, 0
v_add_u32 v47, s51, v3
M 92
W 1
XL 22,256
M 93
XL 24,1024
M 94
M 95
R 34
M 106
W 2
M 107
WL 3
AN 120,122
AN 121,123
M 104
WL 2
SH 122
AN 122,122
M 105
mw 3
AN 123,123
AN 124,126
M 102
AN 125,127
SH 126
M 103
AN 126,126
AN 127,127
M 100
ad 84,2118
W 0
M 101
XL 26,1792
M 98
XL 28,2560
M 99
W 1
M 96
M 97
R 48
M 108
W 2
M 109
M 110
M 111
bc 49,23,66,60
M 112
mh 18
M 113
ad 66,4448
H 0
M 114
M 115
M 116
DG 1024
M 117
M 118
M 119
WL 2
R 50
M 120
bc 48,23,64,56
M 121
mh 21
M 133
ad 64,4448
H 0
M 132
TX 58,0
TX 62,13824
M 134
M 135
H 1
M 123
M 122
TY 66,512
TY 70,14336
M 124
H 2
M 125
M 137
M 136
TX 74,1536
TX 78,15360
M 138
WL 4
Q 0
M 139
Q 2
M 127
Q 4
M 126
TY 82,2048
TY 86,15872
M 128
Q 6
M 129
WL 4
Q 8
M 141
Q 10
M 140
TX 90,3072
TX 94,16896
M 142
Q 12
M 143
R 54
M 131
Q 14
M 130
TY 98,3584
TY 102,17408
M 0
R 52
M 1
WL 10
Q 16
M 13
Q 18
M 12
Q 20
M 14
Q 22
M 15
WL 8
Q 24
M 3
Q 26
M 2
Q 28
M 4
Q 30
M 5
WL 6
Q 32
M 17
Q 34
M 16
Q 36
M 18
Q 38
M 19
WL 2
Q 40
M 7
Q 42
M 6
Q 44
M 8
Q 46
M 9
UL 6,14
UH 7,14
M 21
AD 172,6
AD 196,7
M 20
UL 6,15
UH 7,15
M 22
AD 220,6
AD 244,7
M 23
R 58
M 11
UL 6,16
UH 7,16
M 10
WL 2
R 56
M 34
AD 173,6
AD 197,7
M 35
UL 6,17
UH 7,17
M 32
AD 221,6
AD 245,7
M 33
UL 6,18
UH 7,18
M 30
AD 174,6
AD 198,7
M 31
UL 6,19
UH 7,19
M 28
AD 222,6
AD 246,7
M 29
UL 6,20
UH 7,20
M 26
)"
R"(AD 175,6
AD 199,7
M 27
WV 5
UL 6,21
UH 7,21
M 24
WL 0
FP
AD 223,6
M 25
AD 247,7
M 36
M 37
M 38
M 39
M 40
M 41
M 42
M 43
M 44
M 45
FR
M 46
M 47
M 58
M 59
M 56
M 57
M 54
M 55
WL 0
FQ 164
M 52
M 53
M 50
FZ 164
R 72
M 51
mw 24
R 74
M 48
TY 106,5128
TY 110,18952
M 49
R 76
M 60
ad 84,2160
W 0
M 61
M 62
M 63
W 1
M 64
M 65
M 66
W 2
M 67
WL 3
AN 104,106
AN 105,107
M 68
WL 2
SH 106
AN 106,106
M 69
mw 27
AN 107,107
AN 108,110
M 70
AN 109,111
SH 110
M 71
R 78
M 82
AN 110,110
AN 111,111
M 83
TY 114,6664
TY 118,20488
M 80
ad 84,2166
W 0
M 81
M 78
M 79
W 1
M 76
M 77
M 74
W 2
M 75
M 72
WL 1
AN 112,114
AN 113,115
M 73
R 80
M 84
mw 48
SH 114
AN 114,114
M 85
WL 2
AN 115,115
AN 116,118
M 86
AN 117,119
SH 118
M 87
AN 118,118
AN 119,119
M 88
TY 122,8200
TY 126,22024
M 89
ad 84,2208
W 0
M 90
ae 44,68,480
M 91
s_bfe_u32 s51, s50, 0x1000b
v_mad_i32_i24 v47, v49, s51, v48
XL 30,256
M 92
W 1
M 93
M 94
M 95
R 82
M 106
W 2
XL 32,1024
M 107
WL 3
AN 120,122
AN 121,123
M 104
WL 2
SH 122
AN 122,122
M 105
mw 51
AN 123,123
AN 124,126
M 102
AN 125,127
SH 126
M 103
AN 126,126
AN 127,127
M 100
ad 84,2214
W 0
M 101
XL 34,1792
M 98
M 99
W 1
M 96
M 97
R 0
M 108
W 2
XL 36,2560
M 109
M 110
M 111
bc 49,0,66,60
M 112
mh 0
M 113
ad 66,64
H 0
M 114
M 115
M 116
DG 1024
M 117
M 118
M 119
WL 2
R 2
M 120
bc 48,0,64,56
M 121
mh 3
M 133
ad 64,32
H 0
M 132
TX 58,4608
TX 62,18432
M 134
M 135
H 1
M 123
M 122
TY 66,5120
TY 70,18944
M 124
H 2
M 125
M 137
M 136
TX 74,6144
TX 78,19968
PA 12288
DF
M 138
WL 4
Q 0
M 139
Q 2
M 127
Q 4
M 126
TY 82,6656
TY 86,20480
M 128
Q 6
M 129
WL 4
Q 8
M 141
Q 10
M 140
TX 90,7680
TX 94,21504
M 142
Q 12
M 143
R 6
M 131
Q 14
M 130
TY 98,8192
TY 102,22016
M 0
R 4
M 1
WL 10
Q 16
M 13
Q 18
M 12
Q 20
M 14
Q 22
M 15
WL 8
Q 24
M 3
Q 26
M 2
Q 28
M 4
Q 30
M 5
WL 6
Q 32
M 17
Q 34
M 16
Q 36
M 18
Q 38
M 19
WL 2
Q 40
M 7
Q 42
M 6
Q 44
M 8
Q 46
M 9
UL 6,22
UH 7,22
M 21
AD 176,6
AD 200,7
M 20
UL 6,23
UH 7,23
M 22
AD 224,6
AD 248,7
M 23
R 10
M 11
UL 6,24
UH 7,24
M 10
WL 2
R 8
M 34
AD 177,6
AD 201,7
M 35
UL 6,25
UH 7,25
M 32
AD 225,6
AD 249,7
M 33
UL 6,26
UH 7,26
M 30
AD 178,6
AD 202,7
M 31
UL 6,27
UH 7,27
M 28
AD 226,6
AD 250,7
M 29
UL 6,28
UH 7,28
M 26
AD 179,6
AD 203,7
M 27
WV 7
UL 6,29
UH 7,29
M 24
WL 0
FP
AD 227,6
M 25
AD 251,7
M 36
M 37
M 38
M 39
M 40
M 41
M 42
M 43
M 44
M 45
FR
M 46
M 47
M 58
M 59
M 56
M 57
M 54
M 55
WL 0
FQ 168
M 52
M 53
M 50
FZ 168
R 24
M 51
mw 72
R 26
M 48
TY 106,9736
TY 110,23560
M 49
R 28
M 60
ad 84,2256
W 0
M 61
M 62
M 63
W 1
M 64
M 65
M 66
W 2
M 67
WL 3
AN 104,106
AN 105,107
M 68
WL 2
SH 106
AN 106,106
M 69
mw 75
AN 107,107
AN 108,110
M 70
AN 109,111
SH 110
M 71
R 30
M 82
AN 110,110
AN 111,111
M 83
TY 114,11272
)"
R"(TY 118,25096
M 80
ad 84,2262
W 0
M 81
M 78
M 79
W 1
M 76
M 77
M 74
W 2
M 75
M 72
WL 1
AN 112,114
AN 113,115
M 73
R 32
M 84
mw 0
SH 114
AN 114,114
M 85
WL 2
AN 115,115
AN 116,118
M 86
AN 117,119
SH 118
M 87
AN 118,118
AN 119,119
M 88
TY 122,12808
TY 126,26632
M 89
ad 84,0
W 0
M 90
M 91
M 92
W 1
M 93
M 94
M 95
R 34
M 106
W 2
M 107
WL 3
AN 120,122
AN 121,123
M 104
WL 2
SH 122
AN 122,122
M 105
mw 3
AN 123,123
AN 124,126
M 102
AN 125,127
SH 126
M 103
AN 126,126
AN 127,127
M 100
ad 84,6
W 0
M 101
M 98
M 99
W 1
M 96
M 97
R 48
M 108
W 2
M 109
M 110
M 111
bc 49,1,66,60
M 112
mh 9
M 113
ad 66,256
H 0
M 114
M 115
M 116
DG 1024
M 117
M 118
M 119
WL 2
R 50
M 120
bc 48,1,64,56
M 121
mh 12
M 133
ad 64,256
H 0
M 132
TX 58,9216
TX 62,23040
M 134
M 135
DG 1024
M 123
M 122
TY 66,9728
TY 70,23552
M 124
mh 15
M 125
ad 64,227
H 0
M 137
M 136
TX 74,10752
TX 78,24576
M 138
WL 4
Q 0
M 139
Q 2
M 127
Q 4
M 126
TY 82,11264
TY 86,25088
M 128
Q 6
M 129
WL 4
Q 8
M 141
Q 10
M 140
TX 90,12288
TX 94,26112
M 142
Q 12
M 143
R 54
M 131
Q 14
M 130
TY 98,12800
TY 102,26624
M 0
R 52
M 1
WL 10
Q 16
M 13
Q 18
M 12
Q 20
M 14
Q 22
M 15
WL 8
Q 24
M 3
Q 26
M 2
Q 28
M 4
Q 30
M 5
WL 6
Q 32
M 17
Q 34
M 16
Q 36
M 18
Q 38
M 19
WL 2
Q 40
M 7
Q 42
M 6
Q 44
M 8
Q 46
M 9
DG 1024
M 21
UL 6,30
UH 7,30
M 20
AD 180,6
AD 204,7
M 22
UL 6,31
UH 7,31
M 23
R 58
M 11
AD 228,6
AD 252,7
M 10
WL 2
R 56
M 34
UL 6,32
UH 7,32
M 35
AD 181,6
AD 205,7
M 32
UL 6,33
UH 7,33
BV 52,4608
M 33
AD 229,6
AD 253,7
M 30
UL 6,34
UH 7,34
M 31
AD 182,6
AD 206,7
M 28
UL 6,35
UH 7,35
M 29
AD 230,6
AD 254,7
M 26
WV 24
UL 6,36
UH 7,36
M 27
WV 6
AD 183,6
AD 207,7
M 24
WL 1
FP
UL 6,37
M 25
UH 7,37
AD 231,6
M 36
AD 255,7
M 37
M 38
M 39
M 40
ds_read_b64 v[12:13], v4 offset:4864
ds_read_b64 v[46:47], v4 offset:4872
M 41
AR 6,36
AR 7,60
M 42
AR 8,84
AR 9,108
M 43
WL 3
EF 6,52
EF 7,53
M 44
EF 8,54
EF 9,55
M 45
FR
CV 38,6,7
M 46
CV 39,8,9
AR 6,37
M 47
AR 7,61
AR 8,85
M 58
AR 9,109
EF 6,52
M 59
EF 7,53
EF 8,54
M 56
EF 9,55
CV 40,6,7
M 57
CV 41,8,9
AR 6,38
M 54
AR 7,62
AR 8,86
M 55
WL 0
FQ 172
AR 9,110
M 52
EF 6,52
EF 7,53
M 53
EF 8,54
EF 9,55
M 50
FZ 172
R 72
M 51
mw 24
R 74
M 48
TY 106,520
TY 110,14344
M 49
R 76
M 60
ad 84,48
W 0
M 61
CV 42,6,7
CV 43,8,9
M 62
AR 6,39
AR 7,63
M 63
W 1
AR 8,87
M 64
AR 9,111
EF 6,52
M 65
EF 7,53
EF 8,54
M 66
W 2
EF 9,55
M 67
WL 3
AN 104,106
AN 105,107
M 68
WL 2
SH 106
AN 106,106
M 69
mw 27
AN 107,107
AN 108,110
M 70
AN 109,111
SH 110
M 71
R 78
M 82
AN 110,110
AN 111,111
M 83
TY 114,2056
TY 118,15880
M 80
ad 84,54
W 0
M 81
CV 44,6,7
CV 45,8,9
M 78
ae 42,74,384
AR 6,40
M 79
W 1
AR 7,64
M 76
s_bitcmp1_b32 s50, 6
s_cselect_b32 s52, 24576, 0
v_add_u32 v50, s52, v3
ST 38,0
M 77
AR 8,88
AR 9,112
M 74
W 2
EF 6,52
M 75
EF 7,53
EF 8,54
M 72
WL 1
AN 112,114
AN 113,115
M 73
R 80
M 84
mw 48
SH 114
AN 114,114
M 85
WL 2
AN 115,115
AN 116,118
M 86
)"
R"(AN 117,119
SH 118
M 87
AN 118,118
AN 119,119
M 88
TY 122,3592
TY 126,17416
M 89
ad 84,96
W 0
M 90
EF 9,55
CV 14,6,7
M 91
ST 40,768
CV 15,8,9
M 92
W 1
AR 6,41
M 93
AR 7,65
AR 8,89
M 94
AR 9,113
EF 6,52
M 95
R 82
M 106
W 2
EF 7,53
M 107
WL 3
AN 120,122
AN 121,123
M 104
WL 2
SH 122
AN 122,122
M 105
mw 51
AN 123,123
AN 124,126
M 102
AN 125,127
SH 126
M 103
AN 126,126
AN 127,127
M 100
ad 84,102
W 0
M 101
EF 8,54
EF 9,55
M 98
ST 42,1536
CV 16,6,7
M 99
W 1
CV 17,8,9
M 96
AR 6,42
AR 7,66
M 97
R 0
M 108
W 2
AR 8,90
M 109
AR 9,114
EF 6,52
M 110
EF 7,53
EF 8,54
M 111
bc 49,2,66,60
EF 9,55
M 112
mh 18
CV 18,6,7
CV 19,8,9
M 113
ad 66,448
H 0
M 114
AR 6,43
AR 7,67
M 115
AR 8,91
AR 9,115
M 116
DG 1024
M 117
ST 44,2304
EF 6,52
M 118
EF 7,53
EF 8,54
M 119
WL 0
R 2
M 120
bc 48,2,64,56
EF 9,55
M 121
mh 21
CV 20,6,7
CV 21,8,9
M 133
ad 64,448
H 0
BV 52,5120
M 132
TX 58,0
TX 62,13824
M 134
AR 6,132
AR 7,156
M 135
H 1
AR 8,180
M 123
EF 6,12
EF 7,13
M 122
TY 66,512
TY 70,14336
M 124
H 2
EF 8,46
M 125
EF 136,47
CV 22,6,7
M 137
ae 42,74,384
CV 23,8,136
M 136
WL 2
TX 74,1536
TX 78,15360
PA 12288
DF
M 138
s_bfe_u32 s52, s50, 0x10007
Q 0
v_mad_i32_i24 v50, v49, s52, v48
M 139
Q 2
ST 14,0
M 127
Q 4
M 126
TY 82,2048
TY 86,15872
M 128
Q 6
M 129
WL 4
Q 8
M 141
Q 10
M 140
TX 90,3072
TX 94,16896
M 142
Q 12
M 143
R 6
M 131
Q 14
M 130
TY 98,3584
TY 102,17408
v_mfma_f32_16x16x32_f16 a[36:39], v[56:59], a[0:3], a[32:35]
R 4
M 1
WL 10
Q 16
v_mfma_f32_16x16x32_f16 a[60:63], v[60:63], a[12:15], a[32:35]
Q 18
ST 16,768
M 12
Q 20
v_mfma_f32_16x16x32_f16 a[64:67], v[64:67], a[8:11], a[32:35]
Q 22
M 15
WL 8
Q 24
v_mfma_f32_16x16x32_f16 a[40:43], v[68:71], a[4:7], a[32:35]
Q 26
M 2
Q 28
M 4
Q 30
M 5
WL 6
Q 32
M 17
Q 34
M 16
Q 36
M 18
Q 38
M 19
WL 1
Q 40
M 7
Q 42
M 6
Q 44
ST 18,1536
M 8
Q 46
M 9
AR 6,133
AR 7,157
M 21
AR 8,181
EF 6,12
M 20
EF 7,13
EF 8,46
M 22
EF 137,47
CV 24,6,7
M 23
R 10
M 11
CV 25,8,137
AR 6,134
M 10
WL 2
R 8
M 34
AR 7,158
AR 8,182
M 35
EF 6,12
EF 7,13
M 32
EF 8,46
EF 138,47
M 33
CV 26,6,7
CV 27,8,138
M 30
ST 20,2304
AR 6,135
M 31
AR 7,159
AR 8,183
M 28
EF 6,12
EF 7,13
M 29
EF 8,46
EF 139,47
v_mfma_f32_16x16x32_f16 a[88:91], v[64:67], a[16:19], a[32:35]
CV 28,6,7
CV 29,8,139
M 27
WV 11
AR 6,136
AR 7,160
AR 8,184
v_mfma_f32_16x16x32_f16 a[84:87], v[56:59], a[16:19], a[32:35]
WL 0
FP
EF 6,12
EF 7,13
M 25
EF 8,46
EF 140,47
CV 30,6,7
v_mfma_f32_16x16x32_f16 a[108:111], v[56:59], a[24:27], a[32:35]
CV 31,8,140
AR 6,137
AR 7,161
M 37
AR 8,185
EF 6,12
EF 7,13
v_mfma_f32_16x16x32_f16 a[112:115], v[64:67], a[24:27], a[32:35]
EF 8,46
EF 141,47
CV 32,6,7
M 39
CV 33,8,141
AR 6,138
AR 7,162
M 40
AR 8,186
EF 6,12
EF 7,13
M 41
ae 42,74,384
EF 8,46
EF 142,47
M 42
s_bitcmp1_b32 s50, 6
s_cselect_b32 s52, 24576, 0
v_add_u32 v50, s52, v3
ST 22,128
M 43
CV 34,6,7
CV 35,8,142
AR 6,139
M 44
AR 7,163
AR 8,187
EF 6,12
M 45
FR
EF 7,13
EF 8,46
M 46
EF 143,47
CV 36,6,7
CV 37,8,143
M 47
ds_read_b64 v[12:13], v4 offset:4608
ds_read_b64 v[46:47], v4 offset:4616
M 58
EF 160,52
EF 184,53
M 59
EF 208,54
EF 232,55
M 56
CV 38,160,184
CV 39,208,232
M 57
EF 161,52
EF 185,53
M 54
EF 209,54
EF 233,55
M 55
WL 2
FQ 176
CV 40,161,185
M 52
CV 41,209,233
EF 162,52
M 53
ST 24,896
EF 186,53
v_mfma_f32_16x16x32_f16 a[136:139], v[64:67], a[0:3], a[32:35]
FZ 176
R 24
M 51
mw 72
R 26
v_mfma_f32_16x16x32_f16 a[132:135], v[56:59], a[0:3], a[32:35]
TY 106,5128
TY 110,18952
M 49
R 28
v_mfma_f32_16x16x32_f16 a[156:159], v[56:59], a[8:11], a[32:35]
ad 84,144
W 0
M 61
EF 210,54
EF 234,55
v_mfma_f32_16x16x32_f16 a[160:163], v[64:67], a[8:11], a[32:35]
CV 42,162,186
CV 43,210,234
M 63
W 1
EF 163,52
M 64
EF 187,53
EF 211,54
M 65
EF 235,55
CV 44,163,187
M 66
W 2
CV 45,211,235
M 67
WL 3
AN 104,106
AN 105,107
EF 164,52
M 68
WL 2
SH 106
AN 106,106
ST 26,1664
M 69
mw 75
AN 107,107
AN 108,110
EF 188,53
M 70
AN 109,111
SH 110
EF 212,54
M 71
)"
R"(R 30
EF 236,55
M 82
AN 110,110
AN 111,111
CV 14,164,188
M 83
TY 114,6664
TY 118,20488
CV 15,212,236
M 80
ad 84,150
W 0
EF 165,52
M 81
EF 189,53
EF 213,54
EF 237,55
M 78
CV 16,165,189
CV 17,213,237
EF 166,52
M 79
W 1
EF 190,53
EF 214,54
M 76
EF 238,55
CV 18,166,190
CV 19,214,238
M 77
EF 167,52
EF 191,53
EF 215,54
v_mfma_f32_16x16x32_f16 a[184:187], v[64:67], a[16:19], a[32:35]
W 2
EF 239,55
CV 20,167,191
M 75
ST 28,2432
CV 21,215,239
v_mfma_f32_16x16x32_f16 a[180:183], v[56:59], a[16:19], a[32:35]
WL 1
AN 112,114
AN 113,115
BV 52,4864
M 73
R 32
v_mfma_f32_16x16x32_f16 v[136:139], v[56:59], a[24:27], v[128:131]
mw 0
SH 114
AN 114,114
M 85
WL 3
AN 115,115
AN 116,118
v_mfma_f32_16x16x32_f16 v[140:143], v[64:67], a[24:27], v[128:131]
AN 117,119
SH 118
M 87
AN 118,118
AN 119,119
M 88
TY 122,8200
TY 126,22024
M 89
ad 84,192
W 0
M 90
AR 6,44
AR 7,68
M 91
AR 8,92
AR 9,116
M 92
W 1
EF 6,12
M 93
EF 7,13
EF 8,46
M 94
ae 42,74,384
EF 9,47
M 95
R 34
M 106
WL 3
W 2
CV 22,6,7
M 107
s_bfe_u32 s52, s50, 0x10007
AN 120,122
AN 121,123
v_mad_i32_i24 v50, v49, s52, v48
M 104
WL 2
SH 122
AN 122,122
ST 30,128
M 105
mw 3
AN 123,123
AN 124,126
M 102
AN 125,127
SH 126
M 103
AN 126,126
AN 127,127
M 100
ad 84,198
W 0
M 101
CV 23,8,9
AR 6,45
v_mfma_f32_16x16x32_f16 v[164:167], v[64:67], a[0:3], v[128:131]
AR 7,69
AR 8,93
M 99
W 1
AR 9,117
v_mfma_f32_16x16x32_f16 v[160:163], v[56:59], a[0:3], v[128:131]
EF 6,12
EF 7,13
M 97
R 48
v_mfma_f32_16x16x32_f16 v[184:187], v[56:59], a[8:11], v[128:131]
W 2
EF 8,46
M 109
EF 9,47
CV 24,6,7
v_mfma_f32_16x16x32_f16 v[188:191], v[64:67], a[8:11], v[128:131]
CV 25,8,9
AR 6,46
M 111
bc 49,3,66,60
ST 32,896
M 112
mh 0
AR 7,70
AR 8,94
M 113
ad 66,640
H 0
M 114
AR 9,118
EF 6,12
M 115
EF 7,13
EF 8,46
M 116
DG 1024
M 117
EF 9,47
CV 26,6,7
M 118
CV 27,8,9
AR 6,47
M 119
WL 0
R 50
v_mfma_f32_16x16x32_f16 v[208:211], v[56:59], a[16:19], v[128:131]
bc 48,3,64,56
AR 7,71
M 121
mh 3
AR 8,95
AR 9,119
v_mfma_f32_16x16x32_f16 v[232:235], v[60:63], a[28:31], v[128:131]
ad 64,640
H 0
M 132
TX 58,4608
TX 62,18432
v_mfma_f32_16x16x32_f16 v[236:239], v[64:67], a[24:27], v[128:131]
ST 34,1664
EF 6,12
M 135
H 1
EF 7,13
v_mfma_f32_16x16x32_f16 v[212:215], v[68:71], a[20:23], v[128:131]
EF 8,46
EF 9,47
M 122
TY 66,5120
TY 70,18944
M 124
H 2
CV 28,6,7
M 125
CV 29,8,9
M 137
AR 6,48
AR 7,72
AR 8,96
AR 9,120
EF 6,12
EF 7,13
M 136
TX 74,6144
TX 78,19968
EF 8,46
EF 9,47
CV 6,6,7
CV 7,8,9
M 138
WL 4
Q 0
AW 200,6
AW 201,7
AR 6,49
AR 7,73
M 139
Q 2
AR 8,97
AR 9,121
EF 6,12
EF 7,13
M 127
Q 4
EF 8,46
EF 9,47
CV 6,6,7
CV 7,8,9
M 126
TY 82,6656
TY 86,20480
AW 202,6
AW 203,7
AR 6,50
AR 7,74
M 128
Q 6
AR 8,98
AR 9,122
EF 6,12
EF 7,13
M 129
WL 2
Q 8
ST 36,2432
EF 8,46
EF 9,47
CV 6,6,7
M 141
Q 10
CV 7,8,9
AW 204,6
AW 205,7
AR 6,51
M 140
TX 90,7680
TX 94,21504
AR 7,75
AR 8,99
AR 9,123
EF 6,12
M 142
Q 12
EF 7,13
EF 8,46
EF 9,47
CV 6,6,7
M 143
R 54
CV 7,8,9
AW 206,6
AW 207,7
M 131
Q 14
ds_read_b64 v[12:13], v4 offset:5120
M 130
TY 98,8192
TY 102,22016
ds_read_b64 v[46:47], v4 offset:5128
M 0
R 52
M 1
Q 16
M 13
Q 18
M 12
Q 20
M 14
Q 22
M 15
WL 8
Q 24
M 3
Q 26
M 2
Q 28
v_mfma_f32_16x16x32_f16 a[44:47], v[72:75], a[0:3], a[32:35]
Q 30
M 5
Q 32
v_mfma_f32_16x16x32_f16 a[68:71], v[76:79], a[12:15], a[32:35]
Q 34
M 16
Q 36
v_mfma_f32_16x16x32_f16 a[72:75], v[80:83], a[8:11], a[32:35]
Q 38
M 19
WL 2
Q 40
v_mfma_f32_16x16x32_f16 a[48:51], v[84:87], a[4:7], a[32:35]
Q 42
M 6
Q 44
M 8
Q 46
M 9
ae 42,74,384
M 21
s_bitcmp1_b32 s50, 6
s_cselect_b32 s52, 24576, 0
v_add_u32 v50, s52, v3
ST 38,256
M 20
AR 6,140
AR 7,164
M 22
AR 8,188
EF 6,52
M 23
R 58
M 11
EF 7,53
EF 8,54
M 10
WL 2
R 56
M 34
EF 144,55
CV 30,6,7
M 35
CV 31,8,144
AR 6,141
M 32
AR 7,165
AR 8,189
M 33
EF 6,52
EF 7,53
v_mfma_f32_16x16x32_f16 a[96:99], v[80:83], a[16:19], a[32:35]
EF 8,54
EF 145,55
M 31
CV 32,6,7
CV 33,8,145
v_mfma_f32_16x16x32_f16 a[92:95], v[72:75], a[16:19], a[32:35]
AR 6,142
AR 7,166
M 29
ST 40,1024
AR 8,190
M 26
EF 6,52
EF 7,53
M 27
WV 10
EF 8,54
EF 146,55
M 24
WL 0
FP
CV 34,6,7
M 25
CV 35,8,146
AR 6,143
M 36
AR 7,167
AR 8,191
M 37
EF 6,52
EF 7,53
M 38
EF 8,54
EF 147,55
M 39
CV 36,6,7
CV 37,8,147
v_mfma_f32_16x16x32_f16 a[116:119], v[72:75], a[24:27], a[32:35]
AR 6,144
AR 7,168
AR 8,192
EF 6,52
M 41
EF 7,53
EF 8,54
EF 148,55
CV 6,6,7
v_mfma_f32_16x16x32_f16 a[120:123], v[80:83], a[24:27], a[32:35]
CV 7,8,148
AW 208,6
AW 209,7
AR 6,145
M 43
AR 7,169
)"
R"(AR 8,193
EF 6,52
EF 7,53
M 44
ST 42,1792
EF 8,54
EF 149,55
CV 6,6,7
M 45
FR
CV 7,8,149
AW 210,6
AW 211,7
M 46
AR 6,146
AR 7,170
AR 8,194
EF 6,52
M 47
EF 7,53
EF 8,54
EF 150,55
CV 6,6,7
M 58
CV 7,8,150
AW 212,6
AW 213,7
AR 6,147
M 59
AR 7,171
AR 8,195
EF 6,52
EF 7,53
M 56
EF 8,54
EF 151,55
CV 6,6,7
CV 7,8,151
M 57
AW 214,6
AW 215,7
v_mfma_f32_16x16x32_f16 a[144:147], v[80:83], a[0:3], a[32:35]
BV 52,4608
EF 168,12
EF 192,13
M 55
WL 1
FQ 180
EF 216,46
EF 240,47
v_mfma_f32_16x16x32_f16 a[140:143], v[72:75], a[0:3], a[32:35]
CV 6,168,192
CV 7,216,240
AW 216,6
M 53
AW 217,7
EF 169,12
EF 193,13
M 50
FZ 180
R 72
EF 217,46
M 51
mw 24
R 74
ST 44,2560
M 48
TY 106,9736
TY 110,23560
EF 241,47
M 49
R 76
CV 6,169,193
M 60
ad 84,240
W 0
CV 7,217,241
M 61
AW 218,6
AW 219,7
EF 170,12
M 62
EF 194,13
EF 218,46
EF 242,47
M 63
W 1
CV 6,170,194
CV 7,218,242
v_mfma_f32_16x16x32_f16 a[164:167], v[72:75], a[8:11], a[32:35]
AW 220,6
AW 221,7
EF 171,12
M 65
EF 195,13
EF 219,46
EF 243,47
v_mfma_f32_16x16x32_f16 a[168:171], v[80:83], a[8:11], a[32:35]
W 2
CV 6,171,195
CV 7,219,243
M 67
WL 3
AN 104,106
AN 105,107
AW 222,6
M 68
WL 0
SH 106
AN 106,106
AW 223,7
M 69
mw 27
AN 107,107
AN 108,110
EF 172,12
M 70
AN 109,111
SH 110
EF 196,13
M 71
R 78
EF 220,46
M 82
AN 110,110
AN 111,111
EF 244,47
M 83
TY 114,11272
TY 118,25096
CV 38,172,196
M 80
ad 84,246
W 0
CV 39,220,244
M 81
ae 42,74,384
EF 173,12
EF 197,13
v_mfma_f32_16x16x32_f16 a[192:195], v[80:83], a[16:19], a[32:35]
s_bfe_u32 s52, s50, 0x10007
v_mad_i32_i24 v50, v49, s52, v48
ST 14,256
EF 221,46
M 79
W 1
EF 245,47
CV 40,173,197
v_mfma_f32_16x16x32_f16 a[188:191], v[72:75], a[16:19], a[32:35]
CV 41,221,245
EF 174,12
EF 198,13
M 77
EF 222,46
EF 246,47
CV 42,174,198
M 74
W 2
CV 43,222,246
EF 175,12
M 75
EF 199,13
EF 223,46
EF 247,47
M 72
WL 1
AN 112,114
AN 113,115
CV 44,175,199
M 73
R 80
CV 45,223,247
M 84
mw 48
SH 114
AN 114,114
ds_read_b64 v[12:13], v4 offset:4864
M 85
WL 3
AN 115,115
AN 116,118
ds_read_b64 v[46:47], v4 offset:4872
M 86
AN 117,119
SH 118
AR 6,52
M 87
AN 118,118
AN 119,119
AR 7,76
v_mfma_f32_16x16x32_f16 v[144:147], v[72:75], a[24:27], v[128:131]
TY 122,12808
TY 126,26632
AR 8,100
M 89
ad 84,288
W 0
ST 16,1024
v_mfma_f32_16x16x32_f16 v[148:151], v[80:83], a[24:27], v[128:131]
AR 9,124
EF 6,52
EF 7,53
M 91
EF 8,54
EF 9,55
CV 6,6,7
M 92
W 1
CV 7,8,9
AW 224,6
M 93
AW 225,7
AR 6,53
AR 7,77
M 94
AR 8,101
AR 9,125
EF 6,52
M 95
R 82
EF 7,53
M 106
W 2
EF 8,54
EF 9,55
M 107
WL 3
AN 120,122
AN 121,123
CV 6,6,7
M 104
WL 2
SH 122
AN 122,122
CV 7,8,9
M 105
mw 51
AN 123,123
AN 124,126
AW 226,6
v_mfma_f32_16x16x32_f16 v[172:175], v[80:83], a[0:3], v[128:131]
AN 125,127
SH 126
AW 227,7
M 103
AN 126,126
AN 127,127
AR 6,54
v_mfma_f32_16x16x32_f16 v[168:171], v[72:75], a[0:3], v[128:131]
ad 84,294
W 0
ST 18,1792
M 101
AR 7,78
AR 8,102
AR 9,126
M 98
EF 6,52
EF 7,53
EF 8,54
M 99
W 1
EF 9,55
CV 6,6,7
M 96
CV 7,8,9
AW 228,6
AW 229,7
M 97
R 0
AR 6,55
M 108
W 2
AR 7,79
AR 8,103
M 109
AR 9,127
EF 6,52
EF 7,53
M 110
EF 8,54
EF 9,55
CV 6,6,7
M 111
bc 49,4,66,60
CV 7,8,9
AW 230,6
v_mfma_f32_16x16x32_f16 v[192:195], v[72:75], a[8:11], v[128:131]
mh 9
AW 231,7
M 113
ad 66,832
H 0
AR 6,56
v_mfma_f32_16x16x32_f16 v[196:199], v[80:83], a[8:11], v[128:131]
AR 7,80
AR 8,104
AR 9,128
M 115
ST 20,2560
EF 6,52
EF 7,53
M 116
DG 1024
EF 8,54
M 117
EF 9,55
CV 6,6,7
CV 7,8,9
M 118
AW 232,6
AW 233,7
AR 6,57
M 119
WL 0
R 2
AR 7,81
M 120
bc 48,4,64,56
AR 8,105
AR 9,129
M 121
mh 12
EF 6,52
EF 7,53
EF 8,54
M 133
ad 64,832
H 0
EF 9,55
M 132
TX 58,9216
TX 62,23040
CV 6,6,7
M 134
CV 7,8,9
AW 234,6
AW 235,7
M 135
H 1
AR 6,58
AR 7,82
M 123
AR 8,106
AR 9,130
EF 6,52
M 122
TY 66,9728
TY 70,23552
EF 7,53
v_mfma_f32_16x16x32_f16 v[216:219], v[72:75], a[16:19], v[128:131]
H 2
EF 8,54
EF 9,55
M 125
ae 42,72,384
CV 6,6,7
CV 7,8,9
v_mfma_f32_16x16x32_f16 v[240:243], v[76:79], a[28:31], v[128:131]
s_bitcmp1_b32 s50, 8
s_cselect_b32 s52, 24576, 0
v_add_u32 v50, s52, v3
ST 22,0
M 136
TX 74,10752
TX 78,24576
PA 12288
DF
v_mfma_f32_16x16x32_f16 v[244:247], v[80:83], a[24:27], v[128:131]
WL 4
Q 0
AW 236,6
M 139
Q 2
AW 237,7
v_mfma_f32_16x16x32_f16 v[220:223], v[84:87], a[20:23], v[128:131]
Q 4
AR 6,59
M 126
TY 82,11264
TY 86,25088
AR 7,83
M 128
Q 6
AR 8,107
M 129
WL 4
Q 8
AR 9,131
M 141
Q 10
EF 6,52
M 140
TX 90,12288
TX 94,26112
EF 7,53
M 142
Q 12
EF 8,54
M 143
)"
R"(R 6
EF 9,55
M 131
Q 14
CV 6,6,7
M 130
TY 98,12800
TY 102,26624
ST 24,768
M 0
R 4
CV 7,8,9
M 1
WL 8
Q 16
AW 238,6
M 13
Q 18
AW 239,7
M 12
Q 20
BV 52,5120
M 14
Q 22
M 15
Q 24
M 3
Q 26
M 2
Q 28
M 4
Q 30
M 5
WL 5
Q 32
M 17
Q 34
M 16
Q 36
M 18
Q 38
ST 26,1536
M 19
WL 1
Q 40
M 7
Q 42
M 6
Q 44
v_mfma_f32_16x16x32_f16 a[52:55], v[88:91], a[0:3], a[32:35]
Q 46
M 9
AR 6,148
AR 7,172
v_mfma_f32_16x16x32_f16 a[76:79], v[92:95], a[12:15], a[32:35]
AR 8,196
EF 6,12
M 20
EF 7,13
EF 8,46
v_mfma_f32_16x16x32_f16 a[80:83], v[96:99], a[8:11], a[32:35]
EF 152,47
CV 14,6,7
M 23
R 10
v_mfma_f32_16x16x32_f16 a[56:59], v[100:103], a[4:7], a[32:35]
CV 15,8,152
AR 6,149
M 10
R 8
v_mfma_f32_16x16x32_f16 a[104:107], v[96:99], a[16:19], a[32:35]
AR 7,173
AR 8,197
M 35
ST 28,2304
EF 6,12
v_mfma_f32_16x16x32_f16 a[100:103], v[88:91], a[16:19], a[32:35]
EF 7,13
EF 8,46
M 33
EF 153,47
CV 16,6,7
M 30
CV 17,8,153
AR 6,150
M 31
AR 7,174
AR 8,198
M 28
EF 6,12
EF 7,13
M 29
EF 8,46
EF 154,47
M 26
CV 18,6,7
CV 19,8,154
M 27
WV 11
AR 6,151
AR 7,175
M 24
WL 0
FP
AR 8,199
M 25
EF 6,12
EF 7,13
M 36
EF 8,46
EF 155,47
M 37
CV 20,6,7
CV 21,8,155
M 38
ae 42,72,384
ST 30,128
AR 6,152
AR 7,176
M 39
EF 6,12
EF 7,13
EF 132,46
EF 156,47
M 40
CV 22,6,7
CV 23,132,156
AR 6,153
AR 7,177
M 41
EF 6,12
EF 7,13
EF 133,46
EF 157,47
M 42
CV 24,6,7
CV 25,133,157
AR 6,154
AR 7,178
M 43
EF 6,12
EF 7,13
EF 134,46
EF 158,47
v_mfma_f32_16x16x32_f16 a[124:127], v[88:91], a[24:27], a[32:35]
CV 26,6,7
CV 27,134,158
AR 6,155
AR 7,179
M 45
FR
EF 6,12
EF 7,13
EF 135,46
v_mfma_f32_16x16x32_f16 a[128:131], v[96:99], a[24:27], a[32:35]
EF 159,47
CV 28,6,7
CV 29,135,159
M 47
EF 176,52
EF 200,53
v_mfma_f32_16x16x32_f16 a[152:155], v[96:99], a[0:3], a[32:35]
EF 224,54
EF 248,55
M 59
CV 6,176,200
CV 7,224,248
v_mfma_f32_16x16x32_f16 a[148:151], v[88:91], a[0:3], a[32:35]
AW 240,6
AW 241,7
M 57
ST 32,896
EF 177,52
M 54
EF 201,53
EF 225,54
M 55
WL 0
FQ 184
EF 249,55
M 52
CV 6,177,201
CV 7,225,249
M 53
AW 242,6
AW 243,7
M 50
FZ 184
R 24
M 51
mw 72
R 26
M 48
TY 106,520
TY 110,14344
M 49
R 28
M 60
ad 84,336
W 0
M 61
EF 178,52
EF 202,53
M 62
EF 226,54
EF 250,55
M 63
W 1
CV 6,178,202
M 64
ST 34,1664
CV 7,226,250
M 65
AW 244,6
AW 245,7
M 66
W 2
EF 179,52
M 67
WL 3
AN 104,106
AN 105,107
v_mfma_f32_16x16x32_f16 a[172:175], v[88:91], a[8:11], a[32:35]
WL 2
SH 106
AN 106,106
M 69
mw 75
AN 107,107
AN 108,110
v_mfma_f32_16x16x32_f16 a[176:179], v[96:99], a[8:11], a[32:35]
AN 109,111
SH 110
M 71
R 30
v_mfma_f32_16x16x32_f16 v[132:135], v[96:99], a[16:19], v[128:131]
AN 110,110
AN 111,111
M 83
TY 114,2056
TY 118,15880
v_mfma_f32_16x16x32_f16 a[196:199], v[88:91], a[16:19], a[32:35]
ad 84,342
W 0
M 81
EF 203,53
EF 227,54
M 78
EF 251,55
CV 6,179,203
M 79
W 1
ST 36,2432
M 76
CV 7,227,251
AW 246,6
M 77
AW 247,7
M 74
W 2
EF 180,52
EF 204,53
EF 228,54
M 75
EF 252,55
CV 6,180,204
CV 7,228,252
AW 248,6
M 72
WL 1
AN 112,114
AN 113,115
AW 249,7
EF 181,52
M 73
R 32
EF 205,53
EF 229,54
M 84
mw 0
SH 114
AN 114,114
EF 253,55
CV 6,181,205
M 85
WL 2
AN 115,115
AN 116,118
CV 7,229,253
AW 250,6
M 86
AN 117,119
SH 118
AW 251,7
EF 182,52
M 87
AN 118,118
AN 119,119
EF 206,53
EF 230,54
M 88
TY 122,3592
TY 126,17416
EF 254,55
CV 6,182,206
M 89
ad 84,384
W 0
CV 7,230,254
AW 252,6
M 90
ae 42,72,384
AW 253,7
EF 183,52
EF 207,53
M 91
s_bfe_u32 s52, s50, 0x10009
v_mad_i32_i24 v50, v49, s52, v48
ST 38,256
EF 231,54
EF 255,55
v_mfma_f32_16x16x32_f16 v[152:155], v[88:91], a[24:27], v[128:131]
W 1
CV 6,183,207
CV 7,231,255
AW 254,6
M 93
AW 255,7
v_mfma_f32_16x16x32_f16 v[156:159], v[96:99], a[24:27], v[128:131]
M 95
R 34
v_mfma_f32_16x16x32_f16 v[180:183], v[96:99], a[0:3], v[128:131]
W 2
M 107
WL 3
AN 120,122
AN 121,123
v_mfma_f32_16x16x32_f16 v[176:179], v[88:91], a[0:3], v[128:131]
WL 2
SH 122
AN 122,122
M 105
mw 3
AN 123,123
AN 124,126
M 102
AN 125,127
SH 126
M 103
AN 126,126
AN 127,127
M 100
ad 84,390
W 0
M 101
M 98
ST 40,1024
M 99
W 1
M 96
M 97
R 48
M 108
W 2
M 109
M 110
M 111
bc 49,5,66,60
M 112
mh 18
M 113
ad 66,1024
H 0
M 114
M 115
v_mfma_f32_16x16x32_f16 v[200:203], v[88:91], a[8:11], v[128:131]
DG 1024
M 117
ST 42,1792
v_mfma_f32_16x16x32_f16 v[204:207], v[96:99], a[8:11], v[128:131]
M 119
WL 2
R 50
M 120
bc 48,5,64,56
M 121
mh 21
M 133
ad 64,1024
H 0
M 132
TX 58,0
TX 62,13824
M 134
M 135
H 1
M 123
M 122
TY 66,512
TY 70,14336
)"
R"(M 124
H 2
M 125
M 137
ST 44,2560
M 136
TX 74,1536
TX 78,15360
M 138
WL 4
Q 0
M 139
Q 2
M 127
Q 4
M 126
TY 82,2048
TY 86,15872
v_mfma_f32_16x16x32_f16 v[224:227], v[88:91], a[16:19], v[128:131]
Q 6
M 129
WL 4
Q 8
v_mfma_f32_16x16x32_f16 v[248:251], v[92:95], a[28:31], v[128:131]
Q 10
M 140
TX 90,3072
TX 94,16896
v_mfma_f32_16x16x32_f16 v[252:255], v[96:99], a[24:27], v[128:131]
Q 12
M 143
R 54
v_mfma_f32_16x16x32_f16 v[228:231], v[100:103], a[20:23], v[128:131]
Q 14
M 130
TY 98,3584
TY 102,17408
M 0
R 52
M 1
WL 10
Q 16
M 13
Q 18
M 12
Q 20
M 14
Q 22
M 15
WL 8
Q 24
M 3
Q 26
M 2
Q 28
M 4
Q 30
M 5
WL 6
Q 32
M 17
Q 34
M 16
Q 36
M 18
Q 38
M 19
WL 2
Q 40
M 7
Q 42
M 6
Q 44
M 8
Q 46
M 9
ae 42,72,480
M 21
s_bitcmp1_b32 s50, 10
s_cselect_b32 s52, 24576, 0
v_add_u32 v50, s52, v3
ST 14,128
M 20
M 22
M 23
R 58
M 11
M 10
WL 2
R 56
M 34
M 35
M 32
M 33
M 30
M 31
M 28
M 29
ST 16,896
M 26
M 27
WV 9
M 24
WL 0
FP
M 25
M 36
M 37
M 38
M 39
M 40
M 41
M 42
M 43
M 44
ST 18,1664
M 45
FR
M 46
M 47
M 58
M 59
M 56
M 57
M 54
M 55
WL 0
FQ 188
M 52
M 53
M 50
FZ 188
R 72
M 51
mw 24
R 74
ST 20,2432
M 48
TY 106,5128
TY 110,18952
M 49
R 76
M 60
ad 84,432
W 0
M 61
M 62
M 63
W 1
M 64
M 65
M 66
W 2
M 67
WL 3
AN 104,106
AN 105,107
M 68
WL 2
SH 106
AN 106,106
M 69
mw 27
AN 107,107
AN 108,110
M 70
AN 109,111
SH 110
M 71
R 78
M 82
AN 110,110
AN 111,111
M 83
TY 114,6664
TY 118,20488
M 80
ad 84,438
W 0
M 81
ae 42,72,480
M 78
s_bfe_u32 s52, s50, 0x1000b
v_mad_i32_i24 v50, v49, s52, v48
ST 22,128
M 79
W 1
M 76
M 77
M 74
W 2
M 75
M 72
WL 1
AN 112,114
AN 113,115
M 73
R 80
M 84
mw 48
SH 114
AN 114,114
M 85
WL 2
AN 115,115
AN 116,118
M 86
AN 117,119
SH 118
M 87
AN 118,118
AN 119,119
M 88
TY 122,8200
TY 126,22024
M 89
ad 84,480
W 0
ST 24,896
M 90
M 91
M 92
W 1
M 93
M 94
M 95
R 82
M 106
W 2
M 107
WL 3
AN 120,122
AN 121,123
M 104
WL 2
SH 122
AN 122,122
M 105
mw 51
AN 123,123
AN 124,126
M 102
AN 125,127
SH 126
M 103
AN 126,126
AN 127,127
M 100
ad 84,486
W 0
ST 26,1664
M 101
M 98
M 99
W 1
M 96
M 97
R 0
M 108
W 2
M 109
M 110
M 111
bc 49,6,66,60
M 112
mh 0
M 113
ad 66,1216
H 0
M 114
M 115
ST 28,2432
M 116
DG 1024
M 117
M 118
M 119
WL 2
R 2
M 120
bc 48,6,64,56
M 121
mh 3
M 133
ad 64,1216
H 0
M 132
TX 58,4608
TX 62,18432
M 134
M 135
H 1
M 123
M 122
TY 66,5120
TY 70,18944
M 124
H 2
M 125
ae 44,70,768
M 137
s_bitcmp1_b32 s50, 12
s_cselect_b32 s51, 24576, 0
v_add_u32 v47, s51, v3
M 136
TX 74,6144
TX 78,19968
PA 12288
DF
M 138
WL 4
Q 0
M 139
Q 2
M 127
Q 4
M 126
TY 82,6656
TY 86,20480
M 128
Q 6
M 129
WL 4
Q 8
M 141
Q 10
M 140
TX 90,7680
TX 94,21504
M 142
Q 12
M 143
R 6
M 131
Q 14
M 130
TY 98,8192
TY 102,22016
M 0
R 4
M 1
WL 10
Q 16
M 13
Q 18
M 12
Q 20
M 14
Q 22
M 15
WL 8
Q 24
M 3
Q 26
M 2
Q 28
M 4
Q 30
M 5
WL 6
Q 32
M 17
Q 34
M 16
Q 36
M 18
Q 38
M 19
WL 2
Q 40
M 7
Q 42
M 6
Q 44
M 8
Q 46
M 9
XL 30,0
M 21
XL 32,768
M 20
XL 34,1536
M 22
XL 36,2304
M 23
R 10
M 11
M 10
WL 2
R 8
M 34
M 35
M 32
M 33
M 30
M 31
M 28
M 29
M 26
M 27
)"
R"(WV 11
M 24
WL 0
FP
M 25
M 36
ae 42,72,384
M 37
s_bfe_u32 s52, s50, 0x10009
v_mad_i32_i24 v50, v49, s52, v48
SA 200,0
M 38
M 39
M 40
M 41
M 42
M 43
M 44
M 45
FR
M 46
M 47
M 58
M 59
M 56
M 57
M 54
M 55
WL 0
FQ 192
M 52
M 53
M 50
FZ 192
R 24
M 51
mw 72
R 26
M 48
TY 106,9736
TY 110,23560
M 49
R 28
M 60
ad 84,528
W 0
M 61
M 62
M 63
W 1
M 64
M 65
M 66
W 2
M 67
WL 3
AN 104,106
AN 105,107
M 68
WL 2
SH 106
AN 106,106
M 69
mw 75
AN 107,107
AN 108,110
M 70
AN 109,111
SH 110
M 71
R 30
M 82
AN 110,110
AN 111,111
M 83
TY 114,11272
TY 118,25096
M 80
ad 84,534
W 0
M 81
M 78
M 79
W 1
M 76
M 77
M 74
W 2
M 75
M 72
WL 1
AN 112,114
AN 113,115
M 73
R 32
M 84
mw 0
SH 114
AN 114,114
M 85
WL 2
AN 115,115
AN 116,118
M 86
AN 117,119
SH 118
M 87
AN 118,118
AN 119,119
M 88
TY 122,12808
TY 126,26632
M 89
ad 84,576
W 0
M 90
M 91
M 92
W 1
M 93
M 94
M 95
R 34
M 106
W 2
M 107
WL 3
AN 120,122
AN 121,123
M 104
WL 2
SH 122
AN 122,122
M 105
mw 3
AN 123,123
AN 124,126
M 102
AN 125,127
SH 126
M 103
AN 126,126
AN 127,127
M 100
ad 84,582
W 0
M 101
SA 202,768
M 98
M 99
W 1
M 96
M 97
R 48
M 108
W 2
M 109
M 110
M 111
bc 49,7,66,60
M 112
mh 9
M 113
ad 66,1408
H 0
M 114
M 115
M 116
DG 1024
M 117
M 118
M 119
WL 2
R 50
M 120
bc 48,7,64,56
M 121
mh 12
M 133
ad 64,1408
H 0
M 132
TX 58,9216
TX 62,23040
M 134
M 135
H 1
M 123
M 122
TY 66,9728
TY 70,23552
M 124
H 2
M 125
M 137
ae 44,70,768
M 136
TX 74,10752
TX 78,24576
M 138
s_bfe_u32 s51, s50, 0x1000d
WL 4
Q 0
M 139
Q 2
M 127
Q 4
M 126
TY 82,11264
TY 86,25088
M 128
Q 6
M 129
WL 4
Q 8
M 141
Q 10
M 140
TX 90,12288
TX 94,26112
M 142
Q 12
M 143
R 54
M 131
Q 14
M 130
TY 98,12800
TY 102,26624
M 0
R 52
M 1
WL 10
Q 16
M 13
Q 18
M 12
Q 20
M 14
Q 22
M 15
WL 8
Q 24
M 3
Q 26
M 2
Q 28
M 4
Q 30
M 5
WL 6
Q 32
M 17
Q 34
M 16
Q 36
M 18
Q 38
M 19
WL 2
Q 40
M 7
Q 42
M 6
Q 44
M 8
Q 46
M 9
v_mad_i32_i24 v47, v49, s51, v48
XL 38,0
M 21
XL 40,768
M 20
XL 42,1536
M 22
XL 44,2304
M 23
R 58
M 11
M 10
WL 2
R 56
M 34
SA 204,1536
M 35
M 32
M 33
M 30
M 31
M 28
M 29
M 26
M 27
WV 10
M 24
WL 0
FP
M 25
M 36
M 37
M 38
M 39
M 40
UL 6,30
UH 7,30
M 41
AR 8,36
AR 9,60
M 42
AD 8,6
AD 9,7
M 43
AW 36,8
AW 60,9
M 44
UL 6,31
UH 7,31
M 45
FR
AR 8,84
M 46
AR 9,108
AD 8,6
M 47
AD 9,7
AW 84,8
M 58
AW 108,9
UL 6,32
M 59
UH 7,32
AR 8,37
M 56
AR 9,61
AD 8,6
M 57
AD 9,7
AW 37,8
M 54
AW 61,9
UL 6,33
M 55
WL 0
FQ 196
UH 7,33
M 52
AR 8,85
AR 9,109
M 53
AD 8,6
AD 9,7
M 50
FZ 196
R 72
M 51
mw 24
R 74
M 48
TY 106,520
TY 110,14344
M 49
R 76
M 60
ad 84,624
W 0
M 61
AW 85,8
AW 109,9
M 62
UL 6,34
UH 7,34
M 63
W 1
AR 8,38
M 64
AR 9,62
AD 8,6
M 65
AD 9,7
AW 38,8
M 66
W 2
AW 62,9
M 67
WL 3
AN 104,106
AN 105,107
M 68
WL 2
SH 106
AN 106,106
M 69
mw 27
AN 107,107
AN 108,110
M 70
AN 109,111
SH 110
M 71
R 78
)"
R"(M 82
AN 110,110
AN 111,111
M 83
TY 114,2056
TY 118,15880
M 80
ad 84,630
W 0
M 81
UL 6,35
UH 7,35
M 78
AR 8,86
AR 9,110
M 79
W 1
AD 8,6
M 76
AD 9,7
AW 86,8
M 77
AW 110,9
UL 6,36
M 74
W 2
UH 7,36
M 75
AR 8,39
AR 9,63
M 72
WL 1
AN 112,114
AN 113,115
M 73
R 80
M 84
mw 48
SH 114
AN 114,114
M 85
WL 2
AN 115,115
AN 116,118
M 86
AN 117,119
SH 118
M 87
AN 118,118
AN 119,119
M 88
TY 122,3592
TY 126,17416
M 89
ad 84,672
W 0
M 90
SA 206,2304
AD 8,6
M 91
AD 9,7
AW 39,8
M 92
W 1
AW 63,9
M 93
UL 6,37
UH 7,37
M 94
AR 8,87
AR 9,111
M 95
R 82
M 106
W 2
AD 8,6
M 107
WL 3
AN 120,122
AN 121,123
M 104
WL 2
SH 122
AN 122,122
M 105
mw 51
AN 123,123
AN 124,126
M 102
AN 125,127
SH 126
M 103
AN 126,126
AN 127,127
M 100
ad 84,678
W 0
M 101
AD 9,7
AW 87,8
M 98
AW 111,9
M 99
W 1
M 96
M 97
R 0
M 108
W 2
M 109
M 110
M 111
bc 49,8,66,60
M 112
mh 18
M 113
ad 66,1600
H 0
M 114
M 115
M 116
DG 1024
M 117
M 118
M 119
WL 2
R 2
M 120
bc 48,8,64,56
M 121
mh 21
M 133
ad 64,1600
H 0
M 132
TX 58,0
TX 62,13824
M 134
M 135
H 1
M 123
M 122
TY 66,512
TY 70,14336
M 124
H 2
M 125
M 137
M 136
TX 74,1536
TX 78,15360
PA 12288
DF
M 138
WL 4
Q 0
M 139
Q 2
M 127
Q 4
M 126
TY 82,2048
TY 86,15872
M 128
Q 6
M 129
WL 4
Q 8
M 141
Q 10
M 140
TX 90,3072
TX 94,16896
M 142
Q 12
M 143
R 6
M 131
Q 14
M 130
TY 98,3584
TY 102,17408
M 0
R 4
M 1
WL 10
Q 16
M 13
Q 18
M 12
Q 20
M 14
Q 22
M 15
WL 8
Q 24
M 3
Q 26
M 2
Q 28
M 4
Q 30
M 5
WL 6
Q 32
M 17
Q 34
M 16
Q 36
M 18
Q 38
M 19
WL 2
Q 40
M 7
Q 42
M 6
Q 44
M 8
Q 46
M 9
ae 42,72,384
M 21
SA 208,128
M 20
ae 44,68,768
M 22
s_bitcmp1_b32 s50, 14
s_cselect_b32 s51, 24576, 0
v_add_u32 v47, s51, v3
M 23
R 10
M 11
XL 14,0
M 10
WL 2
R 8
M 34
XL 16,768
M 35
XL 18,1536
M 32
XL 20,2304
M 33
M 30
M 31
M 28
M 29
M 26
M 27
WV 11
M 24
WL 0
FP
M 25
M 36
M 37
M 38
M 39
M 40
M 41
M 42
UL 6,38
UH 7,38
M 43
AR 8,40
AR 9,64
M 44
AD 8,6
AD 9,7
M 45
FR
AW 40,8
M 46
AW 64,9
UL 6,39
M 47
UH 7,39
AR 8,88
M 58
AR 9,112
AD 8,6
M 59
AD 9,7
AW 88,8
M 56
AW 112,9
UL 6,40
M 57
UH 7,40
AR 8,41
M 54
AR 9,65
AD 8,6
M 55
WL 0
FQ 200
AD 9,7
M 52
AW 41,8
AW 65,9
M 53
UL 6,41
UH 7,41
M 50
FZ 200
R 24
M 51
mw 72
R 26
M 48
TY 106,5128
TY 110,18952
M 49
R 28
M 60
ad 84,720
W 0
M 61
AR 8,89
AR 9,113
M 62
AD 8,6
AD 9,7
M 63
W 1
AW 89,8
M 64
AW 113,9
UL 6,42
M 65
UH 7,42
AR 8,42
M 66
W 2
AR 9,66
M 67
WL 3
AN 104,106
AN 105,107
M 68
WL 2
SH 106
AN 106,106
M 69
mw 75
AN 107,107
AN 108,110
M 70
AN 109,111
SH 110
M 71
R 30
M 82
AN 110,110
AN 111,111
M 83
TY 114,6664
TY 118,20488
M 80
ad 84,726
W 0
M 81
AD 8,6
AD 9,7
M 78
AW 42,8
AW 66,9
M 79
W 1
UL 6,43
M 76
UH 7,43
AR 8,90
M 77
AR 9,114
AD 8,6
M 74
W 2
AD 9,7
M 75
AW 90,8
AW 114,9
M 72
WL 1
AN 112,114
AN 113,115
M 73
R 32
M 84
mw 0
SH 114
AN 114,114
M 85
WL 2
AN 115,115
AN 116,118
M 86
AN 117,119
SH 118
)"
R"(M 87
AN 118,118
AN 119,119
M 88
TY 122,8200
TY 126,22024
M 89
ad 84,768
W 0
M 90
SA 210,896
UL 6,44
M 91
UH 7,44
AR 8,43
M 92
W 1
AR 9,67
M 93
AD 8,6
AD 9,7
M 94
AW 43,8
AW 67,9
M 95
R 34
M 106
W 2
UL 6,45
M 107
WL 3
AN 120,122
AN 121,123
M 104
WL 2
SH 122
AN 122,122
M 105
mw 3
AN 123,123
AN 124,126
M 102
AN 125,127
SH 126
M 103
AN 126,126
AN 127,127
M 100
ad 84,774
W 0
M 101
UH 7,45
AR 8,91
M 98
AR 9,115
AD 8,6
M 99
W 1
AD 9,7
M 96
AW 91,8
AW 115,9
M 97
R 48
M 108
W 2
M 109
M 110
M 111
bc 49,9,66,60
M 112
mh 0
M 113
ad 66,1792
H 0
M 114
M 115
M 116
DG 1024
M 117
M 118
M 119
WL 2
R 50
M 120
bc 48,9,64,56
M 121
mh 3
M 133
ad 64,1792
H 0
M 132
TX 58,4608
TX 62,18432
M 134
M 135
H 1
M 123
M 122
TY 66,5120
TY 70,18944
M 124
H 2
M 125
M 137
M 136
TX 74,6144
TX 78,19968
M 138
WL 4
Q 0
M 139
Q 2
M 127
Q 4
M 126
TY 82,6656
TY 86,20480
M 128
Q 6
M 129
WL 4
Q 8
M 141
Q 10
M 140
TX 90,7680
TX 94,21504
M 142
Q 12
M 143
R 54
M 131
Q 14
M 130
TY 98,8192
TY 102,22016
M 0
R 52
M 1
WL 10
Q 16
M 13
Q 18
M 12
Q 20
M 14
Q 22
M 15
WL 8
Q 24
M 3
Q 26
M 2
Q 28
M 4
Q 30
M 5
WL 6
Q 32
M 17
Q 34
M 16
Q 36
M 18
Q 38
M 19
WL 2
Q 40
M 7
Q 42
M 6
Q 44
M 8
Q 46
M 9
SA 212,1664
M 21
ae 44,68,768
M 20
s_bfe_u32 s51, s50, 0x1000f
v_mad_i32_i24 v47, v49, s51, v48
XL 22,0
M 22
XL 24,768
M 23
R 58
M 11
XL 26,1536
M 10
WL 2
R 56
M 34
XL 28,2304
M 35
M 32
M 33
M 30
M 31
M 28
M 29
M 26
M 27
WV 10
M 24
WL 0
FP
M 25
M 36
M 37
M 38
M 39
M 40
M 41
M 42
M 43
M 44
UL 6,14
UH 7,14
M 45
FR
AR 8,44
M 46
AR 9,68
AD 8,6
M 47
AD 9,7
AW 44,8
M 58
AW 68,9
UL 6,15
M 59
UH 7,15
AR 8,92
M 56
AR 9,116
AD 8,6
M 57
AD 9,7
AW 92,8
M 54
AW 116,9
UL 6,16
M 55
WL 0
FQ 204
UH 7,16
M 52
AR 8,45
AR 9,69
M 53
AD 8,6
AD 9,7
M 50
FZ 204
R 72
M 51
mw 24
R 74
M 48
TY 106,9736
TY 110,23560
M 49
R 76
M 60
ad 84,816
W 0
M 61
AW 45,8
AW 69,9
M 62
UL 6,17
UH 7,17
M 63
W 1
AR 8,93
M 64
AR 9,117
AD 8,6
M 65
AD 9,7
AW 93,8
M 66
W 2
AW 117,9
M 67
WL 3
AN 104,106
AN 105,107
M 68
WL 2
SH 106
AN 106,106
M 69
mw 27
AN 107,107
AN 108,110
M 70
AN 109,111
SH 110
M 71
R 78
M 82
AN 110,110
AN 111,111
M 83
TY 114,11272
TY 118,25096
M 80
ad 84,822
W 0
M 81
UL 6,18
UH 7,18
M 78
AR 8,46
AR 9,70
M 79
W 1
AD 8,6
M 76
AD 9,7
AW 46,8
M 77
AW 70,9
UL 6,19
M 74
W 2
UH 7,19
M 75
SA 214,2432
AR 8,94
M 72
WL 1
AN 112,114
AN 113,115
M 73
R 80
M 84
mw 48
SH 114
AN 114,114
M 85
WL 2
AN 115,115
AN 116,118
M 86
AN 117,119
SH 118
M 87
AN 118,118
AN 119,119
M 88
TY 122,12808
TY 126,26632
M 89
ad 84,864
W 0
M 90
AR 9,118
AD 8,6
M 91
AD 9,7
AW 94,8
M 92
W 1
AW 118,9
M 93
UL 6,20
UH 7,20
M 94
AR 8,47
AR 9,71
M 95
R 82
M 106
W 2
AD 8,6
M 107
WL 3
AN 120,122
AN 121,123
M 104
WL 2
SH 122
AN 122,122
M 105
mw 51
AN 123,123
AN 124,126
M 102
AN 125,127
SH 126
M 103
AN 126,126
AN 127,127
M 100
ad 84,870
W 0
M 101
)"
R"(AD 9,7
AW 47,8
M 98
AW 71,9
UL 6,21
M 99
W 1
UH 7,21
M 96
AR 8,95
AR 9,119
M 97
R 0
M 108
W 2
AD 8,6
M 109
AD 9,7
AW 95,8
M 110
AW 119,9
M 111
bc 49,10,66,60
M 112
mh 9
M 113
ad 66,1984
H 0
M 114
M 115
M 116
DG 1024
M 117
M 118
M 119
WL 2
R 2
M 120
bc 48,10,64,56
M 121
mh 12
M 133
ad 64,1984
H 0
M 132
TX 58,9216
TX 62,23040
M 134
M 135
H 1
M 123
M 122
TY 66,9728
TY 70,23552
M 124
H 2
M 125
M 137
M 136
TX 74,10752
TX 78,24576
PA 12288
DF
M 138
WL 4
Q 0
M 139
Q 2
M 127
Q 4
M 126
TY 82,11264
TY 86,25088
M 128
Q 6
M 129
WL 4
Q 8
M 141
Q 10
M 140
TX 90,12288
TX 94,26112
M 142
Q 12
M 143
R 6
M 131
Q 14
M 130
TY 98,12800
TY 102,26624
M 0
R 4
M 1
WL 10
Q 16
M 13
Q 18
M 12
Q 20
M 14
Q 22
M 15
WL 8
Q 24
M 3
Q 26
M 2
Q 28
M 4
Q 30
M 5
WL 6
Q 32
M 17
Q 34
M 16
Q 36
M 18
Q 38
M 19
WL 2
Q 40
M 7
Q 42
M 6
Q 44
M 8
Q 46
M 9
ae 42,72,384
M 21
s_bitcmp1_b32 s50, 8
s_cselect_b32 s52, 24576, 0
v_add_u32 v50, s52, v3
M 20
SA 216,256
M 22
ae 44,68,864
M 23
R 10
M 11
s_bitcmp1_b32 s50, 16
s_cselect_b32 s51, 24576, 0
v_add_u32 v47, s51, v3
M 10
WL 2
R 8
M 34
XL 30,0
M 35
XL 32,768
M 32
XL 34,1536
M 33
XL 36,2304
M 30
M 31
M 28
M 29
M 26
M 27
WV 11
M 24
WL 0
FP
M 25
M 36
M 37
M 38
M 39
M 40
M 41
M 42
M 43
M 44
M 45
FR
M 46
UL 6,22
UH 7,22
M 47
AR 8,48
AR 9,72
M 58
AD 8,6
AD 9,7
M 59
AW 48,8
AW 72,9
M 56
UL 6,23
UH 7,23
M 57
AR 8,96
AR 9,120
M 54
AD 8,6
AD 9,7
M 55
WL 0
FQ 208
AW 96,8
M 52
AW 120,9
UL 6,24
M 53
UH 7,24
AR 8,49
M 50
FZ 208
R 24
M 51
mw 72
R 26
M 48
TY 106,520
TY 110,14344
M 49
R 28
M 60
ad 84,912
W 0
M 61
AR 9,73
AD 8,6
M 62
AD 9,7
AW 49,8
M 63
W 1
AW 73,9
M 64
UL 6,25
UH 7,25
M 65
AR 8,97
AR 9,121
M 66
W 2
AD 8,6
M 67
WL 3
AN 104,106
AN 105,107
M 68
WL 2
SH 106
AN 106,106
M 69
mw 75
AN 107,107
AN 108,110
M 70
AN 109,111
SH 110
M 71
R 30
M 82
AN 110,110
AN 111,111
M 83
TY 114,2056
TY 118,15880
M 80
ad 84,918
W 0
M 81
AD 9,7
AW 97,8
M 78
AW 121,9
UL 6,26
M 79
W 1
UH 7,26
M 76
AR 8,50
AR 9,74
M 77
AD 8,6
AD 9,7
M 74
W 2
AW 50,8
M 75
AW 74,9
UL 6,27
M 72
WL 1
AN 112,114
AN 113,115
M 73
R 32
M 84
mw 0
SH 114
AN 114,114
M 85
WL 2
AN 115,115
AN 116,118
M 86
AN 117,119
SH 118
M 87
AN 118,118
AN 119,119
M 88
TY 122,3592
TY 126,17416
M 89
ad 84,960
W 0
M 90
SA 218,1024
UH 7,27
M 91
AR 8,98
AR 9,122
M 92
W 1
AD 8,6
M 93
AD 9,7
AW 98,8
M 94
AW 122,9
UL 6,28
M 95
R 34
M 106
W 2
UH 7,28
M 107
WL 3
AN 120,122
AN 121,123
M 104
WL 2
SH 122
AN 122,122
M 105
mw 3
AN 123,123
AN 124,126
M 102
AN 125,127
SH 126
M 103
AN 126,126
AN 127,127
M 100
ad 84,966
W 0
M 101
AR 8,51
AR 9,75
M 98
AD 8,6
AD 9,7
M 99
W 1
AW 51,8
M 96
AW 75,9
UL 6,29
M 97
R 48
M 108
W 2
UH 7,29
M 109
AR 8,99
AR 9,123
M 110
AD 8,6
AD 9,7
M 111
bc 49,11,66,60
AW 99,8
M 112
mh 18
AW 123,9
M 113
ad 66,2176
H 0
M 114
M 115
M 116
DG 1024
M 117
M 118
M 119
WL 2
R 50
M 120
bc 48,11,64,56
M 121
)"
R"(mh 21
M 133
ad 64,2176
H 0
M 132
TX 58,0
TX 62,13824
M 134
M 135
H 1
M 123
M 122
TY 66,512
TY 70,14336
M 124
H 2
M 125
M 137
M 136
TX 74,1536
TX 78,15360
M 138
WL 4
Q 0
M 139
Q 2
M 127
Q 4
M 126
TY 82,2048
TY 86,15872
M 128
Q 6
M 129
WL 4
Q 8
M 141
Q 10
M 140
TX 90,3072
TX 94,16896
M 142
Q 12
M 143
R 54
M 131
Q 14
M 130
TY 98,3584
TY 102,17408
M 0
R 52
M 1
WL 10
Q 16
M 13
Q 18
M 12
Q 20
M 14
Q 22
M 15
WL 8
Q 24
M 3
Q 26
M 2
Q 28
M 4
Q 30
M 5
WL 6
Q 32
M 17
Q 34
M 16
Q 36
M 18
Q 38
M 19
WL 2
Q 40
M 7
Q 42
M 6
Q 44
M 8
Q 46
M 9
SA 220,1792
M 21
ae 44,68,864
M 20
s_bfe_u32 s51, s50, 0x10011
v_mad_i32_i24 v47, v49, s51, v48
XL 38,0
M 22
XL 40,768
M 23
R 58
M 11
XL 42,1536
M 10
WL 2
R 56
M 34
XL 44,2304
M 35
M 32
M 33
M 30
M 31
M 28
M 29
M 26
M 27
WV 10
M 24
WL 0
FP
M 25
M 36
M 37
M 38
M 39
M 40
M 41
M 42
M 43
M 44
M 45
FR
M 46
M 47
M 58
UL 6,30
UH 7,30
M 59
AR 8,52
AR 9,76
M 56
AD 8,6
AD 9,7
M 57
AW 52,8
AW 76,9
M 54
UL 6,31
UH 7,31
M 55
WL 0
FQ 212
AR 8,100
M 52
AR 9,124
AD 8,6
M 53
AD 9,7
AW 100,8
M 50
FZ 212
R 72
M 51
mw 24
R 74
M 48
TY 106,5128
TY 110,18952
M 49
R 76
M 60
ad 84,1008
W 0
M 61
AW 124,9
UL 6,32
M 62
UH 7,32
AR 8,53
M 63
W 1
AR 9,77
M 64
AD 8,6
AD 9,7
M 65
AW 53,8
AW 77,9
M 66
W 2
UL 6,33
M 67
WL 3
AN 104,106
AN 105,107
M 68
WL 2
SH 106
AN 106,106
M 69
mw 27
AN 107,107
AN 108,110
M 70
AN 109,111
SH 110
M 71
R 78
M 82
AN 110,110
AN 111,111
M 83
TY 114,6664
TY 118,20488
M 80
ad 84,1014
W 0
M 81
UH 7,33
AR 8,101
M 78
AR 9,125
AD 8,6
M 79
W 1
AD 9,7
M 76
AW 101,8
AW 125,9
M 77
UL 6,34
UH 7,34
M 74
W 2
AR 8,54
M 75
SA 222,2560
AR 9,78
M 72
WL 1
AN 112,114
AN 113,115
M 73
R 80
M 84
mw 48
SH 114
AN 114,114
M 85
WL 2
AN 115,115
AN 116,118
M 86
AN 117,119
SH 118
M 87
AN 118,118
AN 119,119
M 88
TY 122,8200
TY 126,22024
M 89
ad 84,1056
W 0
M 90
AD 8,6
AD 9,7
M 91
AW 54,8
AW 78,9
M 92
W 1
UL 6,35
M 93
UH 7,35
AR 8,102
M 94
AR 9,126
AD 8,6
M 95
R 82
M 106
W 2
AD 9,7
M 107
WL 3
AN 120,122
AN 121,123
M 104
WL 2
SH 122
AN 122,122
M 105
mw 51
AN 123,123
AN 124,126
M 102
AN 125,127
SH 126
M 103
AN 126,126
AN 127,127
M 100
ad 84,1062
W 0
M 101
AW 102,8
AW 126,9
M 98
UL 6,36
UH 7,36
M 99
W 1
AR 8,55
M 96
AR 9,79
AD 8,6
M 97
R 0
M 108
W 2
AD 9,7
M 109
AW 55,8
AW 79,9
M 110
UL 6,37
UH 7,37
M 111
bc 49,12,66,60
AR 8,103
M 112
mh 0
AR 9,127
AD 8,6
M 113
ad 66,2368
H 0
M 114
AD 9,7
AW 103,8
M 115
AW 127,9
M 116
DG 1024
M 117
M 118
M 119
WL 2
R 2
M 120
bc 48,12,64,56
M 121
mh 3
M 133
ad 64,2368
H 0
M 132
TX 58,4608
TX 62,18432
M 134
M 135
H 1
M 123
M 122
TY 66,5120
TY 70,18944
M 124
H 2
M 125
M 137
M 136
TX 74,6144
TX 78,19968
PA 12288
DF
M 138
WL 4
Q 0
M 139
Q 2
M 127
Q 4
M 126
TY 82,6656
TY 86,20480
M 128
Q 6
M 129
WL 4
Q 8
M 141
Q 10
M 140
TX 90,7680
TX 94,21504
M 142
Q 12
M 143
R 6
)"
R"(M 131
Q 14
M 130
TY 98,8192
TY 102,22016
M 0
R 4
M 1
WL 10
Q 16
M 13
Q 18
M 12
Q 20
M 14
Q 22
M 15
WL 8
Q 24
M 3
Q 26
M 2
Q 28
M 4
Q 30
M 5
WL 6
Q 32
M 17
Q 34
M 16
Q 36
M 18
Q 38
M 19
WL 2
Q 40
M 7
Q 42
M 6
Q 44
M 8
Q 46
M 9
ae 42,72,480
M 21
s_bitcmp1_b32 s50, 10
s_cselect_b32 s52, 24576, 0
v_add_u32 v50, s52, v3
M 20
SA 224,0
M 22
M 23
R 10
M 11
M 10
WL 2
R 8
M 34
M 35
M 32
M 33
M 30
M 31
M 28
M 29
M 26
ae 44,70,768
M 27
WV 7
M 24
WL 0
FP
M 25
M 36
s_bitcmp1_b32 s50, 12
s_cselect_b32 s51, 24576, 0
v_add_u32 v47, s51, v3
M 37
XL 14,128
M 38
XL 16,896
M 39
M 40
M 41
M 42
M 43
M 44
XL 18,1664
M 45
FR
M 46
M 47
M 58
M 59
M 56
XL 20,2432
UL 6,38
M 57
UH 7,38
AR 8,56
M 54
AR 9,80
AD 8,6
M 55
WL 0
FQ 216
AD 9,7
M 52
AW 56,8
AW 80,9
M 53
UL 6,39
UH 7,39
M 50
FZ 216
R 24
M 51
mw 72
R 26
M 48
TY 106,9736
TY 110,23560
M 49
R 28
M 60
ad 84,1104
W 0
M 61
AR 8,104
AR 9,128
M 62
AD 8,6
AD 9,7
M 63
W 1
AW 104,8
M 64
AW 128,9
UL 6,40
M 65
UH 7,40
AR 8,57
M 66
W 2
AR 9,81
M 67
WL 3
AN 104,106
AN 105,107
M 68
WL 2
SH 106
AN 106,106
M 69
mw 75
AN 107,107
AN 108,110
M 70
AN 109,111
SH 110
M 71
R 30
M 82
AN 110,110
AN 111,111
M 83
TY 114,11272
TY 118,25096
M 80
ad 84,1110
W 0
M 81
AD 8,6
AD 9,7
M 78
AW 57,8
AW 81,9
M 79
W 1
UL 6,41
M 76
UH 7,41
AR 8,105
M 77
AR 9,129
AD 8,6
M 74
W 2
AD 9,7
M 75
AW 105,8
AW 129,9
M 72
WL 1
AN 112,114
AN 113,115
M 73
R 32
M 84
mw 0
SH 114
AN 114,114
M 85
WL 2
AN 115,115
AN 116,118
M 86
AN 117,119
SH 118
M 87
AN 118,118
AN 119,119
M 88
TY 122,12808
TY 126,26632
M 89
ad 84,1152
W 0
M 90
SA 226,768
UL 6,42
M 91
UH 7,42
AR 8,58
M 92
W 1
AR 9,82
M 93
AD 8,6
AD 9,7
M 94
AW 58,8
AW 82,9
M 95
R 34
M 106
W 2
UL 6,43
M 107
WL 3
AN 120,122
AN 121,123
M 104
WL 2
SH 122
AN 122,122
M 105
mw 3
AN 123,123
AN 124,126
M 102
AN 125,127
SH 126
M 103
AN 126,126
AN 127,127
M 100
ad 84,1158
W 0
M 101
UH 7,43
AR 8,106
M 98
AR 9,130
AD 8,6
M 99
W 1
AD 9,7
M 96
AW 106,8
AW 130,9
M 97
R 48
M 108
W 2
UL 6,44
M 109
UH 7,44
AR 8,59
M 110
AR 9,83
AD 8,6
M 111
bc 49,13,66,60
AD 9,7
M 112
mh 9
AW 59,8
AW 83,9
M 113
ad 66,2560
H 0
M 114
UL 6,45
UH 7,45
M 115
AR 8,107
AR 9,131
M 116
DG 1024
M 117
AD 8,6
AD 9,7
M 118
AW 107,8
AW 131,9
M 119
WL 0
R 50
M 120
bc 48,13,64,56
M 121
mh 12
M 133
ad 64,2560
H 0
M 132
TX 58,9216
TX 62,23040
M 134
M 135
H 1
M 123
M 122
TY 66,9728
TY 70,23552
M 124
H 2
M 125
M 137
M 136
TX 74,10752
TX 78,24576
M 138
WL 4
Q 0
M 139
Q 2
M 127
Q 4
M 126
TY 82,11264
TY 86,25088
M 128
Q 6
M 129
WL 4
Q 8
M 141
Q 10
M 140
TX 90,12288
TX 94,26112
M 142
Q 12
M 143
R 54
M 131
Q 14
M 130
TY 98,12800
TY 102,26624
M 0
R 52
M 1
WL 10
Q 16
M 13
Q 18
M 12
Q 20
M 14
Q 22
M 15
WL 8
Q 24
M 3
Q 26
M 2
Q 28
M 4
Q 30
M 5
WL 6
Q 32
M 17
Q 34
M 16
Q 36
M 18
Q 38
M 19
WL 2
Q 40
M 7
Q 42
M 6
Q 44
M 8
)"
R"(Q 46
M 9
SA 228,1536
M 21
M 20
M 22
M 23
R 58
M 11
M 10
WL 2
R 56
M 34
M 35
M 32
M 33
M 30
M 31
M 28
M 29
M 26
M 27
WV 6
M 24
WL 0
FP
M 25
M 36
ae 44,70,768
M 37
s_bfe_u32 s51, s50, 0x1000d
v_mad_i32_i24 v47, v49, s51, v48
XL 22,128
M 38
M 39
M 40
XL 24,896
M 41
M 42
M 43
M 44
M 45
FR
M 46
XL 26,1664
M 47
M 58
M 59
M 56
M 57
M 54
XL 28,2432
M 55
WL 0
FQ 220
M 52
M 53
M 50
FZ 220
R 72
M 51
mw 24
R 74
M 48
TY 106,520
TY 110,14344
M 49
R 76
M 60
ad 84,1200
W 0
M 61
M 62
M 63
W 1
M 64
M 65
M 66
W 2
M 67
WL 3
AN 104,106
AN 105,107
M 68
WL 2
SH 106
AN 106,106
M 69
mw 27
AN 107,107
AN 108,110
M 70
AN 109,111
SH 110
M 71
R 78
M 82
AN 110,110
AN 111,111
M 83
TY 114,2056
TY 118,15880
M 80
ad 84,1206
W 0
M 81
M 78
M 79
W 1
M 76
M 77
M 74
W 2
M 75
SA 230,2304
M 72
WL 1
AN 112,114
AN 113,115
M 73
R 80
M 84
mw 48
SH 114
AN 114,114
M 85
WL 2
AN 115,115
AN 116,118
M 86
AN 117,119
SH 118
M 87
AN 118,118
AN 119,119
M 88
TY 122,3592
TY 126,17416
M 89
ad 84,1248
W 0
M 90
UL 6,14
UH 7,14
M 91
AR 8,132
AR 9,156
M 92
W 1
AD 8,6
M 93
AD 9,7
AW 132,8
M 94
AW 156,9
UL 6,15
M 95
R 82
M 106
W 2
UH 7,15
M 107
WL 3
AN 120,122
AN 121,123
M 104
WL 2
SH 122
AN 122,122
M 105
mw 51
AN 123,123
AN 124,126
M 102
AN 125,127
SH 126
M 103
AN 126,126
AN 127,127
M 100
ad 84,1254
W 0
M 101
AR 8,180
AD 8,6
M 98
AD 136,7
AW 180,8
M 99
W 1
UL 6,16
M 96
UH 7,16
AR 8,133
M 97
R 0
M 108
W 2
AR 9,157
M 109
AD 8,6
AD 9,7
M 110
AW 133,8
AW 157,9
M 111
bc 49,14,66,60
UL 6,17
M 112
mh 18
UH 7,17
AR 8,181
M 113
ad 66,2752
H 0
M 114
AD 8,6
AD 137,7
M 115
AW 181,8
UL 6,18
M 116
DG 1024
M 117
UH 7,18
AR 8,134
M 118
AR 9,158
AD 8,6
M 119
WL 0
R 2
M 120
bc 48,14,64,56
AD 9,7
M 121
mh 21
AW 134,8
AW 158,9
M 133
ad 64,2752
H 0
M 132
TX 58,0
TX 62,13824
M 134
UL 6,19
UH 7,19
M 135
H 1
AR 8,182
M 123
AD 8,6
AD 138,7
M 122
TY 66,512
TY 70,14336
M 124
H 2
AW 182,8
M 125
UL 6,20
UH 7,20
M 137
AR 8,135
AR 9,159
M 136
TX 74,1536
TX 78,15360
PA 12288
DF
M 138
WL 4
Q 0
M 139
Q 2
M 127
Q 4
M 126
TY 82,2048
TY 86,15872
M 128
Q 6
M 129
WL 4
Q 8
M 141
Q 10
M 140
TX 90,3072
TX 94,16896
M 142
Q 12
M 143
R 6
M 131
Q 14
M 130
TY 98,3584
TY 102,17408
M 0
R 4
M 1
WL 10
Q 16
M 13
Q 18
M 12
Q 20
M 14
Q 22
M 15
WL 8
Q 24
M 3
Q 26
M 2
Q 28
M 4
Q 30
M 5
WL 6
Q 32
M 17
Q 34
M 16
Q 36
M 18
Q 38
M 19
WL 2
Q 40
M 7
Q 42
M 6
Q 44
M 8
Q 46
M 9
ae 42,72,480
M 21
s_bfe_u32 s52, s50, 0x1000b
v_mad_i32_i24 v50, v49, s52, v48
SA 232,0
M 20
AD 8,6
AD 9,7
M 22
AW 135,8
AW 159,9
M 23
R 10
M 11
UL 6,21
UH 7,21
M 10
WL 2
R 8
M 34
AR 8,183
AD 8,6
M 35
AD 139,7
AW 183,8
M 32
M 33
M 30
M 31
M 28
M 29
M 26
M 27
WV 7
M 24
WL 0
FP
M 25
M 36
ae 44,68,768
M 37
s_bitcmp1_b32 s50, 14
s_cselect_b32 s51, 24576, 0
v_add_u32 v47, s51, v3
M 38
XL 30,128
M 39
M 40
M 41
M 42
XL 32,896
M 43
M 44
M 45
FR
M 46
M 47
M 58
XL 34,1664
M 59
M 56
)"
R"(M 57
M 54
M 55
WL 0
FQ 224
M 52
XL 36,2432
M 53
M 50
FZ 224
R 24
M 51
mw 72
R 26
M 48
TY 106,5128
TY 110,18952
M 49
R 28
M 60
ad 84,1296
W 0
M 61
M 62
M 63
W 1
M 64
M 65
M 66
W 2
M 67
WL 3
AN 104,106
AN 105,107
M 68
WL 2
SH 106
AN 106,106
M 69
mw 75
AN 107,107
AN 108,110
M 70
AN 109,111
SH 110
M 71
R 30
M 82
AN 110,110
AN 111,111
M 83
TY 114,6664
TY 118,20488
M 80
ad 84,1302
W 0
M 81
M 78
M 79
W 1
M 76
M 77
M 74
W 2
M 75
M 72
WL 1
AN 112,114
AN 113,115
M 73
R 32
M 84
mw 0
SH 114
AN 114,114
M 85
WL 2
AN 115,115
AN 116,118
M 86
AN 117,119
SH 118
M 87
AN 118,118
AN 119,119
M 88
TY 122,8200
TY 126,22024
M 89
ad 84,1344
W 0
M 90
SA 234,768
UL 6,22
M 91
UH 7,22
AR 8,136
M 92
W 1
AR 9,160
M 93
AD 8,6
AD 9,7
M 94
AW 136,8
AW 160,9
M 95
R 34
M 106
W 2
UL 6,23
M 107
WL 3
AN 120,122
AN 121,123
M 104
WL 2
SH 122
AN 122,122
M 105
mw 3
AN 123,123
AN 124,126
M 102
AN 125,127
SH 126
M 103
AN 126,126
AN 127,127
M 100
ad 84,1350
W 0
M 101
UH 7,23
AR 8,184
M 98
AD 8,6
AD 140,7
M 99
W 1
AW 184,8
M 96
UL 6,24
UH 7,24
M 97
R 48
M 108
W 2
AR 8,137
M 109
AR 9,161
AD 8,6
M 110
AD 9,7
AW 137,8
M 111
bc 49,15,66,60
AW 161,9
M 112
mh 0
UL 6,25
UH 7,25
M 113
ad 66,2944
H 0
M 114
AR 8,185
AD 8,6
M 115
AD 141,7
AW 185,8
M 116
DG 1024
M 117
UL 6,26
UH 7,26
M 118
AR 8,138
AR 9,162
M 119
WL 0
R 50
M 120
bc 48,15,64,56
AD 8,6
M 121
mh 3
AD 9,7
AW 138,8
M 133
ad 64,2944
H 0
M 132
TX 58,4608
TX 62,18432
M 134
AW 162,9
UL 6,27
M 135
H 1
UH 7,27
M 123
AR 8,186
AD 8,6
M 122
TY 66,5120
TY 70,18944
M 124
H 2
AD 142,7
M 125
AW 186,8
UL 6,28
M 137
UH 7,28
AR 8,139
M 136
TX 74,6144
TX 78,19968
M 138
WL 4
Q 0
M 139
Q 2
M 127
Q 4
M 126
TY 82,6656
TY 86,20480
M 128
Q 6
M 129
WL 4
Q 8
M 141
Q 10
M 140
TX 90,7680
TX 94,21504
M 142
Q 12
M 143
R 54
M 131
Q 14
M 130
TY 98,8192
TY 102,22016
M 0
R 52
M 1
WL 10
Q 16
M 13
Q 18
M 12
Q 20
M 14
Q 22
M 15
WL 8
Q 24
M 3
Q 26
M 2
Q 28
M 4
Q 30
M 5
WL 6
Q 32
M 17
Q 34
M 16
Q 36
M 18
Q 38
M 19
WL 2
Q 40
M 7
Q 42
M 6
Q 44
M 8
Q 46
M 9
SA 236,1536
AR 9,163
M 21
AD 8,6
AD 9,7
M 20
AW 139,8
AW 163,9
M 22
UL 6,29
UH 7,29
M 23
R 58
M 11
AR 8,187
AD 8,6
M 10
WL 2
R 56
M 34
AD 143,7
AW 187,8
M 35
M 32
M 33
M 30
M 31
M 28
M 29
M 26
M 27
WV 6
M 24
WL 0
FP
M 25
M 36
M 37
M 38
ae 44,68,768
M 39
s_bfe_u32 s51, s50, 0x1000f
v_mad_i32_i24 v47, v49, s51, v48
XL 38,128
M 40
M 41
M 42
M 43
M 44
XL 40,896
M 45
FR
M 46
M 47
M 58
M 59
M 56
XL 42,1664
M 57
M 54
M 55
WL 0
FQ 228
M 52
M 53
M 50
FZ 228
R 72
M 51
mw 24
R 74
M 48
TY 106,9736
TY 110,23560
M 49
R 76
M 60
ad 84,1392
W 0
M 61
XL 44,2432
M 62
M 63
W 1
M 64
M 65
M 66
W 2
M 67
WL 3
AN 104,106
AN 105,107
M 68
WL 2
SH 106
AN 106,106
M 69
mw 27
AN 107,107
AN 108,110
M 70
AN 109,111
SH 110
M 71
R 78
M 82
AN 110,110
AN 111,111
M 83
TY 114,11272
TY 118,25096
M 80
ad 84,1398
)"
R"(W 0
M 81
M 78
M 79
W 1
M 76
M 77
M 74
W 2
M 75
SA 238,2304
M 72
WL 1
AN 112,114
AN 113,115
M 73
R 80
M 84
mw 48
SH 114
AN 114,114
M 85
WL 2
AN 115,115
AN 116,118
M 86
AN 117,119
SH 118
M 87
AN 118,118
AN 119,119
M 88
TY 122,12808
TY 126,26632
M 89
ad 84,1440
W 0
M 90
M 91
M 92
W 1
UL 6,30
M 93
UH 7,30
AR 8,140
M 94
AR 9,164
AD 8,6
M 95
R 82
M 106
W 2
AD 9,7
M 107
WL 3
AN 120,122
AN 121,123
M 104
WL 2
SH 122
AN 122,122
M 105
mw 51
AN 123,123
AN 124,126
M 102
AN 125,127
SH 126
M 103
AN 126,126
AN 127,127
M 100
ad 84,1446
W 0
M 101
AW 140,8
AW 164,9
M 98
UL 6,31
UH 7,31
M 99
W 1
AR 8,188
M 96
AD 8,6
AD 144,7
M 97
R 0
M 108
W 2
AW 188,8
M 109
UL 6,32
UH 7,32
M 110
AR 8,141
AR 9,165
M 111
bc 49,16,66,60
AD 8,6
M 112
mh 9
AD 9,7
AW 141,8
M 113
ad 66,3136
H 0
M 114
AW 165,9
UL 6,33
M 115
UH 7,33
AR 8,189
M 116
DG 1024
M 117
AD 8,6
AD 145,7
M 118
AW 189,8
UL 6,34
M 119
WL 0
R 2
M 120
bc 48,16,64,56
UH 7,34
M 121
mh 12
AR 8,142
AR 9,166
M 133
ad 64,3136
H 0
M 132
TX 58,9216
TX 62,23040
M 134
AD 8,6
AD 9,7
M 135
H 1
AW 142,8
M 123
AW 166,9
UL 6,35
M 122
TY 66,9728
TY 70,23552
M 124
H 2
UH 7,35
M 125
AR 8,190
AD 8,6
M 137
AD 146,7
AW 190,8
M 136
TX 74,10752
TX 78,24576
PA 12288
DF
M 138
WL 4
Q 0
M 139
Q 2
M 127
Q 4
M 126
TY 82,11264
TY 86,25088
M 128
Q 6
M 129
WL 4
Q 8
M 141
Q 10
M 140
TX 90,12288
TX 94,26112
M 142
Q 12
M 143
R 6
M 131
Q 14
M 130
TY 98,12800
TY 102,26624
M 0
R 4
M 1
WL 10
Q 16
M 13
Q 18
M 12
Q 20
M 14
Q 22
M 15
WL 8
Q 24
M 3
Q 26
M 2
Q 28
M 4
Q 30
M 5
WL 6
Q 32
M 17
Q 34
M 16
Q 36
M 18
Q 38
M 19
WL 2
Q 40
M 7
Q 42
M 6
Q 44
M 8
Q 46
M 9
ae 42,72,480
M 21
s_bitcmp1_b32 s50, 10
s_cselect_b32 s52, 24576, 0
v_add_u32 v50, s52, v3
M 20
SA 240,256
UL 6,36
M 22
UH 7,36
AR 8,143
M 23
R 10
M 11
AR 9,167
AD 8,6
M 10
WL 2
R 8
M 34
AD 9,7
AW 143,8
M 35
AW 167,9
UL 6,37
M 32
UH 7,37
AR 8,191
M 33
AD 8,6
AD 147,7
M 30
AW 191,8
M 31
M 28
M 29
M 26
M 27
WV 7
M 24
WL 0
FP
M 25
M 36
M 37
M 38
M 39
M 40
ae 44,68,864
M 41
s_bitcmp1_b32 s50, 16
s_cselect_b32 s51, 24576, 0
v_add_u32 v47, s51, v3
M 42
XL 14,128
M 43
M 44
M 45
FR
M 46
XL 16,896
M 47
M 58
M 59
M 56
M 57
M 54
XL 18,1664
M 55
WL 0
FQ 232
M 52
M 53
M 50
FZ 232
R 24
M 51
mw 72
R 26
M 48
TY 106,520
TY 110,14344
M 49
R 28
M 60
ad 84,1488
W 0
M 61
XL 20,2432
M 62
M 63
W 1
M 64
M 65
M 66
W 2
M 67
WL 3
AN 104,106
AN 105,107
M 68
WL 2
SH 106
AN 106,106
M 69
mw 75
AN 107,107
AN 108,110
M 70
AN 109,111
SH 110
M 71
R 30
M 82
AN 110,110
AN 111,111
M 83
TY 114,2056
TY 118,15880
M 80
ad 84,1494
W 0
M 81
M 78
M 79
W 1
M 76
M 77
M 74
W 2
M 75
M 72
WL 1
AN 112,114
AN 113,115
M 73
R 32
M 84
mw 0
SH 114
AN 114,114
M 85
WL 2
AN 115,115
AN 116,118
M 86
AN 117,119
SH 118
M 87
AN 118,118
AN 119,119
M 88
TY 122,3592
TY 126,17416
M 89
ad 84,1536
W 0
M 90
SA 242,1024
M 91
M 92
W 1
M 93
M 94
UL 6,38
UH 7,38
M 95
R 34
M 106
)"
R"(W 2
AR 8,144
M 107
WL 3
AN 120,122
AN 121,123
M 104
WL 2
SH 122
AN 122,122
M 105
mw 3
AN 123,123
AN 124,126
M 102
AN 125,127
SH 126
M 103
AN 126,126
AN 127,127
M 100
ad 84,1542
W 0
M 101
AR 9,168
AD 8,6
M 98
AD 9,7
AW 144,8
M 99
W 1
AW 168,9
M 96
UL 6,39
UH 7,39
M 97
R 48
M 108
W 2
AR 8,192
M 109
AD 8,6
AD 148,7
M 110
AW 192,8
UL 6,40
M 111
bc 49,17,66,60
UH 7,40
M 112
mh 18
AR 8,145
AR 9,169
M 113
ad 66,3328
H 0
M 114
AD 8,6
AD 9,7
M 115
AW 145,8
AW 169,9
M 116
DG 1024
M 117
UL 6,41
UH 7,41
M 118
AR 8,193
AD 8,6
M 119
WL 0
R 50
M 120
bc 48,17,64,56
AD 149,7
M 121
mh 21
AW 193,8
UL 6,42
M 133
ad 64,3328
H 0
M 132
TX 58,0
TX 62,13824
M 134
UH 7,42
AR 8,146
M 135
H 1
AR 9,170
M 123
AD 8,6
AD 9,7
M 122
TY 66,512
TY 70,14336
M 124
H 2
AW 146,8
M 125
AW 170,9
UL 6,43
M 137
UH 7,43
AR 8,194
M 136
TX 74,1536
TX 78,15360
M 138
WL 4
Q 0
M 139
Q 2
M 127
Q 4
M 126
TY 82,2048
TY 86,15872
M 128
Q 6
M 129
WL 4
Q 8
M 141
Q 10
M 140
TX 90,3072
TX 94,16896
M 142
Q 12
M 143
R 54
M 131
Q 14
M 130
TY 98,3584
TY 102,17408
M 0
R 52
M 1
WL 10
Q 16
M 13
Q 18
M 12
Q 20
M 14
Q 22
M 15
WL 8
Q 24
M 3
Q 26
M 2
Q 28
M 4
Q 30
M 5
WL 6
Q 32
M 17
Q 34
M 16
Q 36
M 18
Q 38
M 19
WL 2
Q 40
M 7
Q 42
M 6
Q 44
M 8
Q 46
M 9
SA 244,1792
AD 8,6
M 21
AD 150,7
AW 194,8
M 20
UL 6,44
UH 7,44
M 22
AR 8,147
AR 9,171
M 23
R 58
M 11
AD 8,6
AD 9,7
M 10
WL 2
R 56
M 34
AW 147,8
AW 171,9
M 35
UL 6,45
UH 7,45
M 32
AR 8,195
AD 8,6
M 33
AD 151,7
AW 195,8
M 30
M 31
M 28
M 29
M 26
M 27
WV 6
M 24
WL 0
FP
M 25
M 36
M 37
M 38
M 39
M 40
M 41
M 42
ae 44,68,864
M 43
s_bfe_u32 s51, s50, 0x10011
v_mad_i32_i24 v47, v49, s51, v48
XL 22,128
M 44
M 45
FR
M 46
M 47
M 58
XL 24,896
M 59
M 56
M 57
M 54
M 55
WL 0
FQ 236
M 52
XL 26,1664
M 53
M 50
FZ 236
R 72
M 51
mw 24
R 74
M 48
TY 106,5128
TY 110,18952
M 49
R 76
M 60
ad 84,1584
W 0
M 61
XL 28,2432
M 62
M 63
W 1
M 64
M 65
M 66
W 2
M 67
WL 3
AN 104,106
AN 105,107
M 68
WL 2
SH 106
AN 106,106
M 69
mw 27
AN 107,107
AN 108,110
M 70
AN 109,111
SH 110
M 71
R 78
M 82
AN 110,110
AN 111,111
M 83
TY 114,6664
TY 118,20488
M 80
ad 84,1590
W 0
M 81
M 78
M 79
W 1
M 76
M 77
M 74
W 2
M 75
SA 246,2560
M 72
WL 1
AN 112,114
AN 113,115
M 73
R 80
M 84
mw 48
SH 114
AN 114,114
M 85
WL 2
AN 115,115
AN 116,118
M 86
AN 117,119
SH 118
M 87
AN 118,118
AN 119,119
M 88
TY 122,8200
TY 126,22024
M 89
ad 84,1632
W 0
M 90
M 91
M 92
W 1
M 93
M 94
M 95
R 82
M 106
W 2
UL 6,14
M 107
WL 3
AN 120,122
AN 121,123
M 104
WL 2
SH 122
AN 122,122
M 105
mw 51
AN 123,123
AN 124,126
M 102
AN 125,127
SH 126
M 103
AN 126,126
AN 127,127
M 100
ad 84,1638
W 0
M 101
UH 7,14
AR 8,148
M 98
AR 9,172
AD 8,6
M 99
W 1
AD 9,7
M 96
AW 148,8
AW 172,9
M 97
R 0
M 108
W 2
UL 6,15
M 109
UH 7,15
AR 8,196
M 110
AD 8,6
AD 152,7
M 111
bc 49,18,66,60
AW 196,8
M 112
mh 0
UL 6,16
UH 7,16
M 113
ad 66,3520
H 0
M 114
)"
R"(AR 8,149
AR 9,173
M 115
AD 8,6
AD 9,7
M 116
DG 1024
M 117
AW 149,8
AW 173,9
M 118
UL 6,17
UH 7,17
M 119
WL 0
R 2
M 120
bc 48,18,64,56
AR 8,197
M 121
mh 3
AD 8,6
AD 153,7
M 133
ad 64,3520
H 0
M 132
TX 58,4608
TX 62,18432
M 134
AW 197,8
UL 6,18
M 135
H 1
UH 7,18
M 123
AR 8,150
AR 9,174
M 122
TY 66,5120
TY 70,18944
M 124
H 2
AD 8,6
M 125
AD 9,7
AW 150,8
M 137
AW 174,9
UL 6,19
M 136
TX 74,6144
TX 78,19968
PA 12288
DF
M 138
WL 4
Q 0
M 139
Q 2
M 127
Q 4
M 126
TY 82,6656
TY 86,20480
M 128
Q 6
M 129
WL 4
Q 8
M 141
Q 10
M 140
TX 90,7680
TX 94,21504
M 142
Q 12
M 143
R 6
M 131
Q 14
M 130
TY 98,8192
TY 102,22016
M 0
R 4
M 1
WL 10
Q 16
M 13
Q 18
M 12
Q 20
M 14
Q 22
M 15
WL 8
Q 24
M 3
Q 26
M 2
Q 28
M 4
Q 30
M 5
WL 6
Q 32
M 17
Q 34
M 16
Q 36
M 18
Q 38
M 19
WL 2
Q 40
M 7
Q 42
M 6
Q 44
M 8
Q 46
M 9
ae 42,72,480
M 21
s_bfe_u32 s52, s50, 0x1000b
v_mad_i32_i24 v50, v49, s52, v48
SA 248,256
M 20
UH 7,19
AR 8,198
M 22
AD 8,6
AD 154,7
M 23
R 10
M 11
AW 198,8
UL 6,20
M 10
WL 2
R 8
M 34
UH 7,20
AR 8,151
M 35
AR 9,175
AD 8,6
M 32
AD 9,7
AW 151,8
M 33
AW 175,9
UL 6,21
M 30
UH 7,21
AR 8,199
M 31
AD 8,6
AD 155,7
M 28
AW 199,8
M 29
M 26
M 27
WV 7
M 24
WL 0
FP
M 25
M 36
M 37
M 38
M 39
M 40
M 41
M 42
M 43
M 44
M 45
FR
M 46
M 47
M 58
M 59
M 56
M 57
M 54
M 55
WL 0
FQ 240
M 52
M 53
M 50
FZ 240
R 24
M 51
mw 72
R 26
M 48
TY 106,9736
TY 110,23560
M 49
R 28
M 60
ad 84,1680
W 0
M 61
M 62
M 63
W 1
M 64
M 65
M 66
W 2
M 67
WL 3
AN 104,106
AN 105,107
M 68
WL 2
SH 106
AN 106,106
M 69
mw 75
AN 107,107
AN 108,110
M 70
AN 109,111
SH 110
M 71
R 30
M 82
AN 110,110
AN 111,111
M 83
TY 114,11272
TY 118,25096
M 80
ad 84,1686
W 0
M 81
ae 44,70,768
M 78
s_bitcmp1_b32 s50, 12
s_cselect_b32 s51, 24576, 0
v_add_u32 v47, s51, v3
M 79
W 1
XL 30,256
M 76
XL 32,1024
M 77
M 74
W 2
M 75
M 72
WL 1
AN 112,114
AN 113,115
M 73
R 32
M 84
mw 0
SH 114
AN 114,114
M 85
WL 2
AN 115,115
AN 116,118
M 86
AN 117,119
SH 118
M 87
AN 118,118
AN 119,119
M 88
TY 122,12808
TY 126,26632
M 89
ad 84,1728
W 0
M 90
SA 250,1024
XL 34,1792
M 91
XL 36,2560
M 92
W 1
M 93
M 94
M 95
R 34
M 106
W 2
M 107
WL 3
AN 120,122
AN 121,123
M 104
WL 2
SH 122
AN 122,122
M 105
mw 3
AN 123,123
AN 124,126
M 102
AN 125,127
SH 126
M 103
AN 126,126
AN 127,127
M 100
ad 84,1734
W 0
M 101
UL 6,22
UH 7,22
M 98
AR 8,152
AR 9,176
M 99
W 1
AD 8,6
M 96
AD 9,7
AW 152,8
M 97
R 48
M 108
W 2
AW 176,9
M 109
UL 6,23
UH 7,23
M 110
AD 132,6
AD 156,7
M 111
bc 49,19,66,60
UL 6,24
M 112
mh 9
UH 7,24
AR 8,153
M 113
ad 66,3712
H 0
M 114
AR 9,177
AD 8,6
M 115
AD 9,7
AW 153,8
M 116
DG 1024
M 117
AW 177,9
UL 6,25
M 118
UH 7,25
AD 133,6
M 119
WL 0
R 50
M 120
bc 48,19,64,56
AD 157,7
M 121
mh 12
UL 6,26
UH 7,26
M 133
ad 64,3712
H 0
M 132
TX 58,9216
TX 62,23040
M 134
AR 8,154
AR 9,178
M 135
H 1
AD 8,6
M 123
AD 9,7
AW 154,8
M 122
TY 66,9728
TY 70,23552
M 124
H 2
AW 178,9
M 125
UL 6,27
UH 7,27
M 137
AD 134,6
AD 158,7
)"
R"(M 136
TX 74,10752
TX 78,24576
M 138
WL 4
Q 0
M 139
Q 2
M 127
Q 4
M 126
TY 82,11264
TY 86,25088
M 128
Q 6
M 129
WL 4
Q 8
M 141
Q 10
M 140
TX 90,12288
TX 94,26112
M 142
Q 12
M 143
R 54
M 131
Q 14
M 130
TY 98,12800
TY 102,26624
M 0
R 52
M 1
WL 10
Q 16
M 13
Q 18
M 12
Q 20
M 14
Q 22
M 15
WL 8
Q 24
M 3
Q 26
M 2
Q 28
M 4
Q 30
M 5
WL 6
Q 32
M 17
Q 34
M 16
Q 36
M 18
Q 38
M 19
WL 2
Q 40
M 7
Q 42
M 6
Q 44
M 8
Q 46
M 9
SA 252,1792
UL 6,28
M 21
UH 7,28
AR 8,155
M 20
AR 9,179
AD 8,6
M 22
AD 9,7
AW 155,8
M 23
R 58
M 11
AW 179,9
UL 6,29
M 10
WL 2
R 56
M 34
UH 7,29
AD 135,6
M 35
AD 159,7
M 32
M 33
M 30
M 31
M 28
M 29
M 26
M 27
WV 6
M 24
WL 0
FP
M 25
M 36
M 37
M 38
M 39
M 40
M 41
M 42
M 43
M 44
M 45
FR
M 46
M 47
M 58
M 59
M 56
M 57
M 54
M 55
WL 0
FQ 244
M 52
M 53
M 50
FZ 244
R 72
M 51
mw 24
R 74
M 48
TY 106,520
TY 110,14344
M 49
R 76
M 60
ad 84,1776
W 0
M 61
M 62
M 63
W 1
M 64
M 65
M 66
W 2
M 67
WL 3
AN 104,106
AN 105,107
M 68
WL 2
SH 106
AN 106,106
M 69
mw 27
AN 107,107
AN 108,110
M 70
AN 109,111
SH 110
M 71
R 78
M 82
AN 110,110
AN 111,111
M 83
TY 114,2056
TY 118,15880
M 80
ad 84,1782
W 0
M 81
ae 44,70,768
M 78
s_bfe_u32 s51, s50, 0x1000d
v_mad_i32_i24 v47, v49, s51, v48
XL 38,256
M 79
W 1
M 76
M 77
M 74
W 2
XL 40,1024
M 75
SA 254,2560
M 72
WL 1
AN 112,114
AN 113,115
M 73
R 80
M 84
mw 48
SH 114
AN 114,114
M 85
WL 2
AN 115,115
AN 116,118
M 86
AN 117,119
SH 118
M 87
AN 118,118
AN 119,119
M 88
TY 122,3592
TY 126,17416
M 89
ad 84,1824
W 0
M 90
XL 42,1792
M 91
M 92
W 1
XL 44,2560
M 93
M 94
M 95
R 82
M 106
W 2
M 107
WL 3
AN 120,122
AN 121,123
M 104
WL 2
SH 122
AN 122,122
M 105
mw 51
AN 123,123
AN 124,126
M 102
AN 125,127
SH 126
M 103
AN 126,126
AN 127,127
M 100
ad 84,1830
W 0
M 101
M 98
M 99
W 1
M 96
M 97
R 0
M 108
W 2
M 109
M 110
M 111
bc 49,20,66,60
M 112
mh 18
M 113
ad 66,3904
H 0
M 114
M 115
M 116
DG 1024
M 117
M 118
M 119
WL 2
R 2
M 120
bc 48,20,64,56
M 121
mh 21
M 133
ad 64,3904
H 0
M 132
TX 58,0
TX 62,13824
M 134
M 135
H 1
M 123
UL 6,30
UH 7,30
M 122
TY 66,512
TY 70,14336
M 124
H 2
AD 160,6
M 125
AD 184,7
UL 6,31
M 137
UH 7,31
AD 208,6
M 136
TX 74,1536
TX 78,15360
M 138
WL 4
Q 0
M 139
Q 2
M 127
Q 4
M 126
TY 82,2048
TY 86,15872
M 128
Q 6
M 129
WL 4
Q 8
M 141
Q 10
M 140
TX 90,3072
TX 94,16896
M 142
Q 12
M 143
R 6
M 131
Q 14
M 130
TY 98,3584
TY 102,17408
M 0
R 4
M 1
WL 10
Q 16
M 13
Q 18
M 12
Q 20
M 14
Q 22
M 15
WL 8
Q 24
M 3
Q 26
M 2
Q 28
M 4
Q 30
M 5
WL 6
Q 32
M 17
Q 34
M 16
Q 36
M 18
Q 38
M 19
WL 2
Q 40
M 7
Q 42
M 6
Q 44
M 8
Q 46
M 9
AD 232,7
UL 6,32
M 21
UH 7,32
AD 161,6
M 20
AD 185,7
UL 6,33
M 22
UH 7,33
AD 209,6
M 23
R 10
M 11
AD 233,7
UL 6,34
M 10
WL 2
R 8
M 34
UH 7,34
)"
R"(AD 162,6
M 35
AD 186,7
UL 6,35
M 32
UH 7,35
AD 210,6
M 33
AD 234,7
UL 6,36
M 30
UH 7,36
AD 163,6
M 31
AD 187,7
UL 6,37
M 28
UH 7,37
AD 211,6
M 29
AD 235,7
M 26
M 27
WV 5
M 24
WL 0
FP
M 25
M 36
M 37
M 38
M 39
M 40
M 41
M 42
M 43
M 44
M 45
FR
M 46
M 47
M 58
M 59
M 56
M 57
M 54
M 55
WL 0
FQ 248
M 52
M 53
M 50
FZ 248
R 24
M 51
mw 72
R 26
M 48
TY 106,5128
TY 110,18952
M 49
R 28
M 60
ad 84,1872
W 0
M 61
M 62
M 63
W 1
M 64
M 65
M 66
W 2
M 67
WL 3
AN 104,106
AN 105,107
M 68
WL 2
SH 106
AN 106,106
M 69
mw 75
AN 107,107
AN 108,110
M 70
AN 109,111
SH 110
M 71
R 30
M 82
AN 110,110
AN 111,111
M 83
TY 114,6664
TY 118,20488
M 80
ad 84,1878
W 0
M 81
M 78
M 79
W 1
M 76
ae 44,68,768
M 77
s_bitcmp1_b32 s50, 14
s_cselect_b32 s51, 24576, 0
v_add_u32 v47, s51, v3
M 74
W 2
XL 14,256
M 75
M 72
WL 1
AN 112,114
AN 113,115
M 73
R 32
M 84
mw 0
SH 114
AN 114,114
M 85
WL 2
AN 115,115
AN 116,118
M 86
AN 117,119
SH 118
M 87
AN 118,118
AN 119,119
M 88
TY 122,8200
TY 126,22024
M 89
ad 84,1920
W 0
M 90
XL 16,1024
M 91
XL 18,1792
M 92
W 1
M 93
M 94
M 95
R 34
M 106
W 2
XL 20,2560
M 107
WL 3
AN 120,122
AN 121,123
M 104
WL 2
SH 122
AN 122,122
M 105
mw 3
AN 123,123
AN 124,126
M 102
AN 125,127
SH 126
M 103
AN 126,126
AN 127,127
M 100
ad 84,1926
W 0
M 101
M 98
M 99
W 1
M 96
M 97
R 48
M 108
W 2
M 109
M 110
M 111
bc 49,21,66,60
M 112
mh 0
M 113
ad 66,4096
H 0
M 114
M 115
M 116
DG 1024
M 117
M 118
M 119
WL 2
R 50
M 120
bc 48,21,64,56
M 121
mh 3
M 133
ad 64,4096
H 0
M 132
TX 58,4608
TX 62,18432
M 134
M 135
H 1
M 123
M 122
TY 66,5120
TY 70,18944
M 124
H 2
M 125
M 137
UL 6,38
UH 7,38
M 136
TX 74,6144
TX 78,19968
M 138
WL 4
Q 0
M 139
Q 2
M 127
Q 4
M 126
TY 82,6656
TY 86,20480
M 128
Q 6
M 129
WL 4
Q 8
M 141
Q 10
M 140
TX 90,7680
TX 94,21504
M 142
Q 12
M 143
R 54
M 131
Q 14
M 130
TY 98,8192
TY 102,22016
M 0
R 52
M 1
WL 10
Q 16
M 13
Q 18
M 12
Q 20
M 14
Q 22
M 15
WL 8
Q 24
M 3
Q 26
M 2
Q 28
M 4
Q 30
M 5
WL 6
Q 32
M 17
Q 34
M 16
Q 36
M 18
Q 38
M 19
WL 2
Q 40
M 7
Q 42
M 6
Q 44
M 8
Q 46
M 9
AD 164,6
AD 188,7
M 21
UL 6,39
UH 7,39
M 20
AD 212,6
AD 236,7
M 22
UL 6,40
UH 7,40
M 23
R 58
M 11
AD 165,6
AD 189,7
M 10
WL 2
R 56
M 34
UL 6,41
UH 7,41
M 35
AD 213,6
AD 237,7
M 32
UL 6,42
UH 7,42
M 33
AD 166,6
AD 190,7
M 30
UL 6,43
UH 7,43
M 31
AD 214,6
AD 238,7
M 28
UL 6,44
UH 7,44
M 29
AD 167,6
AD 191,7
M 26
UL 6,45
UH 7,45
M 27
WV 5
AD 215,6
AD 239,7
M 24
WL 0
FP
M 25
M 36
M 37
M 38
M 39
M 40
M 41
M 42
M 43
M 44
M 45
FR
M 46
M 47
M 58
M 59
M 56
M 57
M 54
M 55
WL 0
FQ 252
M 52
M 53
M 50
FZ 252
R 72
M 51
mw 24
R 74
M 48
TY 106,9736
TY 110,23560
M 49
R 76
M 60
ad 84,1968
W 0
M 61
M 62
M 63
W 1
M 64
M 65
M 66
W 2
M 67
WL 3
AN 104,106
AN 105,107
M 68
WL 2
SH 106
)"
R"(AN 106,106
M 69
mw 27
AN 107,107
AN 108,110
M 70
AN 109,111
SH 110
M 71
R 78
M 82
AN 110,110
AN 111,111
M 83
TY 114,11272
TY 118,25096
M 80
ad 84,1974
W 0
M 81
M 78
M 79
W 1
M 76
M 77
M 74
W 2
M 75
M 72
WL 1
AN 112,114
AN 113,115
M 73
R 80
M 84
mw 48
SH 114
AN 114,114
M 85
WL 2
AN 115,115
AN 116,118
M 86
AN 117,119
SH 118
M 87
AN 118,118
AN 119,119
M 88
TY 122,12808
TY 126,26632
M 89
ad 84,2016
W 0
M 90
ae 44,68,768
M 91
s_bfe_u32 s51, s50, 0x1000f
v_mad_i32_i24 v47, v49, s51, v48
XL 22,256
M 92
W 1
XL 24,1024
M 93
M 94
XL 26,1792
M 95
R 82
M 106
W 2
M 107
WL 3
AN 120,122
AN 121,123
M 104
WL 2
SH 122
AN 122,122
M 105
mw 51
AN 123,123
AN 124,126
M 102
AN 125,127
SH 126
M 103
AN 126,126
AN 127,127
M 100
ad 84,2022
W 0
M 101
XL 28,2560
M 98
M 99
W 1
M 96
M 97
R 0
M 108
W 2
M 109
M 110
M 111
bc 49,22,66,60
M 112
mh 9
M 113
ad 66,4288
H 0
M 114
M 115
M 116
DG 1024
M 117
M 118
M 119
WL 2
R 2
M 120
bc 48,22,64,56
M 121
mh 12
M 133
ad 64,4288
H 0
M 132
TX 58,9216
TX 62,23040
M 134
M 135
H 1
M 123
M 122
TY 66,9728
TY 70,23552
M 124
H 2
M 125
M 137
M 136
TX 74,10752
TX 78,24576
M 138
WL 4
Q 0
M 139
Q 2
M 127
Q 4
M 126
TY 82,11264
TY 86,25088
M 128
Q 6
M 129
WL 4
Q 8
M 141
Q 10
M 140
TX 90,12288
TX 94,26112
M 142
Q 12
M 143
R 6
M 131
Q 14
M 130
TY 98,12800
TY 102,26624
M 0
R 4
M 1
WL 10
Q 16
M 13
Q 18
M 12
Q 20
M 14
Q 22
M 15
WL 8
Q 24
M 3
Q 26
M 2
Q 28
M 4
Q 30
M 5
WL 6
Q 32
M 17
Q 34
M 16
Q 36
M 18
Q 38
M 19
WL 2
Q 40
M 7
Q 42
M 6
Q 44
M 8
Q 46
M 9
UL 6,14
UH 7,14
M 21
AD 168,6
AD 192,7
M 20
UL 6,15
UH 7,15
M 22
AD 216,6
AD 240,7
M 23
R 10
M 11
UL 6,16
UH 7,16
M 10
WL 2
R 8
M 34
AD 169,6
AD 193,7
M 35
UL 6,17
UH 7,17
M 32
AD 217,6
AD 241,7
M 33
UL 6,18
UH 7,18
M 30
AD 170,6
AD 194,7
M 31
UL 6,19
UH 7,19
M 28
AD 218,6
AD 242,7
M 29
UL 6,20
UH 7,20
M 26
AD 171,6
AD 195,7
M 27
WV 5
UL 6,21
UH 7,21
M 24
WL 0
FP
AD 219,6
M 25
AD 243,7
M 36
M 37
M 38
M 39
M 40
M 41
M 42
M 43
M 44
M 45
FR
M 46
M 47
M 58
M 59
M 56
M 57
M 54
M 55
WL 0
FQ 256
M 52
M 53
M 50
FZ 256
R 24
M 51
mw 72
R 26
M 48
TY 106,520
TY 110,14344
M 49
R 28
M 60
ad 84,2064
W 0
M 61
M 62
M 63
W 1
M 64
M 65
M 66
W 2
M 67
WL 3
AN 104,106
AN 105,107
M 68
WL 2
SH 106
AN 106,106
M 69
mw 75
AN 107,107
AN 108,110
M 70
AN 109,111
SH 110
M 71
R 30
M 82
AN 110,110
AN 111,111
M 83
TY 114,2056
TY 118,15880
M 80
ad 84,2070
W 0
M 81
M 78
M 79
W 1
M 76
M 77
M 74
W 2
M 75
M 72
WL 1
AN 112,114
AN 113,115
M 73
R 32
M 84
mw 0
SH 114
AN 114,114
M 85
WL 2
AN 115,115
AN 116,118
M 86
AN 117,119
SH 118
M 87
AN 118,118
AN 119,119
M 88
TY 122,3592
TY 126,17416
M 89
ad 84,2112
W 0
M 90
ae 44,68,864
M 91
s_bitcmp1_b32 s50, 16
s_cselect_b32 s51, 24576, 0
v_add_u32 v47, s51, v3
M 92
W 1
XL 30,256
M 93
XL 32,1024
M 94
M 95
R 34
M 106
W 2
M 107
WL 3
AN 120,122
AN 121,123
M 104
WL 2
SH 122
AN 122,122
M 105
mw 3
AN 123,123
)"
R"(AN 124,126
M 102
AN 125,127
SH 126
M 103
AN 126,126
AN 127,127
M 100
ad 84,2118
W 0
M 101
XL 34,1792
M 98
XL 36,2560
M 99
W 1
M 96
M 97
R 48
M 108
W 2
M 109
M 110
M 111
bc 49,23,66,60
M 112
mh 18
M 113
ad 66,4480
H 0
M 114
M 115
M 116
DG 1024
M 117
M 118
M 119
WL 2
R 50
M 120
bc 48,23,64,56
M 121
mh 21
M 133
ad 64,4480
H 0
M 132
TX 58,0
TX 62,13824
M 134
M 135
H 1
M 123
M 122
TY 66,512
TY 70,14336
M 124
H 2
M 125
M 137
M 136
TX 74,1536
TX 78,15360
M 138
WL 4
Q 0
M 139
Q 2
M 127
Q 4
M 126
TY 82,2048
TY 86,15872
M 128
Q 6
M 129
WL 4
Q 8
M 141
Q 10
M 140
TX 90,3072
TX 94,16896
M 142
Q 12
M 143
R 54
M 131
Q 14
M 130
TY 98,3584
TY 102,17408
M 0
R 52
M 1
WL 10
Q 16
M 13
Q 18
M 12
Q 20
M 14
Q 22
M 15
WL 8
Q 24
M 3
Q 26
M 2
Q 28
M 4
Q 30
M 5
WL 6
Q 32
M 17
Q 34
M 16
Q 36
M 18
Q 38
M 19
WL 2
Q 40
M 7
Q 42
M 6
Q 44
M 8
Q 46
M 9
UL 6,22
UH 7,22
M 21
AD 172,6
AD 196,7
M 20
UL 6,23
UH 7,23
M 22
AD 220,6
AD 244,7
M 23
R 58
M 11
UL 6,24
UH 7,24
M 10
WL 2
R 56
M 34
AD 173,6
AD 197,7
M 35
UL 6,25
UH 7,25
M 32
AD 221,6
AD 245,7
M 33
UL 6,26
UH 7,26
M 30
AD 174,6
AD 198,7
M 31
UL 6,27
UH 7,27
M 28
AD 222,6
AD 246,7
M 29
UL 6,28
UH 7,28
M 26
AD 175,6
AD 199,7
M 27
WV 5
UL 6,29
UH 7,29
M 24
WL 0
FP
AD 223,6
M 25
AD 247,7
M 36
M 37
M 38
M 39
M 40
M 41
M 42
M 43
M 44
M 45
FR
M 46
M 47
M 58
M 59
M 56
M 57
M 54
M 55
WL 0
FQ 260
M 52
M 53
M 50
FZ 260
R 72
M 51
mw 24
R 74
M 48
TY 106,5128
TY 110,18952
M 49
R 76
M 60
ad 84,2160
W 0
M 61
M 62
M 63
W 1
M 64
M 65
M 66
W 2
M 67
WL 3
AN 104,106
AN 105,107
M 68
WL 2
SH 106
AN 106,106
M 69
mw 27
AN 107,107
AN 108,110
M 70
AN 109,111
SH 110
M 71
R 78
M 82
AN 110,110
AN 111,111
M 83
TY 114,6664
TY 118,20488
M 80
ad 84,2166
W 0
M 81
M 78
M 79
W 1
M 76
M 77
M 74
W 2
M 75
M 72
WL 1
AN 112,114
AN 113,115
M 73
R 80
M 84
mw 48
SH 114
AN 114,114
M 85
WL 2
AN 115,115
AN 116,118
M 86
AN 117,119
SH 118
M 87
AN 118,118
AN 119,119
M 88
TY 122,8200
TY 126,22024
M 89
ad 84,2208
W 0
M 90
ae 44,68,864
M 91
s_bfe_u32 s51, s50, 0x10011
v_mad_i32_i24 v47, v49, s51, v48
XL 38,256
M 92
W 1
M 93
M 94
M 95
R 82
M 106
W 2
XL 40,1024
M 107
WL 3
AN 120,122
AN 121,123
M 104
WL 2
SH 122
AN 122,122
M 105
mw 51
AN 123,123
AN 124,126
M 102
AN 125,127
SH 126
M 103
AN 126,126
AN 127,127
M 100
ad 84,2214
W 0
M 101
XL 42,1792
M 98
M 99
W 1
M 96
M 97
R 0
M 108
W 2
XL 44,2560
M 109
M 110
M 111
bc 48,0,64,56
M 112
mh 3
M 113
ad 64,64
H 0
M 114
M 115
M 116
H 1
M 117
M 118
M 119
WL 2
R 2
M 120
H 2
M 121
M 133
M 132
TX 58,4608
TX 62,18432
M 134
M 135
M 123
M 122
TY 66,5120
TY 70,18944
M 124
M 125
M 137
M 136
TX 74,6144
TX 78,19968
M 138
WL 4
Q 0
M 139
Q 2
M 127
Q 4
M 126
TY 82,6656
TY 86,20480
M 128
Q 6
M 129
WL 4
Q 8
M 141
Q 10
M 140
TX 90,7680
TX 94,21504
M 142
Q 12
M 143
)"
R"(R 6
M 131
Q 14
M 130
TY 98,8192
TY 102,22016
M 0
R 4
M 1
WL 10
Q 16
M 13
Q 18
M 12
Q 20
M 14
Q 22
M 15
WL 8
Q 24
M 3
Q 26
M 2
Q 28
M 4
Q 30
M 5
WL 6
Q 32
M 17
Q 34
M 16
Q 36
M 18
Q 38
M 19
WL 2
Q 40
M 7
Q 42
M 6
Q 44
M 8
Q 46
M 9
UL 6,30
UH 7,30
M 21
AD 176,6
AD 200,7
M 20
UL 6,31
UH 7,31
M 22
AD 224,6
AD 248,7
M 23
R 10
M 11
UL 6,32
UH 7,32
M 10
WL 2
R 8
M 34
AD 177,6
AD 201,7
M 35
UL 6,33
UH 7,33
M 32
AD 225,6
AD 249,7
M 33
UL 6,34
UH 7,34
M 30
AD 178,6
AD 202,7
M 31
UL 6,35
UH 7,35
M 28
AD 226,6
AD 250,7
M 29
UL 6,36
UH 7,36
M 26
AD 179,6
AD 203,7
M 27
WV 4
UL 6,37
UH 7,37
M 24
WL 0
FP
AD 227,6
M 25
AD 251,7
M 36
M 37
M 38
M 39
M 40
M 41
M 42
M 43
M 44
M 45
FR
M 46
M 47
M 58
M 59
M 56
M 57
M 54
M 55
WL 0
FQ 264
M 52
M 53
M 50
FZ 264
R 24
M 51
mw 72
R 26
M 48
TY 106,9736
TY 110,23560
M 49
R 28
M 60
ad 84,2256
W 0
M 61
M 62
M 63
W 1
M 64
M 65
M 66
W 2
M 67
WL 3
AN 104,106
AN 105,107
M 68
WL 2
SH 106
AN 106,106
M 69
mw 75
AN 107,107
AN 108,110
M 70
AN 109,111
SH 110
M 71
R 30
M 82
AN 110,110
AN 111,111
M 83
TY 114,11272
TY 118,25096
M 80
ad 84,2262
W 0
M 81
M 78
M 79
W 1
M 76
M 77
M 74
W 2
M 75
M 72
WL 1
AN 112,114
AN 113,115
M 73
R 32
M 84
mw 0
SH 114
AN 114,114
M 85
WL 2
AN 115,115
AN 116,118
M 86
AN 117,119
SH 118
M 87
AN 118,118
AN 119,119
M 88
TY 122,12808
TY 126,26632
M 89
ad 84,0
W 0
M 90
M 91
M 92
W 1
M 93
M 94
M 95
R 34
M 106
W 2
M 107
WL 3
AN 120,122
AN 121,123
M 104
WL 2
SH 122
AN 122,122
M 105
mw 3
AN 123,123
AN 124,126
M 102
AN 125,127
SH 126
M 103
AN 126,126
AN 127,127
M 100
ad 84,6
W 0
M 101
M 98
M 99
W 1
M 96
M 97
R 48
M 108
W 2
M 109
M 110
M 111
bc 48,1,64,56
M 112
mh 15
M 113
ad 64,259
H 0
M 114
M 115
M 116
DG 1024
M 117
M 118
M 119
WL 2
R 50
M 120
M 121
M 133
M 132
TX 58,9216
TX 62,23040
M 134
M 135
M 123
M 122
TY 66,9728
TY 70,23552
M 124
M 125
M 137
M 136
TX 74,10752
TX 78,24576
M 138
WL 4
Q 0
M 139
Q 2
M 127
Q 4
M 126
TY 82,11264
TY 86,25088
M 128
Q 6
M 129
WL 4
Q 8
M 141
Q 10
M 140
TX 90,12288
TX 94,26112
M 142
Q 12
M 143
R 54
M 131
Q 14
M 130
TY 98,12800
TY 102,26624
M 0
R 52
M 1
WL 10
Q 16
M 13
Q 18
M 12
Q 20
M 14
Q 22
M 15
WL 8
Q 24
M 3
Q 26
M 2
Q 28
M 4
Q 30
M 5
WL 6
Q 32
M 17
Q 34
M 16
Q 36
M 18
Q 38
M 19
WL 2
Q 40
M 7
Q 42
M 6
Q 44
M 8
Q 46
M 9
UL 6,38
UH 7,38
M 21
AD 180,6
AD 204,7
M 20
UL 6,39
UH 7,39
M 22
AD 228,6
AD 252,7
M 23
R 58
M 11
UL 6,40
UH 7,40
M 10
WL 2
R 56
M 34
AD 181,6
AD 205,7
M 35
UL 6,41
UH 7,41
M 32
AD 229,6
AD 253,7
BV 52,4608
M 33
UL 6,42
UH 7,42
M 30
AD 182,6
AD 206,7
M 31
UL 6,43
UH 7,43
M 28
AD 230,6
AD 254,7
M 29
WV 17
UL 6,44
UH 7,44
M 26
AD 183,6
AD 207,7
M 27
)"
R"(WV 8
UL 6,45
UH 7,45
M 24
WL 1
FP
AD 231,6
M 25
AD 255,7
M 36
M 37
M 38
M 39
M 40
ds_read_b64 v[12:13], v4 offset:4864
ds_read_b64 v[46:47], v4 offset:4872
M 41
AR 6,36
AR 7,60
M 42
AR 8,84
AR 9,108
M 43
WL 3
EF 6,52
EF 7,53
M 44
EF 8,54
EF 9,55
M 45
FR
CV 14,6,7
M 46
CV 15,8,9
AR 6,37
M 47
AR 7,61
AR 8,85
M 58
AR 9,109
EF 6,52
M 59
EF 7,53
EF 8,54
M 56
EF 9,55
CV 16,6,7
M 57
CV 17,8,9
AR 6,38
M 54
AR 7,62
AR 8,86
M 55
WL 0
FQ 268
AR 9,110
M 52
EF 6,52
EF 7,53
M 53
EF 8,54
EF 9,55
M 50
FZ 268
R 72
M 51
mw 24
R 74
M 48
TY 114,2056
TY 118,15880
M 49
R 76
M 60
ad 84,48
W 0
M 61
CV 18,6,7
CV 19,8,9
M 62
AR 6,39
AR 7,63
M 63
W 1
AR 8,87
M 64
AR 9,111
EF 6,52
M 65
EF 7,53
EF 8,54
M 66
W 2
EF 9,55
M 67
WL 3
AN 112,114
AN 113,115
M 68
WL 2
SH 114
AN 114,114
M 69
mw 27
AN 115,115
AN 116,118
M 70
AN 117,119
SH 118
M 71
R 78
M 82
AN 118,118
AN 119,119
M 83
TY 122,3592
TY 126,17416
M 80
ad 84,54
W 0
M 81
CV 20,6,7
CV 21,8,9
M 78
ae 42,74,768
AR 6,40
M 79
W 1
AR 7,64
M 76
s_bitcmp1_b32 s50, 12
s_cselect_b32 s52, 24576, 0
v_add_u32 v50, s52, v3
M 77
ST 14,0
AR 8,88
M 74
W 2
AR 9,112
M 75
EF 6,52
EF 7,53
M 72
WL 1
AN 120,122
AN 121,123
M 73
R 80
M 84
mw 48
SH 122
AN 122,122
M 85
WL 2
AN 123,123
AN 124,126
M 86
AN 125,127
SH 126
M 87
AN 126,126
AN 127,127
M 88
ad 84,96
W 0
M 89
ST 16,768
EF 8,54
M 90
EF 9,55
CV 22,6,7
M 91
W 1
CV 23,8,9
M 92
AR 6,41
AR 7,65
M 93
ST 18,1536
AR 8,89
M 94
W 2
AR 9,113
M 95
R 82
M 106
EF 6,52
EF 7,53
M 107
mw 51
ST 20,2304
EF 8,54
M 104
ad 84,102
W 0
M 105
EF 9,55
CV 24,6,7
M 102
CV 25,8,9
AR 6,42
M 103
W 1
AR 7,66
M 100
AR 8,90
AR 9,114
M 101
EF 6,52
EF 7,53
M 98
W 2
EF 8,54
M 99
EF 9,55
CV 26,6,7
M 96
CV 27,8,9
AR 6,43
M 97
AR 7,67
AR 8,91
M 108
AR 9,115
EF 6,52
M 109
EF 7,53
EF 8,54
M 110
EF 9,55
CV 28,6,7
M 111
CV 29,8,9
M 112
ae 42,74,768
BV 52,5120
M 113
s_bfe_u32 s52, s50, 0x1000d
v_mad_i32_i24 v50, v49, s52, v48
ST 22,0
M 114
AR 6,132
AR 7,156
M 115
WV 19
AR 8,180
EF 6,12
M 116
WL 1
EF 7,13
EF 8,46
M 117
s_barrier
R 0
M 118
mw 72
ST 24,768
EF 136,47
M 119
R 2
M 120
ad 84,144
W 0
M 121
CV 30,6,7
CV 31,8,136
M 133
ST 26,1536
AR 6,133
M 132
AR 7,157
AR 8,181
M 134
W 1
EF 6,12
M 135
EF 7,13
EF 8,46
M 123
ST 28,2304
EF 137,47
M 122
CV 32,6,7
CV 33,8,137
M 124
W 2
AR 6,134
M 125
AR 7,158
AR 8,182
M 137
EF 6,12
EF 7,13
M 136
TX 74,1536
TX 78,15360
M 138
mw 75
EF 8,46
EF 138,47
M 139
ad 84,150
W 0
M 127
CV 34,6,7
CV 35,8,138
M 126
TY 82,2048
TY 86,15872
M 128
AR 6,135
AR 7,159
M 129
W 1
AR 8,183
M 141
EF 6,12
EF 7,13
M 140
WL 2
TX 90,3072
TX 94,16896
P 16
M 142
Q 17
P 19
M 143
R 6
P 20
M 131
Q 21
P 23
M 130
WL 4
TY 98,3584
TY 102,17408
P 24
M 4
R 4
P 25
M 5
Q 26
P 28
M 17
Q 29
P 31
M 16
WL 6
Q 32
M 18
Q 34
M 19
Q 36
M 7
Q 38
M 6
WL 2
Q 40
M 8
Q 42
M 9
Q 44
M 21
Q 46
M 20
W 2
EF 8,46
M 22
EF 139,47
CV 36,6,7
M 23
R 10
M 11
CV 37,8,139
M 10
WL 2
R 8
M 34
ae 42,74,768
AR 6,136
M 35
s_bitcmp1_b32 s50, 12
s_cselect_b32 s52, 24576, 0
v_add_u32 v50, s52, v3
M 32
ST 30,128
AR 7,160
M 33
AR 8,184
EF 6,12
M 30
EF 7,13
EF 8,46
M 31
EF 140,47
CV 38,6,7
M 28
ST 32,896
CV 39,8,140
)"
R"(M 29
AR 6,137
AR 7,161
M 40
AR 8,185
EF 6,12
M 41
EF 7,13
EF 8,46
M 42
ST 34,1664
EF 141,47
M 43
CV 40,6,7
CV 41,8,141
M 44
WV 22
AR 6,138
AR 7,162
M 45
WL 0
AR 8,186
EF 6,12
M 46
s_barrier
R 24
M 47
R 26
M 58
TY 122,8200
TY 126,22024
M 59
ST 36,2432
EF 7,13
M 56
EF 8,46
EF 142,47
M 57
CV 42,6,7
CV 43,8,142
M 54
AR 6,139
AR 7,163
M 55
AR 8,187
EF 6,12
M 52
EF 7,13
EF 8,46
M 53
R 28
M 64
EF 143,47
CV 44,6,7
M 65
WL 3
AN 120,122
AN 121,123
M 66
SH 122
AN 122,122
M 67
WL 2
AN 123,123
AN 124,126
M 68
AN 125,127
SH 126
M 69
AN 126,126
AN 127,127
M 70
CV 45,8,143
M 71
R 30
ds_read_b64 v[12:13], v4 offset:4608
M 82
ae 42,74,768
ds_read_b64 v[46:47], v4 offset:4616
M 83
s_bfe_u32 s52, s50, 0x1000d
v_mad_i32_i24 v50, v49, s52, v48
ST 38,128
M 80
EF 160,52
EF 184,53
M 81
EF 208,54
EF 232,55
M 78
CV 14,160,184
CV 15,208,232
M 79
ST 40,896
EF 161,52
M 76
EF 185,53
EF 209,54
M 77
R 32
M 88
TX 58,7680
TX 62,21504
M 89
ST 42,1664
EF 233,55
M 90
CV 16,161,185
CV 17,209,233
M 91
EF 162,52
EF 186,53
M 92
TY 66,8192
TY 70,22016
M 93
ST 44,2432
EF 210,54
M 94
EF 234,55
CV 18,162,186
M 95
WL 6
R 34
M 106
CV 19,210,234
EF 163,52
M 107
WL 4
PA_ 56,120,58
PA_ 57,121,59
M 104
PB_ 58,120,58
PB_ 59,121,59
M 105
PA_ 60,124,62
PA_ 61,125,63
M 102
PB_ 62,124,62
PB_ 63,125,63
M 103
WL 2
PA_ 64,122,66
PA_ 65,123,67
M 100
PB_ 66,122,66
PB_ 67,123,67
M 101
PA_ 68,126,70
PA_ 69,127,71
M 112
PB_ 70,126,70
PB_ 71,127,71
M 113
EF 187,53
EF 211,54
M 114
EF 235,55
CV 20,163,187
M 115
CV 21,211,235
M 116
WV 18
EF 164,52
EF 188,53
M 117
WL 0
EF 212,54
EF 236,55
M 118
s_barrier
R 48
M 119
R 50
M 124
ae 42,74,768
CV 22,164,188
M 125
s_bitcmp1_b32 s50, 12
s_cselect_b32 s52, 24576, 0
v_add_u32 v50, s52, v3
M 137
ST 14,256
CV 23,212,236
M 136
EF 165,52
EF 189,53
M 138
EF 213,54
EF 237,55
M 139
CV 24,165,189
CV 25,213,237
M 127
ST 16,1024
EF 166,52
M 126
EF 190,53
EF 214,54
M 128
EF 238,55
CV 26,166,190
M 129
CV 27,214,238
EF 167,52
M 141
ST 18,1792
EF 191,53
M 140
EF 215,54
EF 239,55
M 142
CV 28,167,191
CV 29,215,239
M 143
R 54
BV 52,4864
M 131
ST 20,2560
AR 6,44
M 130
WL 3
R 52
MA 52,0,56
AR 7,68
AR 8,92
MA 52,4,60
AR 9,116
EF 6,12
MA 76,12,60
ae 42,74,768
EF 7,13
MA 76,8,56
s_bfe_u32 s52, s50, 0x1000d
v_mad_i32_i24 v50, v49, s52, v48
ST 22,256
MA 80,8,64
EF 8,46
EF 9,47
MA 80,12,68
R 58
MA 56,4,68
CV 30,6,7
CV 31,8,9
MA 56,0,64
WL 2
R 56
MA 104,16,64
ST 24,1024
AR 6,45
MA 104,20,68
AR 7,69
AR 8,93
MA 100,16,56
AR 9,117
EF 6,12
MA 100,20,60
EF 7,13
EF 8,46
MA 124,24,56
WV 14
ST 26,1792
EF 9,47
MA 124,28,60
WL 0
CV 32,6,7
CV 33,8,9
MA 128,24,64
s_barrier
R 72
MA 128,28,68
R 74
MA 152,0,64
ST 28,2560
AR 6,46
MA 152,4,68
AR 7,70
AR 8,94
MA 148,0,56
AR 9,118
EF 6,12
MA 148,4,60
R 76
MA 172,8,56
EF 7,13
EF 8,46
MA 172,12,60
EF 9,47
CV 34,6,7
MA 176,8,64
CV 35,8,9
AR 6,47
MA 176,12,68
WL 4
R 78
MV 132,16,64
AR 7,71
AR 8,95
MV 132,20,68
AR 9,119
EF 6,12
MA 196,16,56
EF 7,13
EF 8,46
MA 196,20,60
WL 4
R 80
MV 152,24,56
EF 9,47
CV 36,6,7
MV 152,28,60
CV 37,8,9
MV 156,24,64
ae 42,72,768
AR 6,48
AR 7,72
MV 156,28,68
WL 4
R 82
AR 8,96
MV 180,0,64
s_bitcmp1_b32 s50, 14
s_cselect_b32 s52, 24576, 0
v_add_u32 v50, s52, v3
AR 9,120
MV 180,4,68
ST 30,0
EF 6,12
EF 7,13
MV 176,0,56
EF 8,46
EF 9,47
CV 38,6,7
MV 176,4,60
WL 4
CV 39,8,9
AR 6,49
AR 7,73
MV 200,8,56
AR 8,97
AR 9,121
EF 6,12
MV 200,12,60
ST 32,768
EF 7,13
EF 8,46
MV 204,8,64
EF 9,47
CV 40,6,7
CV 41,8,9
MV 204,12,68
WL 0
AR 6,50
AR 7,74
AR 8,98
MV 224,16,56
AR 9,122
EF 6,12
EF 7,13
MV 224,20,60
ST 34,1536
EF 8,46
EF 9,47
MV 248,28,60
CV 42,6,7
CV 43,8,9
AR 6,51
MV 248,24,56
AR 7,75
AR 8,99
AR 9,123
MV 252,24,64
EF 6,12
EF 7,13
EF 8,46
MV 252,28,68
ST 36,2304
EF 9,47
CV 44,6,7
MV 228,20,68
WV 14
CV 45,8,9
MV 228,16,64
ae 42,72,768
s_bfe_u32 s52, s50, 0x1000f
v_mad_i32_i24 v50, v49, s52, v48
ST 38,0
ST 40,768
ST 42,1536
ST 44,2304
ds_read_b64 v[12:13], v4 offset:5120
ds_read_b64 v[46:47], v4 offset:5128
AR 6,140
AR 7,164
AR 8,188
EF 6,52
EF 7,53
EF 8,54
EF 144,55
CV 14,6,7
)"
R"(CV 15,8,144
AR 6,141
AR 7,165
AR 8,189
EF 6,52
EF 7,53
EF 8,54
EF 145,55
CV 16,6,7
CV 17,8,145
AR 6,142
AR 7,166
AR 8,190
EF 6,52
EF 7,53
EF 8,54
EF 146,55
CV 18,6,7
CV 19,8,146
AR 6,143
AR 7,167
AR 8,191
EF 6,52
EF 7,53
EF 8,54
EF 147,55
CV 20,6,7
CV 21,8,147
ae 42,72,768
s_bitcmp1_b32 s50, 14
s_cselect_b32 s52, 24576, 0
v_add_u32 v50, s52, v3
ST 14,128
ST 16,896
ST 18,1664
ST 20,2432
AR 6,144
AR 7,168
AR 8,192
EF 6,52
EF 7,53
EF 8,54
EF 148,55
CV 22,6,7
CV 23,8,148
AR 6,145
AR 7,169
AR 8,193
EF 6,52
EF 7,53
EF 8,54
EF 149,55
CV 24,6,7
CV 25,8,149
AR 6,146
AR 7,170
AR 8,194
EF 6,52
EF 7,53
EF 8,54
EF 150,55
CV 26,6,7
CV 27,8,150
AR 6,147
AR 7,171
AR 8,195
EF 6,52
EF 7,53
EF 8,54
EF 151,55
CV 28,6,7
CV 29,8,151
ae 42,72,768
s_bfe_u32 s52, s50, 0x1000f
v_mad_i32_i24 v50, v49, s52, v48
ST 22,128
ST 24,896
ST 26,1664
ST 28,2432
BV 52,4608
WL 1
EF 168,12
EF 192,13
EF 216,46
EF 240,47
CV 30,168,192
CV 31,216,240
EF 169,12
EF 193,13
EF 217,46
EF 241,47
CV 32,169,193
CV 33,217,241
EF 170,12
EF 194,13
EF 218,46
EF 242,47
CV 34,170,194
CV 35,218,242
EF 171,12
EF 195,13
EF 219,46
EF 243,47
CV 36,171,195
CV 37,219,243
ae 42,72,768
s_bitcmp1_b32 s50, 14
s_cselect_b32 s52, 24576, 0
v_add_u32 v50, s52, v3
ST 30,256
ST 32,1024
ST 34,1792
ST 36,2560
EF 172,12
EF 196,13
EF 220,46
EF 244,47
CV 38,172,196
CV 39,220,244
EF 173,12
EF 197,13
EF 221,46
EF 245,47
CV 40,173,197
CV 41,221,245
EF 174,12
EF 198,13
EF 222,46
EF 246,47
CV 42,174,198
CV 43,222,246
EF 175,12
EF 199,13
EF 223,46
EF 247,47
CV 44,175,199
CV 45,223,247
ae 42,72,768
s_bfe_u32 s52, s50, 0x1000f
v_mad_i32_i24 v50, v49, s52, v48
ST 38,256
ST 40,1024
ST 42,1792
ST 44,2560
ds_read_b64 v[12:13], v4 offset:4864
ds_read_b64 v[46:47], v4 offset:4872
AR 6,52
AR 7,76
AR 8,100
AR 9,124
WL 2
EF 6,52
EF 7,53
EF 8,54
EF 9,55
CV 14,6,7
CV 15,8,9
AR 6,53
AR 7,77
AR 8,101
AR 9,125
EF 6,52
EF 7,53
EF 8,54
EF 9,55
CV 16,6,7
CV 17,8,9
AR 6,54
AR 7,78
AR 8,102
AR 9,126
EF 6,52
EF 7,53
EF 8,54
EF 9,55
CV 18,6,7
CV 19,8,9
AR 6,55
AR 7,79
AR 8,103
AR 9,127
EF 6,52
EF 7,53
EF 8,54
EF 9,55
CV 20,6,7
CV 21,8,9
ae 42,72,864
s_bitcmp1_b32 s50, 16
s_cselect_b32 s52, 24576, 0
v_add_u32 v50, s52, v3
ST 14,0
ST 16,768
ST 18,1536
ST 20,2304
AR 6,56
AR 7,80
AR 8,104
AR 9,128
EF 6,52
EF 7,53
EF 8,54
EF 9,55
CV 22,6,7
CV 23,8,9
AR 6,57
AR 7,81
AR 8,105
AR 9,129
EF 6,52
EF 7,53
EF 8,54
EF 9,55
CV 24,6,7
CV 25,8,9
AR 6,58
AR 7,82
AR 8,106
AR 9,130
EF 6,52
EF 7,53
EF 8,54
EF 9,55
CV 26,6,7
CV 27,8,9
AR 6,59
AR 7,83
AR 8,107
AR 9,131
EF 6,52
EF 7,53
EF 8,54
EF 9,55
CV 28,6,7
CV 29,8,9
ae 42,72,864
s_bfe_u32 s52, s50, 0x10011
v_mad_i32_i24 v50, v49, s52, v48
ST 22,0
ST 24,768
ST 26,1536
ST 28,2304
BV 52,5120
AR 6,148
AR 7,172
AR 8,196
WL 1
EF 6,12
EF 7,13
EF 8,46
EF 152,47
CV 30,6,7
CV 31,8,152
AR 6,149
AR 7,173
AR 8,197
EF 6,12
EF 7,13
EF 8,46
EF 153,47
CV 32,6,7
CV 33,8,153
AR 6,150
AR 7,174
AR 8,198
EF 6,12
EF 7,13
EF 8,46
EF 154,47
CV 34,6,7
CV 35,8,154
AR 6,151
AR 7,175
AR 8,199
EF 6,12
EF 7,13
EF 8,46
EF 155,47
CV 36,6,7
CV 37,8,155
ae 42,72,864
s_bitcmp1_b32 s50, 16
s_cselect_b32 s52, 24576, 0
v_add_u32 v50, s52, v3
ST 30,128
ST 32,896
ST 34,1664
ST 36,2432
AR 6,152
AR 7,176
EF 6,12
EF 7,13
EF 132,46
EF 156,47
CV 38,6,7
CV 39,132,156
AR 6,153
AR 7,177
EF 6,12
EF 7,13
EF 133,46
EF 157,47
CV 40,6,7
CV 41,133,157
AR 6,154
AR 7,178
EF 6,12
EF 7,13
EF 134,46
EF 158,47
CV 42,6,7
CV 43,134,158
AR 6,155
AR 7,179
EF 6,12
EF 7,13
EF 135,46
EF 159,47
CV 44,6,7
CV 45,135,159
ae 42,72,864
s_bfe_u32 s52, s50, 0x10011
v_mad_i32_i24 v50, v49, s52, v48
ST 38,128
ST 40,896
ST 42,1664
ST 44,2432
WL 0
EF 176,52
EF 200,53
EF 224,54
EF 248,55
CV 14,176,200
CV 15,224,248
EF 177,52
EF 201,53
EF 225,54
EF 249,55
CV 16,177,201
CV 17,225,249
EF 178,52
EF 202,53
EF 226,54
EF 250,55
CV 18,178,202
CV 19,226,250
EF 179,52
EF 203,53
EF 227,54
EF 251,55
CV 20,179,203
CV 21,227,251
ae 42,72,864
s_bitcmp1_b32 s50, 16
s_cselect_b32 s52, 24576, 0
v_add_u32 v50, s52, v3
ST 14,256
ST 16,1024
ST 18,1792
ST 20,2560
EF 180,52
EF 204,53
EF 228,54
EF 252,55
CV 22,180,204
CV 23,228,252
EF 181,52
EF 205,53
EF 229,54
EF 253,55
CV 24,181,205
CV 25,229,253
EF 182,52
EF 206,53
EF 230,54
EF 254,55
CV 26,182,206
CV 27,230,254
EF 183,52
EF 207,53
EF 231,54
EF 255,55
CV 28,183,207
CV 29,231,255
ae 42,72,864
s_bfe_u32 s52, s50, 0x10011
v_mad_i32_i24 v50, v49, s52, v48
ST 22,256
ST 24,1024
ST 26,1792
ST 28,2560
s_waitcnt vmcnt(0) lgkmcnt(0)
s_branch 29f
FY 132
FY 136
FY 140
FY 144
FY 148
FY 152
FY 156
)"
R"(FY 160
FY 164
FY 168
FY 172
FY 176
FY 180
FY 184
FY 188
FY 192
FY 196
FY 200
FY 204
FY 208
FY 212
FY 216
FY 220
FY 224
FY 228
FY 232
FY 236
FY 240
FY 244
FY 248
FY 252
FY 256
FY 260
FY 264
FY 268
29:
.purgem MA
.purgem MV
.purgem NA
.purgem NV
.purgem AN
.purgem SH
.purgem PA_
.purgem PB_
.purgem AW
.purgem AR
.purgem UL
.purgem UH
.purgem AD
.purgem EF
.purgem TY
.purgem TX
.purgem RA
.purgem RC
.purgem BV
.purgem PA
.purgem DF
.purgem DW
.purgem DR
.purgem DH
.purgem DG
.purgem XL
.purgem ST
.purgem SA
.purgem CV
.purgem RS
.purgem PM
.purgem PF
.purgem FS
.purgem FL
.purgem FB
.purgem FC
.purgem FG
.purgem FX
.purgem FP
.purgem FR
.purgem FQ
.purgem FZ
.purgem FY
.purgem WL
.purgem WV
.purgem M
.purgem R1
.purgem R
.purgem P
.purgem Q
.purgem mw
.purgem mh
.purgem ad
.purgem ae
.purgem bc
.purgem W
.purgem H
)"
      : [flg] "=&s"(flg) : K2_OPS : K2_CLOB);
  if (flg) {
    const unsigned lane = __builtin_amdgcn_mbcnt_hi(~0u, __builtin_amdgcn_mbcnt_lo(~0u, 0u));
    const u16* const volatile __attribute__((address_space(4)))* ka = (const u16* const volatile __attribute__((address_space(4)))*)__builtin_amdgcn_kernarg_segment_ptr();
    {
      const u16* w1p = ka[6];
#pragma unroll 1
      for (unsigned f = blockIdx.x * 4 + w; f < 1152; f += 1024) {
        const u16* p = w1p + (32 * (f % 12) + 8 * (lane >> 4)) * HID + 16 * (f / 12) + (lane & 15);
        u32x4 v;
#pragma unroll
        for (int j = 0; j < 4; ++j) v[j] = p[2 * j * HID] | (unsigned)p[(2 * j + 1) * HID] << 16;
        *(u32x4*)(g_buf + f * 1024 + lane * 16) = v;
      }
    }
    slow_bar((unsigned*)(g_buf + G_BAR), w);
    slow_fc1(ka[0], ka[5], (char*)ka[1], (float*)smem + w * HID, w);
    slow_bar((unsigned*)(g_buf + G_BAR + 64), w);
    slow_fc2(ka[0], (const char*)ka[1], (u16*)ka[2], ka[3], ka[4], smem, w);
  }
}

extern "C" void kernel_launch(void* const* d_in, const int* in_sizes, int n_in,
                              void* d_out, int out_size, void* d_ws, size_t ws_size,
                              hipStream_t stream) {
  const u16* x = (const u16*)d_in[0];
  const u16* dw = (const u16*)d_in[1];
  const u16* w1 = (const u16*)d_in[2];
  const u16* w2 = (const u16*)d_in[3];
  const u16* grn_g = (const u16*)d_in[4];
  const u16* grn_b = (const u16*)d_in[5];
  char* ws = (char*)d_ws;
  if (ws_size < H_BYTES) return;
  (void)hipFuncSetAttribute((const void*)k1_fc1, hipFuncAttributeMaxDynamicSharedMemorySize, K1_LDS);
  (void)hipFuncSetAttribute((const void*)k2_fc2, hipFuncAttributeMaxDynamicSharedMemorySize, K2_LDS);

  k0_prep<<<K0_NB + NPAGE + NPAGX, 256, 0, stream>>>(w1, dw, ws, (const char*)x, grn_b, w2);
  k1_fc1<<<NWG + 8, 256, K1_LDS, stream>>>(x, ws, (const u16*)d_out, grn_g);
  k2_fc2<<<NWG, 256, K2_LDS, stream>>>(x, ws, (u16*)d_out, grn_g, w2, dw, w1);
}
```

```cpp
#include <hip/hip_runtime.h>
#include <hip/hip_bf16.h>
#include <stdint.h>

typedef float f32x4 __attribute__((ext_vector_type(4)));
typedef float f32x2 __attribute__((ext_vector_type(2)));
typedef unsigned u32x4 __attribute__((ext_vector_type(4)));
typedef __bf16 bf16x2_t __attribute__((ext_vector_type(2)));
typedef unsigned short u16;

constexpr float TZ = 175.f, TH = 175.f, TW2 = 175.f;
#define DI __device__ __forceinline__
#define LAS __attribute__((address_space(3)))

constexpr int NPIX = 147456;
constexpr int CH = 384;
constexpr int HID = 1536;
constexpr int NWG = 256;

constexpr size_t H_BYTES = (size_t)NPIX * HID * 2;
constexpr unsigned K1_LDS = 163840, K1_PF_OFF = 130048, K1_SMAX = 12, K1_NREP = 4, K1_G_TAB = 1228800, K1_G_CNT = 1239040, K1_G_LOC = 1236992, K1_G_DUM = 1255424,
  K1_G_ACC = 1320960, K1_G_LUT = 1382400, K1_G_BYTES = 1398784;
constexpr unsigned K1_GST = 25600, K1_WCH = 6400, K1_ZC = 1365;
constexpr unsigned K1_SB1 = 32768;
constexpr unsigned K2_SHT = 12483;
constexpr int K2_LDS = 163840, K2_HB = 98304, K2_RSZ = 13824, K2_E_OFF = 153600, K2_PF_OFF = 159744, K2_S16_OFF = 152576, K2_S16_D0 = 8192, K2_NAT = 8;

__device__ __attribute__((aligned(256))) char g_buf[K1_G_BYTES];
__device__ __attribute__((aligned(256))) float g_cs[8 * 384];
__device__ __attribute__((aligned(256))) char g_w2r[96 * 12 * 1024];
__device__ __attribute__((aligned(256))) char g_w2f[96 * 12 * 1024];
__device__ __attribute__((aligned(256))) char g_w2q[16 * 12 * 1024];
__device__ __attribute__((aligned(256))) float g_sc[64];
constexpr unsigned G_FLAG = K1_G_CNT + 14336;
constexpr unsigned G_PUB = K1_G_DUM;
__device__ __attribute__((aligned(256))) unsigned g_prm[1024 * 64];
constexpr unsigned G_BAR = K1_G_CNT + 13312;
constexpr unsigned H_TS = 49152, H_RS = 48 * H_TS, NPAGE = 217, NPAGX = 55;

DI unsigned pack_bf16(float a, float b) {
  f32x2 v = {a, b};
  bf16x2_t r = __builtin_convertvector(v, bf16x2_t);
  return __builtin_bit_cast(unsigned, r);
}
typedef _Float16 f16x2_t __attribute__((ext_vector_type(2)));
DI unsigned pack_f16(float a, float b) {
  f32x2 v = {a, b};
  f16x2_t r = __builtin_convertvector(v, f16x2_t);
  return __builtin_bit_cast(unsigned, r);
}
DI float bf_lo(unsigned u) { return __uint_as_float(u << 16); }
DI float bf_hi(unsigned u) { return __uint_as_float(u & 0xffff0000u); }
DI float round_bf16(float a) { return bf_lo(pack_bf16(a, 0.f)); }
DI float ld_bf16(const u16* p) { return __uint_as_float((unsigned)(*p) << 16); }

DI unsigned lds_addr(const char* p) { return (unsigned)(size_t)(const LAS char*)p; }

DI float gelu_tanh(float x) {
  const float c0 = -2.0f * 0.7978845608028654f * 1.4426950408889634f;
  const float c1 = c0 * 0.044715f;
  float x2 = x * x;
  float t = __builtin_fmaf(x2, c1, c0);
  float e = __builtin_amdgcn_exp2f(x * t);
  float r = __builtin_amdgcn_rcpf(1.0f + e);
  return x * r;
}

DI void st16_wt(void* p, u32x4 v) { asm volatile("global_store_dwordx4 %0, %1, off\n\ts_nop 1" :: "v"(p), "v"(v) : "memory"); }
template <int I> struct IC { static constexpr int value = I; };
template <int N, int I = 0, typename F> DI void static_for(F&& f) {
  if constexpr (I < N) { f(IC<I>{}); static_for<N, I + 1>(f); }
}
template <int C, int M> DI float dpp_add(float v) { return v + __int_as_float(__builtin_amdgcn_update_dpp(0, __float_as_int(v), C, M, 0xf, false)); }
DI float wave_sum63(float v) {
  v = dpp_add<0x111, 0xf>(v); v = dpp_add<0x112, 0xf>(v); v = dpp_add<0x114, 0xf>(v); v = dpp_add<0x118, 0xf>(v);
  v = dpp_add<0x142, 0xa>(v); return dpp_add<0x143, 0xc>(v);
}
DI unsigned cs_index(unsigned n) { return (n & ~63u) + (n & 3) * 16 + ((n >> 2) & 15); }
DI uint64_t uni64(uint64_t v) { return (uint64_t)__builtin_amdgcn_readfirstlane(v >> 32) << 32 | (unsigned)__builtin_amdgcn_readfirstlane(v); }
DI unsigned wg_side(unsigned b) { return (((const unsigned*)(g_buf + K1_G_LOC))[b & 3] ^ (b >> 2)) & 1; }
DI unsigned h_flip(const char* ws, unsigned region, unsigned t) { return __popc(((const unsigned*)(g_buf + K1_G_LOC + 64))[(((unsigned)(size_t)ws & 0x1fffffu) + region * H_RS + t * H_TS) >> 21]) > 2; }
DI unsigned x_flip(const u16* x, unsigned region) { return __popc(((const unsigned*)(g_buf + K1_G_LOC + 1024))[(((unsigned)(size_t)x & 0x1fffffu) + (12 * region + 6) * 49152u) >> 21]) > 2; }
DI unsigned sub_pixel(const u16* x, unsigned sub, unsigned m) {
  const unsigned blk = sub / 24, u = sub % 24;
  const unsigned region = blk < 256 ? (blk >> 3) * 4 + (blk & 3) : 128 + ((blk - 256) >> 1), side = blk < 256 ? wg_side(blk) : blk & 1;
  return (12 * region + (u >> 1)) * 64 + ((0xda43cb52fe987610ull >> (32 * (u & 1) + 16 * (side ^ x_flip(x, region)) + (m & 12))) & 15) * 4 + (m & 3);
}
DI unsigned h_piece(const char* ws, unsigned sub, unsigned t) {
  const unsigned blk = sub / 24, u = sub % 24;
  const unsigned region = blk < 256 ? (blk >> 3) * 4 + (blk & 3) : 128 + ((blk - 256) >> 1), side = blk < 256 ? wg_side(blk) : blk & 1;
  return region * H_RS + t * H_TS + (u >> 3) * 16384 + (u & 7) * 1024 + ((side ^ h_flip(ws, region, t)) << 13);
}
constexpr unsigned K0_NB = 145 + 24 + 96 + 16 + 48;
constexpr float U24 = 0x1p-24f, EPS = 1e-5f;
DI float wave_all(float v) { return __int_as_float(__builtin_amdgcn_readlane(__float_as_int(wave_sum63(v)), 63)); }
DI float pow2f(float v) { return __uint_as_float(__float_as_uint(v) & 0x7f800000u); }
DI u32x4 w_load(const u16* w) { return *(const u32x4*)(w + ((threadIdx.x >> 3) & 7) * 73408 + 36800 + (threadIdx.x & 7) * 8); }
DI float w_ms(u32x4 a) {
  float q = 0.f;
#pragma unroll
  for (int i = 0; i < 4; ++i) { const float lo = bf_lo(a[i]), hi = bf_hi(a[i]); q = __builtin_fmaf(lo, lo, __builtin_fmaf(hi, hi, q)); }
  return wave_all(q) * (1.0f / 512);
}
DI float w1_scale(float ms) { return pow2f(300.f * U24 * __builtin_amdgcn_rsqf(ms)); }
DI float w2_scale(float ms) { return TW2 * U24 * __builtin_amdgcn_rsqf(ms); }
struct Sc { float w1, zs, g, h, vlo, vhi, c; };
DI Sc scales(const u16* w1, const u16* x, const u16* dw) {
  const u32x4 a = w_load(w1);
  const unsigned l = threadIdx.x & 63;
  float s = 0.f, q = 0.f;
#pragma unroll
  for (int k = 0; k < 6; ++k) {
    const u32x4 v = *(const u32x4*)(x + (9001 + 18397 * (l >> 3)) * CH + 64 * k + 8 * (l & 7)), d = *(const u32x4*)(dw + 64 * k + 8 * (l & 7));
#pragma unroll
    for (int i = 0; i < 4; ++i) { const float lo = bf_lo(v[i]) * bf_lo(d[i]), hi = bf_hi(v[i]) * bf_hi(d[i]); s += lo + hi; q = __builtin_fmaf(lo, lo, __builtin_fmaf(hi, hi, q)); }
  }
  s = dpp_add<0x141, 0xf>(dpp_add<0x4e, 0xf>(dpp_add<0xb1, 0xf>(s))); q = dpp_add<0x141, 0xf>(dpp_add<0x4e, 0xf>(dpp_add<0xb1, 0xf>(q)));
  const float var = fmaxf(q * (1.0f / CH) - s * s * (1.0f / (CH * CH)), 0.f);
  const float r2 = wave_all(var * __builtin_amdgcn_rcpf(var + EPS)) * (1.0f / 64), rden = __builtin_amdgcn_exp2f(wave_all(__builtin_amdgcn_logf(var + EPS)) * (-1.0f / 128));
  const float s1 = w_ms(a), p2 = r2 * CH * s1;
  Sc c;
  c.w1 = w1_scale(s1);
  c.g = TZ * U24 * __builtin_amdgcn_rsqf(r2);
  c.h = TH * U24 * __builtin_amdgcn_rsqf(p2 * (0.25f + 0.5f * p2 * __builtin_amdgcn_rcpf(1.0f + 2.0f * p2)));
  c.c = fminf(__builtin_ceilf(0.1701f * 0x1p24f * c.h) + 1.0f, 1400.f);
  c.zs = fminf(pow2f(c.g * 2530.f), pow2f(c.g * 0x1p12f * rden));
  const float e = EPS * c.zs * c.zs, z2 = r2 * (660.f * 660.f / (TZ * TZ));
  c.vlo = 4.2f * c.g * c.g - e;
  c.vhi = fminf(0x1.ep29f * c.g * c.g - e, z2 < 1.0f ? e * z2 * __builtin_amdgcn_rcpf(1.0f - z2) : 0x1p120f);
  return c;
}
__global__ __launch_bounds__(256) void k0_prep(const u16* __restrict__ w1, const u16* __restrict__ dw, const char* __restrict__ ws, const char* __restrict__ x,
                                                const u16* __restrict__ grn_b, const u16* __restrict__ w2) {
  uint64_t pc; int d1;
  asm volatile("s_getpc_b64 %0\n1:\n\ts_mov_b32 %1, k1_code-1b" : "=s"(pc), "=s"(d1));
  if (threadIdx.x >= 192) pc += (int64_t)d1 + ((int)(blockIdx.x >> 3) - 3) * 8192;
  unsigned pfd = 0;
  if (blockIdx.x < 64 && (threadIdx.x < 150 || (blockIdx.x < 48 && threadIdx.x >= 192))) pfd = *(const __attribute__((address_space(1))) unsigned*)((pc & ~127ull) + threadIdx.x * 128);
#define K0_END() do { asm volatile("" :: "v"(pfd)); } while (0)
  unsigned xcc;
  asm volatile("s_getreg_b32 %0, hwreg(HW_REG_XCC_ID)" : "=s"(xcc));
  xcc = (xcc >> 2) & 1;
  if (blockIdx.x < 4 && threadIdx.x == 0) ((unsigned*)(g_buf + K1_G_LOC))[blockIdx.x] = 7 * xcc;
  constexpr unsigned NP = NPAGE + NPAGX;
  __shared__ Sc ssc;
  __shared__ float ss2;
  const bool sw = threadIdx.x >= 192;
  const unsigned bi = blockIdx.x;
  const unsigned bx = bi < NP ? bi + K0_NB : bi - NP;
  if (bx >= K0_NB) {
    __shared__ unsigned lat[8];
    const unsigned pg = bx - K0_NB, w = threadIdx.x >> 6, lane = threadIdx.x & 63;
    auto gran = [&](unsigned p) {
      const bool h = p < NPAGE;
      const char* buf = h ? ws : x;
      const int o0 = (int)((h ? p : p - NPAGE) << 21) - (int)((unsigned)(size_t)buf & 0x1fffffu);
      const unsigned al = h ? 49152 : 12 * 49152;
      const unsigned q = ((((unsigned)max(o0, 0) + al - 1) >> (h ? 14 : 16)) * (h ? 87382u : 29128u)) >> 18;
      return buf + min(q * al, (unsigned)(h ? H_BYTES : H_BYTES / 4) - 16384u);
    };
    if (lane < 5) {
      const char* a = gran(lane < 4 ? min(pg + NP - 1 - (lane >> 1), pg - 1 - (lane >> 1)) : pg) + (lane & 1) * 8192 + w * 2048 + (lane < 4 ? 1024 : 0);
      const char* b = a;
      const char* own = (const char*)uni64(__builtin_amdgcn_readlane((uint64_t)a >> 32, 4) * 4294967296ull + (unsigned)__builtin_amdgcn_readlane((unsigned)(uint64_t)a, 4));
      unsigned long long t0, t1, t2;
      int n = 2;
      asm volatile("" : "+s"(n));
#pragma unroll 1
      do {
        unsigned v;
        asm volatile("s_cmp_eq_u32 %6, 2\n\ts_cbranch_scc1 1f\n\ts_sleep 4\n1:\n\ts_memtime %0\n\ts_waitcnt lgkmcnt(0)\n\tglobal_load_dword %3, %4, off sc1\n\ts_waitcnt vmcnt(0)\n\ts_memtime %1\n\ts_waitcnt lgkmcnt(0)\n\t"
                     "global_load_dword %3, %5, off sc1\n\ts_waitcnt vmcnt(0)\n\ts_memtime %2\n\ts_waitcnt lgkmcnt(0)" : "=&s"(t0), "=&s"(t1), "=&s"(t2), "=&v"(v) : "v"(a), "v"(b), "s"(n) : "memory", "scc");
        a = own + 1024 + (w & 1) * 8192; b = a + 8192 - (w & 1) * 16384;
      } while (--n);
      lat[w + 4 * (w & 1)] = (unsigned)(t1 - t0); lat[w + 4 - 4 * (w & 1)] = (unsigned)(t2 - t1);
    }
    __syncthreads();
    if (threadIdx.x == 0) {
      auto fastest = [&](const unsigned* l) { unsigned m = 9999; for (int i = 0; i < 4; ++i) m = l[i] < 300 ? m : min(m, l[i]); return (int)m; };
      const int df = fastest(lat) - fastest(lat + 4);
      const unsigned v = xcc ^ (df > 0), sure = abs(df) > 80;
      char* e = g_buf + K1_G_LOC + 64 + 4 * (pg < NPAGE ? pg : pg + 240 - NPAGE);
      *(u16*)(e + 2) = v ? 3 + 4 * sure : 1 - sure;
      if (pg == 0 || pg == NPAGE) e[0] = v; else e[-3] = v ^ 1;
      if (pg == NPAGE - 1 || pg == NP - 1) e[1] = v; else e[4] = v ^ 1;
    }
    K0_END();
    return;
  }
  if (bx >= 145 + 24 + 96 + 16) {
    __shared__ __attribute__((aligned(16))) float red[2][16][32];
    const int t = bx - 281, c8 = (threadIdx.x & 3) * 8;
    u32x4 sm, v[6];
    if (sw) sm = w_load(w1);
#pragma unroll
    for (int k = 0; k < 6; ++k) v[k] = *(const u32x4*)(w1 + (size_t)(64 * k + (threadIdx.x >> 2)) * HID + 32 * t + c8);
    if (sw) ss2 = w1_scale(w_ms(sm));
    __syncthreads();
    const float s1 = ss2;
    float a[8], q[8];
#pragma unroll
    for (int i = 0; i < 8; ++i) {
      a[i] = q[i] = 0.f;
#pragma unroll
      for (int k = 0; k < 6; ++k) {
        const float f = (float)(_Float16)(s1 * (i & 1 ? bf_hi(v[k][i >> 1]) : bf_lo(v[k][i >> 1]))) * 0x1p24f;
        a[i] += f; q[i] = __builtin_fmaf(f, f, q[i]);
      }
      a[i] = dpp_add<0x118, 0xf>(dpp_add<0x114, 0xf>(a[i])); q[i] = dpp_add<0x118, 0xf>(dpp_add<0x114, 0xf>(q[i]));
    }
    if ((threadIdx.x & 12) == 12) {
#pragma unroll
      for (int i = 0; i < 2; ++i) {
        *(f32x4*)&red[0][threadIdx.x >> 4][c8 + 4 * i] = (f32x4){a[4 * i], a[4 * i + 1], a[4 * i + 2], a[4 * i + 3]};
        *(f32x4*)&red[1][threadIdx.x >> 4][c8 + 4 * i] = (f32x4){q[4 * i], q[4 * i + 1], q[4 * i + 2], q[4 * i + 3]};
      }
    }
    __syncthreads();
    if (threadIdx.x < 64) {
      const int n = threadIdx.x & 31;
      float s = 0.f;
#pragma unroll
      for (int i = 0; i < 16; ++i) s += red[threadIdx.x >> 5][i][n];
      const float c = (0.f - (float)K1_ZC * s) * 0x1p-48f;
      char* p = g_buf + t * K1_GST + 5120;
      if (threadIdx.x < 32) *(f32x4*)(p + 272 * (n >> 4) + (n & 15) * 16) = (f32x4){c, c, c, c};
      const bool any = __ballot(threadIdx.x >= 32 && !(s >= 384.f * 64 * 64 && s <= 384.f * 1024 * 1024)) != 0;
      if (threadIdx.x == 0) g_buf[G_FLAG + 4 * 98 + t] = any;
    }
    K0_END();
    return;
  }
  if (bx >= 145 + 24 + 96) {
    if (sw) ssc = scales(w1, (const u16*)x, dw);
    __syncthreads();
    const Sc sc = ssc;
    const unsigned e = (bx - 265) * 256 + threadIdx.x;
    const float u = 1.0f / (sc.g * sc.w1);
    float r[2];
#pragma unroll
    for (int i = 0; i < 2; ++i) {
      const unsigned c = 2 * e + i;
      r[i] = __builtin_fmaf(sc.h, gelu_tanh(u * __uint_as_float((c >> 12) << 31 | ((c & 4095) + 79 * 128) << 16 | 0x8000u)), sc.c * U24);
      if (!(r[i] >= 0.f && r[i] < 8189.f * U24)) r[i] = __builtin_inff();
    }
    *(unsigned*)(g_buf + K1_G_LUT + 4 * e) = pack_f16(r[0], r[1]);
    K0_END();
    return;
  }
  if (bx >= 145 + 24) {
    __shared__ __attribute__((aligned(16))) u16 tw[32][200];
    const int kappa = (bx - 169) >> 1, nh = (bx - 169) & 1;
    u32x4 sm, v[3];
    if (sw) sm = w_load(w2);
#pragma unroll
    for (int i = 0; i < 3; ++i) { const int e = threadIdx.x + 256 * i; v[i] = *(const u32x4*)(w2 + (size_t)(32 * kappa + e / 24) * CH + 192 * nh + (e % 24) * 8); }
    if (sw) ss2 = w2_scale(w_ms(sm));
#pragma unroll
    for (int i = 0; i < 3; ++i) { const int e = threadIdx.x + 256 * i; *(u32x4*)&tw[e / 24][(e % 24) * 8] = v[i]; }
    __syncthreads();
    const int lane = threadIdx.x & 63, g = lane >> 4;
    const float s2 = 256.f * ss2;
    bool bad = false;
#pragma unroll
    for (int i = 0; i < 3; ++i) {
      const int fi = (threadIdx.x >> 6) + 4 * i, n = 64 * (fi >> 2) + 4 * (lane & 15) + (fi & 3);
      float wv[8];
      unsigned bits = 0;
#pragma unroll
      for (int j = 0; j < 8; ++j) {
        wv[j] = ld_bf16(&tw[16 * (j >> 2) + 4 * g + (j & 3)][n]);
        bits |= wv[j] != 0.f ? 256u << j : 0u;
        wv[j] *= s2;
        bits |= fabsf(wv[j]) >= 48 * 256 * U24 ? 1u << j : 0u;
        bad |= !(fabsf(wv[j]) <= 500.f);
      }
      const unsigned o = (((kappa & ~1) + fi / 6) * 24 + nh * 12 + (fi % 6) * 2 + (kappa & 1)) * 1024 + lane * 16;
      st16_wt(g_w2r + o, (u32x4){pack_f16(wv[0], wv[1]), pack_f16(wv[2], wv[3]), pack_f16(wv[4], wv[5]), pack_f16(wv[6], wv[7])});
      constexpr float q = 1.0f / 256;
      if (kappa < 8) st16_wt(g_w2q + o, (u32x4){pack_f16(q * wv[0], q * wv[1]), pack_f16(q * wv[2], q * wv[3]), pack_f16(q * wv[4], q * wv[5]), pack_f16(q * wv[6], q * wv[7])});
      static_for<4>([&](auto ic) { bits |= __builtin_amdgcn_update_dpp(0u, bits, 0x120 + (1 << decltype(ic)::value), 0xf, 0xf, false); });
      bad |= ((bits >> 8) & ~bits & 255u) != 0;
    }
    const bool any = __ballot(bad) != 0;
    if (lane == 0) ((char*)(g_buf + G_FLAG))[4 * (bx - 169) + (threadIdx.x >> 6)] = any;
    K0_END();
    return;
  }
  if (bx >= 145) {
    __shared__ __attribute__((aligned(16))) float red[16][64];
    const int c0 = 64 * ((bx - 145) >> 2), rg = (bx - 145) & 3, c8 = (threadIdx.x & 7) * 8;
    float a[8];
#pragma unroll
    for (int i = 0; i < 8; ++i) a[i] = 0.f;
#pragma unroll
    for (int k = 0; k < 12; ++k) {
      const int ch = 384 * rg + 32 * k + (threadIdx.x >> 3);
      const float b = ld_bf16(grn_b + ch);
      const u32x4 v = *(const u32x4*)(w2 + (size_t)ch * CH + c0 + c8);
#pragma unroll
      for (int i = 0; i < 4; ++i) { a[2 * i] = __builtin_fmaf(b, bf_lo(v[i]), a[2 * i]); a[2 * i + 1] = __builtin_fmaf(b, bf_hi(v[i]), a[2 * i + 1]); }
    }
#pragma unroll
    for (int i = 0; i < 8; ++i) a[i] = dpp_add<0x118, 0xf>(a[i]);
    if (threadIdx.x & 8) {
      *(f32x4*)&red[threadIdx.x >> 4][c8] = (f32x4){a[0], a[1], a[2], a[3]};
      *(f32x4*)&red[threadIdx.x >> 4][c8 + 4] = (f32x4){a[4], a[5], a[6], a[7]};
    }
    __syncthreads();
    if (threadIdx.x < 64) {
      float s = 0.f;
#pragma unroll
      for (int i = 0; i < 16; ++i) s += red[i][threadIdx.x];
      g_cs[(4 + rg) * 384 + cs_index(c0 + threadIdx.x)] = s;
      g_cs[64 * (bx - 145) + threadIdx.x] = 0.f;
    }
    K0_END();
    return;
  }
  const unsigned bid = bx;
  if (bid == 0) {
    if (sw) ssc = scales(w1, (const u16*)x, dw);
    if ((threadIdx.x >> 6) == 2) ss2 = w2_scale(w_ms(w_load(w2)));
#pragma unroll
    for (int i = 0; i < 4; ++i) if (threadIdx.x + 256 * i < 896) *(u32x4*)(g_buf + K1_G_CNT + (threadIdx.x + 256 * i) * 16) = (u32x4){0u, 0u, 0u, 0u};
    *(unsigned*)(g_buf + G_PUB + threadIdx.x * 256) = 0u;
    const int e = threadIdx.x, e2 = e < 192 ? 32 * (e >> 4) + 8 * ((e & 15) >> 2) : 8 * min(e - 192, 47);
    const u32x4 d = *(const u32x4*)(dw + e2);
    __syncthreads();
    const Sc sc = ssc;
    const float s2 = ss2;
    u32x4 v;
#pragma unroll
    for (int k = 0; k < 4; ++k) v[k] = pack_bf16(sc.zs * bf_lo(d[k]), sc.zs * bf_hi(d[k]));
    if (e < 192) {
#pragma unroll
      for (int k = 0; k < 4; ++k) v[k] &= (k & 1) != ((e >> 1) & 1) ? 0u : e & 1 ? 0xffff0000u : 0xffffu;
    }
    if (e >= 240) v = (u32x4){__float_as_uint(EPS * sc.zs * sc.zs), __float_as_uint(sc.vhi), __float_as_uint(sc.g), __float_as_uint(sc.vlo)};
    *(u32x4*)(g_buf + K1_G_TAB + (e < 192 ? e : e + 192) * 16) = v;
    auto sane = [](float v) { return v >= 0x1p-45f && v <= 0x1p20f; };
    if (threadIdx.x < 2) ((unsigned*)(g_buf + G_FLAG))[96 + threadIdx.x] = !(sane(sc.w1) && sane(sc.zs) && sane(sc.g) && sane(sc.h) && sane(s2));
    if (threadIdx.x == 0) {
      const float ik = 1.0f / (sc.h * s2);
      *(f32x4*)g_sc = (f32x4){sc.h * s2, ik, 0x1p-28f / (sc.h * sc.h), -sc.c * U24 * ik};
      g_sc[4] = __uint_as_float(pack_f16(-sc.c * U24, -sc.c * U24));
      *(unsigned*)(g_buf + K1_G_CNT + 16320) = pack_f16(-sc.c * 0x1p-10f, -sc.c * 0x1p-10f);
    }
    if (threadIdx.x < 48) *(u32x4*)(g_buf + threadIdx.x * K1_GST + 5376) = (u32x4){pack_f16(sc.c * U24, 0.f), 0u, 0u, 0u};
    K0_END();
    return;
  }
  __shared__ __attribute__((aligned(16))) u16 tl[32][136];
  const int e = (bid - 1) * 256 + threadIdx.x, ks = (bid - 1) % 12, cb = (bid - 1) / 12;
  u32x4 sm, v[2];
  if (sw) sm = w_load(w1);
#pragma unroll
  for (int i = 0; i < 2; ++i) v[i] = *(const u32x4*)(w1 + (size_t)(32 * ks + (threadIdx.x >> 4) + 16 * i) * HID + 128 * cb + (threadIdx.x & 15) * 8);
  if (sw) ss2 = w1_scale(w_ms(sm));
#pragma unroll
  for (int i = 0; i < 2; ++i) *(u32x4*)&tl[(threadIdx.x >> 4) + 16 * i][(threadIdx.x & 15) * 8] = v[i];
  if (e < (int)(K1_NREP + 1) * 768) *(u32x4*)(g_buf + K1_G_ACC + e * 16) = (u32x4){0u, 0u, 0u, 0u};
  __syncthreads();
  const float s1 = ss2;
#pragma unroll
  for (int i = 0; i < 2; ++i) {
    const int lane = threadIdx.x & 63, f = (threadIdx.x >> 6) + 4 * i, n = 16 * f + (lane & 15), k0 = 8 * (lane >> 4);
    u32x4 P;
#pragma unroll
    for (int jj = 0; jj < 4; ++jj) P[jj] = pack_f16(s1 * ld_bf16(&tl[k0 + 2 * jj][n]), s1 * ld_bf16(&tl[k0 + 2 * jj + 1][n]));
    const int fs = (f & 1) * 12 + ks;
    st16_wt(g_buf + (4 * cb + (f >> 1)) * K1_GST + (fs + (fs >= 5)) * 1024 + lane * 16, P);
  }
  K0_END();
}

constexpr unsigned G_ACC2 = K1_G_ACC + K1_NREP * 12288;

#define VA_CLOB \
  "v0", "v1", "v2", "v3", "v4", "v5", "v6", "v7", "v8", "v9", "v10", "v11", "v12", "v13", "v14", "v15", "v16", "v17", "v18", "v19", "v20", "v21", "v22", "v23", "v24", "v25", "v26", "v27", "v28", "v29", "v30", "v31", \
  "v32", "v33", "v34", "v35", "v36", "v37", "v38", "v39", "v40", "v41", "v42", "v43", "v44", "v45", "v46", "v47", "v48", "v49", "v50", "v51", "v52", "v53", "v54", "v55", "v56", "v57", "v58", "v59", "v60", "v61", "v62", "v63", \
  "v64", "v65", "v66", "v67", "v68", "v69", "v70", "v71", "v72", "v73", "v74", "v75", "v76", "v77", "v78", "v79", "v80", "v81", "v82", "v83", "v84", "v85", "v86", "v87", "v88", "v89", "v90", "v91", "v92", "v93", "v94", "v95", \
  "v96", "v97", "v98", "v99", "v100", "v101", "v102", "v103", "v104", "v105", "v106", "v107", "v108", "v109", "v110", "v111", "v112", "v113", "v114", "v115", "v116", "v117", "v118", "v119", "v120", "v121", "v122", "v123", "v124", "v125", "v126", "v127", \
  "v128", "v129", "v130", "v131", "v132", "v133", "v134", "v135", "v136", "v137", "v138", "v139", "v140", "v141", "v142", "v143", "v144", "v145", "v146", "v147", "v148", "v149", "v150", "v151", "v152", "v153", "v154", "v155", "v156", "v157", "v158", "v159", \
  "v160", "v161", "v162", "v163", "v164", "v165", "v166", "v167", "v168", "v169", "v170", "v171", "v172", "v173", "v174", "v175", "v176", "v177", "v178", "v179", "v180", "v181", "v182", "v183", "v184", "v185", "v186", "v187", "v188", "v189", "v190", "v191", \
  "v192", "v193", "v194", "v195", "v196", "v197", "v198", "v199", "v200", "v201", "v202", "v203", "v204", "v205", "v206", "v207", "v208", "v209", "v210", "v211", "v212", "v213", "v214", "v215", "v216", "v217", "v218", "v219", "v220", "v221", "v222", "v223", \
  "v224", "v225", "v226", "v227", "v228", "v229", "v230", "v231", "v232", "v233", "v234", "v235", "v236", "v237", "v238", "v239", "v240", "v241", "v242", "v243", "v244", "v245", "v246", "v247", "v248", "v249", "v250", "v251", "v252", "v253", "v254", "v255", \
  "a0", "a1", "a2", "a3", "a4", "a5", "a6", "a7", "a8", "a9", "a10", "a11", "a12", "a13", "a14", "a15", "a16", "a17", "a18", "a19", "a20", "a21", "a22", "a23", "a24", "a25", "a26", "a27", "a28", "a29", "a30", "a31", \
  "a32", "a33", "a34", "a35", "a36", "a37", "a38", "a39", "a40", "a41", "a42", "a43", "a44", "a45", "a46", "a47", "a48", "a49", "a50", "a51", "a52", "a53", "a54", "a55", "a56", "a57", "a58", "a59", "a60", "a61", "a62", "a63", \
  "a64", "a65", "a66", "a67", "a68", "a69", "a70", "a71", "a72", "a73", "a74", "a75", "a76", "a77", "a78", "a79", "a80", "a81", "a82", "a83", "a84", "a85", "a86", "a87", "a88", "a89", "a90", "a91", "a92", "a93", "a94", "a95", \
  "a96", "a97", "a98", "a99", "a100", "a101", "a102", "a103", "a104", "a105", "a106", "a107", "a108", "a109", "a110", "a111", "a112", "a113", "a114", "a115", "a116", "a117", "a118", "a119", "a120", "a121", "a122", "a123", "a124", "a125", "a126", "a127", \
  "a128", "a129", "a130", "a131", "a132", "a133", "a134", "a135", "a136", "a137", "a138", "a139", "a140", "a141", "a142", "a143", "a144", "a145", "a146", "a147", "a148", "a149", "a150", "a151", "a152", "a153", "a154", "a155", "a156", "a157", "a158", "a159", \
  "a160", "a161", "a162", "a163", "a164", "a165", "a166", "a167", "a168", "a169", "a170", "a171", "a172", "a173", "a174", "a175", "a176", "a177", "a178", "a179", "a180", "a181", "a182", "a183", "a184", "a185", "a186", "a187", "a188", "a189", "a190", "a191", \
  "a192", "a193", "a194", "a195", "a196", "a197", "a198", "a199", "a200", "a201", "a202", "a203", "a204", "a205", "a206", "a207", "a208", "a209", "a210", "a211", "a212", "a213", "a214", "a215", "a216", "a217", "a218", "a219", "a220", "a221", "a222", "a223", \
  "a224", "a225", "a226", "a227", "a228", "a229", "a230", "a231", "a232", "a233", "a234", "a235", "a236", "a237", "a238", "a239", "a240", "a241", "a242", "a243", "a244", "a245", "a246", "a247", "a248", "a249", "a250", "a251", "a252", "a253", "a254", "a255"
#define K1_CLOB "memory", "vcc", "scc", "s36", "s37", "s38", "s39", "s40", "s41", "s42", "s43", "s44", "s45", "s46", "s47", "s48", "s49", "s50", "s51", "s52", "s53", "s54", "s55", "s56", "s57", "s58", "s59", "s60", "s61", "s62", "s63", "s64", "s65", "s66", "s67", "s68", "s69", "s70", "s71", "s72", "s73", "s74", "s75", "s76", "s77", "s78", "s79", "s80", "s81", "s82", "s83", "s84", "s85", "s86", "s87", "s88", "s89", "s90", "s91", "s92", "s93", "s94", "s95", "s96", "s97", VA_CLOB
DI void k2_params(const u16* x, const char* hws, const u16* out, const u16* grn_g, unsigned tid) {
  const unsigned e = (tid >> 2) * 32 + blockIdx.x * 4 + (tid & 3);
  const unsigned b = e >> 2, w = e & 3, pg = w & 1, nh = w >> 1, q = (b >> 3) * 4 + (b & 3), rs = 128 + (q >> 1), hf = q & 1, side = wg_side(b);
  auto fmask = [&](unsigned region) {
    const unsigned o = ((unsigned)(size_t)hws & 0x1fffffu) + region * H_RS + nh * H_TS;
    const unsigned* p = (const unsigned*)(g_buf + K1_G_LOC + 64) + (o >> 21);
    const unsigned r = 0x200000u + 2 * H_TS - 1 - (o & 0x1fffffu), d1 = ((r >> 15) * 171) >> 9, d2 = (((r + 0x200000u) >> 15) * 171) >> 9;
    const unsigned a1 = ~0u << d1, a2 = d2 < 32 ? ~0u << d2 : 0u;
    return ((__popc(p[0]) > 2 ? ~a1 : 0u) | (__popc(p[1]) > 2 ? a1 & ~a2 : 0u) | (__popc(p[2]) > 2 ? a2 : 0u)) ^ (0u - side);
  };
  const unsigned f0 = fmask(q), f1 = fmask(rs);
  const unsigned fx = ((side ^ x_flip(x, q)) ? ~K2_SHT : 0u) | ((side ^ x_flip(x, rs)) ? K2_SHT : 0u);
  const uint64_t h0 = (uint64_t)hws + q * H_RS + nh * (H_TS + 2048) + pg * 3072, h1 = (uint64_t)hws + rs * H_RS + nh * (H_TS + 2048) + hf * 3072 + pg * 1536;
  const unsigned p0 = (12 * q + 2 * pg) * 49152 + nh * 384, p1 = (12 * rs + 2 * hf + pg) * 49152 + nh * 384;
  const unsigned e1 = 1024 + (e >> 3);
  auto kap = [](unsigned f) { return f / 48 * 2 + (f & 1); };
  auto sco = [&](unsigned f) { return kap(f) * 64 + (kap(f) < 16 ? K2_S16_D0 : 0); };
  auto cso = [&](unsigned f) { const unsigned r = f % 48; return ((f < 192 ? min(f / 48, 2u) : 3) * 384 + (r / 12 % 2 * 12 + r / 24 * 6 + r % 12 / 2) * 16) * 4; };
  typedef uint64_t u64x2 __attribute__((ext_vector_type(2)));
  u64x2* P = (u64x2*)(g_prm + 64 * e);
  P[0] = (u64x2){f0 | (uint64_t)f1 << 32, fx | (uint64_t)(sco(e) | (sco(e1) + (e & 6) * 8) << 16) << 32};
  const u32x4 sc = *(const u32x4*)g_sc;
  P[1] = (u64x2){(((b >> 3) * 4 + w) & 15) * 1024 | (uint64_t)sc[0] << 32, sc[1] | (uint64_t)sc[2] << 32};
  P[2] = (u64x2){h0, h0 + 8192};
  P[3] = (u64x2){h1, h1 + 8192};
  P[4] = (u64x2){__float_as_uint(g_sc[4]) | (uint64_t)((e1 - e) * 1024 + (e & 7) * 128) << 32, 0};
  P[5] = (u64x2){(uint64_t)x + p0, (uint64_t)x + p1};
  P[6] = (u64x2){(uint64_t)out + p0, (uint64_t)out + p1};
  P[7] = (u64x2){0x7fffffffffffffull, (uint64_t)g_buf + G_FLAG};
  P[8] = (u64x2){(uint64_t)g_w2f + w * 6144, (uint64_t)g_w2q + w * 6144};
  P[9] = (u64x2){(uint64_t)g_w2r + e * 1024, (uint64_t)g_w2f + e * 1024};
  P[10] = (u64x2){(uint64_t)grn_g + w * 256, (uint64_t)g_cs + w * 384 + 2048};
  P[11] = (u64x2){(uint64_t)g_buf + G_PUB + b * 256, (uint64_t)g_buf + G_PUB + w * 16384};
  P[12] = (u64x2){cso(e) | (uint64_t)(cso(e1) + (e & 1) * 32) << 32, (uint64_t)g_cs};
  P[13] = (u64x2){sc[3] | (uint64_t)1 << (32 + (b >> 3) % K2_NAT), 0};
}
__global__ __launch_bounds__(256, 1) void k1_fc1(const u16* __restrict__ x, char* __restrict__ hws, const u16* __restrict__ out, const u16* __restrict__ grn_g) {
  extern __shared__ __attribute__((aligned(16))) char smem[];
  const unsigned w = __builtin_amdgcn_readfirstlane(threadIdx.x >> 6);
  if (blockIdx.x < 8) {
    if (w < 2) k2_params(x, hws, out, grn_g, w * 64 + __builtin_amdgcn_mbcnt_hi(~0u, __builtin_amdgcn_mbcnt_lo(~0u, 0u)));
    return;
  }
  const unsigned lds0 = lds_addr(smem);
  const unsigned b = blockIdx.x - 8;
  const unsigned dir = (b >> 3) & 1;
  const uint64_t xp = (uint64_t)x, hp = (uint64_t)hws, gp = (uint64_t)g_buf, wp = gp + w * K1_WCH;
  const unsigned d = dir ? ~0u : 1u, wrap = dir ? ~0u : 48u, rst = dir ? 47u : 0u, pos0 = dir ? K1_SMAX - 1 : 0u;
  const unsigned xo0 = (12 * ((b >> 3) * 4 + (b & 3)) + w) * 49152, xs1 = (1536 + w + (dir ? 8 : 0)) * 49152, sb1 = 1536 * w + (dir ? K1_SB1 : 0);
  const unsigned r0o = ((b >> 3) * 4 + (b & 3)) * H_RS, tsel = 4 * (b & 3), s2 = (b >> 2) & 1;
  const uint64_t ap = gp + K1_G_ACC + (b % K1_NREP) * 12288;
  const unsigned ca0 = w == 0 ? dir * 64 : K1_G_DUM - K1_G_CNT + (4 * b + w) * 64;
  const unsigned pm = lds0 + K1_PF_OFF, pf = ((b >> 3) * 4 + w) * 1024;
  unsigned nb;
  asm volatile(
R"(.macro M t,f,w
.if \f < 30
v_mfma_f32_16x16x32_f16 v[56+4*\t:56+4*\t+3], v[128+4*\f:128+4*\f+3], a[216+4*\w:216+4*\w+3], v[56+4*\t:56+4*\t+3]
.else
v_mfma_f32_16x16x32_f16 v[56+4*\t:56+4*\t+3], a[4*\f-4*30:4*\f-4*30+3], a[216+4*\w:216+4*\w+3], v[56+4*\t:56+4*\t+3]
.endif
.endm
.macro Z t,f,w
.if \f < 30
v_mfma_f32_16x16x32_f16 v[56+4*\t:56+4*\t+3], v[128+4*\f:128+4*\f+3], a[216+4*\w:216+4*\w+3], v[252:255]
.else
v_mfma_f32_16x16x32_f16 v[56+4*\t:56+4*\t+3], a[4*\f-4*30:4*\f-4*30+3], a[216+4*\w:216+4*\w+3], v[252:255]
.endif
.endm
.macro LZ a,f
.if \f < 30
v_mfma_f32_16x16x32_bf16 v[36:36+3], v[\a:\a+3], v[128+4*\f:128+4*\f+3], 0
.else
v_mfma_f32_16x16x32_bf16 v[36:36+3], v[\a:\a+3], a[4*\f-4*30:4*\f-4*30+3], 0
.endif
.endm
.macro LS a,f
.if \f < 30
v_mfma_f32_16x16x32_bf16 v[36:36+3], v[\a:\a+3], v[128+4*\f:128+4*\f+3], v[36:36+3]
.else
v_mfma_f32_16x16x32_bf16 v[36:36+3], v[\a:\a+3], a[4*\f-4*30:4*\f-4*30+3], v[36:36+3]
.endif
.endm
.macro GZ f
.if \f < 30
v_mfma_f32_16x16x32_f16 v[52:55], v[128+4*\f:128+4*\f+3], v[128+4*\f:128+4*\f+3], 0
.else
v_mfma_f32_16x16x32_f16 v[52:55], a[4*\f-4*30:4*\f-4*30+3], a[4*\f-4*30:4*\f-4*30+3], 0
.endif
.endm
.macro GS f
.if \f < 30
v_mfma_f32_16x16x32_f16 v[52:55], v[128+4*\f:128+4*\f+3], v[128+4*\f:128+4*\f+3], v[52:55]
.else
v_mfma_f32_16x16x32_f16 v[52:55], a[4*\f-4*30:4*\f-4*30+3], a[4*\f-4*30:4*\f-4*30+3], v[52:55]
.endif
.endm
.macro XL f,o
.if \f < 30
global_load_dwordx4 v[128+4*\f:128+4*\f+3], v3, s[46:47] offset:\o
.else
global_load_dwordx4 a[4*\f-4*30:4*\f-4*30+3], v3, s[46:47] offset:\o
.endif
.endm
.macro C d,a,b
v_cvt_pk_bf16_f32 v[\d], v[\a], v[\b]
.endm
.macro LL t,z
v_lshlrev_b32_sdwa v[\t], 1, v[\z] dst_sel:DWORD dst_unused:UNUSED_PAD src0_sel:DWORD src1_sel:WORD_1
.endm
.macro LA t
ds_read_u16 v[\t], v[\t] offset:5376
.endm
.macro LC_ t
ds_read_u16_d16_hi v[\t], v[\t] offset:5376
.endm
.macro LO p,a,b
v_bfi_b32 v[\p], s36, v[\a], v[\b]
.endm
.macro HZ q,p
v_pk_mul_f16 v[\q], v[\p], v[\p]
.endm
.macro HS q,p
v_pk_fma_f16 v[\q], v[\p], v[\p], v[\q]
.endm
.macro HX q
v_fma_mix_f32 v[\q], v[\q], 1.0, v[\q] op_sel:[0,0,1] op_sel_hi:[1,0,1]
.endm
.macro AW a,s
v_accvgpr_write_b32 a[\a], v[\s]
.endm
.macro RA d,v,o
ds_read_b128 a[\d:\d+3], v[\v] offset:\o
.endm
.macro RV d,v,o
ds_read_b128 v[\d:\d+3], v[\v] offset:\o
.endm
.macro W2 a,b,o
ds_write2st64_b32 v4, v[\a], v[\b] offset0:\o offset1:\o+4
.endm
.macro W1 a,o
ds_write_b32 v4, v[\a] offset:\o
.endm
.macro DW o
global_load_lds_dwordx4 v0, s[40:41] offset:\o
.endm
.macro DF
global_load_lds_dwordx4 v0, s[70:71]
.endm
.macro ST d
.if ((\d - 104) / 4) & 1
global_store_dwordx4 v2, v[\d:\d+3], s[42:43] nt sc1
.else
global_store_dwordx2 v31, v[\d:\d+1], s[42:43] nt sc1
.endif
.endm
.macro SU d
.if ((\d - 104) / 4) & 1
global_store_dwordx4 v2, v[\d:\d+3], s[42:43]  sc1
.else
global_store_dwordx2 v31, v[\d:\d+1], s[42:43]  sc1
.endif
.endm
.macro SP
global_atomic_add_f64 v6, v[14:15], s[48:49]
.endm
.macro AT
global_atomic_add v11, v9, v10, s[68:69] sc0
.endm
.macro PFA
s_getpc_b64 s[70:71]
s_add_u32 s70, s70, 7f-.
s_addc_u32 s71, s71, 0
s_andn2_b32 s70, s70, 0x7f
s_min_u32 s53, %[pf], 8f-7f-1152
s_andn2_b32 s53, s53, 0x7f
s_add_u32 s70, s70, s53
s_addc_u32 s71, s71, 0
.endm
.macro PFN
s_getpc_b64 s[70:71]
s_add_u32 s70, s70, k2_code@rel32@lo-1020
s_addc_u32 s71, s71, k2_code@rel32@hi-1012
s_add_u32 s70, s70, %[pf]
s_addc_u32 s71, s71, 0
s_andn2_b32 s70, s70, 0x7f
.endm
.macro PFW
s_getpc_b64 s[70:71]
s_add_u32 s70, s70, g_w2q@rel32@lo+4
s_addc_u32 s71, s71, g_w2q@rel32@hi+12
s_add_u32 s70, s70, %[pf]
s_addc_u32 s71, s71, 0
.endm
.macro PF0
s_getpc_b64 s[70:71]
s_andn2_b32 s70, s70, 0x7f
s_min_u32 s53, %[pf], 7f-.-1152
s_andn2_b32 s53, s53, 0x7f
s_add_u32 s70, s70, s53
s_addc_u32 s71, s71, 0
.endm
.macro WL n
s_waitcnt lgkmcnt(\n)
.endm
.macro WV n
s_waitcnt vmcnt(\n)
.endm
.macro LU t,p
v_pk_fma_f16 v[\t], v[\p], v34, s67
.endm
.macro CF d,a,b
v_cvt_pk_f16_f32 v[\d], v[\a], v[\b]
.endm
.macro XS d
v_pk_fma_f16 v[\d], v[\d], v13, v33
.endm
.macro AR d,a
v_accvgpr_read_b32 v[\d], a[\a]
.endm
.macro DQ o
global_load_lds_dword v23, s[40:41] offset:\o
.endm
.macro GE
v_cmp_eq_u32 vcc, 0, v35
s_nop 4
s_cbranch_vccz 9f
v_mov_b32 v35, 14720
s_mov_b64 exec, 1
global_store_dword v35, v10, s[68:69] sc1
s_mov_b64 exec, -1
9:
.endm
.macro BW f
.if \f < 30
ds_write_b128 v12, v[128+4*\f:128+4*\f+3]
.else
ds_write_b128 v12, a[4*\f-120:4*\f-120+3]
.endif
.endm
.macro BR f
.if \f < 30
ds_read_b128 v[128+4*\f:128+4*\f+3], v20
.else
ds_read_b128 a[4*\f-120:4*\f-120+3], v20
.endif
.endm
.macro LK d,a,f,h
.if \f < 30
v_mfma_f32_4x4x4_16b_bf16 v[\d:\d+3], a[\a:\a+1], v[128+4*\f+\h:128+4*\f+\h+1], v[36:39]
.else
v_mfma_f32_4x4x4_16b_bf16 v[\d:\d+3], a[\a:\a+1], a[4*\f-120+\h:4*\f-120+\h+1], v[36:39]
.endif
.endm
.macro PK d,a,b,s
v_perm_b32 v[\d], v[\a], v[\b], s[\s]
.endm
.macro PO d
v_lshl_or_b32 v[\d], v[\d], 4, v[\d-4]
.endm
.macro LG a,b
v_cmp_eq_f32 vcc, v[\a], v[\b]
v_mov_b32 v[\b], 0x7f800000
s_nop 0
v_cndmask_b32 v[\a], v[\b], v[\a], vcc
.endm
.macro DY
s_mov_b32 s72, m0
s_mov_b32 m0, %[pm]
s_nop 0
global_load_lds_dword v3, s[46:47]
s_mov_b32 m0, s72
s_nop 0
.endm
.macro FP
ds_write_b32 v6, v19 offset:61440
.endm
.macro FR
ds_read_b32 v15, v31 offset:61440
.endm
k1_code:
v_mbcnt_lo_u32_b32 v16, -1, 0
v_mbcnt_hi_u32_b32 v16, -1, v16
v_lshlrev_b32 v0, 4, v16
v_and_b32 v17, 15, v16
v_lshrrev_b32 v18, 4, v16
s_add_u32 s68, %[glo], 1239040
s_addc_u32 s69, %[ghi], 0
s_mov_b32 s53, %[r0o]
s_load_dword s67, s[68:69], 16320
s_add_u32 s82, %[hlo], s53
s_addc_u32 s83, %[hhi], 0
s_add_u32 s84, s82, 8192
s_addc_u32 s85, s83, 0
s_and_b32 s90, %[hlo], 0x1fffff
s_add_u32 s90, s90, s53
s_mov_b32 s91, 3072
v_mov_b32 v11, s90
v_mad_u32_u24 v11, v0, s91, v11
v_lshrrev_b32 v11, 21, v11
v_lshl_add_u32 v11, v11, 2, 64
v_writelane_b32 v11, %[tsel], 63
s_sub_u32 s53, s82, %[hlo]
s_lshr_b32 s53, s53, 2
s_and_b32 s90, %[xlo], 0x1fffff
s_add_u32 s53, s53, s90
s_add_u32 s53, s53, 0x20048000
s_lshr_b32 s53, s53, 21
s_lshl_b32 s90, s53, 2
v_writelane_b32 v11, s90, 48
global_load_dword v11, v11, s[68:69] offset:-2048
v_lshlrev_b32 v19, 1, v18
s_movk_i32 s53, 0x78
v_bfe_u32 v21, v16, 2, 2
v_lshrrev_b32 v19, v19, s53
v_mul_u32_u24 v21, 0x300, v21
v_xor_b32 v19, v19, v16
v_and_b32 v19, 3, v19
v_lshl_add_u32 v21, v19, 4, v21
v_mov_b32 v32, 3072
s_mov_b32 s50, %[pos0]
s_mov_b32 s51, %[pos0]
s_mov_b32 s52, %[pos0]
s_add_u32 s74, %[lds], 0
s_add_u32 s75, s74, 163840
s_mov_b32 s37, s74
s_mul_i32 s53, %[w], 6400
s_add_u32 s76, s74, s53
s_add_u32 s77, s76, 163840
s_mov_b32 s38, s76
s_mul_i32 s60, %[w], 1536
s_add_u32 s61, s60, 512
s_add_u32 s62, s60, 16384
s_add_u32 s63, s60, 16896
s_add_u32 s64, s60, 32768
s_add_u32 s65, s60, 33280
v_lshlrev_b32 v23, 2, v16
s_lshl_b32 s53, %[w], 11
s_min_u32 s53, s53, 5120
s_add_u32 s72, s53, 1228800
s_add_u32 s40, %[glo], s72
s_addc_u32 s41, %[ghi], 0
s_add_u32 s53, s53, %[lds]
s_add_u32 m0, s53, 58368
s_nop 0
DW 0
DW 1024
s_nop 0 ;PS
s_mul_i32 s53, s50, 25600
s_add_u32 s40, %[wlo], s53
s_addc_u32 s41, %[whi], 0
)"
R"(s_mov_b32 m0, s38
s_nop 0
DW 0
DW 1024
DW 2048
s_add_u32 s40, s40, 3072
s_addc_u32 s41, s41, 0
s_add_u32 m0, s38, 3072
s_nop 0
DW 0
DW 1024
DW 2048
DQ 3072
s_add_u32 s50, s50, %[d]
s_cmp_eq_u32 s50, %[wrap]
s_cselect_b32 s50, %[rst], s50
s_add_u32 s38, s38, 32768
s_cmp_eq_u32 s38, s77
s_cselect_b32 s38, s76, s38
v_add_u32 v1, s37, v0
v_lshl_add_u32 v19, v18, 2, v17
v_and_b32 v19, 15, v19
v_lshlrev_b32 v19, 4, v19
v_lshl_add_u32 v2, v18, 8, v19
s_lshl_b32 s53, %[w], 2
s_add_u32 s53, s53, %[lds]
s_add_u32 s53, s53, 123904
s_add_u32 s53, %[lds], 123904
v_bfe_u32 v4, v17, 1, 2
v_and_b32 v5, 8, v17
v_add3_u32 v4, v4, v5, %[w]
v_lshl_add_u32 v4, v18, 2, v4
v_and_b32 v4, 15, v4
v_lshl_add_u32 v4, v17, 4, v4
v_lshl_add_u32 v4, v4, 2, s53
s_lshl_b32 s53, %[w], 9
s_add_u32 s53, s53, %[lds]
s_add_u32 s53, s53, 123904
v_lshlrev_b32 v5, 3, v16
v_add_u32 v5, s53, v5
s_lshl_b32 s53, %[w], 6
v_and_b32 v6, 0x38, v16
v_add_u32 v6, s53, v6
s_add_u32 s53, %[lds], 58368
v_and_b32 v19, 3, v16
v_lshl_add_u32 v19, v18, 2, v19
v_lshl_add_u32 v7, v19, 4, s53
v_lshlrev_b32 v8, 4, v18
v_add_u32 v8, s53, v8
v_mov_b32 v10, 1
s_mov_b32 s36, 0xffff
s_mov_b32 s56, 0x5550555
s_mov_b32 s39, 32768
s_mov_b32 s54, 0x01010101
s_mov_b32 s55, 0x01010101
s_mov_b32 %[nb], 0
v_and_b32 v28, 12, v17
v_lshl_add_u32 v28, v28, 2, v17
v_lshlrev_b32 v28, 2, v28
v_mov_b32 v34, 0x74007400
v_mov_b32 v35, -1
v_and_b32 v22, 0xfffffcff, v1
v_lshrrev_b32 v31, 1, v2
s_mov_b32 s97, 0x06020400
s_mov_b32 s56, 0x07030501
v_mov_b32 v33, 0x5550555
s_waitcnt lgkmcnt(0)
s_mov_b32 s78, 0xaaaaaaaa
s_mov_b32 s79, 0xaaaaaaaa
s_mov_b32 s80, 0xcccccccc
s_mov_b32 s81, 0xcccccccc
s_and_b32 s53, %[w], 1
s_lshl_b32 s53, s53, 10
s_lshr_b32 s72, %[w], 1
s_mul_i32 s72, s72, 32768
s_add_u32 s72, s72, s53
s_add_u32 s72, s72, 128000
s_movk_i32 s53, 0x78
v_lshrrev_b32 v12, 5, v0
v_and_b32 v12, 6, v12
v_bfe_u32 v12, s53, v12, 2
v_lshrrev_b32 v20, 8, v0
v_xor_b32 v20, v12, v20
v_lshl_add_u32 v20, v20, 4, s72
v_and_b32 v12, 0xf0, v0
v_lshl_add_u32 v20, v12, 2, v20
v_add_u32 v12, s72, v0
WV 9
v_bcnt_u32_b32 v11, v11, 0
v_cmp_lt_u32 s[90:91], 2, v11
s_nop 0
s_lshr_b32 s96, s91, 31
s_xor_b32 s96, s96, %[s2]
s_sub_u32 s53, 0, s96
s_xor_b32 s90, s90, s53
s_xor_b32 s91, s91, s53
s_add_u32 s53, %[xo0], 0
s_add_u32 s46, %[xlo], s53
s_addc_u32 s47, %[xhi], 0
s_bitcmp1_b64 s[90:91], 48
s_cselect_b32 s58, 16, 0
s_lshr_b32 s58, 0xfe987610, s58
v_lshrrev_b32 v3, 6, v0
v_and_b32 v3, 12, v3
v_bfe_u32 v3, s58, v3, 4
v_mad_u32_u24 v3, v3, v32, v21
XL 12,0
XL 13,64
XL 14,128
XL 15,192
XL 16,256
XL 17,320
XL 18,384
XL 19,448
XL 20,512
XL 21,576
XL 22,640
XL 23,704
s_mul_i32 s53, s96, 8320
s_cmp_lt_u32 %[ca0], 128
s_cselect_b32 s53, s53, 0
s_add_u32 s73, s53, %[ca0]
v_mov_b32 v9, s73
WV 19
s_barrier
v_lshrrev_b32 v19, 8, v31
v_sub_u32 v19, 0, v19
ds_write_b32 v31, v19 offset:61440
s_nop 1
s_nop 0
v_mov_b32 v19, 0
WV 11
BW 12
RV 40,8,6144
RV 44,8,6208
RV 48,8,6272
RV 248,8,6336
BR 12
WV 10
BW 13
s_nop 1
BR 13
WL 2
LZ 40,12
WV 9
BW 14
RV 40,8,6400
s_nop 0
BR 14
s_add_u32 s53, %[xo0], 0
s_add_u32 s46, %[xlo], s53
s_addc_u32 s47, %[xhi], 0
s_bitcmp1_b64 s[90:91], 48
s_cselect_b32 s58, 16, 0
s_lshr_b32 s58, 0xda43cb52, s58
v_lshrrev_b32 v3, 6, v0
v_and_b32 v3, 12, v3
v_bfe_u32 v3, s58, v3, 4
v_mad_u32_u24 v3, v3, v32, v21
WL 3
LS 44,13
WV 8
BW 15
RV 44,8,6464
s_nop 0
BR 15
XL 24,0
WL 3
LS 48,14
WV 8
BW 16
RV 48,8,6528
s_nop 0
BR 16
WL 3
LS 248,15
WV 7
BW 17
RV 248,8,6592
s_nop 0
BR 17
XL 25,64
WL 3
LS 40,16
WV 7
BW 18
RV 40,8,6656
s_nop 0
BR 18
XL 26,128
WL 3
LS 44,17
WV 7
BW 19
RV 44,8,6720
s_nop 0
BR 19
WL 3
LS 48,18
WV 6
BW 20
RV 48,8,6784
s_nop 0
BR 20
XL 27,192
WL 3
LS 248,19
WV 6
BW 21
RV 248,8,6848
s_nop 0
BR 21
XL 28,256
WL 3
LS 40,20
WV 6
BW 22
RA 240,7,0
s_nop 0
BR 22
WL 3
LS 44,21
WV 5
BW 23
RA 244,7,256
s_nop 0
BR 23
XL 29,320
WL 3
LS 48,22
RA 248,7,512
XL 30,384
WL 1
LS 248,23
RA 252,7,768
s_nop 6
v_mul_f32 v36, 0xbb2aaaab, v36
v_mul_f32 v37, 0xbb2aaaab, v37
v_mul_f32 v38, 0xbb2aaaab, v38
v_mul_f32 v39, 0xbb2aaaab, v39
s_nop 1
LK 40,240,12,0
LK 44,242,12,2
RA 240,7,1024
XL 31,448
LK 48,244,13,0
s_nop 0
CF 176,40,41
LK 248,246,13,2
WL 2
RA 244,7,1280
CF 177,42,43
CF 178,44,45
CF 179,46,47
XL 32,512
LK 40,248,14,0
CF 180,48,49
LK 44,250,14,2
RA 248,7,1536
CF 181,50,51
GZ 12
WL 3
XL 33,576
CF 182,248,249
CF 183,250,251
LK 48,252,15,0
CF 184,40,41
LK 248,254,15,2
RA 252,7,1792
CF 185,42,43
XL 34,640
GS 13
WL 3
CF 186,44,45
CF 187,46,47
LK 40,240,16,0
CF 188,48,49
LK 44,242,16,2
WL 2
RA 240,7,2048
CF 189,50,51
XL 35,704
GS 14
s_mul_i32 s53, s50, 25600
s_add_u32 s40, %[wlo], s53
s_addc_u32 s41, %[whi], 0
s_mov_b32 m0, s38
CF 190,248,249
CF 191,250,251
LK 48,244,17,0
CF 192,40,41
LK 248,246,17,2
RA 244,7,2304
CF 193,42,43
GS 15
WL 3
DW 0
CF 194,44,45
CF 195,46,47
LK 40,248,18,0
CF 196,48,49
LK 44,250,18,2
RA 248,7,2560
CF 197,50,51
DW 1024
GS 16
WL 3
CF 198,248,249
CF 199,250,251
LK 48,252,19,0
CF 200,40,41
LK 248,254,19,2
WL 2
RA 252,7,2816
CF 201,42,43
DW 2048
GS 17
s_add_u32 s40, s40, 3072
s_addc_u32 s41, s41, 0
s_add_u32 m0, s38, 3072
CF 202,44,45
CF 203,46,47
LK 40,240,20,0
CF 204,48,49
LK 44,242,20,2
CF 205,50,51
GS 18
WL 2
DW 0
CF 206,248,249
CF 207,250,251
LK 48,244,21,0
CF 208,40,41
LK 248,246,21,2
CF 209,42,43
DW 1024
GS 19
WL 1
CF 210,44,45
CF 211,46,47
LK 40,248,22,0
CF 212,48,49
LK 44,250,22,2
CF 213,50,51
DW 2048
GS 20
WL 0
DQ 3072
CF 214,248,249
CF 215,250,251
LK 48,252,23,0
CF 216,40,41
LK 248,254,23,2
CF 217,42,43
GS 21
s_add_u32 s50, s50, %[d]
s_cmp_eq_u32 s50, %[wrap]
s_cselect_b32 s50, %[rst], s50
CF 218,44,45
CF 219,46,47
RV 36,8,6912
s_nop 0
GS 22
s_add_u32 s38, s38, 32768
s_cmp_eq_u32 s38, s77
s_cselect_b32 s38, s76, s38
CF 220,48,49
CF 221,50,51
CF 222,248,249
CF 223,250,251
s_nop 1
GS 23
s_nop 7
v_cndmask_b32 v52, v52, v53, s[78:79]
v_cndmask_b32 v54, v54, v55, s[78:79]
v_cndmask_b32 v52, v52, v54, s[80:81]
v_mul_f32 v52, 0x3b2aaaab, v52
WL 0
v_med3_f32 v53, v52, v39, v37
LG 52,53
s_nop 0
C 52,52,52
v_lshlrev_b32 v52, 16, v52
v_add_f32 v52, v36, v52
C 52,52,52
v_lshlrev_b32 v52, 16, v52
v_sqrt_f32 v52, v52
s_nop 0
C 52,52,52
v_lshlrev_b32 v52, 16, v52
v_rcp_f32 v52, v52
s_nop 0
v_mul_f32 v52, v38, v52
CF 52,52,52
ds_bpermute_b32 v13, v28, v52
WV 18
BW 24
RV 40,8,6144
RV 44,8,6208
RV 48,8,6272
)"
R"(BR 24
WV 17
BW 25
s_nop 1
BR 25
WL 7
XS 176
WL 2
LZ 40,24
s_and_b32 s53, %[w], 1
s_mul_i32 s53, s53, 3072
s_lshr_b32 s72, %[w], 1
s_lshl_b32 s40, s72, 16
s_add_u32 s40, s40, s53
s_add_u32 m0, s40, 25600
s_lshl_b32 s72, s72, 13
s_add_u32 s53, s53, s72
s_add_u32 s53, s53, 1382400
s_add_u32 s40, %[glo], s53
s_addc_u32 s41, %[ghi], 0
XS 177
WV 16
BW 26
XS 178
RV 40,8,6336
XS 179
BR 26
XS 180
WL 3
LS 44,25
WV 15
DW 0
XS 181
BW 27
XS 182
RV 44,8,6400
XS 183
BR 27
v_min_u32 v35, v35, v13
WL 3
LS 48,26
WV 15
DW 1024
XS 184
BW 28
XS 185
RV 48,8,6464
XS 186
BR 28
XS 187
WL 3
LS 40,27
WV 15
XS 188
BW 29
XS 189
RV 40,8,6528
XS 190
XS 191
BR 29
XS 192
WL 3
LS 44,28
WV 14
DW 2048
XS 193
BW 30
XS 194
RV 44,8,6592
XS 195
BR 30
XS 196
WL 3
LS 48,29
WV 14
DW 3072
XS 197
BW 31
XS 198
RV 48,8,6656
XS 199
BR 31
XS 200
WL 3
LS 40,30
WV 13
XS 201
BW 32
XS 202
RV 40,8,6720
XS 203
BR 32
XS 204
WL 3
LS 44,31
PF0
s_mov_b32 m0, %[pm]
s_nop 0
DF
PFA
XS 205
BW 33
XS 206
XS 207
RV 44,8,6784
XS 208
BR 33
XS 209
WL 3
LS 48,32
s_mul_i32 s53, s50, 25600
s_add_u32 s40, %[wlo], s53
s_addc_u32 s41, %[whi], 0
s_mov_b32 m0, s38
XS 210
WV 13
BW 34
XS 211
RV 48,8,6848
XS 212
RA 240,7,0
XS 213
BR 34
XS 214
WL 4
LS 40,33
WV 12
XS 215
BW 35
XS 216
RA 244,7,256
XS 217
BR 35
XS 218
WL 3
LS 44,34
DW 0
XS 219
RA 248,7,512
XS 220
WL 1
LS 48,35
DW 1024
XS 221
RA 252,7,768
XS 222
XS 223
s_nop 2
v_mul_f32 v36, 0xbb2aaaab, v36
v_mul_f32 v37, 0xbb2aaaab, v37
v_mul_f32 v38, 0xbb2aaaab, v38
v_mul_f32 v39, 0xbb2aaaab, v39
s_nop 1
LK 40,240,24,0
LK 44,242,24,2
RA 240,7,1024
LK 48,244,25,0
s_nop 1
CF 224,40,41
LK 248,246,25,2
WL 2
RA 244,7,1280
CF 225,42,43
CF 226,44,45
CF 227,46,47
DW 2048
LK 40,248,26,0
CF 228,48,49
LK 44,250,26,2
RA 248,7,1536
CF 229,50,51
s_add_u32 s40, s40, 3072
s_addc_u32 s41, s41, 0
s_add_u32 m0, s38, 3072
GZ 24
WL 3
CF 230,248,249
CF 231,250,251
LK 48,252,27,0
CF 232,40,41
LK 248,254,27,2
RA 252,7,1792
CF 233,42,43
DW 0
GS 25
WL 3
DW 1024
CF 234,44,45
CF 235,46,47
LK 40,240,28,0
CF 236,48,49
LK 44,242,28,2
RA 240,7,2048
CF 237,50,51
GS 26
WL 3
DW 2048
CF 238,248,249
CF 239,250,251
LK 48,244,29,0
CF 240,40,41
LK 248,246,29,2
RA 244,7,2304
CF 241,42,43
DQ 3072
GS 27
WL 3
CF 242,44,45
CF 243,46,47
LK 40,248,30,0
CF 244,48,49
LK 44,250,30,2
WL 2
RA 248,7,2560
CF 245,50,51
s_add_u32 s50, s50, %[d]
s_cmp_eq_u32 s50, %[wrap]
s_cselect_b32 s50, %[rst], s50
GS 28
s_add_u32 s38, s38, 32768
s_cmp_eq_u32 s38, s77
s_cselect_b32 s38, s76, s38
CF 246,248,249
CF 247,250,251
LK 48,252,31,0
CF 152,40,41
LK 248,254,31,2
WL 2
RA 252,7,2816
CF 153,42,43
GS 29
s_add_u32 s53, %[xo0], 196608
s_add_u32 s46, %[xlo], s53
s_addc_u32 s47, %[xhi], 0
s_bitcmp1_b64 s[90:91], 48
s_cselect_b32 s58, 16, 0
s_lshr_b32 s58, 0xfe987610, s58
v_lshrrev_b32 v3, 6, v0
v_and_b32 v3, 12, v3
v_bfe_u32 v3, s58, v3, 4
v_mad_u32_u24 v3, v3, v32, v21
CF 154,44,45
CF 155,46,47
LK 40,240,32,0
CF 156,48,49
LK 44,242,32,2
CF 157,50,51
XL 36,0
GS 6
WL 2
CF 158,248,249
CF 159,250,251
LK 48,244,33,0
CF 160,40,41
LK 248,246,33,2
CF 161,42,43
XL 37,64
GS 7
WL 1
XL 38,128
CF 162,44,45
CF 163,46,47
LK 40,248,34,0
CF 164,48,49
LK 44,250,34,2
CF 165,50,51
GS 8
WL 0
XL 39,192
CF 166,248,249
CF 167,250,251
LK 48,252,35,0
CF 168,40,41
LK 248,254,35,2
CF 169,42,43
XL 40,256
GS 9
CF 170,44,45
CF 171,46,47
RV 36,8,6912
s_nop 0
GS 10
XL 41,320
CF 172,48,49
CF 173,50,51
CF 174,248,249
CF 175,250,251
s_nop 1
GS 11
XL 42,384
WV 15
s_barrier
RA 216,1,0
RA 220,1,1024
RA 224,1,2048
RA 228,1,3072
RV 252,22,5120
v_cndmask_b32 v52, v52, v53, s[78:79]
v_cndmask_b32 v54, v54, v55, s[78:79]
v_cndmask_b32 v52, v52, v54, s[80:81]
v_mul_f32 v52, 0x3b2aaaab, v52
WL 5
v_med3_f32 v53, v52, v39, v37
LG 52,53
s_nop 0
C 52,52,52
v_lshlrev_b32 v52, 16, v52
v_add_f32 v52, v36, v52
C 52,52,52
XL 43,448
v_lshlrev_b32 v52, 16, v52
v_sqrt_f32 v52, v52
s_nop 0
C 52,52,52
v_lshlrev_b32 v52, 16, v52
v_rcp_f32 v52, v52
XL 44,512
v_mul_f32 v52, v38, v52
CF 52,52,52
ds_bpermute_b32 v13, v28, v52
WL 0
XS 224
XS 225
XS 226
XS 227
XS 228
XS 229
XS 230
XL 45,576
XS 231
v_min_u32 v35, v35, v13
XS 232
XS 233
XS 234
XL 46,640
XS 235
XS 236
XS 237
XS 238
XS 239
XL 47,704
Z 0,12,0
XS 240
XS 241
XS 242
XS 243
XS 244
XS 245
XS 246
XS 247
XS 152
XS 153
XS 154
XS 155
M 0,13,1
s_mul_i32 s53, s50, 25600
RA 232,1,4096
AW 0,152
AW 1,153
Z 1,24,0
s_add_u32 s40, %[wlo], s53
AW 2,154
AW 3,155
M 1,25,1
s_addc_u32 s41, %[whi], 0
RA 236,1,6144
XS 156
XS 157
M 1,26,2
s_mov_b32 m0, s38
s_nop 0
DW 0
XS 158
XS 159
AW 4,156
M 1,27,3
RA 216,1,7168
AW 5,157
AW 6,158
M 0,14,2
DW 1024
AW 7,159
XS 160
M 0,15,3
WL 2
RA 220,1,8192
XS 161
XS 162
M 0,16,4
WL 2
XS 163
AW 8,160
M 0,17,5
RA 224,1,9216
AW 9,161
AW 10,162
AW 11,163
M 1,28,4
WL 1
DW 2048
)"
R"(XS 164
XS 165
M 1,29,5
s_add_u32 s40, s40, 3072
RA 228,1,10240
XS 166
XS 167
M 1,30,0
s_addc_u32 s41, s41, 0
AW 12,164
AW 13,165
M 1,31,1
s_add_u32 m0, s38, 3072
RA 232,1,11264
AW 14,166
AW 15,167
M 0,18,0
RV 252,22,5392
XS 168
XS 169
M 0,19,1
WL 3
RA 236,1,12288
XS 170
XS 171
AW 16,168
M 0,20,2
WL 3
DW 0
AW 17,169
AW 18,170
M 0,21,3
RA 216,1,13312
AW 19,171
XS 172
M 1,32,2
DW 1024
XS 173
XS 174
M 1,33,3
WL 3
RA 220,1,14336
XS 175
AW 20,172
M 0,22,4
WL 2
DW 2048
AW 21,173
AW 22,174
AW 23,175
M 0,23,5
RA 224,1,15360
M 1,34,4
DQ 3072
M 1,35,5
WL 1
RA 228,1,16384
Z 6,12,0
s_add_u32 s50, s50, %[d]
LL 16,56
LL 104,57
M 6,13,1
s_cmp_eq_u32 s50, %[wrap]
LA 16
RA 232,1,17408
Z 7,24,0
s_cselect_b32 s50, %[rst], s50
LC_ 104
M 7,25,1
s_add_u32 s38, s38, 32768
RA 236,1,18432
WL 5
M 7,26,2
s_cmp_eq_u32 s38, s77
WV 18
BW 36
s_cselect_b32 s38, s76, s38
WL 5
M 7,27,3
s_add_u32 s37, s37, 32768
RA 216,1,19456
RV 40,8,6144
M 6,14,2
s_cmp_eq_u32 s37, s75
LL 17,58
LL 105,59
M 6,15,3
s_cselect_b32 s37, s74, s37
LA 17
RA 220,1,20480
s_add_u32 s53, %[xo0], 196608
s_add_u32 s46, %[xlo], s53
s_addc_u32 s47, %[xhi], 0
s_bitcmp1_b64 s[90:91], 48
s_cselect_b32 s58, 16, 0
s_lshr_b32 s58, 0xda43cb52, s58
v_lshrrev_b32 v3, 6, v0
v_and_b32 v3, 12, v3
v_bfe_u32 v3, s58, v3, 4
RV 44,8,6208
RV 48,8,6272
v_mad_u32_u24 v3, v3, v32, v21
WL 9
M 6,16,4
WL 7
LC_ 105
XL 48,0
BR 36
M 6,17,5
s_mul_i32 s53, s51, 24
RA 224,1,21504
WV 18
BW 37
s_nop 1
BR 37
M 7,28,4
WV 17
LO 104,16,104
LU 16,104
M 7,29,5
s_bitcmp1_b32 s51, 0
RA 228,1,22528
HZ 24,16
WL 4
LZ 40,36
s_addc_u32 s53, s53, 0
BW 38
RV 40,8,6336
M 7,30,0
s_lshl_b32 s53, s53, 11
LL 18,60
LL 108,61
s_bitcmp1_b64 s[90:91], s51
s_cselect_b64 s[44:45], s[84:85], s[82:83]
s_add_u32 s44, s44, s53
s_addc_u32 s45, s45, 0
s_add_u32 s51, s51, %[d]
s_cmp_eq_u32 s51, %[wrap]
s_cselect_b32 s51, %[rst], s51
XL 49,64
BR 38
M 7,31,1
WL 4
LA 18
RA 232,1,23552
LS 44,37
WV 17
BW 39
M 6,18,0
LC_ 108
XL 50,128
RV 44,8,6400
M 6,19,1
RA 236,1,24576
BR 39
WL 7
LS 48,38
WV 17
BW 40
M 6,20,2
LO 105,17,105
LU 17,105
M 6,21,3
XL 51,192
RV 48,8,6464
BR 40
HS 24,17
M 7,32,2
LL 16,62
LL 109,63
v_add_u32 v1, s37, v0
M 7,33,3
WL 3
LA 16
RA 216,1,0
XL 52,256
LS 40,39
WV 18
BW 41
RV 40,8,6528
M 6,22,4
WL 4
LC_ 109
XL 53,320
v_and_b32 v22, 0xfffffcff, v1
BR 41
M 6,23,5
s_barrier
RA 220,1,1024
LS 44,40
WV 18
BW 42
M 7,34,4
RV 44,8,6592
LO 108,18,108
LU 18,108
M 7,35,5
RV 252,22,5120
BR 42
HS 24,18
WL 1
Z 0,12,0
WV 17
RA 224,1,2048
LL 17,80
LL 106,81
M 0,13,1
s_mul_i32 s53, s50, 25600
LA 17
RA 228,1,3072
LS 48,41
s_add_u32 s40, %[wlo], s53
BW 43
RV 48,8,6656
s_nop 0
BR 43
Z 1,24,0
s_addc_u32 s41, %[whi], 0
LC_ 106
RA 232,1,4096
M 1,25,1
s_mov_b32 m0, s38
RA 236,1,6144
WL 9
LS 40,42
WV 16
BW 44
RV 40,8,6720
WL 10
M 1,26,2
WL 8
BR 44
LO 109,16,109
LU 16,109
M 1,27,3
WL 6
RA 216,1,7168
HS 24,16
LS 44,43
WV 15
BW 45
M 0,14,2
DW 0
RV 44,8,6784
LL 18,82
LL 107,83
M 0,15,3
WL 3
LA 18
RA 220,1,8192
BR 45
LS 48,44
WV 15
BW 46
M 0,16,4
LC_ 107
DW 1024
M 0,17,5
RA 224,1,9216
RV 48,8,6848
RA 240,7,0
HX 24
M 1,28,4
LO 17,17,106
LU 106,17
W1 24,0
BR 46
M 1,29,5
WL 7
RA 228,1,10240
DW 2048
HZ 25,106
LS 40,45
WV 16
BW 47
M 1,30,0
s_add_u32 s40, s40, 3072
RA 244,7,256
PK 106,17,104,56
PK 104,17,104,97
LL 16,84
LL 110,85
M 1,31,1
s_addc_u32 s41, s41, 0
LA 16
RA 232,1,11264
BR 47
WL 6
LS 44,46
s_add_u32 m0, s38, 3072
RA 248,7,512
M 0,18,0
LC_ 110
RV 252,22,5392
M 0,19,1
RA 236,1,12288
WL 4
LS 48,47
RA 252,7,768
M 0,20,2
LO 18,18,107
LU 107,18
s_nop 3
v_mul_f32 v36, 0xbb2aaaab, v36
v_mul_f32 v37, 0xbb2aaaab, v37
v_mul_f32 v38, 0xbb2aaaab, v38
M 0,21,3
RA 216,1,13312
DW 0
HS 25,107
v_mul_f32 v39, 0xbb2aaaab, v39
s_nop 1
LK 40,240,36,0
LK 44,242,36,2
RA 240,7,1024
LK 48,244,37,0
s_nop 1
CF 128,40,41
LK 248,246,37,2
RA 244,7,1280
CF 129,42,43
M 1,32,2
PK 107,18,105,56
PK 105,18,105,97
LL 17,86
LL 111,87
M 1,33,3
WL 7
LA 17
RA 220,1,14336
DW 1024
CF 130,44,45
CF 131,46,47
LK 40,248,38,0
WL 5
CF 132,48,49
LK 44,250,38,2
s_add_u32 s42, s44, s60
RA 248,7,1536
CF 133,50,51
GZ 0
s_addc_u32 s43, s45, 0
CF 134,248,249
CF 135,250,251
LK 48,252,39,0
CF 136,40,41
LK 248,254,39,2
RA 252,7,1792
CF 137,42,43
GS 1
WL 5
CF 138,44,45
CF 139,46,47
LK 40,240,40,0
CF 140,48,49
LK 44,242,40,2
RA 240,7,2048
CF 141,50,51
GS 2
WL 5
CF 142,248,249
CF 143,250,251
LK 48,244,41,0
CF 144,40,41
LK 248,246,41,2
RA 244,7,2304
CF 145,42,43
GS 3
CF 146,44,45
CF 147,46,47
M 0,22,4
LC_ 111
ST 104
M 0,23,5
WL 4
RA 224,1,15360
LK 40,248,42,0
CF 148,48,49
LK 44,250,42,2
RA 248,7,2560
CF 149,50,51
GS 4
CF 150,248,249
CF 151,250,251
M 1,34,4
DW 2048
LO 16,16,110
LU 110,16
M 1,35,5
WL 5
RA 228,1,16384
HS 25,110
LK 48,252,43,0
CF 152,40,41
LK 248,254,43,2
RA 252,7,2816
CF 153,42,43
GS 5
CF 154,44,45
CF 155,46,47
)"
R"(Z 6,12,0
PK 110,16,108,56
PK 108,16,108,97
LL 18,56
LL 104,57
M 6,13,1
WL 6
LA 18
DQ 3072
PO 110
LK 40,240,44,0
CF 156,48,49
LK 44,242,44,2
s_add_u32 s50, s50, %[d]
CF 157,50,51
GS 6
s_cmp_eq_u32 s50, %[wrap]
CF 158,248,249
CF 159,250,251
Z 7,24,0
s_cselect_b32 s50, %[rst], s50
LC_ 104
RA 232,1,17408
M 7,25,1
WL 8
RA 236,1,18432
LK 48,244,45,0
CF 160,40,41
LK 248,246,45,2
CF 161,42,43
GS 7
WL 7
CF 162,44,45
CF 163,46,47
M 7,26,2
WL 5
LO 17,17,111
LU 111,17
M 7,27,3
s_add_u32 s38, s38, 32768
RA 216,1,19456
HS 25,111
LK 40,248,46,0
WL 5
CF 164,48,49
LK 44,250,46,2
s_cmp_eq_u32 s38, s77
CF 165,50,51
GS 8
s_cselect_b32 s38, s76, s38
CF 166,248,249
CF 167,250,251
M 6,14,2
s_add_u32 s37, s37, 32768
PK 111,17,109,56
PK 109,17,109,97
LL 16,58
LL 105,59
M 6,15,3
s_cmp_eq_u32 s37, s75
LA 16
PO 111
s_cselect_b32 s37, s74, s37
LK 48,252,47,0
CF 168,40,41
LK 248,254,47,2
CF 169,42,43
HX 25
GS 9
WL 3
CF 170,44,45
CF 171,46,47
M 6,16,4
WL 2
LC_ 105
RA 220,1,20480
XL 54,384
M 6,17,5
s_add_u32 s42, s44, s61
RA 224,1,21504
RV 36,8,6912
s_addc_u32 s43, s45, 0
GS 10
CF 172,48,49
CF 173,50,51
M 7,28,4
LO 104,18,104
LU 18,104
W1 25,1024
M 7,29,5
WL 3
RA 228,1,22528
HZ 24,18
ST 108
XL 55,448
M 7,30,0
s_mul_i32 s53, s51, 24
LL 17,60
LL 108,61
s_bitcmp1_b32 s51, 0
s_addc_u32 s53, s53, 0
s_lshl_b32 s53, s53, 11
s_bitcmp1_b64 s[90:91], s51
s_cselect_b64 s[44:45], s[84:85], s[82:83]
s_add_u32 s44, s44, s53
s_addc_u32 s45, s45, 0
s_add_u32 s51, s51, %[d]
s_cmp_eq_u32 s51, %[wrap]
s_cselect_b32 s51, %[rst], s51
M 7,31,1
LA 17
RA 232,1,23552
M 6,18,0
LC_ 108
XL 56,512
v_add_u32 v4, s39, v4
CF 174,248,249
CF 175,250,251
M 6,19,1
RA 236,1,24576
s_nop 1
v_add_u32 v1, s37, v0
GS 11
WL 7
XL 57,576
s_nop 5
v_cndmask_b32 v52, v52, v53, s[78:79]
v_cndmask_b32 v54, v54, v55, s[78:79]
M 6,20,2
WL 4
LO 105,16,105
LU 16,105
v_cndmask_b32 v52, v52, v54, s[80:81]
v_mul_f32 v52, 0x3b2aaaab, v52
M 6,21,3
RA 216,1,0
HS 24,16
v_med3_f32 v53, v52, v39, v37
M 7,32,2
LL 18,62
LL 109,63
LG 52,53
s_nop 0
C 52,52,52
M 7,33,3
WL 1
LA 18
XL 58,640
v_lshlrev_b32 v52, 16, v52
v_add_f32 v52, v36, v52
M 6,22,4
s_lshl_b32 s53, s52, 8
LC_ 109
v_and_b32 v22, 0xfffffcff, v1
C 52,52,52
v_lshlrev_b32 v52, 16, v52
M 6,23,5
s_add_u32 s48, %[alo], s53
RA 220,1,1024
v_sqrt_f32 v52, v52
s_nop 0
C 52,52,52
M 7,34,4
WV 20
LO 108,17,108
LU 17,108
s_addc_u32 s49, %[ahi], 0
s_add_u32 s52, s52, %[d]
s_cmp_eq_u32 s52, %[wrap]
s_cselect_b32 s52, %[rst], s52
XL 59,704
v_lshlrev_b32 v52, 16, v52
v_rcp_f32 v52, v52
M 7,35,5
s_barrier
RV 252,22,5120
HS 24,17
v_mul_f32 v52, v38, v52
CF 52,52,52
WL 0
Z 0,12,0
s_sub_u32 s39, 0, s39
RA 224,1,2048
LL 16,80
LL 106,81
ds_bpermute_b32 v13, v28, v52
M 0,13,1
s_mul_i32 s53, s50, 25600
LA 16
RA 228,1,3072
Z 1,24,0
s_add_u32 s40, %[wlo], s53
LC_ 106
RA 232,1,4096
M 1,25,1
s_addc_u32 s41, %[whi], 0
RA 236,1,6144
ds_read_b64 v[14:15], v5 offset:0
WL 7
M 1,26,2
LO 109,18,109
LU 18,109
s_mov_b32 m0, s38
WL 4
M 1,27,3
RA 216,1,7168
HS 24,18
M 0,14,2
DW 0
LL 17,82
LL 107,83
M 0,15,3
WL 3
LA 17
RA 220,1,8192
v_subrev_u32 v5, s39, v5
M 0,16,4
WL 4
LC_ 107
DW 1024
M 0,17,5
WL 4
RA 224,1,9216
v_add_f32 v14, v14, v15
s_nop 1
v_add_f32_dpp v14, v14, v14 quad_perm:[1,0,3,2] row_mask:0xf bank_mask:0xf
s_nop 1
v_add_f32_dpp v14, v14, v14 quad_perm:[2,3,0,1] row_mask:0xf bank_mask:0xf
s_nop 1
v_add_f32_dpp v14, v14, v14 row_half_mirror row_mask:0xf bank_mask:0xf
HX 24
XS 128
M 1,28,4
WV 21
LO 16,16,106
LU 106,16
v_cvt_f64_f32 v[14:15], v14
W1 24,0
XS 129
BW 48
XS 130
M 1,29,5
WL 4
RA 228,1,10240
HZ 25,106
s_mov_b64 exec, s[54:55]
SP
s_mov_b64 exec, -1
DW 2048
RV 40,8,6144
RV 44,8,6208
XS 131
AW 24,128
AW 25,129
M 1,30,0
s_add_u32 s40, s40, 3072
RV 48,8,6272
PK 106,16,104,56
PK 104,16,104,97
LL 18,84
LL 110,85
AW 26,130
AW 27,131
M 1,31,1
s_addc_u32 s41, s41, 0
LA 18
RA 232,1,11264
s_add_u32 m0, s38, 3072
BR 48
XS 132
XS 133
XS 134
BW 49
M 0,18,0
LC_ 110
RV 252,22,5392
BR 49
XS 135
AW 28,132
M 0,19,1
WL 10
RA 236,1,12288
AW 29,133
AW 30,134
AW 31,135
WL 5
LZ 40,48
WV 22
BW 50
M 0,20,2
DW 0
RV 40,8,6336
LO 17,17,107
LU 107,17
v_min_u32 v35, v35, v13
XS 136
XS 137
M 0,21,3
WL 3
RA 216,1,13312
BR 50
HS 25,107
XS 138
XS 139
LS 44,49
WV 22
BW 51
M 1,32,2
WL 5
DW 1024
PK 107,17,105,56
PK 105,17,105,97
LL 16,86
LL 111,87
AW 32,136
AW 33,137
AW 34,138
M 1,33,3
s_add_u32 s42, s44, s60
LA 16
RA 220,1,14336
RV 44,8,6400
BR 51
AW 35,139
XS 140
M 0,22,4
s_addc_u32 s43, s45, 0
LC_ 111
DW 2048
XS 141
XS 142
XS 143
M 0,23,5
WL 6
RA 224,1,15360
AW 36,140
AW 37,141
AW 38,142
LS 48,50
WV 23
BW 52
RV 48,8,6464
M 1,34,4
LO 18,18,110
LU 110,18
ST 104
BR 52
AW 39,143
XS 144
M 1,35,5
WL 5
RA 228,1,16384
DQ 3072
HS 25,110
XS 145
XS 146
XS 147
LS 40,51
WV 15
BW 53
Z 6,12,0
s_add_u32 s50, s50, %[d]
RV 40,8,6528
PK 110,18,108,56
PK 108,18,108,97
LL 17,56
LL 104,57
AW 40,144
AW 41,145
M 6,13,1
s_cmp_eq_u32 s50, %[wrap]
LA 17
BR 53
PO 110
AW 42,146
AW 43,147
XS 148
WL 5
LS 44,52
s_cselect_b32 s50, %[rst], s50
BW 54
Z 7,24,0
s_add_u32 s38, s38, 32768
LC_ 104
RA 232,1,17408
XS 149
XS 150
XS 151
M 7,25,1
s_cmp_eq_u32 s38, s77
RA 236,1,18432
RV 44,8,6592
BR 54
AW 44,148
AW 45,149
M 7,26,2
WL 6
LO 16,16,111
LU 111,16
s_cselect_b32 s38, s76, s38
)"
R"(AW 46,150
AW 47,151
XS 152
M 7,27,3
s_add_u32 s37, s37, 32768
RA 216,1,19456
HS 25,111
XS 153
XS 154
LS 48,53
s_cmp_eq_u32 s37, s75
WV 13
BW 55
RV 48,8,6656
M 6,14,2
s_cselect_b32 s37, s74, s37
BR 55
PK 111,16,109,56
PK 109,16,109,97
LL 18,58
LL 105,59
XS 155
AW 48,152
AW 49,153
M 6,15,3
s_add_u32 s53, %[xo0], 393216
LA 18
PO 111
s_add_u32 s46, %[xlo], s53
s_addc_u32 s47, %[xhi], 0
s_bitcmp1_b64 s[90:91], 48
s_cselect_b32 s58, 16, 0
s_lshr_b32 s58, 0xfe987610, s58
v_lshrrev_b32 v3, 6, v0
v_and_b32 v3, 12, v3
v_bfe_u32 v3, s58, v3, 4
v_mad_u32_u24 v3, v3, v32, v21
AW 50,154
AW 51,155
WL 5
LS 40,54
s_add_u32 s42, s44, s61
WV 12
BW 56
M 6,16,4
s_addc_u32 s43, s45, 0
LC_ 105
RA 220,1,20480
RV 40,8,6720
XS 156
XS 157
XS 158
M 6,17,5
WL 5
RA 224,1,21504
BR 56
XL 60,0
HX 25
XS 159
AW 52,156
AW 53,157
LS 44,55
WV 12
BW 57
M 7,28,4
LO 104,17,104
LU 17,104
W1 25,1024
AW 54,158
AW 55,159
M 7,29,5
WL 5
RA 228,1,22528
HZ 24,17
ST 108
RV 44,8,6784
BR 57
XS 160
XS 161
XS 162
M 7,30,0
s_mul_i32 s53, s51, 24
LL 16,60
LL 108,61
s_bitcmp1_b32 s51, 0
s_addc_u32 s53, s53, 0
s_lshl_b32 s53, s53, 11
s_bitcmp1_b64 s[90:91], s51
s_cselect_b64 s[44:45], s[84:85], s[82:83]
s_add_u32 s44, s44, s53
s_addc_u32 s45, s45, 0
s_add_u32 s51, s51, %[d]
s_cmp_eq_u32 s51, %[wrap]
s_cselect_b32 s51, %[rst], s51
XL 61,64
XS 163
AW 56,160
M 7,31,1
WL 5
LA 16
RA 232,1,23552
AW 57,161
AW 58,162
AW 59,163
LS 48,56
WV 13
BW 58
RV 48,8,6848
M 6,18,0
LC_ 108
XL 62,128
v_add_u32 v4, s39, v4
RA 240,7,0
XS 164
XS 165
XS 166
M 6,19,1
WL 6
RA 236,1,24576
BR 58
XS 167
AW 60,164
LS 40,57
WV 13
BW 59
M 6,20,2
LO 105,18,105
LU 18,105
AW 61,165
AW 62,166
AW 63,167
M 6,21,3
XL 63,192
RA 244,7,256
BR 59
HS 24,18
XS 168
XS 169
M 7,32,2
LL 17,62
LL 109,63
v_add_u32 v1, s37, v0
XS 170
XS 171
AW 64,168
M 7,33,3
WL 0
LA 17
RA 216,1,0
XL 64,256
AW 65,169
AW 66,170
AW 67,171
LS 44,58
s_lshl_b32 s53, s52, 8
RA 248,7,512
XS 172
LS 48,59
s_add_u32 s48, %[alo], s53
RA 252,7,768
XS 173
M 6,22,4
s_addc_u32 s49, %[ahi], 0
LC_ 109
v_and_b32 v22, 0xfffffcff, v1
M 6,23,5
s_add_u32 s52, s52, %[d]
RA 220,1,1024
XS 174
XS 175
AW 68,172
M 7,34,4
LO 108,16,108
LU 16,108
s_cmp_eq_u32 s52, %[wrap]
s_cselect_b32 s52, %[rst], s52
XL 65,320
AW 69,173
AW 70,174
M 7,35,5
s_barrier
RV 252,22,5120
HS 24,16
AW 71,175
v_mul_f32 v36, 0xbb2aaaab, v36
v_mul_f32 v37, 0xbb2aaaab, v37
v_mul_f32 v38, 0xbb2aaaab, v38
v_mul_f32 v39, 0xbb2aaaab, v39
WL 0
Z 0,12,0
s_sub_u32 s39, 0, s39
RA 224,1,2048
LL 18,80
LL 106,81
M 0,13,1
s_mul_i32 s53, s50, 25600
LA 18
RA 228,1,3072
LK 40,240,48,0
LK 44,242,48,2
s_add_u32 s40, %[wlo], s53
RA 240,7,1024
LK 48,244,49,0
s_nop 0
CF 128,40,41
LK 248,246,49,2
s_addc_u32 s41, %[whi], 0
RA 244,7,1280
CF 129,42,43
CF 130,44,45
CF 131,46,47
LK 40,248,50,0
CF 132,48,49
LK 44,250,50,2
s_mov_b32 m0, s38
RA 248,7,1536
CF 133,50,51
GZ 0
CF 134,248,249
CF 135,250,251
LK 48,252,51,0
CF 136,40,41
LK 248,254,51,2
RA 252,7,1792
CF 137,42,43
GS 1
CF 138,44,45
CF 139,46,47
Z 1,24,0
LC_ 106
RA 232,1,4096
M 1,25,1
WL 8
RA 236,1,6144
ds_read_b64 v[14:15], v5 offset:0
M 1,26,2
WL 8
LO 109,17,109
LU 17,109
M 1,27,3
WL 7
RA 216,1,7168
HS 24,17
LK 40,240,52,0
CF 140,48,49
LK 44,242,52,2
RA 240,7,2048
CF 141,50,51
GS 2
WL 8
CF 142,248,249
CF 143,250,251
LK 48,244,53,0
CF 144,40,41
LK 248,246,53,2
RA 244,7,2304
CF 145,42,43
GS 3
WL 8
CF 146,44,45
CF 147,46,47
LK 40,248,54,0
CF 148,48,49
LK 44,250,54,2
RA 248,7,2560
CF 149,50,51
GS 4
WL 8
CF 150,248,249
CF 151,250,251
LK 48,252,55,0
CF 152,40,41
LK 248,254,55,2
RA 252,7,2816
CF 153,42,43
GS 5
CF 154,44,45
CF 155,46,47
M 0,14,2
DW 0
LL 16,82
LL 107,83
M 0,15,3
WL 3
LA 16
RA 220,1,8192
v_subrev_u32 v5, s39, v5
LK 40,240,56,0
CF 156,48,49
LK 44,242,56,2
CF 157,50,51
HX 24
GS 6
CF 158,248,249
CF 159,250,251
M 0,16,4
LC_ 107
DW 1024
M 0,17,5
WL 5
RA 224,1,9216
v_add_f32 v14, v14, v15
s_nop 1
v_add_f32_dpp v14, v14, v14 quad_perm:[1,0,3,2] row_mask:0xf bank_mask:0xf
s_nop 1
v_add_f32_dpp v14, v14, v14 quad_perm:[2,3,0,1] row_mask:0xf bank_mask:0xf
s_nop 1
v_add_f32_dpp v14, v14, v14 row_half_mirror row_mask:0xf bank_mask:0xf
LK 48,244,57,0
CF 160,40,41
LK 248,246,57,2
CF 161,42,43
v_cvt_f64_f32 v[14:15], v14
GS 7
CF 162,44,45
CF 163,46,47
M 1,28,4
LO 18,18,106
LU 106,18
W1 24,0
M 1,29,5
WL 6
RA 228,1,10240
HZ 25,106
s_mov_b64 exec, s[54:55]
SP
s_mov_b64 exec, -1
DW 2048
LK 40,248,58,0
CF 164,48,49
LK 44,250,58,2
CF 165,50,51
GS 8
WL 4
CF 166,248,249
CF 167,250,251
M 1,30,0
s_add_u32 s40, s40, 3072
PK 106,18,104,56
PK 104,18,104,97
LL 17,84
LL 110,85
M 1,31,1
s_addc_u32 s41, s41, 0
LA 17
RA 232,1,11264
LK 48,252,59,0
CF 168,40,41
LK 248,254,59,2
s_add_u32 m0, s38, 3072
CF 169,42,43
GS 9
CF 170,44,45
CF 171,46,47
M 0,18,0
LC_ 110
RV 252,22,5392
M 0,19,1
RA 236,1,12288
RV 36,8,6912
GS 10
WL 8
CF 172,48,49
CF 173,50,51
M 0,20,2
WL 6
LO 16,16,107
LU 107,16
M 0,21,3
RA 216,1,13312
DW 0
HS 25,107
M 1,32,2
PK 107,16,105,56
PK 105,16,105,97
LL 18,86
LL 111,87
M 1,33,3
WL 2
LA 18
RA 220,1,14336
DW 1024
M 0,22,4
s_add_u32 s42, s44, s60
LC_ 111
s_addc_u32 s43, s45, 0
M 0,23,5
RA 224,1,15360
CF 174,248,249
CF 175,250,251
s_nop 1
GS 11
ST 104
M 1,34,4
DW 2048
LO 17,17,110
LU 110,17
s_nop 2
v_cndmask_b32 v52, v52, v53, s[78:79]
v_cndmask_b32 v54, v54, v55, s[78:79]
)"
R"(M 1,35,5
WL 4
RA 228,1,16384
HS 25,110
v_cndmask_b32 v52, v52, v54, s[80:81]
Z 6,12,0
WL 3
PK 110,17,108,56
PK 108,17,108,97
LL 16,56
LL 104,57
v_mul_f32 v52, 0x3b2aaaab, v52
v_med3_f32 v53, v52, v39, v37
M 6,13,1
WL 1
LA 16
DQ 3072
PO 110
LG 52,53
s_nop 0
C 52,52,52
Z 7,24,0
s_add_u32 s50, s50, %[d]
LC_ 104
RA 232,1,17408
v_lshlrev_b32 v52, 16, v52
M 7,25,1
s_cmp_eq_u32 s50, %[wrap]
RA 236,1,18432
s_cselect_b32 s50, %[rst], s50
v_add_f32 v52, v36, v52
C 52,52,52
M 7,26,2
WL 4
LO 18,18,111
LU 111,18
v_lshlrev_b32 v52, 16, v52
M 7,27,3
s_add_u32 s38, s38, 32768
RA 216,1,19456
HS 25,111
v_sqrt_f32 v52, v52
s_nop 0
C 52,52,52
M 6,14,2
s_cmp_eq_u32 s38, s77
PK 111,18,109,56
PK 109,18,109,97
LL 17,58
LL 105,59
s_cselect_b32 s38, s76, s38
v_lshlrev_b32 v52, 16, v52
M 6,15,3
s_add_u32 s37, s37, 32768
LA 17
PO 111
s_cmp_eq_u32 s37, s75
s_cselect_b32 s37, s74, s37
v_rcp_f32 v52, v52
s_nop 0
v_mul_f32 v52, v38, v52
WL 3
M 6,16,4
WL 2
LC_ 105
RA 220,1,20480
XL 66,384
CF 52,52,52
ds_bpermute_b32 v13, v28, v52
M 6,17,5
s_add_u32 s42, s44, s61
RA 224,1,21504
HX 25
s_addc_u32 s43, s45, 0
M 7,28,4
LO 104,16,104
LU 16,104
W1 25,1024
M 7,29,5
WL 3
RA 228,1,22528
HZ 24,16
ST 108
XL 67,448
M 7,30,0
s_mul_i32 s53, s51, 24
LL 18,60
LL 108,61
s_bitcmp1_b32 s51, 0
s_addc_u32 s53, s53, 0
s_lshl_b32 s53, s53, 11
s_bitcmp1_b64 s[90:91], s51
s_cselect_b64 s[44:45], s[84:85], s[82:83]
s_add_u32 s44, s44, s53
s_addc_u32 s45, s45, 0
s_add_u32 s51, s51, %[d]
s_cmp_eq_u32 s51, %[wrap]
s_cselect_b32 s51, %[rst], s51
M 7,31,1
LA 18
RA 232,1,23552
M 6,18,0
LC_ 108
XL 68,512
v_add_u32 v4, s39, v4
M 6,19,1
WL 5
RA 236,1,24576
s_nop 1
v_add_u32 v1, s37, v0
M 6,20,2
WL 4
XL 69,576
LO 105,17,105
LU 17,105
M 6,21,3
RA 216,1,0
HS 24,17
M 7,32,2
LL 16,62
LL 109,63
XS 128
M 7,33,3
WL 1
LA 16
XL 70,640
XS 129
XS 130
M 6,22,4
s_lshl_b32 s53, s52, 8
LC_ 109
v_and_b32 v22, 0xfffffcff, v1
XS 131
AW 72,128
M 6,23,5
s_add_u32 s48, %[alo], s53
RA 220,1,1024
AW 73,129
AW 74,130
M 7,34,4
WV 22
LO 108,18,108
LU 18,108
s_addc_u32 s49, %[ahi], 0
s_add_u32 s52, s52, %[d]
s_cmp_eq_u32 s52, %[wrap]
s_cselect_b32 s52, %[rst], s52
XL 71,704
AW 75,131
XS 132
M 7,35,5
s_barrier
RV 252,22,5120
HS 24,18
XS 133
XS 134
XS 135
WL 0
Z 0,12,0
s_sub_u32 s39, 0, s39
LL 17,80
LL 106,81
AW 76,132
AW 77,133
AW 78,134
M 0,13,1
s_mul_i32 s53, s50, 25600
LA 17
RA 224,1,2048
AW 79,135
v_min_u32 v35, v35, v13
XS 136
Z 1,24,0
s_add_u32 s40, %[wlo], s53
LC_ 106
RA 228,1,3072
XS 137
XS 138
XS 139
M 1,25,1
s_addc_u32 s41, %[whi], 0
RA 232,1,4096
AW 80,136
AW 81,137
AW 82,138
Z 2,36,0
LO 109,16,109
LU 16,109
AW 83,139
XS 140
XS 141
M 2,37,1
s_mov_b32 m0, s38
ds_read_b64 v[14:15], v5 offset:0
HS 24,16
XS 142
XS 143
AW 84,140
Z 3,48,0
WL 2
LL 18,82
LL 107,83
AW 85,141
AW 86,142
AW 87,143
M 3,49,1
LA 18
RA 236,1,6144
M 3,50,2
LC_ 107
DW 0
M 3,51,3
v_subrev_u32 v5, s39, v5
HX 24
M 2,38,2
LO 17,17,106
LU 106,17
M 2,39,3
RA 216,1,7168
HZ 25,106
XS 144
M 1,26,2
PK 106,17,104,56
PK 104,17,104,97
LL 16,84
LL 110,85
XS 145
M 1,27,3
LA 16
W1 24,0
XS 146
XS 147
AW 88,144
M 0,14,2
WL 4
LC_ 110
DW 1024
AW 89,145
AW 90,146
AW 91,147
M 0,15,3
RA 220,1,8192
v_add_f32 v14, v14, v15
XS 148
XS 149
XS 150
M 0,16,4
WL 5
LO 18,18,107
LU 107,18
XS 151
AW 92,148
AW 93,149
M 0,17,5
DW 2048
HS 25,107
v_add_f32_dpp v14, v14, v14 quad_perm:[1,0,3,2] row_mask:0xf bank_mask:0xf
AW 94,150
AW 95,151
XS 152
M 1,28,4
s_add_u32 s42, s44, s60
PK 107,18,105,56
PK 105,18,105,97
LL 17,86
LL 111,87
XS 153
XS 154
XS 155
M 1,29,5
s_addc_u32 s43, s45, 0
LA 17
RA 224,1,9216
v_add_f32_dpp v14, v14, v14 quad_perm:[2,3,0,1] row_mask:0xf bank_mask:0xf
AW 96,152
AW 97,153
AW 98,154
M 2,40,4
LC_ 111
ST 104
AW 99,155
XS 156
XS 157
M 2,41,5
v_add_f32_dpp v14, v14, v14 row_half_mirror row_mask:0xf bank_mask:0xf
XS 158
XS 159
AW 100,156
M 3,52,4
WL 3
LO 16,16,110
LU 110,16
AW 101,157
AW 102,158
AW 103,159
M 3,53,5
s_add_u32 s40, s40, 3072
RA 228,1,10240
HS 25,110
v_cvt_f64_f32 v[14:15], v14
M 3,54,0
s_addc_u32 s41, s41, 0
PK 110,16,108,56
PK 108,16,108,97
M 3,55,1
s_add_u32 m0, s38, 3072
s_mov_b64 exec, s[54:55]
SP
s_mov_b64 exec, -1
PO 110
M 2,42,0
XS 160
XS 161
M 2,43,1
RA 232,1,11264
XS 162
M 1,30,0
WL 2
LO 17,17,111
LU 111,17
M 1,31,1
RV 252,22,5392
HS 25,111
M 0,18,0
WL 2
PK 111,17,109,56
PK 109,17,109,97
XS 163
AW 104,160
M 0,19,1
RA 236,1,12288
PO 111
AW 105,161
AW 106,162
M 0,20,2
DW 0
HX 25
AW 107,163
XS 164
M 0,21,3
s_add_u32 s42, s44, s61
W1 25,1024
XS 165
XS 166
M 1,32,2
s_addc_u32 s43, s45, 0
ST 108
v_add_u32 v4, s39, v4
XS 167
M 1,33,3
RA 216,1,13312
AW 108,164
AW 109,165
M 2,44,2
DW 1024
AW 110,166
AW 111,167
M 2,45,3
XS 168
XS 169
M 3,56,2
WL 2
XS 170
XS 171
M 3,57,3
RA 220,1,14336
AW 112,168
AW 113,169
M 0,22,4
DW 2048
AW 114,170
AW 115,171
M 0,23,5
XS 172
XS 173
M 1,34,4
XS 174
XS 175
M 1,35,5
RA 224,1,15360
AW 116,172
AW 117,173
M 2,46,4
LL 18,56
LL 104,57
AW 118,174
AW 119,175
M 2,47,5
LA 18
DQ 3072
M 3,58,4
WL 2
LC_ 104
M 3,59,5
RA 228,1,16384
Z 6,12,0
WV 32
BW 60
M 6,13,1
s_add_u32 s50, s50, %[d]
)"
R"(RV 40,8,6144
RV 44,8,6208
Z 7,24,0
s_cmp_eq_u32 s50, %[wrap]
LL 16,58
LL 105,59
M 7,25,1
s_cselect_b32 s50, %[rst], s50
LA 16
RA 232,1,17408
Z 8,36,0
LC_ 105
RV 48,8,6272
M 8,37,1
BR 60
Z 9,48,0
WL 8
LO 104,18,104
LU 18,104
M 9,49,1
s_add_u32 s38, s38, 32768
RA 236,1,18432
HZ 24,18
M 9,50,2
s_cmp_eq_u32 s38, s77
LL 17,60
LL 108,61
M 9,51,3
s_cselect_b32 s38, s76, s38
LA 17
M 8,38,2
s_add_u32 s37, s37, 32768
LC_ 108
M 8,39,3
s_cmp_eq_u32 s37, s75
RA 216,1,19456
M 7,26,2
WL 6
LO 105,16,105
LU 16,105
M 7,27,3
s_cselect_b32 s37, s74, s37
HS 24,16
M 6,14,2
WL 3
LL 18,62
LL 109,63
M 6,15,3
s_add_u32 s53, %[xo0], 393216
LA 18
RA 220,1,20480
M 6,16,4
s_add_u32 s46, %[xlo], s53
LC_ 109
M 6,17,5
s_addc_u32 s47, %[xhi], 0
WV 30
BW 61
M 7,28,4
WL 5
LO 108,17,108
LU 17,108
s_bitcmp1_b64 s[90:91], 48
s_cselect_b32 s58, 16, 0
s_lshr_b32 s58, 0xda43cb52, s58
v_lshrrev_b32 v3, 4, v0
v_and_b32 v3, 12, v3
v_bfe_u32 v3, s58, v3, 4
BR 61
v_bfe_u32 v11, v0, 4, 2
v_lshl_add_u32 v3, v3, 2, v11
v_mul_u32_u24 v3, 0x300, v3
v_lshrrev_b32 v11, 8, v0
v_lshl_add_u32 v3, v11, 4, v3
M 7,29,5
RA 224,1,21504
HS 24,17
LZ 40,60
WV 29
BW 62
M 8,40,4
LL 16,64
LL 112,65
M 8,41,5
LA 16
XL 72,0
M 9,52,4
WL 6
LC_ 112
RV 40,8,6336
M 9,53,5
s_mul_i32 s53, s51, 24
RA 228,1,22528
BR 62
M 9,54,0
WL 7
LO 109,18,109
LU 18,109
M 9,55,1
s_bitcmp1_b32 s51, 0
HS 24,18
s_addc_u32 s53, s53, 0
s_lshl_b32 s53, s53, 11
s_bitcmp1_b64 s[90:91], s51
s_cselect_b64 s[44:45], s[84:85], s[82:83]
s_add_u32 s44, s44, s53
s_addc_u32 s45, s45, 0
s_add_u32 s51, s51, %[d]
s_cmp_eq_u32 s51, %[wrap]
s_cselect_b32 s51, %[rst], s51
LS 44,61
WV 29
BW 63
M 8,42,0
LL 17,66
LL 113,67
M 8,43,1
WV 28
LA 17
RA 232,1,23552
RV 44,8,6400
M 7,30,0
WL 4
LC_ 113
XL 73,64
M 7,31,1
s_lshl_b32 s53, s52, 8
BR 63
LS 48,62
s_add_u32 s48, %[alo], s53
BW 64
M 6,18,0
LO 112,16,112
LU 16,112
M 6,19,1
s_addc_u32 s49, %[ahi], 0
RA 236,1,24576
HS 24,16
M 6,20,2
s_add_u32 s52, s52, %[d]
LL 18,68
LL 116,69
s_cmp_eq_u32 s52, %[wrap]
s_cselect_b32 s52, %[rst], s52
M 6,21,3
LA 18
XL 74,128
RV 48,8,6464
M 7,32,2
LC_ 116
XL 75,192
BR 64
M 7,33,3
WL 6
v_add_u32 v1, s37, v0
LS 40,63
WV 30
BW 65
M 8,44,2
LO 113,17,113
LU 17,113
M 8,45,3
RA 216,1,0
HS 24,17
M 9,56,2
WL 2
RV 40,8,6528
LL 16,70
LL 117,71
M 9,57,3
WV 21
LA 16
XL 76,256
BR 65
M 6,22,4
s_barrier
LC_ 117
v_and_b32 v22, 0xfffffcff, v1
M 6,23,5
s_sub_u32 s39, 0, s39
RA 220,1,1024
LS 44,64
WV 21
BW 66
M 7,34,4
LO 116,18,116
LU 18,116
M 7,35,5
RV 252,22,5120
RV 44,8,6592
HS 24,18
M 8,46,4
LL 17,80
LL 106,81
M 8,47,5
LA 17
RA 224,1,2048
BR 66
M 9,58,4
WL 6
LC_ 106
ds_read_b64 v[14:15], v5 offset:0
M 9,59,5
WL 6
RA 228,1,3072
v_subrev_u32 v5, s39, v5
Z 0,12,0
LO 117,16,117
LU 16,117
M 0,13,1
WV 18
XL 77,320
HS 24,16
LS 48,65
s_mul_i32 s53, s50, 25600
BW 67
RV 48,8,6656
Z 1,24,0
s_add_u32 s40, %[wlo], s53
BR 67
LL 18,82
LL 107,83
M 1,25,1
s_addc_u32 s41, %[whi], 0
LA 18
RA 232,1,4096
WL 8
LS 40,66
s_mov_b32 m0, s38
BW 68
Z 2,36,0
LC_ 107
RV 40,8,6720
M 2,37,1
BR 68
HX 24
Z 3,48,0
WL 9
LO 17,17,106
LU 106,17
M 3,49,1
RA 236,1,6144
HZ 25,106
M 3,50,2
LL 16,84
LL 110,85
M 3,51,3
WL 7
LA 16
PK 106,17,104,56
LS 44,67
WV 18
BW 69
M 2,38,2
LC_ 110
PK 104,17,104,97
W1 24,0
M 2,39,3
WV 17
RA 216,1,7168
RV 44,8,6784
M 1,26,2
WL 9
LO 18,18,107
LU 107,18
M 1,27,3
WL 7
DW 0
BR 69
HS 25,107
LS 48,68
s_add_u32 s42, s44, s60
BW 70
M 0,14,2
WL 8
LL 17,86
LL 111,87
M 0,15,3
s_addc_u32 s43, s45, 0
LA 17
PK 107,18,105,56
v_add_f32 v14, v14, v15
s_nop 1
v_add_f32_dpp v14, v14, v14 quad_perm:[1,0,3,2] row_mask:0xf bank_mask:0xf
s_nop 1
v_add_f32_dpp v14, v14, v14 quad_perm:[2,3,0,1] row_mask:0xf bank_mask:0xf
M 0,16,4
LC_ 111
PK 105,18,105,97
v_add_f32_dpp v14, v14, v14 row_half_mirror row_mask:0xf bank_mask:0xf
M 0,17,5
RA 220,1,8192
RV 48,8,6848
M 1,28,4
WL 9
RA 240,7,0
LO 16,16,110
LU 110,16
v_cvt_f64_f32 v[14:15], v14
M 1,29,5
RA 224,1,9216
DW 1024
BR 70
HS 25,110
M 2,40,4
LL 18,88
LL 114,89
s_mov_b64 exec, s[54:55]
SP
s_mov_b64 exec, -1
M 2,41,5
WL 8
LA 18
PK 110,16,108,56
LS 40,69
WV 19
BW 71
M 3,52,4
WL 6
LC_ 114
PK 108,16,108,97
M 3,53,5
RA 228,1,10240
RA 244,7,256
PO 110
M 3,54,0
WL 5
LO 17,17,111
LU 111,17
ST 104
DW 2048
M 3,55,1
s_add_u32 s40, s40, 3072
BR 71
HS 25,111
M 2,42,0
s_addc_u32 s41, s41, 0
LL 16,90
LL 115,91
M 2,43,1
s_add_u32 m0, s38, 3072
LA 16
PK 111,17,109,56
LS 44,70
s_add_u32 s42, s44, s61
RA 248,7,512
M 1,30,0
s_addc_u32 s43, s45, 0
LC_ 115
PK 109,17,109,97
M 1,31,1
WL 3
RA 232,1,11264
PO 111
LS 48,71
RA 252,7,768
M 0,18,0
LO 18,18,114
LU 114,18
M 0,19,1
RV 252,22,5392
HS 25,114
s_nop 0
v_mul_f32 v36, 0xbb2aaaab, v36
M 0,20,2
LL 17,92
LL 118,93
v_mul_f32 v37, 0xbb2aaaab, v37
v_mul_f32 v38, 0xbb2aaaab, v38
M 0,21,3
LA 17
PK 114,18,112,56
v_mul_f32 v39, 0xbb2aaaab, v39
s_nop 1
LK 40,240,60,0
LK 44,242,60,2
RA 240,7,1024
LK 48,244,61,0
s_nop 1
CF 128,40,41
LK 248,246,61,2
RA 244,7,1280
CF 129,42,43
M 1,32,2
LC_ 118
PK 112,18,112,97
M 1,33,3
RA 236,1,12288
CF 130,44,45
M 2,44,2
WL 8
DW 0
LO 16,16,115
LU 115,16
M 2,45,3
RA 216,1,13312
HS 25,115
ST 108
CF 131,46,47
LK 40,248,62,0
)"
R"(CF 132,48,49
LK 44,250,62,2
RA 248,7,1536
CF 133,50,51
GZ 0
WL 8
CF 134,248,249
CF 135,250,251
LK 48,252,63,0
CF 136,40,41
LK 248,254,63,2
RA 252,7,1792
CF 137,42,43
GS 1
WL 6
CF 138,44,45
CF 139,46,47
LK 40,240,64,0
CF 140,48,49
LK 44,242,64,2
RA 240,7,2048
CF 141,50,51
GS 2
CF 142,248,249
CF 143,250,251
M 3,56,2
WL 4
LL 18,94
LL 119,95
M 3,57,3
LA 18
PK 115,16,113,56
LK 48,244,65,0
CF 144,40,41
LK 248,246,65,2
RA 244,7,2304
CF 145,42,43
GS 3
CF 146,44,45
CF 147,46,47
M 0,22,4
LC_ 119
PK 113,16,113,97
M 0,23,5
RA 220,1,14336
DW 1024
M 1,34,4
WL 6
LO 17,17,118
LU 118,17
M 1,35,5
s_add_u32 s42, s44, s62
RA 224,1,15360
HS 25,118
s_addc_u32 s43, s45, 0
LK 40,248,66,0
CF 148,48,49
LK 44,250,66,2
RA 248,7,2560
CF 149,50,51
GS 4
CF 150,248,249
CF 151,250,251
M 2,46,4
LL 16,56
LL 104,57
M 2,47,5
WL 7
LA 16
PK 118,17,116,56
LK 48,252,67,0
CF 152,40,41
LK 248,254,67,2
RA 252,7,2816
CF 153,42,43
GS 5
CF 154,44,45
CF 155,46,47
M 3,58,4
WL 4
LC_ 104
PK 116,17,116,97
M 3,59,5
RA 228,1,16384
DW 2048
PO 118
Z 6,12,0
LO 18,18,119
LU 119,18
ST 112
M 6,13,1
DQ 3072
HS 25,119
LK 40,240,68,0
CF 156,48,49
LK 44,242,68,2
s_add_u32 s42, s44, s63
CF 157,50,51
GS 6
s_addc_u32 s43, s45, 0
CF 158,248,249
CF 159,250,251
Z 7,24,0
LL 17,58
LL 105,59
M 7,25,1
LA 17
PK 119,18,117,56
Z 8,36,0
s_add_u32 s50, s50, %[d]
LC_ 105
PK 117,18,117,97
M 8,37,1
s_cmp_eq_u32 s50, %[wrap]
RA 232,1,17408
PO 119
LK 48,244,69,0
CF 160,40,41
LK 248,246,69,2
s_cselect_b32 s50, %[rst], s50
CF 161,42,43
HX 25
GS 7
CF 162,44,45
CF 163,46,47
Z 9,48,0
WL 3
LO 104,16,104
LU 16,104
M 9,49,1
RA 236,1,18432
HZ 24,16
M 9,50,2
LL 18,60
LL 108,61
M 9,51,3
LA 18
W1 25,1024
LK 40,248,70,0
CF 164,48,49
LK 44,250,70,2
CF 165,50,51
GS 8
CF 166,248,249
CF 167,250,251
M 8,38,2
LC_ 108
ST 116
M 8,39,3
s_add_u32 s37, s37, 32768
RA 216,1,19456
v_add_u32 v4, s39, v4
LK 48,252,71,0
CF 168,40,41
LK 248,254,71,2
s_cmp_eq_u32 s37, s75
CF 169,42,43
GS 9
s_cselect_b32 s37, s74, s37
CF 170,44,45
CF 171,46,47
M 7,26,2
WL 6
LO 105,17,105
LU 17,105
M 7,27,3
s_add_u32 s38, s38, 32768
RV 36,8,6912
HS 24,17
M 6,14,2
WL 5
LL 16,62
LL 109,63
M 6,15,3
s_cmp_eq_u32 s38, s77
LA 16
RA 220,1,20480
GS 10
s_cselect_b32 s38, s76, s38
CF 172,48,49
M 6,16,4
LC_ 109
XL 78,384
M 6,17,5
CF 173,50,51
CF 174,248,249
M 7,28,4
WL 5
LO 108,18,108
LU 18,108
M 7,29,5
s_mul_i32 s53, s51, 24
RA 224,1,21504
HS 24,18
M 8,40,4
s_bitcmp1_b32 s51, 0
LL 17,64
LL 112,65
M 8,41,5
s_addc_u32 s53, s53, 0
LA 17
CF 175,250,251
M 9,52,4
WL 3
LC_ 112
M 9,53,5
s_lshl_b32 s53, s53, 11
RA 228,1,22528
GS 11
s_bitcmp1_b64 s[90:91], s51
M 9,54,0
WL 4
LO 109,16,109
LU 16,109
M 9,55,1
s_cselect_b64 s[44:45], s[84:85], s[82:83]
HS 24,16
s_add_u32 s44, s44, s53
s_addc_u32 s45, s45, 0
s_add_u32 s51, s51, %[d]
s_cmp_eq_u32 s51, %[wrap]
s_cselect_b32 s51, %[rst], s51
M 8,42,0
LL 18,66
LL 113,67
M 8,43,1
LA 18
RA 232,1,23552
XL 79,448
M 7,30,0
s_lshl_b32 s53, s52, 8
LC_ 113
v_cndmask_b32 v52, v52, v53, s[78:79]
M 7,31,1
s_add_u32 s48, %[alo], s53
v_cndmask_b32 v54, v54, v55, s[78:79]
v_cndmask_b32 v52, v52, v54, s[80:81]
M 6,18,0
WL 3
LO 112,17,112
LU 17,112
M 6,19,1
s_addc_u32 s49, %[ahi], 0
RA 236,1,24576
HS 24,17
M 6,20,2
s_add_u32 s52, s52, %[d]
LL 16,68
LL 116,69
s_cmp_eq_u32 s52, %[wrap]
s_cselect_b32 s52, %[rst], s52
v_mul_f32 v52, 0x3b2aaaab, v52
M 6,21,3
LA 16
XL 80,512
M 7,32,2
LC_ 116
XL 81,576
v_med3_f32 v53, v52, v39, v37
M 7,33,3
v_add_u32 v1, s37, v0
LG 52,53
M 8,44,2
WL 3
LO 113,18,113
LU 18,113
C 52,52,52
M 8,45,3
RA 216,1,0
HS 24,18
v_lshlrev_b32 v52, 16, v52
M 9,56,2
WL 3
LL 17,70
LL 117,71
v_add_f32 v52, v36, v52
M 9,57,3
WV 22
LA 17
XL 82,640
C 52,52,52
M 6,22,4
s_barrier
LC_ 117
v_and_b32 v22, 0xfffffcff, v1
v_lshlrev_b32 v52, 16, v52
M 6,23,5
s_sub_u32 s39, 0, s39
RA 220,1,1024
v_sqrt_f32 v52, v52
M 7,34,4
WL 4
LO 116,16,116
LU 16,116
C 52,52,52
M 7,35,5
RV 252,22,5120
HS 24,16
v_lshlrev_b32 v52, 16, v52
M 8,46,4
LL 18,80
LL 106,81
v_rcp_f32 v52, v52
M 8,47,5
LA 18
RA 224,1,2048
v_mul_f32 v52, v38, v52
M 9,58,4
WL 3
LC_ 106
ds_read_b64 v[14:15], v5 offset:0
CF 52,52,52
M 9,59,5
WL 4
RA 228,1,3072
ds_bpermute_b32 v13, v28, v52
Z 0,12,0
LO 117,17,117
LU 17,117
M 0,13,1
XL 83,704
HS 24,17
Z 1,24,0
s_mul_i32 s53, s50, 25600
LL 16,82
LL 107,83
M 1,25,1
s_add_u32 s40, %[wlo], s53
LA 16
RA 232,1,4096
Z 2,36,0
s_addc_u32 s41, %[whi], 0
LC_ 107
v_subrev_u32 v5, s39, v5
M 2,37,1
s_mov_b32 m0, s38
HX 24
Z 3,48,0
WL 4
LO 18,18,106
LU 106,18
M 3,49,1
RA 236,1,6144
HZ 25,106
M 3,50,2
LL 17,84
LL 110,85
M 3,51,3
WL 4
LA 17
PK 106,18,104,56
XS 128
M 2,38,2
LC_ 110
PK 104,18,104,97
W1 24,0
XS 129
M 2,39,3
RA 216,1,7168
RV 40,8,6144
XS 130
M 1,26,2
WL 6
DW 0
LO 16,16,107
LU 107,16
XS 131
M 1,27,3
DW 1024
HS 25,107
AW 120,128
M 0,14,2
WL 5
LL 18,86
LL 111,87
AW 121,129
M 0,15,3
s_add_u32 s42, s44, s60
LA 18
PK 107,16,105,56
v_add_f32 v14, v14, v15
s_nop 1
v_add_f32_dpp v14, v14, v14 quad_perm:[1,0,3,2] row_mask:0xf bank_mask:0xf
s_nop 1
v_add_f32_dpp v14, v14, v14 quad_perm:[2,3,0,1] row_mask:0xf bank_mask:0xf
AW 122,130
M 0,16,4
s_addc_u32 s43, s45, 0
LC_ 111
PK 105,16,105,97
v_add_f32_dpp v14, v14, v14 row_half_mirror row_mask:0xf bank_mask:0xf
AW 123,131
XS 132
M 0,17,5
RA 220,1,8192
RV 44,8,6208
XS 133
M 1,28,4
WL 7
LO 17,17,110
)"
R"(LU 110,17
v_cvt_f64_f32 v[14:15], v14
XS 134
M 1,29,5
RA 224,1,9216
HS 25,110
XS 135
M 2,40,4
LL 16,88
LL 114,89
s_mov_b64 exec, s[54:55]
SP
s_mov_b64 exec, -1
DW 2048
AW 124,132
M 2,41,5
LA 16
PK 110,17,108,56
AW 125,133
M 3,52,4
WL 3
LC_ 114
PK 108,17,108,97
AW 126,134
AW 127,135
M 3,53,5
RA 228,1,10240
PO 110
v_min_u32 v35, v35, v13
M 3,54,0
WV 26
LO 18,18,111
LU 111,18
ST 104
XS 136
M 3,55,1
s_add_u32 s40, s40, 3072
RV 48,8,6272
HS 25,111
s_addc_u32 s41, s41, 0
s_add_u32 m0, s38, 3072
XS 137
LZ 40,72
WL 5
RV 40,8,6336
XS 138
LS 44,73
RV 44,8,6400
XS 139
M 2,42,0
LL 17,90
LL 115,91
M 2,43,1
LA 17
PK 111,18,109,56
M 1,30,0
LC_ 115
PK 109,18,109,97
AW 128,136
M 1,31,1
WV 25
RA 232,1,11264
DW 0
PO 111
AW 129,137
AW 130,138
M 0,18,0
WL 4
LO 16,16,114
LU 114,16
AW 131,139
M 0,19,1
s_add_u32 s42, s44, s61
RV 252,22,5392
HS 25,114
XS 140
LS 48,74
s_addc_u32 s43, s45, 0
RV 48,8,6464
XS 141
LS 40,75
XS 142
XS 143
M 0,20,2
LL 18,92
LL 118,93
M 0,21,3
LA 18
DW 1024
PK 114,16,112,56
M 1,32,2
WV 26
LC_ 118
PK 112,16,112,97
M 1,33,3
WL 7
RA 236,1,12288
RV 40,8,6528
AW 132,140
AW 133,141
LS 44,76
AW 134,142
AW 135,143
M 2,44,2
WL 7
LO 17,17,115
LU 115,17
M 2,45,3
RA 216,1,13312
HS 25,115
ST 108
M 3,56,2
WL 2
DW 2048
LL 16,94
LL 119,95
XS 144
M 3,57,3
LA 16
PK 115,17,113,56
XS 145
M 0,22,4
LC_ 119
PK 113,17,113,97
XS 146
M 0,23,5
WV 27
RA 220,1,14336
RV 44,8,6592
XS 147
LS 48,77
AW 136,144
AW 137,145
M 1,34,4
DQ 3072
LO 18,18,118
LU 118,18
M 1,35,5
s_add_u32 s42, s44, s62
RA 224,1,15360
HS 25,118
s_addc_u32 s43, s45, 0
AW 138,146
M 2,46,4
s_add_u32 s50, s50, %[d]
LL 17,56
LL 104,57
AW 139,147
M 2,47,5
s_cmp_eq_u32 s50, %[wrap]
LA 17
PK 118,18,116,56
XS 148
M 3,58,4
WL 3
LC_ 104
PK 116,18,116,97
s_cselect_b32 s50, %[rst], s50
XS 149
M 3,59,5
WL 3
RA 228,1,16384
PO 118
XS 150
Z 6,12,0
WV 14
LO 16,16,119
LU 119,16
ST 112
XS 151
AW 140,148
M 6,13,1
s_add_u32 s38, s38, 32768
RV 48,8,6656
HS 25,119
AW 141,149
LS 40,78
s_cmp_eq_u32 s38, s77
RV 40,8,6720
AW 142,150
Z 7,24,0
s_cselect_b32 s38, s76, s38
LL 18,58
LL 105,59
M 7,25,1
s_mul_i32 s53, s50, 25600
LA 18
PK 119,16,117,56
AW 143,151
LS 44,79
s_add_u32 s40, %[wlo], s53
XS 152
XS 153
Z 8,36,0
s_addc_u32 s41, %[whi], 0
LC_ 105
PK 117,16,117,97
M 8,37,1
s_mov_b32 m0, s38
RA 232,1,17408
PO 119
Z 9,48,0
WL 5
LO 104,17,104
LU 17,104
XS 154
M 9,49,1
s_add_u32 s42, s44, s63
RA 236,1,18432
HZ 24,17
XS 155
AW 144,152
M 9,50,2
s_addc_u32 s43, s45, 0
LL 16,60
LL 108,61
AW 145,153
M 9,51,3
LA 16
DW 0
HX 25
AW 146,154
M 8,38,2
WV 15
LC_ 108
W1 25,1024
AW 147,155
M 8,39,3
WL 8
RA 216,1,19456
RV 44,8,6784
XS 156
LS 48,80
RV 48,8,6848
XS 157
M 7,26,2
WL 8
LO 105,18,105
LU 18,105
M 7,27,3
ST 116
HS 24,18
XS 158
XS 159
M 6,14,2
WL 6
LL 17,62
LL 109,63
AW 148,156
M 6,15,3
LA 17
RA 220,1,20480
DW 1024
AW 149,157
M 6,16,4
s_add_u32 s37, s37, 32768
LC_ 109
RA 240,7,0
s_cmp_eq_u32 s37, s75
s_cselect_b32 s37, s74, s37
AW 150,158
M 6,17,5
WV 16
DW 2048
v_add_u32 v4, s39, v4
AW 151,159
LS 40,81
WV 16
RA 244,7,256
XS 160
M 7,28,4
WL 6
LO 108,16,108
LU 16,108
M 7,29,5
s_add_u32 s40, s40, 3072
RA 224,1,21504
HS 24,16
XS 161
LS 44,82
s_addc_u32 s41, s41, 0
XS 162
XS 163
M 8,40,4
s_add_u32 m0, s38, 3072
LL 18,64
LL 112,65
M 8,41,5
s_mul_i32 s53, s51, 24
LA 18
AW 152,160
M 9,52,4
WL 5
LC_ 112
RA 248,7,512
AW 153,161
M 9,53,5
s_bitcmp1_b32 s51, 0
RA 228,1,22528
AW 154,162
M 9,54,0
WL 7
LO 109,17,109
LU 17,109
AW 155,163
M 9,55,1
s_addc_u32 s53, s53, 0
HS 24,17
s_lshl_b32 s53, s53, 11
s_bitcmp1_b64 s[90:91], s51
s_cselect_b64 s[44:45], s[84:85], s[82:83]
s_add_u32 s44, s44, s53
s_addc_u32 s45, s45, 0
s_add_u32 s51, s51, %[d]
s_cmp_eq_u32 s51, %[wrap]
s_cselect_b32 s51, %[rst], s51
XS 164
WV 15
LS 48,83
XS 165
XS 166
M 8,42,0
LL 16,66
LL 113,67
M 8,43,1
LA 16
RA 232,1,23552
XS 167
M 7,30,0
LC_ 113
DW 0
AW 156,164
M 7,31,1
s_lshl_b32 s53, s52, 8
RA 252,7,768
AW 157,165
M 6,18,0
WL 4
LO 112,18,112
LU 18,112
AW 158,166
M 6,19,1
s_add_u32 s48, %[alo], s53
RA 236,1,24576
HS 24,18
AW 159,167
M 6,20,2
s_addc_u32 s49, %[ahi], 0
LL 17,68
LL 116,69
s_add_u32 s52, s52, %[d]
s_cmp_eq_u32 s52, %[wrap]
s_cselect_b32 s52, %[rst], s52
DW 1024
XS 168
M 6,21,3
LA 17
DW 2048
XS 169
XS 170
M 7,32,2
LC_ 116
v_add_u32 v1, s37, v0
XS 171
M 7,33,3
RA 216,1,0
v_and_b32 v22, 0xfffffcff, v1
AW 160,168
M 8,44,2
WL 5
LO 113,16,113
LU 16,113
AW 161,169
M 8,45,3
DQ 3072
HS 24,16
AW 162,170
M 9,56,2
WL 3
LL 18,70
LL 117,71
AW 163,171
M 9,57,3
s_add_u32 s50, s50, %[d]
LA 18
RA 220,1,1024
XS 172
XS 173
M 6,22,4
s_cmp_eq_u32 s50, %[wrap]
LC_ 117
RV 252,22,5120
XS 174
M 6,23,5
s_cselect_b32 s50, %[rst], s50
XS 175
AW 164,172
M 7,34,4
WL 5
LO 116,17,116
)"
R"(LU 17,116
M 7,35,5
s_barrier
RA 224,1,2048
HS 24,17
AW 165,173
M 8,46,4
s_sub_u32 s39, 0, s39
LL 16,80
LL 106,81
AW 166,174
M 8,47,5
s_add_u32 s38, s38, 32768
LA 16
ds_read_b64 v[14:15], v5 offset:0
AW 167,175
v_mul_f32 v36, 0xbb2aaaab, v36
v_mul_f32 v37, 0xbb2aaaab, v37
v_mul_f32 v38, 0xbb2aaaab, v38
v_mul_f32 v39, 0xbb2aaaab, v39
s_nop 1
LK 40,240,72,0
LK 44,242,72,2
s_cmp_eq_u32 s38, s77
RA 240,7,1024
LK 48,244,73,0
s_nop 0
CF 128,40,41
LK 248,246,73,2
s_cselect_b32 s38, s76, s38
RA 244,7,1280
CF 129,42,43
M 9,58,4
WL 7
LC_ 106
v_subrev_u32 v5, s39, v5
M 9,59,5
WL 6
RA 228,1,3072
CF 130,44,45
Z 0,12,0
LO 117,18,117
LU 18,117
CF 131,46,47
M 0,13,1
s_mul_i32 s53, s50, 25600
HS 24,18
LK 40,248,74,0
CF 132,48,49
LK 44,250,74,2
s_add_u32 s40, %[wlo], s53
RA 248,7,1536
CF 133,50,51
GZ 0
s_addc_u32 s41, %[whi], 0
CF 134,248,249
CF 135,250,251
LK 48,252,75,0
CF 136,40,41
LK 248,254,75,2
s_mov_b32 m0, s38
RA 252,7,1792
CF 137,42,43
GS 1
WL 5
CF 138,44,45
CF 139,46,47
LK 40,240,76,0
CF 140,48,49
LK 44,242,76,2
RA 240,7,2048
CF 141,50,51
GS 2
WL 5
CF 142,248,249
CF 143,250,251
LK 48,244,77,0
CF 144,40,41
LK 248,246,77,2
RA 244,7,2304
CF 145,42,43
GS 3
CF 146,44,45
CF 147,46,47
Z 1,24,0
LL 17,82
LL 107,83
M 1,25,1
LA 17
RA 232,1,4096
Z 2,36,0
LC_ 107
HX 24
M 2,37,1
WL 6
W1 24,0
LK 40,248,78,0
CF 148,48,49
LK 44,250,78,2
RA 248,7,2560
CF 149,50,51
GS 4
CF 150,248,249
CF 151,250,251
Z 3,48,0
LO 16,16,106
LU 106,16
M 3,49,1
RA 236,1,6144
HZ 25,106
M 3,50,2
LL 18,84
LL 110,85
M 3,51,3
WL 8
LA 18
PK 106,16,104,56
LK 48,252,79,0
CF 152,40,41
LK 248,254,79,2
RA 252,7,2816
CF 153,42,43
GS 5
CF 154,44,45
CF 155,46,47
M 2,38,2
LC_ 110
PK 104,16,104,97
M 2,39,3
RA 216,1,7168
v_add_f32 v14, v14, v15
M 1,26,2
WL 7
LO 17,17,107
LU 107,17
v_add_f32_dpp v14, v14, v14 quad_perm:[1,0,3,2] row_mask:0xf bank_mask:0xf
M 1,27,3
DW 0
HS 25,107
LK 40,240,80,0
CF 156,48,49
LK 44,242,80,2
s_add_u32 s42, s44, s60
CF 157,50,51
v_add_f32_dpp v14, v14, v14 quad_perm:[2,3,0,1] row_mask:0xf bank_mask:0xf
GS 6
s_addc_u32 s43, s45, 0
CF 158,248,249
CF 159,250,251
M 0,14,2
WL 4
LL 16,86
LL 111,87
M 0,15,3
LA 16
PK 107,17,105,56
M 0,16,4
LC_ 111
PK 105,17,105,97
v_add_f32_dpp v14, v14, v14 row_half_mirror row_mask:0xf bank_mask:0xf
M 0,17,5
RA 220,1,8192
v_cvt_f64_f32 v[14:15], v14
LK 48,244,81,0
CF 160,40,41
LK 248,246,81,2
CF 161,42,43
GS 7
CF 162,44,45
CF 163,46,47
M 1,28,4
WL 4
LO 18,18,110
LU 110,18
M 1,29,5
RA 224,1,9216
HS 25,110
M 2,40,4
LL 17,88
LL 114,89
s_mov_b64 exec, s[54:55]
SP
s_mov_b64 exec, -1
M 2,41,5
LA 17
PK 110,18,108,56
LK 40,248,82,0
CF 164,48,49
LK 44,250,82,2
CF 165,50,51
GS 8
CF 166,248,249
CF 167,250,251
M 3,52,4
WL 2
LC_ 114
PK 108,18,108,97
M 3,53,5
RA 228,1,10240
PO 110
M 3,54,0
LO 16,16,111
LU 111,16
ST 104
M 3,55,1
DW 1024
HS 25,111
LK 48,252,83,0
CF 168,40,41
LK 248,254,83,2
s_add_u32 s42, s44, s61
CF 169,42,43
GS 9
s_addc_u32 s43, s45, 0
CF 170,44,45
CF 171,46,47
M 2,42,0
LL 18,90
LL 115,91
M 2,43,1
LA 18
PK 111,16,109,56
M 1,30,0
LC_ 115
PK 109,16,109,97
M 1,31,1
RA 232,1,11264
PO 111
M 0,18,0
WL 3
LO 17,17,114
LU 114,17
M 0,19,1
RV 252,22,5392
HS 25,114
M 0,20,2
LL 16,92
LL 118,93
M 0,21,3
LA 16
PK 114,17,112,56
M 1,32,2
LC_ 118
PK 112,17,112,97
M 1,33,3
RA 236,1,12288
RV 36,8,6912
DW 2048
GS 10
CF 172,48,49
CF 173,50,51
M 2,44,2
WL 6
LO 18,18,115
LU 115,18
M 2,45,3
RA 216,1,13312
HS 25,115
ST 108
M 3,56,2
WL 2
LL 17,94
LL 119,95
M 3,57,3
s_add_u32 s42, s44, s62
LA 17
PK 115,18,113,56
M 0,22,4
s_addc_u32 s43, s45, 0
LC_ 119
PK 113,18,113,97
M 0,23,5
s_add_u32 s40, s40, 3072
RA 220,1,14336
CF 174,248,249
M 1,34,4
LO 16,16,118
LU 118,16
CF 175,250,251
M 1,35,5
s_addc_u32 s41, s41, 0
RA 224,1,15360
HS 25,118
GS 11
s_add_u32 m0, s38, 3072
ST 112
s_nop 5
v_cndmask_b32 v52, v52, v53, s[78:79]
M 2,46,4
LL 18,56
LL 104,57
v_cndmask_b32 v54, v54, v55, s[78:79]
M 2,47,5
LA 18
PK 118,16,116,56
v_cndmask_b32 v52, v52, v54, s[80:81]
M 3,58,4
WL 2
LC_ 104
PK 116,16,116,97
M 3,59,5
RA 228,1,16384
PO 118
v_mul_f32 v52, 0x3b2aaaab, v52
Z 6,12,0
LO 17,17,119
LU 119,17
v_med3_f32 v53, v52, v39, v37
M 6,13,1
DW 0
HS 25,119
LG 52,53
Z 7,24,0
s_add_u32 s42, s44, s63
LL 16,58
LL 105,59
C 52,52,52
M 7,25,1
s_addc_u32 s43, s45, 0
LA 16
PK 119,17,117,56
Z 8,36,0
LC_ 105
PK 117,17,117,97
v_lshlrev_b32 v52, 16, v52
M 8,37,1
RA 232,1,17408
PO 119
v_add_f32 v52, v36, v52
Z 9,48,0
WL 3
LO 104,18,104
LU 18,104
C 52,52,52
M 9,49,1
RA 236,1,18432
HZ 24,18
M 9,50,2
LL 17,60
LL 108,61
v_lshlrev_b32 v52, 16, v52
M 9,51,3
LA 17
ST 116
HX 25
v_sqrt_f32 v52, v52
M 8,38,2
LC_ 108
W1 25,1024
C 52,52,52
M 8,39,3
RA 216,1,19456
v_add_u32 v4, s39, v4
v_lshlrev_b32 v52, 16, v52
M 7,26,2
WL 6
LO 105,16,105
LU 16,105
M 7,27,3
DW 1024
HS 24,16
v_rcp_f32 v52, v52
M 6,14,2
WL 4
LL 18,62
LL 109,63
v_mul_f32 v52, v38, v52
M 6,15,3
s_add_u32 s37, s37, 32768
LA 18
RA 220,1,20480
CF 52,52,52
M 6,16,4
s_cmp_eq_u32 s37, s75
LC_ 109
ds_bpermute_b32 v13, v28, v52
s_cselect_b32 s37, s74, s37
M 6,17,5
DW 2048
M 7,28,4
WL 6
LO 108,17,108
LU 17,108
M 7,29,5
s_mul_i32 s53, s51, 24
RA 224,1,21504
HS 24,17
M 8,40,4
s_bitcmp1_b32 s51, 0
LL 16,64
LL 112,65
M 8,41,5
s_addc_u32 s53, s53, 0
LA 16
M 9,52,4
WL 4
LC_ 112
M 9,53,5
s_lshl_b32 s53, s53, 11
RA 228,1,22528
)"
R"(M 9,54,0
WL 4
LO 109,18,109
LU 18,109
M 9,55,1
s_bitcmp1_b64 s[90:91], s51
HS 24,18
s_cselect_b64 s[44:45], s[84:85], s[82:83]
s_add_u32 s44, s44, s53
s_addc_u32 s45, s45, 0
s_add_u32 s51, s51, %[d]
s_cmp_eq_u32 s51, %[wrap]
s_cselect_b32 s51, %[rst], s51
M 8,42,0
s_lshl_b32 s53, s52, 8
LL 17,66
LL 113,67
M 8,43,1
s_add_u32 s48, %[alo], s53
LA 17
RA 232,1,23552
XS 128
M 7,30,0
s_addc_u32 s49, %[ahi], 0
LC_ 113
XS 129
M 7,31,1
s_add_u32 s52, s52, %[d]
XS 130
XS 131
M 6,18,0
WL 3
LO 112,16,112
LU 16,112
M 6,19,1
s_cmp_eq_u32 s52, %[wrap]
RA 236,1,24576
HS 24,16
AW 168,128
AW 169,129
M 6,20,2
s_cselect_b32 s52, %[rst], s52
DQ 3072
LL 18,68
LL 116,69
AW 170,130
M 6,21,3
LA 18
v_add_u32 v1, s37, v0
AW 171,131
M 7,32,2
s_add_u32 s50, s50, %[d]
LC_ 116
XS 132
M 7,33,3
s_cmp_eq_u32 s50, %[wrap]
RA 216,1,0
v_and_b32 v22, 0xfffffcff, v1
XS 133
M 8,44,2
WL 4
LO 113,17,113
LU 17,113
XS 134
M 8,45,3
s_cselect_b32 s50, %[rst], s50
HS 24,17
XS 135
M 9,56,2
WL 3
LL 16,70
LL 117,71
AW 172,132
M 9,57,3
WV 21
LA 16
RA 220,1,1024
AW 173,133
M 6,22,4
s_barrier
LC_ 117
RV 252,22,5120
AW 174,134
M 6,23,5
s_sub_u32 s39, 0, s39
AW 175,135
v_min_u32 v35, v35, v13
M 7,34,4
WL 5
LO 116,18,116
LU 18,116
M 7,35,5
s_add_u32 s38, s38, 32768
RA 224,1,2048
HS 24,18
XS 136
M 8,46,4
s_cmp_eq_u32 s38, s77
LL 17,80
LL 106,81
XS 137
M 8,47,5
s_cselect_b32 s38, s76, s38
LA 17
XS 138
XS 139
M 9,58,4
WL 4
LC_ 106
AW 176,136
M 9,59,5
WL 3
RA 228,1,3072
AW 177,137
Z 0,12,0
LO 117,16,117
LU 16,117
AW 178,138
M 0,13,1
HS 24,16
AW 179,139
Z 1,24,0
LL 18,82
LL 107,83
XS 140
M 1,25,1
LA 18
RA 232,1,4096
XS 141
Z 2,36,0
LC_ 107
HX 24
XS 142
M 2,37,1
W1 24,0
XS 143
Z 3,48,0
WL 5
LO 17,17,106
LU 106,17
AW 180,140
M 3,49,1
HZ 25,106
AW 181,141
Z 4,60,0
LL 16,84
LL 110,85
AW 182,142
M 4,61,1
LA 16
PK 106,17,104,56
AW 183,143
Z 5,72,0
WL 5
LC_ 110
PK 104,17,104,97
XS 144
M 5,73,1
RA 236,1,6144
XS 145
XS 146
M 5,74,2
WL 4
LO 18,18,107
LU 107,18
XS 147
M 5,75,3
s_add_u32 s42, s44, s60
HS 25,107
AW 184,144
M 4,62,2
s_addc_u32 s43, s45, 0
LL 17,86
LL 111,87
AW 185,145
M 4,63,3
LA 17
PK 107,18,105,56
AW 186,146
M 3,50,2
LC_ 111
PK 105,18,105,97
AW 187,147
M 3,51,3
RA 216,1,7168
XS 148
M 2,38,2
WL 4
LO 16,16,110
LU 110,16
XS 149
M 2,39,3
ST 104
HS 25,110
XS 150
M 1,26,2
LL 18,88
LL 114,89
XS 151
M 1,27,3
LA 18
PK 110,16,108,56
AW 188,148
M 0,14,2
WL 4
LC_ 114
PK 108,16,108,97
AW 189,149
M 0,15,3
s_mov_b32 m0, %[pm]
RA 220,1,8192
PO 110
AW 190,150
M 0,16,4
WL 4
LO 17,17,111
LU 111,17
AW 191,151
M 0,17,5
DF
HS 25,111
XS 152
M 1,28,4
s_add_u32 s42, s44, s61
LL 16,90
LL 115,91
XS 153
XS 154
M 1,29,5
s_addc_u32 s43, s45, 0
LA 16
PK 111,17,109,56
XS 155
M 2,40,4
LC_ 115
PK 109,17,109,97
AW 192,152
M 2,41,5
RA 224,1,9216
PO 111
AW 193,153
M 3,52,4
WL 4
LO 18,18,114
LU 114,18
AW 194,154
M 3,53,5
ST 108
HS 25,114
AW 195,155
M 4,64,4
LL 17,92
LL 118,93
XS 156
XS 157
M 4,65,5
LA 17
PK 114,18,112,56
XS 158
XS 159
AW 196,156
M 5,76,4
WL 4
LC_ 118
PK 112,18,112,97
AW 197,157
AW 198,158
AW 199,159
M 5,77,5
RA 228,1,10240
XS 160
M 5,78,0
WL 4
LO 16,16,115
LU 115,16
M 5,79,1
s_add_u32 s42, s44, s62
HS 25,115
XS 161
M 4,66,0
s_addc_u32 s43, s45, 0
LL 18,94
LL 119,95
M 4,67,1
LA 18
PK 115,16,113,56
M 3,54,0
LC_ 119
PK 113,16,113,97
M 3,55,1
RA 232,1,11264
XS 162
M 2,42,0
WL 4
LO 17,17,118
LU 118,17
XS 163
M 2,43,1
RV 252,22,5392
HS 25,118
AW 200,160
M 1,30,0
ST 112
AW 201,161
M 1,31,1
RA 236,1,12288
PK 118,17,116,56
AW 202,162
M 0,18,0
WL 5
PK 116,17,116,97
AW 203,163
M 0,19,1
PO 118
XS 164
M 0,20,2
WL 3
LO 18,18,119
LU 119,18
XS 165
M 0,21,3
s_add_u32 s42, s44, s63
HS 25,119
XS 166
M 1,32,2
s_addc_u32 s43, s45, 0
HX 25
XS 167
M 1,33,3
RA 216,1,13312
PK 119,18,117,56
AW 204,164
M 2,44,2
W1 25,1024
PK 117,18,117,97
AW 205,165
M 2,45,3
PO 119
v_add_u32 v4, s39, v4
AW 206,166
M 3,56,2
ST 116
AW 207,167
XS 168
M 3,57,3
XS 169
XS 170
M 4,68,2
XS 171
AW 208,168
M 4,69,3
RA 220,1,14336
AW 209,169
M 5,80,2
WL 3
AW 210,170
AW 211,171
M 5,81,3
XS 172
XS 173
M 0,22,4
XS 174
XS 175
M 0,23,5
AW 212,172
AW 213,173
M 1,34,4
AW 214,174
AW 215,175
M 1,35,5
RA 224,1,15360
M 2,46,4
LL 16,56
LL 104,57
M 2,47,5
LA 16
ds_read_b64 v[14:15], v5 offset:0
M 3,58,4
LC_ 104
v_subrev_u32 v5, s39, v5
M 3,59,5
M 4,70,4
M 4,71,5
RA 228,1,16384
M 5,82,4
WL 5
LL 17,58
LL 105,59
M 5,83,5
LA 17
Z 6,12,0
LC_ 105
M 6,13,1
Z 7,24,0
WL 3
LO 104,16,104
LU 16,104
M 7,25,1
RA 232,1,17408
HZ 24,16
)"
R"(Z 8,36,0
LL 18,60
LL 108,61
M 8,37,1
s_mul_i32 s53, s50, 25600
LA 18
v_add_f32 v14, v14, v15
Z 9,48,0
s_add_u32 s40, %[wlo], s53
LC_ 108
v_add_f32_dpp v14, v14, v14 quad_perm:[1,0,3,2] row_mask:0xf bank_mask:0xf
M 9,49,1
s_addc_u32 s41, %[whi], 0
v_add_f32_dpp v14, v14, v14 quad_perm:[2,3,0,1] row_mask:0xf bank_mask:0xf
Z 10,60,0
WL 3
LO 105,17,105
LU 17,105
M 10,61,1
s_mov_b32 m0, s38
RA 236,1,18432
HS 24,17
Z 11,72,0
LL 16,62
LL 109,63
M 11,73,1
LA 16
DW 0
M 11,74,2
LC_ 109
v_add_f32_dpp v14, v14, v14 row_half_mirror row_mask:0xf bank_mask:0xf
M 11,75,3
v_cvt_f64_f32 v[14:15], v14
M 10,62,2
WL 3
LO 108,18,108
LU 18,108
M 10,63,3
s_add_u32 s37, s37, 32768
RA 216,1,19456
HS 24,18
M 9,50,2
s_cmp_eq_u32 s37, s75
LL 17,64
LL 112,65
M 9,51,3
s_cselect_b32 s37, s74, s37
LA 17
s_mov_b64 exec, s[54:55]
SP
s_mov_b64 exec, -1
M 8,38,2
LC_ 112
DW 1024
M 8,39,3
M 7,26,2
WL 3
LO 109,16,109
LU 16,109
M 7,27,3
RA 220,1,20480
HS 24,16
M 6,14,2
LL 18,66
LL 113,67
M 6,15,3
LA 18
DW 2048
M 6,16,4
LC_ 113
M 6,17,5
M 7,28,4
WL 3
LO 112,17,112
LU 17,112
M 7,29,5
s_mul_i32 s53, s51, 24
RA 224,1,21504
HS 24,17
M 8,40,4
s_bitcmp1_b32 s51, 0
LL 16,68
LL 116,69
M 8,41,5
s_addc_u32 s53, s53, 0
LA 16
M 9,52,4
s_lshl_b32 s53, s53, 11
LC_ 116
M 9,53,5
s_bitcmp1_b64 s[90:91], s51
M 10,64,4
WL 3
LO 113,18,113
LU 18,113
M 10,65,5
s_cselect_b64 s[44:45], s[84:85], s[82:83]
RA 228,1,22528
HS 24,18
M 11,76,4
LL 17,70
LL 117,71
M 11,77,5
s_add_u32 s44, s44, s53
LA 17
M 11,78,0
s_addc_u32 s45, s45, 0
LC_ 117
M 11,79,1
s_add_u32 s51, s51, %[d]
M 10,66,0
WL 3
LO 116,16,116
LU 16,116
M 10,67,1
s_cmp_eq_u32 s51, %[wrap]
RA 232,1,23552
HS 24,16
M 9,54,0
s_cselect_b32 s51, %[rst], s51
LL 18,72
LL 120,73
M 9,55,1
s_add_u32 s40, s40, 3072
LA 18
M 8,42,0
s_addc_u32 s41, s41, 0
LC_ 120
s_add_u32 m0, s38, 3072
M 8,43,1
DW 0
M 7,30,0
WL 3
LO 117,17,117
LU 17,117
M 7,31,1
RA 236,1,24576
HS 24,17
M 6,18,0
LL 16,74
LL 121,75
M 6,19,1
LA 16
DW 1024
M 6,20,2
LC_ 121
DW 2048
M 6,21,3
s_lshl_b32 s53, s52, 8
v_add_u32 v1, s37, v0
M 7,32,2
WL 3
LO 120,18,120
LU 18,120
M 7,33,3
s_add_u32 s48, %[alo], s53
RA 216,1,0
HS 24,18
M 8,44,2
s_addc_u32 s49, %[ahi], 0
LL 17,76
LL 124,77
M 8,45,3
s_add_u32 s52, s52, %[d]
LA 17
v_and_b32 v22, 0xfffffcff, v1
M 9,56,2
s_cmp_eq_u32 s52, %[wrap]
LC_ 124
M 9,57,3
s_cselect_b32 s52, %[rst], s52
DQ 3072
M 10,68,2
WL 3
LO 121,16,121
LU 16,121
M 10,69,3
s_add_u32 s50, s50, %[d]
RA 220,1,1024
HS 24,16
M 11,80,2
LL 18,78
LL 125,79
M 11,81,3
s_cmp_eq_u32 s50, %[wrap]
LA 18
RV 252,22,5120
M 6,22,4
s_cselect_b32 s50, %[rst], s50
LC_ 125
M 6,23,5
WV 25
M 7,34,4
WL 4
LO 124,17,124
LU 17,124
M 7,35,5
s_add_u32 s38, s38, 32768
RA 224,1,2048
HS 24,17
M 8,46,4
s_cmp_eq_u32 s38, s77
LL 16,80
LL 106,81
M 8,47,5
s_cselect_b32 s38, s76, s38
LA 16
M 9,58,4
s_barrier
LC_ 106
M 9,59,5
s_sub_u32 s39, 0, s39
M 10,70,4
WL 3
LO 125,18,125
LU 18,125
M 10,71,5
RA 228,1,3072
HS 24,18
M 11,82,4
LL 17,82
LL 107,83
M 11,83,5
LA 17
HX 24
Z 0,12,0
LC_ 107
W1 24,0
M 0,13,1
Z 1,24,0
WL 4
LO 16,16,106
LU 106,16
M 1,25,1
RA 232,1,4096
HZ 25,106
Z 2,36,0
LL 18,84
LL 110,85
M 2,37,1
LA 18
PK 106,16,104,56
Z 3,48,0
LC_ 110
PK 104,16,104,97
M 3,49,1
Z 4,60,0
WL 4
LO 17,17,107
LU 107,17
M 4,61,1
s_add_u32 s42, s44, s60
RA 236,1,6144
HS 25,107
Z 5,72,0
LL 16,86
LL 111,87
M 5,73,1
s_addc_u32 s43, s45, 0
LA 16
PK 107,17,105,56
M 5,74,2
LC_ 111
PK 105,17,105,97
M 5,75,3
ST 104
M 4,62,2
WL 3
LO 18,18,110
LU 110,18
M 4,63,3
RA 216,1,7168
HS 25,110
M 3,50,2
LL 17,88
LL 114,89
M 3,51,3
LA 17
PK 110,18,108,56
M 2,38,2
LC_ 114
PK 108,18,108,97
M 2,39,3
PO 110
M 1,26,2
WL 3
LO 16,16,111
LU 111,16
M 1,27,3
s_add_u32 s42, s44, s61
RA 220,1,8192
HS 25,111
M 0,14,2
LL 18,90
LL 115,91
M 0,15,3
s_addc_u32 s43, s45, 0
LA 18
PK 111,16,109,56
M 0,16,4
LC_ 115
PK 109,16,109,97
M 0,17,5
PO 111
M 1,28,4
WL 3
LO 17,17,114
LU 114,17
M 1,29,5
RA 224,1,9216
HS 25,114
M 2,40,4
LL 16,92
LL 118,93
M 2,41,5
LA 16
PK 114,17,112,56
M 3,52,4
LC_ 118
PK 112,17,112,97
M 3,53,5
ST 108
M 4,64,4
WL 3
LO 18,18,115
LU 115,18
M 4,65,5
s_add_u32 s42, s44, s62
RA 228,1,10240
HS 25,115
M 5,76,4
LL 17,94
LL 119,95
M 5,77,5
s_addc_u32 s43, s45, 0
LA 17
PK 115,18,113,56
M 5,78,0
LC_ 119
PK 113,18,113,97
M 5,79,1
ST 112
M 4,66,0
WL 3
LO 16,16,118
LU 118,16
M 4,67,1
RA 232,1,11264
HS 25,118
M 3,54,0
LL 18,96
LL 122,97
M 3,55,1
LA 18
PK 118,16,116,56
M 2,42,0
LC_ 122
PK 116,16,116,97
M 2,43,1
RV 252,22,5392
PO 118
M 1,30,0
WL 4
LO 17,17,119
LU 119,17
M 1,31,1
s_add_u32 s42, s44, s63
RA 236,1,12288
HS 25,119
M 0,18,0
LL 16,98
LL 123,99
M 0,19,1
s_addc_u32 s43, s45, 0
LA 16
PK 119,17,117,56
M 0,20,2
LC_ 123
PK 117,17,117,97
M 0,21,3
PO 119
M 1,32,2
WL 4
LO 18,18,122
LU 122,18
M 1,33,3
RA 216,1,13312
HS 25,122
M 2,44,2
LL 17,100
LL 126,101
M 2,45,3
LA 17
PK 122,18,120,56
M 3,56,2
LC_ 126
PK 120,18,120,97
M 3,57,3
ST 116
M 4,68,2
)"
R"(WL 3
LO 16,16,123
LU 123,16
M 4,69,3
s_add_u32 s42, s44, s64
RA 220,1,14336
HS 25,123
M 5,80,2
LL 18,102
LL 127,103
M 5,81,3
s_addc_u32 s43, s45, 0
LA 18
PK 123,16,121,56
M 0,22,4
LC_ 127
PK 121,16,121,97
M 0,23,5
ST 120
M 1,34,4
WL 3
LO 17,17,126
LU 126,17
M 1,35,5
RA 224,1,15360
HS 25,126
M 2,46,4
LL 16,56
LL 104,57
M 2,47,5
LA 16
PK 126,17,124,56
M 3,58,4
LC_ 104
PK 124,17,124,97
M 3,59,5
ds_read_b64 v[14:15], v5 offset:0
PO 126
M 4,70,4
WL 4
LO 18,18,127
LU 127,18
M 4,71,5
s_add_u32 s42, s44, s65
RA 228,1,16384
HS 25,127
M 5,82,4
LL 17,58
LL 105,59
M 5,83,5
s_addc_u32 s43, s45, 0
LA 17
PK 127,18,125,56
Z 6,12,0
LC_ 105
PK 125,18,125,97
M 6,13,1
PO 127
v_subrev_u32 v5, s39, v5
Z 7,24,0
WL 4
LO 104,16,104
LU 16,104
M 7,25,1
RA 232,1,17408
HZ 24,16
Z 8,36,0
WL 4
LL 18,60
LL 108,61
M 8,37,1
s_mul_i32 s53, s50, 25600
LA 18
v_add_f32 v14, v14, v15
Z 9,48,0
s_add_u32 s40, %[wlo], s53
LC_ 108
v_add_f32_dpp v14, v14, v14 quad_perm:[1,0,3,2] row_mask:0xf bank_mask:0xf
M 9,49,1
s_addc_u32 s41, %[whi], 0
v_add_f32_dpp v14, v14, v14 quad_perm:[2,3,0,1] row_mask:0xf bank_mask:0xf
HX 25
Z 10,60,0
WL 3
LO 105,17,105
LU 17,105
M 10,61,1
s_mov_b32 m0, s38
RA 236,1,18432
HS 24,17
Z 11,72,0
LL 16,62
LL 109,63
M 11,73,1
LA 16
W1 25,1024
M 11,74,2
LC_ 109
ST 124
M 11,75,3
DW 0
v_add_f32_dpp v14, v14, v14 row_half_mirror row_mask:0xf bank_mask:0xf
M 10,62,2
WL 4
LO 108,18,108
LU 18,108
M 10,63,3
s_add_u32 s37, s37, 32768
RA 216,1,19456
HS 24,18
M 9,50,2
s_cmp_eq_u32 s37, s75
LL 17,64
LL 112,65
M 9,51,3
s_cselect_b32 s37, s74, s37
LA 17
DW 1024
M 8,38,2
LC_ 112
v_cvt_f64_f32 v[14:15], v14
M 8,39,3
s_mov_b64 exec, s[54:55]
SP
s_mov_b64 exec, -1
v_add_u32 v4, s39, v4
M 7,26,2
WL 3
LO 109,16,109
LU 16,109
M 7,27,3
RA 220,1,20480
HS 24,16
M 6,14,2
LL 18,66
LL 113,67
M 6,15,3
LA 18
DW 2048
M 6,16,4
LC_ 113
M 6,17,5
M 7,28,4
WL 3
LO 112,17,112
LU 17,112
M 7,29,5
s_mul_i32 s53, s51, 24
RA 224,1,21504
HS 24,17
M 8,40,4
s_bitcmp1_b32 s51, 0
LL 16,68
LL 116,69
M 8,41,5
s_addc_u32 s53, s53, 0
LA 16
M 9,52,4
s_lshl_b32 s53, s53, 11
LC_ 116
M 9,53,5
s_bitcmp1_b64 s[90:91], s51
M 10,64,4
WL 3
LO 113,18,113
LU 18,113
M 10,65,5
s_cselect_b64 s[44:45], s[84:85], s[82:83]
RA 228,1,22528
HS 24,18
M 11,76,4
LL 17,70
LL 117,71
M 11,77,5
s_add_u32 s44, s44, s53
LA 17
M 11,78,0
s_addc_u32 s45, s45, 0
LC_ 117
M 11,79,1
s_add_u32 s51, s51, %[d]
M 10,66,0
WL 3
LO 116,16,116
LU 16,116
M 10,67,1
s_cmp_eq_u32 s51, %[wrap]
RA 232,1,23552
HS 24,16
M 9,54,0
s_cselect_b32 s51, %[rst], s51
LL 18,72
LL 120,73
M 9,55,1
s_add_u32 s40, s40, 3072
LA 18
M 8,42,0
s_addc_u32 s41, s41, 0
LC_ 120
s_add_u32 m0, s38, 3072
M 8,43,1
DW 0
M 7,30,0
WL 3
LO 117,17,117
LU 17,117
M 7,31,1
RA 236,1,24576
HS 24,17
M 6,18,0
LL 16,74
LL 121,75
M 6,19,1
LA 16
DW 1024
M 6,20,2
LC_ 121
DW 2048
M 6,21,3
s_lshl_b32 s53, s52, 8
v_add_u32 v1, s37, v0
M 7,32,2
WL 3
LO 120,18,120
LU 18,120
M 7,33,3
s_add_u32 s48, %[alo], s53
RA 216,1,0
HS 24,18
M 8,44,2
s_addc_u32 s49, %[ahi], 0
LL 17,76
LL 124,77
M 8,45,3
s_add_u32 s52, s52, %[d]
LA 17
v_and_b32 v22, 0xfffffcff, v1
M 9,56,2
s_cmp_eq_u32 s52, %[wrap]
LC_ 124
M 9,57,3
s_cselect_b32 s52, %[rst], s52
DQ 3072
M 10,68,2
WL 3
LO 121,16,121
LU 16,121
M 10,69,3
s_add_u32 s50, s50, %[d]
RA 220,1,1024
HS 24,16
M 11,80,2
LL 18,78
LL 125,79
M 11,81,3
s_cmp_eq_u32 s50, %[wrap]
LA 18
RV 252,22,5120
M 6,22,4
s_cselect_b32 s50, %[rst], s50
LC_ 125
M 6,23,5
WV 27
v_add_u32 v19, 1, v19
M 7,34,4
WL 4
LO 124,17,124
LU 17,124
M 7,35,5
s_add_u32 s38, s38, 32768
RA 224,1,2048
HS 24,17
M 8,46,4
s_cmp_eq_u32 s38, s77
LL 16,80
LL 106,81
M 8,47,5
s_cselect_b32 s38, s76, s38
LA 16
FP
M 9,58,4
s_sub_u32 s39, 0, s39
LC_ 106
M 9,59,5
M 10,70,4
WL 4
LO 125,18,125
LU 18,125
M 10,71,5
RA 228,1,3072
HS 24,18
M 11,82,4
LL 17,82
LL 107,83
M 11,83,5
s_mov_b32 s66, 28
1:
LA 17
HX 24
Z 0,12,0
LC_ 107
M 0,13,1
Z 1,24,0
WL 3
LO 16,16,106
LU 106,16
M 1,25,1
RA 232,1,4096
HZ 25,106
Z 2,36,0
LL 18,84
LL 110,85
M 2,37,1
LA 18
PK 106,16,104,56
Z 3,48,0
LC_ 110
PK 104,16,104,97
M 3,49,1
Z 4,60,0
WL 3
LO 17,17,107
LU 107,17
M 4,61,1
s_add_u32 s42, s44, s60
RA 236,1,6144
HS 25,107
Z 5,72,0
LL 16,86
LL 111,87
M 5,73,1
s_addc_u32 s43, s45, 0
LA 16
PK 107,17,105,56
M 5,74,2
LC_ 111
PK 105,17,105,97
M 5,75,3
ST 104
M 4,62,2
WL 3
LO 18,18,110
LU 110,18
M 4,63,3
RA 216,1,7168
HS 25,110
M 3,50,2
LL 17,88
LL 114,89
M 3,51,3
LA 17
PK 110,18,108,56
M 2,38,2
LC_ 114
PK 108,18,108,97
M 2,39,3
FR
PO 110
M 1,26,2
WL 4
LO 16,16,111
LU 111,16
M 1,27,3
s_add_u32 s42, s44, s61
RA 220,1,8192
HS 25,111
M 0,14,2
LL 18,90
LL 115,91
M 0,15,3
s_addc_u32 s43, s45, 0
LA 18
PK 111,16,109,56
M 0,16,4
LC_ 115
PK 109,16,109,97
M 0,17,5
PO 111
M 1,28,4
WL 4
LO 17,17,114
LU 114,17
M 1,29,5
RA 224,1,9216
HS 25,114
M 2,40,4
LL 16,92
LL 118,93
M 2,41,5
LA 16
PK 114,17,112,56
M 3,52,4
LC_ 118
PK 112,17,112,97
M 3,53,5
ST 108
M 4,64,4
WL 3
LO 18,18,115
LU 115,18
)"
R"(M 4,65,5
s_add_u32 s42, s44, s62
RA 228,1,10240
HS 25,115
M 5,76,4
LL 17,94
LL 119,95
M 5,77,5
s_addc_u32 s43, s45, 0
LA 17
PK 115,18,113,56
M 5,78,0
LC_ 119
PK 113,18,113,97
M 5,79,1
ST 112
M 4,66,0
WL 3
LO 16,16,118
LU 118,16
M 4,67,1
RA 232,1,11264
HS 25,118
M 3,54,0
LL 18,96
LL 122,97
M 3,55,1
LA 18
PK 118,16,116,56
v_cmp_gt_u32 vcc, v19, v15
M 2,42,0
LC_ 122
PK 116,16,116,97
M 2,43,1
RV 252,22,5392
PO 118
M 1,30,0
WL 4
LO 17,17,119
LU 119,17
M 1,31,1
s_cbranch_vccnz .Lk1s0
.Lk1r0:
RA 236,1,12288
HS 25,119
M 0,18,0
LL 16,98
LL 123,99
M 0,19,1
s_add_u32 s42, s44, s63
LA 16
PK 119,17,117,56
M 0,20,2
s_addc_u32 s43, s45, 0
LC_ 123
PK 117,17,117,97
M 0,21,3
W1 24,0
PO 119
M 1,32,2
WL 5
LO 18,18,122
LU 122,18
M 1,33,3
RA 216,1,13312
HS 25,122
M 2,44,2
LL 17,100
LL 126,101
M 2,45,3
LA 17
PK 122,18,120,56
M 3,56,2
LC_ 126
PK 120,18,120,97
M 3,57,3
ST 116
M 4,68,2
WL 4
LO 16,16,123
LU 123,16
M 4,69,3
s_add_u32 s42, s44, s64
RA 220,1,14336
HS 25,123
M 5,80,2
LL 18,102
LL 127,103
M 5,81,3
s_addc_u32 s43, s45, 0
LA 18
PK 123,16,121,56
M 0,22,4
LC_ 127
PK 121,16,121,97
M 0,23,5
ST 120
M 1,34,4
WL 3
LO 17,17,126
LU 126,17
M 1,35,5
RA 224,1,15360
HS 25,126
M 2,46,4
LL 16,56
LL 104,57
M 2,47,5
LA 16
PK 126,17,124,56
M 3,58,4
LC_ 104
PK 124,17,124,97
M 3,59,5
ds_read_b64 v[14:15], v5 offset:0
PO 126
M 4,70,4
WL 4
LO 18,18,127
LU 127,18
M 4,71,5
s_add_u32 s42, s44, s65
RA 228,1,16384
HS 25,127
M 5,82,4
LL 17,58
LL 105,59
M 5,83,5
s_addc_u32 s43, s45, 0
LA 17
PK 127,18,125,56
Z 6,12,0
LC_ 105
PK 125,18,125,97
M 6,13,1
PO 127
v_subrev_u32 v5, s39, v5
Z 7,24,0
WL 4
LO 104,16,104
LU 16,104
M 7,25,1
RA 232,1,17408
HZ 24,16
Z 8,36,0
WL 4
LL 18,60
LL 108,61
M 8,37,1
s_mul_i32 s53, s50, 25600
LA 18
v_add_f32 v14, v14, v15
Z 9,48,0
s_add_u32 s40, %[wlo], s53
LC_ 108
v_add_f32_dpp v14, v14, v14 quad_perm:[1,0,3,2] row_mask:0xf bank_mask:0xf
M 9,49,1
s_addc_u32 s41, %[whi], 0
Z 10,60,0
WL 3
LO 105,17,105
LU 17,105
M 10,61,1
s_mov_b32 m0, s38
RA 236,1,18432
HS 24,17
Z 11,72,0
LL 16,62
LL 109,63
M 11,73,1
LA 16
ST 124
M 11,74,2
LC_ 109
DW 0
M 11,75,3
v_add_f32_dpp v14, v14, v14 quad_perm:[2,3,0,1] row_mask:0xf bank_mask:0xf
HX 25
M 10,62,2
WL 3
LO 108,18,108
LU 18,108
W1 25,1024
M 10,63,3
s_add_u32 s37, s37, 32768
RA 216,1,19456
HS 24,18
M 9,50,2
s_cmp_eq_u32 s37, s75
LL 17,64
LL 112,65
M 9,51,3
s_cselect_b32 s37, s74, s37
LA 17
DW 1024
M 8,38,2
LC_ 112
v_add_f32_dpp v14, v14, v14 row_half_mirror row_mask:0xf bank_mask:0xf
M 8,39,3
v_cvt_f64_f32 v[14:15], v14
v_add_u32 v4, s39, v4
M 7,26,2
WL 4
LO 109,16,109
LU 16,109
M 7,27,3
RA 220,1,20480
HS 24,16
M 6,14,2
LL 18,66
LL 113,67
M 6,15,3
LA 18
s_mov_b64 exec, s[54:55]
SP
s_mov_b64 exec, -1
M 6,16,4
LC_ 113
DW 2048
M 6,17,5
M 7,28,4
WL 3
LO 112,17,112
LU 17,112
M 7,29,5
s_mul_i32 s53, s51, 24
RA 224,1,21504
HS 24,17
M 8,40,4
s_bitcmp1_b32 s51, 0
LL 16,68
LL 116,69
M 8,41,5
s_addc_u32 s53, s53, 0
LA 16
M 9,52,4
s_lshl_b32 s53, s53, 11
LC_ 116
M 9,53,5
s_bitcmp1_b64 s[90:91], s51
M 10,64,4
WL 3
LO 113,18,113
LU 18,113
M 10,65,5
s_cselect_b64 s[44:45], s[84:85], s[82:83]
RA 228,1,22528
HS 24,18
M 11,76,4
LL 17,70
LL 117,71
M 11,77,5
s_add_u32 s44, s44, s53
LA 17
M 11,78,0
s_addc_u32 s45, s45, 0
LC_ 117
M 11,79,1
s_add_u32 s51, s51, %[d]
M 10,66,0
WL 3
LO 116,16,116
LU 16,116
M 10,67,1
s_cmp_eq_u32 s51, %[wrap]
RA 232,1,23552
HS 24,16
M 9,54,0
s_cselect_b32 s51, %[rst], s51
LL 18,72
LL 120,73
M 9,55,1
s_add_u32 s40, s40, 3072
LA 18
M 8,42,0
s_addc_u32 s41, s41, 0
LC_ 120
s_add_u32 m0, s38, 3072
M 8,43,1
DW 0
M 7,30,0
WL 3
LO 117,17,117
LU 17,117
M 7,31,1
RA 236,1,24576
HS 24,17
M 6,18,0
LL 16,74
LL 121,75
M 6,19,1
LA 16
DW 1024
M 6,20,2
LC_ 121
DW 2048
M 6,21,3
s_lshl_b32 s53, s52, 8
v_add_u32 v1, s37, v0
M 7,32,2
WL 3
LO 120,18,120
LU 18,120
M 7,33,3
s_add_u32 s48, %[alo], s53
RA 216,1,0
HS 24,18
M 8,44,2
s_addc_u32 s49, %[ahi], 0
LL 17,76
LL 124,77
M 8,45,3
s_add_u32 s52, s52, %[d]
LA 17
v_and_b32 v22, 0xfffffcff, v1
M 9,56,2
s_cmp_eq_u32 s52, %[wrap]
LC_ 124
M 9,57,3
s_cselect_b32 s52, %[rst], s52
DQ 3072
M 10,68,2
WL 3
LO 121,16,121
LU 16,121
M 10,69,3
s_add_u32 s50, s50, %[d]
RA 220,1,1024
HS 24,16
M 11,80,2
LL 18,78
LL 125,79
M 11,81,3
s_cmp_eq_u32 s50, %[wrap]
LA 18
RV 252,22,5120
M 6,22,4
s_cselect_b32 s50, %[rst], s50
LC_ 125
M 6,23,5
WV 28
v_add_u32 v19, 1, v19
M 7,34,4
WL 4
LO 124,17,124
LU 17,124
M 7,35,5
s_add_u32 s38, s38, 32768
RA 224,1,2048
HS 24,17
M 8,46,4
s_cmp_eq_u32 s38, s77
LL 16,80
LL 106,81
M 8,47,5
s_cselect_b32 s38, s76, s38
LA 16
FP
M 9,58,4
s_sub_u32 s39, 0, s39
LC_ 106
M 9,59,5
M 10,70,4
WL 4
LO 125,18,125
LU 18,125
M 10,71,5
RA 228,1,3072
HS 24,18
M 11,82,4
s_sub_u32 s66, s66, 1
LL 17,82
LL 107,83
M 11,83,5
s_cbranch_scc0 1b
7:
LA 17
HX 24
Z 0,12,0
LC_ 107
M 0,13,1
Z 1,24,0
WL 3
LO 16,16,106
LU 106,16
M 1,25,1
RA 232,1,4096
HZ 25,106
Z 2,36,0
LL 18,84
LL 110,85
M 2,37,1
LA 18
PK 106,16,104,56
Z 3,48,0
LC_ 110
PK 104,16,104,97
M 3,49,1
Z 4,60,0
WL 3
)"
R"(LO 17,17,107
LU 107,17
M 4,61,1
s_add_u32 s42, s44, s60
RA 236,1,6144
HS 25,107
Z 5,72,0
LL 16,86
LL 111,87
M 5,73,1
s_addc_u32 s43, s45, 0
LA 16
PK 107,17,105,56
M 5,74,2
LC_ 111
PK 105,17,105,97
M 5,75,3
ST 104
M 4,62,2
WL 3
LO 18,18,110
LU 110,18
M 4,63,3
RA 216,1,7168
HS 25,110
M 3,50,2
LL 17,88
LL 114,89
M 3,51,3
LA 17
PK 110,18,108,56
M 2,38,2
LC_ 114
PK 108,18,108,97
M 2,39,3
FR
PO 110
M 1,26,2
WL 4
LO 16,16,111
LU 111,16
M 1,27,3
s_add_u32 s42, s44, s61
RA 220,1,8192
HS 25,111
M 0,14,2
LL 18,90
LL 115,91
M 0,15,3
s_addc_u32 s43, s45, 0
LA 18
PK 111,16,109,56
M 0,16,4
s_mov_b32 m0, %[pm]
LC_ 115
PK 109,16,109,97
M 0,17,5
DF
PO 111
M 1,28,4
WL 4
LO 17,17,114
LU 114,17
M 1,29,5
RA 224,1,9216
HS 25,114
M 2,40,4
LL 16,92
LL 118,93
M 2,41,5
LA 16
PK 114,17,112,56
M 3,52,4
LC_ 118
PK 112,17,112,97
M 3,53,5
ST 108
M 4,64,4
WL 3
LO 18,18,115
LU 115,18
M 4,65,5
s_add_u32 s42, s44, s62
RA 228,1,10240
HS 25,115
M 5,76,4
LL 17,94
LL 119,95
M 5,77,5
s_addc_u32 s43, s45, 0
LA 17
PK 115,18,113,56
M 5,78,0
LC_ 119
PK 113,18,113,97
M 5,79,1
ST 112
M 4,66,0
WL 3
LO 16,16,118
LU 118,16
M 4,67,1
RA 232,1,11264
HS 25,118
M 3,54,0
LL 18,96
LL 122,97
M 3,55,1
LA 18
PK 118,16,116,56
v_cmp_gt_u32 vcc, v19, v15
M 2,42,0
LC_ 122
PK 116,16,116,97
M 2,43,1
RV 252,22,5392
PO 118
M 1,30,0
WL 4
LO 17,17,119
LU 119,17
M 1,31,1
s_cbranch_vccnz .Lk1s1
.Lk1r1:
RA 236,1,12288
HS 25,119
M 0,18,0
LL 16,98
LL 123,99
M 0,19,1
s_add_u32 s42, s44, s63
LA 16
PK 119,17,117,56
M 0,20,2
s_addc_u32 s43, s45, 0
LC_ 123
PK 117,17,117,97
M 0,21,3
W1 24,0
PO 119
M 1,32,2
WL 5
LO 18,18,122
LU 122,18
M 1,33,3
RA 216,1,13312
HS 25,122
M 2,44,2
LL 17,100
LL 126,101
M 2,45,3
LA 17
PK 122,18,120,56
M 3,56,2
LC_ 126
PK 120,18,120,97
M 3,57,3
ST 116
M 4,68,2
WL 4
LO 16,16,123
LU 123,16
M 4,69,3
s_add_u32 s42, s44, s64
RA 220,1,14336
HS 25,123
M 5,80,2
LL 18,102
LL 127,103
M 5,81,3
s_addc_u32 s43, s45, 0
LA 18
PK 123,16,121,56
M 0,22,4
LC_ 127
PK 121,16,121,97
M 0,23,5
ST 120
M 1,34,4
WL 3
LO 17,17,126
LU 126,17
M 1,35,5
RA 224,1,15360
HS 25,126
M 2,46,4
LL 16,56
LL 104,57
M 2,47,5
LA 16
PK 126,17,124,56
M 3,58,4
LC_ 104
PK 124,17,124,97
M 3,59,5
ds_read_b64 v[14:15], v5 offset:0
PO 126
M 4,70,4
WL 4
LO 18,18,127
LU 127,18
M 4,71,5
s_add_u32 s42, s44, s65
RA 228,1,16384
HS 25,127
M 5,82,4
LL 17,58
LL 105,59
M 5,83,5
s_addc_u32 s43, s45, 0
LA 17
PK 127,18,125,56
Z 6,12,0
LC_ 105
PK 125,18,125,97
M 6,13,1
PO 127
v_subrev_u32 v5, s39, v5
Z 7,24,0
WL 4
LO 104,16,104
LU 16,104
M 7,25,1
RA 232,1,17408
HZ 24,16
Z 8,36,0
WL 4
LL 18,60
LL 108,61
M 8,37,1
s_mul_i32 s53, s50, 25600
LA 18
v_add_f32 v14, v14, v15
Z 9,48,0
s_add_u32 s40, %[wlo], s53
LC_ 108
v_add_f32_dpp v14, v14, v14 quad_perm:[1,0,3,2] row_mask:0xf bank_mask:0xf
M 9,49,1
s_addc_u32 s41, %[whi], 0
Z 10,60,0
WL 3
LO 105,17,105
LU 17,105
M 10,61,1
s_mov_b32 m0, s38
RA 236,1,18432
HS 24,17
Z 11,72,0
LL 16,62
LL 109,63
M 11,73,1
LA 16
ST 124
M 11,74,2
LC_ 109
DW 0
M 11,75,3
v_add_f32_dpp v14, v14, v14 quad_perm:[2,3,0,1] row_mask:0xf bank_mask:0xf
HX 25
M 10,62,2
WL 3
LO 108,18,108
LU 18,108
W1 25,1024
M 10,63,3
s_add_u32 s37, s37, 32768
RA 216,1,19456
HS 24,18
M 9,50,2
s_cmp_eq_u32 s37, s75
LL 17,64
LL 112,65
M 9,51,3
s_cselect_b32 s37, s74, s37
LA 17
DW 1024
M 8,38,2
LC_ 112
v_add_f32_dpp v14, v14, v14 row_half_mirror row_mask:0xf bank_mask:0xf
M 8,39,3
v_cvt_f64_f32 v[14:15], v14
v_add_u32 v4, s39, v4
s_mov_b64 exec, 1
AT
s_mov_b64 exec, -1
M 7,26,2
WL 4
LO 109,16,109
LU 16,109
M 7,27,3
RA 220,1,20480
HS 24,16
M 6,14,2
LL 18,66
LL 113,67
M 6,15,3
LA 18
s_mov_b64 exec, s[54:55]
SP
s_mov_b64 exec, -1
M 6,16,4
LC_ 113
DW 2048
M 6,17,5
M 7,28,4
WL 3
LO 112,17,112
LU 17,112
M 7,29,5
s_mul_i32 s53, s51, 24
RA 224,1,21504
HS 24,17
M 8,40,4
s_bitcmp1_b32 s51, 0
LL 16,68
LL 116,69
M 8,41,5
s_addc_u32 s53, s53, 0
LA 16
M 9,52,4
s_lshl_b32 s53, s53, 11
LC_ 116
M 9,53,5
s_bitcmp1_b64 s[90:91], s51
M 10,64,4
WL 3
LO 113,18,113
LU 18,113
M 10,65,5
s_cselect_b64 s[44:45], s[84:85], s[82:83]
RA 228,1,22528
HS 24,18
M 11,76,4
LL 17,70
LL 117,71
M 11,77,5
s_add_u32 s44, s44, s53
LA 17
M 11,78,0
s_addc_u32 s45, s45, 0
LC_ 117
M 11,79,1
s_add_u32 s51, s51, %[d]
M 10,66,0
WL 3
LO 116,16,116
LU 16,116
M 10,67,1
s_cmp_eq_u32 s51, %[wrap]
RA 232,1,23552
HS 24,16
M 9,54,0
s_cselect_b32 s51, %[rst], s51
LL 18,72
LL 120,73
M 9,55,1
s_add_u32 s40, s40, 3072
LA 18
M 8,42,0
s_addc_u32 s41, s41, 0
LC_ 120
s_add_u32 m0, s38, 3072
M 8,43,1
DW 0
M 7,30,0
WL 3
LO 117,17,117
LU 17,117
M 7,31,1
RA 236,1,24576
HS 24,17
M 6,18,0
LL 16,74
LL 121,75
M 6,19,1
LA 16
DW 1024
M 6,20,2
LC_ 121
DW 2048
M 6,21,3
s_lshl_b32 s53, s52, 8
v_add_u32 v1, s37, v0
M 7,32,2
WL 3
LO 120,18,120
LU 18,120
M 7,33,3
s_add_u32 s48, %[alo], s53
RA 216,1,0
HS 24,18
M 8,44,2
s_addc_u32 s49, %[ahi], 0
LL 17,76
LL 124,77
M 8,45,3
s_add_u32 s52, s52, %[d]
LA 17
v_and_b32 v22, 0xfffffcff, v1
M 9,56,2
)"
R"(s_cmp_eq_u32 s52, %[wrap]
LC_ 124
M 9,57,3
s_cselect_b32 s52, %[rst], s52
DQ 3072
M 10,68,2
WL 3
LO 121,16,121
LU 16,121
M 10,69,3
s_add_u32 s50, s50, %[d]
RA 220,1,1024
HS 24,16
M 11,80,2
LL 18,78
LL 125,79
M 11,81,3
s_cmp_eq_u32 s50, %[wrap]
LA 18
RV 252,22,5120
M 6,22,4
s_cselect_b32 s50, %[rst], s50
LC_ 125
M 6,23,5
WV 30
v_add_u32 v19, 1, v19
M 7,34,4
WL 4
LO 124,17,124
LU 17,124
M 7,35,5
s_add_u32 s38, s38, 32768
RA 224,1,2048
HS 24,17
M 8,46,4
s_cmp_eq_u32 s38, s77
LL 16,80
LL 106,81
M 8,47,5
s_cselect_b32 s38, s76, s38
LA 16
FP
M 9,58,4
s_sub_u32 s39, 0, s39
LC_ 106
M 9,59,5
M 10,70,4
WL 4
LO 125,18,125
LU 18,125
M 10,71,5
RA 228,1,3072
HS 24,18
M 11,82,4
LL 17,82
LL 107,83
M 11,83,5
LA 17
HX 24
Z 0,12,0
LC_ 107
M 0,13,1
Z 1,24,0
WL 3
LO 16,16,106
LU 106,16
M 1,25,1
RA 232,1,4096
HZ 25,106
Z 2,36,0
LL 18,84
LL 110,85
M 2,37,1
LA 18
PK 106,16,104,56
Z 3,48,0
LC_ 110
PK 104,16,104,97
M 3,49,1
Z 4,60,0
WL 3
LO 17,17,107
LU 107,17
M 4,61,1
s_add_u32 s42, s44, s60
RA 236,1,6144
HS 25,107
Z 5,72,0
LL 16,86
LL 111,87
M 5,73,1
s_addc_u32 s43, s45, 0
LA 16
PK 107,17,105,56
M 5,74,2
LC_ 111
PK 105,17,105,97
M 5,75,3
ST 104
M 4,62,2
WL 3
LO 18,18,110
LU 110,18
M 4,63,3
RA 216,1,7168
HS 25,110
M 3,50,2
LL 17,88
LL 114,89
M 3,51,3
LA 17
PK 110,18,108,56
M 2,38,2
LC_ 114
PK 108,18,108,97
M 2,39,3
FR
PO 110
M 1,26,2
WL 4
LO 16,16,111
LU 111,16
M 1,27,3
s_add_u32 s42, s44, s61
RA 220,1,8192
HS 25,111
M 0,14,2
LL 18,90
LL 115,91
M 0,15,3
s_addc_u32 s43, s45, 0
LA 18
PK 111,16,109,56
M 0,16,4
LC_ 115
PK 109,16,109,97
M 0,17,5
PO 111
M 1,28,4
WL 4
LO 17,17,114
LU 114,17
M 1,29,5
RA 224,1,9216
HS 25,114
M 2,40,4
LL 16,92
LL 118,93
M 2,41,5
LA 16
PK 114,17,112,56
M 3,52,4
LC_ 118
PK 112,17,112,97
M 3,53,5
ST 108
M 4,64,4
WL 3
LO 18,18,115
LU 115,18
M 4,65,5
s_add_u32 s42, s44, s62
RA 228,1,10240
HS 25,115
M 5,76,4
LL 17,94
LL 119,95
M 5,77,5
s_addc_u32 s43, s45, 0
LA 17
PK 115,18,113,56
M 5,78,0
LC_ 119
PK 113,18,113,97
M 5,79,1
ST 112
M 4,66,0
WL 3
LO 16,16,118
LU 118,16
M 4,67,1
RA 232,1,11264
HS 25,118
M 3,54,0
LL 18,96
LL 122,97
M 3,55,1
LA 18
PK 118,16,116,56
v_cmp_gt_u32 vcc, v19, v15
M 2,42,0
LC_ 122
PK 116,16,116,97
M 2,43,1
RV 252,22,5392
PO 118
M 1,30,0
WL 4
LO 17,17,119
LU 119,17
M 1,31,1
s_cbranch_vccnz .Lk1s2
.Lk1r2:
RA 236,1,12288
HS 25,119
M 0,18,0
LL 16,98
LL 123,99
M 0,19,1
s_add_u32 s42, s44, s63
LA 16
PK 119,17,117,56
M 0,20,2
s_addc_u32 s43, s45, 0
LC_ 123
PK 117,17,117,97
M 0,21,3
W1 24,0
PO 119
M 1,32,2
WL 5
LO 18,18,122
LU 122,18
M 1,33,3
RA 216,1,13312
HS 25,122
M 2,44,2
LL 17,100
LL 126,101
M 2,45,3
LA 17
PK 122,18,120,56
M 3,56,2
LC_ 126
PK 120,18,120,97
M 3,57,3
ST 116
M 4,68,2
WL 4
LO 16,16,123
LU 123,16
M 4,69,3
s_add_u32 s42, s44, s64
RA 220,1,14336
HS 25,123
M 5,80,2
LL 18,102
LL 127,103
M 5,81,3
s_addc_u32 s43, s45, 0
LA 18
PK 123,16,121,56
M 0,22,4
LC_ 127
PK 121,16,121,97
M 0,23,5
ST 120
M 1,34,4
WL 3
LO 17,17,126
LU 126,17
M 1,35,5
RA 224,1,15360
HS 25,126
M 2,46,4
LL 16,56
LL 104,57
M 2,47,5
LA 16
PK 126,17,124,56
M 3,58,4
LC_ 104
PK 124,17,124,97
M 3,59,5
ds_read_b64 v[14:15], v5 offset:0
PO 126
M 4,70,4
WL 4
LO 18,18,127
LU 127,18
M 4,71,5
s_add_u32 s42, s44, s65
RA 228,1,16384
HS 25,127
M 5,82,4
LL 17,58
LL 105,59
M 5,83,5
s_addc_u32 s43, s45, 0
LA 17
PK 127,18,125,56
Z 6,12,0
LC_ 105
PK 125,18,125,97
M 6,13,1
PO 127
v_subrev_u32 v5, s39, v5
Z 7,24,0
WL 4
LO 104,16,104
LU 16,104
M 7,25,1
RA 232,1,17408
HZ 24,16
Z 8,36,0
WL 4
LL 18,60
LL 108,61
M 8,37,1
s_mul_i32 s53, s50, 25600
LA 18
v_add_f32 v14, v14, v15
Z 9,48,0
s_add_u32 s40, %[wlo], s53
LC_ 108
v_add_f32_dpp v14, v14, v14 quad_perm:[1,0,3,2] row_mask:0xf bank_mask:0xf
M 9,49,1
s_addc_u32 s41, %[whi], 0
Z 10,60,0
WL 3
LO 105,17,105
LU 17,105
M 10,61,1
s_mov_b32 m0, s38
RA 236,1,18432
HS 24,17
Z 11,72,0
LL 16,62
LL 109,63
M 11,73,1
LA 16
ST 124
M 11,74,2
LC_ 109
DW 0
M 11,75,3
WV 13
v_add_f32_dpp v14, v14, v14 quad_perm:[2,3,0,1] row_mask:0xf bank_mask:0xf
HX 25
M 10,62,2
WL 3
LO 108,18,108
LU 18,108
W1 25,1024
M 10,63,3
s_add_u32 s37, s37, 32768
RA 216,1,19456
HS 24,18
v_readfirstlane_b32 s72, v11
M 9,50,2
s_cmp_eq_u32 s37, s75
LL 17,64
LL 112,65
v_mov_b32 v12, s72
M 9,51,3
s_cselect_b32 s37, s74, s37
LA 17
W2 12,12,16
M 8,38,2
LC_ 112
DW 1024
M 8,39,3
v_add_f32_dpp v14, v14, v14 row_half_mirror row_mask:0xf bank_mask:0xf
v_add_u32 v4, s39, v4
M 7,26,2
WL 5
LO 109,16,109
LU 16,109
M 7,27,3
RA 220,1,20480
HS 24,16
M 6,14,2
LL 18,66
LL 113,67
M 6,15,3
LA 18
DW 2048
M 6,16,4
LC_ 113
v_cvt_f64_f32 v[14:15], v14
M 6,17,5
s_mov_b64 exec, s[54:55]
SP
s_mov_b64 exec, -1
M 7,28,4
WL 3
LO 112,17,112
LU 17,112
M 7,29,5
s_mul_i32 s53, s51, 24
RA 224,1,21504
HS 24,17
M 8,40,4
s_bitcmp1_b32 s51, 0
LL 16,68
LL 116,69
M 8,41,5
s_addc_u32 s53, s53, 0
LA 16
M 9,52,4
)"
R"(s_lshl_b32 s53, s53, 11
LC_ 116
M 9,53,5
s_bitcmp1_b64 s[90:91], s51
M 10,64,4
WL 3
LO 113,18,113
LU 18,113
M 10,65,5
s_cselect_b64 s[44:45], s[84:85], s[82:83]
RA 228,1,22528
HS 24,18
M 11,76,4
LL 17,70
LL 117,71
M 11,77,5
s_add_u32 s44, s44, s53
LA 17
M 11,78,0
s_addc_u32 s45, s45, 0
LC_ 117
M 11,79,1
s_add_u32 s51, s51, %[d]
M 10,66,0
WL 3
LO 116,16,116
LU 16,116
M 10,67,1
s_cmp_eq_u32 s51, %[wrap]
RA 232,1,23552
HS 24,16
M 9,54,0
s_cselect_b32 s51, %[rst], s51
LL 18,72
LL 120,73
M 9,55,1
s_add_u32 s40, s40, 3072
LA 18
M 8,42,0
s_addc_u32 s41, s41, 0
LC_ 120
s_add_u32 m0, s38, 3072
M 8,43,1
DW 0
M 7,30,0
WL 3
LO 117,17,117
LU 17,117
M 7,31,1
RA 236,1,24576
HS 24,17
M 6,18,0
LL 16,74
LL 121,75
M 6,19,1
LA 16
DW 1024
M 6,20,2
LC_ 121
DW 2048
M 6,21,3
s_lshl_b32 s53, s52, 8
v_add_u32 v1, s37, v0
M 7,32,2
WL 3
LO 120,18,120
LU 18,120
M 7,33,3
s_add_u32 s48, %[alo], s53
RA 216,1,0
HS 24,18
M 8,44,2
s_addc_u32 s49, %[ahi], 0
LL 17,76
LL 124,77
M 8,45,3
s_add_u32 s52, s52, %[d]
LA 17
v_and_b32 v22, 0xfffffcff, v1
M 9,56,2
s_cmp_eq_u32 s52, %[wrap]
LC_ 124
M 9,57,3
s_cselect_b32 s52, %[rst], s52
DQ 3072
M 10,68,2
WL 3
LO 121,16,121
LU 16,121
M 10,69,3
s_add_u32 s50, s50, %[d]
RA 220,1,1024
HS 24,16
M 11,80,2
LL 18,78
LL 125,79
M 11,81,3
s_cmp_eq_u32 s50, %[wrap]
LA 18
RV 252,22,5120
M 6,22,4
s_cselect_b32 s50, %[rst], s50
LC_ 125
M 6,23,5
v_add_u32 v19, 1, v19
M 7,34,4
WL 4
LO 124,17,124
LU 17,124
M 7,35,5
s_add_u32 s38, s38, 32768
RA 224,1,2048
HS 24,17
M 8,46,4
s_cmp_eq_u32 s38, s77
LL 16,80
LL 106,81
M 8,47,5
s_cselect_b32 s38, s76, s38
LA 16
FP
M 9,58,4
s_sub_u32 s39, 0, s39
LC_ 106
M 9,59,5
M 10,70,4
WL 4
LO 125,18,125
LU 18,125
M 10,71,5
RA 228,1,3072
HS 24,18
M 11,82,4
LL 17,82
LL 107,83
M 11,83,5
LA 17
HX 24
Z 0,12,0
LC_ 107
M 0,13,1
Z 1,24,0
WL 3
LO 16,16,106
LU 106,16
M 1,25,1
RA 232,1,4096
HZ 25,106
Z 2,36,0
LL 18,84
LL 110,85
M 2,37,1
LA 18
PK 106,16,104,56
Z 3,48,0
LC_ 110
PK 104,16,104,97
M 3,49,1
Z 4,60,0
WL 3
LO 17,17,107
LU 107,17
M 4,61,1
s_add_u32 s42, s44, s60
RA 236,1,6144
HS 25,107
Z 5,72,0
LL 16,86
LL 111,87
M 5,73,1
s_addc_u32 s43, s45, 0
LA 16
PK 107,17,105,56
M 5,74,2
LC_ 111
PK 105,17,105,97
M 5,75,3
ST 104
M 4,62,2
WL 3
LO 18,18,110
LU 110,18
M 4,63,3
RA 216,1,7168
HS 25,110
M 3,50,2
LL 17,88
LL 114,89
M 3,51,3
LA 17
PK 110,18,108,56
M 2,38,2
LC_ 114
PK 108,18,108,97
M 2,39,3
FR
PO 110
M 1,26,2
WL 4
LO 16,16,111
LU 111,16
M 1,27,3
s_add_u32 s42, s44, s61
RA 220,1,8192
HS 25,111
M 0,14,2
LL 18,90
LL 115,91
M 0,15,3
s_addc_u32 s43, s45, 0
LA 18
PK 111,16,109,56
M 0,16,4
LC_ 115
PK 109,16,109,97
M 0,17,5
PO 111
M 1,28,4
WL 4
LO 17,17,114
LU 114,17
M 1,29,5
RA 224,1,9216
HS 25,114
M 2,40,4
LL 16,92
LL 118,93
M 2,41,5
LA 16
PK 114,17,112,56
M 3,52,4
LC_ 118
PK 112,17,112,97
M 3,53,5
ST 108
M 4,64,4
WL 3
LO 18,18,115
LU 115,18
M 4,65,5
s_add_u32 s42, s44, s62
RA 228,1,10240
HS 25,115
M 5,76,4
LL 17,94
LL 119,95
M 5,77,5
s_addc_u32 s43, s45, 0
LA 17
PK 115,18,113,56
M 5,78,0
LC_ 119
PK 113,18,113,97
M 5,79,1
ST 112
M 4,66,0
WL 3
LO 16,16,118
LU 118,16
M 4,67,1
RA 232,1,11264
HS 25,118
M 3,54,0
LL 18,96
LL 122,97
M 3,55,1
LA 18
PK 118,16,116,56
v_cmp_gt_u32 vcc, v19, v15
M 2,42,0
LC_ 122
PK 116,16,116,97
M 2,43,1
RV 252,22,5392
PO 118
M 1,30,0
WL 4
LO 17,17,119
LU 119,17
M 1,31,1
s_cbranch_vccnz .Lk1s3
.Lk1r3:
RA 236,1,12288
HS 25,119
M 0,18,0
LL 16,98
LL 123,99
M 0,19,1
s_add_u32 s42, s44, s63
LA 16
PK 119,17,117,56
M 0,20,2
s_addc_u32 s43, s45, 0
LC_ 123
PK 117,17,117,97
M 0,21,3
ds_read_b32 v20, v5 offset:4096
PO 119
M 1,32,2
WL 5
LO 18,18,122
LU 122,18
M 1,33,3
RA 216,1,13312
HS 25,122
M 2,44,2
LL 17,100
LL 126,101
M 2,45,3
LA 17
PK 122,18,120,56
M 3,56,2
LC_ 126
PK 120,18,120,97
M 3,57,3
M 4,68,2
WL 4
LO 16,16,123
LU 123,16
M 4,69,3
RA 220,1,14336
HS 25,123
M 5,80,2
LL 18,102
LL 127,103
M 5,81,3
LA 18
PK 123,16,121,56
M 0,22,4
LC_ 127
PK 121,16,121,97
W1 24,0
M 0,23,5
M 1,34,4
WL 4
LO 17,17,126
LU 126,17
M 1,35,5
RA 224,1,15360
HS 25,126
M 2,46,4
LL 16,56
LL 104,57
M 2,47,5
LA 16
PK 126,17,124,56
M 3,58,4
LC_ 104
PK 124,17,124,97
M 3,59,5
PO 126
v_readfirstlane_b32 s72, v20
M 4,70,4
WL 4
LO 18,18,127
LU 127,18
M 4,71,5
s_sub_u32 s53, 63, s72
RA 228,1,16384
HS 25,127
M 5,82,4
LL 17,58
LL 105,59
M 5,83,5
s_cmp_eq_u32 %[d], 1
LA 17
PK 127,18,125,56
Z 6,12,0
s_cselect_b32 s72, s72, s53
LC_ 105
PK 125,18,125,97
M 6,13,1
s_and_b32 s72, s72, 63
ds_read_b64 v[14:15], v5 offset:0
PO 127
Z 7,24,0
WL 4
LO 104,16,104
LU 16,104
M 7,25,1
s_add_u32 s53, s72, 128
RA 232,1,17408
HZ 24,16
Z 8,36,0
s_mul_i32 s53, s53, 2359296
LL 18,60
LL 108,61
M 8,37,1
s_add_u32 s86, %[hlo], s53
LA 18
v_subrev_u32 v5, s39, v5
Z 9,48,0
s_addc_u32 s87, %[hhi], 0
LC_ 108
HX 25
M 9,49,1
s_add_u32 s88, s86, 8192
Z 10,60,0
WL 4
LO 105,17,105
)"
R"(LU 17,105
M 10,61,1
s_addc_u32 s89, s87, 0
RA 236,1,18432
HS 24,17
Z 11,72,0
WL 4
LL 16,62
LL 109,63
M 11,73,1
s_and_b32 s92, %[hlo], 0x1fffff
LA 16
W1 25,1024
s_add_u32 s92, s92, s53
s_mov_b32 s93, 3072
ST 116
s_add_u32 s42, s44, s64
s_addc_u32 s43, s45, 0
s_mul_i32 s57, s72, 589824
s_add_u32 s57, s57, %[xs1]
s_lshl_b32 s72, s72, 1
s_add_u32 s72, s72, s96
s_lshl_b32 s53, s72, 6
s_add_u32 s53, s53, 128
s_cmp_lt_u32 %[ca0], 128
s_cselect_b32 s73, s53, %[ca0]
ST 120
v_mov_b32 v9, s73
v_mov_b32 v11, s92
M 11,74,2
s_add_u32 s42, s44, s65
LC_ 109
v_add_f32 v14, v14, v15
s_addc_u32 s43, s45, 0
v_mad_u32_u24 v11, v0, s93, v11
M 11,75,3
v_add_f32_dpp v14, v14, v14 quad_perm:[1,0,3,2] row_mask:0xf bank_mask:0xf
v_add_u32 v4, s39, v4
M 10,62,2
WL 4
LO 108,18,108
LU 18,108
M 10,63,3
RA 216,1,19456
HS 24,18
v_lshrrev_b32 v11, 21, v11
M 9,50,2
LL 17,64
LL 112,65
M 9,51,3
LA 17
v_add_f32_dpp v14, v14, v14 quad_perm:[2,3,0,1] row_mask:0xf bank_mask:0xf
M 8,38,2
LC_ 112
v_add_f32_dpp v14, v14, v14 row_half_mirror row_mask:0xf bank_mask:0xf
v_lshl_add_u32 v11, v11, 2, 64
M 8,39,3
s_mul_i32 s53, s50, 25600
v_cvt_f64_f32 v[14:15], v14
M 7,26,2
WL 3
LO 109,16,109
LU 16,109
s_add_u32 s40, %[wlo], s53
s_addc_u32 s41, %[whi], 0
s_mov_b32 m0, s38
M 7,27,3
RA 220,1,20480
HS 24,16
v_writelane_b32 v11, %[tsel], 63
M 6,14,2
LL 18,66
LL 113,67
ST 124
M 6,15,3
s_sub_u32 s53, s86, %[hlo]
LA 18
s_mov_b64 exec, s[54:55]
SP
s_mov_b64 exec, -1
M 6,16,4
s_lshr_b32 s53, s53, 2
LC_ 113
s_and_b32 s92, %[xlo], 0x1fffff
s_add_u32 s53, s53, s92
s_add_u32 s53, s53, 0x20048000
s_lshr_b32 s53, s53, 21
s_lshl_b32 s92, s53, 2
M 6,17,5
DW 0
v_writelane_b32 v11, s92, 48
M 7,28,4
WL 3
LO 112,17,112
LU 17,112
M 7,29,5
s_add_u32 s37, s37, 32768
RA 224,1,21504
HS 24,17
M 8,40,4
s_cmp_eq_u32 s37, s75
LL 16,68
LL 116,69
M 8,41,5
s_cselect_b32 s37, s74, s37
LA 16
global_load_dword v11, v11, s[68:69] offset:-2048
M 9,52,4
LC_ 116
DW 1024
M 9,53,5
DW 2048
M 10,64,4
WL 3
LO 113,18,113
LU 18,113
M 10,65,5
s_mul_i32 s53, s51, 24
RA 228,1,22528
HS 24,18
M 11,76,4
LL 17,70
LL 117,71
M 11,77,5
s_bitcmp1_b32 s51, 0
LA 17
M 11,78,0
s_addc_u32 s53, s53, 0
LC_ 117
M 11,79,1
s_lshl_b32 s53, s53, 11
M 10,66,0
WL 3
LO 116,16,116
LU 16,116
M 10,67,1
s_bitcmp1_b64 s[90:91], s51
RA 232,1,23552
HS 24,16
M 9,54,0
s_cselect_b64 s[44:45], s[84:85], s[82:83]
LL 18,72
LL 120,73
M 9,55,1
s_add_u32 s44, s44, s53
LA 18
s_addc_u32 s45, s45, 0
s_add_u32 s51, s51, %[d]
s_cmp_eq_u32 s51, %[wrap]
s_cselect_b32 s51, %[rst], s51
M 8,42,0
s_add_u32 s40, s40, 3072
LC_ 120
s_addc_u32 s41, s41, 0
s_add_u32 m0, s38, 3072
M 8,43,1
DW 0
M 7,30,0
WL 3
LO 117,17,117
LU 17,117
M 7,31,1
RA 236,1,24576
HS 24,17
M 6,18,0
LL 16,74
LL 121,75
M 6,19,1
LA 16
DW 1024
M 6,20,2
LC_ 121
DW 2048
M 6,21,3
s_lshl_b32 s53, s52, 8
v_add_u32 v1, s37, v0
M 7,32,2
WL 3
LO 120,18,120
LU 18,120
M 7,33,3
s_add_u32 s48, %[alo], s53
RA 216,1,0
HS 24,18
M 8,44,2
s_addc_u32 s49, %[ahi], 0
LL 17,76
LL 124,77
M 8,45,3
s_add_u32 s52, s52, %[d]
LA 17
v_and_b32 v22, 0xfffffcff, v1
M 9,56,2
s_cmp_eq_u32 s52, %[wrap]
LC_ 124
M 9,57,3
s_cselect_b32 s52, %[rst], s52
DQ 3072
M 10,68,2
WL 3
LO 121,16,121
LU 16,121
M 10,69,3
s_add_u32 s50, s50, %[d]
RA 220,1,1024
HS 24,16
M 11,80,2
LL 18,78
LL 125,79
M 11,81,3
s_cmp_eq_u32 s50, %[wrap]
LA 18
RV 252,22,5120
M 6,22,4
s_cselect_b32 s50, %[rst], s50
LC_ 125
M 6,23,5
WV 29
v_add_u32 v19, 1, v19
M 7,34,4
WL 4
LO 124,17,124
LU 17,124
M 7,35,5
s_add_u32 s38, s38, 32768
RA 224,1,2048
HS 24,17
M 8,46,4
s_cmp_eq_u32 s38, s77
LL 16,80
LL 106,81
M 8,47,5
s_cselect_b32 s38, s76, s38
LA 16
FP
M 9,58,4
s_sub_u32 s39, 0, s39
LC_ 106
M 9,59,5
M 10,70,4
WL 4
LO 125,18,125
LU 18,125
M 10,71,5
RA 228,1,3072
HS 24,18
M 11,82,4
LL 17,82
LL 107,83
M 11,83,5
LA 17
HX 24
Z 0,12,0
LC_ 107
M 0,13,1
Z 1,24,0
WL 3
LO 16,16,106
LU 106,16
M 1,25,1
RA 232,1,4096
HZ 25,106
Z 2,36,0
LL 18,84
LL 110,85
M 2,37,1
LA 18
PK 106,16,104,56
Z 3,48,0
LC_ 110
PK 104,16,104,97
M 3,49,1
Z 4,60,0
WL 3
LO 17,17,107
LU 107,17
M 4,61,1
s_add_u32 s42, s44, s60
RA 236,1,6144
HS 25,107
Z 5,72,0
LL 16,86
LL 111,87
M 5,73,1
s_addc_u32 s43, s45, 0
LA 16
PK 107,17,105,56
M 5,74,2
LC_ 111
PK 105,17,105,97
M 5,75,3
ST 104
M 4,62,2
WL 3
LO 18,18,110
LU 110,18
M 4,63,3
RA 216,1,7168
HS 25,110
M 3,50,2
LL 17,88
LL 114,89
M 3,51,3
LA 17
PK 110,18,108,56
M 2,38,2
LC_ 114
PK 108,18,108,97
M 2,39,3
FR
PO 110
M 1,26,2
WL 4
LO 16,16,111
LU 111,16
M 1,27,3
s_add_u32 s42, s44, s61
RA 220,1,8192
HS 25,111
M 0,14,2
LL 18,90
LL 115,91
M 0,15,3
s_addc_u32 s43, s45, 0
LA 18
PK 111,16,109,56
M 0,16,4
LC_ 115
PK 109,16,109,97
M 0,17,5
PO 111
M 1,28,4
WL 4
LO 17,17,114
LU 114,17
M 1,29,5
RA 224,1,9216
HS 25,114
M 2,40,4
LL 16,92
LL 118,93
M 2,41,5
LA 16
PK 114,17,112,56
M 3,52,4
LC_ 118
PK 112,17,112,97
M 3,53,5
ST 108
M 4,64,4
WL 3
LO 18,18,115
LU 115,18
M 4,65,5
s_add_u32 s42, s44, s62
RA 228,1,10240
HS 25,115
M 5,76,4
LL 17,94
LL 119,95
M 5,77,5
s_addc_u32 s43, s45, 0
LA 17
PK 115,18,113,56
M 5,78,0
LC_ 119
PK 113,18,113,97
M 5,79,1
ST 112
M 4,66,0
WL 3
LO 16,16,118
LU 118,16
M 4,67,1
RA 232,1,11264
HS 25,118
M 3,54,0
LL 18,96
LL 122,97
M 3,55,1
LA 18
PK 118,16,116,56
v_cmp_gt_u32 vcc, v19, v15
M 2,42,0
LC_ 122
PK 116,16,116,97
M 2,43,1
RV 252,22,5392
PO 118
M 1,30,0
WL 4
LO 17,17,119
LU 119,17
M 1,31,1
s_cbranch_vccnz .Lk1s4
.Lk1r4:
RA 236,1,12288
HS 25,119
M 0,18,0
LL 16,98
)"
R"(LL 123,99
M 0,19,1
s_add_u32 s42, s44, s63
LA 16
PK 119,17,117,56
M 0,20,2
s_addc_u32 s43, s45, 0
LC_ 123
PK 117,17,117,97
M 0,21,3
W1 24,0
PO 119
M 1,32,2
WL 5
LO 18,18,122
LU 122,18
M 1,33,3
RA 216,1,13312
HS 25,122
M 2,44,2
LL 17,100
LL 126,101
M 2,45,3
LA 17
PK 122,18,120,56
M 3,56,2
LC_ 126
PK 120,18,120,97
M 3,57,3
ST 116
M 4,68,2
WL 4
LO 16,16,123
LU 123,16
M 4,69,3
s_add_u32 s42, s44, s64
RA 220,1,14336
HS 25,123
M 5,80,2
LL 18,102
LL 127,103
M 5,81,3
s_addc_u32 s43, s45, 0
LA 18
PK 123,16,121,56
M 0,22,4
LC_ 127
PK 121,16,121,97
M 0,23,5
ST 120
M 1,34,4
WL 3
LO 17,17,126
LU 126,17
M 1,35,5
RA 224,1,15360
HS 25,126
M 2,46,4
LL 16,56
LL 104,57
M 2,47,5
LA 16
PK 126,17,124,56
M 3,58,4
LC_ 104
PK 124,17,124,97
M 3,59,5
ds_read_b64 v[14:15], v5 offset:0
PO 126
M 4,70,4
WL 4
LO 18,18,127
LU 127,18
M 4,71,5
s_add_u32 s42, s44, s65
RA 228,1,16384
HS 25,127
M 5,82,4
LL 17,58
LL 105,59
M 5,83,5
s_addc_u32 s43, s45, 0
LA 17
PK 127,18,125,56
Z 6,12,0
LC_ 105
PK 125,18,125,97
M 6,13,1
PO 127
v_subrev_u32 v5, s39, v5
Z 7,24,0
WL 4
LO 104,16,104
LU 16,104
M 7,25,1
RA 232,1,17408
HZ 24,16
Z 8,36,0
WL 4
LL 18,60
LL 108,61
M 8,37,1
s_mul_i32 s53, s50, 25600
LA 18
v_add_f32 v14, v14, v15
Z 9,48,0
s_add_u32 s40, %[wlo], s53
LC_ 108
v_add_f32_dpp v14, v14, v14 quad_perm:[1,0,3,2] row_mask:0xf bank_mask:0xf
M 9,49,1
s_addc_u32 s41, %[whi], 0
Z 10,60,0
WL 3
LO 105,17,105
LU 17,105
M 10,61,1
s_mov_b32 m0, s38
RA 236,1,18432
HS 24,17
Z 11,72,0
LL 16,62
LL 109,63
M 11,73,1
LA 16
ST 124
M 11,74,2
LC_ 109
DW 0
M 11,75,3
v_add_f32_dpp v14, v14, v14 quad_perm:[2,3,0,1] row_mask:0xf bank_mask:0xf
HX 25
M 10,62,2
WL 3
LO 108,18,108
LU 18,108
W1 25,1024
M 10,63,3
s_add_u32 s37, s37, 32768
RA 216,1,19456
HS 24,18
M 9,50,2
s_cmp_eq_u32 s37, s75
LL 17,64
LL 112,65
M 9,51,3
s_cselect_b32 s37, s74, s37
LA 17
DW 1024
M 8,38,2
LC_ 112
v_add_f32_dpp v14, v14, v14 row_half_mirror row_mask:0xf bank_mask:0xf
M 8,39,3
v_cvt_f64_f32 v[14:15], v14
v_add_u32 v4, s39, v4
M 7,26,2
WL 4
LO 109,16,109
LU 16,109
M 7,27,3
RA 220,1,20480
HS 24,16
M 6,14,2
LL 18,66
LL 113,67
M 6,15,3
LA 18
s_mov_b64 exec, s[54:55]
SP
s_mov_b64 exec, -1
M 6,16,4
LC_ 113
DW 2048
M 6,17,5
s_and_b32 s53, %[w], 1
M 7,28,4
WL 3
LO 112,17,112
LU 17,112
M 7,29,5
s_lshl_b32 s53, s53, 10
RA 224,1,21504
HS 24,17
M 8,40,4
s_lshr_b32 s72, %[w], 1
LL 16,68
LL 116,69
M 8,41,5
s_mul_i32 s72, s72, 32768
LA 16
M 9,52,4
s_add_u32 s72, s72, s53
LC_ 116
M 9,53,5
s_add_u32 s72, s72, 128000
M 10,64,4
WL 3
LO 113,18,113
LU 18,113
M 10,65,5
s_movk_i32 s53, 0x78
RA 228,1,22528
HS 24,18
M 11,76,4
LL 17,70
LL 117,71
M 11,77,5
LA 17
v_lshrrev_b32 v12, 5, v0
M 11,78,0
LC_ 117
v_and_b32 v12, 6, v12
M 11,79,1
v_bfe_u32 v12, s53, v12, 2
M 10,66,0
WL 3
LO 116,16,116
LU 16,116
M 10,67,1
RA 232,1,23552
HS 24,16
v_lshrrev_b32 v20, 8, v0
v_xor_b32 v20, v12, v20
v_lshl_add_u32 v20, v20, 4, s72
v_and_b32 v12, 0xf0, v0
v_lshl_add_u32 v20, v12, 2, v20
v_add_u32 v12, s72, v0
M 9,54,0
s_mul_i32 s53, s51, 24
LL 18,72
LL 120,73
M 9,55,1
s_bitcmp1_b32 s51, 0
LA 18
s_addc_u32 s53, s53, 0
s_lshl_b32 s53, s53, 11
s_bitcmp1_b64 s[90:91], s51
s_cselect_b64 s[44:45], s[84:85], s[82:83]
s_add_u32 s44, s44, s53
s_addc_u32 s45, s45, 0
s_add_u32 s51, s51, %[d]
s_cmp_eq_u32 s51, %[wrap]
s_cselect_b32 s51, %[rst], s51
M 8,42,0
s_add_u32 s40, s40, 3072
LC_ 120
s_addc_u32 s41, s41, 0
s_add_u32 m0, s38, 3072
M 8,43,1
DW 0
M 7,30,0
WL 3
LO 117,17,117
LU 17,117
M 7,31,1
RA 236,1,24576
HS 24,17
M 6,18,0
LL 16,74
LL 121,75
M 6,19,1
LA 16
DW 1024
M 6,20,2
LC_ 121
DW 2048
M 6,21,3
s_lshl_b32 s53, s52, 8
v_add_u32 v1, s37, v0
M 7,32,2
WL 3
LO 120,18,120
LU 18,120
M 7,33,3
s_add_u32 s48, %[alo], s53
RA 216,1,0
HS 24,18
M 8,44,2
s_addc_u32 s49, %[ahi], 0
LL 17,76
LL 124,77
M 8,45,3
s_add_u32 s52, s52, %[d]
LA 17
v_and_b32 v22, 0xfffffcff, v1
M 9,56,2
s_cmp_eq_u32 s52, %[wrap]
LC_ 124
M 9,57,3
s_cselect_b32 s52, %[rst], s52
DQ 3072
M 10,68,2
WL 3
LO 121,16,121
LU 16,121
M 10,69,3
s_add_u32 s50, s50, %[d]
RA 220,1,1024
HS 24,16
M 11,80,2
LL 18,78
LL 125,79
M 11,81,3
s_cmp_eq_u32 s50, %[wrap]
LA 18
RV 252,22,5120
M 6,22,4
s_cselect_b32 s50, %[rst], s50
LC_ 125
M 6,23,5
WV 29
v_add_u32 v19, 1, v19
M 7,34,4
WL 4
LO 124,17,124
LU 17,124
M 7,35,5
s_add_u32 s38, s38, 32768
RA 224,1,2048
HS 24,17
M 8,46,4
s_cmp_eq_u32 s38, s77
LL 16,80
LL 106,81
M 8,47,5
s_cselect_b32 s38, s76, s38
LA 16
FP
M 9,58,4
s_sub_u32 s39, 0, s39
LC_ 106
M 9,59,5
M 10,70,4
WL 4
LO 125,18,125
LU 18,125
M 10,71,5
RA 228,1,3072
HS 24,18
M 11,82,4
LL 17,82
LL 107,83
M 11,83,5
LA 17
HX 24
Z 0,12,0
LC_ 107
M 0,13,1
Z 1,24,0
WL 3
LO 16,16,106
LU 106,16
M 1,25,1
RA 232,1,4096
HZ 25,106
Z 2,36,0
LL 18,84
LL 110,85
M 2,37,1
LA 18
PK 106,16,104,56
Z 3,48,0
LC_ 110
PK 104,16,104,97
M 3,49,1
WV 20
v_bcnt_u32_b32 v11, v11, 0
Z 4,60,0
WL 3
LO 17,17,107
LU 107,17
M 4,61,1
RA 236,1,6144
HS 25,107
Z 5,72,0
LL 16,86
LL 111,87
M 5,73,1
LA 16
PK 107,17,105,56
M 5,74,2
LC_ 111
PK 105,17,105,97
M 5,75,3
M 4,62,2
WL 3
LO 18,18,110
LU 110,18
M 4,63,3
RA 216,1,7168
HS 25,110
M 3,50,2
LL 17,88
LL 114,89
M 3,51,3
LA 17
PK 110,18,108,56
M 2,38,2
LC_ 114
PK 108,18,108,97
M 2,39,3
)"
R"(FR
PO 110
v_cmp_lt_u32 s[92:93], 2, v11
M 1,26,2
WL 4
LO 16,16,111
LU 111,16
M 1,27,3
s_nop 0
RA 220,1,8192
HS 25,111
M 0,14,2
LL 18,90
LL 115,91
M 0,15,3
s_add_u32 s42, s44, s60
LA 18
PK 111,16,109,56
s_addc_u32 s43, s45, 0
s_sub_u32 s53, 0, s96
s_xor_b32 s92, s92, s53
s_xor_b32 s93, s93, s53
M 0,16,4
s_mul_i32 s53, %[d], 0
LC_ 115
PK 109,16,109,97
M 0,17,5
s_add_u32 s53, s53, s57
PO 111
M 1,28,4
WL 4
LO 17,17,114
LU 114,17
M 1,29,5
s_add_u32 s46, %[xlo], s53
RA 224,1,9216
HS 25,114
s_addc_u32 s47, %[xhi], 0
s_bitcmp1_b64 s[92:93], 48
s_cselect_b32 s58, 16, 0
s_lshr_b32 s58, 0xfe987610, s58
v_lshrrev_b32 v3, 4, v0
v_and_b32 v3, 12, v3
v_bfe_u32 v3, s58, v3, 4
v_bfe_u32 v11, v0, 4, 2
v_lshl_add_u32 v3, v3, 2, v11
v_mul_u32_u24 v3, 0x300, v3
v_lshrrev_b32 v11, 8, v0
v_lshl_add_u32 v3, v11, 4, v3
M 2,40,4
LL 16,92
LL 118,93
ST 104
M 2,41,5
s_add_u32 s42, s44, s61
LA 16
PK 114,17,112,56
M 3,52,4
s_addc_u32 s43, s45, 0
LC_ 118
PK 112,17,112,97
M 3,53,5
s_mov_b32 m0, %[pm]
M 4,64,4
WL 3
LO 18,18,115
LU 115,18
M 4,65,5
RA 228,1,10240
HS 25,115
M 5,76,4
LL 17,94
LL 119,95
M 5,77,5
LA 17
PK 115,18,113,56
M 5,78,0
LC_ 119
PK 113,18,113,97
M 5,79,1
DF
M 4,66,0
WL 3
LO 16,16,118
LU 118,16
M 4,67,1
RA 232,1,11264
HS 25,118
M 3,54,0
LL 18,96
LL 122,97
M 3,55,1
LA 18
PK 118,16,116,56
v_cmp_gt_u32 vcc, v19, v15
M 2,42,0
LC_ 122
PK 116,16,116,97
M 2,43,1
RV 252,22,5392
PO 118
M 1,30,0
WL 4
LO 17,17,119
LU 119,17
M 1,31,1
s_cbranch_vccnz .Lk1s5
.Lk1r5:
RA 236,1,12288
HS 25,119
M 0,18,0
LL 16,98
LL 123,99
ST 108
XL 0,0
M 0,19,1
s_add_u32 s42, s44, s62
LA 16
PK 119,17,117,56
s_addc_u32 s43, s45, 0
M 0,20,2
LC_ 123
PK 117,17,117,97
M 0,21,3
W1 24,0
PO 119
M 1,32,2
WL 5
LO 18,18,122
LU 122,18
M 1,33,3
RA 216,1,13312
HS 25,122
M 2,44,2
LL 17,100
LL 126,101
ST 112
M 2,45,3
s_add_u32 s42, s44, s63
LA 17
PK 122,18,120,56
M 3,56,2
s_addc_u32 s43, s45, 0
LC_ 126
PK 120,18,120,97
M 3,57,3
M 4,68,2
WL 4
LO 16,16,123
LU 123,16
M 4,69,3
RA 220,1,14336
HS 25,123
M 5,80,2
LL 18,102
LL 127,103
M 5,81,3
LA 18
PK 123,16,121,56
M 0,22,4
LC_ 127
PK 121,16,121,97
M 0,23,5
M 1,34,4
WL 3
LO 17,17,126
LU 126,17
M 1,35,5
RA 224,1,15360
HS 25,126
M 2,46,4
LL 16,56
LL 104,57
M 2,47,5
LA 16
XL 1,64
PK 126,17,124,56
M 3,58,4
LC_ 104
PK 124,17,124,97
M 3,59,5
ST 116
PO 126
M 4,70,4
WL 3
LO 18,18,127
LU 127,18
M 4,71,5
s_add_u32 s42, s44, s64
RA 228,1,16384
HS 25,127
M 5,82,4
LL 17,58
LL 105,59
M 5,83,5
s_addc_u32 s43, s45, 0
LA 17
PK 127,18,125,56
Z 6,12,0
LC_ 105
PK 125,18,125,97
M 6,13,1
ST 120
PO 127
Z 7,24,0
WL 3
LO 104,16,104
LU 16,104
M 7,25,1
s_add_u32 s42, s44, s65
RA 232,1,17408
HZ 24,16
Z 8,36,0
s_addc_u32 s43, s45, 0
LL 18,60
LL 108,61
M 8,37,1
LA 18
ds_read_b64 v[14:15], v5 offset:0
Z 9,48,0
LC_ 108
XL 2,128
M 9,49,1
s_mul_i32 s53, s50, 25600
Z 10,60,0
WL 4
LO 105,17,105
LU 17,105
M 10,61,1
s_add_u32 s40, %[wlo], s53
RA 236,1,18432
HS 24,17
Z 11,72,0
LL 16,62
LL 109,63
M 11,73,1
s_addc_u32 s41, %[whi], 0
LA 16
v_subrev_u32 v5, s39, v5
M 11,74,2
s_mov_b32 m0, s38
LC_ 109
ST 124
M 11,75,3
DW 0
HX 25
M 10,62,2
WL 3
LO 108,18,108
LU 18,108
W1 25,1024
M 10,63,3
s_add_u32 s37, s37, 32768
RA 216,1,19456
HS 24,18
M 9,50,2
s_cmp_eq_u32 s37, s75
LL 17,64
LL 112,65
M 9,51,3
s_cselect_b32 s37, s74, s37
LA 17
XL 3,192
M 8,38,2
LC_ 112
DW 1024
M 8,39,3
v_add_f32 v14, v14, v15
s_nop 1
v_add_f32_dpp v14, v14, v14 quad_perm:[1,0,3,2] row_mask:0xf bank_mask:0xf
v_add_u32 v4, s39, v4
M 7,26,2
WL 4
LO 109,16,109
LU 16,109
M 7,27,3
RA 220,1,20480
HS 24,16
v_add_f32_dpp v14, v14, v14 quad_perm:[2,3,0,1] row_mask:0xf bank_mask:0xf
M 6,14,2
LL 18,66
LL 113,67
M 6,15,3
LA 18
DW 2048
M 6,16,4
LC_ 113
v_add_f32_dpp v14, v14, v14 row_half_mirror row_mask:0xf bank_mask:0xf
M 6,17,5
v_cvt_f64_f32 v[14:15], v14
M 7,28,4
WL 3
LO 112,17,112
LU 17,112
M 7,29,5
s_mul_i32 s53, s51, 24
RA 224,1,21504
HS 24,17
M 8,40,4
s_bitcmp1_b32 s51, 0
LL 16,68
LL 116,69
M 8,41,5
s_addc_u32 s53, s53, 0
LA 16
s_mov_b64 exec, s[54:55]
SP
s_mov_b64 exec, -1
M 9,52,4
s_lshl_b32 s53, s53, 11
LC_ 116
M 9,53,5
s_bitcmp1_b64 s[90:91], s51
M 10,64,4
WL 3
LO 113,18,113
LU 18,113
M 10,65,5
s_cselect_b64 s[44:45], s[84:85], s[82:83]
RA 228,1,22528
HS 24,18
M 11,76,4
LL 17,70
LL 117,71
M 11,77,5
s_add_u32 s44, s44, s53
LA 17
M 11,78,0
s_addc_u32 s45, s45, 0
LC_ 117
M 11,79,1
s_add_u32 s51, s51, %[d]
M 10,66,0
WL 3
LO 116,16,116
LU 16,116
M 10,67,1
s_cmp_eq_u32 s51, %[wrap]
RA 232,1,23552
HS 24,16
M 9,54,0
s_cselect_b32 s51, %[rst], s51
LL 18,72
LL 120,73
M 9,55,1
s_add_u32 s40, s40, 3072
LA 18
M 8,42,0
s_addc_u32 s41, s41, 0
LC_ 120
s_add_u32 m0, s38, 3072
M 8,43,1
DW 0
M 7,30,0
WL 3
LO 117,17,117
LU 17,117
M 7,31,1
RA 236,1,24576
HS 24,17
M 6,18,0
LL 16,74
LL 121,75
M 6,19,1
LA 16
XL 4,256
M 6,20,2
LC_ 121
DW 1024
M 6,21,3
DW 2048
v_add_u32 v1, s37, v0
M 7,32,2
WL 3
LO 120,18,120
LU 18,120
M 7,33,3
s_lshl_b32 s53, s52, 8
RA 216,1,0
HS 24,18
M 8,44,2
s_add_u32 s48, %[alo], s53
LL 17,76
LL 124,77
M 8,45,3
s_addc_u32 s49, %[ahi], 0
LA 17
v_and_b32 v22, 0xfffffcff, v1
M 9,56,2
s_add_u32 s52, s52, %[d]
LC_ 124
M 9,57,3
s_cmp_eq_u32 s52, %[wrap]
M 10,68,2
WL 3
LO 121,16,121
LU 16,121
M 10,69,3
s_cselect_b32 s52, %[rst], s52
RA 220,1,1024
)"
R"(HS 24,16
M 11,80,2
LL 18,78
LL 125,79
M 11,81,3
LA 18
RV 252,22,5120
M 6,22,4
LC_ 125
DQ 3072
M 6,23,5
XL 5,320
M 7,34,4
WL 4
LO 124,17,124
LU 17,124
M 7,35,5
s_add_u32 s50, s50, %[d]
RA 224,1,2048
HS 24,17
M 8,46,4
s_cmp_eq_u32 s50, %[wrap]
LL 16,80
LL 106,81
M 8,47,5
s_cselect_b32 s50, %[rst], s50
LA 16
v_add_u32 v19, 1, v19
M 9,58,4
s_add_u32 s38, s38, 32768
LC_ 106
M 9,59,5
M 10,70,4
WL 3
LO 125,18,125
LU 18,125
M 10,71,5
RA 228,1,3072
HS 24,18
M 11,82,4
LL 17,82
LL 107,83
M 11,83,5
LA 17
HX 24
Z 0,12,0
s_cmp_eq_u32 s38, s77
LC_ 107
M 0,13,1
s_cselect_b32 s38, s76, s38
WV 35
FP
Z 1,24,0
WL 4
LO 16,16,106
LU 106,16
M 1,25,1
s_sub_u32 s39, 0, s39
RA 232,1,4096
HZ 25,106
Z 2,36,0
LL 18,84
LL 110,85
M 2,37,1
LA 18
PK 106,16,104,56
Z 3,48,0
LC_ 110
PK 104,16,104,97
M 3,49,1
XL 6,384
Z 4,60,0
WL 4
LO 17,17,107
LU 107,17
M 4,61,1
s_add_u32 s42, s44, s60
RA 236,1,6144
HS 25,107
Z 5,72,0
LL 16,86
LL 111,87
M 5,73,1
s_addc_u32 s43, s45, 0
LA 16
PK 107,17,105,56
M 5,74,2
LC_ 111
PK 105,17,105,97
M 5,75,3
ST 104
M 4,62,2
WL 3
LO 18,18,110
LU 110,18
M 4,63,3
RA 216,1,7168
HS 25,110
M 3,50,2
LL 17,88
LL 114,89
M 3,51,3
LA 17
PK 110,18,108,56
M 2,38,2
LC_ 114
PK 108,18,108,97
M 2,39,3
FR
PO 110
M 1,26,2
WL 4
LO 16,16,111
LU 111,16
M 1,27,3
RA 220,1,8192
HS 25,111
M 0,14,2
LL 18,90
LL 115,91
M 0,15,3
LA 18
PK 111,16,109,56
M 0,16,4
LC_ 115
PK 109,16,109,97
M 0,17,5
XL 7,448
PO 111
M 1,28,4
WL 4
LO 17,17,114
LU 114,17
M 1,29,5
s_add_u32 s42, s44, s61
RA 224,1,9216
HS 25,114
M 2,40,4
s_addc_u32 s43, s45, 0
LL 16,92
LL 118,93
M 2,41,5
LA 16
PK 114,17,112,56
M 3,52,4
LC_ 118
PK 112,17,112,97
M 3,53,5
ST 108
M 4,64,4
WL 3
LO 18,18,115
LU 115,18
M 4,65,5
s_add_u32 s42, s44, s62
RA 228,1,10240
HS 25,115
M 5,76,4
LL 17,94
LL 119,95
M 5,77,5
s_addc_u32 s43, s45, 0
LA 17
PK 115,18,113,56
M 5,78,0
LC_ 119
PK 113,18,113,97
M 5,79,1
ST 112
M 4,66,0
WL 3
LO 16,16,118
LU 118,16
M 4,67,1
RA 232,1,11264
HS 25,118
M 3,54,0
LL 18,96
LL 122,97
M 3,55,1
LA 18
PK 118,16,116,56
v_cmp_gt_u32 vcc, v19, v15
M 2,42,0
LC_ 122
PK 116,16,116,97
M 2,43,1
RV 252,22,5392
PO 118
M 1,30,0
WL 4
LO 17,17,119
LU 119,17
M 1,31,1
s_cbranch_vccnz .Lk1s6
.Lk1r6:
RA 236,1,12288
HS 25,119
M 0,18,0
LL 16,98
LL 123,99
M 0,19,1
LA 16
PK 119,17,117,56
M 0,20,2
LC_ 123
PK 117,17,117,97
M 0,21,3
W1 24,0
PO 119
M 1,32,2
WL 5
LO 18,18,122
LU 122,18
M 1,33,3
RA 216,1,13312
HS 25,122
M 2,44,2
LL 17,100
LL 126,101
M 2,45,3
LA 17
PK 122,18,120,56
M 3,56,2
LC_ 126
XL 8,512
PK 120,18,120,97
M 3,57,3
s_add_u32 s42, s44, s63
M 4,68,2
WL 4
LO 16,16,123
LU 123,16
M 4,69,3
s_addc_u32 s43, s45, 0
RA 220,1,14336
HS 25,123
M 5,80,2
LL 18,102
LL 127,103
M 5,81,3
LA 18
PK 123,16,121,56
M 0,22,4
LC_ 127
PK 121,16,121,97
M 0,23,5
ST 116
M 1,34,4
WL 3
LO 17,17,126
LU 126,17
M 1,35,5
s_add_u32 s42, s44, s64
RA 224,1,15360
HS 25,126
M 2,46,4
s_addc_u32 s43, s45, 0
LL 16,56
LL 104,57
M 2,47,5
LA 16
PK 126,17,124,56
M 3,58,4
LC_ 104
PK 124,17,124,97
M 3,59,5
XL 9,576
PO 126
M 4,70,4
WL 3
LO 18,18,127
LU 127,18
M 4,71,5
RA 228,1,16384
HS 25,127
M 5,82,4
LL 17,58
LL 105,59
M 5,83,5
LA 17
PK 127,18,125,56
Z 6,12,0
LC_ 105
PK 125,18,125,97
M 6,13,1
ST 120
PO 127
Z 7,24,0
WL 3
LO 104,16,104
LU 16,104
M 7,25,1
s_add_u32 s42, s44, s65
RA 232,1,17408
HZ 24,16
Z 8,36,0
s_addc_u32 s43, s45, 0
LL 18,60
LL 108,61
M 8,37,1
s_mul_i32 s53, s50, 25600
LA 18
ds_read_b64 v[14:15], v5 offset:0
Z 9,48,0
s_add_u32 s40, %[wlo], s53
LC_ 108
v_subrev_u32 v5, s39, v5
M 9,49,1
s_addc_u32 s41, %[whi], 0
Z 10,60,0
WL 4
LO 105,17,105
LU 17,105
M 10,61,1
s_mov_b32 m0, s38
RA 236,1,18432
HS 24,17
Z 11,72,0
LL 16,62
LL 109,63
M 11,73,1
LA 16
XL 10,640
M 11,74,2
LC_ 109
ST 124
M 11,75,3
DW 0
HX 25
M 10,62,2
WL 3
LO 108,18,108
LU 18,108
W1 25,1024
M 10,63,3
s_add_u32 s37, s37, 32768
RA 216,1,19456
HS 24,18
M 9,50,2
s_cmp_eq_u32 s37, s75
LL 17,64
LL 112,65
M 9,51,3
s_cselect_b32 s37, s74, s37
LA 17
DW 1024
M 8,38,2
LC_ 112
v_add_f32 v14, v14, v15
s_nop 1
v_add_f32_dpp v14, v14, v14 quad_perm:[1,0,3,2] row_mask:0xf bank_mask:0xf
M 8,39,3
s_nop 0
v_add_f32_dpp v14, v14, v14 quad_perm:[2,3,0,1] row_mask:0xf bank_mask:0xf
v_add_u32 v4, s39, v4
M 7,26,2
WL 4
LO 109,16,109
LU 16,109
M 7,27,3
RA 220,1,20480
HS 24,16
M 6,14,2
LL 18,66
LL 113,67
M 6,15,3
LA 18
DW 2048
M 6,16,4
LC_ 113
XL 11,704
v_add_f32_dpp v14, v14, v14 row_half_mirror row_mask:0xf bank_mask:0xf
M 6,17,5
v_cvt_f64_f32 v[14:15], v14
M 7,28,4
WL 3
LO 112,17,112
LU 17,112
M 7,29,5
s_mul_i32 s53, s51, 24
RA 224,1,21504
HS 24,17
M 8,40,4
s_bitcmp1_b32 s51, 0
LL 16,68
LL 116,69
M 8,41,5
s_addc_u32 s53, s53, 0
LA 16
s_mov_b64 exec, s[54:55]
SP
s_mov_b64 exec, -1
M 9,52,4
s_lshl_b32 s53, s53, 11
LC_ 116
M 9,53,5
s_bitcmp1_b64 s[90:91], s51
M 10,64,4
WL 3
)"
R"(LO 113,18,113
LU 18,113
M 10,65,5
s_cselect_b64 s[44:45], s[84:85], s[82:83]
RA 228,1,22528
HS 24,18
M 11,76,4
LL 17,70
LL 117,71
M 11,77,5
s_add_u32 s44, s44, s53
LA 17
M 11,78,0
s_addc_u32 s45, s45, 0
LC_ 117
M 11,79,1
s_add_u32 s51, s51, %[d]
M 10,66,0
WL 3
LO 116,16,116
LU 16,116
M 10,67,1
s_cmp_eq_u32 s51, %[wrap]
RA 232,1,23552
HS 24,16
M 9,54,0
s_cselect_b32 s51, %[rst], s51
LL 18,72
LL 120,73
M 9,55,1
s_add_u32 s40, s40, 3072
LA 18
M 8,42,0
s_addc_u32 s41, s41, 0
LC_ 120
s_add_u32 m0, s38, 3072
M 8,43,1
DW 0
M 7,30,0
WL 3
LO 117,17,117
LU 17,117
M 7,31,1
RA 236,1,24576
HS 24,17
M 6,18,0
LL 16,74
LL 121,75
M 6,19,1
LA 16
DW 1024
M 6,20,2
s_bitcmp1_b64 s[92:93], 48
LC_ 121
M 6,21,3
s_cselect_b32 s58, 16, 0
v_add_u32 v1, s37, v0
M 7,32,2
WL 3
LO 120,18,120
LU 18,120
M 7,33,3
s_lshr_b32 s58, 0xda43cb52, s58
RA 216,1,0
HS 24,18
M 8,44,2
LL 17,76
LL 124,77
M 8,45,3
LA 17
v_and_b32 v22, 0xfffffcff, v1
M 9,56,2
LC_ 124
v_and_b32 v11, 0x1f0, v0
M 9,57,3
v_min_u32 v11, 368, v11
M 10,68,2
WL 3
LO 121,16,121
LU 16,121
M 10,69,3
RA 220,1,1024
HS 24,16
M 11,80,2
LL 18,78
LL 125,79
M 11,81,3
LA 18
RV 252,22,5120
DW 2048
v_lshlrev_b32 v11, 3, v11
M 6,22,4
s_lshl_b32 s53, s52, 8
LC_ 125
M 6,23,5
s_add_u32 s48, %[alo], s53
M 7,34,4
WL 4
LO 124,17,124
LU 17,124
M 7,35,5
s_addc_u32 s49, %[ahi], 0
RA 224,1,2048
HS 24,17
M 8,46,4
s_add_u32 s52, s52, %[d]
LL 16,80
LL 106,81
s_cmp_eq_u32 s52, %[wrap]
s_cselect_b32 s52, %[rst], s52
M 8,47,5
LA 16
DQ 3072
M 9,58,4
s_add_u32 s50, s50, %[d]
LC_ 106
M 9,59,5
M 10,70,4
WL 3
LO 125,18,125
LU 18,125
M 10,71,5
RA 228,1,3072
HS 24,18
M 11,82,4
LL 17,82
LL 107,83
M 11,83,5
LA 17
HX 24
s_cmp_eq_u32 s50, %[wrap]
s_cselect_b32 s50, %[rst], s50
Z 0,12,0
LC_ 107
v_add_u32 v19, 1, v19
M 0,13,1
s_add_u32 s38, s38, 32768
Z 1,24,0
WL 3
LO 16,16,106
LU 106,16
M 1,25,1
s_cmp_eq_u32 s38, s77
RA 232,1,4096
HZ 25,106
Z 2,36,0
s_cselect_b32 s38, s76, s38
LL 18,84
LL 110,85
M 2,37,1
LA 18
PK 106,16,104,56
Z 3,48,0
LC_ 110
PK 104,16,104,97
M 3,49,1
WV 41
FP
s_sub_u32 s39, 0, s39
Z 4,60,0
WL 4
LO 17,17,107
LU 107,17
M 4,61,1
s_add_u32 s42, s44, s60
RA 236,1,6144
HS 25,107
Z 5,72,0
LL 16,86
LL 111,87
M 5,73,1
s_addc_u32 s43, s45, 0
LA 16
PK 107,17,105,56
M 5,74,2
LC_ 111
PK 105,17,105,97
M 5,75,3
ST 104
M 4,62,2
WL 4
LO 18,18,110
LU 110,18
M 4,63,3
RA 216,1,7168
HS 25,110
M 3,50,2
LL 17,88
LL 114,89
M 3,51,3
LA 17
PK 110,18,108,56
M 2,38,2
LC_ 114
PK 108,18,108,97
M 2,39,3
FR
PO 110
M 1,26,2
WL 4
LO 16,16,111
LU 111,16
M 1,27,3
s_add_u32 s42, s44, s61
RA 220,1,8192
HS 25,111
M 0,14,2
LL 18,90
LL 115,91
M 0,15,3
s_addc_u32 s43, s45, 0
LA 18
PK 111,16,109,56
M 0,16,4
LC_ 115
PK 109,16,109,97
M 0,17,5
PO 111
M 1,28,4
WL 4
LO 17,17,114
LU 114,17
M 1,29,5
RA 224,1,9216
HS 25,114
M 2,40,4
LL 16,92
LL 118,93
M 2,41,5
LA 16
PK 114,17,112,56
M 3,52,4
LC_ 118
PK 112,17,112,97
M 3,53,5
WL 6
ST 108
v_cmp_gt_u32 vcc, v19, v15
M 4,64,4
WL 3
LO 18,18,115
LU 115,18
M 4,65,5
s_add_u32 s42, s44, s62
RA 228,1,10240
HS 25,115
M 5,76,4
LL 17,94
LL 119,95
M 5,77,5
s_cbranch_vccnz .Lk1s7
.Lk1r7:
LA 17
PK 115,18,113,56
M 5,78,0
s_addc_u32 s43, s45, 0
LC_ 119
PK 113,18,113,97
M 5,79,1
W1 24,0
M 4,66,0
WL 4
LO 16,16,118
LU 118,16
M 4,67,1
RA 232,1,11264
HS 25,118
M 3,54,0
LL 18,96
LL 122,97
M 3,55,1
LA 18
PK 118,16,116,56
M 2,42,0
LC_ 122
PK 116,16,116,97
M 2,43,1
RV 252,22,5392
PO 118
M 1,30,0
WL 5
LO 17,17,119
LU 119,17
M 1,31,1
RA 236,1,12288
HS 25,119
M 0,18,0
LL 16,98
LL 123,99
M 0,19,1
LA 16
PK 119,17,117,56
M 0,20,2
LC_ 123
PK 117,17,117,97
M 0,21,3
ST 112
PO 119
M 1,32,2
WL 4
LO 18,18,122
LU 122,18
M 1,33,3
RA 216,1,13312
HS 25,122
M 2,44,2
LL 17,100
LL 126,101
M 2,45,3
LA 17
PK 122,18,120,56
M 3,56,2
LC_ 126
PK 120,18,120,97
M 3,57,3
v_lshrrev_b32 v3, 9, v0
M 4,68,2
WL 3
LO 16,16,123
LU 123,16
M 4,69,3
RA 220,1,14336
HS 25,123
v_lshl_add_u32 v3, v3, 2, 0
v_bfe_u32 v3, s58, v3, 4
v_mad_u32_u24 v3, v3, v32, v11
M 5,80,2
LL 18,102
LL 127,103
M 5,81,3
s_add_u32 s42, s44, s63
LA 18
PK 123,16,121,56
M 0,22,4
s_addc_u32 s43, s45, 0
LC_ 127
PK 121,16,121,97
M 0,23,5
ST 116
M 1,34,4
WL 3
LO 17,17,126
LU 126,17
M 1,35,5
s_add_u32 s42, s44, s64
RA 224,1,15360
HS 25,126
M 2,46,4
s_addc_u32 s43, s45, 0
LL 16,56
LL 104,57
M 2,47,5
LA 16
PK 126,17,124,56
M 3,58,4
LC_ 104
PK 124,17,124,97
M 3,59,5
ST 120
PO 126
M 4,70,4
WL 3
LO 18,18,127
LU 127,18
M 4,71,5
s_add_u32 s42, s44, s65
RA 228,1,16384
HS 25,127
M 5,82,4
LL 17,58
LL 105,59
M 5,83,5
s_addc_u32 s43, s45, 0
LA 17
PK 127,18,125,56
Z 6,12,0
LC_ 105
PK 125,18,125,97
M 6,13,1
ds_read_b64 v[14:15], v5 offset:0
PO 127
Z 7,24,0
WL 4
LO 104,16,104
LU 16,104
M 7,25,1
RA 232,1,17408
HZ 24,16
Z 8,36,0
LL 18,60
LL 108,61
M 8,37,1
s_mul_i32 s53, s50, 25600
LA 18
v_subrev_u32 v5, s39, v5
Z 9,48,0
s_add_u32 s40, %[wlo], s53
LC_ 108
HX 25
M 9,49,1
)"
R"(s_addc_u32 s41, %[whi], 0
W1 25,1024
Z 10,60,0
WL 5
LO 105,17,105
LU 17,105
M 10,61,1
s_mov_b32 m0, s38
RA 236,1,18432
HS 24,17
Z 11,72,0
LL 16,62
LL 109,63
M 11,73,1
LA 16
ST 124
M 11,74,2
LC_ 109
DW 0
M 11,75,3
WL 7
v_add_f32 v14, v14, v15
v_add_u32 v4, s39, v4
M 10,62,2
WL 4
LO 108,18,108
LU 18,108
M 10,63,3
s_add_u32 s37, s37, 32768
RA 216,1,19456
HS 24,18
M 9,50,2
s_cmp_eq_u32 s37, s75
LL 17,64
LL 112,65
M 9,51,3
s_cselect_b32 s37, s74, s37
LA 17
DW 1024
M 8,38,2
LC_ 112
v_add_f32_dpp v14, v14, v14 quad_perm:[1,0,3,2] row_mask:0xf bank_mask:0xf
M 8,39,3
s_nop 0
v_add_f32_dpp v14, v14, v14 quad_perm:[2,3,0,1] row_mask:0xf bank_mask:0xf
M 7,26,2
WL 3
LO 109,16,109
LU 16,109
M 7,27,3
RA 220,1,20480
HS 24,16
M 6,14,2
LL 18,66
LL 113,67
M 6,15,3
LA 18
DW 2048
M 6,16,4
LC_ 113
v_add_f32_dpp v14, v14, v14 row_half_mirror row_mask:0xf bank_mask:0xf
M 6,17,5
v_cvt_f64_f32 v[14:15], v14
M 7,28,4
WL 3
LO 112,17,112
LU 17,112
M 7,29,5
s_mul_i32 s53, s51, 24
RA 224,1,21504
HS 24,17
M 8,40,4
s_bitcmp1_b32 s51, 0
LL 16,68
LL 116,69
M 8,41,5
s_addc_u32 s53, s53, 0
LA 16
s_mov_b64 exec, s[54:55]
SP
s_mov_b64 exec, -1
M 9,52,4
s_lshl_b32 s53, s53, 11
LC_ 116
M 9,53,5
s_bitcmp1_b64 s[90:91], s51
M 10,64,4
WL 3
LO 113,18,113
LU 18,113
M 10,65,5
s_cselect_b64 s[44:45], s[84:85], s[82:83]
RA 228,1,22528
HS 24,18
M 11,76,4
LL 17,70
LL 117,71
M 11,77,5
s_add_u32 s44, s44, s53
LA 17
M 11,78,0
s_addc_u32 s45, s45, 0
LC_ 117
M 11,79,1
s_add_u32 s51, s51, %[d]
M 10,66,0
WL 3
LO 116,16,116
LU 16,116
M 10,67,1
s_cmp_eq_u32 s51, %[wrap]
RA 232,1,23552
HS 24,16
M 9,54,0
s_cselect_b32 s51, %[rst], s51
LL 18,72
LL 120,73
M 9,55,1
s_add_u32 s40, s40, 3072
LA 18
M 8,42,0
s_addc_u32 s41, s41, 0
LC_ 120
s_add_u32 m0, s38, 3072
M 8,43,1
DW 0
M 7,30,0
WL 3
LO 117,17,117
LU 17,117
M 7,31,1
RA 236,1,24576
HS 24,17
M 6,18,0
LL 16,74
LL 121,75
M 6,19,1
LA 16
DW 1024
M 6,20,2
LC_ 121
DY
M 6,21,3
DW 2048
v_add_u32 v1, s37, v0
M 7,32,2
WL 3
LO 120,18,120
LU 18,120
M 7,33,3
s_lshl_b32 s53, s52, 8
RA 216,1,0
HS 24,18
M 8,44,2
s_add_u32 s48, %[alo], s53
LL 17,76
LL 124,77
M 8,45,3
s_addc_u32 s49, %[ahi], 0
LA 17
v_and_b32 v22, 0xfffffcff, v1
M 9,56,2
s_add_u32 s52, s52, %[d]
LC_ 124
M 9,57,3
s_cmp_eq_u32 s52, %[wrap]
M 10,68,2
WL 3
LO 121,16,121
LU 16,121
M 10,69,3
s_cselect_b32 s52, %[rst], s52
RA 220,1,1024
HS 24,16
M 11,80,2
LL 18,78
LL 125,79
M 11,81,3
LA 18
RV 252,22,5120
M 6,22,4
LC_ 125
DQ 3072
M 6,23,5
s_add_u32 s50, s50, %[d]
M 7,34,4
WL 4
LO 124,17,124
LU 17,124
M 7,35,5
s_cmp_eq_u32 s50, %[wrap]
RA 224,1,2048
HS 24,17
M 8,46,4
s_cselect_b32 s50, %[rst], s50
LL 16,80
LL 106,81
M 8,47,5
LA 16
v_add_u32 v19, 1, v19
M 9,58,4
s_add_u32 s38, s38, 32768
LC_ 106
M 9,59,5
M 10,70,4
WL 3
LO 125,18,125
LU 18,125
M 10,71,5
RA 228,1,3072
HS 24,18
M 11,82,4
LL 17,82
LL 107,83
M 11,83,5
LA 17
HX 24
Z 0,12,0
s_cmp_eq_u32 s38, s77
LC_ 107
M 0,13,1
s_cselect_b32 s38, s76, s38
WV 36
FP
Z 1,24,0
WL 4
LO 16,16,106
LU 106,16
M 1,25,1
s_sub_u32 s39, 0, s39
RA 232,1,4096
HZ 25,106
Z 2,36,0
LL 18,84
LL 110,85
M 2,37,1
LA 18
PK 106,16,104,56
Z 3,48,0
LC_ 110
PK 104,16,104,97
M 3,49,1
Z 4,60,0
WL 4
LO 17,17,107
LU 107,17
M 4,61,1
s_add_u32 s42, s44, s60
RA 236,1,6144
HS 25,107
Z 5,72,0
LL 16,86
LL 111,87
M 5,73,1
s_addc_u32 s43, s45, 0
LA 16
PK 107,17,105,56
M 5,74,2
LC_ 111
PK 105,17,105,97
M 5,75,3
ST 104
M 4,62,2
WL 3
LO 18,18,110
LU 110,18
M 4,63,3
RA 216,1,7168
HS 25,110
M 3,50,2
LL 17,88
LL 114,89
M 3,51,3
LA 17
PK 110,18,108,56
M 2,38,2
LC_ 114
PK 108,18,108,97
M 2,39,3
FR
PO 110
M 1,26,2
WL 4
LO 16,16,111
LU 111,16
M 1,27,3
s_add_u32 s42, s44, s61
RA 220,1,8192
HS 25,111
M 0,14,2
LL 18,90
LL 115,91
M 0,15,3
s_addc_u32 s43, s45, 0
LA 18
PK 111,16,109,56
M 0,16,4
LC_ 115
PK 109,16,109,97
M 0,17,5
PO 111
M 1,28,4
WL 4
LO 17,17,114
LU 114,17
M 1,29,5
RA 224,1,9216
HS 25,114
M 2,40,4
LL 16,92
LL 118,93
M 2,41,5
LA 16
PK 114,17,112,56
M 3,52,4
LC_ 118
PK 112,17,112,97
M 3,53,5
WL 6
ST 108
v_cmp_gt_u32 vcc, v19, v15
M 4,64,4
WL 3
LO 18,18,115
LU 115,18
M 4,65,5
s_add_u32 s42, s44, s62
RA 228,1,10240
HS 25,115
M 5,76,4
LL 17,94
LL 119,95
M 5,77,5
s_cbranch_vccnz .Lk1s8
.Lk1r8:
LA 17
PK 115,18,113,56
M 5,78,0
s_addc_u32 s43, s45, 0
LC_ 119
PK 113,18,113,97
M 5,79,1
W1 24,0
M 4,66,0
WL 4
LO 16,16,118
LU 118,16
M 4,67,1
RA 232,1,11264
HS 25,118
M 3,54,0
LL 18,96
LL 122,97
M 3,55,1
LA 18
PK 118,16,116,56
M 2,42,0
LC_ 122
PK 116,16,116,97
M 2,43,1
RV 252,22,5392
PO 118
M 1,30,0
WL 5
LO 17,17,119
LU 119,17
M 1,31,1
RA 236,1,12288
HS 25,119
M 0,18,0
LL 16,98
LL 123,99
M 0,19,1
LA 16
PK 119,17,117,56
M 0,20,2
LC_ 123
PK 117,17,117,97
M 0,21,3
ST 112
PO 119
M 1,32,2
WL 4
LO 18,18,122
LU 122,18
M 1,33,3
RA 216,1,13312
HS 25,122
M 2,44,2
LL 17,100
LL 126,101
M 2,45,3
LA 17
PK 122,18,120,56
v_lshrrev_b32 v3, 9, v0
v_lshl_add_u32 v3, v3, 2, 8
v_bfe_u32 v3, s58, v3, 4
v_mad_u32_u24 v3, v3, v32, v11
M 3,56,2
s_add_u32 s42, s44, s63
)"
R"(LC_ 126
PK 120,18,120,97
M 3,57,3
s_addc_u32 s43, s45, 0
ST 116
M 4,68,2
WL 3
LO 16,16,123
LU 123,16
M 4,69,3
s_add_u32 s42, s44, s64
RA 220,1,14336
HS 25,123
M 5,80,2
LL 18,102
LL 127,103
M 5,81,3
s_addc_u32 s43, s45, 0
LA 18
PK 123,16,121,56
M 0,22,4
LC_ 127
PK 121,16,121,97
M 0,23,5
ST 120
RV 40,8,6144
M 1,34,4
WL 4
LO 17,17,126
LU 126,17
M 1,35,5
RA 224,1,15360
HS 25,126
M 2,46,4
LL 16,56
LL 104,57
M 2,47,5
LA 16
PK 126,17,124,56
M 3,58,4
LC_ 104
PK 124,17,124,97
M 3,59,5
ds_read_b64 v[14:15], v5 offset:0
PO 126
M 4,70,4
WL 5
LO 18,18,127
LU 127,18
M 4,71,5
s_add_u32 s42, s44, s65
RA 228,1,16384
HS 25,127
M 5,82,4
LL 17,58
LL 105,59
M 5,83,5
s_addc_u32 s43, s45, 0
LA 17
PK 127,18,125,56
Z 6,12,0
LC_ 105
PK 125,18,125,97
M 6,13,1
RV 44,8,6208
PO 127
Z 7,24,0
WL 5
LO 104,16,104
LU 16,104
M 7,25,1
RA 232,1,17408
HZ 24,16
Z 8,36,0
WL 5
LL 18,60
LL 108,61
M 8,37,1
s_mul_i32 s53, s50, 25600
LA 18
RV 48,8,6272
LZ 40,0
s_add_u32 s40, %[wlo], s53
RV 40,8,6336
v_subrev_u32 v5, s39, v5
Z 9,48,0
s_addc_u32 s41, %[whi], 0
LC_ 108
v_add_f32 v14, v14, v15
M 9,49,1
s_mov_b32 m0, s38
ST 124
v_add_f32_dpp v14, v14, v14 quad_perm:[1,0,3,2] row_mask:0xf bank_mask:0xf
Z 10,60,0
WL 6
LO 105,17,105
LU 17,105
M 10,61,1
WL 5
RA 236,1,18432
HS 24,17
LS 44,1
RV 44,8,6400
v_add_f32_dpp v14, v14, v14 quad_perm:[2,3,0,1] row_mask:0xf bank_mask:0xf
Z 11,72,0
LL 16,62
LL 109,63
M 11,73,1
LA 16
DW 0
M 11,74,2
LC_ 109
v_add_f32_dpp v14, v14, v14 row_half_mirror row_mask:0xf bank_mask:0xf
M 11,75,3
WL 6
v_cvt_f64_f32 v[14:15], v14
HX 25
LS 48,2
WL 5
RV 48,8,6464
LS 40,3
RV 40,8,6528
M 10,62,2
WL 4
LO 108,18,108
LU 18,108
W1 25,1024
M 10,63,3
s_add_u32 s37, s37, 32768
RA 216,1,19456
HS 24,18
M 9,50,2
s_cmp_eq_u32 s37, s75
LL 17,64
LL 112,65
M 9,51,3
s_cselect_b32 s37, s74, s37
LA 17
s_mov_b64 exec, s[54:55]
SP
s_mov_b64 exec, -1
LS 44,4
RV 44,8,6592
v_add_u32 v4, s39, v4
M 8,38,2
LC_ 112
DW 1024
M 8,39,3
WV 43
M 7,26,2
WL 7
LO 109,16,109
LU 16,109
M 7,27,3
WL 6
RA 220,1,20480
HS 24,16
LS 48,5
WL 6
RV 48,8,6656
LS 40,6
RV 40,8,6720
M 6,14,2
LL 18,66
LL 113,67
M 6,15,3
LA 18
DW 2048
M 6,16,4
WV 42
LC_ 113
M 6,17,5
WL 6
LS 44,7
WV 37
RV 44,8,6784
M 7,28,4
WL 3
LO 112,17,112
LU 17,112
M 7,29,5
s_mul_i32 s53, s51, 24
RA 224,1,21504
HS 24,17
M 8,40,4
s_bitcmp1_b32 s51, 0
LL 16,68
LL 116,69
M 8,41,5
s_addc_u32 s53, s53, 0
LA 16
LS 48,8
s_lshl_b32 s53, s53, 11
RV 48,8,6848
RA 240,7,0
LS 40,9
s_bitcmp1_b64 s[90:91], s51
RA 244,7,256
M 9,52,4
s_cselect_b64 s[44:45], s[84:85], s[82:83]
LC_ 116
M 9,53,5
s_add_u32 s44, s44, s53
M 10,64,4
WL 6
LO 113,18,113
LU 18,113
M 10,65,5
s_addc_u32 s45, s45, 0
RA 228,1,22528
HS 24,18
WV 35
LS 44,10
s_add_u32 s51, s51, %[d]
RA 248,7,512
M 11,76,4
WL 5
LL 17,70
LL 117,71
M 11,77,5
s_cmp_eq_u32 s51, %[wrap]
LA 17
M 11,78,0
s_cselect_b32 s51, %[rst], s51
LC_ 117
M 11,79,1
s_add_u32 s40, s40, 3072
WV 30
LS 48,11
s_addc_u32 s41, s41, 0
RA 252,7,768
M 10,66,0
WL 5
LO 116,16,116
LU 16,116
M 10,67,1
s_add_u32 m0, s38, 3072
RA 232,1,23552
HS 24,16
M 9,54,0
LL 18,72
LL 120,73
M 9,55,1
LA 18
DW 0
M 8,42,0
LC_ 120
DY
M 8,43,1
DW 1024
v_mul_f32 v36, 0xbb2aaaab, v36
M 7,30,0
WL 4
LO 117,17,117
LU 17,117
M 7,31,1
RA 236,1,24576
HS 24,17
M 6,18,0
LL 16,74
LL 121,75
M 6,19,1
LA 16
DW 2048
M 6,20,2
LC_ 121
v_mul_f32 v37, 0xbb2aaaab, v37
M 6,21,3
s_lshl_b32 s53, s52, 8
v_add_u32 v1, s37, v0
v_mul_f32 v38, 0xbb2aaaab, v38
M 7,32,2
WL 3
LO 120,18,120
LU 18,120
M 7,33,3
s_add_u32 s48, %[alo], s53
RA 216,1,0
HS 24,18
M 8,44,2
s_addc_u32 s49, %[ahi], 0
LL 17,76
LL 124,77
M 8,45,3
s_add_u32 s52, s52, %[d]
LA 17
v_and_b32 v22, 0xfffffcff, v1
M 9,56,2
s_cmp_eq_u32 s52, %[wrap]
LC_ 124
v_mul_f32 v39, 0xbb2aaaab, v39
M 9,57,3
s_cselect_b32 s52, %[rst], s52
DQ 3072
LK 40,240,0,0
LK 44,242,0,2
RA 240,7,1024
LK 48,244,1,0
s_nop 1
CF 128,40,41
LK 248,246,1,2
RA 244,7,1280
CF 129,42,43
M 10,68,2
WL 5
LO 121,16,121
LU 16,121
M 10,69,3
s_add_u32 s50, s50, %[d]
RA 220,1,1024
HS 24,16
M 11,80,2
LL 18,78
LL 125,79
M 11,81,3
s_cmp_eq_u32 s50, %[wrap]
LA 18
RV 252,22,5120
M 6,22,4
s_cselect_b32 s50, %[rst], s50
LC_ 125
CF 130,44,45
M 6,23,5
v_add_u32 v19, 1, v19
CF 131,46,47
LK 40,248,2,0
CF 132,48,49
LK 44,250,2,2
RA 248,7,1536
CF 133,50,51
GZ 0
CF 134,248,249
CF 135,250,251
LK 48,252,3,0
CF 136,40,41
LK 248,254,3,2
RA 252,7,1792
CF 137,42,43
GS 1
WL 7
CF 138,44,45
CF 139,46,47
LK 40,240,4,0
CF 140,48,49
LK 44,242,4,2
RA 240,7,2048
CF 141,50,51
GS 2
WL 7
CF 142,248,249
CF 143,250,251
LK 48,244,5,0
CF 144,40,41
LK 248,246,5,2
RA 244,7,2304
CF 145,42,43
GS 3
CF 146,44,45
CF 147,46,47
M 7,34,4
WL 3
LO 124,17,124
LU 17,124
M 7,35,5
s_add_u32 s38, s38, 32768
RA 224,1,2048
HS 24,17
LK 40,248,6,0
WL 3
CF 148,48,49
LK 44,250,6,2
s_cmp_eq_u32 s38, s77
RA 248,7,2560
CF 149,50,51
GS 4
s_cselect_b32 s38, s76, s38
CF 150,248,249
CF 151,250,251
LK 48,252,7,0
CF 152,40,41
LK 248,254,7,2
RA 252,7,2816
CF 153,42,43
GS 5
WL 4
CF 154,44,45
CF 155,46,47
LK 40,240,8,0
CF 156,48,49
LK 44,242,8,2
CF 157,50,51
GS 6
WL 3
CF 158,248,249
CF 159,250,251
LK 48,244,9,0
CF 160,40,41
LK 248,246,9,2
CF 161,42,43
GS 7
)"
R"(CF 162,44,45
CF 163,46,47
M 8,46,4
LL 16,80
LL 106,81
M 8,47,5
WV 30
LA 16
FP
M 9,58,4
s_sub_u32 s39, 0, s39
LC_ 106
M 9,59,5
WL 4
LK 40,248,10,0
CF 164,48,49
LK 44,250,10,2
CF 165,50,51
GS 8
CF 166,248,249
CF 167,250,251
M 10,70,4
LO 125,18,125
LU 18,125
M 10,71,5
WL 3
RA 228,1,3072
HS 24,18
LK 48,252,11,0
CF 168,40,41
LK 248,254,11,2
CF 169,42,43
GS 9
CF 170,44,45
CF 171,46,47
M 11,82,4
LL 17,82
LL 107,83
M 11,83,5
LA 17
HX 24
Z 0,12,0
LC_ 107
RV 36,8,6912
M 0,13,1
GS 10
Z 1,24,0
WL 4
LO 16,16,106
LU 106,16
M 1,25,1
RA 232,1,4096
HZ 25,106
Z 2,36,0
LL 18,84
LL 110,85
M 2,37,1
LA 18
PK 106,16,104,56
Z 3,48,0
LC_ 110
PK 104,16,104,97
M 3,49,1
CF 172,48,49
CF 173,50,51
Z 4,60,0
WL 4
LO 17,17,107
LU 107,17
M 4,61,1
s_add_u32 s42, s44, s60
RA 236,1,6144
HS 25,107
Z 5,72,0
LL 16,86
LL 111,87
M 5,73,1
s_addc_u32 s43, s45, 0
LA 16
PK 107,17,105,56
M 5,74,2
LC_ 111
PK 105,17,105,97
M 5,75,3
ST 104
CF 174,248,249
M 4,62,2
WL 3
LO 18,18,110
LU 110,18
M 4,63,3
RA 216,1,7168
HS 25,110
M 3,50,2
LL 17,88
LL 114,89
M 3,51,3
LA 17
PK 110,18,108,56
M 2,38,2
LC_ 114
PK 108,18,108,97
M 2,39,3
FR
PO 110
M 1,26,2
WL 4
LO 16,16,111
LU 111,16
M 1,27,3
s_add_u32 s42, s44, s61
RA 220,1,8192
HS 25,111
M 0,14,2
LL 18,90
LL 115,91
M 0,15,3
s_addc_u32 s43, s45, 0
LA 18
PK 111,16,109,56
M 0,16,4
s_mov_b32 m0, %[pm]
LC_ 115
PK 109,16,109,97
M 0,17,5
DF
PO 111
M 1,28,4
WL 4
LO 17,17,114
LU 114,17
M 1,29,5
RA 224,1,9216
HS 25,114
M 2,40,4
LL 16,92
LL 118,93
M 2,41,5
LA 16
PK 114,17,112,56
M 3,52,4
LC_ 118
PK 112,17,112,97
M 3,53,5
WL 6
ST 108
v_cmp_gt_u32 vcc, v19, v15
M 4,64,4
WL 3
LO 18,18,115
LU 115,18
M 4,65,5
s_add_u32 s42, s44, s62
RA 228,1,10240
HS 25,115
CF 175,250,251
M 5,76,4
LL 17,94
LL 119,95
M 5,77,5
s_cbranch_vccnz .Lk1s9
.Lk1r9:
LA 17
PK 115,18,113,56
GS 11
s_addc_u32 s43, s45, 0
W1 24,0
s_nop 5
v_cndmask_b32 v52, v52, v53, s[78:79]
M 5,78,0
LC_ 119
PK 113,18,113,97
v_cndmask_b32 v54, v54, v55, s[78:79]
M 5,79,1
ST 112
v_cndmask_b32 v52, v52, v54, s[80:81]
M 4,66,0
WL 4
LO 16,16,118
LU 118,16
v_mul_f32 v52, 0x3b2aaaab, v52
M 4,67,1
RA 232,1,11264
HS 25,118
v_med3_f32 v53, v52, v39, v37
M 3,54,0
LL 18,96
LL 122,97
LG 52,53
M 3,55,1
LA 18
PK 118,16,116,56
C 52,52,52
M 2,42,0
LC_ 122
PK 116,16,116,97
v_lshlrev_b32 v52, 16, v52
M 2,43,1
RV 252,22,5392
PO 118
v_add_f32 v52, v36, v52
M 1,30,0
WL 4
LO 17,17,119
LU 119,17
C 52,52,52
M 1,31,1
s_add_u32 s42, s44, s63
RA 236,1,12288
HS 25,119
M 0,18,0
LL 16,98
LL 123,99
v_lshlrev_b32 v52, 16, v52
M 0,19,1
s_addc_u32 s43, s45, 0
LA 16
PK 119,17,117,56
v_sqrt_f32 v52, v52
M 0,20,2
LC_ 123
PK 117,17,117,97
C 52,52,52
M 0,21,3
PO 119
v_lshlrev_b32 v52, 16, v52
M 1,32,2
WL 4
LO 18,18,122
LU 122,18
v_rcp_f32 v52, v52
M 1,33,3
RA 216,1,13312
HS 25,122
v_mul_f32 v52, v38, v52
M 2,44,2
LL 17,100
LL 126,101
CF 52,52,52
M 2,45,3
LA 17
PK 122,18,120,56
ds_bpermute_b32 v13, v28, v52
M 3,56,2
LC_ 126
PK 120,18,120,97
M 3,57,3
ST 116
M 4,68,2
WL 4
LO 16,16,123
LU 123,16
M 4,69,3
s_add_u32 s42, s44, s64
RA 220,1,14336
HS 25,123
M 5,80,2
LL 18,102
LL 127,103
M 5,81,3
s_addc_u32 s43, s45, 0
LA 18
PK 123,16,121,56
M 0,22,4
LC_ 127
PK 121,16,121,97
M 0,23,5
ST 120
M 1,34,4
WL 3
LO 17,17,126
LU 126,17
M 1,35,5
RA 224,1,15360
HS 25,126
XS 128
M 2,46,4
LL 16,56
LL 104,57
M 2,47,5
LA 16
PK 126,17,124,56
M 3,58,4
LC_ 104
PK 124,17,124,97
XS 129
M 3,59,5
ds_read_b64 v[14:15], v5 offset:0
PO 126
M 4,70,4
WL 4
LO 18,18,127
LU 127,18
M 4,71,5
s_add_u32 s42, s44, s65
RA 228,1,16384
HS 25,127
XS 130
M 5,82,4
LL 17,58
LL 105,59
M 5,83,5
s_addc_u32 s43, s45, 0
LA 17
PK 127,18,125,56
Z 6,12,0
LC_ 105
PK 125,18,125,97
XS 131
M 6,13,1
PO 127
v_subrev_u32 v5, s39, v5
Z 7,24,0
WL 4
LO 104,16,104
LU 16,104
XS 132
M 7,25,1
RA 232,1,17408
HZ 24,16
Z 8,36,0
WL 4
LL 18,60
LL 108,61
M 8,37,1
s_mul_i32 s53, s50, 25600
LA 18
v_add_f32 v14, v14, v15
XS 133
Z 9,48,0
s_add_u32 s40, %[wlo], s53
LC_ 108
v_add_f32_dpp v14, v14, v14 quad_perm:[1,0,3,2] row_mask:0xf bank_mask:0xf
M 9,49,1
s_addc_u32 s41, %[whi], 0
v_add_f32_dpp v14, v14, v14 quad_perm:[2,3,0,1] row_mask:0xf bank_mask:0xf
HX 25
Z 10,60,0
WL 3
LO 105,17,105
LU 17,105
XS 134
M 10,61,1
s_mov_b32 m0, s38
RA 236,1,18432
HS 24,17
Z 11,72,0
LL 16,62
LL 109,63
M 11,73,1
LA 16
W1 25,1024
XS 135
M 11,74,2
LC_ 109
ST 124
M 11,75,3
DW 0
v_add_f32_dpp v14, v14, v14 row_half_mirror row_mask:0xf bank_mask:0xf
v_min_u32 v35, v35, v13
M 10,62,2
WL 4
LO 108,18,108
LU 18,108
M 10,63,3
s_add_u32 s37, s37, 32768
RA 216,1,19456
HS 24,18
M 9,50,2
s_cmp_eq_u32 s37, s75
LL 17,64
LL 112,65
XS 136
M 9,51,3
s_cselect_b32 s37, s74, s37
LA 17
v_cvt_f64_f32 v[14:15], v14
M 8,38,2
s_mul_i32 s53, %[d], 0
LC_ 112
s_mov_b64 exec, s[54:55]
SP
s_mov_b64 exec, -1
M 8,39,3
s_add_u32 s53, s53, s57
v_add_u32 v4, s39, v4
XS 137
M 7,26,2
WL 3
LO 109,16,109
LU 16,109
M 7,27,3
s_add_u32 s46, %[xlo], s53
RA 220,1,20480
HS 24,16
M 6,14,2
LL 18,66
LL 113,67
XS 138
M 6,15,3
)"
R"(s_addc_u32 s47, %[xhi], 0
LA 18
XS 139
M 6,16,4
s_bitcmp1_b64 s[92:93], 48
LC_ 113
XS 140
M 6,17,5
s_cselect_b32 s58, 16, 0
XS 141
XS 142
M 7,28,4
WL 3
LO 112,17,112
LU 17,112
M 7,29,5
s_lshr_b32 s58, 0xda43cb52, s58
RA 224,1,21504
HS 24,17
M 8,40,4
LL 16,68
LL 116,69
M 8,41,5
LA 16
v_lshrrev_b32 v3, 6, v0
M 9,52,4
LC_ 116
v_and_b32 v3, 12, v3
v_bfe_u32 v3, s58, v3, 4
v_mad_u32_u24 v3, v3, v32, v21
M 9,53,5
DW 1024
XS 143
M 10,64,4
WL 3
LO 113,18,113
LU 18,113
M 10,65,5
RA 228,1,22528
HS 24,18
M 11,76,4
LL 17,70
LL 117,71
M 11,77,5
LA 17
XL 12,0
M 11,78,0
LC_ 117
DW 2048
M 11,79,1
XL 13,64
XS 144
M 10,66,0
WL 3
LO 116,16,116
LU 16,116
M 10,67,1
s_mul_i32 s53, s51, 24
RA 232,1,23552
HS 24,16
M 9,54,0
s_bitcmp1_b32 s51, 0
LL 18,72
LL 120,73
M 9,55,1
s_addc_u32 s53, s53, 0
LA 18
s_lshl_b32 s53, s53, 11
s_bitcmp1_b64 s[90:91], s51
s_cselect_b64 s[44:45], s[84:85], s[82:83]
s_add_u32 s44, s44, s53
s_addc_u32 s45, s45, 0
s_add_u32 s51, s51, %[d]
s_cmp_eq_u32 s51, %[wrap]
s_cselect_b32 s51, %[rst], s51
XS 145
M 8,42,0
s_add_u32 s40, s40, 3072
LC_ 120
s_addc_u32 s41, s41, 0
s_add_u32 m0, s38, 3072
M 8,43,1
XL 14,128
XS 146
M 7,30,0
WL 3
LO 117,17,117
LU 17,117
M 7,31,1
RA 236,1,24576
HS 24,17
M 6,18,0
LL 16,74
LL 121,75
XS 147
M 6,19,1
LA 16
DW 0
M 6,20,2
LC_ 121
XL 15,192
M 6,21,3
DW 1024
v_add_u32 v1, s37, v0
XS 148
M 7,32,2
WL 3
LO 120,18,120
LU 18,120
M 7,33,3
RA 216,1,0
HS 24,18
M 8,44,2
LL 17,76
LL 124,77
XS 149
M 8,45,3
LA 17
XL 16,256
M 9,56,2
LC_ 124
DW 2048
XS 150
M 9,57,3
XL 17,320
v_and_b32 v22, 0xfffffcff, v1
M 10,68,2
WL 3
LO 121,16,121
LU 16,121
M 10,69,3
s_lshl_b32 s53, s52, 8
RA 220,1,1024
HS 24,16
XS 151
M 11,80,2
LL 18,78
LL 125,79
M 11,81,3
s_add_u32 s48, %[alo], s53
LA 18
RV 252,22,5120
M 6,22,4
s_addc_u32 s49, %[ahi], 0
LC_ 125
XS 152
M 6,23,5
s_add_u32 s52, s52, %[d]
XS 153
XS 154
M 7,34,4
WL 4
LO 124,17,124
LU 17,124
M 7,35,5
s_cmp_eq_u32 s52, %[wrap]
RA 224,1,2048
HS 24,17
M 8,46,4
s_cselect_b32 s52, %[rst], s52
LL 16,80
LL 106,81
M 8,47,5
LA 16
DQ 3072
M 9,58,4
LC_ 106
XL 18,384
M 9,59,5
s_add_u32 s50, s50, %[d]
XS 155
XS 156
M 10,70,4
WL 3
LO 125,18,125
LU 18,125
M 10,71,5
RA 228,1,3072
HS 24,18
M 11,82,4
LL 17,82
LL 107,83
M 11,83,5
LA 17
HX 24
s_cmp_eq_u32 s50, %[wrap]
s_cselect_b32 s50, %[rst], s50
XL 19,448
Z 2,36,0
WV 38
LC_ 107
XL 20,512
v_add_u32 v19, 1, v19
s_add_u32 s38, s38, 32768
s_cmp_eq_u32 s38, s77
s_cselect_b32 s38, s76, s38
FP
s_sub_u32 s39, 0, s39
M 2,37,1
XL 21,576
XS 157
Z 3,48,0
WL 4
LO 16,16,106
LU 106,16
M 3,49,1
RA 232,1,4096
HZ 25,106
Z 4,60,0
LL 18,84
LL 110,85
XS 158
M 4,61,1
LA 18
PK 106,16,104,56
Z 5,72,0
WL 5
LC_ 110
XL 22,640
PK 104,16,104,97
M 5,73,1
RA 236,1,6144
XS 159
M 5,74,2
WL 5
LO 17,17,107
LU 107,17
M 5,75,3
FR
HS 25,107
M 4,62,2
LL 16,86
LL 111,87
XS 160
M 4,63,3
LA 16
PK 107,17,105,56
M 3,50,2
LC_ 111
XL 23,704
PK 105,17,105,97
XS 161
M 3,51,3
s_add_u32 s42, s44, s60
RA 216,1,7168
XS 162
M 2,38,2
WL 4
LO 18,18,110
LU 110,18
M 2,39,3
s_addc_u32 s43, s45, 0
RA 220,1,8192
HS 25,110
M 2,40,4
LL 17,88
LL 114,89
M 2,41,5
LA 17
PK 110,18,108,56
M 3,52,4
LC_ 114
PK 108,18,108,97
XS 163
M 3,53,5
RA 224,1,9216
PO 110
M 4,64,4
WL 5
LO 16,16,111
LU 111,16
ST 104
XS 164
M 4,65,5
s_add_u32 s42, s44, s61
HS 25,111
v_cmp_gt_u32 vcc, v19, v15
M 5,76,4
WL 3
LL 18,90
LL 115,91
M 5,77,5
s_addc_u32 s43, s45, 0
LA 18
PK 111,16,109,56
XS 165
M 5,78,0
s_cbranch_vccnz .Lk1s10
.Lk1r10:
LC_ 115
PK 109,16,109,97
M 5,79,1
WV 17
RA 228,1,10240
PO 111
BW 12
M 4,66,0
WL 5
LO 17,17,114
LU 114,17
XS 166
M 4,67,1
RA 232,1,11264
RV 40,8,6144
HS 25,114
M 3,54,0
RV 44,8,6208
LL 16,92
LL 118,93
M 3,55,1
LA 16
RV 48,8,6272
PK 114,17,112,56
XS 167
M 2,42,0
WL 6
LC_ 118
BR 12
PK 112,17,112,97
M 2,43,1
WV 15
RV 252,22,5392
ds_read_b64 v[14:15], v5 offset:0
W1 24,0
BW 13
XS 168
M 2,44,2
BR 13
LO 18,18,115
LU 115,18
M 2,45,3
WL 5
RA 236,1,12288
HS 25,115
LZ 40,12
WV 14
BW 14
M 3,56,2
LL 17,94
LL 119,95
XS 169
M 3,57,3
LA 17
RV 40,8,6336
PK 115,18,113,56
M 4,68,2
LC_ 119
BR 14
PK 113,18,113,97
M 4,69,3
WL 6
RA 216,1,13312
v_subrev_u32 v5, s39, v5
XS 170
LS 44,13
WV 12
BW 15
M 5,80,2
WL 7
LO 16,16,118
LU 118,16
ST 108
M 5,81,3
s_add_u32 s42, s44, s62
RA 220,1,14336
HS 25,118
s_addc_u32 s43, s45, 0
RV 44,8,6400
M 2,46,4
BR 15
LL 18,96
LL 122,97
XS 171
M 2,47,5
WL 5
LA 18
PK 118,16,116,56
LS 48,14
WV 11
BW 16
M 3,58,4
LC_ 122
PK 116,16,116,97
XS 172
M 3,59,5
RA 224,1,15360
RV 48,8,6464
PO 118
M 4,70,4
LO 17,17,119
LU 119,17
ST 112
M 4,71,5
s_add_u32 s42, s44, s63
BR 16
HS 25,119
XS 173
M 5,82,4
WL 6
LL 16,98
)"
R"(LL 123,99
M 5,83,5
s_addc_u32 s43, s45, 0
LA 16
PK 119,17,117,56
LS 40,15
WV 10
BW 17
Z 8,36,0
LC_ 123
PK 117,17,117,97
v_add_f32 v14, v14, v15
s_nop 1
v_add_f32_dpp v14, v14, v14 quad_perm:[1,0,3,2] row_mask:0xf bank_mask:0xf
s_nop 1
v_add_f32_dpp v14, v14, v14 quad_perm:[2,3,0,1] row_mask:0xf bank_mask:0xf
s_nop 1
v_add_f32_dpp v14, v14, v14 row_half_mirror row_mask:0xf bank_mask:0xf
v_cvt_f64_f32 v[14:15], v14
s_mov_b64 exec, s[54:55]
SP
s_mov_b64 exec, -1
RV 40,8,6528
XS 174
M 8,37,1
WV 9
RA 228,1,16384
BR 17
PO 119
Z 9,48,0
WL 6
LO 18,18,122
LU 122,18
XS 175
M 9,49,1
s_mul_i32 s53, %[d], 196608
RA 232,1,17408
HS 25,122
LS 44,16
s_add_u32 s53, s53, s57
BW 18
Z 10,60,0
s_add_u32 s46, %[xlo], s53
RV 44,8,6592
LL 17,100
LL 126,101
M 10,61,1
s_addc_u32 s47, %[xhi], 0
LA 17
BR 18
PK 122,18,120,56
Z 0,72,0
WL 5
LC_ 126
PK 120,18,120,97
M 0,73,1
s_bitcmp1_b64 s[92:93], 48
RA 236,1,18432
LS 48,17
s_cselect_b32 s58, 16, 0
WV 8
BW 19
M 0,74,2
RV 48,8,6656
LO 16,16,123
LU 123,16
M 0,75,3
s_lshr_b32 s58, 0xfe987610, s58
BR 19
HS 25,123
v_lshrrev_b32 v3, 6, v0
v_and_b32 v3, 12, v3
v_bfe_u32 v3, s58, v3, 4
v_mad_u32_u24 v3, v3, v32, v21
M 10,62,2
s_mul_i32 s53, s50, 25600
LL 18,102
LL 127,103
s_add_u32 s40, %[wlo], s53
s_addc_u32 s41, %[whi], 0
s_mov_b32 m0, s38
M 10,63,3
WL 5
LA 18
PK 123,16,121,56
LS 40,18
WV 7
BW 20
M 9,50,2
LC_ 127
RV 40,8,6720
PK 121,16,121,97
M 9,51,3
WV 6
RA 216,1,19456
BR 20
DW 0
M 8,38,2
WL 6
LO 17,17,126
LU 126,17
ST 116
M 8,39,3
s_add_u32 s42, s44, s64
RA 220,1,20480
HS 25,126
s_addc_u32 s43, s45, 0
LS 44,19
s_add_u32 s37, s37, 32768
BW 21
M 8,40,4
s_cmp_eq_u32 s37, s75
LL 16,64
LL 112,65
s_cselect_b32 s37, s74, s37
M 8,41,5
LA 16
XL 24,0
DW 1024
RV 44,8,6784
PK 126,17,124,56
M 9,52,4
LC_ 112
BR 21
PK 124,17,124,97
M 9,53,5
WL 6
RA 224,1,21504
PO 126
LS 48,20
WV 9
BW 22
M 10,64,4
LO 18,18,127
LU 127,18
ST 120
M 10,65,5
WV 9
DW 2048
RV 48,8,6848
HS 25,127
M 0,76,4
WL 8
RA 240,7,0
LL 17,66
LL 113,67
M 0,77,5
s_add_u32 s40, s40, 3072
LA 17
BR 22
PK 127,18,125,56
M 0,78,0
s_addc_u32 s41, s41, 0
LC_ 113
PK 125,18,125,97
s_add_u32 m0, s38, 3072
M 0,79,1
WL 7
RA 228,1,22528
XL 25,64
PO 127
LS 40,21
s_add_u32 s42, s44, s65
BW 23
M 10,66,0
RA 244,7,256
LO 112,16,112
LU 16,112
M 10,67,1
s_addc_u32 s43, s45, 0
RA 232,1,23552
BR 23
HZ 24,16
M 9,54,0
WL 6
DW 0
LL 18,68
LL 116,69
M 9,55,1
s_lshl_b32 s53, s52, 8
LA 18
HX 25
LS 44,22
s_add_u32 s48, %[alo], s53
RA 248,7,512
M 8,42,0
WL 2
LC_ 116
W1 25,1024
M 8,43,1
s_addc_u32 s49, %[ahi], 0
RA 236,1,24576
v_add_u32 v4, s39, v4
LS 48,23
s_add_u32 s52, s52, %[d]
RA 252,7,768
M 8,44,2
LO 113,17,113
LU 17,113
s_cmp_eq_u32 s52, %[wrap]
s_cselect_b32 s52, %[rst], s52
DW 1024
v_mul_f32 v36, 0xbb2aaaab, v36
M 8,45,3
XL 26,128
HS 24,17
v_mul_f32 v37, 0xbb2aaaab, v37
M 9,56,2
LL 16,70
LL 117,71
v_mul_f32 v38, 0xbb2aaaab, v38
v_mul_f32 v39, 0xbb2aaaab, v39
M 9,57,3
LA 16
DW 2048
LK 40,240,12,0
LK 44,242,12,2
RA 240,7,1024
v_add_u32 v1, s37, v0
LK 48,244,13,0
s_nop 0
CF 176,40,41
LK 248,246,13,2
RA 244,7,1280
CF 177,42,43
M 10,68,2
LC_ 117
RA 216,1,0
M 10,69,3
ST 124
v_and_b32 v22, 0xfffffcff, v1
M 0,80,2
WL 6
LO 116,18,116
LU 18,116
M 0,81,3
s_mul_i32 s53, s51, 24
RA 220,1,1024
HS 24,18
s_bitcmp1_b32 s51, 0
s_addc_u32 s53, s53, 0
s_lshl_b32 s53, s53, 11
s_bitcmp1_b64 s[90:91], s51
s_cselect_b64 s[44:45], s[84:85], s[82:83]
s_add_u32 s44, s44, s53
s_addc_u32 s45, s45, 0
s_add_u32 s51, s51, %[d]
s_cmp_eq_u32 s51, %[wrap]
s_cselect_b32 s51, %[rst], s51
DQ 3072
CF 178,44,45
M 8,46,4
LL 17,72
LL 120,73
CF 179,46,47
M 8,47,5
LA 17
RV 252,22,5120
LK 40,248,14,0
CF 180,48,49
LK 44,250,14,2
RA 248,7,1536
CF 181,50,51
GZ 12
WL 9
CF 182,248,249
CF 183,250,251
LK 48,252,15,0
CF 184,40,41
LK 248,254,15,2
RA 252,7,1792
CF 185,42,43
GS 13
WL 8
CF 186,44,45
CF 187,46,47
LK 40,240,16,0
CF 188,48,49
LK 44,242,16,2
RA 240,7,2048
CF 189,50,51
GS 14
WL 8
CF 190,248,249
CF 191,250,251
LK 48,244,17,0
CF 192,40,41
LK 248,246,17,2
RA 244,7,2304
CF 193,42,43
GS 15
CF 194,44,45
CF 195,46,47
M 9,58,4
WL 3
LC_ 120
XL 27,192
M 9,59,5
s_add_u32 s50, s50, %[d]
RA 224,1,2048
s_cmp_eq_u32 s50, %[wrap]
s_cselect_b32 s50, %[rst], s50
v_add_u32 v19, 1, v19
LK 40,248,18,0
WL 4
CF 196,48,49
LK 44,250,18,2
s_add_u32 s38, s38, 32768
RA 248,7,2560
CF 197,50,51
GS 16
s_cmp_eq_u32 s38, s77
CF 198,248,249
CF 199,250,251
LK 48,252,19,0
CF 200,40,41
LK 248,254,19,2
s_cselect_b32 s38, s76, s38
RA 252,7,2816
CF 201,42,43
GS 17
WL 5
CF 202,44,45
CF 203,46,47
LK 40,240,20,0
CF 204,48,49
LK 44,242,20,2
CF 205,50,51
GS 18
CF 206,248,249
CF 207,250,251
M 10,70,4
LO 117,16,117
LU 16,117
M 10,71,5
FP
HS 24,16
M 0,82,4
LL 18,74
LL 121,75
M 0,83,5
s_sub_u32 s39, 0, s39
LA 18
RA 228,1,3072
Z 2,36,0
LC_ 121
XL 28,256
M 2,37,1
WL 8
LK 48,244,21,0
CF 208,40,41
LK 248,246,21,2
CF 209,42,43
GS 19
CF 210,44,45
CF 211,46,47
Z 3,48,0
WL 7
LO 120,17,120
LU 17,120
M 3,49,1
WL 5
RA 232,1,4096
HS 24,17
LK 40,248,22,0
CF 212,48,49
LK 44,250,22,2
CF 213,50,51
GS 20
CF 214,248,249
CF 215,250,251
Z 4,60,0
LL 16,76
LL 124,77
M 4,61,1
LA 16
FR
Z 6,72,0
WL 4
LC_ 124
XL 29,320
M 6,73,1
RA 236,1,6144
LK 48,252,23,0
CF 216,40,41
LK 248,254,23,2
CF 217,42,43
GS 21
CF 218,44,45
CF 219,46,47
M 6,74,2
WL 5
LO 121,18,121
LU 18,121
M 6,75,3
RV 36,8,6912
HS 24,18
M 4,62,2
LL 17,78
LL 125,79
)"
R"(M 4,63,3
LA 17
RA 216,1,7168
GS 22
M 3,50,2
WL 5
LC_ 125
v_cmp_gt_u32 vcc, v19, v15
M 3,51,3
XL 30,384
CF 220,48,49
M 2,38,2
WL 4
LO 124,16,124
LU 16,124
M 2,39,3
RA 220,1,8192
HS 24,16
M 2,40,4
s_cbranch_vccnz .Lk1s11
.Lk1r11:
LL 18,88
LL 114,89
M 2,41,5
LA 18
ds_read_b64 v[14:15], v5 offset:0
M 3,52,4
LC_ 114
v_subrev_u32 v5, s39, v5
M 3,53,5
RA 224,1,9216
CF 221,50,51
M 4,64,4
WL 5
LO 125,17,125
LU 17,125
M 4,65,5
XL 31,448
HS 24,17
M 6,76,4
WL 4
LL 16,90
LL 115,91
M 6,77,5
LA 16
RA 228,1,10240
M 6,78,0
LC_ 115
HX 24
M 6,79,1
W1 24,0
CF 222,248,249
M 4,66,0
WL 5
LO 18,18,114
LU 114,18
M 4,67,1
RA 232,1,11264
HZ 25,114
M 3,54,0
LL 17,92
LL 118,93
M 3,55,1
LA 17
PK 114,18,112,56
M 2,42,0
WL 4
LC_ 118
PK 112,18,112,97
M 2,43,1
RV 252,22,5392
v_add_f32 v14, v14, v15
M 2,44,2
WL 5
LO 16,16,115
LU 115,16
M 2,45,3
RA 236,1,12288
HS 25,115
M 3,56,2
LL 18,94
LL 119,95
M 3,57,3
LA 18
PK 115,16,113,56
M 4,68,2
LC_ 119
XL 32,512
PK 113,16,113,97
v_add_f32_dpp v14, v14, v14 quad_perm:[1,0,3,2] row_mask:0xf bank_mask:0xf
M 4,69,3
s_add_u32 s42, s44, s62
RA 216,1,13312
v_add_f32_dpp v14, v14, v14 quad_perm:[2,3,0,1] row_mask:0xf bank_mask:0xf
M 6,80,2
WL 3
LO 17,17,118
LU 118,17
CF 223,250,251
M 6,81,3
s_addc_u32 s43, s45, 0
RA 220,1,14336
HS 25,118
GS 23
ST 112
v_add_f32_dpp v14, v14, v14 row_half_mirror row_mask:0xf bank_mask:0xf
s_nop 5
v_cndmask_b32 v52, v52, v53, s[78:79]
M 2,46,4
LL 16,96
LL 122,97
v_cndmask_b32 v54, v54, v55, s[78:79]
M 2,47,5
LA 16
PK 118,17,116,56
v_cndmask_b32 v52, v52, v54, s[80:81]
M 3,58,4
LC_ 122
PK 116,17,116,97
v_cvt_f64_f32 v[14:15], v14
v_mul_f32 v52, 0x3b2aaaab, v52
M 3,59,5
RA 224,1,15360
PO 118
v_med3_f32 v53, v52, v39, v37
M 4,70,4
WL 5
LO 18,18,119
LU 119,18
s_mov_b64 exec, s[54:55]
SP
s_mov_b64 exec, -1
XL 33,576
LG 52,53
s_nop 0
C 52,52,52
M 4,71,5
s_add_u32 s42, s44, s63
HS 25,119
v_lshlrev_b32 v52, 16, v52
M 6,82,4
WL 3
LL 17,98
LL 123,99
v_add_f32 v52, v36, v52
M 6,83,5
s_addc_u32 s43, s45, 0
LA 17
PK 119,18,117,56
C 52,52,52
Z 8,36,0
LC_ 123
PK 117,18,117,97
v_lshlrev_b32 v52, 16, v52
M 8,37,1
RA 228,1,16384
PO 119
v_sqrt_f32 v52, v52
s_nop 0
C 52,52,52
Z 9,48,0
WL 4
LO 16,16,122
LU 122,16
v_lshlrev_b32 v52, 16, v52
M 9,49,1
RA 232,1,17408
HS 25,122
v_rcp_f32 v52, v52
Z 10,60,0
XL 34,640
LL 18,56
LL 126,57
v_mul_f32 v52, v38, v52
M 10,61,1
LA 18
PK 122,16,120,56
CF 52,52,52
Z 11,72,0
WL 2
LC_ 126
PK 120,16,120,97
ds_bpermute_b32 v13, v28, v52
M 11,73,1
RA 236,1,18432
M 11,74,2
LO 17,17,123
LU 123,17
M 11,75,3
ST 116
HS 25,123
M 10,62,2
s_mul_i32 s53, s50, 25600
LL 16,58
LL 127,59
s_add_u32 s40, %[wlo], s53
s_addc_u32 s41, %[whi], 0
s_mov_b32 m0, s38
M 10,63,3
LA 16
PK 123,17,121,56
M 9,50,2
LC_ 127
PK 121,17,121,97
M 9,51,3
RA 216,1,19456
XL 35,704
DW 0
M 8,38,2
WL 3
LO 18,18,126
LU 126,18
M 8,39,3
s_add_u32 s42, s44, s64
RA 220,1,20480
HS 25,126
s_addc_u32 s43, s45, 0
XS 176
M 8,40,4
s_add_u32 s37, s37, 32768
LL 17,64
LL 112,65
s_cmp_eq_u32 s37, s75
s_cselect_b32 s37, s74, s37
M 8,41,5
LA 17
DW 1024
PK 126,18,124,56
XS 177
M 9,52,4
LC_ 112
PK 124,18,124,97
M 9,53,5
RA 224,1,21504
PO 126
XS 178
M 10,64,4
WL 5
LO 16,16,127
LU 127,16
ST 120
M 10,65,5
DW 2048
HS 25,127
XS 179
M 11,76,4
WL 3
LL 18,66
LL 113,67
M 11,77,5
s_add_u32 s40, s40, 3072
LA 18
PK 127,16,125,56
XS 180
M 11,78,0
s_addc_u32 s41, s41, 0
LC_ 113
PK 125,16,125,97
s_add_u32 m0, s38, 3072
M 11,79,1
s_add_u32 s42, s44, s65
RA 228,1,22528
PO 127
XS 181
M 10,66,0
WL 4
LO 112,17,112
LU 17,112
M 10,67,1
s_addc_u32 s43, s45, 0
RA 232,1,23552
HZ 24,17
XS 182
M 9,54,0
DW 0
LL 16,68
LL 116,69
M 9,55,1
s_lshl_b32 s53, s52, 8
LA 16
HX 25
XS 183
M 8,42,0
WL 2
LC_ 116
W1 25,1024
M 8,43,1
s_add_u32 s48, %[alo], s53
RA 236,1,24576
v_add_u32 v4, s39, v4
v_min_u32 v35, v35, v13
M 8,44,2
LO 113,18,113
LU 18,113
s_addc_u32 s49, %[ahi], 0
s_add_u32 s52, s52, %[d]
s_cmp_eq_u32 s52, %[wrap]
s_cselect_b32 s52, %[rst], s52
DW 1024
M 8,45,3
ST 124
HS 24,18
s_bitcmp1_b64 s[92:93], 48
s_cselect_b32 s58, 16, 0
s_lshr_b32 s58, 0xda43cb52, s58
v_and_b32 v11, 0x1f0, v0
v_min_u32 v11, 368, v11
v_lshlrev_b32 v11, 3, v11
XS 184
M 9,56,2
s_mul_i32 s53, s51, 24
LL 17,70
LL 117,71
s_bitcmp1_b32 s51, 0
s_addc_u32 s53, s53, 0
s_lshl_b32 s53, s53, 11
s_bitcmp1_b64 s[90:91], s51
s_cselect_b64 s[44:45], s[84:85], s[82:83]
s_add_u32 s44, s44, s53
s_addc_u32 s45, s45, 0
s_add_u32 s51, s51, %[d]
s_cmp_eq_u32 s51, %[wrap]
s_cselect_b32 s51, %[rst], s51
M 9,57,3
LA 17
DW 2048
XS 185
M 10,68,2
LC_ 117
DQ 3072
M 10,69,3
s_add_u32 s50, s50, %[d]
v_add_u32 v1, s37, v0
XS 186
M 11,80,2
WL 2
LO 116,16,116
LU 16,116
M 11,81,3
s_cmp_eq_u32 s50, %[wrap]
RA 216,1,0
HS 24,16
XS 187
M 8,46,4
s_cselect_b32 s50, %[rst], s50
LL 18,72
LL 120,73
M 8,47,5
LA 18
v_and_b32 v22, 0xfffffcff, v1
XS 188
M 9,58,4
LC_ 120
RA 220,1,1024
M 9,59,5
RV 252,22,5120
v_add_u32 v19, 1, v19
XS 189
M 10,70,4
WL 5
LO 117,17,117
LU 17,117
M 10,71,5
s_add_u32 s38, s38, 32768
RA 224,1,2048
HS 24,17
XS 190
M 11,82,4
WL 1
LL 16,74
LL 121,75
XS 191
M 11,83,5
s_cmp_eq_u32 s38, s77
LA 16
RA 228,1,3072
s_cselect_b32 s38, s76, s38
FP
s_sub_u32 s39, 0, s39
Z 0,0,0
LC_ 121
XS 192
M 0,1,1
XS 193
XS 194
Z 1,12,0
LO 120,18,120
LU 18,120
M 1,13,1
RA 232,1,4096
HS 24,18
Z 2,36,0
LL 17,80
LL 124,81
M 2,37,1
LA 17
XS 195
Z 3,48,0
LC_ 124
XS 196
)"
R"(M 3,49,1
XS 197
XS 198
Z 4,60,0
WL 3
LO 121,16,121
LU 16,121
M 4,61,1
RA 236,1,6144
HS 24,16
Z 5,72,0
LL 18,82
LL 125,83
M 5,73,1
LA 18
XS 199
M 5,74,2
LC_ 125
XS 200
M 5,75,3
XS 201
XS 202
M 4,62,2
WL 3
LO 124,17,124
LU 17,124
M 4,63,3
RA 216,1,7168
HS 24,17
M 3,50,2
LL 16,88
LL 114,89
M 3,51,3
LA 16
FR
M 2,38,2
LC_ 114
v_lshrrev_b32 v3, 9, v0
M 2,39,3
WV 31
BW 24
M 1,14,2
WL 5
LO 125,18,125
LU 18,125
M 1,15,3
RA 220,1,8192
HS 24,18
M 0,2,2
LL 17,90
LL 115,91
XS 203
M 0,3,3
LA 17
RV 40,8,6144
M 0,4,4
LC_ 115
RV 44,8,6208
XS 204
M 0,5,5
RV 48,8,6272
BR 24
M 1,16,4
WL 8
LO 16,16,114
LU 114,16
XS 205
M 1,17,5
RA 224,1,9216
HZ 25,114
M 2,40,4
LL 18,92
LL 118,93
XS 206
M 2,41,5
LA 18
PK 114,16,112,56
XS 207
M 3,52,4
LC_ 118
PK 112,16,112,97
v_lshl_add_u32 v3, v3, 2, 0
v_bfe_u32 v3, s58, v3, 4
v_mad_u32_u24 v3, v3, v32, v11
M 3,53,5
WV 27
BW 25
XS 208
M 4,64,4
WL 7
LO 17,17,115
LU 115,17
M 4,65,5
s_add_u32 s42, s44, s62
RA 228,1,10240
HS 25,115
XS 209
M 5,76,4
LL 16,94
LL 119,95
M 5,77,5
s_addc_u32 s43, s45, 0
LA 16
PK 115,17,113,56
XS 210
M 5,78,0
LC_ 119
PK 113,17,113,97
M 5,79,1
WL 7
ST 112
BR 25
HX 24
XS 211
LZ 40,24
WV 25
BW 26
M 4,66,0
WL 6
LO 18,18,118
LU 118,18
M 4,67,1
RA 232,1,11264
HS 25,118
v_cmp_gt_u32 vcc, v19, v15
XS 212
M 3,54,0
LL 17,96
LL 122,97
M 3,55,1
LA 17
PK 118,18,116,56
XS 213
M 2,42,0
s_cbranch_vccnz .Lk1s12
.Lk1r12:
LC_ 122
PK 116,18,116,97
M 2,43,1
RV 252,22,5392
PO 118
XS 214
M 1,18,0
WL 6
LO 16,16,119
LU 119,16
M 1,19,1
s_add_u32 s42, s44, s63
RA 236,1,12288
HS 25,119
XS 215
M 0,6,0
LL 18,98
LL 123,99
M 0,7,1
s_addc_u32 s43, s45, 0
LA 18
PK 119,16,117,56
XS 216
M 0,8,2
LC_ 123
PK 117,16,117,97
M 0,9,3
W1 24,0
PO 119
XS 217
M 1,20,2
WL 5
LO 17,17,122
LU 122,17
M 1,21,3
RA 216,1,13312
HS 25,122
XS 218
M 2,44,2
LL 16,100
LL 126,101
M 2,45,3
LA 16
PK 122,17,120,56
XS 219
M 3,56,2
LC_ 126
PK 120,17,120,97
M 3,57,3
ST 116
RV 40,8,6336
XS 220
M 4,68,2
WL 5
LO 18,18,123
LU 123,18
M 4,69,3
RA 220,1,14336
HS 25,123
XS 221
M 5,80,2
LL 17,102
LL 127,103
M 5,81,3
LA 17
PK 123,18,121,56
XS 222
M 0,10,4
LC_ 127
PK 121,18,121,97
XS 223
M 0,11,5
WV 22
DY
BR 26
LS 44,25
s_add_u32 s42, s44, s64
BW 27
M 1,22,4
WL 6
LO 16,16,126
LU 126,16
M 1,23,5
s_addc_u32 s43, s45, 0
RA 224,1,15360
HS 25,126
M 2,46,4
LL 18,56
LL 104,57
M 2,47,5
LA 18
PK 126,16,124,56
M 3,58,4
LC_ 104
RV 44,8,6400
PK 124,16,124,97
M 3,59,5
WV 22
ST 120
BR 27
PO 126
M 4,70,4
WL 6
LO 17,17,127
LU 127,17
M 4,71,5
s_add_u32 s42, s44, s65
RA 228,1,16384
HS 25,127
LS 48,26
s_addc_u32 s43, s45, 0
BW 28
M 5,82,4
LL 16,58
LL 105,59
M 5,83,5
LA 16
RV 48,8,6464
PK 127,17,125,56
Z 6,0,0
LC_ 105
PK 125,17,125,97
M 6,1,1
ds_read_b64 v[14:15], v5 offset:0
BR 28
PO 127
Z 7,12,0
WL 9
LO 104,18,104
LU 18,104
M 7,13,1
WL 7
RA 232,1,17408
HZ 26,18
LS 40,27
WV 22
BW 29
Z 8,36,0
WV 21
LL 17,60
LL 108,61
M 8,37,1
WL 2
LA 17
ST 124
RV 40,8,6528
Z 9,48,0
s_mul_i32 s53, s50, 25600
LC_ 108
BR 29
M 9,49,1
s_add_u32 s40, %[wlo], s53
v_subrev_u32 v5, s39, v5
HX 25
LS 44,28
s_addc_u32 s41, %[whi], 0
BW 30
Z 10,60,0
LO 105,16,105
LU 16,105
M 10,61,1
s_mov_b32 m0, s38
RA 236,1,18432
HS 26,16
Z 11,72,0
RV 44,8,6592
LL 18,62
LL 109,63
M 11,73,1
LA 18
W1 25,1024
BR 30
M 11,74,2
LC_ 109
DW 0
M 11,75,3
WL 7
v_add_f32 v14, v14, v15
v_add_u32 v4, s39, v4
LS 48,29
WV 22
BW 31
M 10,62,2
LO 108,17,108
LU 17,108
M 10,63,3
s_add_u32 s37, s37, 32768
RA 216,1,19456
HS 26,17
M 9,50,2
s_cmp_eq_u32 s37, s75
RV 48,8,6656
LL 16,64
LL 112,65
M 9,51,3
s_cselect_b32 s37, s74, s37
LA 16
DW 1024
BR 31
M 8,38,2
LC_ 112
v_add_f32_dpp v14, v14, v14 quad_perm:[1,0,3,2] row_mask:0xf bank_mask:0xf
M 8,39,3
WL 7
v_add_f32_dpp v14, v14, v14 quad_perm:[2,3,0,1] row_mask:0xf bank_mask:0xf
v_lshrrev_b32 v3, 9, v0
LS 40,30
WV 22
BW 32
M 7,14,2
WL 7
LO 109,18,109
LU 18,109
M 7,15,3
RA 220,1,20480
RV 40,8,6720
HS 26,18
M 6,2,2
BR 32
LL 17,66
LL 113,67
M 6,3,3
LA 17
v_add_f32_dpp v14, v14, v14 row_half_mirror row_mask:0xf bank_mask:0xf
M 6,4,4
WL 6
LC_ 113
v_cvt_f64_f32 v[14:15], v14
M 6,5,5
s_mov_b64 exec, s[54:55]
SP
s_mov_b64 exec, -1
HX 26
LS 44,31
WV 20
BW 33
M 7,16,4
WL 7
RV 44,8,6784
LO 112,16,112
LU 16,112
M 7,17,5
RA 224,1,21504
HZ 24,16
M 8,40,4
BR 33
LL 18,68
LL 116,69
M 8,41,5
WL 6
LA 18
v_lshl_add_u32 v3, v3, 2, 8
LS 48,32
WV 19
BW 34
M 9,52,4
LC_ 116
RV 48,8,6848
M 9,53,5
RA 240,7,0
BR 34
M 10,64,4
WL 10
LO 113,17,113
)"
R"(LU 17,113
M 10,65,5
RA 228,1,22528
HS 24,17
v_bfe_u32 v3, s58, v3, 4
v_mad_u32_u24 v3, v3, v32, v11
M 11,76,4
WV 17
LL 16,70
LL 117,71
M 11,77,5
WL 7
LA 16
DW 2048
M 11,78,0
s_mul_i32 s53, s51, 24
LC_ 117
M 11,79,1
s_bitcmp1_b32 s51, 0
LS 40,33
s_addc_u32 s53, s53, 0
BW 35
M 10,66,0
WL 4
RA 244,7,256
LO 116,18,116
LU 18,116
M 10,67,1
s_lshl_b32 s53, s53, 11
RA 232,1,23552
HS 24,18
M 9,54,0
s_bitcmp1_b64 s[90:91], s51
BR 35
LL 17,72
LL 120,73
M 9,55,1
s_cselect_b64 s[44:45], s[84:85], s[82:83]
LA 17
s_add_u32 s44, s44, s53
s_addc_u32 s45, s45, 0
s_bitcmp1_b64 s[92:93], s51
s_cselect_b64 s[94:95], s[88:89], s[86:87]
s_add_u32 s94, s94, s53
s_addc_u32 s95, s95, 0
s_add_u32 s51, s51, %[d]
s_cmp_eq_u32 s51, %[wrap]
s_cselect_b32 s51, %[rst], s51
s_mul_i32 s53, %[d], 0
s_add_u32 s60, s53, %[sb1]
s_add_u32 s61, s60, 512
LS 44,34
s_add_u32 s40, s40, 3072
RA 248,7,512
M 8,42,0
s_addc_u32 s41, s41, 0
LC_ 120
s_add_u32 m0, s38, 3072
M 8,43,1
DW 0
M 7,18,0
WL 7
LO 117,16,117
LU 16,117
M 7,19,1
WL 3
RA 236,1,24576
HS 24,16
LS 48,35
RA 252,7,768
M 6,6,0
LL 18,74
LL 121,75
s_nop 3
v_mul_f32 v36, 0xbb2aaaab, v36
M 6,7,1
LA 18
DW 1024
v_mul_f32 v37, 0xbb2aaaab, v37
v_mul_f32 v38, 0xbb2aaaab, v38
M 6,8,2
LC_ 121
DW 2048
v_mul_f32 v39, 0xbb2aaaab, v39
M 6,9,3
s_lshl_b32 s53, s52, 8
v_add_u32 v1, s37, v0
LK 40,240,24,0
LK 44,242,24,2
s_add_u32 s48, %[alo], s53
RA 240,7,1024
LK 48,244,25,0
s_nop 0
CF 224,40,41
LK 248,246,25,2
s_addc_u32 s49, %[ahi], 0
RA 244,7,1280
CF 225,42,43
M 7,20,2
WL 6
LO 120,17,120
LU 17,120
M 7,21,3
s_add_u32 s52, s52, %[d]
RA 216,1,0
HS 24,17
M 8,44,2
s_cmp_eq_u32 s52, %[wrap]
LL 16,76
LL 124,77
CF 226,44,45
M 8,45,3
s_cselect_b32 s52, %[rst], s52
LA 16
DQ 3072
CF 227,46,47
LK 40,248,26,0
CF 228,48,49
LK 44,250,26,2
RA 248,7,1536
CF 229,50,51
GZ 24
WL 7
CF 230,248,249
CF 231,250,251
LK 48,252,27,0
CF 232,40,41
LK 248,254,27,2
RA 252,7,1792
CF 233,42,43
GS 25
WL 5
CF 234,44,45
CF 235,46,47
LK 40,240,28,0
CF 236,48,49
LK 44,242,28,2
RA 240,7,2048
CF 237,50,51
GS 26
WL 5
CF 238,248,249
CF 239,250,251
LK 48,244,29,0
CF 240,40,41
LK 248,246,29,2
RA 244,7,2304
CF 241,42,43
GS 27
CF 242,44,45
CF 243,46,47
M 9,56,2
LC_ 124
v_and_b32 v22, 0xfffffcff, v1
M 9,57,3
WL 4
LK 40,248,30,0
CF 244,48,49
LK 44,250,30,2
RA 248,7,2560
CF 245,50,51
GS 28
WL 4
CF 246,248,249
CF 247,250,251
LK 48,252,31,0
CF 40,40,41
LK 248,254,31,2
RA 252,7,2816
CF 42,42,43
GS 29
CF 44,44,45
CF 46,46,47
M 10,68,2
LO 121,18,121
LU 18,121
M 10,69,3
RA 220,1,1024
HS 24,18
M 11,80,2
LL 17,78
LL 125,79
M 11,81,3
LA 17
RV 252,22,5120
M 6,10,4
LC_ 125
DY
M 6,11,5
s_add_u32 s50, s50, %[d]
AW 0,40
AW 1,42
M 7,22,4
WL 6
LO 124,16,124
LU 16,124
M 7,23,5
s_cmp_eq_u32 s50, %[wrap]
RA 224,1,2048
HS 24,16
M 8,46,4
s_cselect_b32 s50, %[rst], s50
LL 18,80
LL 106,81
M 8,47,5
LA 18
v_add_u32 v19, 1, v19
M 9,58,4
s_add_u32 s38, s38, 32768
LC_ 106
AW 2,44
M 9,59,5
AW 3,46
LK 40,240,32,0
CF 48,48,49
LK 44,242,32,2
CF 50,50,51
GS 30
CF 248,248,249
CF 250,250,251
M 10,70,4
WL 3
LO 125,17,125
LU 17,125
M 10,71,5
RA 228,1,3072
HS 24,17
M 11,82,4
LL 16,82
LL 107,83
M 11,83,5
LA 16
HX 24
Z 0,0,0
s_cmp_eq_u32 s38, s77
LC_ 107
AW 4,48
M 0,1,1
s_cselect_b32 s38, s76, s38
FP
AW 5,50
Z 1,12,0
WL 4
LO 18,18,106
LU 106,18
M 1,13,1
s_sub_u32 s39, 0, s39
RA 232,1,4096
HZ 27,106
Z 2,36,0
LL 17,84
LL 110,85
M 2,37,1
LA 17
PK 106,18,104,56
Z 3,48,0
LC_ 110
PK 104,18,104,97
M 3,49,1
AW 6,248
AW 7,250
LK 48,244,33,0
CF 40,40,41
LK 248,246,33,2
CF 42,42,43
GS 31
CF 44,44,45
CF 46,46,47
Z 4,60,0
WL 4
LO 16,16,107
LU 107,16
M 4,61,1
s_add_u32 s42, s94, s60
RA 236,1,6144
HS 27,107
Z 5,72,0
LL 18,86
LL 111,87
M 5,73,1
s_addc_u32 s43, s95, 0
LA 18
PK 107,16,105,56
M 5,74,2
LC_ 111
PK 105,16,105,97
M 5,75,3
SU 104
AW 8,40
M 4,62,2
WL 3
LO 17,17,110
LU 110,17
M 4,63,3
RA 216,1,7168
HS 27,110
M 3,50,2
LL 16,88
LL 114,89
M 3,51,3
LA 16
PK 110,17,108,56
AW 9,42
AW 10,44
M 2,38,2
LC_ 114
PK 108,17,108,97
AW 11,46
M 2,39,3
FR
PO 110
LK 40,248,34,0
CF 48,48,49
LK 44,250,34,2
CF 50,50,51
GS 32
CF 248,248,249
CF 250,250,251
M 1,14,2
WL 4
LO 18,18,111
LU 111,18
M 1,15,3
s_add_u32 s42, s94, s61
RA 220,1,8192
HS 27,111
M 0,2,2
LL 17,90
LL 115,91
AW 12,48
AW 13,50
M 0,3,3
s_addc_u32 s43, s95, 0
LA 17
PK 111,18,109,56
AW 14,248
AW 15,250
LK 48,252,35,0
CF 40,40,41
LK 248,254,35,2
s_mov_b32 m0, %[pm]
CF 42,42,43
HX 27
GS 33
CF 44,44,45
CF 46,46,47
M 0,4,4
LC_ 115
PK 109,18,109,97
M 0,5,5
DF
PO 111
M 1,16,4
WL 4
LO 16,16,114
LU 114,16
M 1,17,5
RA 224,1,9216
HZ 25,114
AW 16,40
M 2,40,4
LL 18,92
LL 118,93
AW 17,42
AW 18,44
M 2,41,5
LA 18
RV 36,8,6912
PK 114,16,112,56
AW 19,46
M 3,52,4
LC_ 118
PK 112,16,112,97
M 3,53,5
WL 7
SU 108
v_cmp_gt_u32 vcc, v19, v15
GS 34
CF 48,48,49
CF 50,50,51
CF 248,248,249
M 4,64,4
WL 4
LO 17,17,115
LU 115,17
CF 250,250,251
AW 20,48
M 4,65,5
s_add_u32 s42, s44, s62
RA 228,1,10240
HS 25,115
AW 21,50
AW 22,248
M 5,76,4
LL 16,94
LL 119,95
AW 23,250
M 5,77,5
s_cbranch_vccnz .Lk1s13
.Lk1r13:
)"
R"(LA 16
PK 115,17,113,56
GS 35
s_addc_u32 s43, s45, 0
W1 24,0
s_nop 5
v_cndmask_b32 v52, v52, v53, s[78:79]
M 5,78,0
LC_ 119
PK 113,17,113,97
v_cndmask_b32 v54, v54, v55, s[78:79]
M 5,79,1
W1 26,2048
v_cndmask_b32 v52, v52, v54, s[80:81]
M 4,66,0
WL 5
LO 18,18,118
LU 118,18
v_mul_f32 v52, 0x3b2aaaab, v52
M 4,67,1
RA 232,1,11264
HS 25,118
v_med3_f32 v53, v52, v39, v37
M 3,54,0
LL 17,96
LL 122,97
LG 52,53
M 3,55,1
LA 17
PK 118,18,116,56
C 52,52,52
M 2,42,0
LC_ 122
PK 116,18,116,97
v_lshlrev_b32 v52, 16, v52
M 2,43,1
RV 252,22,5392
PO 118
v_add_f32 v52, v36, v52
M 1,18,0
WL 5
LO 16,16,119
LU 119,16
C 52,52,52
M 1,19,1
RA 236,1,12288
HS 25,119
M 0,6,0
LL 18,98
LL 123,99
v_lshlrev_b32 v52, 16, v52
M 0,7,1
LA 18
PK 119,16,117,56
v_sqrt_f32 v52, v52
M 0,8,2
LC_ 123
PK 117,16,117,97
C 52,52,52
M 0,9,3
ST 112
PO 119
v_lshlrev_b32 v52, 16, v52
M 1,20,2
WL 4
LO 17,17,122
LU 122,17
v_rcp_f32 v52, v52
M 1,21,3
s_add_u32 s42, s44, s63
RA 216,1,13312
HS 25,122
v_mul_f32 v52, v38, v52
M 2,44,2
s_addc_u32 s43, s45, 0
LL 16,100
LL 126,101
CF 52,52,52
M 2,45,3
LA 16
PK 122,17,120,56
ds_bpermute_b32 v13, v28, v52
M 3,56,2
LC_ 126
PK 120,17,120,97
M 3,57,3
ST 116
AR 248,0
M 4,68,2
WL 4
LO 18,18,123
LU 123,18
M 4,69,3
s_add_u32 s42, s44, s64
RA 220,1,14336
HS 25,123
M 5,80,2
LL 17,102
LL 127,103
M 5,81,3
s_addc_u32 s43, s45, 0
LA 17
PK 123,18,121,56
M 0,10,4
LC_ 127
PK 121,18,121,97
M 0,11,5
ST 120
AR 249,1
M 1,22,4
WL 3
LO 16,16,126
LU 126,16
M 1,23,5
RA 224,1,15360
HS 25,126
M 2,46,4
LL 18,56
LL 104,57
M 2,47,5
LA 18
PK 126,16,124,56
M 3,58,4
LC_ 104
PK 124,16,124,97
AR 250,2
M 3,59,5
ds_read_b64 v[14:15], v5 offset:0
PO 126
M 4,70,4
WL 4
LO 17,17,127
LU 127,17
AR 251,3
M 4,71,5
s_add_u32 s42, s44, s65
RA 228,1,16384
HS 25,127
AR 52,4
M 5,82,4
LL 16,58
LL 105,59
AR 53,5
M 5,83,5
s_addc_u32 s43, s45, 0
LA 16
PK 127,17,125,56
Z 6,0,0
LC_ 105
PK 125,17,125,97
AR 54,6
M 6,1,1
PO 127
v_subrev_u32 v5, s39, v5
AR 55,7
Z 7,12,0
WL 4
LO 104,18,104
LU 18,104
XS 224
M 7,13,1
RA 232,1,17408
HZ 26,18
Z 8,36,0
WL 4
LL 17,60
LL 108,61
XS 225
M 8,37,1
s_mul_i32 s53, s50, 25600
LA 17
v_add_f32 v14, v14, v15
XS 226
Z 9,48,0
s_add_u32 s40, %[wlo], s53
LC_ 108
v_add_f32_dpp v14, v14, v14 quad_perm:[1,0,3,2] row_mask:0xf bank_mask:0xf
M 9,49,1
s_addc_u32 s41, %[whi], 0
v_add_f32_dpp v14, v14, v14 quad_perm:[2,3,0,1] row_mask:0xf bank_mask:0xf
HX 25
XS 227
Z 10,60,0
WL 3
LO 105,16,105
LU 16,105
XS 228
M 10,61,1
s_mov_b32 m0, s38
RA 236,1,18432
HS 26,16
XS 229
Z 11,72,0
LL 18,62
LL 109,63
M 11,73,1
LA 18
W1 25,1024
XS 230
M 11,74,2
LC_ 109
W1 27,3072
XS 231
M 11,75,3
ST 124
v_add_f32_dpp v14, v14, v14 row_half_mirror row_mask:0xf bank_mask:0xf
v_min_u32 v35, v35, v13
M 10,62,2
WL 5
LO 108,17,108
LU 17,108
M 10,63,3
RA 216,1,19456
HS 26,17
XS 232
M 9,50,2
LL 16,64
LL 112,65
XS 233
M 9,51,3
LA 16
DW 0
XS 234
M 8,38,2
s_add_u32 s37, s37, 32768
LC_ 112
v_cvt_f64_f32 v[14:15], v14
M 8,39,3
s_cmp_eq_u32 s37, s75
s_mov_b64 exec, s[54:55]
SP
s_mov_b64 exec, -1
v_add_u32 v4, s39, v4
XS 235
M 7,14,2
WL 4
LO 109,18,109
LU 18,109
XS 236
M 7,15,3
s_cselect_b32 s37, s74, s37
RA 220,1,20480
HS 26,18
M 6,2,2
LL 17,66
LL 113,67
XS 237
M 6,3,3
s_mul_i32 s53, %[d], 196608
LA 17
HX 26
XS 238
M 6,4,4
s_add_u32 s53, s53, s57
LC_ 113
XS 239
M 6,5,5
s_add_u32 s46, %[xlo], s53
XS 240
XS 241
M 7,16,4
WL 3
LO 112,16,112
LU 16,112
M 7,17,5
s_addc_u32 s47, %[xhi], 0
RA 224,1,21504
HZ 24,16
M 8,40,4
s_bitcmp1_b64 s[92:93], 48
LL 18,68
LL 116,69
XS 242
M 8,41,5
s_cselect_b32 s58, 16, 0
LA 18
XS 243
M 9,52,4
s_lshr_b32 s58, 0xda43cb52, s58
LC_ 116
v_lshrrev_b32 v3, 6, v0
v_and_b32 v3, 12, v3
v_bfe_u32 v3, s58, v3, 4
v_mad_u32_u24 v3, v3, v32, v21
M 9,53,5
DW 1024
XS 244
M 10,64,4
WL 3
LO 113,17,113
LU 17,113
XS 245
M 10,65,5
RA 228,1,22528
HS 24,17
M 11,76,4
LL 16,70
LL 117,71
XS 246
M 11,77,5
LA 16
XL 36,0
XS 247
M 11,78,0
LC_ 117
DW 2048
M 11,79,1
XL 37,64
XS 248
M 10,66,0
WL 3
LO 116,18,116
LU 18,116
XS 249
M 10,67,1
s_mul_i32 s53, s51, 24
RA 232,1,23552
HS 24,18
XS 250
M 9,54,0
s_bitcmp1_b32 s51, 0
LL 17,72
LL 120,73
M 9,55,1
s_addc_u32 s53, s53, 0
LA 17
s_lshl_b32 s53, s53, 11
s_bitcmp1_b64 s[90:91], s51
s_cselect_b64 s[44:45], s[84:85], s[82:83]
s_add_u32 s44, s44, s53
s_addc_u32 s45, s45, 0
s_bitcmp1_b64 s[92:93], s51
s_cselect_b64 s[94:95], s[88:89], s[86:87]
s_add_u32 s94, s94, s53
s_addc_u32 s95, s95, 0
s_add_u32 s51, s51, %[d]
s_cmp_eq_u32 s51, %[wrap]
s_cselect_b32 s51, %[rst], s51
XS 251
M 8,42,0
s_add_u32 s40, s40, 3072
LC_ 120
s_addc_u32 s41, s41, 0
s_add_u32 m0, s38, 3072
AW 0,248
M 8,43,1
XL 38,128
AW 1,249
M 7,18,0
WL 3
LO 117,16,117
LU 16,117
M 7,19,1
RA 236,1,24576
HS 24,16
AW 2,250
M 6,6,0
LL 18,74
LL 121,75
AW 3,251
M 6,7,1
LA 18
DW 0
XS 52
M 6,8,2
LC_ 121
XL 39,192
M 6,9,3
DW 1024
v_add_u32 v1, s37, v0
XS 53
M 7,20,2
WL 3
LO 120,17,120
LU 17,120
XS 54
M 7,21,3
RA 216,1,0
HS 24,17
M 8,44,2
LL 16,76
LL 124,77
XS 55
M 8,45,3
LA 16
XL 40,256
AW 4,52
M 9,56,2
LC_ 124
DW 2048
AW 5,53
M 9,57,3
XL 41,320
v_and_b32 v22, 0xfffffcff, v1
M 10,68,2
WL 3
LO 121,18,121
LU 18,121
AW 6,54
M 10,69,3
)"
R"(s_lshl_b32 s53, s52, 8
RA 220,1,1024
HS 24,18
AW 7,55
M 11,80,2
LL 17,78
LL 125,79
AR 248,8
M 11,81,3
s_add_u32 s48, %[alo], s53
LA 17
RV 252,22,5120
M 6,10,4
s_addc_u32 s49, %[ahi], 0
LC_ 125
AR 249,9
M 6,11,5
s_add_u32 s52, s52, %[d]
AR 250,10
AR 251,11
M 7,22,4
WL 4
LO 124,16,124
LU 16,124
M 7,23,5
s_cmp_eq_u32 s52, %[wrap]
RA 224,1,2048
HS 24,16
M 8,46,4
s_cselect_b32 s52, %[rst], s52
LL 18,80
LL 106,81
XS 248
M 8,47,5
LA 18
DQ 3072
XS 249
M 9,58,4
LC_ 106
XL 42,384
M 9,59,5
s_add_u32 s50, s50, %[d]
XS 250
XS 251
M 10,70,4
WL 3
LO 125,17,125
LU 17,125
M 10,71,5
RA 228,1,3072
HS 24,17
AW 8,248
M 11,82,4
LL 16,82
LL 107,83
M 11,83,5
LA 16
HX 24
s_cmp_eq_u32 s50, %[wrap]
s_cselect_b32 s50, %[rst], s50
XL 43,448
AW 9,249
Z 0,0,0
WV 37
LC_ 107
XL 44,512
v_add_u32 v19, 1, v19
s_add_u32 s38, s38, 32768
s_cmp_eq_u32 s38, s77
s_cselect_b32 s38, s76, s38
FP
s_sub_u32 s39, 0, s39
AW 10,250
M 0,1,1
XL 45,576
AW 11,251
Z 1,12,0
WL 4
LO 18,18,106
LU 106,18
M 1,13,1
RA 232,1,4096
HZ 27,106
AR 52,12
Z 4,60,0
LL 17,84
LL 110,85
AR 53,13
M 4,61,1
LA 17
PK 106,18,104,56
AR 54,14
Z 5,72,0
WL 5
LC_ 110
XL 46,640
PK 104,18,104,97
M 5,73,1
RA 236,1,6144
AR 55,15
M 5,74,2
WL 5
LO 16,16,107
LU 107,16
XS 52
M 5,75,3
FR
HS 27,107
M 4,62,2
LL 18,86
LL 111,87
XS 53
M 4,63,3
LA 18
PK 107,16,105,56
XS 54
M 1,14,2
LC_ 111
XL 47,704
PK 105,16,105,97
XS 55
M 1,15,3
s_add_u32 s42, s94, s60
RA 216,1,7168
AW 12,52
M 0,2,2
WL 4
LO 17,17,110
LU 110,17
M 0,3,3
s_addc_u32 s43, s95, 0
RA 220,1,8192
HS 27,110
AW 13,53
M 0,4,4
LL 16,88
LL 114,89
AW 14,54
M 0,5,5
LA 16
PK 110,17,108,56
M 1,16,4
LC_ 114
PK 108,17,108,97
AW 15,55
M 1,17,5
WV 16
RA 224,1,9216
PO 110
AR 248,16
M 4,64,4
WL 5
LO 18,18,111
LU 111,18
SU 104
AR 249,17
M 4,65,5
s_mul_i32 s53, %[d], 393216
HS 27,111
v_cmp_gt_u32 vcc, v19, v15
M 5,76,4
WL 3
LL 17,90
LL 115,91
s_add_u32 s53, s53, s57
s_add_u32 s46, %[xlo], s53
s_addc_u32 s47, %[xhi], 0
s_bitcmp1_b64 s[92:93], 48
s_cselect_b32 s58, 16, 0
s_lshr_b32 s58, 0xfe987610, s58
v_lshrrev_b32 v3, 4, v0
v_and_b32 v3, 12, v3
v_bfe_u32 v3, s58, v3, 4
v_bfe_u32 v11, v0, 4, 2
v_lshl_add_u32 v3, v3, 2, v11
v_mul_u32_u24 v3, 0x300, v3
v_lshrrev_b32 v11, 8, v0
v_lshl_add_u32 v3, v11, 4, v3
AR 250,18
M 5,77,5
s_add_u32 s42, s94, s61
LA 17
PK 111,18,109,56
AR 251,19
M 5,78,0
s_cbranch_vccnz .Lk1s14
.Lk1r14:
LC_ 115
PK 109,18,109,97
M 5,79,1
s_addc_u32 s43, s95, 0
RA 228,1,10240
PO 111
XS 248
BW 36
M 4,66,0
WL 5
LO 16,16,114
LU 114,16
XS 249
M 4,67,1
RA 232,1,11264
RV 40,8,6144
HZ 25,114
XS 250
M 1,18,0
RV 44,8,6208
LL 18,92
LL 118,93
M 1,19,1
LA 18
RV 48,8,6272
PK 114,16,112,56
XS 251
M 0,6,0
WL 6
LC_ 118
BR 36
PK 112,16,112,97
AW 16,248
M 0,7,1
WV 15
RV 252,22,5392
ds_read_b64 v[14:15], v5 offset:0
W1 24,0
W1 26,2048
HX 27
BW 37
AW 17,249
M 0,8,2
BR 37
LO 17,17,115
LU 115,17
M 0,9,3
WL 6
RA 236,1,12288
HS 25,115
AW 18,250
LZ 40,36
WV 14
BW 38
M 1,20,2
LL 16,94
LL 119,95
AW 19,251
M 1,21,3
LA 16
RV 40,8,6336
PK 115,17,113,56
AR 52,20
M 4,68,2
LC_ 119
BR 38
PK 113,17,113,97
M 4,69,3
WL 6
RA 216,1,13312
AR 53,21
LS 44,37
WV 12
BW 39
M 5,80,2
WL 7
LO 18,18,118
LU 118,18
SU 108
AR 54,22
M 5,81,3
s_add_u32 s42, s44, s62
RA 220,1,14336
HS 25,118
s_addc_u32 s43, s45, 0
XL 48,0
RV 44,8,6400
M 0,10,4
BR 39
LL 17,96
LL 122,97
AR 55,23
M 0,11,5
WL 5
LA 17
PK 118,18,116,56
XS 52
LS 48,38
WV 12
BW 40
M 1,22,4
LC_ 122
PK 116,18,116,97
XS 53
M 1,23,5
RA 224,1,15360
RV 48,8,6464
PO 118
M 4,70,4
LO 16,16,119
LU 119,16
ST 112
XS 54
M 4,71,5
s_add_u32 s42, s44, s63
BR 40
HS 25,119
XS 55
M 5,82,4
WL 6
LL 18,98
LL 123,99
AW 20,52
M 5,83,5
s_addc_u32 s43, s45, 0
LA 18
PK 119,16,117,56
LS 40,39
WV 11
BW 41
Z 6,0,0
LC_ 123
PK 117,16,117,97
v_add_f32 v14, v14, v15
s_nop 1
v_add_f32_dpp v14, v14, v14 quad_perm:[1,0,3,2] row_mask:0xf bank_mask:0xf
s_nop 1
v_add_f32_dpp v14, v14, v14 quad_perm:[2,3,0,1] row_mask:0xf bank_mask:0xf
s_nop 1
v_add_f32_dpp v14, v14, v14 row_half_mirror row_mask:0xf bank_mask:0xf
v_cvt_f64_f32 v[14:15], v14
s_mov_b64 exec, s[54:55]
SP
s_mov_b64 exec, -1
s_nop 0
ds_read_b64 v[14:15], v5 offset:2048
RV 40,8,6528
AW 21,53
M 6,1,1
RA 228,1,16384
BR 41
PO 119
AW 22,54
Z 7,12,0
WL 10
LO 17,17,122
LU 122,17
AW 23,55
M 7,13,1
WL 7
RA 232,1,17408
HS 25,122
LS 44,40
WV 10
BW 42
Z 10,60,0
RV 44,8,6592
LL 16,100
LL 126,101
M 10,61,1
LA 16
BR 42
PK 122,17,120,56
Z 2,72,0
WL 6
LC_ 126
PK 120,17,120,97
M 2,73,1
WL 6
RA 236,1,18432
v_subrev_u32 v5, s39, v5
LS 48,41
WV 9
BW 43
M 2,74,2
RV 48,8,6656
LO 18,18,123
LU 123,18
M 2,75,3
XL 49,64
BR 43
HS 25,123
M 10,62,2
s_mul_i32 s53, s50, 25600
LL 17,102
LL 127,103
s_add_u32 s40, %[wlo], s53
s_addc_u32 s41, %[whi], 0
s_mov_b32 m0, s38
M 10,63,3
WL 5
LA 17
PK 123,18,121,56
LS 40,42
WV 9
BW 44
M 7,14,2
LC_ 127
RV 40,8,6720
PK 121,18,121,97
M 7,15,3
WV 8
RA 216,1,19456
)"
R"(BR 44
DW 0
M 6,2,2
WL 6
LO 16,16,126
LU 126,16
ST 116
M 6,3,3
s_add_u32 s42, s44, s64
RA 220,1,20480
HS 25,126
s_addc_u32 s43, s45, 0
LS 44,43
s_add_u32 s37, s37, 32768
BW 45
M 6,4,4
s_cmp_eq_u32 s37, s75
LL 18,56
LL 104,57
s_cselect_b32 s37, s74, s37
M 6,5,5
LA 18
PK 126,16,124,56
v_add_f32 v14, v14, v15
s_nop 1
v_add_f32_dpp v14, v14, v14 quad_perm:[1,0,3,2] row_mask:0xf bank_mask:0xf
s_nop 1
v_add_f32_dpp v14, v14, v14 quad_perm:[2,3,0,1] row_mask:0xf bank_mask:0xf
s_nop 1
v_add_f32_dpp v14, v14, v14 row_half_mirror row_mask:0xf bank_mask:0xf
v_cvt_f64_f32 v[14:15], v14
s_mov_b64 exec, s[54:55]
SP
s_mov_b64 exec, -1
DW 1024
RV 44,8,6784
M 7,16,4
LC_ 104
BR 45
PK 124,16,124,97
M 7,17,5
WL 6
RA 224,1,21504
PO 126
LS 48,44
WV 11
BW 46
M 10,64,4
LO 17,17,127
LU 127,17
ST 120
M 10,65,5
DW 2048
RV 48,8,6848
HS 25,127
M 2,76,4
WL 8
RA 240,7,0
LL 16,58
LL 105,59
M 2,77,5
s_add_u32 s40, s40, 3072
LA 16
BR 46
PK 127,17,125,56
M 2,78,0
s_addc_u32 s41, s41, 0
LC_ 105
PK 125,17,125,97
s_add_u32 m0, s38, 3072
M 2,79,1
WL 7
RA 228,1,22528
PO 127
LS 40,45
WV 12
BW 47
M 10,66,0
RA 244,7,256
LO 104,18,104
LU 18,104
M 10,67,1
WL 4
RA 232,1,23552
XL 50,128
BR 47
HZ 26,18
M 7,18,0
s_add_u32 s42, s44, s65
LL 17,60
LL 108,61
s_addc_u32 s43, s45, 0
DW 0
M 7,19,1
s_lshl_b32 s53, s52, 8
LA 17
HX 25
LS 44,46
s_add_u32 s48, %[alo], s53
RA 248,7,512
M 6,6,0
WL 2
LC_ 108
W1 25,1024
W1 27,3072
M 6,7,1
s_addc_u32 s49, %[ahi], 0
RA 236,1,24576
v_add_u32 v4, s39, v4
LS 48,47
s_add_u32 s52, s52, %[d]
RA 252,7,768
M 6,8,2
LO 105,16,105
LU 16,105
s_cmp_eq_u32 s52, %[wrap]
s_cselect_b32 s52, %[rst], s52
DW 1024
v_mul_f32 v36, 0xbb2aaaab, v36
M 6,9,3
DW 2048
HS 26,16
v_mul_f32 v37, 0xbb2aaaab, v37
v_mul_f32 v38, 0xbb2aaaab, v38
M 7,20,2
LL 18,62
LL 109,63
v_mul_f32 v39, 0xbb2aaaab, v39
M 7,21,3
LA 18
ST 124
LK 40,240,36,0
LK 44,242,36,2
s_mul_i32 s53, s51, 24
RA 240,7,1024
v_add_u32 v1, s37, v0
LK 48,244,37,0
CF 40,40,41
LK 248,246,37,2
s_bitcmp1_b32 s51, 0
RA 244,7,1280
CF 42,42,43
M 10,68,2
s_addc_u32 s53, s53, 0
LC_ 109
RA 216,1,0
M 10,69,3
s_lshl_b32 s53, s53, 11
v_and_b32 v22, 0xfffffcff, v1
CF 44,44,45
CF 46,46,47
M 2,80,2
WL 6
LO 108,17,108
LU 17,108
AW 24,40
AW 25,42
M 2,81,3
s_bitcmp1_b64 s[90:91], s51
RA 220,1,1024
HS 26,17
s_cselect_b64 s[44:45], s[84:85], s[82:83]
s_add_u32 s44, s44, s53
s_addc_u32 s45, s45, 0
s_bitcmp1_b64 s[92:93], s51
s_cselect_b64 s[94:95], s[88:89], s[86:87]
s_add_u32 s94, s94, s53
s_addc_u32 s95, s95, 0
s_add_u32 s51, s51, %[d]
s_cmp_eq_u32 s51, %[wrap]
s_cselect_b32 s51, %[rst], s51
DQ 3072
AW 26,44
AW 27,46
LK 40,248,38,0
CF 48,48,49
LK 44,250,38,2
s_add_u32 s50, s50, %[d]
RA 248,7,1536
CF 50,50,51
GZ 36
s_cmp_eq_u32 s50, %[wrap]
CF 248,248,249
CF 250,250,251
M 6,10,4
s_cselect_b32 s50, %[rst], s50
LL 16,72
LL 120,73
M 6,11,5
LA 16
RV 252,22,5120
M 7,22,4
LC_ 120
v_add_u32 v19, 1, v19
AW 28,48
M 7,23,5
WL 7
RA 224,1,2048
AW 29,50
AW 30,248
AW 31,250
LK 48,252,39,0
CF 40,40,41
LK 248,254,39,2
RA 252,7,1792
CF 42,42,43
GS 37
CF 44,44,45
CF 46,46,47
M 10,70,4
WL 7
LO 109,18,109
LU 18,109
M 10,71,5
XL 51,192
HS 26,18
M 2,82,4
WL 3
LL 17,74
LL 121,75
M 2,83,5
s_add_u32 s38, s38, 32768
LA 17
RA 228,1,3072
s_cmp_eq_u32 s38, s77
s_cselect_b32 s38, s76, s38
FP
s_sub_u32 s39, 0, s39
AW 32,40
AW 33,42
Z 0,0,0
LC_ 121
HX 26
AW 34,44
AW 35,46
M 0,1,1
LK 40,240,40,0
CF 48,48,49
LK 44,242,40,2
RA 240,7,2048
CF 50,50,51
GS 38
CF 248,248,249
CF 250,250,251
Z 1,12,0
WL 7
LO 120,16,120
LU 16,120
M 1,13,1
RA 232,1,4096
HZ 24,16
AW 36,48
Z 4,60,0
LL 18,76
LL 124,77
AW 37,50
AW 38,248
M 4,61,1
LA 18
FR
AW 39,250
LK 48,244,41,0
CF 40,40,41
LK 248,246,41,2
RA 244,7,2304
CF 42,42,43
GS 39
CF 44,44,45
CF 46,46,47
Z 8,72,0
WL 7
LC_ 124
AW 40,40
M 8,73,1
RA 236,1,6144
AW 41,42
M 8,74,2
WL 7
LO 121,17,121
LU 17,121
M 8,75,3
XL 52,256
HS 24,17
AW 42,44
AW 43,46
LK 40,248,42,0
CF 48,48,49
LK 44,250,42,2
RA 248,7,2560
CF 50,50,51
GS 40
CF 248,248,249
CF 250,250,251
M 4,62,2
LL 16,78
LL 125,79
M 4,63,3
LA 16
RA 216,1,7168
M 1,14,2
WL 6
LC_ 125
v_cmp_gt_u32 vcc, v19, v15
M 1,15,3
AW 44,48
AW 45,50
AW 46,248
M 0,2,2
WL 4
LO 124,18,124
LU 18,124
AW 47,250
M 0,3,3
RA 220,1,8192
HS 24,18
LK 48,252,43,0
CF 40,40,41
LK 248,254,43,2
RA 252,7,2816
CF 42,42,43
GS 41
CF 44,44,45
CF 46,46,47
M 0,4,4
s_cbranch_vccnz .Lk1s15
.Lk1r15:
LL 17,80
LL 106,81
M 0,5,5
LA 17
ds_read_b64 v[14:15], v5 offset:0
AW 48,40
AW 49,42
M 1,16,4
LC_ 106
AW 50,44
AW 51,46
M 1,17,5
RA 224,1,9216
LK 40,240,44,0
CF 48,48,49
LK 44,242,44,2
CF 50,50,51
GS 42
CF 248,248,249
CF 250,250,251
M 4,64,4
WL 6
LO 125,16,125
LU 16,125
M 4,65,5
HS 24,16
AW 52,48
M 8,76,4
WL 5
LL 18,82
LL 107,83
AW 53,50
AW 54,248
AW 55,250
M 8,77,5
LA 18
RA 228,1,10240
LK 48,244,45,0
CF 40,40,41
LK 248,246,45,2
CF 42,42,43
HX 24
GS 43
CF 44,44,45
CF 46,46,47
M 8,78,0
LC_ 107
W1 24,0
M 8,79,1
W1 26,2048
AW 56,40
M 4,66,0
WL 6
LO 17,17,106
LU 106,17
AW 57,42
AW 58,44
M 4,67,1
RA 232,1,11264
HZ 27,106
AW 59,46
LK 40,248,46,0
CF 48,48,49
LK 44,250,46,2
CF 50,50,51
v_add_f32 v14, v14, v15
GS 44
CF 248,248,249
CF 250,250,251
M 1,18,0
LL 16,84
LL 110,85
M 1,19,1
LA 16
)"
R"(PK 106,17,104,56
M 0,6,0
WL 5
LC_ 110
PK 104,17,104,97
AW 60,48
AW 61,50
AW 62,248
M 0,7,1
RV 252,22,5392
v_add_f32_dpp v14, v14, v14 quad_perm:[1,0,3,2] row_mask:0xf bank_mask:0xf
AW 63,250
LK 48,252,47,0
CF 40,40,41
LK 248,254,47,2
CF 42,42,43
v_add_f32_dpp v14, v14, v14 quad_perm:[2,3,0,1] row_mask:0xf bank_mask:0xf
GS 45
CF 44,44,45
CF 46,46,47
M 0,8,2
WL 6
LO 18,18,107
LU 107,18
M 0,9,3
RA 236,1,12288
XL 53,320
HS 27,107
M 1,20,2
s_add_u32 s42, s94, s60
LL 17,86
LL 111,87
AW 64,40
AW 65,42
M 1,21,3
s_addc_u32 s43, s95, 0
LA 17
RV 36,8,6912
PK 107,18,105,56
AW 66,44
AW 67,46
M 4,68,2
LC_ 111
PK 105,18,105,97
M 4,69,3
RA 216,1,13312
v_add_f32_dpp v14, v14, v14 row_half_mirror row_mask:0xf bank_mask:0xf
GS 46
CF 48,48,49
CF 50,50,51
CF 248,248,249
CF 250,250,251
M 8,80,2
WL 4
LO 16,16,110
LU 110,16
AW 68,48
AW 69,50
AW 70,248
M 8,81,3
RA 220,1,14336
HS 27,110
AW 71,250
s_nop 1
GS 47
SU 104
v_cvt_f64_f32 v[14:15], v14
s_nop 5
v_cndmask_b32 v52, v52, v53, s[78:79]
M 0,10,4
LL 18,96
LL 122,97
v_cndmask_b32 v54, v54, v55, s[78:79]
M 0,11,5
LA 18
PK 110,16,108,56
v_cndmask_b32 v52, v52, v54, s[80:81]
M 1,22,4
LC_ 122
PK 108,16,108,97
v_mul_f32 v52, 0x3b2aaaab, v52
M 1,23,5
WL 5
RA 224,1,15360
PO 110
v_med3_f32 v53, v52, v39, v37
M 4,70,4
WL 5
LO 17,17,111
LU 111,17
s_mov_b64 exec, s[54:55]
SP
s_mov_b64 exec, -1
LG 52,53
s_nop 0
C 52,52,52
M 4,71,5
ds_read_b64 v[14:15], v5 offset:2048
HS 27,111
v_lshlrev_b32 v52, 16, v52
M 8,82,4
WL 4
LL 16,98
LL 123,99
v_add_f32 v52, v36, v52
M 8,83,5
LA 16
PK 111,17,109,56
C 52,52,52
Z 6,0,0
LC_ 123
PK 109,17,109,97
v_lshlrev_b32 v52, 16, v52
M 6,1,1
RA 228,1,16384
PO 111
v_sqrt_f32 v52, v52
s_nop 0
C 52,52,52
Z 7,12,0
WL 5
LO 18,18,122
LU 122,18
v_lshlrev_b32 v52, 16, v52
M 7,13,1
RA 232,1,17408
XL 54,384
HZ 25,122
v_rcp_f32 v52, v52
Z 10,60,0
s_add_u32 s42, s94, s61
LL 17,64
LL 126,65
s_addc_u32 s43, s95, 0
v_mul_f32 v52, v38, v52
M 10,61,1
LA 17
PK 122,18,120,56
v_subrev_u32 v5, s39, v5
HX 27
CF 52,52,52
Z 11,72,0
WL 2
LC_ 126
PK 120,18,120,97
ds_bpermute_b32 v13, v28, v52
M 11,73,1
RA 236,1,18432
v_add_f32 v14, v14, v15
M 11,74,2
LO 16,16,123
LU 123,16
M 11,75,3
SU 108
HS 25,123
v_add_f32_dpp v14, v14, v14 quad_perm:[1,0,3,2] row_mask:0xf bank_mask:0xf
M 10,62,2
s_mul_i32 s53, s50, 25600
LL 18,66
LL 127,67
s_add_u32 s40, %[wlo], s53
s_addc_u32 s41, %[whi], 0
s_mov_b32 m0, s38
M 10,63,3
LA 18
PK 123,16,121,56
v_add_f32_dpp v14, v14, v14 quad_perm:[2,3,0,1] row_mask:0xf bank_mask:0xf
M 7,14,2
LC_ 127
PK 121,16,121,97
M 7,15,3
RA 216,1,19456
DW 0
v_add_f32_dpp v14, v14, v14 row_half_mirror row_mask:0xf bank_mask:0xf
M 6,2,2
WL 3
LO 17,17,126
LU 126,17
M 6,3,3
s_add_u32 s42, s44, s64
RA 220,1,20480
HS 25,126
v_cvt_f64_f32 v[14:15], v14
s_addc_u32 s43, s45, 0
AR 248,24
M 6,4,4
s_add_u32 s37, s37, 32768
LL 16,56
LL 104,57
s_cmp_eq_u32 s37, s75
s_cselect_b32 s37, s74, s37
AR 249,25
M 6,5,5
LA 16
PK 126,17,124,56
s_mov_b64 exec, s[54:55]
SP
s_mov_b64 exec, -1
DW 1024
AR 250,26
M 7,16,4
LC_ 104
PK 124,17,124,97
AR 251,27
AR 52,28
M 7,17,5
RA 224,1,21504
PO 126
AR 53,29
M 10,64,4
WL 5
LO 18,18,127
LU 127,18
ST 120
AR 54,30
AR 55,31
M 10,65,5
DW 2048
XL 55,448
HS 25,127
XS 248
M 11,76,4
WL 3
LL 17,58
LL 105,59
XS 249
XS 250
M 11,77,5
s_add_u32 s40, s40, 3072
LA 17
PK 127,18,125,56
XS 251
M 11,78,0
s_addc_u32 s41, s41, 0
LC_ 105
PK 125,18,125,97
s_add_u32 m0, s38, 3072
AW 24,248
AW 25,249
M 11,79,1
s_add_u32 s42, s44, s65
RA 228,1,22528
PO 127
AW 26,250
M 10,66,0
WL 4
LO 104,16,104
LU 16,104
AW 27,251
XS 52
M 10,67,1
s_addc_u32 s43, s45, 0
RA 232,1,23552
HZ 26,16
XS 53
M 7,18,0
DW 0
LL 18,60
LL 108,61
XS 54
XS 55
M 7,19,1
s_lshl_b32 s53, s52, 8
LA 18
HX 25
AW 28,52
M 6,6,0
WL 2
LC_ 108
W1 25,1024
W1 27,3072
AW 29,53
AW 30,54
M 6,7,1
s_add_u32 s48, %[alo], s53
RA 236,1,24576
v_add_u32 v4, s39, v4
AW 31,55
M 6,8,2
LO 105,17,105
LU 17,105
s_addc_u32 s49, %[ahi], 0
s_add_u32 s52, s52, %[d]
s_cmp_eq_u32 s52, %[wrap]
s_cselect_b32 s52, %[rst], s52
DW 1024
v_min_u32 v35, v35, v13
AR 248,32
M 6,9,3
ST 124
HS 26,17
AR 249,33
M 7,20,2
s_mul_i32 s53, s51, 24
LL 16,62
LL 109,63
s_bitcmp1_b32 s51, 0
s_addc_u32 s53, s53, 0
s_lshl_b32 s53, s53, 11
s_bitcmp1_b64 s[90:91], s51
s_cselect_b64 s[44:45], s[84:85], s[82:83]
s_add_u32 s44, s44, s53
s_addc_u32 s45, s45, 0
s_bitcmp1_b64 s[92:93], s51
s_cselect_b64 s[94:95], s[88:89], s[86:87]
s_add_u32 s94, s94, s53
s_addc_u32 s95, s95, 0
s_add_u32 s51, s51, %[d]
s_cmp_eq_u32 s51, %[wrap]
s_cselect_b32 s51, %[rst], s51
AR 250,34
AR 251,35
M 7,21,3
LA 16
DW 2048
XS 248
M 10,68,2
LC_ 109
XL 56,512
XS 249
XS 250
M 10,69,3
DQ 3072
v_add_u32 v1, s37, v0
XS 251
M 11,80,2
WL 2
LO 108,18,108
LU 18,108
AW 32,248
AW 33,249
M 11,81,3
s_add_u32 s50, s50, %[d]
RA 216,1,0
HS 26,18
AW 34,250
M 6,10,4
s_cmp_eq_u32 s50, %[wrap]
LL 17,72
LL 120,73
AW 35,251
AR 52,36
M 6,11,5
s_cselect_b32 s50, %[rst], s50
LA 17
v_and_b32 v22, 0xfffffcff, v1
AR 53,37
M 7,22,4
LC_ 120
RA 220,1,1024
AR 54,38
AR 55,39
M 7,23,5
RV 252,22,5120
v_add_u32 v19, 1, v19
XS 52
M 10,70,4
WL 5
LO 109,16,109
LU 16,109
XS 53
XS 54
M 10,71,5
s_add_u32 s38, s38, 32768
RA 224,1,2048
HS 26,16
XS 55
M 11,82,4
WL 1
LL 18,74
LL 121,75
AW 36,52
AW 37,53
M 11,83,5
s_cmp_eq_u32 s38, s77
LA 18
RA 228,1,3072
s_cselect_b32 s38, s76, s38
FP
s_sub_u32 s39, 0, s39
AW 38,54
Z 0,0,0
LC_ 121
HX 26
AW 39,55
M 0,1,1
AR 248,40
AR 249,41
Z 1,12,0
LO 120,17,120
LU 17,120
AR 250,42
M 1,13,1
RA 232,1,4096
HZ 24,17
AR 251,43
XS 248
Z 2,24,0
LL 16,88
LL 124,89
)"
R"(XS 249
M 2,25,1
LA 16
XL 57,576
XS 250
XS 251
Z 3,36,0
LC_ 124
AW 40,248
M 3,37,1
AW 41,249
AW 42,250
Z 4,60,0
WL 3
LO 121,18,121
LU 18,121
AW 43,251
M 4,61,1
RA 236,1,6144
HS 24,18
AR 52,44
AR 53,45
Z 5,72,0
LL 17,90
LL 125,91
AR 54,46
M 5,73,1
LA 17
AR 55,47
XS 52
M 5,74,2
LC_ 125
XS 53
M 5,75,3
XS 54
XS 55
M 4,62,2
WL 3
LO 124,16,124
LU 16,124
AW 44,52
M 4,63,3
RA 216,1,7168
HS 24,16
AW 45,53
AW 46,54
M 3,38,2
LL 18,80
LL 106,81
AW 47,55
M 3,39,3
LA 18
FR
AR 248,48
AR 249,49
M 2,26,2
LC_ 106
AR 250,50
M 2,27,3
AR 251,51
XS 248
M 1,14,2
WL 4
LO 125,17,125
LU 17,125
XS 249
M 1,15,3
RA 220,1,8192
HS 24,17
XS 250
XS 251
M 0,2,2
LL 16,82
LL 107,83
AW 48,248
M 0,3,3
LA 16
XL 58,640
AW 49,249
AW 50,250
M 0,4,4
WL 3
LC_ 107
v_cmp_gt_u32 vcc, v19, v15
AW 51,251
M 0,5,5
HX 24
AR 52,52
AR 53,53
M 1,16,4
WL 3
LO 18,18,106
LU 106,18
AR 54,54
M 1,17,5
RA 224,1,9216
HZ 27,106
AR 55,55
XS 52
M 2,28,4
s_cbranch_vccnz .Lk1s16
.Lk1r16:
LL 17,84
LL 110,85
XS 53
M 2,29,5
LA 17
PK 106,18,104,56
XS 54
XS 55
M 3,40,4
LC_ 110
PK 104,18,104,97
AW 52,52
M 3,41,5
W1 24,0
AW 53,53
AW 54,54
M 4,64,4
WL 4
LO 16,16,107
LU 107,16
AW 55,55
M 4,65,5
s_add_u32 s42, s94, s60
RA 228,1,10240
HS 27,107
AR 248,56
M 5,76,4
LL 18,86
LL 111,87
AR 249,57
AR 250,58
M 5,77,5
s_addc_u32 s43, s95, 0
LA 18
PK 107,16,105,56
AR 251,59
M 5,78,0
LC_ 111
PK 105,16,105,97
XS 248
XS 249
M 5,79,1
W1 26,2048
XS 250
M 4,66,0
WL 5
LO 17,17,110
LU 110,17
XS 251
AW 56,248
M 4,67,1
RA 232,1,11264
HS 27,110
AW 57,249
M 3,42,0
LL 16,96
LL 122,97
AW 58,250
AW 59,251
M 3,43,1
LA 16
PK 110,17,108,56
AR 52,60
M 2,30,0
LC_ 122
PK 108,17,108,97
AR 53,61
AR 54,62
M 2,31,1
RV 252,22,5392
PO 110
AR 55,63
M 1,18,0
WL 5
LO 18,18,111
LU 111,18
XS 52
XS 53
M 1,19,1
RA 236,1,12288
HS 27,111
XS 54
M 0,6,0
LL 17,98
LL 123,99
XS 55
AW 60,52
M 0,7,1
LA 17
PK 111,18,109,56
AW 61,53
M 0,8,2
LC_ 123
PK 109,18,109,97
AW 62,54
AW 63,55
M 0,9,3
SU 104
PO 111
AR 248,64
M 1,20,2
WL 4
LO 16,16,122
LU 122,16
AR 249,65
AR 250,66
M 1,21,3
RA 216,1,13312
HZ 25,122
AR 251,67
M 2,32,2
LL 18,100
LL 126,101
XS 248
XS 249
M 2,33,3
LA 18
XL 59,704
PK 122,16,120,56
XS 250
M 3,44,2
s_add_u32 s42, s94, s61
LC_ 126
ds_read_b64 v[14:15], v5 offset:0
PK 120,16,120,97
XS 251
AW 64,248
M 3,45,3
s_addc_u32 s43, s95, 0
SU 108
HX 27
AW 65,249
M 4,68,2
WL 4
LO 17,17,123
LU 123,17
AW 66,250
AW 67,251
M 4,69,3
s_add_u32 s42, s44, s64
RA 220,1,14336
HS 25,123
AR 52,68
M 5,80,2
LL 16,102
LL 127,103
AR 53,69
AR 54,70
M 5,81,3
s_addc_u32 s43, s45, 0
LA 16
PK 123,17,121,56
AR 55,71
M 0,10,4
LC_ 127
PK 121,17,121,97
XS 52
XS 53
M 0,11,5
ST 120
RV 40,8,6144
XS 54
M 1,22,4
WL 5
LO 18,18,126
LU 126,18
XS 55
AW 68,52
M 1,23,5
RA 224,1,15360
HS 25,126
AW 69,53
M 2,34,4
LL 17,56
LL 104,57
AW 70,54
AW 71,55
M 2,35,5
LA 17
PK 126,18,124,56
M 3,46,4
LC_ 104
PK 124,18,124,97
M 3,47,5
RV 44,8,6208
PO 126
M 4,70,4
WL 5
LO 16,16,127
LU 127,16
M 4,71,5
s_add_u32 s42, s44, s65
RA 228,1,16384
HS 25,127
M 5,82,4
LL 18,58
LL 105,59
M 5,83,5
s_addc_u32 s43, s45, 0
LA 18
PK 127,16,125,56
Z 6,0,0
WV 40
LC_ 105
PK 125,16,125,97
M 6,1,1
WL 7
RV 48,8,6272
PO 127
LZ 40,48
WV 37
RV 40,8,6336
v_add_f32 v14, v14, v15
s_nop 1
v_add_f32_dpp v14, v14, v14 quad_perm:[1,0,3,2] row_mask:0xf bank_mask:0xf
s_nop 1
v_add_f32_dpp v14, v14, v14 quad_perm:[2,3,0,1] row_mask:0xf bank_mask:0xf
s_nop 1
v_add_f32_dpp v14, v14, v14 row_half_mirror row_mask:0xf bank_mask:0xf
Z 7,12,0
WL 6
LO 104,17,104
LU 17,104
M 7,13,1
WL 5
RA 232,1,17408
HZ 26,17
v_cvt_f64_f32 v[14:15], v14
LS 44,49
RV 44,8,6400
HX 25
Z 8,24,0
LL 16,60
LL 108,61
M 8,25,1
LA 16
s_mov_b64 exec, s[54:55]
SP
s_mov_b64 exec, -1
Z 9,36,0
WV 25
LC_ 108
ds_read_b64 v[14:15], v5 offset:2048
M 9,37,1
WL 6
W1 25,1024
v_subrev_u32 v5, s39, v5
LS 48,50
WL 6
RV 48,8,6464
LS 40,51
RV 40,8,6528
Z 10,60,0
LO 105,18,105
LU 18,105
M 10,61,1
RA 236,1,18432
HS 26,18
Z 11,72,0
WV 24
LL 17,62
LL 109,63
M 11,73,1
WL 7
LA 17
W1 27,3072
LS 44,52
RV 44,8,6592
M 11,74,2
WV 20
LC_ 109
ST 124
M 11,75,3
s_mul_i32 s53, s50, 25600
v_add_u32 v4, s39, v4
M 10,62,2
WL 5
LO 108,16,108
LU 16,108
M 10,63,3
s_add_u32 s40, %[wlo], s53
RA 216,1,19456
HS 26,16
LS 48,53
s_addc_u32 s41, %[whi], 0
RV 48,8,6656
v_add_f32 v14, v14, v15
LS 40,54
s_mov_b32 m0, s38
RV 40,8,6720
v_add_f32_dpp v14, v14, v14 quad_perm:[1,0,3,2] row_mask:0xf bank_mask:0xf
M 9,38,2
WV 14
LL 18,64
LL 112,65
M 9,39,3
WL 4
LA 18
)"
R"(DW 0
M 8,26,2
s_add_u32 s37, s37, 32768
LC_ 112
v_add_f32_dpp v14, v14, v14 quad_perm:[2,3,0,1] row_mask:0xf bank_mask:0xf
M 8,27,3
s_cmp_eq_u32 s37, s75
v_add_f32_dpp v14, v14, v14 row_half_mirror row_mask:0xf bank_mask:0xf
LS 44,55
s_cselect_b32 s37, s74, s37
RV 44,8,6784
v_cvt_f64_f32 v[14:15], v14
M 7,14,2
WL 6
LO 109,17,109
LU 17,109
M 7,15,3
RA 220,1,20480
HS 26,17
M 6,2,2
WV 8
LL 16,66
LL 113,67
M 6,3,3
WL 5
LA 16
s_mov_b64 exec, s[54:55]
SP
s_mov_b64 exec, -1
LS 48,56
WL 5
RV 48,8,6848
RA 240,7,0
LS 40,57
RA 244,7,256
M 6,4,4
LC_ 113
DW 1024
M 6,5,5
WV 9
DW 2048
M 7,16,4
WL 6
LO 112,18,112
LU 18,112
M 7,17,5
s_mul_i32 s53, s51, 24
RA 224,1,21504
HZ 29,18
LS 44,58
s_bitcmp1_b32 s51, 0
RA 248,7,512
M 8,28,4
s_addc_u32 s53, s53, 0
LL 17,68
LL 116,69
M 8,29,5
s_lshl_b32 s53, s53, 11
LA 17
M 9,40,4
s_bitcmp1_b64 s[90:91], s51
LC_ 116
M 9,41,5
s_cselect_b64 s[44:45], s[84:85], s[82:83]
WL 7
WV 8
LS 48,59
s_add_u32 s44, s44, s53
RA 252,7,768
M 10,64,4
WL 5
LO 113,16,113
LU 16,113
M 10,65,5
s_addc_u32 s45, s45, 0
RA 228,1,22528
HS 29,16
M 11,76,4
LL 18,70
LL 117,71
M 11,77,5
s_bitcmp1_b64 s[92:93], s51
LA 18
v_mul_f32 v36, 0xbb2aaaab, v36
M 11,78,0
s_cselect_b64 s[94:95], s[88:89], s[86:87]
LC_ 117
v_mul_f32 v37, 0xbb2aaaab, v37
M 11,79,1
s_add_u32 s94, s94, s53
v_mul_f32 v38, 0xbb2aaaab, v38
v_mul_f32 v39, 0xbb2aaaab, v39
M 10,66,0
WL 4
LO 116,17,116
LU 17,116
M 10,67,1
s_addc_u32 s95, s95, 0
RA 232,1,23552
HS 29,17
LK 40,240,48,0
LK 44,242,48,2
s_add_u32 s51, s51, %[d]
RA 240,7,1024
LK 48,244,49,0
s_nop 0
CF 40,40,41
LK 248,246,49,2
s_cmp_eq_u32 s51, %[wrap]
RA 244,7,1280
CF 42,42,43
M 9,42,0
s_cselect_b32 s51, %[rst], s51
LL 16,72
LL 120,73
M 9,43,1
s_mul_i32 s53, %[d], 16384
LA 16
s_add_u32 s62, s53, %[sb1]
s_add_u32 s63, s62, 512
M 8,30,0
s_add_u32 s40, s40, 3072
LC_ 120
s_addc_u32 s41, s41, 0
s_add_u32 m0, s38, 3072
M 8,31,1
DW 0
CF 44,44,45
M 7,18,0
WL 5
LO 117,18,117
LU 18,117
M 7,19,1
RA 236,1,24576
HS 29,18
M 6,6,0
LL 17,74
LL 121,75
M 6,7,1
LA 17
DW 1024
M 6,8,2
LC_ 121
DW 2048
M 6,9,3
s_lshl_b32 s53, s52, 8
v_add_u32 v1, s37, v0
v_pk_add_f16 v26, v26, v29
M 7,20,2
WL 3
LO 120,16,120
LU 16,120
M 7,21,3
s_add_u32 s48, %[alo], s53
RA 216,1,0
HZ 24,16
M 8,32,2
s_addc_u32 s49, %[ahi], 0
LL 18,76
LL 124,77
M 8,33,3
s_add_u32 s52, s52, %[d]
LA 18
v_and_b32 v22, 0xfffffcff, v1
M 9,44,2
s_cmp_eq_u32 s52, %[wrap]
LC_ 124
HX 26
M 9,45,3
s_cselect_b32 s52, %[rst], s52
DQ 3072
CF 46,46,47
M 10,68,2
WL 3
LO 121,17,121
LU 17,121
M 10,69,3
s_add_u32 s50, s50, %[d]
RA 220,1,1024
HS 24,17
M 11,80,2
LL 16,78
LL 125,79
M 11,81,3
s_cmp_eq_u32 s50, %[wrap]
LA 16
RV 252,22,5120
M 6,10,4
s_cselect_b32 s50, %[rst], s50
LC_ 125
AW 72,40
M 6,11,5
v_add_u32 v19, 1, v19
AW 73,42
M 7,22,4
WL 4
LO 124,18,124
LU 18,124
AW 74,44
AW 75,46
M 7,23,5
s_add_u32 s38, s38, 32768
RA 224,1,2048
HS 24,18
LK 40,248,50,0
CF 48,48,49
LK 44,250,50,2
s_cmp_eq_u32 s38, s77
RA 248,7,1536
CF 50,50,51
GZ 48
s_cselect_b32 s38, s76, s38
CF 248,248,249
CF 250,250,251
M 8,34,4
LL 17,80
LL 106,81
M 8,35,5
LA 17
FP
AW 76,48
M 9,46,4
s_sub_u32 s39, 0, s39
LC_ 106
AW 77,50
AW 78,248
AW 79,250
M 9,47,5
LK 48,252,51,0
CF 40,40,41
LK 248,254,51,2
RA 252,7,1792
CF 42,42,43
GS 49
CF 44,44,45
CF 46,46,47
M 10,70,4
WL 6
LO 125,16,125
LU 16,125
M 10,71,5
RA 228,1,3072
HS 24,16
M 11,82,4
LL 18,82
LL 107,83
AW 80,40
AW 81,42
AW 82,44
M 11,83,5
LA 18
HX 24
AW 83,46
Z 0,0,0
LC_ 107
M 0,1,1
LK 40,240,52,0
CF 48,48,49
LK 44,242,52,2
RA 240,7,2048
CF 50,50,51
GS 50
CF 248,248,249
CF 250,250,251
Z 1,12,0
WL 5
LO 17,17,106
LU 106,17
AW 84,48
AW 85,50
AW 86,248
M 1,13,1
RA 232,1,4096
HZ 27,106
AW 87,250
LK 48,244,53,0
CF 40,40,41
LK 248,246,53,2
RA 244,7,2304
CF 42,42,43
GS 51
CF 44,44,45
CF 46,46,47
Z 2,24,0
LL 16,84
LL 110,85
M 2,25,1
LA 16
PK 106,17,104,56
Z 3,36,0
LC_ 110
PK 104,17,104,97
AW 88,40
AW 89,42
M 3,37,1
AW 90,44
AW 91,46
LK 40,248,54,0
CF 48,48,49
LK 44,250,54,2
RA 248,7,2560
CF 50,50,51
GS 52
CF 248,248,249
CF 250,250,251
Z 4,60,0
WL 6
LO 18,18,107
LU 107,18
M 4,61,1
s_add_u32 s42, s94, s60
RA 236,1,6144
HS 27,107
Z 5,72,0
LL 17,86
LL 111,87
AW 92,48
AW 93,50
M 5,73,1
s_addc_u32 s43, s95, 0
LA 17
PK 107,18,105,56
AW 94,248
AW 95,250
LK 48,252,55,0
CF 40,40,41
LK 248,254,55,2
RA 252,7,2816
CF 42,42,43
GS 53
CF 44,44,45
CF 46,46,47
M 5,74,2
LC_ 111
PK 105,18,105,97
M 5,75,3
SU 104
AW 96,40
M 4,62,2
WL 5
LO 16,16,110
LU 110,16
AW 97,42
M 4,63,3
RA 216,1,7168
HS 27,110
AW 98,44
AW 99,46
LK 40,240,56,0
CF 48,48,49
LK 44,242,56,2
CF 50,50,51
GS 54
CF 248,248,249
CF 250,250,251
M 3,38,2
LL 18,88
LL 114,89
M 3,39,3
LA 18
PK 110,16,108,56
M 2,26,2
LC_ 114
PK 108,16,108,97
AW 100,48
AW 101,50
M 2,27,3
FR
PO 110
AW 102,248
AW 103,250
LK 48,244,57,0
CF 40,40,41
LK 248,246,57,2
CF 42,42,43
GS 55
CF 44,44,45
CF 46,46,47
M 1,14,2
WL 4
LO 17,17,111
LU 111,17
M 1,15,3
s_add_u32 s42, s94, s61
RA 220,1,8192
HS 27,111
M 0,2,2
LL 16,90
LL 115,91
AW 104,40
AW 105,42
AW 106,44
M 0,3,3
s_addc_u32 s43, s95, 0
LA 16
PK 111,17,109,56
AW 107,46
LK 40,248,58,0
CF 48,48,49
LK 44,250,58,2
)"
R"(s_mov_b32 m0, %[pm]
CF 50,50,51
GS 56
CF 248,248,249
CF 250,250,251
M 0,4,4
LC_ 115
PK 109,17,109,97
M 0,5,5
DF
PO 111
M 1,16,4
WL 4
LO 18,18,114
LU 114,18
AW 108,48
AW 109,50
AW 110,248
M 1,17,5
RA 224,1,9216
HZ 29,114
AW 111,250
LK 48,252,59,0
CF 40,40,41
LK 248,254,59,2
WL 4
CF 42,42,43
v_cmp_gt_u32 vcc, v19, v15
GS 57
CF 44,44,45
CF 46,46,47
M 2,28,4
LL 17,92
LL 118,93
M 2,29,5
LA 17
PK 114,18,112,56
AW 112,40
M 3,40,4
s_cbranch_vccnz .Lk1s17
.Lk1r17:
LC_ 118
PK 112,18,112,97
AW 113,42
AW 114,44
AW 115,46
M 3,41,5
W1 24,0
RV 36,8,6912
GS 58
CF 48,48,49
CF 50,50,51
M 4,64,4
WL 5
LO 16,16,115
LU 115,16
CF 248,248,249
CF 250,250,251
M 4,65,5
RA 228,1,10240
HS 29,115
AW 116,48
AW 117,50
AW 118,248
M 5,76,4
LL 18,94
LL 119,95
AW 119,250
M 5,77,5
LA 18
PK 115,16,113,56
GS 59
W1 26,2048
s_nop 6
v_cndmask_b32 v52, v52, v53, s[78:79]
M 5,78,0
LC_ 119
PK 113,16,113,97
v_cndmask_b32 v54, v54, v55, s[78:79]
M 5,79,1
SU 108
v_cndmask_b32 v52, v52, v54, s[80:81]
M 4,66,0
WL 4
LO 17,17,118
LU 118,17
v_mul_f32 v52, 0x3b2aaaab, v52
M 4,67,1
s_add_u32 s42, s94, s62
RA 232,1,11264
HS 29,118
v_med3_f32 v53, v52, v39, v37
M 3,42,0
s_addc_u32 s43, s95, 0
LL 16,96
LL 122,97
LG 52,53
M 3,43,1
LA 16
PK 118,17,116,56
C 52,52,52
M 2,30,0
LC_ 122
PK 116,17,116,97
v_lshlrev_b32 v52, 16, v52
M 2,31,1
RV 252,22,5392
PO 118
v_add_f32 v52, v36, v52
M 1,18,0
WL 4
LO 18,18,119
LU 119,18
C 52,52,52
M 1,19,1
RA 236,1,12288
HS 29,119
M 0,6,0
LL 17,98
LL 123,99
v_lshlrev_b32 v52, 16, v52
M 0,7,1
LA 17
PK 119,18,117,56
v_sqrt_f32 v52, v52
M 0,8,2
LC_ 123
PK 117,18,117,97
C 52,52,52
M 0,9,3
SU 112
PO 119
v_lshlrev_b32 v52, 16, v52
M 1,20,2
WL 4
LO 16,16,122
LU 122,16
v_rcp_f32 v52, v52
M 1,21,3
s_add_u32 s42, s94, s63
RA 216,1,13312
HZ 25,122
v_mul_f32 v52, v38, v52
M 2,32,2
s_addc_u32 s43, s95, 0
LL 18,100
LL 126,101
CF 52,52,52
M 2,33,3
LA 18
PK 122,16,120,56
ds_bpermute_b32 v13, v28, v52
M 3,44,2
LC_ 126
ds_read_b64 v[14:15], v5 offset:0
PK 120,16,120,97
M 3,45,3
SU 116
v_pk_add_f16 v27, v27, v29
M 4,68,2
WL 5
LO 17,17,123
LU 123,17
M 4,69,3
s_add_u32 s42, s44, s64
RA 220,1,14336
HS 25,123
M 5,80,2
LL 16,102
LL 127,103
M 5,81,3
s_addc_u32 s43, s45, 0
LA 16
PK 123,17,121,56
M 0,10,4
LC_ 127
PK 121,17,121,97
M 0,11,5
ST 120
HX 27
M 1,22,4
WL 4
LO 18,18,126
LU 126,18
M 1,23,5
RA 224,1,15360
HS 25,126
AR 248,72
M 2,34,4
LL 17,56
LL 104,57
AR 249,73
M 2,35,5
LA 17
PK 126,18,124,56
AR 250,74
M 3,46,4
LC_ 104
PK 124,18,124,97
AR 251,75
M 3,47,5
WL 6
PO 126
v_add_f32 v14, v14, v15
s_nop 1
v_add_f32_dpp v14, v14, v14 quad_perm:[1,0,3,2] row_mask:0xf bank_mask:0xf
AR 52,76
M 4,70,4
WL 3
LO 16,16,127
LU 127,16
AR 53,77
M 4,71,5
s_add_u32 s42, s44, s65
RA 228,1,16384
HS 25,127
v_add_f32_dpp v14, v14, v14 quad_perm:[2,3,0,1] row_mask:0xf bank_mask:0xf
AR 54,78
M 5,82,4
LL 18,58
LL 105,59
AR 55,79
M 5,83,5
s_addc_u32 s43, s45, 0
LA 18
PK 127,16,125,56
XS 248
Z 6,0,0
LC_ 105
PK 125,16,125,97
v_add_f32_dpp v14, v14, v14 row_half_mirror row_mask:0xf bank_mask:0xf
XS 249
M 6,1,1
PO 127
v_cvt_f64_f32 v[14:15], v14
XS 250
Z 7,12,0
WL 3
LO 104,17,104
LU 17,104
XS 251
AW 72,248
M 7,13,1
RA 232,1,17408
HZ 26,17
AW 73,249
Z 8,24,0
LL 16,60
LL 108,61
AW 74,250
M 8,25,1
s_mul_i32 s53, s50, 25600
LA 16
s_mov_b64 exec, s[54:55]
SP
s_mov_b64 exec, -1
AW 75,251
Z 9,36,0
s_add_u32 s40, %[wlo], s53
LC_ 108
ds_read_b64 v[14:15], v5 offset:2048
XS 52
M 9,37,1
s_addc_u32 s41, %[whi], 0
v_subrev_u32 v5, s39, v5
HX 25
XS 53
Z 10,60,0
WL 4
LO 105,18,105
LU 18,105
XS 54
M 10,61,1
s_mov_b32 m0, s38
RA 236,1,18432
HS 26,18
XS 55
Z 11,72,0
LL 17,62
LL 109,63
AW 76,52
M 11,73,1
LA 17
W1 25,1024
AW 77,53
M 11,74,2
LC_ 109
W1 27,3072
AW 78,54
AW 79,55
M 11,75,3
ST 124
v_add_u32 v4, s39, v4
v_min_u32 v35, v35, v13
M 10,62,2
WL 6
LO 108,16,108
LU 16,108
AR 248,80
M 10,63,3
RA 216,1,19456
HS 26,16
AR 249,81
M 9,38,2
LL 18,64
LL 112,65
AR 250,82
M 9,39,3
WL 6
LA 18
DW 0
AR 251,83
M 8,26,2
s_add_u32 s37, s37, 32768
LC_ 112
v_add_f32 v14, v14, v15
XS 248
M 8,27,3
s_cmp_eq_u32 s37, s75
v_add_f32_dpp v14, v14, v14 quad_perm:[1,0,3,2] row_mask:0xf bank_mask:0xf
XS 249
M 7,14,2
WL 4
LO 109,17,109
LU 17,109
XS 250
M 7,15,3
s_cselect_b32 s37, s74, s37
RA 220,1,20480
HS 26,17
XS 251
M 6,2,2
LL 16,66
LL 113,67
v_add_f32_dpp v14, v14, v14 quad_perm:[2,3,0,1] row_mask:0xf bank_mask:0xf
AW 80,248
AW 81,249
M 6,3,3
s_mul_i32 s53, %[d], 393216
LA 16
v_add_f32_dpp v14, v14, v14 row_half_mirror row_mask:0xf bank_mask:0xf
AW 82,250
M 6,4,4
s_add_u32 s53, s53, s57
LC_ 113
v_cvt_f64_f32 v[14:15], v14
AW 83,251
M 6,5,5
s_add_u32 s46, %[xlo], s53
s_mov_b64 exec, s[54:55]
SP
s_mov_b64 exec, -1
AR 52,84
M 7,16,4
WL 3
LO 112,18,112
LU 18,112
AR 53,85
M 7,17,5
s_addc_u32 s47, %[xhi], 0
RA 224,1,21504
HZ 29,18
AR 54,86
M 8,28,4
s_bitcmp1_b64 s[92:93], 48
LL 17,68
LL 116,69
AR 55,87
M 8,29,5
s_cselect_b32 s58, 16, 0
LA 17
XS 52
M 9,40,4
s_lshr_b32 s58, 0xda43cb52, s58
LC_ 116
v_lshrrev_b32 v3, 6, v0
v_and_b32 v3, 12, v3
v_bfe_u32 v3, s58, v3, 4
v_mad_u32_u24 v3, v3, v32, v21
XS 53
M 9,41,5
DW 1024
XS 54
M 10,64,4
WL 3
LO 113,16,113
LU 16,113
XS 55
AW 84,52
M 10,65,5
RA 228,1,22528
HS 29,16
AW 85,53
M 11,76,4
LL 18,70
LL 117,71
AW 86,54
M 11,77,5
LA 18
XL 60,0
)"
R"(AW 87,55
M 11,78,0
LC_ 117
DW 2048
AR 248,88
M 11,79,1
XL 61,64
AR 249,89
M 10,66,0
WL 3
LO 116,17,116
LU 17,116
AR 250,90
M 10,67,1
s_mul_i32 s53, s51, 24
RA 232,1,23552
HS 29,17
AR 251,91
M 9,42,0
s_bitcmp1_b32 s51, 0
LL 16,72
LL 120,73
XS 248
M 9,43,1
s_addc_u32 s53, s53, 0
LA 16
s_lshl_b32 s53, s53, 11
s_bitcmp1_b64 s[90:91], s51
s_cselect_b64 s[44:45], s[84:85], s[82:83]
s_add_u32 s44, s44, s53
s_addc_u32 s45, s45, 0
s_bitcmp1_b64 s[92:93], s51
s_cselect_b64 s[94:95], s[88:89], s[86:87]
s_add_u32 s94, s94, s53
s_addc_u32 s95, s95, 0
s_add_u32 s51, s51, %[d]
s_cmp_eq_u32 s51, %[wrap]
s_cselect_b32 s51, %[rst], s51
XS 249
M 8,30,0
s_add_u32 s40, s40, 3072
LC_ 120
s_addc_u32 s41, s41, 0
s_add_u32 m0, s38, 3072
XS 250
XS 251
M 8,31,1
XL 62,128
AW 88,248
M 7,18,0
WL 3
LO 117,18,117
LU 18,117
AW 89,249
M 7,19,1
RA 236,1,24576
HS 29,18
AW 90,250
M 6,6,0
LL 17,74
LL 121,75
AW 91,251
M 6,7,1
LA 17
DW 0
AR 52,92
M 6,8,2
LC_ 121
XL 63,192
AR 53,93
M 6,9,3
DW 1024
v_add_u32 v1, s37, v0
AR 54,94
M 7,20,2
WL 3
LO 120,16,120
LU 16,120
AR 55,95
M 7,21,3
RA 216,1,0
HZ 24,16
XS 52
M 8,32,2
LL 18,76
LL 124,77
XS 53
XS 54
M 8,33,3
LA 18
XL 64,256
XS 55
M 9,44,2
LC_ 124
DW 2048
AW 92,52
M 9,45,3
XL 65,320
v_and_b32 v22, 0xfffffcff, v1
AW 93,53
M 10,68,2
WL 3
LO 121,17,121
LU 17,121
AW 94,54
M 10,69,3
s_lshl_b32 s53, s52, 8
RA 220,1,1024
HS 24,17
AW 95,55
M 11,80,2
LL 16,78
LL 125,79
AR 248,96
M 11,81,3
s_add_u32 s48, %[alo], s53
LA 16
RV 252,22,5120
AR 249,97
M 6,10,4
s_addc_u32 s49, %[ahi], 0
LC_ 125
v_pk_add_f16 v26, v26, v29
AR 250,98
M 6,11,5
s_add_u32 s52, s52, %[d]
HX 26
AR 251,99
M 7,22,4
WL 4
LO 124,18,124
LU 18,124
XS 248
XS 249
M 7,23,5
s_cmp_eq_u32 s52, %[wrap]
RA 224,1,2048
HS 24,18
XS 250
M 8,34,4
s_cselect_b32 s52, %[rst], s52
LL 17,80
LL 106,81
XS 251
M 8,35,5
LA 17
DQ 3072
AW 96,248
M 9,46,4
LC_ 106
XL 66,384
AW 97,249
M 9,47,5
s_add_u32 s50, s50, %[d]
AW 98,250
AW 99,251
M 10,70,4
WL 3
LO 125,16,125
LU 16,125
M 10,71,5
RA 228,1,3072
HS 24,16
AR 52,100
M 11,82,4
LL 18,82
LL 107,83
AR 53,101
M 11,83,5
LA 18
HX 24
s_cmp_eq_u32 s50, %[wrap]
s_cselect_b32 s50, %[rst], s50
XL 67,448
AR 54,102
Z 0,0,0
LC_ 107
XL 68,512
v_add_u32 v19, 1, v19
s_add_u32 s38, s38, 32768
s_cmp_eq_u32 s38, s77
s_cselect_b32 s38, s76, s38
FP
s_sub_u32 s39, 0, s39
AR 55,103
XS 52
M 0,1,1
XL 69,576
XS 53
Z 1,12,0
WL 4
LO 17,17,106
LU 106,17
XS 54
M 1,13,1
RA 232,1,4096
HZ 27,106
XS 55
Z 2,24,0
LL 16,84
LL 110,85
AW 100,52
M 2,25,1
LA 16
PK 106,17,104,56
AW 101,53
Z 3,36,0
WL 5
LC_ 110
XL 70,640
PK 104,17,104,97
AW 102,54
M 3,37,1
RA 236,1,6144
AW 103,55
M 3,38,2
WL 5
LO 18,18,107
LU 107,18
AR 248,104
M 3,39,3
FR
HS 27,107
AR 249,105
M 2,26,2
LL 17,86
LL 111,87
AR 250,106
AR 251,107
M 2,27,3
LA 17
PK 107,18,105,56
XS 248
M 1,14,2
LC_ 111
XL 71,704
PK 105,18,105,97
XS 249
M 1,15,3
s_add_u32 s42, s94, s60
RA 216,1,7168
XS 250
M 0,2,2
WL 4
LO 16,16,110
LU 110,16
XS 251
M 0,3,3
s_addc_u32 s43, s95, 0
RA 220,1,8192
HS 27,110
AW 104,248
M 0,4,4
LL 18,88
LL 114,89
AW 105,249
M 0,5,5
LA 18
PK 110,16,108,56
AW 106,250
M 1,16,4
LC_ 114
PK 108,16,108,97
AW 107,251
M 1,17,5
RA 224,1,9216
PO 110
AR 52,108
M 2,28,4
WL 5
LO 17,17,111
LU 111,17
SU 104
AR 53,109
AR 54,110
M 2,29,5
s_add_u32 s42, s94, s61
HS 27,111
v_cmp_gt_u32 vcc, v19, v15
AR 55,111
M 3,40,4
WL 3
LL 16,90
LL 115,91
XS 52
M 3,41,5
s_addc_u32 s43, s95, 0
LA 16
PK 111,17,109,56
XS 53
M 3,42,0
s_cbranch_vccnz .Lk1s18
.Lk1r18:
LC_ 115
PK 109,17,109,97
XS 54
M 3,43,1
WV 17
RA 228,1,10240
PO 111
XS 55
BW 60
M 2,30,0
WL 5
LO 18,18,114
LU 114,18
AW 108,52
M 2,31,1
RA 232,1,11264
RV 40,8,6144
HZ 29,114
AW 109,53
M 1,18,0
RV 44,8,6208
LL 17,92
LL 118,93
AW 110,54
M 1,19,1
LA 17
RV 48,8,6272
PK 114,18,112,56
AW 111,55
M 0,6,0
WL 6
LC_ 118
BR 60
PK 112,18,112,97
AR 248,112
AR 249,113
M 0,7,1
WV 15
RV 252,22,5392
ds_read_b64 v[14:15], v5 offset:0
W1 24,0
W1 26,2048
BW 61
AR 250,114
M 0,8,2
BR 61
LO 16,16,115
LU 115,16
AR 251,115
M 0,9,3
WL 6
RA 236,1,12288
HS 29,115
XS 248
LZ 40,60
WV 14
BW 62
M 1,20,2
LL 18,94
LL 119,95
XS 249
M 1,21,3
LA 18
RV 40,8,6336
PK 115,16,113,56
XS 250
M 2,32,2
LC_ 119
BR 62
PK 113,16,113,97
XS 251
M 2,33,3
WL 6
RA 216,1,13312
AW 112,248
LS 44,61
WV 12
BW 63
M 3,44,2
WL 7
LO 17,17,118
LU 118,17
SU 108
AW 113,249
M 3,45,3
s_add_u32 s42, s94, s62
RA 220,1,14336
HS 29,118
s_addc_u32 s43, s95, 0
RV 44,8,6400
AW 114,250
M 0,10,4
BR 63
LL 16,96
LL 122,97
AW 115,251
AR 52,116
M 0,11,5
WL 5
LA 16
PK 118,17,116,56
AR 53,117
LS 48,62
WV 11
BW 64
M 1,22,4
LC_ 122
)"
R"(PK 116,17,116,97
AR 54,118
M 1,23,5
RA 224,1,15360
RV 48,8,6464
PO 118
AR 55,119
M 2,34,4
LO 18,18,119
LU 119,18
SU 112
XS 52
M 2,35,5
s_add_u32 s42, s94, s63
BR 64
HS 29,119
XS 53
M 3,46,4
WL 6
LL 17,98
LL 123,99
XS 54
M 3,47,5
s_addc_u32 s43, s95, 0
LA 17
PK 119,18,117,56
XS 55
LS 40,63
WV 10
BW 65
Z 6,0,0
LC_ 123
PK 117,18,117,97
v_add_f32 v14, v14, v15
s_nop 1
v_add_f32_dpp v14, v14, v14 quad_perm:[1,0,3,2] row_mask:0xf bank_mask:0xf
s_nop 1
v_add_f32_dpp v14, v14, v14 quad_perm:[2,3,0,1] row_mask:0xf bank_mask:0xf
s_nop 1
v_add_f32_dpp v14, v14, v14 row_half_mirror row_mask:0xf bank_mask:0xf
v_cvt_f64_f32 v[14:15], v14
s_mov_b64 exec, s[54:55]
SP
s_mov_b64 exec, -1
s_nop 0
ds_read_b64 v[14:15], v5 offset:2048
RV 40,8,6528
AW 116,52
M 6,1,1
WL 5
RA 228,1,16384
BR 65
PO 119
AW 117,53
LS 44,64
WV 9
BW 66
Z 7,12,0
LO 16,16,122
LU 122,16
AW 118,54
AW 119,55
M 7,13,1
s_mul_i32 s53, s50, 25600
RA 232,1,17408
HZ 25,122
Z 8,24,0
s_add_u32 s40, %[wlo], s53
RV 44,8,6592
LL 18,100
LL 126,101
M 8,25,1
s_addc_u32 s41, %[whi], 0
LA 18
BR 66
PK 122,16,120,56
v_pk_add_f16 v27, v27, v29
Z 9,36,0
WL 5
LC_ 126
PK 120,16,120,97
HX 27
M 9,37,1
s_mov_b32 m0, s38
RA 236,1,18432
v_subrev_u32 v5, s39, v5
LS 48,65
WV 8
BW 67
M 9,38,2
RV 48,8,6656
LO 17,17,123
LU 123,17
M 9,39,3
DW 0
BR 67
HS 25,123
M 8,26,2
LL 16,102
LL 127,103
M 8,27,3
WL 5
LA 16
PK 123,17,121,56
LS 40,66
WV 8
BW 68
M 7,14,2
LC_ 127
RV 40,8,6720
PK 121,17,121,97
M 7,15,3
WV 7
RA 216,1,19456
BR 68
M 6,2,2
WL 6
LO 18,18,126
LU 126,18
SU 116
M 6,3,3
s_add_u32 s42, s44, s64
RA 220,1,20480
HS 25,126
s_addc_u32 s43, s45, 0
LS 44,67
s_add_u32 s37, s37, 32768
BW 69
M 6,4,4
s_cmp_eq_u32 s37, s75
LL 17,56
LL 104,57
s_cselect_b32 s37, s74, s37
M 6,5,5
LA 17
PK 126,18,124,56
v_add_f32 v14, v14, v15
s_nop 1
v_add_f32_dpp v14, v14, v14 quad_perm:[1,0,3,2] row_mask:0xf bank_mask:0xf
s_nop 1
v_add_f32_dpp v14, v14, v14 quad_perm:[2,3,0,1] row_mask:0xf bank_mask:0xf
s_nop 1
v_add_f32_dpp v14, v14, v14 row_half_mirror row_mask:0xf bank_mask:0xf
v_cvt_f64_f32 v[14:15], v14
s_mov_b64 exec, s[54:55]
SP
s_mov_b64 exec, -1
DW 1024
RV 44,8,6784
M 7,16,4
LC_ 104
BR 69
PK 124,18,124,97
M 7,17,5
WL 6
RA 224,1,21504
PO 126
LS 48,68
WV 9
BW 70
M 8,28,4
LO 16,16,127
LU 127,16
ST 120
M 8,29,5
WV 9
DW 2048
RV 48,8,6848
HS 25,127
M 9,40,4
WL 3
RA 240,7,0
LL 18,58
LL 105,59
M 9,41,5
s_add_u32 s40, s40, 3072
LA 18
BR 70
PK 127,16,125,56
M 9,42,0
s_addc_u32 s41, s41, 0
LC_ 105
PK 125,16,125,97
s_add_u32 m0, s38, 3072
M 9,43,1
s_add_u32 s42, s44, s65
RA 228,1,22528
PO 127
LS 40,69
s_addc_u32 s43, s45, 0
BW 71
M 8,30,0
RA 244,7,256
LO 104,17,104
LU 17,104
M 8,31,1
RA 232,1,23552
BR 71
HZ 26,17
M 7,18,0
WL 6
DW 0
LL 16,60
LL 108,61
M 7,19,1
s_lshl_b32 s53, s52, 8
LA 16
HX 25
LS 44,70
s_add_u32 s48, %[alo], s53
RA 248,7,512
M 6,6,0
WL 2
LC_ 108
W1 25,1024
W1 27,3072
M 6,7,1
s_addc_u32 s49, %[ahi], 0
RA 236,1,24576
v_add_u32 v4, s39, v4
LS 48,71
s_add_u32 s52, s52, %[d]
RA 252,7,768
M 6,8,2
LO 105,18,105
LU 18,105
s_cmp_eq_u32 s52, %[wrap]
s_cselect_b32 s52, %[rst], s52
DW 1024
v_mul_f32 v36, 0xbb2aaaab, v36
M 6,9,3
DW 2048
HS 26,18
v_mul_f32 v37, 0xbb2aaaab, v37
v_mul_f32 v38, 0xbb2aaaab, v38
M 7,20,2
LL 17,62
LL 109,63
v_mul_f32 v39, 0xbb2aaaab, v39
M 7,21,3
LA 17
ST 124
LK 40,240,60,0
LK 44,242,60,2
s_mul_i32 s53, s51, 24
RA 240,7,1024
v_add_u32 v1, s37, v0
LK 48,244,61,0
CF 40,40,41
LK 248,246,61,2
s_bitcmp1_b32 s51, 0
RA 244,7,1280
CF 42,42,43
M 8,32,2
s_addc_u32 s53, s53, 0
LC_ 109
RA 216,1,0
M 8,33,3
s_lshl_b32 s53, s53, 11
v_and_b32 v22, 0xfffffcff, v1
CF 44,44,45
CF 46,46,47
M 9,44,2
WL 6
LO 108,16,108
LU 16,108
AW 120,40
AW 121,42
M 9,45,3
s_bitcmp1_b64 s[92:93], s51
RA 220,1,1024
HS 26,16
s_cselect_b64 s[94:95], s[88:89], s[86:87]
s_add_u32 s94, s94, s53
s_addc_u32 s95, s95, 0
s_add_u32 s51, s51, %[d]
s_cmp_eq_u32 s51, %[wrap]
s_cselect_b32 s51, %[rst], s51
DQ 3072
AW 122,44
AW 123,46
LK 40,248,62,0
CF 48,48,49
LK 44,250,62,2
s_add_u32 s50, s50, %[d]
RA 248,7,1536
CF 50,50,51
GZ 60
s_cmp_eq_u32 s50, %[wrap]
CF 248,248,249
CF 250,250,251
M 6,10,4
s_cselect_b32 s50, %[rst], s50
LL 18,64
LL 112,65
M 6,11,5
LA 18
RV 252,22,5120
M 7,22,4
WL 7
LC_ 112
v_add_u32 v19, 1, v19
AW 124,48
M 7,23,5
s_add_u32 s38, s38, 32768
RA 224,1,2048
AW 125,50
AW 126,248
AW 127,250
LK 48,252,63,0
CF 40,40,41
LK 248,254,63,2
s_cmp_eq_u32 s38, s77
RA 252,7,1792
CF 42,42,43
GS 61
s_cselect_b32 s38, s76, s38
CF 44,44,45
CF 46,46,47
M 8,34,4
WL 7
LO 109,17,109
LU 17,109
M 8,35,5
FP
HS 26,17
M 9,46,4
WL 4
LL 16,66
LL 113,67
M 9,47,5
s_sub_u32 s39, 0, s39
LA 16
RA 228,1,3072
AW 128,40
AW 129,42
Z 0,0,0
LC_ 113
AW 130,44
AW 131,46
M 0,1,1
LK 40,240,64,0
CF 48,48,49
LK 44,242,64,2
RA 240,7,2048
CF 50,50,51
GS 62
CF 248,248,249
CF 250,250,251
Z 1,12,0
WL 7
LO 112,18,112
LU 18,112
M 1,13,1
RA 232,1,4096
HZ 29,18
AW 132,48
Z 2,24,0
LL 17,68
LL 116,69
AW 133,50
AW 134,248
M 2,25,1
LA 17
FR
AW 135,250
LK 48,244,65,0
CF 40,40,41
LK 248,246,65,2
RA 244,7,2304
CF 42,42,43
GS 63
CF 44,44,45
CF 46,46,47
Z 10,36,0
WL 6
LC_ 116
AW 136,40
M 10,37,1
RA 236,1,6144
AW 137,42
M 10,38,2
WL 7
LO 113,16,113
LU 16,113
M 10,39,3
HS 29,16
AW 138,44
AW 139,46
LK 40,248,66,0
CF 48,48,49
LK 44,250,66,2
RA 248,7,2560
CF 50,50,51
GS 64
CF 248,248,249
CF 250,250,251
M 2,26,2
LL 18,70
LL 117,71
)"
R"(M 2,27,3
LA 18
RA 216,1,7168
M 1,14,2
WL 6
LC_ 117
v_cmp_gt_u32 vcc, v19, v15
M 1,15,3
AW 140,48
AW 141,50
AW 142,248
M 0,2,2
WL 4
LO 116,17,116
LU 17,116
AW 143,250
M 0,3,3
RA 220,1,8192
HS 29,17
LK 48,252,67,0
CF 40,40,41
LK 248,254,67,2
RA 252,7,2816
CF 42,42,43
GS 65
CF 44,44,45
CF 46,46,47
M 0,4,4
s_cbranch_vccnz .Lk1s19
.Lk1r19:
LL 16,80
LL 106,81
M 0,5,5
LA 16
ds_read_b64 v[14:15], v5 offset:0
AW 144,40
AW 145,42
M 1,16,4
LC_ 106
AW 146,44
AW 147,46
M 1,17,5
RA 224,1,9216
LK 40,240,68,0
CF 48,48,49
LK 44,242,68,2
CF 50,50,51
GS 66
CF 248,248,249
CF 250,250,251
M 2,28,4
WL 6
LO 117,18,117
LU 18,117
M 2,29,5
HS 29,18
AW 148,48
M 10,40,4
WL 5
LL 17,82
LL 107,83
AW 149,50
AW 150,248
AW 151,250
M 10,41,5
LA 17
RA 228,1,10240
LK 48,244,69,0
CF 40,40,41
LK 248,246,69,2
CF 42,42,43
v_pk_add_f16 v26, v26, v29
GS 67
CF 44,44,45
CF 46,46,47
M 10,42,0
LC_ 107
HX 26
M 10,43,1
W1 26,2048
AW 152,40
M 2,30,0
WL 5
LO 16,16,106
LU 106,16
AW 153,42
AW 154,44
M 2,31,1
RA 232,1,11264
HZ 27,106
AW 155,46
LK 40,248,70,0
CF 48,48,49
LK 44,250,70,2
CF 50,50,51
v_add_f32 v14, v14, v15
GS 68
CF 248,248,249
CF 250,250,251
M 1,18,0
LL 18,84
LL 110,85
M 1,19,1
LA 18
PK 106,16,104,56
M 0,6,0
WL 4
LC_ 110
PK 104,16,104,97
AW 156,48
AW 157,50
AW 158,248
M 0,7,1
RV 252,22,5392
v_add_f32_dpp v14, v14, v14 quad_perm:[1,0,3,2] row_mask:0xf bank_mask:0xf
AW 159,250
LK 48,252,71,0
CF 40,40,41
LK 248,254,71,2
CF 42,42,43
v_add_f32_dpp v14, v14, v14 quad_perm:[2,3,0,1] row_mask:0xf bank_mask:0xf
GS 69
CF 44,44,45
CF 46,46,47
M 0,8,2
WL 5
LO 17,17,107
LU 107,17
M 0,9,3
s_add_u32 s42, s94, s60
RA 236,1,12288
HS 27,107
M 1,20,2
s_addc_u32 s43, s95, 0
LL 16,86
LL 111,87
AW 160,40
AW 161,42
M 1,21,3
LA 16
RV 36,8,6912
PK 107,17,105,56
AW 162,44
AW 163,46
M 2,32,2
LC_ 111
PK 105,17,105,97
M 2,33,3
RA 216,1,13312
v_add_f32_dpp v14, v14, v14 row_half_mirror row_mask:0xf bank_mask:0xf
GS 70
CF 48,48,49
CF 50,50,51
CF 248,248,249
CF 250,250,251
M 10,44,2
WL 4
LO 18,18,110
LU 110,18
AW 164,48
AW 165,50
AW 166,248
M 10,45,3
RA 220,1,14336
HS 27,110
AW 167,250
s_nop 1
GS 71
SU 104
v_cvt_f64_f32 v[14:15], v14
s_nop 5
v_cndmask_b32 v52, v52, v53, s[78:79]
M 0,10,4
LL 17,88
LL 114,89
v_cndmask_b32 v54, v54, v55, s[78:79]
M 0,11,5
LA 17
PK 110,18,108,56
v_cndmask_b32 v52, v52, v54, s[80:81]
M 1,22,4
LC_ 114
PK 108,18,108,97
v_mul_f32 v52, 0x3b2aaaab, v52
M 1,23,5
WL 5
RA 224,1,15360
PO 110
v_med3_f32 v53, v52, v39, v37
M 2,34,4
WL 5
LO 16,16,111
LU 111,16
s_mov_b64 exec, s[54:55]
SP
s_mov_b64 exec, -1
LG 52,53
s_nop 0
C 52,52,52
M 2,35,5
s_add_u32 s42, s94, s61
ds_read_b64 v[14:15], v5 offset:2048
HS 27,111
v_lshlrev_b32 v52, 16, v52
M 10,46,4
WL 4
LL 18,90
LL 115,91
v_add_f32 v52, v36, v52
M 10,47,5
s_addc_u32 s43, s95, 0
LA 18
PK 111,16,109,56
C 52,52,52
Z 6,0,0
LC_ 115
PK 109,16,109,97
v_lshlrev_b32 v52, 16, v52
M 6,1,1
RA 228,1,16384
PO 111
v_sqrt_f32 v52, v52
s_nop 0
C 52,52,52
Z 7,12,0
WL 5
LO 17,17,114
LU 114,17
v_lshlrev_b32 v52, 16, v52
M 7,13,1
RA 232,1,17408
HZ 29,114
v_rcp_f32 v52, v52
Z 8,24,0
LL 16,92
LL 118,93
v_mul_f32 v52, v38, v52
M 8,25,1
LA 16
PK 114,17,112,56
v_subrev_u32 v5, s39, v5
CF 52,52,52
Z 11,36,0
WL 2
LC_ 118
PK 112,17,112,97
ds_bpermute_b32 v13, v28, v52
M 11,37,1
RA 236,1,18432
v_add_f32 v14, v14, v15
M 11,38,2
LO 18,18,115
LU 115,18
M 11,39,3
SU 108
HS 29,115
v_add_f32_dpp v14, v14, v14 quad_perm:[1,0,3,2] row_mask:0xf bank_mask:0xf
M 8,26,2
s_mul_i32 s53, s50, 25600
LL 17,94
LL 119,95
s_add_u32 s40, %[wlo], s53
s_addc_u32 s41, %[whi], 0
s_mov_b32 m0, s38
M 8,27,3
LA 17
PK 115,18,113,56
v_add_f32_dpp v14, v14, v14 quad_perm:[2,3,0,1] row_mask:0xf bank_mask:0xf
M 7,14,2
LC_ 119
PK 113,18,113,97
M 7,15,3
RA 216,1,19456
DW 0
v_add_f32_dpp v14, v14, v14 row_half_mirror row_mask:0xf bank_mask:0xf
M 6,2,2
WL 3
LO 16,16,118
LU 118,16
M 6,3,3
s_add_u32 s42, s94, s62
RA 220,1,20480
HS 29,118
v_cvt_f64_f32 v[14:15], v14
s_addc_u32 s43, s95, 0
AR 248,120
M 6,4,4
s_add_u32 s37, s37, 32768
LL 18,56
LL 104,57
s_cmp_eq_u32 s37, s75
s_cselect_b32 s37, s74, s37
AR 249,121
M 6,5,5
LA 18
PK 118,16,116,56
s_mov_b64 exec, s[54:55]
SP
s_mov_b64 exec, -1
DW 1024
AR 250,122
M 7,16,4
LC_ 104
PK 116,16,116,97
AR 251,123
AR 52,124
M 7,17,5
RA 224,1,21504
PO 118
AR 53,125
M 8,28,4
WL 5
LO 17,17,119
LU 119,17
SU 112
AR 54,126
AR 55,127
M 8,29,5
DW 2048
HS 29,119
XS 248
M 11,40,4
WL 3
LL 16,58
LL 105,59
XS 249
M 11,41,5
s_add_u32 s40, s40, 3072
LA 16
PK 119,17,117,56
XS 250
XS 251
M 11,42,0
s_addc_u32 s41, s41, 0
LC_ 105
PK 117,17,117,97
s_add_u32 m0, s38, 3072
AW 120,248
M 11,43,1
s_add_u32 s42, s94, s63
RA 228,1,22528
PO 119
AW 121,249
AW 122,250
M 8,30,0
WL 4
LO 104,18,104
LU 18,104
AW 123,251
M 8,31,1
s_addc_u32 s43, s95, 0
RA 232,1,23552
HZ 26,18
XS 52
XS 53
M 7,18,0
DW 0
LL 17,60
LL 108,61
XS 54
M 7,19,1
s_lshl_b32 s53, s52, 8
LA 17
v_pk_add_f16 v27, v27, v29
XS 55
M 6,6,0
WL 2
LC_ 108
HX 27
AW 124,52
AW 125,53
M 6,7,1
s_add_u32 s48, %[alo], s53
RA 236,1,24576
W1 27,3072
AW 126,54
M 6,8,2
LO 105,16,105
LU 16,105
s_addc_u32 s49, %[ahi], 0
s_add_u32 s52, s52, %[d]
s_cmp_eq_u32 s52, %[wrap]
s_cselect_b32 s52, %[rst], s52
DW 1024
AW 127,55
v_min_u32 v35, v35, v13
M 6,9,3
SU 116
HS 26,16
AR 248,128
M 7,20,2
s_mul_i32 s53, s51, 24
LL 18,62
LL 109,63
s_bitcmp1_b32 s51, 0
s_addc_u32 s53, s53, 0
s_lshl_b32 s53, s53, 11
s_bitcmp1_b64 s[92:93], s51
s_cselect_b64 s[94:95], s[88:89], s[86:87]
s_add_u32 s94, s94, s53
)"
R"(s_addc_u32 s95, s95, 0
s_add_u32 s51, s51, %[d]
s_cmp_eq_u32 s51, %[wrap]
s_cselect_b32 s51, %[rst], s51
AR 249,129
AR 250,130
M 7,21,3
LA 18
DW 2048
AR 251,131
M 8,32,2
LC_ 109
DQ 3072
XS 248
M 8,33,3
s_add_u32 s50, s50, %[d]
v_add_u32 v1, s37, v0
v_add_u32 v4, s39, v4
XS 249
XS 250
M 11,44,2
WL 3
LO 108,17,108
LU 17,108
XS 251
M 11,45,3
s_cmp_eq_u32 s50, %[wrap]
RA 216,1,0
HS 26,17
AW 128,248
AW 129,249
M 6,10,4
s_cselect_b32 s50, %[rst], s50
LL 16,64
LL 112,65
AW 130,250
M 6,11,5
LA 16
v_and_b32 v22, 0xfffffcff, v1
AW 131,251
AR 52,132
M 7,22,4
LC_ 112
RA 220,1,1024
AR 53,133
M 7,23,5
RV 252,22,5120
v_add_u32 v19, 1, v19
AR 54,134
M 8,34,4
WL 5
LO 109,18,109
LU 18,109
AR 55,135
XS 52
M 8,35,5
s_add_u32 s38, s38, 32768
RA 224,1,2048
HS 26,18
XS 53
M 11,46,4
WL 1
LL 17,66
LL 113,67
XS 54
XS 55
M 11,47,5
s_cmp_eq_u32 s38, s77
LA 17
RA 228,1,3072
s_cselect_b32 s38, s76, s38
FP
s_sub_u32 s39, 0, s39
AW 132,52
Z 0,0,0
LC_ 113
AW 133,53
AW 134,54
M 0,1,1
AW 135,55
AR 248,136
Z 1,12,0
LO 112,16,112
LU 16,112
M 1,13,1
RA 232,1,4096
HZ 29,16
AR 249,137
AR 250,138
Z 2,24,0
LL 18,96
LL 116,97
AR 251,139
M 2,25,1
LA 18
XS 248
XS 249
Z 3,36,0
LC_ 116
XS 250
M 3,37,1
XS 251
AW 136,248
Z 4,48,0
WL 3
LO 113,17,113
LU 17,113
AW 137,249
M 4,49,1
RA 236,1,6144
HS 29,17
AW 138,250
Z 5,60,0
LL 16,98
LL 117,99
AW 139,251
AR 52,140
M 5,61,1
LA 16
AR 53,141
M 5,62,2
LC_ 117
AR 54,142
AR 55,143
M 5,63,3
XS 52
XS 53
M 4,50,2
WL 3
LO 116,18,116
LU 18,116
XS 54
M 4,51,3
RA 216,1,7168
HS 29,18
XS 55
M 3,38,2
LL 17,80
LL 106,81
AW 140,52
M 3,39,3
LA 17
FR
AW 141,53
AW 142,54
M 2,26,2
LC_ 106
AW 143,55
M 2,27,3
AR 248,144
AR 249,145
M 1,14,2
WL 4
LO 117,16,117
LU 16,117
AR 250,146
M 1,15,3
RA 220,1,8192
HS 29,16
AR 251,147
M 0,2,2
LL 18,82
LL 107,83
XS 248
XS 249
M 0,3,3
LA 18
v_pk_add_f16 v26, v26, v29
XS 250
M 0,4,4
WL 3
LC_ 107
v_cmp_gt_u32 vcc, v19, v15
XS 251
AW 144,248
M 0,5,5
HX 26
AW 145,249
M 1,16,4
WL 3
LO 17,17,106
LU 106,17
AW 146,250
AW 147,251
M 1,17,5
RA 224,1,9216
HZ 27,106
AR 52,148
M 2,28,4
s_cbranch_vccnz .Lk1s20
.Lk1r20:
LL 16,84
LL 110,85
AR 53,149
M 2,29,5
LA 16
PK 106,17,104,56
AR 54,150
AR 55,151
M 3,40,4
LC_ 110
PK 104,17,104,97
XS 52
M 3,41,5
W1 26,2048
XS 53
XS 54
M 4,52,4
WL 4
LO 18,18,107
LU 107,18
XS 55
M 4,53,5
s_add_u32 s42, s94, s60
RA 228,1,10240
HS 27,107
AW 148,52
AW 149,53
M 5,64,4
LL 17,86
LL 111,87
AW 150,54
AW 151,55
M 5,65,5
s_addc_u32 s43, s95, 0
LA 17
PK 107,18,105,56
M 5,66,0
LC_ 111
PK 105,18,105,97
AR 248,152
AR 249,153
M 5,67,1
SU 104
AR 250,154
M 4,54,0
WL 4
LO 16,16,110
LU 110,16
AR 251,155
XS 248
M 4,55,1
RA 232,1,11264
HS 27,110
XS 249
M 3,42,0
LL 18,88
LL 114,89
XS 250
XS 251
M 3,43,1
LA 18
PK 110,16,108,56
AW 152,248
M 2,30,0
LC_ 114
PK 108,16,108,97
AW 153,249
M 2,31,1
RV 252,22,5392
PO 110
AW 154,250
AW 155,251
M 1,18,0
WL 4
LO 17,17,111
LU 111,17
AR 52,156
M 1,19,1
s_add_u32 s42, s94, s61
RA 236,1,12288
HS 27,111
AR 53,157
AR 54,158
M 0,6,0
LL 16,90
LL 115,91
AR 55,159
M 0,7,1
s_addc_u32 s43, s95, 0
LA 16
PK 111,17,109,56
XS 52
XS 53
M 0,8,2
LC_ 115
PK 109,17,109,97
XS 54
M 0,9,3
PO 111
XS 55
M 1,20,2
WL 4
LO 18,18,114
LU 114,18
AW 156,52
AW 157,53
M 1,21,3
RA 216,1,13312
HZ 29,114
AW 158,54
M 2,32,2
LL 17,100
LL 118,101
AW 159,55
AR 248,160
M 2,33,3
LA 17
PK 114,18,112,56
AR 249,161
M 3,44,2
LC_ 118
PK 112,18,112,97
AR 250,162
AR 251,163
M 3,45,3
SU 108
XS 248
M 4,56,2
WL 3
LO 16,16,115
LU 115,16
XS 249
M 4,57,3
s_add_u32 s42, s94, s62
RA 220,1,14336
HS 29,115
XS 250
XS 251
M 5,68,2
LL 18,102
LL 119,103
AW 160,248
M 5,69,3
s_addc_u32 s43, s95, 0
LA 18
PK 115,16,113,56
AW 161,249
AW 162,250
M 0,10,4
LC_ 119
PK 113,16,113,97
AW 163,251
M 0,11,5
SU 112
AR 52,164
AR 53,165
M 1,22,4
WL 3
LO 17,17,118
LU 118,17
AR 54,166
M 1,23,5
RA 224,1,15360
HS 29,118
AR 55,167
M 2,34,4
LL 16,56
LL 104,57
XS 52
XS 53
M 2,35,5
LA 16
PK 118,17,116,56
XS 54
M 3,46,4
LC_ 104
PK 116,17,116,97
XS 55
AW 164,52
M 3,47,5
ds_read_b64 v[14:15], v5 offset:2048
PO 118
AW 165,53
M 4,58,4
WL 4
LO 18,18,119
LU 119,18
AW 166,54
AW 167,55
M 4,59,5
s_add_u32 s42, s94, s63
RA 228,1,16384
HS 29,119
M 5,70,4
LL 17,58
LL 105,59
M 5,71,5
s_addc_u32 s43, s95, 0
LA 17
PK 119,18,117,56
Z 6,0,0
LC_ 105
PK 117,18,117,97
M 6,1,1
PO 119
v_subrev_u32 v5, s39, v5
Z 7,12,0
WL 4
LO 104,16,104
)"
R"(LU 16,104
M 7,13,1
RA 232,1,17408
HZ 26,16
Z 8,24,0
LL 18,60
LL 108,61
M 8,25,1
WL 4
LA 18
SU 116
Z 9,36,0
s_mul_i32 s53, s50, 25600
LC_ 108
v_add_f32 v14, v14, v15
M 9,37,1
s_add_u32 s40, %[wlo], s53
v_add_f32_dpp v14, v14, v14 quad_perm:[1,0,3,2] row_mask:0xf bank_mask:0xf
v_pk_add_f16 v27, v27, v29
Z 10,48,0
WL 3
LO 105,17,105
LU 17,105
M 10,49,1
s_addc_u32 s41, %[whi], 0
RA 236,1,18432
HS 26,17
Z 11,60,0
LL 16,62
LL 109,63
M 11,61,1
s_mov_b32 m0, s38
LA 16
DW 0
M 11,62,2
LC_ 109
v_add_f32_dpp v14, v14, v14 quad_perm:[2,3,0,1] row_mask:0xf bank_mask:0xf
M 11,63,3
s_nop 0
v_add_f32_dpp v14, v14, v14 row_half_mirror row_mask:0xf bank_mask:0xf
HX 27
M 10,50,2
WL 3
LO 108,18,108
LU 18,108
M 10,51,3
s_add_u32 s37, s37, 32768
RA 216,1,19456
HS 26,18
W1 27,3072
M 9,38,2
s_cmp_eq_u32 s37, s75
LL 17,64
LL 112,65
M 9,39,3
s_cselect_b32 s37, s74, s37
LA 17
DW 1024
M 8,26,2
LC_ 112
v_cvt_f64_f32 v[14:15], v14
M 8,27,3
s_mov_b64 exec, s[54:55]
SP
s_mov_b64 exec, -1
v_add_u32 v4, s39, v4
M 7,14,2
WL 4
LO 109,16,109
LU 16,109
M 7,15,3
RA 220,1,20480
HS 26,16
M 6,2,2
LL 18,66
LL 113,67
M 6,3,3
LA 18
DW 2048
M 6,4,4
LC_ 113
M 6,5,5
M 7,16,4
WL 3
LO 112,17,112
LU 17,112
M 7,17,5
s_mul_i32 s53, s51, 24
RA 224,1,21504
HZ 29,17
M 8,28,4
s_bitcmp1_b32 s51, 0
LL 16,68
LL 116,69
M 8,29,5
s_addc_u32 s53, s53, 0
LA 16
M 9,40,4
s_lshl_b32 s53, s53, 11
LC_ 116
M 9,41,5
s_bitcmp1_b64 s[92:93], s51
M 10,52,4
WL 3
LO 113,18,113
LU 18,113
M 10,53,5
s_cselect_b64 s[94:95], s[88:89], s[86:87]
RA 228,1,22528
HS 29,18
M 11,64,4
LL 17,70
LL 117,71
M 11,65,5
s_add_u32 s94, s94, s53
LA 17
M 11,66,0
s_addc_u32 s95, s95, 0
LC_ 117
M 11,67,1
s_add_u32 s51, s51, %[d]
M 10,54,0
WL 3
LO 116,16,116
LU 16,116
M 10,55,1
s_cmp_eq_u32 s51, %[wrap]
RA 232,1,23552
HS 29,16
M 9,42,0
s_cselect_b32 s51, %[rst], s51
LL 18,72
LL 120,73
M 9,43,1
s_mul_i32 s53, %[d], 32768
LA 18
s_add_u32 s64, s53, %[sb1]
s_add_u32 s65, s64, 512
M 8,30,0
s_add_u32 s40, s40, 3072
LC_ 120
s_addc_u32 s41, s41, 0
s_add_u32 m0, s38, 3072
M 8,31,1
DW 0
M 7,18,0
WL 3
LO 117,17,117
LU 17,117
M 7,19,1
RA 236,1,24576
HS 29,17
M 6,6,0
LL 16,74
LL 121,75
M 6,7,1
LA 16
DW 1024
M 6,8,2
LC_ 121
DW 2048
M 6,9,3
s_lshl_b32 s53, s52, 8
v_add_u32 v1, s37, v0
M 7,20,2
WL 3
LO 120,18,120
LU 18,120
M 7,21,3
s_add_u32 s48, %[alo], s53
RA 216,1,0
HZ 30,18
M 8,32,2
s_addc_u32 s49, %[ahi], 0
LL 17,76
LL 124,77
M 8,33,3
s_add_u32 s52, s52, %[d]
LA 17
v_and_b32 v22, 0xfffffcff, v1
M 9,44,2
s_cmp_eq_u32 s52, %[wrap]
LC_ 124
M 9,45,3
s_cselect_b32 s52, %[rst], s52
DQ 3072
M 10,56,2
WL 3
LO 121,16,121
LU 16,121
M 10,57,3
s_add_u32 s50, s50, %[d]
RA 220,1,1024
HS 30,16
M 11,68,2
LL 18,78
LL 125,79
M 11,69,3
s_cmp_eq_u32 s50, %[wrap]
LA 18
RV 252,22,5120
M 6,10,4
s_cselect_b32 s50, %[rst], s50
LC_ 125
M 6,11,5
WV 25
v_add_u32 v19, 1, v19
M 7,22,4
WL 4
LO 124,17,124
LU 17,124
M 7,23,5
s_add_u32 s38, s38, 32768
RA 224,1,2048
HS 30,17
M 8,34,4
s_cmp_eq_u32 s38, s77
LL 16,80
LL 106,81
M 8,35,5
s_cselect_b32 s38, s76, s38
LA 16
FP
M 9,46,4
s_sub_u32 s39, 0, s39
LC_ 106
M 9,47,5
M 10,58,4
WL 4
LO 125,18,125
LU 18,125
M 10,59,5
RA 228,1,3072
HS 30,18
M 11,70,4
LL 17,82
LL 107,83
M 11,71,5
LA 17
v_pk_add_f16 v26, v26, v30
Z 0,0,0
LC_ 107
v_pk_add_f16 v26, v26, v29
M 0,1,1
HX 26
Z 1,12,0
WL 3
LO 16,16,106
LU 106,16
M 1,13,1
RA 232,1,4096
HZ 27,106
Z 2,24,0
LL 18,84
LL 110,85
M 2,25,1
LA 18
PK 106,16,104,56
Z 3,36,0
LC_ 110
PK 104,16,104,97
M 3,37,1
Z 4,48,0
WL 3
LO 17,17,107
LU 107,17
M 4,49,1
s_add_u32 s42, s94, s60
RA 236,1,6144
HS 27,107
Z 5,60,0
LL 16,86
LL 111,87
M 5,61,1
s_addc_u32 s43, s95, 0
LA 16
PK 107,17,105,56
M 5,62,2
LC_ 111
PK 105,17,105,97
M 5,63,3
SU 104
M 4,50,2
WL 3
LO 18,18,110
LU 110,18
M 4,51,3
RA 216,1,7168
HS 27,110
M 3,38,2
LL 17,88
LL 114,89
M 3,39,3
LA 17
PK 110,18,108,56
M 2,26,2
LC_ 114
PK 108,18,108,97
M 2,27,3
FR
PO 110
M 1,14,2
WL 4
LO 16,16,111
LU 111,16
M 1,15,3
s_add_u32 s42, s94, s61
RA 220,1,8192
HS 27,111
M 0,2,2
LL 18,90
LL 115,91
M 0,3,3
s_addc_u32 s43, s95, 0
LA 18
PK 111,16,109,56
M 0,4,4
PFN
LC_ 115
PK 109,16,109,97
M 0,5,5
s_mov_b32 m0, %[pm]
s_nop 0
DF
PO 111
M 1,16,4
WL 4
LO 17,17,114
LU 114,17
M 1,17,5
RA 224,1,9216
HZ 29,114
M 2,28,4
LL 16,92
LL 118,93
M 2,29,5
LA 16
PK 114,17,112,56
M 3,40,4
LC_ 118
PK 112,17,112,97
M 3,41,5
WL 6
SU 108
v_cmp_gt_u32 vcc, v19, v15
M 4,52,4
WL 3
LO 18,18,115
LU 115,18
M 4,53,5
s_add_u32 s42, s94, s62
RA 228,1,10240
HS 29,115
M 5,64,4
LL 17,94
LL 119,95
M 5,65,5
s_cbranch_vccnz .Lk1s21
.Lk1r21:
LA 17
PK 115,18,113,56
M 5,66,0
s_addc_u32 s43, s95, 0
LC_ 119
PK 113,18,113,97
M 5,67,1
W1 26,2048
M 4,54,0
WL 4
LO 16,16,118
LU 118,16
M 4,55,1
RA 232,1,11264
HS 29,118
M 3,42,0
LL 18,96
LL 122,97
M 3,43,1
LA 18
PK 118,16,116,56
M 2,30,0
LC_ 122
PK 116,16,116,97
M 2,31,1
RV 252,22,5392
PO 118
M 1,18,0
WL 5
LO 17,17,119
LU 119,17
M 1,19,1
RA 236,1,12288
HS 29,119
M 0,6,0
LL 16,98
LL 123,99
M 0,7,1
LA 16
PK 119,17,117,56
M 0,8,2
)"
R"(LC_ 123
PK 117,17,117,97
M 0,9,3
SU 112
PO 119
M 1,20,2
WL 4
LO 18,18,122
LU 122,18
M 1,21,3
s_add_u32 s42, s94, s63
RA 216,1,13312
HZ 30,122
M 2,32,2
s_addc_u32 s43, s95, 0
LL 17,100
LL 126,101
M 2,33,3
LA 17
PK 122,18,120,56
M 3,44,2
LC_ 126
PK 120,18,120,97
M 3,45,3
SU 116
M 4,56,2
WL 3
LO 16,16,123
LU 123,16
M 4,57,3
s_add_u32 s42, s94, s64
RA 220,1,14336
HS 30,123
M 5,68,2
LL 18,102
LL 127,103
M 5,69,3
s_addc_u32 s43, s95, 0
LA 18
PK 123,16,121,56
M 0,10,4
LC_ 127
PK 121,16,121,97
M 0,11,5
SU 120
M 1,22,4
WL 3
LO 17,17,126
LU 126,17
M 1,23,5
RA 224,1,15360
HS 30,126
M 2,34,4
LL 16,56
LL 104,57
M 2,35,5
LA 16
PK 126,17,124,56
M 3,46,4
LC_ 104
PK 124,17,124,97
M 3,47,5
ds_read_b64 v[14:15], v5 offset:2048
PO 126
M 4,58,4
WL 4
LO 18,18,127
LU 127,18
M 4,59,5
s_add_u32 s42, s94, s65
RA 228,1,16384
HS 30,127
M 5,70,4
LL 17,58
LL 105,59
M 5,71,5
s_addc_u32 s43, s95, 0
LA 17
PK 127,18,125,56
Z 6,0,0
LC_ 105
PK 125,18,125,97
M 6,1,1
PO 127
v_subrev_u32 v5, s39, v5
Z 7,12,0
WL 4
LO 104,16,104
LU 16,104
M 7,13,1
RA 232,1,17408
HZ 26,16
Z 8,24,0
WL 4
LL 18,60
LL 108,61
M 8,25,1
s_mul_i32 s53, s50, 25600
LA 18
v_add_f32 v14, v14, v15
Z 9,36,0
s_add_u32 s40, %[wlo], s53
LC_ 108
v_add_f32_dpp v14, v14, v14 quad_perm:[1,0,3,2] row_mask:0xf bank_mask:0xf
M 9,37,1
s_addc_u32 s41, %[whi], 0
v_add_f32_dpp v14, v14, v14 quad_perm:[2,3,0,1] row_mask:0xf bank_mask:0xf
v_pk_add_f16 v27, v27, v30
Z 10,48,0
WL 3
LO 105,17,105
LU 17,105
M 10,49,1
s_mov_b32 m0, s38
RA 236,1,18432
HS 26,17
Z 11,60,0
LL 16,62
LL 109,63
M 11,61,1
LA 16
SU 124
M 11,62,2
LC_ 109
DW 0
M 11,63,3
v_add_f32_dpp v14, v14, v14 row_half_mirror row_mask:0xf bank_mask:0xf
v_pk_add_f16 v27, v27, v29
M 10,50,2
WL 3
LO 108,18,108
LU 18,108
M 10,51,3
s_add_u32 s37, s37, 32768
RA 216,1,19456
HS 26,18
HX 27
M 9,38,2
s_cmp_eq_u32 s37, s75
LL 17,64
LL 112,65
W1 27,3072
M 9,39,3
s_cselect_b32 s37, s74, s37
LA 17
DW 1024
M 8,26,2
LC_ 112
v_cvt_f64_f32 v[14:15], v14
M 8,27,3
s_mov_b64 exec, s[54:55]
SP
s_mov_b64 exec, -1
v_add_u32 v4, s39, v4
s_mov_b64 exec, 1
AT
s_mov_b64 exec, -1
M 7,14,2
WL 4
LO 109,16,109
LU 16,109
M 7,15,3
RA 220,1,20480
HS 26,16
M 6,2,2
LL 18,66
LL 113,67
M 6,3,3
LA 18
DW 2048
M 6,4,4
LC_ 113
M 6,5,5
M 7,16,4
WL 3
LO 112,17,112
LU 17,112
M 7,17,5
s_mul_i32 s53, s51, 24
RA 224,1,21504
HZ 29,17
M 8,28,4
s_bitcmp1_b32 s51, 0
LL 16,68
LL 116,69
M 8,29,5
s_addc_u32 s53, s53, 0
LA 16
M 9,40,4
s_lshl_b32 s53, s53, 11
LC_ 116
M 9,41,5
s_bitcmp1_b64 s[92:93], s51
M 10,52,4
WL 3
LO 113,18,113
LU 18,113
M 10,53,5
s_cselect_b64 s[94:95], s[88:89], s[86:87]
RA 228,1,22528
HS 29,18
M 11,64,4
LL 17,70
LL 117,71
M 11,65,5
s_add_u32 s94, s94, s53
LA 17
M 11,66,0
s_addc_u32 s95, s95, 0
LC_ 117
M 11,67,1
s_add_u32 s51, s51, %[d]
M 10,54,0
WL 3
LO 116,16,116
LU 16,116
M 10,55,1
s_cmp_eq_u32 s51, %[wrap]
RA 232,1,23552
HS 29,16
M 9,42,0
s_cselect_b32 s51, %[rst], s51
LL 18,72
LL 120,73
M 9,43,1
s_add_u32 s40, s40, 3072
LA 18
M 8,30,0
s_addc_u32 s41, s41, 0
LC_ 120
s_add_u32 m0, s38, 3072
M 8,31,1
DW 0
M 7,18,0
WL 3
LO 117,17,117
LU 17,117
M 7,19,1
RA 236,1,24576
HS 29,17
M 6,6,0
LL 16,74
LL 121,75
M 6,7,1
LA 16
DW 1024
M 6,8,2
LC_ 121
DW 2048
M 6,9,3
s_lshl_b32 s53, s52, 8
v_add_u32 v1, s37, v0
M 7,20,2
WL 3
LO 120,18,120
LU 18,120
M 7,21,3
s_add_u32 s48, %[alo], s53
RA 216,1,0
HZ 30,18
M 8,32,2
s_addc_u32 s49, %[ahi], 0
LL 17,76
LL 124,77
M 8,33,3
s_add_u32 s52, s52, %[d]
LA 17
v_and_b32 v22, 0xfffffcff, v1
M 9,44,2
s_cmp_eq_u32 s52, %[wrap]
LC_ 124
M 9,45,3
s_cselect_b32 s52, %[rst], s52
DQ 3072
M 10,56,2
WL 3
LO 121,16,121
LU 16,121
M 10,57,3
s_add_u32 s50, s50, %[d]
RA 220,1,1024
HS 30,16
M 11,68,2
LL 18,78
LL 125,79
M 11,69,3
s_cmp_eq_u32 s50, %[wrap]
LA 18
RV 252,22,5120
M 6,10,4
s_cselect_b32 s50, %[rst], s50
LC_ 125
M 6,11,5
WV 28
M 7,22,4
WL 4
LO 124,17,124
LU 17,124
M 7,23,5
s_add_u32 s38, s38, 32768
RA 224,1,2048
HS 30,17
M 8,34,4
s_cmp_eq_u32 s38, s77
LL 16,80
LL 106,81
M 8,35,5
s_cselect_b32 s38, s76, s38
LA 16
M 9,46,4
s_barrier
LC_ 106
M 9,47,5
s_sub_u32 s39, 0, s39
M 10,58,4
WL 3
LO 125,18,125
LU 18,125
M 10,59,5
RA 228,1,3072
HS 30,18
M 11,70,4
LL 17,82
LL 107,83
M 11,71,5
LA 17
v_pk_add_f16 v26, v26, v30
Z 0,0,0
LC_ 107
v_pk_add_f16 v26, v26, v29
M 0,1,1
HX 26
Z 1,12,0
WL 3
LO 16,16,106
LU 106,16
M 1,13,1
RA 232,1,4096
HZ 27,106
Z 2,24,0
LL 18,84
LL 110,85
M 2,25,1
LA 18
PK 106,16,104,56
Z 3,36,0
LC_ 110
PK 104,16,104,97
M 3,37,1
W1 26,2048
Z 4,48,0
WL 4
LO 17,17,107
LU 107,17
M 4,49,1
s_add_u32 s42, s94, s60
RA 236,1,6144
HS 27,107
Z 5,60,0
LL 16,86
LL 111,87
M 5,61,1
s_addc_u32 s43, s95, 0
LA 16
PK 107,17,105,56
M 5,62,2
LC_ 111
PK 105,17,105,97
M 5,63,3
SU 104
M 4,50,2
WL 4
LO 18,18,110
LU 110,18
M 4,51,3
RA 216,1,7168
HS 27,110
M 3,38,2
LL 17,88
LL 114,89
M 3,39,3
LA 17
PK 110,18,108,56
M 2,26,2
LC_ 114
PK 108,18,108,97
M 2,27,3
PO 110
M 1,14,2
WL 3
LO 16,16,111
LU 111,16
M 1,15,3
s_add_u32 s42, s94, s61
RA 220,1,8192
HS 27,111
M 0,2,2
LL 18,90
LL 115,91
M 0,3,3
)"
R"(s_addc_u32 s43, s95, 0
LA 18
PK 111,16,109,56
M 0,4,4
PFW
LC_ 115
PK 109,16,109,97
M 0,5,5
s_mov_b32 m0, %[pm]
s_nop 0
DF
PO 111
M 1,16,4
WL 3
LO 17,17,114
LU 114,17
M 1,17,5
RA 224,1,9216
HZ 29,114
M 2,28,4
LL 16,92
LL 118,93
M 2,29,5
LA 16
PK 114,17,112,56
M 3,40,4
LC_ 118
PK 112,17,112,97
M 3,41,5
SU 108
M 4,52,4
WL 3
LO 18,18,115
LU 115,18
M 4,53,5
s_add_u32 s42, s94, s62
RA 228,1,10240
HS 29,115
M 5,64,4
LL 17,94
LL 119,95
M 5,65,5
s_addc_u32 s43, s95, 0
LA 17
PK 115,18,113,56
M 5,66,0
LC_ 119
PK 113,18,113,97
M 5,67,1
SU 112
M 4,54,0
WL 3
LO 16,16,118
LU 118,16
M 4,55,1
RA 232,1,11264
HS 29,118
M 3,42,0
LL 18,96
LL 122,97
M 3,43,1
LA 18
PK 118,16,116,56
M 2,30,0
LC_ 122
PK 116,16,116,97
M 2,31,1
RV 252,22,5392
PO 118
M 1,18,0
WL 4
LO 17,17,119
LU 119,17
M 1,19,1
s_add_u32 s42, s94, s63
RA 236,1,12288
HS 29,119
M 0,6,0
LL 16,98
LL 123,99
M 0,7,1
s_addc_u32 s43, s95, 0
LA 16
PK 119,17,117,56
M 0,8,2
LC_ 123
PK 117,17,117,97
M 0,9,3
PO 119
M 1,20,2
WL 4
LO 18,18,122
LU 122,18
M 1,21,3
RA 216,1,13312
HZ 30,122
M 2,32,2
LL 17,100
LL 126,101
M 2,33,3
LA 17
PK 122,18,120,56
M 3,44,2
LC_ 126
PK 120,18,120,97
M 3,45,3
SU 116
M 4,56,2
WL 3
LO 16,16,123
LU 123,16
M 4,57,3
s_add_u32 s42, s94, s64
RA 220,1,14336
HS 30,123
M 5,68,2
LL 18,102
LL 127,103
M 5,69,3
s_addc_u32 s43, s95, 0
LA 18
PK 123,16,121,56
M 0,10,4
LC_ 127
PK 121,16,121,97
M 0,11,5
SU 120
M 1,22,4
WL 3
LO 17,17,126
LU 126,17
M 1,23,5
RA 224,1,15360
HS 30,126
M 2,34,4
LL 16,56
LL 104,57
M 2,35,5
LA 16
PK 126,17,124,56
M 3,46,4
LC_ 104
PK 124,17,124,97
M 3,47,5
ds_read_b64 v[14:15], v5 offset:2048
PO 126
M 4,58,4
WL 4
LO 18,18,127
LU 127,18
M 4,59,5
s_add_u32 s42, s94, s65
RA 228,1,16384
HS 30,127
M 5,70,4
LL 17,58
LL 105,59
M 5,71,5
s_addc_u32 s43, s95, 0
LA 17
PK 127,18,125,56
Z 6,0,0
LC_ 105
PK 125,18,125,97
M 6,1,1
PO 127
v_subrev_u32 v5, s39, v5
Z 7,12,0
WL 4
LO 104,16,104
LU 16,104
M 7,13,1
RA 232,1,17408
HZ 26,16
Z 8,24,0
WL 4
LL 18,60
LL 108,61
M 8,25,1
s_mul_i32 s53, s50, 25600
LA 18
v_add_f32 v14, v14, v15
Z 9,36,0
s_add_u32 s40, %[wlo], s53
LC_ 108
v_add_f32_dpp v14, v14, v14 quad_perm:[1,0,3,2] row_mask:0xf bank_mask:0xf
M 9,37,1
s_addc_u32 s41, %[whi], 0
Z 10,48,0
WL 3
LO 105,17,105
LU 17,105
M 10,49,1
s_mov_b32 m0, s38
RA 236,1,18432
HS 26,17
Z 11,60,0
LL 16,62
LL 109,63
M 11,61,1
LA 16
SU 124
M 11,62,2
LC_ 109
DW 0
M 11,63,3
WV 13
v_add_f32_dpp v14, v14, v14 quad_perm:[2,3,0,1] row_mask:0xf bank_mask:0xf
v_pk_add_f16 v27, v27, v30
M 10,50,2
WL 3
LO 108,18,108
LU 18,108
v_pk_add_f16 v27, v27, v29
M 10,51,3
s_add_u32 s37, s37, 32768
RA 216,1,19456
HS 26,18
HX 27
M 9,38,2
s_cmp_eq_u32 s37, s75
LL 17,64
LL 112,65
W1 27,3072
M 9,39,3
s_cselect_b32 s37, s74, s37
LA 17
DW 1024
v_readfirstlane_b32 s72, v11
M 8,26,2
LC_ 112
v_add_f32_dpp v14, v14, v14 row_half_mirror row_mask:0xf bank_mask:0xf
v_mov_b32 v12, s72
M 8,27,3
W2 12,12,16
v_cvt_f64_f32 v[14:15], v14
M 7,14,2
WL 5
LO 109,16,109
LU 16,109
M 7,15,3
RA 220,1,20480
HS 26,16
M 6,2,2
LL 18,66
LL 113,67
M 6,3,3
LA 18
s_mov_b64 exec, s[54:55]
SP
s_mov_b64 exec, -1
M 6,4,4
LC_ 113
DW 2048
M 6,5,5
v_add_u32 v4, s39, v4
s_mov_b64 exec, 1
AT
s_mov_b64 exec, -1
M 7,16,4
WL 4
LO 112,17,112
LU 17,112
M 7,17,5
s_mul_i32 s53, s51, 24
RA 224,1,21504
HZ 29,17
M 8,28,4
s_bitcmp1_b32 s51, 0
LL 16,68
LL 116,69
M 8,29,5
s_addc_u32 s53, s53, 0
LA 16
M 9,40,4
s_lshl_b32 s53, s53, 11
LC_ 116
M 9,41,5
s_bitcmp1_b64 s[92:93], s51
M 10,52,4
WL 3
LO 113,18,113
LU 18,113
M 10,53,5
s_cselect_b64 s[94:95], s[88:89], s[86:87]
RA 228,1,22528
HS 29,18
M 11,64,4
LL 17,70
LL 117,71
M 11,65,5
s_add_u32 s94, s94, s53
LA 17
M 11,66,0
s_addc_u32 s95, s95, 0
LC_ 117
M 11,67,1
s_add_u32 s51, s51, %[d]
M 10,54,0
WL 3
LO 116,16,116
LU 16,116
M 10,55,1
s_cmp_eq_u32 s51, %[wrap]
RA 232,1,23552
HS 29,16
M 9,42,0
s_cselect_b32 s51, %[rst], s51
LL 18,72
LL 120,73
M 9,43,1
s_add_u32 s40, s40, 3072
LA 18
M 8,30,0
s_addc_u32 s41, s41, 0
LC_ 120
s_add_u32 m0, s38, 3072
M 8,31,1
DW 0
M 7,18,0
WL 3
LO 117,17,117
LU 17,117
M 7,19,1
RA 236,1,24576
HS 29,17
M 6,6,0
LL 16,74
LL 121,75
M 6,7,1
LA 16
DW 1024
M 6,8,2
LC_ 121
DW 2048
M 6,9,3
s_lshl_b32 s53, s52, 8
v_add_u32 v1, s37, v0
M 7,20,2
WL 3
LO 120,18,120
LU 18,120
M 7,21,3
s_add_u32 s48, %[alo], s53
RA 216,1,0
HZ 30,18
M 8,32,2
s_addc_u32 s49, %[ahi], 0
LL 17,76
LL 124,77
M 8,33,3
s_add_u32 s52, s52, %[d]
LA 17
v_and_b32 v22, 0xfffffcff, v1
M 9,44,2
s_cmp_eq_u32 s52, %[wrap]
LC_ 124
M 9,45,3
s_cselect_b32 s52, %[rst], s52
DQ 3072
M 10,56,2
WL 3
LO 121,16,121
LU 16,121
M 10,57,3
s_add_u32 s50, s50, %[d]
RA 220,1,1024
HS 30,16
M 11,68,2
LL 18,78
LL 125,79
M 11,69,3
s_cmp_eq_u32 s50, %[wrap]
LA 18
RV 252,22,5120
M 6,10,4
s_cselect_b32 s50, %[rst], s50
LC_ 125
M 6,11,5
M 7,22,4
WL 4
LO 124,17,124
LU 17,124
M 7,23,5
s_add_u32 s38, s38, 32768
RA 224,1,2048
HS 30,17
M 8,34,4
s_cmp_eq_u32 s38, s77
LL 16,80
LL 106,81
M 8,35,5
s_cselect_b32 s38, s76, s38
LA 16
M 9,46,4
s_barrier
LC_ 106
M 9,47,5
s_sub_u32 s39, 0, s39
M 10,58,4
)"
R"(WL 3
LO 125,18,125
LU 18,125
M 10,59,5
RA 228,1,3072
HS 30,18
M 11,70,4
LL 17,82
LL 107,83
M 11,71,5
2:
LA 17
v_pk_add_f16 v26, v26, v30
Z 0,0,0
LC_ 107
ds_read_b32 v20, v5 offset:4096
M 0,1,1
v_pk_add_f16 v26, v26, v29
Z 1,12,0
WL 4
LO 16,16,106
LU 106,16
M 1,13,1
RA 232,1,4096
HZ 27,106
Z 2,24,0
LL 18,84
LL 110,85
M 2,25,1
LA 18
PK 106,16,104,56
Z 3,36,0
LC_ 110
PK 104,16,104,97
M 3,37,1
HX 26
Z 4,48,0
WL 4
LO 17,17,107
LU 107,17
M 4,49,1
s_add_u32 s42, s94, s60
RA 236,1,6144
HS 27,107
Z 5,60,0
LL 16,86
LL 111,87
M 5,61,1
s_addc_u32 s43, s95, 0
LA 16
PK 107,17,105,56
M 5,62,2
LC_ 111
PK 105,17,105,97
M 5,63,3
SU 104
M 4,50,2
WL 3
LO 18,18,110
LU 110,18
M 4,51,3
RA 216,1,7168
HS 27,110
M 3,38,2
LL 17,88
LL 114,89
W1 26,2048
M 3,39,3
LA 17
PK 110,18,108,56
M 2,26,2
LC_ 114
PK 108,18,108,97
M 2,27,3
PO 110
v_readfirstlane_b32 s59, v20
M 1,14,2
WL 4
LO 16,16,111
LU 111,16
M 1,15,3
s_add_u32 s42, s94, s61
RA 220,1,8192
HS 27,111
M 0,2,2
LL 18,90
LL 115,91
M 0,3,3
s_addc_u32 s43, s95, 0
LA 18
PK 111,16,109,56
M 0,4,4
LC_ 115
PK 109,16,109,97
M 0,5,5
PO 111
M 1,16,4
WL 3
LO 17,17,114
LU 114,17
M 1,17,5
RA 224,1,9216
HZ 29,114
M 2,28,4
LL 16,92
LL 118,93
M 2,29,5
LA 16
PK 114,17,112,56
M 3,40,4
LC_ 118
PK 112,17,112,97
M 3,41,5
SU 108
M 4,52,4
WL 3
LO 18,18,115
LU 115,18
M 4,53,5
s_add_u32 s42, s94, s62
RA 228,1,10240
HS 29,115
M 5,64,4
LL 17,94
LL 119,95
M 5,65,5
s_addc_u32 s43, s95, 0
LA 17
PK 115,18,113,56
M 5,66,0
LC_ 119
PK 113,18,113,97
M 5,67,1
SU 112
M 4,54,0
WL 3
LO 16,16,118
LU 118,16
M 4,55,1
RA 232,1,11264
HS 29,118
M 3,42,0
LL 18,96
LL 122,97
M 3,43,1
LA 18
PK 118,16,116,56
M 2,30,0
LC_ 122
PK 116,16,116,97
M 2,31,1
RV 252,22,5392
PO 118
M 1,18,0
WL 4
LO 17,17,119
LU 119,17
M 1,19,1
s_add_u32 s42, s94, s63
RA 236,1,12288
HS 29,119
M 0,6,0
LL 16,98
LL 123,99
M 0,7,1
s_addc_u32 s43, s95, 0
LA 16
PK 119,17,117,56
M 0,8,2
LC_ 123
PK 117,17,117,97
M 0,9,3
PO 119
M 1,20,2
WL 4
LO 18,18,122
LU 122,18
M 1,21,3
RA 216,1,13312
HZ 30,122
M 2,32,2
LL 17,100
LL 126,101
M 2,33,3
LA 17
PK 122,18,120,56
M 3,44,2
LC_ 126
PK 120,18,120,97
M 3,45,3
SU 116
M 4,56,2
WL 3
LO 16,16,123
LU 123,16
M 4,57,3
s_add_u32 s42, s94, s64
RA 220,1,14336
HS 30,123
M 5,68,2
LL 18,102
LL 127,103
M 5,69,3
s_addc_u32 s43, s95, 0
LA 18
PK 123,16,121,56
M 0,10,4
LC_ 127
PK 121,16,121,97
M 0,11,5
SU 120
M 1,22,4
WL 3
LO 17,17,126
LU 126,17
M 1,23,5
RA 224,1,15360
HS 30,126
M 2,34,4
LL 16,56
LL 104,57
M 2,35,5
LA 16
PK 126,17,124,56
M 3,46,4
LC_ 104
PK 124,17,124,97
M 3,47,5
ds_read_b64 v[14:15], v5 offset:2048
PO 126
M 4,58,4
WL 4
LO 18,18,127
LU 127,18
M 4,59,5
s_add_u32 s42, s94, s65
RA 228,1,16384
HS 30,127
M 5,70,4
LL 17,58
LL 105,59
M 5,71,5
s_addc_u32 s43, s95, 0
LA 17
PK 127,18,125,56
Z 6,0,0
LC_ 105
PK 125,18,125,97
M 6,1,1
PO 127
v_subrev_u32 v5, s39, v5
Z 7,12,0
WL 4
LO 104,16,104
LU 16,104
M 7,13,1
RA 232,1,17408
HZ 26,16
Z 8,24,0
WL 4
LL 18,60
LL 108,61
M 8,25,1
s_mul_i32 s53, s50, 25600
LA 18
v_add_f32 v14, v14, v15
Z 9,36,0
s_add_u32 s40, %[wlo], s53
LC_ 108
v_add_f32_dpp v14, v14, v14 quad_perm:[1,0,3,2] row_mask:0xf bank_mask:0xf
M 9,37,1
s_addc_u32 s41, %[whi], 0
Z 10,48,0
WL 3
LO 105,17,105
LU 17,105
M 10,49,1
s_mov_b32 m0, s38
RA 236,1,18432
HS 26,17
Z 11,60,0
LL 16,62
LL 109,63
M 11,61,1
LA 16
SU 124
M 11,62,2
LC_ 109
DW 0
M 11,63,3
WV 11
v_add_f32_dpp v14, v14, v14 quad_perm:[2,3,0,1] row_mask:0xf bank_mask:0xf
v_pk_add_f16 v27, v27, v30
M 10,50,2
WL 3
LO 108,18,108
LU 18,108
v_pk_add_f16 v27, v27, v29
M 10,51,3
s_add_u32 s37, s37, 32768
RA 216,1,19456
HS 26,18
HX 27
M 9,38,2
s_cmp_eq_u32 s37, s75
LL 17,64
LL 112,65
W1 27,3072
M 9,39,3
s_cselect_b32 s37, s74, s37
LA 17
DW 1024
v_readfirstlane_b32 s72, v11
M 8,26,2
LC_ 112
v_add_f32_dpp v14, v14, v14 row_half_mirror row_mask:0xf bank_mask:0xf
v_mov_b32 v12, s72
M 8,27,3
s_max_u32 s59, s59, %[nb]
W2 12,12,16
v_cvt_f64_f32 v[14:15], v14
M 7,14,2
WL 5
LO 109,16,109
LU 16,109
M 7,15,3
s_add_u32 %[nb], %[nb], 1
RA 220,1,20480
HS 26,16
M 6,2,2
LL 18,66
LL 113,67
M 6,3,3
LA 18
s_mov_b64 exec, s[54:55]
SP
s_mov_b64 exec, -1
M 6,4,4
LC_ 113
DW 2048
M 6,5,5
v_add_u32 v4, s39, v4
s_mov_b64 exec, 1
AT
s_mov_b64 exec, -1
M 7,16,4
WL 4
LO 112,17,112
LU 17,112
M 7,17,5
s_mul_i32 s53, s51, 24
RA 224,1,21504
HZ 29,17
M 8,28,4
s_bitcmp1_b32 s51, 0
LL 16,68
LL 116,69
M 8,29,5
s_addc_u32 s53, s53, 0
LA 16
M 9,40,4
s_lshl_b32 s53, s53, 11
LC_ 116
M 9,41,5
s_bitcmp1_b64 s[92:93], s51
M 10,52,4
WL 3
LO 113,18,113
LU 18,113
M 10,53,5
s_cselect_b64 s[94:95], s[88:89], s[86:87]
RA 228,1,22528
HS 29,18
M 11,64,4
LL 17,70
LL 117,71
M 11,65,5
s_add_u32 s94, s94, s53
LA 17
M 11,66,0
s_addc_u32 s95, s95, 0
LC_ 117
M 11,67,1
s_add_u32 s51, s51, %[d]
M 10,54,0
WL 3
LO 116,16,116
LU 16,116
M 10,55,1
s_cmp_eq_u32 s51, %[wrap]
RA 232,1,23552
HS 29,16
M 9,42,0
s_cselect_b32 s51, %[rst], s51
LL 18,72
LL 120,73
M 9,43,1
s_add_u32 s40, s40, 3072
LA 18
M 8,30,0
)"
R"(s_addc_u32 s41, s41, 0
LC_ 120
s_add_u32 m0, s38, 3072
M 8,31,1
DW 0
M 7,18,0
WL 3
LO 117,17,117
LU 17,117
M 7,19,1
RA 236,1,24576
HS 29,17
M 6,6,0
LL 16,74
LL 121,75
M 6,7,1
LA 16
DW 1024
M 6,8,2
LC_ 121
DW 2048
M 6,9,3
s_lshl_b32 s53, s52, 8
v_add_u32 v1, s37, v0
M 7,20,2
WL 3
LO 120,18,120
LU 18,120
M 7,21,3
s_add_u32 s48, %[alo], s53
RA 216,1,0
HZ 30,18
M 8,32,2
s_addc_u32 s49, %[ahi], 0
LL 17,76
LL 124,77
M 8,33,3
s_add_u32 s52, s52, %[d]
LA 17
v_and_b32 v22, 0xfffffcff, v1
M 9,44,2
s_cmp_eq_u32 s52, %[wrap]
LC_ 124
M 9,45,3
s_cselect_b32 s52, %[rst], s52
DQ 3072
M 10,56,2
WL 3
LO 121,16,121
LU 16,121
M 10,57,3
s_add_u32 s50, s50, %[d]
RA 220,1,1024
HS 30,16
M 11,68,2
LL 18,78
LL 125,79
M 11,69,3
s_cmp_eq_u32 s50, %[wrap]
LA 18
RV 252,22,5120
M 6,10,4
s_cselect_b32 s50, %[rst], s50
LC_ 125
M 6,11,5
M 7,22,4
WL 4
LO 124,17,124
LU 17,124
M 7,23,5
s_add_u32 s38, s38, 32768
RA 224,1,2048
HS 30,17
M 8,34,4
s_cmp_eq_u32 s38, s77
LL 16,80
LL 106,81
M 8,35,5
s_cselect_b32 s38, s76, s38
LA 16
M 9,46,4
s_barrier
LC_ 106
M 9,47,5
s_sub_u32 s39, 0, s39
M 10,58,4
WL 3
LO 125,18,125
LU 18,125
M 10,59,5
RA 228,1,3072
HS 30,18
M 11,70,4
s_cmp_lt_u32 s59, 32
LL 17,82
LL 107,83
M 11,71,5
s_cbranch_scc1 2b
LA 17
LC_ 107
ds_read_b64 v[14:15], v5 offset:2048
v_pk_add_f16 v26, v26, v30
v_pk_add_f16 v26, v26, v29
HX 26
v_subrev_u32 v5, s39, v5
W1 26,2048
WL 0
LO 16,16,106
LU 106,16
HZ 27,106
LL 18,84
LA 18
LL 110,85
PK 106,16,104,56
v_add_f32 v14, v14, v15
s_nop 1
v_add_f32_dpp v14, v14, v14 quad_perm:[1,0,3,2] row_mask:0xf bank_mask:0xf
s_nop 1
v_add_f32_dpp v14, v14, v14 quad_perm:[2,3,0,1] row_mask:0xf bank_mask:0xf
s_nop 1
v_add_f32_dpp v14, v14, v14 row_half_mirror row_mask:0xf bank_mask:0xf
v_cvt_f64_f32 v[14:15], v14
s_mov_b64 exec, s[54:55]
SP
s_mov_b64 exec, -1
LC_ 110
PK 104,16,104,97
LO 17,17,107
LU 107,17
s_add_u32 s42, s94, s60
s_addc_u32 s43, s95, 0
HS 27,107
LL 16,86
LA 16
LL 111,87
LC_ 111
PK 107,17,105,56
PK 105,17,105,97
SU 104
WL 0
LO 18,18,110
LU 110,18
HS 27,110
LL 17,88
LA 17
LL 114,89
LC_ 114
PK 110,18,108,56
PK 108,18,108,97
PO 110
LO 16,16,111
LU 111,16
HS 27,111
s_add_u32 s42, s94, s61
s_addc_u32 s43, s95, 0
LL 18,90
LA 18
LL 115,91
LC_ 115
PK 111,16,109,56
PK 109,16,109,97
PO 111
WL 0
LO 17,17,114
LU 114,17
HZ 29,114
LL 16,92
LA 16
LL 118,93
LC_ 118
PK 114,17,112,56
PK 112,17,112,97
SU 108
LO 18,18,115
LU 115,18
s_add_u32 s42, s94, s62
s_addc_u32 s43, s95, 0
HS 29,115
LL 17,94
LA 17
LL 119,95
LC_ 119
PK 115,18,113,56
PK 113,18,113,97
SU 112
WL 0
LO 16,16,118
LU 118,16
HS 29,118
LL 18,96
LA 18
LL 122,97
LC_ 122
PK 118,16,116,56
PK 116,16,116,97
PO 118
LO 17,17,119
LU 119,17
HS 29,119
s_add_u32 s42, s94, s63
s_addc_u32 s43, s95, 0
LL 16,98
LA 16
LL 123,99
LC_ 123
PK 119,17,117,56
PK 117,17,117,97
PO 119
WL 0
LO 18,18,122
LU 122,18
HZ 30,122
LL 17,100
LA 17
LL 126,101
LC_ 126
PK 122,18,120,56
PK 120,18,120,97
SU 116
LO 16,16,123
LU 123,16
s_add_u32 s42, s94, s64
s_addc_u32 s43, s95, 0
HS 30,123
LL 18,102
LA 18
LL 127,103
LC_ 127
PK 123,16,121,56
PK 121,16,121,97
SU 120
WL 0
LO 17,17,126
LU 126,17
HS 30,126
PK 126,17,124,56
PK 124,17,124,97
PO 126
LO 18,18,127
LU 127,18
HS 30,127
s_add_u32 s42, s94, s65
s_addc_u32 s43, s95, 0
v_pk_add_f16 v27, v27, v30
PK 127,18,125,56
v_pk_add_f16 v27, v27, v29
PK 125,18,125,97
HX 27
PO 127
W1 27,3072
s_nop 1
v_add_u32 v4, s39, v4
SU 124
s_lshl_b32 s53, s52, 8
s_add_u32 s48, %[alo], s53
s_addc_u32 s49, %[ahi], 0
s_add_u32 s52, s52, %[d]
s_cmp_eq_u32 s52, %[wrap]
s_cselect_b32 s52, %[rst], s52
WL 0
WV 0
s_barrier
s_sub_u32 s39, 0, s39
ds_read_b64 v[14:15], v5 offset:2048
s_nop 1
v_subrev_u32 v5, s39, v5
WL 0
v_add_f32 v14, v14, v15
s_nop 1
v_add_f32_dpp v14, v14, v14 quad_perm:[1,0,3,2] row_mask:0xf bank_mask:0xf
s_nop 1
v_add_f32_dpp v14, v14, v14 quad_perm:[2,3,0,1] row_mask:0xf bank_mask:0xf
s_nop 1
v_add_f32_dpp v14, v14, v14 row_half_mirror row_mask:0xf bank_mask:0xf
v_cvt_f64_f32 v[14:15], v14
s_mov_b64 exec, s[54:55]
SP
s_mov_b64 exec, -1
GE
s_waitcnt vmcnt(0) lgkmcnt(0)
8:
s_branch 6f
.Lk1s0:
FR
s_waitcnt lgkmcnt(0)
v_cmp_gt_u32 vcc, v19, v15
s_nop 4
s_cbranch_vccnz .Lk1s0
s_branch .Lk1r0
.Lk1s1:
FR
s_waitcnt lgkmcnt(0)
v_cmp_gt_u32 vcc, v19, v15
s_nop 4
s_cbranch_vccnz .Lk1s1
s_branch .Lk1r1
.Lk1s2:
FR
s_waitcnt lgkmcnt(0)
v_cmp_gt_u32 vcc, v19, v15
s_nop 4
s_cbranch_vccnz .Lk1s2
s_branch .Lk1r2
.Lk1s3:
FR
s_waitcnt lgkmcnt(0)
v_cmp_gt_u32 vcc, v19, v15
s_nop 4
s_cbranch_vccnz .Lk1s3
s_branch .Lk1r3
.Lk1s4:
FR
s_waitcnt lgkmcnt(0)
v_cmp_gt_u32 vcc, v19, v15
s_nop 4
s_cbranch_vccnz .Lk1s4
s_branch .Lk1r4
.Lk1s5:
FR
s_waitcnt lgkmcnt(0)
v_cmp_gt_u32 vcc, v19, v15
s_nop 4
s_cbranch_vccnz .Lk1s5
s_branch .Lk1r5
.Lk1s6:
FR
s_waitcnt lgkmcnt(0)
v_cmp_gt_u32 vcc, v19, v15
s_nop 4
s_cbranch_vccnz .Lk1s6
s_branch .Lk1r6
.Lk1s7:
FR
s_waitcnt lgkmcnt(0)
v_cmp_gt_u32 vcc, v19, v15
s_nop 4
s_cbranch_vccnz .Lk1s7
s_branch .Lk1r7
.Lk1s8:
FR
s_waitcnt lgkmcnt(0)
v_cmp_gt_u32 vcc, v19, v15
s_nop 4
s_cbranch_vccnz .Lk1s8
s_branch .Lk1r8
.Lk1s9:
FR
s_waitcnt lgkmcnt(0)
v_cmp_gt_u32 vcc, v19, v15
s_nop 4
s_cbranch_vccnz .Lk1s9
s_branch .Lk1r9
.Lk1s10:
FR
s_waitcnt lgkmcnt(0)
v_cmp_gt_u32 vcc, v19, v15
s_nop 4
s_cbranch_vccnz .Lk1s10
s_branch .Lk1r10
.Lk1s11:
FR
s_waitcnt lgkmcnt(0)
v_cmp_gt_u32 vcc, v19, v15
s_nop 4
s_cbranch_vccnz .Lk1s11
s_branch .Lk1r11
.Lk1s12:
FR
s_waitcnt lgkmcnt(0)
v_cmp_gt_u32 vcc, v19, v15
s_nop 4
s_cbranch_vccnz .Lk1s12
s_branch .Lk1r12
.Lk1s13:
FR
s_waitcnt lgkmcnt(0)
v_cmp_gt_u32 vcc, v19, v15
s_nop 4
s_cbranch_vccnz .Lk1s13
s_branch .Lk1r13
.Lk1s14:
FR
s_waitcnt lgkmcnt(0)
v_cmp_gt_u32 vcc, v19, v15
s_nop 4
s_cbranch_vccnz .Lk1s14
s_branch .Lk1r14
.Lk1s15:
FR
s_waitcnt lgkmcnt(0)
v_cmp_gt_u32 vcc, v19, v15
s_nop 4
s_cbranch_vccnz .Lk1s15
s_branch .Lk1r15
.Lk1s16:
FR
s_waitcnt lgkmcnt(0)
v_cmp_gt_u32 vcc, v19, v15
s_nop 4
s_cbranch_vccnz .Lk1s16
)"
R"(s_branch .Lk1r16
.Lk1s17:
FR
s_waitcnt lgkmcnt(0)
v_cmp_gt_u32 vcc, v19, v15
s_nop 4
s_cbranch_vccnz .Lk1s17
s_branch .Lk1r17
.Lk1s18:
FR
s_waitcnt lgkmcnt(0)
v_cmp_gt_u32 vcc, v19, v15
s_nop 4
s_cbranch_vccnz .Lk1s18
s_branch .Lk1r18
.Lk1s19:
FR
s_waitcnt lgkmcnt(0)
v_cmp_gt_u32 vcc, v19, v15
s_nop 4
s_cbranch_vccnz .Lk1s19
s_branch .Lk1r19
.Lk1s20:
FR
s_waitcnt lgkmcnt(0)
v_cmp_gt_u32 vcc, v19, v15
s_nop 4
s_cbranch_vccnz .Lk1s20
s_branch .Lk1r20
.Lk1s21:
FR
s_waitcnt lgkmcnt(0)
v_cmp_gt_u32 vcc, v19, v15
s_nop 4
s_cbranch_vccnz .Lk1s21
s_branch .Lk1r21
6:
.purgem M
.purgem Z
.purgem LZ
.purgem LS
.purgem GZ
.purgem GS
.purgem XL
.purgem C
.purgem LL
.purgem LA
.purgem LC_
.purgem LO
.purgem HZ
.purgem HS
.purgem HX
.purgem AW
.purgem RA
.purgem RV
.purgem W2
.purgem W1
.purgem DW
.purgem DF
.purgem ST
.purgem SU
.purgem SP
.purgem AT
.purgem PFA
.purgem PFN
.purgem PFW
.purgem PF0
.purgem WL
.purgem WV
.purgem LU
.purgem CF
.purgem XS
.purgem AR
.purgem DQ
.purgem GE
.purgem BW
.purgem BR
.purgem LK
.purgem PK
.purgem PO
.purgem LG
.purgem DY
.purgem FP
.purgem FR
)"
      : [nb] "=&s"(nb)
      : [xlo] "s"((unsigned)xp), [xhi] "s"((unsigned)(xp >> 32)), [hlo] "s"((unsigned)hp), [hhi] "s"((unsigned)(hp >> 32)),
        [glo] "s"((unsigned)gp), [ghi] "s"((unsigned)(gp >> 32)), [wlo] "s"((unsigned)wp), [whi] "s"((unsigned)(wp >> 32)),
        [w] "s"(w), [lds] "s"(lds0), [d] "s"(d), [wrap] "s"(wrap), [rst] "s"(rst), [pos0] "s"(pos0), [xo0] "s"(xo0), [xs1] "s"(xs1), [sb1] "s"(sb1), [r0o] "s"(r0o), [tsel] "s"(tsel), [s2] "s"(s2),
        [alo] "s"((unsigned)ap), [ahi] "s"((unsigned)(ap >> 32)), [ca0] "s"(ca0), [pm] "s"(pm), [pf] "s"(pf)
      : K1_CLOB);
}

typedef __bf16 bf16x8_t __attribute__((ext_vector_type(8)));
DI f32x4 mfma16(u32x4 a, u32x4 b, f32x4 c) {
  return __builtin_amdgcn_mfma_f32_16x16x32_bf16(__builtin_bit_cast(bf16x8_t, a), __builtin_bit_cast(bf16x8_t, b), c, 0, 0, 0);
}
DI float grn_gx(double s, float scale) { return round_bf16(__builtin_amdgcn_sqrtf(round_bf16(round_bf16((float)s * scale) + 1e-6f))); }
DI void slow_fc1(const u16* __restrict__ x, const u16* __restrict__ dw, char* __restrict__ hws, float* sacc, int w) {
  const int lane = __builtin_amdgcn_mbcnt_hi(~0u, __builtin_amdgcn_mbcnt_lo(~0u, 0u)), m = lane & 15, g = lane >> 4;
  for (int i = lane; i < HID; i += 64) sacc[i] = 0.f;
#pragma unroll 1
  for (int it = 0; it < 9; ++it) {
    const int sub = blockIdx.x * 36 + it * 4 + w;
    const unsigned px = sub_pixel(x, sub, m);
    u32x4 A[12];
    float sum = 0.f;
    const u16* dq = dw;
    asm volatile("" : "+s"(dq));
#pragma unroll
    for (int ks = 0; ks < 12; ++ks) {
      const u32x4 v = *(const u32x4*)(x + (size_t)px * CH + 32 * ks + 8 * g);
      const u32x4 d = *(const u32x4*)(dq + 32 * ks + 8 * g);
#pragma unroll
      for (int j = 0; j < 4; ++j) {
        A[ks][j] = pack_bf16(bf_lo(v[j]) * bf_lo(d[j]), bf_hi(v[j]) * bf_hi(d[j]));
        sum += bf_lo(A[ks][j]) + bf_hi(A[ks][j]);
      }
      asm volatile("" : "+v"(A[ks]));
    }
    sum += __shfl_xor(sum, 16); sum += __shfl_xor(sum, 32);
    const float mean = sum * (1.0f / CH);
    float var = 0.f;
#pragma unroll
    for (int ks = 0; ks < 12; ++ks) {
#pragma unroll
      for (int j = 0; j < 4; ++j) {
        const float a = bf_lo(A[ks][j]) - mean, b = bf_hi(A[ks][j]) - mean;
        var += a * a + b * b;
      }
      asm volatile("" : "+v"(A[ks]));
    }
    var += __shfl_xor(var, 16); var += __shfl_xor(var, 32);
    const float mb = round_bf16(mean);
    const float rd = 1.0f / round_bf16(sqrtf(round_bf16(round_bf16(var * (1.0f / CH)) + 1e-5f)));
#pragma unroll
    for (int ks = 0; ks < 12; ++ks)
#pragma unroll
      for (int j = 0; j < 4; ++j) A[ks][j] = pack_bf16((bf_lo(A[ks][j]) - mb) * rd, (bf_hi(A[ks][j]) - mb) * rd);
#pragma unroll 1
    for (int tc = 0; tc < 96; ++tc) {
      f32x4 acc = {0.f, 0.f, 0.f, 0.f};
#pragma unroll
      for (int ks = 0; ks < 12; ++ks) acc = mfma16(A[ks], *(const u32x4*)(g_buf + (size_t)(tc * 12 + ks) * 1024 + lane * 16), acc);
      float q = 0.f;
#pragma unroll
      for (int r = 0; r < 4; ++r) { acc[r] = gelu_tanh(acc[r]); q = __builtin_fmaf(acc[r], acc[r], q); }
      typedef unsigned u32x2_ __attribute__((ext_vector_type(2)));
      *(u32x2_*)(hws + h_piece(hws, sub, tc >> 1) + g * 256 + ((m + 4 * g) & 15) * 16 + (tc & 1) * 8) = (u32x2_){pack_bf16(acc[0], acc[1]), pack_bf16(acc[2], acc[3])};
      q += __shfl_xor(q, 16); q += __shfl_xor(q, 32);
      if (g == 0) sacc[tc * 16 + m] += q;
    }
  }
  __builtin_amdgcn_wave_barrier();
  __builtin_amdgcn_fence(__ATOMIC_ACQ_REL, "workgroup");
  for (int i = lane; i < HID; i += 64) atomicAdd((double*)(g_buf + G_ACC2) + i, (double)sacc[i]);
}
DI void slow_bar(unsigned* c, int w) {
  const int lane = __builtin_amdgcn_mbcnt_hi(~0u, __builtin_amdgcn_mbcnt_lo(~0u, 0u));
  asm volatile("s_waitcnt vmcnt(0)" ::: "memory");
  __syncthreads();
  if (w == 0 && lane == 0) {
    __threadfence();
    asm volatile("s_waitcnt vmcnt(0)" ::: "memory");
    __hip_atomic_fetch_add(c, 1u, __ATOMIC_RELAXED, __HIP_MEMORY_SCOPE_AGENT);
    for (int i = 0; i < (1 << 22) && __hip_atomic_load(c, __ATOMIC_RELAXED, __HIP_MEMORY_SCOPE_AGENT) < (unsigned)NWG; ++i) __builtin_amdgcn_s_sleep(32);
    __threadfence();
    asm volatile("s_waitcnt vmcnt(0)" ::: "memory");
  }
  __syncthreads();
}

#define K2_CLOB "memory", "vcc", "scc", "s48", "s49", "s50", "s51", "s52", "s53", "s54", "s55", "s56", "s57", "s58", "s59", "s60", "s61", "s62", "s63", "s64", "s65", "s66", "s67", \
  "s68", "s69", "s70", "s71", "s72", "s73", "s74", "s75", "s76", "s77", "s78", "s79", "s80", "s81", "s82", "s83", "s84", "s85", "s86", "s87", "s88", "s89", "s90", "s91", "s92", "s93", "s94", "s95", "s96", "s97", "s98", "s99", "s38", "s39", "s40", "s41", "s42", "s43", "s44", "s45", "s46", "s47", VA_CLOB
DI void slow_fc2(const u16* __restrict__ x, const char* __restrict__ hws, u16* __restrict__ out, const u16* __restrict__ grn_g, const u16* __restrict__ w2,
                 char* smem, int w) {
  float* sc = (float*)smem;
  float* red = sc + HID;
  u16* wf = (u16*)(smem + 8192);
  const int lane = __builtin_amdgcn_mbcnt_hi(~0u, __builtin_amdgcn_mbcnt_lo(~0u, 0u)), tid = w * 64 + lane, m = lane & 15, g = lane >> 4;
  __syncthreads();
  float part = 0.f;
  for (int c = tid; c < HID; c += 256) {
    const float gx = grn_gx(((const double*)(g_buf + G_ACC2))[c], 1.0f);
    sc[c] = gx; part += gx;
  }
#pragma unroll
  for (int o = 32; o > 0; o >>= 1) part += __shfl_xor(part, o);
  if (lane == 0) red[tid >> 6] = part;
  __syncthreads();
  const float gmean = round_bf16(round_bf16(((red[0] + red[1]) + (red[2] + red[3])) * (1.0f / HID)) + 1e-6f);
  for (int c = tid; c < HID; c += 256) sc[c] = (1.0f + ld_bf16(grn_g + c) * round_bf16(sc[c] / gmean)) ;
#pragma unroll 1
  for (int it = 0; it < 9; ++it) {
    const int sub = blockIdx.x * 36 + it * 4 + (tid >> 6);
    f32x4 acc[24];
#pragma unroll
    for (int i = 0; i < 24; ++i) acc[i] = (f32x4){0.f, 0.f, 0.f, 0.f};
#pragma unroll 1
    for (int t = 0; t < 48; ++t) {
      __syncthreads();
      for (int e = tid; e < 32 * CH; e += 256) {
        const int k = e / CH, n = e % CH;
        wf[((n >> 4) * 64 + (k >> 3) * 16 + (n & 15)) * 8 + (k & 7)] = (u16)pack_bf16(sc[32 * t + k] * ld_bf16(w2 + (size_t)(32 * t + k) * CH + n), 0.f);
      }
      __syncthreads();
      const char* hp = hws + h_piece(hws, sub, t) + (m >> 2) * 256 + (g >> 1) * 8 + 2 * (m & 3);
      u32x4 A;
#pragma unroll
      for (int j = 0; j < 4; ++j) {
        unsigned v[2];
#pragma unroll
        for (int k = 0; k < 2; ++k) {
          v[k] = *(const u16*)(hp + ((8 * (g & 1) + 2 * j + k + (m & 12)) & 15) * 16);
        }
        A[j] = v[0] | (v[1] << 16);
      }
#pragma unroll
      for (int i = 0; i < 24; ++i) acc[i] = mfma16(A, *(const u32x4*)(wf + (i * 64 + lane) * 8), acc[i]);
    }
#pragma unroll
    for (int i = 0; i < 24; ++i) {
      const int n = 16 * i + m;
      const float* bp = g_cs + 4 * 384 + cs_index(n);
      const float bias = (bp[0] + bp[384]) + (bp[768] + bp[1152]);
#pragma unroll
      for (int r = 0; r < 4; ++r) {
        const size_t o = (size_t)(sub_pixel(x, sub, 4 * g) + r) * CH + n;
        out[o] = (u16)pack_bf16(acc[i][r] + bias + ld_bf16(x + o), 0.f);
      }
    }
  }
}
__global__ __launch_bounds__(256, 1) void k2_fc2(const u16* __restrict__ x, char* __restrict__ hws, u16* __restrict__ out,
                                                 const u16* __restrict__ grn_g, const u16* __restrict__ w2, const u16* __restrict__ dw,
                                                 const u16* __restrict__ w1) {
  extern __shared__ __attribute__((aligned(16))) char smem[];
  const int w = __builtin_amdgcn_readfirstlane(threadIdx.x >> 6);
  const unsigned pg = w & 1, nh = w >> 1;
  const unsigned lds0 = lds_addr(smem) + 1024;
  const uint64_t pp = uni64((uint64_t)g_prm + (blockIdx.x * 4 + w) * 256), ap = (uint64_t)g_buf + K1_G_ACC + w * 1024;
  const unsigned wm = lds0 + w * 6144, ab = lds0 + nh * 12288;
  const unsigned hb = lds0 + K2_HB + pg * 2 * K2_RSZ, hm = hb + nh * K2_RSZ;
  const unsigned bm = w * 384, bb = lds0 + K2_E_OFF + nh * 768;
  const unsigned pm = lds0 + K2_PF_OFF, sb = lds0 + K2_S16_OFF, sw = sb + w * 256;
  uint64_t flg;
#define K2_OPS [pp] "s"(pp), [alo] "s"((unsigned)ap), [ahi] "s"((unsigned)(ap >> 32)), [wm] "s"(wm), [hm] "s"(hm), [bm] "s"(bm), [ab] "s"(ab), [hb] "s"(hb), [bb] "s"(bb), \
         [sb] "s"(sb), [sw] "s"(sw), [pm] "s"(pm)
  asm volatile(
R"(.macro MA t,a,b
v_mfma_f32_16x16x32_f16 a[\t:\t+3], v[\b:\b+3], a[\a:\a+3], a[\t:\t+3]
.endm
.macro MV t,a,b
v_mfma_f32_16x16x32_f16 v[\t:\t+3], v[\b:\b+3], a[\a:\a+3], v[\t:\t+3]
.endm
.macro NA t,a,b
v_mfma_f32_16x16x32_f16 a[\t:\t+3], a[\b:\b+3], a[\a:\a+3], a[\t:\t+3]
.endm
.macro NV t,a,b
v_mfma_f32_16x16x32_f16 v[\t:\t+3], a[\b:\b+3], a[\a:\a+3], v[\t:\t+3]
.endm
.macro AN d,s
v_and_b32 v[\d], s93, v[\s]
.endm
.macro SH d
v_lshrrev_b64 v[\d:\d+1], 4, v[\d:\d+1]
.endm
.macro PA_ d,h,r
v_perm_b32 v[\d], v[\h], v[\r], s54
.endm
.macro PB_ d,h,r
v_perm_b32 v[\d], v[\h], v[\r], s55
.endm
.macro AW a,s
v_accvgpr_write_b32 a[\a], v[\s]
.endm
.macro AR d,a
v_accvgpr_read_b32 v[\d], a[\a]
.endm
.macro UL d,x
v_lshlrev_b32 v[\d], 16, v[\x]
.endm
.macro UH d,x
v_and_b32 v[\d], s38, v[\x]
.endm
.macro AD d,b
v_fmac_f32 v[\d], s53, v[\b]
.endm
.macro EF d,e
v_fma_f32 v[\d], v[\d], s39, v[\e]
.endm
.macro TY d,o
ds_read_b64_tr_b16 v[\d:\d+1], v2 offset:\o
.endm
.macro TX d,o
ds_read_b64_tr_b16 v[\d:\d+1], v51 offset:\o
.endm
.macro RA d,o
ds_read_b128 a[\d:\d+3], v1 offset:\o
.endm
.macro RC d,o
ds_read_b128 a[\d:\d+3], v5 offset:\o
.endm
.macro BV d,o
ds_read_b128 v[\d:\d+3], v4 offset:\o
.endm
.macro PA a
s_mov_b32 m0, %[pm]
s_getpc_b64 s[46:47]
s_andn2_b32 s46, s46, 0x7f
s_add_u32 s46, s46, s81
s_addc_u32 s47, s47, 0
s_add_u32 s46, s46, \a
s_addc_u32 s47, s47, 0
.endm
.macro DF
global_load_lds_dwordx4 v0, s[46:47]
.endm
.macro DW o
global_load_lds_dwordx4 v0, s[40:41] offset:\o sc1
.endm
.macro DR o
global_load_lds_dwordx4 v0, s[40:41] offset:\o
.endm
.macro DH o
global_load_lds_dwordx4 v0, s[40:41] offset:\o nt
.endm
.macro DG o
s_mov_b32 exec_hi, 0
global_load_lds_dwordx4 v0, s[40:41] offset:\o nt
s_mov_b32 exec_hi, -1
.endm
.macro XL d,o
global_load_dwordx2 v[\d:\d+1], v47, s[44:45] offset:\o
.endm
.macro ST d,o
global_store_dwordx2 v50, v[\d:\d+1], s[42:43] offset:\o nt sc1
.endm
.macro SA d,o
global_store_dwordx2 v50, a[\d:\d+1], s[42:43] offset:\o nt sc1
.endm
.macro CV d,a,b
v_cvt_pk_bf16_f32 v[\d], v[\a], v[\b]
.endm
.macro RS d,o
ds_read_b128 v[\d:\d+3], v46 offset:\o
.endm
.macro PM d,s
v_pk_mul_f16 v[\d], v[\d], v[\s]
.endm
.macro PF d,n
v_pk_fma_f16 v[\d], v[\d], v[14+\n], v[22+\n]
.endm
.macro FS
global_store_dword v0, v54, s[96:97] sc1
.endm
.macro FL
global_load_dword v54, v55, s[98:99] sc1
.endm
.macro FB
s_cbranch_vccnz 21b
22:
.endm
.macro FC
s_cbranch_vccnz 26b
27:
.endm
.macro FG
s_or_b64 s[54:55], s[44:45], vcc
s_cbranch_scc1 23b
24:
.endm
.macro FX
s_branch 25f
21:
s_sleep 4
global_load_dword v54, v55, s[98:99] sc1
s_waitcnt vmcnt(0)
v_cmp_eq_u32 vcc, 0, v54
s_sub_u32 s80, s80, 1
s_cbranch_scc1 22f
s_nop 2
s_cbranch_vccnz 21b
s_branch 22f
26:
s_sleep 4
global_load_dword v54, v55, s[98:99] sc1
s_waitcnt vmcnt(0)
v_cmp_ne_u32 vcc, 2, v54
s_sub_u32 s80, s80, 1
s_cbranch_scc1 27f
s_nop 2
s_cbranch_vccnz 26b
s_branch 27f
23:
v_mov_b32 v10, 1
s_mov_b64 exec, 1
global_store_dword v0, v10, s[78:79] offset:384 sc1
s_mov_b64 exec, -1
s_nop 4
s_branch 24f
25:
.endm
.macro FP
ds_inc_u32 v0, v128
.endm
.macro FR
ds_read_b32 v10, v0
.endm
.macro FQ n
v_cmp_gt_u32 vcc, \n, v10
.endm
.macro FZ n
s_cbranch_vccnz .Lk2s\n
.Lk2r\n:
.endm
.macro FY n
.Lk2s\n:
FR
s_waitcnt lgkmcnt(0)
FQ \n
s_nop 4
s_cbranch_vccnz .Lk2s\n
s_branch .Lk2r\n
.endm
.macro WL n
s_waitcnt lgkmcnt(\n)
.endm
.macro WV n
s_waitcnt vmcnt(\n)
.endm
.macro M n
.if \n < 82
v_mfma_f32_16x16x32_f16 a[36+4*(\n/2):36+4*(\n/2)+3], v[56+8*((\n/2)%%6)+4*(\n&1):56+8*((\n/2)%%6)+4*(\n&1)+3], a[8*((\n/12)%%4)+4*(\n&1):8*((\n/12)%%4)+4*(\n&1)+3], a[36+4*(\n/2):36+4*(\n/2)+3]
.else
v_mfma_f32_16x16x32_f16 v[-32+4*(\n/2):-32+4*(\n/2)+3], v[56+8*((\n/2)%%6)+4*(\n&1):56+8*((\n/2)%%6)+4*(\n&1)+3], a[8*((\n/12)%%4)+4*(\n&1):8*((\n/12)%%4)+4*(\n&1)+3], v[-32+4*(\n/2):-32+4*(\n/2)+3]
.endif
.endm
.macro R1 n
.if \n < 64
RA 8*(((\n%%24)/2+2*((\n/24)&1))%%4)+4*(\n&1),1024*\n
.else
RC 8*(((\n%%24)/2+2*((\n/24)&1))%%4)+4*(\n&1),1024*\n-49152
.endif
.endm
.macro R n
R1 \n
R1 (\n+1)
.endm
.macro P n
.if \n & 2
PB_ 56+\n,104+8*(\n/16)+(\n&4)+2*((\n/8)&1)+(\n&1),56+(\n|2)
.else
PA_ 56+\n,104+8*(\n/16)+(\n&4)+2*((\n/8)&1)+(\n&1),56+(\n|2)
.endif
.endm
.macro Q n
P \n
P (\n+1)
.endm
.macro mw k
s_add_u32 m0, %[wm], 1024*\k
.endm
.macro mh k
s_add_u32 m0, %[hm], 512*\k
.endm
.macro ad r,k
s_add_u32 s40, s[\r], 512*\k
s_addc_u32 s41, s[\r+1], 0
.endm
.macro ae d,r,k
s_add_u32 s[\d], s[\r], 512*\k
s_addc_u32 s[\d+1], s[\r+1], 0
.endm
.macro bc m,b,d,r
s_bitcmp1_b32 s[\m], \b
s_cselect_b64 s[\d:\d+1], s[\r+2:\r+3], s[\r:\r+1]
.endm
.macro W k
DW 1024*\k
.endm
.macro H k
DH 1024*\k
.endm
k2_code:
s_load_dwordx16 s[48:63], %[pp], 0x0
s_load_dwordx16 s[64:79], %[pp], 0x40
s_load_dwordx16 s[84:99], %[pp], 0x80
s_load_dword s42, %[pp], 0xc0
v_mbcnt_lo_u32_b32 v7, -1, 0
v_mbcnt_hi_u32_b32 v7, -1, v7
v_lshlrev_b32 v0, 4, v7
v_add_u32 v88, 0x1000, v0
v_add_u32 v89, 0x2000, v0
s_mov_b32 s44, %[alo]
s_mov_b32 s45, %[ahi]
global_load_dwordx4 v[132:135], v0, s[44:45]
global_load_dwordx4 v[144:147], v88, s[44:45]
global_load_dwordx4 v[148:151], v89, s[44:45]
ae 44,44,24
global_load_dwordx4 v[168:171], v0, s[44:45]
global_load_dwordx4 v[172:175], v88, s[44:45]
global_load_dwordx4 v[192:195], v89, s[44:45]
s_waitcnt lgkmcnt(0)
s_mov_b64 s[82:83], s[66:67]
bc 49,0,66,60
mh 0
ad 66,0
H 0
DG 1024
ae 44,44,24
global_load_dwordx4 v[196:199], v0, s[44:45]
global_load_dwordx4 v[216:219], v88, s[44:45]
global_load_dwordx4 v[220:223], v89, s[44:45]
ae 44,44,24
global_load_dwordx4 v[224:227], v0, s[44:45]
global_load_dwordx4 v[228:231], v88, s[44:45]
global_load_dwordx4 v[240:243], v89, s[44:45]
v_and_b32 v8, 15, v7
v_lshrrev_b32 v9, 4, v7
v_lshlrev_b32 v10, 2, v7
v_add_u32 v202, s65, v0
global_load_dword v244, v10, s[92:93] offset:0
global_load_dword v245, v10, s[92:93] offset:1024
global_load_dword v246, v10, s[92:93] offset:2048
global_load_dwordx4 v[152:155], v0, s[88:89]
s_mov_b64 exec, 0xff
global_load_dwordx4 v[156:159], v202, s[88:89]
s_mov_b64 exec, -1
s_branch 28f
FY 4
FY 8
FY 12
FY 16
FY 20
FY 24
FY 28
FY 32
FY 36
FY 40
FY 44
FY 48
FY 52
FY 56
FY 60
FY 64
FY 68
FY 72
FY 76
FY 80
FY 84
FY 88
FY 92
FY 96
FY 100
FY 104
FY 108
FY 112
FY 116
FY 120
FY 124
FY 128
28:
FX
s_mov_b32 s92, s64
s_mov_b32 s93, 0x0f0f0f0f
v_add_u32 v1, %[ab], v0
v_add_u32 v5, 0xc000, v1
v_and_b32 v10, 3, v7
v_lshrrev_b32 v11, 2, v7
v_lshl_add_u32 v11, v10, 2, v11
v_and_b32 v11, 15, v11
mw 0
ad 86,0
DR 0
v_lshl_add_u32 v11, v10, 4, v11
v_lshl_add_u32 v2, v11, 4, %[hb]
v_lshl_add_u32 v51, v11, 3, %[hb]
v_lshl_add_u32 v4, v8, 4, %[bb]
s_mov_b32 s38, 0xffff0000
s_mov_b32 s80, 0x40000
s_mov_b32 s81, s52
s_mov_b32 s39, s54
v_lshl_add_u32 v46, v9, 4, %[sb]
v_add_u32 v46, 8192, v46
v_bfe_u32 v10, v7, 1, 2
DR 1024
v_bfe_u32 v11, v7, 3, 1
v_and_b32 v12, 1, v7
v_lshl_add_u32 v11, v11, 1, v12
v_lshl_add_u32 v10, v10, 2, v11
v_lshl_add_u32 v10, v9, 4, v10
v_lshl_add_u32 v90, v10, 2, %[sw]
DR 2048
v_lshl_add_u32 v91, v7, 2, %[sw]
s_and_b32 s42, s51, 0xffff
s_lshr_b32 s43, s51, 16
s_add_u32 s42, s42, %[sb]
s_add_u32 s43, s43, %[sb]
v_lshl_add_u32 v200, v9, 4, s42
v_mov_b32 v201, s43
v_mov_b32 v128, 0x362aaa8f
v_mov_b32 v129, 0x362aaa8f
v_mov_b32 v130, 0x362aaa8f
v_mov_b32 v131, 0x362aaa8f
mw 3
ad 86,6
DR 0
AW 32,128
AW 33,129
AW 34,130
AW 35,131
v_mov_b32 v95, 0
ds_write_b32 v0, v95
s_nop 1
v_mov_b32 v95, 0x7f800000
s_mov_b32 s44, 0x44100000
WV 19
v_add_f64 v[132:133], v[132:133], v[168:169]
WV 18
v_add_f64 v[144:145], v[144:145], v[172:173]
DR 1024
WV 18
v_add_f64 v[148:149], v[148:149], v[192:193]
v_add_f64 v[134:135], v[134:135], v[170:171]
v_add_f64 v[146:147], v[146:147], v[174:175]
v_add_f64 v[150:151], v[150:151], v[194:195]
WV 15
v_add_f64 v[132:133], v[132:133], v[196:197]
WV 14
v_add_f64 v[144:145], v[144:145], v[216:217]
DR 2048
WV 14
v_add_f64 v[148:149], v[148:149], v[220:221]
v_add_f64 v[134:135], v[134:135], v[198:199]
v_add_f64 v[146:147], v[146:147], v[218:219]
v_add_f64 v[150:151], v[150:151], v[222:223]
WV 13
v_add_f64 v[132:133], v[132:133], v[224:225]
WV 12
v_add_f64 v[144:145], v[144:145], v[228:229]
bc 49,1,66,60
mh 9
ad 66,192
H 0
WV 12
)"
R"(v_add_f64 v[148:149], v[148:149], v[240:241]
v_add_f64 v[134:135], v[134:135], v[226:227]
v_add_f64 v[146:147], v[146:147], v[230:231]
v_add_f64 v[150:151], v[150:151], v[242:243]
v_cvt_f32_f64 v248, v[132:133]
v_cvt_f32_f64 v252, v[144:145]
DG 1024
v_cvt_f32_f64 v72, v[148:149]
v_cvt_f32_f64 v249, v[134:135]
v_cvt_f32_f64 v253, v[146:147]
v_cvt_f32_f64 v73, v[150:151]
v_mul_f32 v250, s55, v248
v_mul_f32 v254, s55, v252
bc 48,1,64,56
mh 12
ad 64,192
H 0
v_mul_f32 v74, s55, v72
v_mul_f32 v251, s55, v249
v_mul_f32 v255, s55, v253
v_mul_f32 v75, s55, v73
CV 251,250,251
CV 255,254,255
DG 1024
CV 75,74,75
UL 250,251
UL 254,255
UL 74,75
UH 251,251
UH 255,255
mw 24
ad 86,48
DR 0
UH 75,75
v_add_f32 v250, 0x358637bd, v250
v_add_f32 v254, 0x358637bd, v254
v_add_f32 v74, 0x358637bd, v74
v_add_f32 v251, 0x358637bd, v251
v_add_f32 v255, 0x358637bd, v255
DR 1024
v_add_f32 v75, 0x358637bd, v75
CV 251,250,251
CV 255,254,255
CV 75,74,75
UL 250,251
UL 254,255
DR 2048
UL 74,75
UH 251,251
UH 255,255
UH 75,75
v_sqrt_f32 v250, v250
v_sqrt_f32 v254, v254
mw 27
ad 86,54
DR 0
v_sqrt_f32 v74, v74
v_sqrt_f32 v251, v251
v_sqrt_f32 v255, v255
v_sqrt_f32 v75, v75
CV 251,250,251
CV 255,254,255
DR 1024
CV 75,74,75
UL 250,251
UL 254,255
UL 74,75
UH 251,251
UH 255,255
DR 2048
UH 75,75
v_cmp_le_f32 vcc, s44, v248
v_cmp_le_f32 s[54:55], s44, v249
s_nop 0
v_cndmask_b32 v248, v95, v250, vcc
v_cndmask_b32 v249, v95, v251, s[54:55]
v_add_f32 v248, v248, v249
v_cmp_le_f32 vcc, s44, v252
v_cmp_le_f32 s[54:55], s44, v253
s_nop 0
v_cndmask_b32 v252, v95, v254, vcc
v_cndmask_b32 v253, v95, v255, s[54:55]
v_add_f32 v252, v252, v253
v_cmp_le_f32 vcc, s44, v72
v_cmp_le_f32 s[54:55], s44, v73
s_nop 0
v_cndmask_b32 v72, v95, v74, vcc
v_cndmask_b32 v73, v95, v75, s[54:55]
bc 49,2,66,60
mh 18
ad 66,384
H 0
v_add_f32 v72, v72, v73
v_add_f32 v92, v248, v252
v_add_f32 v92, v92, v72
s_nop 1
v_add_f32_dpp v92, v92, v92 row_shr:1 row_mask:0xf bank_mask:0xf
s_nop 1
v_add_f32_dpp v92, v92, v92 row_shr:2 row_mask:0xf bank_mask:0xf
s_nop 1
v_add_f32_dpp v92, v92, v92 row_shr:4 row_mask:0xf bank_mask:0xf
s_nop 1
v_add_f32_dpp v92, v92, v92 row_shr:8 row_mask:0xf bank_mask:0xf
s_nop 1
v_add_f32_dpp v92, v92, v92 row_bcast:15 row_mask:0xa bank_mask:0xf
s_nop 1
v_add_f32_dpp v92, v92, v92 row_bcast:31 row_mask:0xc bank_mask:0xf
ds_write_b32 v91, v92 offset:7168
v_mov_b32 v10, %[sb]
WL 0
s_barrier
ds_read_b32 v96, v10 offset:7420
ds_read_b32 v97, v10 offset:7676
ds_read_b32 v98, v10 offset:7932
ds_read_b32 v99, v10 offset:8188
WV 21
UL 76,244
WV 20
UL 80,245
WV 19
UL 84,246
UH 77,244
DG 1024
UH 81,245
UH 85,246
v_mov_b32 v93, 0
WL 0
v_add_f32 v96, v96, v97
v_add_f32 v98, v98, v99
v_add_f32 v96, v96, v98
bc 48,2,64,56
mh 21
ad 64,384
H 0
v_mul_f32 v94, 0x3a2aaaab, v96
v_cmp_nlt_f32 s[44:45], v96, v95
CV 94,94,94
UL 94,94
v_add_f32 v94, 0x358637bd, v94
CV 94,94,94
UL 94,94
H 1
v_rcp_f32 v94, v94
s_nop 0
v_mul_f32 v250, v94, v250
v_mul_f32 v254, v94, v254
v_mul_f32 v74, v94, v74
v_mul_f32 v251, v94, v251
v_mul_f32 v255, v94, v255
v_mul_f32 v75, v94, v75
H 2
CV 251,250,251
CV 255,254,255
CV 75,74,75
UL 250,251
UL 254,255
UL 74,75
mw 48
ad 86,96
DR 0
UH 251,251
UH 255,255
UH 75,75
v_fma_f32 v250, v76, v250, 1.0
v_fma_f32 v254, v80, v254, 1.0
v_fma_f32 v74, v84, v74, 1.0
DR 1024
v_fma_f32 v251, v77, v251, 1.0
v_fma_f32 v255, v81, v255, 1.0
v_fma_f32 v75, v85, v75, 1.0
v_cvt_pk_f16_f32 v76, v250, v251
v_cvt_pk_f16_f32 v80, v254, v255
v_cvt_pk_f16_f32 v84, v74, v75
ds_write_b32 v90, v76 offset:8192
ds_write_b32 v90, v80 offset:1024
ds_write_b32 v90, v84 offset:2048
v_max3_f32 v93, |v250|, |v251|, v93
DR 2048
v_max3_f32 v93, |v254|, |v255|, v93
v_max3_f32 v93, |v74|, |v75|, v93
v_cmp_lt_f32 vcc, 0x43000000, v93
WL 0
WV 16
s_barrier
TY 106,520
TY 110,14344
TX 58,0
TX 62,13824
TY 66,512
TY 70,14336
RS 14,0
RS 18,64
ds_read_b128 v[176:179], v200
ds_read_b128 v[180:183], v201
R 0
R 2
FG
s_mov_b32 s54, 0x06020400
s_mov_b32 s55, 0x07030501
WL 13
AN 104,106
AN 105,107
SH 106
AN 106,106
mw 51
ad 86,102
DR 0
AN 107,107
WL 12
AN 108,110
AN 109,111
SH 110
AN 110,110
AN 111,111
DR 1024
WL 10
Q 0
Q 2
Q 4
DR 2048
Q 6
R 4
R 6
WL 9
v_pk_mul_f16 v22, v14, s92
v_pk_mul_f16 v23, v15, s92
v_pk_mul_f16 v24, v16, s92
v_pk_mul_f16 v25, v17, s92
v_pk_mul_f16 v26, v18, s92
v_pk_mul_f16 v27, v19, s92
v_pk_mul_f16 v28, v20, s92
v_pk_mul_f16 v29, v21, s92
PF 56,0
PF 57,1
PF 58,2
PF 59,3
PF 60,4
PF 61,5
PF 62,6
PF 63,7
Q 8
Q 10
Q 12
Q 14
PF 64,0
PF 65,1
PF 66,2
PF 67,3
PF 68,4
PF 69,5
PF 70,6
PF 71,7
PM 152,176
PM 153,177
PM 154,178
PM 155,179
WL 2
PM 156,180
PM 157,181
PM 158,182
PM 159,183
s_mov_b32 s42, 0x1c001c00
v_pk_mul_f16 v152, v152, s42
v_pk_mul_f16 v153, v153, s42
v_pk_mul_f16 v154, v154, s42
v_pk_mul_f16 v155, v155, s42
v_pk_mul_f16 v156, v156, s42
v_pk_mul_f16 v157, v157, s42
v_pk_mul_f16 v158, v158, s42
v_pk_mul_f16 v159, v159, s42
global_store_dwordx4 v0, v[152:155], s[90:91] sc1
s_mov_b64 exec, 0xff
global_store_dwordx4 v202, v[156:159], s[90:91] sc1
s_mov_b64 exec, -1
s_mov_b32 s42, 0x3c003c00
v_dot2_f32_f16 v52, v152, s42, 0
v_dot2_f32_f16 v53, v156, s42, 0
s_nop 1
v_dot2_f32_f16 v52, v153, s42, v52
v_dot2_f32_f16 v53, v157, s42, v53
s_nop 1
v_dot2_f32_f16 v52, v154, s42, v52
v_dot2_f32_f16 v53, v158, s42, v53
s_nop 1
v_dot2_f32_f16 v52, v155, s42, v52
v_dot2_f32_f16 v53, v159, s42, v53
s_nop 1
v_mfma_f32_16x16x32_f16 a[36:39], v[56:59], a[0:3], a[32:35]
mw 72
RS 14,128
RS 18,192
TY 106,5128
M 1
ad 86,144
DR 0
TY 110,18952
v_mfma_f32_16x16x32_f16 a[60:63], v[60:63], a[12:15], a[32:35]
DR 1024
TX 32,4608
TX 36,18432
M 12
DR 2048
TY 40,5120
TY 44,18944
v_mfma_f32_16x16x32_f16 a[64:67], v[64:67], a[8:11], a[32:35]
mw 75
M 15
R 10
v_mfma_f32_16x16x32_f16 a[40:43], v[68:71], a[4:7], a[32:35]
ad 86,150
DR 0
M 2
R 8
v_mfma_f32_16x16x32_f16 a[88:91], v[64:67], a[16:19], a[32:35]
WL 10
DR 1024
v_pk_mul_f16 v22, v14, s92
M 27
DR 2048
v_pk_mul_f16 v23, v15, s92
v_mfma_f32_16x16x32_f16 a[84:87], v[56:59], a[16:19], a[32:35]
v_pk_mul_f16 v24, v16, s92
v_pk_mul_f16 v25, v17, s92
M 25
v_pk_mul_f16 v26, v18, s92
v_pk_mul_f16 v27, v19, s92
v_mfma_f32_16x16x32_f16 a[108:111], v[56:59], a[24:27], a[32:35]
WV 19
v_pk_mul_f16 v28, v20, s92
v_pk_mul_f16 v29, v21, s92
M 37
WL 0
AN 104,106
AN 105,107
v_mfma_f32_16x16x32_f16 a[112:115], v[64:67], a[24:27], a[32:35]
s_barrier
R 24
M 39
R 26
v_mfma_f32_16x16x32_f16 a[136:139], v[64:67], a[0:3], a[32:35]
mw 0
SH 106
AN 106,106
M 51
ad 86,192
DR 0
v_mfma_f32_16x16x32_f16 a[132:135], v[56:59], a[0:3], a[32:35]
DR 1024
AN 107,107
M 49
R 28
v_mfma_f32_16x16x32_f16 a[156:159], v[56:59], a[8:11], a[32:35]
DR 2048
AN 108,110
M 61
mw 3
AN 109,111
SH 110
v_mfma_f32_16x16x32_f16 a[160:163], v[64:67], a[8:11], a[32:35]
ad 86,198
DR 0
M 63
WL 4
R 30
v_mfma_f32_16x16x32_f16 a[184:187], v[64:67], a[16:19], a[32:35]
DR 1024
AN 110,110
M 75
DR 2048
AN 111,111
v_mfma_f32_16x16x32_f16 a[180:183], v[56:59], a[16:19], a[32:35]
PA_ 30,104,32
PA_ 31,105,33
M 73
WL 4
R 32
v_mfma_f32_16x16x32_f16 v[136:139], v[56:59], a[24:27], v[128:131]
PB_ 32,104,32
PB_ 33,105,33
M 85
PA_ 34,108,36
PA_ 35,109,37
v_mfma_f32_16x16x32_f16 v[140:143], v[64:67], a[24:27], v[128:131]
PB_ 36,108,36
PB_ 37,109,37
M 87
WL 4
R 34
v_mfma_f32_16x16x32_f16 v[164:167], v[64:67], a[0:3], v[128:131]
PF 30,0
PF 31,1
M 99
PF 32,2
PF 33,3
v_mfma_f32_16x16x32_f16 v[160:163], v[56:59], a[0:3], v[128:131]
PF 34,4
PF 35,5
M 97
WL 4
PF 36,6
)"
R"(PF 37,7
v_mfma_f32_16x16x32_f16 v[184:187], v[56:59], a[8:11], v[128:131]
WV 14
AW 200,30
AW 201,31
M 109
WL 0
AW 202,32
AW 203,33
v_mfma_f32_16x16x32_f16 v[188:191], v[64:67], a[8:11], v[128:131]
s_barrier
R 48
TY 114,6664
M 111
R 50
TY 118,20488
v_mfma_f32_16x16x32_f16 v[208:211], v[56:59], a[16:19], v[128:131]
mw 24
AW 204,34
AW 205,35
M 121
ad 86,240
DR 0
v_mfma_f32_16x16x32_f16 v[232:235], v[60:63], a[28:31], v[128:131]
DR 1024
AW 206,36
M 132
DR 2048
AW 207,37
PA_ 38,106,40
PA_ 39,107,41
PB_ 40,106,40
PB_ 41,107,41
v_mfma_f32_16x16x32_f16 v[236:239], v[64:67], a[24:27], v[128:131]
mw 27
PA_ 42,110,44
PA_ 43,111,45
PB_ 44,110,44
PB_ 45,111,45
PF 38,0
PF 39,1
M 135
WL 0
R 54
PF 40,2
PF 41,3
PF 42,4
PF 43,5
AN 112,114
AN 113,115
SH 114
AN 114,114
v_mfma_f32_16x16x32_f16 v[212:215], v[68:71], a[20:23], v[128:131]
ad 86,246
DR 0
PF 44,6
PF 45,7
AW 208,38
AW 209,39
AN 115,115
AN 116,118
AN 117,119
SH 118
M 122
R 52
AW 210,40
AW 211,41
AW 212,42
AW 213,43
AN 118,118
AN 119,119
NA 36,0,200
DR 1024
AW 214,44
AW 215,45
NA 36,4,204
DR 2048
TX 32,6144
TX 36,19968
NA 60,12,204
bc 49,3,66,60
TY 40,6656
TY 44,20480
NA 60,8,200
mh 0
WL 2
PA_ 30,112,32
PA_ 31,113,33
PB_ 32,112,32
PB_ 33,113,33
PA_ 34,116,36
PA_ 35,117,37
PB_ 36,116,36
PB_ 37,117,37
PF 30,0
PF 31,1
NA 64,8,208
ad 66,576
H 0
PF 32,2
PF 33,3
PF 34,4
PF 35,5
PF 36,6
PF 37,7
AW 216,30
AW 217,31
NA 64,12,212
WL 0
DG 1024
AW 218,32
AW 219,33
AW 220,34
AW 221,35
AW 222,36
AW 223,37
NA 40,4,212
bc 48,3,64,56
TY 106,9736
TY 110,23560
NA 40,0,208
mh 3
PA_ 38,114,40
PA_ 39,115,41
PB_ 40,114,40
PB_ 41,115,41
PA_ 42,118,44
PA_ 43,119,45
PB_ 44,118,44
PB_ 45,119,45
PF 38,0
PF 39,1
v_mfma_f32_16x16x32_f16 a[44:47], a[216:219], a[0:3], a[32:35]
ad 64,576
H 0
PF 40,2
PF 41,3
PF 42,4
PF 43,5
PF 44,6
PF 45,7
AW 224,38
AW 225,39
NA 44,4,220
H 1
AW 226,40
AW 227,41
AW 228,42
AW 229,43
AW 230,44
AW 231,45
v_mfma_f32_16x16x32_f16 a[68:71], a[220:223], a[12:15], a[32:35]
H 2
RS 14,256
TX 58,9216
NA 68,8,216
RS 18,320
TX 62,23040
TY 66,9728
v_mfma_f32_16x16x32_f16 a[72:75], a[224:227], a[8:11], a[32:35]
WL 5
TY 70,23552
AN 104,106
NA 72,12,228
R 58
v_mfma_f32_16x16x32_f16 a[48:51], a[228:231], a[4:7], a[32:35]
AN 105,107
SH 106
NA 48,0,224
R 56
v_mfma_f32_16x16x32_f16 a[96:99], a[224:227], a[16:19], a[32:35]
AN 106,106
AN 107,107
NA 96,20,228
WL 7
v_pk_mul_f16 v22, v14, s92
v_pk_mul_f16 v23, v15, s92
v_mfma_f32_16x16x32_f16 a[92:95], a[216:219], a[16:19], a[32:35]
v_pk_mul_f16 v24, v16, s92
v_pk_mul_f16 v25, v17, s92
NA 92,20,220
v_pk_mul_f16 v26, v18, s92
v_pk_mul_f16 v27, v19, s92
NA 88,16,208
v_pk_mul_f16 v28, v20, s92
v_pk_mul_f16 v29, v21, s92
NA 88,20,212
AN 108,110
AN 109,111
NA 84,16,200
SH 110
AN 110,110
NA 84,20,204
WL 6
AN 111,111
P 0
NA 108,24,200
Q 1
NA 108,28,204
Q 3
NA 112,24,208
Q 5
NA 112,28,212
P 7
PF 56,0
v_mfma_f32_16x16x32_f16 a[116:119], a[216:219], a[24:27], a[32:35]
WV 17
PF 57,1
PF 58,2
NA 116,28,220
WL 0
PF 59,3
PF 60,4
v_mfma_f32_16x16x32_f16 a[120:123], a[224:227], a[24:27], a[32:35]
s_barrier
R 72
TY 114,11272
NA 120,28,228
R 74
TY 118,25096
v_mfma_f32_16x16x32_f16 a[144:147], a[224:227], a[0:3], a[32:35]
mw 48
v_mov_b32 v54, 1
s_mov_b64 exec, 1
FS
s_mov_b64 exec, -1
NA 144,4,228
ad 86,288
DR 0
TX 74,10752
v_mfma_f32_16x16x32_f16 a[140:143], a[216:219], a[0:3], a[32:35]
PF 61,5
PF 62,6
TX 78,24576
NA 140,4,220
DR 1024
PF 63,7
TY 82,11264
NA 136,0,208
Q 8
TY 86,25088
NA 136,4,212
DR 2048
P 10
TY 122,12808
NA 132,0,200
Q 11
TY 126,26632
NA 132,4,204
WL 6
R 76
TX 90,12288
NA 156,8,200
mw 51
Q 13
TX 94,26112
NA 156,12,204
ad 86,294
DR 0
TY 98,12800
NA 160,8,208
P 15
PF 64,0
TY 102,26624
NA 160,12,212
DR 1024
PF 65,1
v_mfma_f32_16x16x32_f16 a[164:167], a[216:219], a[8:11], a[32:35]
PF 66,2
PF 67,3
NA 164,12,220
DR 2048
PF 68,4
v_mfma_f32_16x16x32_f16 a[168:171], a[224:227], a[8:11], a[32:35]
PF 69,5
PF 70,6
NA 168,12,228
R 78
v_mfma_f32_16x16x32_f16 a[192:195], a[224:227], a[16:19], a[32:35]
bc 49,4,66,60
PF 71,7
NA 192,20,228
mh 9
AN 112,114
AN 113,115
v_mfma_f32_16x16x32_f16 a[188:191], a[216:219], a[16:19], a[32:35]
ad 66,768
H 0
NA 188,20,220
SH 114
AN 114,114
NA 184,16,208
DG 1024
NA 184,20,212
AN 115,115
AN 116,118
NA 180,16,200
bc 48,4,64,56
AN 117,119
NA 180,20,204
WL 2
R 80
NV 136,24,200
mh 12
SH 118
AN 118,118
NV 136,28,204
ad 64,768
H 0
NV 140,24,208
AN 119,119
P 16
NV 140,28,212
H 1
P 17
v_mfma_f32_16x16x32_f16 v[144:147], a[216:219], a[24:27], v[128:131]
Q 18
NV 144,28,220
H 2
P 20
v_mfma_f32_16x16x32_f16 v[148:151], a[224:227], a[24:27], v[128:131]
Q 21
NV 148,28,228
R 82
v_mfma_f32_16x16x32_f16 v[172:175], a[224:227], a[0:3], v[128:131]
P 23
PF 72,0
NV 172,4,228
PF 73,1
PF 74,2
v_mfma_f32_16x16x32_f16 v[168:171], a[216:219], a[0:3], v[128:131]
PF 75,3
PF 76,4
NV 168,4,220
PF 77,5
PF 78,6
NV 164,0,208
PF 79,7
P 24
NV 164,4,212
Q 25
NV 160,0,200
Q 27
NV 160,4,204
WL 2
Q 29
NV 184,8,200
P 31
PF 80,0
NV 184,12,204
PF 81,1
PF 82,2
NV 188,8,208
PF 83,3
PF 84,4
NV 188,12,212
PF 85,5
PF 86,6
v_mfma_f32_16x16x32_f16 v[192:195], a[216:219], a[8:11], v[128:131]
WV 23
PF 87,7
AN 120,122
NV 192,12,220
WL 0
AN 121,123
SH 122
v_mfma_f32_16x16x32_f16 v[196:199], a[224:227], a[8:11], v[128:131]
s_barrier
R 0
NV 196,12,228
R 2
NV 208,16,200
mw 72
AN 122,122
AN 123,123
NV 208,20,204
ad 86,336
DR 0
NV 232,28,204
AN 124,126
AN 125,127
NV 232,24,200
DR 1024
SH 126
NV 236,24,208
AN 126,126
AN 127,127
NV 236,28,212
DR 2048
P 32
NV 212,20,212
Q 33
NV 212,16,208
mw 75
Q 35
v_mfma_f32_16x16x32_f16 v[216:219], a[216:219], a[16:19], v[128:131]
ad 86,342
DR 0
NV 216,20,220
Q 37
)"
R"(v_mfma_f32_16x16x32_f16 v[240:243], a[220:223], a[28:31], v[128:131]
DR 1024
P 39
NV 240,24,216
PF 88,0
PF 89,1
v_mfma_f32_16x16x32_f16 v[244:247], a[224:227], a[24:27], v[128:131]
DR 2048
PF 90,2
NV 244,28,228
R 6
v_mfma_f32_16x16x32_f16 v[220:223], a[228:231], a[20:23], v[128:131]
PF 91,3
PF 92,4
NV 220,16,224
WL 2
R 4
M 0
PF 93,5
PF 94,6
M 1
PF 95,7
P 40
M 13
Q 41
M 12
Q 43
M 14
Q 45
M 15
P 47
PF 96,0
M 3
PF 97,1
PF 98,2
M 2
PF 99,3
PF 100,4
M 4
PF 101,5
PF 102,6
M 5
PF 103,7
M 17
RS 14,384
RS 18,448
M 16
M 18
M 19
M 7
M 6
v_mfma_f32_16x16x32_f16 a[52:55], v[88:91], a[0:3], a[32:35]
WL 0
v_pk_mul_f16 v22, v14, s92
v_pk_mul_f16 v23, v15, s92
M 9
v_pk_mul_f16 v24, v16, s92
v_pk_mul_f16 v25, v17, s92
v_mfma_f32_16x16x32_f16 a[76:79], v[92:95], a[12:15], a[32:35]
v_pk_mul_f16 v26, v18, s92
v_pk_mul_f16 v27, v19, s92
M 20
v_pk_mul_f16 v28, v20, s92
v_pk_mul_f16 v29, v21, s92
v_mfma_f32_16x16x32_f16 a[80:83], v[96:99], a[8:11], a[32:35]
v_lshrrev_b32 v9, 8, v0
v_bfe_u32 v8, v0, 4, 4
M 23
R 10
v_mfma_f32_16x16x32_f16 a[56:59], v[100:103], a[4:7], a[32:35]
v_lshlrev_b32 v10, 2, v9
M 10
R 8
v_mfma_f32_16x16x32_f16 a[104:107], v[96:99], a[16:19], a[32:35]
s_mov_b32 s42, 0xcb527610
M 35
s_movk_i32 s43, 0xc00
v_lshlrev_b32 v11, 3, v8
v_bfe_u32 v3, s42, v10, 4
v_mfma_f32_16x16x32_f16 a[100:103], v[88:91], a[16:19], a[32:35]
v_add_u32 v10, 16, v10
v_mad_u32_u24 v3, v3, s43, v11
M 33
v_bfe_u32 v48, s42, v10, 4
v_add_u32 v10, 1, v9
M 30
v_mad_u32_u24 v48, v48, s43, v11
v_and_b32 v10, 2, v10
M 31
v_sub_u32 v10, 1, v10
v_mul_i32_i24 v49, s43, v10
M 28
M 29
M 26
M 27
M 24
M 25
M 36
M 37
M 38
M 39
M 40
M 41
M 42
M 43
WV 18
v_mfma_f32_16x16x32_f16 a[124:127], v[88:91], a[24:27], a[32:35]
WL 0
M 45
s_barrier
R 24
TY 106,520
v_mfma_f32_16x16x32_f16 a[128:131], v[96:99], a[24:27], a[32:35]
TY 110,14344
TX 32,0
TX 36,13824
M 47
R 26
TY 40,512
v_mfma_f32_16x16x32_f16 a[152:155], v[96:99], a[0:3], a[32:35]
TY 44,14336
TY 114,2056
TY 118,15880
M 59
v_mfma_f32_16x16x32_f16 a[148:151], v[88:91], a[0:3], a[32:35]
M 57
M 54
WL 9
AN 104,106
AN 105,107
M 55
bc 49,5,66,60
SH 106
M 52
mh 18
AN 106,106
AN 107,107
M 53
ad 66,960
H 0
M 50
WL 8
AN 108,110
AN 109,111
M 51
SH 110
AN 110,110
M 48
WL 6
AN 111,111
PA_ 30,104,32
M 49
R 28
M 60
DG 1024
M 61
PA_ 31,105,33
PB_ 32,104,32
M 62
PB_ 33,105,33
PA_ 34,108,36
M 63
PA_ 35,109,37
PB_ 36,108,36
M 64
PB_ 37,109,37
PF 30,0
M 65
PF 31,1
PF 32,2
M 66
v_lshlrev_b32 v55, 4, v0
PF 33,3
M 67
FL
PF 34,4
v_mfma_f32_16x16x32_f16 a[172:175], v[88:91], a[8:11], a[32:35]
PF 35,5
PF 36,6
M 69
PF 37,7
AW 200,30
v_mfma_f32_16x16x32_f16 a[176:179], v[96:99], a[8:11], a[32:35]
AW 201,31
AW 202,32
M 71
R 30
v_mfma_f32_16x16x32_f16 v[132:135], v[96:99], a[16:19], v[128:131]
AW 203,33
AW 204,34
M 83
AW 205,35
AW 206,36
v_mfma_f32_16x16x32_f16 a[196:199], v[88:91], a[16:19], a[32:35]
WL 2
AW 207,37
PA_ 38,106,40
M 81
PA_ 39,107,41
PB_ 40,106,40
TX 32,1536
M 78
PB_ 41,107,41
PA_ 42,110,44
TX 36,15360
M 79
PA_ 43,111,45
PB_ 44,110,44
M 76
PB_ 45,111,45
PF 38,0
M 77
PF 39,1
PF 40,2
M 74
PF 41,3
PF 42,4
M 75
PF 43,5
PF 44,6
M 72
PF 45,7
AW 208,38
M 73
R 32
M 84
AW 209,39
AW 210,40
M 85
bc 48,5,64,56
AW 211,41
M 86
mh 21
AW 212,42
AW 213,43
M 87
ad 64,960
H 0
M 88
AW 214,44
AW 215,45
M 89
TY 40,2048
TY 44,15872
TY 122,3592
M 90
AN 112,114
AN 113,115
TY 126,17416
M 91
H 1
SH 114
v_mfma_f32_16x16x32_f16 v[152:155], v[88:91], a[24:27], v[128:131]
AN 114,114
AN 115,115
M 93
AN 116,118
AN 117,119
v_mfma_f32_16x16x32_f16 v[156:159], v[96:99], a[24:27], v[128:131]
SH 118
AN 118,118
M 95
R 34
v_mfma_f32_16x16x32_f16 v[180:183], v[96:99], a[0:3], v[128:131]
H 2
AN 119,119
M 107
WL 6
PA_ 30,112,32
PA_ 31,113,33
v_mfma_f32_16x16x32_f16 v[176:179], v[88:91], a[0:3], v[128:131]
PB_ 32,112,32
PB_ 33,113,33
M 105
PA_ 34,116,36
PA_ 35,117,37
M 102
PB_ 36,116,36
PB_ 37,117,37
M 103
PF 30,0
PF 31,1
M 100
PF 32,2
PF 33,3
M 101
PF 34,4
PF 35,5
M 98
PF 36,6
PF 37,7
M 99
AW 216,30
AW 217,31
M 96
AW 218,32
AW 219,33
M 97
AW 220,34
AW 221,35
M 108
AW 222,36
AW 223,37
M 109
WL 0
PA_ 38,114,40
PA_ 39,115,41
TX 32,3072
M 110
WV 3
v_cmp_eq_u32 vcc, 0, v54
PB_ 40,114,40
TX 36,16896
M 111
PB_ 41,115,41
PA_ 42,118,44
M 112
PA_ 43,119,45
PB_ 44,118,44
M 113
FB
PB_ 45,119,45
PF 38,0
M 114
PF 39,1
PF 40,2
M 115
PF 41,3
PF 42,4
v_mfma_f32_16x16x32_f16 v[200:203], v[88:91], a[8:11], v[128:131]
PF 43,5
PF 44,6
M 117
s_barrier
R 48
v_mfma_f32_16x16x32_f16 v[204:207], v[96:99], a[8:11], v[128:131]
mw 0
PF 45,7
AW 224,38
M 119
R 50
M 120
ad 84,384
W 0
M 121
W 1
AW 225,39
M 133
W 2
AW 226,40
M 132
mw 3
AW 227,41
AW 228,42
M 134
ad 84,390
W 0
M 135
W 1
AW 229,43
M 123
W 2
AW 230,44
M 122
mw 24
AW 231,45
AN 120,122
M 124
ad 84,432
W 0
TY 40,3584
M 125
W 1
TY 44,17408
M 137
W 2
AN 121,123
M 136
mw 27
SH 122
AN 122,122
M 138
ad 84,438
W 0
M 139
W 1
AN 123,123
M 127
W 2
AN 124,126
M 126
AN 125,127
SH 126
v_mfma_f32_16x16x32_f16 v[224:227], v[88:91], a[16:19], v[128:131]
AN 126,126
AN 127,127
M 129
WL 2
PA_ 30,120,32
PA_ 31,121,33
)"
R"(v_mfma_f32_16x16x32_f16 v[248:251], v[92:95], a[28:31], v[128:131]
PB_ 32,120,32
PB_ 33,121,33
M 140
PA_ 34,124,36
PA_ 35,125,37
v_mfma_f32_16x16x32_f16 v[252:255], v[96:99], a[24:27], v[128:131]
PB_ 36,124,36
PB_ 37,125,37
M 143
R 54
v_mfma_f32_16x16x32_f16 v[228:231], v[100:103], a[20:23], v[128:131]
PF 30,0
PF 31,1
M 130
R 52
NA 36,0,200
PF 32,2
PF 33,3
NA 36,4,204
PF 34,4
PF 35,5
NA 60,12,204
PF 36,6
PF 37,7
NA 60,8,200
AW 232,30
AW 233,31
NA 64,8,208
AW 234,32
AW 235,33
NA 64,12,212
AW 236,34
AW 237,35
NA 40,4,212
AW 238,36
AW 239,37
NA 40,0,208
WL 4
PA_ 38,122,40
PA_ 39,123,41
NA 44,0,216
PB_ 40,122,40
PB_ 41,123,41
NA 44,4,220
PA_ 42,126,44
PA_ 43,127,45
NA 68,12,220
PB_ 44,126,44
PB_ 45,127,45
NA 68,8,216
PF 38,0
PF 39,1
NA 72,8,224
PF 40,2
PF 41,3
NA 72,12,228
PF 42,4
PF 43,5
NA 48,4,228
PF 44,6
PF 45,7
NA 48,0,224
AW 240,38
AW 241,39
NA 52,0,232
AW 242,40
AW 243,41
NA 52,4,236
AW 244,42
AW 245,43
NA 76,12,236
AW 246,44
AW 247,45
NA 76,8,232
NA 80,8,240
NA 80,12,244
R 58
NA 56,4,244
NA 56,0,240
WL 2
R 56
NA 104,16,240
NA 104,20,244
NA 100,16,232
NA 100,20,236
NA 96,16,224
NA 96,20,228
NA 92,16,216
NA 92,20,220
NA 88,16,208
NA 88,20,212
NA 84,16,200
NA 84,20,204
NA 108,24,200
NA 108,28,204
NA 112,24,208
NA 112,28,212
NA 116,24,216
NA 116,28,220
NA 120,24,224
NA 120,28,228
NA 124,24,232
WL 0
NA 124,28,236
s_barrier
R 72
TY 106,5128
NA 128,24,240
mw 48
TY 110,18952
TX 58,4608
TX 62,18432
NA 128,28,244
R 74
TY 66,5120
NA 152,0,240
ad 84,480
W 0
TY 70,18944
NA 152,4,244
v_lshrrev_b32 v8, 1, v0
s_mov_b64 exec, s[76:77]
global_load_dwordx2 v[6:7], v8, s[78:79] sc1
s_mov_b64 exec, -1
NA 148,0,232
TY 114,6664
TY 118,20488
TX 74,6144
NA 148,4,236
WL 7
TX 78,19968
TY 82,6656
TY 86,20480
NA 144,0,224
W 1
AN 104,106
TY 122,8200
NA 144,4,228
AN 105,107
SH 106
TY 126,22024
NA 140,0,216
AN 106,106
AN 107,107
TX 90,7680
NA 140,4,220
AN 108,110
AN 109,111
TX 94,21504
NA 136,0,208
W 2
SH 110
TY 98,8192
NA 136,4,212
WL 9
AN 110,110
AN 111,111
TY 102,22016
NA 132,0,200
Q 0
NA 132,4,204
R 76
NA 156,8,200
mw 51
Q 2
NA 156,12,204
ad 84,486
W 0
NA 160,8,208
Q 4
NA 160,12,212
Q 6
NA 164,8,216
Q 8
NA 164,12,220
W 1
P 10
NA 168,8,224
Q 11
NA 168,12,228
Q 13
NA 172,8,232
P 15
AN 112,114
NA 172,12,236
W 2
AN 113,115
NA 176,8,240
SH 114
AN 114,114
NA 176,12,244
R 78
NV 132,16,240
AN 115,115
AN 116,118
NV 132,20,244
bc 49,6,66,60
AN 117,119
NA 196,16,232
mh 0
SH 118
AN 118,118
NA 196,20,236
ad 66,1152
H 0
NA 192,16,224
WL 2
AN 119,119
P 16
NA 192,20,228
Q 17
NA 188,16,216
Q 19
NA 188,20,220
DG 1024
NA 184,16,208
Q 21
NA 184,20,212
Q 23
NA 180,16,200
Q 25
NA 180,20,204
R 80
NV 136,24,200
bc 48,6,64,56
P 27
NV 136,28,204
mh 3
Q 28
NV 140,24,208
ad 64,1152
H 0
NV 140,28,212
Q 30
NV 144,24,216
AN 120,122
AN 121,123
NV 144,28,220
SH 122
AN 122,122
NV 148,24,224
H 1
AN 123,123
NV 148,28,228
AN 124,126
AN 125,127
NV 152,24,232
SH 126
AN 126,126
NV 152,28,236
AN 127,127
P 32
NV 156,24,240
H 2
P 33
NV 156,28,244
R 82
NV 180,0,240
Q 34
NV 180,4,244
Q 36
NV 176,0,232
Q 38
NV 176,4,236
Q 40
NV 172,0,224
Q 42
NV 172,4,228
Q 44
NV 168,0,216
Q 46
NV 168,4,220
NV 164,0,208
NV 164,4,212
NV 160,0,200
NV 160,4,204
WL 2
NV 184,8,200
NV 184,12,204
NV 188,8,208
NV 188,12,212
NV 192,8,216
NV 192,12,220
NV 196,8,224
NV 196,12,228
WV 18
NV 200,8,232
WL 0
NV 200,12,236
s_barrier
R 0
NV 204,8,240
mw 72
NV 204,12,244
R 2
NV 208,16,200
ad 84,528
W 0
NV 208,20,204
NV 232,28,204
NV 232,24,200
NV 236,24,208
W 1
NV 236,28,212
NV 212,20,212
NV 212,16,208
NV 216,16,216
W 2
NV 216,20,220
ae 44,70,0
NV 240,28,220
s_bitcmp1_b32 s50, 0
s_cselect_b32 s51, 24576, 0
v_add_u32 v47, s51, v3
NV 240,24,216
XL 14,0
NV 244,24,224
mw 75
NV 244,28,228
ad 84,534
W 0
NV 220,20,228
XL 16,768
NV 220,16,224
NV 224,16,232
NV 224,20,236
W 1
NV 248,28,236
NV 248,24,232
NV 252,24,240
XL 18,1536
NV 252,28,244
R 6
NV 228,20,244
W 2
NV 228,16,240
WL 2
R 4
M 0
M 1
M 13
XL 20,2304
M 12
M 14
M 15
WV 20
v_or_b32 v6, v6, v7
s_mov_b64 exec, s[76:77]
v_cmp_ne_u32 %[flg], 0, v6
s_mov_b64 exec, -1
M 3
M 2
M 4
M 5
M 17
M 16
M 18
M 19
M 7
M 6
M 8
M 9
M 21
M 20
M 22
M 23
R 10
M 11
M 10
WL 2
R 8
M 34
M 35
M 32
M 33
M 30
M 31
M 28
M 29
M 26
M 27
M 24
M 25
M 36
M 37
M 38
M 39
M 40
M 41
M 42
M 43
WV 15
M 44
WL 0
M 45
s_barrier
R 24
M 46
mw 0
)"
R"(M 47
R 26
M 58
ad 84,576
W 0
M 59
TY 106,9736
TY 110,23560
M 56
M 57
W 1
M 54
M 55
M 52
W 2
M 53
M 50
M 51
mw 3
M 48
ad 84,582
W 0
M 49
R 28
M 60
WL 3
AN 104,106
AN 105,107
M 61
W 1
SH 106
M 62
AN 106,106
AN 107,107
M 63
WL 2
AN 108,110
AN 109,111
M 64
W 2
SH 110
M 65
AN 110,110
AN 111,111
M 66
TY 114,11272
TY 118,25096
M 67
bc 49,7,66,60
M 68
mh 9
M 69
ad 66,1344
H 0
M 70
M 71
R 30
M 82
DG 1024
M 83
M 80
WL 3
M 81
bc 48,7,64,56
AN 112,114
M 78
mh 12
AN 113,115
SH 114
M 79
ad 64,1344
H 0
M 76
AN 114,114
AN 115,115
M 77
WL 2
AN 116,118
AN 117,119
M 74
H 1
SH 118
M 75
AN 118,118
AN 119,119
M 72
TY 122,12808
TY 126,26632
M 73
R 32
M 84
H 2
M 85
M 86
M 87
s_load_dwordx4 s[88:91], %[pp], 0xc0
s_load_dwordx2 s[86:87], %[pp], 0xd0
M 88
M 89
M 90
M 91
WL 3
AN 120,122
AN 121,123
M 92
SH 122
AN 122,122
M 93
WL 2
AN 123,123
AN 124,126
M 94
AN 125,127
SH 126
M 95
R 34
M 106
AN 126,126
AN 127,127
M 107
M 104
M 105
M 102
M 103
M 100
M 101
M 98
M 99
M 96
M 97
R 48
M 108
M 109
M 110
M 111
M 112
M 113
M 114
M 115
M 116
s_waitcnt lgkmcnt(0)
M 117
M 118
M 119
R 50
M 120
v_mov_b32 v54, v52
M 121
M 133
M 132
TX 58,9216
TX 62,23040
M 134
v_permlane32_swap_b32 v52, v54
M 135
M 123
M 122
TY 66,9728
TY 70,23552
M 124
v_add_f32 v52, v52, v54
v_mov_b32 v54, v52
M 125
M 137
ae 44,70,0
M 136
TX 74,10752
TX 78,24576
M 138
s_bfe_u32 s51, s50, 0x10001
WL 4
Q 0
M 139
Q 2
M 127
Q 4
M 126
TY 82,11264
TY 86,25088
M 128
Q 6
M 129
WL 4
Q 8
M 141
Q 10
M 140
TX 90,12288
TX 94,26112
M 142
Q 12
M 143
R 54
M 131
Q 14
M 130
TY 98,12800
TY 102,26624
M 0
R 52
M 1
WL 10
Q 16
M 13
Q 18
M 12
Q 20
M 14
Q 22
M 15
WL 8
Q 24
M 3
Q 26
M 2
Q 28
M 4
Q 30
M 5
WL 6
Q 32
M 17
Q 34
M 16
Q 36
M 18
Q 38
M 19
WL 2
Q 40
M 7
Q 42
M 6
Q 44
M 8
Q 46
M 9
v_permlane16_swap_b32 v52, v54
v_mad_i32_i24 v47, v49, s51, v48
M 21
XL 22,0
M 20
XL 24,768
M 22
XL 26,1536
v_add_f32 v52, v52, v54
M 23
R 58
M 11
XL 28,2304
M 10
WL 2
R 56
M 34
v_permlane16_swap_b32 v52, v53
v_mov_b32 v54, s88
M 35
v_mov_b32 v53, s89
M 32
M 33
M 30
v_permlane16_swap_b32 v54, v53
v_bfe_u32 v53, v0, 2, 6
M 31
M 28
M 29
v_add_u32 v54, v53, v54
M 26
M 27
WV 9
M 24
WL 0
FP
M 25
M 36
M 37
M 38
M 39
M 40
UL 6,14
UH 7,14
M 41
AR 8,36
AR 9,60
M 42
AD 8,6
AD 9,7
M 43
AW 36,8
AW 60,9
M 44
UL 6,15
UH 7,15
M 45
FR
AR 8,84
M 46
AR 9,108
AD 8,6
M 47
AD 9,7
AW 84,8
M 58
AW 108,9
UL 6,16
M 59
UH 7,16
AR 8,37
M 56
AR 9,61
AD 8,6
M 57
AD 9,7
AW 37,8
M 54
AW 61,9
UL 6,17
M 55
WL 0
FQ 4
UH 7,17
M 52
AR 8,85
AR 9,109
M 53
AD 8,6
AD 9,7
M 50
FZ 4
R 72
M 51
mw 24
R 74
M 48
TY 106,520
TY 110,14344
M 49
R 76
M 60
ad 84,624
W 0
M 61
AW 85,8
AW 109,9
M 62
UL 6,18
UH 7,18
M 63
W 1
AR 8,38
M 64
AR 9,62
AD 8,6
M 65
AD 9,7
AW 38,8
M 66
W 2
AW 62,9
M 67
WL 3
AN 104,106
AN 105,107
M 68
WL 2
SH 106
AN 106,106
M 69
mw 27
AN 107,107
AN 108,110
M 70
AN 109,111
SH 110
M 71
R 78
M 82
AN 110,110
AN 111,111
M 83
TY 114,2056
TY 118,15880
M 80
ad 84,630
W 0
M 81
UL 6,19
UH 7,19
M 78
AR 8,86
AR 9,110
M 79
W 1
AD 8,6
M 76
AD 9,7
AW 86,8
M 77
AW 110,9
UL 6,20
)"
R"(M 74
W 2
UH 7,20
M 75
AR 8,39
AR 9,63
M 72
WL 1
AN 112,114
AN 113,115
M 73
R 80
M 84
mw 48
SH 114
AN 114,114
M 85
WL 2
AN 115,115
AN 116,118
M 86
AN 117,119
SH 118
M 87
AN 118,118
AN 119,119
M 88
TY 122,3592
TY 126,17416
M 89
ad 84,672
W 0
M 90
AD 8,6
AD 9,7
M 91
AW 39,8
AW 63,9
M 92
W 1
UL 6,21
M 93
UH 7,21
AR 8,87
M 94
AR 9,111
AD 8,6
M 95
R 82
M 106
W 2
AD 9,7
M 107
WL 3
AN 120,122
AN 121,123
M 104
WL 2
SH 122
AN 122,122
M 105
mw 51
AN 123,123
AN 124,126
M 102
AN 125,127
SH 126
M 103
AN 126,126
AN 127,127
M 100
ad 84,678
W 0
M 101
AW 87,8
AW 111,9
M 98
M 99
W 1
M 96
M 97
R 0
M 108
W 2
M 109
M 110
M 111
bc 49,8,66,60
M 112
mh 18
M 113
ad 66,1536
H 0
M 114
M 115
M 116
DG 1024
M 117
M 118
M 119
WL 2
R 2
M 120
bc 48,8,64,56
M 121
mh 21
s_bitcmp1_b32 s87, 0
s_cselect_b32 exec_lo, 0xffffff, 0
s_mov_b32 exec_hi, 0
global_atomic_add_f32 v54, v52, s[90:91] sc1
s_mov_b64 exec, -1
M 133
ad 64,1536
H 0
M 132
TX 58,0
TX 62,13824
M 134
M 135
H 1
M 123
M 122
TY 66,512
TY 70,14336
M 124
H 2
M 125
M 137
M 136
TX 74,1536
TX 78,15360
M 138
WL 4
Q 0
M 139
Q 2
M 127
Q 4
M 126
TY 82,2048
TY 86,15872
M 128
Q 6
M 129
WL 4
Q 8
M 141
Q 10
M 140
TX 90,3072
TX 94,16896
M 142
Q 12
M 143
R 6
M 131
Q 14
M 130
TY 98,3584
TY 102,17408
M 0
R 4
M 1
WL 10
Q 16
M 13
Q 18
M 12
Q 20
M 14
Q 22
M 15
WL 8
Q 24
M 3
Q 26
M 2
Q 28
M 4
Q 30
M 5
WL 6
Q 32
M 17
Q 34
M 16
Q 36
M 18
Q 38
M 19
WL 2
Q 40
M 7
Q 42
M 6
Q 44
M 8
Q 46
M 9
ae 44,68,0
M 21
s_bitcmp1_b32 s50, 2
s_cselect_b32 s51, 24576, 0
v_add_u32 v47, s51, v3
M 20
XL 30,0
M 22
XL 32,768
M 23
R 10
M 11
XL 34,1536
M 10
WL 2
R 8
M 34
XL 36,2304
M 35
M 32
M 33
M 30
M 31
M 28
M 29
M 26
M 27
WV 10
M 24
WL 0
FP
M 25
M 36
M 37
s_bitcmp1_b32 s87, 1
s_cselect_b32 exec_lo, 0xffffff, 0
s_mov_b32 exec_hi, 0
global_atomic_add_f32 v54, v52, s[90:91] sc1
s_mov_b64 exec, -1
M 38
M 39
M 40
M 41
M 42
UL 6,22
UH 7,22
M 43
AR 8,40
AR 9,64
M 44
AD 8,6
AD 9,7
M 45
FR
AW 40,8
M 46
AW 64,9
UL 6,23
M 47
UH 7,23
AR 8,88
M 58
AR 9,112
AD 8,6
M 59
AD 9,7
AW 88,8
M 56
AW 112,9
UL 6,24
M 57
UH 7,24
AR 8,41
M 54
AR 9,65
AD 8,6
M 55
WL 0
FQ 8
AD 9,7
M 52
AW 41,8
AW 65,9
M 53
UL 6,25
UH 7,25
M 50
FZ 8
R 24
M 51
mw 72
R 26
M 48
TY 106,5128
TY 110,18952
M 49
R 28
M 60
ad 84,720
W 0
M 61
AR 8,89
AR 9,113
M 62
AD 8,6
AD 9,7
M 63
W 1
AW 89,8
M 64
AW 113,9
UL 6,26
M 65
UH 7,26
AR 8,42
M 66
W 2
AR 9,66
M 67
WL 3
AN 104,106
AN 105,107
M 68
WL 2
SH 106
AN 106,106
M 69
mw 75
AN 107,107
AN 108,110
M 70
AN 109,111
SH 110
M 71
R 30
M 82
AN 110,110
AN 111,111
M 83
TY 114,6664
TY 118,20488
M 80
ad 84,726
W 0
M 81
AD 8,6
AD 9,7
M 78
AW 42,8
AW 66,9
M 79
W 1
UL 6,27
M 76
UH 7,27
AR 8,90
M 77
AR 9,114
AD 8,6
M 74
W 2
AD 9,7
M 75
AW 90,8
AW 114,9
M 72
WL 1
AN 112,114
AN 113,115
M 73
R 32
M 84
mw 0
SH 114
AN 114,114
M 85
WL 2
AN 115,115
AN 116,118
M 86
AN 117,119
SH 118
M 87
AN 118,118
AN 119,119
M 88
TY 122,8200
TY 126,22024
M 89
ad 84,768
W 0
M 90
UL 6,28
UH 7,28
M 91
AR 8,43
AR 9,67
M 92
W 1
AD 8,6
M 93
)"
R"(AD 9,7
AW 43,8
M 94
AW 67,9
UL 6,29
M 95
R 34
M 106
W 2
UH 7,29
M 107
WL 3
AN 120,122
AN 121,123
M 104
WL 2
SH 122
AN 122,122
M 105
mw 3
AN 123,123
AN 124,126
M 102
AN 125,127
SH 126
M 103
AN 126,126
AN 127,127
M 100
ad 84,774
W 0
M 101
AR 8,91
AR 9,115
M 98
AD 8,6
AD 9,7
M 99
W 1
AW 91,8
M 96
AW 115,9
M 97
R 48
M 108
W 2
M 109
M 110
M 111
bc 49,9,66,60
M 112
mh 0
M 113
ad 66,1728
H 0
M 114
M 115
M 116
DG 1024
M 117
M 118
M 119
WL 2
R 50
M 120
bc 48,9,64,56
M 121
mh 3
s_bitcmp1_b32 s87, 2
s_cselect_b32 exec_lo, 0xffffff, 0
s_mov_b32 exec_hi, 0
global_atomic_add_f32 v54, v52, s[90:91] sc1
s_mov_b64 exec, -1
M 133
ad 64,1728
H 0
M 132
TX 58,4608
TX 62,18432
M 134
M 135
H 1
M 123
M 122
TY 66,5120
TY 70,18944
M 124
H 2
M 125
M 137
M 136
TX 74,6144
TX 78,19968
M 138
WL 4
Q 0
M 139
Q 2
M 127
Q 4
M 126
TY 82,6656
TY 86,20480
M 128
Q 6
M 129
WL 4
Q 8
M 141
Q 10
M 140
TX 90,7680
TX 94,21504
M 142
Q 12
M 143
R 54
M 131
Q 14
M 130
TY 98,8192
TY 102,22016
M 0
R 52
M 1
WL 10
Q 16
M 13
Q 18
M 12
Q 20
M 14
Q 22
M 15
WL 8
Q 24
M 3
Q 26
M 2
Q 28
M 4
Q 30
M 5
WL 6
Q 32
M 17
Q 34
M 16
Q 36
M 18
Q 38
M 19
WL 2
Q 40
M 7
Q 42
M 6
Q 44
M 8
Q 46
M 9
ae 44,68,0
M 21
s_bfe_u32 s51, s50, 0x10003
v_mad_i32_i24 v47, v49, s51, v48
XL 38,0
M 20
XL 40,768
M 22
XL 42,1536
M 23
R 58
M 11
XL 44,2304
M 10
WL 2
R 56
M 34
M 35
M 32
M 33
M 30
M 31
M 28
M 29
M 26
M 27
WV 10
M 24
WL 0
FP
M 25
M 36
M 37
s_bitcmp1_b32 s87, 3
s_cselect_b32 exec_lo, 0xffffff, 0
s_mov_b32 exec_hi, 0
global_atomic_add_f32 v54, v52, s[90:91] sc1
s_mov_b64 exec, -1
M 38
M 39
M 40
M 41
M 42
M 43
M 44
UL 6,30
UH 7,30
M 45
FR
AR 8,44
M 46
AR 9,68
AD 8,6
M 47
AD 9,7
AW 44,8
M 58
AW 68,9
UL 6,31
M 59
UH 7,31
AR 8,92
M 56
AR 9,116
AD 8,6
M 57
AD 9,7
AW 92,8
M 54
AW 116,9
UL 6,32
M 55
WL 0
FQ 12
UH 7,32
M 52
AR 8,45
AR 9,69
M 53
AD 8,6
AD 9,7
M 50
FZ 12
R 72
M 51
mw 24
R 74
M 48
TY 106,9736
TY 110,23560
M 49
R 76
M 60
ad 84,816
W 0
M 61
AW 45,8
AW 69,9
M 62
UL 6,33
UH 7,33
M 63
W 1
AR 8,93
M 64
AR 9,117
AD 8,6
M 65
AD 9,7
AW 93,8
M 66
W 2
AW 117,9
M 67
WL 3
AN 104,106
AN 105,107
M 68
WL 2
SH 106
AN 106,106
M 69
mw 27
AN 107,107
AN 108,110
M 70
AN 109,111
SH 110
M 71
R 78
M 82
AN 110,110
AN 111,111
M 83
TY 114,11272
TY 118,25096
M 80
ad 84,822
W 0
M 81
UL 6,34
UH 7,34
M 78
AR 8,46
AR 9,70
M 79
W 1
AD 8,6
M 76
AD 9,7
AW 46,8
M 77
AW 70,9
UL 6,35
M 74
W 2
UH 7,35
M 75
AR 8,94
AR 9,118
M 72
WL 1
AN 112,114
AN 113,115
M 73
R 80
M 84
mw 48
SH 114
AN 114,114
M 85
WL 2
AN 115,115
AN 116,118
M 86
AN 117,119
SH 118
M 87
AN 118,118
AN 119,119
M 88
TY 122,12808
TY 126,26632
M 89
ad 84,864
W 0
M 90
AD 8,6
AD 9,7
M 91
AW 94,8
AW 118,9
M 92
W 1
UL 6,36
M 93
UH 7,36
AR 8,47
M 94
AR 9,71
AD 8,6
M 95
R 82
M 106
W 2
AD 9,7
M 107
WL 3
AN 120,122
AN 121,123
M 104
WL 2
SH 122
AN 122,122
M 105
mw 51
AN 123,123
AN 124,126
M 102
AN 125,127
SH 126
M 103
AN 126,126
AN 127,127
M 100
ad 84,870
W 0
M 101
AW 47,8
AW 71,9
M 98
UL 6,37
UH 7,37
M 99
W 1
AR 8,95
M 96
AR 9,119
AD 8,6
)"
R"(M 97
R 0
M 108
W 2
AD 9,7
M 109
AW 95,8
AW 119,9
M 110
M 111
bc 49,10,66,60
M 112
mh 9
M 113
ad 66,1920
H 0
M 114
M 115
M 116
DG 1024
M 117
M 118
M 119
WL 2
R 2
M 120
bc 48,10,64,56
M 121
mh 12
s_bitcmp1_b32 s87, 4
s_cselect_b32 exec_lo, 0xffffff, 0
s_mov_b32 exec_hi, 0
global_atomic_add_f32 v54, v52, s[90:91] sc1
s_mov_b64 exec, -1
M 133
ad 64,1920
H 0
M 132
TX 58,9216
TX 62,23040
M 134
M 135
H 1
M 123
M 122
TY 66,9728
TY 70,23552
M 124
H 2
M 125
M 137
M 136
TX 74,10752
TX 78,24576
M 138
WL 4
Q 0
M 139
Q 2
M 127
Q 4
M 126
TY 82,11264
TY 86,25088
M 128
Q 6
M 129
WL 4
Q 8
M 141
Q 10
M 140
TX 90,12288
TX 94,26112
M 142
Q 12
M 143
R 6
M 131
Q 14
M 130
TY 98,12800
TY 102,26624
M 0
R 4
M 1
WL 10
Q 16
M 13
Q 18
M 12
Q 20
M 14
Q 22
M 15
WL 8
Q 24
M 3
Q 26
M 2
Q 28
M 4
Q 30
M 5
WL 6
Q 32
M 17
Q 34
M 16
Q 36
M 18
Q 38
M 19
WL 2
Q 40
M 7
Q 42
M 6
Q 44
M 8
Q 46
M 9
ae 44,68,96
M 21
s_bitcmp1_b32 s50, 4
s_cselect_b32 s51, 24576, 0
v_add_u32 v47, s51, v3
M 20
XL 14,0
M 22
XL 16,768
M 23
R 10
M 11
XL 18,1536
M 10
WL 2
R 8
M 34
XL 20,2304
M 35
M 32
M 33
M 30
M 31
M 28
M 29
M 26
M 27
WV 10
M 24
WL 0
FP
M 25
M 36
M 37
s_bitcmp1_b32 s87, 5
s_cselect_b32 exec_lo, 0xffffff, 0
s_mov_b32 exec_hi, 0
global_atomic_add_f32 v54, v52, s[90:91] sc1
s_mov_b64 exec, -1
M 38
M 39
M 40
M 41
M 42
M 43
M 44
M 45
FR
M 46
UL 6,38
UH 7,38
M 47
AR 8,48
AR 9,72
M 58
AD 8,6
AD 9,7
M 59
AW 48,8
AW 72,9
M 56
UL 6,39
UH 7,39
M 57
AR 8,96
AR 9,120
M 54
AD 8,6
AD 9,7
M 55
WL 0
FQ 16
AW 96,8
M 52
AW 120,9
UL 6,40
M 53
UH 7,40
AR 8,49
M 50
FZ 16
R 24
M 51
mw 72
R 26
M 48
TY 106,520
TY 110,14344
M 49
R 28
M 60
ad 84,912
W 0
M 61
AR 9,73
AD 8,6
M 62
AD 9,7
AW 49,8
M 63
W 1
AW 73,9
M 64
UL 6,41
UH 7,41
M 65
AR 8,97
AR 9,121
M 66
W 2
AD 8,6
M 67
WL 3
AN 104,106
AN 105,107
M 68
WL 2
SH 106
AN 106,106
M 69
mw 75
AN 107,107
AN 108,110
M 70
AN 109,111
SH 110
M 71
R 30
M 82
AN 110,110
AN 111,111
M 83
TY 114,2056
TY 118,15880
M 80
ad 84,918
W 0
M 81
AD 9,7
AW 97,8
M 78
AW 121,9
UL 6,42
M 79
W 1
UH 7,42
M 76
AR 8,50
AR 9,74
M 77
AD 8,6
AD 9,7
M 74
W 2
AW 50,8
M 75
AW 74,9
UL 6,43
M 72
WL 1
AN 112,114
AN 113,115
M 73
R 32
M 84
mw 0
SH 114
AN 114,114
M 85
WL 2
AN 115,115
AN 116,118
M 86
AN 117,119
SH 118
M 87
AN 118,118
AN 119,119
M 88
TY 122,3592
TY 126,17416
M 89
ad 84,960
W 0
M 90
UH 7,43
AR 8,98
M 91
AR 9,122
AD 8,6
M 92
W 1
AD 9,7
M 93
AW 98,8
AW 122,9
M 94
UL 6,44
UH 7,44
M 95
R 34
M 106
W 2
AR 8,51
M 107
WL 3
AN 120,122
AN 121,123
M 104
WL 2
SH 122
AN 122,122
M 105
mw 3
AN 123,123
AN 124,126
M 102
AN 125,127
SH 126
M 103
AN 126,126
AN 127,127
M 100
ad 84,966
W 0
M 101
AR 9,75
AD 8,6
M 98
AD 9,7
AW 51,8
M 99
W 1
AW 75,9
M 96
UL 6,45
UH 7,45
M 97
R 48
M 108
W 2
AR 8,99
M 109
AR 9,123
AD 8,6
M 110
AD 9,7
AW 99,8
M 111
bc 49,11,66,60
AW 123,9
M 112
mh 18
M 113
ad 66,2112
H 0
M 114
M 115
M 116
DG 1024
M 117
M 118
M 119
WL 2
R 50
M 120
bc 48,11,64,56
M 121
mh 21
s_bitcmp1_b32 s87, 6
s_cselect_b32 exec_lo, 0xffffff, 0
s_mov_b32 exec_hi, 0
global_atomic_add_f32 v54, v52, s[90:91] sc1
s_mov_b64 exec, -1
M 133
ad 64,2112
H 0
M 132
)"
R"(TX 58,0
TX 62,13824
M 134
M 135
H 1
M 123
M 122
TY 66,512
TY 70,14336
M 124
H 2
M 125
M 137
M 136
TX 74,1536
TX 78,15360
PA 10240
DF
M 138
WL 4
Q 0
M 139
Q 2
M 127
Q 4
M 126
TY 82,2048
TY 86,15872
M 128
Q 6
M 129
WL 4
Q 8
M 141
Q 10
M 140
TX 90,3072
TX 94,16896
M 142
Q 12
M 143
R 54
M 131
Q 14
M 130
TY 98,3584
TY 102,17408
M 0
R 52
M 1
WL 10
Q 16
M 13
Q 18
M 12
Q 20
M 14
Q 22
M 15
WL 8
Q 24
M 3
Q 26
M 2
Q 28
M 4
Q 30
M 5
WL 6
Q 32
M 17
Q 34
M 16
Q 36
M 18
Q 38
M 19
WL 2
Q 40
M 7
Q 42
M 6
Q 44
M 8
Q 46
M 9
ae 44,68,96
M 21
s_bfe_u32 s51, s50, 0x10005
v_mad_i32_i24 v47, v49, s51, v48
XL 22,0
M 20
XL 24,768
M 22
XL 26,1536
M 23
R 58
M 11
XL 28,2304
M 10
WL 2
R 56
M 34
M 35
M 32
M 33
M 30
M 31
M 28
M 29
M 26
M 27
WV 11
M 24
WL 0
FP
M 25
M 36
M 37
s_bitcmp1_b32 s87, 7
s_cselect_b32 exec_lo, 0xffffff, 0
s_mov_b32 exec_hi, 0
global_atomic_add_f32 v54, v52, s[90:91] sc1
s_mov_b64 exec, -1
M 38
M 39
M 40
M 41
M 42
M 43
M 44
M 45
FR
M 46
M 47
M 58
UL 6,14
UH 7,14
M 59
AR 8,52
AR 9,76
M 56
AD 8,6
AD 9,7
M 57
AW 52,8
AW 76,9
M 54
UL 6,15
UH 7,15
M 55
WL 0
FQ 20
AR 8,100
M 52
AR 9,124
AD 8,6
M 53
AD 9,7
AW 100,8
M 50
FZ 20
R 72
M 51
mw 24
R 74
M 48
TY 106,5128
TY 110,18952
M 49
R 76
M 60
ad 84,1008
W 0
M 61
AW 124,9
UL 6,16
M 62
UH 7,16
AR 8,53
M 63
W 1
AR 9,77
M 64
AD 8,6
AD 9,7
M 65
AW 53,8
AW 77,9
M 66
W 2
UL 6,17
M 67
WL 3
AN 104,106
AN 105,107
M 68
WL 2
SH 106
AN 106,106
M 69
mw 27
AN 107,107
AN 108,110
M 70
AN 109,111
SH 110
M 71
R 78
M 82
AN 110,110
AN 111,111
M 83
TY 114,6664
TY 118,20488
M 80
ad 84,1014
W 0
M 81
UH 7,17
AR 8,101
M 78
AR 9,125
AD 8,6
M 79
W 1
AD 9,7
M 76
AW 101,8
AW 125,9
M 77
UL 6,18
UH 7,18
M 74
W 2
AR 8,54
M 75
AR 9,78
AD 8,6
M 72
WL 1
AN 112,114
AN 113,115
M 73
R 80
M 84
mw 48
SH 114
AN 114,114
M 85
WL 2
AN 115,115
AN 116,118
M 86
AN 117,119
SH 118
M 87
AN 118,118
AN 119,119
M 88
TY 122,8200
TY 126,22024
M 89
ad 84,1056
W 0
M 90
AD 9,7
AW 54,8
M 91
AW 78,9
UL 6,19
M 92
W 1
UH 7,19
M 93
AR 8,102
AR 9,126
M 94
AD 8,6
AD 9,7
M 95
R 82
M 106
W 2
AW 102,8
M 107
WL 3
AN 120,122
AN 121,123
M 104
WL 2
SH 122
AN 122,122
M 105
mw 51
AN 123,123
AN 124,126
M 102
AN 125,127
SH 126
M 103
AN 126,126
AN 127,127
M 100
ad 84,1062
W 0
M 101
AW 126,9
UL 6,20
M 98
UH 7,20
AR 8,55
M 99
W 1
AR 9,79
M 96
AD 8,6
AD 9,7
M 97
R 0
M 108
W 2
AW 55,8
M 109
AW 79,9
UL 6,21
M 110
UH 7,21
AR 8,103
M 111
bc 49,12,66,60
AR 9,127
M 112
mh 0
AD 8,6
AD 9,7
M 113
ad 66,2304
H 0
M 114
AW 103,8
AW 127,9
M 115
M 116
DG 1024
M 117
M 118
M 119
WL 2
R 2
M 120
bc 48,12,64,56
M 121
mh 3
M 133
ad 64,2304
H 0
M 132
TX 58,4608
TX 62,18432
M 134
M 135
H 1
M 123
M 122
TY 66,5120
TY 70,18944
M 124
H 2
M 125
M 137
M 136
TX 74,6144
TX 78,19968
M 138
WL 4
Q 0
M 139
Q 2
M 127
Q 4
M 126
TY 82,6656
TY 86,20480
M 128
Q 6
M 129
WL 4
Q 8
M 141
Q 10
M 140
TX 90,7680
TX 94,21504
M 142
Q 12
M 143
R 6
M 131
Q 14
)"
R"(M 130
TY 98,8192
TY 102,22016
M 0
R 4
M 1
WL 10
Q 16
M 13
Q 18
M 12
Q 20
M 14
Q 22
M 15
WL 8
Q 24
M 3
Q 26
M 2
Q 28
M 4
Q 30
M 5
WL 6
Q 32
M 17
Q 34
M 16
Q 36
M 18
Q 38
M 19
WL 2
Q 40
M 7
Q 42
M 6
Q 44
M 8
Q 46
M 9
M 21
M 20
M 22
M 23
R 10
M 11
M 10
WL 2
R 8
M 34
M 35
M 32
M 33
M 30
M 31
M 28
M 29
M 26
ae 44,70,0
M 27
WV 5
M 24
WL 0
FP
M 25
M 36
s_bitcmp1_b32 s50, 0
s_cselect_b32 s51, 24576, 0
v_add_u32 v47, s51, v3
M 37
XL 30,128
M 38
XL 32,896
M 39
M 40
M 41
M 42
M 43
M 44
XL 34,1664
M 45
FR
M 46
M 47
M 58
M 59
M 56
XL 36,2432
UL 6,22
M 57
UH 7,22
AR 8,56
M 54
AR 9,80
AD 8,6
M 55
WL 0
FQ 24
AD 9,7
M 52
AW 56,8
AW 80,9
M 53
UL 6,23
UH 7,23
M 50
FZ 24
R 24
M 51
mw 72
R 26
M 48
TY 106,9736
TY 110,23560
M 49
R 28
M 60
ad 84,1104
W 0
M 61
AR 8,104
AR 9,128
M 62
AD 8,6
AD 9,7
M 63
W 1
AW 104,8
M 64
AW 128,9
UL 6,24
M 65
UH 7,24
AR 8,57
M 66
W 2
AR 9,81
M 67
WL 3
AN 104,106
AN 105,107
M 68
WL 2
SH 106
AN 106,106
M 69
mw 75
AN 107,107
AN 108,110
M 70
AN 109,111
SH 110
M 71
R 30
M 82
AN 110,110
AN 111,111
M 83
TY 114,11272
TY 118,25096
M 80
ad 84,1110
W 0
M 81
AD 8,6
AD 9,7
M 78
AW 57,8
AW 81,9
M 79
W 1
UL 6,25
M 76
UH 7,25
AR 8,105
M 77
AR 9,129
AD 8,6
M 74
W 2
AD 9,7
M 75
AW 105,8
AW 129,9
M 72
WL 1
AN 112,114
AN 113,115
M 73
R 32
M 84
mw 0
SH 114
AN 114,114
M 85
WL 2
AN 115,115
AN 116,118
M 86
AN 117,119
SH 118
M 87
AN 118,118
AN 119,119
M 88
TY 122,12808
TY 126,26632
M 89
ad 84,1152
W 0
M 90
UL 6,26
UH 7,26
M 91
AR 8,58
AR 9,82
M 92
W 1
AD 8,6
M 93
AD 9,7
AW 58,8
M 94
AW 82,9
UL 6,27
M 95
R 34
M 106
W 2
UH 7,27
M 107
WL 3
AN 120,122
AN 121,123
M 104
WL 2
SH 122
AN 122,122
M 105
mw 3
AN 123,123
AN 124,126
M 102
AN 125,127
SH 126
M 103
AN 126,126
AN 127,127
M 100
ad 84,1158
W 0
M 101
AR 8,106
AR 9,130
M 98
AD 8,6
AD 9,7
M 99
W 1
AW 106,8
M 96
AW 130,9
UL 6,28
M 97
R 48
M 108
W 2
UH 7,28
M 109
AR 8,59
AR 9,83
M 110
AD 8,6
AD 9,7
M 111
bc 49,13,66,60
AW 59,8
M 112
mh 9
AW 83,9
UL 6,29
M 113
ad 66,2496
H 0
M 114
UH 7,29
AR 8,107
M 115
AR 9,131
AD 8,6
M 116
DG 1024
M 117
AD 9,7
AW 107,8
M 118
AW 131,9
M 119
WL 1
R 50
M 120
bc 48,13,64,56
M 121
mh 12
M 133
ad 64,2496
H 0
M 132
TX 58,9216
TX 62,23040
M 134
M 135
H 1
M 123
M 122
TY 66,9728
TY 70,23552
M 124
H 2
M 125
M 137
M 136
TX 74,10752
TX 78,24576
M 138
WL 4
Q 0
M 139
Q 2
M 127
Q 4
M 126
TY 82,11264
TY 86,25088
M 128
Q 6
M 129
WL 4
Q 8
M 141
Q 10
M 140
TX 90,12288
TX 94,26112
M 142
Q 12
M 143
R 54
M 131
Q 14
M 130
TY 98,12800
TY 102,26624
M 0
R 52
M 1
WL 10
Q 16
M 13
Q 18
M 12
Q 20
M 14
Q 22
M 15
WL 8
Q 24
M 3
Q 26
M 2
Q 28
M 4
Q 30
M 5
WL 6
Q 32
M 17
Q 34
M 16
Q 36
M 18
Q 38
M 19
WL 2
Q 40
M 7
Q 42
M 6
Q 44
M 8
Q 46
M 9
M 21
M 20
M 22
M 23
R 58
M 11
)"
R"(M 10
WL 2
R 56
M 34
M 35
M 32
M 33
M 30
M 31
M 28
M 29
M 26
M 27
WV 5
M 24
WL 0
FP
M 25
M 36
ae 44,70,0
M 37
s_bfe_u32 s51, s50, 0x10001
v_mad_i32_i24 v47, v49, s51, v48
XL 38,128
M 38
M 39
M 40
XL 40,896
M 41
M 42
M 43
M 44
M 45
FR
M 46
XL 42,1664
M 47
M 58
M 59
M 56
M 57
M 54
XL 44,2432
M 55
WL 0
FQ 28
M 52
M 53
M 50
FZ 28
R 72
M 51
R 74
M 48
TY 106,520
TY 110,14344
M 49
R 76
M 60
mw 24
v_mov_b32 v54, 2
s_mov_b64 exec, 1
FS
s_mov_b64 exec, -1
M 61
ad 84,1200
W 0
M 62
M 63
M 64
W 1
M 65
M 66
M 67
WL 3
AN 104,106
AN 105,107
M 68
SH 106
AN 106,106
M 69
WL 2
AN 107,107
AN 108,110
M 70
AN 109,111
SH 110
M 71
R 78
M 82
AN 110,110
AN 111,111
M 83
TY 114,2056
TY 118,15880
M 80
W 2
M 81
M 78
M 79
mw 27
M 76
ad 84,1206
W 0
M 77
M 74
M 75
W 1
M 72
WL 1
AN 112,114
AN 113,115
M 73
R 80
M 84
SH 114
AN 114,114
M 85
WL 2
AN 115,115
AN 116,118
M 86
AN 117,119
SH 118
M 87
AN 118,118
AN 119,119
M 88
TY 122,3592
TY 126,17416
M 89
W 2
UL 6,30
M 90
UH 7,30
AR 8,132
M 91
AR 9,156
AD 8,6
M 92
mw 48
AD 9,7
AW 132,8
M 93
ad 84,1248
W 0
M 94
AW 156,9
UL 6,31
M 95
R 82
M 106
W 1
UH 7,31
M 107
WL 3
AN 120,122
AN 121,123
M 104
SH 122
AN 122,122
M 105
WL 2
AN 123,123
AN 124,126
M 102
AN 125,127
SH 126
M 103
AN 126,126
AN 127,127
M 100
W 2
AR 8,180
M 101
AD 8,6
AD 136,7
M 98
AW 180,8
UL 6,32
M 99
mw 51
UH 7,32
AR 8,133
M 96
ad 84,1254
W 0
M 97
R 0
M 108
AR 9,157
AD 8,6
M 109
W 1
AD 9,7
M 110
AW 133,8
AW 157,9
M 111
UL 6,33
UH 7,33
M 112
W 2
AR 8,181
M 113
AD 8,6
AD 137,7
M 114
AW 181,8
UL 6,34
M 115
bc 49,14,66,60
UH 7,34
M 116
mh 18
AR 8,134
AR 9,158
M 117
ad 66,2688
H 0
M 118
AD 8,6
AD 9,7
M 119
WL 0
R 2
M 120
DG 1024
M 121
AW 134,8
AW 158,9
M 133
UL 6,35
UH 7,35
M 132
TX 58,0
TX 62,13824
M 134
bc 48,14,64,56
AR 8,182
M 135
mh 21
AD 8,6
AD 138,7
M 123
ad 64,2688
H 0
M 122
TY 66,512
TY 70,14336
M 124
AW 182,8
UL 6,36
M 125
H 1
UH 7,36
M 137
AR 8,135
AR 9,159
M 136
TX 74,1536
TX 78,15360
M 138
WL 4
Q 0
M 139
Q 2
M 127
Q 4
M 126
TY 82,2048
TY 86,15872
M 128
Q 6
M 129
WL 4
Q 8
M 141
Q 10
M 140
TX 90,3072
TX 94,16896
M 142
Q 12
M 143
R 6
M 131
Q 14
M 130
TY 98,3584
TY 102,17408
M 0
R 4
M 1
WL 10
Q 16
M 13
Q 18
M 12
Q 20
M 14
Q 22
M 15
WL 8
Q 24
M 3
Q 26
M 2
Q 28
M 4
Q 30
M 5
WL 6
Q 32
M 17
Q 34
M 16
Q 36
M 18
Q 38
M 19
WL 2
Q 40
M 7
Q 42
M 6
Q 44
M 8
Q 46
M 9
H 2
AD 8,6
M 21
AD 9,7
AW 135,8
M 20
AW 159,9
UL 6,37
M 22
UH 7,37
AR 8,183
M 23
R 10
M 11
AD 8,6
AD 139,7
M 10
WL 2
R 8
M 34
AW 183,8
M 35
M 32
M 33
M 30
M 31
M 28
M 29
M 26
M 27
WV 5
M 24
WL 0
FP
M 25
M 36
ae 44,68,0
M 37
s_bitcmp1_b32 s50, 2
s_cselect_b32 s51, 24576, 0
v_add_u32 v47, s51, v3
M 38
XL 14,128
M 39
M 40
M 41
M 42
XL 16,896
M 43
M 44
M 45
FR
M 46
M 47
M 58
XL 18,1664
M 59
M 56
M 57
M 54
M 55
WL 0
FQ 32
M 52
XL 20,2432
M 53
M 50
FZ 32
R 24
M 51
)"
R"(mw 72
R 26
M 48
TY 106,5128
TY 110,18952
M 49
R 28
M 60
ad 84,1296
W 0
M 61
M 62
M 63
W 1
M 64
M 65
M 66
W 2
M 67
WL 3
AN 104,106
AN 105,107
M 68
WL 2
SH 106
AN 106,106
M 69
mw 75
AN 107,107
AN 108,110
M 70
AN 109,111
SH 110
M 71
R 30
M 82
AN 110,110
AN 111,111
M 83
TY 114,6664
TY 118,20488
M 80
ad 84,1302
W 0
M 81
M 78
M 79
W 1
M 76
M 77
M 74
W 2
M 75
M 72
WL 1
AN 112,114
AN 113,115
M 73
R 32
M 84
mw 0
SH 114
AN 114,114
M 85
WL 2
AN 115,115
AN 116,118
M 86
AN 117,119
SH 118
M 87
AN 118,118
AN 119,119
M 88
TY 122,8200
TY 126,22024
M 89
ad 84,1344
W 0
M 90
UL 6,38
UH 7,38
M 91
AR 8,136
AR 9,160
M 92
W 1
AD 8,6
M 93
AD 9,7
AW 136,8
M 94
AW 160,9
UL 6,39
M 95
R 34
M 106
W 2
UH 7,39
M 107
WL 3
AN 120,122
AN 121,123
M 104
WL 2
SH 122
AN 122,122
M 105
mw 3
AN 123,123
AN 124,126
M 102
AN 125,127
SH 126
M 103
AN 126,126
AN 127,127
M 100
ad 84,1350
W 0
M 101
AR 8,184
AD 8,6
M 98
AD 140,7
AW 184,8
M 99
W 1
UL 6,40
M 96
UH 7,40
AR 8,137
M 97
R 48
M 108
W 2
AR 9,161
M 109
AD 8,6
AD 9,7
M 110
AW 137,8
AW 161,9
M 111
bc 49,15,66,60
UL 6,41
M 112
mh 0
UH 7,41
AR 8,185
M 113
ad 66,2880
H 0
M 114
AD 8,6
AD 141,7
M 115
AW 185,8
UL 6,42
M 116
DG 1024
M 117
UH 7,42
AR 8,138
M 118
AR 9,162
AD 8,6
M 119
WL 0
R 50
M 120
bc 48,15,64,56
AD 9,7
M 121
mh 3
AW 138,8
AW 162,9
M 133
ad 64,2880
H 0
M 132
TX 58,4608
TX 62,18432
M 134
UL 6,43
UH 7,43
M 135
H 1
AR 8,186
M 123
AD 8,6
AD 142,7
M 122
TY 66,5120
TY 70,18944
M 124
H 2
AW 186,8
M 125
UL 6,44
UH 7,44
M 137
AR 8,139
AR 9,163
M 136
TX 74,6144
TX 78,19968
PA 10240
DF
M 138
WL 4
Q 0
M 139
Q 2
M 127
Q 4
M 126
TY 82,6656
TY 86,20480
M 128
Q 6
M 129
WL 4
Q 8
M 141
Q 10
M 140
TX 90,7680
TX 94,21504
M 142
Q 12
M 143
R 54
M 131
Q 14
M 130
TY 98,8192
TY 102,22016
M 0
R 52
M 1
WL 10
Q 16
M 13
Q 18
M 12
Q 20
M 14
Q 22
M 15
WL 8
Q 24
M 3
Q 26
M 2
Q 28
M 4
Q 30
M 5
WL 6
Q 32
M 17
Q 34
M 16
Q 36
M 18
Q 38
M 19
WL 2
Q 40
M 7
Q 42
M 6
Q 44
M 8
Q 46
M 9
AD 8,6
AD 9,7
M 21
AW 139,8
AW 163,9
M 20
UL 6,45
UH 7,45
M 22
AR 8,187
AD 8,6
M 23
R 58
M 11
AD 143,7
AW 187,8
M 10
WL 2
R 56
M 34
M 35
M 32
M 33
M 30
M 31
M 28
M 29
M 26
M 27
WV 6
M 24
WL 0
FP
M 25
M 36
M 37
M 38
ae 44,68,0
M 39
s_bfe_u32 s51, s50, 0x10003
v_mad_i32_i24 v47, v49, s51, v48
XL 22,128
M 40
M 41
M 42
M 43
M 44
XL 24,896
M 45
FR
M 46
M 47
M 58
M 59
M 56
XL 26,1664
M 57
M 54
M 55
WL 0
FQ 36
M 52
M 53
M 50
FZ 36
R 72
M 51
mw 24
R 74
M 48
TY 106,9736
TY 110,23560
M 49
R 76
M 60
ad 84,1392
W 0
M 61
XL 28,2432
M 62
M 63
W 1
M 64
M 65
M 66
W 2
M 67
WL 3
AN 104,106
AN 105,107
M 68
WL 2
SH 106
AN 106,106
M 69
mw 27
AN 107,107
AN 108,110
M 70
AN 109,111
SH 110
M 71
R 78
M 82
AN 110,110
AN 111,111
M 83
TY 114,11272
TY 118,25096
M 80
ad 84,1398
W 0
M 81
M 78
M 79
W 1
M 76
M 77
M 74
W 2
M 75
M 72
WL 1
)"
R"(AN 112,114
AN 113,115
M 73
R 80
M 84
mw 48
SH 114
AN 114,114
M 85
WL 2
AN 115,115
AN 116,118
M 86
AN 117,119
SH 118
M 87
AN 118,118
AN 119,119
M 88
TY 122,12808
TY 126,26632
M 89
ad 84,1440
W 0
M 90
M 91
M 92
W 1
UL 6,14
M 93
UH 7,14
AR 8,140
M 94
AR 9,164
AD 8,6
M 95
R 82
M 106
W 2
AD 9,7
M 107
WL 3
AN 120,122
AN 121,123
M 104
WL 2
SH 122
AN 122,122
M 105
mw 51
AN 123,123
AN 124,126
M 102
AN 125,127
SH 126
M 103
AN 126,126
AN 127,127
M 100
ad 84,1446
W 0
M 101
AW 140,8
AW 164,9
M 98
UL 6,15
UH 7,15
M 99
W 1
AR 8,188
M 96
AD 8,6
AD 144,7
M 97
R 0
M 108
W 2
AW 188,8
M 109
UL 6,16
UH 7,16
M 110
AR 8,141
AR 9,165
M 111
bc 49,16,66,60
AD 8,6
M 112
mh 9
AD 9,7
AW 141,8
M 113
ad 66,3072
H 0
M 114
AW 165,9
UL 6,17
M 115
UH 7,17
AR 8,189
M 116
DG 1024
M 117
AD 8,6
AD 145,7
M 118
AW 189,8
UL 6,18
M 119
WL 0
R 2
M 120
bc 48,16,64,56
UH 7,18
M 121
mh 12
AR 8,142
AR 9,166
M 133
ad 64,3072
H 0
M 132
TX 58,9216
TX 62,23040
M 134
AD 8,6
AD 9,7
M 135
H 1
AW 142,8
M 123
AW 166,9
UL 6,19
M 122
TY 66,9728
TY 70,23552
M 124
H 2
UH 7,19
M 125
AR 8,190
AD 8,6
M 137
AD 146,7
AW 190,8
M 136
TX 74,10752
TX 78,24576
M 138
WL 4
Q 0
M 139
Q 2
M 127
Q 4
M 126
TY 82,11264
TY 86,25088
M 128
Q 6
M 129
WL 4
Q 8
M 141
Q 10
M 140
TX 90,12288
TX 94,26112
M 142
Q 12
M 143
R 6
M 131
Q 14
M 130
TY 98,12800
TY 102,26624
M 0
R 4
M 1
WL 10
Q 16
M 13
Q 18
M 12
Q 20
M 14
Q 22
M 15
WL 8
Q 24
M 3
Q 26
M 2
Q 28
M 4
Q 30
M 5
WL 6
Q 32
M 17
Q 34
M 16
Q 36
M 18
Q 38
M 19
WL 2
Q 40
M 7
Q 42
M 6
Q 44
M 8
Q 46
M 9
UL 6,20
UH 7,20
M 21
AR 8,143
AR 9,167
M 20
AD 8,6
AD 9,7
M 22
AW 143,8
AW 167,9
M 23
R 10
M 11
UL 6,21
UH 7,21
M 10
WL 2
R 8
M 34
AR 8,191
AD 8,6
M 35
AD 147,7
AW 191,8
M 32
M 33
M 30
M 31
M 28
M 29
M 26
M 27
WV 5
M 24
WL 0
FP
M 25
M 36
M 37
M 38
M 39
M 40
ae 44,68,96
M 41
s_bitcmp1_b32 s50, 4
s_cselect_b32 s51, 24576, 0
v_add_u32 v47, s51, v3
M 42
XL 30,128
M 43
M 44
M 45
FR
M 46
XL 32,896
M 47
M 58
M 59
M 56
M 57
M 54
XL 34,1664
M 55
WL 0
FQ 40
M 52
M 53
M 50
FZ 40
R 24
M 51
mw 72
R 26
M 48
TY 106,520
TY 110,14344
M 49
R 28
M 60
ad 84,1488
W 0
M 61
XL 36,2432
v_lshlrev_b32 v55, 4, v0
M 62
FL
M 63
W 1
M 64
M 65
M 66
W 2
M 67
WL 3
AN 104,106
AN 105,107
M 68
WL 2
SH 106
AN 106,106
M 69
mw 75
AN 107,107
AN 108,110
M 70
AN 109,111
SH 110
M 71
R 30
M 82
AN 110,110
AN 111,111
M 83
TY 114,2056
TY 118,15880
M 80
ad 84,1494
W 0
M 81
M 78
M 79
W 1
M 76
M 77
M 74
W 2
M 75
M 72
WL 1
AN 112,114
AN 113,115
M 73
R 32
M 84
mw 0
SH 114
AN 114,114
M 85
WL 2
AN 115,115
AN 116,118
M 86
AN 117,119
SH 118
M 87
AN 118,118
AN 119,119
M 88
TY 122,3592
TY 126,17416
M 89
ad 84,1536
W 0
M 90
M 91
M 92
W 1
M 93
M 94
UL 6,22
UH 7,22
M 95
R 34
M 106
W 2
AR 8,144
M 107
WL 3
AN 120,122
AN 121,123
M 104
WL 2
SH 122
AN 122,122
M 105
mw 3
AN 123,123
AN 124,126
M 102
AN 125,127
SH 126
M 103
AN 126,126
)"
R"(AN 127,127
M 100
ad 84,1542
W 0
M 101
AR 9,168
AD 8,6
M 98
AD 9,7
AW 144,8
M 99
W 1
AW 168,9
M 96
UL 6,23
UH 7,23
M 97
R 48
M 108
W 2
AR 8,192
M 109
AD 8,6
AD 148,7
M 110
AW 192,8
UL 6,24
M 111
bc 49,17,66,60
UH 7,24
M 112
mh 18
AR 8,145
AR 9,169
M 113
ad 66,3264
H 0
M 114
AD 8,6
AD 9,7
M 115
AW 145,8
AW 169,9
M 116
DG 1024
M 117
UL 6,25
UH 7,25
M 118
AR 8,193
AD 8,6
M 119
WL 0
R 50
M 120
bc 48,17,64,56
AD 149,7
M 121
mh 21
AW 193,8
UL 6,26
M 133
ad 64,3264
H 0
M 132
TX 58,0
TX 62,13824
M 134
UH 7,26
AR 8,146
M 135
H 1
AR 9,170
M 123
AD 8,6
AD 9,7
M 122
TY 66,512
TY 70,14336
M 124
H 2
AW 146,8
M 125
AW 170,9
UL 6,27
M 137
UH 7,27
AR 8,194
M 136
TX 74,1536
TX 78,15360
M 138
WL 4
Q 0
M 139
Q 2
M 127
Q 4
M 126
TY 82,2048
TY 86,15872
M 128
Q 6
M 129
WL 4
Q 8
M 141
Q 10
M 140
TX 90,3072
TX 94,16896
M 142
Q 12
M 143
R 54
M 131
Q 14
M 130
TY 98,3584
TY 102,17408
M 0
R 52
M 1
WL 10
Q 16
M 13
Q 18
M 12
Q 20
M 14
Q 22
M 15
WL 8
Q 24
M 3
Q 26
M 2
Q 28
M 4
Q 30
M 5
WL 6
Q 32
M 17
Q 34
M 16
Q 36
M 18
Q 38
M 19
WL 2
Q 40
M 7
Q 42
M 6
Q 44
M 8
Q 46
M 9
AD 8,6
AD 150,7
M 21
AW 194,8
UL 6,28
M 20
UH 7,28
AR 8,147
M 22
AR 9,171
AD 8,6
M 23
R 58
M 11
AD 9,7
AW 147,8
M 10
WL 2
R 56
M 34
AW 171,9
UL 6,29
M 35
UH 7,29
AR 8,195
M 32
AD 8,6
AD 151,7
M 33
AW 195,8
M 30
M 31
M 28
M 29
M 26
M 27
WV 5
M 24
WL 0
FP
M 25
M 36
M 37
M 38
M 39
M 40
M 41
M 42
ae 44,68,96
M 43
s_bfe_u32 s51, s50, 0x10005
v_mad_i32_i24 v47, v49, s51, v48
XL 38,128
M 44
M 45
FR
M 46
M 47
M 58
XL 40,896
M 59
M 56
M 57
M 54
M 55
WL 0
FQ 44
M 52
XL 42,1664
M 53
M 50
FZ 44
R 72
M 51
mw 24
R 74
M 48
TY 106,5128
TY 110,18952
M 49
R 76
M 60
ad 84,1584
W 0
M 61
XL 44,2432
M 62
M 63
W 1
M 64
M 65
M 66
W 2
M 67
WL 3
AN 104,106
AN 105,107
M 68
WL 2
SH 106
AN 106,106
M 69
mw 27
AN 107,107
AN 108,110
M 70
AN 109,111
SH 110
M 71
R 78
M 82
AN 110,110
AN 111,111
M 83
TY 114,6664
TY 118,20488
M 80
ad 84,1590
W 0
M 81
M 78
M 79
W 1
M 76
M 77
M 74
W 2
M 75
M 72
WL 1
AN 112,114
AN 113,115
M 73
R 80
M 84
mw 48
SH 114
AN 114,114
M 85
WL 2
AN 115,115
AN 116,118
M 86
AN 117,119
SH 118
M 87
AN 118,118
AN 119,119
M 88
TY 122,8200
TY 126,22024
M 89
ad 84,1632
W 0
M 90
M 91
M 92
W 1
M 93
M 94
M 95
R 82
M 106
W 2
UL 6,30
M 107
WL 3
AN 120,122
AN 121,123
M 104
WL 2
SH 122
AN 122,122
M 105
mw 51
AN 123,123
AN 124,126
M 102
AN 125,127
SH 126
M 103
AN 126,126
AN 127,127
M 100
ad 84,1638
W 0
M 101
UH 7,30
AR 8,148
M 98
AR 9,172
AD 8,6
M 99
W 1
AD 9,7
M 96
AW 148,8
AW 172,9
M 97
R 0
M 108
W 2
UL 6,31
M 109
UH 7,31
AR 8,196
M 110
AD 8,6
AD 152,7
M 111
bc 49,18,66,60
AW 196,8
M 112
mh 0
UL 6,32
UH 7,32
M 113
ad 66,3456
H 0
M 114
AR 8,149
AR 9,173
M 115
AD 8,6
AD 9,7
M 116
DG 1024
M 117
AW 149,8
AW 173,9
M 118
UL 6,33
UH 7,33
M 119
WL 0
R 2
M 120
bc 48,18,64,56
AR 8,197
M 121
mh 3
)"
R"(AD 8,6
AD 153,7
M 133
ad 64,3456
H 0
M 132
TX 58,4608
TX 62,18432
M 134
AW 197,8
UL 6,34
M 135
H 1
UH 7,34
M 123
AR 8,150
AR 9,174
M 122
TY 66,5120
TY 70,18944
M 124
H 2
AD 8,6
M 125
AD 9,7
AW 150,8
M 137
AW 174,9
UL 6,35
M 136
TX 74,6144
TX 78,19968
M 138
WL 4
Q 0
M 139
Q 2
M 127
Q 4
M 126
TY 82,6656
TY 86,20480
M 128
Q 6
M 129
WL 4
Q 8
M 141
Q 10
M 140
TX 90,7680
TX 94,21504
M 142
Q 12
M 143
R 6
M 131
Q 14
M 130
TY 98,8192
TY 102,22016
M 0
R 4
M 1
WL 10
Q 16
M 13
Q 18
M 12
Q 20
M 14
Q 22
M 15
WL 8
Q 24
M 3
Q 26
M 2
Q 28
M 4
Q 30
M 5
WL 6
Q 32
M 17
Q 34
M 16
Q 36
M 18
Q 38
M 19
WL 2
Q 40
M 7
Q 42
M 6
Q 44
M 8
Q 46
M 9
UH 7,35
AR 8,198
M 21
AD 8,6
AD 154,7
M 20
AW 198,8
UL 6,36
M 22
UH 7,36
AR 8,151
M 23
R 10
M 11
AR 9,175
AD 8,6
M 10
WL 2
R 8
M 34
AD 9,7
AW 151,8
M 35
AW 175,9
UL 6,37
M 32
UH 7,37
AR 8,199
M 33
v_cmp_ne_u32 vcc, 2, v54
AD 8,6
M 30
AD 155,7
AW 199,8
M 31
M 28
FC
M 29
M 26
M 27
WV 5
M 24
WL 0
FP
M 25
M 36
M 37
M 38
M 39
M 40
M 41
M 42
M 43
M 44
M 45
FR
M 46
M 47
M 58
M 59
M 56
M 57
M 54
M 55
WL 0
FQ 48
M 52
M 53
M 50
FZ 48
R 24
M 51
mw 72
R 26
M 48
TY 106,9736
TY 110,23560
M 49
R 28
M 60
ad 84,1680
W 0
M 61
M 62
M 63
W 1
M 64
M 65
M 66
W 2
M 67
WL 3
AN 104,106
AN 105,107
M 68
WL 2
SH 106
AN 106,106
M 69
mw 75
AN 107,107
AN 108,110
M 70
AN 109,111
SH 110
M 71
R 30
M 82
AN 110,110
AN 111,111
M 83
TY 114,11272
TY 118,25096
M 80
ad 84,1686
W 0
M 81
ae 44,70,0
M 78
s_bitcmp1_b32 s50, 0
s_cselect_b32 s51, 24576, 0
v_add_u32 v47, s51, v3
M 79
W 1
XL 14,256
M 76
XL 16,1024
M 77
M 74
W 2
M 75
M 72
WL 1
AN 112,114
AN 113,115
M 73
R 32
M 84
mw 0
SH 114
AN 114,114
M 85
WL 2
AN 115,115
AN 116,118
M 86
AN 117,119
SH 118
M 87
AN 118,118
AN 119,119
M 88
TY 122,12808
TY 126,26632
M 89
ad 84,1728
W 0
M 90
XL 18,1792
M 91
XL 20,2560
M 92
W 1
M 93
M 94
M 95
R 34
M 106
W 2
M 107
WL 3
AN 120,122
AN 121,123
M 104
WL 2
SH 122
AN 122,122
M 105
mw 3
AN 123,123
AN 124,126
M 102
AN 125,127
SH 126
M 103
AN 126,126
AN 127,127
M 100
ad 84,1734
W 0
M 101
UL 6,38
UH 7,38
M 98
AR 8,152
AR 9,176
M 99
W 1
AD 8,6
M 96
AD 9,7
AW 152,8
M 97
R 48
M 108
W 2
AW 176,9
M 109
UL 6,39
UH 7,39
M 110
AD 132,6
AD 156,7
M 111
bc 49,19,66,60
UL 6,40
M 112
mh 9
UH 7,40
AR 8,153
M 113
ad 66,3648
H 0
M 114
AR 9,177
AD 8,6
M 115
AD 9,7
AW 153,8
M 116
DG 1024
M 117
AW 177,9
UL 6,41
M 118
UH 7,41
AD 133,6
M 119
WL 0
R 50
M 120
bc 48,19,64,56
AD 157,7
M 121
mh 12
UL 6,42
UH 7,42
M 133
ad 64,3648
H 0
M 132
TX 58,9216
TX 62,23040
M 134
AR 8,154
AR 9,178
M 135
H 1
AD 8,6
M 123
AD 9,7
AW 154,8
M 122
TY 66,9728
TY 70,23552
M 124
H 2
AW 178,9
M 125
UL 6,43
UH 7,43
M 137
AD 134,6
AD 158,7
M 136
TX 74,10752
TX 78,24576
v_min_u32 v52, 752, v0
v_lshrrev_b32 v52, 1, v52
v_add_u32 v53, 0x1800, v52
global_load_dwordx2 a[200:201], v52, s[94:95] offset:-2048 sc1
global_load_dwordx2 a[202:203], v52, s[94:95] offset:-512 sc1
global_load_dwordx2 a[204:205], v52, s[94:95] offset:1024 sc1
global_load_dwordx2 a[206:207], v52, s[94:95] offset:2560 sc1
global_load_dwordx2 a[208:209], v53, s[94:95] offset:-2048 sc1
global_load_dwordx2 a[210:211], v53, s[94:95] offset:-512 sc1
global_load_dwordx2 a[212:213], v53, s[94:95] offset:1024 sc1
global_load_dwordx2 a[214:215], v53, s[94:95] offset:2560 sc1
PA 10240
DF
M 138
WL 4
Q 0
M 139
Q 2
M 127
Q 4
M 126
TY 82,11264
TY 86,25088
)"
R"(M 128
Q 6
M 129
WL 4
Q 8
M 141
Q 10
M 140
TX 90,12288
TX 94,26112
M 142
Q 12
M 143
R 54
M 131
Q 14
M 130
TY 98,12800
TY 102,26624
M 0
R 52
M 1
WL 10
Q 16
M 13
Q 18
M 12
Q 20
M 14
Q 22
M 15
WL 8
Q 24
M 3
Q 26
M 2
Q 28
M 4
Q 30
M 5
WL 6
Q 32
M 17
Q 34
M 16
Q 36
M 18
Q 38
M 19
WL 2
Q 40
M 7
Q 42
M 6
Q 44
M 8
Q 46
M 9
UL 6,44
UH 7,44
M 21
AR 8,155
AR 9,179
M 20
AD 8,6
AD 9,7
M 22
AW 155,8
AW 179,9
M 23
R 58
M 11
UL 6,45
UH 7,45
M 10
WL 2
R 56
M 34
AD 135,6
AD 159,7
M 35
M 32
M 33
M 30
M 31
M 28
M 29
M 26
M 27
WV 14
M 24
WL 0
FP
M 25
M 36
M 37
M 38
M 39
M 40
M 41
M 42
M 43
M 44
M 45
FR
M 46
M 47
M 58
M 59
M 56
M 57
M 54
M 55
WL 0
FQ 52
M 52
M 53
M 50
FZ 52
R 72
M 51
mw 24
R 74
M 48
TY 106,520
TY 110,14344
M 49
R 76
M 60
ad 84,1776
W 0
M 61
M 62
M 63
W 1
M 64
M 65
M 66
W 2
M 67
WL 3
AN 104,106
AN 105,107
M 68
WL 2
SH 106
AN 106,106
M 69
mw 27
AN 107,107
AN 108,110
M 70
AN 109,111
SH 110
M 71
R 78
M 82
AN 110,110
AN 111,111
M 83
TY 114,2056
TY 118,15880
M 80
ad 84,1782
W 0
M 81
ae 44,70,0
M 78
s_bfe_u32 s51, s50, 0x10001
v_mad_i32_i24 v47, v49, s51, v48
XL 22,256
M 79
W 1
M 76
M 77
M 74
W 2
XL 24,1024
M 75
M 72
WL 1
AN 112,114
AN 113,115
M 73
R 80
M 84
mw 48
SH 114
AN 114,114
M 85
WL 2
AN 115,115
AN 116,118
M 86
AN 117,119
SH 118
M 87
AN 118,118
AN 119,119
M 88
TY 122,3592
TY 126,17416
M 89
ad 84,1824
W 0
M 90
XL 26,1792
M 91
M 92
W 1
XL 28,2560
M 93
M 94
M 95
R 82
M 106
W 2
M 107
WL 3
AN 120,122
AN 121,123
M 104
WL 2
SH 122
AN 122,122
M 105
mw 51
AN 123,123
AN 124,126
M 102
AN 125,127
SH 126
M 103
AN 126,126
AN 127,127
M 100
ad 84,1830
W 0
M 101
M 98
M 99
W 1
M 96
M 97
R 0
M 108
W 2
M 109
M 110
M 111
bc 49,20,66,60
M 112
mh 18
M 113
ad 66,3840
H 0
M 114
M 115
M 116
DG 1024
M 117
M 118
M 119
WL 2
R 2
M 120
bc 48,20,64,56
M 121
mh 21
M 133
ad 64,3840
H 0
M 132
TX 58,0
TX 62,13824
M 134
M 135
H 1
M 123
UL 6,14
UH 7,14
M 122
TY 66,512
TY 70,14336
M 124
H 2
AD 160,6
M 125
AD 184,7
UL 6,15
M 137
UH 7,15
AD 208,6
M 136
TX 74,1536
TX 78,15360
M 138
WL 4
Q 0
M 139
Q 2
M 127
Q 4
M 126
TY 82,2048
TY 86,15872
M 128
Q 6
M 129
WL 4
Q 8
M 141
Q 10
M 140
TX 90,3072
TX 94,16896
M 142
Q 12
M 143
R 6
M 131
Q 14
M 130
TY 98,3584
TY 102,17408
M 0
R 4
M 1
WL 10
Q 16
M 13
Q 18
M 12
Q 20
M 14
Q 22
M 15
WL 8
Q 24
M 3
Q 26
M 2
Q 28
M 4
Q 30
M 5
WL 6
Q 32
M 17
Q 34
M 16
Q 36
M 18
Q 38
M 19
WL 2
Q 40
M 7
Q 42
M 6
Q 44
M 8
Q 46
M 9
AD 232,7
UL 6,16
M 21
UH 7,16
AD 161,6
M 20
AD 185,7
UL 6,17
M 22
UH 7,17
AD 209,6
M 23
R 10
M 11
AD 233,7
UL 6,18
M 10
WL 2
R 8
M 34
UH 7,18
AD 162,6
M 35
AD 186,7
UL 6,19
M 32
UH 7,19
AD 210,6
M 33
AD 234,7
UL 6,20
M 30
UH 7,20
AD 163,6
M 31
AD 187,7
)"
R"(UL 6,21
M 28
UH 7,21
AD 211,6
M 29
AD 235,7
M 26
M 27
WV 5
M 24
WL 0
FP
M 25
M 36
M 37
M 38
M 39
M 40
M 41
M 42
M 43
M 44
M 45
FR
M 46
M 47
M 58
M 59
M 56
M 57
M 54
M 55
WL 0
FQ 56
M 52
M 53
M 50
FZ 56
R 24
M 51
mw 72
R 26
M 48
TY 106,5128
TY 110,18952
M 49
R 28
M 60
ad 84,1872
W 0
M 61
M 62
M 63
W 1
M 64
M 65
M 66
W 2
M 67
WL 3
AN 104,106
AN 105,107
M 68
WL 2
SH 106
AN 106,106
M 69
mw 75
AN 107,107
AN 108,110
M 70
AN 109,111
SH 110
M 71
R 30
M 82
AN 110,110
AN 111,111
M 83
TY 114,6664
TY 118,20488
M 80
ad 84,1878
W 0
M 81
M 78
M 79
W 1
M 76
ae 44,68,0
M 77
s_bitcmp1_b32 s50, 2
s_cselect_b32 s51, 24576, 0
v_add_u32 v47, s51, v3
M 74
W 2
XL 30,256
M 75
M 72
WL 1
AN 112,114
AN 113,115
M 73
R 32
M 84
mw 0
SH 114
AN 114,114
M 85
WL 2
AN 115,115
AN 116,118
M 86
AN 117,119
SH 118
M 87
AN 118,118
AN 119,119
M 88
TY 122,8200
TY 126,22024
M 89
ad 84,1920
W 0
M 90
XL 32,1024
M 91
XL 34,1792
M 92
W 1
M 93
M 94
M 95
R 34
M 106
W 2
XL 36,2560
M 107
WL 3
AN 120,122
AN 121,123
M 104
WL 2
SH 122
AN 122,122
M 105
mw 3
AN 123,123
AN 124,126
M 102
AN 125,127
SH 126
M 103
AN 126,126
AN 127,127
M 100
ad 84,1926
W 0
M 101
M 98
M 99
W 1
M 96
M 97
R 48
M 108
W 2
M 109
M 110
M 111
bc 49,21,66,60
M 112
mh 0
M 113
ad 66,4032
H 0
M 114
M 115
M 116
DG 1024
M 117
M 118
M 119
WL 2
R 50
M 120
bc 48,21,64,56
M 121
mh 3
v_mov_b32 v54, s39
v_mul_f32 v54, 0xb62aaa8f, v54
M 133
ad 64,4032
H 0
M 132
TX 58,4608
TX 62,18432
M 134
v_add_u32 v52, %[bm], v52
v_bfe_u32 v53, v52, 6, 2
M 135
H 1
v_and_b32 v55, 63, v52
M 123
v_add_u32 v53, v53, v55
v_and_b32 v52, 0x700, v52
M 122
TY 66,5120
TY 70,18944
M 124
H 2
v_lshl_add_u32 v52, v53, 2, v52
M 125
v_add_u32 v52, %[sb], v52
AR 12,214
M 137
AR 13,215
AR 8,208
M 136
TX 74,6144
TX 78,19968
M 138
WL 4
Q 0
M 139
Q 2
M 127
Q 4
M 126
TY 82,6656
TY 86,20480
M 128
Q 6
M 129
WL 4
Q 8
M 141
Q 10
M 140
TX 90,7680
TX 94,21504
M 142
Q 12
M 143
R 54
M 131
Q 14
M 130
TY 98,8192
TY 102,22016
M 0
R 52
M 1
WL 10
Q 16
M 13
Q 18
M 12
Q 20
M 14
Q 22
M 15
WL 8
Q 24
M 3
Q 26
M 2
Q 28
M 4
Q 30
M 5
WL 6
Q 32
M 17
Q 34
M 16
Q 36
M 18
Q 38
M 19
WL 2
Q 40
M 7
Q 42
M 6
Q 44
M 8
Q 46
M 9
AR 9,209
v_add_f32 v12, v12, v8
M 21
v_add_f32 v13, v13, v9
AR 8,210
M 20
AR 9,211
v_add_f32 v12, v12, v8
M 22
v_add_f32 v13, v13, v9
AR 8,212
M 23
R 58
M 11
AR 9,213
v_add_f32 v12, v12, v8
M 10
WL 2
R 56
M 34
v_add_f32 v13, v13, v9
AR 6,206
M 35
AR 7,207
v_add_f32 v12, v12, v54
M 32
v_add_f32 v13, v13, v54
v_fma_f32 v8, v6, s86, v12
M 33
ds_write_b32 v52, v8 offset:1024
v_fma_f32 v9, v7, s86, v13
M 30
ds_write_b32 v52, v9 offset:1040
AR 8,200
M 31
AR 9,201
v_add_f32 v6, v6, v8
M 28
v_add_f32 v7, v7, v9
v_fma_f32 v8, v6, s86, v12
M 29
ds_write_b32 v52, v8 offset:2560
v_fma_f32 v9, v7, s86, v13
M 26
ds_write_b32 v52, v9 offset:2576
AR 8,202
M 27
WV 5
AR 9,203
v_add_f32 v6, v6, v8
M 24
WL 4
FP
v_add_f32 v7, v7, v9
M 25
v_fma_f32 v8, v6, s86, v12
v_fma_f32 v9, v7, s86, v13
M 36
ds_write_b32 v52, v8 offset:4096
ds_write_b32 v52, v9 offset:4112
M 37
AR 8,204
AR 9,205
M 38
v_add_f32 v6, v6, v8
v_add_f32 v7, v7, v9
M 39
v_fma_f32 v8, v6, s86, v12
v_fma_f32 v9, v7, s86, v13
M 40
ds_write_b32 v52, v8 offset:5632
ds_write_b32 v52, v9 offset:5648
M 41
UL 6,22
UH 7,22
M 42
AD 164,6
AD 188,7
M 43
UL 6,23
UH 7,23
M 44
AD 212,6
AD 236,7
M 45
FR
UL 6,24
M 46
UH 7,24
AD 165,6
M 47
AD 189,7
UL 6,25
M 58
UH 7,25
AD 213,6
M 59
AD 237,7
UL 6,26
M 56
UH 7,26
AD 166,6
M 57
AD 190,7
UL 6,27
M 54
UH 7,27
AD 214,6
M 55
WL 0
FQ 60
)"
R"(AD 238,7
M 52
UL 6,28
UH 7,28
M 53
AD 167,6
AD 191,7
M 50
FZ 60
R 72
M 51
mw 24
R 74
M 48
TY 106,9736
TY 110,23560
M 49
R 76
M 60
ad 84,1968
W 0
M 61
UL 6,29
UH 7,29
M 62
AD 215,6
AD 239,7
M 63
W 1
M 64
M 65
M 66
W 2
M 67
WL 3
AN 104,106
AN 105,107
M 68
WL 2
SH 106
AN 106,106
M 69
mw 27
AN 107,107
AN 108,110
M 70
AN 109,111
SH 110
M 71
R 78
M 82
AN 110,110
AN 111,111
M 83
TY 114,11272
TY 118,25096
M 80
ad 84,1974
W 0
M 81
M 78
M 79
W 1
M 76
M 77
M 74
W 2
M 75
M 72
WL 1
AN 112,114
AN 113,115
M 73
R 80
M 84
mw 48
SH 114
AN 114,114
M 85
WL 2
AN 115,115
AN 116,118
M 86
AN 117,119
SH 118
M 87
AN 118,118
AN 119,119
M 88
TY 122,12808
TY 126,26632
M 89
ad 84,2016
W 0
M 90
ae 44,68,0
M 91
s_bfe_u32 s51, s50, 0x10003
v_mad_i32_i24 v47, v49, s51, v48
XL 38,256
M 92
W 1
XL 40,1024
M 93
M 94
XL 42,1792
M 95
R 82
M 106
W 2
M 107
WL 3
AN 120,122
AN 121,123
M 104
WL 2
SH 122
AN 122,122
M 105
mw 51
AN 123,123
AN 124,126
M 102
AN 125,127
SH 126
M 103
AN 126,126
AN 127,127
M 100
ad 84,2022
W 0
M 101
XL 44,2560
M 98
M 99
W 1
M 96
M 97
R 0
M 108
W 2
M 109
M 110
M 111
bc 49,22,66,60
M 112
mh 9
M 113
ad 66,4224
H 0
M 114
M 115
M 116
DG 1024
M 117
M 118
M 119
WL 2
R 2
M 120
bc 48,22,64,56
M 121
mh 12
M 133
ad 64,4224
H 0
M 132
TX 58,9216
TX 62,23040
M 134
M 135
H 1
M 123
M 122
TY 66,9728
TY 70,23552
M 124
H 2
M 125
M 137
M 136
TX 74,10752
TX 78,24576
M 138
WL 4
Q 0
M 139
Q 2
M 127
Q 4
M 126
TY 82,11264
TY 86,25088
M 128
Q 6
M 129
WL 4
Q 8
M 141
Q 10
M 140
TX 90,12288
TX 94,26112
M 142
Q 12
M 143
R 6
M 131
Q 14
M 130
TY 98,12800
TY 102,26624
M 0
R 4
M 1
WL 10
Q 16
M 13
Q 18
M 12
Q 20
M 14
Q 22
M 15
WL 8
Q 24
M 3
Q 26
M 2
Q 28
M 4
Q 30
M 5
WL 6
Q 32
M 17
Q 34
M 16
Q 36
M 18
Q 38
M 19
WL 2
Q 40
M 7
Q 42
M 6
Q 44
M 8
Q 46
M 9
UL 6,30
UH 7,30
M 21
AD 168,6
AD 192,7
M 20
UL 6,31
UH 7,31
M 22
AD 216,6
AD 240,7
M 23
R 10
M 11
UL 6,32
UH 7,32
M 10
WL 2
R 8
M 34
AD 169,6
AD 193,7
M 35
UL 6,33
UH 7,33
M 32
AD 217,6
AD 241,7
M 33
UL 6,34
UH 7,34
M 30
AD 170,6
AD 194,7
M 31
UL 6,35
UH 7,35
M 28
AD 218,6
AD 242,7
M 29
UL 6,36
UH 7,36
M 26
AD 171,6
AD 195,7
M 27
WV 5
UL 6,37
UH 7,37
M 24
WL 0
FP
AD 219,6
M 25
AD 243,7
M 36
M 37
M 38
M 39
M 40
M 41
M 42
M 43
M 44
M 45
FR
M 46
M 47
M 58
M 59
M 56
M 57
M 54
M 55
WL 0
FQ 64
M 52
M 53
M 50
FZ 64
R 24
M 51
mw 72
R 26
M 48
TY 106,520
TY 110,14344
M 49
R 28
M 60
ad 84,2064
W 0
M 61
M 62
M 63
W 1
M 64
M 65
M 66
W 2
M 67
WL 3
AN 104,106
AN 105,107
M 68
WL 2
SH 106
AN 106,106
M 69
mw 75
AN 107,107
AN 108,110
M 70
AN 109,111
SH 110
M 71
R 30
M 82
AN 110,110
AN 111,111
M 83
TY 114,2056
TY 118,15880
M 80
ad 84,2070
W 0
M 81
M 78
M 79
W 1
M 76
M 77
M 74
W 2
M 75
M 72
WL 1
AN 112,114
AN 113,115
M 73
R 32
M 84
mw 0
SH 114
AN 114,114
M 85
WL 2
AN 115,115
)"
R"(AN 116,118
M 86
AN 117,119
SH 118
M 87
AN 118,118
AN 119,119
M 88
TY 122,3592
TY 126,17416
M 89
ad 84,2112
W 0
M 90
ae 44,68,96
M 91
s_bitcmp1_b32 s50, 4
s_cselect_b32 s51, 24576, 0
v_add_u32 v47, s51, v3
M 92
W 1
XL 14,256
M 93
XL 16,1024
M 94
M 95
R 34
M 106
W 2
M 107
WL 3
AN 120,122
AN 121,123
M 104
WL 2
SH 122
AN 122,122
M 105
mw 3
AN 123,123
AN 124,126
M 102
AN 125,127
SH 126
M 103
AN 126,126
AN 127,127
M 100
ad 84,2118
W 0
M 101
XL 18,1792
M 98
XL 20,2560
M 99
W 1
M 96
M 97
R 48
M 108
W 2
M 109
M 110
M 111
bc 49,23,66,60
M 112
mh 18
M 113
ad 66,4416
H 0
M 114
M 115
M 116
DG 1024
M 117
M 118
M 119
WL 2
R 50
M 120
bc 48,23,64,56
M 121
mh 21
M 133
ad 64,4416
H 0
M 132
TX 58,0
TX 62,13824
M 134
M 135
H 1
M 123
M 122
TY 66,512
TY 70,14336
M 124
H 2
M 125
M 137
M 136
TX 74,1536
TX 78,15360
PA 10240
DF
M 138
WL 4
Q 0
M 139
Q 2
M 127
Q 4
M 126
TY 82,2048
TY 86,15872
M 128
Q 6
M 129
WL 4
Q 8
M 141
Q 10
M 140
TX 90,3072
TX 94,16896
M 142
Q 12
M 143
R 54
M 131
Q 14
M 130
TY 98,3584
TY 102,17408
M 0
R 52
M 1
WL 10
Q 16
M 13
Q 18
M 12
Q 20
M 14
Q 22
M 15
WL 8
Q 24
M 3
Q 26
M 2
Q 28
M 4
Q 30
M 5
WL 6
Q 32
M 17
Q 34
M 16
Q 36
M 18
Q 38
M 19
WL 2
Q 40
M 7
Q 42
M 6
Q 44
M 8
Q 46
M 9
UL 6,38
UH 7,38
M 21
AD 172,6
AD 196,7
M 20
UL 6,39
UH 7,39
M 22
AD 220,6
AD 244,7
M 23
R 58
M 11
UL 6,40
UH 7,40
M 10
WL 2
R 56
M 34
AD 173,6
AD 197,7
M 35
UL 6,41
UH 7,41
M 32
AD 221,6
AD 245,7
M 33
UL 6,42
UH 7,42
M 30
AD 174,6
AD 198,7
M 31
UL 6,43
UH 7,43
M 28
AD 222,6
AD 246,7
M 29
UL 6,44
UH 7,44
M 26
AD 175,6
AD 199,7
M 27
WV 6
UL 6,45
UH 7,45
M 24
WL 0
FP
AD 223,6
M 25
AD 247,7
M 36
M 37
M 38
M 39
M 40
M 41
M 42
M 43
M 44
M 45
FR
M 46
M 47
M 58
M 59
M 56
M 57
M 54
M 55
WL 0
FQ 68
M 52
M 53
M 50
FZ 68
R 72
M 51
mw 24
R 74
M 48
TY 106,5128
TY 110,18952
M 49
R 76
M 60
ad 84,2160
W 0
M 61
M 62
M 63
W 1
M 64
M 65
M 66
W 2
M 67
WL 3
AN 104,106
AN 105,107
M 68
WL 2
SH 106
AN 106,106
M 69
mw 27
AN 107,107
AN 108,110
M 70
AN 109,111
SH 110
M 71
R 78
M 82
AN 110,110
AN 111,111
M 83
TY 114,6664
TY 118,20488
M 80
ad 84,2166
W 0
M 81
M 78
M 79
W 1
M 76
M 77
M 74
W 2
M 75
M 72
WL 1
AN 112,114
AN 113,115
M 73
R 80
M 84
mw 48
SH 114
AN 114,114
M 85
WL 2
AN 115,115
AN 116,118
M 86
AN 117,119
SH 118
M 87
AN 118,118
AN 119,119
M 88
TY 122,8200
TY 126,22024
M 89
ad 84,2208
W 0
M 90
ae 44,68,96
M 91
s_bfe_u32 s51, s50, 0x10005
v_mad_i32_i24 v47, v49, s51, v48
XL 22,256
M 92
W 1
M 93
M 94
M 95
R 82
M 106
W 2
XL 24,1024
M 107
WL 3
AN 120,122
AN 121,123
M 104
WL 2
SH 122
AN 122,122
M 105
mw 51
AN 123,123
AN 124,126
M 102
AN 125,127
SH 126
M 103
AN 126,126
AN 127,127
M 100
ad 84,2214
W 0
M 101
XL 26,1792
M 98
M 99
W 1
M 96
M 97
R 0
M 108
W 2
XL 28,2560
M 109
M 110
M 111
bc 49,0,66,60
M 112
mh 0
M 113
ad 66,32
H 0
M 114
M 115
M 116
DG 1024
M 117
M 118
M 119
WL 2
R 2
)"
R"(M 120
bc 48,0,64,56
M 121
mh 3
M 133
ad 64,0
H 0
M 132
TX 58,4608
TX 62,18432
M 134
M 135
H 1
M 123
M 122
TY 66,5120
TY 70,18944
M 124
H 2
M 125
M 137
M 136
TX 74,6144
TX 78,19968
M 138
WL 4
Q 0
M 139
Q 2
M 127
Q 4
M 126
TY 82,6656
TY 86,20480
M 128
Q 6
M 129
WL 4
Q 8
M 141
Q 10
M 140
TX 90,7680
TX 94,21504
M 142
Q 12
M 143
R 6
M 131
Q 14
M 130
TY 98,8192
TY 102,22016
M 0
R 4
M 1
WL 10
Q 16
M 13
Q 18
M 12
Q 20
M 14
Q 22
M 15
WL 8
Q 24
M 3
Q 26
M 2
Q 28
M 4
Q 30
M 5
WL 6
Q 32
M 17
Q 34
M 16
Q 36
M 18
Q 38
M 19
WL 2
Q 40
M 7
Q 42
M 6
Q 44
M 8
Q 46
M 9
UL 6,14
UH 7,14
M 21
AD 176,6
AD 200,7
M 20
UL 6,15
UH 7,15
M 22
AD 224,6
AD 248,7
M 23
R 10
M 11
UL 6,16
UH 7,16
M 10
WL 2
R 8
M 34
AD 177,6
AD 201,7
M 35
UL 6,17
UH 7,17
M 32
AD 225,6
AD 249,7
M 33
UL 6,18
UH 7,18
M 30
AD 178,6
AD 202,7
M 31
UL 6,19
UH 7,19
M 28
AD 226,6
AD 250,7
M 29
UL 6,20
UH 7,20
M 26
AD 179,6
AD 203,7
M 27
WV 6
UL 6,21
UH 7,21
M 24
WL 0
FP
AD 227,6
M 25
AD 251,7
M 36
M 37
M 38
M 39
M 40
M 41
M 42
M 43
M 44
M 45
FR
M 46
M 47
M 58
M 59
M 56
M 57
M 54
M 55
WL 0
FQ 72
M 52
M 53
M 50
FZ 72
R 24
M 51
mw 72
R 26
M 48
TY 106,9736
TY 110,23560
M 49
R 28
M 60
ad 84,2256
W 0
M 61
M 62
M 63
W 1
M 64
M 65
M 66
W 2
M 67
WL 3
AN 104,106
AN 105,107
M 68
WL 2
SH 106
AN 106,106
M 69
mw 75
AN 107,107
AN 108,110
M 70
AN 109,111
SH 110
M 71
R 30
M 82
AN 110,110
AN 111,111
M 83
TY 114,11272
TY 118,25096
M 80
ad 84,2262
W 0
M 81
M 78
M 79
W 1
M 76
M 77
M 74
W 2
M 75
M 72
WL 1
AN 112,114
AN 113,115
M 73
R 32
M 84
mw 0
SH 114
AN 114,114
M 85
WL 2
AN 115,115
AN 116,118
M 86
AN 117,119
SH 118
M 87
AN 118,118
AN 119,119
M 88
TY 122,12808
TY 126,26632
M 89
ad 84,0
W 0
M 90
M 91
M 92
W 1
M 93
M 94
M 95
R 34
M 106
W 2
M 107
WL 3
AN 120,122
AN 121,123
M 104
WL 2
SH 122
AN 122,122
M 105
mw 3
AN 123,123
AN 124,126
M 102
AN 125,127
SH 126
M 103
AN 126,126
AN 127,127
M 100
ad 84,6
W 0
M 101
M 98
M 99
W 1
M 96
M 97
R 48
M 108
W 2
M 109
M 110
M 111
bc 49,1,66,60
M 112
mh 9
M 113
ad 66,224
H 0
M 114
M 115
M 116
DG 1024
M 117
M 118
M 119
WL 2
R 50
M 120
bc 48,1,64,56
M 121
mh 12
M 133
ad 64,224
H 0
M 132
TX 58,9216
TX 62,23040
M 134
M 135
DG 1024
M 123
M 122
TY 66,9728
TY 70,23552
M 124
mh 15
M 125
ad 64,195
H 0
M 137
M 136
TX 74,10752
TX 78,24576
M 138
WL 4
Q 0
M 139
Q 2
M 127
Q 4
M 126
TY 82,11264
TY 86,25088
M 128
Q 6
M 129
WL 4
Q 8
M 141
Q 10
M 140
TX 90,12288
TX 94,26112
M 142
Q 12
M 143
R 54
M 131
Q 14
M 130
TY 98,12800
TY 102,26624
M 0
R 52
M 1
WL 10
Q 16
M 13
Q 18
M 12
Q 20
M 14
Q 22
M 15
WL 8
Q 24
M 3
Q 26
M 2
Q 28
M 4
Q 30
M 5
WL 6
Q 32
M 17
Q 34
M 16
Q 36
M 18
Q 38
M 19
WL 2
Q 40
M 7
)"
R"(Q 42
M 6
Q 44
M 8
Q 46
M 9
DG 1024
M 21
UL 6,22
UH 7,22
M 20
AD 180,6
AD 204,7
M 22
UL 6,23
UH 7,23
M 23
R 58
M 11
AD 228,6
AD 252,7
M 10
WL 2
R 56
M 34
UL 6,24
UH 7,24
M 35
AD 181,6
AD 205,7
M 32
UL 6,25
UH 7,25
BV 52,0
M 33
AD 229,6
AD 253,7
M 30
UL 6,26
UH 7,26
M 31
AD 182,6
AD 206,7
M 28
UL 6,27
UH 7,27
M 29
AD 230,6
AD 254,7
M 26
WV 23
UL 6,28
UH 7,28
M 27
WV 6
AD 183,6
AD 207,7
M 24
WL 1
FP
UL 6,29
M 25
UH 7,29
AD 231,6
M 36
AD 255,7
M 37
M 38
M 39
M 40
ds_read_b64 v[12:13], v4 offset:256
ds_read_b64 v[46:47], v4 offset:264
M 41
AR 6,36
AR 7,60
M 42
AR 8,84
AR 9,108
M 43
WL 3
EF 6,52
EF 7,53
M 44
EF 8,54
EF 9,55
M 45
FR
CV 30,6,7
M 46
CV 31,8,9
AR 6,37
M 47
AR 7,61
AR 8,85
M 58
AR 9,109
EF 6,52
M 59
EF 7,53
EF 8,54
M 56
EF 9,55
CV 32,6,7
M 57
CV 33,8,9
AR 6,38
M 54
AR 7,62
AR 8,86
M 55
WL 0
FQ 76
AR 9,110
M 52
EF 6,52
EF 7,53
M 53
EF 8,54
EF 9,55
M 50
FZ 76
R 72
M 51
mw 24
R 74
M 48
TY 106,520
TY 110,14344
M 49
R 76
M 60
ad 84,48
W 0
M 61
CV 34,6,7
CV 35,8,9
M 62
AR 6,39
AR 7,63
M 63
W 1
AR 8,87
M 64
AR 9,111
EF 6,52
M 65
EF 7,53
EF 8,54
M 66
W 2
EF 9,55
M 67
WL 3
AN 104,106
AN 105,107
M 68
WL 2
SH 106
AN 106,106
M 69
mw 27
AN 107,107
AN 108,110
M 70
AN 109,111
SH 110
M 71
R 78
M 82
AN 110,110
AN 111,111
M 83
TY 114,2056
TY 118,15880
M 80
ad 84,54
W 0
M 81
CV 36,6,7
CV 37,8,9
M 78
ae 42,74,0
AR 6,40
M 79
W 1
AR 7,64
M 76
s_bitcmp1_b32 s50, 0
s_cselect_b32 s52, 24576, 0
v_add_u32 v50, s52, v3
ST 30,0
M 77
AR 8,88
AR 9,112
M 74
W 2
EF 6,52
M 75
EF 7,53
EF 8,54
M 72
WL 1
AN 112,114
AN 113,115
M 73
R 80
M 84
mw 48
SH 114
AN 114,114
M 85
WL 2
AN 115,115
AN 116,118
M 86
AN 117,119
SH 118
M 87
AN 118,118
AN 119,119
M 88
TY 122,3592
TY 126,17416
M 89
ad 84,96
W 0
M 90
EF 9,55
CV 38,6,7
M 91
ST 32,768
CV 39,8,9
M 92
W 1
AR 6,41
M 93
AR 7,65
AR 8,89
M 94
AR 9,113
EF 6,52
M 95
R 82
M 106
W 2
EF 7,53
M 107
WL 3
AN 120,122
AN 121,123
M 104
WL 2
SH 122
AN 122,122
M 105
mw 51
AN 123,123
AN 124,126
M 102
AN 125,127
SH 126
M 103
AN 126,126
AN 127,127
M 100
ad 84,102
W 0
M 101
EF 8,54
EF 9,55
M 98
ST 34,1536
CV 40,6,7
M 99
W 1
CV 41,8,9
M 96
AR 6,42
AR 7,66
M 97
R 0
M 108
W 2
AR 8,90
M 109
AR 9,114
EF 6,52
M 110
EF 7,53
EF 8,54
M 111
bc 49,2,66,60
EF 9,55
M 112
mh 18
CV 42,6,7
CV 43,8,9
M 113
ad 66,416
H 0
M 114
AR 6,43
AR 7,67
M 115
AR 8,91
AR 9,115
M 116
DG 1024
M 117
ST 36,2304
EF 6,52
M 118
EF 7,53
EF 8,54
M 119
WL 0
R 2
M 120
bc 48,2,64,56
EF 9,55
M 121
mh 21
CV 44,6,7
CV 45,8,9
M 133
ad 64,416
H 0
BV 52,512
M 132
TX 58,0
TX 62,13824
M 134
AR 6,132
AR 7,156
M 135
H 1
AR 8,180
M 123
EF 6,12
EF 7,13
M 122
TY 66,512
TY 70,14336
M 124
H 2
EF 8,46
M 125
EF 136,47
CV 14,6,7
M 137
ae 42,74,0
CV 15,8,136
M 136
WL 2
TX 74,1536
TX 78,15360
M 138
s_bfe_u32 s52, s50, 0x10001
Q 0
v_mad_i32_i24 v50, v49, s52, v48
M 139
Q 2
ST 38,0
M 127
Q 4
M 126
TY 82,2048
TY 86,15872
M 128
Q 6
M 129
WL 4
Q 8
M 141
Q 10
M 140
TX 90,3072
TX 94,16896
M 142
Q 12
M 143
R 6
M 131
Q 14
M 130
TY 98,3584
TY 102,17408
v_mfma_f32_16x16x32_f16 a[36:39], v[56:59], a[0:3], a[32:35]
R 4
M 1
WL 10
Q 16
v_mfma_f32_16x16x32_f16 a[60:63], v[60:63], a[12:15], a[32:35]
Q 18
ST 40,768
M 12
Q 20
v_mfma_f32_16x16x32_f16 a[64:67], v[64:67], a[8:11], a[32:35]
Q 22
M 15
)"
R"(WL 8
Q 24
v_mfma_f32_16x16x32_f16 a[40:43], v[68:71], a[4:7], a[32:35]
Q 26
M 2
Q 28
M 4
Q 30
M 5
WL 6
Q 32
M 17
Q 34
M 16
Q 36
M 18
Q 38
M 19
WL 1
Q 40
M 7
Q 42
M 6
Q 44
ST 42,1536
M 8
Q 46
M 9
AR 6,133
AR 7,157
M 21
AR 8,181
EF 6,12
M 20
EF 7,13
EF 8,46
M 22
EF 137,47
CV 16,6,7
M 23
R 10
M 11
CV 17,8,137
AR 6,134
M 10
WL 2
R 8
M 34
AR 7,158
AR 8,182
M 35
EF 6,12
EF 7,13
M 32
EF 8,46
EF 138,47
M 33
CV 18,6,7
CV 19,8,138
M 30
ST 44,2304
AR 6,135
M 31
AR 7,159
AR 8,183
M 28
EF 6,12
EF 7,13
M 29
EF 8,46
EF 139,47
v_mfma_f32_16x16x32_f16 a[88:91], v[64:67], a[16:19], a[32:35]
CV 20,6,7
CV 21,8,139
M 27
WV 10
AR 6,136
AR 7,160
AR 8,184
v_mfma_f32_16x16x32_f16 a[84:87], v[56:59], a[16:19], a[32:35]
WL 0
FP
EF 6,12
EF 7,13
M 25
EF 8,46
EF 140,47
CV 22,6,7
v_mfma_f32_16x16x32_f16 a[108:111], v[56:59], a[24:27], a[32:35]
CV 23,8,140
AR 6,137
AR 7,161
M 37
AR 8,185
EF 6,12
EF 7,13
v_mfma_f32_16x16x32_f16 a[112:115], v[64:67], a[24:27], a[32:35]
EF 8,46
EF 141,47
CV 24,6,7
M 39
CV 25,8,141
AR 6,138
AR 7,162
M 40
AR 8,186
EF 6,12
EF 7,13
M 41
ae 42,74,0
EF 8,46
EF 142,47
M 42
s_bitcmp1_b32 s50, 0
s_cselect_b32 s52, 24576, 0
v_add_u32 v50, s52, v3
ST 14,128
M 43
CV 26,6,7
CV 27,8,142
AR 6,139
M 44
AR 7,163
AR 8,187
EF 6,12
M 45
FR
EF 7,13
EF 8,46
M 46
EF 143,47
CV 28,6,7
CV 29,8,143
M 47
ds_read_b64 v[12:13], v4 offset:1536
ds_read_b64 v[46:47], v4 offset:1544
M 58
EF 160,52
EF 184,53
M 59
EF 208,54
EF 232,55
M 56
CV 30,160,184
CV 31,208,232
M 57
EF 161,52
EF 185,53
M 54
EF 209,54
EF 233,55
M 55
WL 2
FQ 80
CV 32,161,185
M 52
CV 33,209,233
EF 162,52
M 53
ST 16,896
EF 186,53
v_mfma_f32_16x16x32_f16 a[136:139], v[64:67], a[0:3], a[32:35]
FZ 80
R 24
M 51
mw 72
R 26
v_mfma_f32_16x16x32_f16 a[132:135], v[56:59], a[0:3], a[32:35]
TY 106,5128
TY 110,18952
M 49
R 28
v_mfma_f32_16x16x32_f16 a[156:159], v[56:59], a[8:11], a[32:35]
ad 84,144
W 0
M 61
EF 210,54
EF 234,55
v_mfma_f32_16x16x32_f16 a[160:163], v[64:67], a[8:11], a[32:35]
CV 34,162,186
CV 35,210,234
M 63
W 1
EF 163,52
M 64
EF 187,53
EF 211,54
M 65
EF 235,55
CV 36,163,187
M 66
W 2
CV 37,211,235
M 67
WL 3
AN 104,106
AN 105,107
EF 164,52
M 68
WL 2
SH 106
AN 106,106
ST 18,1664
M 69
mw 75
AN 107,107
AN 108,110
EF 188,53
M 70
AN 109,111
SH 110
EF 212,54
M 71
R 30
EF 236,55
M 82
AN 110,110
AN 111,111
CV 38,164,188
M 83
TY 114,6664
TY 118,20488
CV 39,212,236
M 80
ad 84,150
W 0
EF 165,52
M 81
EF 189,53
EF 213,54
EF 237,55
M 78
CV 40,165,189
CV 41,213,237
EF 166,52
M 79
W 1
EF 190,53
EF 214,54
M 76
EF 238,55
CV 42,166,190
CV 43,214,238
M 77
EF 167,52
EF 191,53
EF 215,54
v_mfma_f32_16x16x32_f16 a[184:187], v[64:67], a[16:19], a[32:35]
W 2
EF 239,55
CV 44,167,191
M 75
ST 20,2432
CV 45,215,239
v_mfma_f32_16x16x32_f16 a[180:183], v[56:59], a[16:19], a[32:35]
WL 1
AN 112,114
AN 113,115
BV 52,1792
M 73
R 32
v_mfma_f32_16x16x32_f16 v[136:139], v[56:59], a[24:27], v[128:131]
mw 0
SH 114
AN 114,114
M 85
WL 3
AN 115,115
AN 116,118
v_mfma_f32_16x16x32_f16 v[140:143], v[64:67], a[24:27], v[128:131]
AN 117,119
SH 118
M 87
AN 118,118
AN 119,119
M 88
TY 122,8200
TY 126,22024
M 89
ad 84,192
W 0
M 90
AR 6,44
AR 7,68
M 91
AR 8,92
AR 9,116
M 92
W 1
EF 6,12
M 93
EF 7,13
EF 8,46
M 94
ae 42,74,0
EF 9,47
M 95
R 34
M 106
WL 3
W 2
CV 14,6,7
M 107
s_bfe_u32 s52, s50, 0x10001
AN 120,122
AN 121,123
v_mad_i32_i24 v50, v49, s52, v48
M 104
WL 2
SH 122
AN 122,122
ST 22,128
M 105
mw 3
AN 123,123
AN 124,126
M 102
AN 125,127
SH 126
M 103
AN 126,126
AN 127,127
M 100
ad 84,198
W 0
M 101
CV 15,8,9
AR 6,45
v_mfma_f32_16x16x32_f16 v[164:167], v[64:67], a[0:3], v[128:131]
AR 7,69
AR 8,93
M 99
W 1
AR 9,117
v_mfma_f32_16x16x32_f16 v[160:163], v[56:59], a[0:3], v[128:131]
EF 6,12
EF 7,13
M 97
R 48
v_mfma_f32_16x16x32_f16 v[184:187], v[56:59], a[8:11], v[128:131]
W 2
EF 8,46
M 109
EF 9,47
CV 16,6,7
v_mfma_f32_16x16x32_f16 v[188:191], v[64:67], a[8:11], v[128:131]
CV 17,8,9
AR 6,46
M 111
bc 49,3,66,60
ST 24,896
M 112
mh 0
AR 7,70
AR 8,94
M 113
ad 66,608
H 0
M 114
AR 9,118
EF 6,12
M 115
EF 7,13
EF 8,46
M 116
DG 1024
M 117
EF 9,47
CV 18,6,7
M 118
CV 19,8,9
AR 6,47
M 119
WL 0
R 50
v_mfma_f32_16x16x32_f16 v[208:211], v[56:59], a[16:19], v[128:131]
bc 48,3,64,56
AR 7,71
M 121
mh 3
AR 8,95
AR 9,119
v_mfma_f32_16x16x32_f16 v[232:235], v[60:63], a[28:31], v[128:131]
ad 64,608
H 0
M 132
TX 58,4608
TX 62,18432
v_mfma_f32_16x16x32_f16 v[236:239], v[64:67], a[24:27], v[128:131]
ST 26,1664
EF 6,12
M 135
H 1
EF 7,13
v_mfma_f32_16x16x32_f16 v[212:215], v[68:71], a[20:23], v[128:131]
EF 8,46
EF 9,47
M 122
TY 66,5120
TY 70,18944
)"
R"(M 124
H 2
CV 20,6,7
M 125
CV 21,8,9
M 137
AR 6,48
AR 7,72
AR 8,96
AR 9,120
EF 6,12
EF 7,13
M 136
TX 74,6144
TX 78,19968
PA 10240
DF
EF 8,46
EF 9,47
M 138
WL 4
Q 0
CV 6,6,7
CV 7,8,9
AW 200,6
AW 201,7
M 139
Q 2
AR 6,49
AR 7,73
AR 8,97
AR 9,121
M 127
Q 4
EF 6,12
EF 7,13
EF 8,46
EF 9,47
M 126
TY 82,6656
TY 86,20480
CV 6,6,7
CV 7,8,9
AW 202,6
AW 203,7
M 128
Q 6
AR 6,50
AR 7,74
AR 8,98
AR 9,122
M 129
WL 2
Q 8
ST 28,2432
EF 6,12
EF 7,13
EF 8,46
M 141
Q 10
EF 9,47
CV 6,6,7
CV 7,8,9
AW 204,6
M 140
TX 90,7680
TX 94,21504
AW 205,7
AR 6,51
AR 7,75
AR 8,99
M 142
Q 12
AR 9,123
EF 6,12
EF 7,13
EF 8,46
M 143
R 54
EF 9,47
CV 6,6,7
CV 7,8,9
AW 206,6
M 131
Q 14
AW 207,7
M 130
TY 98,8192
TY 102,22016
ds_read_b64 v[12:13], v4 offset:2048
M 0
R 52
ds_read_b64 v[46:47], v4 offset:2056
M 1
Q 16
M 13
Q 18
M 12
Q 20
M 14
Q 22
M 15
WL 8
Q 24
M 3
Q 26
M 2
Q 28
v_mfma_f32_16x16x32_f16 a[44:47], v[72:75], a[0:3], a[32:35]
Q 30
M 5
Q 32
v_mfma_f32_16x16x32_f16 a[68:71], v[76:79], a[12:15], a[32:35]
Q 34
M 16
Q 36
v_mfma_f32_16x16x32_f16 a[72:75], v[80:83], a[8:11], a[32:35]
Q 38
M 19
WL 2
Q 40
v_mfma_f32_16x16x32_f16 a[48:51], v[84:87], a[4:7], a[32:35]
Q 42
M 6
Q 44
M 8
Q 46
M 9
ae 42,74,0
M 21
s_bitcmp1_b32 s50, 0
s_cselect_b32 s52, 24576, 0
v_add_u32 v50, s52, v3
ST 30,256
M 20
AR 6,140
AR 7,164
M 22
AR 8,188
EF 6,52
M 23
R 58
M 11
EF 7,53
EF 8,54
M 10
WL 2
R 56
M 34
EF 144,55
CV 22,6,7
M 35
CV 23,8,144
AR 6,141
M 32
AR 7,165
AR 8,189
M 33
EF 6,52
EF 7,53
v_mfma_f32_16x16x32_f16 a[96:99], v[80:83], a[16:19], a[32:35]
EF 8,54
EF 145,55
M 31
CV 24,6,7
CV 25,8,145
v_mfma_f32_16x16x32_f16 a[92:95], v[72:75], a[16:19], a[32:35]
AR 6,142
AR 7,166
M 29
ST 32,1024
AR 8,190
M 26
EF 6,52
EF 7,53
M 27
WV 11
EF 8,54
EF 146,55
M 24
WL 0
FP
CV 26,6,7
M 25
CV 27,8,146
AR 6,143
M 36
AR 7,167
AR 8,191
M 37
EF 6,52
EF 7,53
M 38
EF 8,54
EF 147,55
M 39
CV 28,6,7
CV 29,8,147
v_mfma_f32_16x16x32_f16 a[116:119], v[72:75], a[24:27], a[32:35]
AR 6,144
AR 7,168
AR 8,192
EF 6,52
M 41
EF 7,53
EF 8,54
EF 148,55
CV 6,6,7
v_mfma_f32_16x16x32_f16 a[120:123], v[80:83], a[24:27], a[32:35]
CV 7,8,148
AW 208,6
AW 209,7
AR 6,145
M 43
AR 7,169
AR 8,193
EF 6,52
EF 7,53
M 44
ST 34,1792
EF 8,54
EF 149,55
CV 6,6,7
M 45
FR
CV 7,8,149
AW 210,6
AW 211,7
M 46
AR 6,146
AR 7,170
AR 8,194
EF 6,52
M 47
EF 7,53
EF 8,54
EF 150,55
CV 6,6,7
M 58
CV 7,8,150
AW 212,6
AW 213,7
AR 6,147
M 59
AR 7,171
AR 8,195
EF 6,52
EF 7,53
M 56
EF 8,54
EF 151,55
CV 6,6,7
CV 7,8,151
M 57
AW 214,6
AW 215,7
v_mfma_f32_16x16x32_f16 a[144:147], v[80:83], a[0:3], a[32:35]
BV 52,3072
EF 168,12
EF 192,13
M 55
WL 1
FQ 84
EF 216,46
EF 240,47
v_mfma_f32_16x16x32_f16 a[140:143], v[72:75], a[0:3], a[32:35]
CV 6,168,192
CV 7,216,240
AW 216,6
M 53
AW 217,7
EF 169,12
EF 193,13
M 50
FZ 84
R 72
EF 217,46
M 51
mw 24
R 74
ST 36,2560
M 48
TY 106,9736
TY 110,23560
EF 241,47
M 49
R 76
CV 6,169,193
M 60
ad 84,240
W 0
CV 7,217,241
M 61
AW 218,6
AW 219,7
EF 170,12
M 62
EF 194,13
EF 218,46
EF 242,47
M 63
W 1
CV 6,170,194
CV 7,218,242
v_mfma_f32_16x16x32_f16 a[164:167], v[72:75], a[8:11], a[32:35]
AW 220,6
AW 221,7
EF 171,12
M 65
EF 195,13
EF 219,46
EF 243,47
v_mfma_f32_16x16x32_f16 a[168:171], v[80:83], a[8:11], a[32:35]
W 2
CV 6,171,195
CV 7,219,243
M 67
WL 3
AN 104,106
AN 105,107
AW 222,6
M 68
WL 0
SH 106
AN 106,106
AW 223,7
M 69
mw 27
AN 107,107
AN 108,110
EF 172,12
M 70
AN 109,111
SH 110
EF 196,13
M 71
R 78
EF 220,46
M 82
AN 110,110
AN 111,111
EF 244,47
M 83
TY 114,11272
TY 118,25096
CV 30,172,196
M 80
ad 84,246
W 0
CV 31,220,244
M 81
ae 42,74,0
EF 173,12
EF 197,13
v_mfma_f32_16x16x32_f16 a[192:195], v[80:83], a[16:19], a[32:35]
s_bfe_u32 s52, s50, 0x10001
v_mad_i32_i24 v50, v49, s52, v48
ST 38,256
EF 221,46
M 79
W 1
EF 245,47
CV 32,173,197
v_mfma_f32_16x16x32_f16 a[188:191], v[72:75], a[16:19], a[32:35]
CV 33,221,245
EF 174,12
EF 198,13
M 77
EF 222,46
EF 246,47
CV 34,174,198
M 74
W 2
CV 35,222,246
EF 175,12
M 75
EF 199,13
EF 223,46
EF 247,47
M 72
WL 1
AN 112,114
AN 113,115
CV 36,175,199
M 73
R 80
CV 37,223,247
M 84
mw 48
SH 114
AN 114,114
ds_read_b64 v[12:13], v4 offset:3328
M 85
WL 3
AN 115,115
AN 116,118
ds_read_b64 v[46:47], v4 offset:3336
M 86
AN 117,119
SH 118
)"
R"(AR 6,52
M 87
AN 118,118
AN 119,119
AR 7,76
v_mfma_f32_16x16x32_f16 v[144:147], v[72:75], a[24:27], v[128:131]
TY 122,12808
TY 126,26632
AR 8,100
M 89
ad 84,288
W 0
ST 40,1024
v_mfma_f32_16x16x32_f16 v[148:151], v[80:83], a[24:27], v[128:131]
AR 9,124
EF 6,52
EF 7,53
M 91
EF 8,54
EF 9,55
CV 6,6,7
M 92
W 1
CV 7,8,9
AW 224,6
M 93
AW 225,7
AR 6,53
AR 7,77
M 94
AR 8,101
AR 9,125
EF 6,52
M 95
R 82
EF 7,53
M 106
W 2
EF 8,54
EF 9,55
M 107
WL 3
AN 120,122
AN 121,123
CV 6,6,7
M 104
WL 2
SH 122
AN 122,122
CV 7,8,9
M 105
mw 51
AN 123,123
AN 124,126
AW 226,6
v_mfma_f32_16x16x32_f16 v[172:175], v[80:83], a[0:3], v[128:131]
AN 125,127
SH 126
AW 227,7
M 103
AN 126,126
AN 127,127
AR 6,54
v_mfma_f32_16x16x32_f16 v[168:171], v[72:75], a[0:3], v[128:131]
ad 84,294
W 0
ST 42,1792
M 101
AR 7,78
AR 8,102
AR 9,126
M 98
EF 6,52
EF 7,53
EF 8,54
M 99
W 1
EF 9,55
CV 6,6,7
M 96
CV 7,8,9
AW 228,6
AW 229,7
M 97
R 0
AR 6,55
M 108
W 2
AR 7,79
AR 8,103
M 109
AR 9,127
EF 6,52
EF 7,53
M 110
EF 8,54
EF 9,55
CV 6,6,7
M 111
bc 49,4,66,60
CV 7,8,9
AW 230,6
v_mfma_f32_16x16x32_f16 v[192:195], v[72:75], a[8:11], v[128:131]
mh 9
AW 231,7
M 113
ad 66,800
H 0
AR 6,56
v_mfma_f32_16x16x32_f16 v[196:199], v[80:83], a[8:11], v[128:131]
AR 7,80
AR 8,104
AR 9,128
M 115
ST 44,2560
EF 6,52
EF 7,53
M 116
DG 1024
EF 8,54
M 117
EF 9,55
CV 6,6,7
CV 7,8,9
M 118
AW 232,6
AW 233,7
AR 6,57
M 119
WL 0
R 2
AR 7,81
M 120
bc 48,4,64,56
AR 8,105
AR 9,129
M 121
mh 12
EF 6,52
EF 7,53
EF 8,54
M 133
ad 64,800
H 0
EF 9,55
M 132
TX 58,9216
TX 62,23040
CV 6,6,7
M 134
CV 7,8,9
AW 234,6
AW 235,7
M 135
H 1
AR 6,58
AR 7,82
M 123
AR 8,106
AR 9,130
EF 6,52
M 122
TY 66,9728
TY 70,23552
EF 7,53
v_mfma_f32_16x16x32_f16 v[216:219], v[72:75], a[16:19], v[128:131]
H 2
EF 8,54
EF 9,55
M 125
ae 42,72,0
CV 6,6,7
CV 7,8,9
v_mfma_f32_16x16x32_f16 v[240:243], v[76:79], a[28:31], v[128:131]
s_bitcmp1_b32 s50, 2
s_cselect_b32 s52, 24576, 0
v_add_u32 v50, s52, v3
ST 14,0
M 136
TX 74,10752
TX 78,24576
AW 236,6
v_mfma_f32_16x16x32_f16 v[244:247], v[80:83], a[24:27], v[128:131]
WL 4
Q 0
AW 237,7
M 139
Q 2
AR 6,59
v_mfma_f32_16x16x32_f16 v[220:223], v[84:87], a[20:23], v[128:131]
Q 4
AR 7,83
M 126
TY 82,11264
TY 86,25088
AR 8,107
M 128
Q 6
AR 9,131
M 129
WL 4
Q 8
EF 6,52
M 141
Q 10
EF 7,53
M 140
TX 90,12288
TX 94,26112
EF 8,54
M 142
Q 12
EF 9,55
M 143
R 6
CV 6,6,7
M 131
Q 14
CV 7,8,9
M 130
TY 98,12800
TY 102,26624
ST 16,768
M 0
R 4
AW 238,6
M 1
WL 8
Q 16
AW 239,7
M 13
Q 18
BV 52,3584
M 12
Q 20
M 14
Q 22
M 15
Q 24
M 3
Q 26
M 2
Q 28
M 4
Q 30
M 5
WL 5
Q 32
M 17
Q 34
M 16
Q 36
M 18
Q 38
ST 18,1536
M 19
WL 1
Q 40
M 7
Q 42
M 6
Q 44
v_mfma_f32_16x16x32_f16 a[52:55], v[88:91], a[0:3], a[32:35]
Q 46
M 9
AR 6,148
AR 7,172
v_mfma_f32_16x16x32_f16 a[76:79], v[92:95], a[12:15], a[32:35]
AR 8,196
EF 6,12
M 20
EF 7,13
EF 8,46
v_mfma_f32_16x16x32_f16 a[80:83], v[96:99], a[8:11], a[32:35]
EF 152,47
CV 38,6,7
M 23
R 10
v_mfma_f32_16x16x32_f16 a[56:59], v[100:103], a[4:7], a[32:35]
CV 39,8,152
AR 6,149
M 10
R 8
v_mfma_f32_16x16x32_f16 a[104:107], v[96:99], a[16:19], a[32:35]
AR 7,173
AR 8,197
M 35
ST 20,2304
EF 6,12
v_mfma_f32_16x16x32_f16 a[100:103], v[88:91], a[16:19], a[32:35]
EF 7,13
EF 8,46
M 33
EF 153,47
CV 40,6,7
M 30
CV 41,8,153
AR 6,150
M 31
AR 7,174
AR 8,198
M 28
EF 6,12
EF 7,13
M 29
EF 8,46
EF 154,47
M 26
CV 42,6,7
CV 43,8,154
M 27
WV 10
AR 6,151
AR 7,175
M 24
WL 0
FP
AR 8,199
M 25
EF 6,12
EF 7,13
M 36
EF 8,46
EF 155,47
M 37
CV 44,6,7
CV 45,8,155
M 38
ae 42,72,0
ST 22,128
AR 6,152
AR 7,176
M 39
EF 6,12
EF 7,13
EF 132,46
EF 156,47
M 40
CV 14,6,7
CV 15,132,156
AR 6,153
AR 7,177
M 41
EF 6,12
EF 7,13
EF 133,46
EF 157,47
M 42
CV 16,6,7
CV 17,133,157
AR 6,154
AR 7,178
M 43
EF 6,12
EF 7,13
EF 134,46
EF 158,47
v_mfma_f32_16x16x32_f16 a[124:127], v[88:91], a[24:27], a[32:35]
CV 18,6,7
CV 19,134,158
AR 6,155
AR 7,179
M 45
FR
EF 6,12
EF 7,13
EF 135,46
v_mfma_f32_16x16x32_f16 a[128:131], v[96:99], a[24:27], a[32:35]
EF 159,47
CV 20,6,7
CV 21,135,159
M 47
EF 176,52
EF 200,53
v_mfma_f32_16x16x32_f16 a[152:155], v[96:99], a[0:3], a[32:35]
EF 224,54
EF 248,55
M 59
CV 6,176,200
CV 7,224,248
v_mfma_f32_16x16x32_f16 a[148:151], v[88:91], a[0:3], a[32:35]
AW 240,6
AW 241,7
M 57
ST 24,896
EF 177,52
M 54
EF 201,53
EF 225,54
M 55
WL 0
FQ 88
EF 249,55
M 52
CV 6,177,201
CV 7,225,249
M 53
AW 242,6
AW 243,7
M 50
)"
R"(FZ 88
R 24
M 51
mw 72
R 26
M 48
TY 106,520
TY 110,14344
M 49
R 28
M 60
ad 84,336
W 0
M 61
EF 178,52
EF 202,53
M 62
EF 226,54
EF 250,55
M 63
W 1
CV 6,178,202
M 64
ST 26,1664
CV 7,226,250
M 65
AW 244,6
AW 245,7
M 66
W 2
EF 179,52
M 67
WL 3
AN 104,106
AN 105,107
v_mfma_f32_16x16x32_f16 a[172:175], v[88:91], a[8:11], a[32:35]
WL 2
SH 106
AN 106,106
M 69
mw 75
AN 107,107
AN 108,110
v_mfma_f32_16x16x32_f16 a[176:179], v[96:99], a[8:11], a[32:35]
AN 109,111
SH 110
M 71
R 30
v_mfma_f32_16x16x32_f16 v[132:135], v[96:99], a[16:19], v[128:131]
AN 110,110
AN 111,111
M 83
TY 114,2056
TY 118,15880
v_mfma_f32_16x16x32_f16 a[196:199], v[88:91], a[16:19], a[32:35]
ad 84,342
W 0
M 81
EF 203,53
EF 227,54
M 78
EF 251,55
CV 6,179,203
M 79
W 1
ST 28,2432
M 76
CV 7,227,251
AW 246,6
M 77
AW 247,7
M 74
W 2
EF 180,52
EF 204,53
EF 228,54
M 75
EF 252,55
CV 6,180,204
CV 7,228,252
AW 248,6
M 72
WL 1
AN 112,114
AN 113,115
AW 249,7
EF 181,52
M 73
R 32
EF 205,53
EF 229,54
M 84
mw 0
SH 114
AN 114,114
EF 253,55
CV 6,181,205
M 85
WL 2
AN 115,115
AN 116,118
CV 7,229,253
AW 250,6
M 86
AN 117,119
SH 118
AW 251,7
EF 182,52
M 87
AN 118,118
AN 119,119
EF 206,53
EF 230,54
M 88
TY 122,3592
TY 126,17416
EF 254,55
CV 6,182,206
M 89
ad 84,384
W 0
CV 7,230,254
AW 252,6
M 90
ae 42,72,0
AW 253,7
EF 183,52
EF 207,53
M 91
s_bfe_u32 s52, s50, 0x10003
v_mad_i32_i24 v50, v49, s52, v48
ST 30,256
EF 231,54
EF 255,55
v_mfma_f32_16x16x32_f16 v[152:155], v[88:91], a[24:27], v[128:131]
W 1
CV 6,183,207
CV 7,231,255
AW 254,6
M 93
AW 255,7
v_mfma_f32_16x16x32_f16 v[156:159], v[96:99], a[24:27], v[128:131]
M 95
R 34
v_mfma_f32_16x16x32_f16 v[180:183], v[96:99], a[0:3], v[128:131]
W 2
M 107
WL 3
AN 120,122
AN 121,123
v_mfma_f32_16x16x32_f16 v[176:179], v[88:91], a[0:3], v[128:131]
WL 2
SH 122
AN 122,122
M 105
mw 3
AN 123,123
AN 124,126
M 102
AN 125,127
SH 126
M 103
AN 126,126
AN 127,127
M 100
ad 84,390
W 0
M 101
M 98
ST 32,1024
M 99
W 1
M 96
M 97
R 48
M 108
W 2
M 109
M 110
M 111
bc 49,5,66,60
M 112
mh 18
M 113
ad 66,992
H 0
M 114
M 115
v_mfma_f32_16x16x32_f16 v[200:203], v[88:91], a[8:11], v[128:131]
DG 1024
M 117
ST 34,1792
v_mfma_f32_16x16x32_f16 v[204:207], v[96:99], a[8:11], v[128:131]
M 119
WL 2
R 50
M 120
bc 48,5,64,56
M 121
mh 21
M 133
ad 64,992
H 0
M 132
TX 58,0
TX 62,13824
M 134
M 135
H 1
M 123
M 122
TY 66,512
TY 70,14336
M 124
H 2
M 125
M 137
ST 36,2560
M 136
TX 74,1536
TX 78,15360
M 138
WL 4
Q 0
M 139
Q 2
M 127
Q 4
M 126
TY 82,2048
TY 86,15872
v_mfma_f32_16x16x32_f16 v[224:227], v[88:91], a[16:19], v[128:131]
Q 6
M 129
WL 4
Q 8
v_mfma_f32_16x16x32_f16 v[248:251], v[92:95], a[28:31], v[128:131]
Q 10
M 140
TX 90,3072
TX 94,16896
v_mfma_f32_16x16x32_f16 v[252:255], v[96:99], a[24:27], v[128:131]
Q 12
M 143
R 54
v_mfma_f32_16x16x32_f16 v[228:231], v[100:103], a[20:23], v[128:131]
Q 14
M 130
TY 98,3584
TY 102,17408
M 0
R 52
M 1
WL 10
Q 16
M 13
Q 18
M 12
Q 20
M 14
Q 22
M 15
WL 8
Q 24
M 3
Q 26
M 2
Q 28
M 4
Q 30
M 5
WL 6
Q 32
M 17
Q 34
M 16
Q 36
M 18
Q 38
M 19
WL 2
Q 40
M 7
Q 42
M 6
Q 44
M 8
Q 46
M 9
ae 42,72,96
M 21
s_bitcmp1_b32 s50, 4
s_cselect_b32 s52, 24576, 0
v_add_u32 v50, s52, v3
ST 38,128
M 20
M 22
M 23
R 58
M 11
M 10
WL 2
R 56
M 34
M 35
M 32
M 33
M 30
M 31
M 28
M 29
ST 40,896
M 26
M 27
WV 9
M 24
WL 0
FP
M 25
M 36
M 37
M 38
M 39
M 40
M 41
M 42
M 43
M 44
ST 42,1664
M 45
FR
M 46
M 47
M 58
M 59
M 56
M 57
M 54
M 55
WL 0
FQ 92
M 52
M 53
M 50
FZ 92
R 72
M 51
mw 24
R 74
ST 44,2432
M 48
TY 106,5128
TY 110,18952
M 49
R 76
M 60
ad 84,432
W 0
M 61
M 62
M 63
W 1
M 64
M 65
M 66
W 2
M 67
WL 3
AN 104,106
AN 105,107
M 68
WL 2
SH 106
AN 106,106
M 69
mw 27
AN 107,107
AN 108,110
M 70
AN 109,111
SH 110
M 71
R 78
M 82
AN 110,110
AN 111,111
M 83
TY 114,6664
TY 118,20488
M 80
ad 84,438
W 0
M 81
ae 42,72,96
M 78
s_bfe_u32 s52, s50, 0x10005
v_mad_i32_i24 v50, v49, s52, v48
ST 14,128
M 79
W 1
)"
R"(M 76
M 77
M 74
W 2
M 75
M 72
WL 1
AN 112,114
AN 113,115
M 73
R 80
M 84
mw 48
SH 114
AN 114,114
M 85
WL 2
AN 115,115
AN 116,118
M 86
AN 117,119
SH 118
M 87
AN 118,118
AN 119,119
M 88
TY 122,8200
TY 126,22024
M 89
ad 84,480
W 0
ST 16,896
M 90
M 91
M 92
W 1
M 93
M 94
M 95
R 82
M 106
W 2
M 107
WL 3
AN 120,122
AN 121,123
M 104
WL 2
SH 122
AN 122,122
M 105
mw 51
AN 123,123
AN 124,126
M 102
AN 125,127
SH 126
M 103
AN 126,126
AN 127,127
M 100
ad 84,486
W 0
ST 18,1664
M 101
M 98
M 99
W 1
M 96
M 97
R 0
M 108
W 2
M 109
M 110
M 111
bc 49,6,66,60
M 112
mh 0
M 113
ad 66,1184
H 0
M 114
M 115
ST 20,2432
M 116
DG 1024
M 117
M 118
M 119
WL 2
R 2
M 120
bc 48,6,64,56
M 121
mh 3
M 133
ad 64,1184
H 0
M 132
TX 58,4608
TX 62,18432
M 134
M 135
H 1
M 123
M 122
TY 66,5120
TY 70,18944
M 124
H 2
M 125
ae 44,70,384
M 137
s_bitcmp1_b32 s50, 6
s_cselect_b32 s51, 24576, 0
v_add_u32 v47, s51, v3
M 136
TX 74,6144
TX 78,19968
M 138
WL 4
Q 0
M 139
Q 2
M 127
Q 4
M 126
TY 82,6656
TY 86,20480
M 128
Q 6
M 129
WL 4
Q 8
M 141
Q 10
M 140
TX 90,7680
TX 94,21504
M 142
Q 12
M 143
R 6
M 131
Q 14
M 130
TY 98,8192
TY 102,22016
M 0
R 4
M 1
WL 10
Q 16
M 13
Q 18
M 12
Q 20
M 14
Q 22
M 15
WL 8
Q 24
M 3
Q 26
M 2
Q 28
M 4
Q 30
M 5
WL 6
Q 32
M 17
Q 34
M 16
Q 36
M 18
Q 38
M 19
WL 2
Q 40
M 7
Q 42
M 6
Q 44
M 8
Q 46
M 9
XL 22,0
M 21
XL 24,768
M 20
XL 26,1536
M 22
XL 28,2304
M 23
R 10
M 11
M 10
WL 2
R 8
M 34
M 35
M 32
M 33
M 30
M 31
M 28
M 29
M 26
M 27
WV 10
M 24
WL 0
FP
M 25
M 36
ae 42,72,0
M 37
s_bfe_u32 s52, s50, 0x10003
v_mad_i32_i24 v50, v49, s52, v48
SA 200,0
M 38
M 39
M 40
M 41
M 42
M 43
M 44
M 45
FR
M 46
M 47
M 58
M 59
M 56
M 57
M 54
M 55
WL 0
FQ 96
M 52
M 53
M 50
FZ 96
R 24
M 51
mw 72
R 26
M 48
TY 106,9736
TY 110,23560
M 49
R 28
M 60
ad 84,528
W 0
M 61
M 62
M 63
W 1
M 64
M 65
M 66
W 2
M 67
WL 3
AN 104,106
AN 105,107
M 68
WL 2
SH 106
AN 106,106
M 69
mw 75
AN 107,107
AN 108,110
M 70
AN 109,111
SH 110
M 71
R 30
M 82
AN 110,110
AN 111,111
M 83
TY 114,11272
TY 118,25096
M 80
ad 84,534
W 0
M 81
M 78
M 79
W 1
M 76
M 77
M 74
W 2
M 75
M 72
WL 1
AN 112,114
AN 113,115
M 73
R 32
M 84
mw 0
SH 114
AN 114,114
M 85
WL 2
AN 115,115
AN 116,118
M 86
AN 117,119
SH 118
M 87
AN 118,118
AN 119,119
M 88
TY 122,12808
TY 126,26632
M 89
ad 84,576
W 0
M 90
M 91
M 92
W 1
M 93
M 94
M 95
R 34
M 106
W 2
M 107
WL 3
AN 120,122
AN 121,123
M 104
WL 2
SH 122
AN 122,122
M 105
mw 3
AN 123,123
AN 124,126
M 102
AN 125,127
SH 126
M 103
AN 126,126
AN 127,127
M 100
ad 84,582
W 0
M 101
SA 202,768
M 98
M 99
W 1
M 96
M 97
R 48
M 108
W 2
M 109
M 110
M 111
bc 49,7,66,60
M 112
mh 9
M 113
ad 66,1376
H 0
M 114
M 115
M 116
DG 1024
M 117
M 118
M 119
WL 2
R 50
M 120
bc 48,7,64,56
M 121
mh 12
M 133
ad 64,1376
H 0
M 132
TX 58,9216
TX 62,23040
M 134
M 135
H 1
M 123
M 122
)"
R"(TY 66,9728
TY 70,23552
M 124
H 2
M 125
M 137
ae 44,70,384
M 136
TX 74,10752
TX 78,24576
PA 10240
DF
M 138
s_bfe_u32 s51, s50, 0x10007
WL 4
Q 0
M 139
Q 2
M 127
Q 4
M 126
TY 82,11264
TY 86,25088
M 128
Q 6
M 129
WL 4
Q 8
M 141
Q 10
M 140
TX 90,12288
TX 94,26112
M 142
Q 12
M 143
R 54
M 131
Q 14
M 130
TY 98,12800
TY 102,26624
M 0
R 52
M 1
WL 10
Q 16
M 13
Q 18
M 12
Q 20
M 14
Q 22
M 15
WL 8
Q 24
M 3
Q 26
M 2
Q 28
M 4
Q 30
M 5
WL 6
Q 32
M 17
Q 34
M 16
Q 36
M 18
Q 38
M 19
WL 2
Q 40
M 7
Q 42
M 6
Q 44
M 8
Q 46
M 9
v_mad_i32_i24 v47, v49, s51, v48
XL 30,0
M 21
XL 32,768
M 20
XL 34,1536
M 22
XL 36,2304
M 23
R 58
M 11
M 10
WL 2
R 56
M 34
SA 204,1536
M 35
M 32
M 33
M 30
M 31
M 28
M 29
M 26
M 27
WV 11
M 24
WL 0
FP
M 25
M 36
M 37
M 38
M 39
M 40
UL 6,22
UH 7,22
M 41
AR 8,36
AR 9,60
M 42
AD 8,6
AD 9,7
M 43
AW 36,8
AW 60,9
M 44
UL 6,23
UH 7,23
M 45
FR
AR 8,84
M 46
AR 9,108
AD 8,6
M 47
AD 9,7
AW 84,8
M 58
AW 108,9
UL 6,24
M 59
UH 7,24
AR 8,37
M 56
AR 9,61
AD 8,6
M 57
AD 9,7
AW 37,8
M 54
AW 61,9
UL 6,25
M 55
WL 0
FQ 100
UH 7,25
M 52
AR 8,85
AR 9,109
M 53
AD 8,6
AD 9,7
M 50
FZ 100
R 72
M 51
mw 24
R 74
M 48
TY 106,520
TY 110,14344
M 49
R 76
M 60
ad 84,624
W 0
M 61
AW 85,8
AW 109,9
M 62
UL 6,26
UH 7,26
M 63
W 1
AR 8,38
M 64
AR 9,62
AD 8,6
M 65
AD 9,7
AW 38,8
M 66
W 2
AW 62,9
M 67
WL 3
AN 104,106
AN 105,107
M 68
WL 2
SH 106
AN 106,106
M 69
mw 27
AN 107,107
AN 108,110
M 70
AN 109,111
SH 110
M 71
R 78
M 82
AN 110,110
AN 111,111
M 83
TY 114,2056
TY 118,15880
M 80
ad 84,630
W 0
M 81
UL 6,27
UH 7,27
M 78
AR 8,86
AR 9,110
M 79
W 1
AD 8,6
M 76
AD 9,7
AW 86,8
M 77
AW 110,9
UL 6,28
M 74
W 2
UH 7,28
M 75
AR 8,39
AR 9,63
M 72
WL 1
AN 112,114
AN 113,115
M 73
R 80
M 84
mw 48
SH 114
AN 114,114
M 85
WL 2
AN 115,115
AN 116,118
M 86
AN 117,119
SH 118
M 87
AN 118,118
AN 119,119
M 88
TY 122,3592
TY 126,17416
M 89
ad 84,672
W 0
M 90
SA 206,2304
AD 8,6
M 91
AD 9,7
AW 39,8
M 92
W 1
AW 63,9
M 93
UL 6,29
UH 7,29
M 94
AR 8,87
AR 9,111
M 95
R 82
M 106
W 2
AD 8,6
M 107
WL 3
AN 120,122
AN 121,123
M 104
WL 2
SH 122
AN 122,122
M 105
mw 51
AN 123,123
AN 124,126
M 102
AN 125,127
SH 126
M 103
AN 126,126
AN 127,127
M 100
ad 84,678
W 0
M 101
AD 9,7
AW 87,8
M 98
AW 111,9
M 99
W 1
M 96
M 97
R 0
M 108
W 2
M 109
M 110
M 111
bc 49,8,66,60
M 112
mh 18
M 113
ad 66,1568
H 0
M 114
M 115
M 116
DG 1024
M 117
M 118
M 119
WL 2
R 2
M 120
bc 48,8,64,56
M 121
mh 21
M 133
ad 64,1568
H 0
M 132
TX 58,0
TX 62,13824
M 134
M 135
H 1
M 123
M 122
TY 66,512
TY 70,14336
M 124
H 2
M 125
M 137
M 136
TX 74,1536
TX 78,15360
M 138
WL 4
Q 0
M 139
Q 2
M 127
Q 4
M 126
TY 82,2048
TY 86,15872
M 128
Q 6
M 129
WL 4
Q 8
M 141
Q 10
M 140
TX 90,3072
TX 94,16896
M 142
Q 12
M 143
R 6
M 131
Q 14
M 130
TY 98,3584
TY 102,17408
M 0
R 4
M 1
WL 10
Q 16
M 13
Q 18
)"
R"(M 12
Q 20
M 14
Q 22
M 15
WL 8
Q 24
M 3
Q 26
M 2
Q 28
M 4
Q 30
M 5
WL 6
Q 32
M 17
Q 34
M 16
Q 36
M 18
Q 38
M 19
WL 2
Q 40
M 7
Q 42
M 6
Q 44
M 8
Q 46
M 9
ae 42,72,0
M 21
SA 208,128
M 20
ae 44,68,384
M 22
s_bitcmp1_b32 s50, 8
s_cselect_b32 s51, 24576, 0
v_add_u32 v47, s51, v3
M 23
R 10
M 11
XL 38,0
M 10
WL 2
R 8
M 34
XL 40,768
M 35
XL 42,1536
M 32
XL 44,2304
M 33
M 30
M 31
M 28
M 29
M 26
M 27
WV 10
M 24
WL 0
FP
M 25
M 36
M 37
M 38
M 39
M 40
M 41
M 42
UL 6,30
UH 7,30
M 43
AR 8,40
AR 9,64
M 44
AD 8,6
AD 9,7
M 45
FR
AW 40,8
M 46
AW 64,9
UL 6,31
M 47
UH 7,31
AR 8,88
M 58
AR 9,112
AD 8,6
M 59
AD 9,7
AW 88,8
M 56
AW 112,9
UL 6,32
M 57
UH 7,32
AR 8,41
M 54
AR 9,65
AD 8,6
M 55
WL 0
FQ 104
AD 9,7
M 52
AW 41,8
AW 65,9
M 53
UL 6,33
UH 7,33
M 50
FZ 104
R 24
M 51
mw 72
R 26
M 48
TY 106,5128
TY 110,18952
M 49
R 28
M 60
ad 84,720
W 0
M 61
AR 8,89
AR 9,113
M 62
AD 8,6
AD 9,7
M 63
W 1
AW 89,8
M 64
AW 113,9
UL 6,34
M 65
UH 7,34
AR 8,42
M 66
W 2
AR 9,66
M 67
WL 3
AN 104,106
AN 105,107
M 68
WL 2
SH 106
AN 106,106
M 69
mw 75
AN 107,107
AN 108,110
M 70
AN 109,111
SH 110
M 71
R 30
M 82
AN 110,110
AN 111,111
M 83
TY 114,6664
TY 118,20488
M 80
ad 84,726
W 0
M 81
AD 8,6
AD 9,7
M 78
AW 42,8
AW 66,9
M 79
W 1
UL 6,35
M 76
UH 7,35
AR 8,90
M 77
AR 9,114
AD 8,6
M 74
W 2
AD 9,7
M 75
AW 90,8
AW 114,9
M 72
WL 1
AN 112,114
AN 113,115
M 73
R 32
M 84
mw 0
SH 114
AN 114,114
M 85
WL 2
AN 115,115
AN 116,118
M 86
AN 117,119
SH 118
M 87
AN 118,118
AN 119,119
M 88
TY 122,8200
TY 126,22024
M 89
ad 84,768
W 0
M 90
SA 210,896
UL 6,36
M 91
UH 7,36
AR 8,43
M 92
W 1
AR 9,67
M 93
AD 8,6
AD 9,7
M 94
AW 43,8
AW 67,9
M 95
R 34
M 106
W 2
UL 6,37
M 107
WL 3
AN 120,122
AN 121,123
M 104
WL 2
SH 122
AN 122,122
M 105
mw 3
AN 123,123
AN 124,126
M 102
AN 125,127
SH 126
M 103
AN 126,126
AN 127,127
M 100
ad 84,774
W 0
M 101
UH 7,37
AR 8,91
M 98
AR 9,115
AD 8,6
M 99
W 1
AD 9,7
M 96
AW 91,8
AW 115,9
M 97
R 48
M 108
W 2
M 109
M 110
M 111
bc 49,9,66,60
M 112
mh 0
M 113
ad 66,1760
H 0
M 114
M 115
M 116
DG 1024
M 117
M 118
M 119
WL 2
R 50
M 120
bc 48,9,64,56
M 121
mh 3
M 133
ad 64,1760
H 0
M 132
TX 58,4608
TX 62,18432
M 134
M 135
H 1
M 123
M 122
TY 66,5120
TY 70,18944
M 124
H 2
M 125
M 137
M 136
TX 74,6144
TX 78,19968
M 138
WL 4
Q 0
M 139
Q 2
M 127
Q 4
M 126
TY 82,6656
TY 86,20480
M 128
Q 6
M 129
WL 4
Q 8
M 141
Q 10
M 140
TX 90,7680
TX 94,21504
M 142
Q 12
M 143
R 54
M 131
Q 14
M 130
TY 98,8192
TY 102,22016
M 0
R 52
M 1
WL 10
Q 16
M 13
Q 18
M 12
Q 20
M 14
Q 22
M 15
WL 8
Q 24
M 3
Q 26
M 2
Q 28
M 4
Q 30
M 5
WL 6
Q 32
M 17
Q 34
M 16
Q 36
M 18
Q 38
M 19
WL 2
Q 40
M 7
Q 42
M 6
Q 44
M 8
Q 46
M 9
SA 212,1664
M 21
ae 44,68,384
M 20
s_bfe_u32 s51, s50, 0x10009
v_mad_i32_i24 v47, v49, s51, v48
XL 14,0
M 22
XL 16,768
M 23
R 58
M 11
XL 18,1536
)"
R"(M 10
WL 2
R 56
M 34
XL 20,2304
M 35
M 32
M 33
M 30
M 31
M 28
M 29
M 26
M 27
WV 10
M 24
WL 0
FP
M 25
M 36
M 37
M 38
M 39
M 40
M 41
M 42
M 43
M 44
UL 6,38
UH 7,38
M 45
FR
AR 8,44
M 46
AR 9,68
AD 8,6
M 47
AD 9,7
AW 44,8
M 58
AW 68,9
UL 6,39
M 59
UH 7,39
AR 8,92
M 56
AR 9,116
AD 8,6
M 57
AD 9,7
AW 92,8
M 54
AW 116,9
UL 6,40
M 55
WL 0
FQ 108
UH 7,40
M 52
AR 8,45
AR 9,69
M 53
AD 8,6
AD 9,7
M 50
FZ 108
R 72
M 51
mw 24
R 74
M 48
TY 106,9736
TY 110,23560
M 49
R 76
M 60
ad 84,816
W 0
M 61
AW 45,8
AW 69,9
M 62
UL 6,41
UH 7,41
M 63
W 1
AR 8,93
M 64
AR 9,117
AD 8,6
M 65
AD 9,7
AW 93,8
M 66
W 2
AW 117,9
M 67
WL 3
AN 104,106
AN 105,107
M 68
WL 2
SH 106
AN 106,106
M 69
mw 27
AN 107,107
AN 108,110
M 70
AN 109,111
SH 110
M 71
R 78
M 82
AN 110,110
AN 111,111
M 83
TY 114,11272
TY 118,25096
M 80
ad 84,822
W 0
M 81
UL 6,42
UH 7,42
M 78
AR 8,46
AR 9,70
M 79
W 1
AD 8,6
M 76
AD 9,7
AW 46,8
M 77
AW 70,9
UL 6,43
M 74
W 2
UH 7,43
M 75
SA 214,2432
AR 8,94
M 72
WL 1
AN 112,114
AN 113,115
M 73
R 80
M 84
mw 48
SH 114
AN 114,114
M 85
WL 2
AN 115,115
AN 116,118
M 86
AN 117,119
SH 118
M 87
AN 118,118
AN 119,119
M 88
TY 122,12808
TY 126,26632
M 89
ad 84,864
W 0
M 90
AR 9,118
AD 8,6
M 91
AD 9,7
AW 94,8
M 92
W 1
AW 118,9
M 93
UL 6,44
UH 7,44
M 94
AR 8,47
AR 9,71
M 95
R 82
M 106
W 2
AD 8,6
M 107
WL 3
AN 120,122
AN 121,123
M 104
WL 2
SH 122
AN 122,122
M 105
mw 51
AN 123,123
AN 124,126
M 102
AN 125,127
SH 126
M 103
AN 126,126
AN 127,127
M 100
ad 84,870
W 0
M 101
AD 9,7
AW 47,8
M 98
AW 71,9
UL 6,45
M 99
W 1
UH 7,45
M 96
AR 8,95
AR 9,119
M 97
R 0
M 108
W 2
AD 8,6
M 109
AD 9,7
AW 95,8
M 110
AW 119,9
M 111
bc 49,10,66,60
M 112
mh 9
M 113
ad 66,1952
H 0
M 114
M 115
M 116
DG 1024
M 117
M 118
M 119
WL 2
R 2
M 120
bc 48,10,64,56
M 121
mh 12
M 133
ad 64,1952
H 0
M 132
TX 58,9216
TX 62,23040
M 134
M 135
H 1
M 123
M 122
TY 66,9728
TY 70,23552
M 124
H 2
M 125
M 137
M 136
TX 74,10752
TX 78,24576
M 138
WL 4
Q 0
M 139
Q 2
M 127
Q 4
M 126
TY 82,11264
TY 86,25088
M 128
Q 6
M 129
WL 4
Q 8
M 141
Q 10
M 140
TX 90,12288
TX 94,26112
M 142
Q 12
M 143
R 6
M 131
Q 14
M 130
TY 98,12800
TY 102,26624
M 0
R 4
M 1
WL 10
Q 16
M 13
Q 18
M 12
Q 20
M 14
Q 22
M 15
WL 8
Q 24
M 3
Q 26
M 2
Q 28
M 4
Q 30
M 5
WL 6
Q 32
M 17
Q 34
M 16
Q 36
M 18
Q 38
M 19
WL 2
Q 40
M 7
Q 42
M 6
Q 44
M 8
Q 46
M 9
ae 42,72,0
M 21
s_bitcmp1_b32 s50, 2
s_cselect_b32 s52, 24576, 0
v_add_u32 v50, s52, v3
M 20
SA 216,256
M 22
ae 44,68,480
M 23
R 10
M 11
s_bitcmp1_b32 s50, 10
s_cselect_b32 s51, 24576, 0
v_add_u32 v47, s51, v3
M 10
WL 2
R 8
M 34
XL 22,0
M 35
XL 24,768
M 32
XL 26,1536
M 33
XL 28,2304
M 30
M 31
M 28
M 29
M 26
M 27
WV 10
M 24
WL 0
FP
M 25
M 36
M 37
M 38
M 39
M 40
M 41
M 42
M 43
M 44
M 45
FR
M 46
UL 6,14
UH 7,14
M 47
AR 8,48
AR 9,72
M 58
AD 8,6
AD 9,7
M 59
AW 48,8
AW 72,9
)"
R"(M 56
UL 6,15
UH 7,15
M 57
AR 8,96
AR 9,120
M 54
AD 8,6
AD 9,7
M 55
WL 0
FQ 112
AW 96,8
M 52
AW 120,9
UL 6,16
M 53
UH 7,16
AR 8,49
M 50
FZ 112
R 24
M 51
mw 72
R 26
M 48
TY 106,520
TY 110,14344
M 49
R 28
M 60
ad 84,912
W 0
M 61
AR 9,73
AD 8,6
M 62
AD 9,7
AW 49,8
M 63
W 1
AW 73,9
M 64
UL 6,17
UH 7,17
M 65
AR 8,97
AR 9,121
M 66
W 2
AD 8,6
M 67
WL 3
AN 104,106
AN 105,107
M 68
WL 2
SH 106
AN 106,106
M 69
mw 75
AN 107,107
AN 108,110
M 70
AN 109,111
SH 110
M 71
R 30
M 82
AN 110,110
AN 111,111
M 83
TY 114,2056
TY 118,15880
M 80
ad 84,918
W 0
M 81
AD 9,7
AW 97,8
M 78
AW 121,9
UL 6,18
M 79
W 1
UH 7,18
M 76
AR 8,50
AR 9,74
M 77
AD 8,6
AD 9,7
M 74
W 2
AW 50,8
M 75
AW 74,9
UL 6,19
M 72
WL 1
AN 112,114
AN 113,115
M 73
R 32
M 84
mw 0
SH 114
AN 114,114
M 85
WL 2
AN 115,115
AN 116,118
M 86
AN 117,119
SH 118
M 87
AN 118,118
AN 119,119
M 88
TY 122,3592
TY 126,17416
M 89
ad 84,960
W 0
M 90
SA 218,1024
UH 7,19
M 91
AR 8,98
AR 9,122
M 92
W 1
AD 8,6
M 93
AD 9,7
AW 98,8
M 94
AW 122,9
UL 6,20
M 95
R 34
M 106
W 2
UH 7,20
M 107
WL 3
AN 120,122
AN 121,123
M 104
WL 2
SH 122
AN 122,122
M 105
mw 3
AN 123,123
AN 124,126
M 102
AN 125,127
SH 126
M 103
AN 126,126
AN 127,127
M 100
ad 84,966
W 0
M 101
AR 8,51
AR 9,75
M 98
AD 8,6
AD 9,7
M 99
W 1
AW 51,8
M 96
AW 75,9
UL 6,21
M 97
R 48
M 108
W 2
UH 7,21
M 109
AR 8,99
AR 9,123
M 110
AD 8,6
AD 9,7
M 111
bc 49,11,66,60
AW 99,8
M 112
mh 18
AW 123,9
M 113
ad 66,2144
H 0
M 114
M 115
M 116
DG 1024
M 117
M 118
M 119
WL 2
R 50
M 120
bc 48,11,64,56
M 121
mh 21
M 133
ad 64,2144
H 0
M 132
TX 58,0
TX 62,13824
M 134
M 135
H 1
M 123
M 122
TY 66,512
TY 70,14336
M 124
H 2
M 125
M 137
M 136
TX 74,1536
TX 78,15360
PA 10240
DF
M 138
WL 4
Q 0
M 139
Q 2
M 127
Q 4
M 126
TY 82,2048
TY 86,15872
M 128
Q 6
M 129
WL 4
Q 8
M 141
Q 10
M 140
TX 90,3072
TX 94,16896
M 142
Q 12
M 143
R 54
M 131
Q 14
M 130
TY 98,3584
TY 102,17408
M 0
R 52
M 1
WL 10
Q 16
M 13
Q 18
M 12
Q 20
M 14
Q 22
M 15
WL 8
Q 24
M 3
Q 26
M 2
Q 28
M 4
Q 30
M 5
WL 6
Q 32
M 17
Q 34
M 16
Q 36
M 18
Q 38
M 19
WL 2
Q 40
M 7
Q 42
M 6
Q 44
M 8
Q 46
M 9
SA 220,1792
M 21
ae 44,68,480
M 20
s_bfe_u32 s51, s50, 0x1000b
v_mad_i32_i24 v47, v49, s51, v48
XL 30,0
M 22
XL 32,768
M 23
R 58
M 11
XL 34,1536
M 10
WL 2
R 56
M 34
XL 36,2304
M 35
M 32
M 33
M 30
M 31
M 28
M 29
M 26
M 27
WV 11
M 24
WL 0
FP
M 25
M 36
M 37
M 38
M 39
M 40
M 41
M 42
M 43
M 44
M 45
FR
M 46
M 47
M 58
UL 6,22
UH 7,22
M 59
AR 8,52
AR 9,76
M 56
AD 8,6
AD 9,7
M 57
AW 52,8
AW 76,9
M 54
UL 6,23
UH 7,23
M 55
WL 0
FQ 116
AR 8,100
M 52
AR 9,124
AD 8,6
M 53
AD 9,7
AW 100,8
M 50
FZ 116
R 72
M 51
mw 24
R 74
M 48
TY 106,5128
TY 110,18952
M 49
R 76
M 60
ad 84,1008
W 0
M 61
AW 124,9
UL 6,24
M 62
UH 7,24
AR 8,53
M 63
W 1
AR 9,77
M 64
AD 8,6
AD 9,7
M 65
AW 53,8
AW 77,9
M 66
)"
R"(W 2
UL 6,25
M 67
WL 3
AN 104,106
AN 105,107
M 68
WL 2
SH 106
AN 106,106
M 69
mw 27
AN 107,107
AN 108,110
M 70
AN 109,111
SH 110
M 71
R 78
M 82
AN 110,110
AN 111,111
M 83
TY 114,6664
TY 118,20488
M 80
ad 84,1014
W 0
M 81
UH 7,25
AR 8,101
M 78
AR 9,125
AD 8,6
M 79
W 1
AD 9,7
M 76
AW 101,8
AW 125,9
M 77
UL 6,26
UH 7,26
M 74
W 2
AR 8,54
M 75
SA 222,2560
AR 9,78
M 72
WL 1
AN 112,114
AN 113,115
M 73
R 80
M 84
mw 48
SH 114
AN 114,114
M 85
WL 2
AN 115,115
AN 116,118
M 86
AN 117,119
SH 118
M 87
AN 118,118
AN 119,119
M 88
TY 122,8200
TY 126,22024
M 89
ad 84,1056
W 0
M 90
AD 8,6
AD 9,7
M 91
AW 54,8
AW 78,9
M 92
W 1
UL 6,27
M 93
UH 7,27
AR 8,102
M 94
AR 9,126
AD 8,6
M 95
R 82
M 106
W 2
AD 9,7
M 107
WL 3
AN 120,122
AN 121,123
M 104
WL 2
SH 122
AN 122,122
M 105
mw 51
AN 123,123
AN 124,126
M 102
AN 125,127
SH 126
M 103
AN 126,126
AN 127,127
M 100
ad 84,1062
W 0
M 101
AW 102,8
AW 126,9
M 98
UL 6,28
UH 7,28
M 99
W 1
AR 8,55
M 96
AR 9,79
AD 8,6
M 97
R 0
M 108
W 2
AD 9,7
M 109
AW 55,8
AW 79,9
M 110
UL 6,29
UH 7,29
M 111
bc 49,12,66,60
AR 8,103
M 112
mh 0
AR 9,127
AD 8,6
M 113
ad 66,2336
H 0
M 114
AD 9,7
AW 103,8
M 115
AW 127,9
M 116
DG 1024
M 117
M 118
M 119
WL 2
R 2
M 120
bc 48,12,64,56
M 121
mh 3
M 133
ad 64,2336
H 0
M 132
TX 58,4608
TX 62,18432
M 134
M 135
H 1
M 123
M 122
TY 66,5120
TY 70,18944
M 124
H 2
M 125
M 137
M 136
TX 74,6144
TX 78,19968
M 138
WL 4
Q 0
M 139
Q 2
M 127
Q 4
M 126
TY 82,6656
TY 86,20480
M 128
Q 6
M 129
WL 4
Q 8
M 141
Q 10
M 140
TX 90,7680
TX 94,21504
M 142
Q 12
M 143
R 6
M 131
Q 14
M 130
TY 98,8192
TY 102,22016
M 0
R 4
M 1
WL 10
Q 16
M 13
Q 18
M 12
Q 20
M 14
Q 22
M 15
WL 8
Q 24
M 3
Q 26
M 2
Q 28
M 4
Q 30
M 5
WL 6
Q 32
M 17
Q 34
M 16
Q 36
M 18
Q 38
M 19
WL 2
Q 40
M 7
Q 42
M 6
Q 44
M 8
Q 46
M 9
ae 42,72,96
M 21
s_bitcmp1_b32 s50, 4
s_cselect_b32 s52, 24576, 0
v_add_u32 v50, s52, v3
M 20
SA 224,0
M 22
M 23
R 10
M 11
M 10
WL 2
R 8
M 34
M 35
M 32
M 33
M 30
M 31
M 28
M 29
M 26
ae 44,70,384
M 27
WV 6
M 24
WL 0
FP
M 25
M 36
s_bitcmp1_b32 s50, 6
s_cselect_b32 s51, 24576, 0
v_add_u32 v47, s51, v3
M 37
XL 38,128
M 38
XL 40,896
M 39
M 40
M 41
M 42
M 43
M 44
XL 42,1664
M 45
FR
M 46
M 47
M 58
M 59
M 56
XL 44,2432
UL 6,30
M 57
UH 7,30
AR 8,56
M 54
AR 9,80
AD 8,6
M 55
WL 0
FQ 120
AD 9,7
M 52
AW 56,8
AW 80,9
M 53
UL 6,31
UH 7,31
M 50
FZ 120
R 24
M 51
mw 72
R 26
M 48
TY 106,9736
TY 110,23560
M 49
R 28
M 60
ad 84,1104
W 0
M 61
AR 8,104
AR 9,128
M 62
AD 8,6
AD 9,7
M 63
W 1
AW 104,8
M 64
AW 128,9
UL 6,32
M 65
UH 7,32
AR 8,57
M 66
W 2
AR 9,81
M 67
WL 3
AN 104,106
AN 105,107
M 68
WL 2
SH 106
AN 106,106
M 69
mw 75
AN 107,107
AN 108,110
M 70
AN 109,111
SH 110
M 71
R 30
M 82
AN 110,110
AN 111,111
M 83
TY 114,11272
TY 118,25096
M 80
ad 84,1110
W 0
M 81
AD 8,6
AD 9,7
M 78
AW 57,8
AW 81,9
M 79
W 1
UL 6,33
M 76
UH 7,33
AR 8,105
M 77
AR 9,129
AD 8,6
M 74
W 2
AD 9,7
M 75
)"
R"(AW 105,8
AW 129,9
M 72
WL 1
AN 112,114
AN 113,115
M 73
R 32
M 84
mw 0
SH 114
AN 114,114
M 85
WL 2
AN 115,115
AN 116,118
M 86
AN 117,119
SH 118
M 87
AN 118,118
AN 119,119
M 88
TY 122,12808
TY 126,26632
M 89
ad 84,1152
W 0
M 90
SA 226,768
UL 6,34
M 91
UH 7,34
AR 8,58
M 92
W 1
AR 9,82
M 93
AD 8,6
AD 9,7
M 94
AW 58,8
AW 82,9
M 95
R 34
M 106
W 2
UL 6,35
M 107
WL 3
AN 120,122
AN 121,123
M 104
WL 2
SH 122
AN 122,122
M 105
mw 3
AN 123,123
AN 124,126
M 102
AN 125,127
SH 126
M 103
AN 126,126
AN 127,127
M 100
ad 84,1158
W 0
M 101
UH 7,35
AR 8,106
M 98
AR 9,130
AD 8,6
M 99
W 1
AD 9,7
M 96
AW 106,8
AW 130,9
M 97
R 48
M 108
W 2
UL 6,36
M 109
UH 7,36
AR 8,59
M 110
AR 9,83
AD 8,6
M 111
bc 49,13,66,60
AD 9,7
M 112
mh 9
AW 59,8
AW 83,9
M 113
ad 66,2528
H 0
M 114
UL 6,37
UH 7,37
M 115
AR 8,107
AR 9,131
M 116
DG 1024
M 117
AD 8,6
AD 9,7
M 118
AW 107,8
AW 131,9
M 119
WL 0
R 50
M 120
bc 48,13,64,56
M 121
mh 12
M 133
ad 64,2528
H 0
M 132
TX 58,9216
TX 62,23040
M 134
M 135
H 1
M 123
M 122
TY 66,9728
TY 70,23552
M 124
H 2
M 125
M 137
M 136
TX 74,10752
TX 78,24576
M 138
WL 4
Q 0
M 139
Q 2
M 127
Q 4
M 126
TY 82,11264
TY 86,25088
M 128
Q 6
M 129
WL 4
Q 8
M 141
Q 10
M 140
TX 90,12288
TX 94,26112
M 142
Q 12
M 143
R 54
M 131
Q 14
M 130
TY 98,12800
TY 102,26624
M 0
R 52
M 1
WL 10
Q 16
M 13
Q 18
M 12
Q 20
M 14
Q 22
M 15
WL 8
Q 24
M 3
Q 26
M 2
Q 28
M 4
Q 30
M 5
WL 6
Q 32
M 17
Q 34
M 16
Q 36
M 18
Q 38
M 19
WL 2
Q 40
M 7
Q 42
M 6
Q 44
M 8
Q 46
M 9
SA 228,1536
M 21
M 20
M 22
M 23
R 58
M 11
M 10
WL 2
R 56
M 34
M 35
M 32
M 33
M 30
M 31
M 28
M 29
M 26
M 27
WV 6
M 24
WL 0
FP
M 25
M 36
ae 44,70,384
M 37
s_bfe_u32 s51, s50, 0x10007
v_mad_i32_i24 v47, v49, s51, v48
XL 14,128
M 38
M 39
M 40
XL 16,896
M 41
M 42
M 43
M 44
M 45
FR
M 46
XL 18,1664
M 47
M 58
M 59
M 56
M 57
M 54
XL 20,2432
M 55
WL 0
FQ 124
M 52
M 53
M 50
FZ 124
R 72
M 51
mw 24
R 74
M 48
TY 106,520
TY 110,14344
M 49
R 76
M 60
ad 84,1200
W 0
M 61
M 62
M 63
W 1
M 64
M 65
M 66
W 2
M 67
WL 3
AN 104,106
AN 105,107
M 68
WL 2
SH 106
AN 106,106
M 69
mw 27
AN 107,107
AN 108,110
M 70
AN 109,111
SH 110
M 71
R 78
M 82
AN 110,110
AN 111,111
M 83
TY 114,2056
TY 118,15880
M 80
ad 84,1206
W 0
M 81
M 78
M 79
W 1
M 76
M 77
M 74
W 2
M 75
SA 230,2304
M 72
WL 1
AN 112,114
AN 113,115
M 73
R 80
M 84
mw 48
SH 114
AN 114,114
M 85
WL 2
AN 115,115
AN 116,118
M 86
AN 117,119
SH 118
M 87
AN 118,118
AN 119,119
M 88
TY 122,3592
TY 126,17416
M 89
ad 84,1248
W 0
M 90
UL 6,38
UH 7,38
M 91
AR 8,132
AR 9,156
M 92
W 1
AD 8,6
M 93
AD 9,7
AW 132,8
M 94
AW 156,9
UL 6,39
M 95
R 82
M 106
W 2
UH 7,39
M 107
WL 3
AN 120,122
AN 121,123
M 104
WL 2
SH 122
AN 122,122
M 105
mw 51
AN 123,123
AN 124,126
M 102
AN 125,127
SH 126
M 103
AN 126,126
AN 127,127
M 100
ad 84,1254
W 0
M 101
AR 8,180
AD 8,6
M 98
AD 136,7
AW 180,8
M 99
W 1
UL 6,40
)"
R"(M 96
UH 7,40
AR 8,133
M 97
R 0
M 108
W 2
AR 9,157
M 109
AD 8,6
AD 9,7
M 110
AW 133,8
AW 157,9
M 111
bc 49,14,66,60
UL 6,41
M 112
mh 18
UH 7,41
AR 8,181
M 113
ad 66,2720
H 0
M 114
AD 8,6
AD 137,7
M 115
AW 181,8
UL 6,42
M 116
DG 1024
M 117
UH 7,42
AR 8,134
M 118
AR 9,158
AD 8,6
M 119
WL 0
R 2
M 120
bc 48,14,64,56
AD 9,7
M 121
mh 21
AW 134,8
AW 158,9
M 133
ad 64,2720
H 0
M 132
TX 58,0
TX 62,13824
M 134
UL 6,43
UH 7,43
M 135
H 1
AR 8,182
M 123
AD 8,6
AD 138,7
M 122
TY 66,512
TY 70,14336
M 124
H 2
AW 182,8
M 125
UL 6,44
UH 7,44
M 137
AR 8,135
AR 9,159
M 136
TX 74,1536
TX 78,15360
M 138
WL 4
Q 0
M 139
Q 2
M 127
Q 4
M 126
TY 82,2048
TY 86,15872
M 128
Q 6
M 129
WL 4
Q 8
M 141
Q 10
M 140
TX 90,3072
TX 94,16896
M 142
Q 12
M 143
R 6
M 131
Q 14
M 130
TY 98,3584
TY 102,17408
M 0
R 4
M 1
WL 10
Q 16
M 13
Q 18
M 12
Q 20
M 14
Q 22
M 15
WL 8
Q 24
M 3
Q 26
M 2
Q 28
M 4
Q 30
M 5
WL 6
Q 32
M 17
Q 34
M 16
Q 36
M 18
Q 38
M 19
WL 2
Q 40
M 7
Q 42
M 6
Q 44
M 8
Q 46
M 9
ae 42,72,96
M 21
s_bfe_u32 s52, s50, 0x10005
v_mad_i32_i24 v50, v49, s52, v48
SA 232,0
M 20
AD 8,6
AD 9,7
M 22
AW 135,8
AW 159,9
M 23
R 10
M 11
UL 6,45
UH 7,45
M 10
WL 2
R 8
M 34
AR 8,183
AD 8,6
M 35
AD 139,7
AW 183,8
M 32
M 33
M 30
M 31
M 28
M 29
M 26
M 27
WV 6
M 24
WL 0
FP
M 25
M 36
ae 44,68,384
M 37
s_bitcmp1_b32 s50, 8
s_cselect_b32 s51, 24576, 0
v_add_u32 v47, s51, v3
M 38
XL 22,128
M 39
M 40
M 41
M 42
XL 24,896
M 43
M 44
M 45
FR
M 46
M 47
M 58
XL 26,1664
M 59
M 56
M 57
M 54
M 55
WL 0
FQ 128
M 52
XL 28,2432
M 53
M 50
FZ 128
R 24
M 51
mw 72
R 26
M 48
TY 106,5128
TY 110,18952
M 49
R 28
M 60
ad 84,1296
W 0
M 61
M 62
M 63
W 1
M 64
M 65
M 66
W 2
M 67
WL 3
AN 104,106
AN 105,107
M 68
WL 2
SH 106
AN 106,106
M 69
mw 75
AN 107,107
AN 108,110
M 70
AN 109,111
SH 110
M 71
R 30
M 82
AN 110,110
AN 111,111
M 83
TY 114,6664
TY 118,20488
M 80
ad 84,1302
W 0
M 81
M 78
M 79
W 1
M 76
M 77
M 74
W 2
M 75
M 72
WL 1
AN 112,114
AN 113,115
M 73
R 32
M 84
mw 0
SH 114
AN 114,114
M 85
WL 2
AN 115,115
AN 116,118
M 86
AN 117,119
SH 118
M 87
AN 118,118
AN 119,119
M 88
TY 122,8200
TY 126,22024
M 89
ad 84,1344
W 0
M 90
SA 234,768
UL 6,14
M 91
UH 7,14
AR 8,136
M 92
W 1
AR 9,160
M 93
AD 8,6
AD 9,7
M 94
AW 136,8
AW 160,9
M 95
R 34
M 106
W 2
UL 6,15
M 107
WL 3
AN 120,122
AN 121,123
M 104
WL 2
SH 122
AN 122,122
M 105
mw 3
AN 123,123
AN 124,126
M 102
AN 125,127
SH 126
M 103
AN 126,126
AN 127,127
M 100
ad 84,1350
W 0
M 101
UH 7,15
AR 8,184
M 98
AD 8,6
AD 140,7
M 99
W 1
AW 184,8
M 96
UL 6,16
UH 7,16
M 97
R 48
M 108
W 2
AR 8,137
M 109
AR 9,161
AD 8,6
M 110
AD 9,7
AW 137,8
M 111
bc 49,15,66,60
AW 161,9
M 112
mh 0
UL 6,17
UH 7,17
M 113
ad 66,2912
H 0
M 114
AR 8,185
AD 8,6
M 115
AD 141,7
AW 185,8
M 116
DG 1024
M 117
UL 6,18
UH 7,18
M 118
AR 8,138
AR 9,162
M 119
WL 0
R 50
M 120
bc 48,15,64,56
AD 8,6
M 121
mh 3
AD 9,7
AW 138,8
M 133
ad 64,2912
H 0
M 132
)"
R"(TX 58,4608
TX 62,18432
M 134
AW 162,9
UL 6,19
M 135
H 1
UH 7,19
M 123
AR 8,186
AD 8,6
M 122
TY 66,5120
TY 70,18944
M 124
H 2
AD 142,7
M 125
AW 186,8
UL 6,20
M 137
UH 7,20
AR 8,139
M 136
TX 74,6144
TX 78,19968
PA 10240
DF
M 138
WL 4
Q 0
M 139
Q 2
M 127
Q 4
M 126
TY 82,6656
TY 86,20480
M 128
Q 6
M 129
WL 4
Q 8
M 141
Q 10
M 140
TX 90,7680
TX 94,21504
M 142
Q 12
M 143
R 54
M 131
Q 14
M 130
TY 98,8192
TY 102,22016
M 0
R 52
M 1
WL 10
Q 16
M 13
Q 18
M 12
Q 20
M 14
Q 22
M 15
WL 8
Q 24
M 3
Q 26
M 2
Q 28
M 4
Q 30
M 5
WL 6
Q 32
M 17
Q 34
M 16
Q 36
M 18
Q 38
M 19
WL 2
Q 40
M 7
Q 42
M 6
Q 44
M 8
Q 46
M 9
SA 236,1536
AR 9,163
M 21
AD 8,6
AD 9,7
M 20
AW 139,8
AW 163,9
M 22
UL 6,21
UH 7,21
M 23
R 58
M 11
AR 8,187
AD 8,6
M 10
WL 2
R 56
M 34
AD 143,7
AW 187,8
M 35
M 32
M 33
M 30
M 31
M 28
M 29
M 26
M 27
WV 7
M 24
WL 0
FP
M 25
M 36
M 37
M 38
ae 44,68,384
M 39
s_bfe_u32 s51, s50, 0x10009
v_mad_i32_i24 v47, v49, s51, v48
XL 30,128
M 40
M 41
M 42
M 43
M 44
XL 32,896
M 45
FR
M 46
M 47
M 58
M 59
M 56
XL 34,1664
M 57
M 54
M 55
WL 0
FQ 132
M 52
M 53
M 50
FZ 132
R 72
M 51
mw 24
R 74
M 48
TY 106,9736
TY 110,23560
M 49
R 76
M 60
ad 84,1392
W 0
M 61
XL 36,2432
M 62
M 63
W 1
M 64
M 65
M 66
W 2
M 67
WL 3
AN 104,106
AN 105,107
M 68
WL 2
SH 106
AN 106,106
M 69
mw 27
AN 107,107
AN 108,110
M 70
AN 109,111
SH 110
M 71
R 78
M 82
AN 110,110
AN 111,111
M 83
TY 114,11272
TY 118,25096
M 80
ad 84,1398
W 0
M 81
M 78
M 79
W 1
M 76
M 77
M 74
W 2
M 75
SA 238,2304
M 72
WL 1
AN 112,114
AN 113,115
M 73
R 80
M 84
mw 48
SH 114
AN 114,114
M 85
WL 2
AN 115,115
AN 116,118
M 86
AN 117,119
SH 118
M 87
AN 118,118
AN 119,119
M 88
TY 122,12808
TY 126,26632
M 89
ad 84,1440
W 0
M 90
M 91
M 92
W 1
UL 6,22
M 93
UH 7,22
AR 8,140
M 94
AR 9,164
AD 8,6
M 95
R 82
M 106
W 2
AD 9,7
M 107
WL 3
AN 120,122
AN 121,123
M 104
WL 2
SH 122
AN 122,122
M 105
mw 51
AN 123,123
AN 124,126
M 102
AN 125,127
SH 126
M 103
AN 126,126
AN 127,127
M 100
ad 84,1446
W 0
M 101
AW 140,8
AW 164,9
M 98
UL 6,23
UH 7,23
M 99
W 1
AR 8,188
M 96
AD 8,6
AD 144,7
M 97
R 0
M 108
W 2
AW 188,8
M 109
UL 6,24
UH 7,24
M 110
AR 8,141
AR 9,165
M 111
bc 49,16,66,60
AD 8,6
M 112
mh 9
AD 9,7
AW 141,8
M 113
ad 66,3104
H 0
M 114
AW 165,9
UL 6,25
M 115
UH 7,25
AR 8,189
M 116
DG 1024
M 117
AD 8,6
AD 145,7
M 118
AW 189,8
UL 6,26
M 119
WL 0
R 2
M 120
bc 48,16,64,56
UH 7,26
M 121
mh 12
AR 8,142
AR 9,166
M 133
ad 64,3104
H 0
M 132
TX 58,9216
TX 62,23040
M 134
AD 8,6
AD 9,7
M 135
H 1
AW 142,8
M 123
AW 166,9
UL 6,27
M 122
TY 66,9728
TY 70,23552
M 124
H 2
UH 7,27
M 125
AR 8,190
AD 8,6
M 137
AD 146,7
AW 190,8
M 136
TX 74,10752
TX 78,24576
M 138
WL 4
Q 0
M 139
Q 2
M 127
Q 4
M 126
TY 82,11264
TY 86,25088
M 128
Q 6
M 129
WL 4
Q 8
M 141
Q 10
M 140
TX 90,12288
TX 94,26112
M 142
Q 12
M 143
R 6
M 131
Q 14
M 130
TY 98,12800
TY 102,26624
M 0
)"
R"(R 4
M 1
WL 10
Q 16
M 13
Q 18
M 12
Q 20
M 14
Q 22
M 15
WL 8
Q 24
M 3
Q 26
M 2
Q 28
M 4
Q 30
M 5
WL 6
Q 32
M 17
Q 34
M 16
Q 36
M 18
Q 38
M 19
WL 2
Q 40
M 7
Q 42
M 6
Q 44
M 8
Q 46
M 9
ae 42,72,96
M 21
s_bitcmp1_b32 s50, 4
s_cselect_b32 s52, 24576, 0
v_add_u32 v50, s52, v3
M 20
SA 240,256
UL 6,28
M 22
UH 7,28
AR 8,143
M 23
R 10
M 11
AR 9,167
AD 8,6
M 10
WL 2
R 8
M 34
AD 9,7
AW 143,8
M 35
AW 167,9
UL 6,29
M 32
UH 7,29
AR 8,191
M 33
AD 8,6
AD 147,7
M 30
AW 191,8
M 31
M 28
M 29
M 26
M 27
WV 6
M 24
WL 0
FP
M 25
M 36
M 37
M 38
M 39
M 40
ae 44,68,480
M 41
s_bitcmp1_b32 s50, 10
s_cselect_b32 s51, 24576, 0
v_add_u32 v47, s51, v3
M 42
XL 38,128
M 43
M 44
M 45
FR
M 46
XL 40,896
M 47
M 58
M 59
M 56
M 57
M 54
XL 42,1664
M 55
WL 0
FQ 136
M 52
M 53
M 50
FZ 136
R 24
M 51
mw 72
R 26
M 48
TY 106,520
TY 110,14344
M 49
R 28
M 60
ad 84,1488
W 0
M 61
XL 44,2432
M 62
M 63
W 1
M 64
M 65
M 66
W 2
M 67
WL 3
AN 104,106
AN 105,107
M 68
WL 2
SH 106
AN 106,106
M 69
mw 75
AN 107,107
AN 108,110
M 70
AN 109,111
SH 110
M 71
R 30
M 82
AN 110,110
AN 111,111
M 83
TY 114,2056
TY 118,15880
M 80
ad 84,1494
W 0
M 81
M 78
M 79
W 1
M 76
M 77
M 74
W 2
M 75
M 72
WL 1
AN 112,114
AN 113,115
M 73
R 32
M 84
mw 0
SH 114
AN 114,114
M 85
WL 2
AN 115,115
AN 116,118
M 86
AN 117,119
SH 118
M 87
AN 118,118
AN 119,119
M 88
TY 122,3592
TY 126,17416
M 89
ad 84,1536
W 0
M 90
SA 242,1024
M 91
M 92
W 1
M 93
M 94
UL 6,30
UH 7,30
M 95
R 34
M 106
W 2
AR 8,144
M 107
WL 3
AN 120,122
AN 121,123
M 104
WL 2
SH 122
AN 122,122
M 105
mw 3
AN 123,123
AN 124,126
M 102
AN 125,127
SH 126
M 103
AN 126,126
AN 127,127
M 100
ad 84,1542
W 0
M 101
AR 9,168
AD 8,6
M 98
AD 9,7
AW 144,8
M 99
W 1
AW 168,9
M 96
UL 6,31
UH 7,31
M 97
R 48
M 108
W 2
AR 8,192
M 109
AD 8,6
AD 148,7
M 110
AW 192,8
UL 6,32
M 111
bc 49,17,66,60
UH 7,32
M 112
mh 18
AR 8,145
AR 9,169
M 113
ad 66,3296
H 0
M 114
AD 8,6
AD 9,7
M 115
AW 145,8
AW 169,9
M 116
DG 1024
M 117
UL 6,33
UH 7,33
M 118
AR 8,193
AD 8,6
M 119
WL 0
R 50
M 120
bc 48,17,64,56
AD 149,7
M 121
mh 21
AW 193,8
UL 6,34
M 133
ad 64,3296
H 0
M 132
TX 58,0
TX 62,13824
M 134
UH 7,34
AR 8,146
M 135
H 1
AR 9,170
M 123
AD 8,6
AD 9,7
M 122
TY 66,512
TY 70,14336
M 124
H 2
AW 146,8
M 125
AW 170,9
UL 6,35
M 137
UH 7,35
AR 8,194
M 136
TX 74,1536
TX 78,15360
M 138
WL 4
Q 0
M 139
Q 2
M 127
Q 4
M 126
TY 82,2048
TY 86,15872
M 128
Q 6
M 129
WL 4
Q 8
M 141
Q 10
M 140
TX 90,3072
TX 94,16896
M 142
Q 12
M 143
R 54
M 131
Q 14
M 130
TY 98,3584
TY 102,17408
M 0
R 52
M 1
WL 10
Q 16
M 13
Q 18
M 12
Q 20
M 14
Q 22
M 15
WL 8
Q 24
M 3
Q 26
M 2
Q 28
M 4
Q 30
M 5
WL 6
Q 32
M 17
Q 34
M 16
Q 36
M 18
Q 38
M 19
WL 2
Q 40
M 7
Q 42
M 6
Q 44
M 8
Q 46
M 9
SA 244,1792
AD 8,6
M 21
AD 150,7
AW 194,8
M 20
UL 6,36
UH 7,36
M 22
AR 8,147
AR 9,171
M 23
R 58
M 11
AD 8,6
)"
R"(AD 9,7
M 10
WL 2
R 56
M 34
AW 147,8
AW 171,9
M 35
UL 6,37
UH 7,37
M 32
AR 8,195
AD 8,6
M 33
AD 151,7
AW 195,8
M 30
M 31
M 28
M 29
M 26
M 27
WV 6
M 24
WL 0
FP
M 25
M 36
M 37
M 38
M 39
M 40
M 41
M 42
ae 44,68,480
M 43
s_bfe_u32 s51, s50, 0x1000b
v_mad_i32_i24 v47, v49, s51, v48
XL 14,128
M 44
M 45
FR
M 46
M 47
M 58
XL 16,896
M 59
M 56
M 57
M 54
M 55
WL 0
FQ 140
M 52
XL 18,1664
M 53
M 50
FZ 140
R 72
M 51
mw 24
R 74
M 48
TY 106,5128
TY 110,18952
M 49
R 76
M 60
ad 84,1584
W 0
M 61
XL 20,2432
M 62
M 63
W 1
M 64
M 65
M 66
W 2
M 67
WL 3
AN 104,106
AN 105,107
M 68
WL 2
SH 106
AN 106,106
M 69
mw 27
AN 107,107
AN 108,110
M 70
AN 109,111
SH 110
M 71
R 78
M 82
AN 110,110
AN 111,111
M 83
TY 114,6664
TY 118,20488
M 80
ad 84,1590
W 0
M 81
M 78
M 79
W 1
M 76
M 77
M 74
W 2
M 75
SA 246,2560
M 72
WL 1
AN 112,114
AN 113,115
M 73
R 80
M 84
mw 48
SH 114
AN 114,114
M 85
WL 2
AN 115,115
AN 116,118
M 86
AN 117,119
SH 118
M 87
AN 118,118
AN 119,119
M 88
TY 122,8200
TY 126,22024
M 89
ad 84,1632
W 0
M 90
M 91
M 92
W 1
M 93
M 94
M 95
R 82
M 106
W 2
UL 6,38
M 107
WL 3
AN 120,122
AN 121,123
M 104
WL 2
SH 122
AN 122,122
M 105
mw 51
AN 123,123
AN 124,126
M 102
AN 125,127
SH 126
M 103
AN 126,126
AN 127,127
M 100
ad 84,1638
W 0
M 101
UH 7,38
AR 8,148
M 98
AR 9,172
AD 8,6
M 99
W 1
AD 9,7
M 96
AW 148,8
AW 172,9
M 97
R 0
M 108
W 2
UL 6,39
M 109
UH 7,39
AR 8,196
M 110
AD 8,6
AD 152,7
M 111
bc 49,18,66,60
AW 196,8
M 112
mh 0
UL 6,40
UH 7,40
M 113
ad 66,3488
H 0
M 114
AR 8,149
AR 9,173
M 115
AD 8,6
AD 9,7
M 116
DG 1024
M 117
AW 149,8
AW 173,9
M 118
UL 6,41
UH 7,41
M 119
WL 0
R 2
M 120
bc 48,18,64,56
AR 8,197
M 121
mh 3
AD 8,6
AD 153,7
M 133
ad 64,3488
H 0
M 132
TX 58,4608
TX 62,18432
M 134
AW 197,8
UL 6,42
M 135
H 1
UH 7,42
M 123
AR 8,150
AR 9,174
M 122
TY 66,5120
TY 70,18944
M 124
H 2
AD 8,6
M 125
AD 9,7
AW 150,8
M 137
AW 174,9
UL 6,43
M 136
TX 74,6144
TX 78,19968
M 138
WL 4
Q 0
M 139
Q 2
M 127
Q 4
M 126
TY 82,6656
TY 86,20480
M 128
Q 6
M 129
WL 4
Q 8
M 141
Q 10
M 140
TX 90,7680
TX 94,21504
M 142
Q 12
M 143
R 6
M 131
Q 14
M 130
TY 98,8192
TY 102,22016
M 0
R 4
M 1
WL 10
Q 16
M 13
Q 18
M 12
Q 20
M 14
Q 22
M 15
WL 8
Q 24
M 3
Q 26
M 2
Q 28
M 4
Q 30
M 5
WL 6
Q 32
M 17
Q 34
M 16
Q 36
M 18
Q 38
M 19
WL 2
Q 40
M 7
Q 42
M 6
Q 44
M 8
Q 46
M 9
ae 42,72,96
M 21
s_bfe_u32 s52, s50, 0x10005
v_mad_i32_i24 v50, v49, s52, v48
SA 248,256
M 20
UH 7,43
AR 8,198
M 22
AD 8,6
AD 154,7
M 23
R 10
M 11
AW 198,8
UL 6,44
M 10
WL 2
R 8
M 34
UH 7,44
AR 8,151
M 35
AR 9,175
AD 8,6
M 32
AD 9,7
AW 151,8
M 33
AW 175,9
UL 6,45
M 30
UH 7,45
AR 8,199
M 31
AD 8,6
AD 155,7
M 28
AW 199,8
M 29
M 26
M 27
WV 6
M 24
WL 0
FP
M 25
M 36
M 37
M 38
M 39
M 40
M 41
M 42
M 43
M 44
M 45
FR
M 46
M 47
M 58
M 59
M 56
M 57
M 54
M 55
WL 0
FQ 144
M 52
M 53
M 50
FZ 144
)"
R"(R 24
M 51
mw 72
R 26
M 48
TY 106,9736
TY 110,23560
M 49
R 28
M 60
ad 84,1680
W 0
M 61
M 62
M 63
W 1
M 64
M 65
M 66
W 2
M 67
WL 3
AN 104,106
AN 105,107
M 68
WL 2
SH 106
AN 106,106
M 69
mw 75
AN 107,107
AN 108,110
M 70
AN 109,111
SH 110
M 71
R 30
M 82
AN 110,110
AN 111,111
M 83
TY 114,11272
TY 118,25096
M 80
ad 84,1686
W 0
M 81
ae 44,70,384
M 78
s_bitcmp1_b32 s50, 6
s_cselect_b32 s51, 24576, 0
v_add_u32 v47, s51, v3
M 79
W 1
XL 22,256
M 76
XL 24,1024
M 77
M 74
W 2
M 75
M 72
WL 1
AN 112,114
AN 113,115
M 73
R 32
M 84
mw 0
SH 114
AN 114,114
M 85
WL 2
AN 115,115
AN 116,118
M 86
AN 117,119
SH 118
M 87
AN 118,118
AN 119,119
M 88
TY 122,12808
TY 126,26632
M 89
ad 84,1728
W 0
M 90
SA 250,1024
XL 26,1792
M 91
XL 28,2560
M 92
W 1
M 93
M 94
M 95
R 34
M 106
W 2
M 107
WL 3
AN 120,122
AN 121,123
M 104
WL 2
SH 122
AN 122,122
M 105
mw 3
AN 123,123
AN 124,126
M 102
AN 125,127
SH 126
M 103
AN 126,126
AN 127,127
M 100
ad 84,1734
W 0
M 101
UL 6,14
UH 7,14
M 98
AR 8,152
AR 9,176
M 99
W 1
AD 8,6
M 96
AD 9,7
AW 152,8
M 97
R 48
M 108
W 2
AW 176,9
M 109
UL 6,15
UH 7,15
M 110
AD 132,6
AD 156,7
M 111
bc 49,19,66,60
UL 6,16
M 112
mh 9
UH 7,16
AR 8,153
M 113
ad 66,3680
H 0
M 114
AR 9,177
AD 8,6
M 115
AD 9,7
AW 153,8
M 116
DG 1024
M 117
AW 177,9
UL 6,17
M 118
UH 7,17
AD 133,6
M 119
WL 0
R 50
M 120
bc 48,19,64,56
AD 157,7
M 121
mh 12
UL 6,18
UH 7,18
M 133
ad 64,3680
H 0
M 132
TX 58,9216
TX 62,23040
M 134
AR 8,154
AR 9,178
M 135
H 1
AD 8,6
M 123
AD 9,7
AW 154,8
M 122
TY 66,9728
TY 70,23552
M 124
H 2
AW 178,9
M 125
UL 6,19
UH 7,19
M 137
AD 134,6
AD 158,7
M 136
TX 74,10752
TX 78,24576
PA 10240
DF
M 138
WL 4
Q 0
M 139
Q 2
M 127
Q 4
M 126
TY 82,11264
TY 86,25088
M 128
Q 6
M 129
WL 4
Q 8
M 141
Q 10
M 140
TX 90,12288
TX 94,26112
M 142
Q 12
M 143
R 54
M 131
Q 14
M 130
TY 98,12800
TY 102,26624
M 0
R 52
M 1
WL 10
Q 16
M 13
Q 18
M 12
Q 20
M 14
Q 22
M 15
WL 8
Q 24
M 3
Q 26
M 2
Q 28
M 4
Q 30
M 5
WL 6
Q 32
M 17
Q 34
M 16
Q 36
M 18
Q 38
M 19
WL 2
Q 40
M 7
Q 42
M 6
Q 44
M 8
Q 46
M 9
SA 252,1792
UL 6,20
M 21
UH 7,20
AR 8,155
M 20
AR 9,179
AD 8,6
M 22
AD 9,7
AW 155,8
M 23
R 58
M 11
AW 179,9
UL 6,21
M 10
WL 2
R 56
M 34
UH 7,21
AD 135,6
M 35
AD 159,7
M 32
M 33
M 30
M 31
M 28
M 29
M 26
M 27
WV 7
M 24
WL 0
FP
M 25
M 36
M 37
M 38
M 39
M 40
M 41
M 42
M 43
M 44
M 45
FR
M 46
M 47
M 58
M 59
M 56
M 57
M 54
M 55
WL 0
FQ 148
M 52
M 53
M 50
FZ 148
R 72
M 51
mw 24
R 74
M 48
TY 106,520
TY 110,14344
M 49
R 76
M 60
ad 84,1776
W 0
M 61
M 62
M 63
W 1
M 64
M 65
M 66
W 2
M 67
WL 3
AN 104,106
AN 105,107
M 68
WL 2
SH 106
AN 106,106
M 69
mw 27
AN 107,107
AN 108,110
M 70
AN 109,111
SH 110
M 71
R 78
M 82
AN 110,110
AN 111,111
M 83
TY 114,2056
TY 118,15880
M 80
ad 84,1782
W 0
M 81
ae 44,70,384
M 78
s_bfe_u32 s51, s50, 0x10007
v_mad_i32_i24 v47, v49, s51, v48
XL 30,256
M 79
W 1
M 76
M 77
M 74
W 2
XL 32,1024
M 75
)"
R"(SA 254,2560
M 72
WL 1
AN 112,114
AN 113,115
M 73
R 80
M 84
mw 48
SH 114
AN 114,114
M 85
WL 2
AN 115,115
AN 116,118
M 86
AN 117,119
SH 118
M 87
AN 118,118
AN 119,119
M 88
TY 122,3592
TY 126,17416
M 89
ad 84,1824
W 0
M 90
XL 34,1792
M 91
M 92
W 1
XL 36,2560
M 93
M 94
M 95
R 82
M 106
W 2
M 107
WL 3
AN 120,122
AN 121,123
M 104
WL 2
SH 122
AN 122,122
M 105
mw 51
AN 123,123
AN 124,126
M 102
AN 125,127
SH 126
M 103
AN 126,126
AN 127,127
M 100
ad 84,1830
W 0
M 101
M 98
M 99
W 1
M 96
M 97
R 0
M 108
W 2
M 109
M 110
M 111
bc 49,20,66,60
M 112
mh 18
M 113
ad 66,3872
H 0
M 114
M 115
M 116
DG 1024
M 117
M 118
M 119
WL 2
R 2
M 120
bc 48,20,64,56
M 121
mh 21
M 133
ad 64,3872
H 0
M 132
TX 58,0
TX 62,13824
M 134
M 135
H 1
M 123
UL 6,22
UH 7,22
M 122
TY 66,512
TY 70,14336
M 124
H 2
AD 160,6
M 125
AD 184,7
UL 6,23
M 137
UH 7,23
AD 208,6
M 136
TX 74,1536
TX 78,15360
M 138
WL 4
Q 0
M 139
Q 2
M 127
Q 4
M 126
TY 82,2048
TY 86,15872
M 128
Q 6
M 129
WL 4
Q 8
M 141
Q 10
M 140
TX 90,3072
TX 94,16896
M 142
Q 12
M 143
R 6
M 131
Q 14
M 130
TY 98,3584
TY 102,17408
M 0
R 4
M 1
WL 10
Q 16
M 13
Q 18
M 12
Q 20
M 14
Q 22
M 15
WL 8
Q 24
M 3
Q 26
M 2
Q 28
M 4
Q 30
M 5
WL 6
Q 32
M 17
Q 34
M 16
Q 36
M 18
Q 38
M 19
WL 2
Q 40
M 7
Q 42
M 6
Q 44
M 8
Q 46
M 9
AD 232,7
UL 6,24
M 21
UH 7,24
AD 161,6
M 20
AD 185,7
UL 6,25
M 22
UH 7,25
AD 209,6
M 23
R 10
M 11
AD 233,7
UL 6,26
M 10
WL 2
R 8
M 34
UH 7,26
AD 162,6
M 35
AD 186,7
UL 6,27
M 32
UH 7,27
AD 210,6
M 33
AD 234,7
UL 6,28
M 30
UH 7,28
AD 163,6
M 31
AD 187,7
UL 6,29
M 28
UH 7,29
AD 211,6
M 29
AD 235,7
M 26
M 27
WV 5
M 24
WL 0
FP
M 25
M 36
M 37
M 38
M 39
M 40
M 41
M 42
M 43
M 44
M 45
FR
M 46
M 47
M 58
M 59
M 56
M 57
M 54
M 55
WL 0
FQ 152
M 52
M 53
M 50
FZ 152
R 24
M 51
mw 72
R 26
M 48
TY 106,5128
TY 110,18952
M 49
R 28
M 60
ad 84,1872
W 0
M 61
M 62
M 63
W 1
M 64
M 65
M 66
W 2
M 67
WL 3
AN 104,106
AN 105,107
M 68
WL 2
SH 106
AN 106,106
M 69
mw 75
AN 107,107
AN 108,110
M 70
AN 109,111
SH 110
M 71
R 30
M 82
AN 110,110
AN 111,111
M 83
TY 114,6664
TY 118,20488
M 80
ad 84,1878
W 0
M 81
M 78
M 79
W 1
M 76
ae 44,68,384
M 77
s_bitcmp1_b32 s50, 8
s_cselect_b32 s51, 24576, 0
v_add_u32 v47, s51, v3
M 74
W 2
XL 38,256
M 75
M 72
WL 1
AN 112,114
AN 113,115
M 73
R 32
M 84
mw 0
SH 114
AN 114,114
M 85
WL 2
AN 115,115
AN 116,118
M 86
AN 117,119
SH 118
M 87
AN 118,118
AN 119,119
M 88
TY 122,8200
TY 126,22024
M 89
ad 84,1920
W 0
M 90
XL 40,1024
M 91
XL 42,1792
M 92
W 1
M 93
M 94
M 95
R 34
M 106
W 2
XL 44,2560
M 107
WL 3
AN 120,122
AN 121,123
M 104
WL 2
SH 122
AN 122,122
M 105
mw 3
AN 123,123
AN 124,126
M 102
AN 125,127
SH 126
M 103
AN 126,126
AN 127,127
M 100
ad 84,1926
W 0
M 101
M 98
M 99
W 1
M 96
M 97
R 48
M 108
W 2
M 109
M 110
M 111
bc 49,21,66,60
M 112
mh 0
M 113
ad 66,4064
H 0
M 114
M 115
)"
R"(M 116
DG 1024
M 117
M 118
M 119
WL 2
R 50
M 120
bc 48,21,64,56
M 121
mh 3
M 133
ad 64,4064
H 0
M 132
TX 58,4608
TX 62,18432
M 134
M 135
H 1
M 123
M 122
TY 66,5120
TY 70,18944
M 124
H 2
M 125
M 137
UL 6,30
UH 7,30
M 136
TX 74,6144
TX 78,19968
M 138
WL 4
Q 0
M 139
Q 2
M 127
Q 4
M 126
TY 82,6656
TY 86,20480
M 128
Q 6
M 129
WL 4
Q 8
M 141
Q 10
M 140
TX 90,7680
TX 94,21504
M 142
Q 12
M 143
R 54
M 131
Q 14
M 130
TY 98,8192
TY 102,22016
M 0
R 52
M 1
WL 10
Q 16
M 13
Q 18
M 12
Q 20
M 14
Q 22
M 15
WL 8
Q 24
M 3
Q 26
M 2
Q 28
M 4
Q 30
M 5
WL 6
Q 32
M 17
Q 34
M 16
Q 36
M 18
Q 38
M 19
WL 2
Q 40
M 7
Q 42
M 6
Q 44
M 8
Q 46
M 9
AD 164,6
AD 188,7
M 21
UL 6,31
UH 7,31
M 20
AD 212,6
AD 236,7
M 22
UL 6,32
UH 7,32
M 23
R 58
M 11
AD 165,6
AD 189,7
M 10
WL 2
R 56
M 34
UL 6,33
UH 7,33
M 35
AD 213,6
AD 237,7
M 32
UL 6,34
UH 7,34
M 33
AD 166,6
AD 190,7
M 30
UL 6,35
UH 7,35
M 31
AD 214,6
AD 238,7
M 28
UL 6,36
UH 7,36
M 29
AD 167,6
AD 191,7
M 26
UL 6,37
UH 7,37
M 27
WV 5
AD 215,6
AD 239,7
M 24
WL 0
FP
M 25
M 36
M 37
M 38
M 39
M 40
M 41
M 42
M 43
M 44
M 45
FR
M 46
M 47
M 58
M 59
M 56
M 57
M 54
M 55
WL 0
FQ 156
M 52
M 53
M 50
FZ 156
R 72
M 51
mw 24
R 74
M 48
TY 106,9736
TY 110,23560
M 49
R 76
M 60
ad 84,1968
W 0
M 61
M 62
M 63
W 1
M 64
M 65
M 66
W 2
M 67
WL 3
AN 104,106
AN 105,107
M 68
WL 2
SH 106
AN 106,106
M 69
mw 27
AN 107,107
AN 108,110
M 70
AN 109,111
SH 110
M 71
R 78
M 82
AN 110,110
AN 111,111
M 83
TY 114,11272
TY 118,25096
M 80
ad 84,1974
W 0
M 81
M 78
M 79
W 1
M 76
M 77
M 74
W 2
M 75
M 72
WL 1
AN 112,114
AN 113,115
M 73
R 80
M 84
mw 48
SH 114
AN 114,114
M 85
WL 2
AN 115,115
AN 116,118
M 86
AN 117,119
SH 118
M 87
AN 118,118
AN 119,119
M 88
TY 122,12808
TY 126,26632
M 89
ad 84,2016
W 0
M 90
ae 44,68,384
M 91
s_bfe_u32 s51, s50, 0x10009
v_mad_i32_i24 v47, v49, s51, v48
XL 14,256
M 92
W 1
XL 16,1024
M 93
M 94
XL 18,1792
M 95
R 82
M 106
W 2
M 107
WL 3
AN 120,122
AN 121,123
M 104
WL 2
SH 122
AN 122,122
M 105
mw 51
AN 123,123
AN 124,126
M 102
AN 125,127
SH 126
M 103
AN 126,126
AN 127,127
M 100
ad 84,2022
W 0
M 101
XL 20,2560
M 98
M 99
W 1
M 96
M 97
R 0
M 108
W 2
M 109
M 110
M 111
bc 49,22,66,60
M 112
mh 9
M 113
ad 66,4256
H 0
M 114
M 115
M 116
DG 1024
M 117
M 118
M 119
WL 2
R 2
M 120
bc 48,22,64,56
M 121
mh 12
M 133
ad 64,4256
H 0
M 132
TX 58,9216
TX 62,23040
M 134
M 135
H 1
M 123
M 122
TY 66,9728
TY 70,23552
M 124
H 2
M 125
M 137
M 136
TX 74,10752
TX 78,24576
M 138
WL 4
Q 0
M 139
Q 2
M 127
Q 4
M 126
TY 82,11264
TY 86,25088
M 128
Q 6
M 129
WL 4
Q 8
M 141
Q 10
M 140
TX 90,12288
TX 94,26112
M 142
Q 12
M 143
R 6
M 131
Q 14
M 130
TY 98,12800
TY 102,26624
M 0
R 4
M 1
WL 10
Q 16
M 13
Q 18
M 12
Q 20
M 14
Q 22
M 15
WL 8
Q 24
M 3
Q 26
M 2
Q 28
M 4
Q 30
M 5
)"
R"(WL 6
Q 32
M 17
Q 34
M 16
Q 36
M 18
Q 38
M 19
WL 2
Q 40
M 7
Q 42
M 6
Q 44
M 8
Q 46
M 9
UL 6,38
UH 7,38
M 21
AD 168,6
AD 192,7
M 20
UL 6,39
UH 7,39
M 22
AD 216,6
AD 240,7
M 23
R 10
M 11
UL 6,40
UH 7,40
M 10
WL 2
R 8
M 34
AD 169,6
AD 193,7
M 35
UL 6,41
UH 7,41
M 32
AD 217,6
AD 241,7
M 33
UL 6,42
UH 7,42
M 30
AD 170,6
AD 194,7
M 31
UL 6,43
UH 7,43
M 28
AD 218,6
AD 242,7
M 29
UL 6,44
UH 7,44
M 26
AD 171,6
AD 195,7
M 27
WV 5
UL 6,45
UH 7,45
M 24
WL 0
FP
AD 219,6
M 25
AD 243,7
M 36
M 37
M 38
M 39
M 40
M 41
M 42
M 43
M 44
M 45
FR
M 46
M 47
M 58
M 59
M 56
M 57
M 54
M 55
WL 0
FQ 160
M 52
M 53
M 50
FZ 160
R 24
M 51
mw 72
R 26
M 48
TY 106,520
TY 110,14344
M 49
R 28
M 60
ad 84,2064
W 0
M 61
M 62
M 63
W 1
M 64
M 65
M 66
W 2
M 67
WL 3
AN 104,106
AN 105,107
M 68
WL 2
SH 106
AN 106,106
M 69
mw 75
AN 107,107
AN 108,110
M 70
AN 109,111
SH 110
M 71
R 30
M 82
AN 110,110
AN 111,111
M 83
TY 114,2056
TY 118,15880
M 80
ad 84,2070
W 0
M 81
M 78
M 79
W 1
M 76
M 77
M 74
W 2
M 75
M 72
WL 1
AN 112,114
AN 113,115
M 73
R 32
M 84
mw 0
SH 114
AN 114,114
M 85
WL 2
AN 115,115
AN 116,118
M 86
AN 117,119
SH 118
M 87
AN 118,118
AN 119,119
M 88
TY 122,3592
TY 126,17416
M 89
ad 84,2112
W 0
M 90
ae 44,68,480
M 91
s_bitcmp1_b32 s50, 10
s_cselect_b32 s51, 24576, 0
v_add_u32 v47, s51, v3
M 92
W 1
XL 22,256
M 93
XL 24,1024
M 94
M 95
R 34
M 106
W 2
M 107
WL 3
AN 120,122
AN 121,123
M 104
WL 2
SH 122
AN 122,122
M 105
mw 3
AN 123,123
AN 124,126
M 102
AN 125,127
SH 126
M 103
AN 126,126
AN 127,127
M 100
ad 84,2118
W 0
M 101
XL 26,1792
M 98
XL 28,2560
M 99
W 1
M 96
M 97
R 48
M 108
W 2
M 109
M 110
M 111
bc 49,23,66,60
M 112
mh 18
M 113
ad 66,4448
H 0
M 114
M 115
M 116
DG 1024
M 117
M 118
M 119
WL 2
R 50
M 120
bc 48,23,64,56
M 121
mh 21
M 133
ad 64,4448
H 0
M 132
TX 58,0
TX 62,13824
M 134
M 135
H 1
M 123
M 122
TY 66,512
TY 70,14336
M 124
H 2
M 125
M 137
M 136
TX 74,1536
TX 78,15360
PA 10240
DF
M 138
WL 4
Q 0
M 139
Q 2
M 127
Q 4
M 126
TY 82,2048
TY 86,15872
M 128
Q 6
M 129
WL 4
Q 8
M 141
Q 10
M 140
TX 90,3072
TX 94,16896
M 142
Q 12
M 143
R 54
M 131
Q 14
M 130
TY 98,3584
TY 102,17408
M 0
R 52
M 1
WL 10
Q 16
M 13
Q 18
M 12
Q 20
M 14
Q 22
M 15
WL 8
Q 24
M 3
Q 26
M 2
Q 28
M 4
Q 30
M 5
WL 6
Q 32
M 17
Q 34
M 16
Q 36
M 18
Q 38
M 19
WL 2
Q 40
M 7
Q 42
M 6
Q 44
M 8
Q 46
M 9
UL 6,14
UH 7,14
M 21
AD 172,6
AD 196,7
M 20
UL 6,15
UH 7,15
M 22
AD 220,6
AD 244,7
M 23
R 58
M 11
UL 6,16
UH 7,16
M 10
WL 2
R 56
M 34
AD 173,6
AD 197,7
M 35
UL 6,17
UH 7,17
M 32
AD 221,6
AD 245,7
M 33
UL 6,18
UH 7,18
M 30
AD 174,6
AD 198,7
M 31
UL 6,19
UH 7,19
M 28
AD 222,6
AD 246,7
M 29
UL 6,20
UH 7,20
M 26
AD 175,6
AD 199,7
M 27
WV 6
UL 6,21
UH 7,21
M 24
WL 0
FP
AD 223,6
M 25
AD 247,7
M 36
M 37
M 38
M 39
)"
R"(M 40
M 41
M 42
M 43
M 44
M 45
FR
M 46
M 47
M 58
M 59
M 56
M 57
M 54
M 55
WL 0
FQ 164
M 52
M 53
M 50
FZ 164
R 72
M 51
mw 24
R 74
M 48
TY 106,5128
TY 110,18952
M 49
R 76
M 60
ad 84,2160
W 0
M 61
M 62
M 63
W 1
M 64
M 65
M 66
W 2
M 67
WL 3
AN 104,106
AN 105,107
M 68
WL 2
SH 106
AN 106,106
M 69
mw 27
AN 107,107
AN 108,110
M 70
AN 109,111
SH 110
M 71
R 78
M 82
AN 110,110
AN 111,111
M 83
TY 114,6664
TY 118,20488
M 80
ad 84,2166
W 0
M 81
M 78
M 79
W 1
M 76
M 77
M 74
W 2
M 75
M 72
WL 1
AN 112,114
AN 113,115
M 73
R 80
M 84
mw 48
SH 114
AN 114,114
M 85
WL 2
AN 115,115
AN 116,118
M 86
AN 117,119
SH 118
M 87
AN 118,118
AN 119,119
M 88
TY 122,8200
TY 126,22024
M 89
ad 84,2208
W 0
M 90
ae 44,68,480
M 91
s_bfe_u32 s51, s50, 0x1000b
v_mad_i32_i24 v47, v49, s51, v48
XL 30,256
M 92
W 1
M 93
M 94
M 95
R 82
M 106
W 2
XL 32,1024
M 107
WL 3
AN 120,122
AN 121,123
M 104
WL 2
SH 122
AN 122,122
M 105
mw 51
AN 123,123
AN 124,126
M 102
AN 125,127
SH 126
M 103
AN 126,126
AN 127,127
M 100
ad 84,2214
W 0
M 101
XL 34,1792
M 98
M 99
W 1
M 96
M 97
R 0
M 108
W 2
XL 36,2560
M 109
M 110
M 111
bc 49,0,66,60
M 112
mh 0
M 113
ad 66,64
H 0
M 114
M 115
M 116
DG 1024
M 117
M 118
M 119
WL 2
R 2
M 120
bc 48,0,64,56
M 121
mh 3
M 133
ad 64,32
H 0
M 132
TX 58,4608
TX 62,18432
M 134
M 135
H 1
M 123
M 122
TY 66,5120
TY 70,18944
M 124
H 2
M 125
M 137
M 136
TX 74,6144
TX 78,19968
M 138
WL 4
Q 0
M 139
Q 2
M 127
Q 4
M 126
TY 82,6656
TY 86,20480
M 128
Q 6
M 129
WL 4
Q 8
M 141
Q 10
M 140
TX 90,7680
TX 94,21504
M 142
Q 12
M 143
R 6
M 131
Q 14
M 130
TY 98,8192
TY 102,22016
M 0
R 4
M 1
WL 10
Q 16
M 13
Q 18
M 12
Q 20
M 14
Q 22
M 15
WL 8
Q 24
M 3
Q 26
M 2
Q 28
M 4
Q 30
M 5
WL 6
Q 32
M 17
Q 34
M 16
Q 36
M 18
Q 38
M 19
WL 2
Q 40
M 7
Q 42
M 6
Q 44
M 8
Q 46
M 9
UL 6,22
UH 7,22
M 21
AD 176,6
AD 200,7
M 20
UL 6,23
UH 7,23
M 22
AD 224,6
AD 248,7
M 23
R 10
M 11
UL 6,24
UH 7,24
M 10
WL 2
R 8
M 34
AD 177,6
AD 201,7
M 35
UL 6,25
UH 7,25
M 32
AD 225,6
AD 249,7
M 33
UL 6,26
UH 7,26
M 30
AD 178,6
AD 202,7
M 31
UL 6,27
UH 7,27
M 28
AD 226,6
AD 250,7
M 29
UL 6,28
UH 7,28
M 26
AD 179,6
AD 203,7
M 27
WV 6
UL 6,29
UH 7,29
M 24
WL 0
FP
AD 227,6
M 25
AD 251,7
M 36
M 37
M 38
M 39
M 40
M 41
M 42
M 43
M 44
M 45
FR
M 46
M 47
M 58
M 59
M 56
M 57
M 54
M 55
WL 0
FQ 168
M 52
M 53
M 50
FZ 168
R 24
M 51
mw 72
R 26
M 48
TY 106,9736
TY 110,23560
M 49
R 28
M 60
ad 84,2256
W 0
M 61
M 62
M 63
W 1
M 64
M 65
M 66
W 2
M 67
WL 3
AN 104,106
AN 105,107
M 68
WL 2
SH 106
AN 106,106
M 69
mw 75
AN 107,107
AN 108,110
M 70
AN 109,111
SH 110
M 71
R 30
M 82
AN 110,110
AN 111,111
M 83
TY 114,11272
TY 118,25096
M 80
ad 84,2262
W 0
M 81
M 78
M 79
W 1
M 76
M 77
M 74
W 2
M 75
M 72
WL 1
AN 112,114
AN 113,115
M 73
)"
R"(R 32
M 84
mw 0
SH 114
AN 114,114
M 85
WL 2
AN 115,115
AN 116,118
M 86
AN 117,119
SH 118
M 87
AN 118,118
AN 119,119
M 88
TY 122,12808
TY 126,26632
M 89
ad 84,0
W 0
M 90
M 91
M 92
W 1
M 93
M 94
M 95
R 34
M 106
W 2
M 107
WL 3
AN 120,122
AN 121,123
M 104
WL 2
SH 122
AN 122,122
M 105
mw 3
AN 123,123
AN 124,126
M 102
AN 125,127
SH 126
M 103
AN 126,126
AN 127,127
M 100
ad 84,6
W 0
M 101
M 98
M 99
W 1
M 96
M 97
R 48
M 108
W 2
M 109
M 110
M 111
bc 49,1,66,60
M 112
mh 9
M 113
ad 66,256
H 0
M 114
M 115
M 116
DG 1024
M 117
M 118
M 119
WL 2
R 50
M 120
bc 48,1,64,56
M 121
mh 12
M 133
ad 64,256
H 0
M 132
TX 58,9216
TX 62,23040
M 134
M 135
DG 1024
M 123
M 122
TY 66,9728
TY 70,23552
M 124
mh 15
M 125
ad 64,227
H 0
M 137
M 136
TX 74,10752
TX 78,24576
M 138
WL 4
Q 0
M 139
Q 2
M 127
Q 4
M 126
TY 82,11264
TY 86,25088
M 128
Q 6
M 129
WL 4
Q 8
M 141
Q 10
M 140
TX 90,12288
TX 94,26112
M 142
Q 12
M 143
R 54
M 131
Q 14
M 130
TY 98,12800
TY 102,26624
M 0
R 52
M 1
WL 10
Q 16
M 13
Q 18
M 12
Q 20
M 14
Q 22
M 15
WL 8
Q 24
M 3
Q 26
M 2
Q 28
M 4
Q 30
M 5
WL 6
Q 32
M 17
Q 34
M 16
Q 36
M 18
Q 38
M 19
WL 2
Q 40
M 7
Q 42
M 6
Q 44
M 8
Q 46
M 9
DG 1024
M 21
UL 6,30
UH 7,30
M 20
AD 180,6
AD 204,7
M 22
UL 6,31
UH 7,31
M 23
R 58
M 11
AD 228,6
AD 252,7
M 10
WL 2
R 56
M 34
UL 6,32
UH 7,32
M 35
AD 181,6
AD 205,7
M 32
UL 6,33
UH 7,33
BV 52,4608
M 33
AD 229,6
AD 253,7
M 30
UL 6,34
UH 7,34
M 31
AD 182,6
AD 206,7
M 28
UL 6,35
UH 7,35
M 29
AD 230,6
AD 254,7
M 26
WV 23
UL 6,36
UH 7,36
M 27
WV 6
AD 183,6
AD 207,7
M 24
WL 1
FP
UL 6,37
M 25
UH 7,37
AD 231,6
M 36
AD 255,7
M 37
M 38
M 39
M 40
ds_read_b64 v[12:13], v4 offset:4864
ds_read_b64 v[46:47], v4 offset:4872
M 41
AR 6,36
AR 7,60
M 42
AR 8,84
AR 9,108
M 43
WL 3
EF 6,52
EF 7,53
M 44
EF 8,54
EF 9,55
M 45
FR
CV 38,6,7
M 46
CV 39,8,9
AR 6,37
M 47
AR 7,61
AR 8,85
M 58
AR 9,109
EF 6,52
M 59
EF 7,53
EF 8,54
M 56
EF 9,55
CV 40,6,7
M 57
CV 41,8,9
AR 6,38
M 54
AR 7,62
AR 8,86
M 55
WL 0
FQ 172
AR 9,110
M 52
EF 6,52
EF 7,53
M 53
EF 8,54
EF 9,55
M 50
FZ 172
R 72
M 51
mw 24
R 74
M 48
TY 106,520
TY 110,14344
M 49
R 76
M 60
ad 84,48
W 0
M 61
CV 42,6,7
CV 43,8,9
M 62
AR 6,39
AR 7,63
M 63
W 1
AR 8,87
M 64
AR 9,111
EF 6,52
M 65
EF 7,53
EF 8,54
M 66
W 2
EF 9,55
M 67
WL 3
AN 104,106
AN 105,107
M 68
WL 2
SH 106
AN 106,106
M 69
mw 27
AN 107,107
AN 108,110
M 70
AN 109,111
SH 110
M 71
R 78
M 82
AN 110,110
AN 111,111
M 83
TY 114,2056
TY 118,15880
M 80
ad 84,54
W 0
M 81
CV 44,6,7
CV 45,8,9
M 78
ae 42,74,384
AR 6,40
M 79
W 1
AR 7,64
M 76
s_bitcmp1_b32 s50, 6
s_cselect_b32 s52, 24576, 0
v_add_u32 v50, s52, v3
ST 38,0
M 77
AR 8,88
AR 9,112
M 74
W 2
EF 6,52
M 75
EF 7,53
EF 8,54
M 72
WL 1
AN 112,114
AN 113,115
M 73
R 80
M 84
mw 48
SH 114
AN 114,114
M 85
WL 2
AN 115,115
AN 116,118
M 86
AN 117,119
SH 118
M 87
AN 118,118
AN 119,119
M 88
TY 122,3592
TY 126,17416
M 89
ad 84,96
W 0
M 90
EF 9,55
CV 14,6,7
M 91
ST 40,768
CV 15,8,9
M 92
)"
R"(W 1
AR 6,41
M 93
AR 7,65
AR 8,89
M 94
AR 9,113
EF 6,52
M 95
R 82
M 106
W 2
EF 7,53
M 107
WL 3
AN 120,122
AN 121,123
M 104
WL 2
SH 122
AN 122,122
M 105
mw 51
AN 123,123
AN 124,126
M 102
AN 125,127
SH 126
M 103
AN 126,126
AN 127,127
M 100
ad 84,102
W 0
M 101
EF 8,54
EF 9,55
M 98
ST 42,1536
CV 16,6,7
M 99
W 1
CV 17,8,9
M 96
AR 6,42
AR 7,66
M 97
R 0
M 108
W 2
AR 8,90
M 109
AR 9,114
EF 6,52
M 110
EF 7,53
EF 8,54
M 111
bc 49,2,66,60
EF 9,55
M 112
mh 18
CV 18,6,7
CV 19,8,9
M 113
ad 66,448
H 0
M 114
AR 6,43
AR 7,67
M 115
AR 8,91
AR 9,115
M 116
DG 1024
M 117
ST 44,2304
EF 6,52
M 118
EF 7,53
EF 8,54
M 119
WL 0
R 2
M 120
bc 48,2,64,56
EF 9,55
M 121
mh 21
CV 20,6,7
CV 21,8,9
M 133
ad 64,448
H 0
BV 52,5120
M 132
TX 58,0
TX 62,13824
M 134
AR 6,132
AR 7,156
M 135
H 1
AR 8,180
M 123
EF 6,12
EF 7,13
M 122
TY 66,512
TY 70,14336
M 124
H 2
EF 8,46
M 125
EF 136,47
CV 22,6,7
M 137
ae 42,74,384
CV 23,8,136
M 136
WL 2
TX 74,1536
TX 78,15360
M 138
s_bfe_u32 s52, s50, 0x10007
Q 0
v_mad_i32_i24 v50, v49, s52, v48
M 139
Q 2
ST 14,0
M 127
Q 4
M 126
TY 82,2048
TY 86,15872
M 128
Q 6
M 129
WL 4
Q 8
M 141
Q 10
M 140
TX 90,3072
TX 94,16896
M 142
Q 12
M 143
R 6
M 131
Q 14
M 130
TY 98,3584
TY 102,17408
v_mfma_f32_16x16x32_f16 a[36:39], v[56:59], a[0:3], a[32:35]
R 4
M 1
WL 10
Q 16
v_mfma_f32_16x16x32_f16 a[60:63], v[60:63], a[12:15], a[32:35]
Q 18
ST 16,768
M 12
Q 20
v_mfma_f32_16x16x32_f16 a[64:67], v[64:67], a[8:11], a[32:35]
Q 22
M 15
WL 8
Q 24
v_mfma_f32_16x16x32_f16 a[40:43], v[68:71], a[4:7], a[32:35]
Q 26
M 2
Q 28
M 4
Q 30
M 5
WL 6
Q 32
M 17
Q 34
M 16
Q 36
M 18
Q 38
M 19
WL 1
Q 40
M 7
Q 42
M 6
Q 44
ST 18,1536
M 8
Q 46
M 9
AR 6,133
AR 7,157
M 21
AR 8,181
EF 6,12
M 20
EF 7,13
EF 8,46
M 22
EF 137,47
CV 24,6,7
M 23
R 10
M 11
CV 25,8,137
AR 6,134
M 10
WL 2
R 8
M 34
AR 7,158
AR 8,182
M 35
EF 6,12
EF 7,13
M 32
EF 8,46
EF 138,47
M 33
CV 26,6,7
CV 27,8,138
M 30
ST 20,2304
AR 6,135
M 31
AR 7,159
AR 8,183
M 28
EF 6,12
EF 7,13
M 29
EF 8,46
EF 139,47
v_mfma_f32_16x16x32_f16 a[88:91], v[64:67], a[16:19], a[32:35]
CV 28,6,7
CV 29,8,139
M 27
WV 10
AR 6,136
AR 7,160
AR 8,184
v_mfma_f32_16x16x32_f16 a[84:87], v[56:59], a[16:19], a[32:35]
WL 0
FP
EF 6,12
EF 7,13
M 25
EF 8,46
EF 140,47
CV 30,6,7
v_mfma_f32_16x16x32_f16 a[108:111], v[56:59], a[24:27], a[32:35]
CV 31,8,140
AR 6,137
AR 7,161
M 37
AR 8,185
EF 6,12
EF 7,13
v_mfma_f32_16x16x32_f16 a[112:115], v[64:67], a[24:27], a[32:35]
EF 8,46
EF 141,47
CV 32,6,7
M 39
CV 33,8,141
AR 6,138
AR 7,162
M 40
AR 8,186
EF 6,12
EF 7,13
M 41
ae 42,74,384
EF 8,46
EF 142,47
M 42
s_bitcmp1_b32 s50, 6
s_cselect_b32 s52, 24576, 0
v_add_u32 v50, s52, v3
ST 22,128
M 43
CV 34,6,7
CV 35,8,142
AR 6,139
M 44
AR 7,163
AR 8,187
EF 6,12
M 45
FR
EF 7,13
EF 8,46
M 46
EF 143,47
CV 36,6,7
CV 37,8,143
M 47
ds_read_b64 v[12:13], v4 offset:4608
ds_read_b64 v[46:47], v4 offset:4616
M 58
EF 160,52
EF 184,53
M 59
EF 208,54
EF 232,55
M 56
CV 38,160,184
CV 39,208,232
M 57
EF 161,52
EF 185,53
M 54
EF 209,54
EF 233,55
M 55
WL 2
FQ 176
CV 40,161,185
M 52
CV 41,209,233
EF 162,52
M 53
ST 24,896
EF 186,53
v_mfma_f32_16x16x32_f16 a[136:139], v[64:67], a[0:3], a[32:35]
FZ 176
R 24
M 51
mw 72
R 26
v_mfma_f32_16x16x32_f16 a[132:135], v[56:59], a[0:3], a[32:35]
TY 106,5128
TY 110,18952
M 49
R 28
v_mfma_f32_16x16x32_f16 a[156:159], v[56:59], a[8:11], a[32:35]
ad 84,144
W 0
M 61
EF 210,54
EF 234,55
v_mfma_f32_16x16x32_f16 a[160:163], v[64:67], a[8:11], a[32:35]
CV 42,162,186
CV 43,210,234
M 63
W 1
EF 163,52
M 64
EF 187,53
EF 211,54
M 65
EF 235,55
CV 44,163,187
M 66
W 2
CV 45,211,235
M 67
WL 3
AN 104,106
AN 105,107
EF 164,52
M 68
WL 2
SH 106
AN 106,106
ST 26,1664
M 69
mw 75
AN 107,107
AN 108,110
EF 188,53
M 70
AN 109,111
SH 110
EF 212,54
M 71
R 30
EF 236,55
M 82
AN 110,110
AN 111,111
CV 14,164,188
M 83
TY 114,6664
TY 118,20488
CV 15,212,236
M 80
ad 84,150
W 0
EF 165,52
M 81
EF 189,53
EF 213,54
EF 237,55
M 78
CV 16,165,189
)"
R"(CV 17,213,237
EF 166,52
M 79
W 1
EF 190,53
EF 214,54
M 76
EF 238,55
CV 18,166,190
CV 19,214,238
M 77
EF 167,52
EF 191,53
EF 215,54
v_mfma_f32_16x16x32_f16 a[184:187], v[64:67], a[16:19], a[32:35]
W 2
EF 239,55
CV 20,167,191
M 75
ST 28,2432
CV 21,215,239
v_mfma_f32_16x16x32_f16 a[180:183], v[56:59], a[16:19], a[32:35]
WL 1
AN 112,114
AN 113,115
BV 52,4864
M 73
R 32
v_mfma_f32_16x16x32_f16 v[136:139], v[56:59], a[24:27], v[128:131]
mw 0
SH 114
AN 114,114
M 85
WL 3
AN 115,115
AN 116,118
v_mfma_f32_16x16x32_f16 v[140:143], v[64:67], a[24:27], v[128:131]
AN 117,119
SH 118
M 87
AN 118,118
AN 119,119
M 88
TY 122,8200
TY 126,22024
M 89
ad 84,192
W 0
M 90
AR 6,44
AR 7,68
M 91
AR 8,92
AR 9,116
M 92
W 1
EF 6,12
M 93
EF 7,13
EF 8,46
M 94
ae 42,74,384
EF 9,47
M 95
R 34
M 106
WL 3
W 2
CV 22,6,7
M 107
s_bfe_u32 s52, s50, 0x10007
AN 120,122
AN 121,123
v_mad_i32_i24 v50, v49, s52, v48
M 104
WL 2
SH 122
AN 122,122
ST 30,128
M 105
mw 3
AN 123,123
AN 124,126
M 102
AN 125,127
SH 126
M 103
AN 126,126
AN 127,127
M 100
ad 84,198
W 0
M 101
CV 23,8,9
AR 6,45
v_mfma_f32_16x16x32_f16 v[164:167], v[64:67], a[0:3], v[128:131]
AR 7,69
AR 8,93
M 99
W 1
AR 9,117
v_mfma_f32_16x16x32_f16 v[160:163], v[56:59], a[0:3], v[128:131]
EF 6,12
EF 7,13
M 97
R 48
v_mfma_f32_16x16x32_f16 v[184:187], v[56:59], a[8:11], v[128:131]
W 2
EF 8,46
M 109
EF 9,47
CV 24,6,7
v_mfma_f32_16x16x32_f16 v[188:191], v[64:67], a[8:11], v[128:131]
CV 25,8,9
AR 6,46
M 111
bc 49,3,66,60
ST 32,896
M 112
mh 0
AR 7,70
AR 8,94
M 113
ad 66,640
H 0
M 114
AR 9,118
EF 6,12
M 115
EF 7,13
EF 8,46
M 116
DG 1024
M 117
EF 9,47
CV 26,6,7
M 118
CV 27,8,9
AR 6,47
M 119
WL 0
R 50
v_mfma_f32_16x16x32_f16 v[208:211], v[56:59], a[16:19], v[128:131]
bc 48,3,64,56
AR 7,71
M 121
mh 3
AR 8,95
AR 9,119
v_mfma_f32_16x16x32_f16 v[232:235], v[60:63], a[28:31], v[128:131]
ad 64,640
H 0
M 132
TX 58,4608
TX 62,18432
v_mfma_f32_16x16x32_f16 v[236:239], v[64:67], a[24:27], v[128:131]
ST 34,1664
EF 6,12
M 135
H 1
EF 7,13
v_mfma_f32_16x16x32_f16 v[212:215], v[68:71], a[20:23], v[128:131]
EF 8,46
EF 9,47
M 122
TY 66,5120
TY 70,18944
M 124
H 2
CV 28,6,7
M 125
CV 29,8,9
M 137
AR 6,48
AR 7,72
AR 8,96
AR 9,120
EF 6,12
EF 7,13
M 136
TX 74,6144
TX 78,19968
PA 10240
DF
EF 8,46
EF 9,47
M 138
WL 4
Q 0
CV 6,6,7
CV 7,8,9
AW 200,6
AW 201,7
M 139
Q 2
AR 6,49
AR 7,73
AR 8,97
AR 9,121
M 127
Q 4
EF 6,12
EF 7,13
EF 8,46
EF 9,47
M 126
TY 82,6656
TY 86,20480
CV 6,6,7
CV 7,8,9
AW 202,6
AW 203,7
M 128
Q 6
AR 6,50
AR 7,74
AR 8,98
AR 9,122
M 129
WL 2
Q 8
ST 36,2432
EF 6,12
EF 7,13
EF 8,46
M 141
Q 10
EF 9,47
CV 6,6,7
CV 7,8,9
AW 204,6
M 140
TX 90,7680
TX 94,21504
AW 205,7
AR 6,51
AR 7,75
AR 8,99
M 142
Q 12
AR 9,123
EF 6,12
EF 7,13
EF 8,46
M 143
R 54
EF 9,47
CV 6,6,7
CV 7,8,9
AW 206,6
M 131
Q 14
AW 207,7
M 130
TY 98,8192
TY 102,22016
ds_read_b64 v[12:13], v4 offset:5120
M 0
R 52
ds_read_b64 v[46:47], v4 offset:5128
M 1
Q 16
M 13
Q 18
M 12
Q 20
M 14
Q 22
M 15
WL 8
Q 24
M 3
Q 26
M 2
Q 28
v_mfma_f32_16x16x32_f16 a[44:47], v[72:75], a[0:3], a[32:35]
Q 30
M 5
Q 32
v_mfma_f32_16x16x32_f16 a[68:71], v[76:79], a[12:15], a[32:35]
Q 34
M 16
Q 36
v_mfma_f32_16x16x32_f16 a[72:75], v[80:83], a[8:11], a[32:35]
Q 38
M 19
WL 2
Q 40
v_mfma_f32_16x16x32_f16 a[48:51], v[84:87], a[4:7], a[32:35]
Q 42
M 6
Q 44
M 8
Q 46
M 9
ae 42,74,384
M 21
s_bitcmp1_b32 s50, 6
s_cselect_b32 s52, 24576, 0
v_add_u32 v50, s52, v3
ST 38,256
M 20
AR 6,140
AR 7,164
M 22
AR 8,188
EF 6,52
M 23
R 58
M 11
EF 7,53
EF 8,54
M 10
WL 2
R 56
M 34
EF 144,55
CV 30,6,7
M 35
CV 31,8,144
AR 6,141
M 32
AR 7,165
AR 8,189
M 33
EF 6,52
EF 7,53
v_mfma_f32_16x16x32_f16 a[96:99], v[80:83], a[16:19], a[32:35]
EF 8,54
EF 145,55
M 31
CV 32,6,7
CV 33,8,145
v_mfma_f32_16x16x32_f16 a[92:95], v[72:75], a[16:19], a[32:35]
AR 6,142
AR 7,166
M 29
ST 40,1024
AR 8,190
M 26
EF 6,52
EF 7,53
M 27
WV 11
EF 8,54
EF 146,55
M 24
WL 0
FP
CV 34,6,7
M 25
CV 35,8,146
AR 6,143
M 36
AR 7,167
AR 8,191
M 37
EF 6,52
EF 7,53
M 38
EF 8,54
EF 147,55
M 39
CV 36,6,7
CV 37,8,147
v_mfma_f32_16x16x32_f16 a[116:119], v[72:75], a[24:27], a[32:35]
AR 6,144
AR 7,168
AR 8,192
EF 6,52
M 41
EF 7,53
EF 8,54
EF 148,55
CV 6,6,7
v_mfma_f32_16x16x32_f16 a[120:123], v[80:83], a[24:27], a[32:35]
CV 7,8,148
AW 208,6
AW 209,7
AR 6,145
M 43
AR 7,169
AR 8,193
EF 6,52
EF 7,53
M 44
ST 42,1792
EF 8,54
EF 149,55
CV 6,6,7
M 45
FR
CV 7,8,149
AW 210,6
AW 211,7
M 46
AR 6,146
AR 7,170
AR 8,194
EF 6,52
)"
R"(M 47
EF 7,53
EF 8,54
EF 150,55
CV 6,6,7
M 58
CV 7,8,150
AW 212,6
AW 213,7
AR 6,147
M 59
AR 7,171
AR 8,195
EF 6,52
EF 7,53
M 56
EF 8,54
EF 151,55
CV 6,6,7
CV 7,8,151
M 57
AW 214,6
AW 215,7
v_mfma_f32_16x16x32_f16 a[144:147], v[80:83], a[0:3], a[32:35]
BV 52,4608
EF 168,12
EF 192,13
M 55
WL 1
FQ 180
EF 216,46
EF 240,47
v_mfma_f32_16x16x32_f16 a[140:143], v[72:75], a[0:3], a[32:35]
CV 6,168,192
CV 7,216,240
AW 216,6
M 53
AW 217,7
EF 169,12
EF 193,13
M 50
FZ 180
R 72
EF 217,46
M 51
mw 24
R 74
ST 44,2560
M 48
TY 106,9736
TY 110,23560
EF 241,47
M 49
R 76
CV 6,169,193
M 60
ad 84,240
W 0
CV 7,217,241
M 61
AW 218,6
AW 219,7
EF 170,12
M 62
EF 194,13
EF 218,46
EF 242,47
M 63
W 1
CV 6,170,194
CV 7,218,242
v_mfma_f32_16x16x32_f16 a[164:167], v[72:75], a[8:11], a[32:35]
AW 220,6
AW 221,7
EF 171,12
M 65
EF 195,13
EF 219,46
EF 243,47
v_mfma_f32_16x16x32_f16 a[168:171], v[80:83], a[8:11], a[32:35]
W 2
CV 6,171,195
CV 7,219,243
M 67
WL 3
AN 104,106
AN 105,107
AW 222,6
M 68
WL 0
SH 106
AN 106,106
AW 223,7
M 69
mw 27
AN 107,107
AN 108,110
EF 172,12
M 70
AN 109,111
SH 110
EF 196,13
M 71
R 78
EF 220,46
M 82
AN 110,110
AN 111,111
EF 244,47
M 83
TY 114,11272
TY 118,25096
CV 38,172,196
M 80
ad 84,246
W 0
CV 39,220,244
M 81
ae 42,74,384
EF 173,12
EF 197,13
v_mfma_f32_16x16x32_f16 a[192:195], v[80:83], a[16:19], a[32:35]
s_bfe_u32 s52, s50, 0x10007
v_mad_i32_i24 v50, v49, s52, v48
ST 14,256
EF 221,46
M 79
W 1
EF 245,47
CV 40,173,197
v_mfma_f32_16x16x32_f16 a[188:191], v[72:75], a[16:19], a[32:35]
CV 41,221,245
EF 174,12
EF 198,13
M 77
EF 222,46
EF 246,47
CV 42,174,198
M 74
W 2
CV 43,222,246
EF 175,12
M 75
EF 199,13
EF 223,46
EF 247,47
M 72
WL 1
AN 112,114
AN 113,115
CV 44,175,199
M 73
R 80
CV 45,223,247
M 84
mw 48
SH 114
AN 114,114
ds_read_b64 v[12:13], v4 offset:4864
M 85
WL 3
AN 115,115
AN 116,118
ds_read_b64 v[46:47], v4 offset:4872
M 86
AN 117,119
SH 118
AR 6,52
M 87
AN 118,118
AN 119,119
AR 7,76
v_mfma_f32_16x16x32_f16 v[144:147], v[72:75], a[24:27], v[128:131]
TY 122,12808
TY 126,26632
AR 8,100
M 89
ad 84,288
W 0
ST 16,1024
v_mfma_f32_16x16x32_f16 v[148:151], v[80:83], a[24:27], v[128:131]
AR 9,124
EF 6,52
EF 7,53
M 91
EF 8,54
EF 9,55
CV 6,6,7
M 92
W 1
CV 7,8,9
AW 224,6
M 93
AW 225,7
AR 6,53
AR 7,77
M 94
AR 8,101
AR 9,125
EF 6,52
M 95
R 82
EF 7,53
M 106
W 2
EF 8,54
EF 9,55
M 107
WL 3
AN 120,122
AN 121,123
CV 6,6,7
M 104
WL 2
SH 122
AN 122,122
CV 7,8,9
M 105
mw 51
AN 123,123
AN 124,126
AW 226,6
v_mfma_f32_16x16x32_f16 v[172:175], v[80:83], a[0:3], v[128:131]
AN 125,127
SH 126
AW 227,7
M 103
AN 126,126
AN 127,127
AR 6,54
v_mfma_f32_16x16x32_f16 v[168:171], v[72:75], a[0:3], v[128:131]
ad 84,294
W 0
ST 18,1792
M 101
AR 7,78
AR 8,102
AR 9,126
M 98
EF 6,52
EF 7,53
EF 8,54
M 99
W 1
EF 9,55
CV 6,6,7
M 96
CV 7,8,9
AW 228,6
AW 229,7
M 97
R 0
AR 6,55
M 108
W 2
AR 7,79
AR 8,103
M 109
AR 9,127
EF 6,52
EF 7,53
M 110
EF 8,54
EF 9,55
CV 6,6,7
M 111
bc 49,4,66,60
CV 7,8,9
AW 230,6
v_mfma_f32_16x16x32_f16 v[192:195], v[72:75], a[8:11], v[128:131]
mh 9
AW 231,7
M 113
ad 66,832
H 0
AR 6,56
v_mfma_f32_16x16x32_f16 v[196:199], v[80:83], a[8:11], v[128:131]
AR 7,80
AR 8,104
AR 9,128
M 115
ST 20,2560
EF 6,52
EF 7,53
M 116
DG 1024
EF 8,54
M 117
EF 9,55
CV 6,6,7
CV 7,8,9
M 118
AW 232,6
AW 233,7
AR 6,57
M 119
WL 0
R 2
AR 7,81
M 120
bc 48,4,64,56
AR 8,105
AR 9,129
M 121
mh 12
EF 6,52
EF 7,53
EF 8,54
M 133
ad 64,832
H 0
EF 9,55
M 132
TX 58,9216
TX 62,23040
CV 6,6,7
M 134
CV 7,8,9
AW 234,6
AW 235,7
M 135
H 1
AR 6,58
AR 7,82
M 123
AR 8,106
AR 9,130
EF 6,52
M 122
TY 66,9728
TY 70,23552
EF 7,53
v_mfma_f32_16x16x32_f16 v[216:219], v[72:75], a[16:19], v[128:131]
H 2
EF 8,54
EF 9,55
M 125
ae 42,72,384
CV 6,6,7
CV 7,8,9
v_mfma_f32_16x16x32_f16 v[240:243], v[76:79], a[28:31], v[128:131]
s_bitcmp1_b32 s50, 8
s_cselect_b32 s52, 24576, 0
v_add_u32 v50, s52, v3
ST 22,0
M 136
TX 74,10752
TX 78,24576
AW 236,6
v_mfma_f32_16x16x32_f16 v[244:247], v[80:83], a[24:27], v[128:131]
WL 4
Q 0
AW 237,7
M 139
Q 2
AR 6,59
v_mfma_f32_16x16x32_f16 v[220:223], v[84:87], a[20:23], v[128:131]
Q 4
AR 7,83
M 126
TY 82,11264
TY 86,25088
AR 8,107
M 128
Q 6
AR 9,131
M 129
WL 4
Q 8
EF 6,52
M 141
Q 10
EF 7,53
M 140
TX 90,12288
TX 94,26112
EF 8,54
M 142
Q 12
EF 9,55
M 143
R 6
CV 6,6,7
M 131
Q 14
CV 7,8,9
M 130
TY 98,12800
TY 102,26624
ST 24,768
M 0
R 4
AW 238,6
M 1
WL 8
Q 16
AW 239,7
M 13
Q 18
BV 52,5120
)"
R"(M 12
Q 20
M 14
Q 22
M 15
Q 24
M 3
Q 26
M 2
Q 28
M 4
Q 30
M 5
WL 5
Q 32
M 17
Q 34
M 16
Q 36
M 18
Q 38
ST 26,1536
M 19
WL 1
Q 40
M 7
Q 42
M 6
Q 44
v_mfma_f32_16x16x32_f16 a[52:55], v[88:91], a[0:3], a[32:35]
Q 46
M 9
AR 6,148
AR 7,172
v_mfma_f32_16x16x32_f16 a[76:79], v[92:95], a[12:15], a[32:35]
AR 8,196
EF 6,12
M 20
EF 7,13
EF 8,46
v_mfma_f32_16x16x32_f16 a[80:83], v[96:99], a[8:11], a[32:35]
EF 152,47
CV 14,6,7
M 23
R 10
v_mfma_f32_16x16x32_f16 a[56:59], v[100:103], a[4:7], a[32:35]
CV 15,8,152
AR 6,149
M 10
R 8
v_mfma_f32_16x16x32_f16 a[104:107], v[96:99], a[16:19], a[32:35]
AR 7,173
AR 8,197
M 35
ST 28,2304
EF 6,12
v_mfma_f32_16x16x32_f16 a[100:103], v[88:91], a[16:19], a[32:35]
EF 7,13
EF 8,46
M 33
EF 153,47
CV 16,6,7
M 30
CV 17,8,153
AR 6,150
M 31
AR 7,174
AR 8,198
M 28
EF 6,12
EF 7,13
M 29
EF 8,46
EF 154,47
M 26
CV 18,6,7
CV 19,8,154
M 27
WV 10
AR 6,151
AR 7,175
M 24
WL 0
FP
AR 8,199
M 25
EF 6,12
EF 7,13
M 36
EF 8,46
EF 155,47
M 37
CV 20,6,7
CV 21,8,155
M 38
ae 42,72,384
ST 30,128
AR 6,152
AR 7,176
M 39
EF 6,12
EF 7,13
EF 132,46
EF 156,47
M 40
CV 22,6,7
CV 23,132,156
AR 6,153
AR 7,177
M 41
EF 6,12
EF 7,13
EF 133,46
EF 157,47
M 42
CV 24,6,7
CV 25,133,157
AR 6,154
AR 7,178
M 43
EF 6,12
EF 7,13
EF 134,46
EF 158,47
v_mfma_f32_16x16x32_f16 a[124:127], v[88:91], a[24:27], a[32:35]
CV 26,6,7
CV 27,134,158
AR 6,155
AR 7,179
M 45
FR
EF 6,12
EF 7,13
EF 135,46
v_mfma_f32_16x16x32_f16 a[128:131], v[96:99], a[24:27], a[32:35]
EF 159,47
CV 28,6,7
CV 29,135,159
M 47
EF 176,52
EF 200,53
v_mfma_f32_16x16x32_f16 a[152:155], v[96:99], a[0:3], a[32:35]
EF 224,54
EF 248,55
M 59
CV 6,176,200
CV 7,224,248
v_mfma_f32_16x16x32_f16 a[148:151], v[88:91], a[0:3], a[32:35]
AW 240,6
AW 241,7
M 57
ST 32,896
EF 177,52
M 54
EF 201,53
EF 225,54
M 55
WL 0
FQ 184
EF 249,55
M 52
CV 6,177,201
CV 7,225,249
M 53
AW 242,6
AW 243,7
M 50
FZ 184
R 24
M 51
mw 72
R 26
M 48
TY 106,520
TY 110,14344
M 49
R 28
M 60
ad 84,336
W 0
M 61
EF 178,52
EF 202,53
M 62
EF 226,54
EF 250,55
M 63
W 1
CV 6,178,202
M 64
ST 34,1664
CV 7,226,250
M 65
AW 244,6
AW 245,7
M 66
W 2
EF 179,52
M 67
WL 3
AN 104,106
AN 105,107
v_mfma_f32_16x16x32_f16 a[172:175], v[88:91], a[8:11], a[32:35]
WL 2
SH 106
AN 106,106
M 69
mw 75
AN 107,107
AN 108,110
v_mfma_f32_16x16x32_f16 a[176:179], v[96:99], a[8:11], a[32:35]
AN 109,111
SH 110
M 71
R 30
v_mfma_f32_16x16x32_f16 v[132:135], v[96:99], a[16:19], v[128:131]
AN 110,110
AN 111,111
M 83
TY 114,2056
TY 118,15880
v_mfma_f32_16x16x32_f16 a[196:199], v[88:91], a[16:19], a[32:35]
ad 84,342
W 0
M 81
EF 203,53
EF 227,54
M 78
EF 251,55
CV 6,179,203
M 79
W 1
ST 36,2432
M 76
CV 7,227,251
AW 246,6
M 77
AW 247,7
M 74
W 2
EF 180,52
EF 204,53
EF 228,54
M 75
EF 252,55
CV 6,180,204
CV 7,228,252
AW 248,6
M 72
WL 1
AN 112,114
AN 113,115
AW 249,7
EF 181,52
M 73
R 32
EF 205,53
EF 229,54
M 84
mw 0
SH 114
AN 114,114
EF 253,55
CV 6,181,205
M 85
WL 2
AN 115,115
AN 116,118
CV 7,229,253
AW 250,6
M 86
AN 117,119
SH 118
AW 251,7
EF 182,52
M 87
AN 118,118
AN 119,119
EF 206,53
EF 230,54
M 88
TY 122,3592
TY 126,17416
EF 254,55
CV 6,182,206
M 89
ad 84,384
W 0
CV 7,230,254
AW 252,6
M 90
ae 42,72,384
AW 253,7
EF 183,52
EF 207,53
M 91
s_bfe_u32 s52, s50, 0x10009
v_mad_i32_i24 v50, v49, s52, v48
ST 38,256
EF 231,54
EF 255,55
v_mfma_f32_16x16x32_f16 v[152:155], v[88:91], a[24:27], v[128:131]
W 1
CV 6,183,207
CV 7,231,255
AW 254,6
M 93
AW 255,7
v_mfma_f32_16x16x32_f16 v[156:159], v[96:99], a[24:27], v[128:131]
M 95
R 34
v_mfma_f32_16x16x32_f16 v[180:183], v[96:99], a[0:3], v[128:131]
W 2
M 107
WL 3
AN 120,122
AN 121,123
v_mfma_f32_16x16x32_f16 v[176:179], v[88:91], a[0:3], v[128:131]
WL 2
SH 122
AN 122,122
M 105
mw 3
AN 123,123
AN 124,126
M 102
AN 125,127
SH 126
M 103
AN 126,126
AN 127,127
M 100
ad 84,390
W 0
M 101
M 98
ST 40,1024
M 99
W 1
M 96
M 97
R 48
M 108
W 2
M 109
M 110
M 111
bc 49,5,66,60
M 112
mh 18
M 113
ad 66,1024
H 0
M 114
M 115
v_mfma_f32_16x16x32_f16 v[200:203], v[88:91], a[8:11], v[128:131]
DG 1024
M 117
ST 42,1792
v_mfma_f32_16x16x32_f16 v[204:207], v[96:99], a[8:11], v[128:131]
M 119
WL 2
R 50
M 120
bc 48,5,64,56
M 121
mh 21
M 133
ad 64,1024
H 0
M 132
TX 58,0
TX 62,13824
M 134
M 135
H 1
M 123
M 122
TY 66,512
TY 70,14336
M 124
H 2
M 125
M 137
ST 44,2560
M 136
TX 74,1536
TX 78,15360
M 138
WL 4
Q 0
M 139
Q 2
M 127
Q 4
M 126
TY 82,2048
TY 86,15872
v_mfma_f32_16x16x32_f16 v[224:227], v[88:91], a[16:19], v[128:131]
Q 6
)"
R"(M 129
WL 4
Q 8
v_mfma_f32_16x16x32_f16 v[248:251], v[92:95], a[28:31], v[128:131]
Q 10
M 140
TX 90,3072
TX 94,16896
v_mfma_f32_16x16x32_f16 v[252:255], v[96:99], a[24:27], v[128:131]
Q 12
M 143
R 54
v_mfma_f32_16x16x32_f16 v[228:231], v[100:103], a[20:23], v[128:131]
Q 14
M 130
TY 98,3584
TY 102,17408
M 0
R 52
M 1
WL 10
Q 16
M 13
Q 18
M 12
Q 20
M 14
Q 22
M 15
WL 8
Q 24
M 3
Q 26
M 2
Q 28
M 4
Q 30
M 5
WL 6
Q 32
M 17
Q 34
M 16
Q 36
M 18
Q 38
M 19
WL 2
Q 40
M 7
Q 42
M 6
Q 44
M 8
Q 46
M 9
ae 42,72,480
M 21
s_bitcmp1_b32 s50, 10
s_cselect_b32 s52, 24576, 0
v_add_u32 v50, s52, v3
ST 14,128
M 20
M 22
M 23
R 58
M 11
M 10
WL 2
R 56
M 34
M 35
M 32
M 33
M 30
M 31
M 28
M 29
ST 16,896
M 26
M 27
WV 9
M 24
WL 0
FP
M 25
M 36
M 37
M 38
M 39
M 40
M 41
M 42
M 43
M 44
ST 18,1664
M 45
FR
M 46
M 47
M 58
M 59
M 56
M 57
M 54
M 55
WL 0
FQ 188
M 52
M 53
M 50
FZ 188
R 72
M 51
mw 24
R 74
ST 20,2432
M 48
TY 106,5128
TY 110,18952
M 49
R 76
M 60
ad 84,432
W 0
M 61
M 62
M 63
W 1
M 64
M 65
M 66
W 2
M 67
WL 3
AN 104,106
AN 105,107
M 68
WL 2
SH 106
AN 106,106
M 69
mw 27
AN 107,107
AN 108,110
M 70
AN 109,111
SH 110
M 71
R 78
M 82
AN 110,110
AN 111,111
M 83
TY 114,6664
TY 118,20488
M 80
ad 84,438
W 0
M 81
ae 42,72,480
M 78
s_bfe_u32 s52, s50, 0x1000b
v_mad_i32_i24 v50, v49, s52, v48
ST 22,128
M 79
W 1
M 76
M 77
M 74
W 2
M 75
M 72
WL 1
AN 112,114
AN 113,115
M 73
R 80
M 84
mw 48
SH 114
AN 114,114
M 85
WL 2
AN 115,115
AN 116,118
M 86
AN 117,119
SH 118
M 87
AN 118,118
AN 119,119
M 88
TY 122,8200
TY 126,22024
M 89
ad 84,480
W 0
ST 24,896
M 90
M 91
M 92
W 1
M 93
M 94
M 95
R 82
M 106
W 2
M 107
WL 3
AN 120,122
AN 121,123
M 104
WL 2
SH 122
AN 122,122
M 105
mw 51
AN 123,123
AN 124,126
M 102
AN 125,127
SH 126
M 103
AN 126,126
AN 127,127
M 100
ad 84,486
W 0
ST 26,1664
M 101
M 98
M 99
W 1
M 96
M 97
R 0
M 108
W 2
M 109
M 110
M 111
bc 49,6,66,60
M 112
mh 0
M 113
ad 66,1216
H 0
M 114
M 115
ST 28,2432
M 116
DG 1024
M 117
M 118
M 119
WL 2
R 2
M 120
bc 48,6,64,56
M 121
mh 3
M 133
ad 64,1216
H 0
M 132
TX 58,4608
TX 62,18432
M 134
M 135
H 1
M 123
M 122
TY 66,5120
TY 70,18944
M 124
H 2
M 125
ae 44,70,768
M 137
s_bitcmp1_b32 s50, 12
s_cselect_b32 s51, 24576, 0
v_add_u32 v47, s51, v3
M 136
TX 74,6144
TX 78,19968
M 138
WL 4
Q 0
M 139
Q 2
M 127
Q 4
M 126
TY 82,6656
TY 86,20480
M 128
Q 6
M 129
WL 4
Q 8
M 141
Q 10
M 140
TX 90,7680
TX 94,21504
M 142
Q 12
M 143
R 6
M 131
Q 14
M 130
TY 98,8192
TY 102,22016
M 0
R 4
M 1
WL 10
Q 16
M 13
Q 18
M 12
Q 20
M 14
Q 22
M 15
WL 8
Q 24
M 3
Q 26
M 2
Q 28
M 4
Q 30
M 5
WL 6
Q 32
M 17
Q 34
M 16
Q 36
M 18
Q 38
M 19
WL 2
Q 40
M 7
Q 42
M 6
Q 44
M 8
Q 46
M 9
XL 30,0
M 21
XL 32,768
M 20
XL 34,1536
M 22
XL 36,2304
M 23
R 10
M 11
M 10
WL 2
R 8
M 34
M 35
M 32
M 33
M 30
M 31
M 28
M 29
M 26
M 27
WV 10
M 24
WL 0
FP
M 25
M 36
ae 42,72,384
M 37
s_bfe_u32 s52, s50, 0x10009
v_mad_i32_i24 v50, v49, s52, v48
SA 200,0
M 38
M 39
M 40
M 41
M 42
M 43
M 44
M 45
FR
M 46
M 47
)"
R"(M 58
M 59
M 56
M 57
M 54
M 55
WL 0
FQ 192
M 52
M 53
M 50
FZ 192
R 24
M 51
mw 72
R 26
M 48
TY 106,9736
TY 110,23560
M 49
R 28
M 60
ad 84,528
W 0
M 61
M 62
M 63
W 1
M 64
M 65
M 66
W 2
M 67
WL 3
AN 104,106
AN 105,107
M 68
WL 2
SH 106
AN 106,106
M 69
mw 75
AN 107,107
AN 108,110
M 70
AN 109,111
SH 110
M 71
R 30
M 82
AN 110,110
AN 111,111
M 83
TY 114,11272
TY 118,25096
M 80
ad 84,534
W 0
M 81
M 78
M 79
W 1
M 76
M 77
M 74
W 2
M 75
M 72
WL 1
AN 112,114
AN 113,115
M 73
R 32
M 84
mw 0
SH 114
AN 114,114
M 85
WL 2
AN 115,115
AN 116,118
M 86
AN 117,119
SH 118
M 87
AN 118,118
AN 119,119
M 88
TY 122,12808
TY 126,26632
M 89
ad 84,576
W 0
M 90
M 91
M 92
W 1
M 93
M 94
M 95
R 34
M 106
W 2
M 107
WL 3
AN 120,122
AN 121,123
M 104
WL 2
SH 122
AN 122,122
M 105
mw 3
AN 123,123
AN 124,126
M 102
AN 125,127
SH 126
M 103
AN 126,126
AN 127,127
M 100
ad 84,582
W 0
M 101
SA 202,768
M 98
M 99
W 1
M 96
M 97
R 48
M 108
W 2
M 109
M 110
M 111
bc 49,7,66,60
M 112
mh 9
M 113
ad 66,1408
H 0
M 114
M 115
M 116
DG 1024
M 117
M 118
M 119
WL 2
R 50
M 120
bc 48,7,64,56
M 121
mh 12
M 133
ad 64,1408
H 0
M 132
TX 58,9216
TX 62,23040
M 134
M 135
H 1
M 123
M 122
TY 66,9728
TY 70,23552
M 124
H 2
M 125
M 137
ae 44,70,768
M 136
TX 74,10752
TX 78,24576
PA 10240
DF
M 138
s_bfe_u32 s51, s50, 0x1000d
WL 4
Q 0
M 139
Q 2
M 127
Q 4
M 126
TY 82,11264
TY 86,25088
M 128
Q 6
M 129
WL 4
Q 8
M 141
Q 10
M 140
TX 90,12288
TX 94,26112
M 142
Q 12
M 143
R 54
M 131
Q 14
M 130
TY 98,12800
TY 102,26624
M 0
R 52
M 1
WL 10
Q 16
M 13
Q 18
M 12
Q 20
M 14
Q 22
M 15
WL 8
Q 24
M 3
Q 26
M 2
Q 28
M 4
Q 30
M 5
WL 6
Q 32
M 17
Q 34
M 16
Q 36
M 18
Q 38
M 19
WL 2
Q 40
M 7
Q 42
M 6
Q 44
M 8
Q 46
M 9
v_mad_i32_i24 v47, v49, s51, v48
XL 38,0
M 21
XL 40,768
M 20
XL 42,1536
M 22
XL 44,2304
M 23
R 58
M 11
M 10
WL 2
R 56
M 34
SA 204,1536
M 35
M 32
M 33
M 30
M 31
M 28
M 29
M 26
M 27
WV 11
M 24
WL 0
FP
M 25
M 36
M 37
M 38
M 39
M 40
UL 6,30
UH 7,30
M 41
AR 8,36
AR 9,60
M 42
AD 8,6
AD 9,7
M 43
AW 36,8
AW 60,9
M 44
UL 6,31
UH 7,31
M 45
FR
AR 8,84
M 46
AR 9,108
AD 8,6
M 47
AD 9,7
AW 84,8
M 58
AW 108,9
UL 6,32
M 59
UH 7,32
AR 8,37
M 56
AR 9,61
AD 8,6
M 57
AD 9,7
AW 37,8
M 54
AW 61,9
UL 6,33
M 55
WL 0
FQ 196
UH 7,33
M 52
AR 8,85
AR 9,109
M 53
AD 8,6
AD 9,7
M 50
FZ 196
R 72
M 51
mw 24
R 74
M 48
TY 106,520
TY 110,14344
M 49
R 76
M 60
ad 84,624
W 0
M 61
AW 85,8
AW 109,9
M 62
UL 6,34
UH 7,34
M 63
W 1
AR 8,38
M 64
AR 9,62
AD 8,6
M 65
AD 9,7
AW 38,8
M 66
W 2
AW 62,9
M 67
WL 3
AN 104,106
AN 105,107
M 68
WL 2
SH 106
AN 106,106
M 69
mw 27
AN 107,107
AN 108,110
M 70
AN 109,111
SH 110
M 71
R 78
M 82
AN 110,110
AN 111,111
M 83
TY 114,2056
TY 118,15880
M 80
ad 84,630
W 0
M 81
UL 6,35
UH 7,35
M 78
AR 8,86
AR 9,110
M 79
W 1
AD 8,6
M 76
AD 9,7
)"
R"(AW 86,8
M 77
AW 110,9
UL 6,36
M 74
W 2
UH 7,36
M 75
AR 8,39
AR 9,63
M 72
WL 1
AN 112,114
AN 113,115
M 73
R 80
M 84
mw 48
SH 114
AN 114,114
M 85
WL 2
AN 115,115
AN 116,118
M 86
AN 117,119
SH 118
M 87
AN 118,118
AN 119,119
M 88
TY 122,3592
TY 126,17416
M 89
ad 84,672
W 0
M 90
SA 206,2304
AD 8,6
M 91
AD 9,7
AW 39,8
M 92
W 1
AW 63,9
M 93
UL 6,37
UH 7,37
M 94
AR 8,87
AR 9,111
M 95
R 82
M 106
W 2
AD 8,6
M 107
WL 3
AN 120,122
AN 121,123
M 104
WL 2
SH 122
AN 122,122
M 105
mw 51
AN 123,123
AN 124,126
M 102
AN 125,127
SH 126
M 103
AN 126,126
AN 127,127
M 100
ad 84,678
W 0
M 101
AD 9,7
AW 87,8
M 98
AW 111,9
M 99
W 1
M 96
M 97
R 0
M 108
W 2
M 109
M 110
M 111
bc 49,8,66,60
M 112
mh 18
M 113
ad 66,1600
H 0
M 114
M 115
M 116
DG 1024
M 117
M 118
M 119
WL 2
R 2
M 120
bc 48,8,64,56
M 121
mh 21
M 133
ad 64,1600
H 0
M 132
TX 58,0
TX 62,13824
M 134
M 135
H 1
M 123
M 122
TY 66,512
TY 70,14336
M 124
H 2
M 125
M 137
M 136
TX 74,1536
TX 78,15360
M 138
WL 4
Q 0
M 139
Q 2
M 127
Q 4
M 126
TY 82,2048
TY 86,15872
M 128
Q 6
M 129
WL 4
Q 8
M 141
Q 10
M 140
TX 90,3072
TX 94,16896
M 142
Q 12
M 143
R 6
M 131
Q 14
M 130
TY 98,3584
TY 102,17408
M 0
R 4
M 1
WL 10
Q 16
M 13
Q 18
M 12
Q 20
M 14
Q 22
M 15
WL 8
Q 24
M 3
Q 26
M 2
Q 28
M 4
Q 30
M 5
WL 6
Q 32
M 17
Q 34
M 16
Q 36
M 18
Q 38
M 19
WL 2
Q 40
M 7
Q 42
M 6
Q 44
M 8
Q 46
M 9
ae 42,72,384
M 21
SA 208,128
M 20
ae 44,68,768
M 22
s_bitcmp1_b32 s50, 14
s_cselect_b32 s51, 24576, 0
v_add_u32 v47, s51, v3
M 23
R 10
M 11
XL 14,0
M 10
WL 2
R 8
M 34
XL 16,768
M 35
XL 18,1536
M 32
XL 20,2304
M 33
M 30
M 31
M 28
M 29
M 26
M 27
WV 10
M 24
WL 0
FP
M 25
M 36
M 37
M 38
M 39
M 40
M 41
M 42
UL 6,38
UH 7,38
M 43
AR 8,40
AR 9,64
M 44
AD 8,6
AD 9,7
M 45
FR
AW 40,8
M 46
AW 64,9
UL 6,39
M 47
UH 7,39
AR 8,88
M 58
AR 9,112
AD 8,6
M 59
AD 9,7
AW 88,8
M 56
AW 112,9
UL 6,40
M 57
UH 7,40
AR 8,41
M 54
AR 9,65
AD 8,6
M 55
WL 0
FQ 200
AD 9,7
M 52
AW 41,8
AW 65,9
M 53
UL 6,41
UH 7,41
M 50
FZ 200
R 24
M 51
mw 72
R 26
M 48
TY 106,5128
TY 110,18952
M 49
R 28
M 60
ad 84,720
W 0
M 61
AR 8,89
AR 9,113
M 62
AD 8,6
AD 9,7
M 63
W 1
AW 89,8
M 64
AW 113,9
UL 6,42
M 65
UH 7,42
AR 8,42
M 66
W 2
AR 9,66
M 67
WL 3
AN 104,106
AN 105,107
M 68
WL 2
SH 106
AN 106,106
M 69
mw 75
AN 107,107
AN 108,110
M 70
AN 109,111
SH 110
M 71
R 30
M 82
AN 110,110
AN 111,111
M 83
TY 114,6664
TY 118,20488
M 80
ad 84,726
W 0
M 81
AD 8,6
AD 9,7
M 78
AW 42,8
AW 66,9
M 79
W 1
UL 6,43
M 76
UH 7,43
AR 8,90
M 77
AR 9,114
AD 8,6
M 74
W 2
AD 9,7
M 75
AW 90,8
AW 114,9
M 72
WL 1
AN 112,114
AN 113,115
M 73
R 32
M 84
mw 0
SH 114
AN 114,114
M 85
WL 2
AN 115,115
AN 116,118
M 86
AN 117,119
SH 118
M 87
AN 118,118
AN 119,119
M 88
TY 122,8200
TY 126,22024
M 89
ad 84,768
W 0
M 90
SA 210,896
UL 6,44
M 91
UH 7,44
AR 8,43
M 92
W 1
AR 9,67
M 93
AD 8,6
AD 9,7
M 94
)"
R"(AW 43,8
AW 67,9
M 95
R 34
M 106
W 2
UL 6,45
M 107
WL 3
AN 120,122
AN 121,123
M 104
WL 2
SH 122
AN 122,122
M 105
mw 3
AN 123,123
AN 124,126
M 102
AN 125,127
SH 126
M 103
AN 126,126
AN 127,127
M 100
ad 84,774
W 0
M 101
UH 7,45
AR 8,91
M 98
AR 9,115
AD 8,6
M 99
W 1
AD 9,7
M 96
AW 91,8
AW 115,9
M 97
R 48
M 108
W 2
M 109
M 110
M 111
bc 49,9,66,60
M 112
mh 0
M 113
ad 66,1792
H 0
M 114
M 115
M 116
DG 1024
M 117
M 118
M 119
WL 2
R 50
M 120
bc 48,9,64,56
M 121
mh 3
M 133
ad 64,1792
H 0
M 132
TX 58,4608
TX 62,18432
M 134
M 135
H 1
M 123
M 122
TY 66,5120
TY 70,18944
M 124
H 2
M 125
M 137
M 136
TX 74,6144
TX 78,19968
M 138
WL 4
Q 0
M 139
Q 2
M 127
Q 4
M 126
TY 82,6656
TY 86,20480
M 128
Q 6
M 129
WL 4
Q 8
M 141
Q 10
M 140
TX 90,7680
TX 94,21504
M 142
Q 12
M 143
R 54
M 131
Q 14
M 130
TY 98,8192
TY 102,22016
M 0
R 52
M 1
WL 10
Q 16
M 13
Q 18
M 12
Q 20
M 14
Q 22
M 15
WL 8
Q 24
M 3
Q 26
M 2
Q 28
M 4
Q 30
M 5
WL 6
Q 32
M 17
Q 34
M 16
Q 36
M 18
Q 38
M 19
WL 2
Q 40
M 7
Q 42
M 6
Q 44
M 8
Q 46
M 9
SA 212,1664
M 21
ae 44,68,768
M 20
s_bfe_u32 s51, s50, 0x1000f
v_mad_i32_i24 v47, v49, s51, v48
XL 22,0
M 22
XL 24,768
M 23
R 58
M 11
XL 26,1536
M 10
WL 2
R 56
M 34
XL 28,2304
M 35
M 32
M 33
M 30
M 31
M 28
M 29
M 26
M 27
WV 10
M 24
WL 0
FP
M 25
M 36
M 37
M 38
M 39
M 40
M 41
M 42
M 43
M 44
UL 6,14
UH 7,14
M 45
FR
AR 8,44
M 46
AR 9,68
AD 8,6
M 47
AD 9,7
AW 44,8
M 58
AW 68,9
UL 6,15
M 59
UH 7,15
AR 8,92
M 56
AR 9,116
AD 8,6
M 57
AD 9,7
AW 92,8
M 54
AW 116,9
UL 6,16
M 55
WL 0
FQ 204
UH 7,16
M 52
AR 8,45
AR 9,69
M 53
AD 8,6
AD 9,7
M 50
FZ 204
R 72
M 51
mw 24
R 74
M 48
TY 106,9736
TY 110,23560
M 49
R 76
M 60
ad 84,816
W 0
M 61
AW 45,8
AW 69,9
M 62
UL 6,17
UH 7,17
M 63
W 1
AR 8,93
M 64
AR 9,117
AD 8,6
M 65
AD 9,7
AW 93,8
M 66
W 2
AW 117,9
M 67
WL 3
AN 104,106
AN 105,107
M 68
WL 2
SH 106
AN 106,106
M 69
mw 27
AN 107,107
AN 108,110
M 70
AN 109,111
SH 110
M 71
R 78
M 82
AN 110,110
AN 111,111
M 83
TY 114,11272
TY 118,25096
M 80
ad 84,822
W 0
M 81
UL 6,18
UH 7,18
M 78
AR 8,46
AR 9,70
M 79
W 1
AD 8,6
M 76
AD 9,7
AW 46,8
M 77
AW 70,9
UL 6,19
M 74
W 2
UH 7,19
M 75
SA 214,2432
AR 8,94
M 72
WL 1
AN 112,114
AN 113,115
M 73
R 80
M 84
mw 48
SH 114
AN 114,114
M 85
WL 2
AN 115,115
AN 116,118
M 86
AN 117,119
SH 118
M 87
AN 118,118
AN 119,119
M 88
TY 122,12808
TY 126,26632
M 89
ad 84,864
W 0
M 90
AR 9,118
AD 8,6
M 91
AD 9,7
AW 94,8
M 92
W 1
AW 118,9
M 93
UL 6,20
UH 7,20
M 94
AR 8,47
AR 9,71
M 95
R 82
M 106
W 2
AD 8,6
M 107
WL 3
AN 120,122
AN 121,123
M 104
WL 2
SH 122
AN 122,122
M 105
mw 51
AN 123,123
AN 124,126
M 102
AN 125,127
SH 126
M 103
AN 126,126
AN 127,127
M 100
ad 84,870
W 0
M 101
AD 9,7
AW 47,8
M 98
AW 71,9
UL 6,21
M 99
W 1
UH 7,21
M 96
AR 8,95
AR 9,119
M 97
R 0
M 108
W 2
AD 8,6
M 109
AD 9,7
AW 95,8
M 110
AW 119,9
M 111
)"
R"(bc 49,10,66,60
M 112
mh 9
M 113
ad 66,1984
H 0
M 114
M 115
M 116
DG 1024
M 117
M 118
M 119
WL 2
R 2
M 120
bc 48,10,64,56
M 121
mh 12
M 133
ad 64,1984
H 0
M 132
TX 58,9216
TX 62,23040
M 134
M 135
H 1
M 123
M 122
TY 66,9728
TY 70,23552
M 124
H 2
M 125
M 137
M 136
TX 74,10752
TX 78,24576
M 138
WL 4
Q 0
M 139
Q 2
M 127
Q 4
M 126
TY 82,11264
TY 86,25088
M 128
Q 6
M 129
WL 4
Q 8
M 141
Q 10
M 140
TX 90,12288
TX 94,26112
M 142
Q 12
M 143
R 6
M 131
Q 14
M 130
TY 98,12800
TY 102,26624
M 0
R 4
M 1
WL 10
Q 16
M 13
Q 18
M 12
Q 20
M 14
Q 22
M 15
WL 8
Q 24
M 3
Q 26
M 2
Q 28
M 4
Q 30
M 5
WL 6
Q 32
M 17
Q 34
M 16
Q 36
M 18
Q 38
M 19
WL 2
Q 40
M 7
Q 42
M 6
Q 44
M 8
Q 46
M 9
ae 42,72,384
M 21
s_bitcmp1_b32 s50, 8
s_cselect_b32 s52, 24576, 0
v_add_u32 v50, s52, v3
M 20
SA 216,256
M 22
ae 44,68,864
M 23
R 10
M 11
s_bitcmp1_b32 s50, 16
s_cselect_b32 s51, 24576, 0
v_add_u32 v47, s51, v3
M 10
WL 2
R 8
M 34
XL 30,0
M 35
XL 32,768
M 32
XL 34,1536
M 33
XL 36,2304
M 30
M 31
M 28
M 29
M 26
M 27
WV 10
M 24
WL 0
FP
M 25
M 36
M 37
M 38
M 39
M 40
M 41
M 42
M 43
M 44
M 45
FR
M 46
UL 6,22
UH 7,22
M 47
AR 8,48
AR 9,72
M 58
AD 8,6
AD 9,7
M 59
AW 48,8
AW 72,9
M 56
UL 6,23
UH 7,23
M 57
AR 8,96
AR 9,120
M 54
AD 8,6
AD 9,7
M 55
WL 0
FQ 208
AW 96,8
M 52
AW 120,9
UL 6,24
M 53
UH 7,24
AR 8,49
M 50
FZ 208
R 24
M 51
mw 72
R 26
M 48
TY 106,520
TY 110,14344
M 49
R 28
M 60
ad 84,912
W 0
M 61
AR 9,73
AD 8,6
M 62
AD 9,7
AW 49,8
M 63
W 1
AW 73,9
M 64
UL 6,25
UH 7,25
M 65
AR 8,97
AR 9,121
M 66
W 2
AD 8,6
M 67
WL 3
AN 104,106
AN 105,107
M 68
WL 2
SH 106
AN 106,106
M 69
mw 75
AN 107,107
AN 108,110
M 70
AN 109,111
SH 110
M 71
R 30
M 82
AN 110,110
AN 111,111
M 83
TY 114,2056
TY 118,15880
M 80
ad 84,918
W 0
M 81
AD 9,7
AW 97,8
M 78
AW 121,9
UL 6,26
M 79
W 1
UH 7,26
M 76
AR 8,50
AR 9,74
M 77
AD 8,6
AD 9,7
M 74
W 2
AW 50,8
M 75
AW 74,9
UL 6,27
M 72
WL 1
AN 112,114
AN 113,115
M 73
R 32
M 84
mw 0
SH 114
AN 114,114
M 85
WL 2
AN 115,115
AN 116,118
M 86
AN 117,119
SH 118
M 87
AN 118,118
AN 119,119
M 88
TY 122,3592
TY 126,17416
M 89
ad 84,960
W 0
M 90
SA 218,1024
UH 7,27
M 91
AR 8,98
AR 9,122
M 92
W 1
AD 8,6
M 93
AD 9,7
AW 98,8
M 94
AW 122,9
UL 6,28
M 95
R 34
M 106
W 2
UH 7,28
M 107
WL 3
AN 120,122
AN 121,123
M 104
WL 2
SH 122
AN 122,122
M 105
mw 3
AN 123,123
AN 124,126
M 102
AN 125,127
SH 126
M 103
AN 126,126
AN 127,127
M 100
ad 84,966
W 0
M 101
AR 8,51
AR 9,75
M 98
AD 8,6
AD 9,7
M 99
W 1
AW 51,8
M 96
AW 75,9
UL 6,29
M 97
R 48
M 108
W 2
UH 7,29
M 109
AR 8,99
AR 9,123
M 110
AD 8,6
AD 9,7
M 111
bc 49,11,66,60
AW 99,8
M 112
mh 18
AW 123,9
M 113
ad 66,2176
H 0
M 114
M 115
M 116
DG 1024
M 117
M 118
M 119
WL 2
R 50
M 120
bc 48,11,64,56
M 121
mh 21
M 133
ad 64,2176
H 0
M 132
TX 58,0
TX 62,13824
M 134
M 135
H 1
M 123
M 122
TY 66,512
TY 70,14336
M 124
H 2
M 125
M 137
M 136
TX 74,1536
TX 78,15360
PA 10240
DF
M 138
)"
R"(WL 4
Q 0
M 139
Q 2
M 127
Q 4
M 126
TY 82,2048
TY 86,15872
M 128
Q 6
M 129
WL 4
Q 8
M 141
Q 10
M 140
TX 90,3072
TX 94,16896
M 142
Q 12
M 143
R 54
M 131
Q 14
M 130
TY 98,3584
TY 102,17408
M 0
R 52
M 1
WL 10
Q 16
M 13
Q 18
M 12
Q 20
M 14
Q 22
M 15
WL 8
Q 24
M 3
Q 26
M 2
Q 28
M 4
Q 30
M 5
WL 6
Q 32
M 17
Q 34
M 16
Q 36
M 18
Q 38
M 19
WL 2
Q 40
M 7
Q 42
M 6
Q 44
M 8
Q 46
M 9
SA 220,1792
M 21
ae 44,68,864
M 20
s_bfe_u32 s51, s50, 0x10011
v_mad_i32_i24 v47, v49, s51, v48
XL 38,0
M 22
XL 40,768
M 23
R 58
M 11
XL 42,1536
M 10
WL 2
R 56
M 34
XL 44,2304
M 35
M 32
M 33
M 30
M 31
M 28
M 29
M 26
M 27
WV 11
M 24
WL 0
FP
M 25
M 36
M 37
M 38
M 39
M 40
M 41
M 42
M 43
M 44
M 45
FR
M 46
M 47
M 58
UL 6,30
UH 7,30
M 59
AR 8,52
AR 9,76
M 56
AD 8,6
AD 9,7
M 57
AW 52,8
AW 76,9
M 54
UL 6,31
UH 7,31
M 55
WL 0
FQ 212
AR 8,100
M 52
AR 9,124
AD 8,6
M 53
AD 9,7
AW 100,8
M 50
FZ 212
R 72
M 51
mw 24
R 74
M 48
TY 106,5128
TY 110,18952
M 49
R 76
M 60
ad 84,1008
W 0
M 61
AW 124,9
UL 6,32
M 62
UH 7,32
AR 8,53
M 63
W 1
AR 9,77
M 64
AD 8,6
AD 9,7
M 65
AW 53,8
AW 77,9
M 66
W 2
UL 6,33
M 67
WL 3
AN 104,106
AN 105,107
M 68
WL 2
SH 106
AN 106,106
M 69
mw 27
AN 107,107
AN 108,110
M 70
AN 109,111
SH 110
M 71
R 78
M 82
AN 110,110
AN 111,111
M 83
TY 114,6664
TY 118,20488
M 80
ad 84,1014
W 0
M 81
UH 7,33
AR 8,101
M 78
AR 9,125
AD 8,6
M 79
W 1
AD 9,7
M 76
AW 101,8
AW 125,9
M 77
UL 6,34
UH 7,34
M 74
W 2
AR 8,54
M 75
SA 222,2560
AR 9,78
M 72
WL 1
AN 112,114
AN 113,115
M 73
R 80
M 84
mw 48
SH 114
AN 114,114
M 85
WL 2
AN 115,115
AN 116,118
M 86
AN 117,119
SH 118
M 87
AN 118,118
AN 119,119
M 88
TY 122,8200
TY 126,22024
M 89
ad 84,1056
W 0
M 90
AD 8,6
AD 9,7
M 91
AW 54,8
AW 78,9
M 92
W 1
UL 6,35
M 93
UH 7,35
AR 8,102
M 94
AR 9,126
AD 8,6
M 95
R 82
M 106
W 2
AD 9,7
M 107
WL 3
AN 120,122
AN 121,123
M 104
WL 2
SH 122
AN 122,122
M 105
mw 51
AN 123,123
AN 124,126
M 102
AN 125,127
SH 126
M 103
AN 126,126
AN 127,127
M 100
ad 84,1062
W 0
M 101
AW 102,8
AW 126,9
M 98
UL 6,36
UH 7,36
M 99
W 1
AR 8,55
M 96
AR 9,79
AD 8,6
M 97
R 0
M 108
W 2
AD 9,7
M 109
AW 55,8
AW 79,9
M 110
UL 6,37
UH 7,37
M 111
bc 49,12,66,60
AR 8,103
M 112
mh 0
AR 9,127
AD 8,6
M 113
ad 66,2368
H 0
M 114
AD 9,7
AW 103,8
M 115
AW 127,9
M 116
DG 1024
M 117
M 118
M 119
WL 2
R 2
M 120
bc 48,12,64,56
M 121
mh 3
M 133
ad 64,2368
H 0
M 132
TX 58,4608
TX 62,18432
M 134
M 135
H 1
M 123
M 122
TY 66,5120
TY 70,18944
M 124
H 2
M 125
M 137
M 136
TX 74,6144
TX 78,19968
M 138
WL 4
Q 0
M 139
Q 2
M 127
Q 4
M 126
TY 82,6656
TY 86,20480
M 128
Q 6
M 129
WL 4
Q 8
M 141
Q 10
M 140
TX 90,7680
TX 94,21504
M 142
Q 12
M 143
R 6
M 131
Q 14
M 130
TY 98,8192
TY 102,22016
M 0
R 4
M 1
WL 10
Q 16
M 13
Q 18
M 12
Q 20
M 14
Q 22
M 15
WL 8
Q 24
M 3
Q 26
M 2
Q 28
M 4
)"
R"(Q 30
M 5
WL 6
Q 32
M 17
Q 34
M 16
Q 36
M 18
Q 38
M 19
WL 2
Q 40
M 7
Q 42
M 6
Q 44
M 8
Q 46
M 9
ae 42,72,480
M 21
s_bitcmp1_b32 s50, 10
s_cselect_b32 s52, 24576, 0
v_add_u32 v50, s52, v3
M 20
SA 224,0
M 22
M 23
R 10
M 11
M 10
WL 2
R 8
M 34
M 35
M 32
M 33
M 30
M 31
M 28
M 29
M 26
ae 44,70,768
M 27
WV 6
M 24
WL 0
FP
M 25
M 36
s_bitcmp1_b32 s50, 12
s_cselect_b32 s51, 24576, 0
v_add_u32 v47, s51, v3
M 37
XL 14,128
M 38
XL 16,896
M 39
M 40
M 41
M 42
M 43
M 44
XL 18,1664
M 45
FR
M 46
M 47
M 58
M 59
M 56
XL 20,2432
UL 6,38
M 57
UH 7,38
AR 8,56
M 54
AR 9,80
AD 8,6
M 55
WL 0
FQ 216
AD 9,7
M 52
AW 56,8
AW 80,9
M 53
UL 6,39
UH 7,39
M 50
FZ 216
R 24
M 51
mw 72
R 26
M 48
TY 106,9736
TY 110,23560
M 49
R 28
M 60
ad 84,1104
W 0
M 61
AR 8,104
AR 9,128
M 62
AD 8,6
AD 9,7
M 63
W 1
AW 104,8
M 64
AW 128,9
UL 6,40
M 65
UH 7,40
AR 8,57
M 66
W 2
AR 9,81
M 67
WL 3
AN 104,106
AN 105,107
M 68
WL 2
SH 106
AN 106,106
M 69
mw 75
AN 107,107
AN 108,110
M 70
AN 109,111
SH 110
M 71
R 30
M 82
AN 110,110
AN 111,111
M 83
TY 114,11272
TY 118,25096
M 80
ad 84,1110
W 0
M 81
AD 8,6
AD 9,7
M 78
AW 57,8
AW 81,9
M 79
W 1
UL 6,41
M 76
UH 7,41
AR 8,105
M 77
AR 9,129
AD 8,6
M 74
W 2
AD 9,7
M 75
AW 105,8
AW 129,9
M 72
WL 1
AN 112,114
AN 113,115
M 73
R 32
M 84
mw 0
SH 114
AN 114,114
M 85
WL 2
AN 115,115
AN 116,118
M 86
AN 117,119
SH 118
M 87
AN 118,118
AN 119,119
M 88
TY 122,12808
TY 126,26632
M 89
ad 84,1152
W 0
M 90
SA 226,768
UL 6,42
M 91
UH 7,42
AR 8,58
M 92
W 1
AR 9,82
M 93
AD 8,6
AD 9,7
M 94
AW 58,8
AW 82,9
M 95
R 34
M 106
W 2
UL 6,43
M 107
WL 3
AN 120,122
AN 121,123
M 104
WL 2
SH 122
AN 122,122
M 105
mw 3
AN 123,123
AN 124,126
M 102
AN 125,127
SH 126
M 103
AN 126,126
AN 127,127
M 100
ad 84,1158
W 0
M 101
UH 7,43
AR 8,106
M 98
AR 9,130
AD 8,6
M 99
W 1
AD 9,7
M 96
AW 106,8
AW 130,9
M 97
R 48
M 108
W 2
UL 6,44
M 109
UH 7,44
AR 8,59
M 110
AR 9,83
AD 8,6
M 111
bc 49,13,66,60
AD 9,7
M 112
mh 9
AW 59,8
AW 83,9
M 113
ad 66,2560
H 0
M 114
UL 6,45
UH 7,45
M 115
AR 8,107
AR 9,131
M 116
DG 1024
M 117
AD 8,6
AD 9,7
M 118
AW 107,8
AW 131,9
M 119
WL 0
R 50
M 120
bc 48,13,64,56
M 121
mh 12
M 133
ad 64,2560
H 0
M 132
TX 58,9216
TX 62,23040
M 134
M 135
H 1
M 123
M 122
TY 66,9728
TY 70,23552
M 124
H 2
M 125
M 137
M 136
TX 74,10752
TX 78,24576
M 138
WL 4
Q 0
M 139
Q 2
M 127
Q 4
M 126
TY 82,11264
TY 86,25088
M 128
Q 6
M 129
WL 4
Q 8
M 141
Q 10
M 140
TX 90,12288
TX 94,26112
M 142
Q 12
M 143
R 54
M 131
Q 14
M 130
TY 98,12800
TY 102,26624
M 0
R 52
M 1
WL 10
Q 16
M 13
Q 18
M 12
Q 20
M 14
Q 22
M 15
WL 8
Q 24
M 3
Q 26
M 2
Q 28
M 4
Q 30
M 5
WL 6
Q 32
M 17
Q 34
M 16
Q 36
M 18
Q 38
M 19
WL 2
Q 40
M 7
Q 42
M 6
Q 44
M 8
Q 46
M 9
SA 228,1536
M 21
M 20
M 22
M 23
R 58
M 11
M 10
WL 2
R 56
M 34
M 35
M 32
M 33
M 30
M 31
M 28
M 29
M 26
M 27
WV 6
M 24
)"
R"(WL 0
FP
M 25
M 36
ae 44,70,768
M 37
s_bfe_u32 s51, s50, 0x1000d
v_mad_i32_i24 v47, v49, s51, v48
XL 22,128
M 38
M 39
M 40
XL 24,896
M 41
M 42
M 43
M 44
M 45
FR
M 46
XL 26,1664
M 47
M 58
M 59
M 56
M 57
M 54
XL 28,2432
M 55
WL 0
FQ 220
M 52
M 53
M 50
FZ 220
R 72
M 51
mw 24
R 74
M 48
TY 106,520
TY 110,14344
M 49
R 76
M 60
ad 84,1200
W 0
M 61
M 62
M 63
W 1
M 64
M 65
M 66
W 2
M 67
WL 3
AN 104,106
AN 105,107
M 68
WL 2
SH 106
AN 106,106
M 69
mw 27
AN 107,107
AN 108,110
M 70
AN 109,111
SH 110
M 71
R 78
M 82
AN 110,110
AN 111,111
M 83
TY 114,2056
TY 118,15880
M 80
ad 84,1206
W 0
M 81
M 78
M 79
W 1
M 76
M 77
M 74
W 2
M 75
SA 230,2304
M 72
WL 1
AN 112,114
AN 113,115
M 73
R 80
M 84
mw 48
SH 114
AN 114,114
M 85
WL 2
AN 115,115
AN 116,118
M 86
AN 117,119
SH 118
M 87
AN 118,118
AN 119,119
M 88
TY 122,3592
TY 126,17416
M 89
ad 84,1248
W 0
M 90
UL 6,14
UH 7,14
M 91
AR 8,132
AR 9,156
M 92
W 1
AD 8,6
M 93
AD 9,7
AW 132,8
M 94
AW 156,9
UL 6,15
M 95
R 82
M 106
W 2
UH 7,15
M 107
WL 3
AN 120,122
AN 121,123
M 104
WL 2
SH 122
AN 122,122
M 105
mw 51
AN 123,123
AN 124,126
M 102
AN 125,127
SH 126
M 103
AN 126,126
AN 127,127
M 100
ad 84,1254
W 0
M 101
AR 8,180
AD 8,6
M 98
AD 136,7
AW 180,8
M 99
W 1
UL 6,16
M 96
UH 7,16
AR 8,133
M 97
R 0
M 108
W 2
AR 9,157
M 109
AD 8,6
AD 9,7
M 110
AW 133,8
AW 157,9
M 111
bc 49,14,66,60
UL 6,17
M 112
mh 18
UH 7,17
AR 8,181
M 113
ad 66,2752
H 0
M 114
AD 8,6
AD 137,7
M 115
AW 181,8
UL 6,18
M 116
DG 1024
M 117
UH 7,18
AR 8,134
M 118
AR 9,158
AD 8,6
M 119
WL 0
R 2
M 120
bc 48,14,64,56
AD 9,7
M 121
mh 21
AW 134,8
AW 158,9
M 133
ad 64,2752
H 0
M 132
TX 58,0
TX 62,13824
M 134
UL 6,19
UH 7,19
M 135
H 1
AR 8,182
M 123
AD 8,6
AD 138,7
M 122
TY 66,512
TY 70,14336
M 124
H 2
AW 182,8
M 125
UL 6,20
UH 7,20
M 137
AR 8,135
AR 9,159
M 136
TX 74,1536
TX 78,15360
M 138
WL 4
Q 0
M 139
Q 2
M 127
Q 4
M 126
TY 82,2048
TY 86,15872
M 128
Q 6
M 129
WL 4
Q 8
M 141
Q 10
M 140
TX 90,3072
TX 94,16896
M 142
Q 12
M 143
R 6
M 131
Q 14
M 130
TY 98,3584
TY 102,17408
M 0
R 4
M 1
WL 10
Q 16
M 13
Q 18
M 12
Q 20
M 14
Q 22
M 15
WL 8
Q 24
M 3
Q 26
M 2
Q 28
M 4
Q 30
M 5
WL 6
Q 32
M 17
Q 34
M 16
Q 36
M 18
Q 38
M 19
WL 2
Q 40
M 7
Q 42
M 6
Q 44
M 8
Q 46
M 9
ae 42,72,480
M 21
s_bfe_u32 s52, s50, 0x1000b
v_mad_i32_i24 v50, v49, s52, v48
SA 232,0
M 20
AD 8,6
AD 9,7
M 22
AW 135,8
AW 159,9
M 23
R 10
M 11
UL 6,21
UH 7,21
M 10
WL 2
R 8
M 34
AR 8,183
AD 8,6
M 35
AD 139,7
AW 183,8
M 32
M 33
M 30
M 31
M 28
M 29
M 26
M 27
WV 6
M 24
WL 0
FP
M 25
M 36
ae 44,68,768
M 37
s_bitcmp1_b32 s50, 14
s_cselect_b32 s51, 24576, 0
v_add_u32 v47, s51, v3
M 38
XL 30,128
M 39
M 40
M 41
M 42
XL 32,896
M 43
M 44
M 45
FR
M 46
M 47
M 58
XL 34,1664
M 59
M 56
M 57
M 54
M 55
WL 0
FQ 224
M 52
XL 36,2432
M 53
M 50
FZ 224
R 24
M 51
mw 72
R 26
M 48
TY 106,5128
TY 110,18952
M 49
R 28
M 60
ad 84,1296
W 0
M 61
M 62
M 63
W 1
)"
R"(M 64
M 65
M 66
W 2
M 67
WL 3
AN 104,106
AN 105,107
M 68
WL 2
SH 106
AN 106,106
M 69
mw 75
AN 107,107
AN 108,110
M 70
AN 109,111
SH 110
M 71
R 30
M 82
AN 110,110
AN 111,111
M 83
TY 114,6664
TY 118,20488
M 80
ad 84,1302
W 0
M 81
M 78
M 79
W 1
M 76
M 77
M 74
W 2
M 75
M 72
WL 1
AN 112,114
AN 113,115
M 73
R 32
M 84
mw 0
SH 114
AN 114,114
M 85
WL 2
AN 115,115
AN 116,118
M 86
AN 117,119
SH 118
M 87
AN 118,118
AN 119,119
M 88
TY 122,8200
TY 126,22024
M 89
ad 84,1344
W 0
M 90
SA 234,768
UL 6,22
M 91
UH 7,22
AR 8,136
M 92
W 1
AR 9,160
M 93
AD 8,6
AD 9,7
M 94
AW 136,8
AW 160,9
M 95
R 34
M 106
W 2
UL 6,23
M 107
WL 3
AN 120,122
AN 121,123
M 104
WL 2
SH 122
AN 122,122
M 105
mw 3
AN 123,123
AN 124,126
M 102
AN 125,127
SH 126
M 103
AN 126,126
AN 127,127
M 100
ad 84,1350
W 0
M 101
UH 7,23
AR 8,184
M 98
AD 8,6
AD 140,7
M 99
W 1
AW 184,8
M 96
UL 6,24
UH 7,24
M 97
R 48
M 108
W 2
AR 8,137
M 109
AR 9,161
AD 8,6
M 110
AD 9,7
AW 137,8
M 111
bc 49,15,66,60
AW 161,9
M 112
mh 0
UL 6,25
UH 7,25
M 113
ad 66,2944
H 0
M 114
AR 8,185
AD 8,6
M 115
AD 141,7
AW 185,8
M 116
DG 1024
M 117
UL 6,26
UH 7,26
M 118
AR 8,138
AR 9,162
M 119
WL 0
R 50
M 120
bc 48,15,64,56
AD 8,6
M 121
mh 3
AD 9,7
AW 138,8
M 133
ad 64,2944
H 0
M 132
TX 58,4608
TX 62,18432
M 134
AW 162,9
UL 6,27
M 135
H 1
UH 7,27
M 123
AR 8,186
AD 8,6
M 122
TY 66,5120
TY 70,18944
M 124
H 2
AD 142,7
M 125
AW 186,8
UL 6,28
M 137
UH 7,28
AR 8,139
M 136
TX 74,6144
TX 78,19968
PA 10240
DF
M 138
WL 4
Q 0
M 139
Q 2
M 127
Q 4
M 126
TY 82,6656
TY 86,20480
M 128
Q 6
M 129
WL 4
Q 8
M 141
Q 10
M 140
TX 90,7680
TX 94,21504
M 142
Q 12
M 143
R 54
M 131
Q 14
M 130
TY 98,8192
TY 102,22016
M 0
R 52
M 1
WL 10
Q 16
M 13
Q 18
M 12
Q 20
M 14
Q 22
M 15
WL 8
Q 24
M 3
Q 26
M 2
Q 28
M 4
Q 30
M 5
WL 6
Q 32
M 17
Q 34
M 16
Q 36
M 18
Q 38
M 19
WL 2
Q 40
M 7
Q 42
M 6
Q 44
M 8
Q 46
M 9
SA 236,1536
AR 9,163
M 21
AD 8,6
AD 9,7
M 20
AW 139,8
AW 163,9
M 22
UL 6,29
UH 7,29
M 23
R 58
M 11
AR 8,187
AD 8,6
M 10
WL 2
R 56
M 34
AD 143,7
AW 187,8
M 35
M 32
M 33
M 30
M 31
M 28
M 29
M 26
M 27
WV 7
M 24
WL 0
FP
M 25
M 36
M 37
M 38
ae 44,68,768
M 39
s_bfe_u32 s51, s50, 0x1000f
v_mad_i32_i24 v47, v49, s51, v48
XL 38,128
M 40
M 41
M 42
M 43
M 44
XL 40,896
M 45
FR
M 46
M 47
M 58
M 59
M 56
XL 42,1664
M 57
M 54
M 55
WL 0
FQ 228
M 52
M 53
M 50
FZ 228
R 72
M 51
mw 24
R 74
M 48
TY 106,9736
TY 110,23560
M 49
R 76
M 60
ad 84,1392
W 0
M 61
XL 44,2432
M 62
M 63
W 1
M 64
M 65
M 66
W 2
M 67
WL 3
AN 104,106
AN 105,107
M 68
WL 2
SH 106
AN 106,106
M 69
mw 27
AN 107,107
AN 108,110
M 70
AN 109,111
SH 110
M 71
R 78
M 82
AN 110,110
AN 111,111
M 83
TY 114,11272
TY 118,25096
M 80
ad 84,1398
W 0
M 81
M 78
M 79
W 1
M 76
M 77
M 74
W 2
M 75
SA 238,2304
M 72
WL 1
AN 112,114
AN 113,115
M 73
R 80
M 84
mw 48
SH 114
AN 114,114
M 85
WL 2
AN 115,115
)"
R"(AN 116,118
M 86
AN 117,119
SH 118
M 87
AN 118,118
AN 119,119
M 88
TY 122,12808
TY 126,26632
M 89
ad 84,1440
W 0
M 90
M 91
M 92
W 1
UL 6,30
M 93
UH 7,30
AR 8,140
M 94
AR 9,164
AD 8,6
M 95
R 82
M 106
W 2
AD 9,7
M 107
WL 3
AN 120,122
AN 121,123
M 104
WL 2
SH 122
AN 122,122
M 105
mw 51
AN 123,123
AN 124,126
M 102
AN 125,127
SH 126
M 103
AN 126,126
AN 127,127
M 100
ad 84,1446
W 0
M 101
AW 140,8
AW 164,9
M 98
UL 6,31
UH 7,31
M 99
W 1
AR 8,188
M 96
AD 8,6
AD 144,7
M 97
R 0
M 108
W 2
AW 188,8
M 109
UL 6,32
UH 7,32
M 110
AR 8,141
AR 9,165
M 111
bc 49,16,66,60
AD 8,6
M 112
mh 9
AD 9,7
AW 141,8
M 113
ad 66,3136
H 0
M 114
AW 165,9
UL 6,33
M 115
UH 7,33
AR 8,189
M 116
DG 1024
M 117
AD 8,6
AD 145,7
M 118
AW 189,8
UL 6,34
M 119
WL 0
R 2
M 120
bc 48,16,64,56
UH 7,34
M 121
mh 12
AR 8,142
AR 9,166
M 133
ad 64,3136
H 0
M 132
TX 58,9216
TX 62,23040
M 134
AD 8,6
AD 9,7
M 135
H 1
AW 142,8
M 123
AW 166,9
UL 6,35
M 122
TY 66,9728
TY 70,23552
M 124
H 2
UH 7,35
M 125
AR 8,190
AD 8,6
M 137
AD 146,7
AW 190,8
M 136
TX 74,10752
TX 78,24576
M 138
WL 4
Q 0
M 139
Q 2
M 127
Q 4
M 126
TY 82,11264
TY 86,25088
M 128
Q 6
M 129
WL 4
Q 8
M 141
Q 10
M 140
TX 90,12288
TX 94,26112
M 142
Q 12
M 143
R 6
M 131
Q 14
M 130
TY 98,12800
TY 102,26624
M 0
R 4
M 1
WL 10
Q 16
M 13
Q 18
M 12
Q 20
M 14
Q 22
M 15
WL 8
Q 24
M 3
Q 26
M 2
Q 28
M 4
Q 30
M 5
WL 6
Q 32
M 17
Q 34
M 16
Q 36
M 18
Q 38
M 19
WL 2
Q 40
M 7
Q 42
M 6
Q 44
M 8
Q 46
M 9
ae 42,72,480
M 21
s_bitcmp1_b32 s50, 10
s_cselect_b32 s52, 24576, 0
v_add_u32 v50, s52, v3
M 20
SA 240,256
UL 6,36
M 22
UH 7,36
AR 8,143
M 23
R 10
M 11
AR 9,167
AD 8,6
M 10
WL 2
R 8
M 34
AD 9,7
AW 143,8
M 35
AW 167,9
UL 6,37
M 32
UH 7,37
AR 8,191
M 33
AD 8,6
AD 147,7
M 30
AW 191,8
M 31
M 28
M 29
M 26
M 27
WV 6
M 24
WL 0
FP
M 25
M 36
M 37
M 38
M 39
M 40
ae 44,68,864
M 41
s_bitcmp1_b32 s50, 16
s_cselect_b32 s51, 24576, 0
v_add_u32 v47, s51, v3
M 42
XL 14,128
M 43
M 44
M 45
FR
M 46
XL 16,896
M 47
M 58
M 59
M 56
M 57
M 54
XL 18,1664
M 55
WL 0
FQ 232
M 52
M 53
M 50
FZ 232
R 24
M 51
mw 72
R 26
M 48
TY 106,520
TY 110,14344
M 49
R 28
M 60
ad 84,1488
W 0
M 61
XL 20,2432
M 62
M 63
W 1
M 64
M 65
M 66
W 2
M 67
WL 3
AN 104,106
AN 105,107
M 68
WL 2
SH 106
AN 106,106
M 69
mw 75
AN 107,107
AN 108,110
M 70
AN 109,111
SH 110
M 71
R 30
M 82
AN 110,110
AN 111,111
M 83
TY 114,2056
TY 118,15880
M 80
ad 84,1494
W 0
M 81
M 78
M 79
W 1
M 76
M 77
M 74
W 2
M 75
M 72
WL 1
AN 112,114
AN 113,115
M 73
R 32
M 84
mw 0
SH 114
AN 114,114
M 85
WL 2
AN 115,115
AN 116,118
M 86
AN 117,119
SH 118
M 87
AN 118,118
AN 119,119
M 88
TY 122,3592
TY 126,17416
M 89
ad 84,1536
W 0
M 90
SA 242,1024
M 91
M 92
W 1
M 93
M 94
UL 6,38
UH 7,38
M 95
R 34
M 106
W 2
AR 8,144
M 107
WL 3
AN 120,122
AN 121,123
M 104
WL 2
SH 122
AN 122,122
M 105
mw 3
AN 123,123
AN 124,126
M 102
AN 125,127
SH 126
M 103
AN 126,126
AN 127,127
M 100
ad 84,1542
W 0
M 101
AR 9,168
AD 8,6
)"
R"(M 98
AD 9,7
AW 144,8
M 99
W 1
AW 168,9
M 96
UL 6,39
UH 7,39
M 97
R 48
M 108
W 2
AR 8,192
M 109
AD 8,6
AD 148,7
M 110
AW 192,8
UL 6,40
M 111
bc 49,17,66,60
UH 7,40
M 112
mh 18
AR 8,145
AR 9,169
M 113
ad 66,3328
H 0
M 114
AD 8,6
AD 9,7
M 115
AW 145,8
AW 169,9
M 116
DG 1024
M 117
UL 6,41
UH 7,41
M 118
AR 8,193
AD 8,6
M 119
WL 0
R 50
M 120
bc 48,17,64,56
AD 149,7
M 121
mh 21
AW 193,8
UL 6,42
M 133
ad 64,3328
H 0
M 132
TX 58,0
TX 62,13824
M 134
UH 7,42
AR 8,146
M 135
H 1
AR 9,170
M 123
AD 8,6
AD 9,7
M 122
TY 66,512
TY 70,14336
M 124
H 2
AW 146,8
M 125
AW 170,9
UL 6,43
M 137
UH 7,43
AR 8,194
M 136
TX 74,1536
TX 78,15360
M 138
WL 4
Q 0
M 139
Q 2
M 127
Q 4
M 126
TY 82,2048
TY 86,15872
M 128
Q 6
M 129
WL 4
Q 8
M 141
Q 10
M 140
TX 90,3072
TX 94,16896
M 142
Q 12
M 143
R 54
M 131
Q 14
M 130
TY 98,3584
TY 102,17408
M 0
R 52
M 1
WL 10
Q 16
M 13
Q 18
M 12
Q 20
M 14
Q 22
M 15
WL 8
Q 24
M 3
Q 26
M 2
Q 28
M 4
Q 30
M 5
WL 6
Q 32
M 17
Q 34
M 16
Q 36
M 18
Q 38
M 19
WL 2
Q 40
M 7
Q 42
M 6
Q 44
M 8
Q 46
M 9
SA 244,1792
AD 8,6
M 21
AD 150,7
AW 194,8
M 20
UL 6,44
UH 7,44
M 22
AR 8,147
AR 9,171
M 23
R 58
M 11
AD 8,6
AD 9,7
M 10
WL 2
R 56
M 34
AW 147,8
AW 171,9
M 35
UL 6,45
UH 7,45
M 32
AR 8,195
AD 8,6
M 33
AD 151,7
AW 195,8
M 30
M 31
M 28
M 29
M 26
M 27
WV 6
M 24
WL 0
FP
M 25
M 36
M 37
M 38
M 39
M 40
M 41
M 42
ae 44,68,864
M 43
s_bfe_u32 s51, s50, 0x10011
v_mad_i32_i24 v47, v49, s51, v48
XL 22,128
M 44
M 45
FR
M 46
M 47
M 58
XL 24,896
M 59
M 56
M 57
M 54
M 55
WL 0
FQ 236
M 52
XL 26,1664
M 53
M 50
FZ 236
R 72
M 51
mw 24
R 74
M 48
TY 106,5128
TY 110,18952
M 49
R 76
M 60
ad 84,1584
W 0
M 61
XL 28,2432
M 62
M 63
W 1
M 64
M 65
M 66
W 2
M 67
WL 3
AN 104,106
AN 105,107
M 68
WL 2
SH 106
AN 106,106
M 69
mw 27
AN 107,107
AN 108,110
M 70
AN 109,111
SH 110
M 71
R 78
M 82
AN 110,110
AN 111,111
M 83
TY 114,6664
TY 118,20488
M 80
ad 84,1590
W 0
M 81
M 78
M 79
W 1
M 76
M 77
M 74
W 2
M 75
SA 246,2560
M 72
WL 1
AN 112,114
AN 113,115
M 73
R 80
M 84
mw 48
SH 114
AN 114,114
M 85
WL 2
AN 115,115
AN 116,118
M 86
AN 117,119
SH 118
M 87
AN 118,118
AN 119,119
M 88
TY 122,8200
TY 126,22024
M 89
ad 84,1632
W 0
M 90
M 91
M 92
W 1
M 93
M 94
M 95
R 82
M 106
W 2
UL 6,14
M 107
WL 3
AN 120,122
AN 121,123
M 104
WL 2
SH 122
AN 122,122
M 105
mw 51
AN 123,123
AN 124,126
M 102
AN 125,127
SH 126
M 103
AN 126,126
AN 127,127
M 100
ad 84,1638
W 0
M 101
UH 7,14
AR 8,148
M 98
AR 9,172
AD 8,6
M 99
W 1
AD 9,7
M 96
AW 148,8
AW 172,9
M 97
R 0
M 108
W 2
UL 6,15
M 109
UH 7,15
AR 8,196
M 110
AD 8,6
AD 152,7
M 111
bc 49,18,66,60
AW 196,8
M 112
mh 0
UL 6,16
UH 7,16
M 113
ad 66,3520
H 0
M 114
AR 8,149
AR 9,173
M 115
AD 8,6
AD 9,7
M 116
DG 1024
M 117
AW 149,8
AW 173,9
M 118
UL 6,17
UH 7,17
M 119
WL 0
R 2
M 120
bc 48,18,64,56
AR 8,197
M 121
mh 3
AD 8,6
AD 153,7
M 133
ad 64,3520
H 0
)"
R"(M 132
TX 58,4608
TX 62,18432
M 134
AW 197,8
UL 6,18
M 135
H 1
UH 7,18
M 123
AR 8,150
AR 9,174
M 122
TY 66,5120
TY 70,18944
M 124
H 2
AD 8,6
M 125
AD 9,7
AW 150,8
M 137
AW 174,9
UL 6,19
M 136
TX 74,6144
TX 78,19968
M 138
WL 4
Q 0
M 139
Q 2
M 127
Q 4
M 126
TY 82,6656
TY 86,20480
M 128
Q 6
M 129
WL 4
Q 8
M 141
Q 10
M 140
TX 90,7680
TX 94,21504
M 142
Q 12
M 143
R 6
M 131
Q 14
M 130
TY 98,8192
TY 102,22016
M 0
R 4
M 1
WL 10
Q 16
M 13
Q 18
M 12
Q 20
M 14
Q 22
M 15
WL 8
Q 24
M 3
Q 26
M 2
Q 28
M 4
Q 30
M 5
WL 6
Q 32
M 17
Q 34
M 16
Q 36
M 18
Q 38
M 19
WL 2
Q 40
M 7
Q 42
M 6
Q 44
M 8
Q 46
M 9
ae 42,72,480
M 21
s_bfe_u32 s52, s50, 0x1000b
v_mad_i32_i24 v50, v49, s52, v48
SA 248,256
M 20
UH 7,19
AR 8,198
M 22
AD 8,6
AD 154,7
M 23
R 10
M 11
AW 198,8
UL 6,20
M 10
WL 2
R 8
M 34
UH 7,20
AR 8,151
M 35
AR 9,175
AD 8,6
M 32
AD 9,7
AW 151,8
M 33
AW 175,9
UL 6,21
M 30
UH 7,21
AR 8,199
M 31
AD 8,6
AD 155,7
M 28
AW 199,8
M 29
M 26
M 27
WV 6
M 24
WL 0
FP
M 25
M 36
M 37
M 38
M 39
M 40
M 41
M 42
M 43
M 44
M 45
FR
M 46
M 47
M 58
M 59
M 56
M 57
M 54
M 55
WL 0
FQ 240
M 52
M 53
M 50
FZ 240
R 24
M 51
mw 72
R 26
M 48
TY 106,9736
TY 110,23560
M 49
R 28
M 60
ad 84,1680
W 0
M 61
M 62
M 63
W 1
M 64
M 65
M 66
W 2
M 67
WL 3
AN 104,106
AN 105,107
M 68
WL 2
SH 106
AN 106,106
M 69
mw 75
AN 107,107
AN 108,110
M 70
AN 109,111
SH 110
M 71
R 30
M 82
AN 110,110
AN 111,111
M 83
TY 114,11272
TY 118,25096
M 80
ad 84,1686
W 0
M 81
ae 44,70,768
M 78
s_bitcmp1_b32 s50, 12
s_cselect_b32 s51, 24576, 0
v_add_u32 v47, s51, v3
M 79
W 1
XL 30,256
M 76
XL 32,1024
M 77
M 74
W 2
M 75
M 72
WL 1
AN 112,114
AN 113,115
M 73
R 32
M 84
mw 0
SH 114
AN 114,114
M 85
WL 2
AN 115,115
AN 116,118
M 86
AN 117,119
SH 118
M 87
AN 118,118
AN 119,119
M 88
TY 122,12808
TY 126,26632
M 89
ad 84,1728
W 0
M 90
SA 250,1024
XL 34,1792
M 91
XL 36,2560
M 92
W 1
M 93
M 94
M 95
R 34
M 106
W 2
M 107
WL 3
AN 120,122
AN 121,123
M 104
WL 2
SH 122
AN 122,122
M 105
mw 3
AN 123,123
AN 124,126
M 102
AN 125,127
SH 126
M 103
AN 126,126
AN 127,127
M 100
ad 84,1734
W 0
M 101
UL 6,22
UH 7,22
M 98
AR 8,152
AR 9,176
M 99
W 1
AD 8,6
M 96
AD 9,7
AW 152,8
M 97
R 48
M 108
W 2
AW 176,9
M 109
UL 6,23
UH 7,23
M 110
AD 132,6
AD 156,7
M 111
bc 49,19,66,60
UL 6,24
M 112
mh 9
UH 7,24
AR 8,153
M 113
ad 66,3712
H 0
M 114
AR 9,177
AD 8,6
M 115
AD 9,7
AW 153,8
M 116
DG 1024
M 117
AW 177,9
UL 6,25
M 118
UH 7,25
AD 133,6
M 119
WL 0
R 50
M 120
bc 48,19,64,56
AD 157,7
M 121
mh 12
UL 6,26
UH 7,26
M 133
ad 64,3712
H 0
M 132
TX 58,9216
TX 62,23040
M 134
AR 8,154
AR 9,178
M 135
H 1
AD 8,6
M 123
AD 9,7
AW 154,8
M 122
TY 66,9728
TY 70,23552
M 124
H 2
AW 178,9
M 125
UL 6,27
UH 7,27
M 137
AD 134,6
AD 158,7
M 136
TX 74,10752
TX 78,24576
PA 10240
DF
M 138
WL 4
Q 0
M 139
Q 2
M 127
Q 4
M 126
TY 82,11264
TY 86,25088
M 128
Q 6
M 129
WL 4
Q 8
M 141
Q 10
M 140
TX 90,12288
TX 94,26112
M 142
Q 12
M 143
)"
R"(R 54
M 131
Q 14
M 130
TY 98,12800
TY 102,26624
M 0
R 52
M 1
WL 10
Q 16
M 13
Q 18
M 12
Q 20
M 14
Q 22
M 15
WL 8
Q 24
M 3
Q 26
M 2
Q 28
M 4
Q 30
M 5
WL 6
Q 32
M 17
Q 34
M 16
Q 36
M 18
Q 38
M 19
WL 2
Q 40
M 7
Q 42
M 6
Q 44
M 8
Q 46
M 9
SA 252,1792
UL 6,28
M 21
UH 7,28
AR 8,155
M 20
AR 9,179
AD 8,6
M 22
AD 9,7
AW 155,8
M 23
R 58
M 11
AW 179,9
UL 6,29
M 10
WL 2
R 56
M 34
UH 7,29
AD 135,6
M 35
AD 159,7
M 32
M 33
M 30
M 31
M 28
M 29
M 26
M 27
WV 7
M 24
WL 0
FP
M 25
M 36
M 37
M 38
M 39
M 40
M 41
M 42
M 43
M 44
M 45
FR
M 46
M 47
M 58
M 59
M 56
M 57
M 54
M 55
WL 0
FQ 244
M 52
M 53
M 50
FZ 244
R 72
M 51
mw 24
R 74
M 48
TY 106,520
TY 110,14344
M 49
R 76
M 60
ad 84,1776
W 0
M 61
M 62
M 63
W 1
M 64
M 65
M 66
W 2
M 67
WL 3
AN 104,106
AN 105,107
M 68
WL 2
SH 106
AN 106,106
M 69
mw 27
AN 107,107
AN 108,110
M 70
AN 109,111
SH 110
M 71
R 78
M 82
AN 110,110
AN 111,111
M 83
TY 114,2056
TY 118,15880
M 80
ad 84,1782
W 0
M 81
ae 44,70,768
M 78
s_bfe_u32 s51, s50, 0x1000d
v_mad_i32_i24 v47, v49, s51, v48
XL 38,256
M 79
W 1
M 76
M 77
M 74
W 2
XL 40,1024
M 75
SA 254,2560
M 72
WL 1
AN 112,114
AN 113,115
M 73
R 80
M 84
mw 48
SH 114
AN 114,114
M 85
WL 2
AN 115,115
AN 116,118
M 86
AN 117,119
SH 118
M 87
AN 118,118
AN 119,119
M 88
TY 122,3592
TY 126,17416
M 89
ad 84,1824
W 0
M 90
XL 42,1792
M 91
M 92
W 1
XL 44,2560
M 93
M 94
M 95
R 82
M 106
W 2
M 107
WL 3
AN 120,122
AN 121,123
M 104
WL 2
SH 122
AN 122,122
M 105
mw 51
AN 123,123
AN 124,126
M 102
AN 125,127
SH 126
M 103
AN 126,126
AN 127,127
M 100
ad 84,1830
W 0
M 101
M 98
M 99
W 1
M 96
M 97
R 0
M 108
W 2
M 109
M 110
M 111
bc 49,20,66,60
M 112
mh 18
M 113
ad 66,3904
H 0
M 114
M 115
M 116
DG 1024
M 117
M 118
M 119
WL 2
R 2
M 120
bc 48,20,64,56
M 121
mh 21
M 133
ad 64,3904
H 0
M 132
TX 58,0
TX 62,13824
M 134
M 135
H 1
M 123
UL 6,30
UH 7,30
M 122
TY 66,512
TY 70,14336
M 124
H 2
AD 160,6
M 125
AD 184,7
UL 6,31
M 137
UH 7,31
AD 208,6
M 136
TX 74,1536
TX 78,15360
M 138
WL 4
Q 0
M 139
Q 2
M 127
Q 4
M 126
TY 82,2048
TY 86,15872
M 128
Q 6
M 129
WL 4
Q 8
M 141
Q 10
M 140
TX 90,3072
TX 94,16896
M 142
Q 12
M 143
R 6
M 131
Q 14
M 130
TY 98,3584
TY 102,17408
M 0
R 4
M 1
WL 10
Q 16
M 13
Q 18
M 12
Q 20
M 14
Q 22
M 15
WL 8
Q 24
M 3
Q 26
M 2
Q 28
M 4
Q 30
M 5
WL 6
Q 32
M 17
Q 34
M 16
Q 36
M 18
Q 38
M 19
WL 2
Q 40
M 7
Q 42
M 6
Q 44
M 8
Q 46
M 9
AD 232,7
UL 6,32
M 21
UH 7,32
AD 161,6
M 20
AD 185,7
UL 6,33
M 22
UH 7,33
AD 209,6
M 23
R 10
M 11
AD 233,7
UL 6,34
M 10
WL 2
R 8
M 34
UH 7,34
AD 162,6
M 35
AD 186,7
UL 6,35
M 32
UH 7,35
AD 210,6
M 33
AD 234,7
UL 6,36
M 30
UH 7,36
AD 163,6
M 31
AD 187,7
UL 6,37
M 28
UH 7,37
AD 211,6
M 29
AD 235,7
M 26
M 27
WV 5
M 24
WL 0
)"
R"(FP
M 25
M 36
M 37
M 38
M 39
M 40
M 41
M 42
M 43
M 44
M 45
FR
M 46
M 47
M 58
M 59
M 56
M 57
M 54
M 55
WL 0
FQ 248
M 52
M 53
M 50
FZ 248
R 24
M 51
mw 72
R 26
M 48
TY 106,5128
TY 110,18952
M 49
R 28
M 60
ad 84,1872
W 0
M 61
M 62
M 63
W 1
M 64
M 65
M 66
W 2
M 67
WL 3
AN 104,106
AN 105,107
M 68
WL 2
SH 106
AN 106,106
M 69
mw 75
AN 107,107
AN 108,110
M 70
AN 109,111
SH 110
M 71
R 30
M 82
AN 110,110
AN 111,111
M 83
TY 114,6664
TY 118,20488
M 80
ad 84,1878
W 0
M 81
M 78
M 79
W 1
M 76
ae 44,68,768
M 77
s_bitcmp1_b32 s50, 14
s_cselect_b32 s51, 24576, 0
v_add_u32 v47, s51, v3
M 74
W 2
XL 14,256
M 75
M 72
WL 1
AN 112,114
AN 113,115
M 73
R 32
M 84
mw 0
SH 114
AN 114,114
M 85
WL 2
AN 115,115
AN 116,118
M 86
AN 117,119
SH 118
M 87
AN 118,118
AN 119,119
M 88
TY 122,8200
TY 126,22024
M 89
ad 84,1920
W 0
M 90
XL 16,1024
M 91
XL 18,1792
M 92
W 1
M 93
M 94
M 95
R 34
M 106
W 2
XL 20,2560
M 107
WL 3
AN 120,122
AN 121,123
M 104
WL 2
SH 122
AN 122,122
M 105
mw 3
AN 123,123
AN 124,126
M 102
AN 125,127
SH 126
M 103
AN 126,126
AN 127,127
M 100
ad 84,1926
W 0
M 101
M 98
M 99
W 1
M 96
M 97
R 48
M 108
W 2
M 109
M 110
M 111
bc 49,21,66,60
M 112
mh 0
M 113
ad 66,4096
H 0
M 114
M 115
M 116
DG 1024
M 117
M 118
M 119
WL 2
R 50
M 120
bc 48,21,64,56
M 121
mh 3
M 133
ad 64,4096
H 0
M 132
TX 58,4608
TX 62,18432
M 134
M 135
H 1
M 123
M 122
TY 66,5120
TY 70,18944
M 124
H 2
M 125
M 137
UL 6,38
UH 7,38
M 136
TX 74,6144
TX 78,19968
M 138
WL 4
Q 0
M 139
Q 2
M 127
Q 4
M 126
TY 82,6656
TY 86,20480
M 128
Q 6
M 129
WL 4
Q 8
M 141
Q 10
M 140
TX 90,7680
TX 94,21504
M 142
Q 12
M 143
R 54
M 131
Q 14
M 130
TY 98,8192
TY 102,22016
M 0
R 52
M 1
WL 10
Q 16
M 13
Q 18
M 12
Q 20
M 14
Q 22
M 15
WL 8
Q 24
M 3
Q 26
M 2
Q 28
M 4
Q 30
M 5
WL 6
Q 32
M 17
Q 34
M 16
Q 36
M 18
Q 38
M 19
WL 2
Q 40
M 7
Q 42
M 6
Q 44
M 8
Q 46
M 9
AD 164,6
AD 188,7
M 21
UL 6,39
UH 7,39
M 20
AD 212,6
AD 236,7
M 22
UL 6,40
UH 7,40
M 23
R 58
M 11
AD 165,6
AD 189,7
M 10
WL 2
R 56
M 34
UL 6,41
UH 7,41
M 35
AD 213,6
AD 237,7
M 32
UL 6,42
UH 7,42
M 33
AD 166,6
AD 190,7
M 30
UL 6,43
UH 7,43
M 31
AD 214,6
AD 238,7
M 28
UL 6,44
UH 7,44
M 29
AD 167,6
AD 191,7
M 26
UL 6,45
UH 7,45
M 27
WV 5
AD 215,6
AD 239,7
M 24
WL 0
FP
M 25
M 36
M 37
M 38
M 39
M 40
M 41
M 42
M 43
M 44
M 45
FR
M 46
M 47
M 58
M 59
M 56
M 57
M 54
M 55
WL 0
FQ 252
M 52
M 53
M 50
FZ 252
R 72
M 51
mw 24
R 74
M 48
TY 106,9736
TY 110,23560
M 49
R 76
M 60
ad 84,1968
W 0
M 61
M 62
M 63
W 1
M 64
M 65
M 66
W 2
M 67
WL 3
AN 104,106
AN 105,107
M 68
WL 2
SH 106
AN 106,106
M 69
mw 27
AN 107,107
AN 108,110
M 70
AN 109,111
SH 110
M 71
R 78
M 82
AN 110,110
AN 111,111
M 83
TY 114,11272
TY 118,25096
M 80
ad 84,1974
W 0
M 81
M 78
M 79
W 1
M 76
M 77
M 74
)"
R"(W 2
M 75
M 72
WL 1
AN 112,114
AN 113,115
M 73
R 80
M 84
mw 48
SH 114
AN 114,114
M 85
WL 2
AN 115,115
AN 116,118
M 86
AN 117,119
SH 118
M 87
AN 118,118
AN 119,119
M 88
TY 122,12808
TY 126,26632
M 89
ad 84,2016
W 0
M 90
ae 44,68,768
M 91
s_bfe_u32 s51, s50, 0x1000f
v_mad_i32_i24 v47, v49, s51, v48
XL 22,256
M 92
W 1
XL 24,1024
M 93
M 94
XL 26,1792
M 95
R 82
M 106
W 2
M 107
WL 3
AN 120,122
AN 121,123
M 104
WL 2
SH 122
AN 122,122
M 105
mw 51
AN 123,123
AN 124,126
M 102
AN 125,127
SH 126
M 103
AN 126,126
AN 127,127
M 100
ad 84,2022
W 0
M 101
XL 28,2560
M 98
M 99
W 1
M 96
M 97
R 0
M 108
W 2
M 109
M 110
M 111
bc 49,22,66,60
M 112
mh 9
M 113
ad 66,4288
H 0
M 114
M 115
M 116
DG 1024
M 117
M 118
M 119
WL 2
R 2
M 120
bc 48,22,64,56
M 121
mh 12
M 133
ad 64,4288
H 0
M 132
TX 58,9216
TX 62,23040
M 134
M 135
H 1
M 123
M 122
TY 66,9728
TY 70,23552
M 124
H 2
M 125
M 137
M 136
TX 74,10752
TX 78,24576
M 138
WL 4
Q 0
M 139
Q 2
M 127
Q 4
M 126
TY 82,11264
TY 86,25088
M 128
Q 6
M 129
WL 4
Q 8
M 141
Q 10
M 140
TX 90,12288
TX 94,26112
M 142
Q 12
M 143
R 6
M 131
Q 14
M 130
TY 98,12800
TY 102,26624
M 0
R 4
M 1
WL 10
Q 16
M 13
Q 18
M 12
Q 20
M 14
Q 22
M 15
WL 8
Q 24
M 3
Q 26
M 2
Q 28
M 4
Q 30
M 5
WL 6
Q 32
M 17
Q 34
M 16
Q 36
M 18
Q 38
M 19
WL 2
Q 40
M 7
Q 42
M 6
Q 44
M 8
Q 46
M 9
UL 6,14
UH 7,14
M 21
AD 168,6
AD 192,7
M 20
UL 6,15
UH 7,15
M 22
AD 216,6
AD 240,7
M 23
R 10
M 11
UL 6,16
UH 7,16
M 10
WL 2
R 8
M 34
AD 169,6
AD 193,7
M 35
UL 6,17
UH 7,17
M 32
AD 217,6
AD 241,7
M 33
UL 6,18
UH 7,18
M 30
AD 170,6
AD 194,7
M 31
UL 6,19
UH 7,19
M 28
AD 218,6
AD 242,7
M 29
UL 6,20
UH 7,20
M 26
AD 171,6
AD 195,7
M 27
WV 5
UL 6,21
UH 7,21
M 24
WL 0
FP
AD 219,6
M 25
AD 243,7
M 36
M 37
M 38
M 39
M 40
M 41
M 42
M 43
M 44
M 45
FR
M 46
M 47
M 58
M 59
M 56
M 57
M 54
M 55
WL 0
FQ 256
M 52
M 53
M 50
FZ 256
R 24
M 51
mw 72
R 26
M 48
TY 106,520
TY 110,14344
M 49
R 28
M 60
ad 84,2064
W 0
M 61
M 62
M 63
W 1
M 64
M 65
M 66
W 2
M 67
WL 3
AN 104,106
AN 105,107
M 68
WL 2
SH 106
AN 106,106
M 69
mw 75
AN 107,107
AN 108,110
M 70
AN 109,111
SH 110
M 71
R 30
M 82
AN 110,110
AN 111,111
M 83
TY 114,2056
TY 118,15880
M 80
ad 84,2070
W 0
M 81
M 78
M 79
W 1
M 76
M 77
M 74
W 2
M 75
M 72
WL 1
AN 112,114
AN 113,115
M 73
R 32
M 84
mw 0
SH 114
AN 114,114
M 85
WL 2
AN 115,115
AN 116,118
M 86
AN 117,119
SH 118
M 87
AN 118,118
AN 119,119
M 88
TY 122,3592
TY 126,17416
M 89
ad 84,2112
W 0
M 90
ae 44,68,864
M 91
s_bitcmp1_b32 s50, 16
s_cselect_b32 s51, 24576, 0
v_add_u32 v47, s51, v3
M 92
W 1
XL 30,256
M 93
XL 32,1024
M 94
M 95
R 34
M 106
W 2
M 107
WL 3
AN 120,122
AN 121,123
M 104
WL 2
SH 122
AN 122,122
M 105
mw 3
AN 123,123
AN 124,126
M 102
AN 125,127
SH 126
M 103
AN 126,126
AN 127,127
M 100
ad 84,2118
W 0
M 101
XL 34,1792
M 98
XL 36,2560
M 99
W 1
M 96
M 97
R 48
M 108
W 2
M 109
M 110
M 111
bc 49,23,66,60
M 112
)"
R"(mh 18
M 113
ad 66,4480
H 0
M 114
M 115
M 116
DG 1024
M 117
M 118
M 119
WL 2
R 50
M 120
bc 48,23,64,56
M 121
mh 21
M 133
ad 64,4480
H 0
M 132
TX 58,0
TX 62,13824
M 134
M 135
H 1
M 123
M 122
TY 66,512
TY 70,14336
M 124
H 2
M 125
M 137
M 136
TX 74,1536
TX 78,15360
M 138
WL 4
Q 0
M 139
Q 2
M 127
Q 4
M 126
TY 82,2048
TY 86,15872
M 128
Q 6
M 129
WL 4
Q 8
M 141
Q 10
M 140
TX 90,3072
TX 94,16896
M 142
Q 12
M 143
R 54
M 131
Q 14
M 130
TY 98,3584
TY 102,17408
M 0
R 52
M 1
WL 10
Q 16
M 13
Q 18
M 12
Q 20
M 14
Q 22
M 15
WL 8
Q 24
M 3
Q 26
M 2
Q 28
M 4
Q 30
M 5
WL 6
Q 32
M 17
Q 34
M 16
Q 36
M 18
Q 38
M 19
WL 2
Q 40
M 7
Q 42
M 6
Q 44
M 8
Q 46
M 9
UL 6,22
UH 7,22
M 21
AD 172,6
AD 196,7
M 20
UL 6,23
UH 7,23
M 22
AD 220,6
AD 244,7
M 23
R 58
M 11
UL 6,24
UH 7,24
M 10
WL 2
R 56
M 34
AD 173,6
AD 197,7
M 35
UL 6,25
UH 7,25
M 32
AD 221,6
AD 245,7
M 33
UL 6,26
UH 7,26
M 30
AD 174,6
AD 198,7
M 31
UL 6,27
UH 7,27
M 28
AD 222,6
AD 246,7
M 29
UL 6,28
UH 7,28
M 26
AD 175,6
AD 199,7
M 27
WV 5
UL 6,29
UH 7,29
M 24
WL 0
FP
AD 223,6
M 25
AD 247,7
M 36
M 37
M 38
M 39
M 40
M 41
M 42
M 43
M 44
M 45
FR
M 46
M 47
M 58
M 59
M 56
M 57
M 54
M 55
WL 0
FQ 260
M 52
M 53
M 50
FZ 260
R 72
M 51
mw 24
R 74
M 48
TY 106,5128
TY 110,18952
M 49
R 76
M 60
ad 84,2160
W 0
M 61
M 62
M 63
W 1
M 64
M 65
M 66
W 2
M 67
WL 3
AN 104,106
AN 105,107
M 68
WL 2
SH 106
AN 106,106
M 69
mw 27
AN 107,107
AN 108,110
M 70
AN 109,111
SH 110
M 71
R 78
M 82
AN 110,110
AN 111,111
M 83
TY 114,6664
TY 118,20488
M 80
ad 84,2166
W 0
M 81
M 78
M 79
W 1
M 76
M 77
M 74
W 2
M 75
M 72
WL 1
AN 112,114
AN 113,115
M 73
R 80
M 84
mw 48
SH 114
AN 114,114
M 85
WL 2
AN 115,115
AN 116,118
M 86
AN 117,119
SH 118
M 87
AN 118,118
AN 119,119
M 88
TY 122,8200
TY 126,22024
M 89
ad 84,2208
W 0
M 90
ae 44,68,864
M 91
s_bfe_u32 s51, s50, 0x10011
v_mad_i32_i24 v47, v49, s51, v48
XL 38,256
M 92
W 1
M 93
M 94
M 95
R 82
M 106
W 2
XL 40,1024
M 107
WL 3
AN 120,122
AN 121,123
M 104
WL 2
SH 122
AN 122,122
M 105
mw 51
AN 123,123
AN 124,126
M 102
AN 125,127
SH 126
M 103
AN 126,126
AN 127,127
M 100
ad 84,2214
W 0
M 101
XL 42,1792
M 98
M 99
W 1
M 96
M 97
R 0
M 108
W 2
XL 44,2560
M 109
M 110
M 111
bc 48,0,64,56
M 112
mh 3
M 113
ad 64,64
H 0
M 114
M 115
M 116
H 1
M 117
M 118
M 119
WL 2
R 2
M 120
H 2
M 121
M 133
M 132
TX 58,4608
TX 62,18432
M 134
M 135
M 123
M 122
TY 66,5120
TY 70,18944
M 124
M 125
M 137
M 136
TX 74,6144
TX 78,19968
M 138
WL 4
Q 0
M 139
Q 2
M 127
Q 4
M 126
TY 82,6656
TY 86,20480
M 128
Q 6
M 129
WL 4
Q 8
M 141
Q 10
M 140
TX 90,7680
TX 94,21504
M 142
Q 12
M 143
R 6
M 131
Q 14
M 130
TY 98,8192
TY 102,22016
M 0
R 4
M 1
WL 10
Q 16
M 13
Q 18
M 12
Q 20
M 14
Q 22
M 15
WL 8
Q 24
M 3
Q 26
M 2
Q 28
M 4
Q 30
)"
R"(M 5
WL 6
Q 32
M 17
Q 34
M 16
Q 36
M 18
Q 38
M 19
WL 2
Q 40
M 7
Q 42
M 6
Q 44
M 8
Q 46
M 9
UL 6,30
UH 7,30
M 21
AD 176,6
AD 200,7
M 20
UL 6,31
UH 7,31
M 22
AD 224,6
AD 248,7
M 23
R 10
M 11
UL 6,32
UH 7,32
M 10
WL 2
R 8
M 34
AD 177,6
AD 201,7
M 35
UL 6,33
UH 7,33
M 32
AD 225,6
AD 249,7
M 33
UL 6,34
UH 7,34
M 30
AD 178,6
AD 202,7
M 31
UL 6,35
UH 7,35
M 28
AD 226,6
AD 250,7
M 29
UL 6,36
UH 7,36
M 26
AD 179,6
AD 203,7
M 27
WV 4
UL 6,37
UH 7,37
M 24
WL 0
FP
AD 227,6
M 25
AD 251,7
M 36
M 37
M 38
M 39
M 40
M 41
M 42
M 43
M 44
M 45
FR
M 46
M 47
M 58
M 59
M 56
M 57
M 54
M 55
WL 0
FQ 264
M 52
M 53
M 50
FZ 264
R 24
M 51
mw 72
R 26
M 48
TY 106,9736
TY 110,23560
M 49
R 28
M 60
ad 84,2256
W 0
M 61
M 62
M 63
W 1
M 64
M 65
M 66
W 2
M 67
WL 3
AN 104,106
AN 105,107
M 68
WL 2
SH 106
AN 106,106
M 69
mw 75
AN 107,107
AN 108,110
M 70
AN 109,111
SH 110
M 71
R 30
M 82
AN 110,110
AN 111,111
M 83
TY 114,11272
TY 118,25096
M 80
ad 84,2262
W 0
M 81
M 78
M 79
W 1
M 76
M 77
M 74
W 2
M 75
M 72
WL 1
AN 112,114
AN 113,115
M 73
R 32
M 84
mw 0
SH 114
AN 114,114
M 85
WL 2
AN 115,115
AN 116,118
M 86
AN 117,119
SH 118
M 87
AN 118,118
AN 119,119
M 88
TY 122,12808
TY 126,26632
M 89
ad 84,0
W 0
M 90
M 91
M 92
W 1
M 93
M 94
M 95
R 34
M 106
W 2
M 107
WL 3
AN 120,122
AN 121,123
M 104
WL 2
SH 122
AN 122,122
M 105
mw 3
AN 123,123
AN 124,126
M 102
AN 125,127
SH 126
M 103
AN 126,126
AN 127,127
M 100
ad 84,6
W 0
M 101
M 98
M 99
W 1
M 96
M 97
R 48
M 108
W 2
M 109
M 110
M 111
bc 48,1,64,56
M 112
mh 15
M 113
ad 64,259
H 0
M 114
M 115
M 116
DG 1024
M 117
M 118
M 119
WL 2
R 50
M 120
M 121
M 133
M 132
TX 58,9216
TX 62,23040
M 134
M 135
M 123
M 122
TY 66,9728
TY 70,23552
M 124
M 125
M 137
M 136
TX 74,10752
TX 78,24576
M 138
WL 4
Q 0
M 139
Q 2
M 127
Q 4
M 126
TY 82,11264
TY 86,25088
M 128
Q 6
M 129
WL 4
Q 8
M 141
Q 10
M 140
TX 90,12288
TX 94,26112
M 142
Q 12
M 143
R 54
M 131
Q 14
M 130
TY 98,12800
TY 102,26624
M 0
R 52
M 1
WL 10
Q 16
M 13
Q 18
M 12
Q 20
M 14
Q 22
M 15
WL 8
Q 24
M 3
Q 26
M 2
Q 28
M 4
Q 30
M 5
WL 6
Q 32
M 17
Q 34
M 16
Q 36
M 18
Q 38
M 19
WL 2
Q 40
M 7
Q 42
M 6
Q 44
M 8
Q 46
M 9
UL 6,38
UH 7,38
M 21
AD 180,6
AD 204,7
M 20
UL 6,39
UH 7,39
M 22
AD 228,6
AD 252,7
M 23
R 58
M 11
UL 6,40
UH 7,40
M 10
WL 2
R 56
M 34
AD 181,6
AD 205,7
M 35
UL 6,41
UH 7,41
M 32
AD 229,6
AD 253,7
BV 52,4608
M 33
UL 6,42
UH 7,42
M 30
AD 182,6
AD 206,7
M 31
UL 6,43
UH 7,43
M 28
AD 230,6
AD 254,7
M 29
WV 17
UL 6,44
UH 7,44
M 26
AD 183,6
AD 207,7
M 27
WV 8
UL 6,45
UH 7,45
M 24
WL 1
FP
AD 231,6
M 25
AD 255,7
M 36
M 37
M 38
M 39
M 40
ds_read_b64 v[12:13], v4 offset:4864
ds_read_b64 v[46:47], v4 offset:4872
M 41
AR 6,36
AR 7,60
M 42
AR 8,84
AR 9,108
M 43
WL 3
EF 6,52
EF 7,53
)"
R"(M 44
EF 8,54
EF 9,55
M 45
FR
CV 14,6,7
M 46
CV 15,8,9
AR 6,37
M 47
AR 7,61
AR 8,85
M 58
AR 9,109
EF 6,52
M 59
EF 7,53
EF 8,54
M 56
EF 9,55
CV 16,6,7
M 57
CV 17,8,9
AR 6,38
M 54
AR 7,62
AR 8,86
M 55
WL 0
FQ 268
AR 9,110
M 52
EF 6,52
EF 7,53
M 53
EF 8,54
EF 9,55
M 50
FZ 268
R 72
M 51
mw 24
R 74
M 48
TY 114,2056
TY 118,15880
M 49
R 76
M 60
ad 84,48
W 0
M 61
CV 18,6,7
CV 19,8,9
M 62
AR 6,39
AR 7,63
M 63
W 1
AR 8,87
M 64
AR 9,111
EF 6,52
M 65
EF 7,53
EF 8,54
M 66
W 2
EF 9,55
M 67
WL 3
AN 112,114
AN 113,115
M 68
WL 2
SH 114
AN 114,114
M 69
mw 27
AN 115,115
AN 116,118
M 70
AN 117,119
SH 118
M 71
R 78
M 82
AN 118,118
AN 119,119
M 83
TY 122,3592
TY 126,17416
M 80
ad 84,54
W 0
M 81
CV 20,6,7
CV 21,8,9
M 78
ae 42,74,768
AR 6,40
M 79
W 1
AR 7,64
M 76
s_bitcmp1_b32 s50, 12
s_cselect_b32 s52, 24576, 0
v_add_u32 v50, s52, v3
M 77
ST 14,0
AR 8,88
M 74
W 2
AR 9,112
M 75
EF 6,52
EF 7,53
M 72
WL 1
AN 120,122
AN 121,123
M 73
R 80
M 84
mw 48
SH 122
AN 122,122
M 85
WL 2
AN 123,123
AN 124,126
M 86
AN 125,127
SH 126
M 87
AN 126,126
AN 127,127
M 88
ad 84,96
W 0
M 89
ST 16,768
EF 8,54
M 90
EF 9,55
CV 22,6,7
M 91
W 1
CV 23,8,9
M 92
AR 6,41
AR 7,65
M 93
ST 18,1536
AR 8,89
M 94
W 2
AR 9,113
M 95
R 82
M 106
EF 6,52
EF 7,53
M 107
mw 51
ST 20,2304
EF 8,54
M 104
ad 84,102
W 0
M 105
EF 9,55
CV 24,6,7
M 102
CV 25,8,9
AR 6,42
M 103
W 1
AR 7,66
M 100
AR 8,90
AR 9,114
M 101
EF 6,52
EF 7,53
M 98
W 2
EF 8,54
M 99
EF 9,55
CV 26,6,7
M 96
CV 27,8,9
AR 6,43
M 97
AR 7,67
AR 8,91
M 108
AR 9,115
EF 6,52
M 109
EF 7,53
EF 8,54
M 110
EF 9,55
CV 28,6,7
M 111
CV 29,8,9
M 112
ae 42,74,768
BV 52,5120
M 113
s_bfe_u32 s52, s50, 0x1000d
v_mad_i32_i24 v50, v49, s52, v48
ST 22,0
M 114
AR 6,132
AR 7,156
M 115
WV 19
AR 8,180
EF 6,12
M 116
WL 1
EF 7,13
EF 8,46
M 117
s_barrier
R 0
M 118
mw 72
ST 24,768
EF 136,47
M 119
R 2
M 120
ad 84,144
W 0
M 121
CV 30,6,7
CV 31,8,136
M 133
ST 26,1536
AR 6,133
M 132
AR 7,157
AR 8,181
M 134
W 1
EF 6,12
M 135
EF 7,13
EF 8,46
M 123
ST 28,2304
EF 137,47
M 122
CV 32,6,7
CV 33,8,137
M 124
W 2
AR 6,134
M 125
AR 7,158
AR 8,182
M 137
EF 6,12
EF 7,13
M 136
TX 74,1536
TX 78,15360
M 138
mw 75
EF 8,46
EF 138,47
M 139
ad 84,150
W 0
M 127
CV 34,6,7
CV 35,8,138
M 126
TY 82,2048
TY 86,15872
M 128
AR 6,135
AR 7,159
M 129
W 1
AR 8,183
M 141
EF 6,12
EF 7,13
M 140
WL 2
TX 90,3072
TX 94,16896
P 16
M 142
Q 17
P 19
M 143
R 6
P 20
M 131
Q 21
P 23
M 130
WL 4
TY 98,3584
TY 102,17408
P 24
M 4
R 4
P 25
M 5
Q 26
P 28
M 17
Q 29
P 31
M 16
WL 6
Q 32
M 18
Q 34
M 19
Q 36
M 7
Q 38
M 6
WL 2
Q 40
M 8
Q 42
M 9
Q 44
M 21
Q 46
M 20
W 2
EF 8,46
M 22
EF 139,47
CV 36,6,7
M 23
R 10
M 11
CV 37,8,139
M 10
WL 2
R 8
M 34
ae 42,74,768
AR 6,136
M 35
s_bitcmp1_b32 s50, 12
s_cselect_b32 s52, 24576, 0
v_add_u32 v50, s52, v3
M 32
ST 30,128
AR 7,160
M 33
AR 8,184
EF 6,12
M 30
EF 7,13
EF 8,46
M 31
EF 140,47
CV 38,6,7
M 28
ST 32,896
CV 39,8,140
M 29
AR 6,137
AR 7,161
M 40
AR 8,185
EF 6,12
M 41
EF 7,13
EF 8,46
M 42
ST 34,1664
EF 141,47
M 43
CV 40,6,7
CV 41,8,141
M 44
WV 22
AR 6,138
AR 7,162
M 45
WL 0
AR 8,186
EF 6,12
M 46
s_barrier
R 24
)"
R"(M 47
R 26
M 58
TY 122,8200
TY 126,22024
M 59
ST 36,2432
EF 7,13
M 56
EF 8,46
EF 142,47
M 57
CV 42,6,7
CV 43,8,142
M 54
AR 6,139
AR 7,163
M 55
AR 8,187
EF 6,12
M 52
EF 7,13
EF 8,46
M 53
R 28
M 64
EF 143,47
CV 44,6,7
M 65
WL 3
AN 120,122
AN 121,123
M 66
SH 122
AN 122,122
M 67
WL 2
AN 123,123
AN 124,126
M 68
AN 125,127
SH 126
M 69
AN 126,126
AN 127,127
M 70
CV 45,8,143
M 71
R 30
ds_read_b64 v[12:13], v4 offset:4608
M 82
ae 42,74,768
ds_read_b64 v[46:47], v4 offset:4616
M 83
s_bfe_u32 s52, s50, 0x1000d
v_mad_i32_i24 v50, v49, s52, v48
ST 38,128
M 80
EF 160,52
EF 184,53
M 81
EF 208,54
EF 232,55
M 78
CV 14,160,184
CV 15,208,232
M 79
ST 40,896
EF 161,52
M 76
EF 185,53
EF 209,54
M 77
R 32
M 88
TX 58,7680
TX 62,21504
M 89
ST 42,1664
EF 233,55
M 90
CV 16,161,185
CV 17,209,233
M 91
EF 162,52
EF 186,53
M 92
TY 66,8192
TY 70,22016
M 93
ST 44,2432
EF 210,54
M 94
EF 234,55
CV 18,162,186
M 95
WL 6
R 34
M 106
CV 19,210,234
EF 163,52
M 107
WL 4
PA_ 56,120,58
PA_ 57,121,59
M 104
PB_ 58,120,58
PB_ 59,121,59
M 105
PA_ 60,124,62
PA_ 61,125,63
M 102
PB_ 62,124,62
PB_ 63,125,63
M 103
WL 2
PA_ 64,122,66
PA_ 65,123,67
M 100
PB_ 66,122,66
PB_ 67,123,67
M 101
PA_ 68,126,70
PA_ 69,127,71
M 112
PB_ 70,126,70
PB_ 71,127,71
M 113
EF 187,53
EF 211,54
M 114
EF 235,55
CV 20,163,187
M 115
CV 21,211,235
M 116
WV 18
EF 164,52
EF 188,53
M 117
WL 0
EF 212,54
EF 236,55
M 118
s_barrier
R 48
M 119
R 50
M 124
ae 42,74,768
CV 22,164,188
M 125
s_bitcmp1_b32 s50, 12
s_cselect_b32 s52, 24576, 0
v_add_u32 v50, s52, v3
M 137
ST 14,256
CV 23,212,236
M 136
EF 165,52
EF 189,53
M 138
EF 213,54
EF 237,55
M 139
CV 24,165,189
CV 25,213,237
M 127
ST 16,1024
EF 166,52
M 126
EF 190,53
EF 214,54
M 128
EF 238,55
CV 26,166,190
M 129
CV 27,214,238
EF 167,52
M 141
ST 18,1792
EF 191,53
M 140
EF 215,54
EF 239,55
M 142
CV 28,167,191
CV 29,215,239
M 143
R 54
BV 52,4864
M 131
ST 20,2560
AR 6,44
M 130
WL 3
R 52
MA 52,0,56
AR 7,68
AR 8,92
MA 52,4,60
AR 9,116
EF 6,12
MA 76,12,60
ae 42,74,768
EF 7,13
MA 76,8,56
s_bfe_u32 s52, s50, 0x1000d
v_mad_i32_i24 v50, v49, s52, v48
ST 22,256
MA 80,8,64
EF 8,46
EF 9,47
MA 80,12,68
R 58
MA 56,4,68
CV 30,6,7
CV 31,8,9
MA 56,0,64
WL 2
R 56
MA 104,16,64
ST 24,1024
AR 6,45
MA 104,20,68
AR 7,69
AR 8,93
MA 100,16,56
AR 9,117
EF 6,12
MA 100,20,60
EF 7,13
EF 8,46
MA 124,24,56
WV 14
ST 26,1792
EF 9,47
MA 124,28,60
WL 0
CV 32,6,7
CV 33,8,9
MA 128,24,64
s_barrier
R 72
MA 128,28,68
R 74
MA 152,0,64
ST 28,2560
AR 6,46
MA 152,4,68
AR 7,70
AR 8,94
MA 148,0,56
AR 9,118
EF 6,12
MA 148,4,60
R 76
MA 172,8,56
EF 7,13
EF 8,46
MA 172,12,60
EF 9,47
CV 34,6,7
MA 176,8,64
CV 35,8,9
AR 6,47
MA 176,12,68
WL 4
R 78
MV 132,16,64
AR 7,71
AR 8,95
MV 132,20,68
AR 9,119
EF 6,12
MA 196,16,56
EF 7,13
EF 8,46
MA 196,20,60
WL 4
R 80
MV 152,24,56
EF 9,47
CV 36,6,7
MV 152,28,60
CV 37,8,9
MV 156,24,64
ae 42,72,768
AR 6,48
AR 7,72
MV 156,28,68
WL 4
R 82
AR 8,96
MV 180,0,64
s_bitcmp1_b32 s50, 14
s_cselect_b32 s52, 24576, 0
v_add_u32 v50, s52, v3
AR 9,120
MV 180,4,68
ST 30,0
EF 6,12
EF 7,13
MV 176,0,56
EF 8,46
EF 9,47
CV 38,6,7
MV 176,4,60
WL 4
CV 39,8,9
AR 6,49
AR 7,73
MV 200,8,56
AR 8,97
AR 9,121
EF 6,12
MV 200,12,60
ST 32,768
EF 7,13
EF 8,46
MV 204,8,64
EF 9,47
CV 40,6,7
CV 41,8,9
MV 204,12,68
WL 0
AR 6,50
AR 7,74
AR 8,98
MV 224,16,56
AR 9,122
EF 6,12
EF 7,13
MV 224,20,60
ST 34,1536
EF 8,46
EF 9,47
MV 248,28,60
CV 42,6,7
CV 43,8,9
AR 6,51
MV 248,24,56
AR 7,75
AR 8,99
AR 9,123
MV 252,24,64
EF 6,12
EF 7,13
EF 8,46
MV 252,28,68
ST 36,2304
EF 9,47
CV 44,6,7
MV 228,20,68
WV 14
CV 45,8,9
MV 228,16,64
ae 42,72,768
s_bfe_u32 s52, s50, 0x1000f
v_mad_i32_i24 v50, v49, s52, v48
ST 38,0
ST 40,768
ST 42,1536
ST 44,2304
ds_read_b64 v[12:13], v4 offset:5120
ds_read_b64 v[46:47], v4 offset:5128
AR 6,140
AR 7,164
AR 8,188
EF 6,52
EF 7,53
EF 8,54
EF 144,55
CV 14,6,7
CV 15,8,144
AR 6,141
AR 7,165
AR 8,189
EF 6,52
EF 7,53
EF 8,54
EF 145,55
CV 16,6,7
CV 17,8,145
AR 6,142
AR 7,166
AR 8,190
EF 6,52
EF 7,53
EF 8,54
EF 146,55
CV 18,6,7
CV 19,8,146
AR 6,143
AR 7,167
AR 8,191
EF 6,52
EF 7,53
EF 8,54
EF 147,55
)"
R"(CV 20,6,7
CV 21,8,147
ae 42,72,768
s_bitcmp1_b32 s50, 14
s_cselect_b32 s52, 24576, 0
v_add_u32 v50, s52, v3
ST 14,128
ST 16,896
ST 18,1664
ST 20,2432
AR 6,144
AR 7,168
AR 8,192
EF 6,52
EF 7,53
EF 8,54
EF 148,55
CV 22,6,7
CV 23,8,148
AR 6,145
AR 7,169
AR 8,193
EF 6,52
EF 7,53
EF 8,54
EF 149,55
CV 24,6,7
CV 25,8,149
AR 6,146
AR 7,170
AR 8,194
EF 6,52
EF 7,53
EF 8,54
EF 150,55
CV 26,6,7
CV 27,8,150
AR 6,147
AR 7,171
AR 8,195
EF 6,52
EF 7,53
EF 8,54
EF 151,55
CV 28,6,7
CV 29,8,151
ae 42,72,768
s_bfe_u32 s52, s50, 0x1000f
v_mad_i32_i24 v50, v49, s52, v48
ST 22,128
ST 24,896
ST 26,1664
ST 28,2432
BV 52,4608
WL 1
EF 168,12
EF 192,13
EF 216,46
EF 240,47
CV 30,168,192
CV 31,216,240
EF 169,12
EF 193,13
EF 217,46
EF 241,47
CV 32,169,193
CV 33,217,241
EF 170,12
EF 194,13
EF 218,46
EF 242,47
CV 34,170,194
CV 35,218,242
EF 171,12
EF 195,13
EF 219,46
EF 243,47
CV 36,171,195
CV 37,219,243
ae 42,72,768
s_bitcmp1_b32 s50, 14
s_cselect_b32 s52, 24576, 0
v_add_u32 v50, s52, v3
ST 30,256
ST 32,1024
ST 34,1792
ST 36,2560
EF 172,12
EF 196,13
EF 220,46
EF 244,47
CV 38,172,196
CV 39,220,244
EF 173,12
EF 197,13
EF 221,46
EF 245,47
CV 40,173,197
CV 41,221,245
EF 174,12
EF 198,13
EF 222,46
EF 246,47
CV 42,174,198
CV 43,222,246
EF 175,12
EF 199,13
EF 223,46
EF 247,47
CV 44,175,199
CV 45,223,247
ae 42,72,768
s_bfe_u32 s52, s50, 0x1000f
v_mad_i32_i24 v50, v49, s52, v48
ST 38,256
ST 40,1024
ST 42,1792
ST 44,2560
ds_read_b64 v[12:13], v4 offset:4864
ds_read_b64 v[46:47], v4 offset:4872
AR 6,52
AR 7,76
AR 8,100
AR 9,124
WL 2
EF 6,52
EF 7,53
EF 8,54
EF 9,55
CV 14,6,7
CV 15,8,9
AR 6,53
AR 7,77
AR 8,101
AR 9,125
EF 6,52
EF 7,53
EF 8,54
EF 9,55
CV 16,6,7
CV 17,8,9
AR 6,54
AR 7,78
AR 8,102
AR 9,126
EF 6,52
EF 7,53
EF 8,54
EF 9,55
CV 18,6,7
CV 19,8,9
AR 6,55
AR 7,79
AR 8,103
AR 9,127
EF 6,52
EF 7,53
EF 8,54
EF 9,55
CV 20,6,7
CV 21,8,9
ae 42,72,864
s_bitcmp1_b32 s50, 16
s_cselect_b32 s52, 24576, 0
v_add_u32 v50, s52, v3
ST 14,0
ST 16,768
ST 18,1536
ST 20,2304
AR 6,56
AR 7,80
AR 8,104
AR 9,128
EF 6,52
EF 7,53
EF 8,54
EF 9,55
CV 22,6,7
CV 23,8,9
AR 6,57
AR 7,81
AR 8,105
AR 9,129
EF 6,52
EF 7,53
EF 8,54
EF 9,55
CV 24,6,7
CV 25,8,9
AR 6,58
AR 7,82
AR 8,106
AR 9,130
EF 6,52
EF 7,53
EF 8,54
EF 9,55
CV 26,6,7
CV 27,8,9
AR 6,59
AR 7,83
AR 8,107
AR 9,131
EF 6,52
EF 7,53
EF 8,54
EF 9,55
CV 28,6,7
CV 29,8,9
ae 42,72,864
s_bfe_u32 s52, s50, 0x10011
v_mad_i32_i24 v50, v49, s52, v48
ST 22,0
ST 24,768
ST 26,1536
ST 28,2304
BV 52,5120
AR 6,148
AR 7,172
AR 8,196
WL 1
EF 6,12
EF 7,13
EF 8,46
EF 152,47
CV 30,6,7
CV 31,8,152
AR 6,149
AR 7,173
AR 8,197
EF 6,12
EF 7,13
EF 8,46
EF 153,47
CV 32,6,7
CV 33,8,153
AR 6,150
AR 7,174
AR 8,198
EF 6,12
EF 7,13
EF 8,46
EF 154,47
CV 34,6,7
CV 35,8,154
AR 6,151
AR 7,175
AR 8,199
EF 6,12
EF 7,13
EF 8,46
EF 155,47
CV 36,6,7
CV 37,8,155
ae 42,72,864
s_bitcmp1_b32 s50, 16
s_cselect_b32 s52, 24576, 0
v_add_u32 v50, s52, v3
ST 30,128
ST 32,896
ST 34,1664
ST 36,2432
AR 6,152
AR 7,176
EF 6,12
EF 7,13
EF 132,46
EF 156,47
CV 38,6,7
CV 39,132,156
AR 6,153
AR 7,177
EF 6,12
EF 7,13
EF 133,46
EF 157,47
CV 40,6,7
CV 41,133,157
AR 6,154
AR 7,178
EF 6,12
EF 7,13
EF 134,46
EF 158,47
CV 42,6,7
CV 43,134,158
AR 6,155
AR 7,179
EF 6,12
EF 7,13
EF 135,46
EF 159,47
CV 44,6,7
CV 45,135,159
ae 42,72,864
s_bfe_u32 s52, s50, 0x10011
v_mad_i32_i24 v50, v49, s52, v48
ST 38,128
ST 40,896
ST 42,1664
ST 44,2432
WL 0
EF 176,52
EF 200,53
EF 224,54
EF 248,55
CV 14,176,200
CV 15,224,248
EF 177,52
EF 201,53
EF 225,54
EF 249,55
CV 16,177,201
CV 17,225,249
EF 178,52
EF 202,53
EF 226,54
EF 250,55
CV 18,178,202
CV 19,226,250
EF 179,52
EF 203,53
EF 227,54
EF 251,55
CV 20,179,203
CV 21,227,251
ae 42,72,864
s_bitcmp1_b32 s50, 16
s_cselect_b32 s52, 24576, 0
v_add_u32 v50, s52, v3
ST 14,256
ST 16,1024
ST 18,1792
ST 20,2560
EF 180,52
EF 204,53
EF 228,54
EF 252,55
CV 22,180,204
CV 23,228,252
EF 181,52
EF 205,53
EF 229,54
EF 253,55
CV 24,181,205
CV 25,229,253
EF 182,52
EF 206,53
EF 230,54
EF 254,55
CV 26,182,206
CV 27,230,254
EF 183,52
EF 207,53
EF 231,54
EF 255,55
CV 28,183,207
CV 29,231,255
ae 42,72,864
s_bfe_u32 s52, s50, 0x10011
v_mad_i32_i24 v50, v49, s52, v48
ST 22,256
ST 24,1024
ST 26,1792
ST 28,2560
s_waitcnt vmcnt(0) lgkmcnt(0)
s_branch 29f
FY 132
FY 136
FY 140
FY 144
FY 148
FY 152
FY 156
FY 160
FY 164
FY 168
FY 172
FY 176
FY 180
FY 184
FY 188
FY 192
FY 196
FY 200
FY 204
FY 208
FY 212
FY 216
FY 220
FY 224
FY 228
FY 232
FY 236
FY 240
FY 244
FY 248
FY 252
FY 256
FY 260
)"
R"(FY 264
FY 268
29:
.purgem MA
.purgem MV
.purgem NA
.purgem NV
.purgem AN
.purgem SH
.purgem PA_
.purgem PB_
.purgem AW
.purgem AR
.purgem UL
.purgem UH
.purgem AD
.purgem EF
.purgem TY
.purgem TX
.purgem RA
.purgem RC
.purgem BV
.purgem PA
.purgem DF
.purgem DW
.purgem DR
.purgem DH
.purgem DG
.purgem XL
.purgem ST
.purgem SA
.purgem CV
.purgem RS
.purgem PM
.purgem PF
.purgem FS
.purgem FL
.purgem FB
.purgem FC
.purgem FG
.purgem FX
.purgem FP
.purgem FR
.purgem FQ
.purgem FZ
.purgem FY
.purgem WL
.purgem WV
.purgem M
.purgem R1
.purgem R
.purgem P
.purgem Q
.purgem mw
.purgem mh
.purgem ad
.purgem ae
.purgem bc
.purgem W
.purgem H
)"
      : [flg] "=&s"(flg) : K2_OPS : K2_CLOB);
  if (flg) {
    const unsigned lane = __builtin_amdgcn_mbcnt_hi(~0u, __builtin_amdgcn_mbcnt_lo(~0u, 0u));
    const u16* const volatile __attribute__((address_space(4)))* ka = (const u16* const volatile __attribute__((address_space(4)))*)__builtin_amdgcn_kernarg_segment_ptr();
    {
      const u16* w1p = ka[6];
#pragma unroll 1
      for (unsigned f = blockIdx.x * 4 + w; f < 1152; f += 1024) {
        const u16* p = w1p + (32 * (f % 12) + 8 * (lane >> 4)) * HID + 16 * (f / 12) + (lane & 15);
        u32x4 v;
#pragma unroll
        for (int j = 0; j < 4; ++j) v[j] = p[2 * j * HID] | (unsigned)p[(2 * j + 1) * HID] << 16;
        *(u32x4*)(g_buf + f * 1024 + lane * 16) = v;
      }
    }
    slow_bar((unsigned*)(g_buf + G_BAR), w);
    slow_fc1(ka[0], ka[5], (char*)ka[1], (float*)smem + w * HID, w);
    slow_bar((unsigned*)(g_buf + G_BAR + 64), w);
    slow_fc2(ka[0], (const char*)ka[1], (u16*)ka[2], ka[3], ka[4], smem, w);
  }
}

extern "C" void kernel_launch(void* const* d_in, const int* in_sizes, int n_in,
                              void* d_out, int out_size, void* d_ws, size_t ws_size,
                              hipStream_t stream) {
  const u16* x = (const u16*)d_in[0];
  const u16* dw = (const u16*)d_in[1];
  const u16* w1 = (const u16*)d_in[2];
  const u16* w2 = (const u16*)d_in[3];
  const u16* grn_g = (const u16*)d_in[4];
  const u16* grn_b = (const u16*)d_in[5];
  char* ws = (char*)d_ws;
  if (ws_size < H_BYTES) return;
  (void)hipFuncSetAttribute((const void*)k1_fc1, hipFuncAttributeMaxDynamicSharedMemorySize, K1_LDS);
  (void)hipFuncSetAttribute((const void*)k2_fc2, hipFuncAttributeMaxDynamicSharedMemorySize, K2_LDS);

  k0_prep<<<K0_NB + NPAGE + NPAGX, 256, 0, stream>>>(w1, dw, ws, (const char*)x, grn_b, w2);
  k1_fc1<<<NWG + 8, 256, K1_LDS, stream>>>(x, ws, (const u16*)d_out, grn_g);
  k2_fc2<<<NWG, 256, K2_LDS, stream>>>(x, ws, (u16*)d_out, grn_g, w2, dw, w1);
}
```
